```python
import torch

def _softmax(x, axis=-1):
    return torch.softmax(x, dim=axis)

def _gelu(x):
    return torch.nn.functional.gelu(x, approximate='tanh')

def _silu(x):
    return torch.nn.functional.silu(x)

def _sigmoid(x):
    return torch.sigmoid(x)

def _layernorm(x, axis=-1, eps=1e-05):
    m = x.mean(dim=axis, keepdim=True)
    v = x.var(dim=axis, keepdim=True, unbiased=False)
    return (x - m) / torch.sqrt(v + eps)

def _scatter_add(tgt, idx, src, axis=0):
    return tgt.clone().index_add_(axis, idx, src)

def _segment_sum(src, seg, n):
    out = torch.zeros((n,) + src.shape[1:], dtype=src.dtype)
    return out.index_add_(0, seg, src)

def _pad(x, pw):
    flat = tuple((v for pair in reversed(pw) for v in pair))
    return torch.nn.functional.pad(x, flat)
kernel_name = 'adaLN_cond__compressed_kv_cla__moe_arch_mod__prompt'

def setup_inputs():

    _w = lambda a: (lambda t: t.to(torch.bfloat16) if t.is_floating_point() else t)(torch.as_tensor(a))

    def _A_setup():
        B, H, Sq, Sk, D, M = (1, 32, 2048, 2048, 128, 512)
        Dc = 4 * D
        return {'x': _w((lambda *s: torch.randn(*s, dtype=torch.bfloat16) * 0.1)(B, Sq, M)), 'c_kv': _w((lambda *s: torch.randn(*s, dtype=torch.bfloat16) * 3.2)(B, Sk, Dc)), 'w_q': _w((lambda *s: torch.randn(*s, dtype=torch.bfloat16) * 0.8)(M, H, D) * M ** (-0.5)), 'w_uk': _w((lambda *s: torch.randn(*s, dtype=torch.bfloat16) * 0.8)(Dc, H, D) * Dc ** (-0.5)), 'w_uv': _w((lambda *s: torch.randn(*s, dtype=torch.bfloat16) * 3.2)(Dc, H, D) * Dc ** (-0.5)), 'w_o': _w((lambda *s: torch.randn(*s, dtype=torch.bfloat16) * 3.2)(H, D, M) * (H * D) ** (-0.5))}
    _A_in = _A_setup()

    def _B_setup():
        B, S, D, F, C = (1, 2048, 512, 1792, 1)
        return {'x': _w((lambda *s: torch.randn(*s, dtype=torch.bfloat16) * 0.1)(B, S, D)), 'w_r': _w((lambda *s: torch.randn(*s, dtype=torch.bfloat16) * 3.2)(D)), 'w_gate': _w((lambda *s: torch.randn(*s, dtype=torch.bfloat16) * 3.2)(D, F)), 'w_up': _w((lambda *s: torch.randn(*s, dtype=torch.bfloat16) * 3.2)(D, F)), 'w_down': _w((lambda *s: torch.randn(*s, dtype=torch.bfloat16) * 3.2)(F, D))}
    _B_in = _B_setup()
    return {'A_x': _A_in['x'], 'A_c_kv': _A_in['c_kv'], 'A_w_q': _A_in['w_q'], 'A_w_uk': _A_in['w_uk'], 'A_w_uv': _A_in['w_uv'], 'A_w_o': _A_in['w_o'], 'B_x': _B_in['x'], 'B_w_r': _B_in['w_r'], 'B_w_gate': _B_in['w_gate'], 'B_w_up': _B_in['w_up'], 'B_w_down': _B_in['w_down'], 'c': _w((lambda *s: torch.randn(*s, dtype=torch.bfloat16) * 0.2)(1, 512) * 8.0), 'w_mod': _w((lambda *s: torch.randn(*s, dtype=torch.bfloat16) * 0.2)(512, 6 * 512) * 512 ** (-0.5) * 8.0)}

def reference(A_x, A_c_kv, A_w_q, A_w_uk, A_w_uv, A_w_o, B_x, B_w_r, B_w_gate, B_w_up, B_w_down, c, w_mod):

    def _A_ref(x, c_kv, w_q, w_uk, w_uv, w_o):
        B, Sq, M = (x.shape[0], x.shape[1], x.shape[2])
        Sk = c_kv.shape[1]
        H, D = (w_q.shape[1], w_q.shape[2])
        q = torch.einsum('bqm,mhd->bhqd', x, w_q)
        k = torch.einsum('bkc,chd->bhkd', c_kv, w_uk)
        v = torch.einsum('bkc,chd->bhkd', c_kv, w_uv)
        scores = torch.einsum('bhqd,bhkd->bhqk', q, k) * D ** (-0.5)
        off = Sk - Sq
        qi = torch.arange(Sq)[:, None]
        kj = torch.arange(Sk)[None, :]
        cmask = kj <= qi + off
        scores = torch.where(cmask[None, None], scores, -1000000000.0)
        p = _softmax(scores, axis=-1)
        ctx = torch.einsum('bhqk,bhkd->bhqd', p, v)
        return torch.einsum('bhqd,hdm->bqm', ctx, w_o)

    def _B_ref(x, w_r, w_gate, w_up, w_down):
        B, S, D = x.shape
        C = 1
        r = x @ w_r
        idx = torch.argsort(-r, axis=-1)[:, :C]
        idx_e = (lambda x, n, axis: torch.repeat_interleave(x, n, dim=axis))(idx.reshape(B, C, 1), D, axis=2)
        x_sel = (lambda x, i, axis: torch.take_along_dim(x, i, dim=axis))(x, idx_e, axis=1)
        g = _silu(x_sel @ w_gate)
        u = x_sel @ w_up
        y = g * u @ w_down
        r_sel = _sigmoid((lambda x, i, axis: torch.take_along_dim(x, i, dim=axis))(r, idx, axis=1))
        y = y * r_sel.reshape(B, C, 1)
        b_off = (torch.arange(B) * S).reshape(B, 1)
        flat_idx = (idx + b_off).reshape(B * C)
        zero = (x * 0.0).reshape(B * S, D)
        upd = _scatter_add(zero, flat_idx, y.reshape(B * C, D), axis=0)
        return x + upd.reshape(B, S, D)
    x = A_x
    mod = (_silu(c) @ w_mod).reshape(c.shape[0], 6, 1, -1)
    sa, ca, ga, sb, cb, gb = (mod[:, 0].clone(), mod[:, 1].clone(), mod[:, 2].clone(), mod[:, 3].clone(), mod[:, 4].clone(), mod[:, 5].clone())
    h = _layernorm(x, axis=-1) * (1 + ca) + sa
    x = x + ga * _A_ref(h, A_c_kv, A_w_q, A_w_uk, A_w_uv, A_w_o)
    h = _layernorm(x, axis=-1) * (1 + cb) + sb
    x = x + gb * _B_ref(h, B_w_r, B_w_gate, B_w_up, B_w_down)
    return x
```

```cpp
#include <hip/hip_runtime.h>
#include <hip/hip_bf16.h>
#include <stdint.h>
typedef unsigned short u16;
typedef float f32x4 __attribute__((ext_vector_type(4)));
typedef float f32x2 __attribute__((ext_vector_type(2)));
typedef unsigned u32x4 __attribute__((ext_vector_type(4)));
typedef unsigned u32x2 __attribute__((ext_vector_type(2)));
typedef unsigned u32x16 __attribute__((ext_vector_type(16)));
typedef __bf16 bf16x2_t __attribute__((ext_vector_type(2)));
constexpr int SEQ = 2048, DM = 512, FF = 1792;
#define DI __device__ __forceinline__
#define LDSBAR() do { asm volatile("s_waitcnt lgkmcnt(0)" ::: "memory"); __builtin_amdgcn_s_barrier(); asm volatile("" ::: "memory"); } while (0)
DI unsigned lane_id() { unsigned l; asm volatile("v_mbcnt_lo_u32_b32 %0, -1, 0\n\tv_mbcnt_hi_u32_b32 %0, -1, %0" : "=v"(l)); return l; }
DI unsigned rfl(unsigned x) { return __builtin_amdgcn_readfirstlane(x); }
DI unsigned cvt2(float lo, float hi) { f32x2 v = {lo, hi}; bf16x2_t b = __builtin_convertvector(v, bf16x2_t); return __builtin_bit_cast(unsigned, b); }
DI float rb(float x) { return __uint_as_float(cvt2(0.f, x)); }
DI float dot2bf(unsigned a, unsigned b, float c) { return __builtin_amdgcn_fdot2_f32_bf16(__builtin_bit_cast(bf16x2_t, a), __builtin_bit_cast(bf16x2_t, b), c, false); }
DI float lo2f(unsigned w) { return __uint_as_float(w << 16); }
DI float hi2f(unsigned w) { return __uint_as_float(w & 0xffff0000u); }
#define DPPF(v, ctl) __builtin_bit_cast(float, __builtin_amdgcn_update_dpp(0, __builtin_bit_cast(int, v), ctl, 0xF, 0xF, true))
DI float row16_sum(float v) { v += DPPF(v, 0xB1); v += DPPF(v, 0x4E); v += DPPF(v, 0x141); v += DPPF(v, 0x140); return v; }
DI float swap16_sum(float a, float b) { auto r = __builtin_amdgcn_permlane16_swap(__float_as_uint(a), __float_as_uint(b), false, false); return __uint_as_float(r[0]) + __uint_as_float(r[1]); }
DI float swap32_sum(float a, float b) { auto r = __builtin_amdgcn_permlane32_swap(__float_as_uint(a), __float_as_uint(b), false, false); return __uint_as_float(r[0]) + __uint_as_float(r[1]); }
DI float wave_sum2(float v) { v = row16_sum(v); v = swap16_sum(v, v); return swap32_sum(v, v); }
DI unsigned wave_umax(unsigned v) {
    v = max(v, (unsigned)__builtin_amdgcn_update_dpp(0, (int)v, 0xB1, 0xF, 0xF, true)); v = max(v, (unsigned)__builtin_amdgcn_update_dpp(0, (int)v, 0x4E, 0xF, 0xF, true));
    v = max(v, (unsigned)__builtin_amdgcn_update_dpp(0, (int)v, 0x141, 0xF, 0xF, true)); v = max(v, (unsigned)__builtin_amdgcn_update_dpp(0, (int)v, 0x140, 0xF, 0xF, true));
    { auto r = __builtin_amdgcn_permlane16_swap(v, v, false, false); v = max((unsigned)r[0], (unsigned)r[1]); }
    { auto r = __builtin_amdgcn_permlane32_swap(v, v, false, false); v = max((unsigned)r[0], (unsigned)r[1]); }
    return v;
}
#define BODY_ASM ".macro M a,b,c,d\nv_mfma_f32_16x16x32_bf16 \\a,\\b,\\c,\\d\n.endm\n" \
".Lcode_b:\ns_load_dwordx16 s[8:23],s[4:5],0x0\ns_load_dwordx8 s[24:31],s[4:5],0x40\ns_load_dwordx2 s[34:35],s[4:5],0x60\nv_mbcnt_lo_u32_b32 v249,-1,0\nv_mbcnt_hi_u32_b32 v249,-1,v249\ns_getpc_b64 s[56:57]\n.Lpc0:\n" \
"s_and_b32 s57,s57,0xffff\ns_mov_b32 s58,.Lcode_e-.Lpc0\ns_mov_b32 s59,0x20000\ns_bfe_u32 s61,s6,0x20003\nv_lshlrev_b32 v251,7,v249\ns_lshl_b32 s64,s7,12\ns_lshl_b32 s70,s7,13\ns_cmp_eq_u32 s61,0\n" \
"s_cselect_b32 s70,s70,0x8000000\nbuffer_load_dword v247,v251,s[56:59],s70 offen\ns_and_b32 s60,s6,7\ns_lshr_b32 s62,s6,5\ns_lshl2_add_u32 s63,s60,s61\nv_lshlrev_b32 v240,4,v249\ns_cmp_lt_u32 s6,192\ns_cselect_b32 s70,s6,0\n" \
"s_mul_i32 s71,s70,171\ns_lshr_b32 s71,s71,13\ns_mul_i32 s72,s71,48\ns_sub_u32 s70,s70,s72\ns_lshl2_add_u32 s72,s71,s7\ns_lshl_b32 s72,s72,5\nv_lshrrev_b32 v0,3,v249\nv_lshl_add_u32 v1,v0,2,s72\n" \
"v_and_b32 v2,7,v249\nv_mul_u32_u24 v3,6144,v1\nv_lshl_add_u32 v3,v2,4,v3\ns_lshl_b32 s72,s70,7\ns_add_u32 s72,s72,3072\nv_add_u32 v3,s72,v3\nv_add_u32 v4,12288,v3\nv_lshlrev_b32 v1,1,v1\n" \
"s_mul_i32 s71,s71,12288\ns_lshl_b32 s70,s70,8\ns_add_u32 s79,s70,s71\ns_add_u32 s79,s79,786432\ns_cmp_eq_u32 s7,0\ns_cselect_b32 s75,0,0x8000000\ns_cmp_lt_u32 s6,192\ns_cselect_b32 s70,s75,0x8000000\n" \
"s_or_b32 s79,s79,s70\ns_lshl_b32 s70,s62,7\ns_xor_b32 s71,s63,12\ns_lshl3_add_u32 s71,s71,s70\ns_lshl_b32 s72,s63,3\ns_sub_u32 s72,s72,s70\ns_add_u32 s72,s72,1792\ns_cmp_lt_u32 s63,16\n" \
"s_cselect_b32 s71,s71,s72\ns_lshl_b32 s71,s71,10\ns_lshl_b32 s72,s7,7\ns_add_u32 s71,s71,s72\nv_lshlrev_b32 v5,10,v2\nv_lshl_add_u32 v5,v0,4,v5\nv_add_u32 v5,s71,v5\nv_bfe_u32 v6,v249,2,3\n" \
"v_lshrrev_b32 v7,5,v249\nv_and_b32 v8,48,v240\nv_lshlrev_b32 v6,13,v6\nv_lshl_or_b32 v7,v7,6,v8\nv_or_b32 v6,0x1000,v6\nv_or_b32 v243,v6,v7\nv_add_u32 v244,0xfffffc80,v243\nv_xor_b32 v6,32,v243\n" \
"v_add_u32 v245,63488,v6\nv_add_u32 v246,62592,v6\nv_lshrrev_b32 v6,4,v249\nv_xor_b32 v7,v6,v2\nv_lshlrev_b32 v7,4,v7\nv_lshl_or_b32 v224,v0,10,v7\nv_xor_b32 v7,64,v224\nv_add_u32 v225,7168,v7\n" \
"v_add_u32 v226,14336,v224\nv_add_u32 v227,21504,v7\ns_lshl_b32 s70,s62,1\ns_sub_u32 s71,s7,s70\ns_add_u32 s71,s71,30\ns_sub_u32 s72,s70,s7\ns_add_u32 s72,s72,3\ns_cmp_lt_u32 s7,2\n" \
"s_cselect_b32 s74,s71,s72\ns_lshl_b32 s66,s74,16\ns_mul_i32 s65,s7,24576\ns_lshl_b32 s68,s7,17\ns_lshl_b32 s70,s63,8\ns_add_u32 s68,s68,s70\ns_mov_b32 s38,2097152\ns_mov_b32 s39,0x20000\n" \
"s_mov_b32 s42,4198400\ns_mov_b32 s43,0x20000\ns_mov_b32 s54,71303168\ns_mov_b32 s55,0x20000\ns_mov_b32 s70,94208\ns_waitcnt lgkmcnt(0)\ns_mov_b64 s[52:53],s[34:35]\ns_mov_b64 s[36:37],s[10:11]\n" \
"s_sub_u32 s40,s14,0x1000\ns_subb_u32 s41,s15,0\nbuffer_load_dwordx2 v[230:231],off,s[52:55],s70 sc1\nglobal_load_dwordx2 v[10:11],v1,s[28:29]\nglobal_load_dwordx4 v[12:15],v3,s[30:31] offset:-3072\nglobal_load_dwordx4 v[16:19],v3,s[30:31] offset:3072\nglobal_load_dwordx4 v[20:23],v4,s[30:31] offset:-3072\nglobal_load_dwordx4 v[24:27],v4,s[30:31] offset:3072\n" \
"global_load_dwordx4 v[128:131],v5,s[8:9]\nglobal_load_dwordx4 v[132:135],v5,s[8:9] offset:512\ns_add_u32 m0,s64,98304\ns_add_u32 s69,s68,0\nbuffer_load_dwordx4 v243,s[40:43],s69 offen offset:0 lds\nbuffer_load_dwordx4 v244,s[40:43],s69 offen offset:1024 lds\nbuffer_load_dwordx4 v245,s[40:43],s69 offen offset:2048 lds\nbuffer_load_dwordx4 v246,s[40:43],s69 offen offset:3072 lds\n" \
"s_add_u32 m0,s65,0\ns_add_u32 s67,s66,0\nbuffer_load_dwordx4 v224,s[36:39],s67 offen offset:0 lds\nbuffer_load_dwordx4 v225,s[36:39],s67 offen offset:1024 lds\nbuffer_load_dwordx4 v226,s[36:39],s67 offen offset:2048 lds\nbuffer_load_dwordx4 v227,s[36:39],s67 offen offset:3072 lds\ns_add_u32 m0,s65,4096\ns_add_u32 s67,s66,32768\n" \
"buffer_load_dwordx4 v224,s[36:39],s67 offen offset:0 lds\nbuffer_load_dwordx4 v225,s[36:39],s67 offen offset:1024 lds\nbuffer_load_dwordx4 v226,s[36:39],s67 offen offset:2048 lds\nbuffer_load_dwordx4 v227,s[36:39],s67 offen offset:3072 lds\ns_min_u32 s70,s6,223\ns_lshl_b32 s70,s70,11\ns_lshl_b32 s71,s7,9\ns_add_u32 s70,s70,s71\n" \
"s_add_u32 s70,s70,98304\nv_mov_b32 v28,-1\nv_mov_b32 v29,-1\nv_lshlrev_b32 v30,3,v249\nbuffer_store_dwordx2 v[28:29],v30,s[52:55],s70 offen sc1\ns_lshl_b32 s70,s6,7\ns_cmp_lt_u32 s6,224\ns_cselect_b32 s71,65536,0x40000000\n" \
"s_cmp_lt_u32 s6,128\ns_cselect_b32 s72,556928,0x40000000\nv_mov_b32 v31,s71\nv_mov_b32 v32,s72\nv_cmp_lt_u32 vcc,31,v249\ns_or_b32 s70,s70,s75\nv_mov_b32 v33,0\nv_cndmask_b32 v31,v31,v32,vcc\n" \
"v_lshl_add_u32 v31,v249,2,v31\nbuffer_store_dword v33,v31,s[52:55],s70 offen sc1\nv_and_b32 v34,15,v249\nv_lshrrev_b32 v35,1,v34\nv_xor_b32 v35,v35,v6\nv_lshlrev_b32 v35,4,v35\nv_lshl_or_b32 v35,v34,7,v35\nv_add_u32 v228,s65,v35\n" \
"v_xor_b32 v229,64,v228\nv_bfe_u32 v34,v249,2,2\nv_and_b32 v35,3,v249\nv_lshlrev_b32 v35,3,v35\nv_lshl_or_b32 v35,v34,6,v35\nv_lshl_or_b32 v35,v6,11,v35\nv_and_b32 v34,16,v249\nv_lshl_or_b32 v35,v34,1,v35\n" \
"v_add_u32 v241,98304,v35\nv_xor_b32 v242,32,v241\ns_mov_b32 s73,0x400000\nv_mov_b32 v254,1\ns_mov_b32 s70,0x05040100\ns_mov_b32 s71,0x07060302\ns_waitcnt vmcnt(20)\nv_lshlrev_b32 v36,16,v10\n" \
"v_and_b32 v37,0xffff0000,v10\nv_lshlrev_b32 v38,16,v11\nv_and_b32 v39,0xffff0000,v11\nv_mul_f32 v40,0xbfb8aa3b,v36\nv_mul_f32 v41,0xbfb8aa3b,v37\nv_mul_f32 v42,0xbfb8aa3b,v38\nv_mul_f32 v43,0xbfb8aa3b,v39\nv_exp_f32 v40,v40\n" \
"v_exp_f32 v41,v41\nv_exp_f32 v42,v42\nv_exp_f32 v43,v43\nv_add_f32 v40,1.0,v40\nv_add_f32 v41,1.0,v41\nv_add_f32 v42,1.0,v42\nv_add_f32 v43,1.0,v43\nv_rcp_f32 v40,v40\n" \
"v_rcp_f32 v41,v41\nv_rcp_f32 v42,v42\nv_rcp_f32 v43,v43\nv_mul_f32 v36,v36,v40\nv_mul_f32 v37,v37,v41\nv_mul_f32 v38,v38,v42\nv_mul_f32 v39,v39,v43\nv_cvt_pk_bf16_f32 v36,v36,v37\n" \
"v_cvt_pk_bf16_f32 v37,v38,v39\ns_waitcnt vmcnt(18)\nv_perm_b32 v48,v16,v12,s70\nv_perm_b32 v49,v16,v12,s71\nv_perm_b32 v50,v17,v13,s70\nv_perm_b32 v51,v17,v13,s71\nv_perm_b32 v52,v18,v14,s70\nv_perm_b32 v53,v18,v14,s71\n" \
"v_perm_b32 v54,v19,v15,s70\nv_perm_b32 v55,v19,v15,s71\nv_dot2_f32_bf16 v40,v48,v36,0\nv_dot2_f32_bf16 v41,v49,v36,0\nv_dot2_f32_bf16 v42,v50,v36,0\nv_dot2_f32_bf16 v43,v51,v36,0\nv_dot2_f32_bf16 v44,v52,v36,0\nv_dot2_f32_bf16 v45,v53,v36,0\n" \
"v_dot2_f32_bf16 v46,v54,v36,0\nv_dot2_f32_bf16 v47,v55,v36,0\ns_waitcnt vmcnt(16)\nv_perm_b32 v48,v24,v20,s70\nv_perm_b32 v49,v24,v20,s71\nv_perm_b32 v50,v25,v21,s70\nv_perm_b32 v51,v25,v21,s71\nv_perm_b32 v52,v26,v22,s70\n" \
"v_perm_b32 v53,v26,v22,s71\nv_perm_b32 v54,v27,v23,s70\nv_perm_b32 v55,v27,v23,s71\nv_dot2_f32_bf16 v40,v48,v37,v40\nv_dot2_f32_bf16 v41,v49,v37,v41\nv_dot2_f32_bf16 v42,v50,v37,v42\nv_dot2_f32_bf16 v43,v51,v37,v43\nv_dot2_f32_bf16 v44,v52,v37,v44\n" \
"v_dot2_f32_bf16 v45,v53,v37,v45\nv_dot2_f32_bf16 v46,v54,v37,v46\nv_dot2_f32_bf16 v47,v55,v37,v47\ns_waitcnt vmcnt(15)\nv_lshlrev_b32 v136,16,v128\nv_and_b32 v137,0xffff0000,v128\nv_lshlrev_b32 v138,16,v129\nv_and_b32 v139,0xffff0000,v129\n" \
"v_lshlrev_b32 v140,16,v130\nv_and_b32 v141,0xffff0000,v130\nv_lshlrev_b32 v142,16,v131\nv_and_b32 v143,0xffff0000,v131\ns_waitcnt vmcnt(14)\nv_lshlrev_b32 v144,16,v132\nv_and_b32 v145,0xffff0000,v132\nv_lshlrev_b32 v146,16,v133\n" \
"v_and_b32 v147,0xffff0000,v133\nv_lshlrev_b32 v148,16,v134\nv_and_b32 v149,0xffff0000,v134\nv_lshlrev_b32 v150,16,v135\nv_and_b32 v151,0xffff0000,v135\nv_permlane32_swap_b32 v40,v44\nv_permlane32_swap_b32 v41,v45\nv_permlane32_swap_b32 v42,v46\n" \
"v_permlane32_swap_b32 v43,v47\nv_add_f32 v40,v40,v44\nv_add_f32 v41,v41,v45\nv_add_f32 v42,v42,v46\nv_add_f32 v43,v43,v47\nv_add_f32 v48,v136,v140\nv_add_f32 v49,v137,v141\nv_add_f32 v50,v138,v142\n" \
"v_add_f32 v51,v139,v143\nv_permlane16_swap_b32 v40,v42\nv_permlane16_swap_b32 v41,v43\nv_add_f32 v52,v144,v148\nv_add_f32 v53,v145,v149\nv_add_f32 v54,v146,v150\nv_add_f32 v55,v147,v151\nv_add_f32 v40,v40,v42\n" \
"v_add_f32 v41,v41,v43\nv_add_f32 v48,v48,v52\nv_add_f32 v49,v49,v53\nv_add_f32 v50,v50,v54\nv_add_f32 v51,v51,v55\nv_add_f32_dpp v40,v40,v40 row_ror:8 row_mask:0xf bank_mask:0xf\nv_add_f32_dpp v41,v41,v41 row_ror:8 row_mask:0xf bank_mask:0xf\nv_add_f32 v48,v48,v49\n" \
"v_add_f32 v50,v50,v51\nv_lshlrev_b32 v42,5,v2\nv_add_f32 v48,v48,v50\nv_lshl_or_b32 v42,v6,3,v42\ns_lshl_b32 s72,s7,8\ns_add_u32 s72,s72,147456\nv_add_u32 v42,s72,v42\nds_write_b64 v42,v[40:41]\n" \
"v_add_f32_dpp v48,v48,v48 row_ror:8 row_mask:0xf bank_mask:0xf\nv_mov_b32 v50,v48\ns_nop 1\nv_permlane16_swap_b32 v48,v50\nv_add_f32 v48,v48,v50\nv_mov_b32 v50,v48\ns_nop 1\nv_permlane32_swap_b32 v48,v50\n" \
"v_add_f32 v48,v48,v50\nv_mul_f32 v48,0x3c000000,v48\nv_sub_f32 v56,v136,v48\nv_sub_f32 v57,v137,v48\nv_sub_f32 v58,v138,v48\nv_sub_f32 v59,v139,v48\nv_sub_f32 v60,v140,v48\nv_sub_f32 v61,v141,v48\n" \
"v_sub_f32 v62,v142,v48\nv_sub_f32 v63,v143,v48\nv_sub_f32 v64,v144,v48\nv_sub_f32 v65,v145,v48\nv_sub_f32 v66,v146,v48\nv_sub_f32 v67,v147,v48\nv_sub_f32 v68,v148,v48\nv_sub_f32 v69,v149,v48\n" \
"v_sub_f32 v70,v150,v48\nv_sub_f32 v71,v151,v48\nv_mul_f32 v52,v56,v56\nv_mul_f32 v53,v57,v57\nv_mul_f32 v54,v58,v58\nv_mul_f32 v55,v59,v59\nv_fmac_f32 v52,v60,v60\nv_fmac_f32 v53,v61,v61\n" \
"v_fmac_f32 v54,v62,v62\nv_fmac_f32 v55,v63,v63\nv_fmac_f32 v52,v64,v64\nv_fmac_f32 v53,v65,v65\nv_fmac_f32 v54,v66,v66\nv_fmac_f32 v55,v67,v67\nv_fmac_f32 v52,v68,v68\nv_fmac_f32 v53,v69,v69\n" \
"v_fmac_f32 v54,v70,v70\nv_fmac_f32 v55,v71,v71\nv_add_f32 v52,v52,v53\nv_add_f32 v54,v54,v55\ns_lshl_b32 s72,s7,3\nv_add_f32 v49,v52,v54\ns_add_u32 s72,s72,148480\nv_lshl_add_u32 v42,v2,5,s72\n" \
"v_add_f32_dpp v49,v49,v49 row_ror:8 row_mask:0xf bank_mask:0xf\nv_mov_b32 v50,v49\ns_nop 1\nv_permlane16_swap_b32 v49,v50\nv_add_f32 v49,v49,v50\nv_mov_b32 v50,v49\ns_nop 1\nv_permlane32_swap_b32 v49,v50\n" \
"v_add_f32 v49,v49,v50\nds_write_b64 v42,v[48:49]\nv_lshlrev_b32 v251,5,v2\nv_add_u32 v251,148480,v251\nv_lshlrev_b32 v250,2,v249\nv_add_u32 v250,147456,v250\ns_add_u32 m0,s64,114688\ns_add_u32 s69,s68,524288\n" \
"buffer_load_dwordx4 v243,s[40:43],s69 offen offset:0 lds\nbuffer_load_dwordx4 v244,s[40:43],s69 offen offset:1024 lds\nbuffer_load_dwordx4 v245,s[40:43],s69 offen offset:2048 lds\nbuffer_load_dwordx4 v246,s[40:43],s69 offen offset:3072 lds\ns_add_u32 m0,s65,8192\ns_add_u32 s67,s66,128\nbuffer_load_dwordx4 v224,s[36:39],s67 offen offset:0 lds\nbuffer_load_dwordx4 v225,s[36:39],s67 offen offset:1024 lds\n" \
"buffer_load_dwordx4 v226,s[36:39],s67 offen offset:2048 lds\nbuffer_load_dwordx4 v227,s[36:39],s67 offen offset:3072 lds\ns_add_u32 m0,s65,12288\ns_add_u32 s67,s66,32896\nbuffer_load_dwordx4 v224,s[36:39],s67 offen offset:0 lds\nbuffer_load_dwordx4 v225,s[36:39],s67 offen offset:1024 lds\nbuffer_load_dwordx4 v226,s[36:39],s67 offen offset:2048 lds\nbuffer_load_dwordx4 v227,s[36:39],s67 offen offset:3072 lds\n" \
"s_waitcnt lgkmcnt(0)\ns_waitcnt vmcnt(14)\ns_barrier\ns_add_u32 m0,s64,131072\ns_add_u32 s69,s68,1048576\nds_read_b128 a[0:3],v228 offset:0\nds_read_b128 a[4:7],v228 offset:2048\nds_read_b128 a[8:11],v228 offset:4096\n" \
"ds_read_b128 a[12:15],v228 offset:6144\nds_read_b128 a[16:19],v229 offset:0\nds_read_b128 a[20:23],v229 offset:2048\nds_read_b128 a[24:27],v229 offset:4096\nds_read_b128 a[28:31],v229 offset:6144\nds_read_b32 v152,v250 offset:0\nds_read_b32 v153,v250 offset:256\nds_read_b32 v154,v250 offset:512\n" \
"ds_read_b32 v155,v250 offset:768\ns_waitcnt lgkmcnt(2)\nv_add_f32 v152,v152,v153\ns_waitcnt lgkmcnt(0)\nv_add_f32 v154,v154,v155\nv_lshlrev_b32 v153,2,v249\nv_add_f32 v152,v152,v154\nds_read_b64_tr_b16 v[192:193],v241 offset:0\n" \
"ds_read_b64_tr_b16 v[194:195],v241 offset:256\nds_read_b64_tr_b16 v[196:197],v242 offset:0\nds_read_b64_tr_b16 v[198:199],v242 offset:256\nds_read_b64_tr_b16 v[200:201],v241 offset:512\nds_read_b64_tr_b16 v[202:203],v241 offset:768\nds_read_b64_tr_b16 v[204:205],v242 offset:512\nds_read_b64_tr_b16 v[206:207],v242 offset:768\nds_read_b64_tr_b16 v[208:209],v241 offset:1024\n" \
"ds_read_b64_tr_b16 v[210:211],v241 offset:1280\nds_read_b64_tr_b16 v[212:213],v242 offset:1024\nds_read_b64_tr_b16 v[214:215],v242 offset:1280\nds_read_b64_tr_b16 v[216:217],v241 offset:1536\nds_read_b64_tr_b16 v[218:219],v241 offset:1792\nds_read_b64_tr_b16 v[220:221],v242 offset:1536\nds_read_b64_tr_b16 v[222:223],v242 offset:1792\nbuffer_store_dword v152,v153,s[52:55],s79 offen sc1\n" \
"ds_read_b128 v[156:159],v251\nds_read_b128 v[160:163],v251 offset:16\ns_waitcnt lgkmcnt(15)\nM v[0:3],v[192:195],a[0:3],0\nbuffer_load_dwordx4 v243,s[40:43],s69 offen offset:0 lds\nM v[4:7],v[192:195],a[4:7],0\nv_bfe_u32 v253,v249,1,4\nv_and_b32 v252,1,v249\n" \
"M v[64:67],v[192:195],a[8:11],0\nv_lshlrev_b32 v253,13,v253\ns_lshl_b32 s78,s62,19\nM v[68:71],v[192:195],a[12:15],0\nds_read_b64_tr_b16 v[192:193],v241 offset:8192\nds_read_b64_tr_b16 v[194:195],v241 offset:8448\ns_waitcnt lgkmcnt(15)\nM v[8:11],v[196:199],a[0:3],0\n" \
"v_lshl_or_b32 v253,v252,7,v253\ns_add_u32 s78,s78,s68\nM v[12:15],v[196:199],a[4:7],0\nbuffer_load_dwordx4 v244,s[40:43],s69 offen offset:1024 lds\nM v[72:75],v[196:199],a[8:11],0\nv_add_u32 v253,s78,v253\ns_lshl3_add_u32 s72,s6,s75\nM v[76:79],v[196:199],a[12:15],0\n" \
"ds_read_b64_tr_b16 v[196:197],v242 offset:8192\nds_read_b64_tr_b16 v[198:199],v242 offset:8448\ns_waitcnt lgkmcnt(8)\nM v[16:19],v[200:203],a[0:3],0\nglobal_load_dword v247,v253,s[16:17]\nM v[20:23],v[200:203],a[4:7],0\nv_readfirstlane_b32 s92,v230\nv_readfirstlane_b32 s93,v231\n" \
"M v[80:83],v[200:203],a[8:11],0\nbuffer_load_dwordx4 v245,s[40:43],s69 offen offset:2048 lds\nM v[84:87],v[200:203],a[12:15],0\nds_read_b64_tr_b16 v[200:201],v241 offset:8704\nds_read_b64_tr_b16 v[202:203],v241 offset:8960\nM v[24:27],v[204:207],a[0:3],0\ns_add_u32 s70,s92,1\ns_addc_u32 s71,s93,0\n" \
"M v[28:31],v[204:207],a[4:7],0\ns_waitcnt lgkmcnt(4)\nv_add_f32 v164,v156,v158\nM v[88:91],v[204:207],a[8:11],0\nv_add_f32 v165,v160,v162\nv_add_f32 v164,v164,v165\nM v[92:95],v[204:207],a[12:15],0\nbuffer_load_dwordx4 v246,s[40:43],s69 offen offset:3072 lds\n" \
"ds_read_b64_tr_b16 v[204:205],v242 offset:8704\nds_read_b64_tr_b16 v[206:207],v242 offset:8960\nM v[32:35],v[208:211],a[0:3],0\ns_add_u32 s92,s92,0x7f4a7c15\ns_addc_u32 s93,s93,0x9e3779b9\nM v[36:39],v[208:211],a[4:7],0\ns_add_u32 m0,s65,16384\ns_add_u32 s67,s66,256\n" \
"M v[96:99],v[208:211],a[8:11],0\nv_and_b32 v165,7,v249\nv_mov_b32 v230,s92\nM v[100:103],v[208:211],a[12:15],0\nds_read_b64_tr_b16 v[208:209],v241 offset:9216\nds_read_b64_tr_b16 v[210:211],v241 offset:9472\nM v[40:43],v[212:215],a[0:3],0\nbuffer_load_dwordx4 v224,s[36:39],s67 offen offset:0 lds\n" \
"M v[44:47],v[212:215],a[4:7],0\nv_mov_b32 v231,s93\nv_lshlrev_b32 v165,11,v165\nM v[104:107],v[212:215],a[8:11],0\ns_add_u32 s72,s72,573440\ns_lshl2_add_u32 s80,s60,s7\nM v[108:111],v[212:215],a[12:15],0\nds_read_b64_tr_b16 v[212:213],v242 offset:9216\n" \
"ds_read_b64_tr_b16 v[214:215],v242 offset:9472\nM v[48:51],v[216:219],a[0:3],0\ns_lshl_b32 s80,s80,9\ns_add_u32 s80,s80,573440\nM v[52:55],v[216:219],a[4:7],0\nbuffer_load_dwordx4 v225,s[36:39],s67 offen offset:1024 lds\nM v[112:115],v[216:219],a[8:11],0\nv_mul_f32 v164,0x3e800000,v164\n" \
"v_sub_f32 v156,v156,v164\nM v[116:119],v[216:219],a[12:15],0\nds_read_b64_tr_b16 v[216:217],v241 offset:9728\nds_read_b64_tr_b16 v[218:219],v241 offset:9984\nM v[56:59],v[220:223],a[0:3],0\nv_sub_f32 v158,v158,v164\nv_mul_f32 v156,v156,v156\nM v[60:63],v[220:223],a[4:7],0\n" \
"v_sub_f32 v160,v160,v164\nv_fmac_f32 v156,v158,v158\nM v[120:123],v[220:223],a[8:11],0\nbuffer_load_dwordx4 v226,s[36:39],s67 offen offset:2048 lds\nM v[124:127],v[220:223],a[12:15],0\nds_read_b64_tr_b16 v[220:221],v242 offset:9728\nds_read_b64_tr_b16 v[222:223],v242 offset:9984\nM v[0:3],v[192:195],a[16:19],v[0:3]\n" \
"v_add_f32 v157,v157,v159\nv_add_f32 v161,v161,v163\nM v[4:7],v[192:195],a[20:23],v[4:7]\nv_sub_f32 v162,v162,v164\nv_fmac_f32 v156,v160,v160\nM v[64:67],v[192:195],a[24:27],v[64:67]\nv_add_f32 v157,v157,v161\nv_fmac_f32 v156,v162,v162\n" \
"M v[68:71],v[192:195],a[28:31],v[68:71]\nbuffer_load_dwordx4 v227,s[36:39],s67 offen offset:3072 lds\ns_waitcnt lgkmcnt(4)\nM v[8:11],v[196:199],a[16:19],v[8:11]\nv_fmac_f32 v157,0x43000000,v156\nv_mul_f32 v157,0x3b000000,v157\nM v[12:15],v[196:199],a[20:23],v[12:15]\ns_add_u32 m0,s65,20480\n" \
"s_add_u32 s67,s66,33024\nM v[72:75],v[196:199],a[24:27],v[72:75]\nv_cvt_pk_bf16_f32 v157,0,v157\nv_add_f32 v157,0x3727c5ac,v157\nM v[76:79],v[196:199],a[28:31],v[76:79]\nv_cvt_pk_bf16_f32 v157,0,v157\nv_mov_b32 v232,s70\nM v[16:19],v[200:203],a[16:19],v[16:19]\n" \
"buffer_load_dwordx4 v224,s[36:39],s67 offen offset:0 lds\nM v[20:23],v[200:203],a[20:23],v[20:23]\nv_sqrt_f32 v157,v157\nM v[80:83],v[200:203],a[24:27],v[80:83]\nv_mov_b32 v233,s71\ns_lshr_b32 s70,s64,2\nM v[84:87],v[200:203],a[28:31],v[84:87]\nv_cvt_pk_bf16_f32 v164,0,v164\n" \
"v_cvt_pk_bf16_f32 v157,0,v157\nM v[24:27],v[204:207],a[16:19],v[24:27]\ns_add_u32 s71,s70,786432\nv_sub_f32 v136,v136,v164\nM v[28:31],v[204:207],a[20:23],v[28:31]\nbuffer_load_dwordx4 v225,s[36:39],s67 offen offset:1024 lds\nM v[88:91],v[204:207],a[24:27],v[88:91]\ns_waitcnt vmcnt(11) lgkmcnt(0)\n" \
"s_barrier\nds_read_b128 a[32:35],v228 offset:8192\nds_read_b128 a[36:39],v228 offset:10240\nds_read_b128 a[40:43],v228 offset:12288\nds_read_b128 a[44:47],v228 offset:14336\nds_read_b128 a[48:51],v229 offset:8192\nds_read_b128 a[52:55],v229 offset:10240\nds_read_b128 a[56:59],v229 offset:12288\n" \
"ds_read_b128 a[60:63],v229 offset:14336\nds_read_b64_tr_b16 v[192:193],v241 offset:16384\nds_read_b64_tr_b16 v[194:195],v241 offset:16640\nds_read_b64_tr_b16 v[196:197],v242 offset:16384\nds_read_b64_tr_b16 v[198:199],v242 offset:16640\nds_read_b64_tr_b16 v[200:201],v241 offset:16896\nds_read_b64_tr_b16 v[202:203],v241 offset:17152\nM v[92:95],v[204:207],a[28:31],v[92:95]\n" \
"ds_read_b64_tr_b16 v[204:205],v242 offset:16896\nds_read_b64_tr_b16 v[206:207],v242 offset:17152\nM v[32:35],v[208:211],a[16:19],v[32:35]\nbuffer_store_dwordx2 v[230:231],v165,s[52:55],s72 offen sc1\nM v[36:39],v[208:211],a[20:23],v[36:39]\nv_lshlrev_b32 v165,3,v249\nv_sub_f32 v137,v137,v164\nM v[96:99],v[208:211],a[24:27],v[96:99]\n" \
"buffer_load_dwordx4 v226,s[36:39],s67 offen offset:2048 lds\nM v[100:103],v[208:211],a[28:31],v[100:103]\nds_read_b64_tr_b16 v[208:209],v241 offset:17408\nds_read_b64_tr_b16 v[210:211],v241 offset:17664\nM v[40:43],v[212:215],a[16:19],v[40:43]\nv_rcp_f32 v157,v157\nM v[44:47],v[212:215],a[20:23],v[44:47]\nv_sub_f32 v138,v138,v164\n" \
"v_sub_f32 v139,v139,v164\nM v[104:107],v[212:215],a[24:27],v[104:107]\nv_sub_f32 v140,v140,v164\nv_sub_f32 v141,v141,v164\nM v[108:111],v[212:215],a[28:31],v[108:111]\nbuffer_load_dwordx4 v227,s[36:39],s67 offen offset:3072 lds\nds_read_b64_tr_b16 v[212:213],v242 offset:17408\nds_read_b64_tr_b16 v[214:215],v242 offset:17664\n" \
"M v[48:51],v[216:219],a[16:19],v[48:51]\nv_sub_f32 v142,v142,v164\nv_sub_f32 v143,v143,v164\nM v[52:55],v[216:219],a[20:23],v[52:55]\ns_add_u32 m0,s65,0\ns_add_u32 s67,s66,384\nM v[112:115],v[216:219],a[24:27],v[112:115]\nv_sub_f32 v144,v144,v164\n" \
"v_sub_f32 v145,v145,v164\nM v[116:119],v[216:219],a[28:31],v[116:119]\nds_read_b64_tr_b16 v[216:217],v241 offset:17920\nds_read_b64_tr_b16 v[218:219],v241 offset:18176\nM v[56:59],v[220:223],a[16:19],v[56:59]\nbuffer_load_dwordx4 v224,s[36:39],s67 offen offset:0 lds\nM v[60:63],v[220:223],a[20:23],v[60:63]\nv_sub_f32 v146,v146,v164\n" \
"v_sub_f32 v147,v147,v164\nM v[120:123],v[220:223],a[24:27],v[120:123]\nv_sub_f32 v148,v148,v164\nv_sub_f32 v149,v149,v164\nM v[124:127],v[220:223],a[28:31],v[124:127]\nds_read_b64_tr_b16 v[220:221],v242 offset:17920\nds_read_b64_tr_b16 v[222:223],v242 offset:18176\ns_waitcnt lgkmcnt(6)\n" \
"M v[0:3],v[192:195],a[32:35],v[0:3]\nv_sub_f32 v150,v150,v164\nv_sub_f32 v151,v151,v164\nM v[4:7],v[192:195],a[36:39],v[4:7]\nbuffer_load_dwordx4 v225,s[36:39],s67 offen offset:1024 lds\nM v[64:67],v[192:195],a[40:43],v[64:67]\nv_cvt_pk_bf16_f32 v136,0,v136\nv_cvt_pk_bf16_f32 v137,0,v137\n" \
"M v[68:71],v[192:195],a[44:47],v[68:71]\nds_read_b64_tr_b16 v[192:193],v241 offset:24576\nds_read_b64_tr_b16 v[194:195],v241 offset:24832\nM v[8:11],v[196:199],a[32:35],v[8:11]\nv_cvt_pk_bf16_f32 v138,0,v138\nv_cvt_pk_bf16_f32 v139,0,v139\nM v[12:15],v[196:199],a[36:39],v[12:15]\nv_cvt_pk_bf16_f32 v140,0,v140\n" \
"v_cvt_pk_bf16_f32 v141,0,v141\nM v[72:75],v[196:199],a[40:43],v[72:75]\nbuffer_load_dwordx4 v226,s[36:39],s67 offen offset:2048 lds\nM v[76:79],v[196:199],a[44:47],v[76:79]\nds_read_b64_tr_b16 v[196:197],v242 offset:24576\nds_read_b64_tr_b16 v[198:199],v242 offset:24832\nM v[16:19],v[200:203],a[32:35],v[16:19]\nv_cvt_pk_bf16_f32 v142,0,v142\n" \
"v_cvt_pk_bf16_f32 v143,0,v143\nM v[20:23],v[200:203],a[36:39],v[20:23]\nv_cvt_pk_bf16_f32 v144,0,v144\nv_cvt_pk_bf16_f32 v145,0,v145\nM v[80:83],v[200:203],a[40:43],v[80:83]\nv_cvt_pk_bf16_f32 v146,0,v146\nv_cvt_pk_bf16_f32 v147,0,v147\nM v[84:87],v[200:203],a[44:47],v[84:87]\n" \
"buffer_load_dwordx4 v227,s[36:39],s67 offen offset:3072 lds\nds_read_b64_tr_b16 v[200:201],v241 offset:25088\nds_read_b64_tr_b16 v[202:203],v241 offset:25344\nM v[24:27],v[204:207],a[32:35],v[24:27]\nv_cvt_pk_bf16_f32 v148,0,v148\nv_cvt_pk_bf16_f32 v149,0,v149\nM v[28:31],v[204:207],a[36:39],v[28:31]\ns_add_u32 m0,s65,4096\n" \
"s_add_u32 s67,s66,33152\nM v[88:91],v[204:207],a[40:43],v[88:91]\nv_cvt_pk_bf16_f32 v150,0,v150\nv_cvt_pk_bf16_f32 v151,0,v151\nM v[92:95],v[204:207],a[44:47],v[92:95]\nds_read_b64_tr_b16 v[204:205],v242 offset:25088\nds_read_b64_tr_b16 v[206:207],v242 offset:25344\nM v[32:35],v[208:211],a[32:35],v[32:35]\n" \
"buffer_load_dwordx4 v224,s[36:39],s67 offen offset:0 lds\nM v[36:39],v[208:211],a[36:39],v[36:39]\nv_mul_f32 v136,v136,v157\nv_mul_f32 v137,v137,v157\nM v[96:99],v[208:211],a[40:43],v[96:99]\nv_mul_f32 v138,v138,v157\nv_mul_f32 v139,v139,v157\nM v[100:103],v[208:211],a[44:47],v[100:103]\n" \
"ds_read_b64_tr_b16 v[208:209],v241 offset:25600\nds_read_b64_tr_b16 v[210:211],v241 offset:25856\ns_waitcnt lgkmcnt(6)\nM v[40:43],v[212:215],a[32:35],v[40:43]\nv_mul_f32 v140,v140,v157\nv_mul_f32 v141,v141,v157\nM v[44:47],v[212:215],a[36:39],v[44:47]\nbuffer_load_dwordx4 v225,s[36:39],s67 offen offset:1024 lds\n" \
"M v[104:107],v[212:215],a[40:43],v[104:107]\nv_mul_f32 v142,v142,v157\nv_mul_f32 v143,v143,v157\nM v[108:111],v[212:215],a[44:47],v[108:111]\nds_read_b64_tr_b16 v[212:213],v242 offset:25600\nds_read_b64_tr_b16 v[214:215],v242 offset:25856\nM v[48:51],v[216:219],a[32:35],v[48:51]\nv_mul_f32 v144,v144,v157\n" \
"v_mul_f32 v145,v145,v157\nM v[52:55],v[216:219],a[36:39],v[52:55]\nv_mul_f32 v146,v146,v157\nv_mul_f32 v147,v147,v157\nM v[112:115],v[216:219],a[40:43],v[112:115]\nbuffer_load_dwordx4 v226,s[36:39],s67 offen offset:2048 lds\nM v[116:119],v[216:219],a[44:47],v[116:119]\nds_read_b64_tr_b16 v[216:217],v241 offset:26112\n" \
"ds_read_b64_tr_b16 v[218:219],v241 offset:26368\nM v[56:59],v[220:223],a[32:35],v[56:59]\nv_mul_f32 v148,v148,v157\nv_mul_f32 v149,v149,v157\nM v[60:63],v[220:223],a[36:39],v[60:63]\nv_mul_f32 v150,v150,v157\nv_mul_f32 v151,v151,v157\nM v[120:123],v[220:223],a[40:43],v[120:123]\n" \
"v_and_b32 v169,3,v249\nv_mul_u32_u24 v169,12288,v169\nM v[124:127],v[220:223],a[44:47],v[124:127]\nbuffer_load_dwordx4 v227,s[36:39],s67 offen offset:3072 lds\nds_read_b64_tr_b16 v[220:221],v242 offset:26112\nds_read_b64_tr_b16 v[222:223],v242 offset:26368\nM v[0:3],v[192:195],a[48:51],v[0:3]\nv_and_b32 v170,28,v249\n" \
"v_add_u32 v169,v169,v170\nM v[4:7],v[192:195],a[52:55],v[4:7]\ns_add_u32 m0,s64,147456\ns_add_u32 s69,s68,1572864\nM v[64:67],v[192:195],a[56:59],v[64:67]\ns_and_b32 s78,s62,1\ns_lshl2_add_u32 s78,s78,s7\nM v[68:71],v[192:195],a[60:63],v[68:71]\n" \
"s_lshl_b32 s78,s78,13\nv_lshlrev_b32 v252,7,v249\nM v[8:11],v[196:199],a[48:51],v[8:11]\nbuffer_load_dwordx4 v243,s[40:43],s69 offen offset:0 lds\nM v[12:15],v[196:199],a[52:55],v[12:15]\ns_add_u32 s78,s78,.Lattn-.Lpc0\nv_lshrrev_b32 v251,3,v249\nM v[72:75],v[196:199],a[56:59],v[72:75]\n" \
"s_cmp_eq_u32 s6,0\ns_cselect_b32 s72,s75,0x8000000\nM v[76:79],v[196:199],a[60:63],v[76:79]\ns_add_u32 s72,s72,94208\ns_waitcnt lgkmcnt(2)\nM v[16:19],v[200:203],a[48:51],v[16:19]\nv_lshlrev_b32 v170,3,v170\nv_cvt_pk_bf16_f32 v144,0,v144\n" \
"M v[20:23],v[200:203],a[52:55],v[20:23]\nbuffer_load_dwordx4 v244,s[40:43],s69 offen offset:1024 lds\nM v[80:83],v[200:203],a[56:59],v[80:83]\nv_cvt_pk_bf16_f32 v145,0,v145\nv_cvt_pk_bf16_f32 v146,0,v146\nM v[84:87],v[200:203],a[60:63],v[84:87]\nv_cvt_pk_bf16_f32 v147,0,v147\nv_cvt_pk_bf16_f32 v136,0,v136\n" \
"M v[24:27],v[204:207],a[48:51],v[24:27]\nv_cvt_pk_bf16_f32 v137,0,v137\nv_cvt_pk_bf16_f32 v138,0,v138\nM v[28:31],v[204:207],a[52:55],v[28:31]\nv_cvt_pk_bf16_f32 v139,0,v139\nv_cvt_pk_bf16_f32 v140,0,v140\nM v[88:91],v[204:207],a[56:59],v[88:91]\nbuffer_load_dwordx4 v245,s[40:43],s69 offen offset:2048 lds\n" \
"s_waitcnt vmcnt(11) lgkmcnt(0)\ns_barrier\nds_read_b128 a[64:67],v228 offset:16384\nds_read_b128 a[68:71],v228 offset:18432\nds_read_b128 a[72:75],v228 offset:20480\nds_read_b128 a[76:79],v228 offset:22528\nds_read_b128 a[80:83],v229 offset:16384\nds_read_b128 a[84:87],v229 offset:18432\n" \
"ds_read_b128 a[88:91],v229 offset:20480\nds_read_b128 a[92:95],v229 offset:22528\nM v[92:95],v[204:207],a[60:63],v[92:95]\nds_read_b64_tr_b16 v[192:193],v241 offset:32768\nds_read_b64_tr_b16 v[194:195],v241 offset:33024\nM v[32:35],v[208:211],a[48:51],v[32:35]\nds_read_b64_tr_b16 v[196:197],v242 offset:32768\nds_read_b64_tr_b16 v[198:199],v242 offset:33024\n" \
"M v[36:39],v[208:211],a[52:55],v[36:39]\nds_read_b64_tr_b16 v[200:201],v241 offset:33280\nds_read_b64_tr_b16 v[202:203],v241 offset:33536\nM v[96:99],v[208:211],a[56:59],v[96:99]\nds_read_b64_tr_b16 v[204:205],v242 offset:33280\nds_read_b64_tr_b16 v[206:207],v242 offset:33536\nM v[100:103],v[208:211],a[60:63],v[100:103]\nbuffer_load_dwordx4 v246,s[40:43],s69 offen offset:3072 lds\n" \
"M v[40:43],v[212:215],a[48:51],v[40:43]\nds_read_b64_tr_b16 v[208:209],v241 offset:33792\nds_read_b64_tr_b16 v[210:211],v241 offset:34048\nM v[44:47],v[212:215],a[52:55],v[44:47]\ns_add_u32 m0,s65,8192\ns_add_u32 s67,s66,512\nM v[104:107],v[212:215],a[56:59],v[104:107]\nv_cvt_pk_bf16_f32 v141,0,v141\n" \
"v_cvt_pk_bf16_f32 v142,0,v142\nM v[108:111],v[212:215],a[60:63],v[108:111]\nds_read_b64_tr_b16 v[212:213],v242 offset:33792\nds_read_b64_tr_b16 v[214:215],v242 offset:34048\nM v[48:51],v[216:219],a[48:51],v[48:51]\nbuffer_load_dwordx4 v224,s[36:39],s67 offen offset:0 lds\nM v[52:55],v[216:219],a[52:55],v[52:55]\nv_cvt_pk_bf16_f32 v143,0,v143\n" \
"v_cvt_pk_bf16_f32 v148,0,v148\nM v[112:115],v[216:219],a[56:59],v[112:115]\nv_cvt_pk_bf16_f32 v149,0,v149\nv_bfe_u32 v250,v249,3,2\nM v[116:119],v[216:219],a[60:63],v[116:119]\nds_read_b64_tr_b16 v[216:217],v241 offset:34304\nds_read_b64_tr_b16 v[218:219],v241 offset:34560\nM v[56:59],v[220:223],a[48:51],v[56:59]\n" \
"v_cvt_pk_bf16_f32 v150,0,v150\nv_cvt_pk_bf16_f32 v151,0,v151\nM v[60:63],v[220:223],a[52:55],v[60:63]\nbuffer_load_dwordx4 v225,s[36:39],s67 offen offset:1024 lds\nM v[120:123],v[220:223],a[56:59],v[120:123]\ns_mov_b32 s98,0x4000\ns_add_u32 s10,s61,1\nM v[124:127],v[220:223],a[60:63],v[124:127]\n" \
"ds_read_b64_tr_b16 v[220:221],v242 offset:34304\ns_waitcnt lgkmcnt(5)\nM v[0:3],v[192:195],a[64:67],v[0:3]\nds_read_b64_tr_b16 v[222:223],v242 offset:34560\ns_add_u32 s11,s61,2\nM v[4:7],v[192:195],a[68:71],v[4:7]\ns_add_u32 s14,s61,3\ns_and_b32 s10,s10,3\n" \
"M v[64:67],v[192:195],a[72:75],v[64:67]\nbuffer_load_dwordx4 v226,s[36:39],s67 offen offset:2048 lds\nM v[68:71],v[192:195],a[76:79],v[68:71]\nds_read_b64_tr_b16 v[192:193],v241 offset:40960\nds_read_b64_tr_b16 v[194:195],v241 offset:41216\nM v[8:11],v[196:199],a[64:67],v[8:11]\ns_and_b32 s11,s11,3\ns_and_b32 s14,s14,3\n" \
"M v[12:15],v[196:199],a[68:71],v[12:15]\ns_mov_b32 s47,0x20000\ns_mov_b32 s51,0x20000\nM v[72:75],v[196:199],a[72:75],v[72:75]\ns_mov_b32 s94,0xc000\ns_lshl_b32 s10,s10,13\nM v[76:79],v[196:199],a[76:79],v[76:79]\nbuffer_load_dwordx4 v227,s[36:39],s67 offen offset:3072 lds\n" \
"M v[16:19],v[200:203],a[64:67],v[16:19]\nds_read_b64_tr_b16 v[196:197],v242 offset:40960\nds_read_b64_tr_b16 v[198:199],v242 offset:41216\nM v[20:23],v[200:203],a[68:71],v[20:23]\ns_add_u32 m0,s65,12288\ns_add_u32 s67,s66,33280\nM v[80:83],v[200:203],a[72:75],v[80:83]\ns_lshl_b32 s11,s11,13\n" \
"s_lshl_b32 s14,s14,13\nM v[84:87],v[200:203],a[76:79],v[84:87]\nds_read_b64_tr_b16 v[200:201],v241 offset:41472\nds_read_b64_tr_b16 v[202:203],v241 offset:41728\nM v[24:27],v[204:207],a[64:67],v[24:27]\nbuffer_load_dwordx4 v224,s[36:39],s67 offen offset:0 lds\nM v[28:31],v[204:207],a[68:71],v[28:31]\ns_mov_b32 s82,0xf0000\n" \
"s_mov_b32 s83,0xfff00ff\nM v[88:91],v[204:207],a[72:75],v[88:91]\ns_mov_b32 s84,0x1f0001\ns_mov_b32 s85,0x1fff01ff\nM v[92:95],v[204:207],a[76:79],v[92:95]\nds_read_b64_tr_b16 v[204:205],v242 offset:41472\nds_read_b64_tr_b16 v[206:207],v242 offset:41728\nM v[32:35],v[208:211],a[64:67],v[32:35]\n" \
"s_mov_b32 s86,0x3f0003\ns_mov_b32 s87,0x3fff03ff\nM v[36:39],v[208:211],a[68:71],v[36:39]\nbuffer_load_dwordx4 v225,s[36:39],s67 offen offset:1024 lds\nM v[96:99],v[208:211],a[72:75],v[96:99]\ns_mov_b32 s88,0x7f0007\ns_mov_b32 s89,0x7fff07ff\nM v[100:103],v[208:211],a[76:79],v[100:103]\n" \
"ds_read_b64_tr_b16 v[208:209],v241 offset:41984\ns_waitcnt lgkmcnt(5)\nM v[40:43],v[212:215],a[64:67],v[40:43]\nds_read_b64_tr_b16 v[210:211],v241 offset:42240\ns_mov_b64 s[90:91],-1\nM v[44:47],v[212:215],a[68:71],v[44:47]\ns_mov_b32 s28,0x5f800000\ns_mov_b32 s99,0\n" \
"M v[104:107],v[212:215],a[72:75],v[104:107]\nbuffer_load_dwordx4 v226,s[36:39],s67 offen offset:2048 lds\nM v[108:111],v[212:215],a[76:79],v[108:111]\nds_read_b64_tr_b16 v[212:213],v242 offset:41984\nds_read_b64_tr_b16 v[214:215],v242 offset:42240\nM v[48:51],v[216:219],a[64:67],v[48:51]\nM v[52:55],v[216:219],a[68:71],v[52:55]\nM v[112:115],v[216:219],a[72:75],v[112:115]\n" \
"M v[116:119],v[216:219],a[76:79],v[116:119]\nbuffer_load_dwordx4 v227,s[36:39],s67 offen offset:3072 lds\nM v[56:59],v[220:223],a[64:67],v[56:59]\nds_read_b64_tr_b16 v[216:217],v241 offset:42496\nds_read_b64_tr_b16 v[218:219],v241 offset:42752\nM v[60:63],v[220:223],a[68:71],v[60:63]\ns_add_u32 m0,s64,98304\ns_add_u32 s69,s68,2097152\n" \
"M v[120:123],v[220:223],a[72:75],v[120:123]\nM v[124:127],v[220:223],a[76:79],v[124:127]\nds_read_b64_tr_b16 v[220:221],v242 offset:42496\nds_read_b64_tr_b16 v[222:223],v242 offset:42752\nM v[0:3],v[192:195],a[80:83],v[0:3]\nbuffer_load_dwordx4 v243,s[40:43],s69 offen offset:0 lds\nM v[4:7],v[192:195],a[84:87],v[4:7]\nM v[64:67],v[192:195],a[88:91],v[64:67]\n" \
"M v[68:71],v[192:195],a[92:95],v[68:71]\nM v[8:11],v[196:199],a[80:83],v[8:11]\nM v[12:15],v[196:199],a[84:87],v[12:15]\nbuffer_load_dwordx4 v244,s[40:43],s69 offen offset:1024 lds\nM v[72:75],v[196:199],a[88:91],v[72:75]\nM v[76:79],v[196:199],a[92:95],v[76:79]\ns_waitcnt lgkmcnt(4)\nM v[16:19],v[200:203],a[80:83],v[16:19]\n" \
"M v[20:23],v[200:203],a[84:87],v[20:23]\nM v[80:83],v[200:203],a[88:91],v[80:83]\nbuffer_load_dwordx4 v245,s[40:43],s69 offen offset:2048 lds\nM v[84:87],v[200:203],a[92:95],v[84:87]\nM v[24:27],v[204:207],a[80:83],v[24:27]\nM v[28:31],v[204:207],a[84:87],v[28:31]\nM v[88:91],v[204:207],a[88:91],v[88:91]\ns_waitcnt vmcnt(11) lgkmcnt(0)\n" \
"s_barrier\nds_read_b128 a[96:99],v228 offset:0\nds_read_b128 a[100:103],v228 offset:2048\nds_read_b128 a[104:107],v228 offset:4096\nds_read_b128 a[108:111],v228 offset:6144\nds_read_b128 a[112:115],v229 offset:0\nds_read_b128 a[116:119],v229 offset:2048\nds_read_b128 a[120:123],v229 offset:4096\n" \
"ds_read_b128 a[124:127],v229 offset:6144\nM v[92:95],v[204:207],a[92:95],v[92:95]\nbuffer_load_dwordx4 v246,s[40:43],s69 offen offset:3072 lds\nM v[32:35],v[208:211],a[80:83],v[32:35]\nds_read_b64_tr_b16 v[192:193],v241 offset:49152\nds_read_b64_tr_b16 v[194:195],v241 offset:49408\nM v[36:39],v[208:211],a[84:87],v[36:39]\ns_add_u32 m0,s65,16384\n" \
"s_add_u32 s67,s66,640\nM v[96:99],v[208:211],a[88:91],v[96:99]\nds_read_b64_tr_b16 v[196:197],v242 offset:49152\nds_read_b64_tr_b16 v[198:199],v242 offset:49408\nM v[100:103],v[208:211],a[92:95],v[100:103]\nds_read_b64_tr_b16 v[200:201],v241 offset:49664\nds_read_b64_tr_b16 v[202:203],v241 offset:49920\nM v[40:43],v[212:215],a[80:83],v[40:43]\n" \
"buffer_load_dwordx4 v224,s[36:39],s67 offen offset:0 lds\nM v[44:47],v[212:215],a[84:87],v[44:47]\nds_read_b64_tr_b16 v[204:205],v242 offset:49664\nds_read_b64_tr_b16 v[206:207],v242 offset:49920\nM v[104:107],v[212:215],a[88:91],v[104:107]\nds_read_b64_tr_b16 v[208:209],v241 offset:50176\nds_read_b64_tr_b16 v[210:211],v241 offset:50432\nM v[108:111],v[212:215],a[92:95],v[108:111]\n" \
"ds_read_b64_tr_b16 v[212:213],v242 offset:50176\nds_read_b64_tr_b16 v[214:215],v242 offset:50432\nM v[48:51],v[216:219],a[80:83],v[48:51]\nM v[52:55],v[216:219],a[84:87],v[52:55]\nbuffer_load_dwordx4 v225,s[36:39],s67 offen offset:1024 lds\nM v[112:115],v[216:219],a[88:91],v[112:115]\nM v[116:119],v[216:219],a[92:95],v[116:119]\nds_read_b64_tr_b16 v[216:217],v241 offset:50688\n" \
"ds_read_b64_tr_b16 v[218:219],v241 offset:50944\nM v[56:59],v[220:223],a[80:83],v[56:59]\nM v[60:63],v[220:223],a[84:87],v[60:63]\nM v[120:123],v[220:223],a[88:91],v[120:123]\nbuffer_load_dwordx4 v226,s[36:39],s67 offen offset:2048 lds\nM v[124:127],v[220:223],a[92:95],v[124:127]\nds_read_b64_tr_b16 v[220:221],v242 offset:50688\ns_waitcnt lgkmcnt(10)\n" \
"M v[0:3],v[192:195],a[96:99],v[0:3]\nds_read_b64_tr_b16 v[222:223],v242 offset:50944\nM v[4:7],v[192:195],a[100:103],v[4:7]\nM v[64:67],v[192:195],a[104:107],v[64:67]\nM v[68:71],v[192:195],a[108:111],v[68:71]\nbuffer_load_dwordx4 v227,s[36:39],s67 offen offset:3072 lds\nM v[8:11],v[196:199],a[96:99],v[8:11]\nds_read_b64_tr_b16 v[192:193],v241 offset:57344\n" \
"ds_read_b64_tr_b16 v[194:195],v241 offset:57600\nM v[12:15],v[196:199],a[100:103],v[12:15]\ns_add_u32 m0,s65,20480\ns_add_u32 s67,s66,33408\nM v[72:75],v[196:199],a[104:107],v[72:75]\nM v[76:79],v[196:199],a[108:111],v[76:79]\nds_read_b64_tr_b16 v[196:197],v242 offset:57344\ns_waitcnt lgkmcnt(7)\n" \
"M v[16:19],v[200:203],a[96:99],v[16:19]\nbuffer_load_dwordx4 v224,s[36:39],s67 offen offset:0 lds\nM v[20:23],v[200:203],a[100:103],v[20:23]\nds_read_b64_tr_b16 v[198:199],v242 offset:57600\nM v[80:83],v[200:203],a[104:107],v[80:83]\nM v[84:87],v[200:203],a[108:111],v[84:87]\nds_read_b64_tr_b16 v[200:201],v241 offset:57856\nds_read_b64_tr_b16 v[202:203],v241 offset:58112\n" \
"M v[24:27],v[204:207],a[96:99],v[24:27]\nM v[28:31],v[204:207],a[100:103],v[28:31]\nbuffer_load_dwordx4 v225,s[36:39],s67 offen offset:1024 lds\nM v[88:91],v[204:207],a[104:107],v[88:91]\nM v[92:95],v[204:207],a[108:111],v[92:95]\nds_read_b64_tr_b16 v[204:205],v242 offset:57856\nds_read_b64_tr_b16 v[206:207],v242 offset:58112\nM v[32:35],v[208:211],a[96:99],v[32:35]\n" \
"M v[36:39],v[208:211],a[100:103],v[36:39]\nM v[96:99],v[208:211],a[104:107],v[96:99]\nbuffer_load_dwordx4 v226,s[36:39],s67 offen offset:2048 lds\nM v[100:103],v[208:211],a[108:111],v[100:103]\nds_read_b64_tr_b16 v[208:209],v241 offset:58368\nds_read_b64_tr_b16 v[210:211],v241 offset:58624\nM v[40:43],v[212:215],a[96:99],v[40:43]\nM v[44:47],v[212:215],a[100:103],v[44:47]\n" \
"M v[104:107],v[212:215],a[104:107],v[104:107]\nM v[108:111],v[212:215],a[108:111],v[108:111]\nbuffer_load_dwordx4 v227,s[36:39],s67 offen offset:3072 lds\ns_waitcnt lgkmcnt(6)\nM v[48:51],v[216:219],a[96:99],v[48:51]\nds_read_b64_tr_b16 v[212:213],v242 offset:58368\nds_read_b64_tr_b16 v[214:215],v242 offset:58624\nM v[52:55],v[216:219],a[100:103],v[52:55]\n" \
"s_add_u32 m0,s64,114688\ns_add_u32 s69,s68,2621440\nM v[112:115],v[216:219],a[104:107],v[112:115]\nM v[116:119],v[216:219],a[108:111],v[116:119]\nds_read_b64_tr_b16 v[216:217],v241 offset:58880\nds_read_b64_tr_b16 v[218:219],v241 offset:59136\nM v[56:59],v[220:223],a[96:99],v[56:59]\nbuffer_load_dwordx4 v243,s[40:43],s69 offen offset:0 lds\n" \
"M v[60:63],v[220:223],a[100:103],v[60:63]\nM v[120:123],v[220:223],a[104:107],v[120:123]\nM v[124:127],v[220:223],a[108:111],v[124:127]\nds_read_b64_tr_b16 v[220:221],v242 offset:58880\nds_read_b64_tr_b16 v[222:223],v242 offset:59136\nM v[0:3],v[192:195],a[112:115],v[0:3]\nbuffer_load_dwordx2 v[166:167],v165,s[52:55],s80 offen sc1\nM v[4:7],v[192:195],a[116:119],v[4:7]\n" \
"buffer_load_dwordx4 v244,s[40:43],s69 offen offset:1024 lds\nM v[64:67],v[192:195],a[120:123],v[64:67]\nM v[68:71],v[192:195],a[124:127],v[68:71]\nM v[8:11],v[196:199],a[112:115],v[8:11]\nM v[12:15],v[196:199],a[116:119],v[12:15]\nM v[72:75],v[196:199],a[120:123],v[72:75]\nbuffer_load_dwordx4 v245,s[40:43],s69 offen offset:2048 lds\nM v[76:79],v[196:199],a[124:127],v[76:79]\n" \
"s_waitcnt lgkmcnt(4)\nM v[16:19],v[200:203],a[112:115],v[16:19]\nM v[20:23],v[200:203],a[116:119],v[20:23]\nM v[80:83],v[200:203],a[120:123],v[80:83]\nM v[84:87],v[200:203],a[124:127],v[84:87]\nbuffer_load_dwordx4 v246,s[40:43],s69 offen offset:3072 lds\nM v[24:27],v[204:207],a[112:115],v[24:27]\nM v[28:31],v[204:207],a[116:119],v[28:31]\n" \
"s_add_u32 m0,s65,0\ns_add_u32 s67,s66,768\nM v[88:91],v[204:207],a[120:123],v[88:91]\ns_waitcnt vmcnt(13) lgkmcnt(0)\ns_barrier\nM v[92:95],v[204:207],a[124:127],v[92:95]\nds_read_b128 a[128:131],v228 offset:8192\nds_read_b64_tr_b16 v[192:193],v241 offset:0\n" \
"M v[32:35],v[208:211],a[112:115],v[32:35]\nbuffer_load_dwordx4 v224,s[36:39],s67 offen offset:0 lds\nM v[36:39],v[208:211],a[116:119],v[36:39]\nds_read_b64_tr_b16 v[194:195],v241 offset:256\nds_read_b128 a[132:135],v228 offset:10240\nM v[96:99],v[208:211],a[120:123],v[96:99]\nds_read_b128 a[136:139],v228 offset:12288\nds_read_b128 a[140:143],v228 offset:14336\n" \
"M v[100:103],v[208:211],a[124:127],v[100:103]\nds_read_b64_tr_b16 v[196:197],v242 offset:0\nds_read_b64_tr_b16 v[198:199],v242 offset:256\nM v[40:43],v[212:215],a[112:115],v[40:43]\nds_read_b64_tr_b16 v[200:201],v241 offset:512\nds_read_b64_tr_b16 v[202:203],v241 offset:768\nM v[44:47],v[212:215],a[116:119],v[44:47]\nbuffer_load_dwordx4 v225,s[36:39],s67 offen offset:1024 lds\n" \
"M v[104:107],v[212:215],a[120:123],v[104:107]\nds_read_b64_tr_b16 v[204:205],v242 offset:512\nds_read_b64_tr_b16 v[206:207],v242 offset:768\nM v[108:111],v[212:215],a[124:127],v[108:111]\nds_read_b64_tr_b16 v[208:209],v241 offset:1024\nds_read_b64_tr_b16 v[210:211],v241 offset:1280\nM v[48:51],v[216:219],a[112:115],v[48:51]\nds_read_b64_tr_b16 v[212:213],v242 offset:1024\n" \
"ds_read_b64_tr_b16 v[214:215],v242 offset:1280\nM v[52:55],v[216:219],a[116:119],v[52:55]\nds_read_b128 a[144:147],v229 offset:8192\nds_read_b128 a[148:151],v229 offset:10240\nM v[112:115],v[216:219],a[120:123],v[112:115]\nbuffer_load_dwordx4 v226,s[36:39],s67 offen offset:2048 lds\nM v[116:119],v[216:219],a[124:127],v[116:119]\nds_read_b64_tr_b16 v[216:217],v241 offset:1536\n" \
"ds_read_b64_tr_b16 v[218:219],v241 offset:1792\nM v[56:59],v[220:223],a[112:115],v[56:59]\nds_read_b128 a[152:155],v229 offset:12288\nds_read_b128 a[156:159],v229 offset:14336\nM v[60:63],v[220:223],a[116:119],v[60:63]\nM v[120:123],v[220:223],a[120:123],v[120:123]\nM v[124:127],v[220:223],a[124:127],v[124:127]\nbuffer_load_dwordx4 v227,s[36:39],s67 offen offset:3072 lds\n" \
"s_waitcnt lgkmcnt(12)\nM v[0:3],v[192:195],a[128:131],v[0:3]\nds_read_b64_tr_b16 v[220:221],v242 offset:1536\nds_read_b64_tr_b16 v[222:223],v242 offset:1792\nM v[4:7],v[192:195],a[132:135],v[4:7]\ns_add_u32 m0,s65,4096\ns_add_u32 s67,s66,33536\nM v[64:67],v[192:195],a[136:139],v[64:67]\n" \
"M v[68:71],v[192:195],a[140:143],v[68:71]\nds_read_b64_tr_b16 v[192:193],v241 offset:8192\nds_read_b64_tr_b16 v[194:195],v241 offset:8448\nM v[8:11],v[196:199],a[128:131],v[8:11]\nbuffer_load_dwordx4 v224,s[36:39],s67 offen offset:0 lds\nM v[12:15],v[196:199],a[132:135],v[12:15]\nM v[72:75],v[196:199],a[136:139],v[72:75]\nM v[76:79],v[196:199],a[140:143],v[76:79]\n" \
"ds_read_b64_tr_b16 v[196:197],v242 offset:8192\nds_read_b64_tr_b16 v[198:199],v242 offset:8448\nM v[16:19],v[200:203],a[128:131],v[16:19]\nM v[20:23],v[200:203],a[132:135],v[20:23]\nbuffer_load_dwordx4 v225,s[36:39],s67 offen offset:1024 lds\nM v[80:83],v[200:203],a[136:139],v[80:83]\nM v[84:87],v[200:203],a[140:143],v[84:87]\nds_read_b64_tr_b16 v[200:201],v241 offset:8704\n" \
"s_waitcnt lgkmcnt(7)\nM v[24:27],v[204:207],a[128:131],v[24:27]\nds_read_b64_tr_b16 v[202:203],v241 offset:8960\nM v[28:31],v[204:207],a[132:135],v[28:31]\nM v[88:91],v[204:207],a[136:139],v[88:91]\nbuffer_load_dwordx4 v226,s[36:39],s67 offen offset:2048 lds\nM v[92:95],v[204:207],a[140:143],v[92:95]\nds_read_b64_tr_b16 v[204:205],v242 offset:8704\n" \
"ds_read_b64_tr_b16 v[206:207],v242 offset:8960\nM v[32:35],v[208:211],a[128:131],v[32:35]\nM v[36:39],v[208:211],a[132:135],v[36:39]\nM v[96:99],v[208:211],a[136:139],v[96:99]\nM v[100:103],v[208:211],a[140:143],v[100:103]\nbuffer_load_dwordx4 v227,s[36:39],s67 offen offset:3072 lds\nM v[40:43],v[212:215],a[128:131],v[40:43]\nds_read_b64_tr_b16 v[208:209],v241 offset:9216\n" \
"ds_read_b64_tr_b16 v[210:211],v241 offset:9472\nM v[44:47],v[212:215],a[132:135],v[44:47]\ns_add_u32 m0,s64,131072\ns_add_u32 s69,s68,3145728\nM v[104:107],v[212:215],a[136:139],v[104:107]\nM v[108:111],v[212:215],a[140:143],v[108:111]\nds_read_b64_tr_b16 v[212:213],v242 offset:9216\nds_read_b64_tr_b16 v[214:215],v242 offset:9472\n" \
"M v[48:51],v[216:219],a[128:131],v[48:51]\nbuffer_load_dwordx4 v243,s[40:43],s69 offen offset:0 lds\nM v[52:55],v[216:219],a[132:135],v[52:55]\nM v[112:115],v[216:219],a[136:139],v[112:115]\nM v[116:119],v[216:219],a[140:143],v[116:119]\nds_read_b64_tr_b16 v[216:217],v241 offset:9728\ns_waitcnt lgkmcnt(7)\nM v[56:59],v[220:223],a[128:131],v[56:59]\n" \
"ds_read_b64_tr_b16 v[218:219],v241 offset:9984\nM v[60:63],v[220:223],a[132:135],v[60:63]\nbuffer_load_dwordx4 v244,s[40:43],s69 offen offset:1024 lds\nM v[120:123],v[220:223],a[136:139],v[120:123]\nM v[124:127],v[220:223],a[140:143],v[124:127]\nds_read_b64_tr_b16 v[220:221],v242 offset:9728\nds_read_b64_tr_b16 v[222:223],v242 offset:9984\nM v[0:3],v[192:195],a[144:147],v[0:3]\n" \
"M v[4:7],v[192:195],a[148:151],v[4:7]\nM v[64:67],v[192:195],a[152:155],v[64:67]\nbuffer_load_dwordx4 v245,s[40:43],s69 offen offset:2048 lds\nM v[68:71],v[192:195],a[156:159],v[68:71]\nM v[8:11],v[196:199],a[144:147],v[8:11]\ns_waitcnt vmcnt(14)\nv_xor_b32 v166,v166,v230\nM v[12:15],v[196:199],a[148:151],v[12:15]\n" \
"v_xor_b32 v167,v167,v231\nv_or_b32 v166,v166,v167\nM v[72:75],v[196:199],a[152:155],v[72:75]\nv_cmp_ne_u32 vcc,0,v166\nM v[76:79],v[196:199],a[156:159],v[76:79]\nbuffer_load_dwordx4 v246,s[40:43],s69 offen offset:3072 lds\nM v[16:19],v[200:203],a[144:147],v[16:19]\ns_cbranch_vccnz .Lhs\n" \
".Lhb:\nv_bfe_u32 v230,v249,2,3\nM v[20:23],v[200:203],a[148:151],v[20:23]\ns_add_u32 m0,s65,8192\ns_add_u32 s67,s66,896\nM v[80:83],v[200:203],a[152:155],v[80:83]\nv_and_b32 v231,0x70,v243\nv_lshl_or_b32 v230,v230,10,v231\n" \
"M v[84:87],v[200:203],a[156:159],v[84:87]\nv_and_b32 v231,16,v249\ns_waitcnt lgkmcnt(0)\nM v[24:27],v[204:207],a[144:147],v[24:27]\nbuffer_load_dwordx4 v224,s[36:39],s67 offen offset:0 lds\nM v[28:31],v[204:207],a[148:151],v[28:31]\nv_lshlrev_b32 v231,1,v231\nv_xor_b32 v230,v230,v231\n" \
"M v[88:91],v[204:207],a[152:155],v[88:91]\ns_waitcnt vmcnt(13)\ns_barrier\nM v[92:95],v[204:207],a[156:159],v[92:95]\nbuffer_load_dwordx4 v[152:155],v240,s[52:55],s71 offen sc1\nM v[32:35],v[208:211],a[144:147],v[32:35]\ns_add_u32 s71,s70,798720\nds_read_b128 a[160:163],v228 offset:16384\n" \
"M v[36:39],v[208:211],a[148:151],v[36:39]\nbuffer_load_dwordx4 v[156:159],v240,s[52:55],s71 offen sc1\nM v[96:99],v[208:211],a[152:155],v[96:99]\nbuffer_load_dwordx4 v225,s[36:39],s67 offen offset:1024 lds\nM v[100:103],v[208:211],a[156:159],v[100:103]\ns_add_u32 s71,s70,811008\nds_read_b64_tr_b16 v[192:193],v241 offset:16384\nM v[40:43],v[212:215],a[144:147],v[40:43]\n" \
"buffer_load_dwordx4 v[160:163],v240,s[52:55],s71 offen sc1\nM v[44:47],v[212:215],a[148:151],v[44:47]\nds_read_b64_tr_b16 v[194:195],v241 offset:16640\ns_add_u32 s71,s70,823296\nM v[104:107],v[212:215],a[152:155],v[104:107]\nbuffer_load_dwordx4 v226,s[36:39],s67 offen offset:2048 lds\nds_read_b128 a[164:167],v228 offset:18432\nbuffer_load_dwordx4 v[164:167],v240,s[52:55],s71 offen sc1\n" \
"M v[108:111],v[212:215],a[156:159],v[108:111]\nds_read_b128 a[168:171],v228 offset:20480\nds_read_b128 a[172:175],v228 offset:22528\nM v[48:51],v[216:219],a[144:147],v[48:51]\nds_read_b64_tr_b16 v[196:197],v242 offset:16384\nds_read_b64_tr_b16 v[198:199],v242 offset:16640\nM v[52:55],v[216:219],a[148:151],v[52:55]\nds_read_b64_tr_b16 v[200:201],v241 offset:16896\n" \
"ds_read_b64_tr_b16 v[202:203],v241 offset:17152\nM v[112:115],v[216:219],a[152:155],v[112:115]\nds_read_b64_tr_b16 v[204:205],v242 offset:16896\nds_read_b64_tr_b16 v[206:207],v242 offset:17152\nM v[116:119],v[216:219],a[156:159],v[116:119]\nbuffer_load_dwordx4 v227,s[36:39],s67 offen offset:3072 lds\nM v[56:59],v[220:223],a[144:147],v[56:59]\nds_read_b64_tr_b16 v[208:209],v241 offset:17408\n" \
"ds_read_b64_tr_b16 v[210:211],v241 offset:17664\nM v[60:63],v[220:223],a[148:151],v[60:63]\ns_add_u32 m0,s65,12288\ns_add_u32 s67,s66,33664\nM v[120:123],v[220:223],a[152:155],v[120:123]\nds_read_b64_tr_b16 v[212:213],v242 offset:17408\nds_read_b64_tr_b16 v[214:215],v242 offset:17664\nM v[124:127],v[220:223],a[156:159],v[124:127]\n" \
"ds_read_b64_tr_b16 v[216:217],v241 offset:17920\ns_waitcnt lgkmcnt(14)\nM v[0:3],v[192:195],a[160:163],v[0:3]\nds_read_b64_tr_b16 v[218:219],v241 offset:18176\ns_waitcnt lgkmcnt(14)\nM v[4:7],v[192:195],a[164:167],v[4:7]\nds_read_b64_tr_b16 v[220:221],v242 offset:17920\ns_waitcnt lgkmcnt(14)\n" \
"M v[64:67],v[192:195],a[168:171],v[64:67]\nds_read_b64_tr_b16 v[222:223],v242 offset:18176\ns_waitcnt lgkmcnt(13)\nM v[68:71],v[192:195],a[172:175],v[68:71]\nds_read_b128 a[176:179],v229 offset:16384\ns_waitcnt lgkmcnt(12)\nM v[8:11],v[196:199],a[160:163],v[8:11]\nds_read_b64_tr_b16 v[192:193],v241 offset:24576\n" \
"ds_read_b64_tr_b16 v[194:195],v241 offset:24832\nM v[12:15],v[196:199],a[164:167],v[12:15]\nds_read_b128 a[180:183],v229 offset:18432\nds_read_b128 a[184:187],v229 offset:20480\nM v[72:75],v[196:199],a[168:171],v[72:75]\nds_read_b128 a[188:191],v229 offset:22528\ns_lshl_b32 s71,s6,5\nM v[76:79],v[196:199],a[172:175],v[76:79]\n" \
"ds_read_b64_tr_b16 v[196:197],v242 offset:24576\ns_waitcnt lgkmcnt(13)\nM v[16:19],v[200:203],a[160:163],v[16:19]\nds_read_b64_tr_b16 v[198:199],v242 offset:24832\ns_add_u32 s71,s71,790528\nM v[20:23],v[200:203],a[164:167],v[20:23]\nbuffer_load_dwordx4 v224,s[36:39],s67 offen offset:0 lds\nM v[80:83],v[200:203],a[168:171],v[80:83]\n" \
"buffer_load_dwordx4 v225,s[36:39],s67 offen offset:1024 lds\nM v[84:87],v[200:203],a[172:175],v[84:87]\nbuffer_load_dwordx4 v226,s[36:39],s67 offen offset:2048 lds\nM v[24:27],v[204:207],a[160:163],v[24:27]\nds_read_b64_tr_b16 v[200:201],v241 offset:25088\nds_read_b64_tr_b16 v[202:203],v241 offset:25344\nM v[28:31],v[204:207],a[164:167],v[28:31]\nbuffer_load_dword v168,v169,s[52:55],s71 offen sc1\n" \
"M v[88:91],v[204:207],a[168:171],v[88:91]\ns_cmp_lt_u32 s7,2\ns_cselect_b32 s70,0,1.0\nM v[92:95],v[204:207],a[172:175],v[92:95]\nbuffer_load_dwordx4 v227,s[36:39],s67 offen offset:3072 lds\nM v[32:35],v[208:211],a[160:163],v[32:35]\nds_read_b64_tr_b16 v[204:205],v242 offset:25088\nds_read_b64_tr_b16 v[206:207],v242 offset:25344\n" \
"M v[36:39],v[208:211],a[164:167],v[36:39]\ns_add_u32 m0,s64,147456\ns_add_u32 s69,s68,3670016\nM v[96:99],v[208:211],a[168:171],v[96:99]\ns_lshr_b32 s71,s64,2\nv_add_u32 v171,s71,v240\nM v[100:103],v[208:211],a[172:175],v[100:103]\nds_read_b64_tr_b16 v[208:209],v241 offset:25600\n" \
"s_waitcnt lgkmcnt(5)\nM v[40:43],v[212:215],a[160:163],v[40:43]\nds_read_b64_tr_b16 v[210:211],v241 offset:25856\ns_lshl_b32 s71,s6,5\nM v[44:47],v[212:215],a[164:167],v[44:47]\nbuffer_load_dwordx4 v243,s[40:43],s69 offen offset:0 lds\nM v[104:107],v[212:215],a[168:171],v[104:107]\nbuffer_store_dwordx2 v[232:233],off,s[52:55],s72 sc1\n" \
"M v[108:111],v[212:215],a[172:175],v[108:111]\nds_read_b64_tr_b16 v[212:213],v242 offset:25600\nds_read_b64_tr_b16 v[214:215],v242 offset:25856\nM v[48:51],v[216:219],a[160:163],v[48:51]\ns_and_b32 s72,s6,1\ns_and_b32 s71,s71,0xfffffe00\nM v[52:55],v[216:219],a[164:167],v[52:55]\nbuffer_load_dwordx4 v244,s[40:43],s69 offen offset:1024 lds\n" \
"M v[112:115],v[216:219],a[168:171],v[112:115]\ns_lshl_b32 s72,s72,8\ns_add_u32 s71,s71,s72\nM v[116:119],v[216:219],a[172:175],v[116:119]\nds_read_b64_tr_b16 v[216:217],v241 offset:26112\nds_read_b64_tr_b16 v[218:219],v241 offset:26368\nM v[56:59],v[220:223],a[160:163],v[56:59]\ns_and_b32 s72,s6,14\n" \
"s_lshl1_add_u32 s71,s72,s71\nM v[60:63],v[220:223],a[164:167],v[60:63]\ns_or_b32 s71,s71,s75\ns_mov_b32 s72,0x3e0293ee\nM v[120:123],v[220:223],a[168:171],v[120:123]\nbuffer_load_dwordx4 v245,s[40:43],s69 offen offset:2048 lds\nM v[124:127],v[220:223],a[172:175],v[124:127]\nds_read_b64_tr_b16 v[220:221],v242 offset:26112\n" \
"ds_read_b64_tr_b16 v[222:223],v242 offset:26368\nM v[0:3],v[192:195],a[176:179],v[0:3]\nv_mov_b32 v224,-1\nv_mov_b32 v225,-1\nM v[4:7],v[192:195],a[180:183],v[4:7]\nv_mov_b32 v226,-1\nv_mov_b32 v227,-1\nM v[64:67],v[192:195],a[184:187],v[64:67]\n" \
"s_cmp_eq_u32 s62,0\ns_cselect_b32 s36,0x8000,0\nM v[68:71],v[192:195],a[188:191],v[68:71]\nbuffer_load_dwordx4 v246,s[40:43],s69 offen offset:3072 lds\nM v[8:11],v[196:199],a[176:179],v[8:11]\ns_add_u32 s36,s36,s64\nv_and_b32 v231,16,v249\nM v[12:15],v[196:199],a[180:183],v[12:15]\n" \
"s_sub_u32 s40,s16,0x1000\ns_subb_u32 s41,s17,0\nM v[72:75],v[196:199],a[184:187],v[72:75]\ns_add_u32 m0,s64,98304\ns_add_u32 s69,s68,0\nM v[76:79],v[196:199],a[188:191],v[76:79]\ns_mov_b32 s16,0\ns_waitcnt lgkmcnt(2)\n" \
"M v[16:19],v[200:203],a[176:179],v[16:19]\nbuffer_load_dwordx4 v243,s[40:43],s69 offen offset:0 lds\nM v[20:23],v[200:203],a[180:183],v[20:23]\ns_mov_b32 s17,0\nM v[80:83],v[200:203],a[184:187],v[80:83]\nM v[84:87],v[200:203],a[188:191],v[84:87]\nM v[24:27],v[204:207],a[176:179],v[24:27]\nM v[28:31],v[204:207],a[180:183],v[28:31]\n" \
"buffer_load_dwordx4 v244,s[40:43],s69 offen offset:1024 lds\nM v[88:91],v[204:207],a[184:187],v[88:91]\ns_waitcnt vmcnt(20) lgkmcnt(0)\ns_barrier\nM v[92:95],v[204:207],a[188:191],v[92:95]\nds_read_b128 a[192:195],v228 offset:0\nds_read_b64_tr_b16 v[192:193],v241 offset:32768\nM v[32:35],v[208:211],a[176:179],v[32:35]\n" \
"ds_read_b64_tr_b16 v[194:195],v241 offset:33024\nds_read_b128 a[196:199],v228 offset:2048\nM v[36:39],v[208:211],a[180:183],v[36:39]\nds_read_b128 a[200:203],v228 offset:4096\nds_read_b128 a[204:207],v228 offset:6144\nM v[96:99],v[208:211],a[184:187],v[96:99]\nds_read_b64_tr_b16 v[196:197],v242 offset:32768\nds_read_b64_tr_b16 v[198:199],v242 offset:33024\n" \
"M v[100:103],v[208:211],a[188:191],v[100:103]\nds_read_b64_tr_b16 v[200:201],v241 offset:33280\nds_read_b64_tr_b16 v[202:203],v241 offset:33536\nM v[40:43],v[212:215],a[176:179],v[40:43]\nds_read_b64_tr_b16 v[204:205],v242 offset:33280\nds_read_b64_tr_b16 v[206:207],v242 offset:33536\nM v[44:47],v[212:215],a[180:183],v[44:47]\nds_read_b64_tr_b16 v[208:209],v241 offset:33792\n" \
"ds_read_b64_tr_b16 v[210:211],v241 offset:34048\nM v[104:107],v[212:215],a[184:187],v[104:107]\nds_read_b128 a[208:211],v229 offset:0\nds_read_b128 a[212:215],v229 offset:2048\nM v[108:111],v[212:215],a[188:191],v[108:111]\nds_read_b64_tr_b16 v[212:213],v242 offset:33792\nds_read_b64_tr_b16 v[214:215],v242 offset:34048\nM v[48:51],v[216:219],a[176:179],v[48:51]\n" \
"ds_read_b128 a[216:219],v229 offset:4096\nds_read_b128 a[220:223],v229 offset:6144\nM v[52:55],v[216:219],a[180:183],v[52:55]\ns_waitcnt vmcnt(17)\nv_add_f32 v152,v152,v156\nM v[112:115],v[216:219],a[184:187],v[112:115]\nv_add_f32 v153,v153,v157\nv_add_f32 v154,v154,v158\n" \
"M v[116:119],v[216:219],a[188:191],v[116:119]\nds_read_b64_tr_b16 v[216:217],v241 offset:34304\nds_read_b64_tr_b16 v[218:219],v241 offset:34560\nM v[56:59],v[220:223],a[176:179],v[56:59]\ns_waitcnt vmcnt(13)\nv_add_f32 v160,v160,v164\nM v[60:63],v[220:223],a[180:183],v[60:63]\nv_add_f32 v161,v161,v165\n" \
"v_add_f32 v162,v162,v166\nM v[120:123],v[220:223],a[184:187],v[120:123]\nv_add_f32 v155,v155,v159\nv_add_f32 v163,v163,v167\nM v[124:127],v[220:223],a[188:191],v[124:127]\nds_read_b64_tr_b16 v[220:221],v242 offset:34304\ns_waitcnt lgkmcnt(10)\nM v[0:3],v[192:195],a[192:195],v[0:3]\n" \
"ds_read_b64_tr_b16 v[222:223],v242 offset:34560\nv_add_f32 v152,v152,v160\nM v[4:7],v[192:195],a[196:199],v[4:7]\nv_add_f32 v153,v153,v161\nv_add_f32 v154,v154,v162\nM v[64:67],v[192:195],a[200:203],v[64:67]\nv_add_f32 v155,v155,v163\nv_cvt_pk_bf16_f32 v152,0,v152\n" \
"M v[68:71],v[192:195],a[204:207],v[68:71]\nds_read_b64_tr_b16 v[192:193],v241 offset:40960\nds_read_b64_tr_b16 v[194:195],v241 offset:41216\nM v[8:11],v[196:199],a[192:195],v[8:11]\nv_cvt_pk_bf16_f32 v153,0,v153\nv_cvt_pk_bf16_f32 v154,0,v154\nM v[12:15],v[196:199],a[196:199],v[12:15]\nv_cvt_pk_bf16_f32 v155,0,v155\n" \
"v_add_f32 v152,s70,v152\nM v[72:75],v[196:199],a[200:203],v[72:75]\nv_add_f32 v153,s70,v153\nv_add_f32 v154,s70,v154\nM v[76:79],v[196:199],a[204:207],v[76:79]\nds_read_b64_tr_b16 v[196:197],v242 offset:40960\nds_read_b64_tr_b16 v[198:199],v242 offset:41216\nM v[16:19],v[200:203],a[192:195],v[16:19]\n" \
"v_add_f32 v155,s70,v155\nv_cvt_pk_bf16_f32 v152,0,v152\nM v[20:23],v[200:203],a[196:199],v[20:23]\nv_cvt_pk_bf16_f32 v153,0,v153\nv_cvt_pk_bf16_f32 v154,0,v154\nM v[80:83],v[200:203],a[200:203],v[80:83]\nv_cvt_pk_bf16_f32 v155,0,v155\ns_lshl_b32 s70,s7,8\n" \
"M v[84:87],v[200:203],a[204:207],v[84:87]\nds_read_b64_tr_b16 v[200:201],v241 offset:41472\nds_read_b64_tr_b16 v[202:203],v241 offset:41728\nM v[24:27],v[204:207],a[192:195],v[24:27]\nds_write_b128 v171,v[152:155] offset:16384\nM v[28:31],v[204:207],a[196:199],v[28:31]\nbuffer_load_dwordx4 v245,s[40:43],s69 offen offset:2048 lds\nM v[88:91],v[204:207],a[200:203],v[88:91]\n" \
"buffer_load_dwordx4 v246,s[40:43],s69 offen offset:3072 lds\nM v[92:95],v[204:207],a[204:207],v[92:95]\nds_read_b64_tr_b16 v[204:205],v242 offset:41472\ns_waitcnt lgkmcnt(6)\nM v[32:35],v[208:211],a[192:195],v[32:35]\nds_read_b64_tr_b16 v[206:207],v242 offset:41728\ns_add_u32 m0,s64,114688\nM v[36:39],v[208:211],a[196:199],v[36:39]\n" \
"s_add_u32 s69,s68,524288\nv_lshl_add_u32 v251,v251,5,s70\nM v[96:99],v[208:211],a[200:203],v[96:99]\ns_xor_b32 s70,s63,16\ns_lshr_b32 s70,s70,2\nM v[100:103],v[208:211],a[204:207],v[100:103]\nds_read_b64_tr_b16 v[208:209],v241 offset:41984\nds_read_b64_tr_b16 v[210:211],v241 offset:42240\n" \
"M v[40:43],v[212:215],a[192:195],v[40:43]\ns_waitcnt vmcnt(10)\nv_add_f32_dpp v168,v168,v168 quad_perm:[1,0,3,2] row_mask:0xf bank_mask:0xf\nM v[44:47],v[212:215],a[196:199],v[44:47]\ns_lshl3_add_u32 s70,s62,s70\nv_add_f32_dpp v168,v168,v168 quad_perm:[2,3,0,1] row_mask:0xf bank_mask:0xf\nM v[104:107],v[212:215],a[200:203],v[104:107]\nv_cvt_pk_bf16_f32 v168,0,v168\n" \
"s_lshl_b32 s70,s70,15\nM v[108:111],v[212:215],a[204:207],v[108:111]\nds_read_b64_tr_b16 v[212:213],v242 offset:41984\nds_read_b64_tr_b16 v[214:215],v242 offset:42240\nM v[48:51],v[216:219],a[192:195],v[48:51]\nbuffer_store_dword v168,v170,s[52:55],s71 offen sc1\nM v[52:55],v[216:219],a[196:199],v[52:55]\ns_and_b32 s71,s63,2\n" \
"s_add_u32 s70,s70,s64\nM v[112:115],v[216:219],a[200:203],v[112:115]\ns_lshl_b32 s71,s71,9\ns_add_u32 s70,s70,s71\nM v[116:119],v[216:219],a[204:207],v[116:119]\nds_read_b64_tr_b16 v[216:217],v241 offset:42496\nds_read_b64_tr_b16 v[218:219],v241 offset:42752\nM v[56:59],v[220:223],a[192:195],v[56:59]\n" \
"buffer_load_dwordx4 v243,s[40:43],s69 offen offset:0 lds\nM v[60:63],v[220:223],a[196:199],v[60:63]\nbuffer_load_dwordx4 v244,s[40:43],s69 offen offset:1024 lds\nM v[120:123],v[220:223],a[200:203],v[120:123]\nbuffer_load_dwordx4 v245,s[40:43],s69 offen offset:2048 lds\nM v[124:127],v[220:223],a[204:207],v[124:127]\nds_read_b64_tr_b16 v[220:221],v242 offset:42496\nds_read_b64_tr_b16 v[222:223],v242 offset:42752\n" \
"M v[0:3],v[192:195],a[208:211],v[0:3]\nbuffer_load_dwordx4 v246,s[40:43],s69 offen offset:3072 lds\nM v[4:7],v[192:195],a[212:215],v[4:7]\ns_and_b32 s71,s63,1\ns_lshl_b32 s71,s71,7\nM v[64:67],v[192:195],a[216:219],v[64:67]\ns_add_u32 s70,s70,s71\ns_add_u32 s70,s70,1048576\n" \
"M v[68:71],v[192:195],a[220:223],v[68:71]\ns_add_u32 s71,s70,16384\ns_waitcnt lgkmcnt(4)\nM v[8:11],v[196:199],a[208:211],v[8:11]\nglobal_load_dword v247,v253,s[12:13]\nM v[12:15],v[196:199],a[212:215],v[12:15]\nM v[72:75],v[196:199],a[216:219],v[72:75]\nM v[76:79],v[196:199],a[220:223],v[76:79]\n" \
"M v[16:19],v[200:203],a[208:211],v[16:19]\nbuffer_load_dword v247,v252,s[56:59],s78 offen\nM v[20:23],v[200:203],a[212:215],v[20:23]\nM v[80:83],v[200:203],a[216:219],v[80:83]\nM v[84:87],v[200:203],a[220:223],v[84:87]\nM v[24:27],v[204:207],a[208:211],v[24:27]\ns_add_u32 m0,s64,131072\ns_add_u32 s69,s68,1048576\n" \
"M v[28:31],v[204:207],a[212:215],v[28:31]\nv_lshrrev_b32 v253,4,v249\nM v[88:91],v[204:207],a[216:219],v[88:91]\ns_waitcnt vmcnt(11) lgkmcnt(0)\ns_barrier\nM v[92:95],v[204:207],a[220:223],v[92:95]\nds_read_b128 a[224:227],v228 offset:8192\nds_read_b64_tr_b16 v[192:193],v241 offset:49152\n" \
"M v[32:35],v[208:211],a[208:211],v[32:35]\nds_read_b64_tr_b16 v[194:195],v241 offset:49408\nds_read_b128 a[228:231],v228 offset:10240\nM v[36:39],v[208:211],a[212:215],v[36:39]\nds_read_b128 a[232:235],v228 offset:12288\nds_read_b128 a[236:239],v228 offset:14336\nM v[96:99],v[208:211],a[216:219],v[96:99]\nds_read_b64_tr_b16 v[196:197],v242 offset:49152\n" \
"ds_read_b64_tr_b16 v[198:199],v242 offset:49408\nM v[100:103],v[208:211],a[220:223],v[100:103]\nds_read_b64_tr_b16 v[200:201],v241 offset:49664\nds_read_b64_tr_b16 v[202:203],v241 offset:49920\nM v[40:43],v[212:215],a[208:211],v[40:43]\nds_read_b64_tr_b16 v[204:205],v242 offset:49664\nds_read_b64_tr_b16 v[206:207],v242 offset:49920\nM v[44:47],v[212:215],a[212:215],v[44:47]\n" \
"ds_read_b64_tr_b16 v[208:209],v241 offset:50176\nds_read_b64_tr_b16 v[210:211],v241 offset:50432\nM v[104:107],v[212:215],a[216:219],v[104:107]\nds_read_b128 a[240:243],v229 offset:8192\nds_read_b128 a[244:247],v229 offset:10240\nM v[108:111],v[212:215],a[220:223],v[108:111]\nds_read_b64_tr_b16 v[212:213],v242 offset:50176\nds_read_b64_tr_b16 v[214:215],v242 offset:50432\n" \
"M v[48:51],v[216:219],a[208:211],v[48:51]\nds_read_b128 a[248:251],v229 offset:12288\nds_read_b128 a[252:255],v229 offset:14336\nM v[52:55],v[216:219],a[212:215],v[52:55]\nds_read_b128 v[176:179],v251 offset:19456\nds_read_b128 v[160:163],v251 offset:17408\nM v[112:115],v[216:219],a[216:219],v[112:115]\nds_read_b128 v[168:171],v251 offset:18432\n" \
"ds_read_b128 v[172:175],v251 offset:18448\nM v[116:119],v[216:219],a[220:223],v[116:119]\nds_read_b64_tr_b16 v[216:217],v241 offset:50688\nds_read_b64_tr_b16 v[218:219],v241 offset:50944\nM v[56:59],v[220:223],a[208:211],v[56:59]\nds_read_b128 v[180:183],v251 offset:19472\nds_read_b128 v[152:155],v251 offset:16384\nM v[60:63],v[220:223],a[212:215],v[60:63]\n" \
"ds_read_b128 v[156:159],v251 offset:16400\nds_read_b128 v[164:167],v251 offset:17424\nM v[120:123],v[220:223],a[216:219],v[120:123]\nv_lshrrev_b32 v251,5,v249\nv_lshlrev_b32 v251,11,v251\nM v[124:127],v[220:223],a[220:223],v[124:127]\nds_read_b64_tr_b16 v[220:221],v242 offset:50688\ns_waitcnt lgkmcnt(15)\n" \
"M v[0:3],v[192:195],a[224:227],v[0:3]\nds_read_b64_tr_b16 v[222:223],v242 offset:50944\nv_lshl_or_b32 v251,v250,8,v251\nM v[4:7],v[192:195],a[228:231],v[4:7]\nv_and_b32 v250,0x70,v240\nv_or_b32 v251,v251,v250\nM v[64:67],v[192:195],a[232:235],v[64:67]\nbuffer_load_dwordx4 v243,s[40:43],s69 offen offset:0 lds\n" \
"M v[68:71],v[192:195],a[236:239],v[68:71]\nds_read_b64_tr_b16 v[192:193],v241 offset:57344\nds_read_b64_tr_b16 v[194:195],v241 offset:57600\nM v[8:11],v[196:199],a[224:227],v[8:11]\ns_waitcnt lgkmcnt(12)\nv_mul_f32 v144,v144,v176\nM v[12:15],v[196:199],a[228:231],v[12:15]\nv_mul_f32 v145,v145,v177\n" \
"v_cvt_pk_bf16_f32 v144,0,v144\nM v[72:75],v[196:199],a[232:235],v[72:75]\nv_cvt_pk_bf16_f32 v145,0,v145\nv_add_f32 v144,v144,v160\nM v[76:79],v[196:199],a[236:239],v[76:79]\nds_read_b64_tr_b16 v[196:197],v242 offset:57344\nds_read_b64_tr_b16 v[198:199],v242 offset:57600\nM v[16:19],v[200:203],a[224:227],v[16:19]\n" \
"v_add_f32 v145,v145,v161\nv_cvt_pk_bf16_f32 v144,0,v144\nM v[20:23],v[200:203],a[228:231],v[20:23]\nv_cvt_pk_bf16_f32 v145,0,v145\nv_mul_f32 v144,s72,v144\nM v[80:83],v[200:203],a[232:235],v[80:83]\nv_mul_f32 v145,s72,v145\nv_cvt_pk_bf16_f32 v236,v144,v145\n" \
"M v[84:87],v[200:203],a[236:239],v[84:87]\nds_read_b64_tr_b16 v[200:201],v241 offset:57856\nds_read_b64_tr_b16 v[202:203],v241 offset:58112\nM v[24:27],v[204:207],a[224:227],v[24:27]\nv_mul_f32 v146,v146,v178\nv_mul_f32 v147,v147,v179\nM v[28:31],v[204:207],a[228:231],v[28:31]\nv_cvt_pk_bf16_f32 v146,0,v146\n" \
"v_cvt_pk_bf16_f32 v147,0,v147\nM v[88:91],v[204:207],a[232:235],v[88:91]\nv_add_f32 v146,v146,v162\nv_add_f32 v147,v147,v163\nM v[92:95],v[204:207],a[236:239],v[92:95]\nds_read_b64_tr_b16 v[204:205],v242 offset:57856\nds_read_b64_tr_b16 v[206:207],v242 offset:58112\nM v[32:35],v[208:211],a[224:227],v[32:35]\n" \
"v_cvt_pk_bf16_f32 v146,0,v146\nv_cvt_pk_bf16_f32 v147,0,v147\nM v[36:39],v[208:211],a[228:231],v[36:39]\nv_mul_f32 v146,s72,v146\nv_mul_f32 v147,s72,v147\nM v[96:99],v[208:211],a[232:235],v[96:99]\nv_cvt_pk_bf16_f32 v237,v146,v147\ns_waitcnt lgkmcnt(4)\n" \
"v_mul_f32 v136,v136,v168\nM v[100:103],v[208:211],a[236:239],v[100:103]\nds_read_b64_tr_b16 v[208:209],v241 offset:58368\nds_read_b64_tr_b16 v[210:211],v241 offset:58624\nM v[40:43],v[212:215],a[224:227],v[40:43]\nv_mul_f32 v137,v137,v169\nv_mul_f32 v138,v138,v170\nM v[44:47],v[212:215],a[228:231],v[44:47]\n" \
"v_mul_f32 v139,v139,v171\nv_mul_f32 v140,v140,v172\nM v[104:107],v[212:215],a[232:235],v[104:107]\nv_mul_f32 v141,v141,v173\nv_mul_f32 v142,v142,v174\nM v[108:111],v[212:215],a[236:239],v[108:111]\nds_read_b64_tr_b16 v[212:213],v242 offset:58368\nds_read_b64_tr_b16 v[214:215],v242 offset:58624\n" \
"M v[48:51],v[216:219],a[224:227],v[48:51]\nv_mul_f32 v143,v143,v175\nv_mul_f32 v148,v148,v180\nM v[52:55],v[216:219],a[228:231],v[52:55]\nv_mul_f32 v149,v149,v181\nv_cvt_pk_bf16_f32 v136,0,v136\nM v[112:115],v[216:219],a[232:235],v[112:115]\nv_cvt_pk_bf16_f32 v137,0,v137\n" \
"v_cvt_pk_bf16_f32 v138,0,v138\nM v[116:119],v[216:219],a[236:239],v[116:119]\nds_read_b64_tr_b16 v[216:217],v241 offset:58880\nds_read_b64_tr_b16 v[218:219],v241 offset:59136\nM v[56:59],v[220:223],a[224:227],v[56:59]\nv_cvt_pk_bf16_f32 v139,0,v139\nv_cvt_pk_bf16_f32 v140,0,v140\nM v[60:63],v[220:223],a[228:231],v[60:63]\n" \
"v_cvt_pk_bf16_f32 v141,0,v141\nv_cvt_pk_bf16_f32 v142,0,v142\nM v[120:123],v[220:223],a[232:235],v[120:123]\nv_cvt_pk_bf16_f32 v143,0,v143\nv_cvt_pk_bf16_f32 v148,0,v148\nM v[124:127],v[220:223],a[236:239],v[124:127]\nds_read_b64_tr_b16 v[220:221],v242 offset:58880\nds_read_b64_tr_b16 v[222:223],v242 offset:59136\n" \
"M v[0:3],v[192:195],a[240:243],v[0:3]\nv_cvt_pk_bf16_f32 v149,0,v149\nv_add_f32 v136,v136,v152\nM v[4:7],v[192:195],a[244:247],v[4:7]\nv_add_f32 v137,v137,v153\nv_add_f32 v138,v138,v154\nM v[64:67],v[192:195],a[248:251],v[64:67]\nv_add_f32 v139,v139,v155\n" \
"v_add_f32 v140,v140,v156\nM v[68:71],v[192:195],a[252:255],v[68:71]\nv_cvt_pk_bf16_f32 v128,v0,v1\nv_cvt_pk_bf16_f32 v129,v2,v3\nM v[8:11],v[196:199],a[240:243],v[8:11]\nv_cvt_pk_bf16_f32 v144,v4,v5\nv_cvt_pk_bf16_f32 v145,v6,v7\nM v[12:15],v[196:199],a[244:247],v[12:15]\n" \
"v_cvt_pk_bf16_f32 v160,v64,v65\nv_cvt_pk_bf16_f32 v161,v66,v67\nM v[72:75],v[196:199],a[248:251],v[72:75]\nv_cvt_pk_bf16_f32 v176,v68,v69\nv_cvt_pk_bf16_f32 v177,v70,v71\nM v[76:79],v[196:199],a[252:255],v[76:79]\nv_cvt_pk_bf16_f32 v130,v8,v9\ns_waitcnt lgkmcnt(2)\n" \
"M v[16:19],v[200:203],a[240:243],v[16:19]\nv_cvt_pk_bf16_f32 v131,v10,v11\nv_cvt_pk_bf16_f32 v146,v12,v13\nM v[20:23],v[200:203],a[244:247],v[20:23]\nv_cvt_pk_bf16_f32 v147,v14,v15\nv_cvt_pk_bf16_f32 v162,v72,v73\nM v[80:83],v[200:203],a[248:251],v[80:83]\nv_cvt_pk_bf16_f32 v163,v74,v75\n" \
"v_add_f32 v141,v141,v157\nM v[84:87],v[200:203],a[252:255],v[84:87]\nv_add_f32 v142,v142,v158\nv_add_f32 v143,v143,v159\nM v[24:27],v[204:207],a[240:243],v[24:27]\nv_add_f32 v148,v148,v164\nv_add_f32 v149,v149,v165\nM v[28:31],v[204:207],a[244:247],v[28:31]\n" \
"v_cvt_pk_bf16_f32 v178,v76,v77\nv_cvt_pk_bf16_f32 v179,v78,v79\nM v[88:91],v[204:207],a[248:251],v[88:91]\ns_waitcnt vmcnt(8) lgkmcnt(0)\ns_barrier\nM v[92:95],v[204:207],a[252:255],v[92:95]\nds_read_b64_tr_b16 v[192:193],v241 offset:0\nds_read_b64_tr_b16 v[194:195],v241 offset:256\n" \
"M v[32:35],v[208:211],a[240:243],v[32:35]\nds_read_b64_tr_b16 v[196:197],v242 offset:0\nds_read_b64_tr_b16 v[198:199],v242 offset:256\nM v[36:39],v[208:211],a[244:247],v[36:39]\nds_read_b64_tr_b16 v[200:201],v241 offset:512\nds_read_b64_tr_b16 v[202:203],v241 offset:768\nM v[96:99],v[208:211],a[248:251],v[96:99]\nds_read_b64_tr_b16 v[204:205],v242 offset:512\n" \
"ds_read_b64_tr_b16 v[206:207],v242 offset:768\nM v[100:103],v[208:211],a[252:255],v[100:103]\nds_read_b64_tr_b16 v[208:209],v241 offset:1024\nds_read_b64_tr_b16 v[210:211],v241 offset:1280\nM v[40:43],v[212:215],a[240:243],v[40:43]\nv_cvt_pk_bf16_f32 v136,0,v136\nv_cvt_pk_bf16_f32 v137,0,v137\nM v[44:47],v[212:215],a[244:247],v[44:47]\n" \
"v_cvt_pk_bf16_f32 v138,0,v138\nv_cvt_pk_bf16_f32 v139,0,v139\nM v[104:107],v[212:215],a[248:251],v[104:107]\nv_cvt_pk_bf16_f32 v140,0,v140\nv_cvt_pk_bf16_f32 v141,0,v141\nM v[108:111],v[212:215],a[252:255],v[108:111]\nv_cvt_pk_bf16_f32 v142,0,v142\nv_cvt_pk_bf16_f32 v143,0,v143\n" \
"M v[48:51],v[216:219],a[240:243],v[48:51]\nv_cvt_pk_bf16_f32 v148,0,v148\nv_cvt_pk_bf16_f32 v149,0,v149\nM v[52:55],v[216:219],a[244:247],v[52:55]\nv_mul_f32 v136,s72,v136\nv_mul_f32 v137,s72,v137\nM v[112:115],v[216:219],a[248:251],v[112:115]\nv_mul_f32 v138,s72,v138\n" \
"v_mul_f32 v139,s72,v139\nM v[116:119],v[216:219],a[252:255],v[116:119]\nv_mul_f32 v140,s72,v140\nv_mul_f32 v141,s72,v141\nM v[56:59],v[220:223],a[240:243],v[56:59]\nv_mul_f32 v142,s72,v142\nv_mul_f32 v143,s72,v143\nM v[60:63],v[220:223],a[244:247],v[60:63]\n" \
"v_mul_f32 v148,s72,v148\nv_mul_f32 v149,s72,v149\nM v[120:123],v[220:223],a[248:251],v[120:123]\nv_cvt_pk_bf16_f32 v132,v16,v17\nv_cvt_pk_bf16_f32 v133,v18,v19\nM v[124:127],v[220:223],a[252:255],v[124:127]\nv_cvt_pk_bf16_f32 v232,v136,v137\ns_waitcnt lgkmcnt(0)\n" \
"M v[0:3],a[0:3],v[192:195],0\nv_cvt_pk_bf16_f32 v233,v138,v139\nv_cvt_pk_bf16_f32 v234,v140,v141\nM v[4:7],a[4:7],v[192:195],0\nv_cvt_pk_bf16_f32 v235,v142,v143\nv_cvt_pk_bf16_f32 v238,v148,v149\nM v[64:67],a[8:11],v[192:195],0\nbuffer_store_dwordx4 v[232:235],v251,s[52:55],s70 offen sc1\n" \
"M v[68:71],a[12:15],v[192:195],0\nv_cvt_pk_bf16_f32 v148,v20,v21\nv_cvt_pk_bf16_f32 v149,v22,v23\nM v[8:11],a[0:3],v[196:199],0\nv_mul_f32 v150,v150,v182\nv_mul_f32 v151,v151,v183\nM v[12:15],a[4:7],v[196:199],0\nv_cvt_pk_bf16_f32 v164,v80,v81\n" \
"v_cvt_pk_bf16_f32 v165,v82,v83\nM v[72:75],a[8:11],v[196:199],0\nds_read_b64_tr_b16 v[212:213],v242 offset:1024\nds_read_b64_tr_b16 v[214:215],v242 offset:1280\nM v[76:79],a[12:15],v[196:199],0\nv_cvt_pk_bf16_f32 v150,0,v150\nv_cvt_pk_bf16_f32 v151,0,v151\nM v[16:19],a[0:3],v[200:203],0\n" \
"v_add_f32 v150,v150,v166\nv_add_f32 v151,v151,v167\nM v[20:23],a[4:7],v[200:203],0\nv_cvt_pk_bf16_f32 v180,v84,v85\nv_cvt_pk_bf16_f32 v181,v86,v87\nM v[80:83],a[8:11],v[200:203],0\nv_cvt_pk_bf16_f32 v150,0,v150\nv_cvt_pk_bf16_f32 v151,0,v151\n" \
"M v[84:87],a[12:15],v[200:203],0\nv_mul_f32 v150,s72,v150\nv_mul_f32 v151,s72,v151\nv_cvt_pk_bf16_f32 v134,v24,v25\nv_cvt_pk_bf16_f32 v135,v26,v27\nM v[24:27],a[0:3],v[204:207],0\nv_cvt_pk_bf16_f32 v239,v150,v151\nbuffer_store_dwordx4 v[236:239],v251,s[52:55],s71 offen sc1\n" \
"v_cvt_pk_bf16_f32 v150,v28,v29\nv_cvt_pk_bf16_f32 v151,v30,v31\nM v[28:31],a[4:7],v[204:207],0\nv_cvt_pk_bf16_f32 v166,v88,v89\nv_cvt_pk_bf16_f32 v167,v90,v91\nds_read_b64_tr_b16 v[216:217],v241 offset:1536\nds_read_b64_tr_b16 v[218:219],v241 offset:1792\nM v[88:91],a[8:11],v[204:207],0\n" \
"v_cvt_pk_bf16_f32 v182,v92,v93\nv_cvt_pk_bf16_f32 v183,v94,v95\nM v[92:95],a[12:15],v[204:207],0\nv_cvt_pk_bf16_f32 v136,v32,v33\nv_cvt_pk_bf16_f32 v137,v34,v35\nM v[32:35],a[0:3],v[208:211],0\ns_lshl_b32 s70,s62,5\ns_add_u32 s70,s70,s63\n" \
"v_cvt_pk_bf16_f32 v152,v36,v37\nv_cvt_pk_bf16_f32 v153,v38,v39\nM v[36:39],a[4:7],v[208:211],0\ns_lshl2_add_u32 s70,s70,s7\ns_lshl2_add_u32 s79,s70,90112\nv_cvt_pk_bf16_f32 v168,v96,v97\nv_cvt_pk_bf16_f32 v169,v98,v99\nds_read_b64_tr_b16 v[220:221],v242 offset:1536\n" \
"ds_read_b64_tr_b16 v[222:223],v242 offset:1792\nM v[96:99],a[8:11],v[208:211],0\ns_lshl_b32 s70,s60,4\nv_cvt_pk_bf16_f32 v184,v100,v101\nv_cvt_pk_bf16_f32 v185,v102,v103\ns_sub_u32 s70,s70,s62\nM v[100:103],a[12:15],v[208:211],0\ns_add_u32 s70,s70,15\n" \
"v_cvt_pk_bf16_f32 v138,v40,v41\nv_cvt_pk_bf16_f32 v139,v42,v43\ns_lshl2_add_u32 s70,s70,s7\ns_waitcnt lgkmcnt(4)\nM v[40:43],a[0:3],v[212:215],0\ns_lshl_b32 s70,s70,15\ns_lshl_b32 s71,s61,8\nv_cvt_pk_bf16_f32 v154,v44,v45\n" \
"v_cvt_pk_bf16_f32 v155,v46,v47\ns_add_u32 s70,s70,s71\nM v[44:47],a[4:7],v[212:215],0\ns_add_u32 s78,s70,54525952\nv_cvt_pk_bf16_f32 v170,v104,v105\nv_cvt_pk_bf16_f32 v171,v106,v107\ns_lshl3_add_u32 s70,s62,s7\nds_read_b64_tr_b16 v[192:193],v241 offset:8192\n" \
"ds_read_b64_tr_b16 v[194:195],v241 offset:8448\nM v[104:107],a[8:11],v[212:215],0\ns_lshl_b32 s80,s70,6\ns_lshl_b32 s76,s70,15\nv_cvt_pk_bf16_f32 v186,v108,v109\nv_cvt_pk_bf16_f32 v187,v110,v111\ns_lshl_b32 s70,s63,5\nM v[108:111],a[12:15],v[212:215],0\n" \
"s_add_u32 s70,s70,s74\nv_cvt_pk_bf16_f32 v140,v48,v49\nv_cvt_pk_bf16_f32 v141,v50,v51\ns_lshl_b32 s74,s70,14\ns_waitcnt lgkmcnt(2)\nM v[48:51],a[0:3],v[216:219],0\ns_add_u32 s74,s74,3145728\nv_cvt_pk_bf16_f32 v156,v52,v53\n" \
"v_cvt_pk_bf16_f32 v157,v54,v55\nbuffer_store_dwordx4 v[128:131],v240,s[52:55],s74 offen offset:0 sc1\nM v[52:55],a[4:7],v[216:219],0\nv_cvt_pk_bf16_f32 v172,v112,v113\nv_cvt_pk_bf16_f32 v173,v114,v115\nds_read_b64_tr_b16 v[196:197],v242 offset:8192\nds_read_b64_tr_b16 v[198:199],v242 offset:8448\nM v[112:115],a[8:11],v[216:219],0\n" \
"v_cvt_pk_bf16_f32 v188,v116,v117\nv_cvt_pk_bf16_f32 v189,v118,v119\nM v[116:119],a[12:15],v[216:219],0\nv_cvt_pk_bf16_f32 v142,v56,v57\nv_cvt_pk_bf16_f32 v143,v58,v59\nM v[56:59],a[0:3],v[220:223],0\nv_cvt_pk_bf16_f32 v158,v60,v61\nv_cvt_pk_bf16_f32 v159,v62,v63\n" \
"M v[60:63],a[4:7],v[220:223],0\nv_cvt_pk_bf16_f32 v174,v120,v121\nv_cvt_pk_bf16_f32 v175,v122,v123\nds_read_b64_tr_b16 v[200:201],v241 offset:8704\nds_read_b64_tr_b16 v[202:203],v241 offset:8960\nM v[120:123],a[8:11],v[220:223],0\nv_cvt_pk_bf16_f32 v190,v124,v125\nv_cvt_pk_bf16_f32 v191,v126,v127\n" \
"M v[124:127],a[12:15],v[220:223],0\nds_read_b64_tr_b16 v[204:205],v242 offset:8704\ns_waitcnt lgkmcnt(5)\nM v[0:3],a[16:19],v[192:195],v[0:3]\nds_read_b64_tr_b16 v[206:207],v242 offset:8960\nds_read_b64_tr_b16 v[208:209],v241 offset:9216\nM v[4:7],a[20:23],v[192:195],v[4:7]\nds_read_b64_tr_b16 v[210:211],v241 offset:9472\n" \
"ds_read_b64_tr_b16 v[212:213],v242 offset:9216\nM v[64:67],a[24:27],v[192:195],v[64:67]\nds_read_b64_tr_b16 v[214:215],v242 offset:9472\nds_read_b64_tr_b16 v[216:217],v241 offset:9728\nM v[68:71],a[28:31],v[192:195],v[68:71]\nds_read_b64_tr_b16 v[218:219],v241 offset:9984\ns_waitcnt lgkmcnt(10)\nM v[8:11],a[16:19],v[196:199],v[8:11]\n" \
"ds_read_b64_tr_b16 v[220:221],v242 offset:9728\nds_read_b64_tr_b16 v[222:223],v242 offset:9984\nM v[12:15],a[20:23],v[196:199],v[12:15]\nbuffer_load_dwordx4 v244,s[40:43],s69 offen offset:1024 lds\nM v[72:75],a[24:27],v[196:199],v[72:75]\nbuffer_load_dwordx4 v245,s[40:43],s69 offen offset:2048 lds\nM v[76:79],a[28:31],v[196:199],v[76:79]\nv_and_b32 v251,0xf0,v240\n" \
"s_waitcnt lgkmcnt(10)\nM v[16:19],a[16:19],v[200:203],v[16:19]\nbuffer_load_dwordx4 v246,s[40:43],s69 offen offset:3072 lds\nM v[20:23],a[20:23],v[200:203],v[20:23]\ns_add_u32 m0,s64,147456\ns_add_u32 s69,s68,1572864\nM v[80:83],a[24:27],v[200:203],v[80:83]\nbuffer_load_dwordx4 v243,s[40:43],s69 offen offset:0 lds\n" \
"M v[84:87],a[28:31],v[200:203],v[84:87]\nv_lshl_or_b32 v253,v253,12,v251\ns_waitcnt lgkmcnt(6)\nM v[24:27],a[16:19],v[204:207],v[24:27]\nbuffer_load_dwordx4 v244,s[40:43],s69 offen offset:1024 lds\nM v[28:31],a[20:23],v[204:207],v[28:31]\nbuffer_load_dwordx4 v245,s[40:43],s69 offen offset:2048 lds\nM v[88:91],a[24:27],v[204:207],v[88:91]\n" \
"s_waitcnt vmcnt(12) lgkmcnt(0)\ns_barrier\nM v[92:95],a[28:31],v[204:207],v[92:95]\nds_read_b64_tr_b16 v[192:193],v241 offset:16384\nds_read_b64_tr_b16 v[194:195],v241 offset:16640\nM v[32:35],a[16:19],v[208:211],v[32:35]\nds_read_b64_tr_b16 v[196:197],v242 offset:16384\nds_read_b64_tr_b16 v[198:199],v242 offset:16640\n" \
"M v[36:39],a[20:23],v[208:211],v[36:39]\nds_read_b64_tr_b16 v[200:201],v241 offset:16896\nds_read_b64_tr_b16 v[202:203],v241 offset:17152\nM v[96:99],a[24:27],v[208:211],v[96:99]\nbuffer_store_dwordx4 v[132:135],v240,s[52:55],s74 offen offset:1024 sc1\nM v[100:103],a[28:31],v[208:211],v[100:103]\nds_read_b64_tr_b16 v[204:205],v242 offset:16896\nds_read_b64_tr_b16 v[206:207],v242 offset:17152\n" \
"M v[40:43],a[16:19],v[212:215],v[40:43]\nds_read_b64_tr_b16 v[208:209],v241 offset:17408\nds_read_b64_tr_b16 v[210:211],v241 offset:17664\nM v[44:47],a[20:23],v[212:215],v[44:47]\nbuffer_load_dwordx4 v246,s[40:43],s69 offen offset:3072 lds\nM v[104:107],a[24:27],v[212:215],v[104:107]\ns_add_u32 m0,s64,98304\ns_add_u32 s69,s68,2097152\n" \
"M v[108:111],a[28:31],v[212:215],v[108:111]\nds_read_b64_tr_b16 v[212:213],v242 offset:17408\nds_read_b64_tr_b16 v[214:215],v242 offset:17664\nM v[48:51],a[16:19],v[216:219],v[48:51]\nbuffer_load_dwordx4 v243,s[40:43],s69 offen offset:0 lds\nM v[52:55],a[20:23],v[216:219],v[52:55]\nbuffer_load_dwordx4 v244,s[40:43],s69 offen offset:1024 lds\nM v[112:115],a[24:27],v[216:219],v[112:115]\n" \
"buffer_load_dwordx4 v245,s[40:43],s69 offen offset:2048 lds\nM v[116:119],a[28:31],v[216:219],v[116:119]\nds_read_b64_tr_b16 v[216:217],v241 offset:17920\nds_read_b64_tr_b16 v[218:219],v241 offset:18176\nM v[56:59],a[16:19],v[220:223],v[56:59]\ns_add_u32 s72,s78,16384\nv_and_b32 v228,15,v249\nM v[60:63],a[20:23],v[220:223],v[60:63]\n" \
"v_and_b32 v229,16,v249\nv_lshlrev_b32 v228,2,v228\nM v[120:123],a[24:27],v[220:223],v[120:123]\nbuffer_load_dwordx4 v246,s[40:43],s69 offen offset:3072 lds\nM v[124:127],a[28:31],v[220:223],v[124:127]\nds_read_b64_tr_b16 v[220:221],v242 offset:17920\ns_waitcnt lgkmcnt(5)\nM v[0:3],a[32:35],v[192:195],v[0:3]\n" \
"ds_read_b64_tr_b16 v[222:223],v242 offset:18176\nv_lshl_or_b32 v228,v229,4,v228\nM v[4:7],a[36:39],v[192:195],v[4:7]\ns_add_u32 s80,s80,90112\ns_add_u32 s76,s76,1048576\nM v[64:67],a[40:43],v[192:195],v[64:67]\ns_add_u32 s77,s76,131072\ns_lshl2_add_u32 s29,s70,81920\n" \
"M v[68:71],a[44:47],v[192:195],v[68:71]\nds_read_b64_tr_b16 v[192:193],v241 offset:24576\nds_read_b64_tr_b16 v[194:195],v241 offset:24832\nM v[8:11],a[32:35],v[196:199],v[8:11]\ns_lshl_b32 s70,s7,14\nv_add_u32 v250,s70,v240\nM v[12:15],a[36:39],v[196:199],v[12:15]\nds_write_b128 v250,v[128:131] offset:0\n" \
"M v[72:75],a[40:43],v[196:199],v[72:75]\ns_lshl_b32 s70,s63,19\ns_lshl_b32 s71,s60,6\nM v[76:79],a[44:47],v[196:199],v[76:79]\nds_read_b64_tr_b16 v[196:197],v242 offset:24576\nds_read_b64_tr_b16 v[198:199],v242 offset:24832\nM v[16:19],a[32:35],v[200:203],v[16:19]\ns_add_u32 s44,s34,s70\n" \
"s_addc_u32 s45,s35,0\nM v[20:23],a[36:39],v[200:203],v[20:23]\ns_lshl_b32 s70,s62,15\ns_sub_u32 s46,491520,s70\nM v[80:83],a[40:43],v[200:203],v[80:83]\ns_add_u32 s96,s70,s64\ns_lshl3_add_u32 s70,s62,s7\nM v[84:87],a[44:47],v[200:203],v[84:87]\n" \
"ds_read_b64_tr_b16 v[200:201],v241 offset:25088\nds_read_b64_tr_b16 v[202:203],v241 offset:25344\nM v[24:27],a[32:35],v[204:207],v[24:27]\ns_lshl3_add_u32 s70,s70,s60\ns_lshl_b32 s67,s70,15\nM v[28:31],a[36:39],v[204:207],v[28:31]\ns_lshl3_add_u32 s70,s62,s60\ns_lshl2_add_u32 s70,s70,s7\n" \
"M v[88:91],a[40:43],v[204:207],v[88:91]\nv_lshlrev_b32 v229,7,v249\ns_sub_u32 s96,s96,16384\nM v[92:95],a[44:47],v[204:207],v[92:95]\nds_read_b64_tr_b16 v[204:205],v242 offset:25088\nds_read_b64_tr_b16 v[206:207],v242 offset:25344\nM v[32:35],a[32:35],v[208:211],v[32:35]\nbuffer_store_dwordx4 v[136:139],v240,s[52:55],s74 offen offset:2048 sc1\n" \
"M v[36:39],a[36:39],v[208:211],v[36:39]\nds_write_b128 v250,v[132:135] offset:1024\nM v[96:99],a[40:43],v[208:211],v[96:99]\ns_add_u32 s48,s44,19922944\ns_addc_u32 s49,s45,0\nM v[100:103],a[44:47],v[208:211],v[100:103]\nds_read_b64_tr_b16 v[208:209],v241 offset:25600\ns_waitcnt lgkmcnt(6)\n" \
"M v[40:43],a[32:35],v[212:215],v[40:43]\nds_read_b64_tr_b16 v[210:211],v241 offset:25856\ns_add_u32 s15,s64,s46\nM v[44:47],a[36:39],v[212:215],v[44:47]\ns_add_u32 s44,s44,3145728\ns_addc_u32 s45,s45,0\nM v[104:107],a[40:43],v[212:215],v[104:107]\ns_mov_b32 s50,s46\n" \
"s_add_u32 s97,s96,98304\nM v[108:111],a[44:47],v[212:215],v[108:111]\nds_read_b64_tr_b16 v[212:213],v242 offset:25600\nds_read_b64_tr_b16 v[214:215],v242 offset:25856\nM v[48:51],a[32:35],v[216:219],v[48:51]\ns_add_u32 s15,s15,49152\ns_add_u32 s67,s67,36700160\nM v[52:55],a[36:39],v[216:219],v[52:55]\n" \
"v_mov_b32 v252,0xff800000\nM v[112:115],a[40:43],v[216:219],v[112:115]\nM v[116:119],a[44:47],v[216:219],v[116:119]\nds_read_b64_tr_b16 v[216:217],v241 offset:26112\nds_read_b64_tr_b16 v[218:219],v241 offset:26368\nM v[56:59],a[32:35],v[220:223],v[56:59]\nM v[60:63],a[36:39],v[220:223],v[60:63]\nM v[120:123],a[40:43],v[220:223],v[120:123]\n" \
"M v[124:127],a[44:47],v[220:223],v[124:127]\nds_read_b64_tr_b16 v[220:221],v242 offset:26112\nds_read_b64_tr_b16 v[222:223],v242 offset:26368\nM v[0:3],a[48:51],v[192:195],v[0:3]\nM v[4:7],a[52:55],v[192:195],v[4:7]\nM v[64:67],a[56:59],v[192:195],v[64:67]\nM v[68:71],a[60:63],v[192:195],v[68:71]\nM v[8:11],a[48:51],v[196:199],v[8:11]\n" \
"M v[12:15],a[52:55],v[196:199],v[12:15]\nM v[72:75],a[56:59],v[196:199],v[72:75]\nM v[76:79],a[60:63],v[196:199],v[76:79]\ns_waitcnt lgkmcnt(5)\nM v[16:19],a[48:51],v[200:203],v[16:19]\nds_write_b128 v250,v[136:139] offset:2048\nM v[20:23],a[52:55],v[200:203],v[20:23]\nM v[80:83],a[56:59],v[200:203],v[80:83]\n" \
"M v[84:87],a[60:63],v[200:203],v[84:87]\nM v[24:27],a[48:51],v[204:207],v[24:27]\ns_add_u32 m0,s64,114688\ns_add_u32 s69,s68,2621440\nM v[28:31],a[52:55],v[204:207],v[28:31]\nM v[88:91],a[56:59],v[204:207],v[88:91]\ns_waitcnt vmcnt(10) lgkmcnt(1)\ns_barrier\n" \
"M v[92:95],a[60:63],v[204:207],v[92:95]\nbuffer_store_dwordx4 v[140:143],v240,s[52:55],s74 offen offset:3072 sc1\nM v[32:35],a[48:51],v[208:211],v[32:35]\nds_read_b64_tr_b16 v[192:193],v241 offset:32768\nds_read_b64_tr_b16 v[194:195],v241 offset:33024\nM v[36:39],a[52:55],v[208:211],v[36:39]\nbuffer_store_dword v254,off,s[52:55],s79 sc1\nM v[96:99],a[56:59],v[208:211],v[96:99]\n" \
"ds_read_b64_tr_b16 v[196:197],v242 offset:32768\nds_read_b64_tr_b16 v[198:199],v242 offset:33024\nM v[100:103],a[60:63],v[208:211],v[100:103]\nds_read_b64_tr_b16 v[200:201],v241 offset:33280\nds_read_b64_tr_b16 v[202:203],v241 offset:33536\nM v[40:43],a[48:51],v[212:215],v[40:43]\nds_read_b64_tr_b16 v[204:205],v242 offset:33280\nds_read_b64_tr_b16 v[206:207],v242 offset:33536\n" \
"M v[44:47],a[52:55],v[212:215],v[44:47]\nds_read_b64_tr_b16 v[208:209],v241 offset:33792\nds_read_b64_tr_b16 v[210:211],v241 offset:34048\nM v[104:107],a[56:59],v[212:215],v[104:107]\ns_add_u32 s74,s74,4096\ns_lshl4_add_u32 s79,s70,65536\nM v[108:111],a[60:63],v[212:215],v[108:111]\nds_read_b64_tr_b16 v[212:213],v242 offset:33792\n" \
"ds_read_b64_tr_b16 v[214:215],v242 offset:34048\nM v[48:51],a[48:51],v[216:219],v[48:51]\nbuffer_load_dwordx4 v243,s[40:43],s69 offen offset:0 lds\nM v[52:55],a[52:55],v[216:219],v[52:55]\nbuffer_load_dwordx4 v244,s[40:43],s69 offen offset:1024 lds\nM v[112:115],a[56:59],v[216:219],v[112:115]\nbuffer_load_dwordx4 v245,s[40:43],s69 offen offset:2048 lds\nM v[116:119],a[60:63],v[216:219],v[116:119]\n" \
"ds_read_b64_tr_b16 v[216:217],v241 offset:34304\nds_read_b64_tr_b16 v[218:219],v241 offset:34560\nM v[56:59],a[48:51],v[220:223],v[56:59]\ns_lshr_b32 s70,s6,3\ns_lshl2_add_u32 s70,s70,s7\nM v[60:63],a[52:55],v[220:223],v[60:63]\ns_and_b32 s70,s70,63\ns_add_u32 s70,s70,s71\n" \
"M v[120:123],a[56:59],v[220:223],v[120:123]\nbuffer_load_dwordx4 v246,s[40:43],s69 offen offset:3072 lds\nM v[124:127],a[60:63],v[220:223],v[124:127]\nds_read_b64_tr_b16 v[220:221],v242 offset:34304\ns_waitcnt lgkmcnt(6)\nM v[0:3],a[64:67],v[192:195],v[0:3]\nds_read_b64_tr_b16 v[222:223],v242 offset:34560\ns_lshl_b32 s70,s70,13\n" \
"M v[4:7],a[68:71],v[192:195],v[4:7]\nds_write_b128 v250,v[140:143] offset:3072\nM v[64:67],a[72:75],v[192:195],v[64:67]\nv_add_u32 v229,s70,v229\ns_lshl_b32 s70,s61,8\nM v[68:71],a[76:79],v[192:195],v[68:71]\nds_read_b64_tr_b16 v[192:193],v241 offset:40960\nds_read_b64_tr_b16 v[194:195],v241 offset:41216\n" \
"M v[8:11],a[64:67],v[196:199],v[8:11]\nM v[12:15],a[68:71],v[196:199],v[12:15]\nM v[72:75],a[72:75],v[196:199],v[72:75]\nM v[76:79],a[76:79],v[196:199],v[76:79]\nds_read_b64_tr_b16 v[196:197],v242 offset:40960\nds_read_b64_tr_b16 v[198:199],v242 offset:41216\nM v[16:19],a[64:67],v[200:203],v[16:19]\nM v[20:23],a[68:71],v[200:203],v[20:23]\n" \
"M v[80:83],a[72:75],v[200:203],v[80:83]\nM v[84:87],a[76:79],v[200:203],v[84:87]\nds_read_b64_tr_b16 v[200:201],v241 offset:41472\nds_read_b64_tr_b16 v[202:203],v241 offset:41728\nM v[24:27],a[64:67],v[204:207],v[24:27]\nbuffer_store_dwordx4 v[144:147],v240,s[52:55],s74 offen offset:0 sc1\nM v[28:31],a[68:71],v[204:207],v[28:31]\nM v[88:91],a[72:75],v[204:207],v[88:91]\n" \
"M v[92:95],a[76:79],v[204:207],v[92:95]\nds_read_b64_tr_b16 v[204:205],v242 offset:41472\ns_waitcnt lgkmcnt(8)\nM v[32:35],a[64:67],v[208:211],v[32:35]\nbuffer_store_dwordx4 v[224:227],v253,s[52:55],s78 offen offset:0 sc1\nM v[36:39],a[68:71],v[208:211],v[36:39]\nds_read_b64_tr_b16 v[206:207],v242 offset:41728\nM v[96:99],a[72:75],v[208:211],v[96:99]\n" \
"M v[100:103],a[76:79],v[208:211],v[100:103]\nds_read_b64_tr_b16 v[208:209],v241 offset:41984\nds_read_b64_tr_b16 v[210:211],v241 offset:42240\nM v[40:43],a[64:67],v[212:215],v[40:43]\nM v[44:47],a[68:71],v[212:215],v[44:47]\nM v[104:107],a[72:75],v[212:215],v[104:107]\nM v[108:111],a[76:79],v[212:215],v[108:111]\nds_read_b64_tr_b16 v[212:213],v242 offset:41984\n" \
"ds_read_b64_tr_b16 v[214:215],v242 offset:42240\nM v[48:51],a[64:67],v[216:219],v[48:51]\nds_write_b128 v250,v[144:147] offset:4096\nM v[52:55],a[68:71],v[216:219],v[52:55]\nM v[112:115],a[72:75],v[216:219],v[112:115]\nM v[116:119],a[76:79],v[216:219],v[116:119]\nds_read_b64_tr_b16 v[216:217],v241 offset:42496\nds_read_b64_tr_b16 v[218:219],v241 offset:42752\n" \
"M v[56:59],a[64:67],v[220:223],v[56:59]\nM v[60:63],a[68:71],v[220:223],v[60:63]\nM v[120:123],a[72:75],v[220:223],v[120:123]\nM v[124:127],a[76:79],v[220:223],v[124:127]\nds_read_b64_tr_b16 v[220:221],v242 offset:42496\ns_waitcnt lgkmcnt(9)\nM v[0:3],a[80:83],v[192:195],v[0:3]\nds_read_b64_tr_b16 v[222:223],v242 offset:42752\n" \
"M v[4:7],a[84:87],v[192:195],v[4:7]\nM v[64:67],a[88:91],v[192:195],v[64:67]\nM v[68:71],a[92:95],v[192:195],v[68:71]\nM v[8:11],a[80:83],v[196:199],v[8:11]\nM v[12:15],a[84:87],v[196:199],v[12:15]\nM v[72:75],a[88:91],v[196:199],v[72:75]\nM v[76:79],a[92:95],v[196:199],v[76:79]\nM v[16:19],a[80:83],v[200:203],v[16:19]\n" \
"M v[20:23],a[84:87],v[200:203],v[20:23]\nM v[80:83],a[88:91],v[200:203],v[80:83]\nbuffer_store_dwordx4 v[148:151],v240,s[52:55],s74 offen offset:1024 sc1\nM v[84:87],a[92:95],v[200:203],v[84:87]\ns_waitcnt lgkmcnt(4)\nM v[24:27],a[80:83],v[204:207],v[24:27]\ns_add_u32 m0,s64,131072\ns_add_u32 s69,s68,3145728\n" \
"M v[28:31],a[84:87],v[204:207],v[28:31]\nbuffer_store_dwordx4 v[224:227],v253,s[52:55],s78 offen offset:1024 sc1\nM v[88:91],a[88:91],v[204:207],v[88:91]\ns_waitcnt vmcnt(15) lgkmcnt(0)\ns_barrier\nM v[92:95],a[92:95],v[204:207],v[92:95]\nds_read_b64_tr_b16 v[192:193],v241 offset:49152\nds_read_b64_tr_b16 v[194:195],v241 offset:49408\n" \
"M v[32:35],a[80:83],v[208:211],v[32:35]\nds_read_b64_tr_b16 v[196:197],v242 offset:49152\nds_read_b64_tr_b16 v[198:199],v242 offset:49408\nM v[36:39],a[84:87],v[208:211],v[36:39]\nds_read_b64_tr_b16 v[200:201],v241 offset:49664\nds_read_b64_tr_b16 v[202:203],v241 offset:49920\nM v[96:99],a[88:91],v[208:211],v[96:99]\nds_read_b64_tr_b16 v[204:205],v242 offset:49664\n" \
"ds_read_b64_tr_b16 v[206:207],v242 offset:49920\nM v[100:103],a[92:95],v[208:211],v[100:103]\nds_read_b64_tr_b16 v[208:209],v241 offset:50176\nds_read_b64_tr_b16 v[210:211],v241 offset:50432\nM v[40:43],a[80:83],v[212:215],v[40:43]\nbuffer_load_dwordx4 v243,s[40:43],s69 offen offset:0 lds\nM v[44:47],a[84:87],v[212:215],v[44:47]\nbuffer_load_dwordx4 v244,s[40:43],s69 offen offset:1024 lds\n" \
"M v[104:107],a[88:91],v[212:215],v[104:107]\nds_write_b128 v250,v[148:151] offset:5120\nM v[108:111],a[92:95],v[212:215],v[108:111]\nds_read_b64_tr_b16 v[212:213],v242 offset:50176\nds_read_b64_tr_b16 v[214:215],v242 offset:50432\nM v[48:51],a[80:83],v[216:219],v[48:51]\nM v[52:55],a[84:87],v[216:219],v[52:55]\nbuffer_load_dwordx4 v245,s[40:43],s69 offen offset:2048 lds\n" \
"M v[112:115],a[88:91],v[216:219],v[112:115]\nM v[116:119],a[92:95],v[216:219],v[116:119]\nds_read_b64_tr_b16 v[216:217],v241 offset:50688\nds_read_b64_tr_b16 v[218:219],v241 offset:50944\nM v[56:59],a[80:83],v[220:223],v[56:59]\nM v[60:63],a[84:87],v[220:223],v[60:63]\nM v[120:123],a[88:91],v[220:223],v[120:123]\nbuffer_load_dwordx4 v246,s[40:43],s69 offen offset:3072 lds\n" \
"M v[124:127],a[92:95],v[220:223],v[124:127]\nds_read_b64_tr_b16 v[220:221],v242 offset:50688\ns_waitcnt lgkmcnt(7)\nM v[0:3],a[96:99],v[192:195],v[0:3]\nds_read_b64_tr_b16 v[222:223],v242 offset:50944\nM v[4:7],a[100:103],v[192:195],v[4:7]\nM v[64:67],a[104:107],v[192:195],v[64:67]\nM v[68:71],a[108:111],v[192:195],v[68:71]\n" \
"ds_read_b64_tr_b16 v[192:193],v241 offset:57344\nds_read_b64_tr_b16 v[194:195],v241 offset:57600\nM v[8:11],a[96:99],v[196:199],v[8:11]\nM v[12:15],a[100:103],v[196:199],v[12:15]\nM v[72:75],a[104:107],v[196:199],v[72:75]\nM v[76:79],a[108:111],v[196:199],v[76:79]\nds_read_b64_tr_b16 v[196:197],v242 offset:57344\nds_read_b64_tr_b16 v[198:199],v242 offset:57600\n" \
"M v[16:19],a[96:99],v[200:203],v[16:19]\nbuffer_store_dwordx4 v[152:155],v240,s[52:55],s74 offen offset:2048 sc1\nM v[20:23],a[100:103],v[200:203],v[20:23]\nds_write_b128 v250,v[152:155] offset:6144\nM v[80:83],a[104:107],v[200:203],v[80:83]\nM v[84:87],a[108:111],v[200:203],v[84:87]\nbuffer_store_dwordx4 v[224:227],v253,s[52:55],s78 offen offset:2048 sc1\nM v[24:27],a[96:99],v[204:207],v[24:27]\n" \
"ds_read_b64_tr_b16 v[200:201],v241 offset:57856\nds_read_b64_tr_b16 v[202:203],v241 offset:58112\nM v[28:31],a[100:103],v[204:207],v[28:31]\nM v[88:91],a[104:107],v[204:207],v[88:91]\nM v[92:95],a[108:111],v[204:207],v[92:95]\nds_read_b64_tr_b16 v[204:205],v242 offset:57856\ns_waitcnt lgkmcnt(8)\nM v[32:35],a[96:99],v[208:211],v[32:35]\n" \
"ds_read_b64_tr_b16 v[206:207],v242 offset:58112\nM v[36:39],a[100:103],v[208:211],v[36:39]\nM v[96:99],a[104:107],v[208:211],v[96:99]\nM v[100:103],a[108:111],v[208:211],v[100:103]\nds_read_b64_tr_b16 v[208:209],v241 offset:58368\nds_read_b64_tr_b16 v[210:211],v241 offset:58624\nM v[40:43],a[96:99],v[212:215],v[40:43]\nM v[44:47],a[100:103],v[212:215],v[44:47]\n" \
"M v[104:107],a[104:107],v[212:215],v[104:107]\nM v[108:111],a[108:111],v[212:215],v[108:111]\nds_read_b64_tr_b16 v[212:213],v242 offset:58368\nds_read_b64_tr_b16 v[214:215],v242 offset:58624\nM v[48:51],a[96:99],v[216:219],v[48:51]\nM v[52:55],a[100:103],v[216:219],v[52:55]\nM v[112:115],a[104:107],v[216:219],v[112:115]\nM v[116:119],a[108:111],v[216:219],v[116:119]\n" \
"ds_read_b64_tr_b16 v[216:217],v241 offset:58880\nds_read_b64_tr_b16 v[218:219],v241 offset:59136\nM v[56:59],a[96:99],v[220:223],v[56:59]\nM v[60:63],a[100:103],v[220:223],v[60:63]\nM v[120:123],a[104:107],v[220:223],v[120:123]\nM v[124:127],a[108:111],v[220:223],v[124:127]\nds_read_b64_tr_b16 v[220:221],v242 offset:58880\ns_waitcnt lgkmcnt(8)\n" \
"M v[0:3],a[112:115],v[192:195],v[0:3]\nds_read_b64_tr_b16 v[222:223],v242 offset:59136\nM v[4:7],a[116:119],v[192:195],v[4:7]\nds_write_b128 v250,v[156:159] offset:7168\nM v[64:67],a[120:123],v[192:195],v[64:67]\nM v[68:71],a[124:127],v[192:195],v[68:71]\nM v[8:11],a[112:115],v[196:199],v[8:11]\nM v[12:15],a[116:119],v[196:199],v[12:15]\n" \
"M v[72:75],a[120:123],v[196:199],v[72:75]\nbuffer_store_dwordx4 v[156:159],v240,s[52:55],s74 offen offset:3072 sc1\nM v[76:79],a[124:127],v[196:199],v[76:79]\ns_add_u32 s74,s74,4096\nM v[16:19],a[112:115],v[200:203],v[16:19]\nM v[20:23],a[116:119],v[200:203],v[20:23]\nbuffer_store_dwordx4 v[224:227],v253,s[52:55],s78 offen offset:3072 sc1\nM v[80:83],a[120:123],v[200:203],v[80:83]\n" \
"M v[84:87],a[124:127],v[200:203],v[84:87]\ns_waitcnt lgkmcnt(3)\nM v[24:27],a[112:115],v[204:207],v[24:27]\ns_add_u32 m0,s64,147456\ns_add_u32 s69,s68,3670016\nM v[28:31],a[116:119],v[204:207],v[28:31]\nM v[88:91],a[120:123],v[204:207],v[88:91]\ns_waitcnt vmcnt(19) lgkmcnt(1)\n" \
"s_barrier\nM v[92:95],a[124:127],v[204:207],v[92:95]\nds_read_b64_tr_b16 v[192:193],v241 offset:0\nds_read_b64_tr_b16 v[194:195],v241 offset:256\nM v[32:35],a[112:115],v[208:211],v[32:35]\nds_read_b64_tr_b16 v[196:197],v242 offset:0\nds_read_b64_tr_b16 v[198:199],v242 offset:256\nM v[36:39],a[116:119],v[208:211],v[36:39]\n" \
"ds_read_b64_tr_b16 v[200:201],v241 offset:512\nds_read_b64_tr_b16 v[202:203],v241 offset:768\nM v[96:99],a[120:123],v[208:211],v[96:99]\nds_read_b64_tr_b16 v[204:205],v242 offset:512\nds_read_b64_tr_b16 v[206:207],v242 offset:768\nM v[100:103],a[124:127],v[208:211],v[100:103]\nds_read_b64_tr_b16 v[208:209],v241 offset:1024\nds_read_b64_tr_b16 v[210:211],v241 offset:1280\n" \
"M v[40:43],a[112:115],v[212:215],v[40:43]\nbuffer_load_dwordx4 v243,s[40:43],s69 offen offset:0 lds\nM v[44:47],a[116:119],v[212:215],v[44:47]\nbuffer_load_dwordx4 v244,s[40:43],s69 offen offset:1024 lds\nM v[104:107],a[120:123],v[212:215],v[104:107]\nM v[108:111],a[124:127],v[212:215],v[108:111]\nds_read_b64_tr_b16 v[212:213],v242 offset:1024\nds_read_b64_tr_b16 v[214:215],v242 offset:1280\n" \
"M v[48:51],a[112:115],v[216:219],v[48:51]\nds_write_b128 v250,v[160:163] offset:8192\nM v[52:55],a[116:119],v[216:219],v[52:55]\nbuffer_load_dwordx4 v245,s[40:43],s69 offen offset:2048 lds\nM v[112:115],a[120:123],v[216:219],v[112:115]\nM v[116:119],a[124:127],v[216:219],v[116:119]\nds_read_b64_tr_b16 v[216:217],v241 offset:1536\nds_read_b64_tr_b16 v[218:219],v241 offset:1792\n" \
"M v[56:59],a[112:115],v[220:223],v[56:59]\nM v[60:63],a[116:119],v[220:223],v[60:63]\nM v[120:123],a[120:123],v[220:223],v[120:123]\nbuffer_load_dwordx4 v246,s[40:43],s69 offen offset:3072 lds\nM v[124:127],a[124:127],v[220:223],v[124:127]\nds_read_b64_tr_b16 v[220:221],v242 offset:1536\ns_waitcnt lgkmcnt(7)\nM v[0:3],a[128:131],v[192:195],v[0:3]\n" \
"ds_read_b64_tr_b16 v[222:223],v242 offset:1792\nM v[4:7],a[132:135],v[192:195],v[4:7]\nM v[64:67],a[136:139],v[192:195],v[64:67]\nM v[68:71],a[140:143],v[192:195],v[68:71]\nds_read_b64_tr_b16 v[192:193],v241 offset:8192\nds_read_b64_tr_b16 v[194:195],v241 offset:8448\nM v[8:11],a[128:131],v[196:199],v[8:11]\nbuffer_store_dwordx4 v[160:163],v240,s[52:55],s74 offen offset:0 sc1\n" \
"M v[12:15],a[132:135],v[196:199],v[12:15]\nM v[72:75],a[136:139],v[196:199],v[72:75]\nM v[76:79],a[140:143],v[196:199],v[76:79]\nbuffer_store_dwordx4 v[224:227],v253,s[52:55],s72 offen offset:0 sc1\nM v[16:19],a[128:131],v[200:203],v[16:19]\nds_read_b64_tr_b16 v[196:197],v242 offset:8192\nds_read_b64_tr_b16 v[198:199],v242 offset:8448\nM v[20:23],a[132:135],v[200:203],v[20:23]\n" \
"M v[80:83],a[136:139],v[200:203],v[80:83]\nM v[84:87],a[140:143],v[200:203],v[84:87]\nds_read_b64_tr_b16 v[200:201],v241 offset:8704\nds_read_b64_tr_b16 v[202:203],v241 offset:8960\nM v[24:27],a[128:131],v[204:207],v[24:27]\nM v[28:31],a[132:135],v[204:207],v[28:31]\nM v[88:91],a[136:139],v[204:207],v[88:91]\nM v[92:95],a[140:143],v[204:207],v[92:95]\n" \
"ds_read_b64_tr_b16 v[204:205],v242 offset:8704\ns_waitcnt lgkmcnt(7)\nM v[32:35],a[128:131],v[208:211],v[32:35]\nds_read_b64_tr_b16 v[206:207],v242 offset:8960\nM v[36:39],a[132:135],v[208:211],v[36:39]\nds_write_b128 v250,v[164:167] offset:9216\nM v[96:99],a[136:139],v[208:211],v[96:99]\nM v[100:103],a[140:143],v[208:211],v[100:103]\n" \
"ds_read_b64_tr_b16 v[208:209],v241 offset:9216\nds_read_b64_tr_b16 v[210:211],v241 offset:9472\nM v[40:43],a[128:131],v[212:215],v[40:43]\nM v[44:47],a[132:135],v[212:215],v[44:47]\nM v[104:107],a[136:139],v[212:215],v[104:107]\nbuffer_load_dword v248,v228,s[52:55],s80 offen sc1\nM v[108:111],a[140:143],v[212:215],v[108:111]\nds_read_b64_tr_b16 v[212:213],v242 offset:9216\n" \
"ds_read_b64_tr_b16 v[214:215],v242 offset:9472\nM v[48:51],a[128:131],v[216:219],v[48:51]\nM v[52:55],a[132:135],v[216:219],v[52:55]\nM v[112:115],a[136:139],v[216:219],v[112:115]\nM v[116:119],a[140:143],v[216:219],v[116:119]\nds_read_b64_tr_b16 v[216:217],v241 offset:9728\nds_read_b64_tr_b16 v[218:219],v241 offset:9984\nM v[56:59],a[128:131],v[220:223],v[56:59]\n" \
"M v[60:63],a[132:135],v[220:223],v[60:63]\nM v[120:123],a[136:139],v[220:223],v[120:123]\nM v[124:127],a[140:143],v[220:223],v[124:127]\nds_read_b64_tr_b16 v[220:221],v242 offset:9728\ns_waitcnt lgkmcnt(8)\nM v[0:3],a[144:147],v[192:195],v[0:3]\nds_read_b64_tr_b16 v[222:223],v242 offset:9984\nM v[4:7],a[148:151],v[192:195],v[4:7]\n" \
"M v[64:67],a[152:155],v[192:195],v[64:67]\nbuffer_store_dwordx4 v[164:167],v240,s[52:55],s74 offen offset:1024 sc1\nM v[68:71],a[156:159],v[192:195],v[68:71]\nM v[8:11],a[144:147],v[196:199],v[8:11]\nM v[12:15],a[148:151],v[196:199],v[12:15]\nbuffer_store_dwordx4 v[224:227],v253,s[52:55],s72 offen offset:1024 sc1\nM v[72:75],a[152:155],v[196:199],v[72:75]\nM v[76:79],a[156:159],v[196:199],v[76:79]\n" \
"M v[16:19],a[144:147],v[200:203],v[16:19]\nds_write_b128 v250,v[168:171] offset:10240\nM v[20:23],a[148:151],v[200:203],v[20:23]\nM v[80:83],a[152:155],v[200:203],v[80:83]\nM v[84:87],a[156:159],v[200:203],v[84:87]\nM v[24:27],a[144:147],v[204:207],v[24:27]\ns_sub_u32 s40,s12,0x1000\ns_subb_u32 s41,s13,0\n" \
"M v[28:31],a[148:151],v[204:207],v[28:31]\ns_add_u32 m0,s64,98304\ns_add_u32 s69,s68,0\nM v[88:91],a[152:155],v[204:207],v[88:91]\ns_waitcnt vmcnt(21) lgkmcnt(1)\ns_barrier\nM v[92:95],a[156:159],v[204:207],v[92:95]\nds_read_b64_tr_b16 v[192:193],v241 offset:16384\n" \
"ds_read_b64_tr_b16 v[194:195],v241 offset:16640\nM v[32:35],a[144:147],v[208:211],v[32:35]\nds_read_b64_tr_b16 v[196:197],v242 offset:16384\nds_read_b64_tr_b16 v[198:199],v242 offset:16640\nM v[36:39],a[148:151],v[208:211],v[36:39]\nds_read_b64_tr_b16 v[200:201],v241 offset:16896\nds_read_b64_tr_b16 v[202:203],v241 offset:17152\nM v[96:99],a[152:155],v[208:211],v[96:99]\n" \
"ds_read_b64_tr_b16 v[204:205],v242 offset:16896\nds_read_b64_tr_b16 v[206:207],v242 offset:17152\nM v[100:103],a[156:159],v[208:211],v[100:103]\nds_read_b64_tr_b16 v[208:209],v241 offset:17408\nds_read_b64_tr_b16 v[210:211],v241 offset:17664\nM v[40:43],a[144:147],v[212:215],v[40:43]\nbuffer_load_dwordx4 v243,s[40:43],s69 offen offset:0 lds\nM v[44:47],a[148:151],v[212:215],v[44:47]\n" \
"buffer_load_dwordx4 v244,s[40:43],s69 offen offset:1024 lds\nM v[104:107],a[152:155],v[212:215],v[104:107]\nM v[108:111],a[156:159],v[212:215],v[108:111]\nds_read_b64_tr_b16 v[212:213],v242 offset:17408\nds_read_b64_tr_b16 v[214:215],v242 offset:17664\nM v[48:51],a[144:147],v[216:219],v[48:51]\nM v[52:55],a[148:151],v[216:219],v[52:55]\nbuffer_load_dwordx4 v245,s[40:43],s69 offen offset:2048 lds\n" \
"M v[112:115],a[152:155],v[216:219],v[112:115]\nM v[116:119],a[156:159],v[216:219],v[116:119]\nds_read_b64_tr_b16 v[216:217],v241 offset:17920\nds_read_b64_tr_b16 v[218:219],v241 offset:18176\nM v[56:59],a[144:147],v[220:223],v[56:59]\nM v[60:63],a[148:151],v[220:223],v[60:63]\nM v[120:123],a[152:155],v[220:223],v[120:123]\nbuffer_load_dwordx4 v246,s[40:43],s69 offen offset:3072 lds\n" \
"M v[124:127],a[156:159],v[220:223],v[124:127]\nds_read_b64_tr_b16 v[220:221],v242 offset:17920\ns_waitcnt lgkmcnt(7)\nM v[0:3],a[160:163],v[192:195],v[0:3]\nbuffer_store_dwordx4 v[168:171],v240,s[52:55],s74 offen offset:2048 sc1\nM v[4:7],a[164:167],v[192:195],v[4:7]\nds_read_b64_tr_b16 v[222:223],v242 offset:18176\nM v[64:67],a[168:171],v[192:195],v[64:67]\n" \
"ds_write_b128 v250,v[172:175] offset:11264\nM v[68:71],a[172:175],v[192:195],v[68:71]\nbuffer_store_dwordx4 v[224:227],v253,s[52:55],s72 offen offset:2048 sc1\nM v[8:11],a[160:163],v[196:199],v[8:11]\nds_read_b64_tr_b16 v[192:193],v241 offset:24576\nds_read_b64_tr_b16 v[194:195],v241 offset:24832\nM v[12:15],a[164:167],v[196:199],v[12:15]\nM v[72:75],a[168:171],v[196:199],v[72:75]\n" \
"M v[76:79],a[172:175],v[196:199],v[76:79]\nds_read_b64_tr_b16 v[196:197],v242 offset:24576\nds_read_b64_tr_b16 v[198:199],v242 offset:24832\nM v[16:19],a[160:163],v[200:203],v[16:19]\nM v[20:23],a[164:167],v[200:203],v[20:23]\nM v[80:83],a[168:171],v[200:203],v[80:83]\nM v[84:87],a[172:175],v[200:203],v[84:87]\nds_read_b64_tr_b16 v[200:201],v241 offset:25088\n" \
"ds_read_b64_tr_b16 v[202:203],v241 offset:25344\nM v[24:27],a[160:163],v[204:207],v[24:27]\nM v[28:31],a[164:167],v[204:207],v[28:31]\ns_waitcnt vmcnt(8)\nv_cmp_eq_u32 vcc,0,v248\nM v[88:91],a[168:171],v[204:207],v[88:91]\ns_cbranch_vccnz .Lps0\n.Lpb0:\n" \
"s_lshl_b32 s80,s63,7\nM v[92:95],a[172:175],v[204:207],v[92:95]\nds_read_b64_tr_b16 v[204:205],v242 offset:25088\ns_waitcnt lgkmcnt(8)\nM v[32:35],a[160:163],v[208:211],v[32:35]\nds_read_b64_tr_b16 v[206:207],v242 offset:25344\ns_add_u32 s80,s80,81920\nM v[36:39],a[164:167],v[208:211],v[36:39]\n" \
"buffer_load_dwordx4 a[0:3],v240,s[52:55],s76 offen offset:0 sc1\nM v[96:99],a[168:171],v[208:211],v[96:99]\nbuffer_load_dwordx4 a[4:7],v240,s[52:55],s76 offen offset:1024 sc1\nM v[100:103],a[172:175],v[208:211],v[100:103]\nds_read_b64_tr_b16 v[208:209],v241 offset:25600\nds_read_b64_tr_b16 v[210:211],v241 offset:25856\nM v[40:43],a[160:163],v[212:215],v[40:43]\nbuffer_load_dwordx4 a[8:11],v240,s[52:55],s77 offen offset:0 sc1\n" \
"M v[44:47],a[164:167],v[212:215],v[44:47]\nv_and_b32 v228,31,v249\nv_lshlrev_b32 v228,2,v228\nM v[104:107],a[168:171],v[212:215],v[104:107]\nv_mov_b32 v251,v228\nM v[108:111],a[172:175],v[212:215],v[108:111]\nds_read_b64_tr_b16 v[212:213],v242 offset:25600\nds_read_b64_tr_b16 v[214:215],v242 offset:25856\n" \
"M v[48:51],a[160:163],v[216:219],v[48:51]\nds_write_b128 v250,v[176:179] offset:12288\nM v[52:55],a[164:167],v[216:219],v[52:55]\nM v[112:115],a[168:171],v[216:219],v[112:115]\nbuffer_load_dwordx4 a[12:15],v240,s[52:55],s77 offen offset:1024 sc1\nM v[116:119],a[172:175],v[216:219],v[116:119]\nds_read_b64_tr_b16 v[216:217],v241 offset:26112\nds_read_b64_tr_b16 v[218:219],v241 offset:26368\n" \
"M v[56:59],a[160:163],v[220:223],v[56:59]\nM v[60:63],a[164:167],v[220:223],v[60:63]\nM v[120:123],a[168:171],v[220:223],v[120:123]\nbuffer_store_dwordx4 v[172:175],v240,s[52:55],s74 offen offset:3072 sc1\nM v[124:127],a[172:175],v[220:223],v[124:127]\nds_read_b64_tr_b16 v[220:221],v242 offset:26112\ns_waitcnt lgkmcnt(6)\nM v[0:3],a[176:179],v[192:195],v[0:3]\n" \
"ds_read_b64_tr_b16 v[222:223],v242 offset:26368\ns_add_u32 s74,s74,4096\nM v[4:7],a[180:183],v[192:195],v[4:7]\nbuffer_store_dwordx4 v[224:227],v253,s[52:55],s72 offen offset:3072 sc1\nM v[64:67],a[184:187],v[192:195],v[64:67]\nbuffer_load_dwordx4 a[16:19],v240,s[52:55],s76 offen offset:2048 sc1\nM v[68:71],a[188:191],v[192:195],v[68:71]\ns_cmp_eq_u32 s36,s96\n" \
"s_cselect_b32 s72,0xc000,s98\ns_add_u32 s65,s36,s72\nM v[8:11],a[176:179],v[196:199],v[8:11]\ns_cmp_eq_u32 s65,s96\ns_cselect_b32 s72,0xc000,s98\ns_add_u32 s66,s65,s72\nM v[12:15],a[180:183],v[196:199],v[12:15]\ns_cmp_eq_u32 s66,s96\n" \
"s_cselect_b32 s72,0xc000,s98\ns_add_u32 s95,s66,s72\nM v[72:75],a[184:187],v[196:199],v[72:75]\nM v[76:79],a[188:191],v[196:199],v[76:79]\nM v[16:19],a[176:179],v[200:203],v[16:19]\nM v[20:23],a[180:183],v[200:203],v[20:23]\nM v[80:83],a[184:187],v[200:203],v[80:83]\nbuffer_load_dwordx4 a[20:23],v240,s[52:55],s76 offen offset:3072 sc1\n" \
"M v[84:87],a[188:191],v[200:203],v[84:87]\ns_add_u32 s76,s76,4096\nM v[24:27],a[176:179],v[204:207],v[24:27]\ns_add_u32 m0,s64,114688\ns_add_u32 s69,s68,524288\nM v[28:31],a[180:183],v[204:207],v[28:31]\nM v[88:91],a[184:187],v[204:207],v[88:91]\ns_waitcnt lgkmcnt(0)\n" \
"s_barrier\nM v[92:95],a[188:191],v[204:207],v[92:95]\nds_read_b64_tr_b16 v[192:193],v241 offset:32768\nds_read_b64_tr_b16 v[194:195],v241 offset:33024\nM v[32:35],a[176:179],v[208:211],v[32:35]\nds_read_b64_tr_b16 v[196:197],v242 offset:32768\nds_read_b64_tr_b16 v[198:199],v242 offset:33024\nM v[36:39],a[180:183],v[208:211],v[36:39]\n" \
"ds_read_b64_tr_b16 v[200:201],v241 offset:33280\nds_read_b64_tr_b16 v[202:203],v241 offset:33536\nM v[96:99],a[184:187],v[208:211],v[96:99]\nds_read_b64_tr_b16 v[204:205],v242 offset:33280\nds_read_b64_tr_b16 v[206:207],v242 offset:33536\nM v[100:103],a[188:191],v[208:211],v[100:103]\nds_read_b64_tr_b16 v[208:209],v241 offset:33792\nds_read_b64_tr_b16 v[210:211],v241 offset:34048\n" \
"M v[40:43],a[176:179],v[212:215],v[40:43]\nbuffer_load_dwordx4 a[24:27],v240,s[52:55],s77 offen offset:2048 sc1\nM v[44:47],a[180:183],v[212:215],v[44:47]\nbuffer_load_dwordx4 v243,s[40:43],s69 offen offset:0 lds\nM v[104:107],a[184:187],v[212:215],v[104:107]\nbuffer_load_dwordx4 v244,s[40:43],s69 offen offset:1024 lds\nM v[108:111],a[188:191],v[212:215],v[108:111]\nds_read_b64_tr_b16 v[212:213],v242 offset:33792\n" \
"ds_read_b64_tr_b16 v[214:215],v242 offset:34048\nM v[48:51],a[176:179],v[216:219],v[48:51]\nds_write_b128 v250,v[180:183] offset:13312\nM v[52:55],a[180:183],v[216:219],v[52:55]\nbuffer_load_dwordx4 v245,s[40:43],s69 offen offset:2048 lds\nM v[112:115],a[184:187],v[216:219],v[112:115]\nbuffer_load_dwordx4 a[28:31],v240,s[52:55],s77 offen offset:3072 sc1\nM v[116:119],a[188:191],v[216:219],v[116:119]\n" \
"ds_read_b64_tr_b16 v[216:217],v241 offset:34304\nds_read_b64_tr_b16 v[218:219],v241 offset:34560\nM v[56:59],a[176:179],v[220:223],v[56:59]\nbuffer_store_dwordx4 v[176:179],v240,s[52:55],s74 offen offset:0 sc1\nM v[60:63],a[180:183],v[220:223],v[60:63]\ns_add_u32 s77,s77,4096\nM v[120:123],a[184:187],v[220:223],v[120:123]\nbuffer_load_dwordx4 v246,s[40:43],s69 offen offset:3072 lds\n" \
"M v[124:127],a[188:191],v[220:223],v[124:127]\nds_read_b64_tr_b16 v[220:221],v242 offset:34304\ns_waitcnt lgkmcnt(6)\nM v[0:3],a[192:195],v[192:195],v[0:3]\nds_read_b64_tr_b16 v[222:223],v242 offset:34560\nM v[4:7],a[196:199],v[192:195],v[4:7]\nM v[64:67],a[200:203],v[192:195],v[64:67]\nbuffer_load_dwordx4 a[32:35],v240,s[52:55],s76 offen offset:0 sc1\n" \
"M v[68:71],a[204:207],v[192:195],v[68:71]\nds_read_b64_tr_b16 v[192:193],v241 offset:40960\nds_read_b64_tr_b16 v[194:195],v241 offset:41216\nM v[8:11],a[192:195],v[196:199],v[8:11]\nM v[12:15],a[196:199],v[196:199],v[12:15]\nM v[72:75],a[200:203],v[196:199],v[72:75]\nM v[76:79],a[204:207],v[196:199],v[76:79]\nds_read_b64_tr_b16 v[196:197],v242 offset:40960\n" \
"ds_read_b64_tr_b16 v[198:199],v242 offset:41216\nM v[16:19],a[192:195],v[200:203],v[16:19]\nds_write_b128 v250,v[184:187] offset:14336\nM v[20:23],a[196:199],v[200:203],v[20:23]\nM v[80:83],a[200:203],v[200:203],v[80:83]\nbuffer_load_dwordx4 a[36:39],v240,s[52:55],s76 offen offset:1024 sc1\nM v[84:87],a[204:207],v[200:203],v[84:87]\nds_read_b64_tr_b16 v[200:201],v241 offset:41472\n" \
"ds_read_b64_tr_b16 v[202:203],v241 offset:41728\nM v[24:27],a[192:195],v[204:207],v[24:27]\nM v[28:31],a[196:199],v[204:207],v[28:31]\nM v[88:91],a[200:203],v[204:207],v[88:91]\nM v[92:95],a[204:207],v[204:207],v[92:95]\nds_read_b64_tr_b16 v[204:205],v242 offset:41472\nds_read_b64_tr_b16 v[206:207],v242 offset:41728\nM v[32:35],a[192:195],v[208:211],v[32:35]\n" \
"M v[36:39],a[196:199],v[208:211],v[36:39]\nM v[96:99],a[200:203],v[208:211],v[96:99]\nbuffer_load_dwordx4 a[40:43],v240,s[52:55],s77 offen offset:0 sc1\nM v[100:103],a[204:207],v[208:211],v[100:103]\nds_read_b64_tr_b16 v[208:209],v241 offset:41984\ns_waitcnt lgkmcnt(8)\nM v[40:43],a[192:195],v[212:215],v[40:43]\nds_read_b64_tr_b16 v[210:211],v241 offset:42240\n" \
"M v[44:47],a[196:199],v[212:215],v[44:47]\nM v[104:107],a[200:203],v[212:215],v[104:107]\nM v[108:111],a[204:207],v[212:215],v[108:111]\nds_read_b64_tr_b16 v[212:213],v242 offset:41984\nds_read_b64_tr_b16 v[214:215],v242 offset:42240\nM v[48:51],a[192:195],v[216:219],v[48:51]\nM v[52:55],a[196:199],v[216:219],v[52:55]\nM v[112:115],a[200:203],v[216:219],v[112:115]\n" \
"buffer_store_dwordx4 v[180:183],v240,s[52:55],s74 offen offset:1024 sc1\nM v[116:119],a[204:207],v[216:219],v[116:119]\nds_read_b64_tr_b16 v[216:217],v241 offset:42496\nds_read_b64_tr_b16 v[218:219],v241 offset:42752\nM v[56:59],a[192:195],v[220:223],v[56:59]\nbuffer_load_dwordx4 a[44:47],v240,s[52:55],s77 offen offset:1024 sc1\nM v[60:63],a[196:199],v[220:223],v[60:63]\nM v[120:123],a[200:203],v[220:223],v[120:123]\n" \
"M v[124:127],a[204:207],v[220:223],v[124:127]\nds_read_b64_tr_b16 v[220:221],v242 offset:42496\nds_read_b64_tr_b16 v[222:223],v242 offset:42752\nM v[0:3],a[208:211],v[192:195],v[0:3]\nds_write_b128 v250,v[188:191] offset:15360\nM v[4:7],a[212:215],v[192:195],v[4:7]\nv_add_u32 v250,65536,v250\nM v[64:67],a[216:219],v[192:195],v[64:67]\n" \
"buffer_load_dwordx4 a[48:51],v240,s[52:55],s76 offen offset:2048 sc1\nM v[68:71],a[220:223],v[192:195],v[68:71]\ns_waitcnt lgkmcnt(7)\nM v[8:11],a[208:211],v[196:199],v[8:11]\nM v[12:15],a[212:215],v[196:199],v[12:15]\nM v[72:75],a[216:219],v[196:199],v[72:75]\nM v[76:79],a[220:223],v[196:199],v[76:79]\nM v[16:19],a[208:211],v[200:203],v[16:19]\n" \
"M v[20:23],a[212:215],v[200:203],v[20:23]\nM v[80:83],a[216:219],v[200:203],v[80:83]\nbuffer_load_dwordx4 a[52:55],v240,s[52:55],s76 offen offset:3072 sc1\nM v[84:87],a[220:223],v[200:203],v[84:87]\ns_add_u32 s76,s76,4096\nM v[24:27],a[208:211],v[204:207],v[24:27]\ns_add_u32 m0,s64,131072\ns_add_u32 s69,s68,1048576\n" \
"M v[28:31],a[212:215],v[204:207],v[28:31]\nM v[88:91],a[216:219],v[204:207],v[88:91]\ns_waitcnt lgkmcnt(1)\ns_barrier\nM v[92:95],a[220:223],v[204:207],v[92:95]\nds_read_b64_tr_b16 v[192:193],v241 offset:49152\nds_read_b64_tr_b16 v[194:195],v241 offset:49408\nM v[32:35],a[208:211],v[208:211],v[32:35]\n" \
"ds_read_b64_tr_b16 v[196:197],v242 offset:49152\nds_read_b64_tr_b16 v[198:199],v242 offset:49408\nM v[36:39],a[212:215],v[208:211],v[36:39]\nds_read_b64_tr_b16 v[200:201],v241 offset:49664\nds_read_b64_tr_b16 v[202:203],v241 offset:49920\nM v[96:99],a[216:219],v[208:211],v[96:99]\nds_read_b64_tr_b16 v[204:205],v242 offset:49664\nds_read_b64_tr_b16 v[206:207],v242 offset:49920\n" \
"M v[100:103],a[220:223],v[208:211],v[100:103]\nds_read_b64_tr_b16 v[208:209],v241 offset:50176\nds_read_b64_tr_b16 v[210:211],v241 offset:50432\nM v[40:43],a[208:211],v[212:215],v[40:43]\nbuffer_load_dwordx4 a[56:59],v240,s[52:55],s77 offen offset:2048 sc1\nM v[44:47],a[212:215],v[212:215],v[44:47]\nbuffer_load_dwordx4 v243,s[40:43],s69 offen offset:0 lds\nM v[104:107],a[216:219],v[212:215],v[104:107]\n" \
"buffer_load_dwordx4 v244,s[40:43],s69 offen offset:1024 lds\nM v[108:111],a[220:223],v[212:215],v[108:111]\nds_read_b64_tr_b16 v[212:213],v242 offset:50176\nds_read_b64_tr_b16 v[214:215],v242 offset:50432\nM v[48:51],a[208:211],v[216:219],v[48:51]\nbuffer_store_dwordx4 v[184:187],v240,s[52:55],s74 offen offset:2048 sc1\nM v[52:55],a[212:215],v[216:219],v[52:55]\nbuffer_load_dwordx4 v245,s[40:43],s69 offen offset:2048 lds\n" \
"M v[112:115],a[216:219],v[216:219],v[112:115]\nbuffer_load_dwordx4 a[60:63],v240,s[52:55],s77 offen offset:3072 sc1\nM v[116:119],a[220:223],v[216:219],v[116:119]\nds_read_b64_tr_b16 v[216:217],v241 offset:50688\nds_read_b64_tr_b16 v[218:219],v241 offset:50944\nM v[56:59],a[208:211],v[220:223],v[56:59]\ns_add_u32 s77,s77,4096\nM v[60:63],a[212:215],v[220:223],v[60:63]\n" \
"M v[120:123],a[216:219],v[220:223],v[120:123]\nbuffer_load_dwordx4 v246,s[40:43],s69 offen offset:3072 lds\nM v[124:127],a[220:223],v[220:223],v[124:127]\nds_read_b64_tr_b16 v[220:221],v242 offset:50688\ns_waitcnt lgkmcnt(5)\nM v[0:3],a[224:227],v[192:195],v[0:3]\nds_read_b64_tr_b16 v[222:223],v242 offset:50944\nM v[4:7],a[228:231],v[192:195],v[4:7]\n" \
"M v[64:67],a[232:235],v[192:195],v[64:67]\nbuffer_load_dwordx4 a[64:67],v240,s[52:55],s76 offen offset:0 sc1\nM v[68:71],a[236:239],v[192:195],v[68:71]\nds_read_b64_tr_b16 v[192:193],v241 offset:57344\nds_read_b64_tr_b16 v[194:195],v241 offset:57600\nM v[8:11],a[224:227],v[196:199],v[8:11]\nM v[12:15],a[228:231],v[196:199],v[12:15]\nM v[72:75],a[232:235],v[196:199],v[72:75]\n" \
"M v[76:79],a[236:239],v[196:199],v[76:79]\nds_read_b64_tr_b16 v[196:197],v242 offset:57344\nds_read_b64_tr_b16 v[198:199],v242 offset:57600\nM v[16:19],a[224:227],v[200:203],v[16:19]\nM v[20:23],a[228:231],v[200:203],v[20:23]\nM v[80:83],a[232:235],v[200:203],v[80:83]\nbuffer_load_dwordx4 a[68:71],v240,s[52:55],s76 offen offset:1024 sc1\nM v[84:87],a[236:239],v[200:203],v[84:87]\n" \
"ds_read_b64_tr_b16 v[200:201],v241 offset:57856\nds_read_b64_tr_b16 v[202:203],v241 offset:58112\nM v[24:27],a[224:227],v[204:207],v[24:27]\nM v[28:31],a[228:231],v[204:207],v[28:31]\nM v[88:91],a[232:235],v[204:207],v[88:91]\nM v[92:95],a[236:239],v[204:207],v[92:95]\nds_read_b64_tr_b16 v[204:205],v242 offset:57856\nds_read_b64_tr_b16 v[206:207],v242 offset:58112\n" \
"M v[32:35],a[224:227],v[208:211],v[32:35]\nM v[36:39],a[228:231],v[208:211],v[36:39]\nM v[96:99],a[232:235],v[208:211],v[96:99]\nbuffer_load_dwordx4 a[72:75],v240,s[52:55],s77 offen offset:0 sc1\nM v[100:103],a[236:239],v[208:211],v[100:103]\nds_read_b64_tr_b16 v[208:209],v241 offset:58368\ns_waitcnt lgkmcnt(7)\nM v[40:43],a[224:227],v[212:215],v[40:43]\n" \
"ds_read_b64_tr_b16 v[210:211],v241 offset:58624\nM v[44:47],a[228:231],v[212:215],v[44:47]\nM v[104:107],a[232:235],v[212:215],v[104:107]\nbuffer_store_dwordx4 v[188:191],v240,s[52:55],s74 offen offset:3072 sc1\nM v[108:111],a[236:239],v[212:215],v[108:111]\nds_read_b64_tr_b16 v[212:213],v242 offset:58368\nds_read_b64_tr_b16 v[214:215],v242 offset:58624\nM v[48:51],a[224:227],v[216:219],v[48:51]\n" \
"s_add_u32 s74,s74,16764928\nM v[52:55],a[228:231],v[216:219],v[52:55]\nM v[112:115],a[232:235],v[216:219],v[112:115]\nbuffer_load_dwordx4 a[76:79],v240,s[52:55],s77 offen offset:1024 sc1\nM v[116:119],a[236:239],v[216:219],v[116:119]\nds_read_b64_tr_b16 v[216:217],v241 offset:58880\nds_read_b64_tr_b16 v[218:219],v241 offset:59136\nM v[56:59],a[224:227],v[220:223],v[56:59]\n" \
"M v[60:63],a[228:231],v[220:223],v[60:63]\nM v[120:123],a[232:235],v[220:223],v[120:123]\nM v[124:127],a[236:239],v[220:223],v[124:127]\nds_read_b64_tr_b16 v[220:221],v242 offset:58880\nds_read_b64_tr_b16 v[222:223],v242 offset:59136\nM v[0:3],a[240:243],v[192:195],v[0:3]\nM v[4:7],a[244:247],v[192:195],v[4:7]\nM v[64:67],a[248:251],v[192:195],v[64:67]\n" \
"buffer_load_dwordx4 a[80:83],v240,s[52:55],s76 offen offset:2048 sc1\nM v[68:71],a[252:255],v[192:195],v[68:71]\ns_waitcnt lgkmcnt(6)\nM v[8:11],a[240:243],v[196:199],v[8:11]\nM v[12:15],a[244:247],v[196:199],v[12:15]\nM v[72:75],a[248:251],v[196:199],v[72:75]\nv_cvt_pk_bf16_f32 v128,v0,v1\nv_cvt_pk_bf16_f32 v129,v2,v3\n" \
"M v[76:79],a[252:255],v[196:199],v[76:79]\nv_cvt_pk_bf16_f32 v130,v4,v5\nv_cvt_pk_bf16_f32 v131,v6,v7\nM v[16:19],a[240:243],v[200:203],v[16:19]\nv_cvt_pk_bf16_f32 v160,v64,v65\nv_cvt_pk_bf16_f32 v161,v66,v67\nM v[20:23],a[244:247],v[200:203],v[20:23]\nv_cvt_pk_bf16_f32 v162,v68,v69\n" \
"v_cvt_pk_bf16_f32 v163,v70,v71\nM v[80:83],a[248:251],v[200:203],v[80:83]\nv_cvt_pk_bf16_f32 v132,v8,v9\nv_cvt_pk_bf16_f32 v133,v10,v11\nM v[84:87],a[252:255],v[200:203],v[84:87]\nv_cvt_pk_bf16_f32 v134,v12,v13\nv_cvt_pk_bf16_f32 v135,v14,v15\nM v[24:27],a[240:243],v[204:207],v[24:27]\n" \
"v_cvt_pk_bf16_f32 v164,v72,v73\nv_cvt_pk_bf16_f32 v165,v74,v75\nM v[28:31],a[244:247],v[204:207],v[28:31]\nv_cvt_pk_bf16_f32 v166,v76,v77\nv_cvt_pk_bf16_f32 v167,v78,v79\nM v[88:91],a[248:251],v[204:207],v[88:91]\ns_waitcnt vmcnt(37) lgkmcnt(0)\ns_barrier\n" \
"M v[92:95],a[252:255],v[204:207],v[92:95]\nds_read_b64_tr_b16 v[192:193],v241 offset:0\nds_read_b64_tr_b16 v[194:195],v241 offset:256\nM v[32:35],a[240:243],v[208:211],v[32:35]\nds_read_b64_tr_b16 v[196:197],v242 offset:0\nds_read_b64_tr_b16 v[198:199],v242 offset:256\nM v[36:39],a[244:247],v[208:211],v[36:39]\nds_read_b64_tr_b16 v[200:201],v241 offset:512\n" \
"ds_read_b64_tr_b16 v[202:203],v241 offset:768\nM v[96:99],a[248:251],v[208:211],v[96:99]\nds_read_b64_tr_b16 v[204:205],v242 offset:512\nds_read_b64_tr_b16 v[206:207],v242 offset:768\nM v[100:103],a[252:255],v[208:211],v[100:103]\nds_read_b64_tr_b16 v[208:209],v241 offset:1024\nds_read_b64_tr_b16 v[210:211],v241 offset:1280\nM v[40:43],a[240:243],v[212:215],v[40:43]\n" \
"v_cvt_pk_bf16_f32 v136,v16,v17\nv_cvt_pk_bf16_f32 v137,v18,v19\nM v[44:47],a[244:247],v[212:215],v[44:47]\nv_cvt_pk_bf16_f32 v138,v20,v21\nv_cvt_pk_bf16_f32 v139,v22,v23\nM v[104:107],a[248:251],v[212:215],v[104:107]\nv_cvt_pk_bf16_f32 v168,v80,v81\nv_cvt_pk_bf16_f32 v169,v82,v83\n" \
"M v[108:111],a[252:255],v[212:215],v[108:111]\nds_read_b64_tr_b16 v[212:213],v242 offset:1024\nds_read_b64_tr_b16 v[214:215],v242 offset:1280\nM v[48:51],a[240:243],v[216:219],v[48:51]\nv_cvt_pk_bf16_f32 v170,v84,v85\nv_cvt_pk_bf16_f32 v171,v86,v87\nM v[52:55],a[244:247],v[216:219],v[52:55]\nv_cvt_pk_bf16_f32 v140,v24,v25\n" \
"v_cvt_pk_bf16_f32 v141,v26,v27\nM v[112:115],a[248:251],v[216:219],v[112:115]\nv_cvt_pk_bf16_f32 v142,v28,v29\nv_cvt_pk_bf16_f32 v143,v30,v31\nM v[116:119],a[252:255],v[216:219],v[116:119]\nv_cvt_pk_bf16_f32 v172,v88,v89\nv_cvt_pk_bf16_f32 v173,v90,v91\nM v[56:59],a[240:243],v[220:223],v[56:59]\n" \
"ds_read_b64_tr_b16 v[216:217],v241 offset:1536\nds_read_b64_tr_b16 v[218:219],v241 offset:1792\nM v[60:63],a[244:247],v[220:223],v[60:63]\nv_cvt_pk_bf16_f32 v174,v92,v93\nv_cvt_pk_bf16_f32 v175,v94,v95\nM v[120:123],a[248:251],v[220:223],v[120:123]\nv_cvt_pk_bf16_f32 v144,v32,v33\nv_cvt_pk_bf16_f32 v145,v34,v35\n" \
"M v[124:127],a[252:255],v[220:223],v[124:127]\nv_cvt_pk_bf16_f32 v146,v36,v37\ns_waitcnt vmcnt(34) lgkmcnt(4)\nM v[0:3],v[192:195],a[0:3],0\nv_cvt_pk_bf16_f32 v147,v38,v39\ns_waitcnt vmcnt(33)\nM v[4:7],v[192:195],a[4:7],0\nv_cvt_pk_bf16_f32 v176,v96,v97\n" \
"s_waitcnt vmcnt(32)\nM v[64:67],v[192:195],a[8:11],0\nv_cvt_pk_bf16_f32 v177,v98,v99\ns_waitcnt vmcnt(31)\nM v[68:71],v[192:195],a[12:15],0\nds_read_b64_tr_b16 v[220:221],v242 offset:1536\nds_read_b64_tr_b16 v[222:223],v242 offset:1792\nM v[8:11],v[196:199],a[0:3],0\n" \
"v_cvt_pk_bf16_f32 v178,v100,v101\nv_cvt_pk_bf16_f32 v179,v102,v103\nM v[12:15],v[196:199],a[4:7],0\nv_cvt_pk_bf16_f32 v148,v40,v41\nv_cvt_pk_bf16_f32 v149,v42,v43\nM v[72:75],v[196:199],a[8:11],0\nv_cvt_pk_bf16_f32 v150,v44,v45\nv_cvt_pk_bf16_f32 v151,v46,v47\n" \
"M v[76:79],v[196:199],a[12:15],0\nv_cvt_pk_bf16_f32 v180,v104,v105\nv_cvt_pk_bf16_f32 v181,v106,v107\nM v[16:19],v[200:203],a[0:3],0\nds_read_b64_tr_b16 v[192:193],v241 offset:8192\nds_read_b64_tr_b16 v[194:195],v241 offset:8448\nM v[20:23],v[200:203],a[4:7],0\nv_cvt_pk_bf16_f32 v182,v108,v109\n" \
"v_cvt_pk_bf16_f32 v183,v110,v111\nM v[80:83],v[200:203],a[8:11],0\nv_cvt_pk_bf16_f32 v152,v48,v49\nv_cvt_pk_bf16_f32 v153,v50,v51\nM v[84:87],v[200:203],a[12:15],0\nv_cvt_pk_bf16_f32 v154,v52,v53\nv_cvt_pk_bf16_f32 v155,v54,v55\nM v[24:27],v[204:207],a[0:3],0\n" \
"v_cvt_pk_bf16_f32 v184,v112,v113\nv_cvt_pk_bf16_f32 v185,v114,v115\nM v[28:31],v[204:207],a[4:7],0\nds_read_b64_tr_b16 v[196:197],v242 offset:8192\nds_read_b64_tr_b16 v[198:199],v242 offset:8448\nM v[88:91],v[204:207],a[8:11],0\nv_cvt_pk_bf16_f32 v186,v116,v117\nv_cvt_pk_bf16_f32 v187,v118,v119\n" \
"M v[92:95],v[204:207],a[12:15],0\nv_cvt_pk_bf16_f32 v156,v56,v57\nv_cvt_pk_bf16_f32 v157,v58,v59\nM v[32:35],v[208:211],a[0:3],0\nv_cvt_pk_bf16_f32 v158,v60,v61\nv_cvt_pk_bf16_f32 v159,v62,v63\nM v[36:39],v[208:211],a[4:7],0\nv_cvt_pk_bf16_f32 v188,v120,v121\n" \
"v_cvt_pk_bf16_f32 v189,v122,v123\nM v[96:99],v[208:211],a[8:11],0\nds_read_b64_tr_b16 v[200:201],v241 offset:8704\nds_read_b64_tr_b16 v[202:203],v241 offset:8960\nM v[100:103],v[208:211],a[12:15],0\nv_cvt_pk_bf16_f32 v190,v124,v125\ns_waitcnt lgkmcnt(4)\nM v[40:43],v[212:215],a[0:3],0\n" \
"buffer_store_dwordx4 v[128:131],v240,s[52:55],s74 offen offset:0 sc1\nM v[44:47],v[212:215],a[4:7],0\nv_cvt_pk_bf16_f32 v191,v126,v127\nds_read_b64_tr_b16 v[204:205],v242 offset:8704\nM v[104:107],v[212:215],a[8:11],0\nds_read_b64_tr_b16 v[206:207],v242 offset:8960\nds_read_b64_tr_b16 v[208:209],v241 offset:9216\nM v[108:111],v[212:215],a[12:15],0\n" \
"ds_read_b64_tr_b16 v[210:211],v241 offset:9472\nds_read_b64_tr_b16 v[212:213],v242 offset:9216\nM v[48:51],v[216:219],a[0:3],0\nds_read_b64_tr_b16 v[214:215],v242 offset:9472\ns_add_u32 m0,s64,147456\nM v[52:55],v[216:219],a[4:7],0\nbuffer_load_dwordx4 a[84:87],v240,s[52:55],s76 offen offset:3072 sc1\nM v[112:115],v[216:219],a[8:11],0\n" \
"buffer_load_dwordx4 a[88:91],v240,s[52:55],s77 offen offset:2048 sc1\nM v[116:119],v[216:219],a[12:15],0\nds_read_b64_tr_b16 v[216:217],v241 offset:9728\nds_read_b64_tr_b16 v[218:219],v241 offset:9984\nM v[56:59],v[220:223],a[0:3],0\nbuffer_load_dwordx4 a[92:95],v240,s[52:55],s77 offen offset:3072 sc1\nM v[60:63],v[220:223],a[4:7],0\ns_add_u32 s69,s68,1572864\n" \
"s_add_u32 s76,s76,4096\nM v[120:123],v[220:223],a[8:11],0\nbuffer_load_dwordx4 a[96:99],v240,s[52:55],s76 offen offset:0 sc1\nM v[124:127],v[220:223],a[12:15],0\nds_read_b64_tr_b16 v[220:221],v242 offset:9728\ns_waitcnt vmcnt(33)\nM v[0:3],v[192:195],a[16:19],v[0:3]\nds_read_b64_tr_b16 v[222:223],v242 offset:9984\n" \
"s_waitcnt vmcnt(32)\nM v[4:7],v[192:195],a[20:23],v[4:7]\ns_add_u32 s77,s77,4096\ns_waitcnt vmcnt(31)\nM v[64:67],v[192:195],a[24:27],v[64:67]\ns_waitcnt vmcnt(27)\nM v[68:71],v[192:195],a[28:31],v[68:71]\ns_waitcnt lgkmcnt(4)\n" \
"M v[8:11],v[196:199],a[16:19],v[8:11]\nbuffer_load_dwordx4 a[100:103],v240,s[52:55],s76 offen offset:1024 sc1\nM v[12:15],v[196:199],a[20:23],v[12:15]\nbuffer_load_dwordx4 a[104:107],v240,s[52:55],s77 offen offset:0 sc1\nM v[72:75],v[196:199],a[24:27],v[72:75]\nbuffer_load_dwordx4 a[108:111],v240,s[52:55],s77 offen offset:1024 sc1\nM v[76:79],v[196:199],a[28:31],v[76:79]\nbuffer_load_dwordx4 a[112:115],v240,s[52:55],s76 offen offset:2048 sc1\n" \
"M v[16:19],v[200:203],a[16:19],v[16:19]\nbuffer_load_dwordx4 v243,s[40:43],s69 offen offset:0 lds\nM v[20:23],v[200:203],a[20:23],v[20:23]\nbuffer_load_dwordx4 v244,s[40:43],s69 offen offset:1024 lds\nM v[80:83],v[200:203],a[24:27],v[80:83]\nbuffer_load_dwordx4 v245,s[40:43],s69 offen offset:2048 lds\nM v[84:87],v[200:203],a[28:31],v[84:87]\nbuffer_load_dwordx4 v246,s[40:43],s69 offen offset:3072 lds\n" \
"M v[24:27],v[204:207],a[16:19],v[24:27]\nbuffer_load_dwordx4 a[116:119],v240,s[52:55],s76 offen offset:3072 sc1\nM v[28:31],v[204:207],a[20:23],v[28:31]\ns_add_u32 m0,s64,98304\ns_add_u32 s69,s68,2097152\nM v[88:91],v[204:207],a[24:27],v[88:91]\ns_waitcnt vmcnt(34) lgkmcnt(0)\ns_barrier\n" \
"M v[92:95],v[204:207],a[28:31],v[92:95]\nbuffer_store_dwordx4 v[132:135],v240,s[52:55],s74 offen offset:1024 sc1\nM v[32:35],v[208:211],a[16:19],v[32:35]\nds_read_b64_tr_b16 v[192:193],v241 offset:16384\nds_read_b64_tr_b16 v[194:195],v241 offset:16640\nM v[36:39],v[208:211],a[20:23],v[36:39]\nds_read_b64_tr_b16 v[196:197],v242 offset:16384\nds_read_b64_tr_b16 v[198:199],v242 offset:16640\n" \
"M v[96:99],v[208:211],a[24:27],v[96:99]\nds_read_b64_tr_b16 v[200:201],v241 offset:16896\nds_read_b64_tr_b16 v[202:203],v241 offset:17152\nM v[100:103],v[208:211],a[28:31],v[100:103]\nds_read_b64_tr_b16 v[204:205],v242 offset:16896\nds_read_b64_tr_b16 v[206:207],v242 offset:17152\nM v[40:43],v[212:215],a[16:19],v[40:43]\nds_read_b64_tr_b16 v[208:209],v241 offset:17408\n" \
"ds_read_b64_tr_b16 v[210:211],v241 offset:17664\nM v[44:47],v[212:215],a[20:23],v[44:47]\nbuffer_load_dwordx4 a[120:123],v240,s[52:55],s77 offen offset:2048 sc1\nM v[104:107],v[212:215],a[24:27],v[104:107]\ns_add_u32 s76,s76,4096\nM v[108:111],v[212:215],a[28:31],v[108:111]\nds_read_b64_tr_b16 v[212:213],v242 offset:17408\nds_read_b64_tr_b16 v[214:215],v242 offset:17664\n" \
"M v[48:51],v[216:219],a[16:19],v[48:51]\nbuffer_load_dwordx4 v243,s[40:43],s69 offen offset:0 lds\nM v[52:55],v[216:219],a[20:23],v[52:55]\nbuffer_load_dwordx4 v244,s[40:43],s69 offen offset:1024 lds\nM v[112:115],v[216:219],a[24:27],v[112:115]\nbuffer_load_dwordx4 a[124:127],v240,s[52:55],s77 offen offset:3072 sc1\nM v[116:119],v[216:219],a[28:31],v[116:119]\nds_read_b64_tr_b16 v[216:217],v241 offset:17920\n" \
"ds_read_b64_tr_b16 v[218:219],v241 offset:18176\nM v[56:59],v[220:223],a[16:19],v[56:59]\ns_add_u32 s77,s77,4096\nM v[60:63],v[220:223],a[20:23],v[60:63]\nbuffer_load_dwordx4 v245,s[40:43],s69 offen offset:2048 lds\nM v[120:123],v[220:223],a[24:27],v[120:123]\nbuffer_load_dwordx4 v246,s[40:43],s69 offen offset:3072 lds\nM v[124:127],v[220:223],a[28:31],v[124:127]\n" \
"ds_read_b64_tr_b16 v[220:221],v242 offset:17920\ns_waitcnt vmcnt(40) lgkmcnt(5)\nM v[0:3],v[192:195],a[32:35],v[0:3]\nds_read_b64_tr_b16 v[222:223],v242 offset:18176\ns_waitcnt vmcnt(39)\nM v[4:7],v[192:195],a[36:39],v[4:7]\ns_waitcnt vmcnt(23)\nbuffer_store_dword v254,off,s[52:55],s29 sc1\n" \
"M v[64:67],v[192:195],a[40:43],v[64:67]\nbuffer_load_dwordx4 a[128:131],v240,s[52:55],s76 offen offset:0 sc1\nM v[68:71],v[192:195],a[44:47],v[68:71]\nds_read_b64_tr_b16 v[192:193],v241 offset:24576\nds_read_b64_tr_b16 v[194:195],v241 offset:24832\nM v[8:11],v[196:199],a[32:35],v[8:11]\ns_add_u32 s29,s29,4096\nM v[12:15],v[196:199],a[36:39],v[12:15]\n" \
"M v[72:75],v[196:199],a[40:43],v[72:75]\nM v[76:79],v[196:199],a[44:47],v[76:79]\nds_read_b64_tr_b16 v[196:197],v242 offset:24576\nds_read_b64_tr_b16 v[198:199],v242 offset:24832\nM v[16:19],v[200:203],a[32:35],v[16:19]\nbuffer_store_dwordx4 v[136:139],v240,s[52:55],s74 offen offset:2048 sc1\nM v[20:23],v[200:203],a[36:39],v[20:23]\nM v[80:83],v[200:203],a[40:43],v[80:83]\n" \
"buffer_load_dwordx4 a[132:135],v240,s[52:55],s76 offen offset:1024 sc1\nM v[84:87],v[200:203],a[44:47],v[84:87]\nds_read_b64_tr_b16 v[200:201],v241 offset:25088\nds_read_b64_tr_b16 v[202:203],v241 offset:25344\nM v[24:27],v[204:207],a[32:35],v[24:27]\nM v[28:31],v[204:207],a[36:39],v[28:31]\nM v[88:91],v[204:207],a[40:43],v[88:91]\nM v[92:95],v[204:207],a[44:47],v[92:95]\n" \
"ds_read_b64_tr_b16 v[204:205],v242 offset:25088\nds_read_b64_tr_b16 v[206:207],v242 offset:25344\nM v[32:35],v[208:211],a[32:35],v[32:35]\nM v[36:39],v[208:211],a[36:39],v[36:39]\nM v[96:99],v[208:211],a[40:43],v[96:99]\nbuffer_load_dwordx4 a[136:139],v240,s[52:55],s77 offen offset:0 sc1\nM v[100:103],v[208:211],a[44:47],v[100:103]\nds_read_b64_tr_b16 v[208:209],v241 offset:25600\n" \
"s_waitcnt lgkmcnt(7)\nM v[40:43],v[212:215],a[32:35],v[40:43]\nds_read_b64_tr_b16 v[210:211],v241 offset:25856\nM v[44:47],v[212:215],a[36:39],v[44:47]\nM v[104:107],v[212:215],a[40:43],v[104:107]\nM v[108:111],v[212:215],a[44:47],v[108:111]\nds_read_b64_tr_b16 v[212:213],v242 offset:25600\nds_read_b64_tr_b16 v[214:215],v242 offset:25856\n" \
"M v[48:51],v[216:219],a[32:35],v[48:51]\nM v[52:55],v[216:219],a[36:39],v[52:55]\nM v[112:115],v[216:219],a[40:43],v[112:115]\nbuffer_load_dwordx4 a[140:143],v240,s[52:55],s77 offen offset:1024 sc1\nM v[116:119],v[216:219],a[44:47],v[116:119]\nds_read_b64_tr_b16 v[216:217],v241 offset:26112\nds_read_b64_tr_b16 v[218:219],v241 offset:26368\nM v[56:59],v[220:223],a[32:35],v[56:59]\n" \
"M v[60:63],v[220:223],a[36:39],v[60:63]\nM v[120:123],v[220:223],a[40:43],v[120:123]\nM v[124:127],v[220:223],a[44:47],v[124:127]\nds_read_b64_tr_b16 v[220:221],v242 offset:26112\nds_read_b64_tr_b16 v[222:223],v242 offset:26368\nM v[0:3],v[192:195],a[48:51],v[0:3]\nM v[4:7],v[192:195],a[52:55],v[4:7]\nM v[64:67],v[192:195],a[56:59],v[64:67]\n" \
"buffer_store_dwordx4 v[140:143],v240,s[52:55],s74 offen offset:3072 sc1\nM v[68:71],v[192:195],a[60:63],v[68:71]\ns_add_u32 s74,s74,4096\ns_waitcnt lgkmcnt(6)\nM v[8:11],v[196:199],a[48:51],v[8:11]\nbuffer_load_dwordx4 a[144:147],v240,s[52:55],s76 offen offset:2048 sc1\nM v[12:15],v[196:199],a[52:55],v[12:15]\nM v[72:75],v[196:199],a[56:59],v[72:75]\n" \
"M v[76:79],v[196:199],a[60:63],v[76:79]\nM v[16:19],v[200:203],a[48:51],v[16:19]\nM v[20:23],v[200:203],a[52:55],v[20:23]\nM v[80:83],v[200:203],a[56:59],v[80:83]\nbuffer_load_dwordx4 a[148:151],v240,s[52:55],s76 offen offset:3072 sc1\nM v[84:87],v[200:203],a[60:63],v[84:87]\ns_add_u32 s76,s76,4096\nM v[24:27],v[204:207],a[48:51],v[24:27]\n" \
"s_add_u32 m0,s64,114688\ns_add_u32 s69,s68,2621440\nM v[28:31],v[204:207],a[52:55],v[28:31]\nM v[88:91],v[204:207],a[56:59],v[88:91]\ns_waitcnt lgkmcnt(0)\ns_barrier\nM v[92:95],v[204:207],a[60:63],v[92:95]\nds_read_b64_tr_b16 v[192:193],v241 offset:32768\n" \
"ds_read_b64_tr_b16 v[194:195],v241 offset:33024\nM v[32:35],v[208:211],a[48:51],v[32:35]\nds_read_b64_tr_b16 v[196:197],v242 offset:32768\nds_read_b64_tr_b16 v[198:199],v242 offset:33024\nM v[36:39],v[208:211],a[52:55],v[36:39]\nds_read_b64_tr_b16 v[200:201],v241 offset:33280\nds_read_b64_tr_b16 v[202:203],v241 offset:33536\nM v[96:99],v[208:211],a[56:59],v[96:99]\n" \
"ds_read_b64_tr_b16 v[204:205],v242 offset:33280\nds_read_b64_tr_b16 v[206:207],v242 offset:33536\nM v[100:103],v[208:211],a[60:63],v[100:103]\nds_read_b64_tr_b16 v[208:209],v241 offset:33792\nds_read_b64_tr_b16 v[210:211],v241 offset:34048\nM v[40:43],v[212:215],a[48:51],v[40:43]\nbuffer_load_dwordx4 a[152:155],v240,s[52:55],s77 offen offset:2048 sc1\nM v[44:47],v[212:215],a[52:55],v[44:47]\n" \
"buffer_load_dwordx4 v243,s[40:43],s69 offen offset:0 lds\nM v[104:107],v[212:215],a[56:59],v[104:107]\nbuffer_load_dwordx4 v244,s[40:43],s69 offen offset:1024 lds\nM v[108:111],v[212:215],a[60:63],v[108:111]\nds_read_b64_tr_b16 v[212:213],v242 offset:33792\nds_read_b64_tr_b16 v[214:215],v242 offset:34048\nM v[48:51],v[216:219],a[48:51],v[48:51]\nM v[52:55],v[216:219],a[52:55],v[52:55]\n" \
"buffer_load_dwordx4 v245,s[40:43],s69 offen offset:2048 lds\nM v[112:115],v[216:219],a[56:59],v[112:115]\nbuffer_load_dwordx4 a[156:159],v240,s[52:55],s77 offen offset:3072 sc1\nM v[116:119],v[216:219],a[60:63],v[116:119]\nds_read_b64_tr_b16 v[216:217],v241 offset:34304\nds_read_b64_tr_b16 v[218:219],v241 offset:34560\nM v[56:59],v[220:223],a[48:51],v[56:59]\nbuffer_store_dwordx4 v[144:147],v240,s[52:55],s74 offen offset:0 sc1\n" \
"M v[60:63],v[220:223],a[52:55],v[60:63]\ns_add_u32 s77,s77,4096\nM v[120:123],v[220:223],a[56:59],v[120:123]\nbuffer_load_dwordx4 v246,s[40:43],s69 offen offset:3072 lds\nM v[124:127],v[220:223],a[60:63],v[124:127]\nds_read_b64_tr_b16 v[220:221],v242 offset:34304\ns_waitcnt lgkmcnt(5)\nM v[0:3],v[192:195],a[64:67],v[0:3]\n" \
"ds_read_b64_tr_b16 v[222:223],v242 offset:34560\nM v[4:7],v[192:195],a[68:71],v[4:7]\nM v[64:67],v[192:195],a[72:75],v[64:67]\ns_waitcnt vmcnt(38)\nM v[68:71],v[192:195],a[76:79],v[68:71]\nds_read_b64_tr_b16 v[192:193],v241 offset:40960\nds_read_b64_tr_b16 v[194:195],v241 offset:41216\nM v[8:11],v[196:199],a[64:67],v[8:11]\n" \
"buffer_load_dwordx4 a[160:163],v240,s[52:55],s76 offen offset:0 sc1\nM v[12:15],v[196:199],a[68:71],v[12:15]\nM v[72:75],v[196:199],a[72:75],v[72:75]\nM v[76:79],v[196:199],a[76:79],v[76:79]\nds_read_b64_tr_b16 v[196:197],v242 offset:40960\nds_read_b64_tr_b16 v[198:199],v242 offset:41216\nM v[16:19],v[200:203],a[64:67],v[16:19]\nM v[20:23],v[200:203],a[68:71],v[20:23]\n" \
"M v[80:83],v[200:203],a[72:75],v[80:83]\nbuffer_load_dwordx4 a[164:167],v240,s[52:55],s76 offen offset:1024 sc1\nM v[84:87],v[200:203],a[76:79],v[84:87]\nds_read_b64_tr_b16 v[200:201],v241 offset:41472\nds_read_b64_tr_b16 v[202:203],v241 offset:41728\nM v[24:27],v[204:207],a[64:67],v[24:27]\nM v[28:31],v[204:207],a[68:71],v[28:31]\nM v[88:91],v[204:207],a[72:75],v[88:91]\n" \
"M v[92:95],v[204:207],a[76:79],v[92:95]\nds_read_b64_tr_b16 v[204:205],v242 offset:41472\nds_read_b64_tr_b16 v[206:207],v242 offset:41728\nM v[32:35],v[208:211],a[64:67],v[32:35]\nM v[36:39],v[208:211],a[68:71],v[36:39]\nM v[96:99],v[208:211],a[72:75],v[96:99]\nbuffer_load_dwordx4 a[168:171],v240,s[52:55],s77 offen offset:0 sc1\nM v[100:103],v[208:211],a[76:79],v[100:103]\n" \
"ds_read_b64_tr_b16 v[208:209],v241 offset:41984\ns_waitcnt lgkmcnt(7)\nM v[40:43],v[212:215],a[64:67],v[40:43]\nds_read_b64_tr_b16 v[210:211],v241 offset:42240\nM v[44:47],v[212:215],a[68:71],v[44:47]\nM v[104:107],v[212:215],a[72:75],v[104:107]\nbuffer_store_dwordx4 v[148:151],v240,s[52:55],s74 offen offset:1024 sc1\nM v[108:111],v[212:215],a[76:79],v[108:111]\n" \
"ds_read_b64_tr_b16 v[212:213],v242 offset:41984\nds_read_b64_tr_b16 v[214:215],v242 offset:42240\nM v[48:51],v[216:219],a[64:67],v[48:51]\nM v[52:55],v[216:219],a[68:71],v[52:55]\nM v[112:115],v[216:219],a[72:75],v[112:115]\nbuffer_load_dwordx4 a[172:175],v240,s[52:55],s77 offen offset:1024 sc1\nM v[116:119],v[216:219],a[76:79],v[116:119]\nds_read_b64_tr_b16 v[216:217],v241 offset:42496\n" \
"ds_read_b64_tr_b16 v[218:219],v241 offset:42752\nM v[56:59],v[220:223],a[64:67],v[56:59]\nM v[60:63],v[220:223],a[68:71],v[60:63]\nM v[120:123],v[220:223],a[72:75],v[120:123]\nM v[124:127],v[220:223],a[76:79],v[124:127]\nds_read_b64_tr_b16 v[220:221],v242 offset:42496\ns_waitcnt vmcnt(42)\nM v[0:3],v[192:195],a[80:83],v[0:3]\n" \
"ds_read_b64_tr_b16 v[222:223],v242 offset:42752\ns_waitcnt vmcnt(40)\nM v[4:7],v[192:195],a[84:87],v[4:7]\ns_waitcnt vmcnt(39)\nM v[64:67],v[192:195],a[88:91],v[64:67]\ns_waitcnt vmcnt(38)\nM v[68:71],v[192:195],a[92:95],v[68:71]\ns_waitcnt lgkmcnt(6)\n" \
"M v[8:11],v[196:199],a[80:83],v[8:11]\nbuffer_load_dwordx4 a[176:179],v240,s[52:55],s76 offen offset:2048 sc1\nM v[12:15],v[196:199],a[84:87],v[12:15]\nM v[72:75],v[196:199],a[88:91],v[72:75]\nM v[76:79],v[196:199],a[92:95],v[76:79]\nM v[16:19],v[200:203],a[80:83],v[16:19]\nM v[20:23],v[200:203],a[84:87],v[20:23]\nM v[80:83],v[200:203],a[88:91],v[80:83]\n" \
"buffer_load_dwordx4 a[180:183],v240,s[52:55],s76 offen offset:3072 sc1\nM v[84:87],v[200:203],a[92:95],v[84:87]\ns_add_u32 s76,s76,4096\nM v[24:27],v[204:207],a[80:83],v[24:27]\ns_add_u32 m0,s64,131072\ns_add_u32 s69,s68,3145728\nM v[28:31],v[204:207],a[84:87],v[28:31]\nM v[88:91],v[204:207],a[88:91],v[88:91]\n" \
"s_waitcnt vmcnt(31) lgkmcnt(0)\ns_barrier\nM v[92:95],v[204:207],a[92:95],v[92:95]\nds_read_b64_tr_b16 v[192:193],v241 offset:49152\nds_read_b64_tr_b16 v[194:195],v241 offset:49408\nM v[32:35],v[208:211],a[80:83],v[32:35]\nbuffer_store_dwordx4 v[152:155],v240,s[52:55],s74 offen offset:2048 sc1\nM v[36:39],v[208:211],a[84:87],v[36:39]\n" \
"ds_read_b64_tr_b16 v[196:197],v242 offset:49152\nds_read_b64_tr_b16 v[198:199],v242 offset:49408\nM v[96:99],v[208:211],a[88:91],v[96:99]\nds_read_b64_tr_b16 v[200:201],v241 offset:49664\nds_read_b64_tr_b16 v[202:203],v241 offset:49920\nM v[100:103],v[208:211],a[92:95],v[100:103]\nds_read_b64_tr_b16 v[204:205],v242 offset:49664\nds_read_b64_tr_b16 v[206:207],v242 offset:49920\n" \
"M v[40:43],v[212:215],a[80:83],v[40:43]\nds_read_b64_tr_b16 v[208:209],v241 offset:50176\nds_read_b64_tr_b16 v[210:211],v241 offset:50432\nM v[44:47],v[212:215],a[84:87],v[44:47]\nbuffer_load_dwordx4 a[184:187],v240,s[52:55],s77 offen offset:2048 sc1\nM v[104:107],v[212:215],a[88:91],v[104:107]\nbuffer_load_dwordx4 v243,s[40:43],s69 offen offset:0 lds\nM v[108:111],v[212:215],a[92:95],v[108:111]\n" \
"ds_read_b64_tr_b16 v[212:213],v242 offset:50176\nds_read_b64_tr_b16 v[214:215],v242 offset:50432\nM v[48:51],v[216:219],a[80:83],v[48:51]\nbuffer_load_dwordx4 v244,s[40:43],s69 offen offset:1024 lds\nM v[52:55],v[216:219],a[84:87],v[52:55]\nbuffer_load_dwordx4 v245,s[40:43],s69 offen offset:2048 lds\nM v[112:115],v[216:219],a[88:91],v[112:115]\nbuffer_load_dwordx4 a[188:191],v240,s[52:55],s77 offen offset:3072 sc1\n" \
"M v[116:119],v[216:219],a[92:95],v[116:119]\nds_read_b64_tr_b16 v[216:217],v241 offset:50688\nds_read_b64_tr_b16 v[218:219],v241 offset:50944\nM v[56:59],v[220:223],a[80:83],v[56:59]\ns_add_u32 s77,s77,4096\nM v[60:63],v[220:223],a[84:87],v[60:63]\nM v[120:123],v[220:223],a[88:91],v[120:123]\nbuffer_load_dwordx4 v246,s[40:43],s69 offen offset:3072 lds\n" \
"M v[124:127],v[220:223],a[92:95],v[124:127]\nds_read_b64_tr_b16 v[220:221],v242 offset:50688\ns_waitcnt lgkmcnt(5)\nM v[0:3],v[192:195],a[96:99],v[0:3]\nds_read_b64_tr_b16 v[222:223],v242 offset:50944\nM v[4:7],v[192:195],a[100:103],v[4:7]\nM v[64:67],v[192:195],a[104:107],v[64:67]\nbuffer_load_dwordx4 a[192:195],v240,s[52:55],s76 offen offset:0 sc1\n" \
"M v[68:71],v[192:195],a[108:111],v[68:71]\nds_read_b64_tr_b16 v[192:193],v241 offset:57344\nds_read_b64_tr_b16 v[194:195],v241 offset:57600\nM v[8:11],v[196:199],a[96:99],v[8:11]\nM v[12:15],v[196:199],a[100:103],v[12:15]\nM v[72:75],v[196:199],a[104:107],v[72:75]\nM v[76:79],v[196:199],a[108:111],v[76:79]\nds_read_b64_tr_b16 v[196:197],v242 offset:57344\n" \
"ds_read_b64_tr_b16 v[198:199],v242 offset:57600\nM v[16:19],v[200:203],a[96:99],v[16:19]\nM v[20:23],v[200:203],a[100:103],v[20:23]\nM v[80:83],v[200:203],a[104:107],v[80:83]\nbuffer_store_dwordx4 v[156:159],v240,s[52:55],s74 offen offset:3072 sc1\nM v[84:87],v[200:203],a[108:111],v[84:87]\nds_read_b64_tr_b16 v[200:201],v241 offset:57856\nds_read_b64_tr_b16 v[202:203],v241 offset:58112\n" \
"M v[24:27],v[204:207],a[96:99],v[24:27]\ns_add_u32 s74,s74,4096\nM v[28:31],v[204:207],a[100:103],v[28:31]\nbuffer_load_dwordx4 a[196:199],v240,s[52:55],s76 offen offset:1024 sc1\nM v[88:91],v[204:207],a[104:107],v[88:91]\nM v[92:95],v[204:207],a[108:111],v[92:95]\nds_read_b64_tr_b16 v[204:205],v242 offset:57856\nds_read_b64_tr_b16 v[206:207],v242 offset:58112\n" \
"M v[32:35],v[208:211],a[96:99],v[32:35]\nM v[36:39],v[208:211],a[100:103],v[36:39]\nM v[96:99],v[208:211],a[104:107],v[96:99]\nbuffer_load_dwordx4 a[200:203],v240,s[52:55],s77 offen offset:0 sc1\nM v[100:103],v[208:211],a[108:111],v[100:103]\nds_read_b64_tr_b16 v[208:209],v241 offset:58368\ns_waitcnt lgkmcnt(7)\nM v[40:43],v[212:215],a[96:99],v[40:43]\n" \
"ds_read_b64_tr_b16 v[210:211],v241 offset:58624\nM v[44:47],v[212:215],a[100:103],v[44:47]\nM v[104:107],v[212:215],a[104:107],v[104:107]\nM v[108:111],v[212:215],a[108:111],v[108:111]\nds_read_b64_tr_b16 v[212:213],v242 offset:58368\nds_read_b64_tr_b16 v[214:215],v242 offset:58624\nM v[48:51],v[216:219],a[96:99],v[48:51]\nM v[52:55],v[216:219],a[100:103],v[52:55]\n" \
"M v[112:115],v[216:219],a[104:107],v[112:115]\nbuffer_load_dwordx4 a[204:207],v240,s[52:55],s77 offen offset:1024 sc1\nM v[116:119],v[216:219],a[108:111],v[116:119]\nds_read_b64_tr_b16 v[216:217],v241 offset:58880\nds_read_b64_tr_b16 v[218:219],v241 offset:59136\nM v[56:59],v[220:223],a[96:99],v[56:59]\nM v[60:63],v[220:223],a[100:103],v[60:63]\nM v[120:123],v[220:223],a[104:107],v[120:123]\n" \
"M v[124:127],v[220:223],a[108:111],v[124:127]\nds_read_b64_tr_b16 v[220:221],v242 offset:58880\nds_read_b64_tr_b16 v[222:223],v242 offset:59136\nM v[0:3],v[192:195],a[112:115],v[0:3]\ns_waitcnt vmcnt(42)\nM v[4:7],v[192:195],a[116:119],v[4:7]\ns_waitcnt vmcnt(40)\nM v[64:67],v[192:195],a[120:123],v[64:67]\n" \
"s_waitcnt vmcnt(37)\nM v[68:71],v[192:195],a[124:127],v[68:71]\ns_waitcnt lgkmcnt(6)\nM v[8:11],v[196:199],a[112:115],v[8:11]\nbuffer_store_dwordx4 v[160:163],v240,s[52:55],s74 offen offset:0 sc1\nM v[12:15],v[196:199],a[116:119],v[12:15]\nbuffer_load_dwordx4 a[208:211],v240,s[52:55],s76 offen offset:2048 sc1\nM v[72:75],v[196:199],a[120:123],v[72:75]\n" \
"M v[76:79],v[196:199],a[124:127],v[76:79]\nM v[16:19],v[200:203],a[112:115],v[16:19]\nM v[20:23],v[200:203],a[116:119],v[20:23]\nM v[80:83],v[200:203],a[120:123],v[80:83]\nbuffer_load_dwordx4 a[212:215],v240,s[52:55],s76 offen offset:3072 sc1\nM v[84:87],v[200:203],a[124:127],v[84:87]\ns_add_u32 s76,s76,4096\nM v[24:27],v[204:207],a[112:115],v[24:27]\n" \
"s_add_u32 m0,s64,147456\ns_add_u32 s69,s68,3670016\nM v[28:31],v[204:207],a[116:119],v[28:31]\ns_lshl_b32 s68,s60,19\ns_lshl2_add_u32 s68,s64,s68\nM v[88:91],v[204:207],a[120:123],v[88:91]\ns_waitcnt vmcnt(38) lgkmcnt(0)\ns_barrier\n" \
"M v[92:95],v[204:207],a[124:127],v[92:95]\nds_read_b64_tr_b16 v[192:193],v241 offset:0\nds_read_b64_tr_b16 v[194:195],v241 offset:256\nM v[32:35],v[208:211],a[112:115],v[32:35]\nds_read_b64_tr_b16 v[196:197],v242 offset:0\nds_read_b64_tr_b16 v[198:199],v242 offset:256\nM v[36:39],v[208:211],a[116:119],v[36:39]\nds_read_b64_tr_b16 v[200:201],v241 offset:512\n" \
"ds_read_b64_tr_b16 v[202:203],v241 offset:768\nM v[96:99],v[208:211],a[120:123],v[96:99]\nds_read_b64_tr_b16 v[204:205],v242 offset:512\nds_read_b64_tr_b16 v[206:207],v242 offset:768\nM v[100:103],v[208:211],a[124:127],v[100:103]\nds_read_b64_tr_b16 v[208:209],v241 offset:1024\nds_read_b64_tr_b16 v[210:211],v241 offset:1280\nM v[40:43],v[212:215],a[112:115],v[40:43]\n" \
"buffer_load_dwordx4 a[216:219],v240,s[52:55],s77 offen offset:2048 sc1\nM v[44:47],v[212:215],a[116:119],v[44:47]\nbuffer_load_dwordx4 v243,s[40:43],s69 offen offset:0 lds\nM v[104:107],v[212:215],a[120:123],v[104:107]\nbuffer_load_dwordx4 v244,s[40:43],s69 offen offset:1024 lds\nM v[108:111],v[212:215],a[124:127],v[108:111]\nds_read_b64_tr_b16 v[212:213],v242 offset:1024\nds_read_b64_tr_b16 v[214:215],v242 offset:1280\n" \
"M v[48:51],v[216:219],a[112:115],v[48:51]\nv_add_u32 v243,4096,v230\nv_add_u32 v244,3200,v230\nM v[52:55],v[216:219],a[116:119],v[52:55]\nbuffer_load_dwordx4 v245,s[40:43],s69 offen offset:2048 lds\nM v[112:115],v[216:219],a[120:123],v[112:115]\nbuffer_load_dwordx4 a[220:223],v240,s[52:55],s77 offen offset:3072 sc1\nM v[116:119],v[216:219],a[124:127],v[116:119]\n" \
"ds_read_b64_tr_b16 v[216:217],v241 offset:1536\nds_read_b64_tr_b16 v[218:219],v241 offset:1792\nM v[56:59],v[220:223],a[112:115],v[56:59]\ns_add_u32 s77,s77,4096\nv_add_u32 v245,10240,v230\nM v[60:63],v[220:223],a[116:119],v[60:63]\ns_add_u32 s68,s68,s70\nM v[120:123],v[220:223],a[120:123],v[120:123]\n" \
"buffer_store_dwordx4 v[164:167],v240,s[52:55],s74 offen offset:1024 sc1\nM v[124:127],v[220:223],a[124:127],v[124:127]\nds_read_b64_tr_b16 v[220:221],v242 offset:1536\ns_waitcnt vmcnt(42) lgkmcnt(5)\nM v[0:3],v[192:195],a[128:131],v[0:3]\nds_read_b64_tr_b16 v[222:223],v242 offset:1792\ns_waitcnt vmcnt(40)\nM v[4:7],v[192:195],a[132:135],v[4:7]\n" \
"s_waitcnt vmcnt(39)\nM v[64:67],v[192:195],a[136:139],v[64:67]\ns_waitcnt vmcnt(38)\nM v[68:71],v[192:195],a[140:143],v[68:71]\nds_read_b64_tr_b16 v[192:193],v241 offset:8192\nds_read_b64_tr_b16 v[194:195],v241 offset:8448\nM v[8:11],v[196:199],a[128:131],v[8:11]\nbuffer_load_dwordx4 a[224:227],v240,s[52:55],s76 offen offset:0 sc1\n" \
"M v[12:15],v[196:199],a[132:135],v[12:15]\nbuffer_load_dwordx4 v246,s[40:43],s69 offen offset:3072 lds\nM v[72:75],v[196:199],a[136:139],v[72:75]\nv_add_u32 v246,9344,v230\nv_mov_b32 v230,0x3f803f80\nM v[76:79],v[196:199],a[140:143],v[76:79]\nds_read_b64_tr_b16 v[196:197],v242 offset:8192\nds_read_b64_tr_b16 v[198:199],v242 offset:8448\n" \
"M v[16:19],v[200:203],a[128:131],v[16:19]\ns_sub_u32 s40,s18,0x1000\ns_subb_u32 s41,s19,0\nM v[20:23],v[200:203],a[132:135],v[20:23]\nM v[80:83],v[200:203],a[136:139],v[80:83]\nbuffer_load_dwordx4 a[228:231],v240,s[52:55],s76 offen offset:1024 sc1\nM v[84:87],v[200:203],a[140:143],v[84:87]\nds_read_b64_tr_b16 v[200:201],v241 offset:8704\n" \
"ds_read_b64_tr_b16 v[202:203],v241 offset:8960\nM v[24:27],v[204:207],a[128:131],v[24:27]\nM v[28:31],v[204:207],a[132:135],v[28:31]\nM v[88:91],v[204:207],a[136:139],v[88:91]\nM v[92:95],v[204:207],a[140:143],v[92:95]\nds_read_b64_tr_b16 v[204:205],v242 offset:8704\nds_read_b64_tr_b16 v[206:207],v242 offset:8960\nM v[32:35],v[208:211],a[128:131],v[32:35]\n" \
"M v[36:39],v[208:211],a[132:135],v[36:39]\nM v[96:99],v[208:211],a[136:139],v[96:99]\nbuffer_load_dwordx4 a[232:235],v240,s[52:55],s77 offen offset:0 sc1\nM v[100:103],v[208:211],a[140:143],v[100:103]\nds_read_b64_tr_b16 v[208:209],v241 offset:9216\ns_waitcnt lgkmcnt(7)\nM v[40:43],v[212:215],a[128:131],v[40:43]\nds_read_b64_tr_b16 v[210:211],v241 offset:9472\n" \
"ds_read_b128 a[0:3],v240 offset:0\nM v[44:47],v[212:215],a[132:135],v[44:47]\nbuffer_load_dword v248,v228,s[52:55],s80 offen sc1\nM v[104:107],v[212:215],a[136:139],v[104:107]\nM v[108:111],v[212:215],a[140:143],v[108:111]\nds_read_b64_tr_b16 v[212:213],v242 offset:9216\nds_read_b64_tr_b16 v[214:215],v242 offset:9472\nM v[48:51],v[216:219],a[128:131],v[48:51]\n" \
"buffer_store_dwordx4 v[168:171],v240,s[52:55],s74 offen offset:2048 sc1\nM v[52:55],v[216:219],a[132:135],v[52:55]\nM v[112:115],v[216:219],a[136:139],v[112:115]\nbuffer_load_dwordx4 a[236:239],v240,s[52:55],s77 offen offset:1024 sc1\nM v[116:119],v[216:219],a[140:143],v[116:119]\nds_read_b64_tr_b16 v[216:217],v241 offset:9728\nds_read_b64_tr_b16 v[218:219],v241 offset:9984\nM v[56:59],v[220:223],a[128:131],v[56:59]\n" \
"ds_read_b128 a[4:7],v240 offset:1024\nM v[60:63],v[220:223],a[132:135],v[60:63]\nM v[120:123],v[220:223],a[136:139],v[120:123]\nM v[124:127],v[220:223],a[140:143],v[124:127]\nds_read_b64_tr_b16 v[220:221],v242 offset:9728\ns_waitcnt vmcnt(43)\nM v[0:3],v[192:195],a[144:147],v[0:3]\nds_read_b64_tr_b16 v[222:223],v242 offset:9984\n" \
"s_waitcnt vmcnt(42)\nM v[4:7],v[192:195],a[148:151],v[4:7]\nds_read_b128 a[8:11],v240 offset:2048\ns_waitcnt vmcnt(41)\nM v[64:67],v[192:195],a[152:155],v[64:67]\ns_waitcnt vmcnt(37)\nM v[68:71],v[192:195],a[156:159],v[68:71]\ns_waitcnt lgkmcnt(8)\n" \
"M v[8:11],v[196:199],a[144:147],v[8:11]\nbuffer_load_dwordx4 a[240:243],v240,s[52:55],s76 offen offset:2048 sc1\nM v[12:15],v[196:199],a[148:151],v[12:15]\nM v[72:75],v[196:199],a[152:155],v[72:75]\nds_read_b128 a[12:15],v240 offset:3072\nM v[76:79],v[196:199],a[156:159],v[76:79]\nM v[16:19],v[200:203],a[144:147],v[16:19]\nM v[20:23],v[200:203],a[148:151],v[20:23]\n" \
"M v[80:83],v[200:203],a[152:155],v[80:83]\nbuffer_load_dwordx4 a[244:247],v240,s[52:55],s76 offen offset:3072 sc1\nM v[84:87],v[200:203],a[156:159],v[84:87]\nM v[24:27],v[204:207],a[144:147],v[24:27]\nds_read_b128 a[16:19],v240 offset:4096\nM v[28:31],v[204:207],a[148:151],v[28:31]\nM v[88:91],v[204:207],a[152:155],v[88:91]\ns_waitcnt vmcnt(37) lgkmcnt(3)\n" \
"s_barrier\nM v[92:95],v[204:207],a[156:159],v[92:95]\nds_read_b64_tr_b16 v[192:193],v241 offset:16384\nds_read_b64_tr_b16 v[194:195],v241 offset:16640\nM v[32:35],v[208:211],a[144:147],v[32:35]\nds_read_b64_tr_b16 v[196:197],v242 offset:16384\nds_read_b64_tr_b16 v[198:199],v242 offset:16640\nM v[36:39],v[208:211],a[148:151],v[36:39]\n" \
"ds_read_b64_tr_b16 v[200:201],v241 offset:16896\nds_read_b64_tr_b16 v[202:203],v241 offset:17152\nM v[96:99],v[208:211],a[152:155],v[96:99]\nbuffer_store_dwordx4 v[172:175],v240,s[52:55],s74 offen offset:3072 sc1\nM v[100:103],v[208:211],a[156:159],v[100:103]\nds_read_b64_tr_b16 v[204:205],v242 offset:16896\nds_read_b64_tr_b16 v[206:207],v242 offset:17152\nM v[40:43],v[212:215],a[144:147],v[40:43]\n" \
"ds_read_b64_tr_b16 v[208:209],v241 offset:17408\nds_read_b64_tr_b16 v[210:211],v241 offset:17664\nM v[44:47],v[212:215],a[148:151],v[44:47]\ns_add_u32 s74,s74,4096\nds_read_b128 a[20:23],v240 offset:5120\nM v[104:107],v[212:215],a[152:155],v[104:107]\nbuffer_load_dwordx4 a[248:251],v240,s[52:55],s77 offen offset:2048 sc1\nM v[108:111],v[212:215],a[156:159],v[108:111]\n" \
"ds_read_b64_tr_b16 v[212:213],v242 offset:17408\nds_read_b64_tr_b16 v[214:215],v242 offset:17664\nM v[48:51],v[216:219],a[144:147],v[48:51]\nds_read_b128 a[24:27],v240 offset:6144\nM v[52:55],v[216:219],a[148:151],v[52:55]\nM v[112:115],v[216:219],a[152:155],v[112:115]\nbuffer_load_dwordx4 a[252:255],v240,s[52:55],s77 offen offset:3072 sc1\nM v[116:119],v[216:219],a[156:159],v[116:119]\n" \
"ds_read_b64_tr_b16 v[216:217],v241 offset:17920\nds_read_b64_tr_b16 v[218:219],v241 offset:18176\nM v[56:59],v[220:223],a[144:147],v[56:59]\nM v[60:63],v[220:223],a[148:151],v[60:63]\nM v[120:123],v[220:223],a[152:155],v[120:123]\nds_read_b128 a[28:31],v240 offset:7168\nM v[124:127],v[220:223],a[156:159],v[124:127]\nds_read_b64_tr_b16 v[220:221],v242 offset:17920\n" \
"s_waitcnt vmcnt(39) lgkmcnt(11)\nM v[0:3],v[192:195],a[160:163],v[0:3]\nds_read_b64_tr_b16 v[222:223],v242 offset:18176\ns_waitcnt vmcnt(38)\nM v[4:7],v[192:195],a[164:167],v[4:7]\ns_waitcnt vmcnt(37)\nM v[64:67],v[192:195],a[168:171],v[64:67]\ns_waitcnt vmcnt(35)\n" \
"M v[68:71],v[192:195],a[172:175],v[68:71]\nds_read_b64_tr_b16 v[192:193],v241 offset:24576\nds_read_b64_tr_b16 v[194:195],v241 offset:24832\nM v[8:11],v[196:199],a[160:163],v[8:11]\nds_read_b128 a[32:35],v240 offset:8192\nM v[12:15],v[196:199],a[164:167],v[12:15]\nM v[72:75],v[196:199],a[168:171],v[72:75]\nM v[76:79],v[196:199],a[172:175],v[76:79]\n" \
"ds_read_b64_tr_b16 v[196:197],v242 offset:24576\nds_read_b64_tr_b16 v[198:199],v242 offset:24832\nM v[16:19],v[200:203],a[160:163],v[16:19]\nM v[20:23],v[200:203],a[164:167],v[20:23]\nM v[80:83],v[200:203],a[168:171],v[80:83]\nds_read_b128 a[36:39],v240 offset:9216\nM v[84:87],v[200:203],a[172:175],v[84:87]\nds_read_b64_tr_b16 v[200:201],v241 offset:25088\n" \
"s_waitcnt lgkmcnt(10)\nM v[24:27],v[204:207],a[160:163],v[24:27]\nbuffer_store_dwordx4 v[176:179],v240,s[52:55],s74 offen offset:0 sc1\nM v[28:31],v[204:207],a[164:167],v[28:31]\nds_read_b64_tr_b16 v[202:203],v241 offset:25344\nM v[88:91],v[204:207],a[168:171],v[88:91]\nM v[92:95],v[204:207],a[172:175],v[92:95]\nds_read_b64_tr_b16 v[204:205],v242 offset:25088\n" \
"ds_read_b64_tr_b16 v[206:207],v242 offset:25344\nM v[32:35],v[208:211],a[160:163],v[32:35]\nds_read_b128 a[40:43],v240 offset:10240\nM v[36:39],v[208:211],a[164:167],v[36:39]\ns_waitcnt vmcnt(8)\nv_cmp_eq_u32 vcc,0,v248\nM v[96:99],v[208:211],a[168:171],v[96:99]\ns_cbranch_vccnz .Lps1\n" \
".Lpb1:\ns_add_u32 s80,s80,4096\nM v[100:103],v[208:211],a[172:175],v[100:103]\nds_read_b64_tr_b16 v[208:209],v241 offset:25600\nds_read_b64_tr_b16 v[210:211],v241 offset:25856\nM v[40:43],v[212:215],a[160:163],v[40:43]\nM v[44:47],v[212:215],a[164:167],v[44:47]\nM v[104:107],v[212:215],a[168:171],v[104:107]\n" \
"ds_read_b128 a[44:47],v240 offset:11264\nM v[108:111],v[212:215],a[172:175],v[108:111]\nds_read_b64_tr_b16 v[212:213],v242 offset:25600\nds_read_b64_tr_b16 v[214:215],v242 offset:25856\nM v[48:51],v[216:219],a[160:163],v[48:51]\nM v[52:55],v[216:219],a[164:167],v[52:55]\nM v[112:115],v[216:219],a[168:171],v[112:115]\nM v[116:119],v[216:219],a[172:175],v[116:119]\n" \
"ds_read_b64_tr_b16 v[216:217],v241 offset:26112\ns_waitcnt lgkmcnt(9)\nM v[56:59],v[220:223],a[160:163],v[56:59]\nds_read_b64_tr_b16 v[218:219],v241 offset:26368\nds_read_b128 a[48:51],v240 offset:12288\nM v[60:63],v[220:223],a[164:167],v[60:63]\nM v[120:123],v[220:223],a[168:171],v[120:123]\nM v[124:127],v[220:223],a[172:175],v[124:127]\n" \
"ds_read_b64_tr_b16 v[220:221],v242 offset:26112\nds_read_b64_tr_b16 v[222:223],v242 offset:26368\nM v[0:3],v[192:195],a[176:179],v[0:3]\nM v[4:7],v[192:195],a[180:183],v[4:7]\nM v[64:67],v[192:195],a[184:187],v[64:67]\nds_read_b128 a[52:55],v240 offset:13312\nM v[68:71],v[192:195],a[188:191],v[68:71]\nM v[8:11],v[196:199],a[176:179],v[8:11]\n" \
"M v[12:15],v[196:199],a[180:183],v[12:15]\nM v[72:75],v[196:199],a[184:187],v[72:75]\nbuffer_store_dwordx4 v[180:183],v240,s[52:55],s74 offen offset:1024 sc1\nM v[76:79],v[196:199],a[188:191],v[76:79]\nM v[16:19],v[200:203],a[176:179],v[16:19]\nds_read_b128 a[56:59],v240 offset:14336\nM v[20:23],v[200:203],a[180:183],v[20:23]\nM v[80:83],v[200:203],a[184:187],v[80:83]\n" \
"M v[84:87],v[200:203],a[188:191],v[84:87]\ns_waitcnt lgkmcnt(6)\nM v[24:27],v[204:207],a[176:179],v[24:27]\nM v[28:31],v[204:207],a[180:183],v[28:31]\nM v[88:91],v[204:207],a[184:187],v[88:91]\ns_waitcnt lgkmcnt(2)\ns_barrier\nM v[92:95],v[204:207],a[188:191],v[92:95]\n" \
"ds_read_b64_tr_b16 v[192:193],v241 offset:32768\nds_read_b64_tr_b16 v[194:195],v241 offset:33024\nM v[32:35],v[208:211],a[176:179],v[32:35]\nds_read_b64_tr_b16 v[196:197],v242 offset:32768\nds_read_b64_tr_b16 v[198:199],v242 offset:33024\nM v[36:39],v[208:211],a[180:183],v[36:39]\nds_read_b64_tr_b16 v[200:201],v241 offset:33280\nds_read_b64_tr_b16 v[202:203],v241 offset:33536\n" \
"M v[96:99],v[208:211],a[184:187],v[96:99]\nds_read_b64_tr_b16 v[204:205],v242 offset:33280\nds_read_b64_tr_b16 v[206:207],v242 offset:33536\nM v[100:103],v[208:211],a[188:191],v[100:103]\nds_read_b64_tr_b16 v[208:209],v241 offset:33792\nds_read_b64_tr_b16 v[210:211],v241 offset:34048\nM v[40:43],v[212:215],a[176:179],v[40:43]\nds_write_b128 v250,v[128:131] offset:0\n" \
"M v[44:47],v[212:215],a[180:183],v[44:47]\nds_read_b128 a[60:63],v240 offset:15360\nM v[104:107],v[212:215],a[184:187],v[104:107]\nds_write_b128 v250,v[132:135] offset:1024\nM v[108:111],v[212:215],a[188:191],v[108:111]\nds_read_b64_tr_b16 v[212:213],v242 offset:33792\nds_read_b64_tr_b16 v[214:215],v242 offset:34048\nM v[48:51],v[216:219],a[176:179],v[48:51]\n" \
"M v[52:55],v[216:219],a[180:183],v[52:55]\nM v[112:115],v[216:219],a[184:187],v[112:115]\nM v[116:119],v[216:219],a[188:191],v[116:119]\nds_read_b64_tr_b16 v[216:217],v241 offset:34304\nds_read_b64_tr_b16 v[218:219],v241 offset:34560\nM v[56:59],v[220:223],a[176:179],v[56:59]\nds_write_b128 v250,v[136:139] offset:2048\nM v[60:63],v[220:223],a[180:183],v[60:63]\n" \
"M v[120:123],v[220:223],a[184:187],v[120:123]\nM v[124:127],v[220:223],a[188:191],v[124:127]\nds_read_b64_tr_b16 v[220:221],v242 offset:34304\ns_waitcnt lgkmcnt(11)\nM v[0:3],v[192:195],a[192:195],v[0:3]\nbuffer_store_dwordx4 v[184:187],v240,s[52:55],s74 offen offset:2048 sc1\nM v[4:7],v[192:195],a[196:199],v[4:7]\nds_read_b64_tr_b16 v[222:223],v242 offset:34560\n" \
"M v[64:67],v[192:195],a[200:203],v[64:67]\nds_write_b128 v250,v[140:143] offset:3072\nM v[68:71],v[192:195],a[204:207],v[68:71]\nds_read_b64_tr_b16 v[192:193],v241 offset:40960\nds_read_b64_tr_b16 v[194:195],v241 offset:41216\nM v[8:11],v[196:199],a[192:195],v[8:11]\nM v[12:15],v[196:199],a[196:199],v[12:15]\nM v[72:75],v[196:199],a[200:203],v[72:75]\n" \
"M v[76:79],v[196:199],a[204:207],v[76:79]\nds_read_b64_tr_b16 v[196:197],v242 offset:40960\nds_read_b64_tr_b16 v[198:199],v242 offset:41216\nM v[16:19],v[200:203],a[192:195],v[16:19]\nds_write_b128 v250,v[144:147] offset:4096\nM v[20:23],v[200:203],a[196:199],v[20:23]\nM v[80:83],v[200:203],a[200:203],v[80:83]\nM v[84:87],v[200:203],a[204:207],v[84:87]\n" \
"ds_read_b64_tr_b16 v[200:201],v241 offset:41472\nds_read_b64_tr_b16 v[202:203],v241 offset:41728\nM v[24:27],v[204:207],a[192:195],v[24:27]\nM v[28:31],v[204:207],a[196:199],v[28:31]\nM v[88:91],v[204:207],a[200:203],v[88:91]\nds_write_b128 v250,v[148:151] offset:5120\nM v[92:95],v[204:207],a[204:207],v[92:95]\nds_read_b64_tr_b16 v[204:205],v242 offset:41472\n" \
"s_waitcnt lgkmcnt(10)\nM v[32:35],v[208:211],a[192:195],v[32:35]\nds_read_b64_tr_b16 v[206:207],v242 offset:41728\nM v[36:39],v[208:211],a[196:199],v[36:39]\nM v[96:99],v[208:211],a[200:203],v[96:99]\nM v[100:103],v[208:211],a[204:207],v[100:103]\nds_read_b64_tr_b16 v[208:209],v241 offset:41984\nds_read_b64_tr_b16 v[210:211],v241 offset:42240\n" \
"M v[40:43],v[212:215],a[192:195],v[40:43]\nds_write_b128 v250,v[152:155] offset:6144\nM v[44:47],v[212:215],a[196:199],v[44:47]\nM v[104:107],v[212:215],a[200:203],v[104:107]\nM v[108:111],v[212:215],a[204:207],v[108:111]\nds_read_b64_tr_b16 v[212:213],v242 offset:41984\nds_read_b64_tr_b16 v[214:215],v242 offset:42240\nM v[48:51],v[216:219],a[192:195],v[48:51]\n" \
"M v[52:55],v[216:219],a[196:199],v[52:55]\nM v[112:115],v[216:219],a[200:203],v[112:115]\nbuffer_store_dwordx4 v[188:191],v240,s[52:55],s74 offen offset:3072 sc1\nM v[116:119],v[216:219],a[204:207],v[116:119]\nds_read_b64_tr_b16 v[216:217],v241 offset:42496\nds_read_b64_tr_b16 v[218:219],v241 offset:42752\nM v[56:59],v[220:223],a[192:195],v[56:59]\nds_write_b128 v250,v[156:159] offset:7168\n" \
"M v[60:63],v[220:223],a[196:199],v[60:63]\nM v[120:123],v[220:223],a[200:203],v[120:123]\nM v[124:127],v[220:223],a[204:207],v[124:127]\nds_read_b64_tr_b16 v[220:221],v242 offset:42496\ns_waitcnt lgkmcnt(9)\nM v[0:3],v[192:195],a[208:211],v[0:3]\nglobal_load_dword v247,v229,s[18:19]\nM v[4:7],v[192:195],a[212:215],v[4:7]\n" \
"ds_read_b64_tr_b16 v[222:223],v242 offset:42752\nv_and_b32 v229,3,v249\nM v[64:67],v[192:195],a[216:219],v[64:67]\nv_cmp_eq_u32 vcc,0,v229\nM v[68:71],v[192:195],a[220:223],v[68:71]\nds_write_b128 v250,v[160:163] offset:8192\nM v[8:11],v[196:199],a[208:211],v[8:11]\nv_cndmask_b32 v229,0,v230,vcc\n" \
"v_and_b32 v230,0xff,v241\nM v[12:15],v[196:199],a[212:215],v[12:15]\nv_lshl_or_b32 v230,v231,4,v230\nv_and_b32 v231,32,v249\nM v[72:75],v[196:199],a[216:219],v[72:75]\nds_write_b128 v250,v[164:167] offset:9216\nM v[76:79],v[196:199],a[220:223],v[76:79]\nM v[16:19],v[200:203],a[208:211],v[16:19]\n" \
"M v[20:23],v[200:203],a[212:215],v[20:23]\nM v[80:83],v[200:203],a[216:219],v[80:83]\nM v[84:87],v[200:203],a[220:223],v[84:87]\nM v[24:27],v[204:207],a[208:211],v[24:27]\nds_write_b128 v250,v[168:171] offset:10240\nM v[28:31],v[204:207],a[212:215],v[28:31]\nM v[88:91],v[204:207],a[216:219],v[88:91]\ns_waitcnt lgkmcnt(3)\n" \
"s_barrier\nM v[92:95],v[204:207],a[220:223],v[92:95]\nds_read_b64_tr_b16 v[192:193],v241 offset:49152\nds_read_b64_tr_b16 v[194:195],v241 offset:49408\nM v[32:35],v[208:211],a[208:211],v[32:35]\nds_read_b64_tr_b16 v[196:197],v242 offset:49152\nds_read_b64_tr_b16 v[198:199],v242 offset:49408\nM v[36:39],v[208:211],a[212:215],v[36:39]\n" \
"ds_read_b64_tr_b16 v[200:201],v241 offset:49664\nds_read_b64_tr_b16 v[202:203],v241 offset:49920\nM v[96:99],v[208:211],a[216:219],v[96:99]\nds_read_b64_tr_b16 v[204:205],v242 offset:49664\nds_read_b64_tr_b16 v[206:207],v242 offset:49920\nM v[100:103],v[208:211],a[220:223],v[100:103]\nds_read_b64_tr_b16 v[208:209],v241 offset:50176\nds_read_b64_tr_b16 v[210:211],v241 offset:50432\n" \
"M v[40:43],v[212:215],a[208:211],v[40:43]\nds_write_b128 v250,v[172:175] offset:11264\nM v[44:47],v[212:215],a[212:215],v[44:47]\nM v[104:107],v[212:215],a[216:219],v[104:107]\nM v[108:111],v[212:215],a[220:223],v[108:111]\nds_read_b64_tr_b16 v[212:213],v242 offset:50176\nds_read_b64_tr_b16 v[214:215],v242 offset:50432\nM v[48:51],v[216:219],a[208:211],v[48:51]\n" \
"ds_write_b128 v250,v[176:179] offset:12288\nM v[52:55],v[216:219],a[212:215],v[52:55]\nM v[112:115],v[216:219],a[216:219],v[112:115]\nM v[116:119],v[216:219],a[220:223],v[116:119]\nds_read_b64_tr_b16 v[216:217],v241 offset:50688\nds_read_b64_tr_b16 v[218:219],v241 offset:50944\nM v[56:59],v[220:223],a[208:211],v[56:59]\nv_accvgpr_write_b32 a208,v229\n" \
"v_accvgpr_write_b32 a209,v229\nM v[60:63],v[220:223],a[212:215],v[60:63]\nv_accvgpr_write_b32 a210,v229\nv_accvgpr_write_b32 a211,v229\nM v[120:123],v[220:223],a[216:219],v[120:123]\nds_write_b128 v250,v[180:183] offset:13312\nM v[124:127],v[220:223],a[220:223],v[124:127]\nds_read_b64_tr_b16 v[220:221],v242 offset:50688\n" \
"s_waitcnt lgkmcnt(9)\nM v[0:3],v[192:195],a[224:227],v[0:3]\nds_read_b64_tr_b16 v[222:223],v242 offset:50944\nM v[4:7],v[192:195],a[228:231],v[4:7]\nM v[64:67],v[192:195],a[232:235],v[64:67]\ns_waitcnt vmcnt(10)\nM v[68:71],v[192:195],a[236:239],v[68:71]\nds_read_b64_tr_b16 v[192:193],v241 offset:57344\n" \
"ds_read_b64_tr_b16 v[194:195],v241 offset:57600\nM v[8:11],v[196:199],a[224:227],v[8:11]\nds_write_b128 v250,v[184:187] offset:14336\nM v[12:15],v[196:199],a[228:231],v[12:15]\nM v[72:75],v[196:199],a[232:235],v[72:75]\nM v[76:79],v[196:199],a[236:239],v[76:79]\nds_read_b64_tr_b16 v[196:197],v242 offset:57344\nds_read_b64_tr_b16 v[198:199],v242 offset:57600\n" \
"M v[16:19],v[200:203],a[224:227],v[16:19]\nM v[20:23],v[200:203],a[228:231],v[20:23]\nM v[80:83],v[200:203],a[232:235],v[80:83]\nds_write_b128 v250,v[188:191] offset:15360\nM v[84:87],v[200:203],a[236:239],v[84:87]\nds_read_b64_tr_b16 v[200:201],v241 offset:57856\nds_read_b64_tr_b16 v[202:203],v241 offset:58112\nM v[24:27],v[204:207],a[224:227],v[24:27]\n" \
"v_add_u32 v250,114688,v240\nM v[28:31],v[204:207],a[228:231],v[28:31]\nM v[88:91],v[204:207],a[232:235],v[88:91]\nM v[92:95],v[204:207],a[236:239],v[92:95]\nds_read_b64_tr_b16 v[204:205],v242 offset:57856\ns_waitcnt lgkmcnt(9)\nM v[32:35],v[208:211],a[224:227],v[32:35]\nds_read_b64_tr_b16 v[206:207],v242 offset:58112\n" \
"M v[36:39],v[208:211],a[228:231],v[36:39]\nM v[96:99],v[208:211],a[232:235],v[96:99]\nM v[100:103],v[208:211],a[236:239],v[100:103]\nds_read_b64_tr_b16 v[208:209],v241 offset:58368\nds_read_b64_tr_b16 v[210:211],v241 offset:58624\nM v[40:43],v[212:215],a[224:227],v[40:43]\nM v[44:47],v[212:215],a[228:231],v[44:47]\nM v[104:107],v[212:215],a[232:235],v[104:107]\n" \
"M v[108:111],v[212:215],a[236:239],v[108:111]\nds_read_b64_tr_b16 v[212:213],v242 offset:58368\nds_read_b64_tr_b16 v[214:215],v242 offset:58624\nM v[48:51],v[216:219],a[224:227],v[48:51]\nM v[52:55],v[216:219],a[228:231],v[52:55]\nM v[112:115],v[216:219],a[232:235],v[112:115]\nM v[116:119],v[216:219],a[236:239],v[116:119]\nds_read_b64_tr_b16 v[216:217],v241 offset:58880\n" \
"ds_read_b64_tr_b16 v[218:219],v241 offset:59136\nM v[56:59],v[220:223],a[224:227],v[56:59]\nv_add_u32 v241,65536,v240\nM v[60:63],v[220:223],a[228:231],v[60:63]\nM v[120:123],v[220:223],a[232:235],v[120:123]\nM v[124:127],v[220:223],a[236:239],v[124:127]\nds_read_b64_tr_b16 v[220:221],v242 offset:58880\nds_read_b64_tr_b16 v[222:223],v242 offset:59136\n" \
"s_waitcnt vmcnt(9) lgkmcnt(9)\nM v[0:3],v[192:195],a[240:243],v[0:3]\nv_lshl_or_b32 v242,v231,6,v230\nM v[8:11],v[196:199],a[240:243],v[8:11]\nM v[16:19],v[200:203],a[240:243],v[16:19]\ns_waitcnt lgkmcnt(7)\nM v[24:27],v[204:207],a[240:243],v[24:27]\ns_waitcnt lgkmcnt(6)\n" \
"M v[32:35],v[208:211],a[240:243],v[32:35]\ns_waitcnt lgkmcnt(4)\nM v[40:43],v[212:215],a[240:243],v[40:43]\nv_cvt_pk_bf16_f32 v0,v0,v1\ns_waitcnt lgkmcnt(2)\nM v[48:51],v[216:219],a[240:243],v[48:51]\nv_cvt_pk_bf16_f32 v16,v16,v17\ns_waitcnt lgkmcnt(0)\n" \
"M v[56:59],v[220:223],a[240:243],v[56:59]\nv_cvt_pk_bf16_f32 v32,v32,v33\ns_waitcnt vmcnt(8)\nM v[4:7],v[192:195],a[244:247],v[4:7]\nv_cvt_pk_bf16_f32 v1,v2,v3\nv_cvt_pk_bf16_f32 v17,v18,v19\nM v[12:15],v[196:199],a[244:247],v[12:15]\nv_cvt_pk_bf16_f32 v48,v48,v49\n" \
"v_cvt_pk_bf16_f32 v33,v34,v35\nM v[20:23],v[200:203],a[244:247],v[20:23]\nv_cvt_pk_bf16_f32 v49,v50,v51\nv_cvt_pk_bf16_f32 v2,v8,v9\nM v[28:31],v[204:207],a[244:247],v[28:31]\nv_cvt_pk_bf16_f32 v4,v4,v5\nv_cvt_pk_bf16_f32 v5,v6,v7\nM v[36:39],v[208:211],a[244:247],v[36:39]\n" \
"v_cvt_pk_bf16_f32 v3,v10,v11\nv_cvt_pk_bf16_f32 v18,v24,v25\nM v[44:47],v[212:215],a[244:247],v[44:47]\nv_cvt_pk_bf16_f32 v20,v20,v21\nv_cvt_pk_bf16_f32 v21,v22,v23\nM v[52:55],v[216:219],a[244:247],v[52:55]\nv_cvt_pk_bf16_f32 v19,v26,v27\nv_cvt_pk_bf16_f32 v34,v40,v41\n" \
"M v[60:63],v[220:223],a[244:247],v[60:63]\nv_cvt_pk_bf16_f32 v36,v36,v37\ns_waitcnt vmcnt(6)\nM v[64:67],v[192:195],a[248:251],v[64:67]\nv_cvt_pk_bf16_f32 v37,v38,v39\nv_cvt_pk_bf16_f32 v35,v42,v43\nM v[72:75],v[196:199],a[248:251],v[72:75]\nv_cvt_pk_bf16_f32 v52,v52,v53\n" \
"v_cvt_pk_bf16_f32 v53,v54,v55\nM v[80:83],v[200:203],a[248:251],v[80:83]\nv_cvt_pk_bf16_f32 v50,v56,v57\nv_cvt_pk_bf16_f32 v51,v58,v59\nM v[88:91],v[204:207],a[248:251],v[88:91]\nv_cvt_pk_bf16_f32 v64,v64,v65\nv_cvt_pk_bf16_f32 v65,v66,v67\nM v[96:99],v[208:211],a[248:251],v[96:99]\n" \
"v_cvt_pk_bf16_f32 v6,v12,v13\nv_cvt_pk_bf16_f32 v7,v14,v15\nM v[104:107],v[212:215],a[248:251],v[104:107]\nv_cvt_pk_bf16_f32 v80,v80,v81\nv_cvt_pk_bf16_f32 v81,v82,v83\nM v[112:115],v[216:219],a[248:251],v[112:115]\nv_cvt_pk_bf16_f32 v22,v28,v29\nv_cvt_pk_bf16_f32 v23,v30,v31\n" \
"M v[120:123],v[220:223],a[248:251],v[120:123]\nv_cvt_pk_bf16_f32 v96,v96,v97\ns_waitcnt vmcnt(5)\nM v[68:71],v[192:195],a[252:255],v[68:71]\nv_cvt_pk_bf16_f32 v97,v98,v99\nv_cvt_pk_bf16_f32 v38,v44,v45\nM v[76:79],v[196:199],a[252:255],v[76:79]\nv_cvt_pk_bf16_f32 v112,v112,v113\n" \
"v_cvt_pk_bf16_f32 v113,v114,v115\nM v[84:87],v[200:203],a[252:255],v[84:87]\nv_cvt_pk_bf16_f32 v39,v46,v47\nv_cvt_pk_bf16_f32 v54,v60,v61\nM v[92:95],v[204:207],a[252:255],v[92:95]\nv_cvt_pk_bf16_f32 v68,v68,v69\nv_cvt_pk_bf16_f32 v69,v70,v71\nM v[100:103],v[208:211],a[252:255],v[100:103]\n" \
"v_cvt_pk_bf16_f32 v55,v62,v63\nv_cvt_pk_bf16_f32 v66,v72,v73\nM v[108:111],v[212:215],a[252:255],v[108:111]\nv_cvt_pk_bf16_f32 v84,v84,v85\nv_cvt_pk_bf16_f32 v85,v86,v87\nM v[116:119],v[216:219],a[252:255],v[116:119]\nv_cvt_pk_bf16_f32 v67,v74,v75\nv_cvt_pk_bf16_f32 v82,v88,v89\n" \
"v_cvt_pk_bf16_f32 v100,v100,v101\nv_cvt_pk_bf16_f32 v101,v102,v103\nM v[124:127],v[220:223],a[252:255],v[124:127]\nv_cvt_pk_bf16_f32 v83,v90,v91\nv_cvt_pk_bf16_f32 v98,v104,v105\nv_cvt_pk_bf16_f32 v99,v106,v107\nv_cvt_pk_bf16_f32 v116,v116,v117\nv_cvt_pk_bf16_f32 v117,v118,v119\n" \
"v_cvt_pk_bf16_f32 v114,v120,v121\nv_cvt_pk_bf16_f32 v115,v122,v123\nv_cvt_pk_bf16_f32 v70,v76,v77\nv_cvt_pk_bf16_f32 v71,v78,v79\nv_cvt_pk_bf16_f32 v86,v92,v93\nv_cvt_pk_bf16_f32 v87,v94,v95\nv_cvt_pk_bf16_f32 v102,v108,v109\nv_cvt_pk_bf16_f32 v103,v110,v111\n" \
"v_cvt_pk_bf16_f32 v118,v124,v125\nv_cvt_pk_bf16_f32 v119,v126,v127\ns_waitcnt vmcnt(1)\nbuffer_store_dword v254,off,s[52:55],s29 sc1\nv_accvgpr_write_b32 a244,0\nv_accvgpr_write_b32 a245,0\nv_accvgpr_write_b32 a246,0\nv_accvgpr_write_b32 a247,0\n" \
"v_accvgpr_write_b32 a128,v0\nv_accvgpr_write_b32 a129,v1\nv_accvgpr_write_b32 a130,v2\nv_accvgpr_write_b32 a131,v3\nv_accvgpr_write_b32 a132,v16\nv_accvgpr_write_b32 a133,v17\nv_accvgpr_write_b32 a134,v18\nv_accvgpr_write_b32 a135,v19\n" \
"v_accvgpr_write_b32 a136,v32\nv_accvgpr_write_b32 a137,v33\nv_accvgpr_write_b32 a138,v34\nv_accvgpr_write_b32 a139,v35\nv_accvgpr_write_b32 a140,v48\nv_accvgpr_write_b32 a141,v49\nv_accvgpr_write_b32 a142,v50\nv_accvgpr_write_b32 a143,v51\n" \
"v_accvgpr_write_b32 a144,v4\nv_accvgpr_write_b32 a145,v5\nv_accvgpr_write_b32 a146,v6\nv_accvgpr_write_b32 a147,v7\nv_accvgpr_write_b32 a148,v20\nv_accvgpr_write_b32 a149,v21\nv_accvgpr_write_b32 a150,v22\nv_accvgpr_write_b32 a151,v23\n" \
"v_accvgpr_write_b32 a152,v36\nv_accvgpr_write_b32 a153,v37\nv_accvgpr_write_b32 a154,v38\nv_accvgpr_write_b32 a155,v39\nv_accvgpr_write_b32 a156,v52\nv_accvgpr_write_b32 a157,v53\nv_accvgpr_write_b32 a158,v54\nv_accvgpr_write_b32 a159,v55\n" \
"v_accvgpr_write_b32 a160,v64\nv_accvgpr_write_b32 a161,v65\nv_accvgpr_write_b32 a162,v66\nv_accvgpr_write_b32 a163,v67\nv_accvgpr_write_b32 a164,v80\nv_accvgpr_write_b32 a165,v81\nv_accvgpr_write_b32 a166,v82\nv_accvgpr_write_b32 a167,v83\n" \
"v_accvgpr_write_b32 a168,v96\nv_accvgpr_write_b32 a169,v97\nv_accvgpr_write_b32 a170,v98\nv_accvgpr_write_b32 a171,v99\nv_accvgpr_write_b32 a172,v112\nv_accvgpr_write_b32 a173,v113\nv_accvgpr_write_b32 a174,v114\nv_accvgpr_write_b32 a175,v115\n" \
"v_accvgpr_write_b32 a176,v68\nv_accvgpr_write_b32 a177,v69\nv_accvgpr_write_b32 a178,v70\nv_accvgpr_write_b32 a179,v71\nv_accvgpr_write_b32 a180,v84\nv_accvgpr_write_b32 a181,v85\nv_accvgpr_write_b32 a182,v86\nv_accvgpr_write_b32 a183,v87\n" \
"v_accvgpr_write_b32 a184,v100\nv_accvgpr_write_b32 a185,v101\nv_accvgpr_write_b32 a186,v102\nv_accvgpr_write_b32 a187,v103\nv_accvgpr_write_b32 a188,v116\nv_accvgpr_write_b32 a189,v117\nv_accvgpr_write_b32 a190,v118\nv_accvgpr_write_b32 a191,v119\n" \
"s_waitcnt lgkmcnt(0)\n.Lattn:\ns_cmp_lt_u32 s7,2\ns_cbranch_scc0 .Lp23\ns_cmp_eq_u32 s7,0\ns_cbranch_scc0 .Lpro1\n.Lpro0:\ns_barrier\n" \
"M v[64:67],a[0:3],a[128:131],0\nds_read_b128 a[32:35],v240 offset:40960\nds_read_b128 a[36:39],v240 offset:41984\nM v[64:67],a[4:7],a[132:135],v[64:67]\nds_read_b128 a[40:43],v240 offset:43008\nds_read_b128 a[44:47],v240 offset:44032\nM v[64:67],a[8:11],a[136:139],v[64:67]\nds_read_b128 a[48:51],v240 offset:45056\n" \
"ds_read_b128 a[52:55],v240 offset:46080\nM v[64:67],a[12:15],a[140:143],v[64:67]\nds_read_b128 a[56:59],v240 offset:47104\nds_read_b128 a[60:63],v240 offset:48128\nM v[68:71],a[0:3],a[144:147],0\nds_read_b128 a[0:3],v240 offset:32768\nds_read_b128 a[64:67],v241 offset:0\nM v[68:71],a[4:7],a[148:151],v[68:71]\n" \
"ds_read_b128 a[4:7],v240 offset:33792\nds_read_b128 a[68:71],v241 offset:1024\nM v[68:71],a[8:11],a[152:155],v[68:71]\nds_read_b128 a[8:11],v240 offset:34816\nv_max3_f32 v225,v64,v65,v66\nM v[68:71],a[12:15],a[156:159],v[68:71]\nds_read_b128 a[12:15],v240 offset:35840\nv_max_f32 v225,v225,v67\n" \
"M v[76:79],a[16:19],a[144:147],0\nds_read_b128 a[16:19],v240 offset:36864\nv_cndmask_b32 v225,v224,v225,s[90:91]\nM v[76:79],a[20:23],a[148:151],v[76:79]\nds_read_b128 a[20:23],v240 offset:37888\nds_read_b128 a[72:75],v241 offset:2048\nM v[76:79],a[24:27],a[152:155],v[76:79]\nds_read_b128 a[24:27],v240 offset:38912\n" \
"v_max3_f32 v229,v68,v69,v70\nM v[76:79],a[28:31],a[156:159],v[76:79]\nds_read_b128 a[28:31],v240 offset:39936\ns_waitcnt lgkmcnt(10)\nM v[136:139],a[0:3],a[160:163],0\nv_cndmask_b32 v64,v64,v252,s[82:83]\ns_waitcnt lgkmcnt(8)\nM v[136:139],a[4:7],a[164:167],v[136:139]\n" \
"v_cndmask_b32 v65,v65,v252,s[84:85]\ns_waitcnt lgkmcnt(6)\nM v[136:139],a[8:11],a[168:171],v[136:139]\nv_max3_f32 v229,v229,v71,v76\ns_waitcnt lgkmcnt(5)\nM v[136:139],a[12:15],a[172:175],v[136:139]\nv_max3_f32 v229,v229,v77,v78\nv_max_f32 v229,v229,v79\n" \
"M v[140:143],a[0:3],a[176:179],0\nv_cndmask_b32 v229,v228,v229,s[90:91]\nds_read_b128 a[76:79],v241 offset:3072\nM v[140:143],a[4:7],a[180:183],v[140:143]\nv_permlane16_swap_b32 v225,v229\nv_max_f32 v225,v225,v229\nM v[140:143],a[8:11],a[184:187],v[140:143]\nv_mov_b32 v229,v225\n" \
"v_max3_f32 v233,v136,v137,v138\nM v[140:143],a[12:15],a[188:191],v[140:143]\nv_permlane32_swap_b32 v225,v229\ns_waitcnt lgkmcnt(4)\nM v[152:155],a[16:19],a[160:163],0\nv_max_f32 v225,v225,v229\nv_mov_b32 v229,v225\nM v[152:155],a[20:23],a[164:167],v[152:155]\n" \
"v_cndmask_b32 v66,v66,v252,s[86:87]\ns_waitcnt lgkmcnt(1)\nM v[152:155],a[24:27],a[168:171],v[152:155]\nv_permlane16_swap_b32 v225,v229\nv_sub_f32 v224,0,v225\nM v[152:155],a[28:31],a[172:175],v[152:155]\nv_sub_f32 v226,0,v225\nv_sub_f32 v227,0,v225\n" \
"M v[156:159],a[16:19],a[176:179],0\nv_sub_f32 v225,0,v225\nv_sub_f32 v228,0,v229\nM v[156:159],a[20:23],a[180:183],v[156:159]\nv_sub_f32 v230,0,v229\nv_sub_f32 v231,0,v229\nM v[156:159],a[24:27],a[184:187],v[156:159]\nv_sub_f32 v229,0,v229\n" \
"v_max3_f32 v237,v140,v141,v142\nM v[156:159],a[28:31],a[188:191],v[156:159]\nv_max3_f32 v233,v233,v139,v152\nv_max3_f32 v233,v233,v153,v154\nM v[128:131],a[0:3],a[128:131],v[224:227]\nv_max_f32 v233,v233,v155\nv_cndmask_b32 v233,v232,v233,s[90:91]\nM v[128:131],a[4:7],a[132:135],v[128:131]\n" \
"v_cndmask_b32 v67,v67,v252,s[88:89]\nv_add_f32 v64,v64,v224\nM v[128:131],a[8:11],a[136:139],v[128:131]\nv_max3_f32 v237,v237,v143,v156\nv_max3_f32 v237,v237,v157,v158\nM v[128:131],a[12:15],a[140:143],v[128:131]\nv_max_f32 v237,v237,v159\nv_cndmask_b32 v237,v236,v237,s[90:91]\n" \
"M v[132:135],a[0:3],a[144:147],v[228:231]\nv_add_f32 v65,v65,v224\nv_permlane16_swap_b32 v233,v237\nM v[132:135],a[4:7],a[148:151],v[132:135]\nv_max_f32 v233,v233,v237\nv_mov_b32 v237,v233\nM v[132:135],a[8:11],a[152:155],v[132:135]\nv_add_f32 v66,v66,v224\n" \
"v_permlane32_swap_b32 v233,v237\nM v[132:135],a[12:15],a[156:159],v[132:135]\nv_max_f32 v233,v233,v237\nv_mov_b32 v237,v233\nM v[144:147],a[16:19],a[128:131],v[224:227]\nv_add_f32 v67,v67,v224\nv_permlane16_swap_b32 v233,v237\nM v[144:147],a[20:23],a[132:135],v[144:147]\n" \
"v_sub_f32 v232,0,v233\nv_sub_f32 v234,0,v233\nM v[144:147],a[24:27],a[136:139],v[144:147]\nv_sub_f32 v235,0,v233\nv_sub_f32 v233,0,v233\nM v[144:147],a[28:31],a[140:143],v[144:147]\nv_sub_f32 v236,0,v237\nv_sub_f32 v238,0,v237\n" \
"M v[148:151],a[16:19],a[144:147],v[228:231]\nv_sub_f32 v239,0,v237\nv_sub_f32 v237,0,v237\nM v[148:151],a[20:23],a[148:151],v[148:151]\nv_exp_f32 v64,v64\nM v[148:151],a[24:27],a[152:155],v[148:151]\nv_exp_f32 v65,v65\nM v[148:151],a[28:31],a[156:159],v[148:151]\n" \
"v_exp_f32 v66,v66\nM v[160:163],a[32:35],a[128:131],v[224:227]\nv_exp_f32 v67,v67\nM v[160:163],a[36:39],a[132:135],v[160:163]\nv_add_f32 v68,v68,v228\nv_add_f32 v69,v69,v228\nM v[160:163],a[40:43],a[136:139],v[160:163]\nv_cndmask_b32 v76,v76,v252,s[82:83]\n" \
"v_cndmask_b32 v77,v77,v252,s[84:85]\nM v[160:163],a[44:47],a[140:143],v[160:163]\nv_cndmask_b32 v78,v78,v252,s[86:87]\nv_cndmask_b32 v79,v79,v252,s[88:89]\nM v[164:167],a[32:35],a[144:147],v[228:231]\nv_add_f32 v70,v70,v228\nv_add_f32 v71,v71,v228\nM v[164:167],a[36:39],a[148:151],v[164:167]\n" \
"v_exp_f32 v68,v68\nM v[164:167],a[40:43],a[152:155],v[164:167]\nv_exp_f32 v69,v69\nM v[164:167],a[44:47],a[156:159],v[164:167]\nv_cvt_pk_bf16_f32 v64,v64,v65\nds_read_b128 a[80:83],v241 offset:4096\nM v[176:179],a[48:51],a[128:131],v[224:227]\nv_exp_f32 v70,v70\n" \
"M v[176:179],a[52:55],a[132:135],v[176:179]\nv_exp_f32 v71,v71\nM v[176:179],a[56:59],a[136:139],v[176:179]\nv_add_f32 v76,v76,v228\nv_add_f32 v77,v77,v228\nM v[176:179],a[60:63],a[140:143],v[176:179]\nv_add_f32 v78,v78,v228\nv_add_f32 v79,v79,v228\n" \
"M v[180:183],a[48:51],a[144:147],v[228:231]\nv_cvt_pk_bf16_f32 v65,v66,v67\nv_mov_b32 v66,0\nM v[180:183],a[52:55],a[148:151],v[180:183]\nv_exp_f32 v76,v76\nM v[180:183],a[56:59],a[152:155],v[180:183]\nv_exp_f32 v77,v77\nM v[180:183],a[60:63],a[156:159],v[180:183]\n" \
"v_exp_f32 v78,v78\nM v[168:171],a[32:35],a[160:163],v[232:235]\nv_exp_f32 v79,v79\nM v[168:171],a[36:39],a[164:167],v[168:171]\nv_mov_b32 v67,0\nv_cvt_pk_bf16_f32 v68,v68,v69\nM v[168:171],a[40:43],a[168:171],v[168:171]\nv_cvt_pk_bf16_f32 v69,v70,v71\n" \
"v_cvt_pk_bf16_f32 v70,v76,v77\nM v[168:171],a[44:47],a[172:175],v[168:171]\nv_cvt_pk_bf16_f32 v71,v78,v79\nds_read_b128 a[84:87],v241 offset:5120\nM v[172:175],a[32:35],a[176:179],v[236:239]\ns_mov_b32 m0,s64\nds_read_b128 a[88:91],v241 offset:6144\nM v[172:175],a[36:39],a[180:183],v[172:175]\n" \
"buffer_load_dwordx4 v240,s[44:47],s36 offen offset:0 sc1 lds\nM v[172:175],a[40:43],a[184:187],v[172:175]\nbuffer_load_dwordx4 v240,s[44:47],s36 offen offset:1024 sc1 lds\nM v[172:175],a[44:47],a[188:191],v[172:175]\nbuffer_load_dwordx4 v240,s[44:47],s36 offen offset:2048 sc1 lds\nM v[188:191],a[48:51],a[176:179],v[236:239]\nbuffer_load_dwordx4 v240,s[44:47],s36 offen offset:3072 sc1 lds\nM v[188:191],a[52:55],a[180:183],v[188:191]\n" \
"ds_read_b128 a[92:95],v241 offset:7168\nds_read_b128 a[0:3],v240 offset:49152\nM v[188:191],a[56:59],a[184:187],v[188:191]\nds_read_b128 a[4:7],v240 offset:50176\nds_read_b128 a[8:11],v240 offset:51200\nM v[188:191],a[60:63],a[188:191],v[188:191]\nds_read_b128 a[12:15],v240 offset:52224\nds_read_b128 a[16:19],v240 offset:53248\n" \
"M v[0:3],a[64:67],v[64:67],0\nv_exp_f32 v128,v128\nM v[4:7],a[64:67],v[68:71],0\nv_exp_f32 v129,v129\nM v[8:11],a[68:71],v[64:67],0\nv_exp_f32 v130,v130\nM v[12:15],a[68:71],v[68:71],0\nv_exp_f32 v131,v131\n" \
"M v[16:19],a[72:75],v[64:67],0\nds_read_b128 a[20:23],v240 offset:54272\nv_cvt_pk_bf16_f32 v192,v128,v129\nM v[20:23],a[72:75],v[68:71],0\nv_cvt_pk_bf16_f32 v193,v130,v131\ns_waitcnt lgkmcnt(7)\nM v[24:27],a[76:79],v[64:67],0\nds_read_b128 a[24:27],v240 offset:55296\n" \
"ds_read_b128 a[28:31],v240 offset:56320\nM v[28:31],a[76:79],v[68:71],0\nv_exp_f32 v132,v132\nM v[32:35],a[80:83],v[64:67],0\nv_exp_f32 v133,v133\nM v[36:39],a[80:83],v[68:71],0\nv_exp_f32 v134,v134\nM v[40:43],a[84:87],v[64:67],0\n" \
"v_exp_f32 v135,v135\nM v[44:47],a[84:87],v[68:71],0\nv_cvt_pk_bf16_f32 v196,v132,v133\nv_cvt_pk_bf16_f32 v197,v134,v135\nM v[48:51],a[88:91],v[64:67],0\nv_exp_f32 v144,v144\nM v[52:55],a[88:91],v[68:71],0\nv_add_f32 v136,v136,v232\n" \
"s_waitcnt lgkmcnt(3)\nM v[56:59],a[92:95],v[64:67],0\nv_exp_f32 v145,v145\nM v[60:63],a[92:95],v[68:71],0\nv_exp_f32 v146,v146\nM a[192:195],a[208:211],v[64:67],0\nv_exp_f32 v147,v147\nM a[196:199],a[208:211],v[68:71],0\n" \
"v_cvt_pk_bf16_f32 v194,v144,v145\nv_cvt_pk_bf16_f32 v195,v146,v147\nM v[128:131],a[0:3],a[128:131],v[224:227]\nv_exp_f32 v148,v148\nM v[128:131],a[4:7],a[132:135],v[128:131]\nv_exp_f32 v149,v149\nM v[128:131],a[8:11],a[136:139],v[128:131]\nv_exp_f32 v150,v150\n" \
"M v[128:131],a[12:15],a[140:143],v[128:131]\nv_exp_f32 v151,v151\nM v[132:135],a[0:3],a[144:147],v[228:231]\nv_cvt_pk_bf16_f32 v198,v148,v149\nv_cvt_pk_bf16_f32 v199,v150,v151\nM v[132:135],a[4:7],a[148:151],v[132:135]\nv_add_f32 v137,v137,v232\nv_add_f32 v138,v138,v232\n" \
"M v[132:135],a[8:11],a[152:155],v[132:135]\nv_add_f32 v139,v139,v232\nv_add_f32 v140,v140,v236\nM v[132:135],a[12:15],a[156:159],v[132:135]\nv_exp_f32 v136,v136\nM v[144:147],a[16:19],a[128:131],v[224:227]\nv_add_f32 v141,v141,v236\ns_waitcnt lgkmcnt(0)\n" \
"M v[144:147],a[20:23],a[132:135],v[144:147]\nv_exp_f32 v137,v137\nM v[144:147],a[24:27],a[136:139],v[144:147]\nv_exp_f32 v138,v138\nM v[144:147],a[28:31],a[140:143],v[144:147]\nv_exp_f32 v139,v139\nM v[148:151],a[16:19],a[144:147],v[228:231]\nv_cvt_pk_bf16_f32 v200,v136,v137\n" \
"v_cvt_pk_bf16_f32 v201,v138,v139\nM v[148:151],a[20:23],a[148:151],v[148:151]\nv_add_f32 v142,v142,v236\nv_add_f32 v143,v143,v236\nM v[148:151],a[24:27],a[152:155],v[148:151]\nv_exp_f32 v140,v140\nM v[148:151],a[28:31],a[156:159],v[148:151]\nv_exp_f32 v141,v141\n" \
"M v[136:139],a[0:3],a[160:163],v[232:235]\nv_exp_f32 v142,v142\nM v[136:139],a[4:7],a[164:167],v[136:139]\nv_exp_f32 v143,v143\nM v[136:139],a[8:11],a[168:171],v[136:139]\nv_cvt_pk_bf16_f32 v204,v140,v141\nv_cvt_pk_bf16_f32 v205,v142,v143\nM v[136:139],a[12:15],a[172:175],v[136:139]\n" \
"ds_read_b128 a[32:35],v240 offset:57344\nv_add_f32 v152,v152,v232\nM v[140:143],a[0:3],a[176:179],v[236:239]\nv_add_f32 v153,v153,v232\nv_add_f32 v154,v154,v232\nM v[140:143],a[4:7],a[180:183],v[140:143]\nv_add_f32 v155,v155,v232\nds_read_b128 a[36:39],v240 offset:58368\n" \
"M v[140:143],a[8:11],a[184:187],v[140:143]\nv_exp_f32 v152,v152\nv_exp_f32 v153,v153\nv_exp_f32 v154,v154\nv_exp_f32 v155,v155\nM v[140:143],a[12:15],a[188:191],v[140:143]\nv_cvt_pk_bf16_f32 v202,v152,v153\nv_cvt_pk_bf16_f32 v203,v154,v155\n" \
"ds_read_b128 a[40:43],v240 offset:59392\nM v[152:155],a[16:19],a[160:163],v[232:235]\nds_read_b128 a[44:47],v240 offset:60416\nv_add_f32 v156,v156,v236\nM v[152:155],a[20:23],a[164:167],v[152:155]\nv_add_f32 v157,v157,v236\nv_add_f32 v158,v158,v236\nM v[152:155],a[24:27],a[168:171],v[152:155]\n" \
"v_add_f32 v159,v159,v236\nv_exp_f32 v156,v156\nv_exp_f32 v157,v157\nv_exp_f32 v158,v158\nv_exp_f32 v159,v159\nM v[152:155],a[28:31],a[172:175],v[152:155]\nv_cvt_pk_bf16_f32 v206,v156,v157\nv_cvt_pk_bf16_f32 v207,v158,v159\n" \
"M v[156:159],a[16:19],a[176:179],v[236:239]\nds_read_b128 a[48:51],v240 offset:61440\nds_read_b128 a[52:55],v240 offset:62464\nM v[156:159],a[20:23],a[180:183],v[156:159]\nv_exp_f32 v160,v160\nM v[156:159],a[24:27],a[184:187],v[156:159]\nv_exp_f32 v161,v161\nv_exp_f32 v162,v162\n" \
"v_exp_f32 v163,v163\nM v[156:159],a[28:31],a[188:191],v[156:159]\nv_cvt_pk_bf16_f32 v208,v160,v161\nv_cvt_pk_bf16_f32 v209,v162,v163\nds_read_b128 a[56:59],v240 offset:63488\ns_waitcnt lgkmcnt(3)\nM v[160:163],a[32:35],a[128:131],v[224:227]\nds_read_b128 a[60:63],v240 offset:64512\n" \
"v_cndmask_b32 v168,v168,v252,s[82:83]\nM v[160:163],a[36:39],a[132:135],v[160:163]\nv_exp_f32 v164,v164\nM v[160:163],a[40:43],a[136:139],v[160:163]\nv_exp_f32 v165,v165\nv_exp_f32 v166,v166\nv_exp_f32 v167,v167\nM v[160:163],a[44:47],a[140:143],v[160:163]\n" \
"v_cvt_pk_bf16_f32 v212,v164,v165\nv_cvt_pk_bf16_f32 v213,v166,v167\nM v[164:167],a[32:35],a[144:147],v[228:231]\nv_exp_f32 v176,v176\nM v[164:167],a[36:39],a[148:151],v[164:167]\nv_exp_f32 v177,v177\nM v[164:167],a[40:43],a[152:155],v[164:167]\nv_exp_f32 v178,v178\n" \
"v_exp_f32 v179,v179\nM v[164:167],a[44:47],a[156:159],v[164:167]\nv_cvt_pk_bf16_f32 v210,v176,v177\nv_cvt_pk_bf16_f32 v211,v178,v179\ns_waitcnt lgkmcnt(2)\nM v[176:179],a[48:51],a[128:131],v[224:227]\nv_exp_f32 v180,v180\nM v[176:179],a[52:55],a[132:135],v[176:179]\n" \
"v_cndmask_b32 v169,v169,v252,s[84:85]\ns_waitcnt lgkmcnt(0)\nM v[176:179],a[56:59],a[136:139],v[176:179]\nv_exp_f32 v181,v181\nv_exp_f32 v182,v182\nv_exp_f32 v183,v183\ns_waitcnt vmcnt(0)\ns_barrier\n" \
"M v[176:179],a[60:63],a[140:143],v[176:179]\nv_cvt_pk_bf16_f32 v214,v180,v181\nv_cvt_pk_bf16_f32 v215,v182,v183\nM v[180:183],a[48:51],a[144:147],v[228:231]\nv_cndmask_b32 v170,v170,v252,s[86:87]\nv_cndmask_b32 v171,v171,v252,s[88:89]\nM v[180:183],a[52:55],a[148:151],v[180:183]\nv_exp_f32 v168,v168\n" \
"M v[180:183],a[56:59],a[152:155],v[180:183]\nv_exp_f32 v169,v169\nv_exp_f32 v170,v170\nv_exp_f32 v171,v171\nM v[180:183],a[60:63],a[156:159],v[180:183]\nv_cvt_pk_bf16_f32 v216,v168,v169\nv_cvt_pk_bf16_f32 v217,v170,v171\nM v[168:171],a[32:35],a[160:163],v[232:235]\n" \
"v_exp_f32 v172,v172\nM v[168:171],a[36:39],a[164:167],v[168:171]\nv_exp_f32 v173,v173\nM v[168:171],a[40:43],a[168:171],v[168:171]\nv_exp_f32 v174,v174\nv_exp_f32 v175,v175\nM v[168:171],a[44:47],a[172:175],v[168:171]\nv_cvt_pk_bf16_f32 v220,v172,v173\n" \
"v_cvt_pk_bf16_f32 v221,v174,v175\nM v[172:175],a[32:35],a[176:179],v[236:239]\nds_read_b128 a[64:67],v241 offset:32768\nds_read_b128 a[68:71],v241 offset:33792\nM v[172:175],a[36:39],a[180:183],v[172:175]\nv_cndmask_b32 v188,v188,v252,s[82:83]\nv_cndmask_b32 v189,v189,v252,s[84:85]\nM v[172:175],a[40:43],a[184:187],v[172:175]\n" \
"v_cndmask_b32 v190,v190,v252,s[86:87]\nv_cndmask_b32 v191,v191,v252,s[88:89]\nM v[172:175],a[44:47],a[188:191],v[172:175]\nv_exp_f32 v188,v188\nM v[184:187],a[48:51],a[160:163],v[232:235]\nv_exp_f32 v189,v189\nM v[184:187],a[52:55],a[164:167],v[184:187]\nv_exp_f32 v190,v190\n" \
"M v[184:187],a[56:59],a[168:171],v[184:187]\nv_exp_f32 v191,v191\nM v[184:187],a[60:63],a[172:175],v[184:187]\nv_cvt_pk_bf16_f32 v222,v188,v189\nv_cvt_pk_bf16_f32 v223,v190,v191\nM v[188:191],a[48:51],a[176:179],v[236:239]\nds_read_b128 a[72:75],v241 offset:34816\nds_read_b128 a[76:79],v241 offset:35840\n" \
"M v[188:191],a[52:55],a[180:183],v[188:191]\nds_read_b128 a[80:83],v241 offset:36864\nds_read_b128 a[84:87],v241 offset:37888\nM v[188:191],a[56:59],a[184:187],v[188:191]\nds_read_b128 a[88:91],v241 offset:38912\nds_read_b128 a[92:95],v241 offset:39936\nM v[188:191],a[60:63],a[188:191],v[188:191]\nds_read_b128 a[96:99],v241 offset:40960\n" \
"s_waitcnt lgkmcnt(7)\nM v[0:3],a[64:67],v[192:195],v[0:3]\nbuffer_load_dword v248,v251,s[52:55],s80 offen sc1\nM v[4:7],a[64:67],v[196:199],v[4:7]\nds_read_b128 a[100:103],v241 offset:41984\nds_read_b128 a[104:107],v241 offset:43008\nM v[64:67],a[64:67],v[200:203],0\nv_mov_b32 v218,0\n" \
"v_mov_b32 v219,0\nM v[68:71],a[64:67],v[204:207],0\nds_read_b128 a[108:111],v241 offset:44032\nds_read_b128 a[112:115],v241 offset:45056\nM v[8:11],a[68:71],v[192:195],v[8:11]\nds_read_b128 a[116:119],v241 offset:46080\nds_read_b128 a[120:123],v241 offset:47104\nM v[12:15],a[68:71],v[196:199],v[12:15]\n" \
"ds_read_b128 a[124:127],v241 offset:48128\ns_lshl_b32 s69,s61,17\nM v[72:75],a[68:71],v[200:203],0\ns_add_u32 s69,s69,s68\ns_add_u32 m0,s64,131072\nM v[76:79],a[68:71],v[204:207],0\nds_read_b128 a[0:3],v240 offset:0\ns_waitcnt lgkmcnt(12)\n" \
"M v[16:19],a[72:75],v[192:195],v[16:19]\nbuffer_load_dwordx4 v243,s[40:43],s69 offen offset:0 lds\nM v[20:23],a[72:75],v[196:199],v[20:23]\nbuffer_load_dwordx4 v244,s[40:43],s69 offen offset:1024 lds\nM v[80:83],a[72:75],v[200:203],0\nbuffer_load_dwordx4 v245,s[40:43],s69 offen offset:2048 lds\nM v[84:87],a[72:75],v[204:207],0\nbuffer_load_dwordx4 v246,s[40:43],s69 offen offset:3072 lds\n" \
"M v[24:27],a[76:79],v[192:195],v[24:27]\nds_read_b128 a[4:7],v240 offset:1024\nds_read_b128 a[8:11],v240 offset:2048\nM v[28:31],a[76:79],v[196:199],v[28:31]\ns_lshl_b32 s69,s61,17\nds_read_b128 a[12:15],v240 offset:3072\nM v[88:91],a[76:79],v[200:203],0\ns_add_u32 s69,s69,s68\n" \
"ds_read_b128 a[16:19],v240 offset:4096\nM v[92:95],a[76:79],v[204:207],0\ns_add_u32 s69,s69,0x10000\ns_add_u32 m0,s64,147456\nM v[32:35],a[80:83],v[192:195],v[32:35]\nds_read_b128 a[20:23],v240 offset:5120\nds_read_b128 a[24:27],v240 offset:6144\nM v[36:39],a[80:83],v[196:199],v[36:39]\n" \
"buffer_load_dwordx4 v243,s[40:43],s69 offen offset:0 lds\nM v[96:99],a[80:83],v[200:203],0\nbuffer_load_dwordx4 v244,s[40:43],s69 offen offset:1024 lds\nM v[100:103],a[80:83],v[204:207],0\nds_read_b128 a[28:31],v240 offset:7168\ns_waitcnt lgkmcnt(7)\nM v[40:43],a[84:87],v[192:195],v[40:43]\nbuffer_load_dwordx4 v245,s[40:43],s69 offen offset:2048 lds\n" \
"M v[44:47],a[84:87],v[196:199],v[44:47]\nbuffer_load_dwordx4 v246,s[40:43],s69 offen offset:3072 lds\nM v[104:107],a[84:87],v[200:203],0\ns_add_u32 m0,s64,65536\nds_read_b128 a[32:35],v240 offset:8192\nM v[108:111],a[84:87],v[204:207],0\nds_read_b128 a[36:39],v240 offset:9216\nds_read_b128 a[40:43],v240 offset:10240\n" \
"M v[48:51],a[88:91],v[192:195],v[48:51]\nds_read_b128 a[44:47],v240 offset:11264\nds_read_b128 a[48:51],v240 offset:12288\nM v[52:55],a[88:91],v[196:199],v[52:55]\nds_read_b128 a[52:55],v240 offset:13312\nds_read_b128 a[56:59],v240 offset:14336\nM v[112:115],a[88:91],v[200:203],0\nds_read_b128 a[60:63],v240 offset:15360\n" \
"M v[116:119],a[88:91],v[204:207],0\nv_exp_f32 v128,v128\nM v[56:59],a[92:95],v[192:195],v[56:59]\nv_exp_f32 v129,v129\nM v[60:63],a[92:95],v[196:199],v[60:63]\nv_exp_f32 v130,v130\nM v[120:123],a[92:95],v[200:203],0\nv_exp_f32 v131,v131\n" \
"M v[124:127],a[92:95],v[204:207],0\nv_exp_f32 v132,v132\nM a[192:195],a[208:211],v[192:195],a[192:195]\nv_exp_f32 v133,v133\nM a[196:199],a[208:211],v[196:199],a[196:199]\nv_exp_f32 v134,v134\nM a[200:203],a[208:211],v[200:203],0\nv_exp_f32 v135,v135\n" \
"M a[204:207],a[208:211],v[204:207],0\nv_exp_f32 v144,v144\nM v[0:3],a[96:99],v[208:211],v[0:3]\nv_exp_f32 v145,v145\nM v[4:7],a[96:99],v[212:215],v[4:7]\nv_exp_f32 v146,v146\nM v[64:67],a[96:99],v[216:219],v[64:67]\nv_exp_f32 v147,v147\n" \
"M v[68:71],a[96:99],v[220:223],v[68:71]\nv_exp_f32 v148,v148\nM v[8:11],a[100:103],v[208:211],v[8:11]\nv_exp_f32 v149,v149\nM v[12:15],a[100:103],v[212:215],v[12:15]\nv_exp_f32 v150,v150\nM v[72:75],a[100:103],v[216:219],v[72:75]\ns_barrier\n" \
"v_cvt_pk_bf16_f32 v192,v128,v129\nM v[76:79],a[100:103],v[220:223],v[76:79]\nv_exp_f32 v151,v151\nM v[16:19],a[104:107],v[208:211],v[16:19]\nv_exp_f32 v136,v136\nM v[20:23],a[104:107],v[212:215],v[20:23]\nv_exp_f32 v137,v137\nM v[80:83],a[104:107],v[216:219],v[80:83]\n" \
"s_waitcnt vmcnt(8)\nv_cmp_eq_u32 vcc,0,v248\nM v[84:87],a[104:107],v[220:223],v[84:87]\ns_cbranch_vccnz .Lps2\n.Lpb2:\nv_cvt_pk_bf16_f32 v193,v130,v131\nM v[24:27],a[108:111],v[208:211],v[24:27]\nbuffer_load_dwordx4 v240,s[48:51],s36 offen offset:0 sc1 lds\n" \
"M v[28:31],a[108:111],v[212:215],v[28:31]\nbuffer_load_dwordx4 v240,s[48:51],s36 offen offset:1024 sc1 lds\nM v[88:91],a[108:111],v[216:219],v[88:91]\nbuffer_load_dwordx4 v240,s[48:51],s36 offen offset:2048 sc1 lds\nM v[92:95],a[108:111],v[220:223],v[92:95]\nbuffer_load_dwordx4 v240,s[48:51],s36 offen offset:3072 sc1 lds\nM v[32:35],a[112:115],v[208:211],v[32:35]\ns_add_u32 m0,s64,16384\n" \
"v_cvt_pk_bf16_f32 v194,v144,v145\nM v[36:39],a[112:115],v[212:215],v[36:39]\nbuffer_load_dwordx4 v240,s[44:47],s65 offen offset:0 sc1 lds\nM v[96:99],a[112:115],v[216:219],v[96:99]\nbuffer_load_dwordx4 v240,s[44:47],s65 offen offset:1024 sc1 lds\nM v[100:103],a[112:115],v[220:223],v[100:103]\nbuffer_load_dwordx4 v240,s[44:47],s65 offen offset:2048 sc1 lds\nM v[40:43],a[116:119],v[208:211],v[40:43]\n" \
"buffer_load_dwordx4 v240,s[44:47],s65 offen offset:3072 sc1 lds\nM v[44:47],a[116:119],v[212:215],v[44:47]\ns_add_u32 m0,s64,81920\nv_cvt_pk_bf16_f32 v195,v146,v147\nM v[104:107],a[116:119],v[216:219],v[104:107]\nbuffer_load_dwordx4 v240,s[48:51],s65 offen offset:0 sc1 lds\nM v[108:111],a[116:119],v[220:223],v[108:111]\nbuffer_load_dwordx4 v240,s[48:51],s65 offen offset:1024 sc1 lds\n" \
"M v[48:51],a[120:123],v[208:211],v[48:51]\nbuffer_load_dwordx4 v240,s[48:51],s65 offen offset:2048 sc1 lds\nM v[52:55],a[120:123],v[212:215],v[52:55]\nbuffer_load_dwordx4 v240,s[48:51],s65 offen offset:3072 sc1 lds\nM v[112:115],a[120:123],v[216:219],v[112:115]\ns_add_u32 m0,s64,32768\nv_cvt_pk_bf16_f32 v196,v132,v133\nM v[116:119],a[120:123],v[220:223],v[116:119]\n" \
"buffer_load_dwordx4 v240,s[44:47],s66 offen offset:0 sc1 lds\nM v[56:59],a[124:127],v[208:211],v[56:59]\nbuffer_load_dwordx4 v240,s[44:47],s66 offen offset:1024 sc1 lds\nM v[60:63],a[124:127],v[212:215],v[60:63]\nbuffer_load_dwordx4 v240,s[44:47],s66 offen offset:2048 sc1 lds\nM v[120:123],a[124:127],v[216:219],v[120:123]\nbuffer_load_dwordx4 v240,s[44:47],s66 offen offset:3072 sc1 lds\nM v[124:127],a[124:127],v[220:223],v[124:127]\n" \
"v_exp_f32 v138,v138\nM a[192:195],a[208:211],v[208:211],a[192:195]\nv_exp_f32 v139,v139\nM a[196:199],a[208:211],v[212:215],a[196:199]\nv_exp_f32 v140,v140\nv_exp_f32 v141,v141\nv_exp_f32 v142,v142\nv_exp_f32 v143,v143\n" \
"v_exp_f32 v152,v152\nv_exp_f32 v153,v153\nv_exp_f32 v154,v154\nv_exp_f32 v155,v155\nv_exp_f32 v156,v156\nv_exp_f32 v157,v157\nv_exp_f32 v158,v158\nv_exp_f32 v159,v159\n" \
"M a[200:203],a[208:211],v[216:219],a[200:203]\ns_add_u32 m0,s64,98304\nv_exp_f32 v160,v160\nv_exp_f32 v161,v161\nv_exp_f32 v162,v162\nv_exp_f32 v163,v163\nv_exp_f32 v164,v164\nv_exp_f32 v165,v165\n" \
"v_exp_f32 v166,v166\nv_exp_f32 v167,v167\nv_exp_f32 v176,v176\nv_exp_f32 v177,v177\nv_exp_f32 v178,v178\nv_exp_f32 v179,v179\nv_exp_f32 v180,v180\nv_exp_f32 v181,v181\n" \
"v_exp_f32 v182,v182\nv_exp_f32 v183,v183\nv_exp_f32 v168,v168\nv_exp_f32 v169,v169\nv_exp_f32 v170,v170\nv_exp_f32 v171,v171\nv_cvt_pk_bf16_f32 v197,v134,v135\nv_cvt_pk_bf16_f32 v198,v148,v149\n" \
"v_cvt_pk_bf16_f32 v199,v150,v151\nv_cvt_pk_bf16_f32 v200,v136,v137\nv_cvt_pk_bf16_f32 v201,v138,v139\nv_cvt_pk_bf16_f32 v202,v152,v153\nv_cvt_pk_bf16_f32 v203,v154,v155\nv_cvt_pk_bf16_f32 v204,v140,v141\nv_cvt_pk_bf16_f32 v205,v142,v143\nv_cvt_pk_bf16_f32 v206,v156,v157\n" \
"v_cvt_pk_bf16_f32 v207,v158,v159\nbuffer_load_dwordx4 v240,s[48:51],s66 offen offset:0 sc1 lds\nbuffer_load_dwordx4 v240,s[48:51],s66 offen offset:1024 sc1 lds\nbuffer_load_dwordx4 v240,s[48:51],s66 offen offset:2048 sc1 lds\nbuffer_load_dwordx4 v240,s[48:51],s66 offen offset:3072 sc1 lds\nM a[204:207],a[208:211],v[220:223],a[204:207]\ns_cmp_eq_u32 s62,0\ns_cbranch_scc1 .Ltrans\n" \
"s_branch .Ldual\n.Lpro1:\ns_barrier\nM v[64:67],a[0:3],a[128:131],0\nds_read_b128 a[64:67],v241 offset:0\nds_read_b128 a[68:71],v241 offset:1024\nM v[64:67],a[4:7],a[132:135],v[64:67]\nds_read_b128 a[72:75],v241 offset:2048\n" \
"ds_read_b128 a[76:79],v241 offset:3072\nM v[64:67],a[8:11],a[136:139],v[64:67]\nds_read_b128 a[80:83],v241 offset:4096\nds_read_b128 a[84:87],v241 offset:5120\nM v[64:67],a[12:15],a[140:143],v[64:67]\ns_mov_b32 m0,s64\nds_read_b128 a[88:91],v241 offset:6144\nM v[68:71],a[0:3],a[144:147],0\n" \
"ds_read_b128 a[0:3],v240 offset:32768\nds_read_b128 a[92:95],v241 offset:7168\nM v[68:71],a[4:7],a[148:151],v[68:71]\nds_read_b128 a[4:7],v240 offset:33792\nds_read_b128 a[96:99],v241 offset:8192\nM v[68:71],a[8:11],a[152:155],v[68:71]\nds_read_b128 a[8:11],v240 offset:34816\nv_max3_f32 v225,v64,v65,v66\n" \
"M v[68:71],a[12:15],a[156:159],v[68:71]\nds_read_b128 a[12:15],v240 offset:35840\nds_read_b128 a[100:103],v241 offset:9216\nM v[72:75],a[16:19],a[128:131],0\nbuffer_load_dwordx4 v240,s[44:47],s36 offen offset:0 sc1 lds\nM v[72:75],a[20:23],a[132:135],v[72:75]\nbuffer_load_dwordx4 v240,s[44:47],s36 offen offset:1024 sc1 lds\nM v[72:75],a[24:27],a[136:139],v[72:75]\n" \
"buffer_load_dwordx4 v240,s[44:47],s36 offen offset:2048 sc1 lds\nM v[72:75],a[28:31],a[140:143],v[72:75]\nv_max3_f32 v229,v68,v69,v70\nds_read_b128 a[104:107],v241 offset:10240\nM v[76:79],a[16:19],a[144:147],0\nds_read_b128 a[16:19],v240 offset:36864\nds_read_b128 a[108:111],v241 offset:11264\nM v[76:79],a[20:23],a[148:151],v[76:79]\n" \
"ds_read_b128 a[20:23],v240 offset:37888\nds_read_b128 a[112:115],v241 offset:12288\nM v[76:79],a[24:27],a[152:155],v[76:79]\nds_read_b128 a[24:27],v240 offset:38912\nv_max3_f32 v225,v225,v67,v72\nM v[76:79],a[28:31],a[156:159],v[76:79]\nds_read_b128 a[28:31],v240 offset:39936\nv_max3_f32 v225,v225,v73,v74\n" \
"M v[80:83],a[32:35],a[128:131],0\nv_max_f32 v225,v225,v75\nv_cndmask_b32 v225,v224,v225,s[90:91]\nM v[80:83],a[36:39],a[132:135],v[80:83]\nbuffer_load_dwordx4 v240,s[44:47],s36 offen offset:3072 sc1 lds\nM v[80:83],a[40:43],a[136:139],v[80:83]\nv_max3_f32 v229,v229,v71,v76\nv_max3_f32 v229,v229,v77,v78\n" \
"M v[80:83],a[44:47],a[140:143],v[80:83]\nv_max_f32 v229,v229,v79\nv_cndmask_b32 v229,v228,v229,s[90:91]\nM v[84:87],a[32:35],a[144:147],0\nds_read_b128 a[32:35],v240 offset:40960\nv_permlane16_swap_b32 v225,v229\nM v[84:87],a[36:39],a[148:151],v[84:87]\nv_max_f32 v225,v225,v229\n" \
"v_mov_b32 v229,v225\nM v[84:87],a[40:43],a[152:155],v[84:87]\nds_read_b128 a[36:39],v240 offset:41984\nv_permlane32_swap_b32 v225,v229\nM v[84:87],a[44:47],a[156:159],v[84:87]\nv_max_f32 v225,v225,v229\nv_mov_b32 v229,v225\nM v[92:95],a[48:51],a[144:147],0\n" \
"ds_read_b128 a[40:43],v240 offset:43008\nv_permlane16_swap_b32 v225,v229\nM v[92:95],a[52:55],a[148:151],v[92:95]\nv_sub_f32 v224,0,v225\nv_sub_f32 v226,0,v225\nM v[92:95],a[56:59],a[152:155],v[92:95]\nv_sub_f32 v227,0,v225\nv_sub_f32 v225,0,v225\n" \
"M v[92:95],a[60:63],a[156:159],v[92:95]\nv_sub_f32 v228,0,v229\ns_waitcnt lgkmcnt(3)\nM v[136:139],a[0:3],a[160:163],0\nv_sub_f32 v230,0,v229\nv_sub_f32 v231,0,v229\nM v[136:139],a[4:7],a[164:167],v[136:139]\nv_sub_f32 v229,0,v229\n" \
"ds_read_b128 a[44:47],v240 offset:44032\nM v[136:139],a[8:11],a[168:171],v[136:139]\nds_read_b128 a[48:51],v240 offset:45056\nds_read_b128 a[52:55],v240 offset:46080\nM v[136:139],a[12:15],a[172:175],v[136:139]\nds_read_b128 a[56:59],v240 offset:47104\nds_read_b128 a[60:63],v240 offset:48128\nM v[140:143],a[0:3],a[176:179],0\n" \
"v_add_f32 v64,v64,v224\nv_add_f32 v65,v65,v224\nM v[140:143],a[4:7],a[180:183],v[140:143]\nv_add_f32 v66,v66,v224\nv_add_f32 v67,v67,v224\nM v[140:143],a[8:11],a[184:187],v[140:143]\nv_exp_f32 v64,v64\nM v[140:143],a[12:15],a[188:191],v[140:143]\n" \
"v_exp_f32 v65,v65\nM v[156:159],a[16:19],a[176:179],0\nv_exp_f32 v66,v66\nM v[156:159],a[20:23],a[180:183],v[156:159]\nv_exp_f32 v67,v67\nM v[156:159],a[24:27],a[184:187],v[156:159]\nv_add_f32 v68,v68,v228\nv_add_f32 v69,v69,v228\n" \
"M v[156:159],a[28:31],a[188:191],v[156:159]\nv_add_f32 v72,v72,v224\nv_add_f32 v73,v73,v224\nM v[128:131],a[0:3],a[128:131],v[224:227]\nv_add_f32 v74,v74,v224\nv_add_f32 v75,v75,v224\nM v[128:131],a[4:7],a[132:135],v[128:131]\nv_add_f32 v70,v70,v228\n" \
"v_add_f32 v71,v71,v228\nM v[128:131],a[8:11],a[136:139],v[128:131]\nv_exp_f32 v68,v68\nM v[128:131],a[12:15],a[140:143],v[128:131]\nv_exp_f32 v69,v69\nM v[132:135],a[0:3],a[144:147],v[228:231]\nv_exp_f32 v72,v72\nM v[132:135],a[4:7],a[148:151],v[132:135]\n" \
"v_exp_f32 v73,v73\nM v[132:135],a[8:11],a[152:155],v[132:135]\nv_exp_f32 v74,v74\nM v[132:135],a[12:15],a[156:159],v[132:135]\nv_exp_f32 v75,v75\nM v[144:147],a[16:19],a[128:131],v[224:227]\nv_cvt_pk_bf16_f32 v64,v64,v65\nv_add_f32 v76,v76,v228\n" \
"M v[144:147],a[20:23],a[132:135],v[144:147]\nv_exp_f32 v70,v70\nM v[144:147],a[24:27],a[136:139],v[144:147]\nv_exp_f32 v71,v71\nM v[144:147],a[28:31],a[140:143],v[144:147]\nv_add_f32 v77,v77,v228\nv_add_f32 v78,v78,v228\nM v[148:151],a[16:19],a[144:147],v[228:231]\n" \
"v_add_f32 v79,v79,v228\nv_cvt_pk_bf16_f32 v65,v66,v67\nM v[148:151],a[20:23],a[148:151],v[148:151]\nv_exp_f32 v76,v76\nM v[148:151],a[24:27],a[152:155],v[148:151]\nv_exp_f32 v77,v77\nM v[148:151],a[28:31],a[156:159],v[148:151]\nv_cvt_pk_bf16_f32 v66,v72,v73\n" \
"s_waitcnt lgkmcnt(0)\nM v[160:163],a[32:35],a[128:131],v[224:227]\nv_exp_f32 v78,v78\nM v[160:163],a[36:39],a[132:135],v[160:163]\nv_exp_f32 v79,v79\nM v[160:163],a[40:43],a[136:139],v[160:163]\nv_cvt_pk_bf16_f32 v67,v74,v75\nv_cvt_pk_bf16_f32 v68,v68,v69\n" \
"M v[160:163],a[44:47],a[140:143],v[160:163]\nv_cvt_pk_bf16_f32 v69,v70,v71\nv_cvt_pk_bf16_f32 v70,v76,v77\nM v[164:167],a[32:35],a[144:147],v[228:231]\nv_cvt_pk_bf16_f32 v71,v78,v79\nv_cndmask_b32 v80,v80,v252,s[82:83]\nM v[164:167],a[36:39],a[148:151],v[164:167]\nv_cndmask_b32 v81,v81,v252,s[84:85]\n" \
"v_cndmask_b32 v82,v82,v252,s[86:87]\nM v[164:167],a[40:43],a[152:155],v[164:167]\nv_cndmask_b32 v83,v83,v252,s[88:89]\nv_add_f32 v80,v80,v224\nM v[164:167],a[44:47],a[156:159],v[164:167]\nv_add_f32 v81,v81,v224\nv_add_f32 v82,v82,v224\nM v[176:179],a[48:51],a[128:131],v[224:227]\n" \
"v_add_f32 v83,v83,v224\nv_add_f32 v84,v84,v228\nM v[176:179],a[52:55],a[132:135],v[176:179]\nv_exp_f32 v80,v80\nM v[176:179],a[56:59],a[136:139],v[176:179]\nv_exp_f32 v81,v81\nM v[176:179],a[60:63],a[140:143],v[176:179]\nv_exp_f32 v82,v82\n" \
"M v[180:183],a[48:51],a[144:147],v[228:231]\nv_exp_f32 v83,v83\nM v[180:183],a[52:55],a[148:151],v[180:183]\nv_add_f32 v85,v85,v228\nv_cndmask_b32 v92,v92,v252,s[82:83]\nM v[180:183],a[56:59],a[152:155],v[180:183]\nv_cndmask_b32 v93,v93,v252,s[84:85]\nv_cndmask_b32 v94,v94,v252,s[86:87]\n" \
"M v[180:183],a[60:63],a[156:159],v[180:183]\nv_cndmask_b32 v95,v95,v252,s[88:89]\nv_add_f32 v86,v86,v228\nM v[0:3],a[64:67],v[64:67],0\nv_add_f32 v87,v87,v228\nv_cvt_pk_bf16_f32 v80,v80,v81\nM v[4:7],a[64:67],v[68:71],0\nv_exp_f32 v84,v84\n" \
"M v[8:11],a[68:71],v[64:67],0\nv_exp_f32 v85,v85\nM v[12:15],a[68:71],v[68:71],0\nv_exp_f32 v86,v86\nM v[16:19],a[72:75],v[64:67],0\nv_exp_f32 v87,v87\nM v[20:23],a[72:75],v[68:71],0\nv_add_f32 v92,v92,v228\n" \
"v_add_f32 v93,v93,v228\nM v[24:27],a[76:79],v[64:67],0\nv_add_f32 v94,v94,v228\nv_add_f32 v95,v95,v228\nM v[28:31],a[76:79],v[68:71],0\nv_cvt_pk_bf16_f32 v81,v82,v83\nv_mov_b32 v82,0\nM v[32:35],a[80:83],v[64:67],0\n" \
"v_exp_f32 v92,v92\nM v[36:39],a[80:83],v[68:71],0\nv_exp_f32 v93,v93\nM v[40:43],a[84:87],v[64:67],0\nv_exp_f32 v94,v94\nM v[44:47],a[84:87],v[68:71],0\nv_exp_f32 v95,v95\nM v[48:51],a[88:91],v[64:67],0\n" \
"v_mov_b32 v83,0\nv_cvt_pk_bf16_f32 v84,v84,v85\nM v[52:55],a[88:91],v[68:71],0\nv_cvt_pk_bf16_f32 v85,v86,v87\nv_cvt_pk_bf16_f32 v86,v92,v93\nM v[56:59],a[92:95],v[64:67],0\nv_cvt_pk_bf16_f32 v87,v94,v95\nds_read_b128 a[116:119],v241 offset:13312\n" \
"M v[60:63],a[92:95],v[68:71],0\nds_read_b128 a[120:123],v241 offset:14336\nds_read_b128 a[124:127],v241 offset:15360\nM a[192:195],a[208:211],v[64:67],0\nds_read_b128 a[0:3],v240 offset:49152\nds_read_b128 a[4:7],v240 offset:50176\nM a[196:199],a[208:211],v[68:71],0\nds_read_b128 a[8:11],v240 offset:51200\n" \
"ds_read_b128 a[12:15],v240 offset:52224\nM v[0:3],a[96:99],v[80:83],v[0:3]\nds_read_b128 a[16:19],v240 offset:53248\nds_read_b128 a[20:23],v240 offset:54272\nM v[4:7],a[96:99],v[84:87],v[4:7]\nv_exp_f32 v128,v128\nM v[8:11],a[100:103],v[80:83],v[8:11]\nv_exp_f32 v129,v129\n" \
"M v[12:15],a[100:103],v[84:87],v[12:15]\nv_exp_f32 v130,v130\nM v[16:19],a[104:107],v[80:83],v[16:19]\nv_exp_f32 v131,v131\nM v[20:23],a[104:107],v[84:87],v[20:23]\nv_cvt_pk_bf16_f32 v192,v128,v129\nv_cvt_pk_bf16_f32 v193,v130,v131\nM v[24:27],a[108:111],v[80:83],v[24:27]\n" \
"ds_read_b128 a[24:27],v240 offset:55296\nds_read_b128 a[28:31],v240 offset:56320\nM v[28:31],a[108:111],v[84:87],v[28:31]\nv_exp_f32 v132,v132\nM v[32:35],a[112:115],v[80:83],v[32:35]\nv_exp_f32 v133,v133\nM v[36:39],a[112:115],v[84:87],v[36:39]\nv_cvt_pk_bf16_f32 v196,v132,v133\n" \
"s_waitcnt lgkmcnt(5)\nM v[40:43],a[116:119],v[80:83],v[40:43]\nv_exp_f32 v134,v134\nM v[44:47],a[116:119],v[84:87],v[44:47]\nv_exp_f32 v135,v135\nM v[48:51],a[120:123],v[80:83],v[48:51]\nv_cvt_pk_bf16_f32 v197,v134,v135\nv_max3_f32 v237,v140,v141,v142\n" \
"M v[52:55],a[120:123],v[84:87],v[52:55]\nv_exp_f32 v144,v144\nM v[56:59],a[124:127],v[80:83],v[56:59]\nv_exp_f32 v145,v145\nM v[60:63],a[124:127],v[84:87],v[60:63]\nv_exp_f32 v146,v146\nM a[192:195],a[208:211],v[80:83],a[192:195]\nv_exp_f32 v147,v147\n" \
"M a[196:199],a[208:211],v[84:87],a[196:199]\nv_cvt_pk_bf16_f32 v194,v144,v145\nv_cvt_pk_bf16_f32 v195,v146,v147\nM v[128:131],a[0:3],a[128:131],v[224:227]\nv_max3_f32 v237,v237,v143,v156\nv_max3_f32 v233,v136,v137,v138\nM v[128:131],a[4:7],a[132:135],v[128:131]\nv_max3_f32 v237,v237,v157,v158\n" \
"v_max_f32 v233,v233,v139\nM v[128:131],a[8:11],a[136:139],v[128:131]\nv_max_f32 v237,v237,v159\ns_waitcnt lgkmcnt(0)\nM v[128:131],a[12:15],a[140:143],v[128:131]\nv_cndmask_b32 v233,v232,v233,s[90:91]\nv_cndmask_b32 v237,v236,v237,s[90:91]\nM v[132:135],a[0:3],a[144:147],v[228:231]\n" \
"v_exp_f32 v148,v148\nM v[132:135],a[4:7],a[148:151],v[132:135]\nv_permlane16_swap_b32 v233,v237\nv_max_f32 v233,v233,v237\nM v[132:135],a[8:11],a[152:155],v[132:135]\nv_exp_f32 v149,v149\nM v[132:135],a[12:15],a[156:159],v[132:135]\nv_exp_f32 v150,v150\n" \
"M v[144:147],a[16:19],a[128:131],v[224:227]\nv_exp_f32 v151,v151\nM v[144:147],a[20:23],a[132:135],v[144:147]\nv_mov_b32 v237,v233\nv_cvt_pk_bf16_f32 v198,v148,v149\nM v[144:147],a[24:27],a[136:139],v[144:147]\nv_permlane32_swap_b32 v233,v237\nv_cvt_pk_bf16_f32 v199,v150,v151\n" \
"M v[144:147],a[28:31],a[140:143],v[144:147]\nv_max_f32 v233,v233,v237\nv_mov_b32 v237,v233\nM v[148:151],a[16:19],a[144:147],v[228:231]\nv_cndmask_b32 v136,v136,v252,s[82:83]\nv_permlane16_swap_b32 v233,v237\nM v[148:151],a[20:23],a[148:151],v[148:151]\nv_cndmask_b32 v137,v137,v252,s[84:85]\n" \
"v_cndmask_b32 v138,v138,v252,s[86:87]\nv_cndmask_b32 v139,v139,v252,s[88:89]\nv_sub_f32 v232,0,v233\nM v[148:151],a[24:27],a[152:155],v[148:151]\nv_sub_f32 v234,0,v233\nv_sub_f32 v235,0,v233\nv_add_f32 v136,v136,v232\nv_add_f32 v137,v137,v232\n" \
"v_add_f32 v138,v138,v232\nv_add_f32 v139,v139,v232\nv_sub_f32 v233,0,v233\nv_exp_f32 v136,v136\nv_exp_f32 v137,v137\nv_exp_f32 v138,v138\nv_exp_f32 v139,v139\nM v[148:151],a[28:31],a[156:159],v[148:151]\n" \
"v_cvt_pk_bf16_f32 v200,v136,v137\nv_cvt_pk_bf16_f32 v201,v138,v139\nM v[136:139],a[0:3],a[160:163],v[232:235]\nv_sub_f32 v236,0,v237\nv_sub_f32 v238,0,v237\nM v[136:139],a[4:7],a[164:167],v[136:139]\nv_sub_f32 v239,0,v237\nv_add_f32 v140,v140,v236\n" \
"M v[136:139],a[8:11],a[168:171],v[136:139]\nv_add_f32 v141,v141,v236\nv_add_f32 v142,v142,v236\nv_add_f32 v143,v143,v236\nv_sub_f32 v237,0,v237\nv_exp_f32 v140,v140\nv_exp_f32 v141,v141\nv_exp_f32 v142,v142\n" \
"v_exp_f32 v143,v143\nM v[136:139],a[12:15],a[172:175],v[136:139]\nv_cvt_pk_bf16_f32 v204,v140,v141\nv_cvt_pk_bf16_f32 v205,v142,v143\nM v[140:143],a[0:3],a[176:179],v[236:239]\nds_read_b128 a[32:35],v240 offset:57344\nds_read_b128 a[36:39],v240 offset:58368\nM v[140:143],a[4:7],a[180:183],v[140:143]\n" \
"ds_read_b128 a[40:43],v240 offset:59392\nds_read_b128 a[44:47],v240 offset:60416\nM v[140:143],a[8:11],a[184:187],v[140:143]\ns_waitcnt vmcnt(0)\ns_barrier\nM v[140:143],a[12:15],a[188:191],v[140:143]\nv_cndmask_b32 v156,v156,v252,s[82:83]\nv_cndmask_b32 v157,v157,v252,s[84:85]\n" \
"M v[152:155],a[16:19],a[160:163],v[232:235]\nv_cndmask_b32 v158,v158,v252,s[86:87]\nv_cndmask_b32 v159,v159,v252,s[88:89]\nM v[152:155],a[20:23],a[164:167],v[152:155]\nv_add_f32 v156,v156,v236\nv_add_f32 v157,v157,v236\nM v[152:155],a[24:27],a[168:171],v[152:155]\nv_add_f32 v158,v158,v236\n" \
"v_add_f32 v159,v159,v236\nv_exp_f32 v156,v156\nv_exp_f32 v157,v157\nv_exp_f32 v158,v158\nv_exp_f32 v159,v159\nM v[152:155],a[28:31],a[172:175],v[152:155]\nv_cvt_pk_bf16_f32 v206,v156,v157\nv_cvt_pk_bf16_f32 v207,v158,v159\n" \
"M v[156:159],a[16:19],a[176:179],v[236:239]\nds_read_b128 a[48:51],v240 offset:61440\nds_read_b128 a[52:55],v240 offset:62464\nM v[156:159],a[20:23],a[180:183],v[156:159]\nv_exp_f32 v160,v160\nM v[156:159],a[24:27],a[184:187],v[156:159]\nv_exp_f32 v161,v161\nv_exp_f32 v162,v162\n" \
"v_exp_f32 v163,v163\nM v[156:159],a[28:31],a[188:191],v[156:159]\nv_cvt_pk_bf16_f32 v208,v160,v161\nv_cvt_pk_bf16_f32 v209,v162,v163\nds_read_b128 a[56:59],v240 offset:63488\ns_waitcnt lgkmcnt(3)\nM v[160:163],a[32:35],a[128:131],v[224:227]\nds_read_b128 a[60:63],v240 offset:64512\n" \
"ds_read_b128 a[64:67],v241 offset:32768\nM v[160:163],a[36:39],a[132:135],v[160:163]\nv_exp_f32 v164,v164\nM v[160:163],a[40:43],a[136:139],v[160:163]\nv_exp_f32 v165,v165\nv_exp_f32 v166,v166\nv_exp_f32 v167,v167\nM v[160:163],a[44:47],a[140:143],v[160:163]\n" \
"v_cvt_pk_bf16_f32 v212,v164,v165\nv_cvt_pk_bf16_f32 v213,v166,v167\nM v[164:167],a[32:35],a[144:147],v[228:231]\nv_exp_f32 v176,v176\nM v[164:167],a[36:39],a[148:151],v[164:167]\nv_exp_f32 v177,v177\nM v[164:167],a[40:43],a[152:155],v[164:167]\nv_exp_f32 v178,v178\n" \
"v_exp_f32 v179,v179\nM v[164:167],a[44:47],a[156:159],v[164:167]\nv_cvt_pk_bf16_f32 v210,v176,v177\nv_cvt_pk_bf16_f32 v211,v178,v179\ns_waitcnt lgkmcnt(3)\nM v[176:179],a[48:51],a[128:131],v[224:227]\nv_exp_f32 v180,v180\nM v[176:179],a[52:55],a[132:135],v[176:179]\n" \
"ds_read_b128 a[68:71],v241 offset:33792\ns_waitcnt lgkmcnt(1)\nM v[176:179],a[56:59],a[136:139],v[176:179]\nv_exp_f32 v181,v181\nv_exp_f32 v182,v182\nv_exp_f32 v183,v183\nM v[176:179],a[60:63],a[140:143],v[176:179]\nv_cvt_pk_bf16_f32 v214,v180,v181\n" \
"v_cvt_pk_bf16_f32 v215,v182,v183\nM v[180:183],a[48:51],a[144:147],v[228:231]\nds_read_b128 a[72:75],v241 offset:34816\nv_mov_b32 v202,0\nM v[180:183],a[52:55],a[148:151],v[180:183]\nbuffer_load_dword v248,v251,s[52:55],s80 offen sc1\nM v[180:183],a[56:59],a[152:155],v[180:183]\nv_mov_b32 v203,0\n" \
"ds_read_b128 a[76:79],v241 offset:35840\nM v[180:183],a[60:63],a[156:159],v[180:183]\nds_read_b128 a[80:83],v241 offset:36864\nds_read_b128 a[84:87],v241 offset:37888\nM v[168:171],a[32:35],a[160:163],v[232:235]\nds_read_b128 a[88:91],v241 offset:38912\nds_read_b128 a[92:95],v241 offset:39936\nM v[168:171],a[36:39],a[164:167],v[168:171]\n" \
"ds_read_b128 a[96:99],v241 offset:40960\nds_read_b128 a[100:103],v241 offset:41984\nM v[168:171],a[40:43],a[168:171],v[168:171]\nds_read_b128 a[104:107],v241 offset:43008\nds_read_b128 a[108:111],v241 offset:44032\nM v[168:171],a[44:47],a[172:175],v[168:171]\nds_read_b128 a[112:115],v241 offset:45056\nds_read_b128 a[116:119],v241 offset:46080\n" \
"M v[172:175],a[32:35],a[176:179],v[236:239]\nds_read_b128 a[120:123],v241 offset:47104\nds_read_b128 a[124:127],v241 offset:48128\nM v[172:175],a[36:39],a[180:183],v[172:175]\ns_lshl_b32 s69,s61,17\ns_add_u32 s69,s69,s68\nM v[172:175],a[40:43],a[184:187],v[172:175]\ns_add_u32 m0,s64,131072\n" \
"ds_read_b128 a[0:3],v240 offset:0\nM v[172:175],a[44:47],a[188:191],v[172:175]\nbuffer_load_dwordx4 v243,s[40:43],s69 offen offset:0 lds\nM v[184:187],a[48:51],a[160:163],v[232:235]\nbuffer_load_dwordx4 v244,s[40:43],s69 offen offset:1024 lds\nM v[184:187],a[52:55],a[164:167],v[184:187]\nbuffer_load_dwordx4 v245,s[40:43],s69 offen offset:2048 lds\nM v[184:187],a[56:59],a[168:171],v[184:187]\n" \
"buffer_load_dwordx4 v246,s[40:43],s69 offen offset:3072 lds\nM v[184:187],a[60:63],a[172:175],v[184:187]\nds_read_b128 a[4:7],v240 offset:1024\nds_read_b128 a[8:11],v240 offset:2048\nM v[188:191],a[48:51],a[176:179],v[236:239]\ns_lshl_b32 s69,s61,17\nds_read_b128 a[12:15],v240 offset:3072\nM v[188:191],a[52:55],a[180:183],v[188:191]\n" \
"s_add_u32 s69,s69,s68\nds_read_b128 a[16:19],v240 offset:4096\nM v[188:191],a[56:59],a[184:187],v[188:191]\ns_add_u32 s69,s69,0x10000\ns_add_u32 m0,s64,147456\nM v[188:191],a[60:63],a[188:191],v[188:191]\nds_read_b128 a[20:23],v240 offset:5120\nds_read_b128 a[24:27],v240 offset:6144\n" \
"M v[0:3],a[64:67],v[192:195],v[0:3]\nbuffer_load_dwordx4 v243,s[40:43],s69 offen offset:0 lds\nM v[4:7],a[64:67],v[196:199],v[4:7]\nbuffer_load_dwordx4 v244,s[40:43],s69 offen offset:1024 lds\nM v[64:67],a[64:67],v[200:203],0\nbuffer_load_dwordx4 v245,s[40:43],s69 offen offset:2048 lds\nM v[68:71],a[64:67],v[204:207],0\nds_read_b128 a[28:31],v240 offset:7168\n" \
"s_waitcnt lgkmcnt(7)\nM v[8:11],a[68:71],v[192:195],v[8:11]\nbuffer_load_dwordx4 v246,s[40:43],s69 offen offset:3072 lds\nM v[12:15],a[68:71],v[196:199],v[12:15]\ns_add_u32 m0,s64,65536\nds_read_b128 a[32:35],v240 offset:8192\nM v[72:75],a[68:71],v[200:203],0\nds_read_b128 a[36:39],v240 offset:9216\n" \
"ds_read_b128 a[40:43],v240 offset:10240\nM v[76:79],a[68:71],v[204:207],0\nds_read_b128 a[44:47],v240 offset:11264\nds_read_b128 a[48:51],v240 offset:12288\nM v[16:19],a[72:75],v[192:195],v[16:19]\nds_read_b128 a[52:55],v240 offset:13312\nds_read_b128 a[56:59],v240 offset:14336\nM v[20:23],a[72:75],v[196:199],v[20:23]\n" \
"ds_read_b128 a[60:63],v240 offset:15360\nM v[80:83],a[72:75],v[200:203],0\nv_exp_f32 v128,v128\nM v[84:87],a[72:75],v[204:207],0\nv_exp_f32 v129,v129\nM v[24:27],a[76:79],v[192:195],v[24:27]\nv_exp_f32 v130,v130\nM v[28:31],a[76:79],v[196:199],v[28:31]\n" \
"v_exp_f32 v131,v131\nM v[88:91],a[76:79],v[200:203],0\nv_exp_f32 v132,v132\nM v[92:95],a[76:79],v[204:207],0\nv_exp_f32 v133,v133\nM v[32:35],a[80:83],v[192:195],v[32:35]\nv_exp_f32 v134,v134\nM v[36:39],a[80:83],v[196:199],v[36:39]\n" \
"v_exp_f32 v135,v135\nM v[96:99],a[80:83],v[200:203],0\nv_exp_f32 v144,v144\nM v[100:103],a[80:83],v[204:207],0\nv_exp_f32 v145,v145\nM v[40:43],a[84:87],v[192:195],v[40:43]\nv_exp_f32 v146,v146\nM v[44:47],a[84:87],v[196:199],v[44:47]\n" \
"v_exp_f32 v147,v147\nM v[104:107],a[84:87],v[200:203],0\nv_exp_f32 v148,v148\nM v[108:111],a[84:87],v[204:207],0\nv_exp_f32 v149,v149\nM v[48:51],a[88:91],v[192:195],v[48:51]\ns_barrier\nM v[52:55],a[88:91],v[196:199],v[52:55]\n" \
"v_exp_f32 v150,v150\nM v[112:115],a[88:91],v[200:203],0\nv_exp_f32 v151,v151\nM v[116:119],a[88:91],v[204:207],0\ns_waitcnt vmcnt(8)\nv_cmp_eq_u32 vcc,0,v248\nM v[56:59],a[92:95],v[192:195],v[56:59]\ns_cbranch_vccnz .Lps3\n" \
".Lpb3:\nM v[60:63],a[92:95],v[196:199],v[60:63]\nbuffer_load_dwordx4 v240,s[48:51],s36 offen offset:0 sc1 lds\nM v[120:123],a[92:95],v[200:203],0\nbuffer_load_dwordx4 v240,s[48:51],s36 offen offset:1024 sc1 lds\nM v[124:127],a[92:95],v[204:207],0\nbuffer_load_dwordx4 v240,s[48:51],s36 offen offset:2048 sc1 lds\nM a[192:195],a[208:211],v[192:195],a[192:195]\n" \
"buffer_load_dwordx4 v240,s[48:51],s36 offen offset:3072 sc1 lds\nM a[196:199],a[208:211],v[196:199],a[196:199]\ns_add_u32 m0,s64,16384\nv_cvt_pk_bf16_f32 v192,v128,v129\nM a[200:203],a[208:211],v[200:203],0\nbuffer_load_dwordx4 v240,s[44:47],s65 offen offset:0 sc1 lds\nM a[204:207],a[208:211],v[204:207],0\nbuffer_load_dwordx4 v240,s[44:47],s65 offen offset:1024 sc1 lds\n" \
"M v[0:3],a[96:99],v[208:211],v[0:3]\nbuffer_load_dwordx4 v240,s[44:47],s65 offen offset:2048 sc1 lds\nM v[4:7],a[96:99],v[212:215],v[4:7]\nbuffer_load_dwordx4 v240,s[44:47],s65 offen offset:3072 sc1 lds\nM v[8:11],a[100:103],v[208:211],v[8:11]\ns_add_u32 m0,s64,81920\nv_cvt_pk_bf16_f32 v193,v130,v131\nM v[12:15],a[100:103],v[212:215],v[12:15]\n" \
"buffer_load_dwordx4 v240,s[48:51],s65 offen offset:0 sc1 lds\nM v[16:19],a[104:107],v[208:211],v[16:19]\nbuffer_load_dwordx4 v240,s[48:51],s65 offen offset:1024 sc1 lds\nM v[20:23],a[104:107],v[212:215],v[20:23]\nbuffer_load_dwordx4 v240,s[48:51],s65 offen offset:2048 sc1 lds\nM v[24:27],a[108:111],v[208:211],v[24:27]\nbuffer_load_dwordx4 v240,s[48:51],s65 offen offset:3072 sc1 lds\nM v[28:31],a[108:111],v[212:215],v[28:31]\n" \
"s_add_u32 m0,s64,32768\nv_cvt_pk_bf16_f32 v194,v144,v145\nM v[32:35],a[112:115],v[208:211],v[32:35]\nbuffer_load_dwordx4 v240,s[44:47],s66 offen offset:0 sc1 lds\nM v[36:39],a[112:115],v[212:215],v[36:39]\nbuffer_load_dwordx4 v240,s[44:47],s66 offen offset:1024 sc1 lds\nM v[40:43],a[116:119],v[208:211],v[40:43]\nbuffer_load_dwordx4 v240,s[44:47],s66 offen offset:2048 sc1 lds\n" \
"M v[44:47],a[116:119],v[212:215],v[44:47]\nbuffer_load_dwordx4 v240,s[44:47],s66 offen offset:3072 sc1 lds\nM v[48:51],a[120:123],v[208:211],v[48:51]\nv_exp_f32 v136,v136\nM v[52:55],a[120:123],v[212:215],v[52:55]\nv_exp_f32 v137,v137\nM v[56:59],a[124:127],v[208:211],v[56:59]\nv_exp_f32 v138,v138\n" \
"M v[60:63],a[124:127],v[212:215],v[60:63]\nv_exp_f32 v139,v139\nv_exp_f32 v140,v140\nv_exp_f32 v141,v141\nv_exp_f32 v142,v142\nv_exp_f32 v143,v143\nv_exp_f32 v152,v152\nv_exp_f32 v153,v153\n" \
"v_exp_f32 v154,v154\nv_exp_f32 v155,v155\nv_exp_f32 v156,v156\nv_exp_f32 v157,v157\nv_exp_f32 v158,v158\nv_exp_f32 v159,v159\nM a[192:195],a[208:211],v[208:211],a[192:195]\ns_add_u32 m0,s64,98304\n" \
"v_exp_f32 v160,v160\nv_exp_f32 v161,v161\nv_exp_f32 v162,v162\nv_exp_f32 v163,v163\nv_exp_f32 v164,v164\nv_exp_f32 v165,v165\nv_exp_f32 v166,v166\nv_exp_f32 v167,v167\n" \
"v_exp_f32 v176,v176\nv_exp_f32 v177,v177\nv_exp_f32 v178,v178\nv_exp_f32 v179,v179\nv_exp_f32 v180,v180\nv_exp_f32 v181,v181\nv_exp_f32 v182,v182\nv_exp_f32 v183,v183\n" \
"v_exp_f32 v168,v168\nv_exp_f32 v169,v169\nv_exp_f32 v170,v170\nv_exp_f32 v171,v171\nv_cvt_pk_bf16_f32 v195,v146,v147\nv_cvt_pk_bf16_f32 v196,v132,v133\nv_cvt_pk_bf16_f32 v197,v134,v135\nv_cvt_pk_bf16_f32 v198,v148,v149\n" \
"v_cvt_pk_bf16_f32 v199,v150,v151\nv_cvt_pk_bf16_f32 v200,v136,v137\nv_cvt_pk_bf16_f32 v201,v138,v139\nv_cvt_pk_bf16_f32 v202,v152,v153\nv_cvt_pk_bf16_f32 v203,v154,v155\nv_cvt_pk_bf16_f32 v204,v140,v141\nv_cvt_pk_bf16_f32 v205,v142,v143\nv_cvt_pk_bf16_f32 v206,v156,v157\n" \
"v_cvt_pk_bf16_f32 v207,v158,v159\nbuffer_load_dwordx4 v240,s[48:51],s66 offen offset:0 sc1 lds\nbuffer_load_dwordx4 v240,s[48:51],s66 offen offset:1024 sc1 lds\nbuffer_load_dwordx4 v240,s[48:51],s66 offen offset:2048 sc1 lds\nbuffer_load_dwordx4 v240,s[48:51],s66 offen offset:3072 sc1 lds\nM a[196:199],a[208:211],v[212:215],a[196:199]\ns_cmp_eq_u32 s62,0\ns_cbranch_scc1 .Ltrans\n" \
"s_branch .Ldual\n.Lp23:\ns_cmp_eq_u32 s7,2\ns_cbranch_scc0 .Lpro3\n.Lpro2:\ns_barrier\nM v[64:67],a[0:3],a[128:131],0\nds_read_b128 a[64:67],v241 offset:0\n" \
"ds_read_b128 a[68:71],v241 offset:1024\nM v[64:67],a[4:7],a[132:135],v[64:67]\nds_read_b128 a[72:75],v241 offset:2048\nds_read_b128 a[76:79],v241 offset:3072\nM v[64:67],a[8:11],a[136:139],v[64:67]\nds_read_b128 a[80:83],v241 offset:4096\nds_read_b128 a[84:87],v241 offset:5120\nM v[64:67],a[12:15],a[140:143],v[64:67]\n" \
"ds_read_b128 a[88:91],v241 offset:6144\nds_read_b128 a[92:95],v241 offset:7168\nM v[68:71],a[0:3],a[144:147],0\nds_read_b128 a[0:3],v240 offset:16384\nds_read_b128 a[96:99],v241 offset:8192\nM v[68:71],a[4:7],a[148:151],v[68:71]\nds_read_b128 a[4:7],v240 offset:17408\nds_read_b128 a[100:103],v241 offset:9216\n" \
"M v[68:71],a[8:11],a[152:155],v[68:71]\nds_read_b128 a[8:11],v240 offset:18432\nv_max3_f32 v225,v64,v65,v66\nM v[68:71],a[12:15],a[156:159],v[68:71]\nds_read_b128 a[12:15],v240 offset:19456\nds_read_b128 a[104:107],v241 offset:10240\nM v[72:75],a[16:19],a[128:131],0\nds_read_b128 a[108:111],v241 offset:11264\n" \
"ds_read_b128 a[112:115],v241 offset:12288\nM v[72:75],a[20:23],a[132:135],v[72:75]\nds_read_b128 a[116:119],v241 offset:13312\nds_read_b128 a[120:123],v241 offset:14336\nM v[72:75],a[24:27],a[136:139],v[72:75]\nv_max3_f32 v229,v68,v69,v70\nds_read_b128 a[124:127],v241 offset:15360\nM v[72:75],a[28:31],a[140:143],v[72:75]\n" \
"s_mov_b32 m0,s64\ns_lshl_b32 s69,s61,17\nM v[76:79],a[16:19],a[144:147],0\nds_read_b128 a[16:19],v240 offset:20480\ns_add_u32 s69,s69,s68\nM v[76:79],a[20:23],a[148:151],v[76:79]\nds_read_b128 a[20:23],v240 offset:21504\nv_mov_b32 v184,0xff800000\n" \
"M v[76:79],a[24:27],a[152:155],v[76:79]\nv_max3_f32 v225,v225,v67,v72\nv_max3_f32 v225,v225,v73,v74\nM v[76:79],a[28:31],a[156:159],v[76:79]\nv_max_f32 v225,v225,v75\nv_cndmask_b32 v225,v224,v225,s[90:91]\nM v[80:83],a[32:35],a[128:131],0\nds_read_b128 a[24:27],v240 offset:22528\n" \
"ds_read_b128 a[28:31],v240 offset:23552\nM v[80:83],a[36:39],a[132:135],v[80:83]\nbuffer_load_dwordx4 v240,s[44:47],s36 offen offset:0 sc1 lds\nM v[80:83],a[40:43],a[136:139],v[80:83]\nv_max3_f32 v229,v229,v71,v76\nv_max3_f32 v229,v229,v77,v78\nM v[80:83],a[44:47],a[140:143],v[80:83]\nv_max_f32 v229,v229,v79\n" \
"v_cndmask_b32 v229,v228,v229,s[90:91]\nM v[84:87],a[32:35],a[144:147],0\nbuffer_load_dwordx4 v240,s[44:47],s36 offen offset:1024 sc1 lds\nM v[84:87],a[36:39],a[148:151],v[84:87]\nv_permlane16_swap_b32 v225,v229\nv_max_f32 v225,v225,v229\nM v[84:87],a[40:43],a[152:155],v[84:87]\nv_mov_b32 v229,v225\n" \
"ds_read_b128 a[32:35],v240 offset:40960\nM v[84:87],a[44:47],a[156:159],v[84:87]\nv_permlane32_swap_b32 v225,v229\nv_max_f32 v225,v225,v229\nM v[88:91],a[48:51],a[128:131],0\nv_mov_b32 v229,v225\nds_read_b128 a[36:39],v240 offset:41984\nM v[88:91],a[52:55],a[132:135],v[88:91]\n" \
"v_permlane16_swap_b32 v225,v229\nv_sub_f32 v224,0,v225\nM v[88:91],a[56:59],a[136:139],v[88:91]\nv_sub_f32 v226,0,v225\nv_sub_f32 v227,0,v225\nM v[88:91],a[60:63],a[140:143],v[88:91]\nv_sub_f32 v225,0,v225\nv_sub_f32 v228,0,v229\n" \
"M v[92:95],a[48:51],a[144:147],0\nv_sub_f32 v230,0,v229\nv_sub_f32 v231,0,v229\nM v[92:95],a[52:55],a[148:151],v[92:95]\nv_sub_f32 v229,0,v229\nv_add_f32 v64,v64,v224\nM v[92:95],a[56:59],a[152:155],v[92:95]\nv_add_f32 v65,v65,v224\n" \
"v_add_f32 v66,v66,v224\nM v[92:95],a[60:63],a[156:159],v[92:95]\nv_add_f32 v67,v67,v224\ns_waitcnt lgkmcnt(2)\nM v[96:99],a[0:3],a[128:131],v[224:227]\nv_exp_f32 v64,v64\nM v[96:99],a[4:7],a[132:135],v[96:99]\nv_exp_f32 v65,v65\n" \
"M v[96:99],a[8:11],a[136:139],v[96:99]\nv_exp_f32 v66,v66\nM v[96:99],a[12:15],a[140:143],v[96:99]\nv_exp_f32 v67,v67\nM v[100:103],a[0:3],a[144:147],v[228:231]\nv_add_f32 v68,v68,v228\nv_add_f32 v69,v69,v228\nM v[100:103],a[4:7],a[148:151],v[100:103]\n" \
"v_add_f32 v72,v72,v224\nv_add_f32 v73,v73,v224\nM v[100:103],a[8:11],a[152:155],v[100:103]\nv_add_f32 v74,v74,v224\nv_add_f32 v75,v75,v224\nM v[100:103],a[12:15],a[156:159],v[100:103]\nv_add_f32 v70,v70,v228\nv_add_f32 v71,v71,v228\n" \
"M v[108:111],a[16:19],a[144:147],v[228:231]\nv_exp_f32 v68,v68\nM v[108:111],a[20:23],a[148:151],v[108:111]\nv_exp_f32 v69,v69\nv_exp_f32 v72,v72\nv_exp_f32 v73,v73\nv_exp_f32 v74,v74\nv_exp_f32 v75,v75\n" \
"v_cvt_pk_bf16_f32 v64,v64,v65\nM v[108:111],a[24:27],a[152:155],v[108:111]\nv_exp_f32 v70,v70\nv_exp_f32 v71,v71\nv_add_f32 v76,v76,v228\nv_add_f32 v77,v77,v228\nv_add_f32 v78,v78,v228\nv_add_f32 v79,v79,v228\n" \
"v_cvt_pk_bf16_f32 v65,v66,v67\nv_exp_f32 v76,v76\nv_exp_f32 v77,v77\nv_exp_f32 v78,v78\nv_exp_f32 v79,v79\nv_cvt_pk_bf16_f32 v66,v72,v73\nv_cvt_pk_bf16_f32 v67,v74,v75\nv_cvt_pk_bf16_f32 v68,v68,v69\n" \
"M v[108:111],a[28:31],a[156:159],v[108:111]\nv_cvt_pk_bf16_f32 v69,v70,v71\nv_cvt_pk_bf16_f32 v70,v76,v77\nv_cvt_pk_bf16_f32 v71,v78,v79\nM v[0:3],a[64:67],v[64:67],0\nv_add_f32 v80,v80,v224\nv_add_f32 v81,v81,v224\nM v[4:7],a[64:67],v[68:71],0\n" \
"v_add_f32 v82,v82,v224\nv_add_f32 v83,v83,v224\nM v[8:11],a[68:71],v[64:67],0\nv_exp_f32 v80,v80\nM v[12:15],a[68:71],v[68:71],0\nv_exp_f32 v81,v81\nM v[16:19],a[72:75],v[64:67],0\nv_exp_f32 v82,v82\n" \
"M v[20:23],a[72:75],v[68:71],0\nv_exp_f32 v83,v83\nM v[24:27],a[76:79],v[64:67],0\nv_add_f32 v84,v84,v228\nv_add_f32 v85,v85,v228\nM v[28:31],a[76:79],v[68:71],0\nv_add_f32 v88,v88,v224\nv_add_f32 v89,v89,v224\n" \
"M v[32:35],a[80:83],v[64:67],0\nv_add_f32 v90,v90,v224\nv_add_f32 v91,v91,v224\nM v[36:39],a[80:83],v[68:71],0\nv_add_f32 v86,v86,v228\nv_add_f32 v87,v87,v228\nM v[40:43],a[84:87],v[64:67],0\nv_exp_f32 v84,v84\n" \
"M v[44:47],a[84:87],v[68:71],0\nv_exp_f32 v85,v85\nM v[48:51],a[88:91],v[64:67],0\nv_exp_f32 v88,v88\nM v[52:55],a[88:91],v[68:71],0\nv_exp_f32 v89,v89\nM v[56:59],a[92:95],v[64:67],0\nv_exp_f32 v90,v90\n" \
"M v[60:63],a[92:95],v[68:71],0\nv_exp_f32 v91,v91\nv_cvt_pk_bf16_f32 v80,v80,v81\nM a[192:195],a[208:211],v[64:67],0\nv_exp_f32 v86,v86\nv_exp_f32 v87,v87\nv_add_f32 v92,v92,v228\nv_add_f32 v93,v93,v228\n" \
"v_add_f32 v94,v94,v228\nv_add_f32 v95,v95,v228\nv_cvt_pk_bf16_f32 v81,v82,v83\nv_exp_f32 v92,v92\nv_exp_f32 v93,v93\nv_exp_f32 v94,v94\nv_exp_f32 v95,v95\nv_cvt_pk_bf16_f32 v82,v88,v89\n" \
"v_cvt_pk_bf16_f32 v83,v90,v91\nv_cvt_pk_bf16_f32 v84,v84,v85\nM a[196:199],a[208:211],v[68:71],0\nv_cvt_pk_bf16_f32 v85,v86,v87\nv_cvt_pk_bf16_f32 v86,v92,v93\nv_cvt_pk_bf16_f32 v87,v94,v95\nM v[0:3],a[96:99],v[80:83],v[0:3]\nds_read_b128 a[0:3],v240 offset:32768\n" \
"ds_read_b128 a[4:7],v240 offset:33792\nM v[4:7],a[96:99],v[84:87],v[4:7]\nds_read_b128 a[8:11],v240 offset:34816\nds_read_b128 a[12:15],v240 offset:35840\nM v[8:11],a[100:103],v[80:83],v[8:11]\nbuffer_load_dwordx4 v240,s[44:47],s36 offen offset:2048 sc1 lds\nM v[12:15],a[100:103],v[84:87],v[12:15]\nbuffer_load_dwordx4 v240,s[44:47],s36 offen offset:3072 sc1 lds\n" \
"M v[16:19],a[104:107],v[80:83],v[16:19]\nds_read_b128 a[16:19],v240 offset:36864\nds_read_b128 a[20:23],v240 offset:37888\nM v[20:23],a[104:107],v[84:87],v[20:23]\nds_read_b128 a[24:27],v240 offset:38912\nds_read_b128 a[28:31],v240 offset:39936\nM v[24:27],a[108:111],v[80:83],v[24:27]\nds_read_b128 a[40:43],v240 offset:43008\n" \
"ds_read_b128 a[44:47],v240 offset:44032\nM v[28:31],a[108:111],v[84:87],v[28:31]\nds_read_b128 a[48:51],v240 offset:45056\nds_read_b128 a[52:55],v240 offset:46080\nM v[32:35],a[112:115],v[80:83],v[32:35]\nds_read_b128 a[56:59],v240 offset:47104\nds_read_b128 a[60:63],v240 offset:48128\nM v[36:39],a[112:115],v[84:87],v[36:39]\n" \
"ds_read_b128 a[64:67],v241 offset:16384\nds_read_b128 a[68:71],v241 offset:17408\nM v[40:43],a[116:119],v[80:83],v[40:43]\nds_read_b128 a[72:75],v241 offset:18432\nds_read_b128 a[76:79],v241 offset:19456\nM v[44:47],a[116:119],v[84:87],v[44:47]\nv_cndmask_b32 v96,v96,v252,s[82:83]\nv_cndmask_b32 v97,v97,v252,s[84:85]\n" \
"M v[48:51],a[120:123],v[80:83],v[48:51]\nv_cndmask_b32 v98,v98,v252,s[86:87]\nv_cndmask_b32 v99,v99,v252,s[88:89]\nM v[52:55],a[120:123],v[84:87],v[52:55]\nv_exp_f32 v96,v96\nM v[56:59],a[124:127],v[80:83],v[56:59]\nv_exp_f32 v97,v97\nM v[60:63],a[124:127],v[84:87],v[60:63]\n" \
"v_exp_f32 v98,v98\nM a[192:195],a[208:211],v[80:83],a[192:195]\nv_exp_f32 v99,v99\nM a[196:199],a[208:211],v[84:87],a[196:199]\nv_cvt_pk_bf16_f32 v96,v96,v97\ns_waitcnt lgkmcnt(7)\nM v[128:131],a[0:3],a[128:131],v[224:227]\nv_exp_f32 v100,v100\n" \
"M v[128:131],a[4:7],a[132:135],v[128:131]\nv_exp_f32 v101,v101\nM v[128:131],a[8:11],a[136:139],v[128:131]\nds_read_b128 a[80:83],v241 offset:20480\nv_cndmask_b32 v108,v108,v252,s[82:83]\nM v[128:131],a[12:15],a[140:143],v[128:131]\nv_cndmask_b32 v109,v109,v252,s[84:85]\nv_cndmask_b32 v110,v110,v252,s[86:87]\n" \
"M v[132:135],a[0:3],a[144:147],v[228:231]\nv_cndmask_b32 v111,v111,v252,s[88:89]\nv_cvt_pk_bf16_f32 v97,v98,v99\nM v[132:135],a[4:7],a[148:151],v[132:135]\nv_exp_f32 v102,v102\nM v[132:135],a[8:11],a[152:155],v[132:135]\nv_exp_f32 v103,v103\nM v[132:135],a[12:15],a[156:159],v[132:135]\n" \
"v_exp_f32 v108,v108\nM v[144:147],a[16:19],a[128:131],v[224:227]\nv_exp_f32 v109,v109\nM v[144:147],a[20:23],a[132:135],v[144:147]\nv_exp_f32 v110,v110\nM v[144:147],a[24:27],a[136:139],v[144:147]\nv_exp_f32 v111,v111\nM v[144:147],a[28:31],a[140:143],v[144:147]\n" \
"v_mov_b32 v98,0\nv_mov_b32 v99,0\nM v[148:151],a[16:19],a[144:147],v[228:231]\nv_cvt_pk_bf16_f32 v100,v100,v101\nv_cvt_pk_bf16_f32 v101,v102,v103\nM v[148:151],a[20:23],a[148:151],v[148:151]\nv_cvt_pk_bf16_f32 v102,v108,v109\nv_cvt_pk_bf16_f32 v103,v110,v111\n" \
"M v[148:151],a[24:27],a[152:155],v[148:151]\nds_read_b128 a[84:87],v241 offset:21504\nds_read_b128 a[88:91],v241 offset:22528\nM v[148:151],a[28:31],a[156:159],v[148:151]\nds_read_b128 a[92:95],v241 offset:23552\nds_read_b128 a[0:3],v240 offset:49152\nM v[160:163],a[32:35],a[128:131],v[224:227]\nds_read_b128 a[4:7],v240 offset:50176\n" \
"ds_read_b128 a[8:11],v240 offset:51200\nM v[160:163],a[36:39],a[132:135],v[160:163]\nds_read_b128 a[12:15],v240 offset:52224\nds_read_b128 a[16:19],v240 offset:53248\nM v[160:163],a[40:43],a[136:139],v[160:163]\nds_read_b128 a[20:23],v240 offset:54272\nds_read_b128 a[24:27],v240 offset:55296\nM v[160:163],a[44:47],a[140:143],v[160:163]\n" \
"ds_read_b128 a[28:31],v240 offset:56320\nds_read_b128 a[96:99],v241 offset:40960\nM v[164:167],a[32:35],a[144:147],v[228:231]\nv_exp_f32 v128,v128\nM v[164:167],a[36:39],a[148:151],v[164:167]\nv_exp_f32 v129,v129\nM v[164:167],a[40:43],a[152:155],v[164:167]\nv_exp_f32 v130,v130\n" \
"M v[164:167],a[44:47],a[156:159],v[164:167]\nv_exp_f32 v131,v131\nM v[176:179],a[48:51],a[128:131],v[224:227]\nv_cvt_pk_bf16_f32 v192,v128,v129\ns_waitcnt lgkmcnt(11)\nM v[176:179],a[52:55],a[132:135],v[176:179]\nv_cvt_pk_bf16_f32 v193,v130,v131\nds_read_b128 a[32:35],v240 offset:57344\n" \
"M v[176:179],a[56:59],a[136:139],v[176:179]\nv_exp_f32 v132,v132\nM v[176:179],a[60:63],a[140:143],v[176:179]\nv_exp_f32 v133,v133\nM v[180:183],a[48:51],a[144:147],v[228:231]\nv_exp_f32 v134,v134\nM v[180:183],a[52:55],a[148:151],v[180:183]\nv_exp_f32 v135,v135\n" \
"M v[180:183],a[56:59],a[152:155],v[180:183]\nv_cvt_pk_bf16_f32 v196,v132,v133\nv_cvt_pk_bf16_f32 v197,v134,v135\nM v[180:183],a[60:63],a[156:159],v[180:183]\nv_exp_f32 v144,v144\nM v[0:3],a[64:67],v[96:99],v[0:3]\nv_exp_f32 v145,v145\nM v[4:7],a[64:67],v[100:103],v[4:7]\n" \
"v_exp_f32 v146,v146\nM v[8:11],a[68:71],v[96:99],v[8:11]\nv_exp_f32 v147,v147\nM v[12:15],a[68:71],v[100:103],v[12:15]\nds_read_b128 a[36:39],v240 offset:58368\nv_cvt_pk_bf16_f32 v194,v144,v145\nM v[16:19],a[72:75],v[96:99],v[16:19]\nv_cvt_pk_bf16_f32 v195,v146,v147\n" \
"ds_read_b128 a[40:43],v240 offset:59392\nM v[20:23],a[72:75],v[100:103],v[20:23]\nds_read_b128 a[44:47],v240 offset:60416\nds_read_b128 a[48:51],v240 offset:61440\nM v[24:27],a[76:79],v[96:99],v[24:27]\nv_exp_f32 v148,v148\nM v[28:31],a[76:79],v[100:103],v[28:31]\nv_exp_f32 v149,v149\n" \
"M v[32:35],a[80:83],v[96:99],v[32:35]\nv_exp_f32 v150,v150\nM v[36:39],a[80:83],v[100:103],v[36:39]\nv_exp_f32 v151,v151\nM v[40:43],a[84:87],v[96:99],v[40:43]\nv_cvt_pk_bf16_f32 v198,v148,v149\nv_cvt_pk_bf16_f32 v199,v150,v151\nM v[44:47],a[84:87],v[100:103],v[44:47]\n" \
"ds_read_b128 a[52:55],v240 offset:62464\ns_waitcnt lgkmcnt(5)\nM v[48:51],a[88:91],v[96:99],v[48:51]\nv_exp_f32 v160,v160\nM v[52:55],a[88:91],v[100:103],v[52:55]\ns_waitcnt vmcnt(0)\ns_barrier\nM v[56:59],a[92:95],v[96:99],v[56:59]\n" \
"v_exp_f32 v161,v161\nM v[60:63],a[92:95],v[100:103],v[60:63]\nv_exp_f32 v162,v162\nM a[192:195],a[208:211],v[96:99],a[192:195]\nv_exp_f32 v163,v163\nM a[196:199],a[208:211],v[100:103],a[196:199]\nv_cvt_pk_bf16_f32 v208,v160,v161\nv_cvt_pk_bf16_f32 v209,v162,v163\n" \
"M v[64:67],a[244:247],a[244:247],0\nds_read_b128 a[56:59],v240 offset:63488\nds_read_b128 a[60:63],v240 offset:64512\nM v[68:71],a[244:247],a[244:247],0\nv_exp_f32 v164,v164\nM v[72:75],a[244:247],a[244:247],0\nv_exp_f32 v165,v165\nM v[76:79],a[244:247],a[244:247],0\n" \
"v_exp_f32 v166,v166\nM v[80:83],a[244:247],a[244:247],0\nv_exp_f32 v167,v167\nM v[84:87],a[244:247],a[244:247],0\nv_cvt_pk_bf16_f32 v212,v164,v165\nv_cvt_pk_bf16_f32 v213,v166,v167\nM v[88:91],a[244:247],a[244:247],0\nv_exp_f32 v176,v176\n" \
"M v[92:95],a[244:247],a[244:247],0\nv_exp_f32 v177,v177\nM v[96:99],a[244:247],a[244:247],0\nv_exp_f32 v178,v178\nM v[100:103],a[244:247],a[244:247],0\nv_exp_f32 v179,v179\nM v[104:107],a[244:247],a[244:247],0\nv_cvt_pk_bf16_f32 v210,v176,v177\n" \
"v_cvt_pk_bf16_f32 v211,v178,v179\nM v[108:111],a[244:247],a[244:247],0\nbuffer_load_dword v248,v251,s[52:55],s80 offen sc1\nM v[112:115],a[244:247],a[244:247],0\nv_exp_f32 v180,v180\nM v[116:119],a[244:247],a[244:247],0\nv_exp_f32 v181,v181\nM v[120:123],a[244:247],a[244:247],0\n" \
"v_exp_f32 v182,v182\nM v[124:127],a[244:247],a[244:247],0\nv_exp_f32 v183,v183\nM a[200:203],a[244:247],a[244:247],0\nv_cvt_pk_bf16_f32 v214,v180,v181\nv_cvt_pk_bf16_f32 v215,v182,v183\nM a[204:207],a[244:247],a[244:247],0\nds_read_b128 a[64:67],v241 offset:32768\n" \
"ds_read_b128 a[68:71],v241 offset:33792\nM v[136:139],a[0:3],a[160:163],0\nds_read_b128 a[72:75],v241 offset:34816\nds_read_b128 a[76:79],v241 offset:35840\nM v[136:139],a[4:7],a[164:167],v[136:139]\nds_read_b128 a[80:83],v241 offset:36864\nds_read_b128 a[84:87],v241 offset:37888\nM v[136:139],a[8:11],a[168:171],v[136:139]\n" \
"ds_read_b128 a[88:91],v241 offset:38912\nds_read_b128 a[92:95],v241 offset:39936\nM v[136:139],a[12:15],a[172:175],v[136:139]\nds_read_b128 a[100:103],v241 offset:41984\nds_read_b128 a[104:107],v241 offset:43008\nM v[140:143],a[0:3],a[176:179],0\nds_read_b128 a[108:111],v241 offset:44032\nds_read_b128 a[112:115],v241 offset:45056\n" \
"M v[140:143],a[4:7],a[180:183],v[140:143]\nds_read_b128 a[116:119],v241 offset:46080\nds_read_b128 a[120:123],v241 offset:47104\nM v[140:143],a[8:11],a[184:187],v[140:143]\nv_max3_f32 v233,v136,v137,v138\nds_read_b128 a[124:127],v241 offset:48128\nM v[140:143],a[12:15],a[188:191],v[140:143]\ns_add_u32 m0,s64,131072\n" \
"v_mov_b32 v185,0xff800000\nM v[152:155],a[16:19],a[160:163],0\nbuffer_load_dwordx4 v243,s[40:43],s69 offen offset:0 lds\nM v[152:155],a[20:23],a[164:167],v[152:155]\nbuffer_load_dwordx4 v244,s[40:43],s69 offen offset:1024 lds\nM v[152:155],a[24:27],a[168:171],v[152:155]\nbuffer_load_dwordx4 v245,s[40:43],s69 offen offset:2048 lds\nM v[152:155],a[28:31],a[172:175],v[152:155]\n" \
"v_max3_f32 v237,v140,v141,v142\nv_mov_b32 v186,0xff800000\nM v[156:159],a[16:19],a[176:179],0\nbuffer_load_dwordx4 v246,s[40:43],s69 offen offset:3072 lds\nM v[156:159],a[20:23],a[180:183],v[156:159]\ns_lshl_b32 s69,s61,17\ns_add_u32 s69,s69,s68\nM v[156:159],a[24:27],a[184:187],v[156:159]\n" \
"v_max3_f32 v233,v233,v139,v152\nv_max3_f32 v233,v233,v153,v154\nM v[156:159],a[28:31],a[188:191],v[156:159]\nv_max_f32 v233,v233,v155\nv_cndmask_b32 v233,v232,v233,s[90:91]\nM v[128:131],a[0:3],a[128:131],v[224:227]\ns_add_u32 s69,s69,0x10000\ns_add_u32 m0,s64,147456\n" \
"M v[128:131],a[4:7],a[132:135],v[128:131]\nbuffer_load_dwordx4 v243,s[40:43],s69 offen offset:0 lds\nM v[128:131],a[8:11],a[136:139],v[128:131]\nv_max3_f32 v237,v237,v143,v156\nv_max3_f32 v237,v237,v157,v158\nM v[128:131],a[12:15],a[140:143],v[128:131]\nv_max_f32 v237,v237,v159\nv_cndmask_b32 v237,v236,v237,s[90:91]\n" \
"M v[132:135],a[0:3],a[144:147],v[228:231]\nds_read_b128 a[0:3],v240 offset:0\nv_permlane16_swap_b32 v233,v237\nM v[132:135],a[4:7],a[148:151],v[132:135]\nv_max_f32 v233,v233,v237\nv_mov_b32 v237,v233\nM v[132:135],a[8:11],a[152:155],v[132:135]\nds_read_b128 a[4:7],v240 offset:1024\n" \
"v_permlane32_swap_b32 v233,v237\nM v[132:135],a[12:15],a[156:159],v[132:135]\nv_max_f32 v233,v233,v237\nv_mov_b32 v237,v233\nM v[144:147],a[16:19],a[128:131],v[224:227]\nds_read_b128 a[8:11],v240 offset:2048\nv_permlane16_swap_b32 v233,v237\nM v[144:147],a[20:23],a[132:135],v[144:147]\n" \
"v_sub_f32 v232,0,v233\nv_sub_f32 v234,0,v233\nM v[144:147],a[24:27],a[136:139],v[144:147]\nv_sub_f32 v235,0,v233\nv_sub_f32 v233,0,v233\nM v[144:147],a[28:31],a[140:143],v[144:147]\nv_sub_f32 v236,0,v237\nv_sub_f32 v238,0,v237\n" \
"M v[148:151],a[16:19],a[144:147],v[228:231]\nv_sub_f32 v239,0,v237\nv_sub_f32 v237,0,v237\nM v[148:151],a[20:23],a[148:151],v[148:151]\nds_read_b128 a[12:15],v240 offset:3072\nds_read_b128 a[16:19],v240 offset:4096\nM v[148:151],a[24:27],a[152:155],v[148:151]\nds_read_b128 a[20:23],v240 offset:5120\n" \
"ds_read_b128 a[24:27],v240 offset:6144\nM v[148:151],a[28:31],a[156:159],v[148:151]\nbuffer_load_dwordx4 v244,s[40:43],s69 offen offset:1024 lds\nM v[160:163],a[32:35],a[128:131],v[224:227]\nds_read_b128 a[28:31],v240 offset:7168\ns_waitcnt lgkmcnt(6)\nM v[160:163],a[36:39],a[132:135],v[160:163]\nbuffer_load_dwordx4 v245,s[40:43],s69 offen offset:2048 lds\n" \
"M v[160:163],a[40:43],a[136:139],v[160:163]\nbuffer_load_dwordx4 v246,s[40:43],s69 offen offset:3072 lds\nM v[160:163],a[44:47],a[140:143],v[160:163]\ns_add_u32 m0,s64,65536\nv_add_f32 v136,v136,v232\nM v[164:167],a[32:35],a[144:147],v[228:231]\nv_add_f32 v137,v137,v232\nv_add_f32 v138,v138,v232\n" \
"M v[164:167],a[36:39],a[148:151],v[164:167]\nv_add_f32 v139,v139,v232\nv_add_f32 v140,v140,v236\nM v[164:167],a[40:43],a[152:155],v[164:167]\nv_add_f32 v141,v141,v236\nv_add_f32 v142,v142,v236\nM v[164:167],a[44:47],a[156:159],v[164:167]\ns_waitcnt vmcnt(8)\n" \
"v_cmp_eq_u32 vcc,0,v248\nM v[176:179],a[48:51],a[128:131],v[224:227]\ns_cbranch_vccnz .Lps4\n.Lpb4:\nv_add_f32 v143,v143,v236\nM v[176:179],a[52:55],a[132:135],v[176:179]\nbuffer_load_dwordx4 v240,s[48:51],s36 offen offset:0 sc1 lds\nM v[176:179],a[56:59],a[136:139],v[176:179]\n" \
"buffer_load_dwordx4 v240,s[48:51],s36 offen offset:1024 sc1 lds\nM v[176:179],a[60:63],a[140:143],v[176:179]\nbuffer_load_dwordx4 v240,s[48:51],s36 offen offset:2048 sc1 lds\nM v[180:183],a[48:51],a[144:147],v[228:231]\nbuffer_load_dwordx4 v240,s[48:51],s36 offen offset:3072 sc1 lds\nM v[180:183],a[52:55],a[148:151],v[180:183]\ns_add_u32 m0,s64,16384\nv_add_f32 v152,v152,v232\n" \
"M v[180:183],a[56:59],a[152:155],v[180:183]\nbuffer_load_dwordx4 v240,s[44:47],s65 offen offset:0 sc1 lds\nM v[180:183],a[60:63],a[156:159],v[180:183]\nbuffer_load_dwordx4 v240,s[44:47],s65 offen offset:1024 sc1 lds\nM v[168:171],a[32:35],a[160:163],v[232:235]\ns_barrier\nv_add_f32 v153,v153,v232\nM v[168:171],a[36:39],a[164:167],v[168:171]\n" \
"buffer_load_dwordx4 v240,s[44:47],s65 offen offset:2048 sc1 lds\nM v[168:171],a[40:43],a[168:171],v[168:171]\nbuffer_load_dwordx4 v240,s[44:47],s65 offen offset:3072 sc1 lds\nM v[168:171],a[44:47],a[172:175],v[168:171]\ns_add_u32 m0,s64,81920\nv_add_f32 v154,v154,v232\nM v[172:175],a[32:35],a[176:179],v[236:239]\nds_read_b128 a[32:35],v240 offset:8192\n" \
"v_add_f32 v155,v155,v232\nM v[172:175],a[36:39],a[180:183],v[172:175]\nds_read_b128 a[36:39],v240 offset:9216\nv_add_f32 v156,v156,v236\nM v[172:175],a[40:43],a[184:187],v[172:175]\nds_read_b128 a[40:43],v240 offset:10240\nv_add_f32 v157,v157,v236\nM v[172:175],a[44:47],a[188:191],v[172:175]\n" \
"ds_read_b128 a[44:47],v240 offset:11264\nv_add_f32 v158,v158,v236\nM v[188:191],a[48:51],a[176:179],v[236:239]\nds_read_b128 a[48:51],v240 offset:12288\nv_add_f32 v159,v159,v236\nM v[188:191],a[52:55],a[180:183],v[188:191]\nds_read_b128 a[52:55],v240 offset:13312\nv_cndmask_b32 v168,v168,v252,s[82:83]\n" \
"M v[188:191],a[56:59],a[184:187],v[188:191]\nds_read_b128 a[56:59],v240 offset:14336\nv_cndmask_b32 v169,v169,v252,s[84:85]\nM v[188:191],a[60:63],a[188:191],v[188:191]\nbuffer_load_dwordx4 v240,s[48:51],s65 offen offset:0 sc1 lds\nM v[0:3],a[64:67],v[192:195],v[0:3]\nbuffer_load_dwordx4 v240,s[48:51],s65 offen offset:1024 sc1 lds\nM v[4:7],a[64:67],v[196:199],v[4:7]\n" \
"buffer_load_dwordx4 v240,s[48:51],s65 offen offset:2048 sc1 lds\nM v[8:11],a[68:71],v[192:195],v[8:11]\nbuffer_load_dwordx4 v240,s[48:51],s65 offen offset:3072 sc1 lds\nM v[12:15],a[68:71],v[196:199],v[12:15]\nds_read_b128 a[60:63],v240 offset:15360\ns_add_u32 m0,s64,32768\nM v[16:19],a[72:75],v[192:195],v[16:19]\nbuffer_load_dwordx4 v240,s[44:47],s66 offen offset:0 sc1 lds\n" \
"M v[20:23],a[72:75],v[196:199],v[20:23]\nbuffer_load_dwordx4 v240,s[44:47],s66 offen offset:1024 sc1 lds\nM v[24:27],a[76:79],v[192:195],v[24:27]\nbuffer_load_dwordx4 v240,s[44:47],s66 offen offset:2048 sc1 lds\nM v[28:31],a[76:79],v[196:199],v[28:31]\nbuffer_load_dwordx4 v240,s[44:47],s66 offen offset:3072 sc1 lds\nM v[32:35],a[80:83],v[192:195],v[32:35]\nv_exp_f32 v136,v136\n" \
"M v[36:39],a[80:83],v[196:199],v[36:39]\nv_exp_f32 v137,v137\nM v[40:43],a[84:87],v[192:195],v[40:43]\nv_exp_f32 v138,v138\nM v[44:47],a[84:87],v[196:199],v[44:47]\nv_exp_f32 v139,v139\nM v[48:51],a[88:91],v[192:195],v[48:51]\nv_exp_f32 v140,v140\n" \
"M v[52:55],a[88:91],v[196:199],v[52:55]\nv_exp_f32 v141,v141\nM v[56:59],a[92:95],v[192:195],v[56:59]\nv_exp_f32 v142,v142\nM v[60:63],a[92:95],v[196:199],v[60:63]\nv_exp_f32 v143,v143\nM a[192:195],a[208:211],v[192:195],a[192:195]\nv_exp_f32 v152,v152\n" \
"M a[196:199],a[208:211],v[196:199],a[196:199]\nv_exp_f32 v153,v153\nM v[0:3],a[96:99],v[208:211],v[0:3]\nv_exp_f32 v154,v154\nM v[4:7],a[96:99],v[212:215],v[4:7]\nv_exp_f32 v155,v155\nM v[8:11],a[100:103],v[208:211],v[8:11]\nv_exp_f32 v156,v156\n" \
"M v[12:15],a[100:103],v[212:215],v[12:15]\nv_exp_f32 v157,v157\nM v[16:19],a[104:107],v[208:211],v[16:19]\nv_exp_f32 v158,v158\nM v[20:23],a[104:107],v[212:215],v[20:23]\nv_exp_f32 v159,v159\nM v[24:27],a[108:111],v[208:211],v[24:27]\nv_exp_f32 v128,v128\n" \
"M v[28:31],a[108:111],v[212:215],v[28:31]\nv_exp_f32 v129,v129\nM v[32:35],a[112:115],v[208:211],v[32:35]\nv_exp_f32 v130,v130\nM v[36:39],a[112:115],v[212:215],v[36:39]\nv_exp_f32 v131,v131\nM v[40:43],a[116:119],v[208:211],v[40:43]\nv_exp_f32 v132,v132\n" \
"M v[44:47],a[116:119],v[212:215],v[44:47]\nv_exp_f32 v133,v133\nM v[48:51],a[120:123],v[208:211],v[48:51]\nv_exp_f32 v134,v134\nM v[52:55],a[120:123],v[212:215],v[52:55]\nv_exp_f32 v135,v135\nM v[56:59],a[124:127],v[208:211],v[56:59]\nv_exp_f32 v144,v144\n" \
"M v[60:63],a[124:127],v[212:215],v[60:63]\nv_exp_f32 v145,v145\nv_exp_f32 v146,v146\nv_exp_f32 v147,v147\nv_exp_f32 v148,v148\nv_exp_f32 v149,v149\nv_exp_f32 v150,v150\nv_exp_f32 v151,v151\n" \
"M a[192:195],a[208:211],v[208:211],a[192:195]\nv_cndmask_b32 v170,v170,v252,s[86:87]\nv_cndmask_b32 v171,v171,v252,s[88:89]\ns_add_u32 m0,s64,98304\nv_exp_f32 v160,v160\nv_exp_f32 v161,v161\nv_exp_f32 v162,v162\nv_exp_f32 v163,v163\n" \
"v_exp_f32 v164,v164\nv_exp_f32 v165,v165\nv_exp_f32 v166,v166\nv_exp_f32 v167,v167\nv_exp_f32 v176,v176\nv_exp_f32 v177,v177\nv_exp_f32 v178,v178\nv_exp_f32 v179,v179\n" \
"v_exp_f32 v180,v180\nv_exp_f32 v181,v181\nv_exp_f32 v182,v182\nv_exp_f32 v183,v183\nv_cndmask_b32 v188,v188,v252,s[82:83]\nv_cndmask_b32 v189,v189,v252,s[84:85]\nv_cndmask_b32 v190,v190,v252,s[86:87]\nv_cndmask_b32 v191,v191,v252,s[88:89]\n" \
"v_exp_f32 v168,v168\nv_exp_f32 v169,v169\nv_exp_f32 v170,v170\nv_exp_f32 v171,v171\nv_mov_b32 v187,0xff800000\nv_cvt_pk_bf16_f32 v200,v136,v137\nv_cvt_pk_bf16_f32 v201,v138,v139\nv_cvt_pk_bf16_f32 v202,v152,v153\n" \
"v_cvt_pk_bf16_f32 v203,v154,v155\nv_cvt_pk_bf16_f32 v204,v140,v141\nv_cvt_pk_bf16_f32 v205,v142,v143\nv_cvt_pk_bf16_f32 v206,v156,v157\nv_cvt_pk_bf16_f32 v207,v158,v159\nv_cvt_pk_bf16_f32 v192,v128,v129\nv_cvt_pk_bf16_f32 v193,v130,v131\nv_cvt_pk_bf16_f32 v194,v144,v145\n" \
"v_cvt_pk_bf16_f32 v195,v146,v147\nv_cvt_pk_bf16_f32 v196,v132,v133\nv_cvt_pk_bf16_f32 v197,v134,v135\nv_cvt_pk_bf16_f32 v198,v148,v149\nv_cvt_pk_bf16_f32 v199,v150,v151\nbuffer_load_dwordx4 v240,s[48:51],s66 offen offset:0 sc1 lds\nbuffer_load_dwordx4 v240,s[48:51],s66 offen offset:1024 sc1 lds\nbuffer_load_dwordx4 v240,s[48:51],s66 offen offset:2048 sc1 lds\n" \
"buffer_load_dwordx4 v240,s[48:51],s66 offen offset:3072 sc1 lds\nM a[196:199],a[208:211],v[212:215],a[196:199]\ns_cmp_eq_u32 s62,0\ns_cbranch_scc1 .Ltrans\ns_branch .Ldual\n.Lpro3:\ns_barrier\nM v[64:67],a[0:3],a[128:131],0\n" \
"ds_read_b128 a[64:67],v241 offset:0\nds_read_b128 a[68:71],v241 offset:1024\nM v[64:67],a[4:7],a[132:135],v[64:67]\nds_read_b128 a[72:75],v241 offset:2048\nds_read_b128 a[76:79],v241 offset:3072\nM v[64:67],a[8:11],a[136:139],v[64:67]\nds_read_b128 a[80:83],v241 offset:4096\nds_read_b128 a[84:87],v241 offset:5120\n" \
"M v[64:67],a[12:15],a[140:143],v[64:67]\nds_read_b128 a[88:91],v241 offset:6144\nds_read_b128 a[92:95],v241 offset:7168\nM v[68:71],a[0:3],a[144:147],0\nds_read_b128 a[0:3],v240 offset:16384\nds_read_b128 a[96:99],v241 offset:8192\nM v[68:71],a[4:7],a[148:151],v[68:71]\nds_read_b128 a[4:7],v240 offset:17408\n" \
"ds_read_b128 a[100:103],v241 offset:9216\nM v[68:71],a[8:11],a[152:155],v[68:71]\nds_read_b128 a[8:11],v240 offset:18432\nv_max3_f32 v225,v64,v65,v66\nM v[68:71],a[12:15],a[156:159],v[68:71]\nds_read_b128 a[12:15],v240 offset:19456\nds_read_b128 a[104:107],v241 offset:10240\nM v[72:75],a[16:19],a[128:131],0\n" \
"s_mov_b32 m0,s64\nds_read_b128 a[108:111],v241 offset:11264\nM v[72:75],a[20:23],a[132:135],v[72:75]\nbuffer_load_dwordx4 v240,s[44:47],s36 offen offset:0 sc1 lds\nM v[72:75],a[24:27],a[136:139],v[72:75]\nv_max3_f32 v229,v68,v69,v70\nds_read_b128 a[112:115],v241 offset:12288\nM v[72:75],a[28:31],a[140:143],v[72:75]\n" \
"buffer_load_dwordx4 v240,s[44:47],s36 offen offset:1024 sc1 lds\nM v[76:79],a[16:19],a[144:147],0\nds_read_b128 a[16:19],v240 offset:20480\nds_read_b128 a[116:119],v241 offset:13312\nM v[76:79],a[20:23],a[148:151],v[76:79]\nds_read_b128 a[20:23],v240 offset:21504\nds_read_b128 a[120:123],v241 offset:14336\nM v[76:79],a[24:27],a[152:155],v[76:79]\n" \
"v_max3_f32 v225,v225,v67,v72\nv_max3_f32 v225,v225,v73,v74\nM v[76:79],a[28:31],a[156:159],v[76:79]\nv_max_f32 v225,v225,v75\nv_cndmask_b32 v225,v224,v225,s[90:91]\nM v[80:83],a[32:35],a[128:131],0\nds_read_b128 a[24:27],v240 offset:22528\nds_read_b128 a[28:31],v240 offset:23552\n" \
"M v[80:83],a[36:39],a[132:135],v[80:83]\nbuffer_load_dwordx4 v240,s[44:47],s36 offen offset:2048 sc1 lds\nM v[80:83],a[40:43],a[136:139],v[80:83]\nv_max3_f32 v229,v229,v71,v76\nv_max3_f32 v229,v229,v77,v78\nM v[80:83],a[44:47],a[140:143],v[80:83]\nv_max_f32 v229,v229,v79\nv_cndmask_b32 v229,v228,v229,s[90:91]\n" \
"M v[84:87],a[32:35],a[144:147],0\nds_read_b128 a[32:35],v240 offset:24576\nv_permlane16_swap_b32 v225,v229\nM v[84:87],a[36:39],a[148:151],v[84:87]\nv_max_f32 v225,v225,v229\nv_mov_b32 v229,v225\nM v[84:87],a[40:43],a[152:155],v[84:87]\nds_read_b128 a[36:39],v240 offset:25600\n" \
"v_permlane32_swap_b32 v225,v229\nM v[84:87],a[44:47],a[156:159],v[84:87]\nv_max_f32 v225,v225,v229\nv_mov_b32 v229,v225\nM v[88:91],a[48:51],a[128:131],0\nds_read_b128 a[40:43],v240 offset:26624\nv_permlane16_swap_b32 v225,v229\nM v[88:91],a[52:55],a[132:135],v[88:91]\n" \
"v_sub_f32 v224,0,v225\nv_sub_f32 v226,0,v225\nM v[88:91],a[56:59],a[136:139],v[88:91]\nv_sub_f32 v227,0,v225\nv_sub_f32 v225,0,v225\nM v[88:91],a[60:63],a[140:143],v[88:91]\nv_sub_f32 v228,0,v229\nv_sub_f32 v230,0,v229\n" \
"M v[92:95],a[48:51],a[144:147],0\nv_sub_f32 v231,0,v229\nv_sub_f32 v229,0,v229\nM v[92:95],a[52:55],a[148:151],v[92:95]\nds_read_b128 a[44:47],v240 offset:27648\nds_read_b128 a[48:51],v240 offset:28672\nM v[92:95],a[56:59],a[152:155],v[92:95]\nds_read_b128 a[52:55],v240 offset:29696\n" \
"ds_read_b128 a[56:59],v240 offset:30720\nM v[92:95],a[60:63],a[156:159],v[92:95]\nds_read_b128 a[60:63],v240 offset:31744\ns_waitcnt lgkmcnt(7)\nM v[96:99],a[0:3],a[128:131],v[224:227]\nv_add_f32 v64,v64,v224\nv_add_f32 v65,v65,v224\nM v[96:99],a[4:7],a[132:135],v[96:99]\n" \
"v_add_f32 v66,v66,v224\nv_add_f32 v67,v67,v224\nM v[96:99],a[8:11],a[136:139],v[96:99]\nv_exp_f32 v64,v64\nM v[96:99],a[12:15],a[140:143],v[96:99]\nv_exp_f32 v65,v65\nM v[100:103],a[0:3],a[144:147],v[228:231]\nv_exp_f32 v66,v66\n" \
"M v[100:103],a[4:7],a[148:151],v[100:103]\nv_exp_f32 v67,v67\nM v[100:103],a[8:11],a[152:155],v[100:103]\nv_add_f32 v68,v68,v228\nv_add_f32 v69,v69,v228\nM v[100:103],a[12:15],a[156:159],v[100:103]\nv_add_f32 v72,v72,v224\nv_add_f32 v73,v73,v224\n" \
"M v[104:107],a[16:19],a[128:131],v[224:227]\nv_add_f32 v74,v74,v224\nv_add_f32 v75,v75,v224\nM v[104:107],a[20:23],a[132:135],v[104:107]\nv_add_f32 v70,v70,v228\nv_add_f32 v71,v71,v228\nM v[104:107],a[24:27],a[136:139],v[104:107]\nv_exp_f32 v68,v68\n" \
"M v[104:107],a[28:31],a[140:143],v[104:107]\nv_exp_f32 v69,v69\nM v[108:111],a[16:19],a[144:147],v[228:231]\nv_exp_f32 v72,v72\nM v[108:111],a[20:23],a[148:151],v[108:111]\nv_exp_f32 v73,v73\nM v[108:111],a[24:27],a[152:155],v[108:111]\nv_exp_f32 v74,v74\n" \
"M v[108:111],a[28:31],a[156:159],v[108:111]\nv_exp_f32 v75,v75\nM v[112:115],a[32:35],a[128:131],v[224:227]\nv_cvt_pk_bf16_f32 v64,v64,v65\ns_waitcnt lgkmcnt(0)\nM v[112:115],a[36:39],a[132:135],v[112:115]\nv_exp_f32 v70,v70\nM v[112:115],a[40:43],a[136:139],v[112:115]\n" \
"v_exp_f32 v71,v71\nM v[112:115],a[44:47],a[140:143],v[112:115]\nv_add_f32 v76,v76,v228\nv_add_f32 v77,v77,v228\nM v[116:119],a[32:35],a[144:147],v[228:231]\nv_add_f32 v78,v78,v228\nv_add_f32 v79,v79,v228\nM v[116:119],a[36:39],a[148:151],v[116:119]\n" \
"v_cvt_pk_bf16_f32 v65,v66,v67\nv_cvt_pk_bf16_f32 v66,v72,v73\nM v[116:119],a[40:43],a[152:155],v[116:119]\nv_exp_f32 v76,v76\nM v[116:119],a[44:47],a[156:159],v[116:119]\nv_exp_f32 v77,v77\nM v[124:127],a[48:51],a[144:147],v[228:231]\nv_exp_f32 v78,v78\n" \
"M v[124:127],a[52:55],a[148:151],v[124:127]\nv_exp_f32 v79,v79\nM v[124:127],a[56:59],a[152:155],v[124:127]\nv_cvt_pk_bf16_f32 v67,v74,v75\nv_cvt_pk_bf16_f32 v68,v68,v69\nM v[124:127],a[60:63],a[156:159],v[124:127]\nv_cvt_pk_bf16_f32 v69,v70,v71\nv_cvt_pk_bf16_f32 v70,v76,v77\n" \
"v_cvt_pk_bf16_f32 v71,v78,v79\nM v[0:3],a[64:67],v[64:67],0\nbuffer_load_dwordx4 v240,s[44:47],s36 offen offset:3072 sc1 lds\nM v[4:7],a[64:67],v[68:71],0\nv_add_f32 v80,v80,v224\nv_add_f32 v81,v81,v224\nM v[8:11],a[68:71],v[64:67],0\nv_add_f32 v82,v82,v224\n" \
"v_add_f32 v83,v83,v224\nM v[12:15],a[68:71],v[68:71],0\nv_exp_f32 v80,v80\nM v[16:19],a[72:75],v[64:67],0\nv_exp_f32 v81,v81\nM v[20:23],a[72:75],v[68:71],0\nv_exp_f32 v82,v82\nM v[24:27],a[76:79],v[64:67],0\n" \
"v_exp_f32 v83,v83\nM v[28:31],a[76:79],v[68:71],0\nv_add_f32 v84,v84,v228\nv_add_f32 v85,v85,v228\nM v[32:35],a[80:83],v[64:67],0\nv_add_f32 v88,v88,v224\nv_add_f32 v89,v89,v224\nM v[36:39],a[80:83],v[68:71],0\n" \
"v_add_f32 v90,v90,v224\nv_add_f32 v91,v91,v224\nM v[40:43],a[84:87],v[64:67],0\nv_add_f32 v86,v86,v228\nv_add_f32 v87,v87,v228\nM v[44:47],a[84:87],v[68:71],0\nv_exp_f32 v84,v84\nM v[48:51],a[88:91],v[64:67],0\n" \
"v_exp_f32 v85,v85\nM v[52:55],a[88:91],v[68:71],0\nv_exp_f32 v88,v88\nM v[56:59],a[92:95],v[64:67],0\nv_exp_f32 v89,v89\nM v[60:63],a[92:95],v[68:71],0\nv_exp_f32 v90,v90\nv_exp_f32 v91,v91\n" \
"v_cvt_pk_bf16_f32 v80,v80,v81\nM a[192:195],a[208:211],v[64:67],0\nv_exp_f32 v86,v86\nv_exp_f32 v87,v87\nv_add_f32 v92,v92,v228\nv_add_f32 v93,v93,v228\nv_add_f32 v94,v94,v228\nv_add_f32 v95,v95,v228\n" \
"v_cvt_pk_bf16_f32 v81,v82,v83\nv_exp_f32 v92,v92\nv_exp_f32 v93,v93\nv_exp_f32 v94,v94\nv_exp_f32 v95,v95\nv_cvt_pk_bf16_f32 v82,v88,v89\nv_cvt_pk_bf16_f32 v83,v90,v91\nv_cvt_pk_bf16_f32 v84,v84,v85\n" \
"M a[196:199],a[208:211],v[68:71],0\nv_cvt_pk_bf16_f32 v85,v86,v87\nv_cvt_pk_bf16_f32 v86,v92,v93\nv_cvt_pk_bf16_f32 v87,v94,v95\nM v[0:3],a[96:99],v[80:83],v[0:3]\nds_read_b128 a[124:127],v241 offset:15360\nds_read_b128 a[0:3],v240 offset:32768\nM v[4:7],a[96:99],v[84:87],v[4:7]\n" \
"ds_read_b128 a[4:7],v240 offset:33792\nds_read_b128 a[8:11],v240 offset:34816\nM v[8:11],a[100:103],v[80:83],v[8:11]\nds_read_b128 a[12:15],v240 offset:35840\nds_read_b128 a[16:19],v240 offset:36864\nM v[12:15],a[100:103],v[84:87],v[12:15]\nds_read_b128 a[20:23],v240 offset:37888\nds_read_b128 a[24:27],v240 offset:38912\n" \
"M v[16:19],a[104:107],v[80:83],v[16:19]\nds_read_b128 a[28:31],v240 offset:39936\nds_read_b128 a[32:35],v240 offset:40960\nM v[20:23],a[104:107],v[84:87],v[20:23]\nds_read_b128 a[36:39],v240 offset:41984\nds_read_b128 a[40:43],v240 offset:43008\nM v[24:27],a[108:111],v[80:83],v[24:27]\nds_read_b128 a[44:47],v240 offset:44032\n" \
"ds_read_b128 a[48:51],v240 offset:45056\nM v[28:31],a[108:111],v[84:87],v[28:31]\nds_read_b128 a[52:55],v240 offset:46080\nds_read_b128 a[56:59],v240 offset:47104\nM v[32:35],a[112:115],v[80:83],v[32:35]\nds_read_b128 a[60:63],v240 offset:48128\nds_read_b128 a[64:67],v241 offset:16384\nM v[36:39],a[112:115],v[84:87],v[36:39]\n" \
"ds_read_b128 a[68:71],v241 offset:17408\nds_read_b128 a[72:75],v241 offset:18432\nM v[40:43],a[116:119],v[80:83],v[40:43]\nds_read_b128 a[76:79],v241 offset:19456\nds_read_b128 a[80:83],v241 offset:20480\nM v[44:47],a[116:119],v[84:87],v[44:47]\nds_read_b128 a[84:87],v241 offset:21504\nds_read_b128 a[88:91],v241 offset:22528\n" \
"M v[48:51],a[120:123],v[80:83],v[48:51]\nds_read_b128 a[92:95],v241 offset:23552\nds_read_b128 a[96:99],v241 offset:24576\nM v[52:55],a[120:123],v[84:87],v[52:55]\nds_read_b128 a[100:103],v241 offset:25600\ns_waitcnt lgkmcnt(15)\nM v[56:59],a[124:127],v[80:83],v[56:59]\nds_read_b128 a[104:107],v241 offset:26624\n" \
"ds_read_b128 a[108:111],v241 offset:27648\nM v[60:63],a[124:127],v[84:87],v[60:63]\nds_read_b128 a[112:115],v241 offset:28672\nds_read_b128 a[116:119],v241 offset:29696\nM a[192:195],a[208:211],v[80:83],a[192:195]\nds_read_b128 a[120:123],v241 offset:30720\nds_read_b128 a[124:127],v241 offset:31744\nM a[196:199],a[208:211],v[84:87],a[196:199]\n" \
"v_exp_f32 v96,v96\nM v[128:131],a[0:3],a[128:131],v[224:227]\nv_exp_f32 v97,v97\nM v[128:131],a[4:7],a[132:135],v[128:131]\nv_exp_f32 v98,v98\nM v[128:131],a[8:11],a[136:139],v[128:131]\nv_exp_f32 v99,v99\nM v[128:131],a[12:15],a[140:143],v[128:131]\n" \
"v_exp_f32 v100,v100\nM v[132:135],a[0:3],a[144:147],v[228:231]\nv_exp_f32 v101,v101\nM v[132:135],a[4:7],a[148:151],v[132:135]\nv_exp_f32 v104,v104\nM v[132:135],a[8:11],a[152:155],v[132:135]\nv_exp_f32 v105,v105\nM v[132:135],a[12:15],a[156:159],v[132:135]\n" \
"v_exp_f32 v106,v106\nM v[144:147],a[16:19],a[128:131],v[224:227]\nv_exp_f32 v107,v107\nM v[144:147],a[20:23],a[132:135],v[144:147]\nv_cvt_pk_bf16_f32 v96,v96,v97\nv_cvt_pk_bf16_f32 v97,v98,v99\nM v[144:147],a[24:27],a[136:139],v[144:147]\nv_exp_f32 v102,v102\n" \
"M v[144:147],a[28:31],a[140:143],v[144:147]\nv_exp_f32 v103,v103\nM v[148:151],a[16:19],a[144:147],v[228:231]\nv_exp_f32 v108,v108\nM v[148:151],a[20:23],a[148:151],v[148:151]\nv_exp_f32 v109,v109\nM v[148:151],a[24:27],a[152:155],v[148:151]\nv_exp_f32 v110,v110\n" \
"M v[148:151],a[28:31],a[156:159],v[148:151]\nv_exp_f32 v111,v111\nM v[160:163],a[32:35],a[128:131],v[224:227]\nv_cvt_pk_bf16_f32 v98,v104,v105\nv_cvt_pk_bf16_f32 v99,v106,v107\nM v[160:163],a[36:39],a[132:135],v[160:163]\nv_cvt_pk_bf16_f32 v100,v100,v101\nv_cvt_pk_bf16_f32 v101,v102,v103\n" \
"M v[160:163],a[40:43],a[136:139],v[160:163]\nv_cvt_pk_bf16_f32 v102,v108,v109\ns_waitcnt lgkmcnt(0)\nM v[160:163],a[44:47],a[140:143],v[160:163]\nv_cvt_pk_bf16_f32 v103,v110,v111\nv_cndmask_b32 v112,v112,v252,s[82:83]\nM v[164:167],a[32:35],a[144:147],v[228:231]\nv_cndmask_b32 v113,v113,v252,s[84:85]\n" \
"v_cndmask_b32 v114,v114,v252,s[86:87]\nM v[164:167],a[36:39],a[148:151],v[164:167]\nv_cndmask_b32 v115,v115,v252,s[88:89]\nv_cndmask_b32 v124,v124,v252,s[82:83]\nM v[164:167],a[40:43],a[152:155],v[164:167]\nv_exp_f32 v112,v112\nM v[164:167],a[44:47],a[156:159],v[164:167]\nv_exp_f32 v113,v113\n" \
"M v[176:179],a[48:51],a[128:131],v[224:227]\nv_exp_f32 v114,v114\nM v[176:179],a[52:55],a[132:135],v[176:179]\nv_exp_f32 v115,v115\nM v[176:179],a[56:59],a[136:139],v[176:179]\nv_exp_f32 v116,v116\nM v[176:179],a[60:63],a[140:143],v[176:179]\nv_exp_f32 v117,v117\n" \
"M v[180:183],a[48:51],a[144:147],v[228:231]\ns_waitcnt vmcnt(0)\ns_barrier\nM v[180:183],a[52:55],a[148:151],v[180:183]\nv_cvt_pk_bf16_f32 v112,v112,v113\nv_cndmask_b32 v125,v125,v252,s[84:85]\nM v[180:183],a[56:59],a[152:155],v[180:183]\nv_cndmask_b32 v126,v126,v252,s[86:87]\n" \
"v_cndmask_b32 v127,v127,v252,s[88:89]\nM v[180:183],a[60:63],a[156:159],v[180:183]\nv_exp_f32 v118,v118\nM v[0:3],a[64:67],v[96:99],v[0:3]\nv_exp_f32 v119,v119\nM v[4:7],a[64:67],v[100:103],v[4:7]\nv_cvt_pk_bf16_f32 v113,v114,v115\nv_mov_b32 v114,0\n" \
"M v[8:11],a[68:71],v[96:99],v[8:11]\nv_exp_f32 v124,v124\nM v[12:15],a[68:71],v[100:103],v[12:15]\nv_exp_f32 v125,v125\nM v[16:19],a[72:75],v[96:99],v[16:19]\nv_exp_f32 v126,v126\nM v[20:23],a[72:75],v[100:103],v[20:23]\nv_exp_f32 v127,v127\n" \
"M v[24:27],a[76:79],v[96:99],v[24:27]\nv_mov_b32 v115,0\nv_cvt_pk_bf16_f32 v116,v116,v117\nM v[28:31],a[76:79],v[100:103],v[28:31]\nv_cvt_pk_bf16_f32 v117,v118,v119\nv_cvt_pk_bf16_f32 v118,v124,v125\nM v[32:35],a[80:83],v[96:99],v[32:35]\nv_cvt_pk_bf16_f32 v119,v126,v127\n" \
"ds_read_b128 a[0:3],v240 offset:49152\nM v[36:39],a[80:83],v[100:103],v[36:39]\nds_read_b128 a[4:7],v240 offset:50176\nds_read_b128 a[8:11],v240 offset:51200\nM v[40:43],a[84:87],v[96:99],v[40:43]\nds_read_b128 a[12:15],v240 offset:52224\nds_read_b128 a[16:19],v240 offset:53248\nM v[44:47],a[84:87],v[100:103],v[44:47]\n" \
"ds_read_b128 a[20:23],v240 offset:54272\nds_read_b128 a[24:27],v240 offset:55296\nM v[48:51],a[88:91],v[96:99],v[48:51]\nds_read_b128 a[28:31],v240 offset:56320\nds_read_b128 a[32:35],v240 offset:57344\nM v[52:55],a[88:91],v[100:103],v[52:55]\nv_exp_f32 v128,v128\nM v[56:59],a[92:95],v[96:99],v[56:59]\n" \
"v_exp_f32 v129,v129\nM v[60:63],a[92:95],v[100:103],v[60:63]\nv_exp_f32 v130,v130\nM a[192:195],a[208:211],v[96:99],a[192:195]\nv_exp_f32 v131,v131\nM a[196:199],a[208:211],v[100:103],a[196:199]\nv_cvt_pk_bf16_f32 v192,v128,v129\nv_cvt_pk_bf16_f32 v193,v130,v131\n" \
"M v[0:3],a[96:99],v[112:115],v[0:3]\nds_read_b128 a[36:39],v240 offset:58368\nds_read_b128 a[40:43],v240 offset:59392\nM v[4:7],a[96:99],v[116:119],v[4:7]\nds_read_b128 a[44:47],v240 offset:60416\nds_read_b128 a[48:51],v240 offset:61440\nM v[8:11],a[100:103],v[112:115],v[8:11]\nds_read_b128 a[52:55],v240 offset:62464\n" \
"ds_read_b128 a[56:59],v240 offset:63488\nM v[12:15],a[100:103],v[116:119],v[12:15]\nds_read_b128 a[60:63],v240 offset:64512\nds_read_b128 a[64:67],v241 offset:32768\nM v[16:19],a[104:107],v[112:115],v[16:19]\nds_read_b128 a[68:71],v241 offset:33792\nds_read_b128 a[72:75],v241 offset:34816\nM v[20:23],a[104:107],v[116:119],v[20:23]\n" \
"ds_read_b128 a[76:79],v241 offset:35840\nds_read_b128 a[80:83],v241 offset:36864\nM v[24:27],a[108:111],v[112:115],v[24:27]\nds_read_b128 a[84:87],v241 offset:37888\nds_read_b128 a[88:91],v241 offset:38912\nM v[28:31],a[108:111],v[116:119],v[28:31]\nds_read_b128 a[92:95],v241 offset:39936\nds_read_b128 a[96:99],v241 offset:40960\n" \
"M v[32:35],a[112:115],v[112:115],v[32:35]\nds_read_b128 a[100:103],v241 offset:41984\nds_read_b128 a[104:107],v241 offset:43008\nM v[36:39],a[112:115],v[116:119],v[36:39]\nds_read_b128 a[108:111],v241 offset:44032\nds_read_b128 a[112:115],v241 offset:45056\nM v[40:43],a[116:119],v[112:115],v[40:43]\nv_exp_f32 v132,v132\n" \
"M v[44:47],a[116:119],v[116:119],v[44:47]\nds_read_b128 a[116:119],v241 offset:46080\ns_lshl_b32 s69,s61,17\nM v[48:51],a[120:123],v[112:115],v[48:51]\nv_exp_f32 v133,v133\nM v[52:55],a[120:123],v[116:119],v[52:55]\nds_read_b128 a[120:123],v241 offset:47104\nv_cvt_pk_bf16_f32 v196,v132,v133\n" \
"M v[56:59],a[124:127],v[112:115],v[56:59]\nv_exp_f32 v134,v134\nM v[60:63],a[124:127],v[116:119],v[60:63]\nds_read_b128 a[124:127],v241 offset:48128\ns_add_u32 s69,s69,s68\nM a[192:195],a[208:211],v[112:115],a[192:195]\nv_exp_f32 v135,v135\nM a[196:199],a[208:211],v[116:119],a[196:199]\n" \
"v_cvt_pk_bf16_f32 v197,v134,v135\ns_add_u32 m0,s64,131072\nM v[64:67],a[244:247],a[244:247],0\nv_exp_f32 v144,v144\nM v[68:71],a[244:247],a[244:247],0\nv_exp_f32 v145,v145\nM v[72:75],a[244:247],a[244:247],0\nv_exp_f32 v146,v146\n" \
"M v[76:79],a[244:247],a[244:247],0\nv_exp_f32 v147,v147\nM v[80:83],a[244:247],a[244:247],0\nv_cvt_pk_bf16_f32 v194,v144,v145\nv_cvt_pk_bf16_f32 v195,v146,v147\nM v[84:87],a[244:247],a[244:247],0\nv_exp_f32 v148,v148\nM v[88:91],a[244:247],a[244:247],0\n" \
"v_exp_f32 v149,v149\nM v[92:95],a[244:247],a[244:247],0\nv_exp_f32 v150,v150\nM v[96:99],a[244:247],a[244:247],0\nv_exp_f32 v151,v151\nM v[100:103],a[244:247],a[244:247],0\nv_cvt_pk_bf16_f32 v198,v148,v149\nv_cvt_pk_bf16_f32 v199,v150,v151\n" \
"M v[104:107],a[244:247],a[244:247],0\nbuffer_load_dword v248,v251,s[52:55],s80 offen sc1\nM v[108:111],a[244:247],a[244:247],0\nv_exp_f32 v160,v160\nM v[112:115],a[244:247],a[244:247],0\nv_exp_f32 v161,v161\nM v[116:119],a[244:247],a[244:247],0\nv_exp_f32 v162,v162\n" \
"M v[120:123],a[244:247],a[244:247],0\nv_exp_f32 v163,v163\nM v[124:127],a[244:247],a[244:247],0\nv_cvt_pk_bf16_f32 v208,v160,v161\nv_cvt_pk_bf16_f32 v209,v162,v163\nM a[200:203],a[244:247],a[244:247],0\nv_exp_f32 v164,v164\nM a[204:207],a[244:247],a[244:247],0\n" \
"v_mov_b32 v168,0\ns_waitcnt lgkmcnt(0)\nM v[136:139],a[0:3],a[160:163],0\nv_exp_f32 v165,v165\nM v[136:139],a[4:7],a[164:167],v[136:139]\nv_exp_f32 v166,v166\nM v[136:139],a[8:11],a[168:171],v[136:139]\nv_exp_f32 v167,v167\n" \
"M v[136:139],a[12:15],a[172:175],v[136:139]\nv_cvt_pk_bf16_f32 v212,v164,v165\nv_cvt_pk_bf16_f32 v213,v166,v167\nM v[140:143],a[0:3],a[176:179],0\nv_exp_f32 v176,v176\nM v[140:143],a[4:7],a[180:183],v[140:143]\nv_exp_f32 v177,v177\nM v[140:143],a[8:11],a[184:187],v[140:143]\n" \
"v_exp_f32 v178,v178\nM v[140:143],a[12:15],a[188:191],v[140:143]\nv_exp_f32 v179,v179\nM v[156:159],a[16:19],a[176:179],0\nv_cvt_pk_bf16_f32 v210,v176,v177\nv_cvt_pk_bf16_f32 v211,v178,v179\nM v[156:159],a[20:23],a[180:183],v[156:159]\nv_exp_f32 v180,v180\n" \
"M v[156:159],a[24:27],a[184:187],v[156:159]\nv_exp_f32 v181,v181\nM v[156:159],a[28:31],a[188:191],v[156:159]\nv_exp_f32 v182,v182\nM v[128:131],a[0:3],a[128:131],v[224:227]\nv_exp_f32 v183,v183\nM v[128:131],a[4:7],a[132:135],v[128:131]\nv_cvt_pk_bf16_f32 v214,v180,v181\n" \
"v_cvt_pk_bf16_f32 v215,v182,v183\nM v[128:131],a[8:11],a[136:139],v[128:131]\ns_barrier\nv_max3_f32 v237,v140,v141,v142\nM v[128:131],a[12:15],a[140:143],v[128:131]\nbuffer_load_dwordx4 v243,s[40:43],s69 offen offset:0 lds\nM v[132:135],a[0:3],a[144:147],v[228:231]\nds_read_b128 a[0:3],v240 offset:0\n" \
"v_max3_f32 v237,v237,v143,v156\nM v[132:135],a[4:7],a[148:151],v[132:135]\nbuffer_load_dwordx4 v244,s[40:43],s69 offen offset:1024 lds\nM v[132:135],a[8:11],a[152:155],v[132:135]\nbuffer_load_dwordx4 v245,s[40:43],s69 offen offset:2048 lds\nM v[132:135],a[12:15],a[156:159],v[132:135]\nbuffer_load_dwordx4 v246,s[40:43],s69 offen offset:3072 lds\nM v[144:147],a[16:19],a[128:131],v[224:227]\n" \
"ds_read_b128 a[4:7],v240 offset:1024\nds_read_b128 a[8:11],v240 offset:2048\nM v[144:147],a[20:23],a[132:135],v[144:147]\ns_lshl_b32 s69,s61,17\nds_read_b128 a[12:15],v240 offset:3072\nM v[144:147],a[24:27],a[136:139],v[144:147]\ns_add_u32 s69,s69,s68\ns_add_u32 s69,s69,0x10000\n" \
"M v[144:147],a[28:31],a[140:143],v[144:147]\ns_add_u32 m0,s64,147456\nv_max3_f32 v233,v136,v137,v138\nM v[148:151],a[16:19],a[144:147],v[228:231]\nds_read_b128 a[16:19],v240 offset:4096\nv_max3_f32 v237,v237,v157,v158\nM v[148:151],a[20:23],a[148:151],v[148:151]\nds_read_b128 a[20:23],v240 offset:5120\n" \
"v_max_f32 v233,v233,v139\nM v[148:151],a[24:27],a[152:155],v[148:151]\nbuffer_load_dwordx4 v243,s[40:43],s69 offen offset:0 lds\nM v[148:151],a[28:31],a[156:159],v[148:151]\nbuffer_load_dwordx4 v244,s[40:43],s69 offen offset:1024 lds\nM v[160:163],a[32:35],a[128:131],v[224:227]\nbuffer_load_dwordx4 v245,s[40:43],s69 offen offset:2048 lds\nM v[160:163],a[36:39],a[132:135],v[160:163]\n" \
"buffer_load_dwordx4 v246,s[40:43],s69 offen offset:3072 lds\nM v[160:163],a[40:43],a[136:139],v[160:163]\nds_read_b128 a[24:27],v240 offset:6144\nds_read_b128 a[28:31],v240 offset:7168\nM v[160:163],a[44:47],a[140:143],v[160:163]\nv_max_f32 v237,v237,v159\ns_add_u32 m0,s64,65536\nM v[164:167],a[32:35],a[144:147],v[228:231]\n" \
"ds_read_b128 a[32:35],v240 offset:8192\nv_cndmask_b32 v233,v232,v233,s[90:91]\nM v[164:167],a[36:39],a[148:151],v[164:167]\nv_cndmask_b32 v237,v236,v237,s[90:91]\nds_read_b128 a[36:39],v240 offset:9216\nM v[164:167],a[40:43],a[152:155],v[164:167]\nv_permlane16_swap_b32 v233,v237\nds_read_b128 a[40:43],v240 offset:10240\n" \
"M v[164:167],a[44:47],a[156:159],v[164:167]\nv_max_f32 v233,v233,v237\nds_read_b128 a[44:47],v240 offset:11264\nM v[176:179],a[48:51],a[128:131],v[224:227]\nv_mov_b32 v237,v233\nv_cndmask_b32 v136,v136,v252,s[82:83]\nM v[176:179],a[52:55],a[132:135],v[176:179]\nv_permlane32_swap_b32 v233,v237\n" \
"v_max_f32 v233,v233,v237\nM v[176:179],a[56:59],a[136:139],v[176:179]\ns_waitcnt vmcnt(8)\nv_cmp_eq_u32 vcc,0,v248\nM v[176:179],a[60:63],a[140:143],v[176:179]\ns_cbranch_vccnz .Lps5\n.Lpb5:\nv_mov_b32 v237,v233\n" \
"M v[180:183],a[48:51],a[144:147],v[228:231]\nbuffer_load_dwordx4 v240,s[48:51],s36 offen offset:0 sc1 lds\nM v[180:183],a[52:55],a[148:151],v[180:183]\nbuffer_load_dwordx4 v240,s[48:51],s36 offen offset:1024 sc1 lds\nM v[180:183],a[56:59],a[152:155],v[180:183]\nbuffer_load_dwordx4 v240,s[48:51],s36 offen offset:2048 sc1 lds\nM v[180:183],a[60:63],a[156:159],v[180:183]\nbuffer_load_dwordx4 v240,s[48:51],s36 offen offset:3072 sc1 lds\n" \
"M v[0:3],a[64:67],v[192:195],v[0:3]\ns_add_u32 m0,s64,16384\nds_read_b128 a[48:51],v240 offset:12288\nM v[4:7],a[64:67],v[196:199],v[4:7]\nbuffer_load_dwordx4 v240,s[44:47],s65 offen offset:0 sc1 lds\nM v[8:11],a[68:71],v[192:195],v[8:11]\nbuffer_load_dwordx4 v240,s[44:47],s65 offen offset:1024 sc1 lds\nM v[12:15],a[68:71],v[196:199],v[12:15]\n" \
"buffer_load_dwordx4 v240,s[44:47],s65 offen offset:2048 sc1 lds\nM v[16:19],a[72:75],v[192:195],v[16:19]\nbuffer_load_dwordx4 v240,s[44:47],s65 offen offset:3072 sc1 lds\nM v[20:23],a[72:75],v[196:199],v[20:23]\nds_read_b128 a[52:55],v240 offset:13312\ns_add_u32 m0,s64,81920\nM v[24:27],a[76:79],v[192:195],v[24:27]\nds_read_b128 a[56:59],v240 offset:14336\n" \
"ds_read_b128 a[60:63],v240 offset:15360\nM v[28:31],a[76:79],v[196:199],v[28:31]\nbuffer_load_dwordx4 v240,s[48:51],s65 offen offset:0 sc1 lds\nM v[32:35],a[80:83],v[192:195],v[32:35]\nbuffer_load_dwordx4 v240,s[48:51],s65 offen offset:1024 sc1 lds\nM v[36:39],a[80:83],v[196:199],v[36:39]\nbuffer_load_dwordx4 v240,s[48:51],s65 offen offset:2048 sc1 lds\nM v[40:43],a[84:87],v[192:195],v[40:43]\n" \
"buffer_load_dwordx4 v240,s[48:51],s65 offen offset:3072 sc1 lds\nM v[44:47],a[84:87],v[196:199],v[44:47]\nv_permlane16_swap_b32 v233,v237\nv_cndmask_b32 v137,v137,v252,s[84:85]\nM v[48:51],a[88:91],v[192:195],v[48:51]\nv_cndmask_b32 v138,v138,v252,s[86:87]\nv_cndmask_b32 v139,v139,v252,s[88:89]\nM v[52:55],a[88:91],v[196:199],v[52:55]\n" \
"v_cndmask_b32 v156,v156,v252,s[82:83]\nv_cndmask_b32 v157,v157,v252,s[84:85]\nM v[56:59],a[92:95],v[192:195],v[56:59]\nv_cndmask_b32 v158,v158,v252,s[86:87]\nv_cndmask_b32 v159,v159,v252,s[88:89]\nM v[60:63],a[92:95],v[196:199],v[60:63]\nv_sub_f32 v232,0,v233\nv_sub_f32 v236,0,v237\n" \
"M a[192:195],a[208:211],v[192:195],a[192:195]\ns_add_u32 m0,s64,32768\nv_add_f32 v136,v136,v232\nM a[196:199],a[208:211],v[196:199],a[196:199]\nv_add_f32 v137,v137,v232\nv_add_f32 v138,v138,v232\nM v[0:3],a[96:99],v[208:211],v[0:3]\nv_add_f32 v139,v139,v232\n" \
"v_add_f32 v140,v140,v236\nM v[4:7],a[96:99],v[212:215],v[4:7]\nv_add_f32 v141,v141,v236\nv_add_f32 v142,v142,v236\nM v[8:11],a[100:103],v[208:211],v[8:11]\nv_add_f32 v143,v143,v236\nv_add_f32 v156,v156,v236\nM v[12:15],a[100:103],v[212:215],v[12:15]\n" \
"v_add_f32 v157,v157,v236\nv_add_f32 v158,v158,v236\nM v[16:19],a[104:107],v[208:211],v[16:19]\nv_add_f32 v159,v159,v236\nv_sub_f32 v234,0,v233\nM v[20:23],a[104:107],v[212:215],v[20:23]\nbuffer_load_dwordx4 v240,s[44:47],s66 offen offset:0 sc1 lds\nM v[24:27],a[108:111],v[208:211],v[24:27]\n" \
"buffer_load_dwordx4 v240,s[44:47],s66 offen offset:1024 sc1 lds\nM v[28:31],a[108:111],v[212:215],v[28:31]\nbuffer_load_dwordx4 v240,s[44:47],s66 offen offset:2048 sc1 lds\nM v[32:35],a[112:115],v[208:211],v[32:35]\nbuffer_load_dwordx4 v240,s[44:47],s66 offen offset:3072 sc1 lds\nM v[36:39],a[112:115],v[212:215],v[36:39]\nv_exp_f32 v136,v136\nM v[40:43],a[116:119],v[208:211],v[40:43]\n" \
"v_exp_f32 v137,v137\nM v[44:47],a[116:119],v[212:215],v[44:47]\nv_exp_f32 v138,v138\nM v[48:51],a[120:123],v[208:211],v[48:51]\nv_exp_f32 v139,v139\nM v[52:55],a[120:123],v[212:215],v[52:55]\nv_exp_f32 v140,v140\nM v[56:59],a[124:127],v[208:211],v[56:59]\n" \
"v_exp_f32 v141,v141\nM v[60:63],a[124:127],v[212:215],v[60:63]\nv_exp_f32 v142,v142\nv_exp_f32 v143,v143\nv_exp_f32 v156,v156\nv_exp_f32 v157,v157\nv_exp_f32 v158,v158\nv_exp_f32 v159,v159\n" \
"v_exp_f32 v128,v128\nv_exp_f32 v129,v129\nv_exp_f32 v130,v130\nv_exp_f32 v131,v131\nv_exp_f32 v132,v132\nv_exp_f32 v133,v133\nv_exp_f32 v134,v134\nv_exp_f32 v135,v135\n" \
"v_exp_f32 v144,v144\nv_exp_f32 v145,v145\nv_exp_f32 v146,v146\nv_exp_f32 v147,v147\nv_exp_f32 v148,v148\nv_exp_f32 v149,v149\nv_exp_f32 v150,v150\nv_exp_f32 v151,v151\n" \
"M a[192:195],a[208:211],v[208:211],a[192:195]\nv_sub_f32 v235,0,v233\nv_sub_f32 v238,0,v237\nv_sub_f32 v239,0,v237\ns_add_u32 m0,s64,98304\nv_sub_f32 v233,0,v233\nv_sub_f32 v237,0,v237\nv_exp_f32 v160,v160\n" \
"v_exp_f32 v161,v161\nv_exp_f32 v162,v162\nv_exp_f32 v163,v163\nv_exp_f32 v164,v164\nv_exp_f32 v165,v165\nv_exp_f32 v166,v166\nv_exp_f32 v167,v167\nv_exp_f32 v176,v176\n" \
"v_exp_f32 v177,v177\nv_exp_f32 v178,v178\nv_exp_f32 v179,v179\nv_exp_f32 v180,v180\nv_exp_f32 v181,v181\nv_exp_f32 v182,v182\nv_exp_f32 v183,v183\nv_mov_b32 v169,0\n" \
"v_mov_b32 v170,0\nv_mov_b32 v171,0\nv_mov_b32 v172,0xff800000\nv_mov_b32 v173,0xff800000\nv_mov_b32 v174,0xff800000\nv_mov_b32 v175,0xff800000\nv_mov_b32 v184,0xff800000\nv_mov_b32 v185,0xff800000\n" \
"v_mov_b32 v186,0xff800000\nv_mov_b32 v187,0xff800000\nv_mov_b32 v188,0xff800000\nv_mov_b32 v189,0xff800000\nv_mov_b32 v190,0xff800000\nv_mov_b32 v191,0xff800000\nv_cvt_pk_bf16_f32 v200,v136,v137\nv_cvt_pk_bf16_f32 v201,v138,v139\n" \
"v_mov_b32 v202,0\nv_mov_b32 v203,0\nv_cvt_pk_bf16_f32 v204,v140,v141\nv_cvt_pk_bf16_f32 v205,v142,v143\nv_cvt_pk_bf16_f32 v206,v156,v157\nv_cvt_pk_bf16_f32 v207,v158,v159\nv_cvt_pk_bf16_f32 v192,v128,v129\nv_cvt_pk_bf16_f32 v193,v130,v131\n" \
"v_cvt_pk_bf16_f32 v194,v144,v145\nv_cvt_pk_bf16_f32 v195,v146,v147\nv_cvt_pk_bf16_f32 v196,v132,v133\nv_cvt_pk_bf16_f32 v197,v134,v135\nv_cvt_pk_bf16_f32 v198,v148,v149\nv_cvt_pk_bf16_f32 v199,v150,v151\nbuffer_load_dwordx4 v240,s[48:51],s66 offen offset:0 sc1 lds\nbuffer_load_dwordx4 v240,s[48:51],s66 offen offset:1024 sc1 lds\n" \
"buffer_load_dwordx4 v240,s[48:51],s66 offen offset:2048 sc1 lds\nbuffer_load_dwordx4 v240,s[48:51],s66 offen offset:3072 sc1 lds\nM a[196:199],a[208:211],v[212:215],a[196:199]\ns_cmp_eq_u32 s62,0\ns_cbranch_scc1 .Ltrans\n.Ldual:\ns_waitcnt lgkmcnt(8)\nM v[128:131],a[0:3],a[128:131],v[224:227]\n" \
"v_cvt_pk_bf16_f32 v208,v160,v161\nv_cvt_pk_bf16_f32 v209,v162,v163\nM v[128:131],a[4:7],a[132:135],v[128:131]\nv_cvt_pk_bf16_f32 v212,v164,v165\nv_cvt_pk_bf16_f32 v213,v166,v167\nM v[128:131],a[8:11],a[136:139],v[128:131]\nv_cvt_pk_bf16_f32 v216,v168,v169\nv_cvt_pk_bf16_f32 v217,v170,v171\n" \
"M v[128:131],a[12:15],a[140:143],v[128:131]\nv_exp_f32 v172,v172\nM v[132:135],a[0:3],a[144:147],v[228:231]\nv_exp_f32 v173,v173\nM v[132:135],a[4:7],a[148:151],v[132:135]\nv_exp_f32 v174,v174\nM v[132:135],a[8:11],a[152:155],v[132:135]\nv_exp_f32 v175,v175\n" \
"M v[132:135],a[12:15],a[156:159],v[132:135]\nv_cvt_pk_bf16_f32 v220,v172,v173\nv_cvt_pk_bf16_f32 v221,v174,v175\nM v[136:139],a[0:3],a[160:163],v[232:235]\nv_cvt_pk_bf16_f32 v210,v176,v177\nv_cvt_pk_bf16_f32 v211,v178,v179\nM v[136:139],a[4:7],a[164:167],v[136:139]\nv_cvt_pk_bf16_f32 v214,v180,v181\n" \
"v_cvt_pk_bf16_f32 v215,v182,v183\nM v[136:139],a[8:11],a[168:171],v[136:139]\nds_read_b128 a[64:67],v250 offset:0\nds_read_b128 a[68:71],v250 offset:1024\nM v[136:139],a[12:15],a[172:175],v[136:139]\nv_exp_f32 v184,v184\nM v[140:143],a[0:3],a[176:179],v[236:239]\nv_exp_f32 v185,v185\n" \
"M v[140:143],a[4:7],a[180:183],v[140:143]\nv_exp_f32 v186,v186\nM v[140:143],a[8:11],a[184:187],v[140:143]\nv_exp_f32 v187,v187\nM v[140:143],a[12:15],a[188:191],v[140:143]\nds_read_b128 a[72:75],v250 offset:2048\nds_read_b128 a[76:79],v250 offset:3072\nM v[144:147],a[16:19],a[128:131],v[224:227]\n" \
"ds_read_b128 a[80:83],v250 offset:4096\nds_read_b128 a[84:87],v250 offset:5120\nM v[144:147],a[20:23],a[132:135],v[144:147]\nds_read_b128 a[88:91],v250 offset:6144\nds_read_b128 a[92:95],v250 offset:7168\nM v[144:147],a[24:27],a[136:139],v[144:147]\nds_read_b128 a[96:99],v250 offset:8192\nds_read_b128 a[100:103],v250 offset:9216\n" \
"M v[144:147],a[28:31],a[140:143],v[144:147]\nds_read_b128 a[104:107],v250 offset:10240\nds_read_b128 a[108:111],v250 offset:11264\nM v[148:151],a[16:19],a[144:147],v[228:231]\nds_read_b128 a[112:115],v250 offset:12288\nds_read_b128 a[116:119],v250 offset:13312\nM v[148:151],a[20:23],a[148:151],v[148:151]\nds_read_b128 a[120:123],v250 offset:14336\n" \
"ds_read_b128 a[124:127],v250 offset:15360\nM v[148:151],a[24:27],a[152:155],v[148:151]\nv_cvt_pk_bf16_f32 v218,v184,v185\nv_cvt_pk_bf16_f32 v219,v186,v187\nM v[148:151],a[28:31],a[156:159],v[148:151]\nv_exp_f32 v188,v188\nM v[152:155],a[16:19],a[160:163],v[232:235]\nv_exp_f32 v189,v189\n" \
"M v[152:155],a[20:23],a[164:167],v[152:155]\nv_exp_f32 v190,v190\nM v[152:155],a[24:27],a[168:171],v[152:155]\nv_exp_f32 v191,v191\nM v[152:155],a[28:31],a[172:175],v[152:155]\nv_cvt_pk_bf16_f32 v222,v188,v189\nv_cvt_pk_bf16_f32 v223,v190,v191\nM v[156:159],a[16:19],a[176:179],v[236:239]\n" \
"s_add_u32 m0,s94,s64\nv_xor_b32 v250,0x18000,v250\nM v[156:159],a[20:23],a[180:183],v[156:159]\nbuffer_load_dwordx4 v240,s[44:47],s95 offen offset:0 sc1 lds\nM v[156:159],a[24:27],a[184:187],v[156:159]\nbuffer_load_dwordx4 v240,s[44:47],s95 offen offset:1024 sc1 lds\nM v[156:159],a[28:31],a[188:191],v[156:159]\ns_add_u32 s94,s94,0x4000\n" \
"s_waitcnt lgkmcnt(1)\nM v[160:163],a[32:35],a[128:131],v[224:227]\nbuffer_load_dwordx4 v240,s[44:47],s95 offen offset:2048 sc1 lds\nM v[160:163],a[36:39],a[132:135],v[160:163]\nv_exp_f32 v128,v128\nM v[160:163],a[40:43],a[136:139],v[160:163]\nv_exp_f32 v129,v129\nM v[160:163],a[44:47],a[140:143],v[160:163]\n" \
"v_exp_f32 v130,v130\nM v[164:167],a[32:35],a[144:147],v[228:231]\nv_exp_f32 v131,v131\nM v[164:167],a[36:39],a[148:151],v[164:167]\nv_exp_f32 v144,v144\nM v[164:167],a[40:43],a[152:155],v[164:167]\nv_exp_f32 v145,v145\nM v[164:167],a[44:47],a[156:159],v[164:167]\n" \
"v_exp_f32 v146,v146\nM v[168:171],a[32:35],a[160:163],v[232:235]\nv_exp_f32 v147,v147\nM v[168:171],a[36:39],a[164:167],v[168:171]\nv_exp_f32 v132,v132\nM v[168:171],a[40:43],a[168:171],v[168:171]\nv_exp_f32 v133,v133\nM v[168:171],a[44:47],a[172:175],v[168:171]\n" \
"v_exp_f32 v134,v134\nM v[172:175],a[32:35],a[176:179],v[236:239]\nv_exp_f32 v135,v135\nM v[172:175],a[36:39],a[180:183],v[172:175]\nbuffer_load_dwordx4 v240,s[44:47],s95 offen offset:3072 sc1 lds\nM v[172:175],a[40:43],a[184:187],v[172:175]\nv_exp_f32 v148,v148\nM v[172:175],a[44:47],a[188:191],v[172:175]\n" \
"v_exp_f32 v149,v149\nM v[176:179],a[48:51],a[128:131],v[224:227]\nv_exp_f32 v150,v150\nM v[176:179],a[52:55],a[132:135],v[176:179]\nv_exp_f32 v151,v151\nM v[176:179],a[56:59],a[136:139],v[176:179]\nv_exp_f32 v136,v136\nM v[176:179],a[60:63],a[140:143],v[176:179]\n" \
"v_exp_f32 v137,v137\nM v[180:183],a[48:51],a[144:147],v[228:231]\nv_exp_f32 v138,v138\nM v[180:183],a[52:55],a[148:151],v[180:183]\nv_exp_f32 v139,v139\nM v[180:183],a[56:59],a[152:155],v[180:183]\nv_exp_f32 v152,v152\nM v[180:183],a[60:63],a[156:159],v[180:183]\n" \
"v_exp_f32 v153,v153\nM v[184:187],a[48:51],a[160:163],v[232:235]\nv_exp_f32 v154,v154\nM v[184:187],a[52:55],a[164:167],v[184:187]\nv_exp_f32 v155,v155\nM v[184:187],a[56:59],a[168:171],v[184:187]\nv_exp_f32 v140,v140\nM v[184:187],a[60:63],a[172:175],v[184:187]\n" \
"v_exp_f32 v141,v141\nM v[188:191],a[48:51],a[176:179],v[236:239]\nv_exp_f32 v142,v142\nM v[188:191],a[52:55],a[180:183],v[188:191]\nv_exp_f32 v143,v143\nM v[188:191],a[56:59],a[184:187],v[188:191]\nv_exp_f32 v156,v156\nM v[188:191],a[60:63],a[188:191],v[188:191]\n" \
"v_exp_f32 v157,v157\nM v[0:3],a[64:67],v[192:195],v[0:3]\ns_waitcnt vmcnt(16) lgkmcnt(0)\ns_barrier\nM v[4:7],a[64:67],v[196:199],v[4:7]\nds_read_b128 a[0:3],v250 offset:0\nds_read_b128 a[4:7],v250 offset:1024\nM v[64:67],a[64:67],v[200:203],v[64:67]\n" \
"ds_read_b128 a[8:11],v250 offset:2048\nds_read_b128 a[12:15],v250 offset:3072\nM v[68:71],a[64:67],v[204:207],v[68:71]\nds_read_b128 a[16:19],v250 offset:4096\nds_read_b128 a[20:23],v250 offset:5120\nM v[8:11],a[68:71],v[192:195],v[8:11]\nds_read_b128 a[24:27],v250 offset:6144\nds_read_b128 a[28:31],v250 offset:7168\n" \
"M v[12:15],a[68:71],v[196:199],v[12:15]\nds_read_b128 a[32:35],v250 offset:8192\nds_read_b128 a[36:39],v250 offset:9216\nM v[72:75],a[68:71],v[200:203],v[72:75]\nds_read_b128 a[40:43],v250 offset:10240\nds_read_b128 a[44:47],v250 offset:11264\nM v[76:79],a[68:71],v[204:207],v[76:79]\nds_read_b128 a[48:51],v250 offset:12288\n" \
"ds_read_b128 a[52:55],v250 offset:13312\nM v[16:19],a[72:75],v[192:195],v[16:19]\nds_read_b128 a[56:59],v250 offset:14336\nds_read_b128 a[60:63],v250 offset:15360\nM v[20:23],a[72:75],v[196:199],v[20:23]\ns_add_u32 m0,m0,0x10000\ns_and_b32 s94,s94,0xffff\nM v[80:83],a[72:75],v[200:203],v[80:83]\n" \
"v_exp_f32 v158,v158\nM v[84:87],a[72:75],v[204:207],v[84:87]\nv_exp_f32 v159,v159\nM v[24:27],a[76:79],v[192:195],v[24:27]\nbuffer_load_dwordx4 v240,s[48:51],s95 offen offset:0 sc1 lds\nM v[28:31],a[76:79],v[196:199],v[28:31]\nv_exp_f32 v160,v160\nM v[88:91],a[76:79],v[200:203],v[88:91]\n" \
"v_exp_f32 v161,v161\nM v[92:95],a[76:79],v[204:207],v[92:95]\nv_exp_f32 v162,v162\nM v[32:35],a[80:83],v[192:195],v[32:35]\nv_exp_f32 v163,v163\nM v[36:39],a[80:83],v[196:199],v[36:39]\nv_exp_f32 v176,v176\nM v[96:99],a[80:83],v[200:203],v[96:99]\n" \
"buffer_load_dwordx4 v240,s[48:51],s95 offen offset:1024 sc1 lds\nM v[100:103],a[80:83],v[204:207],v[100:103]\nv_exp_f32 v177,v177\nM v[40:43],a[84:87],v[192:195],v[40:43]\nv_exp_f32 v178,v178\nM v[44:47],a[84:87],v[196:199],v[44:47]\nv_exp_f32 v179,v179\nM v[104:107],a[84:87],v[200:203],v[104:107]\n" \
"v_exp_f32 v164,v164\nM v[108:111],a[84:87],v[204:207],v[108:111]\nv_exp_f32 v165,v165\nM v[48:51],a[88:91],v[192:195],v[48:51]\nv_exp_f32 v166,v166\nM v[52:55],a[88:91],v[196:199],v[52:55]\nv_exp_f32 v167,v167\nM v[112:115],a[88:91],v[200:203],v[112:115]\n" \
"v_exp_f32 v180,v180\nM v[116:119],a[88:91],v[204:207],v[116:119]\nv_exp_f32 v181,v181\nM v[56:59],a[92:95],v[192:195],v[56:59]\nv_exp_f32 v182,v182\nM v[60:63],a[92:95],v[196:199],v[60:63]\nv_exp_f32 v183,v183\nM v[120:123],a[92:95],v[200:203],v[120:123]\n" \
"v_exp_f32 v168,v168\nM v[124:127],a[92:95],v[204:207],v[124:127]\nv_exp_f32 v169,v169\nM a[192:195],a[208:211],v[192:195],a[192:195]\nv_cvt_pk_bf16_f32 v192,v128,v129\nv_cvt_pk_bf16_f32 v193,v130,v131\nM a[196:199],a[208:211],v[196:199],a[196:199]\nbuffer_load_dwordx4 v240,s[48:51],s95 offen offset:2048 sc1 lds\n" \
"M a[200:203],a[208:211],v[200:203],a[200:203]\nv_cvt_pk_bf16_f32 v194,v144,v145\nv_cvt_pk_bf16_f32 v195,v146,v147\nM a[204:207],a[208:211],v[204:207],a[204:207]\nv_cvt_pk_bf16_f32 v196,v132,v133\nv_cvt_pk_bf16_f32 v197,v134,v135\nM v[0:3],a[96:99],v[208:211],v[0:3]\nv_cvt_pk_bf16_f32 v198,v148,v149\n" \
"v_cvt_pk_bf16_f32 v199,v150,v151\nM v[4:7],a[96:99],v[212:215],v[4:7]\nv_cvt_pk_bf16_f32 v200,v136,v137\nv_cvt_pk_bf16_f32 v201,v138,v139\nM v[64:67],a[96:99],v[216:219],v[64:67]\nv_cvt_pk_bf16_f32 v202,v152,v153\nv_cvt_pk_bf16_f32 v203,v154,v155\nM v[68:71],a[96:99],v[220:223],v[68:71]\n" \
"v_cvt_pk_bf16_f32 v204,v140,v141\nv_cvt_pk_bf16_f32 v205,v142,v143\nM v[8:11],a[100:103],v[208:211],v[8:11]\nv_cvt_pk_bf16_f32 v206,v156,v157\nv_cvt_pk_bf16_f32 v207,v158,v159\nM v[12:15],a[100:103],v[212:215],v[12:15]\nv_exp_f32 v170,v170\nM v[72:75],a[100:103],v[216:219],v[72:75]\n" \
"v_exp_f32 v171,v171\nM v[76:79],a[100:103],v[220:223],v[76:79]\nv_add_u32 v250,s94,v241\nM v[16:19],a[104:107],v[208:211],v[16:19]\nM v[20:23],a[104:107],v[212:215],v[20:23]\nM v[80:83],a[104:107],v[216:219],v[80:83]\nM v[84:87],a[104:107],v[220:223],v[84:87]\nM v[24:27],a[108:111],v[208:211],v[24:27]\n" \
"buffer_load_dwordx4 v240,s[48:51],s95 offen offset:3072 sc1 lds\nM v[28:31],a[108:111],v[212:215],v[28:31]\ns_cmp_eq_u32 s95,s96\ns_cselect_b32 s72,0xc000,s98\ns_add_u32 s95,s95,s72\nM v[88:91],a[108:111],v[216:219],v[88:91]\nM v[92:95],a[108:111],v[220:223],v[92:95]\nM v[32:35],a[112:115],v[208:211],v[32:35]\n" \
"M v[36:39],a[112:115],v[212:215],v[36:39]\nM v[96:99],a[112:115],v[216:219],v[96:99]\nM v[100:103],a[112:115],v[220:223],v[100:103]\nM v[40:43],a[116:119],v[208:211],v[40:43]\nM v[44:47],a[116:119],v[212:215],v[44:47]\nM v[104:107],a[116:119],v[216:219],v[104:107]\nM v[108:111],a[116:119],v[220:223],v[108:111]\nM v[48:51],a[120:123],v[208:211],v[48:51]\n" \
"M v[52:55],a[120:123],v[212:215],v[52:55]\nM v[112:115],a[120:123],v[216:219],v[112:115]\nM v[116:119],a[120:123],v[220:223],v[116:119]\nM v[56:59],a[124:127],v[208:211],v[56:59]\nM v[60:63],a[124:127],v[212:215],v[60:63]\nM v[120:123],a[124:127],v[216:219],v[120:123]\nM v[124:127],a[124:127],v[220:223],v[124:127]\nM a[192:195],a[208:211],v[208:211],a[192:195]\n" \
"M a[196:199],a[208:211],v[212:215],a[196:199]\nM a[200:203],a[208:211],v[216:219],a[200:203]\nM a[204:207],a[208:211],v[220:223],a[204:207]\ns_cmp_lg_u32 s95,s97\ns_cbranch_scc1 .Ldual\ns_cmp_eq_u32 s62,7\ns_cbranch_scc1 .Lod1x\n.Ltrans:\n" \
"s_waitcnt lgkmcnt(8)\nM v[128:131],a[0:3],a[128:131],v[224:227]\nv_cvt_pk_bf16_f32 v208,v160,v161\nv_cvt_pk_bf16_f32 v209,v162,v163\nM v[128:131],a[4:7],a[132:135],v[128:131]\nv_cvt_pk_bf16_f32 v212,v164,v165\nv_cvt_pk_bf16_f32 v213,v166,v167\nM v[128:131],a[8:11],a[136:139],v[128:131]\n" \
"ds_read_b128 a[64:67],v250 offset:0\nv_cvt_pk_bf16_f32 v210,v176,v177\nM v[128:131],a[12:15],a[140:143],v[128:131]\nv_cvt_pk_bf16_f32 v211,v178,v179\nds_read_b128 a[68:71],v250 offset:1024\nM v[132:135],a[0:3],a[144:147],v[228:231]\nds_read_b128 a[72:75],v250 offset:2048\nds_read_b128 a[76:79],v250 offset:3072\n" \
"M v[132:135],a[4:7],a[148:151],v[132:135]\nds_read_b128 a[80:83],v250 offset:4096\nds_read_b128 a[84:87],v250 offset:5120\nM v[132:135],a[8:11],a[152:155],v[132:135]\nds_read_b128 a[88:91],v250 offset:6144\nds_read_b128 a[92:95],v250 offset:7168\nM v[132:135],a[12:15],a[156:159],v[132:135]\nds_read_b128 a[96:99],v250 offset:8192\n" \
"ds_read_b128 a[100:103],v250 offset:9216\nM v[144:147],a[16:19],a[128:131],v[224:227]\nds_read_b128 a[104:107],v250 offset:10240\nds_read_b128 a[108:111],v250 offset:11264\nM v[144:147],a[20:23],a[132:135],v[144:147]\nds_read_b128 a[112:115],v250 offset:12288\nds_read_b128 a[116:119],v250 offset:13312\nM v[144:147],a[24:27],a[136:139],v[144:147]\n" \
"ds_read_b128 a[120:123],v250 offset:14336\nds_read_b128 a[124:127],v250 offset:15360\nM v[144:147],a[28:31],a[140:143],v[144:147]\nv_cvt_pk_bf16_f32 v214,v180,v181\nv_cvt_pk_bf16_f32 v215,v182,v183\nM v[148:151],a[16:19],a[144:147],v[228:231]\ns_add_u32 m0,s94,s64\nv_cvt_pk_bf16_f32 v216,v168,v169\n" \
"M v[148:151],a[20:23],a[148:151],v[148:151]\nbuffer_load_dwordx4 v240,s[44:47],s95 offen offset:0 sc1 lds\nM v[148:151],a[24:27],a[152:155],v[148:151]\nbuffer_load_dwordx4 v240,s[44:47],s95 offen offset:1024 sc1 lds\nM v[148:151],a[28:31],a[156:159],v[148:151]\nv_cvt_pk_bf16_f32 v217,v170,v171\ns_waitcnt lgkmcnt(11)\nM v[160:163],a[32:35],a[128:131],v[224:227]\n" \
"buffer_load_dwordx4 v240,s[44:47],s95 offen offset:2048 sc1 lds\nM v[160:163],a[36:39],a[132:135],v[160:163]\nv_exp_f32 v184,v184\nM v[160:163],a[40:43],a[136:139],v[160:163]\nv_exp_f32 v185,v185\nM v[160:163],a[44:47],a[140:143],v[160:163]\nv_exp_f32 v186,v186\nM v[164:167],a[32:35],a[144:147],v[228:231]\n" \
"v_exp_f32 v187,v187\nM v[164:167],a[36:39],a[148:151],v[164:167]\nbuffer_load_dwordx4 v240,s[44:47],s95 offen offset:3072 sc1 lds\nM v[164:167],a[40:43],a[152:155],v[164:167]\nv_exp_f32 v172,v172\nM v[164:167],a[44:47],a[156:159],v[164:167]\nv_exp_f32 v173,v173\nM v[176:179],a[48:51],a[128:131],v[224:227]\n" \
"v_exp_f32 v174,v174\nM v[176:179],a[52:55],a[132:135],v[176:179]\nv_exp_f32 v175,v175\nM v[176:179],a[56:59],a[136:139],v[176:179]\nv_exp_f32 v188,v188\nM v[176:179],a[60:63],a[140:143],v[176:179]\nv_exp_f32 v189,v189\nM v[180:183],a[48:51],a[144:147],v[228:231]\n" \
"v_exp_f32 v190,v190\nM v[180:183],a[52:55],a[148:151],v[180:183]\nv_exp_f32 v191,v191\nM v[180:183],a[56:59],a[152:155],v[180:183]\nv_cvt_pk_bf16_f32 v218,v184,v185\nv_cvt_pk_bf16_f32 v219,v186,v187\nM v[180:183],a[60:63],a[156:159],v[180:183]\nv_cvt_pk_bf16_f32 v220,v172,v173\n" \
"v_cvt_pk_bf16_f32 v221,v174,v175\nM v[0:3],a[64:67],v[192:195],v[0:3]\nv_cvt_pk_bf16_f32 v222,v188,v189\nv_cvt_pk_bf16_f32 v223,v190,v191\nM v[4:7],a[64:67],v[196:199],v[4:7]\nv_xor_b32 v250,0x18000,v250\ns_add_u32 s94,s94,0x4000\nM v[64:67],a[64:67],v[200:203],v[64:67]\n" \
"s_waitcnt vmcnt(16) lgkmcnt(0)\ns_barrier\nM v[68:71],a[64:67],v[204:207],v[68:71]\nds_read_b128 a[0:3],v250 offset:0\nds_read_b128 a[4:7],v250 offset:1024\nM v[8:11],a[68:71],v[192:195],v[8:11]\nds_read_b128 a[8:11],v250 offset:2048\nds_read_b128 a[12:15],v250 offset:3072\n" \
"M v[12:15],a[68:71],v[196:199],v[12:15]\nds_read_b128 a[16:19],v250 offset:4096\nds_read_b128 a[20:23],v250 offset:5120\nM v[72:75],a[68:71],v[200:203],v[72:75]\nds_read_b128 a[24:27],v250 offset:6144\nds_read_b128 a[28:31],v250 offset:7168\nM v[76:79],a[68:71],v[204:207],v[76:79]\nds_read_b128 a[32:35],v250 offset:8192\n" \
"ds_read_b128 a[36:39],v250 offset:9216\nM v[16:19],a[72:75],v[192:195],v[16:19]\nds_read_b128 a[40:43],v250 offset:10240\nds_read_b128 a[44:47],v250 offset:11264\nM v[20:23],a[72:75],v[196:199],v[20:23]\nds_read_b128 a[48:51],v250 offset:12288\nds_read_b128 a[52:55],v250 offset:13312\nM v[80:83],a[72:75],v[200:203],v[80:83]\n" \
"ds_read_b128 a[56:59],v250 offset:14336\nds_read_b128 a[60:63],v250 offset:15360\nM v[84:87],a[72:75],v[204:207],v[84:87]\ns_add_u32 m0,m0,0x10000\ns_and_b32 s94,s94,0xffff\nM v[24:27],a[76:79],v[192:195],v[24:27]\nv_exp_f32 v128,v128\nM v[28:31],a[76:79],v[196:199],v[28:31]\n" \
"v_exp_f32 v129,v129\nM v[88:91],a[76:79],v[200:203],v[88:91]\nv_exp_f32 v130,v130\nM v[92:95],a[76:79],v[204:207],v[92:95]\nv_exp_f32 v131,v131\nM v[32:35],a[80:83],v[192:195],v[32:35]\nv_exp_f32 v144,v144\nM v[36:39],a[80:83],v[196:199],v[36:39]\n" \
"v_exp_f32 v145,v145\nM v[96:99],a[80:83],v[200:203],v[96:99]\nv_exp_f32 v146,v146\nM v[100:103],a[80:83],v[204:207],v[100:103]\nv_exp_f32 v147,v147\nM v[40:43],a[84:87],v[192:195],v[40:43]\nv_exp_f32 v132,v132\nM v[44:47],a[84:87],v[196:199],v[44:47]\n" \
"v_exp_f32 v133,v133\nM v[104:107],a[84:87],v[200:203],v[104:107]\nv_exp_f32 v134,v134\nM v[108:111],a[84:87],v[204:207],v[108:111]\nv_exp_f32 v135,v135\nM v[48:51],a[88:91],v[192:195],v[48:51]\nv_exp_f32 v148,v148\nM v[52:55],a[88:91],v[196:199],v[52:55]\n" \
"v_exp_f32 v149,v149\nM v[112:115],a[88:91],v[200:203],v[112:115]\nv_exp_f32 v150,v150\nM v[116:119],a[88:91],v[204:207],v[116:119]\nv_exp_f32 v151,v151\nM v[56:59],a[92:95],v[192:195],v[56:59]\nbuffer_load_dwordx4 v240,s[48:51],s95 offen offset:0 sc1 lds\nM v[60:63],a[92:95],v[196:199],v[60:63]\n" \
"buffer_load_dwordx4 v240,s[48:51],s95 offen offset:1024 sc1 lds\nM v[120:123],a[92:95],v[200:203],v[120:123]\nv_exp_f32 v160,v160\nM v[124:127],a[92:95],v[204:207],v[124:127]\nv_exp_f32 v161,v161\nM a[192:195],a[208:211],v[192:195],a[192:195]\nv_cvt_pk_bf16_f32 v192,v128,v129\nv_cvt_pk_bf16_f32 v193,v130,v131\n" \
"M a[196:199],a[208:211],v[196:199],a[196:199]\nbuffer_load_dwordx4 v240,s[48:51],s95 offen offset:2048 sc1 lds\nM a[200:203],a[208:211],v[200:203],a[200:203]\nv_cvt_pk_bf16_f32 v194,v144,v145\nv_cvt_pk_bf16_f32 v195,v146,v147\nM a[204:207],a[208:211],v[204:207],a[204:207]\nv_cvt_pk_bf16_f32 v196,v132,v133\nv_cvt_pk_bf16_f32 v197,v134,v135\n" \
"M v[0:3],a[96:99],v[208:211],v[0:3]\nv_cvt_pk_bf16_f32 v198,v148,v149\nv_cvt_pk_bf16_f32 v199,v150,v151\nM v[4:7],a[96:99],v[212:215],v[4:7]\nv_exp_f32 v162,v162\nM v[64:67],a[96:99],v[216:219],v[64:67]\nv_exp_f32 v163,v163\nM v[68:71],a[96:99],v[220:223],v[68:71]\n" \
"v_exp_f32 v176,v176\nM v[8:11],a[100:103],v[208:211],v[8:11]\nv_exp_f32 v177,v177\nM v[12:15],a[100:103],v[212:215],v[12:15]\nv_exp_f32 v178,v178\nM v[72:75],a[100:103],v[216:219],v[72:75]\nv_exp_f32 v179,v179\nM v[76:79],a[100:103],v[220:223],v[76:79]\n" \
"v_exp_f32 v164,v164\nM v[16:19],a[104:107],v[208:211],v[16:19]\nv_exp_f32 v165,v165\nM v[20:23],a[104:107],v[212:215],v[20:23]\nv_exp_f32 v166,v166\nM v[80:83],a[104:107],v[216:219],v[80:83]\nv_exp_f32 v167,v167\nM v[84:87],a[104:107],v[220:223],v[84:87]\n" \
"buffer_load_dwordx4 v240,s[48:51],s95 offen offset:3072 sc1 lds\nM v[24:27],a[108:111],v[208:211],v[24:27]\nv_add_u32 v250,s94,v241\ns_add_u32 s95,s95,0x4000\nM v[28:31],a[108:111],v[212:215],v[28:31]\nM v[88:91],a[108:111],v[216:219],v[88:91]\nM v[92:95],a[108:111],v[220:223],v[92:95]\nM v[32:35],a[112:115],v[208:211],v[32:35]\n" \
"M v[36:39],a[112:115],v[212:215],v[36:39]\nM v[96:99],a[112:115],v[216:219],v[96:99]\nM v[100:103],a[112:115],v[220:223],v[100:103]\nM v[40:43],a[116:119],v[208:211],v[40:43]\nM v[44:47],a[116:119],v[212:215],v[44:47]\nM v[104:107],a[116:119],v[216:219],v[104:107]\nM v[108:111],a[116:119],v[220:223],v[108:111]\nM v[48:51],a[120:123],v[208:211],v[48:51]\n" \
"M v[52:55],a[120:123],v[212:215],v[52:55]\nM v[112:115],a[120:123],v[216:219],v[112:115]\nM v[116:119],a[120:123],v[220:223],v[116:119]\nM v[56:59],a[124:127],v[208:211],v[56:59]\nM v[60:63],a[124:127],v[212:215],v[60:63]\nM v[120:123],a[124:127],v[216:219],v[120:123]\nM v[124:127],a[124:127],v[220:223],v[124:127]\nM a[192:195],a[208:211],v[208:211],a[192:195]\n" \
"M a[196:199],a[208:211],v[212:215],a[196:199]\nM a[200:203],a[208:211],v[216:219],a[200:203]\nM a[204:207],a[208:211],v[220:223],a[204:207]\ns_waitcnt lgkmcnt(8)\nM v[128:131],a[0:3],a[128:131],v[224:227]\ns_lshl2_add_u32 s77,s61,s79\ns_add_u32 s77,s77,4096\nM v[128:131],a[4:7],a[132:135],v[128:131]\n" \
"s_lshl_b32 s37,s61,13\ns_add_u32 s37,s37,s67\nv_accvgpr_read_b32 v232,a200\nv_accvgpr_read_b32 v236,a204\nv_add_f32 v233,v232,v236\nv_rcp_f32 v232,v232\nM v[128:131],a[8:11],a[136:139],v[128:131]\nv_rcp_f32 v236,v236\n" \
"v_add_f32 v233,v233,v232\nM v[128:131],a[12:15],a[140:143],v[128:131]\nv_add_f32 v233,v233,v236\nv_cmp_ngt_f32 vcc,s28,v233\nM v[132:135],a[0:3],a[144:147],v[228:231]\ns_and_b64 vcc,vcc,s[90:91]\ns_cselect_b32 s39,0x8000000,s16\ns_cselect_b32 s16,s16,0x8000000\n" \
"s_cselect_b32 s77,0x8000000,s77\ns_addc_u32 s99,s99,0\nM v[132:135],a[4:7],a[148:151],v[132:135]\nv_mul_f32 v64,v64,v232\nv_mul_f32 v65,v65,v232\nv_mul_f32 v68,v68,v236\nv_mul_f32 v69,v69,v236\nv_mul_f32 v80,v80,v232\n" \
"v_mul_f32 v81,v81,v232\nv_mul_f32 v84,v84,v236\nv_mul_f32 v85,v85,v236\nv_mul_f32 v96,v96,v232\nv_mul_f32 v97,v97,v232\nv_mul_f32 v100,v100,v236\nv_mul_f32 v101,v101,v236\nv_mul_f32 v112,v112,v232\n" \
"v_mul_f32 v113,v113,v232\nv_mul_f32 v116,v116,v236\nv_mul_f32 v117,v117,v236\nM v[132:135],a[8:11],a[152:155],v[132:135]\ns_add_u32 s37,s37,1048576\nv_mul_f32 v66,v66,v232\nv_mul_f32 v67,v67,v232\nv_cvt_pk_bf16_f32 v64,v64,v65\n" \
"v_mul_f32 v70,v70,v236\nv_mul_f32 v71,v71,v236\nv_cvt_pk_bf16_f32 v68,v68,v69\nv_mul_f32 v82,v82,v232\nv_mul_f32 v83,v83,v232\nv_cvt_pk_bf16_f32 v80,v80,v81\nv_mul_f32 v86,v86,v236\nv_mul_f32 v87,v87,v236\n" \
"v_cvt_pk_bf16_f32 v84,v84,v85\nv_mul_f32 v98,v98,v232\nv_mul_f32 v99,v99,v232\nv_cvt_pk_bf16_f32 v96,v96,v97\nv_mul_f32 v102,v102,v236\nv_mul_f32 v103,v103,v236\nv_cvt_pk_bf16_f32 v100,v100,v101\nv_mul_f32 v114,v114,v232\n" \
"v_mul_f32 v115,v115,v232\nv_cvt_pk_bf16_f32 v112,v112,v113\nv_mul_f32 v118,v118,v236\nv_mul_f32 v119,v119,v236\nv_cvt_pk_bf16_f32 v116,v116,v117\ns_or_b32 s37,s37,s39\nv_mul_f32 v72,v72,v232\nv_mul_f32 v73,v73,v232\n" \
"v_mul_f32 v74,v74,v232\nv_mul_f32 v75,v75,v232\nv_cvt_pk_bf16_f32 v65,v66,v67\nv_mul_f32 v76,v76,v236\nv_mul_f32 v77,v77,v236\nv_mul_f32 v78,v78,v236\nv_mul_f32 v79,v79,v236\nv_cvt_pk_bf16_f32 v69,v70,v71\n" \
"v_mul_f32 v88,v88,v232\nv_mul_f32 v89,v89,v232\nv_mul_f32 v90,v90,v232\nv_mul_f32 v91,v91,v232\nv_cvt_pk_bf16_f32 v81,v82,v83\nv_mul_f32 v92,v92,v236\nv_mul_f32 v93,v93,v236\nv_mul_f32 v94,v94,v236\n" \
"v_mul_f32 v95,v95,v236\nv_cvt_pk_bf16_f32 v85,v86,v87\nv_mul_f32 v104,v104,v232\nv_mul_f32 v105,v105,v232\nv_mul_f32 v106,v106,v232\nv_mul_f32 v107,v107,v232\nv_cvt_pk_bf16_f32 v97,v98,v99\nv_mul_f32 v108,v108,v236\n" \
"v_mul_f32 v109,v109,v236\nv_mul_f32 v110,v110,v236\nv_mul_f32 v111,v111,v236\nv_cvt_pk_bf16_f32 v101,v102,v103\nv_mul_f32 v120,v120,v232\nv_mul_f32 v121,v121,v232\nv_mul_f32 v122,v122,v232\nv_mul_f32 v123,v123,v232\n" \
"v_cvt_pk_bf16_f32 v113,v114,v115\nv_mul_f32 v124,v124,v236\nv_mul_f32 v125,v125,v236\nv_mul_f32 v126,v126,v236\nv_mul_f32 v127,v127,v236\nv_cvt_pk_bf16_f32 v117,v118,v119\nM v[132:135],a[12:15],a[156:159],v[132:135]\ns_add_u32 s38,s37,4096\n" \
"v_cvt_pk_bf16_f32 v66,v72,v73\nv_cvt_pk_bf16_f32 v67,v74,v75\nv_cvt_pk_bf16_f32 v70,v76,v77\nv_cvt_pk_bf16_f32 v71,v78,v79\nv_cvt_pk_bf16_f32 v82,v88,v89\nv_cvt_pk_bf16_f32 v83,v90,v91\nv_cvt_pk_bf16_f32 v86,v92,v93\nv_cvt_pk_bf16_f32 v87,v94,v95\n" \
"v_cvt_pk_bf16_f32 v98,v104,v105\nv_cvt_pk_bf16_f32 v99,v106,v107\nv_cvt_pk_bf16_f32 v102,v108,v109\nv_cvt_pk_bf16_f32 v103,v110,v111\nv_cvt_pk_bf16_f32 v114,v120,v121\nv_cvt_pk_bf16_f32 v115,v122,v123\nv_cvt_pk_bf16_f32 v118,v124,v125\nv_cvt_pk_bf16_f32 v119,v126,v127\n" \
"buffer_store_dwordx4 v[64:67],v240,s[52:55],s37 offen offset:0 sc1\nbuffer_store_dwordx4 v[68:71],v240,s[52:55],s37 offen offset:1024 sc1\nbuffer_store_dwordx4 v[80:83],v240,s[52:55],s37 offen offset:2048 sc1\nbuffer_store_dwordx4 v[84:87],v240,s[52:55],s37 offen offset:3072 sc1\nbuffer_store_dwordx4 v[96:99],v240,s[52:55],s38 offen offset:0 sc1\nbuffer_store_dwordx4 v[100:103],v240,s[52:55],s38 offen offset:1024 sc1\nbuffer_store_dwordx4 v[112:115],v240,s[52:55],s38 offen offset:2048 sc1\nbuffer_store_dwordx4 v[116:119],v240,s[52:55],s38 offen offset:3072 sc1\n" \
"M v[144:147],a[16:19],a[128:131],v[224:227]\nv_cvt_pk_bf16_f32 v208,v160,v161\nv_cvt_pk_bf16_f32 v209,v162,v163\nM v[144:147],a[20:23],a[132:135],v[144:147]\nv_cvt_pk_bf16_f32 v212,v164,v165\nv_cvt_pk_bf16_f32 v213,v166,v167\nM v[144:147],a[24:27],a[136:139],v[144:147]\nds_read_b128 a[64:67],v250 offset:0\n" \
"v_cvt_pk_bf16_f32 v210,v176,v177\nM v[144:147],a[28:31],a[140:143],v[144:147]\nv_cvt_pk_bf16_f32 v211,v178,v179\nds_read_b128 a[68:71],v250 offset:1024\nM v[148:151],a[16:19],a[144:147],v[228:231]\nds_read_b128 a[72:75],v250 offset:2048\nds_read_b128 a[76:79],v250 offset:3072\nM v[148:151],a[20:23],a[148:151],v[148:151]\n" \
"ds_read_b128 a[80:83],v250 offset:4096\nds_read_b128 a[84:87],v250 offset:5120\nM v[148:151],a[24:27],a[152:155],v[148:151]\nds_read_b128 a[88:91],v250 offset:6144\nds_read_b128 a[92:95],v250 offset:7168\nM v[148:151],a[28:31],a[156:159],v[148:151]\nds_read_b128 a[96:99],v250 offset:8192\ns_waitcnt lgkmcnt(9)\n" \
"M v[160:163],a[32:35],a[128:131],v[224:227]\nds_read_b128 a[100:103],v250 offset:9216\nds_read_b128 a[104:107],v250 offset:10240\nM v[160:163],a[36:39],a[132:135],v[160:163]\nds_read_b128 a[108:111],v250 offset:11264\nds_read_b128 a[112:115],v250 offset:12288\nM v[160:163],a[40:43],a[136:139],v[160:163]\nds_read_b128 a[116:119],v250 offset:13312\n" \
"ds_read_b128 a[120:123],v250 offset:14336\nM v[160:163],a[44:47],a[140:143],v[160:163]\nds_read_b128 a[124:127],v250 offset:15360\ns_add_u32 m0,s94,s64\nM v[164:167],a[32:35],a[144:147],v[228:231]\nv_exp_f32 v180,v180\nM v[164:167],a[36:39],a[148:151],v[164:167]\nv_exp_f32 v181,v181\n" \
"M v[164:167],a[40:43],a[152:155],v[164:167]\nv_exp_f32 v182,v182\nM v[164:167],a[44:47],a[156:159],v[164:167]\nv_exp_f32 v183,v183\nM v[176:179],a[48:51],a[128:131],v[224:227]\nv_cvt_pk_bf16_f32 v214,v180,v181\nv_cvt_pk_bf16_f32 v215,v182,v183\nM v[176:179],a[52:55],a[132:135],v[176:179]\n" \
"buffer_load_dwordx4 v240,s[44:47],s95 offen offset:0 sc1 lds\nM v[176:179],a[56:59],a[136:139],v[176:179]\nbuffer_load_dwordx4 v240,s[44:47],s95 offen offset:1024 sc1 lds\nM v[176:179],a[60:63],a[140:143],v[176:179]\nbuffer_load_dwordx4 v240,s[44:47],s95 offen offset:2048 sc1 lds\nM v[180:183],a[48:51],a[144:147],v[228:231]\nbuffer_load_dwordx4 v240,s[44:47],s95 offen offset:3072 sc1 lds\nM v[180:183],a[52:55],a[148:151],v[180:183]\n" \
"v_xor_b32 v250,0x18000,v250\ns_add_u32 s94,s94,0x4000\nM v[180:183],a[56:59],a[152:155],v[180:183]\nv_exp_f32 v128,v128\nM v[180:183],a[60:63],a[156:159],v[180:183]\ns_and_b32 s94,s94,0xffff\ns_waitcnt lgkmcnt(0)\nM v[0:3],a[64:67],v[192:195],v[0:3]\n" \
"v_exp_f32 v129,v129\nM v[4:7],a[64:67],v[196:199],v[4:7]\nv_exp_f32 v130,v130\nM v[8:11],a[68:71],v[192:195],v[8:11]\ns_waitcnt vmcnt(24) lgkmcnt(0)\ns_barrier\nM v[12:15],a[68:71],v[196:199],v[12:15]\nds_read_b128 a[0:3],v250 offset:0\n" \
"ds_read_b128 a[4:7],v250 offset:1024\nM v[16:19],a[72:75],v[192:195],v[16:19]\nds_read_b128 a[8:11],v250 offset:2048\nds_read_b128 a[12:15],v250 offset:3072\nM v[20:23],a[72:75],v[196:199],v[20:23]\nds_read_b128 a[16:19],v250 offset:4096\nds_read_b128 a[20:23],v250 offset:5120\nM v[24:27],a[76:79],v[192:195],v[24:27]\n" \
"ds_read_b128 a[24:27],v250 offset:6144\nds_read_b128 a[28:31],v250 offset:7168\nM v[28:31],a[76:79],v[196:199],v[28:31]\nds_read_b128 a[32:35],v250 offset:8192\nds_read_b128 a[36:39],v250 offset:9216\nM v[32:35],a[80:83],v[192:195],v[32:35]\nds_read_b128 a[40:43],v250 offset:10240\nds_read_b128 a[44:47],v250 offset:11264\n" \
"M v[36:39],a[80:83],v[196:199],v[36:39]\nds_read_b128 a[48:51],v250 offset:12288\nds_read_b128 a[52:55],v250 offset:13312\nM v[40:43],a[84:87],v[192:195],v[40:43]\nds_read_b128 a[56:59],v250 offset:14336\nds_read_b128 a[60:63],v250 offset:15360\nM v[44:47],a[84:87],v[196:199],v[44:47]\ns_add_u32 m0,m0,0x10000\n" \
"v_add_u32 v250,s94,v241\nM v[48:51],a[88:91],v[192:195],v[48:51]\nv_exp_f32 v131,v131\nM v[52:55],a[88:91],v[196:199],v[52:55]\nv_exp_f32 v144,v144\nM v[56:59],a[92:95],v[192:195],v[56:59]\nv_exp_f32 v145,v145\nM v[60:63],a[92:95],v[196:199],v[60:63]\n" \
"v_exp_f32 v146,v146\nM a[192:195],a[208:211],v[192:195],a[192:195]\nv_exp_f32 v147,v147\nM a[196:199],a[208:211],v[196:199],a[196:199]\nv_exp_f32 v132,v132\nM v[0:3],a[96:99],v[208:211],v[0:3]\nv_exp_f32 v133,v133\nM v[4:7],a[96:99],v[212:215],v[4:7]\n" \
"v_exp_f32 v134,v134\nM v[8:11],a[100:103],v[208:211],v[8:11]\nv_exp_f32 v135,v135\nM v[12:15],a[100:103],v[212:215],v[12:15]\nv_exp_f32 v148,v148\nM v[16:19],a[104:107],v[208:211],v[16:19]\nv_exp_f32 v149,v149\nM v[20:23],a[104:107],v[212:215],v[20:23]\n" \
"v_exp_f32 v150,v150\nM v[24:27],a[108:111],v[208:211],v[24:27]\nv_exp_f32 v151,v151\nM v[28:31],a[108:111],v[212:215],v[28:31]\nbuffer_load_dwordx4 v240,s[48:51],s95 offen offset:0 sc1 lds\nM v[32:35],a[112:115],v[208:211],v[32:35]\nbuffer_load_dwordx4 v240,s[48:51],s95 offen offset:1024 sc1 lds\nM v[36:39],a[112:115],v[212:215],v[36:39]\n" \
"buffer_load_dwordx4 v240,s[48:51],s95 offen offset:2048 sc1 lds\nM v[40:43],a[116:119],v[208:211],v[40:43]\nbuffer_load_dwordx4 v240,s[48:51],s95 offen offset:3072 sc1 lds\nM v[44:47],a[116:119],v[212:215],v[44:47]\nv_cvt_pk_bf16_f32 v192,v128,v129\nv_cvt_pk_bf16_f32 v193,v130,v131\nM v[48:51],a[120:123],v[208:211],v[48:51]\nv_cvt_pk_bf16_f32 v194,v144,v145\n" \
"v_cvt_pk_bf16_f32 v195,v146,v147\nM v[52:55],a[120:123],v[212:215],v[52:55]\nv_cvt_pk_bf16_f32 v196,v132,v133\nv_cvt_pk_bf16_f32 v197,v134,v135\nM v[56:59],a[124:127],v[208:211],v[56:59]\nv_cvt_pk_bf16_f32 v198,v148,v149\nv_cvt_pk_bf16_f32 v199,v150,v151\nM v[60:63],a[124:127],v[212:215],v[60:63]\n" \
"v_exp_f32 v160,v160\nM a[192:195],a[208:211],v[208:211],a[192:195]\nv_exp_f32 v161,v161\nM a[196:199],a[208:211],v[212:215],a[196:199]\nv_exp_f32 v162,v162\nv_exp_f32 v163,v163\nv_exp_f32 v176,v176\nv_exp_f32 v177,v177\n" \
"v_exp_f32 v178,v178\nv_exp_f32 v179,v179\nv_exp_f32 v164,v164\nv_exp_f32 v165,v165\nv_exp_f32 v166,v166\nv_exp_f32 v167,v167\ns_add_u32 s95,s95,0x4000\ns_waitcnt vmcnt(8)\n" \
"buffer_store_dword v254,off,s[52:55],s77 sc1\n.Lsingle:\ns_waitcnt lgkmcnt(8)\nM v[128:131],a[0:3],a[128:131],v[224:227]\nv_cvt_pk_bf16_f32 v208,v160,v161\nv_cvt_pk_bf16_f32 v209,v162,v163\nM v[128:131],a[4:7],a[132:135],v[128:131]\nv_cvt_pk_bf16_f32 v212,v164,v165\n" \
"v_cvt_pk_bf16_f32 v213,v166,v167\nM v[128:131],a[8:11],a[136:139],v[128:131]\nds_read_b128 a[64:67],v250 offset:0\nv_cvt_pk_bf16_f32 v210,v176,v177\nM v[128:131],a[12:15],a[140:143],v[128:131]\nv_cvt_pk_bf16_f32 v211,v178,v179\nds_read_b128 a[68:71],v250 offset:1024\nM v[132:135],a[0:3],a[144:147],v[228:231]\n" \
"ds_read_b128 a[72:75],v250 offset:2048\nds_read_b128 a[76:79],v250 offset:3072\nM v[132:135],a[4:7],a[148:151],v[132:135]\nds_read_b128 a[80:83],v250 offset:4096\nds_read_b128 a[84:87],v250 offset:5120\nM v[132:135],a[8:11],a[152:155],v[132:135]\nds_read_b128 a[88:91],v250 offset:6144\nds_read_b128 a[92:95],v250 offset:7168\n" \
"M v[132:135],a[12:15],a[156:159],v[132:135]\nds_read_b128 a[96:99],v250 offset:8192\nds_read_b128 a[100:103],v250 offset:9216\nM v[144:147],a[16:19],a[128:131],v[224:227]\nds_read_b128 a[104:107],v250 offset:10240\nds_read_b128 a[108:111],v250 offset:11264\nM v[144:147],a[20:23],a[132:135],v[144:147]\nds_read_b128 a[112:115],v250 offset:12288\n" \
"ds_read_b128 a[116:119],v250 offset:13312\nM v[144:147],a[24:27],a[136:139],v[144:147]\nds_read_b128 a[120:123],v250 offset:14336\nds_read_b128 a[124:127],v250 offset:15360\nM v[144:147],a[28:31],a[140:143],v[144:147]\nv_exp_f32 v180,v180\nM v[148:151],a[16:19],a[144:147],v[228:231]\nv_exp_f32 v181,v181\n" \
"M v[148:151],a[20:23],a[148:151],v[148:151]\nv_exp_f32 v182,v182\nM v[148:151],a[24:27],a[152:155],v[148:151]\nv_exp_f32 v183,v183\nM v[148:151],a[28:31],a[156:159],v[148:151]\nv_cvt_pk_bf16_f32 v214,v180,v181\ns_waitcnt lgkmcnt(11)\nM v[160:163],a[32:35],a[128:131],v[224:227]\n" \
"v_cvt_pk_bf16_f32 v215,v182,v183\ns_add_u32 m0,s94,s64\nM v[160:163],a[36:39],a[132:135],v[160:163]\nbuffer_load_dwordx4 v240,s[44:47],s95 offen offset:0 sc1 lds\nM v[160:163],a[40:43],a[136:139],v[160:163]\nbuffer_load_dwordx4 v240,s[44:47],s95 offen offset:1024 sc1 lds\nM v[160:163],a[44:47],a[140:143],v[160:163]\nbuffer_load_dwordx4 v240,s[44:47],s95 offen offset:2048 sc1 lds\n" \
"M v[164:167],a[32:35],a[144:147],v[228:231]\nv_xor_b32 v250,0x18000,v250\ns_add_u32 s94,s94,0x4000\nM v[164:167],a[36:39],a[148:151],v[164:167]\nbuffer_load_dwordx4 v240,s[44:47],s95 offen offset:3072 sc1 lds\nM v[164:167],a[40:43],a[152:155],v[164:167]\nv_exp_f32 v128,v128\nM v[164:167],a[44:47],a[156:159],v[164:167]\n" \
"v_exp_f32 v129,v129\nM v[176:179],a[48:51],a[128:131],v[224:227]\nv_exp_f32 v130,v130\nM v[176:179],a[52:55],a[132:135],v[176:179]\nv_exp_f32 v131,v131\nM v[176:179],a[56:59],a[136:139],v[176:179]\nv_exp_f32 v144,v144\nM v[176:179],a[60:63],a[140:143],v[176:179]\n" \
"v_exp_f32 v145,v145\nM v[180:183],a[48:51],a[144:147],v[228:231]\nv_exp_f32 v146,v146\nM v[180:183],a[52:55],a[148:151],v[180:183]\nv_exp_f32 v147,v147\nM v[180:183],a[56:59],a[152:155],v[180:183]\nv_exp_f32 v132,v132\nM v[180:183],a[60:63],a[156:159],v[180:183]\n" \
"v_exp_f32 v133,v133\nM v[0:3],a[64:67],v[192:195],v[0:3]\nv_exp_f32 v134,v134\nM v[4:7],a[64:67],v[196:199],v[4:7]\nv_exp_f32 v135,v135\nM v[8:11],a[68:71],v[192:195],v[8:11]\ns_waitcnt vmcnt(16) lgkmcnt(0)\ns_barrier\n" \
"M v[12:15],a[68:71],v[196:199],v[12:15]\nds_read_b128 a[0:3],v250 offset:0\nds_read_b128 a[4:7],v250 offset:1024\nM v[16:19],a[72:75],v[192:195],v[16:19]\nds_read_b128 a[8:11],v250 offset:2048\nds_read_b128 a[12:15],v250 offset:3072\nM v[20:23],a[72:75],v[196:199],v[20:23]\nds_read_b128 a[16:19],v250 offset:4096\n" \
"ds_read_b128 a[20:23],v250 offset:5120\nM v[24:27],a[76:79],v[192:195],v[24:27]\nds_read_b128 a[24:27],v250 offset:6144\nds_read_b128 a[28:31],v250 offset:7168\nM v[28:31],a[76:79],v[196:199],v[28:31]\nds_read_b128 a[32:35],v250 offset:8192\nds_read_b128 a[36:39],v250 offset:9216\nM v[32:35],a[80:83],v[192:195],v[32:35]\n" \
"ds_read_b128 a[40:43],v250 offset:10240\nds_read_b128 a[44:47],v250 offset:11264\nM v[36:39],a[80:83],v[196:199],v[36:39]\nds_read_b128 a[48:51],v250 offset:12288\nds_read_b128 a[52:55],v250 offset:13312\nM v[40:43],a[84:87],v[192:195],v[40:43]\nds_read_b128 a[56:59],v250 offset:14336\nds_read_b128 a[60:63],v250 offset:15360\n" \
"M v[44:47],a[84:87],v[196:199],v[44:47]\ns_add_u32 m0,m0,0x10000\ns_and_b32 s94,s94,0xffff\nM v[48:51],a[88:91],v[192:195],v[48:51]\nv_exp_f32 v148,v148\nM v[52:55],a[88:91],v[196:199],v[52:55]\nv_exp_f32 v149,v149\nM v[56:59],a[92:95],v[192:195],v[56:59]\n" \
"v_exp_f32 v150,v150\nM v[60:63],a[92:95],v[196:199],v[60:63]\nv_exp_f32 v151,v151\nM a[192:195],a[208:211],v[192:195],a[192:195]\nbuffer_load_dwordx4 v240,s[48:51],s95 offen offset:0 sc1 lds\nM a[196:199],a[208:211],v[196:199],a[196:199]\nbuffer_load_dwordx4 v240,s[48:51],s95 offen offset:1024 sc1 lds\nM v[0:3],a[96:99],v[208:211],v[0:3]\n" \
"buffer_load_dwordx4 v240,s[48:51],s95 offen offset:2048 sc1 lds\nM v[4:7],a[96:99],v[212:215],v[4:7]\nv_cvt_pk_bf16_f32 v192,v128,v129\nv_cvt_pk_bf16_f32 v193,v130,v131\nM v[8:11],a[100:103],v[208:211],v[8:11]\nbuffer_load_dwordx4 v240,s[48:51],s95 offen offset:3072 sc1 lds\nM v[12:15],a[100:103],v[212:215],v[12:15]\nv_cvt_pk_bf16_f32 v194,v144,v145\n" \
"v_cvt_pk_bf16_f32 v195,v146,v147\nM v[16:19],a[104:107],v[208:211],v[16:19]\nv_cvt_pk_bf16_f32 v196,v132,v133\nv_cvt_pk_bf16_f32 v197,v134,v135\nM v[20:23],a[104:107],v[212:215],v[20:23]\nv_cvt_pk_bf16_f32 v198,v148,v149\nv_cvt_pk_bf16_f32 v199,v150,v151\nM v[24:27],a[108:111],v[208:211],v[24:27]\n" \
"v_exp_f32 v160,v160\nM v[28:31],a[108:111],v[212:215],v[28:31]\nv_exp_f32 v161,v161\nM v[32:35],a[112:115],v[208:211],v[32:35]\nv_exp_f32 v162,v162\nM v[36:39],a[112:115],v[212:215],v[36:39]\nv_exp_f32 v163,v163\nM v[40:43],a[116:119],v[208:211],v[40:43]\n" \
"v_exp_f32 v176,v176\nM v[44:47],a[116:119],v[212:215],v[44:47]\nv_exp_f32 v177,v177\nM v[48:51],a[120:123],v[208:211],v[48:51]\nv_exp_f32 v178,v178\nM v[52:55],a[120:123],v[212:215],v[52:55]\nv_exp_f32 v179,v179\nM v[56:59],a[124:127],v[208:211],v[56:59]\n" \
"v_exp_f32 v164,v164\nM v[60:63],a[124:127],v[212:215],v[60:63]\nv_exp_f32 v165,v165\nM a[192:195],a[208:211],v[208:211],a[192:195]\nv_exp_f32 v166,v166\nv_exp_f32 v167,v167\nv_add_u32 v250,s94,v241\ns_add_u32 s95,s95,0x4000\n" \
"M a[196:199],a[208:211],v[212:215],a[196:199]\ns_cmp_lg_u32 s95,s15\ns_cbranch_scc1 .Lsingle\nv_add_u32 v226,131072,v242\nds_read_b64_tr_b16 a[212:213],v226 offset:0\nds_read_b64_tr_b16 a[214:215],v226 offset:4096\nv_xor_b32 v227,32,v226\nds_read_b64_tr_b16 a[216:217],v227 offset:0\n" \
"ds_read_b64_tr_b16 a[218:219],v227 offset:4096\nds_read_b64_tr_b16 a[220:221],v226 offset:512\nds_read_b64_tr_b16 a[222:223],v226 offset:4608\nds_read_b64_tr_b16 a[224:225],v227 offset:512\nds_read_b64_tr_b16 a[226:227],v227 offset:4608\nds_read_b128 a[64:67],v250 offset:0\nds_read_b128 a[68:71],v250 offset:1024\ns_waitcnt lgkmcnt(8)\n" \
"M v[128:131],v[64:67],a[212:215],0\nds_read_b128 a[72:75],v250 offset:2048\nds_read_b128 a[76:79],v250 offset:3072\nM v[132:135],v[68:71],a[212:215],0\nds_read_b128 a[80:83],v250 offset:4096\ns_waitcnt lgkmcnt(9)\nM v[136:139],v[64:67],a[216:219],0\nds_read_b128 a[84:87],v250 offset:5120\n" \
"ds_read_b128 a[88:91],v250 offset:6144\nM v[140:143],v[68:71],a[216:219],0\nds_read_b128 a[92:95],v250 offset:7168\ns_waitcnt lgkmcnt(10)\nM v[144:147],v[64:67],a[220:223],0\nds_read_b128 a[96:99],v250 offset:8192\nds_read_b128 a[100:103],v250 offset:9216\nM v[148:151],v[68:71],a[220:223],0\n" \
"ds_read_b128 a[104:107],v250 offset:10240\ns_waitcnt lgkmcnt(11)\nM v[152:155],v[64:67],a[224:227],0\nds_read_b128 a[108:111],v250 offset:11264\nds_read_b128 a[112:115],v250 offset:12288\nM v[156:159],v[68:71],a[224:227],0\nv_cvt_pk_bf16_f32 v208,v160,v161\ns_waitcnt lgkmcnt(12)\n" \
"M v[0:3],a[64:67],v[192:195],v[0:3]\nv_exp_f32 v180,v180\nM v[4:7],a[64:67],v[196:199],v[4:7]\nv_cvt_pk_bf16_f32 v209,v162,v163\ns_waitcnt lgkmcnt(11)\nM v[8:11],a[68:71],v[192:195],v[8:11]\nv_exp_f32 v181,v181\nM v[12:15],a[68:71],v[196:199],v[12:15]\n" \
"v_cvt_pk_bf16_f32 v210,v176,v177\ns_waitcnt lgkmcnt(8)\nM v[16:19],a[72:75],v[192:195],v[16:19]\nv_exp_f32 v182,v182\nM v[20:23],a[72:75],v[196:199],v[20:23]\nv_exp_f32 v183,v183\nM v[24:27],a[76:79],v[192:195],v[24:27]\nv_cvt_pk_bf16_f32 v211,v178,v179\n" \
"v_cvt_pk_bf16_f32 v212,v164,v165\nM v[28:31],a[76:79],v[196:199],v[28:31]\nv_cvt_pk_bf16_f32 v213,v166,v167\nv_cvt_pk_bf16_f32 v214,v180,v181\nM v[32:35],a[80:83],v[192:195],v[32:35]\nv_cvt_pk_bf16_f32 v215,v182,v183\nds_read_b128 a[116:119],v250 offset:13312\nM v[36:39],a[80:83],v[196:199],v[36:39]\n" \
"ds_read_b128 a[120:123],v250 offset:14336\ns_waitcnt lgkmcnt(2)\nM v[40:43],a[84:87],v[192:195],v[40:43]\nds_read_b128 a[124:127],v250 offset:15360\nds_read_b64_tr_b16 a[228:229],v226 offset:1024\nM v[44:47],a[84:87],v[196:199],v[44:47]\nds_read_b64_tr_b16 a[230:231],v226 offset:5120\nds_read_b64_tr_b16 a[232:233],v227 offset:1024\n" \
"M v[48:51],a[88:91],v[192:195],v[48:51]\nds_read_b64_tr_b16 a[234:235],v227 offset:5120\nds_read_b64_tr_b16 a[236:237],v226 offset:1536\nM v[52:55],a[88:91],v[196:199],v[52:55]\nds_read_b64_tr_b16 a[238:239],v226 offset:5632\nds_read_b64_tr_b16 a[240:241],v227 offset:1536\nM v[56:59],a[92:95],v[192:195],v[56:59]\nds_read_b64_tr_b16 a[242:243],v227 offset:5632\n" \
"ds_read_b64_tr_b16 a[212:213],v226 offset:8192\nM v[60:63],a[92:95],v[196:199],v[60:63]\nds_read_b64_tr_b16 a[214:215],v226 offset:12288\nds_read_b64_tr_b16 a[216:217],v227 offset:8192\nM a[192:195],a[208:211],v[192:195],a[192:195]\nds_read_b64_tr_b16 a[218:219],v227 offset:12288\nds_read_b64_tr_b16 a[220:221],v226 offset:8704\nM a[196:199],a[208:211],v[196:199],a[196:199]\n" \
"ds_read_b64_tr_b16 a[222:223],v226 offset:12800\nds_read_b64_tr_b16 a[224:225],v227 offset:8704\nM v[0:3],a[96:99],v[208:211],v[0:3]\nds_read_b64_tr_b16 a[226:227],v227 offset:12800\ns_lshl2_add_u32 s76,s61,s79\nM v[4:7],a[96:99],v[212:215],v[4:7]\nv_xor_b32 v250,0x8000,v250\nv_add_u32 v229,s64,v250\n" \
"M v[8:11],a[100:103],v[208:211],v[8:11]\nv_and_b32 v251,3,v249\ns_lshl4_add_u32 s69,s10,s68\nM v[12:15],a[100:103],v[212:215],v[12:15]\ns_add_u32 m0,s64,0\ns_lshl_b32 s37,s61,13\nM v[16:19],a[104:107],v[208:211],v[16:19]\nbuffer_load_dwordx4 v243,s[40:43],s69 offen offset:0 lds\n" \
"M v[20:23],a[104:107],v[212:215],v[20:23]\nbuffer_load_dwordx4 v244,s[40:43],s69 offen offset:1024 lds\nM v[24:27],a[108:111],v[208:211],v[24:27]\nbuffer_load_dwordx4 v245,s[40:43],s69 offen offset:2048 lds\nM v[28:31],a[108:111],v[212:215],v[28:31]\nbuffer_load_dwordx4 v246,s[40:43],s69 offen offset:3072 lds\nM v[32:35],a[112:115],v[208:211],v[32:35]\ns_add_u32 s37,s37,s67\n" \
"s_lshl4_add_u32 s69,s10,s68\nM v[36:39],a[112:115],v[212:215],v[36:39]\ns_add_u32 s69,s69,0x10000\ns_waitcnt lgkmcnt(2)\nM v[40:43],a[116:119],v[208:211],v[40:43]\ns_add_u32 m0,s64,16384\nM v[44:47],a[116:119],v[212:215],v[44:47]\nbuffer_load_dwordx4 v243,s[40:43],s69 offen offset:0 lds\n" \
"M v[48:51],a[120:123],v[208:211],v[48:51]\nbuffer_load_dwordx4 v244,s[40:43],s69 offen offset:1024 lds\nM v[52:55],a[120:123],v[212:215],v[52:55]\nbuffer_load_dwordx4 v245,s[40:43],s69 offen offset:2048 lds\nM v[56:59],a[124:127],v[208:211],v[56:59]\nbuffer_load_dwordx4 v246,s[40:43],s69 offen offset:3072 lds\nM v[60:63],a[124:127],v[212:215],v[60:63]\nM a[192:195],a[208:211],v[208:211],a[192:195]\n" \
"M a[196:199],a[208:211],v[212:215],a[196:199]\nM v[160:163],v[64:67],a[228:231],0\nM v[164:167],v[68:71],a[228:231],0\nds_read_b64_tr_b16 a[228:229],v226 offset:9216\nds_read_b64_tr_b16 a[230:231],v226 offset:13312\nM v[168:171],v[64:67],a[232:235],0\nM v[172:175],v[68:71],a[232:235],0\nds_read_b64_tr_b16 a[232:233],v227 offset:9216\n" \
"ds_read_b64_tr_b16 a[234:235],v227 offset:13312\nM v[176:179],v[64:67],a[236:239],0\nv_accvgpr_read_b32 v224,a192\nv_accvgpr_read_b32 v228,a196\nM v[180:183],v[68:71],a[236:239],0\nds_read_b64_tr_b16 a[236:237],v226 offset:9728\nds_read_b64_tr_b16 a[238:239],v226 offset:13824\nM v[184:187],v[64:67],a[240:243],0\n" \
"v_add_f32 v225,v224,v228\nM v[188:191],v[68:71],a[240:243],0\nds_read_b64_tr_b16 a[240:241],v227 offset:9728\nds_read_b64_tr_b16 a[242:243],v227 offset:13824\nM v[128:131],v[80:83],a[212:215],v[128:131]\nv_rcp_f32 v224,v224\nM v[132:135],v[84:87],a[212:215],v[132:135]\nds_read_b64_tr_b16 a[212:213],v226 offset:16384\n" \
"ds_read_b64_tr_b16 a[214:215],v226 offset:20480\nM v[136:139],v[80:83],a[216:219],v[136:139]\nv_rcp_f32 v228,v228\nM v[140:143],v[84:87],a[216:219],v[140:143]\nds_read_b64_tr_b16 a[216:217],v227 offset:16384\nds_read_b64_tr_b16 a[218:219],v227 offset:20480\nM v[144:147],v[80:83],a[220:223],v[144:147]\nv_add_f32 v225,v225,v224\n" \
"v_add_f32 v225,v225,v228\nM v[148:151],v[84:87],a[220:223],v[148:151]\nds_read_b64_tr_b16 a[220:221],v226 offset:16896\ns_waitcnt lgkmcnt(11)\nM v[152:155],v[80:83],a[224:227],v[152:155]\nds_read_b64_tr_b16 a[222:223],v226 offset:20992\nv_cmp_ngt_f32 vcc,s28,v225\nM v[156:159],v[84:87],a[224:227],v[156:159]\n" \
"ds_read_b64_tr_b16 a[224:225],v227 offset:16896\nds_read_b64_tr_b16 a[226:227],v227 offset:20992\nM v[160:163],v[80:83],a[228:231],v[160:163]\ns_and_b64 vcc,vcc,s[90:91]\ns_cselect_b32 s39,0x8000000,s17\ns_cselect_b32 s17,s17,0x8000000\ns_cselect_b32 s76,0x8000000,s76\ns_addc_u32 s99,s99,0\n" \
"M v[164:167],v[84:87],a[228:231],v[164:167]\nds_read_b64_tr_b16 a[228:229],v226 offset:17408\ns_waitcnt lgkmcnt(9)\nM v[168:171],v[80:83],a[232:235],v[168:171]\nds_read_b64_tr_b16 a[230:231],v226 offset:21504\nv_mov_b32 v230,s99\nM v[172:175],v[84:87],a[232:235],v[172:175]\nds_read_b64_tr_b16 a[232:233],v227 offset:17408\n" \
"ds_read_b64_tr_b16 a[234:235],v227 offset:21504\nM v[176:179],v[80:83],a[236:239],v[176:179]\ns_waitcnt vmcnt(8)\nds_write_b32 v229,v230\nM v[180:183],v[84:87],a[236:239],v[180:183]\nds_read_b64_tr_b16 a[236:237],v226 offset:17920\nds_read_b64_tr_b16 a[238:239],v226 offset:22016\nM v[184:187],v[80:83],a[240:243],v[184:187]\n" \
"v_lshl_add_u32 v229,v251,12,v250\nv_mul_f32 v0,v0,v224\nM v[188:191],v[84:87],a[240:243],v[188:191]\nds_read_b64_tr_b16 a[240:241],v227 offset:17920\ns_waitcnt lgkmcnt(11)\nM v[128:131],v[96:99],a[212:215],v[128:131]\nds_read_b64_tr_b16 a[242:243],v227 offset:22016\nv_mul_f32 v1,v1,v224\n" \
"M v[132:135],v[100:103],a[212:215],v[132:135]\nds_read_b64_tr_b16 a[212:213],v226 offset:24576\nds_read_b64_tr_b16 a[214:215],v226 offset:28672\nM v[136:139],v[96:99],a[216:219],v[136:139]\nv_mul_f32 v4,v4,v228\nv_mul_f32 v5,v5,v228\nM v[140:143],v[100:103],a[216:219],v[140:143]\nds_read_b64_tr_b16 a[216:217],v227 offset:24576\n" \
"s_waitcnt lgkmcnt(11)\nM v[144:147],v[96:99],a[220:223],v[144:147]\nds_read_b64_tr_b16 a[218:219],v227 offset:28672\nv_mul_f32 v16,v16,v224\nM v[148:151],v[100:103],a[220:223],v[148:151]\nds_read_b64_tr_b16 a[220:221],v226 offset:25088\nds_read_b64_tr_b16 a[222:223],v226 offset:29184\nM v[152:155],v[96:99],a[224:227],v[152:155]\n" \
"s_waitcnt lgkmcnt(10)\ns_barrier\nM v[156:159],v[100:103],a[224:227],v[156:159]\nds_read_b64_tr_b16 a[224:225],v227 offset:25088\nds_read_b64_tr_b16 a[226:227],v227 offset:29184\nM v[160:163],v[96:99],a[228:231],v[160:163]\nds_read_b32 v230,v229\nv_mul_f32 v17,v17,v224\n" \
"M v[164:167],v[100:103],a[228:231],v[164:167]\nds_read_b64_tr_b16 a[228:229],v226 offset:25600\nds_read_b64_tr_b16 a[230:231],v226 offset:29696\nM v[168:171],v[96:99],a[232:235],v[168:171]\nv_mul_f32 v20,v20,v228\nv_mul_f32 v21,v21,v228\nM v[172:175],v[100:103],a[232:235],v[172:175]\nds_read_b64_tr_b16 a[232:233],v227 offset:25600\n" \
"s_waitcnt lgkmcnt(11)\nM v[176:179],v[96:99],a[236:239],v[176:179]\nds_read_b64_tr_b16 a[234:235],v227 offset:29696\nv_mul_f32 v32,v32,v224\nM v[180:183],v[100:103],a[236:239],v[180:183]\nds_read_b64_tr_b16 a[236:237],v226 offset:26112\nds_read_b64_tr_b16 a[238:239],v226 offset:30208\nM v[184:187],v[96:99],a[240:243],v[184:187]\n" \
"v_mul_f32 v33,v33,v224\nv_mul_f32 v36,v36,v228\nM v[188:191],v[100:103],a[240:243],v[188:191]\nds_read_b64_tr_b16 a[240:241],v227 offset:26112\ns_waitcnt lgkmcnt(11)\nM v[128:131],v[112:115],a[212:215],v[128:131]\nds_read_b64_tr_b16 a[242:243],v227 offset:30208\nv_mul_f32 v37,v37,v228\n" \
"M v[132:135],v[116:119],a[212:215],v[132:135]\nds_read_b64_tr_b16 a[212:213],v226 offset:0\nds_read_b64_tr_b16 a[214:215],v226 offset:4096\nM v[136:139],v[112:115],a[216:219],v[136:139]\nv_mul_f32 v48,v48,v224\nv_mul_f32 v49,v49,v224\nM v[140:143],v[116:119],a[216:219],v[140:143]\nds_read_b64_tr_b16 a[216:217],v227 offset:0\n" \
"s_waitcnt lgkmcnt(10)\nM v[144:147],v[112:115],a[220:223],v[144:147]\nds_read_b64_tr_b16 a[218:219],v227 offset:4096\nv_mul_f32 v52,v52,v228\nM v[148:151],v[116:119],a[220:223],v[148:151]\nds_read_b64_tr_b16 a[220:221],v226 offset:512\nv_mul_f32 v53,v53,v228\nM v[152:155],v[112:115],a[224:227],v[152:155]\n" \
"ds_read_b64_tr_b16 a[222:223],v226 offset:4608\nv_mul_f32 v2,v2,v224\nM v[156:159],v[116:119],a[224:227],v[156:159]\nv_mul_f32 v3,v3,v224\ns_waitcnt lgkmcnt(9)\nM v[160:163],v[112:115],a[228:231],v[160:163]\nv_cvt_pk_bf16_f32 v0,v0,v1\nv_mul_f32 v6,v6,v228\n" \
"M v[164:167],v[116:119],a[228:231],v[164:167]\nv_mul_f32 v7,v7,v228\nv_cvt_pk_bf16_f32 v4,v4,v5\nM v[168:171],v[112:115],a[232:235],v[168:171]\nv_mul_f32 v18,v18,v224\nv_mul_f32 v19,v19,v224\nM v[172:175],v[116:119],a[232:235],v[172:175]\nv_cvt_pk_bf16_f32 v16,v16,v17\n" \
"s_waitcnt lgkmcnt(5)\nM v[176:179],v[112:115],a[236:239],v[176:179]\nv_mul_f32 v22,v22,v228\nv_mul_f32 v23,v23,v228\nv_cvt_pk_bf16_f32 v20,v20,v21\nv_mul_f32 v34,v34,v224\nv_mul_f32 v35,v35,v224\nv_cvt_pk_bf16_f32 v32,v32,v33\n" \
"v_mul_f32 v38,v38,v228\nv_mul_f32 v39,v39,v228\nv_cvt_pk_bf16_f32 v36,v36,v37\nv_mul_f32 v50,v50,v224\nv_mul_f32 v51,v51,v224\nv_cvt_pk_bf16_f32 v48,v48,v49\nv_mul_f32 v54,v54,v228\nv_mul_f32 v55,v55,v228\n" \
"v_cvt_pk_bf16_f32 v52,v52,v53\nds_read_b64_tr_b16 a[224:225],v227 offset:512\ns_or_b32 s37,s37,s39\nv_mul_f32 v8,v8,v224\nv_mul_f32 v9,v9,v224\nv_mul_f32 v10,v10,v224\nv_mul_f32 v11,v11,v224\nv_cvt_pk_bf16_f32 v1,v2,v3\n" \
"v_mul_f32 v12,v12,v228\nv_mul_f32 v13,v13,v228\nv_mul_f32 v14,v14,v228\nv_mul_f32 v15,v15,v228\nv_cvt_pk_bf16_f32 v5,v6,v7\nv_mul_f32 v24,v24,v224\nv_mul_f32 v25,v25,v224\nv_mul_f32 v26,v26,v224\n" \
"v_mul_f32 v27,v27,v224\nv_cvt_pk_bf16_f32 v17,v18,v19\nv_mul_f32 v28,v28,v228\nv_mul_f32 v29,v29,v228\nv_mul_f32 v30,v30,v228\nv_mul_f32 v31,v31,v228\nv_cvt_pk_bf16_f32 v21,v22,v23\nv_mul_f32 v40,v40,v224\n" \
"v_mul_f32 v41,v41,v224\nv_mul_f32 v42,v42,v224\nv_mul_f32 v43,v43,v224\nv_cvt_pk_bf16_f32 v33,v34,v35\nv_mul_f32 v44,v44,v228\nv_mul_f32 v45,v45,v228\nv_mul_f32 v46,v46,v228\nv_mul_f32 v47,v47,v228\n" \
"v_cvt_pk_bf16_f32 v37,v38,v39\nv_mul_f32 v56,v56,v224\nv_mul_f32 v57,v57,v224\nv_mul_f32 v58,v58,v224\nv_mul_f32 v59,v59,v224\nv_cvt_pk_bf16_f32 v49,v50,v51\nv_mul_f32 v60,v60,v228\nv_mul_f32 v61,v61,v228\n" \
"v_mul_f32 v62,v62,v228\nv_mul_f32 v63,v63,v228\nv_cvt_pk_bf16_f32 v53,v54,v55\nds_read_b64_tr_b16 a[226:227],v227 offset:4608\nM v[180:183],v[116:119],a[236:239],v[180:183]\ns_add_u32 s38,s37,4096\nv_cvt_pk_bf16_f32 v2,v8,v9\nv_cvt_pk_bf16_f32 v3,v10,v11\n" \
"v_cvt_pk_bf16_f32 v6,v12,v13\nv_cvt_pk_bf16_f32 v7,v14,v15\nv_cvt_pk_bf16_f32 v18,v24,v25\nv_cvt_pk_bf16_f32 v19,v26,v27\nv_cvt_pk_bf16_f32 v22,v28,v29\nv_cvt_pk_bf16_f32 v23,v30,v31\nv_cvt_pk_bf16_f32 v34,v40,v41\nv_cvt_pk_bf16_f32 v35,v42,v43\n" \
"v_cvt_pk_bf16_f32 v38,v44,v45\nv_cvt_pk_bf16_f32 v39,v46,v47\nv_cvt_pk_bf16_f32 v50,v56,v57\nv_cvt_pk_bf16_f32 v51,v58,v59\nv_cvt_pk_bf16_f32 v54,v60,v61\nv_cvt_pk_bf16_f32 v55,v62,v63\nds_read_b64_tr_b16 a[228:229],v226 offset:1024\nbuffer_store_dwordx4 v[0:3],v240,s[52:55],s37 offen offset:0 sc1\n" \
"buffer_store_dwordx4 v[4:7],v240,s[52:55],s37 offen offset:1024 sc1\nbuffer_store_dwordx4 v[16:19],v240,s[52:55],s37 offen offset:2048 sc1\nbuffer_store_dwordx4 v[20:23],v240,s[52:55],s37 offen offset:3072 sc1\nbuffer_store_dwordx4 v[32:35],v240,s[52:55],s38 offen offset:0 sc1\nbuffer_store_dwordx4 v[36:39],v240,s[52:55],s38 offen offset:1024 sc1\nbuffer_store_dwordx4 v[48:51],v240,s[52:55],s38 offen offset:2048 sc1\nbuffer_store_dwordx4 v[52:55],v240,s[52:55],s38 offen offset:3072 sc1\nds_read_b64_tr_b16 a[230:231],v226 offset:5120\n" \
"M v[184:187],v[112:115],a[240:243],v[184:187]\nv_cmp_ne_u32 vcc,0,v230\nds_read_b64_tr_b16 a[232:233],v227 offset:1024\nv_lshlrev_b32 v251,2,v251\ns_cbranch_vccnz .Lredo\nds_read_b64_tr_b16 a[234:235],v227 offset:5120\nM v[188:191],v[116:119],a[240:243],v[188:191]\ns_branch .Lod2\n" \
".Lod1x:\nds_read_b128 a[64:67],v250 offset:0\nds_read_b128 a[68:71],v250 offset:1024\nds_read_b128 a[72:75],v250 offset:2048\ns_waitcnt lgkmcnt(2)\nM v[0:3],a[64:67],v[192:195],v[0:3]\nds_read_b128 a[76:79],v250 offset:3072\nds_read_b128 a[80:83],v250 offset:4096\n" \
"M v[4:7],a[64:67],v[196:199],v[4:7]\nds_read_b128 a[84:87],v250 offset:5120\nds_read_b128 a[88:91],v250 offset:6144\nM v[64:67],a[64:67],v[200:203],v[64:67]\nds_read_b128 a[92:95],v250 offset:7168\nds_read_b128 a[96:99],v250 offset:8192\nM v[68:71],a[64:67],v[204:207],v[68:71]\nds_read_b128 a[100:103],v250 offset:9216\n" \
"s_waitcnt lgkmcnt(8)\nM v[8:11],a[68:71],v[192:195],v[8:11]\nds_read_b128 a[104:107],v250 offset:10240\nds_read_b128 a[108:111],v250 offset:11264\nM v[12:15],a[68:71],v[196:199],v[12:15]\nv_exp_f32 v184,v184\nM v[72:75],a[68:71],v[200:203],v[72:75]\nv_exp_f32 v185,v185\n" \
"M v[76:79],a[68:71],v[204:207],v[76:79]\nds_read_b128 a[112:115],v250 offset:12288\ns_waitcnt lgkmcnt(10)\nM v[16:19],a[72:75],v[192:195],v[16:19]\nv_exp_f32 v186,v186\nM v[20:23],a[72:75],v[196:199],v[20:23]\nv_exp_f32 v187,v187\nM v[80:83],a[72:75],v[200:203],v[80:83]\n" \
"v_exp_f32 v172,v172\nM v[84:87],a[72:75],v[204:207],v[84:87]\nv_cvt_pk_bf16_f32 v216,v168,v169\ns_waitcnt lgkmcnt(7)\nM v[24:27],a[76:79],v[192:195],v[24:27]\nv_exp_f32 v173,v173\nM v[28:31],a[76:79],v[196:199],v[28:31]\nv_exp_f32 v174,v174\n" \
"M v[88:91],a[76:79],v[200:203],v[88:91]\nv_exp_f32 v175,v175\nM v[92:95],a[76:79],v[204:207],v[92:95]\nv_exp_f32 v188,v188\nM v[32:35],a[80:83],v[192:195],v[32:35]\nv_exp_f32 v189,v189\nM v[36:39],a[80:83],v[196:199],v[36:39]\nv_exp_f32 v190,v190\n" \
"M v[96:99],a[80:83],v[200:203],v[96:99]\nv_exp_f32 v191,v191\nM v[100:103],a[80:83],v[204:207],v[100:103]\nv_cvt_pk_bf16_f32 v217,v170,v171\nv_cvt_pk_bf16_f32 v218,v184,v185\nM v[40:43],a[84:87],v[192:195],v[40:43]\nv_cvt_pk_bf16_f32 v219,v186,v187\nv_cvt_pk_bf16_f32 v220,v172,v173\n" \
"M v[44:47],a[84:87],v[196:199],v[44:47]\nv_cvt_pk_bf16_f32 v221,v174,v175\nv_cvt_pk_bf16_f32 v222,v188,v189\nM v[104:107],a[84:87],v[200:203],v[104:107]\nv_cvt_pk_bf16_f32 v223,v190,v191\nds_read_b128 a[116:119],v250 offset:13312\nM v[108:111],a[84:87],v[204:207],v[108:111]\nds_read_b128 a[120:123],v250 offset:14336\n" \
"s_waitcnt lgkmcnt(2)\nM v[48:51],a[88:91],v[192:195],v[48:51]\nds_read_b128 a[124:127],v250 offset:15360\nv_cvt_pk_bf16_f32 v208,v160,v161\nM v[52:55],a[88:91],v[196:199],v[52:55]\nv_cvt_pk_bf16_f32 v209,v162,v163\nv_cvt_pk_bf16_f32 v210,v176,v177\nM v[112:115],a[88:91],v[200:203],v[112:115]\n" \
"v_cvt_pk_bf16_f32 v211,v178,v179\nv_cvt_pk_bf16_f32 v212,v164,v165\nM v[116:119],a[88:91],v[204:207],v[116:119]\nv_cvt_pk_bf16_f32 v213,v166,v167\nv_cvt_pk_bf16_f32 v214,v180,v181\nM v[56:59],a[92:95],v[192:195],v[56:59]\nv_cvt_pk_bf16_f32 v215,v182,v183\nv_add_u32 v226,131072,v242\n" \
"M v[60:63],a[92:95],v[196:199],v[60:63]\nds_read_b64_tr_b16 a[212:213],v226 offset:0\nds_read_b64_tr_b16 a[214:215],v226 offset:4096\nM v[120:123],a[92:95],v[200:203],v[120:123]\nv_xor_b32 v227,32,v226\nds_read_b64_tr_b16 a[216:217],v227 offset:0\nM v[124:127],a[92:95],v[204:207],v[124:127]\nds_read_b64_tr_b16 a[218:219],v227 offset:4096\n" \
"ds_read_b64_tr_b16 a[220:221],v226 offset:512\nM a[192:195],a[208:211],v[192:195],a[192:195]\nds_read_b64_tr_b16 a[222:223],v226 offset:4608\nds_read_b64_tr_b16 a[224:225],v227 offset:512\nM a[196:199],a[208:211],v[196:199],a[196:199]\nds_read_b64_tr_b16 a[226:227],v227 offset:4608\nds_read_b64_tr_b16 a[228:229],v226 offset:1024\nM a[200:203],a[208:211],v[200:203],a[200:203]\n" \
"ds_read_b64_tr_b16 a[230:231],v226 offset:5120\ns_lshl2_add_u32 s77,s61,s79\nM a[204:207],a[208:211],v[204:207],a[204:207]\ns_add_u32 s77,s77,4096\nds_read_b64_tr_b16 a[232:233],v227 offset:1024\nM v[64:67],a[96:99],v[216:219],v[64:67]\nds_read_b64_tr_b16 a[234:235],v227 offset:5120\ns_lshl_b32 s37,s61,13\n" \
"M v[68:71],a[96:99],v[220:223],v[68:71]\ns_add_u32 s37,s37,s67\nds_read_b64_tr_b16 a[236:237],v226 offset:1536\nM v[72:75],a[100:103],v[216:219],v[72:75]\nds_read_b64_tr_b16 a[238:239],v226 offset:5632\ns_add_u32 s37,s37,1048576\nM v[76:79],a[100:103],v[220:223],v[76:79]\nds_read_b64_tr_b16 a[240:241],v227 offset:1536\n" \
"ds_read_b64_tr_b16 a[242:243],v227 offset:5632\nM v[80:83],a[104:107],v[216:219],v[80:83]\ns_lshl2_add_u32 s76,s61,s79\nv_xor_b32 v250,0x8000,v250\nM v[84:87],a[104:107],v[220:223],v[84:87]\nv_add_u32 v229,s64,v250\nv_and_b32 v251,3,v249\nM v[88:91],a[108:111],v[216:219],v[88:91]\n" \
"s_lshl4_add_u32 s69,s10,s68\ns_add_u32 m0,s64,0\nM v[92:95],a[108:111],v[220:223],v[92:95]\nbuffer_load_dwordx4 v243,s[40:43],s69 offen offset:0 lds\nM v[96:99],a[112:115],v[216:219],v[96:99]\nbuffer_load_dwordx4 v244,s[40:43],s69 offen offset:1024 lds\nM v[100:103],a[112:115],v[220:223],v[100:103]\ns_waitcnt lgkmcnt(6)\n" \
"M v[104:107],a[116:119],v[216:219],v[104:107]\nbuffer_load_dwordx4 v245,s[40:43],s69 offen offset:2048 lds\nM v[108:111],a[116:119],v[220:223],v[108:111]\nbuffer_load_dwordx4 v246,s[40:43],s69 offen offset:3072 lds\nM v[112:115],a[120:123],v[216:219],v[112:115]\ns_lshl4_add_u32 s69,s10,s68\ns_add_u32 s69,s69,0x10000\nM v[116:119],a[120:123],v[220:223],v[116:119]\n" \
"s_add_u32 m0,s64,16384\nM v[120:123],a[124:127],v[216:219],v[120:123]\nbuffer_load_dwordx4 v243,s[40:43],s69 offen offset:0 lds\nM v[124:127],a[124:127],v[220:223],v[124:127]\nbuffer_load_dwordx4 v244,s[40:43],s69 offen offset:1024 lds\nM a[200:203],a[208:211],v[216:219],a[200:203]\nbuffer_load_dwordx4 v245,s[40:43],s69 offen offset:2048 lds\nM a[204:207],a[208:211],v[220:223],a[204:207]\n" \
"buffer_load_dwordx4 v246,s[40:43],s69 offen offset:3072 lds\nM v[0:3],a[96:99],v[208:211],v[0:3]\nM v[4:7],a[96:99],v[212:215],v[4:7]\nM v[8:11],a[100:103],v[208:211],v[8:11]\nM v[12:15],a[100:103],v[212:215],v[12:15]\nM v[16:19],a[104:107],v[208:211],v[16:19]\nv_accvgpr_read_b32 v232,a200\nM v[20:23],a[104:107],v[212:215],v[20:23]\n" \
"v_accvgpr_read_b32 v236,a204\nv_add_f32 v233,v232,v236\nM v[24:27],a[108:111],v[208:211],v[24:27]\nv_rcp_f32 v232,v232\nM v[28:31],a[108:111],v[212:215],v[28:31]\nv_rcp_f32 v236,v236\nM v[32:35],a[112:115],v[208:211],v[32:35]\nv_mul_f32 v64,v64,v232\n" \
"v_mul_f32 v65,v65,v232\nM v[36:39],a[112:115],v[212:215],v[36:39]\nv_mul_f32 v66,v66,v232\nv_mul_f32 v67,v67,v232\nM v[40:43],a[116:119],v[208:211],v[40:43]\nv_cvt_pk_bf16_f32 v64,v64,v65\nv_mul_f32 v72,v72,v232\nM v[44:47],a[116:119],v[212:215],v[44:47]\n" \
"v_mul_f32 v73,v73,v232\nv_mul_f32 v74,v74,v232\nM v[48:51],a[120:123],v[208:211],v[48:51]\nv_mul_f32 v75,v75,v232\nv_cvt_pk_bf16_f32 v65,v66,v67\nM v[52:55],a[120:123],v[212:215],v[52:55]\nv_mul_f32 v68,v68,v236\nv_mul_f32 v69,v69,v236\n" \
"M v[56:59],a[124:127],v[208:211],v[56:59]\nv_add_f32 v233,v233,v232\nv_cvt_pk_bf16_f32 v66,v72,v73\nM v[60:63],a[124:127],v[212:215],v[60:63]\nv_cvt_pk_bf16_f32 v67,v74,v75\nv_mul_f32 v70,v70,v236\nM a[192:195],a[208:211],v[208:211],a[192:195]\nv_mul_f32 v71,v71,v236\n" \
"v_cvt_pk_bf16_f32 v68,v68,v69\nM a[196:199],a[208:211],v[212:215],a[196:199]\nv_add_f32 v233,v233,v236\nv_mul_f32 v76,v76,v236\nv_mul_f32 v77,v77,v236\nv_mul_f32 v78,v78,v236\nv_mul_f32 v79,v79,v236\nv_cvt_pk_bf16_f32 v69,v70,v71\n" \
"v_cmp_ngt_f32 vcc,s28,v233\nv_cvt_pk_bf16_f32 v70,v76,v77\nv_cvt_pk_bf16_f32 v71,v78,v79\nM v[128:131],v[64:67],a[212:215],0\ns_and_b64 vcc,vcc,s[90:91]\ns_cselect_b32 s39,0x8000000,s16\ns_cselect_b32 s16,s16,0x8000000\ns_cselect_b32 s77,0x8000000,s77\n" \
"s_addc_u32 s99,s99,0\nM v[132:135],v[68:71],a[212:215],0\nv_mul_f32 v80,v80,v232\nv_mul_f32 v81,v81,v232\nv_mul_f32 v84,v84,v236\nv_mul_f32 v85,v85,v236\nv_mul_f32 v96,v96,v232\nv_mul_f32 v97,v97,v232\n" \
"v_mul_f32 v100,v100,v236\nv_mul_f32 v101,v101,v236\nv_mul_f32 v112,v112,v232\nv_mul_f32 v113,v113,v232\nv_mul_f32 v116,v116,v236\nv_mul_f32 v117,v117,v236\nM v[136:139],v[64:67],a[216:219],0\nv_mul_f32 v82,v82,v232\n" \
"v_mul_f32 v83,v83,v232\nv_cvt_pk_bf16_f32 v80,v80,v81\nv_mul_f32 v86,v86,v236\nv_mul_f32 v87,v87,v236\nv_cvt_pk_bf16_f32 v84,v84,v85\nv_mul_f32 v98,v98,v232\nv_mul_f32 v99,v99,v232\nv_cvt_pk_bf16_f32 v96,v96,v97\n" \
"v_mul_f32 v102,v102,v236\nv_mul_f32 v103,v103,v236\nv_cvt_pk_bf16_f32 v100,v100,v101\nv_mul_f32 v114,v114,v232\nv_mul_f32 v115,v115,v232\nv_cvt_pk_bf16_f32 v112,v112,v113\nv_mul_f32 v118,v118,v236\nv_mul_f32 v119,v119,v236\n" \
"v_cvt_pk_bf16_f32 v116,v116,v117\ns_or_b32 s37,s37,s39\nv_mul_f32 v88,v88,v232\nv_mul_f32 v89,v89,v232\nv_mul_f32 v90,v90,v232\nv_mul_f32 v91,v91,v232\nv_cvt_pk_bf16_f32 v81,v82,v83\nv_mul_f32 v92,v92,v236\n" \
"v_mul_f32 v93,v93,v236\nv_mul_f32 v94,v94,v236\nv_mul_f32 v95,v95,v236\nv_cvt_pk_bf16_f32 v85,v86,v87\nv_mul_f32 v104,v104,v232\nv_mul_f32 v105,v105,v232\nv_mul_f32 v106,v106,v232\nv_mul_f32 v107,v107,v232\n" \
"v_cvt_pk_bf16_f32 v97,v98,v99\nv_mul_f32 v108,v108,v236\nv_mul_f32 v109,v109,v236\nv_mul_f32 v110,v110,v236\nv_mul_f32 v111,v111,v236\nv_cvt_pk_bf16_f32 v101,v102,v103\nv_mul_f32 v120,v120,v232\nv_mul_f32 v121,v121,v232\n" \
"v_mul_f32 v122,v122,v232\nv_mul_f32 v123,v123,v232\nv_cvt_pk_bf16_f32 v113,v114,v115\nv_mul_f32 v124,v124,v236\nv_mul_f32 v125,v125,v236\nv_mul_f32 v126,v126,v236\nv_mul_f32 v127,v127,v236\nv_cvt_pk_bf16_f32 v117,v118,v119\n" \
"M v[140:143],v[68:71],a[216:219],0\ns_add_u32 s38,s37,4096\nv_cvt_pk_bf16_f32 v82,v88,v89\nv_cvt_pk_bf16_f32 v83,v90,v91\nv_cvt_pk_bf16_f32 v86,v92,v93\nv_cvt_pk_bf16_f32 v87,v94,v95\nv_cvt_pk_bf16_f32 v98,v104,v105\nv_cvt_pk_bf16_f32 v99,v106,v107\n" \
"v_cvt_pk_bf16_f32 v102,v108,v109\nv_cvt_pk_bf16_f32 v103,v110,v111\nv_cvt_pk_bf16_f32 v114,v120,v121\nv_cvt_pk_bf16_f32 v115,v122,v123\nv_cvt_pk_bf16_f32 v118,v124,v125\nv_cvt_pk_bf16_f32 v119,v126,v127\nbuffer_store_dwordx4 v[64:67],v240,s[52:55],s37 offen offset:0 sc1\nbuffer_store_dwordx4 v[68:71],v240,s[52:55],s37 offen offset:1024 sc1\n" \
"buffer_store_dwordx4 v[80:83],v240,s[52:55],s37 offen offset:2048 sc1\nbuffer_store_dwordx4 v[84:87],v240,s[52:55],s37 offen offset:3072 sc1\nbuffer_store_dwordx4 v[96:99],v240,s[52:55],s38 offen offset:0 sc1\nbuffer_store_dwordx4 v[100:103],v240,s[52:55],s38 offen offset:1024 sc1\nbuffer_store_dwordx4 v[112:115],v240,s[52:55],s38 offen offset:2048 sc1\nbuffer_store_dwordx4 v[116:119],v240,s[52:55],s38 offen offset:3072 sc1\nM v[144:147],v[64:67],a[220:223],0\nds_read_b64_tr_b16 a[212:213],v226 offset:8192\n" \
"ds_read_b64_tr_b16 a[214:215],v226 offset:12288\nM v[148:151],v[68:71],a[220:223],0\nds_read_b64_tr_b16 a[216:217],v227 offset:8192\nds_read_b64_tr_b16 a[218:219],v227 offset:12288\nM v[152:155],v[64:67],a[224:227],0\nds_read_b64_tr_b16 a[220:221],v226 offset:8704\nds_read_b64_tr_b16 a[222:223],v226 offset:12800\nM v[156:159],v[68:71],a[224:227],0\n" \
"ds_read_b64_tr_b16 a[224:225],v227 offset:8704\nds_read_b64_tr_b16 a[226:227],v227 offset:12800\nM v[160:163],v[64:67],a[228:231],0\nv_accvgpr_read_b32 v224,a192\nv_accvgpr_read_b32 v228,a196\nM v[164:167],v[68:71],a[228:231],0\nds_read_b64_tr_b16 a[228:229],v226 offset:9216\ns_waitcnt lgkmcnt(9)\n" \
"M v[168:171],v[64:67],a[232:235],0\nds_read_b64_tr_b16 a[230:231],v226 offset:13312\nv_add_f32 v225,v224,v228\nM v[172:175],v[68:71],a[232:235],0\nds_read_b64_tr_b16 a[232:233],v227 offset:9216\nds_read_b64_tr_b16 a[234:235],v227 offset:13312\nM v[176:179],v[64:67],a[236:239],0\nv_rcp_f32 v224,v224\n" \
"M v[180:183],v[68:71],a[236:239],0\nds_read_b64_tr_b16 a[236:237],v226 offset:9728\nds_read_b64_tr_b16 a[238:239],v226 offset:13824\nM v[184:187],v[64:67],a[240:243],0\nv_rcp_f32 v228,v228\nM v[188:191],v[68:71],a[240:243],0\nds_read_b64_tr_b16 a[240:241],v227 offset:9728\ns_waitcnt lgkmcnt(12)\n" \
"M v[128:131],v[80:83],a[212:215],v[128:131]\nds_read_b64_tr_b16 a[242:243],v227 offset:13824\nv_add_f32 v225,v225,v224\nM v[132:135],v[84:87],a[212:215],v[132:135]\nds_read_b64_tr_b16 a[212:213],v226 offset:16384\ns_waitcnt lgkmcnt(10)\nM v[136:139],v[80:83],a[216:219],v[136:139]\nds_read_b64_tr_b16 a[214:215],v226 offset:20480\n" \
"v_add_f32 v225,v225,v228\nM v[140:143],v[84:87],a[216:219],v[140:143]\nds_read_b64_tr_b16 a[216:217],v227 offset:16384\nds_read_b64_tr_b16 a[218:219],v227 offset:20480\nM v[144:147],v[80:83],a[220:223],v[144:147]\nv_cmp_ngt_f32 vcc,s28,v225\ns_lshl_b32 s37,s61,13\nM v[148:151],v[84:87],a[220:223],v[148:151]\n" \
"ds_read_b64_tr_b16 a[220:221],v226 offset:16896\ns_waitcnt lgkmcnt(10)\nM v[152:155],v[80:83],a[224:227],v[152:155]\nds_read_b64_tr_b16 a[222:223],v226 offset:20992\nv_mul_f32 v0,v0,v224\nM v[156:159],v[84:87],a[224:227],v[156:159]\nds_read_b64_tr_b16 a[224:225],v227 offset:16896\nds_read_b64_tr_b16 a[226:227],v227 offset:20992\n" \
"M v[160:163],v[80:83],a[228:231],v[160:163]\ns_and_b64 vcc,vcc,s[90:91]\ns_cselect_b32 s39,0x8000000,s17\ns_cselect_b32 s17,s17,0x8000000\ns_cselect_b32 s76,0x8000000,s76\ns_addc_u32 s99,s99,0\nM v[164:167],v[84:87],a[228:231],v[164:167]\nds_read_b64_tr_b16 a[228:229],v226 offset:17408\n" \
"s_waitcnt lgkmcnt(10)\nM v[168:171],v[80:83],a[232:235],v[168:171]\nds_read_b64_tr_b16 a[230:231],v226 offset:21504\nv_mov_b32 v230,s99\nM v[172:175],v[84:87],a[232:235],v[172:175]\nds_read_b64_tr_b16 a[232:233],v227 offset:17408\nds_read_b64_tr_b16 a[234:235],v227 offset:21504\nM v[176:179],v[80:83],a[236:239],v[176:179]\n" \
"s_waitcnt vmcnt(16)\nds_write_b32 v229,v230\nM v[180:183],v[84:87],a[236:239],v[180:183]\nds_read_b64_tr_b16 a[236:237],v226 offset:17920\ns_waitcnt lgkmcnt(10)\nM v[184:187],v[80:83],a[240:243],v[184:187]\nds_read_b64_tr_b16 a[238:239],v226 offset:22016\nv_lshl_add_u32 v229,v251,12,v250\n" \
"M v[188:191],v[84:87],a[240:243],v[188:191]\nds_read_b64_tr_b16 a[240:241],v227 offset:17920\nds_read_b64_tr_b16 a[242:243],v227 offset:22016\nM v[128:131],v[96:99],a[212:215],v[128:131]\nv_mul_f32 v1,v1,v224\nv_mul_f32 v4,v4,v228\nM v[132:135],v[100:103],a[212:215],v[132:135]\nds_read_b64_tr_b16 a[212:213],v226 offset:24576\n" \
"ds_read_b64_tr_b16 a[214:215],v226 offset:28672\nM v[136:139],v[96:99],a[216:219],v[136:139]\nv_mul_f32 v5,v5,v228\nv_mul_f32 v16,v16,v224\nM v[140:143],v[100:103],a[216:219],v[140:143]\nds_read_b64_tr_b16 a[216:217],v227 offset:24576\ns_waitcnt lgkmcnt(11)\nM v[144:147],v[96:99],a[220:223],v[144:147]\n" \
"ds_read_b64_tr_b16 a[218:219],v227 offset:28672\nv_mul_f32 v17,v17,v224\nM v[148:151],v[100:103],a[220:223],v[148:151]\nds_read_b64_tr_b16 a[220:221],v226 offset:25088\nds_read_b64_tr_b16 a[222:223],v226 offset:29184\nM v[152:155],v[96:99],a[224:227],v[152:155]\ns_waitcnt lgkmcnt(10)\ns_barrier\n" \
"M v[156:159],v[100:103],a[224:227],v[156:159]\nds_read_b64_tr_b16 a[224:225],v227 offset:25088\nds_read_b64_tr_b16 a[226:227],v227 offset:29184\nM v[160:163],v[96:99],a[228:231],v[160:163]\nds_read_b32 v230,v229\nv_mul_f32 v20,v20,v228\nM v[164:167],v[100:103],a[228:231],v[164:167]\nds_read_b64_tr_b16 a[228:229],v226 offset:25600\n" \
"ds_read_b64_tr_b16 a[230:231],v226 offset:29696\nM v[168:171],v[96:99],a[232:235],v[168:171]\nv_mul_f32 v21,v21,v228\nv_mul_f32 v32,v32,v224\nM v[172:175],v[100:103],a[232:235],v[172:175]\nds_read_b64_tr_b16 a[232:233],v227 offset:25600\ns_waitcnt lgkmcnt(11)\nM v[176:179],v[96:99],a[236:239],v[176:179]\n" \
"ds_read_b64_tr_b16 a[234:235],v227 offset:29696\nv_mul_f32 v33,v33,v224\nM v[180:183],v[100:103],a[236:239],v[180:183]\nds_read_b64_tr_b16 a[236:237],v226 offset:26112\nds_read_b64_tr_b16 a[238:239],v226 offset:30208\nM v[184:187],v[96:99],a[240:243],v[184:187]\nv_mul_f32 v36,v36,v228\nv_mul_f32 v37,v37,v228\n" \
"M v[188:191],v[100:103],a[240:243],v[188:191]\nds_read_b64_tr_b16 a[240:241],v227 offset:26112\ns_waitcnt lgkmcnt(11)\nM v[128:131],v[112:115],a[212:215],v[128:131]\nds_read_b64_tr_b16 a[242:243],v227 offset:30208\nv_mul_f32 v48,v48,v224\nM v[132:135],v[116:119],a[212:215],v[132:135]\nds_read_b64_tr_b16 a[212:213],v226 offset:0\n" \
"ds_read_b64_tr_b16 a[214:215],v226 offset:4096\nM v[136:139],v[112:115],a[216:219],v[136:139]\nv_mul_f32 v49,v49,v224\nv_mul_f32 v52,v52,v228\nM v[140:143],v[116:119],a[216:219],v[140:143]\nds_read_b64_tr_b16 a[216:217],v227 offset:0\ns_waitcnt lgkmcnt(10)\nM v[144:147],v[112:115],a[220:223],v[144:147]\n" \
"ds_read_b64_tr_b16 a[218:219],v227 offset:4096\nv_mul_f32 v53,v53,v228\nM v[148:151],v[116:119],a[220:223],v[148:151]\nds_read_b64_tr_b16 a[220:221],v226 offset:512\nds_read_b64_tr_b16 a[222:223],v226 offset:4608\nM v[152:155],v[112:115],a[224:227],v[152:155]\ns_add_u32 s37,s37,s67\nv_mul_f32 v2,v2,v224\n" \
"M v[156:159],v[116:119],a[224:227],v[156:159]\nv_mul_f32 v3,v3,v224\ns_waitcnt lgkmcnt(9)\nM v[160:163],v[112:115],a[228:231],v[160:163]\nv_cvt_pk_bf16_f32 v0,v0,v1\nv_mul_f32 v6,v6,v228\nM v[164:167],v[116:119],a[228:231],v[164:167]\nv_mul_f32 v7,v7,v228\n" \
"v_cvt_pk_bf16_f32 v4,v4,v5\nM v[168:171],v[112:115],a[232:235],v[168:171]\nv_mul_f32 v18,v18,v224\nv_mul_f32 v19,v19,v224\nM v[172:175],v[116:119],a[232:235],v[172:175]\nv_cvt_pk_bf16_f32 v16,v16,v17\ns_waitcnt lgkmcnt(5)\nM v[176:179],v[112:115],a[236:239],v[176:179]\n" \
"v_mul_f32 v22,v22,v228\nv_mul_f32 v23,v23,v228\nM v[180:183],v[116:119],a[236:239],v[180:183]\nv_cvt_pk_bf16_f32 v20,v20,v21\nv_mul_f32 v34,v34,v224\nv_mul_f32 v35,v35,v224\nv_cvt_pk_bf16_f32 v32,v32,v33\nv_mul_f32 v38,v38,v228\n" \
"v_mul_f32 v39,v39,v228\nv_cvt_pk_bf16_f32 v36,v36,v37\nv_mul_f32 v50,v50,v224\nv_mul_f32 v51,v51,v224\nv_cvt_pk_bf16_f32 v48,v48,v49\nv_mul_f32 v54,v54,v228\nv_mul_f32 v55,v55,v228\nv_cvt_pk_bf16_f32 v52,v52,v53\n" \
"ds_read_b64_tr_b16 a[224:225],v227 offset:512\ns_or_b32 s37,s37,s39\nv_mul_f32 v8,v8,v224\nv_mul_f32 v9,v9,v224\nv_mul_f32 v10,v10,v224\nv_mul_f32 v11,v11,v224\nv_cvt_pk_bf16_f32 v1,v2,v3\nv_mul_f32 v12,v12,v228\n" \
"v_mul_f32 v13,v13,v228\nv_mul_f32 v14,v14,v228\nv_mul_f32 v15,v15,v228\nv_cvt_pk_bf16_f32 v5,v6,v7\nv_mul_f32 v24,v24,v224\nv_mul_f32 v25,v25,v224\nv_mul_f32 v26,v26,v224\nv_mul_f32 v27,v27,v224\n" \
"v_cvt_pk_bf16_f32 v17,v18,v19\nv_mul_f32 v28,v28,v228\nv_mul_f32 v29,v29,v228\nv_mul_f32 v30,v30,v228\nv_mul_f32 v31,v31,v228\nv_cvt_pk_bf16_f32 v21,v22,v23\nv_mul_f32 v40,v40,v224\nv_mul_f32 v41,v41,v224\n" \
"v_mul_f32 v42,v42,v224\nv_mul_f32 v43,v43,v224\nv_cvt_pk_bf16_f32 v33,v34,v35\nv_mul_f32 v44,v44,v228\nv_mul_f32 v45,v45,v228\nv_mul_f32 v46,v46,v228\nv_mul_f32 v47,v47,v228\nv_cvt_pk_bf16_f32 v37,v38,v39\n" \
"v_mul_f32 v56,v56,v224\nv_mul_f32 v57,v57,v224\nv_mul_f32 v58,v58,v224\nv_mul_f32 v59,v59,v224\nv_cvt_pk_bf16_f32 v49,v50,v51\nv_mul_f32 v60,v60,v228\nv_mul_f32 v61,v61,v228\nv_mul_f32 v62,v62,v228\n" \
"v_mul_f32 v63,v63,v228\nv_cvt_pk_bf16_f32 v53,v54,v55\nds_read_b64_tr_b16 a[226:227],v227 offset:4608\nM v[184:187],v[112:115],a[240:243],v[184:187]\ns_add_u32 s38,s37,4096\nv_cvt_pk_bf16_f32 v2,v8,v9\nv_cvt_pk_bf16_f32 v3,v10,v11\nv_cvt_pk_bf16_f32 v6,v12,v13\n" \
"v_cvt_pk_bf16_f32 v7,v14,v15\nv_cvt_pk_bf16_f32 v18,v24,v25\nv_cvt_pk_bf16_f32 v19,v26,v27\nv_cvt_pk_bf16_f32 v22,v28,v29\nv_cvt_pk_bf16_f32 v23,v30,v31\nv_cvt_pk_bf16_f32 v34,v40,v41\nv_cvt_pk_bf16_f32 v35,v42,v43\nv_cvt_pk_bf16_f32 v38,v44,v45\n" \
"v_cvt_pk_bf16_f32 v39,v46,v47\nv_cvt_pk_bf16_f32 v50,v56,v57\nv_cvt_pk_bf16_f32 v51,v58,v59\nv_cvt_pk_bf16_f32 v54,v60,v61\nv_cvt_pk_bf16_f32 v55,v62,v63\nds_read_b64_tr_b16 a[228:229],v226 offset:1024\nbuffer_store_dwordx4 v[0:3],v240,s[52:55],s37 offen offset:0 sc1\nbuffer_store_dwordx4 v[4:7],v240,s[52:55],s37 offen offset:1024 sc1\n" \
"buffer_store_dwordx4 v[16:19],v240,s[52:55],s37 offen offset:2048 sc1\nbuffer_store_dwordx4 v[20:23],v240,s[52:55],s37 offen offset:3072 sc1\nbuffer_store_dwordx4 v[32:35],v240,s[52:55],s38 offen offset:0 sc1\nbuffer_store_dwordx4 v[36:39],v240,s[52:55],s38 offen offset:1024 sc1\nbuffer_store_dwordx4 v[48:51],v240,s[52:55],s38 offen offset:2048 sc1\nbuffer_store_dwordx4 v[52:55],v240,s[52:55],s38 offen offset:3072 sc1\nds_read_b64_tr_b16 a[230:231],v226 offset:5120\nM v[188:191],v[116:119],a[240:243],v[188:191]\n" \
"v_cmp_ne_u32 vcc,0,v230\nds_read_b64_tr_b16 a[232:233],v227 offset:1024\ns_waitcnt vmcnt(8)\nbuffer_store_dword v254,off,s[52:55],s77 sc1\nv_lshlrev_b32 v251,2,v251\ns_cbranch_vccnz .Lredo\nds_read_b64_tr_b16 a[234:235],v227 offset:5120\n.Lod2:\n" \
"s_waitcnt lgkmcnt(10)\nM v[8:11],v[0:3],a[212:215],0\nds_read_b64_tr_b16 a[236:237],v226 offset:1536\nds_read_b64_tr_b16 a[238:239],v226 offset:5632\nM v[12:15],v[4:7],a[212:215],0\ns_add_u32 s80,s79,4096\ns_waitcnt lgkmcnt(10)\nM v[24:27],v[0:3],a[216:219],0\n" \
"buffer_load_dword v248,v251,s[52:55],s80 offen sc1\nM v[28:31],v[4:7],a[216:219],0\nds_read_b64_tr_b16 a[240:241],v227 offset:1536\nds_read_b64_tr_b16 a[242:243],v227 offset:5632\ns_waitcnt lgkmcnt(10)\nM v[40:43],v[0:3],a[220:223],0\nds_read_b64_tr_b16 a[212:213],v226 offset:8192\nds_read_b64_tr_b16 a[214:215],v226 offset:12288\n" \
"M v[44:47],v[4:7],a[220:223],0\nds_read_b64_tr_b16 a[216:217],v227 offset:8192\ns_waitcnt lgkmcnt(10)\nM v[56:59],v[0:3],a[224:227],0\nds_read_b64_tr_b16 a[218:219],v227 offset:12288\nds_read_b64_tr_b16 a[220:221],v226 offset:8704\nM v[60:63],v[4:7],a[224:227],0\nds_read_b64_tr_b16 a[222:223],v226 offset:12800\n" \
"s_waitcnt lgkmcnt(10)\nM v[72:75],v[0:3],a[228:231],0\nds_read_b64_tr_b16 a[224:225],v227 offset:8704\nds_read_b64_tr_b16 a[226:227],v227 offset:12800\nM v[76:79],v[4:7],a[228:231],0\nds_read_b64_tr_b16 a[228:229],v226 offset:9216\nds_read_b64_tr_b16 a[230:231],v226 offset:13312\nM v[88:91],v[0:3],a[232:235],0\n" \
"s_add_u32 s37,s10,s67\ns_add_u32 s37,s37,1048576\nM v[92:95],v[4:7],a[232:235],0\nds_read_b64_tr_b16 a[232:233],v227 offset:9216\ns_waitcnt lgkmcnt(13)\nM v[104:107],v[0:3],a[236:239],0\nds_read_b64_tr_b16 a[234:235],v227 offset:13312\ns_lshl_b32 s70,s60,4\n" \
"M v[108:111],v[4:7],a[236:239],0\nds_read_b64_tr_b16 a[236:237],v226 offset:9728\ns_waitcnt lgkmcnt(13)\nM v[120:123],v[0:3],a[240:243],0\nds_read_b64_tr_b16 a[238:239],v226 offset:13824\ns_add_u32 s70,s70,s62\nM v[124:127],v[4:7],a[240:243],0\nds_read_b64_tr_b16 a[240:241],v227 offset:9728\n" \
"s_waitcnt lgkmcnt(12)\nM v[8:11],v[16:19],a[212:215],v[8:11]\nds_read_b64_tr_b16 a[242:243],v227 offset:13824\ns_xor_b32 s71,s7,3\nM v[12:15],v[20:23],a[212:215],v[12:15]\ns_waitcnt vmcnt(0)\nv_cmp_eq_u32 vcc,0,v248\ns_waitcnt lgkmcnt(10)\n" \
"M v[24:27],v[16:19],a[216:219],v[24:27]\ns_cbranch_vccnz .Lps6\n.Lpb6:\nds_read_b64_tr_b16 a[212:213],v226 offset:16384\nM v[28:31],v[20:23],a[216:219],v[28:31]\nbuffer_load_dwordx4 a[0:3],v240,s[52:55],s37 offen offset:0 sc1\nM v[40:43],v[16:19],a[220:223],v[40:43]\nbuffer_load_dwordx4 a[4:7],v240,s[52:55],s37 offen offset:1024 sc1\n" \
"M v[44:47],v[20:23],a[220:223],v[44:47]\nds_read_b64_tr_b16 a[214:215],v226 offset:20480\ns_waitcnt lgkmcnt(7)\nM v[56:59],v[16:19],a[224:227],v[56:59]\nds_read_b64_tr_b16 a[216:217],v227 offset:16384\nds_read_b64_tr_b16 a[218:219],v227 offset:20480\nM v[60:63],v[20:23],a[224:227],v[60:63]\nds_read_b64_tr_b16 a[220:221],v226 offset:16896\n" \
"ds_read_b64_tr_b16 a[222:223],v226 offset:20992\nM v[72:75],v[16:19],a[228:231],v[72:75]\nds_read_b64_tr_b16 a[224:225],v227 offset:16896\nds_read_b64_tr_b16 a[226:227],v227 offset:20992\nM v[76:79],v[20:23],a[228:231],v[76:79]\nds_read_b64_tr_b16 a[228:229],v226 offset:17408\ns_waitcnt lgkmcnt(10)\nM v[88:91],v[16:19],a[232:235],v[88:91]\n" \
"ds_read_b64_tr_b16 a[230:231],v226 offset:21504\ns_lshl2_add_u32 s70,s70,s71\nM v[92:95],v[20:23],a[232:235],v[92:95]\nds_read_b64_tr_b16 a[232:233],v227 offset:17408\nds_read_b64_tr_b16 a[234:235],v227 offset:21504\nM v[104:107],v[16:19],a[236:239],v[104:107]\nbuffer_load_dwordx4 a[8:11],v240,s[52:55],s37 offen offset:2048 sc1\nM v[108:111],v[20:23],a[236:239],v[108:111]\n" \
"ds_read_b64_tr_b16 a[236:237],v226 offset:17920\ns_waitcnt lgkmcnt(12)\nM v[120:123],v[16:19],a[240:243],v[120:123]\nds_read_b64_tr_b16 a[238:239],v226 offset:22016\ns_lshl_b32 s70,s70,15\nM v[124:127],v[20:23],a[240:243],v[124:127]\nbuffer_store_dword v254,off,s[52:55],s76 sc1\ns_waitcnt lgkmcnt(12)\n" \
"M v[8:11],v[32:35],a[212:215],v[8:11]\nds_read_b64_tr_b16 a[240:241],v227 offset:17920\nds_read_b64_tr_b16 a[242:243],v227 offset:22016\nM v[12:15],v[36:39],a[212:215],v[12:15]\nds_read_b64_tr_b16 a[212:213],v226 offset:24576\ns_waitcnt lgkmcnt(11)\nM v[24:27],v[32:35],a[216:219],v[24:27]\nbuffer_load_dwordx4 a[12:15],v240,s[52:55],s37 offen offset:3072 sc1\n" \
"M v[28:31],v[36:39],a[216:219],v[28:31]\nds_read_b64_tr_b16 a[214:215],v226 offset:28672\nds_read_b64_tr_b16 a[216:217],v227 offset:24576\nM v[40:43],v[32:35],a[220:223],v[40:43]\nds_read_b64_tr_b16 a[218:219],v227 offset:28672\ns_lshl4_add_u32 s69,s11,s68\nM v[44:47],v[36:39],a[220:223],v[44:47]\nds_read_b64_tr_b16 a[220:221],v226 offset:25088\n" \
"s_waitcnt lgkmcnt(9)\nM v[56:59],v[32:35],a[224:227],v[56:59]\nds_read_b64_tr_b16 a[222:223],v226 offset:29184\ns_add_u32 m0,s64,32768\nM v[60:63],v[36:39],a[224:227],v[60:63]\nds_read_b64_tr_b16 a[224:225],v227 offset:25088\nds_read_b64_tr_b16 a[226:227],v227 offset:29184\nM v[72:75],v[32:35],a[228:231],v[72:75]\n" \
"buffer_load_dwordx4 v243,s[40:43],s69 offen offset:0 lds\nM v[76:79],v[36:39],a[228:231],v[76:79]\nds_read_b64_tr_b16 a[228:229],v226 offset:25600\nds_read_b64_tr_b16 a[230:231],v226 offset:29696\nM v[88:91],v[32:35],a[232:235],v[88:91]\ns_add_u32 s37,s37,4096\ns_lshl_b32 s72,s61,8\nM v[92:95],v[36:39],a[232:235],v[92:95]\n" \
"ds_read_b64_tr_b16 a[232:233],v227 offset:25600\ns_waitcnt lgkmcnt(10)\nM v[104:107],v[32:35],a[236:239],v[104:107]\nds_read_b64_tr_b16 a[234:235],v227 offset:29696\ns_add_u32 s70,s70,s72\nM v[108:111],v[36:39],a[236:239],v[108:111]\nbuffer_load_dwordx4 v244,s[40:43],s69 offen offset:1024 lds\nM v[120:123],v[32:35],a[240:243],v[120:123]\n" \
"s_barrier\nds_read_b64_tr_b16 a[236:237],v226 offset:26112\nM v[124:127],v[36:39],a[240:243],v[124:127]\nds_read_b64_tr_b16 a[238:239],v226 offset:30208\ns_waitcnt lgkmcnt(9)\nM v[8:11],v[48:51],a[212:215],v[8:11]\nbuffer_load_dwordx4 a[16:19],v240,s[52:55],s37 offen offset:0 sc1\nM v[12:15],v[52:55],a[212:215],v[12:15]\n" \
"ds_read_b64_tr_b16 a[240:241],v227 offset:26112\nds_read_b64_tr_b16 a[242:243],v227 offset:30208\nM v[24:27],v[48:51],a[216:219],v[24:27]\nbuffer_load_dwordx4 v245,s[40:43],s69 offen offset:2048 lds\nM v[28:31],v[52:55],a[216:219],v[28:31]\nv_add_u32 v226,0,v242\ns_waitcnt lgkmcnt(7)\nM v[40:43],v[48:51],a[220:223],v[40:43]\n" \
"buffer_load_dwordx4 a[20:23],v240,s[52:55],s37 offen offset:1024 sc1\nM v[44:47],v[52:55],a[220:223],v[44:47]\nds_read_b64_tr_b16 a[212:213],v226 offset:0\nds_read_b64_tr_b16 a[214:215],v226 offset:4096\nM v[56:59],v[48:51],a[224:227],v[56:59]\nv_xor_b32 v227,32,v226\nds_read_b64_tr_b16 a[216:217],v227 offset:0\nM v[60:63],v[52:55],a[224:227],v[60:63]\n" \
"buffer_load_dwordx4 v246,s[40:43],s69 offen offset:3072 lds\nds_read_b64_tr_b16 a[218:219],v227 offset:4096\ns_waitcnt lgkmcnt(8)\nM v[72:75],v[48:51],a[228:231],v[72:75]\nds_read_b64_tr_b16 a[220:221],v226 offset:512\nds_read_b64_tr_b16 a[222:223],v226 offset:4608\nM v[76:79],v[52:55],a[228:231],v[76:79]\nds_read_b64_tr_b16 a[224:225],v227 offset:512\n" \
"ds_read_b64_tr_b16 a[226:227],v227 offset:4608\nM v[88:91],v[48:51],a[232:235],v[88:91]\ns_lshl4_add_u32 s69,s11,s68\ns_add_u32 s69,s69,0x10000\nM v[92:95],v[52:55],a[232:235],v[92:95]\nds_read_b64_tr_b16 a[228:229],v226 offset:1024\ns_waitcnt lgkmcnt(9)\nM v[104:107],v[48:51],a[236:239],v[104:107]\n" \
"s_add_u32 m0,s64,49152\nds_read_b64_tr_b16 a[230:231],v226 offset:5120\nM v[108:111],v[52:55],a[236:239],v[108:111]\nds_read_b64_tr_b16 a[232:233],v227 offset:1024\nds_read_b64_tr_b16 a[234:235],v227 offset:5120\nM v[120:123],v[48:51],a[240:243],v[120:123]\nbuffer_load_dwordx4 v243,s[40:43],s69 offen offset:0 lds\nM v[124:127],v[52:55],a[240:243],v[124:127]\n" \
"ds_read_b64_tr_b16 a[236:237],v226 offset:1536\ns_waitcnt vmcnt(11) lgkmcnt(11)\nM v[128:131],a[0:3],a[212:215],v[128:131]\nds_read_b64_tr_b16 a[238:239],v226 offset:5632\ns_waitcnt vmcnt(10)\nM v[132:135],a[4:7],a[212:215],v[132:135]\nds_read_b64_tr_b16 a[240:241],v227 offset:1536\ns_waitcnt lgkmcnt(11)\n" \
"M v[136:139],a[0:3],a[216:219],v[136:139]\nbuffer_load_dwordx4 a[24:27],v240,s[52:55],s37 offen offset:2048 sc1\nM v[140:143],a[4:7],a[216:219],v[140:143]\nbuffer_load_dwordx4 v244,s[40:43],s69 offen offset:1024 lds\nds_read_b64_tr_b16 a[242:243],v227 offset:5632\ns_waitcnt lgkmcnt(8)\nM v[144:147],a[0:3],a[220:223],v[144:147]\nbuffer_load_dwordx4 a[28:31],v240,s[52:55],s37 offen offset:3072 sc1\n" \
"M v[148:151],a[4:7],a[220:223],v[148:151]\nds_read_b64_tr_b16 a[212:213],v226 offset:8192\nds_read_b64_tr_b16 a[214:215],v226 offset:12288\nM v[152:155],a[0:3],a[224:227],v[152:155]\nds_read_b64_tr_b16 a[216:217],v227 offset:8192\nds_read_b64_tr_b16 a[218:219],v227 offset:12288\nM v[156:159],a[4:7],a[224:227],v[156:159]\nds_read_b64_tr_b16 a[220:221],v226 offset:8704\n" \
"s_waitcnt lgkmcnt(9)\nM v[160:163],a[0:3],a[228:231],v[160:163]\nbuffer_load_dwordx4 v245,s[40:43],s69 offen offset:2048 lds\nM v[164:167],a[4:7],a[228:231],v[164:167]\nds_read_b64_tr_b16 a[222:223],v226 offset:12800\nds_read_b64_tr_b16 a[224:225],v227 offset:8704\nM v[168:171],a[0:3],a[232:235],v[168:171]\nds_read_b64_tr_b16 a[226:227],v227 offset:12800\n" \
"ds_read_b64_tr_b16 a[228:229],v226 offset:9216\nM v[172:175],a[4:7],a[232:235],v[172:175]\nds_read_b64_tr_b16 a[230:231],v226 offset:13312\ns_waitcnt lgkmcnt(11)\nM v[176:179],a[0:3],a[236:239],v[176:179]\nds_read_b64_tr_b16 a[232:233],v227 offset:9216\nds_read_b64_tr_b16 a[234:235],v227 offset:13312\nM v[180:183],a[4:7],a[236:239],v[180:183]\n" \
"buffer_load_dwordx4 v246,s[40:43],s69 offen offset:3072 lds\ns_waitcnt lgkmcnt(12)\nM v[184:187],a[0:3],a[240:243],v[184:187]\nds_read_b64_tr_b16 a[236:237],v226 offset:9728\nds_read_b64_tr_b16 a[238:239],v226 offset:13824\nM v[188:191],a[4:7],a[240:243],v[188:191]\ns_add_u32 s37,s11,s67\ns_waitcnt vmcnt(14) lgkmcnt(12)\n" \
"M v[128:131],a[8:11],a[212:215],v[128:131]\ns_add_u32 s37,s37,1048576\ns_waitcnt vmcnt(12)\nM v[132:135],a[12:15],a[212:215],v[132:135]\nds_read_b64_tr_b16 a[240:241],v227 offset:9728\ns_waitcnt lgkmcnt(10)\nM v[136:139],a[8:11],a[216:219],v[136:139]\nbuffer_load_dwordx4 a[32:35],v240,s[52:55],s37 offen offset:0 sc1\n" \
"M v[140:143],a[12:15],a[216:219],v[140:143]\nds_read_b64_tr_b16 a[242:243],v227 offset:13824\ns_waitcnt lgkmcnt(9)\nM v[144:147],a[8:11],a[220:223],v[144:147]\nbuffer_load_dwordx4 a[36:39],v240,s[52:55],s37 offen offset:1024 sc1\nM v[148:151],a[12:15],a[220:223],v[148:151]\nds_read_b64_tr_b16 a[212:213],v226 offset:16384\nds_read_b64_tr_b16 a[214:215],v226 offset:20480\n" \
"s_waitcnt lgkmcnt(8)\nM v[152:155],a[8:11],a[224:227],v[152:155]\nds_read_b64_tr_b16 a[216:217],v227 offset:16384\nds_read_b64_tr_b16 a[218:219],v227 offset:20480\nM v[156:159],a[12:15],a[224:227],v[156:159]\nds_read_b64_tr_b16 a[220:221],v226 offset:16896\nds_read_b64_tr_b16 a[222:223],v226 offset:20992\nM v[160:163],a[8:11],a[228:231],v[160:163]\n" \
"ds_read_b64_tr_b16 a[224:225],v227 offset:16896\nds_read_b64_tr_b16 a[226:227],v227 offset:20992\nM v[164:167],a[12:15],a[228:231],v[164:167]\nds_read_b64_tr_b16 a[228:229],v226 offset:17408\ns_waitcnt lgkmcnt(11)\nM v[168:171],a[8:11],a[232:235],v[168:171]\nds_read_b64_tr_b16 a[230:231],v226 offset:21504\ns_lshl4_add_u32 s69,s14,s68\n" \
"M v[172:175],a[12:15],a[232:235],v[172:175]\nds_read_b64_tr_b16 a[232:233],v227 offset:17408\nds_read_b64_tr_b16 a[234:235],v227 offset:21504\nM v[176:179],a[8:11],a[236:239],v[176:179]\nbuffer_load_dwordx4 a[40:43],v240,s[52:55],s37 offen offset:2048 sc1\nM v[180:183],a[12:15],a[236:239],v[180:183]\nds_read_b64_tr_b16 a[236:237],v226 offset:17920\ns_waitcnt lgkmcnt(13)\n" \
"M v[184:187],a[8:11],a[240:243],v[184:187]\nds_read_b64_tr_b16 a[238:239],v226 offset:22016\ns_add_u32 m0,s64,65536\nM v[188:191],a[12:15],a[240:243],v[188:191]\nds_read_b64_tr_b16 a[240:241],v227 offset:17920\ns_waitcnt vmcnt(12) lgkmcnt(13)\nM v[128:131],a[16:19],a[212:215],v[128:131]\nds_read_b64_tr_b16 a[242:243],v227 offset:22016\n" \
"s_waitcnt vmcnt(10)\nM v[132:135],a[20:23],a[212:215],v[132:135]\nds_read_b64_tr_b16 a[212:213],v226 offset:24576\ns_waitcnt lgkmcnt(12)\nM v[136:139],a[16:19],a[216:219],v[136:139]\nbuffer_load_dwordx4 a[44:47],v240,s[52:55],s37 offen offset:3072 sc1\nM v[140:143],a[20:23],a[216:219],v[140:143]\nds_read_b64_tr_b16 a[214:215],v226 offset:28672\n" \
"s_waitcnt lgkmcnt(9)\nM v[144:147],a[16:19],a[220:223],v[144:147]\nds_read_b64_tr_b16 a[216:217],v227 offset:24576\nds_read_b64_tr_b16 a[218:219],v227 offset:28672\nM v[148:151],a[20:23],a[220:223],v[148:151]\nds_read_b64_tr_b16 a[220:221],v226 offset:25088\nds_read_b64_tr_b16 a[222:223],v226 offset:29184\nM v[152:155],a[16:19],a[224:227],v[152:155]\n" \
"buffer_load_dwordx4 v243,s[40:43],s69 offen offset:0 lds\nM v[156:159],a[20:23],a[224:227],v[156:159]\nds_read_b64_tr_b16 a[224:225],v227 offset:25088\ns_waitcnt lgkmcnt(10)\nM v[160:163],a[16:19],a[228:231],v[160:163]\nds_read_b64_tr_b16 a[226:227],v227 offset:29184\ns_add_u32 s37,s37,4096\nM v[164:167],a[20:23],a[228:231],v[164:167]\n" \
"ds_read_b64_tr_b16 a[228:229],v226 offset:25600\nds_read_b64_tr_b16 a[230:231],v226 offset:29696\nM v[168:171],a[16:19],a[232:235],v[168:171]\nbuffer_load_dwordx4 v244,s[40:43],s69 offen offset:1024 lds\nM v[172:175],a[20:23],a[232:235],v[172:175]\nds_read_b64_tr_b16 a[232:233],v227 offset:25600\ns_waitcnt lgkmcnt(10)\nM v[176:179],a[16:19],a[236:239],v[176:179]\n" \
"ds_read_b64_tr_b16 a[234:235],v227 offset:29696\ns_add_u32 s70,s70,54525952\nM v[180:183],a[20:23],a[236:239],v[180:183]\nbuffer_load_dwordx4 v245,s[40:43],s69 offen offset:2048 lds\nM v[184:187],a[16:19],a[240:243],v[184:187]\ns_waitcnt vmcnt(7)\ns_barrier\nM v[188:191],a[20:23],a[240:243],v[188:191]\n" \
"ds_read_b64_tr_b16 a[236:237],v226 offset:26112\ns_waitcnt lgkmcnt(8)\nM v[128:131],a[24:27],a[212:215],v[128:131]\nbuffer_load_dwordx4 v246,s[40:43],s69 offen offset:3072 lds\nM v[132:135],a[28:31],a[212:215],v[132:135]\ns_lshl4_add_u32 s69,s14,s68\nds_read_b64_tr_b16 a[238:239],v226 offset:30208\nM v[136:139],a[24:27],a[216:219],v[136:139]\n" \
"s_add_u32 s69,s69,0x10000\ns_add_u32 m0,s64,98304\nM v[140:143],a[28:31],a[216:219],v[140:143]\nbuffer_load_dwordx4 v243,s[40:43],s69 offen offset:0 lds\nbuffer_load_dwordx4 v244,s[40:43],s69 offen offset:1024 lds\nbuffer_load_dwordx4 a[48:51],v240,s[52:55],s37 offen offset:0 sc1\nds_read_b64_tr_b16 a[240:241],v227 offset:26112\nds_read_b64_tr_b16 a[242:243],v227 offset:30208\n" \
"s_waitcnt lgkmcnt(6)\nM v[144:147],a[24:27],a[220:223],v[144:147]\nbuffer_load_dwordx4 v245,s[40:43],s69 offen offset:2048 lds\nbuffer_load_dwordx4 a[52:55],v240,s[52:55],s37 offen offset:1024 sc1\nM v[148:151],a[28:31],a[220:223],v[148:151]\nds_read_b64_tr_b16 a[212:213],v226 offset:32768\nds_read_b64_tr_b16 a[214:215],v226 offset:36864\nM v[152:155],a[24:27],a[224:227],v[152:155]\n" \
"ds_read_b64_tr_b16 a[216:217],v227 offset:32768\nds_read_b64_tr_b16 a[218:219],v227 offset:36864\nM v[156:159],a[28:31],a[224:227],v[156:159]\nds_read_b64_tr_b16 a[220:221],v226 offset:33280\nds_read_b64_tr_b16 a[222:223],v226 offset:37376\nM v[160:163],a[24:27],a[228:231],v[160:163]\nds_read_b64_tr_b16 a[224:225],v227 offset:33280\nds_read_b64_tr_b16 a[226:227],v227 offset:37376\n" \
"M v[164:167],a[28:31],a[228:231],v[164:167]\nbuffer_load_dwordx4 v246,s[40:43],s69 offen offset:3072 lds\ns_waitcnt lgkmcnt(10)\nM v[168:171],a[24:27],a[232:235],v[168:171]\nds_read_b64_tr_b16 a[228:229],v226 offset:33792\nds_read_b64_tr_b16 a[230:231],v226 offset:37888\nM v[172:175],a[28:31],a[232:235],v[172:175]\nds_read_b64_tr_b16 a[232:233],v227 offset:33792\n" \
"ds_read_b64_tr_b16 a[234:235],v227 offset:37888\nM v[176:179],a[24:27],a[236:239],v[176:179]\nbuffer_load_dwordx4 a[56:59],v240,s[52:55],s37 offen offset:2048 sc1\nM v[180:183],a[28:31],a[236:239],v[180:183]\nds_read_b64_tr_b16 a[236:237],v226 offset:34304\ns_waitcnt lgkmcnt(13)\nM v[184:187],a[24:27],a[240:243],v[184:187]\nds_read_b64_tr_b16 a[238:239],v226 offset:38400\n" \
"s_add_u32 s38,s70,16384\nM v[188:191],a[28:31],a[240:243],v[188:191]\nds_read_b64_tr_b16 a[240:241],v227 offset:34304\ns_waitcnt vmcnt(14) lgkmcnt(13)\nM v[128:131],a[32:35],a[212:215],v[128:131]\nds_read_b64_tr_b16 a[242:243],v227 offset:38400\ns_waitcnt vmcnt(13)\nM v[132:135],a[36:39],a[212:215],v[132:135]\n" \
"ds_read_b64_tr_b16 a[212:213],v226 offset:40960\ns_waitcnt lgkmcnt(11)\nM v[136:139],a[32:35],a[216:219],v[136:139]\nbuffer_load_dwordx4 a[60:63],v240,s[52:55],s37 offen offset:3072 sc1\nM v[140:143],a[36:39],a[216:219],v[140:143]\nds_read_b64_tr_b16 a[214:215],v226 offset:45056\nds_read_b64_tr_b16 a[216:217],v227 offset:40960\nM v[144:147],a[32:35],a[220:223],v[144:147]\n" \
"ds_read_b64_tr_b16 a[218:219],v227 offset:45056\ns_add_u32 s37,s14,s67\nM v[148:151],a[36:39],a[220:223],v[148:151]\nds_read_b64_tr_b16 a[220:221],v226 offset:41472\ns_waitcnt lgkmcnt(10)\nM v[152:155],a[32:35],a[224:227],v[152:155]\nds_read_b64_tr_b16 a[222:223],v226 offset:45568\ns_add_u32 s37,s37,1048576\n" \
"M v[156:159],a[36:39],a[224:227],v[156:159]\nds_read_b64_tr_b16 a[224:225],v227 offset:41472\nds_read_b64_tr_b16 a[226:227],v227 offset:45568\nM v[160:163],a[32:35],a[228:231],v[160:163]\nbuffer_load_dwordx4 a[64:67],v240,s[52:55],s37 offen offset:0 sc1\nM v[164:167],a[36:39],a[228:231],v[164:167]\nds_read_b64_tr_b16 a[228:229],v226 offset:41984\ns_waitcnt lgkmcnt(10)\n" \
"M v[168:171],a[32:35],a[232:235],v[168:171]\nds_read_b64_tr_b16 a[230:231],v226 offset:46080\ns_lshl2_add_u32 s71,s62,s71\nM v[172:175],a[36:39],a[232:235],v[172:175]\nds_read_b64_tr_b16 a[232:233],v227 offset:41984\nds_read_b64_tr_b16 a[234:235],v227 offset:46080\nM v[176:179],a[32:35],a[236:239],v[176:179]\nbuffer_load_dwordx4 a[68:71],v240,s[52:55],s37 offen offset:1024 sc1\n" \
"M v[180:183],a[36:39],a[236:239],v[180:183]\nds_read_b64_tr_b16 a[236:237],v226 offset:42496\ns_waitcnt lgkmcnt(11)\nM v[184:187],a[32:35],a[240:243],v[184:187]\nds_read_b64_tr_b16 a[238:239],v226 offset:46592\ns_lshl_b32 s71,s71,6\nM v[188:191],a[36:39],a[240:243],v[188:191]\nds_read_b64_tr_b16 a[240:241],v227 offset:42496\n" \
"s_waitcnt vmcnt(15)\nM v[128:131],a[40:43],a[212:215],v[128:131]\nds_read_b64_tr_b16 a[242:243],v227 offset:46592\ns_waitcnt vmcnt(14)\nM v[132:135],a[44:47],a[212:215],v[132:135]\nds_read_b64_tr_b16 a[212:213],v226 offset:49152\ns_waitcnt lgkmcnt(10)\nM v[136:139],a[40:43],a[216:219],v[136:139]\n" \
"ds_read_b64_tr_b16 a[214:215],v226 offset:53248\ns_add_u32 s71,s71,s63\nM v[140:143],a[44:47],a[216:219],v[140:143]\nds_read_b64_tr_b16 a[216:217],v227 offset:49152\nds_read_b64_tr_b16 a[218:219],v227 offset:53248\nM v[144:147],a[40:43],a[220:223],v[144:147]\nbuffer_load_dword v248,v251,s[52:55],s79 offen sc1\nM v[148:151],a[44:47],a[220:223],v[148:151]\n" \
"ds_read_b64_tr_b16 a[220:221],v226 offset:49664\ns_waitcnt lgkmcnt(10)\nM v[152:155],a[40:43],a[224:227],v[152:155]\nds_read_b64_tr_b16 a[222:223],v226 offset:53760\nv_and_b32 v232,1,v249\nM v[156:159],a[44:47],a[224:227],v[156:159]\nds_read_b64_tr_b16 a[224:225],v227 offset:49664\nds_read_b64_tr_b16 a[226:227],v227 offset:53760\n" \
"M v[160:163],a[40:43],a[228:231],v[160:163]\nbuffer_load_dwordx4 a[72:75],v240,s[52:55],s37 offen offset:2048 sc1\nM v[164:167],a[44:47],a[228:231],v[164:167]\nds_read_b64_tr_b16 a[228:229],v226 offset:50176\ns_waitcnt lgkmcnt(10)\nM v[168:171],a[40:43],a[232:235],v[168:171]\nds_read_b64_tr_b16 a[230:231],v226 offset:54272\ns_lshl2_add_u32 s71,s71,73728\n" \
"M v[172:175],a[44:47],a[232:235],v[172:175]\nds_read_b64_tr_b16 a[232:233],v227 offset:50176\nds_read_b64_tr_b16 a[234:235],v227 offset:54272\nM v[176:179],a[40:43],a[236:239],v[176:179]\nbuffer_load_dwordx4 a[76:79],v240,s[52:55],s37 offen offset:3072 sc1\nM v[180:183],a[44:47],a[236:239],v[180:183]\nds_read_b64_tr_b16 a[236:237],v226 offset:50688\ns_waitcnt lgkmcnt(10)\n" \
"M v[184:187],a[40:43],a[240:243],v[184:187]\nds_read_b64_tr_b16 a[238:239],v226 offset:54784\ns_add_u32 s37,s37,4096\nM v[188:191],a[44:47],a[240:243],v[188:191]\nds_read_b64_tr_b16 a[240:241],v227 offset:50688\ns_waitcnt vmcnt(10)\nM v[128:131],a[48:51],a[212:215],v[128:131]\nds_read_b64_tr_b16 a[242:243],v227 offset:54784\n" \
"s_waitcnt vmcnt(8)\nM v[132:135],a[52:55],a[212:215],v[132:135]\nds_read_b64_tr_b16 a[212:213],v226 offset:57344\ns_waitcnt lgkmcnt(10)\nM v[136:139],a[48:51],a[216:219],v[136:139]\nds_read_b64_tr_b16 a[214:215],v226 offset:61440\nv_lshlrev_b32 v232,7,v232\nM v[140:143],a[52:55],a[216:219],v[140:143]\n" \
"ds_read_b64_tr_b16 a[216:217],v227 offset:57344\nds_read_b64_tr_b16 a[218:219],v227 offset:61440\nM v[144:147],a[48:51],a[220:223],v[144:147]\nbuffer_load_dwordx4 a[80:83],v240,s[52:55],s37 offen offset:0 sc1\nM v[148:151],a[52:55],a[220:223],v[148:151]\nds_read_b64_tr_b16 a[220:221],v226 offset:57856\ns_waitcnt lgkmcnt(10)\nM v[152:155],a[48:51],a[224:227],v[152:155]\n" \
"ds_read_b64_tr_b16 a[222:223],v226 offset:61952\ns_lshl2_add_u32 s74,s63,s7\nM v[156:159],a[52:55],a[224:227],v[156:159]\nds_read_b64_tr_b16 a[224:225],v227 offset:57856\nds_read_b64_tr_b16 a[226:227],v227 offset:61952\nM v[160:163],a[48:51],a[228:231],v[160:163]\nbuffer_load_dwordx4 a[84:87],v240,s[52:55],s37 offen offset:1024 sc1\nM v[164:167],a[52:55],a[228:231],v[164:167]\n" \
"ds_read_b64_tr_b16 a[228:229],v226 offset:58368\ns_waitcnt lgkmcnt(10)\nM v[168:171],a[48:51],a[232:235],v[168:171]\nds_read_b64_tr_b16 a[230:231],v226 offset:62464\ns_lshl_b32 s72,s62,7\nM v[172:175],a[52:55],a[232:235],v[172:175]\nds_read_b64_tr_b16 a[232:233],v227 offset:58368\nds_read_b64_tr_b16 a[234:235],v227 offset:62464\n" \
"M v[176:179],a[48:51],a[236:239],v[176:179]\nbuffer_load_dwordx4 a[88:91],v240,s[52:55],s37 offen offset:2048 sc1\nM v[180:183],a[52:55],a[236:239],v[180:183]\nds_read_b64_tr_b16 a[236:237],v226 offset:58880\ns_waitcnt lgkmcnt(10)\nM v[184:187],a[48:51],a[240:243],v[184:187]\ns_waitcnt vmcnt(10)\ns_barrier\n" \
"M v[188:191],a[52:55],a[240:243],v[188:191]\nds_read_b64_tr_b16 a[238:239],v226 offset:62976\ns_waitcnt vmcnt(9)\nM v[128:131],a[56:59],a[212:215],v[128:131]\nds_read_b64_tr_b16 a[240:241],v227 offset:58880\ns_waitcnt vmcnt(8)\nM v[132:135],a[60:63],a[212:215],v[132:135]\nds_read_b64_tr_b16 a[242:243],v227 offset:62976\n" \
"s_waitcnt lgkmcnt(9)\nM v[136:139],a[56:59],a[216:219],v[136:139]\nv_add_u32 v226,65536,v242\nds_read_b64_tr_b16 a[212:213],v226 offset:0\nM v[140:143],a[60:63],a[216:219],v[140:143]\nds_read_b64_tr_b16 a[214:215],v226 offset:4096\nv_xor_b32 v227,32,v226\nM v[144:147],a[56:59],a[220:223],v[144:147]\n" \
"ds_read_b64_tr_b16 a[216:217],v227 offset:0\nds_read_b64_tr_b16 a[218:219],v227 offset:4096\nM v[148:151],a[60:63],a[220:223],v[148:151]\nds_read_b64_tr_b16 a[220:221],v226 offset:512\ns_waitcnt lgkmcnt(10)\nM v[152:155],a[56:59],a[224:227],v[152:155]\nds_read_b64_tr_b16 a[222:223],v226 offset:4608\ns_add_u32 s95,s74,s72\n" \
"M v[156:159],a[60:63],a[224:227],v[156:159]\nds_read_b64_tr_b16 a[224:225],v227 offset:512\nds_read_b64_tr_b16 a[226:227],v227 offset:4608\nM v[160:163],a[56:59],a[228:231],v[160:163]\nbuffer_load_dwordx4 a[92:95],v240,s[52:55],s37 offen offset:3072 sc1\nM v[164:167],a[60:63],a[228:231],v[164:167]\nds_read_b64_tr_b16 a[228:229],v226 offset:1024\ns_waitcnt lgkmcnt(11)\n" \
"M v[168:171],a[56:59],a[232:235],v[168:171]\nds_read_b64_tr_b16 a[230:231],v226 offset:5120\ns_add_u32 s37,s10,s67\nM v[172:175],a[60:63],a[232:235],v[172:175]\nds_read_b64_tr_b16 a[232:233],v227 offset:1024\nds_read_b64_tr_b16 a[234:235],v227 offset:5120\nM v[176:179],a[56:59],a[236:239],v[176:179]\ns_waitcnt vmcnt(6)\n" \
"v_cmp_eq_u32 vcc,0,v248\nM v[180:183],a[60:63],a[236:239],v[180:183]\nds_read_b64_tr_b16 a[236:237],v226 offset:1536\ns_waitcnt lgkmcnt(11)\nM v[184:187],a[56:59],a[240:243],v[184:187]\nds_read_b64_tr_b16 a[238:239],v226 offset:5632\ns_cbranch_vccnz .Lps7\n.Lpb7:\n" \
"M v[188:191],a[60:63],a[240:243],v[188:191]\nbuffer_load_dwordx4 a[96:99],v240,s[52:55],s37 offen offset:0 sc1\nM v[128:131],a[64:67],a[212:215],v[128:131]\nds_read_b64_tr_b16 a[240:241],v227 offset:1536\nds_read_b64_tr_b16 a[242:243],v227 offset:5632\nM v[132:135],a[68:71],a[212:215],v[132:135]\nds_read_b64_tr_b16 a[212:213],v226 offset:8192\ns_waitcnt lgkmcnt(10)\n" \
"M v[136:139],a[64:67],a[216:219],v[136:139]\nbuffer_load_dwordx4 a[100:103],v240,s[52:55],s37 offen offset:1024 sc1\nM v[140:143],a[68:71],a[216:219],v[140:143]\nds_read_b64_tr_b16 a[214:215],v226 offset:12288\nds_read_b64_tr_b16 a[216:217],v227 offset:8192\nM v[144:147],a[64:67],a[220:223],v[144:147]\nds_read_b64_tr_b16 a[218:219],v227 offset:12288\nv_and_b32 v234,15,v249\n" \
"M v[148:151],a[68:71],a[220:223],v[148:151]\nds_read_b64_tr_b16 a[220:221],v226 offset:8704\ns_waitcnt lgkmcnt(10)\nM v[152:155],a[64:67],a[224:227],v[152:155]\nds_read_b64_tr_b16 a[222:223],v226 offset:12800\ns_sub_u32 s74,s74,s72\nM v[156:159],a[68:71],a[224:227],v[156:159]\nds_read_b64_tr_b16 a[224:225],v227 offset:8704\n" \
"ds_read_b64_tr_b16 a[226:227],v227 offset:12800\nM v[160:163],a[64:67],a[228:231],v[160:163]\nbuffer_load_dwordx4 a[104:107],v240,s[52:55],s37 offen offset:2048 sc1\nM v[164:167],a[68:71],a[228:231],v[164:167]\nds_read_b64_tr_b16 a[228:229],v226 offset:9216\ns_waitcnt lgkmcnt(11)\nM v[168:171],a[64:67],a[232:235],v[168:171]\nds_read_b64_tr_b16 a[230:231],v226 offset:13312\n" \
"v_lshrrev_b32 v233,4,v249\nM v[172:175],a[68:71],a[232:235],v[172:175]\nds_read_b64_tr_b16 a[232:233],v227 offset:9216\nds_read_b64_tr_b16 a[234:235],v227 offset:13312\nM v[176:179],a[64:67],a[236:239],v[176:179]\nbuffer_load_dwordx4 a[108:111],v240,s[52:55],s37 offen offset:3072 sc1\nM v[180:183],a[68:71],a[236:239],v[180:183]\nds_read_b64_tr_b16 a[236:237],v226 offset:9728\n" \
"s_waitcnt lgkmcnt(11)\nM v[184:187],a[64:67],a[240:243],v[184:187]\nds_read_b64_tr_b16 a[238:239],v226 offset:13824\ns_add_u32 s37,s37,4096\nM v[188:191],a[68:71],a[240:243],v[188:191]\nds_read_b64_tr_b16 a[240:241],v227 offset:9728\ns_waitcnt vmcnt(9)\nM v[128:131],a[72:75],a[212:215],v[128:131]\n" \
"ds_read_b64_tr_b16 a[242:243],v227 offset:13824\ns_waitcnt vmcnt(8)\nM v[132:135],a[76:79],a[212:215],v[132:135]\nds_read_b64_tr_b16 a[212:213],v226 offset:32768\ns_waitcnt lgkmcnt(10)\nM v[136:139],a[72:75],a[216:219],v[136:139]\nds_read_b64_tr_b16 a[214:215],v226 offset:36864\nv_lshlrev_b32 v234,1,v234\n" \
"M v[140:143],a[76:79],a[216:219],v[140:143]\nds_read_b64_tr_b16 a[216:217],v227 offset:32768\nds_read_b64_tr_b16 a[218:219],v227 offset:36864\nM v[144:147],a[72:75],a[220:223],v[144:147]\nbuffer_load_dwordx4 a[112:115],v240,s[52:55],s37 offen offset:0 sc1\nM v[148:151],a[76:79],a[220:223],v[148:151]\nds_read_b64_tr_b16 a[220:221],v226 offset:33280\ns_waitcnt lgkmcnt(10)\n" \
"M v[152:155],a[72:75],a[224:227],v[152:155]\nds_read_b64_tr_b16 a[222:223],v226 offset:37376\ns_add_u32 s74,s74,1920\nM v[156:159],a[76:79],a[224:227],v[156:159]\nds_read_b64_tr_b16 a[224:225],v227 offset:33280\nds_read_b64_tr_b16 a[226:227],v227 offset:37376\nM v[160:163],a[72:75],a[228:231],v[160:163]\nbuffer_load_dwordx4 a[116:119],v240,s[52:55],s37 offen offset:1024 sc1\n" \
"M v[164:167],a[76:79],a[228:231],v[164:167]\nds_read_b64_tr_b16 a[228:229],v226 offset:33792\ns_waitcnt lgkmcnt(10)\nM v[168:171],a[72:75],a[232:235],v[168:171]\nds_read_b64_tr_b16 a[230:231],v226 offset:37888\nv_lshl_or_b32 v233,v233,8,v234\nM v[172:175],a[76:79],a[232:235],v[172:175]\nds_read_b64_tr_b16 a[232:233],v227 offset:33792\n" \
"ds_read_b64_tr_b16 a[234:235],v227 offset:37888\nM v[176:179],a[72:75],a[236:239],v[176:179]\nbuffer_load_dwordx4 a[120:123],v240,s[52:55],s37 offen offset:2048 sc1\nM v[180:183],a[76:79],a[236:239],v[180:183]\nds_read_b64_tr_b16 a[236:237],v226 offset:34304\ns_waitcnt lgkmcnt(10)\nM v[184:187],a[72:75],a[240:243],v[184:187]\nds_read_b64_tr_b16 a[238:239],v226 offset:38400\n" \
"s_lshl_b32 s95,s95,10\nM v[188:191],a[76:79],a[240:243],v[188:191]\nds_read_b64_tr_b16 a[240:241],v227 offset:34304\ns_waitcnt vmcnt(10)\nM v[128:131],a[80:83],a[212:215],v[128:131]\nds_read_b64_tr_b16 a[242:243],v227 offset:38400\ns_waitcnt vmcnt(9)\nM v[132:135],a[84:87],a[212:215],v[132:135]\n" \
"ds_read_b64_tr_b16 a[212:213],v226 offset:40960\ns_waitcnt lgkmcnt(10)\nM v[136:139],a[80:83],a[216:219],v[136:139]\nds_read_b64_tr_b16 a[214:215],v226 offset:45056\ns_lshl_b32 s74,s74,10\nM v[140:143],a[84:87],a[216:219],v[140:143]\nds_read_b64_tr_b16 a[216:217],v227 offset:40960\nds_read_b64_tr_b16 a[218:219],v227 offset:45056\n" \
"M v[144:147],a[80:83],a[220:223],v[144:147]\nbuffer_load_dwordx4 a[124:127],v240,s[52:55],s37 offen offset:3072 sc1\nM v[148:151],a[84:87],a[220:223],v[148:151]\nds_read_b64_tr_b16 a[220:221],v226 offset:41472\ns_waitcnt lgkmcnt(10)\nM v[152:155],a[80:83],a[224:227],v[152:155]\nds_read_b64_tr_b16 a[222:223],v226 offset:45568\ns_add_u32 s37,s11,s67\n" \
"M v[156:159],a[84:87],a[224:227],v[156:159]\nds_read_b64_tr_b16 a[224:225],v227 offset:41472\nds_read_b64_tr_b16 a[226:227],v227 offset:45568\nM v[160:163],a[80:83],a[228:231],v[160:163]\nbuffer_load_dwordx4 a[128:131],v240,s[52:55],s37 offen offset:0 sc1\nM v[164:167],a[84:87],a[228:231],v[164:167]\nds_read_b64_tr_b16 a[228:229],v226 offset:41984\ns_waitcnt lgkmcnt(10)\n" \
"M v[168:171],a[80:83],a[232:235],v[168:171]\nds_read_b64_tr_b16 a[230:231],v226 offset:46080\ns_lshl_b32 s72,s7,9\nM v[172:175],a[84:87],a[232:235],v[172:175]\nds_read_b64_tr_b16 a[232:233],v227 offset:41984\nds_read_b64_tr_b16 a[234:235],v227 offset:46080\nM v[176:179],a[80:83],a[236:239],v[176:179]\nbuffer_load_dwordx4 a[132:135],v240,s[52:55],s37 offen offset:1024 sc1\n" \
"M v[180:183],a[84:87],a[236:239],v[180:183]\nds_read_b64_tr_b16 a[236:237],v226 offset:42496\ns_waitcnt lgkmcnt(10)\nM v[184:187],a[80:83],a[240:243],v[184:187]\nds_read_b64_tr_b16 a[238:239],v226 offset:46592\nv_add_u32 v226,0,v242\nM v[188:191],a[84:87],a[240:243],v[188:191]\nds_read_b64_tr_b16 a[240:241],v227 offset:42496\n" \
"s_waitcnt vmcnt(11)\nM v[128:131],a[88:91],a[212:215],v[128:131]\nds_read_b64_tr_b16 a[242:243],v227 offset:46592\ns_waitcnt vmcnt(10)\nM v[132:135],a[92:95],a[212:215],v[132:135]\nds_read_b64_tr_b16 a[212:213],v226 offset:0\ns_waitcnt lgkmcnt(10)\nM v[136:139],a[88:91],a[216:219],v[136:139]\n" \
"ds_read_b64_tr_b16 a[214:215],v226 offset:4096\nv_xor_b32 v227,32,v226\nM v[140:143],a[92:95],a[216:219],v[140:143]\nds_read_b64_tr_b16 a[216:217],v227 offset:0\nds_read_b64_tr_b16 a[218:219],v227 offset:4096\nM v[144:147],a[88:91],a[220:223],v[144:147]\nbuffer_load_dwordx4 a[136:139],v240,s[52:55],s37 offen offset:2048 sc1\nM v[148:151],a[92:95],a[220:223],v[148:151]\n" \
"ds_read_b64_tr_b16 a[220:221],v226 offset:512\ns_waitcnt lgkmcnt(10)\nM v[152:155],a[88:91],a[224:227],v[152:155]\nds_read_b64_tr_b16 a[222:223],v226 offset:4608\nv_cvt_pk_bf16_f32 v192,v128,v136\nM v[156:159],a[92:95],a[224:227],v[156:159]\nds_read_b64_tr_b16 a[224:225],v227 offset:512\nds_read_b64_tr_b16 a[226:227],v227 offset:4608\n" \
"M v[160:163],a[88:91],a[228:231],v[160:163]\nbuffer_load_dwordx4 a[140:143],v240,s[52:55],s37 offen offset:3072 sc1\nM v[164:167],a[92:95],a[228:231],v[164:167]\nds_read_b64_tr_b16 a[228:229],v226 offset:1024\ns_waitcnt lgkmcnt(10)\nM v[168:171],a[88:91],a[232:235],v[168:171]\nds_read_b64_tr_b16 a[230:231],v226 offset:5120\ns_add_u32 s37,s37,4096\n" \
"M v[172:175],a[92:95],a[232:235],v[172:175]\nds_read_b64_tr_b16 a[232:233],v227 offset:1024\nds_read_b64_tr_b16 a[234:235],v227 offset:5120\nM v[176:179],a[88:91],a[236:239],v[176:179]\nbuffer_load_dwordx4 a[144:147],v240,s[52:55],s37 offen offset:0 sc1\nM v[180:183],a[92:95],a[236:239],v[180:183]\nds_read_b64_tr_b16 a[236:237],v226 offset:1536\ns_waitcnt lgkmcnt(10)\n" \
"M v[184:187],a[88:91],a[240:243],v[184:187]\nds_read_b64_tr_b16 a[238:239],v226 offset:5632\nv_cvt_pk_bf16_f32 v193,v144,v152\nM v[188:191],a[92:95],a[240:243],v[188:191]\nds_read_b64_tr_b16 a[240:241],v227 offset:1536\ns_waitcnt vmcnt(12)\nM v[8:11],a[96:99],a[212:215],v[8:11]\nds_read_b64_tr_b16 a[242:243],v227 offset:5632\n" \
"s_waitcnt vmcnt(11)\nM v[12:15],a[100:103],a[212:215],v[12:15]\nds_read_b64_tr_b16 a[212:213],v226 offset:8192\ns_waitcnt lgkmcnt(10)\nM v[24:27],a[96:99],a[216:219],v[24:27]\nds_read_b64_tr_b16 a[214:215],v226 offset:12288\nv_cvt_pk_bf16_f32 v194,v160,v168\nM v[28:31],a[100:103],a[216:219],v[28:31]\n" \
"ds_read_b64_tr_b16 a[216:217],v227 offset:8192\nds_read_b64_tr_b16 a[218:219],v227 offset:12288\nM v[40:43],a[96:99],a[220:223],v[40:43]\nbuffer_load_dwordx4 a[148:151],v240,s[52:55],s37 offen offset:1024 sc1\nM v[44:47],a[100:103],a[220:223],v[44:47]\nds_read_b64_tr_b16 a[220:221],v226 offset:8704\ns_waitcnt lgkmcnt(10)\nM v[56:59],a[96:99],a[224:227],v[56:59]\n" \
"ds_read_b64_tr_b16 a[222:223],v226 offset:12800\nv_cvt_pk_bf16_f32 v195,v176,v184\nM v[60:63],a[100:103],a[224:227],v[60:63]\nds_read_b64_tr_b16 a[224:225],v227 offset:8704\nds_read_b64_tr_b16 a[226:227],v227 offset:12800\nM v[72:75],a[96:99],a[228:231],v[72:75]\nbuffer_load_dwordx4 a[152:155],v240,s[52:55],s37 offen offset:2048 sc1\nM v[76:79],a[100:103],a[228:231],v[76:79]\n" \
"ds_read_b64_tr_b16 a[228:229],v226 offset:9216\ns_waitcnt lgkmcnt(10)\nM v[88:91],a[96:99],a[232:235],v[88:91]\nds_read_b64_tr_b16 a[230:231],v226 offset:13312\nv_cvt_pk_bf16_f32 v196,v129,v137\nM v[92:95],a[100:103],a[232:235],v[92:95]\nds_read_b64_tr_b16 a[232:233],v227 offset:9216\nds_read_b64_tr_b16 a[234:235],v227 offset:13312\n" \
"M v[104:107],a[96:99],a[236:239],v[104:107]\nbuffer_load_dwordx4 a[156:159],v240,s[52:55],s37 offen offset:3072 sc1\nM v[108:111],a[100:103],a[236:239],v[108:111]\nds_read_b64_tr_b16 a[236:237],v226 offset:9728\ns_waitcnt lgkmcnt(10)\nM v[120:123],a[96:99],a[240:243],v[120:123]\nds_read_b64_tr_b16 a[238:239],v226 offset:13824\ns_add_u32 s37,s14,s67\n" \
"M v[124:127],a[100:103],a[240:243],v[124:127]\nds_read_b64_tr_b16 a[240:241],v227 offset:9728\ns_waitcnt vmcnt(13)\nM v[8:11],a[104:107],a[212:215],v[8:11]\nds_read_b64_tr_b16 a[242:243],v227 offset:13824\ns_waitcnt vmcnt(12)\nM v[12:15],a[108:111],a[212:215],v[12:15]\nds_read_b64_tr_b16 a[212:213],v226 offset:16384\n" \
"s_waitcnt lgkmcnt(10)\nM v[24:27],a[104:107],a[216:219],v[24:27]\nds_read_b64_tr_b16 a[214:215],v226 offset:20480\nv_cvt_pk_bf16_f32 v197,v145,v153\nM v[28:31],a[108:111],a[216:219],v[28:31]\nds_read_b64_tr_b16 a[216:217],v227 offset:16384\nds_read_b64_tr_b16 a[218:219],v227 offset:20480\nM v[40:43],a[104:107],a[220:223],v[40:43]\n" \
"buffer_load_dwordx4 a[160:163],v240,s[52:55],s37 offen offset:0 sc1\nM v[44:47],a[108:111],a[220:223],v[44:47]\nds_read_b64_tr_b16 a[220:221],v226 offset:16896\ns_waitcnt lgkmcnt(10)\nM v[56:59],a[104:107],a[224:227],v[56:59]\nds_read_b64_tr_b16 a[222:223],v226 offset:20992\nv_cvt_pk_bf16_f32 v198,v161,v169\nM v[60:63],a[108:111],a[224:227],v[60:63]\n" \
"ds_read_b64_tr_b16 a[224:225],v227 offset:16896\nds_read_b64_tr_b16 a[226:227],v227 offset:20992\nM v[72:75],a[104:107],a[228:231],v[72:75]\nbuffer_load_dwordx4 a[164:167],v240,s[52:55],s37 offen offset:1024 sc1\nM v[76:79],a[108:111],a[228:231],v[76:79]\nds_read_b64_tr_b16 a[228:229],v226 offset:17408\ns_waitcnt lgkmcnt(10)\nM v[88:91],a[104:107],a[232:235],v[88:91]\n" \
"ds_read_b64_tr_b16 a[230:231],v226 offset:21504\nv_cvt_pk_bf16_f32 v199,v177,v185\nM v[92:95],a[108:111],a[232:235],v[92:95]\nds_read_b64_tr_b16 a[232:233],v227 offset:17408\nds_read_b64_tr_b16 a[234:235],v227 offset:21504\nM v[104:107],a[104:107],a[236:239],v[104:107]\nv_cvt_pk_bf16_f32 v200,v130,v138\nv_cvt_pk_bf16_f32 v201,v146,v154\n" \
"M v[108:111],a[108:111],a[236:239],v[108:111]\nds_read_b64_tr_b16 a[236:237],v226 offset:17920\ns_waitcnt lgkmcnt(10)\nM v[120:123],a[104:107],a[240:243],v[120:123]\nds_read_b64_tr_b16 a[238:239],v226 offset:22016\nv_cvt_pk_bf16_f32 v202,v162,v170\nM v[124:127],a[108:111],a[240:243],v[124:127]\nds_read_b64_tr_b16 a[240:241],v227 offset:17920\n" \
"s_waitcnt vmcnt(13)\nM v[8:11],a[112:115],a[212:215],v[8:11]\nds_read_b64_tr_b16 a[242:243],v227 offset:22016\ns_waitcnt vmcnt(12)\nM v[12:15],a[116:119],a[212:215],v[12:15]\nds_read_b64_tr_b16 a[212:213],v226 offset:24576\ns_waitcnt lgkmcnt(10)\nM v[24:27],a[112:115],a[216:219],v[24:27]\n" \
"ds_read_b64_tr_b16 a[214:215],v226 offset:28672\nv_cvt_pk_bf16_f32 v203,v178,v186\nM v[28:31],a[116:119],a[216:219],v[28:31]\nds_read_b64_tr_b16 a[216:217],v227 offset:24576\nds_read_b64_tr_b16 a[218:219],v227 offset:28672\nM v[40:43],a[112:115],a[220:223],v[40:43]\nv_cvt_pk_bf16_f32 v204,v131,v139\nv_cvt_pk_bf16_f32 v205,v147,v155\n" \
"M v[44:47],a[116:119],a[220:223],v[44:47]\nds_read_b64_tr_b16 a[220:221],v226 offset:25088\ns_waitcnt lgkmcnt(10)\nM v[56:59],a[112:115],a[224:227],v[56:59]\nds_read_b64_tr_b16 a[222:223],v226 offset:29184\nv_cvt_pk_bf16_f32 v206,v163,v171\nM v[60:63],a[116:119],a[224:227],v[60:63]\nds_read_b64_tr_b16 a[224:225],v227 offset:25088\n" \
"ds_read_b64_tr_b16 a[226:227],v227 offset:29184\nM v[72:75],a[112:115],a[228:231],v[72:75]\nv_cvt_pk_bf16_f32 v207,v179,v187\nv_cvt_pk_bf16_f32 v208,v132,v140\nM v[76:79],a[116:119],a[228:231],v[76:79]\nds_read_b64_tr_b16 a[228:229],v226 offset:25600\ns_waitcnt lgkmcnt(10)\nM v[88:91],a[112:115],a[232:235],v[88:91]\n" \
"ds_read_b64_tr_b16 a[230:231],v226 offset:29696\nv_cvt_pk_bf16_f32 v209,v148,v156\nM v[92:95],a[116:119],a[232:235],v[92:95]\nds_read_b64_tr_b16 a[232:233],v227 offset:25600\nds_read_b64_tr_b16 a[234:235],v227 offset:29696\nM v[104:107],a[112:115],a[236:239],v[104:107]\nv_cvt_pk_bf16_f32 v210,v164,v172\nv_cvt_pk_bf16_f32 v211,v180,v188\n" \
"M v[108:111],a[116:119],a[236:239],v[108:111]\nds_read_b64_tr_b16 a[236:237],v226 offset:26112\ns_waitcnt lgkmcnt(10)\nM v[120:123],a[112:115],a[240:243],v[120:123]\nds_read_b64_tr_b16 a[238:239],v226 offset:30208\nv_cvt_pk_bf16_f32 v212,v133,v141\nM v[124:127],a[116:119],a[240:243],v[124:127]\nds_read_b64_tr_b16 a[240:241],v227 offset:26112\n" \
"s_waitcnt vmcnt(11)\nM v[8:11],a[120:123],a[212:215],v[8:11]\nds_read_b64_tr_b16 a[242:243],v227 offset:30208\ns_waitcnt vmcnt(10)\nM v[12:15],a[124:127],a[212:215],v[12:15]\nds_read_b64_tr_b16 a[212:213],v226 offset:32768\ns_waitcnt lgkmcnt(10)\nM v[24:27],a[120:123],a[216:219],v[24:27]\n" \
"ds_read_b64_tr_b16 a[214:215],v226 offset:36864\nv_cvt_pk_bf16_f32 v213,v149,v157\nM v[28:31],a[124:127],a[216:219],v[28:31]\nds_read_b64_tr_b16 a[216:217],v227 offset:32768\nds_read_b64_tr_b16 a[218:219],v227 offset:36864\nM v[40:43],a[120:123],a[220:223],v[40:43]\nv_cvt_pk_bf16_f32 v214,v165,v173\nv_cvt_pk_bf16_f32 v215,v181,v189\n" \
"M v[44:47],a[124:127],a[220:223],v[44:47]\nds_read_b64_tr_b16 a[220:221],v226 offset:33280\ns_waitcnt lgkmcnt(10)\nM v[56:59],a[120:123],a[224:227],v[56:59]\nds_read_b64_tr_b16 a[222:223],v226 offset:37376\nv_cvt_pk_bf16_f32 v216,v134,v142\nM v[60:63],a[124:127],a[224:227],v[60:63]\nds_read_b64_tr_b16 a[224:225],v227 offset:33280\n" \
"ds_read_b64_tr_b16 a[226:227],v227 offset:37376\nM v[72:75],a[120:123],a[228:231],v[72:75]\nv_cvt_pk_bf16_f32 v217,v150,v158\nv_cvt_pk_bf16_f32 v218,v166,v174\nM v[76:79],a[124:127],a[228:231],v[76:79]\nds_read_b64_tr_b16 a[228:229],v226 offset:33792\ns_waitcnt lgkmcnt(10)\nM v[88:91],a[120:123],a[232:235],v[88:91]\n" \
"ds_read_b64_tr_b16 a[230:231],v226 offset:37888\nv_cvt_pk_bf16_f32 v219,v182,v190\nM v[92:95],a[124:127],a[232:235],v[92:95]\nds_read_b64_tr_b16 a[232:233],v227 offset:33792\nds_read_b64_tr_b16 a[234:235],v227 offset:37888\nM v[104:107],a[120:123],a[236:239],v[104:107]\nv_cvt_pk_bf16_f32 v220,v135,v143\nv_cvt_pk_bf16_f32 v221,v151,v159\n" \
"M v[108:111],a[124:127],a[236:239],v[108:111]\nds_read_b64_tr_b16 a[236:237],v226 offset:34304\ns_waitcnt lgkmcnt(10)\nM v[120:123],a[120:123],a[240:243],v[120:123]\nds_read_b64_tr_b16 a[238:239],v226 offset:38400\nv_cvt_pk_bf16_f32 v222,v167,v175\nM v[124:127],a[124:127],a[240:243],v[124:127]\nds_read_b64_tr_b16 a[240:241],v227 offset:34304\n" \
"s_waitcnt vmcnt(9)\nM v[8:11],a[128:131],a[212:215],v[8:11]\nds_read_b64_tr_b16 a[242:243],v227 offset:38400\ns_waitcnt vmcnt(8)\nM v[12:15],a[132:135],a[212:215],v[12:15]\nds_read_b64_tr_b16 a[212:213],v226 offset:40960\ns_waitcnt lgkmcnt(10)\nM v[24:27],a[128:131],a[216:219],v[24:27]\n" \
"ds_read_b64_tr_b16 a[214:215],v226 offset:45056\nv_cvt_pk_bf16_f32 v223,v183,v191\nM v[28:31],a[132:135],a[216:219],v[28:31]\nds_read_b64_tr_b16 a[216:217],v227 offset:40960\nds_read_b64_tr_b16 a[218:219],v227 offset:45056\nM v[40:43],a[128:131],a[220:223],v[40:43]\nbuffer_store_dwordx4 v[192:195],v253,s[52:55],s70 offen offset:0 sc1\nM v[44:47],a[132:135],a[220:223],v[44:47]\n" \
"ds_read_b64_tr_b16 a[220:221],v226 offset:41472\ns_waitcnt lgkmcnt(10)\nM v[56:59],a[128:131],a[224:227],v[56:59]\nds_read_b64_tr_b16 a[222:223],v226 offset:45568\nv_add_u32 v234,s95,v233\nM v[60:63],a[132:135],a[224:227],v[60:63]\nds_read_b64_tr_b16 a[224:225],v227 offset:41472\nds_read_b64_tr_b16 a[226:227],v227 offset:45568\n" \
"M v[72:75],a[128:131],a[228:231],v[72:75]\nbuffer_store_dwordx4 v[196:199],v253,s[52:55],s70 offen offset:1024 sc1\nM v[76:79],a[132:135],a[228:231],v[76:79]\nds_read_b64_tr_b16 a[228:229],v226 offset:41984\ns_waitcnt lgkmcnt(10)\nM v[88:91],a[128:131],a[232:235],v[88:91]\nds_read_b64_tr_b16 a[230:231],v226 offset:46080\nv_add_u32 v235,s74,v233\n" \
"M v[92:95],a[132:135],a[232:235],v[92:95]\nbuffer_store_dwordx4 v[200:203],v253,s[52:55],s70 offen offset:2048 sc1\nM v[104:107],a[128:131],a[236:239],v[104:107]\nbuffer_store_dwordx4 v[204:207],v253,s[52:55],s70 offen offset:3072 sc1\nM v[108:111],a[132:135],a[236:239],v[108:111]\nds_read_b64_tr_b16 a[232:233],v227 offset:41984\ns_waitcnt lgkmcnt(8)\nM v[120:123],a[128:131],a[240:243],v[120:123]\n" \
"buffer_store_dwordx4 v[208:211],v253,s[52:55],s38 offen offset:0 sc1\nbuffer_store_dwordx4 v[212:215],v253,s[52:55],s38 offen offset:1024 sc1\nbuffer_store_dwordx4 v[216:219],v253,s[52:55],s38 offen offset:2048 sc1\nbuffer_store_dwordx4 v[220:223],v253,s[52:55],s38 offen offset:3072 sc1\nM v[124:127],a[132:135],a[240:243],v[124:127]\nds_read_b64_tr_b16 a[234:235],v227 offset:46080\ns_waitcnt vmcnt(15)\nM v[8:11],a[136:139],a[212:215],v[8:11]\n" \
"ds_read_b64_tr_b16 a[236:237],v226 offset:42496\ns_waitcnt vmcnt(14)\nM v[12:15],a[140:143],a[212:215],v[12:15]\nds_read_b64_tr_b16 a[238:239],v226 offset:46592\ns_waitcnt lgkmcnt(6)\nM v[24:27],a[136:139],a[216:219],v[24:27]\nbuffer_load_dwordx4 a[168:171],v240,s[52:55],s37 offen offset:2048 sc1\nM v[28:31],a[140:143],a[216:219],v[28:31]\n" \
"ds_read_b64_tr_b16 a[240:241],v227 offset:42496\nds_read_b64_tr_b16 a[242:243],v227 offset:46592\nM v[40:43],a[136:139],a[220:223],v[40:43]\nbuffer_load_dwordx4 a[172:175],v240,s[52:55],s37 offen offset:3072 sc1\nM v[44:47],a[140:143],a[220:223],v[44:47]\nds_read_b64_tr_b16 a[212:213],v226 offset:49152\nds_read_b64_tr_b16 a[214:215],v226 offset:53248\nM v[56:59],a[136:139],a[224:227],v[56:59]\n" \
"ds_read_b64_tr_b16 a[216:217],v227 offset:49152\nds_read_b64_tr_b16 a[218:219],v227 offset:53248\nM v[60:63],a[140:143],a[224:227],v[60:63]\nds_read_b64_tr_b16 a[220:221],v226 offset:49664\ns_waitcnt lgkmcnt(10)\nM v[72:75],a[136:139],a[228:231],v[72:75]\nds_read_b64_tr_b16 a[222:223],v226 offset:53760\nds_read_b64_tr_b16 a[224:225],v227 offset:49664\n" \
"M v[76:79],a[140:143],a[228:231],v[76:79]\nds_read_b64_tr_b16 a[226:227],v227 offset:53760\ns_waitcnt lgkmcnt(12)\nM v[88:91],a[136:139],a[232:235],v[88:91]\nds_read_b64_tr_b16 a[228:229],v226 offset:50176\nds_read_b64_tr_b16 a[230:231],v226 offset:54272\nM v[92:95],a[140:143],a[232:235],v[92:95]\nds_read_b64_tr_b16 a[232:233],v227 offset:50176\n" \
"s_waitcnt lgkmcnt(13)\nM v[104:107],a[136:139],a[236:239],v[104:107]\nds_read_b64_tr_b16 a[234:235],v227 offset:54272\ns_add_u32 s37,s37,4096\nM v[108:111],a[140:143],a[236:239],v[108:111]\nds_read_b64_tr_b16 a[236:237],v226 offset:50688\ns_waitcnt lgkmcnt(13)\nM v[120:123],a[136:139],a[240:243],v[120:123]\n" \
"ds_read_b64_tr_b16 a[238:239],v226 offset:54784\nv_lshlrev_b32 v236,1,v240\nM v[124:127],a[140:143],a[240:243],v[124:127]\nds_read_b64_tr_b16 a[240:241],v227 offset:50688\ns_waitcnt vmcnt(15) lgkmcnt(13)\nM v[8:11],a[144:147],a[212:215],v[8:11]\nds_read_b64_tr_b16 a[242:243],v227 offset:54784\ns_waitcnt vmcnt(14)\n" \
"M v[12:15],a[148:151],a[212:215],v[12:15]\nds_read_b64_tr_b16 a[212:213],v226 offset:57344\ns_waitcnt lgkmcnt(12)\nM v[24:27],a[144:147],a[216:219],v[24:27]\nbuffer_load_dwordx4 a[176:179],v240,s[52:55],s37 offen offset:0 sc1\nM v[28:31],a[148:151],a[216:219],v[28:31]\nds_read_b64_tr_b16 a[214:215],v226 offset:61440\ns_waitcnt lgkmcnt(10)\n" \
"M v[40:43],a[144:147],a[220:223],v[40:43]\nbuffer_load_dwordx4 a[180:183],v240,s[52:55],s37 offen offset:1024 sc1\nM v[44:47],a[148:151],a[220:223],v[44:47]\nds_read_b64_tr_b16 a[216:217],v227 offset:57344\nds_read_b64_tr_b16 a[218:219],v227 offset:61440\nM v[56:59],a[144:147],a[224:227],v[56:59]\nds_read_b64_tr_b16 a[220:221],v226 offset:57856\nds_read_b64_tr_b16 a[222:223],v226 offset:61952\n" \
"M v[60:63],a[148:151],a[224:227],v[60:63]\nds_read_b64_tr_b16 a[224:225],v227 offset:57856\ns_waitcnt lgkmcnt(10)\nM v[72:75],a[144:147],a[228:231],v[72:75]\nds_read_b64_tr_b16 a[226:227],v227 offset:61952\nv_lshl_add_u32 v237,v249,3,s72\nM v[76:79],a[148:151],a[228:231],v[76:79]\nds_read_b64_tr_b16 a[228:229],v226 offset:58368\n" \
"ds_read_b64_tr_b16 a[230:231],v226 offset:62464\nM v[88:91],a[144:147],a[232:235],v[88:91]\nbuffer_load_dwordx4 a[184:187],v240,s[52:55],s37 offen offset:2048 sc1\nM v[92:95],a[148:151],a[232:235],v[92:95]\nds_read_b64_tr_b16 a[232:233],v227 offset:58368\ns_waitcnt lgkmcnt(10)\nM v[104:107],a[144:147],a[236:239],v[104:107]\nds_read_b64_tr_b16 a[234:235],v227 offset:62464\n" \
"s_mov_b32 s97,4096\nM v[108:111],a[148:151],a[236:239],v[108:111]\nds_read_b64_tr_b16 a[236:237],v226 offset:58880\nds_read_b64_tr_b16 a[238:239],v226 offset:62976\nM v[120:123],a[144:147],a[240:243],v[120:123]\nbuffer_load_dwordx4 a[188:191],v240,s[52:55],s37 offen offset:3072 sc1\nM v[124:127],a[148:151],a[240:243],v[124:127]\nds_read_b64_tr_b16 a[240:241],v227 offset:58880\n" \
"s_waitcnt vmcnt(17) lgkmcnt(12)\nM v[8:11],a[152:155],a[212:215],v[8:11]\nds_read_b64_tr_b16 a[242:243],v227 offset:62976\ns_waitcnt vmcnt(16)\nM v[12:15],a[156:159],a[212:215],v[12:15]\nv_add_u32 v226,65536,v242\ns_waitcnt lgkmcnt(9)\nM v[24:27],a[152:155],a[216:219],v[24:27]\n" \
"ds_read_b64_tr_b16 a[212:213],v226 offset:0\nds_read_b64_tr_b16 a[214:215],v226 offset:4096\nM v[28:31],a[156:159],a[216:219],v[28:31]\nv_xor_b32 v227,32,v226\nds_read_b64_tr_b16 a[216:217],v227 offset:0\nM v[40:43],a[152:155],a[220:223],v[40:43]\nds_read_b64_tr_b16 a[218:219],v227 offset:4096\ns_add_u32 s38,s78,16384\n" \
"M v[44:47],a[156:159],a[220:223],v[44:47]\nds_read_b64_tr_b16 a[220:221],v226 offset:512\ns_waitcnt lgkmcnt(10)\nM v[56:59],a[152:155],a[224:227],v[56:59]\nds_read_b64_tr_b16 a[222:223],v226 offset:4608\nM v[60:63],a[156:159],a[224:227],v[60:63]\nds_read_b64_tr_b16 a[224:225],v227 offset:512\nds_read_b64_tr_b16 a[226:227],v227 offset:4608\n" \
"M v[72:75],a[152:155],a[228:231],v[72:75]\nglobal_load_ushort v128,v234,s[8:9] offset:0\nM v[76:79],a[156:159],a[228:231],v[76:79]\nds_read_b64_tr_b16 a[228:229],v226 offset:1024\ns_waitcnt lgkmcnt(11)\nM v[88:91],a[152:155],a[232:235],v[88:91]\nds_read_b64_tr_b16 a[230:231],v226 offset:5120\nM v[92:95],a[156:159],a[232:235],v[92:95]\n" \
"ds_read_b64_tr_b16 a[232:233],v227 offset:1024\nds_read_b64_tr_b16 a[234:235],v227 offset:5120\nM v[104:107],a[152:155],a[236:239],v[104:107]\nglobal_load_ushort v136,v235,s[8:9] offset:0\nM v[108:111],a[156:159],a[236:239],v[108:111]\nds_read_b64_tr_b16 a[236:237],v226 offset:1536\ns_waitcnt lgkmcnt(11)\nM v[120:123],a[152:155],a[240:243],v[120:123]\n" \
"ds_read_b64_tr_b16 a[238:239],v226 offset:5632\nM v[124:127],a[156:159],a[240:243],v[124:127]\nds_read_b64_tr_b16 a[240:241],v227 offset:1536\ns_waitcnt vmcnt(17)\nM v[8:11],a[160:163],a[212:215],v[8:11]\nds_read_b64_tr_b16 a[242:243],v227 offset:5632\ns_waitcnt vmcnt(16)\nM v[12:15],a[164:167],a[212:215],v[12:15]\n" \
"ds_read_b64_tr_b16 a[212:213],v226 offset:8192\ns_waitcnt lgkmcnt(11)\nM v[24:27],a[160:163],a[216:219],v[24:27]\nds_read_b64_tr_b16 a[214:215],v226 offset:12288\nM v[28:31],a[164:167],a[216:219],v[28:31]\nds_read_b64_tr_b16 a[216:217],v227 offset:8192\nds_read_b64_tr_b16 a[218:219],v227 offset:12288\nM v[40:43],a[160:163],a[220:223],v[40:43]\n" \
"global_load_ushort v144,v233,s[20:21] offset:0\nM v[44:47],a[164:167],a[220:223],v[44:47]\nds_read_b64_tr_b16 a[220:221],v226 offset:8704\ns_waitcnt lgkmcnt(11)\nM v[56:59],a[160:163],a[224:227],v[56:59]\nds_read_b64_tr_b16 a[222:223],v226 offset:12800\nM v[60:63],a[164:167],a[224:227],v[60:63]\nds_read_b64_tr_b16 a[224:225],v227 offset:8704\n" \
"ds_read_b64_tr_b16 a[226:227],v227 offset:12800\nM v[72:75],a[160:163],a[228:231],v[72:75]\nglobal_load_ushort v129,v234,s[8:9] offset:32\nM v[76:79],a[164:167],a[228:231],v[76:79]\nds_read_b64_tr_b16 a[228:229],v226 offset:9216\ns_waitcnt lgkmcnt(11)\nM v[88:91],a[160:163],a[232:235],v[88:91]\nds_read_b64_tr_b16 a[230:231],v226 offset:13312\n" \
"M v[92:95],a[164:167],a[232:235],v[92:95]\nds_read_b64_tr_b16 a[232:233],v227 offset:9216\nds_read_b64_tr_b16 a[234:235],v227 offset:13312\nM v[104:107],a[160:163],a[236:239],v[104:107]\nglobal_load_ushort v137,v235,s[8:9] offset:32\nM v[108:111],a[164:167],a[236:239],v[108:111]\nds_read_b64_tr_b16 a[236:237],v226 offset:9728\ns_waitcnt lgkmcnt(11)\n" \
"M v[120:123],a[160:163],a[240:243],v[120:123]\nds_read_b64_tr_b16 a[238:239],v226 offset:13824\nM v[124:127],a[164:167],a[240:243],v[124:127]\nds_read_b64_tr_b16 a[240:241],v227 offset:9728\ns_waitcnt vmcnt(10)\nM v[8:11],a[168:171],a[212:215],v[8:11]\nds_read_b64_tr_b16 a[242:243],v227 offset:13824\ns_waitcnt vmcnt(9)\n" \
"M v[12:15],a[172:175],a[212:215],v[12:15]\nds_read_b64_tr_b16 a[212:213],v226 offset:32768\ns_waitcnt lgkmcnt(11)\nM v[24:27],a[168:171],a[216:219],v[24:27]\nds_read_b64_tr_b16 a[214:215],v226 offset:36864\nM v[28:31],a[172:175],a[216:219],v[28:31]\nds_read_b64_tr_b16 a[216:217],v227 offset:32768\nds_read_b64_tr_b16 a[218:219],v227 offset:36864\n" \
"M v[40:43],a[168:171],a[220:223],v[40:43]\nbuffer_store_dword v254,v232,s[52:55],s71 offen sc1\nM v[44:47],a[172:175],a[220:223],v[44:47]\nds_read_b64_tr_b16 a[220:221],v226 offset:33280\ns_waitcnt lgkmcnt(11)\nM v[56:59],a[168:171],a[224:227],v[56:59]\nds_read_b64_tr_b16 a[222:223],v226 offset:37376\nM v[60:63],a[172:175],a[224:227],v[60:63]\n" \
"ds_read_b64_tr_b16 a[224:225],v227 offset:33280\nds_read_b64_tr_b16 a[226:227],v227 offset:37376\nM v[72:75],a[168:171],a[228:231],v[72:75]\nglobal_load_ushort v145,v233,s[20:21] offset:32\nM v[76:79],a[172:175],a[228:231],v[76:79]\nds_read_b64_tr_b16 a[228:229],v226 offset:33792\ns_waitcnt lgkmcnt(11)\nM v[88:91],a[168:171],a[232:235],v[88:91]\n" \
"ds_read_b64_tr_b16 a[230:231],v226 offset:37888\nM v[92:95],a[172:175],a[232:235],v[92:95]\nds_read_b64_tr_b16 a[232:233],v227 offset:33792\nds_read_b64_tr_b16 a[234:235],v227 offset:37888\nM v[104:107],a[168:171],a[236:239],v[104:107]\nglobal_load_ushort v130,v234,s[8:9] offset:64\nM v[108:111],a[172:175],a[236:239],v[108:111]\nds_read_b64_tr_b16 a[236:237],v226 offset:34304\n" \
"s_waitcnt lgkmcnt(11)\nM v[120:123],a[168:171],a[240:243],v[120:123]\nds_read_b64_tr_b16 a[238:239],v226 offset:38400\nM v[124:127],a[172:175],a[240:243],v[124:127]\nds_read_b64_tr_b16 a[240:241],v227 offset:34304\ns_waitcnt vmcnt(11)\nM v[8:11],a[176:179],a[212:215],v[8:11]\nds_read_b64_tr_b16 a[242:243],v227 offset:38400\n" \
"s_waitcnt vmcnt(10)\nM v[12:15],a[180:183],a[212:215],v[12:15]\nds_read_b64_tr_b16 a[212:213],v226 offset:40960\ns_waitcnt lgkmcnt(11)\nM v[24:27],a[176:179],a[216:219],v[24:27]\nds_read_b64_tr_b16 a[214:215],v226 offset:45056\nM v[28:31],a[180:183],a[216:219],v[28:31]\nds_read_b64_tr_b16 a[216:217],v227 offset:40960\n" \
"ds_read_b64_tr_b16 a[218:219],v227 offset:45056\nM v[40:43],a[176:179],a[220:223],v[40:43]\nglobal_load_ushort v138,v235,s[8:9] offset:64\nM v[44:47],a[180:183],a[220:223],v[44:47]\nds_read_b64_tr_b16 a[220:221],v226 offset:41472\ns_waitcnt lgkmcnt(11)\nM v[56:59],a[176:179],a[224:227],v[56:59]\nds_read_b64_tr_b16 a[222:223],v226 offset:45568\n" \
"M v[60:63],a[180:183],a[224:227],v[60:63]\nds_read_b64_tr_b16 a[224:225],v227 offset:41472\nds_read_b64_tr_b16 a[226:227],v227 offset:45568\nM v[72:75],a[176:179],a[228:231],v[72:75]\nglobal_load_ushort v146,v233,s[20:21] offset:64\nM v[76:79],a[180:183],a[228:231],v[76:79]\nds_read_b64_tr_b16 a[228:229],v226 offset:41984\ns_waitcnt lgkmcnt(11)\n" \
"M v[88:91],a[176:179],a[232:235],v[88:91]\nds_read_b64_tr_b16 a[230:231],v226 offset:46080\nM v[92:95],a[180:183],a[232:235],v[92:95]\nds_read_b64_tr_b16 a[232:233],v227 offset:41984\nds_read_b64_tr_b16 a[234:235],v227 offset:46080\nM v[104:107],a[176:179],a[236:239],v[104:107]\nglobal_load_ushort v131,v234,s[8:9] offset:96\nM v[108:111],a[180:183],a[236:239],v[108:111]\n" \
"ds_read_b64_tr_b16 a[236:237],v226 offset:42496\ns_waitcnt lgkmcnt(11)\nM v[120:123],a[176:179],a[240:243],v[120:123]\nds_read_b64_tr_b16 a[238:239],v226 offset:46592\nM v[124:127],a[180:183],a[240:243],v[124:127]\nds_read_b64_tr_b16 a[240:241],v227 offset:42496\ns_waitcnt vmcnt(12)\nM v[8:11],a[184:187],a[212:215],v[8:11]\n" \
"ds_read_b64_tr_b16 a[242:243],v227 offset:46592\ns_waitcnt vmcnt(11)\nM v[12:15],a[188:191],a[212:215],v[12:15]\ns_waitcnt lgkmcnt(10)\nM v[24:27],a[184:187],a[216:219],v[24:27]\nglobal_load_ushort v139,v235,s[8:9] offset:96\nM v[28:31],a[188:191],a[216:219],v[28:31]\nglobal_load_ushort v147,v233,s[20:21] offset:96\n" \
"M v[40:43],a[184:187],a[220:223],v[40:43]\nglobal_load_ushort v132,v234,s[8:9] offset:128\nM v[44:47],a[188:191],a[220:223],v[44:47]\ns_waitcnt lgkmcnt(6)\nM v[56:59],a[184:187],a[224:227],v[56:59]\nglobal_load_ushort v140,v235,s[8:9] offset:128\nM v[60:63],a[188:191],a[224:227],v[60:63]\nglobal_load_ushort v148,v233,s[20:21] offset:128\n" \
"M v[72:75],a[184:187],a[228:231],v[72:75]\nglobal_load_ushort v133,v234,s[8:9] offset:160\nglobal_load_ushort v141,v235,s[8:9] offset:160\nglobal_load_ushort v149,v233,s[20:21] offset:160\nglobal_load_ushort v134,v234,s[8:9] offset:192\nglobal_load_ushort v142,v235,s[8:9] offset:192\nglobal_load_ushort v150,v233,s[20:21] offset:192\nglobal_load_ushort v135,v234,s[8:9] offset:224\n" \
"global_load_ushort v143,v235,s[8:9] offset:224\nglobal_load_ushort v151,v233,s[20:21] offset:224\nbuffer_load_dwordx4 v[152:155],v236,s[52:55],0 offen offset:0 sc1\nbuffer_load_dwordx4 v[156:159],v236,s[52:55],0 offen offset:16 sc1\nbuffer_load_dwordx4 v[160:163],v236,s[52:55],0 offen offset:2048 sc1\nbuffer_load_dwordx4 v[164:167],v236,s[52:55],0 offen offset:2064 sc1\nbuffer_load_dwordx4 v[168:171],v236,s[52:55],s97 offen offset:0 sc1\nbuffer_load_dwordx4 v[172:175],v236,s[52:55],s97 offen offset:16 sc1\n" \
"buffer_load_dwordx4 v[176:179],v236,s[52:55],s97 offen offset:2048 sc1\nbuffer_load_dwordx4 v[180:183],v236,s[52:55],s97 offen offset:2064 sc1\nbuffer_load_dwordx2 v[184:185],v237,s[52:55],0 offen offset:0 sc1\nbuffer_load_dwordx2 v[186:187],v237,s[52:55],0 offen offset:2048 sc1\nbuffer_load_dwordx2 v[188:189],v237,s[52:55],s97 offen offset:0 sc1\nbuffer_load_dwordx2 v[190:191],v237,s[52:55],s97 offen offset:2048 sc1\nM v[76:79],a[188:191],a[228:231],v[76:79]\nv_cvt_pk_bf16_f32 v192,v8,v24\n" \
"s_waitcnt lgkmcnt(0)\nM v[88:91],a[184:187],a[232:235],v[88:91]\nv_cvt_pk_bf16_f32 v193,v40,v56\nv_cvt_pk_bf16_f32 v196,v9,v25\nM v[92:95],a[188:191],a[232:235],v[92:95]\nv_cvt_pk_bf16_f32 v197,v41,v57\nv_cvt_pk_bf16_f32 v200,v10,v26\nM v[104:107],a[184:187],a[236:239],v[104:107]\n" \
"v_cvt_pk_bf16_f32 v201,v42,v58\nv_cvt_pk_bf16_f32 v204,v11,v27\nM v[108:111],a[188:191],a[236:239],v[108:111]\nv_cvt_pk_bf16_f32 v194,v72,v88\nv_cvt_pk_bf16_f32 v198,v73,v89\nM v[120:123],a[184:187],a[240:243],v[120:123]\nv_cvt_pk_bf16_f32 v202,v74,v90\nv_cvt_pk_bf16_f32 v205,v43,v59\n" \
"v_cvt_pk_bf16_f32 v206,v75,v91\nv_cvt_pk_bf16_f32 v208,v12,v28\nv_cvt_pk_bf16_f32 v209,v44,v60\nv_cvt_pk_bf16_f32 v210,v76,v92\nv_cvt_pk_bf16_f32 v212,v13,v29\nv_cvt_pk_bf16_f32 v213,v45,v61\nv_cvt_pk_bf16_f32 v195,v104,v120\nv_cvt_pk_bf16_f32 v199,v105,v121\n" \
"v_cvt_pk_bf16_f32 v203,v106,v122\nv_cvt_pk_bf16_f32 v207,v107,v123\nv_cvt_pk_bf16_f32 v214,v77,v93\nv_cvt_pk_bf16_f32 v216,v14,v30\nv_cvt_pk_bf16_f32 v217,v46,v62\nv_cvt_pk_bf16_f32 v218,v78,v94\nv_cvt_pk_bf16_f32 v220,v15,v31\nv_cvt_pk_bf16_f32 v221,v47,v63\n" \
"v_cvt_pk_bf16_f32 v222,v79,v95\nM v[124:127],a[188:191],a[240:243],v[124:127]\nv_min_u32 v192,-2,v192\nv_min_u32 v193,-2,v193\nv_min_u32 v194,-2,v194\nv_min_u32 v195,-2,v195\nv_min_u32 v196,-2,v196\nv_min_u32 v197,-2,v197\n" \
"v_min_u32 v198,-2,v198\nv_min_u32 v199,-2,v199\nv_cvt_pk_bf16_f32 v211,v108,v124\nv_cvt_pk_bf16_f32 v215,v109,v125\nv_cvt_pk_bf16_f32 v219,v110,v126\nv_cvt_pk_bf16_f32 v223,v111,v127\nv_min_u32 v200,-2,v200\nv_min_u32 v201,-2,v201\n" \
"v_min_u32 v202,-2,v202\nv_min_u32 v203,-2,v203\nv_min_u32 v204,-2,v204\nv_min_u32 v205,-2,v205\nv_min_u32 v206,-2,v206\nv_min_u32 v207,-2,v207\nv_min_u32 v208,-2,v208\nv_min_u32 v209,-2,v209\n" \
"v_min_u32 v210,-2,v210\nv_min_u32 v211,-2,v211\nv_min_u32 v212,-2,v212\nv_min_u32 v213,-2,v213\nv_min_u32 v214,-2,v214\nv_min_u32 v215,-2,v215\nv_min_u32 v216,-2,v216\nv_min_u32 v217,-2,v217\n" \
"v_min_u32 v218,-2,v218\nv_min_u32 v219,-2,v219\nv_min_u32 v220,-2,v220\nv_min_u32 v221,-2,v221\nv_min_u32 v222,-2,v222\nv_min_u32 v223,-2,v223\nbuffer_store_dwordx4 v[192:195],v253,s[52:55],s78 offen offset:0 sc1\nbuffer_store_dwordx4 v[196:199],v253,s[52:55],s78 offen offset:1024 sc1\n" \
"buffer_store_dwordx4 v[200:203],v253,s[52:55],s78 offen offset:2048 sc1\nbuffer_store_dwordx4 v[204:207],v253,s[52:55],s78 offen offset:3072 sc1\nbuffer_store_dwordx4 v[208:211],v253,s[52:55],s38 offen offset:0 sc1\nbuffer_store_dwordx4 v[212:215],v253,s[52:55],s38 offen offset:1024 sc1\nbuffer_store_dwordx4 v[216:219],v253,s[52:55],s38 offen offset:2048 sc1\nbuffer_store_dwordx4 v[220:223],v253,s[52:55],s38 offen offset:3072 sc1\ns_waitcnt vmcnt(8)\ns_nop 0\n" \
"s_branch .Lfin\n.Lhs:\ns_sub_u32 s73,s73,1\ns_cbranch_scc1 .Lhb\ns_sleep 2\nbuffer_load_dwordx2 v[166:167],v165,s[52:55],s80 offen sc1\ns_waitcnt vmcnt(0)\nv_xor_b32 v166,v166,v230\n" \
"v_xor_b32 v167,v167,v231\nv_or_b32 v166,v166,v167\nv_cmp_ne_u32 vcc,0,v166\ns_nop 0\ns_cbranch_vccnz .Lhs\ns_branch .Lhb\n.Lps0:\ns_sub_u32 s73,s73,1\n" \
"s_cbranch_scc1 .Lpb0\ns_sleep 1\nbuffer_load_dword v248,v228,s[52:55],s80 offen sc1\ns_waitcnt vmcnt(0)\nv_cmp_eq_u32 vcc,0,v248\ns_nop 0\ns_cbranch_vccnz .Lps0\ns_branch .Lpb0\n" \
".Lps1:\ns_sub_u32 s73,s73,1\ns_cbranch_scc1 .Lpb1\ns_sleep 1\nbuffer_load_dword v248,v228,s[52:55],s80 offen sc1\ns_waitcnt vmcnt(0)\nv_cmp_eq_u32 vcc,0,v248\ns_nop 0\n" \
"s_cbranch_vccnz .Lps1\ns_branch .Lpb1\n.Lps2:\ns_sub_u32 s73,s73,1\ns_cbranch_scc1 .Lpb2\ns_sleep 1\nbuffer_load_dword v248,v251,s[52:55],s80 offen sc1\ns_waitcnt vmcnt(0)\n" \
"v_cmp_eq_u32 vcc,0,v248\ns_nop 0\ns_cbranch_vccnz .Lps2\ns_branch .Lpb2\n.Lps3:\ns_sub_u32 s73,s73,1\ns_cbranch_scc1 .Lpb3\ns_sleep 1\n" \
"buffer_load_dword v248,v251,s[52:55],s80 offen sc1\ns_waitcnt vmcnt(0)\nv_cmp_eq_u32 vcc,0,v248\ns_nop 0\ns_cbranch_vccnz .Lps3\ns_branch .Lpb3\n.Lps4:\ns_sub_u32 s73,s73,1\n" \
"s_cbranch_scc1 .Lpb4\ns_sleep 1\nbuffer_load_dword v248,v251,s[52:55],s80 offen sc1\ns_waitcnt vmcnt(0)\nv_cmp_eq_u32 vcc,0,v248\ns_nop 0\ns_cbranch_vccnz .Lps4\ns_branch .Lpb4\n" \
".Lps5:\ns_sub_u32 s73,s73,1\ns_cbranch_scc1 .Lpb5\ns_sleep 1\nbuffer_load_dword v248,v251,s[52:55],s80 offen sc1\ns_waitcnt vmcnt(0)\nv_cmp_eq_u32 vcc,0,v248\ns_nop 0\n" \
"s_cbranch_vccnz .Lps5\ns_branch .Lpb5\n.Lps6:\ns_sub_u32 s73,s73,1\ns_cbranch_scc1 .Lpb6\ns_sleep 1\nbuffer_load_dword v248,v251,s[52:55],s80 offen sc1\ns_waitcnt vmcnt(0)\n" \
"v_cmp_eq_u32 vcc,0,v248\ns_nop 0\ns_cbranch_vccnz .Lps6\ns_branch .Lpb6\n.Lps7:\ns_sub_u32 s73,s73,1\ns_cbranch_scc1 .Lpb7\ns_sleep 1\n" \
"buffer_load_dword v248,v251,s[52:55],s79 offen sc1\ns_waitcnt vmcnt(0)\nv_cmp_eq_u32 vcc,0,v248\ns_nop 0\ns_cbranch_vccnz .Lps7\ns_branch .Lpb7\n.Lredo:\ns_waitcnt vmcnt(0) lgkmcnt(0)\n" \
"s_barrier\ns_mov_b64 s[90:91],0\ns_mov_b32 s99,0\ns_lshl_b32 s70,s63,19\ns_add_u32 s37,s70,3145728\ns_lshl_b32 s71,s62,15\ns_add_u32 s72,s37,s64\ns_add_u32 s72,s72,491520\n" \
"s_sub_u32 s72,s72,s71\ns_add_u32 m0,s64,0\ns_nop 0\nbuffer_load_dwordx4 v240,s[52:55],s72 offen offset:0 sc1 lds\nbuffer_load_dwordx4 v240,s[52:55],s72 offen offset:1024 sc1 lds\nbuffer_load_dwordx4 v240,s[52:55],s72 offen offset:2048 sc1 lds\nbuffer_load_dwordx4 v240,s[52:55],s72 offen offset:3072 sc1 lds\ns_add_u32 m0,s64,65536\n" \
"s_add_u32 s72,s72,16777216\nbuffer_load_dwordx4 v240,s[52:55],s72 offen offset:0 sc1 lds\nbuffer_load_dwordx4 v240,s[52:55],s72 offen offset:1024 sc1 lds\nbuffer_load_dwordx4 v240,s[52:55],s72 offen offset:2048 sc1 lds\nbuffer_load_dwordx4 v240,s[52:55],s72 offen offset:3072 sc1 lds\ns_add_u32 s72,s37,s64\ns_add_u32 s72,s72,507904\ns_sub_u32 s72,s72,s71\n" \
"s_add_u32 m0,s64,16384\ns_nop 0\nbuffer_load_dwordx4 v240,s[52:55],s72 offen offset:0 sc1 lds\nbuffer_load_dwordx4 v240,s[52:55],s72 offen offset:1024 sc1 lds\nbuffer_load_dwordx4 v240,s[52:55],s72 offen offset:2048 sc1 lds\nbuffer_load_dwordx4 v240,s[52:55],s72 offen offset:3072 sc1 lds\ns_add_u32 m0,s64,81920\ns_add_u32 s72,s72,16777216\n" \
"buffer_load_dwordx4 v240,s[52:55],s72 offen offset:0 sc1 lds\nbuffer_load_dwordx4 v240,s[52:55],s72 offen offset:1024 sc1 lds\nbuffer_load_dwordx4 v240,s[52:55],s72 offen offset:2048 sc1 lds\nbuffer_load_dwordx4 v240,s[52:55],s72 offen offset:3072 sc1 lds\ns_add_u32 s72,s37,s64\ns_add_u32 s72,s72,16384\ns_add_u32 s72,s72,s71\ns_add_u32 m0,s64,32768\n" \
"s_nop 0\nbuffer_load_dwordx4 v240,s[52:55],s72 offen offset:0 sc1 lds\nbuffer_load_dwordx4 v240,s[52:55],s72 offen offset:1024 sc1 lds\nbuffer_load_dwordx4 v240,s[52:55],s72 offen offset:2048 sc1 lds\nbuffer_load_dwordx4 v240,s[52:55],s72 offen offset:3072 sc1 lds\ns_add_u32 m0,s64,98304\ns_add_u32 s72,s72,16777216\nbuffer_load_dwordx4 v240,s[52:55],s72 offen offset:0 sc1 lds\n" \
"buffer_load_dwordx4 v240,s[52:55],s72 offen offset:1024 sc1 lds\nbuffer_load_dwordx4 v240,s[52:55],s72 offen offset:2048 sc1 lds\nbuffer_load_dwordx4 v240,s[52:55],s72 offen offset:3072 sc1 lds\ns_add_u32 s72,s37,s64\ns_add_u32 s72,s72,0\ns_add_u32 s72,s72,s71\ns_add_u32 m0,s64,49152\ns_nop 0\n" \
"buffer_load_dwordx4 v240,s[52:55],s72 offen offset:0 sc1 lds\nbuffer_load_dwordx4 v240,s[52:55],s72 offen offset:1024 sc1 lds\nbuffer_load_dwordx4 v240,s[52:55],s72 offen offset:2048 sc1 lds\nbuffer_load_dwordx4 v240,s[52:55],s72 offen offset:3072 sc1 lds\ns_add_u32 m0,s64,114688\ns_add_u32 s72,s72,16777216\nbuffer_load_dwordx4 v240,s[52:55],s72 offen offset:0 sc1 lds\nbuffer_load_dwordx4 v240,s[52:55],s72 offen offset:1024 sc1 lds\n" \
"buffer_load_dwordx4 v240,s[52:55],s72 offen offset:2048 sc1 lds\nbuffer_load_dwordx4 v240,s[52:55],s72 offen offset:3072 sc1 lds\nv_and_b32 v251,15,v249\nv_lshrrev_b32 v250,4,v249\nv_lshlrev_b32 v250,2,v250\nv_sub_u32 v251,v251,v250\ns_lshl_b32 s70,s62,7\ns_lshl_b32 s72,s7,5\n" \
"s_sub_u32 s38,s72,s70\ns_add_u32 s38,s38,1920\ns_sub_u32 s39,s70,s72\ns_add_u32 s39,s39,96\nv_add_u32 v225,s38,v251\nv_add_u32 v225,0,v225\nv_mov_b32 v224,0xff800000\nv_add_u32 v229,s38,v251\n" \
"v_add_u32 v229,16,v229\nv_mov_b32 v228,0xff800000\nv_add_u32 v233,s39,v251\nv_add_u32 v233,0,v233\nv_mov_b32 v232,0xff800000\nv_add_u32 v237,s39,v251\nv_add_u32 v237,16,v237\nv_mov_b32 v236,0xff800000\n" \
"s_sub_u32 s38,524288,s71\ns_add_u32 s38,s38,s37\n.Lrl:\nbuffer_load_dwordx4 a[0:3],v240,s[52:55],s37 offen offset:0 sc1\nbuffer_load_dwordx4 a[4:7],v240,s[52:55],s37 offen offset:1024 sc1\nbuffer_load_dwordx4 a[8:11],v240,s[52:55],s37 offen offset:2048 sc1\nbuffer_load_dwordx4 a[12:15],v240,s[52:55],s37 offen offset:3072 sc1\ns_add_u32 s37,s37,4096\n" \
"buffer_load_dwordx4 a[16:19],v240,s[52:55],s37 offen offset:0 sc1\nbuffer_load_dwordx4 a[20:23],v240,s[52:55],s37 offen offset:1024 sc1\nbuffer_load_dwordx4 a[24:27],v240,s[52:55],s37 offen offset:2048 sc1\nbuffer_load_dwordx4 a[28:31],v240,s[52:55],s37 offen offset:3072 sc1\ns_add_u32 s37,s37,4096\nbuffer_load_dwordx4 a[32:35],v240,s[52:55],s37 offen offset:0 sc1\nbuffer_load_dwordx4 a[36:39],v240,s[52:55],s37 offen offset:1024 sc1\nbuffer_load_dwordx4 a[40:43],v240,s[52:55],s37 offen offset:2048 sc1\n" \
"buffer_load_dwordx4 a[44:47],v240,s[52:55],s37 offen offset:3072 sc1\ns_add_u32 s37,s37,4096\nbuffer_load_dwordx4 a[48:51],v240,s[52:55],s37 offen offset:0 sc1\nbuffer_load_dwordx4 a[52:55],v240,s[52:55],s37 offen offset:1024 sc1\nbuffer_load_dwordx4 a[56:59],v240,s[52:55],s37 offen offset:2048 sc1\nbuffer_load_dwordx4 a[60:63],v240,s[52:55],s37 offen offset:3072 sc1\ns_add_u32 s37,s37,4096\ns_waitcnt vmcnt(15)\n" \
"M v[128:131],a[0:3],a[128:131],0\ns_waitcnt vmcnt(14)\nM v[128:131],a[4:7],a[132:135],v[128:131]\ns_waitcnt vmcnt(13)\nM v[128:131],a[8:11],a[136:139],v[128:131]\ns_waitcnt vmcnt(12)\nM v[128:131],a[12:15],a[140:143],v[128:131]\nM v[132:135],a[0:3],a[144:147],0\n" \
"M v[132:135],a[4:7],a[148:151],v[132:135]\nM v[132:135],a[8:11],a[152:155],v[132:135]\nM v[132:135],a[12:15],a[156:159],v[132:135]\nM v[136:139],a[0:3],a[160:163],0\nM v[136:139],a[4:7],a[164:167],v[136:139]\nM v[136:139],a[8:11],a[168:171],v[136:139]\nM v[136:139],a[12:15],a[172:175],v[136:139]\nM v[140:143],a[0:3],a[176:179],0\n" \
"M v[140:143],a[4:7],a[180:183],v[140:143]\nM v[140:143],a[8:11],a[184:187],v[140:143]\nM v[140:143],a[12:15],a[188:191],v[140:143]\ns_waitcnt vmcnt(11)\nM v[144:147],a[16:19],a[128:131],0\ns_waitcnt vmcnt(10)\nM v[144:147],a[20:23],a[132:135],v[144:147]\ns_waitcnt vmcnt(9)\n" \
"M v[144:147],a[24:27],a[136:139],v[144:147]\ns_waitcnt vmcnt(8)\nM v[144:147],a[28:31],a[140:143],v[144:147]\nM v[148:151],a[16:19],a[144:147],0\nM v[148:151],a[20:23],a[148:151],v[148:151]\nM v[148:151],a[24:27],a[152:155],v[148:151]\nM v[148:151],a[28:31],a[156:159],v[148:151]\nM v[152:155],a[16:19],a[160:163],0\n" \
"M v[152:155],a[20:23],a[164:167],v[152:155]\nM v[152:155],a[24:27],a[168:171],v[152:155]\nM v[152:155],a[28:31],a[172:175],v[152:155]\nM v[156:159],a[16:19],a[176:179],0\nM v[156:159],a[20:23],a[180:183],v[156:159]\nM v[156:159],a[24:27],a[184:187],v[156:159]\nM v[156:159],a[28:31],a[188:191],v[156:159]\ns_waitcnt vmcnt(7)\n" \
"M v[160:163],a[32:35],a[128:131],0\ns_waitcnt vmcnt(6)\nM v[160:163],a[36:39],a[132:135],v[160:163]\ns_waitcnt vmcnt(5)\nM v[160:163],a[40:43],a[136:139],v[160:163]\ns_waitcnt vmcnt(4)\nM v[160:163],a[44:47],a[140:143],v[160:163]\nM v[164:167],a[32:35],a[144:147],0\n" \
"M v[164:167],a[36:39],a[148:151],v[164:167]\nM v[164:167],a[40:43],a[152:155],v[164:167]\nM v[164:167],a[44:47],a[156:159],v[164:167]\nM v[168:171],a[32:35],a[160:163],0\nM v[168:171],a[36:39],a[164:167],v[168:171]\nM v[168:171],a[40:43],a[168:171],v[168:171]\nM v[168:171],a[44:47],a[172:175],v[168:171]\nM v[172:175],a[32:35],a[176:179],0\n" \
"M v[172:175],a[36:39],a[180:183],v[172:175]\nM v[172:175],a[40:43],a[184:187],v[172:175]\nM v[172:175],a[44:47],a[188:191],v[172:175]\ns_waitcnt vmcnt(3)\nM v[176:179],a[48:51],a[128:131],0\ns_waitcnt vmcnt(2)\nM v[176:179],a[52:55],a[132:135],v[176:179]\ns_waitcnt vmcnt(1)\n" \
"M v[176:179],a[56:59],a[136:139],v[176:179]\ns_waitcnt vmcnt(0)\nM v[176:179],a[60:63],a[140:143],v[176:179]\nM v[180:183],a[48:51],a[144:147],0\nM v[180:183],a[52:55],a[148:151],v[180:183]\nM v[180:183],a[56:59],a[152:155],v[180:183]\nM v[180:183],a[60:63],a[156:159],v[180:183]\nM v[184:187],a[48:51],a[160:163],0\n" \
"M v[184:187],a[52:55],a[164:167],v[184:187]\nM v[184:187],a[56:59],a[168:171],v[184:187]\nM v[184:187],a[60:63],a[172:175],v[184:187]\nM v[188:191],a[48:51],a[176:179],0\nM v[188:191],a[52:55],a[180:183],v[188:191]\nM v[188:191],a[56:59],a[184:187],v[188:191]\nM v[188:191],a[60:63],a[188:191],v[188:191]\nv_cmp_le_i32 vcc,0,v225\n" \
"s_nop 1\nv_cndmask_b32 v128,v252,v128,vcc\nv_max_f32 v224,v224,v128\nv_cmp_le_i32 vcc,1,v225\ns_nop 1\nv_cndmask_b32 v129,v252,v129,vcc\nv_max_f32 v224,v224,v129\nv_cmp_le_i32 vcc,2,v225\n" \
"s_nop 1\nv_cndmask_b32 v130,v252,v130,vcc\nv_max_f32 v224,v224,v130\nv_cmp_le_i32 vcc,3,v225\ns_nop 1\nv_cndmask_b32 v131,v252,v131,vcc\nv_max_f32 v224,v224,v131\nv_cmp_le_i32 vcc,0,v229\n" \
"s_nop 1\nv_cndmask_b32 v132,v252,v132,vcc\nv_max_f32 v228,v228,v132\nv_cmp_le_i32 vcc,1,v229\ns_nop 1\nv_cndmask_b32 v133,v252,v133,vcc\nv_max_f32 v228,v228,v133\nv_cmp_le_i32 vcc,2,v229\n" \
"s_nop 1\nv_cndmask_b32 v134,v252,v134,vcc\nv_max_f32 v228,v228,v134\nv_cmp_le_i32 vcc,3,v229\ns_nop 1\nv_cndmask_b32 v135,v252,v135,vcc\nv_max_f32 v228,v228,v135\nv_cmp_le_i32 vcc,0,v233\n" \
"s_nop 1\nv_cndmask_b32 v136,v252,v136,vcc\nv_max_f32 v232,v232,v136\nv_cmp_le_i32 vcc,1,v233\ns_nop 1\nv_cndmask_b32 v137,v252,v137,vcc\nv_max_f32 v232,v232,v137\nv_cmp_le_i32 vcc,2,v233\n" \
"s_nop 1\nv_cndmask_b32 v138,v252,v138,vcc\nv_max_f32 v232,v232,v138\nv_cmp_le_i32 vcc,3,v233\ns_nop 1\nv_cndmask_b32 v139,v252,v139,vcc\nv_max_f32 v232,v232,v139\nv_cmp_le_i32 vcc,0,v237\n" \
"s_nop 1\nv_cndmask_b32 v140,v252,v140,vcc\nv_max_f32 v236,v236,v140\nv_cmp_le_i32 vcc,1,v237\ns_nop 1\nv_cndmask_b32 v141,v252,v141,vcc\nv_max_f32 v236,v236,v141\nv_cmp_le_i32 vcc,2,v237\n" \
"s_nop 1\nv_cndmask_b32 v142,v252,v142,vcc\nv_max_f32 v236,v236,v142\nv_cmp_le_i32 vcc,3,v237\ns_nop 1\nv_cndmask_b32 v143,v252,v143,vcc\nv_max_f32 v236,v236,v143\nv_cmp_le_i32 vcc,16,v225\n" \
"s_nop 1\nv_cndmask_b32 v144,v252,v144,vcc\nv_max_f32 v224,v224,v144\nv_cmp_le_i32 vcc,17,v225\ns_nop 1\nv_cndmask_b32 v145,v252,v145,vcc\nv_max_f32 v224,v224,v145\nv_cmp_le_i32 vcc,18,v225\n" \
"s_nop 1\nv_cndmask_b32 v146,v252,v146,vcc\nv_max_f32 v224,v224,v146\nv_cmp_le_i32 vcc,19,v225\ns_nop 1\nv_cndmask_b32 v147,v252,v147,vcc\nv_max_f32 v224,v224,v147\nv_cmp_le_i32 vcc,16,v229\n" \
"s_nop 1\nv_cndmask_b32 v148,v252,v148,vcc\nv_max_f32 v228,v228,v148\nv_cmp_le_i32 vcc,17,v229\ns_nop 1\nv_cndmask_b32 v149,v252,v149,vcc\nv_max_f32 v228,v228,v149\nv_cmp_le_i32 vcc,18,v229\n" \
"s_nop 1\nv_cndmask_b32 v150,v252,v150,vcc\nv_max_f32 v228,v228,v150\nv_cmp_le_i32 vcc,19,v229\ns_nop 1\nv_cndmask_b32 v151,v252,v151,vcc\nv_max_f32 v228,v228,v151\nv_cmp_le_i32 vcc,16,v233\n" \
"s_nop 1\nv_cndmask_b32 v152,v252,v152,vcc\nv_max_f32 v232,v232,v152\nv_cmp_le_i32 vcc,17,v233\ns_nop 1\nv_cndmask_b32 v153,v252,v153,vcc\nv_max_f32 v232,v232,v153\nv_cmp_le_i32 vcc,18,v233\n" \
"s_nop 1\nv_cndmask_b32 v154,v252,v154,vcc\nv_max_f32 v232,v232,v154\nv_cmp_le_i32 vcc,19,v233\ns_nop 1\nv_cndmask_b32 v155,v252,v155,vcc\nv_max_f32 v232,v232,v155\nv_cmp_le_i32 vcc,16,v237\n" \
"s_nop 1\nv_cndmask_b32 v156,v252,v156,vcc\nv_max_f32 v236,v236,v156\nv_cmp_le_i32 vcc,17,v237\ns_nop 1\nv_cndmask_b32 v157,v252,v157,vcc\nv_max_f32 v236,v236,v157\nv_cmp_le_i32 vcc,18,v237\n" \
"s_nop 1\nv_cndmask_b32 v158,v252,v158,vcc\nv_max_f32 v236,v236,v158\nv_cmp_le_i32 vcc,19,v237\ns_nop 1\nv_cndmask_b32 v159,v252,v159,vcc\nv_max_f32 v236,v236,v159\nv_cmp_le_i32 vcc,32,v225\n" \
"s_nop 1\nv_cndmask_b32 v160,v252,v160,vcc\nv_max_f32 v224,v224,v160\nv_cmp_le_i32 vcc,33,v225\ns_nop 1\nv_cndmask_b32 v161,v252,v161,vcc\nv_max_f32 v224,v224,v161\nv_cmp_le_i32 vcc,34,v225\n" \
"s_nop 1\nv_cndmask_b32 v162,v252,v162,vcc\nv_max_f32 v224,v224,v162\nv_cmp_le_i32 vcc,35,v225\ns_nop 1\nv_cndmask_b32 v163,v252,v163,vcc\nv_max_f32 v224,v224,v163\nv_cmp_le_i32 vcc,32,v229\n" \
"s_nop 1\nv_cndmask_b32 v164,v252,v164,vcc\nv_max_f32 v228,v228,v164\nv_cmp_le_i32 vcc,33,v229\ns_nop 1\nv_cndmask_b32 v165,v252,v165,vcc\nv_max_f32 v228,v228,v165\nv_cmp_le_i32 vcc,34,v229\n" \
"s_nop 1\nv_cndmask_b32 v166,v252,v166,vcc\nv_max_f32 v228,v228,v166\nv_cmp_le_i32 vcc,35,v229\ns_nop 1\nv_cndmask_b32 v167,v252,v167,vcc\nv_max_f32 v228,v228,v167\nv_cmp_le_i32 vcc,32,v233\n" \
"s_nop 1\nv_cndmask_b32 v168,v252,v168,vcc\nv_max_f32 v232,v232,v168\nv_cmp_le_i32 vcc,33,v233\ns_nop 1\nv_cndmask_b32 v169,v252,v169,vcc\nv_max_f32 v232,v232,v169\nv_cmp_le_i32 vcc,34,v233\n" \
"s_nop 1\nv_cndmask_b32 v170,v252,v170,vcc\nv_max_f32 v232,v232,v170\nv_cmp_le_i32 vcc,35,v233\ns_nop 1\nv_cndmask_b32 v171,v252,v171,vcc\nv_max_f32 v232,v232,v171\nv_cmp_le_i32 vcc,32,v237\n" \
"s_nop 1\nv_cndmask_b32 v172,v252,v172,vcc\nv_max_f32 v236,v236,v172\nv_cmp_le_i32 vcc,33,v237\ns_nop 1\nv_cndmask_b32 v173,v252,v173,vcc\nv_max_f32 v236,v236,v173\nv_cmp_le_i32 vcc,34,v237\n" \
"s_nop 1\nv_cndmask_b32 v174,v252,v174,vcc\nv_max_f32 v236,v236,v174\nv_cmp_le_i32 vcc,35,v237\ns_nop 1\nv_cndmask_b32 v175,v252,v175,vcc\nv_max_f32 v236,v236,v175\nv_cmp_le_i32 vcc,48,v225\n" \
"s_nop 1\nv_cndmask_b32 v176,v252,v176,vcc\nv_max_f32 v224,v224,v176\nv_cmp_le_i32 vcc,49,v225\ns_nop 1\nv_cndmask_b32 v177,v252,v177,vcc\nv_max_f32 v224,v224,v177\nv_cmp_le_i32 vcc,50,v225\n" \
"s_nop 1\nv_cndmask_b32 v178,v252,v178,vcc\nv_max_f32 v224,v224,v178\nv_cmp_le_i32 vcc,51,v225\ns_nop 1\nv_cndmask_b32 v179,v252,v179,vcc\nv_max_f32 v224,v224,v179\nv_cmp_le_i32 vcc,48,v229\n" \
"s_nop 1\nv_cndmask_b32 v180,v252,v180,vcc\nv_max_f32 v228,v228,v180\nv_cmp_le_i32 vcc,49,v229\ns_nop 1\nv_cndmask_b32 v181,v252,v181,vcc\nv_max_f32 v228,v228,v181\nv_cmp_le_i32 vcc,50,v229\n" \
"s_nop 1\nv_cndmask_b32 v182,v252,v182,vcc\nv_max_f32 v228,v228,v182\nv_cmp_le_i32 vcc,51,v229\ns_nop 1\nv_cndmask_b32 v183,v252,v183,vcc\nv_max_f32 v228,v228,v183\nv_cmp_le_i32 vcc,48,v233\n" \
"s_nop 1\nv_cndmask_b32 v184,v252,v184,vcc\nv_max_f32 v232,v232,v184\nv_cmp_le_i32 vcc,49,v233\ns_nop 1\nv_cndmask_b32 v185,v252,v185,vcc\nv_max_f32 v232,v232,v185\nv_cmp_le_i32 vcc,50,v233\n" \
"s_nop 1\nv_cndmask_b32 v186,v252,v186,vcc\nv_max_f32 v232,v232,v186\nv_cmp_le_i32 vcc,51,v233\ns_nop 1\nv_cndmask_b32 v187,v252,v187,vcc\nv_max_f32 v232,v232,v187\nv_cmp_le_i32 vcc,48,v237\n" \
"s_nop 1\nv_cndmask_b32 v188,v252,v188,vcc\nv_max_f32 v236,v236,v188\nv_cmp_le_i32 vcc,49,v237\ns_nop 1\nv_cndmask_b32 v189,v252,v189,vcc\nv_max_f32 v236,v236,v189\nv_cmp_le_i32 vcc,50,v237\n" \
"s_nop 1\nv_cndmask_b32 v190,v252,v190,vcc\nv_max_f32 v236,v236,v190\nv_cmp_le_i32 vcc,51,v237\ns_nop 1\nv_cndmask_b32 v191,v252,v191,vcc\nv_max_f32 v236,v236,v191\nv_subrev_u32 v225,64,v225\n" \
"v_subrev_u32 v229,64,v229\nv_subrev_u32 v233,64,v233\nv_subrev_u32 v237,64,v237\ns_cmp_lt_u32 s37,s38\ns_cbranch_scc1 .Lrl\ns_waitcnt vmcnt(0)\ns_barrier\nds_read_b128 a[0:3],v240 offset:0\n" \
"ds_read_b128 a[4:7],v240 offset:1024\nds_read_b128 a[8:11],v240 offset:2048\nds_read_b128 a[12:15],v240 offset:3072\nds_read_b128 a[16:19],v240 offset:4096\nds_read_b128 a[20:23],v240 offset:5120\nds_read_b128 a[24:27],v240 offset:6144\nds_read_b128 a[28:31],v240 offset:7168\nds_read_b128 a[32:35],v240 offset:8192\n" \
"ds_read_b128 a[36:39],v240 offset:9216\nds_read_b128 a[40:43],v240 offset:10240\nds_read_b128 a[44:47],v240 offset:11264\nds_read_b128 a[48:51],v240 offset:12288\nds_read_b128 a[52:55],v240 offset:13312\nds_read_b128 a[56:59],v240 offset:14336\nds_read_b128 a[60:63],v240 offset:15360\ns_cmp_eq_u32 s62,0\n" \
"s_cselect_b32 s36,0x8000,0\ns_add_u32 s36,s36,s64\ns_cmp_eq_u32 s36,s96\ns_cselect_b32 s72,0xc000,s98\ns_add_u32 s65,s36,s72\ns_cmp_eq_u32 s65,s96\ns_cselect_b32 s72,0xc000,s98\ns_add_u32 s66,s65,s72\n" \
"s_cmp_eq_u32 s66,s96\ns_cselect_b32 s72,0xc000,s98\ns_add_u32 s95,s66,s72\ns_add_u32 s97,s96,98304\ns_add_u32 s15,s64,s46\ns_add_u32 s15,s15,49152\ns_mov_b32 s94,0xc000\nv_and_b32 v251,31,v249\n" \
"v_lshlrev_b32 v251,2,v251\ns_lshl_b32 s80,s63,7\ns_add_u32 s80,s80,86016\nv_add_u32 v250,0xc000,v241\ns_waitcnt lgkmcnt(0)\ns_branch .Lattn\n.Lfin:\n" \
""
typedef __attribute__((address_space(1))) unsigned gu32;
typedef __attribute__((address_space(1))) unsigned long long gu64;
#define RLX_AGENT __ATOMIC_RELAXED, __HIP_MEMORY_SCOPE_AGENT
constexpr size_t WS_HO = 65536, WS_DBG = (size_t)51 << 20, WS_PART = (size_t)52 << 20;
constexpr unsigned HO_FP = 8192, HO_P2 = 32768, HO_KR = HO_P2 + 224 * DM * 4, LDS_BYTES = 163840, WX = 131072, SPIN_CAP = 1u << 20;
#define PSLEEP 4
struct Args {
    const u16 *x, *ckv, *w_q, *w_uk, *w_uv, *w_o, *w_r, *wg, *wu, *wd, *cvec, *w_mod;
    char* ws; u16* out;
};
typedef const __attribute__((address_space(4))) Args* kargs_t;
DI void st16_dev(void* p, const u32x4& v) { asm volatile("global_store_dwordx4 %0, %1, off sc1\n\ts_nop 1" :: "v"(p), "v"(v) : "memory"); }
DI void ld16_dev(u32x4& v, const void* p) { asm volatile("global_load_dwordx4 %0, %1, off sc1" : "=v"(v) : "v"(p) : "memory"); }
DI void sum8(const unsigned* w, float& lo, float& hi) {
    lo = ((lo2f(w[0]) + lo2f(w[1])) + (lo2f(w[2]) + lo2f(w[3]))) + ((lo2f(w[4]) + lo2f(w[5])) + (lo2f(w[6]) + lo2f(w[7])));
    hi = ((hi2f(w[0]) + hi2f(w[1])) + (hi2f(w[2]) + hi2f(w[3]))) + ((hi2f(w[4]) + hi2f(w[5])) + (hi2f(w[6]) + hi2f(w[7])));
}
DI unsigned ord16(float r) { const unsigned u = __float_as_uint(r) >> 16; return (r != r) ? 0u : ((u & 0x8000u) ? (~u & 0xffffu) : (u | 0x8000u)); }
DI float ord16_val(unsigned o) { return __uint_as_float(((o & 0x8000u) ? (o & 0x7fffu) : (~o & 0xffffu)) << 16); }
#define STAMPD(i) do {} while (0)
#define C8(p, a) p #a "0", p #a "1", p #a "2", p #a "3", p #a "4", p #a "5", p #a "6", p #a "7", p #a "8", p #a "9"
#define C100(p, a) C8(p, a##0), C8(p, a##1), C8(p, a##2), C8(p, a##3), C8(p, a##4), C8(p, a##5), C8(p, a##6), C8(p, a##7), C8(p, a##8), C8(p, a##9)
#define C10_99(p) C8(p, 1), C8(p, 2), C8(p, 3), C8(p, 4), C8(p, 5), C8(p, 6), C8(p, 7), C8(p, 8), C8(p, 9)
#define C200_249(p) C8(p, 20), C8(p, 21), C8(p, 22), C8(p, 23), C8(p, 24)
#define BODY_CLOB "memory", "vcc", "scc", "s8", "s9", C8("s", 1), C8("s", 2), "s30", "s31", "s34", "s35", "s36", "s37", "s38", "s39", C8("s", 4), C8("s", 5), C8("s", 6), C8("s", 7), C8("s", 8), C8("s", 9), C8("v", ), C10_99("v"), C8("v", 10), C8("v", 11), "v120", "v121", "v122", "v123", "v124", "v125", "v126", "v127", \
    "v192", "v193", "v194", "v195", "v196", "v197", "v198", "v199", C200_249("v"), "v250", "v251", "v252", "v253", "v254", \
    C8("a", ), C10_99("a"), C100("a", 1), C200_249("a"), "a250", "a251", "a252", "a253", "a254", "a255"

__global__ __launch_bounds__(256, 1) void k_all(const Args a_) {
    extern __shared__ __attribute__((aligned(16))) char lds[];
    kargs_t ap = (kargs_t)__builtin_amdgcn_kernarg_segment_ptr(); unsigned bid = blockIdx.x, wid = rfl(threadIdx.x >> 6);
    u32x16 M0, M1, M2, M3; unsigned sv_;
    asm volatile(BODY_ASM : "+{s[4:5]}"(ap), "+{s6}"(bid), "+{s7}"(wid), "={v[128:143]}"(M0), "={v[144:159]}"(M1), "={v[160:175]}"(M2), "={v[176:191]}"(M3), "={v255}"(sv_) :: BODY_CLOB);
    STAMPD(12);
    char* const ws = ap->ws; char* const ho = ws + WS_HO; const unsigned lane = lane_id();
    const unsigned g = bid & 7u, idx = bid >> 3, j = idx & 3u, xx = idx >> 2, head = 4u * g + j, tid = wid * 64u + lane;
    const u16* __restrict__ x = ap->x; u16* const out = ap->out; const char* const part = ws + WS_PART;
    const unsigned rowL = xx * 128u + 4u * head + wid, rowH = (15u - xx) * 128u + 4u * head + wid;
    {   const gu32* fpl = (gu32*)(ho + HO_FP) + (xx * 8u + g) * 32u + (lane & 31u);
        for (unsigned spins = 0; spins < SPIN_CAP; ++spins) { if (__all(__hip_atomic_load(fpl, RLX_AGENT) != 0u)) break; __builtin_amdgcn_s_sleep(1); } }
    STAMPD(17);
    u32x4 pp[8];
    const char* ppb = part + lane * 16u;
#pragma unroll
    for (int zz = 0; zz < 8; ++zz) ld16_dev(pp[zz], ppb + ((size_t)zz * SEQ + rowL) * (DM * 2));
    const bool ffn = idx < 28u; const unsigned fb = g * 28u + (ffn ? idx : 0u);
    const unsigned c0 = wid * 128u + (lane & 3u) * 32u + (lane >> 2);
    u32x4 fw[4]; unsigned fd[8];
    {   const unsigned o1 = c0 * (2u * FF) + fb * 16u; const u16* wd = ap->wd + (size_t)(fb * 8u) * DM;
#pragma unroll
        for (int i = 0; i < 4; ++i) asm volatile("global_load_dwordx4 %0, %1, %2" : "=v"(fw[i]) : "v"(o1), "s"((i < 2 ? ap->wg : ap->wu) + (i & 1) * 16 * FF) : "memory");
#pragma unroll
        for (int i = 0; i < 8; ++i) asm volatile("global_load_dword %0, %1, %2 offset:%3" : "=v"(fd[i]) : "v"(tid * 4u), "s"(wd + (i & 4) * DM), "i"((i & 3) * 1024) : "memory"); }
    float wr[8], ga[8], sb[8], c1[8], gb[8];
#pragma unroll
    for (int i = 0; i < 8; ++i) { wr[i] = lo2f(M1[i]); ga[i] = __uint_as_float(M1[8 + i]); sb[i] = __uint_as_float(M2[i]); c1[i] = rb(1.f + __uint_as_float(M2[8 + i])); gb[i] = __uint_as_float(M3[i]); }
    unsigned kL = 0, kH = 0, sL = 0, sH = 0;
    unsigned nit = 2; asm volatile("" : "+s"(nit));
    char* const stg = lds + WX + wid * 4096u;
#define PPWAIT() asm volatile("s_waitcnt vmcnt(0)" : "+v"(pp[0]), "+v"(pp[1]), "+v"(pp[2]), "+v"(pp[3]), "+v"(pp[4]), "+v"(pp[5]), "+v"(pp[6]), "+v"(pp[7]) :: "memory")
#pragma nounroll
    for (unsigned it = 1; it <= nit; ++it) {
        const bool hv = it == nit; const unsigned row = hv ? rowH : rowL;
        asm volatile("s_waitcnt vmcnt(12)\n\ts_cmp_lg_u32 %8, %9\n\ts_cbranch_scc1 .Lpw_%=\n\ts_waitcnt vmcnt(0)\n\t.Lpw_%=:"
                     : "+v"(pp[0]), "+v"(pp[1]), "+v"(pp[2]), "+v"(pp[3]), "+v"(pp[4]), "+v"(pp[5]), "+v"(pp[6]), "+v"(pp[7]) : "s"(it), "s"(nit) : "memory", "scc");
        for (unsigned spins = 0; hv && spins < SPIN_CAP; ++spins) {
            unsigned mx = 0;
#pragma unroll
            for (int zz = 0; zz < 8; ++zz) mx = max(max(mx, max(pp[zz][0], pp[zz][1])), max(pp[zz][2], pp[zz][3]));
            if (__all(mx != 0xffffffffu)) break;
#pragma unroll
            for (int zz = 0; zz < 8; ++zz) ld16_dev(pp[zz], ppb + ((size_t)zz * SEQ + rowH) * (DM * 2));
            PPWAIT();
        }
        float as[8];
#pragma unroll
        for (int q4 = 0; q4 < 4; ++q4) {
            const unsigned w8[8] = {pp[0][q4], pp[1][q4], pp[2][q4], pp[3][q4], pp[4][q4], pp[5][q4], pp[6][q4], pp[7][q4]};
            sum8(w8, as[2 * q4], as[2 * q4 + 1]); }
        asm volatile("" : "+v"(as[0]), "+v"(as[1]), "+v"(as[2]), "+v"(as[3]), "+v"(as[4]), "+v"(as[5]), "+v"(as[6]), "+v"(as[7]) :: "memory");
        if (!hv) {
#pragma unroll
            for (int zz = 0; zz < 8; ++zz) ld16_dev(pp[zz], ppb + ((size_t)zz * SEQ + rowH) * (DM * 2)); }
        float x1[8], h2[8];
#pragma unroll
        for (int i = 0; i < 8; ++i) x1[i] = rb(lo2f(hv ? M0[8 + i] : M0[i]) + rb(ga[i] * rb(as[i])));
        const float mean = wave_sum2(((x1[0] + x1[1]) + (x1[2] + x1[3])) + ((x1[4] + x1[5]) + (x1[6] + x1[7]))) * (1.f / DM);
        float q0 = 0.f, q1 = 0.f;
#pragma unroll
        for (int i = 0; i < 8; i += 2) { const float d0 = x1[i] - mean, d1 = x1[i + 1] - mean; q0 = fmaf(d0, d0, q0); q1 = fmaf(d1, d1, q1); }
        const float qv = wave_sum2(q0 + q1);
        const float m = rb(mean), den = rb(__builtin_amdgcn_sqrtf(rb(rb(qv * (1.f / DM)) + 1e-5f))), rden = __builtin_amdgcn_rcpf(den); float a0 = 0.f, a1 = 0.f;
#pragma unroll
        for (int i = 0; i < 8; ++i) { h2[i] = rb(rb(rb(rb(x1[i] - m) * rden) * c1[i]) + sb[i]); if (i & 1) a1 = fmaf(h2[i], wr[i], a1); else a0 = fmaf(h2[i], wr[i], a0); }
        const unsigned k = (ord16(rb(wave_sum2(a0 + a1))) << 16) | ((2047u - row) << 5) | 1u, s = __float_as_uint(m) | (__float_as_uint(den) >> 16);
#pragma unroll
        for (int i = 0; i < 8; ++i) *reinterpret_cast<u16*>(stg + (hv ? 1024u : 0u) + (lane >> 4) * 256u + i * 32u + (lane & 15u) * 2u) = (u16)(__float_as_uint(rb(x1[i] + rb(gb[i] * h2[i]))) >> 16);
        if (hv) { kH = k; sH = s; } else { kL = k; sL = s; }
    }
    asm volatile("" : "+v"(fw[0]), "+v"(fw[1]), "+v"(fw[2]), "+v"(fw[3]), "+v"(fd[0]), "+v"(fd[1]), "+v"(fd[2]), "+v"(fd[3]), "+v"(fd[4]), "+v"(fd[5]), "+v"(fd[6]), "+v"(fd[7]) :: "memory");
    u32x2* sk = (u32x2*)lds;
    u32x2* skw = (u32x2*)(lds + WX + 2048);
    if (lane == 0) skw[wid * 512u] = kH > kL ? (u32x2){sH, kH} : (u32x2){sL, kL};
    LDSBAR();
    unsigned kwg, kwl;
    {   const u32x2 a = skw[0], b = skw[512], cc = skw[1024], d = skw[1536];
        const u32x2 p = a[1] > b[1] ? a : b, q = cc[1] > d[1] ? cc : d, r = p[1] > q[1] ? p : q;
        if (tid < 8u) __hip_atomic_store((gu64*)(ho + HO_KR) + tid * 256u + bid, ((unsigned long long)r[1] << 32) | r[0], RLX_AGENT);
        kwg = r[1]; kwl = r[0]; }
    STAMPD(13);
    const u32x4 ooL = *reinterpret_cast<const u32x4*>(stg + lane * 16u), ooH = *reinterpret_cast<const u32x4*>(stg + 1024u + lane * 16u);
    if (kL != kwg) st16_dev(out + (size_t)rowL * DM + lane * 8, ooL);
    if (kH != kwg) st16_dev(out + (size_t)rowH * DM + lane * 8, ooH);
    float* red = (float*)(lds + 1024);
    float* red3 = (float*)(lds + 2048);
    float* fin = (float*)(lds + 4096);
    if (wid == 0) {
        unsigned kh = 0, kl = 0;
        for (unsigned spins = 0; spins < SPIN_CAP; ++spins) {
            u32x4 ka, kb; ld16_dev(ka, ho + HO_KR + g * 2048u + lane * 32u); ld16_dev(kb, ho + HO_KR + g * 2048u + lane * 32u + 16u);
            asm volatile("s_waitcnt vmcnt(0)" : "+v"(ka), "+v"(kb) :: "memory");
            if (lane == (bid >> 2)) { const unsigned o = bid & 3u;
                if (o == 0) { ka[0] = kwl; ka[1] = kwg; } if (o == 1) { ka[2] = kwl; ka[3] = kwg; } if (o == 2) { kb[0] = kwl; kb[1] = kwg; } if (o == 3) { kb[2] = kwl; kb[3] = kwg; } }
            const u32x2 p = ka[1] > ka[3] ? (u32x2){ka[0], ka[1]} : (u32x2){ka[2], ka[3]}, q = kb[1] > kb[3] ? (u32x2){kb[0], kb[1]} : (u32x2){kb[2], kb[3]};
            kh = p[1] > q[1] ? p[1] : q[1]; kl = p[1] > q[1] ? p[0] : q[0];
            if (__all(ka[1] != 0u && ka[3] != 0u && kb[1] != 0u && kb[3] != 0u)) break;
            __builtin_amdgcn_s_sleep(PSLEEP);
        }
        const unsigned mh = wave_umax(kh);
        if (kh == mh) sk[4] = (u32x2){kl, kh};
    }
    LDSBAR();
    const u32x2 key = sk[4];
    const unsigned sel = (2047u - ((key[1] >> 5) & 2047u)) & 2047u;
    STAMPD(14);
    unsigned h2w;
    {   unsigned p[8];
#pragma unroll
        for (int zz = 0; zz < 8; ++zz) p[zz] = __hip_atomic_load((gu32*)(part + ((size_t)zz * SEQ + sel) * (DM * 2) + 4u * tid), RLX_AGENT);
        const unsigned xa = x[(size_t)sel * DM + c0], xb = x[(size_t)sel * DM + c0 + 16u];
        if (key[1] != kwg) {
            if (kL == kwg) st16_dev(out + (size_t)rowL * DM + lane * 8, ooL);
            if (kH == kwg) st16_dev(out + (size_t)rowH * DM + lane * 8, ooH); }
        float a0, a1; sum8(p, a0, a1);
        const float m = hi2f(key[0]), rden = __builtin_amdgcn_rcpf(lo2f(key[0]));
#define MP(i) __uint_as_float(M3[8 + (i)])
        const float x10 = rb(lo2f(xa) + rb(MP(0) * rb(a0))), x11 = rb(lo2f(xb) + rb(MP(1) * rb(a1)));
        h2w = cvt2(rb(rb(rb(x10 - m) * rden) * rb(1.f + MP(4))) + MP(2), rb(rb(rb(x11 - m) * rden) * rb(1.f + MP(5))) + MP(3));
        if (bid < 64u) { fin[c0] = x10; fin[c0 + 16u] = x11; fin[512u + c0] = lo2f(h2w); fin[528u + c0] = hi2f(h2w); fin[1024u + c0] = MP(6); fin[1040u + c0] = MP(7); } }
    if (ffn) {
        float v[16];
#pragma unroll
        for (int mtx = 0; mtx < 2; ++mtx)
#pragma unroll
            for (int e = 0; e < 4; ++e) { const unsigned w0 = fw[2 * mtx][e], w1 = fw[2 * mtx + 1][e];
                v[8 * mtx + 2 * e] = dot2bf(__builtin_amdgcn_perm(w1, w0, 0x05040100u), h2w, 0.f); v[8 * mtx + 2 * e + 1] = dot2bf(__builtin_amdgcn_perm(w1, w0, 0x07060302u), h2w, 0.f); }
        float v8[8], v4[4];
#pragma unroll
        for (int i = 0; i < 8; ++i) v8[i] = swap32_sum(v[i], v[8 + i]);
#pragma unroll
        for (int i = 0; i < 4; ++i) v4[i] = row16_sum(swap16_sum(v8[i], v8[4 + i]));
        if ((lane & 15u) == 0) *reinterpret_cast<f32x4*>(red + wid * 16u + (lane >> 4) * 4u) = (f32x4){v4[0], v4[1], v4[2], v4[3]};
        LDSBAR();
        const unsigned f8 = lane & 7u;
        const float gg = rb((red[f8] + red[16 + f8]) + (red[32 + f8] + red[48 + f8])), uu = rb((red[8 + f8] + red[24 + f8]) + (red[40 + f8] + red[56 + f8]));
        const float gu = rb(rb(gg / (1.f + __expf(-gg))) * uu);
        float a0 = 0.f, a1 = 0.f;
#pragma unroll
        for (int i = 0; i < 8; ++i) { const float gv = __builtin_bit_cast(float, __builtin_amdgcn_readlane(__builtin_bit_cast(int, gu), i)); a0 = fmaf(gv, lo2f(fd[i]), a0); a1 = fmaf(gv, hi2f(fd[i]), a1); }
        const unsigned long long o = ((unsigned long long)min(__float_as_uint(a1), 0xfffffffeu) << 32) | min(__float_as_uint(a0), 0xfffffffeu);
        __hip_atomic_store((gu64*)(ho + HO_P2) + fb * 256u + tid, o, RLX_AGENT);
    }
    STAMPD(15);
    if (bid < 64u) {
        const unsigned cg = g * 8u + idx;
        const char* gp = ho + HO_P2 + (tid >> 1) * 2048u + cg * 32u + (tid & 1u) * 16u;
        u32x4 ga_ = {}, gb_ = {};
        for (unsigned spins = 0; spins < SPIN_CAP; ++spins) {
            ld16_dev(ga_, gp); ld16_dev(gb_, gp + (tid < 192u ? 128u * 2048u : 0u));
            asm volatile("s_waitcnt vmcnt(0)" : "+v"(ga_), "+v"(gb_) :: "memory");
            const unsigned mx = max(max(max(ga_[0], ga_[1]), max(ga_[2], ga_[3])), max(max(gb_[0], gb_[1]), max(gb_[2], gb_[3])));
            if (__all(mx != 0xffffffffu)) break;
            __builtin_amdgcn_s_sleep(PSLEEP);
        }
        float s4[4];
#pragma unroll
        for (int i = 0; i < 4; ++i) { float s = __uint_as_float(ga_[i]) + (tid < 192u ? __uint_as_float(gb_[i]) : 0.f);
            s += DPPF(s, 0x4E); s += DPPF(s, 0x124); s += DPPF(s, 0x128);
            s = swap16_sum(s, s); s4[i] = swap32_sum(s, s); }
        if (lane < 2) *reinterpret_cast<f32x4*>(red3 + wid * 8u + lane * 4u) = (f32x4){s4[0], s4[1], s4[2], s4[3]};
        LDSBAR();
        if (tid < 4u) { const unsigned t = tid, cc = cg * 8u + 2u * t;
            const float s0 = (red3[2 * t] + red3[8 + 2 * t]) + (red3[16 + 2 * t] + red3[24 + 2 * t]), s1 = (red3[2 * t + 1] + red3[9 + 2 * t]) + (red3[17 + 2 * t] + red3[25 + 2 * t]);
            const float rsel = rb(1.f / (1.f + __expf(-ord16_val(key[1] >> 16))));
            const float y0 = rb(rb(s0) * rsel), y1 = rb(rb(s1) * rsel);
            const unsigned ow = cvt2(fin[cc] + rb(fin[1024u + cc] * rb(fin[512u + cc] + y0)), fin[cc + 1u] + rb(fin[1025u + cc] * rb(fin[513u + cc] + y1)));
            __hip_atomic_store((gu32*)(out + (size_t)sel * DM + cc), ow, RLX_AGENT);
        }
    }
    asm volatile(".Lcode_e:");
}
extern "C" void kernel_launch(void* const* d_in, const int* in_sizes, int n_in, void* d_out, int out_size, void* d_ws, size_t ws_size, hipStream_t stream) {
    Args a;
    a.x = (const u16*)d_in[0]; a.ckv = (const u16*)d_in[1]; a.w_q = (const u16*)d_in[2]; a.w_uk = (const u16*)d_in[3]; a.w_uv = (const u16*)d_in[4]; a.w_o = (const u16*)d_in[5];
    a.w_r = (const u16*)d_in[7]; a.wg = (const u16*)d_in[8]; a.wu = (const u16*)d_in[9]; a.wd = (const u16*)d_in[10]; a.cvec = (const u16*)d_in[11]; a.w_mod = (const u16*)d_in[12];
    a.ws = (char*)d_ws; a.out = (u16*)d_out;
    (void)hipFuncSetAttribute((const void*)k_all, hipFuncAttributeMaxDynamicSharedMemorySize, LDS_BYTES);
    k_all<<<256, 256, LDS_BYTES, stream>>>(a);
}
```

```cpp
#include <hip/hip_runtime.h>
#include <hip/hip_bf16.h>
#include <stdint.h>
typedef unsigned short u16;
typedef float f32x4 __attribute__((ext_vector_type(4)));
typedef float f32x2 __attribute__((ext_vector_type(2)));
typedef unsigned u32x4 __attribute__((ext_vector_type(4)));
typedef unsigned u32x2 __attribute__((ext_vector_type(2)));
typedef unsigned u32x16 __attribute__((ext_vector_type(16)));
typedef __bf16 bf16x2_t __attribute__((ext_vector_type(2)));
constexpr int SEQ = 2048, DM = 512, FF = 1792;
#define DI __device__ __forceinline__
#define LDSBAR() do { asm volatile("s_waitcnt lgkmcnt(0)" ::: "memory"); __builtin_amdgcn_s_barrier(); asm volatile("" ::: "memory"); } while (0)
DI unsigned lane_id() { unsigned l; asm volatile("v_mbcnt_lo_u32_b32 %0, -1, 0\n\tv_mbcnt_hi_u32_b32 %0, -1, %0" : "=v"(l)); return l; }
DI unsigned rfl(unsigned x) { return __builtin_amdgcn_readfirstlane(x); }
DI unsigned cvt2(float lo, float hi) { f32x2 v = {lo, hi}; bf16x2_t b = __builtin_convertvector(v, bf16x2_t); return __builtin_bit_cast(unsigned, b); }
DI float rb(float x) { return __uint_as_float(cvt2(0.f, x)); }
DI float dot2bf(unsigned a, unsigned b, float c) { return __builtin_amdgcn_fdot2_f32_bf16(__builtin_bit_cast(bf16x2_t, a), __builtin_bit_cast(bf16x2_t, b), c, false); }
DI float lo2f(unsigned w) { return __uint_as_float(w << 16); }
DI float hi2f(unsigned w) { return __uint_as_float(w & 0xffff0000u); }
#define DPPF(v, ctl) __builtin_bit_cast(float, __builtin_amdgcn_update_dpp(0, __builtin_bit_cast(int, v), ctl, 0xF, 0xF, true))
DI float row16_sum(float v) { v += DPPF(v, 0xB1); v += DPPF(v, 0x4E); v += DPPF(v, 0x141); v += DPPF(v, 0x140); return v; }
DI float swap16_sum(float a, float b) { auto r = __builtin_amdgcn_permlane16_swap(__float_as_uint(a), __float_as_uint(b), false, false); return __uint_as_float(r[0]) + __uint_as_float(r[1]); }
DI float swap32_sum(float a, float b) { auto r = __builtin_amdgcn_permlane32_swap(__float_as_uint(a), __float_as_uint(b), false, false); return __uint_as_float(r[0]) + __uint_as_float(r[1]); }
DI float wave_sum2(float v) { v = row16_sum(v); v = swap16_sum(v, v); return swap32_sum(v, v); }
DI unsigned wave_umax(unsigned v) {
    v = max(v, (unsigned)__builtin_amdgcn_update_dpp(0, (int)v, 0xB1, 0xF, 0xF, true)); v = max(v, (unsigned)__builtin_amdgcn_update_dpp(0, (int)v, 0x4E, 0xF, 0xF, true));
    v = max(v, (unsigned)__builtin_amdgcn_update_dpp(0, (int)v, 0x141, 0xF, 0xF, true)); v = max(v, (unsigned)__builtin_amdgcn_update_dpp(0, (int)v, 0x140, 0xF, 0xF, true));
    { auto r = __builtin_amdgcn_permlane16_swap(v, v, false, false); v = max((unsigned)r[0], (unsigned)r[1]); }
    { auto r = __builtin_amdgcn_permlane32_swap(v, v, false, false); v = max((unsigned)r[0], (unsigned)r[1]); }
    return v;
}
#define BODY_ASM ".macro M a,b,c,d\nv_mfma_f32_16x16x32_bf16 \\a,\\b,\\c,\\d\n.endm\n" \
".Lcode_b:\ns_load_dwordx16 s[8:23],s[4:5],0x0\ns_load_dwordx8 s[24:31],s[4:5],0x40\ns_load_dwordx2 s[34:35],s[4:5],0x60\nv_mbcnt_lo_u32_b32 v249,-1,0\nv_mbcnt_hi_u32_b32 v249,-1,v249\ns_getpc_b64 s[56:57]\n.Lpc0:\n" \
"s_and_b32 s57,s57,0xffff\ns_mov_b32 s58,.Lcode_e-.Lpc0\ns_mov_b32 s59,0x20000\ns_bfe_u32 s61,s6,0x20003\nv_lshlrev_b32 v251,7,v249\ns_lshl_b32 s64,s7,12\ns_lshl_b32 s70,s7,13\ns_cmp_eq_u32 s61,0\n" \
"s_cselect_b32 s70,s70,0x8000000\nbuffer_load_dword v247,v251,s[56:59],s70 offen\ns_and_b32 s60,s6,7\ns_lshr_b32 s62,s6,5\ns_lshl2_add_u32 s63,s60,s61\nv_lshlrev_b32 v240,4,v249\ns_cmp_lt_u32 s6,192\ns_cselect_b32 s70,s6,0\n" \
"s_mul_i32 s71,s70,171\ns_lshr_b32 s71,s71,13\ns_mul_i32 s72,s71,48\ns_sub_u32 s70,s70,s72\ns_lshl2_add_u32 s72,s71,s7\ns_lshl_b32 s72,s72,5\nv_lshrrev_b32 v0,3,v249\nv_lshl_add_u32 v1,v0,2,s72\n" \
"v_and_b32 v2,7,v249\nv_mul_u32_u24 v3,6144,v1\nv_lshl_add_u32 v3,v2,4,v3\ns_lshl_b32 s72,s70,7\ns_add_u32 s72,s72,3072\nv_add_u32 v3,s72,v3\nv_add_u32 v4,12288,v3\nv_lshlrev_b32 v1,1,v1\n" \
"s_mul_i32 s71,s71,12288\ns_lshl_b32 s70,s70,8\ns_add_u32 s79,s70,s71\ns_add_u32 s79,s79,786432\ns_cmp_eq_u32 s7,0\ns_cselect_b32 s75,0,0x8000000\ns_cmp_lt_u32 s6,192\ns_cselect_b32 s70,s75,0x8000000\n" \
"s_or_b32 s79,s79,s70\ns_lshl_b32 s70,s62,7\ns_xor_b32 s71,s63,12\ns_lshl3_add_u32 s71,s71,s70\ns_lshl_b32 s72,s63,3\ns_sub_u32 s72,s72,s70\ns_add_u32 s72,s72,1792\ns_cmp_lt_u32 s63,16\n" \
"s_cselect_b32 s71,s71,s72\ns_lshl_b32 s71,s71,10\ns_lshl_b32 s72,s7,7\ns_add_u32 s71,s71,s72\nv_lshlrev_b32 v5,10,v2\nv_lshl_add_u32 v5,v0,4,v5\nv_add_u32 v5,s71,v5\nv_bfe_u32 v6,v249,2,3\n" \
"v_lshrrev_b32 v7,5,v249\nv_and_b32 v8,48,v240\nv_lshlrev_b32 v6,13,v6\nv_lshl_or_b32 v7,v7,6,v8\nv_or_b32 v6,0x1000,v6\nv_or_b32 v243,v6,v7\nv_add_u32 v244,0xfffffc80,v243\nv_xor_b32 v6,32,v243\n" \
"v_add_u32 v245,63488,v6\nv_add_u32 v246,62592,v6\nv_lshrrev_b32 v6,4,v249\nv_xor_b32 v7,v6,v2\nv_lshlrev_b32 v7,4,v7\nv_lshl_or_b32 v224,v0,10,v7\nv_xor_b32 v7,64,v224\nv_add_u32 v225,7168,v7\n" \
"v_add_u32 v226,14336,v224\nv_add_u32 v227,21504,v7\ns_lshl_b32 s70,s62,1\ns_sub_u32 s71,s7,s70\ns_add_u32 s71,s71,30\ns_sub_u32 s72,s70,s7\ns_add_u32 s72,s72,3\ns_cmp_lt_u32 s7,2\n" \
"s_cselect_b32 s74,s71,s72\ns_lshl_b32 s66,s74,16\ns_mul_i32 s65,s7,24576\ns_lshl_b32 s68,s7,17\ns_lshl_b32 s70,s63,8\ns_add_u32 s68,s68,s70\ns_mov_b32 s38,2097152\ns_mov_b32 s39,0x20000\n" \
"s_mov_b32 s42,4198400\ns_mov_b32 s43,0x20000\ns_mov_b32 s54,71303168\ns_mov_b32 s55,0x20000\ns_mov_b32 s70,94208\ns_waitcnt lgkmcnt(0)\ns_mov_b64 s[52:53],s[34:35]\ns_mov_b64 s[36:37],s[10:11]\n" \
"s_sub_u32 s40,s14,0x1000\ns_subb_u32 s41,s15,0\nbuffer_load_dwordx2 v[230:231],off,s[52:55],s70 sc1\nglobal_load_dwordx2 v[10:11],v1,s[28:29]\nglobal_load_dwordx4 v[12:15],v3,s[30:31] offset:-3072\nglobal_load_dwordx4 v[16:19],v3,s[30:31] offset:3072\nglobal_load_dwordx4 v[20:23],v4,s[30:31] offset:-3072\nglobal_load_dwordx4 v[24:27],v4,s[30:31] offset:3072\n" \
"global_load_dwordx4 v[128:131],v5,s[8:9]\nglobal_load_dwordx4 v[132:135],v5,s[8:9] offset:512\ns_add_u32 m0,s64,98304\ns_add_u32 s69,s68,0\nbuffer_load_dwordx4 v243,s[40:43],s69 offen offset:0 lds\nbuffer_load_dwordx4 v244,s[40:43],s69 offen offset:1024 lds\nbuffer_load_dwordx4 v245,s[40:43],s69 offen offset:2048 lds\nbuffer_load_dwordx4 v246,s[40:43],s69 offen offset:3072 lds\n" \
"s_add_u32 m0,s65,0\ns_add_u32 s67,s66,0\nbuffer_load_dwordx4 v224,s[36:39],s67 offen offset:0 lds\nbuffer_load_dwordx4 v225,s[36:39],s67 offen offset:1024 lds\nbuffer_load_dwordx4 v226,s[36:39],s67 offen offset:2048 lds\nbuffer_load_dwordx4 v227,s[36:39],s67 offen offset:3072 lds\ns_add_u32 m0,s65,4096\ns_add_u32 s67,s66,32768\n" \
"buffer_load_dwordx4 v224,s[36:39],s67 offen offset:0 lds\nbuffer_load_dwordx4 v225,s[36:39],s67 offen offset:1024 lds\nbuffer_load_dwordx4 v226,s[36:39],s67 offen offset:2048 lds\nbuffer_load_dwordx4 v227,s[36:39],s67 offen offset:3072 lds\ns_min_u32 s70,s6,223\ns_lshl_b32 s70,s70,11\ns_lshl_b32 s71,s7,9\ns_add_u32 s70,s70,s71\n" \
"s_add_u32 s70,s70,98304\nv_mov_b32 v28,-1\nv_mov_b32 v29,-1\nv_lshlrev_b32 v30,3,v249\nbuffer_store_dwordx2 v[28:29],v30,s[52:55],s70 offen sc1\ns_lshl_b32 s70,s6,7\ns_cmp_lt_u32 s6,224\ns_cselect_b32 s71,65536,0x40000000\n" \
"s_cmp_lt_u32 s6,128\ns_cselect_b32 s72,556928,0x40000000\nv_mov_b32 v31,s71\nv_mov_b32 v32,s72\nv_cmp_lt_u32 vcc,31,v249\ns_or_b32 s70,s70,s75\nv_mov_b32 v33,0\nv_cndmask_b32 v31,v31,v32,vcc\n" \
"v_lshl_add_u32 v31,v249,2,v31\nbuffer_store_dword v33,v31,s[52:55],s70 offen sc1\nv_and_b32 v34,15,v249\nv_lshrrev_b32 v35,1,v34\nv_xor_b32 v35,v35,v6\nv_lshlrev_b32 v35,4,v35\nv_lshl_or_b32 v35,v34,7,v35\nv_add_u32 v228,s65,v35\n" \
"v_xor_b32 v229,64,v228\nv_bfe_u32 v34,v249,2,2\nv_and_b32 v35,3,v249\nv_lshlrev_b32 v35,3,v35\nv_lshl_or_b32 v35,v34,6,v35\nv_lshl_or_b32 v35,v6,11,v35\nv_and_b32 v34,16,v249\nv_lshl_or_b32 v35,v34,1,v35\n" \
"v_add_u32 v241,98304,v35\nv_xor_b32 v242,32,v241\ns_mov_b32 s73,0x400000\nv_mov_b32 v254,1\ns_mov_b32 s70,0x05040100\ns_mov_b32 s71,0x07060302\ns_waitcnt vmcnt(20)\nv_lshlrev_b32 v36,16,v10\n" \
"v_and_b32 v37,0xffff0000,v10\nv_lshlrev_b32 v38,16,v11\nv_and_b32 v39,0xffff0000,v11\nv_mul_f32 v40,0xbfb8aa3b,v36\nv_mul_f32 v41,0xbfb8aa3b,v37\nv_mul_f32 v42,0xbfb8aa3b,v38\nv_mul_f32 v43,0xbfb8aa3b,v39\nv_exp_f32 v40,v40\n" \
"v_exp_f32 v41,v41\nv_exp_f32 v42,v42\nv_exp_f32 v43,v43\nv_add_f32 v40,1.0,v40\nv_add_f32 v41,1.0,v41\nv_add_f32 v42,1.0,v42\nv_add_f32 v43,1.0,v43\nv_rcp_f32 v40,v40\n" \
"v_rcp_f32 v41,v41\nv_rcp_f32 v42,v42\nv_rcp_f32 v43,v43\nv_mul_f32 v36,v36,v40\nv_mul_f32 v37,v37,v41\nv_mul_f32 v38,v38,v42\nv_mul_f32 v39,v39,v43\nv_cvt_pk_bf16_f32 v36,v36,v37\n" \
"v_cvt_pk_bf16_f32 v37,v38,v39\ns_waitcnt vmcnt(18)\nv_perm_b32 v48,v16,v12,s70\nv_perm_b32 v49,v16,v12,s71\nv_perm_b32 v50,v17,v13,s70\nv_perm_b32 v51,v17,v13,s71\nv_perm_b32 v52,v18,v14,s70\nv_perm_b32 v53,v18,v14,s71\n" \
"v_perm_b32 v54,v19,v15,s70\nv_perm_b32 v55,v19,v15,s71\nv_dot2_f32_bf16 v40,v48,v36,0\nv_dot2_f32_bf16 v41,v49,v36,0\nv_dot2_f32_bf16 v42,v50,v36,0\nv_dot2_f32_bf16 v43,v51,v36,0\nv_dot2_f32_bf16 v44,v52,v36,0\nv_dot2_f32_bf16 v45,v53,v36,0\n" \
"v_dot2_f32_bf16 v46,v54,v36,0\nv_dot2_f32_bf16 v47,v55,v36,0\ns_waitcnt vmcnt(16)\nv_perm_b32 v48,v24,v20,s70\nv_perm_b32 v49,v24,v20,s71\nv_perm_b32 v50,v25,v21,s70\nv_perm_b32 v51,v25,v21,s71\nv_perm_b32 v52,v26,v22,s70\n" \
"v_perm_b32 v53,v26,v22,s71\nv_perm_b32 v54,v27,v23,s70\nv_perm_b32 v55,v27,v23,s71\nv_dot2_f32_bf16 v40,v48,v37,v40\nv_dot2_f32_bf16 v41,v49,v37,v41\nv_dot2_f32_bf16 v42,v50,v37,v42\nv_dot2_f32_bf16 v43,v51,v37,v43\nv_dot2_f32_bf16 v44,v52,v37,v44\n" \
"v_dot2_f32_bf16 v45,v53,v37,v45\nv_dot2_f32_bf16 v46,v54,v37,v46\nv_dot2_f32_bf16 v47,v55,v37,v47\ns_waitcnt vmcnt(15)\nv_lshlrev_b32 v136,16,v128\nv_and_b32 v137,0xffff0000,v128\nv_lshlrev_b32 v138,16,v129\nv_and_b32 v139,0xffff0000,v129\n" \
"v_lshlrev_b32 v140,16,v130\nv_and_b32 v141,0xffff0000,v130\nv_lshlrev_b32 v142,16,v131\nv_and_b32 v143,0xffff0000,v131\ns_waitcnt vmcnt(14)\nv_lshlrev_b32 v144,16,v132\nv_and_b32 v145,0xffff0000,v132\nv_lshlrev_b32 v146,16,v133\n" \
"v_and_b32 v147,0xffff0000,v133\nv_lshlrev_b32 v148,16,v134\nv_and_b32 v149,0xffff0000,v134\nv_lshlrev_b32 v150,16,v135\nv_and_b32 v151,0xffff0000,v135\nv_permlane32_swap_b32 v40,v44\nv_permlane32_swap_b32 v41,v45\nv_permlane32_swap_b32 v42,v46\n" \
"v_permlane32_swap_b32 v43,v47\nv_add_f32 v40,v40,v44\nv_add_f32 v41,v41,v45\nv_add_f32 v42,v42,v46\nv_add_f32 v43,v43,v47\nv_add_f32 v48,v136,v140\nv_add_f32 v49,v137,v141\nv_add_f32 v50,v138,v142\n" \
"v_add_f32 v51,v139,v143\nv_permlane16_swap_b32 v40,v42\nv_permlane16_swap_b32 v41,v43\nv_add_f32 v52,v144,v148\nv_add_f32 v53,v145,v149\nv_add_f32 v54,v146,v150\nv_add_f32 v55,v147,v151\nv_add_f32 v40,v40,v42\n" \
"v_add_f32 v41,v41,v43\nv_add_f32 v48,v48,v52\nv_add_f32 v49,v49,v53\nv_add_f32 v50,v50,v54\nv_add_f32 v51,v51,v55\nv_add_f32_dpp v40,v40,v40 row_ror:8 row_mask:0xf bank_mask:0xf\nv_add_f32_dpp v41,v41,v41 row_ror:8 row_mask:0xf bank_mask:0xf\nv_add_f32 v48,v48,v49\n" \
"v_add_f32 v50,v50,v51\nv_lshlrev_b32 v42,5,v2\nv_add_f32 v48,v48,v50\nv_lshl_or_b32 v42,v6,3,v42\ns_lshl_b32 s72,s7,8\ns_add_u32 s72,s72,147456\nv_add_u32 v42,s72,v42\nds_write_b64 v42,v[40:41]\n" \
"v_add_f32_dpp v48,v48,v48 row_ror:8 row_mask:0xf bank_mask:0xf\nv_mov_b32 v50,v48\ns_nop 1\nv_permlane16_swap_b32 v48,v50\nv_add_f32 v48,v48,v50\nv_mov_b32 v50,v48\ns_nop 1\nv_permlane32_swap_b32 v48,v50\n" \
"v_add_f32 v48,v48,v50\nv_mul_f32 v48,0x3c000000,v48\nv_sub_f32 v56,v136,v48\nv_sub_f32 v57,v137,v48\nv_sub_f32 v58,v138,v48\nv_sub_f32 v59,v139,v48\nv_sub_f32 v60,v140,v48\nv_sub_f32 v61,v141,v48\n" \
"v_sub_f32 v62,v142,v48\nv_sub_f32 v63,v143,v48\nv_sub_f32 v64,v144,v48\nv_sub_f32 v65,v145,v48\nv_sub_f32 v66,v146,v48\nv_sub_f32 v67,v147,v48\nv_sub_f32 v68,v148,v48\nv_sub_f32 v69,v149,v48\n" \
"v_sub_f32 v70,v150,v48\nv_sub_f32 v71,v151,v48\nv_mul_f32 v52,v56,v56\nv_mul_f32 v53,v57,v57\nv_mul_f32 v54,v58,v58\nv_mul_f32 v55,v59,v59\nv_fmac_f32 v52,v60,v60\nv_fmac_f32 v53,v61,v61\n" \
"v_fmac_f32 v54,v62,v62\nv_fmac_f32 v55,v63,v63\nv_fmac_f32 v52,v64,v64\nv_fmac_f32 v53,v65,v65\nv_fmac_f32 v54,v66,v66\nv_fmac_f32 v55,v67,v67\nv_fmac_f32 v52,v68,v68\nv_fmac_f32 v53,v69,v69\n" \
"v_fmac_f32 v54,v70,v70\nv_fmac_f32 v55,v71,v71\nv_add_f32 v52,v52,v53\nv_add_f32 v54,v54,v55\ns_lshl_b32 s72,s7,3\nv_add_f32 v49,v52,v54\ns_add_u32 s72,s72,148480\nv_lshl_add_u32 v42,v2,5,s72\n" \
"v_add_f32_dpp v49,v49,v49 row_ror:8 row_mask:0xf bank_mask:0xf\nv_mov_b32 v50,v49\ns_nop 1\nv_permlane16_swap_b32 v49,v50\nv_add_f32 v49,v49,v50\nv_mov_b32 v50,v49\ns_nop 1\nv_permlane32_swap_b32 v49,v50\n" \
"v_add_f32 v49,v49,v50\nds_write_b64 v42,v[48:49]\nv_lshlrev_b32 v251,5,v2\nv_add_u32 v251,148480,v251\nv_lshlrev_b32 v250,2,v249\nv_add_u32 v250,147456,v250\ns_add_u32 m0,s64,114688\ns_add_u32 s69,s68,524288\n" \
"buffer_load_dwordx4 v243,s[40:43],s69 offen offset:0 lds\nbuffer_load_dwordx4 v244,s[40:43],s69 offen offset:1024 lds\nbuffer_load_dwordx4 v245,s[40:43],s69 offen offset:2048 lds\nbuffer_load_dwordx4 v246,s[40:43],s69 offen offset:3072 lds\ns_add_u32 m0,s65,8192\ns_add_u32 s67,s66,128\nbuffer_load_dwordx4 v224,s[36:39],s67 offen offset:0 lds\nbuffer_load_dwordx4 v225,s[36:39],s67 offen offset:1024 lds\n" \
"buffer_load_dwordx4 v226,s[36:39],s67 offen offset:2048 lds\nbuffer_load_dwordx4 v227,s[36:39],s67 offen offset:3072 lds\ns_add_u32 m0,s65,12288\ns_add_u32 s67,s66,32896\nbuffer_load_dwordx4 v224,s[36:39],s67 offen offset:0 lds\nbuffer_load_dwordx4 v225,s[36:39],s67 offen offset:1024 lds\nbuffer_load_dwordx4 v226,s[36:39],s67 offen offset:2048 lds\nbuffer_load_dwordx4 v227,s[36:39],s67 offen offset:3072 lds\n" \
"s_waitcnt lgkmcnt(0)\ns_waitcnt vmcnt(14)\ns_barrier\ns_add_u32 m0,s64,131072\ns_add_u32 s69,s68,1048576\nds_read_b128 a[0:3],v228 offset:0\nds_read_b128 a[4:7],v228 offset:2048\nds_read_b128 a[8:11],v228 offset:4096\n" \
"ds_read_b128 a[12:15],v228 offset:6144\nds_read_b128 a[16:19],v229 offset:0\nds_read_b128 a[20:23],v229 offset:2048\nds_read_b128 a[24:27],v229 offset:4096\nds_read_b128 a[28:31],v229 offset:6144\nds_read_b32 v152,v250 offset:0\nds_read_b32 v153,v250 offset:256\nds_read_b32 v154,v250 offset:512\n" \
"ds_read_b32 v155,v250 offset:768\ns_waitcnt lgkmcnt(2)\nv_add_f32 v152,v152,v153\ns_waitcnt lgkmcnt(0)\nv_add_f32 v154,v154,v155\nv_lshlrev_b32 v153,2,v249\nv_add_f32 v152,v152,v154\nds_read_b64_tr_b16 v[192:193],v241 offset:0\n" \
"ds_read_b64_tr_b16 v[194:195],v241 offset:256\nds_read_b64_tr_b16 v[196:197],v242 offset:0\nds_read_b64_tr_b16 v[198:199],v242 offset:256\nds_read_b64_tr_b16 v[200:201],v241 offset:512\nds_read_b64_tr_b16 v[202:203],v241 offset:768\nds_read_b64_tr_b16 v[204:205],v242 offset:512\nds_read_b64_tr_b16 v[206:207],v242 offset:768\nds_read_b64_tr_b16 v[208:209],v241 offset:1024\n" \
"ds_read_b64_tr_b16 v[210:211],v241 offset:1280\nds_read_b64_tr_b16 v[212:213],v242 offset:1024\nds_read_b64_tr_b16 v[214:215],v242 offset:1280\nds_read_b64_tr_b16 v[216:217],v241 offset:1536\nds_read_b64_tr_b16 v[218:219],v241 offset:1792\nds_read_b64_tr_b16 v[220:221],v242 offset:1536\nds_read_b64_tr_b16 v[222:223],v242 offset:1792\nbuffer_store_dword v152,v153,s[52:55],s79 offen sc1\n" \
"ds_read_b128 v[156:159],v251\nds_read_b128 v[160:163],v251 offset:16\ns_waitcnt lgkmcnt(15)\nM v[0:3],v[192:195],a[0:3],0\nbuffer_load_dwordx4 v243,s[40:43],s69 offen offset:0 lds\nM v[4:7],v[192:195],a[4:7],0\nv_bfe_u32 v253,v249,1,4\nv_and_b32 v252,1,v249\n" \
"M v[64:67],v[192:195],a[8:11],0\nv_lshlrev_b32 v253,13,v253\ns_lshl_b32 s78,s62,19\nM v[68:71],v[192:195],a[12:15],0\nds_read_b64_tr_b16 v[192:193],v241 offset:8192\nds_read_b64_tr_b16 v[194:195],v241 offset:8448\ns_waitcnt lgkmcnt(15)\nM v[8:11],v[196:199],a[0:3],0\n" \
"v_lshl_or_b32 v253,v252,7,v253\ns_add_u32 s78,s78,s68\nM v[12:15],v[196:199],a[4:7],0\nbuffer_load_dwordx4 v244,s[40:43],s69 offen offset:1024 lds\nM v[72:75],v[196:199],a[8:11],0\nv_add_u32 v253,s78,v253\ns_lshl3_add_u32 s72,s6,s75\nM v[76:79],v[196:199],a[12:15],0\n" \
"ds_read_b64_tr_b16 v[196:197],v242 offset:8192\nds_read_b64_tr_b16 v[198:199],v242 offset:8448\ns_waitcnt lgkmcnt(8)\nM v[16:19],v[200:203],a[0:3],0\nglobal_load_dword v247,v253,s[16:17]\nM v[20:23],v[200:203],a[4:7],0\nv_readfirstlane_b32 s92,v230\nv_readfirstlane_b32 s93,v231\n" \
"M v[80:83],v[200:203],a[8:11],0\nbuffer_load_dwordx4 v245,s[40:43],s69 offen offset:2048 lds\nM v[84:87],v[200:203],a[12:15],0\nds_read_b64_tr_b16 v[200:201],v241 offset:8704\nds_read_b64_tr_b16 v[202:203],v241 offset:8960\nM v[24:27],v[204:207],a[0:3],0\ns_add_u32 s70,s92,1\ns_addc_u32 s71,s93,0\n" \
"M v[28:31],v[204:207],a[4:7],0\ns_waitcnt lgkmcnt(4)\nv_add_f32 v164,v156,v158\nM v[88:91],v[204:207],a[8:11],0\nv_add_f32 v165,v160,v162\nv_add_f32 v164,v164,v165\nM v[92:95],v[204:207],a[12:15],0\nbuffer_load_dwordx4 v246,s[40:43],s69 offen offset:3072 lds\n" \
"ds_read_b64_tr_b16 v[204:205],v242 offset:8704\nds_read_b64_tr_b16 v[206:207],v242 offset:8960\nM v[32:35],v[208:211],a[0:3],0\ns_add_u32 s92,s92,0x7f4a7c15\ns_addc_u32 s93,s93,0x9e3779b9\nM v[36:39],v[208:211],a[4:7],0\ns_add_u32 m0,s65,16384\ns_add_u32 s67,s66,256\n" \
"M v[96:99],v[208:211],a[8:11],0\nv_and_b32 v165,7,v249\nv_mov_b32 v230,s92\nM v[100:103],v[208:211],a[12:15],0\nds_read_b64_tr_b16 v[208:209],v241 offset:9216\nds_read_b64_tr_b16 v[210:211],v241 offset:9472\nM v[40:43],v[212:215],a[0:3],0\nbuffer_load_dwordx4 v224,s[36:39],s67 offen offset:0 lds\n" \
"M v[44:47],v[212:215],a[4:7],0\nv_mov_b32 v231,s93\nv_lshlrev_b32 v165,11,v165\nM v[104:107],v[212:215],a[8:11],0\ns_add_u32 s72,s72,573440\ns_lshl2_add_u32 s80,s60,s7\nM v[108:111],v[212:215],a[12:15],0\nds_read_b64_tr_b16 v[212:213],v242 offset:9216\n" \
"ds_read_b64_tr_b16 v[214:215],v242 offset:9472\nM v[48:51],v[216:219],a[0:3],0\ns_lshl_b32 s80,s80,9\ns_add_u32 s80,s80,573440\nM v[52:55],v[216:219],a[4:7],0\nbuffer_load_dwordx4 v225,s[36:39],s67 offen offset:1024 lds\nM v[112:115],v[216:219],a[8:11],0\nv_mul_f32 v164,0x3e800000,v164\n" \
"v_sub_f32 v156,v156,v164\nM v[116:119],v[216:219],a[12:15],0\nds_read_b64_tr_b16 v[216:217],v241 offset:9728\nds_read_b64_tr_b16 v[218:219],v241 offset:9984\nM v[56:59],v[220:223],a[0:3],0\nv_sub_f32 v158,v158,v164\nv_mul_f32 v156,v156,v156\nM v[60:63],v[220:223],a[4:7],0\n" \
"v_sub_f32 v160,v160,v164\nv_fmac_f32 v156,v158,v158\nM v[120:123],v[220:223],a[8:11],0\nbuffer_load_dwordx4 v226,s[36:39],s67 offen offset:2048 lds\nM v[124:127],v[220:223],a[12:15],0\nds_read_b64_tr_b16 v[220:221],v242 offset:9728\nds_read_b64_tr_b16 v[222:223],v242 offset:9984\nM v[0:3],v[192:195],a[16:19],v[0:3]\n" \
"v_add_f32 v157,v157,v159\nv_add_f32 v161,v161,v163\nM v[4:7],v[192:195],a[20:23],v[4:7]\nv_sub_f32 v162,v162,v164\nv_fmac_f32 v156,v160,v160\nM v[64:67],v[192:195],a[24:27],v[64:67]\nv_add_f32 v157,v157,v161\nv_fmac_f32 v156,v162,v162\n" \
"M v[68:71],v[192:195],a[28:31],v[68:71]\nbuffer_load_dwordx4 v227,s[36:39],s67 offen offset:3072 lds\ns_waitcnt lgkmcnt(4)\nM v[8:11],v[196:199],a[16:19],v[8:11]\nv_fmac_f32 v157,0x43000000,v156\nv_mul_f32 v157,0x3b000000,v157\nM v[12:15],v[196:199],a[20:23],v[12:15]\ns_add_u32 m0,s65,20480\n" \
"s_add_u32 s67,s66,33024\nM v[72:75],v[196:199],a[24:27],v[72:75]\nv_cvt_pk_bf16_f32 v157,0,v157\nv_add_f32 v157,0x3727c5ac,v157\nM v[76:79],v[196:199],a[28:31],v[76:79]\nv_cvt_pk_bf16_f32 v157,0,v157\nv_mov_b32 v232,s70\nM v[16:19],v[200:203],a[16:19],v[16:19]\n" \
"buffer_load_dwordx4 v224,s[36:39],s67 offen offset:0 lds\nM v[20:23],v[200:203],a[20:23],v[20:23]\nv_sqrt_f32 v157,v157\nM v[80:83],v[200:203],a[24:27],v[80:83]\nv_mov_b32 v233,s71\ns_lshr_b32 s70,s64,2\nM v[84:87],v[200:203],a[28:31],v[84:87]\nv_cvt_pk_bf16_f32 v164,0,v164\n" \
"v_cvt_pk_bf16_f32 v157,0,v157\nM v[24:27],v[204:207],a[16:19],v[24:27]\ns_add_u32 s71,s70,786432\nv_sub_f32 v136,v136,v164\nM v[28:31],v[204:207],a[20:23],v[28:31]\nbuffer_load_dwordx4 v225,s[36:39],s67 offen offset:1024 lds\nM v[88:91],v[204:207],a[24:27],v[88:91]\ns_waitcnt vmcnt(11) lgkmcnt(0)\n" \
"s_barrier\nds_read_b128 a[32:35],v228 offset:8192\nds_read_b128 a[36:39],v228 offset:10240\nds_read_b128 a[40:43],v228 offset:12288\nds_read_b128 a[44:47],v228 offset:14336\nds_read_b128 a[48:51],v229 offset:8192\nds_read_b128 a[52:55],v229 offset:10240\nds_read_b128 a[56:59],v229 offset:12288\n" \
"ds_read_b128 a[60:63],v229 offset:14336\nds_read_b64_tr_b16 v[192:193],v241 offset:16384\nds_read_b64_tr_b16 v[194:195],v241 offset:16640\nds_read_b64_tr_b16 v[196:197],v242 offset:16384\nds_read_b64_tr_b16 v[198:199],v242 offset:16640\nds_read_b64_tr_b16 v[200:201],v241 offset:16896\nds_read_b64_tr_b16 v[202:203],v241 offset:17152\nM v[92:95],v[204:207],a[28:31],v[92:95]\n" \
"ds_read_b64_tr_b16 v[204:205],v242 offset:16896\nds_read_b64_tr_b16 v[206:207],v242 offset:17152\nM v[32:35],v[208:211],a[16:19],v[32:35]\nbuffer_store_dwordx2 v[230:231],v165,s[52:55],s72 offen sc1\nM v[36:39],v[208:211],a[20:23],v[36:39]\nv_lshlrev_b32 v165,3,v249\nv_sub_f32 v137,v137,v164\nM v[96:99],v[208:211],a[24:27],v[96:99]\n" \
"buffer_load_dwordx4 v226,s[36:39],s67 offen offset:2048 lds\nM v[100:103],v[208:211],a[28:31],v[100:103]\nds_read_b64_tr_b16 v[208:209],v241 offset:17408\nds_read_b64_tr_b16 v[210:211],v241 offset:17664\nM v[40:43],v[212:215],a[16:19],v[40:43]\nv_rcp_f32 v157,v157\nM v[44:47],v[212:215],a[20:23],v[44:47]\nv_sub_f32 v138,v138,v164\n" \
"v_sub_f32 v139,v139,v164\nM v[104:107],v[212:215],a[24:27],v[104:107]\nv_sub_f32 v140,v140,v164\nv_sub_f32 v141,v141,v164\nM v[108:111],v[212:215],a[28:31],v[108:111]\nbuffer_load_dwordx4 v227,s[36:39],s67 offen offset:3072 lds\nds_read_b64_tr_b16 v[212:213],v242 offset:17408\nds_read_b64_tr_b16 v[214:215],v242 offset:17664\n" \
"M v[48:51],v[216:219],a[16:19],v[48:51]\nv_sub_f32 v142,v142,v164\nv_sub_f32 v143,v143,v164\nM v[52:55],v[216:219],a[20:23],v[52:55]\ns_add_u32 m0,s65,0\ns_add_u32 s67,s66,384\nM v[112:115],v[216:219],a[24:27],v[112:115]\nv_sub_f32 v144,v144,v164\n" \
"v_sub_f32 v145,v145,v164\nM v[116:119],v[216:219],a[28:31],v[116:119]\nds_read_b64_tr_b16 v[216:217],v241 offset:17920\nds_read_b64_tr_b16 v[218:219],v241 offset:18176\nM v[56:59],v[220:223],a[16:19],v[56:59]\nbuffer_load_dwordx4 v224,s[36:39],s67 offen offset:0 lds\nM v[60:63],v[220:223],a[20:23],v[60:63]\nv_sub_f32 v146,v146,v164\n" \
"v_sub_f32 v147,v147,v164\nM v[120:123],v[220:223],a[24:27],v[120:123]\nv_sub_f32 v148,v148,v164\nv_sub_f32 v149,v149,v164\nM v[124:127],v[220:223],a[28:31],v[124:127]\nds_read_b64_tr_b16 v[220:221],v242 offset:17920\nds_read_b64_tr_b16 v[222:223],v242 offset:18176\ns_waitcnt lgkmcnt(6)\n" \
"M v[0:3],v[192:195],a[32:35],v[0:3]\nv_sub_f32 v150,v150,v164\nv_sub_f32 v151,v151,v164\nM v[4:7],v[192:195],a[36:39],v[4:7]\nbuffer_load_dwordx4 v225,s[36:39],s67 offen offset:1024 lds\nM v[64:67],v[192:195],a[40:43],v[64:67]\nv_cvt_pk_bf16_f32 v136,0,v136\nv_cvt_pk_bf16_f32 v137,0,v137\n" \
"M v[68:71],v[192:195],a[44:47],v[68:71]\nds_read_b64_tr_b16 v[192:193],v241 offset:24576\nds_read_b64_tr_b16 v[194:195],v241 offset:24832\nM v[8:11],v[196:199],a[32:35],v[8:11]\nv_cvt_pk_bf16_f32 v138,0,v138\nv_cvt_pk_bf16_f32 v139,0,v139\nM v[12:15],v[196:199],a[36:39],v[12:15]\nv_cvt_pk_bf16_f32 v140,0,v140\n" \
"v_cvt_pk_bf16_f32 v141,0,v141\nM v[72:75],v[196:199],a[40:43],v[72:75]\nbuffer_load_dwordx4 v226,s[36:39],s67 offen offset:2048 lds\nM v[76:79],v[196:199],a[44:47],v[76:79]\nds_read_b64_tr_b16 v[196:197],v242 offset:24576\nds_read_b64_tr_b16 v[198:199],v242 offset:24832\nM v[16:19],v[200:203],a[32:35],v[16:19]\nv_cvt_pk_bf16_f32 v142,0,v142\n" \
"v_cvt_pk_bf16_f32 v143,0,v143\nM v[20:23],v[200:203],a[36:39],v[20:23]\nv_cvt_pk_bf16_f32 v144,0,v144\nv_cvt_pk_bf16_f32 v145,0,v145\nM v[80:83],v[200:203],a[40:43],v[80:83]\nv_cvt_pk_bf16_f32 v146,0,v146\nv_cvt_pk_bf16_f32 v147,0,v147\nM v[84:87],v[200:203],a[44:47],v[84:87]\n" \
"buffer_load_dwordx4 v227,s[36:39],s67 offen offset:3072 lds\nds_read_b64_tr_b16 v[200:201],v241 offset:25088\nds_read_b64_tr_b16 v[202:203],v241 offset:25344\nM v[24:27],v[204:207],a[32:35],v[24:27]\nv_cvt_pk_bf16_f32 v148,0,v148\nv_cvt_pk_bf16_f32 v149,0,v149\nM v[28:31],v[204:207],a[36:39],v[28:31]\ns_add_u32 m0,s65,4096\n" \
"s_add_u32 s67,s66,33152\nM v[88:91],v[204:207],a[40:43],v[88:91]\nv_cvt_pk_bf16_f32 v150,0,v150\nv_cvt_pk_bf16_f32 v151,0,v151\nM v[92:95],v[204:207],a[44:47],v[92:95]\nds_read_b64_tr_b16 v[204:205],v242 offset:25088\nds_read_b64_tr_b16 v[206:207],v242 offset:25344\nM v[32:35],v[208:211],a[32:35],v[32:35]\n" \
"buffer_load_dwordx4 v224,s[36:39],s67 offen offset:0 lds\nM v[36:39],v[208:211],a[36:39],v[36:39]\nv_mul_f32 v136,v136,v157\nv_mul_f32 v137,v137,v157\nM v[96:99],v[208:211],a[40:43],v[96:99]\nv_mul_f32 v138,v138,v157\nv_mul_f32 v139,v139,v157\nM v[100:103],v[208:211],a[44:47],v[100:103]\n" \
"ds_read_b64_tr_b16 v[208:209],v241 offset:25600\nds_read_b64_tr_b16 v[210:211],v241 offset:25856\ns_waitcnt lgkmcnt(6)\nM v[40:43],v[212:215],a[32:35],v[40:43]\nv_mul_f32 v140,v140,v157\nv_mul_f32 v141,v141,v157\nM v[44:47],v[212:215],a[36:39],v[44:47]\nbuffer_load_dwordx4 v225,s[36:39],s67 offen offset:1024 lds\n" \
"M v[104:107],v[212:215],a[40:43],v[104:107]\nv_mul_f32 v142,v142,v157\nv_mul_f32 v143,v143,v157\nM v[108:111],v[212:215],a[44:47],v[108:111]\nds_read_b64_tr_b16 v[212:213],v242 offset:25600\nds_read_b64_tr_b16 v[214:215],v242 offset:25856\nM v[48:51],v[216:219],a[32:35],v[48:51]\nv_mul_f32 v144,v144,v157\n" \
"v_mul_f32 v145,v145,v157\nM v[52:55],v[216:219],a[36:39],v[52:55]\nv_mul_f32 v146,v146,v157\nv_mul_f32 v147,v147,v157\nM v[112:115],v[216:219],a[40:43],v[112:115]\nbuffer_load_dwordx4 v226,s[36:39],s67 offen offset:2048 lds\nM v[116:119],v[216:219],a[44:47],v[116:119]\nds_read_b64_tr_b16 v[216:217],v241 offset:26112\n" \
"ds_read_b64_tr_b16 v[218:219],v241 offset:26368\nM v[56:59],v[220:223],a[32:35],v[56:59]\nv_mul_f32 v148,v148,v157\nv_mul_f32 v149,v149,v157\nM v[60:63],v[220:223],a[36:39],v[60:63]\nv_mul_f32 v150,v150,v157\nv_mul_f32 v151,v151,v157\nM v[120:123],v[220:223],a[40:43],v[120:123]\n" \
"v_and_b32 v169,3,v249\nv_mul_u32_u24 v169,12288,v169\nM v[124:127],v[220:223],a[44:47],v[124:127]\nbuffer_load_dwordx4 v227,s[36:39],s67 offen offset:3072 lds\nds_read_b64_tr_b16 v[220:221],v242 offset:26112\nds_read_b64_tr_b16 v[222:223],v242 offset:26368\nM v[0:3],v[192:195],a[48:51],v[0:3]\nv_and_b32 v170,28,v249\n" \
"v_add_u32 v169,v169,v170\nM v[4:7],v[192:195],a[52:55],v[4:7]\ns_add_u32 m0,s64,147456\ns_add_u32 s69,s68,1572864\nM v[64:67],v[192:195],a[56:59],v[64:67]\ns_and_b32 s78,s62,1\ns_lshl2_add_u32 s78,s78,s7\nM v[68:71],v[192:195],a[60:63],v[68:71]\n" \
"s_lshl_b32 s78,s78,13\nv_lshlrev_b32 v252,7,v249\nM v[8:11],v[196:199],a[48:51],v[8:11]\nbuffer_load_dwordx4 v243,s[40:43],s69 offen offset:0 lds\nM v[12:15],v[196:199],a[52:55],v[12:15]\ns_add_u32 s78,s78,.Lattn-.Lpc0\nv_lshrrev_b32 v251,3,v249\nM v[72:75],v[196:199],a[56:59],v[72:75]\n" \
"s_cmp_eq_u32 s6,0\ns_cselect_b32 s72,s75,0x8000000\nM v[76:79],v[196:199],a[60:63],v[76:79]\ns_add_u32 s72,s72,94208\ns_waitcnt lgkmcnt(2)\nM v[16:19],v[200:203],a[48:51],v[16:19]\nv_lshlrev_b32 v170,3,v170\nv_cvt_pk_bf16_f32 v144,0,v144\n" \
"M v[20:23],v[200:203],a[52:55],v[20:23]\nbuffer_load_dwordx4 v244,s[40:43],s69 offen offset:1024 lds\nM v[80:83],v[200:203],a[56:59],v[80:83]\nv_cvt_pk_bf16_f32 v145,0,v145\nv_cvt_pk_bf16_f32 v146,0,v146\nM v[84:87],v[200:203],a[60:63],v[84:87]\nv_cvt_pk_bf16_f32 v147,0,v147\nv_cvt_pk_bf16_f32 v136,0,v136\n" \
"M v[24:27],v[204:207],a[48:51],v[24:27]\nv_cvt_pk_bf16_f32 v137,0,v137\nv_cvt_pk_bf16_f32 v138,0,v138\nM v[28:31],v[204:207],a[52:55],v[28:31]\nv_cvt_pk_bf16_f32 v139,0,v139\nv_cvt_pk_bf16_f32 v140,0,v140\nM v[88:91],v[204:207],a[56:59],v[88:91]\nbuffer_load_dwordx4 v245,s[40:43],s69 offen offset:2048 lds\n" \
"s_waitcnt vmcnt(11) lgkmcnt(0)\ns_barrier\nds_read_b128 a[64:67],v228 offset:16384\nds_read_b128 a[68:71],v228 offset:18432\nds_read_b128 a[72:75],v228 offset:20480\nds_read_b128 a[76:79],v228 offset:22528\nds_read_b128 a[80:83],v229 offset:16384\nds_read_b128 a[84:87],v229 offset:18432\n" \
"ds_read_b128 a[88:91],v229 offset:20480\nds_read_b128 a[92:95],v229 offset:22528\nM v[92:95],v[204:207],a[60:63],v[92:95]\nv_cvt_pk_bf16_f32 v141,0,v141\nv_cvt_pk_bf16_f32 v142,0,v142\nM v[32:35],v[208:211],a[48:51],v[32:35]\nv_cvt_pk_bf16_f32 v143,0,v143\nv_cvt_pk_bf16_f32 v148,0,v148\n" \
"M v[36:39],v[208:211],a[52:55],v[36:39]\nv_cvt_pk_bf16_f32 v149,0,v149\nv_bfe_u32 v250,v249,3,2\nM v[96:99],v[208:211],a[56:59],v[96:99]\nv_cvt_pk_bf16_f32 v150,0,v150\nv_cvt_pk_bf16_f32 v151,0,v151\nM v[100:103],v[208:211],a[60:63],v[100:103]\nbuffer_load_dwordx4 v246,s[40:43],s69 offen offset:3072 lds\n" \
"M v[40:43],v[212:215],a[48:51],v[40:43]\ns_mov_b32 s98,0x4000\ns_add_u32 s10,s61,1\nM v[44:47],v[212:215],a[52:55],v[44:47]\ns_add_u32 m0,s65,8192\ns_add_u32 s67,s66,512\nds_read_b64_tr_b16 v[192:193],v241 offset:32768\nds_read_b64_tr_b16 v[194:195],v241 offset:33024\n" \
"M v[104:107],v[212:215],a[56:59],v[104:107]\nds_read_b64_tr_b16 v[196:197],v242 offset:32768\ns_add_u32 s11,s61,2\nM v[108:111],v[212:215],a[60:63],v[108:111]\nds_read_b64_tr_b16 v[198:199],v242 offset:33024\nds_read_b64_tr_b16 v[200:201],v241 offset:33280\nM v[48:51],v[216:219],a[48:51],v[48:51]\nbuffer_load_dwordx4 v224,s[36:39],s67 offen offset:0 lds\n" \
"M v[52:55],v[216:219],a[52:55],v[52:55]\nds_read_b64_tr_b16 v[202:203],v241 offset:33536\nds_read_b64_tr_b16 v[204:205],v242 offset:33280\nM v[112:115],v[216:219],a[56:59],v[112:115]\nds_read_b64_tr_b16 v[206:207],v242 offset:33536\nds_read_b64_tr_b16 v[208:209],v241 offset:33792\nM v[116:119],v[216:219],a[60:63],v[116:119]\nds_read_b64_tr_b16 v[210:211],v241 offset:34048\n" \
"ds_read_b64_tr_b16 v[212:213],v242 offset:33792\nM v[56:59],v[220:223],a[48:51],v[56:59]\nds_read_b64_tr_b16 v[214:215],v242 offset:34048\nds_read_b64_tr_b16 v[216:217],v241 offset:34304\nM v[60:63],v[220:223],a[52:55],v[60:63]\nbuffer_load_dwordx4 v225,s[36:39],s67 offen offset:1024 lds\nM v[120:123],v[220:223],a[56:59],v[120:123]\nds_read_b64_tr_b16 v[218:219],v241 offset:34560\n" \
"s_add_u32 s14,s61,3\nM v[124:127],v[220:223],a[60:63],v[124:127]\nds_read_b64_tr_b16 v[220:221],v242 offset:34304\ns_waitcnt lgkmcnt(13)\nM v[0:3],v[192:195],a[64:67],v[0:3]\nds_read_b64_tr_b16 v[222:223],v242 offset:34560\ns_and_b32 s10,s10,3\nM v[4:7],v[192:195],a[68:71],v[4:7]\n" \
"s_and_b32 s11,s11,3\ns_and_b32 s14,s14,3\nM v[64:67],v[192:195],a[72:75],v[64:67]\nbuffer_load_dwordx4 v226,s[36:39],s67 offen offset:2048 lds\nM v[68:71],v[192:195],a[76:79],v[68:71]\nds_read_b64_tr_b16 v[192:193],v241 offset:40960\ns_waitcnt lgkmcnt(11)\nM v[8:11],v[196:199],a[64:67],v[8:11]\n" \
"ds_read_b64_tr_b16 v[194:195],v241 offset:41216\ns_mov_b32 s47,0x20000\nM v[12:15],v[196:199],a[68:71],v[12:15]\ns_mov_b32 s51,0x20000\ns_mov_b32 s94,0xc000\nM v[72:75],v[196:199],a[72:75],v[72:75]\ns_lshl_b32 s10,s10,13\ns_lshl_b32 s11,s11,13\n" \
"M v[76:79],v[196:199],a[76:79],v[76:79]\nbuffer_load_dwordx4 v227,s[36:39],s67 offen offset:3072 lds\nM v[16:19],v[200:203],a[64:67],v[16:19]\nds_read_b64_tr_b16 v[196:197],v242 offset:40960\nds_read_b64_tr_b16 v[198:199],v242 offset:41216\nM v[20:23],v[200:203],a[68:71],v[20:23]\ns_add_u32 m0,s65,12288\ns_add_u32 s67,s66,33280\n" \
"M v[80:83],v[200:203],a[72:75],v[80:83]\ns_lshl_b32 s14,s14,13\ns_mov_b32 s82,0xf0000\nM v[84:87],v[200:203],a[76:79],v[84:87]\nds_read_b64_tr_b16 v[200:201],v241 offset:41472\ns_waitcnt lgkmcnt(5)\nM v[24:27],v[204:207],a[64:67],v[24:27]\nbuffer_load_dwordx4 v224,s[36:39],s67 offen offset:0 lds\n" \
"M v[28:31],v[204:207],a[68:71],v[28:31]\nds_read_b64_tr_b16 v[202:203],v241 offset:41728\ns_mov_b32 s83,0xfff00ff\nM v[88:91],v[204:207],a[72:75],v[88:91]\ns_mov_b32 s84,0x1f0001\ns_mov_b32 s85,0x1fff01ff\nM v[92:95],v[204:207],a[76:79],v[92:95]\nds_read_b64_tr_b16 v[204:205],v242 offset:41472\n" \
"ds_read_b64_tr_b16 v[206:207],v242 offset:41728\nM v[32:35],v[208:211],a[64:67],v[32:35]\ns_mov_b32 s86,0x3f0003\ns_mov_b32 s87,0x3fff03ff\nM v[36:39],v[208:211],a[68:71],v[36:39]\nbuffer_load_dwordx4 v225,s[36:39],s67 offen offset:1024 lds\nM v[96:99],v[208:211],a[72:75],v[96:99]\ns_mov_b32 s88,0x7f0007\n" \
"s_mov_b32 s89,0x7fff07ff\nM v[100:103],v[208:211],a[76:79],v[100:103]\nds_read_b64_tr_b16 v[208:209],v241 offset:41984\nds_read_b64_tr_b16 v[210:211],v241 offset:42240\nM v[40:43],v[212:215],a[64:67],v[40:43]\ns_mov_b64 s[90:91],-1\ns_mov_b32 s28,0x5f800000\nM v[44:47],v[212:215],a[68:71],v[44:47]\n" \
"s_mov_b32 s99,0\nM v[104:107],v[212:215],a[72:75],v[104:107]\nbuffer_load_dwordx4 v226,s[36:39],s67 offen offset:2048 lds\nM v[108:111],v[212:215],a[76:79],v[108:111]\nds_read_b64_tr_b16 v[212:213],v242 offset:41984\nds_read_b64_tr_b16 v[214:215],v242 offset:42240\nM v[48:51],v[216:219],a[64:67],v[48:51]\nM v[52:55],v[216:219],a[68:71],v[52:55]\n" \
"M v[112:115],v[216:219],a[72:75],v[112:115]\nM v[116:119],v[216:219],a[76:79],v[116:119]\nbuffer_load_dwordx4 v227,s[36:39],s67 offen offset:3072 lds\nM v[56:59],v[220:223],a[64:67],v[56:59]\nds_read_b64_tr_b16 v[216:217],v241 offset:42496\nds_read_b64_tr_b16 v[218:219],v241 offset:42752\nM v[60:63],v[220:223],a[68:71],v[60:63]\ns_add_u32 m0,s64,98304\n" \
"s_add_u32 s69,s68,2097152\nM v[120:123],v[220:223],a[72:75],v[120:123]\nM v[124:127],v[220:223],a[76:79],v[124:127]\nds_read_b64_tr_b16 v[220:221],v242 offset:42496\ns_waitcnt lgkmcnt(7)\nM v[0:3],v[192:195],a[80:83],v[0:3]\nbuffer_load_dwordx4 v243,s[40:43],s69 offen offset:0 lds\nM v[4:7],v[192:195],a[84:87],v[4:7]\n" \
"ds_read_b64_tr_b16 v[222:223],v242 offset:42752\nM v[64:67],v[192:195],a[88:91],v[64:67]\nM v[68:71],v[192:195],a[92:95],v[68:71]\nM v[8:11],v[196:199],a[80:83],v[8:11]\nM v[12:15],v[196:199],a[84:87],v[12:15]\nbuffer_load_dwordx4 v244,s[40:43],s69 offen offset:1024 lds\nM v[72:75],v[196:199],a[88:91],v[72:75]\nM v[76:79],v[196:199],a[92:95],v[76:79]\n" \
"M v[16:19],v[200:203],a[80:83],v[16:19]\nM v[20:23],v[200:203],a[84:87],v[20:23]\nM v[80:83],v[200:203],a[88:91],v[80:83]\nbuffer_load_dwordx4 v245,s[40:43],s69 offen offset:2048 lds\nM v[84:87],v[200:203],a[92:95],v[84:87]\nM v[24:27],v[204:207],a[80:83],v[24:27]\nM v[28:31],v[204:207],a[84:87],v[28:31]\nM v[88:91],v[204:207],a[88:91],v[88:91]\n" \
"s_waitcnt vmcnt(11) lgkmcnt(0)\ns_barrier\nds_read_b128 a[96:99],v228 offset:0\nds_read_b128 a[100:103],v228 offset:2048\nds_read_b128 a[104:107],v228 offset:4096\nds_read_b128 a[108:111],v228 offset:6144\nds_read_b128 a[112:115],v229 offset:0\nds_read_b128 a[116:119],v229 offset:2048\n" \
"ds_read_b128 a[120:123],v229 offset:4096\nds_read_b128 a[124:127],v229 offset:6144\nM v[92:95],v[204:207],a[92:95],v[92:95]\nbuffer_load_dwordx4 v246,s[40:43],s69 offen offset:3072 lds\nM v[32:35],v[208:211],a[80:83],v[32:35]\nM v[36:39],v[208:211],a[84:87],v[36:39]\ns_add_u32 m0,s65,16384\ns_add_u32 s67,s66,640\n" \
"M v[96:99],v[208:211],a[88:91],v[96:99]\nM v[100:103],v[208:211],a[92:95],v[100:103]\nM v[40:43],v[212:215],a[80:83],v[40:43]\nbuffer_load_dwordx4 v224,s[36:39],s67 offen offset:0 lds\nM v[44:47],v[212:215],a[84:87],v[44:47]\nds_read_b64_tr_b16 v[192:193],v241 offset:49152\nds_read_b64_tr_b16 v[194:195],v241 offset:49408\nM v[104:107],v[212:215],a[88:91],v[104:107]\n" \
"ds_read_b64_tr_b16 v[196:197],v242 offset:49152\nM v[108:111],v[212:215],a[92:95],v[108:111]\nds_read_b64_tr_b16 v[198:199],v242 offset:49408\nds_read_b64_tr_b16 v[200:201],v241 offset:49664\nM v[48:51],v[216:219],a[80:83],v[48:51]\nds_read_b64_tr_b16 v[202:203],v241 offset:49920\nds_read_b64_tr_b16 v[204:205],v242 offset:49664\nM v[52:55],v[216:219],a[84:87],v[52:55]\n" \
"buffer_load_dwordx4 v225,s[36:39],s67 offen offset:1024 lds\nM v[112:115],v[216:219],a[88:91],v[112:115]\nds_read_b64_tr_b16 v[206:207],v242 offset:49920\nds_read_b64_tr_b16 v[208:209],v241 offset:50176\nM v[116:119],v[216:219],a[92:95],v[116:119]\nds_read_b64_tr_b16 v[210:211],v241 offset:50432\nds_read_b64_tr_b16 v[212:213],v242 offset:50176\nM v[56:59],v[220:223],a[80:83],v[56:59]\n" \
"ds_read_b64_tr_b16 v[214:215],v242 offset:50432\nds_read_b64_tr_b16 v[216:217],v241 offset:50688\nM v[60:63],v[220:223],a[84:87],v[60:63]\nds_read_b64_tr_b16 v[218:219],v241 offset:50944\nM v[120:123],v[220:223],a[88:91],v[120:123]\nbuffer_load_dwordx4 v226,s[36:39],s67 offen offset:2048 lds\nM v[124:127],v[220:223],a[92:95],v[124:127]\nds_read_b64_tr_b16 v[220:221],v242 offset:50688\n" \
"s_waitcnt lgkmcnt(13)\nM v[0:3],v[192:195],a[96:99],v[0:3]\nds_read_b64_tr_b16 v[222:223],v242 offset:50944\nM v[4:7],v[192:195],a[100:103],v[4:7]\nM v[64:67],v[192:195],a[104:107],v[64:67]\nM v[68:71],v[192:195],a[108:111],v[68:71]\nbuffer_load_dwordx4 v227,s[36:39],s67 offen offset:3072 lds\ns_waitcnt lgkmcnt(11)\n" \
"M v[8:11],v[196:199],a[96:99],v[8:11]\nds_read_b64_tr_b16 v[192:193],v241 offset:57344\nds_read_b64_tr_b16 v[194:195],v241 offset:57600\nM v[12:15],v[196:199],a[100:103],v[12:15]\ns_add_u32 m0,s65,20480\ns_add_u32 s67,s66,33408\nM v[72:75],v[196:199],a[104:107],v[72:75]\nM v[76:79],v[196:199],a[108:111],v[76:79]\n" \
"ds_read_b64_tr_b16 v[196:197],v242 offset:57344\ns_waitcnt lgkmcnt(8)\nM v[16:19],v[200:203],a[96:99],v[16:19]\nbuffer_load_dwordx4 v224,s[36:39],s67 offen offset:0 lds\nM v[20:23],v[200:203],a[100:103],v[20:23]\nds_read_b64_tr_b16 v[198:199],v242 offset:57600\nM v[80:83],v[200:203],a[104:107],v[80:83]\nM v[84:87],v[200:203],a[108:111],v[84:87]\n" \
"ds_read_b64_tr_b16 v[200:201],v241 offset:57856\nds_read_b64_tr_b16 v[202:203],v241 offset:58112\nM v[24:27],v[204:207],a[96:99],v[24:27]\nM v[28:31],v[204:207],a[100:103],v[28:31]\nbuffer_load_dwordx4 v225,s[36:39],s67 offen offset:1024 lds\nM v[88:91],v[204:207],a[104:107],v[88:91]\nM v[92:95],v[204:207],a[108:111],v[92:95]\nds_read_b64_tr_b16 v[204:205],v242 offset:57856\n" \
"ds_read_b64_tr_b16 v[206:207],v242 offset:58112\nM v[32:35],v[208:211],a[96:99],v[32:35]\nM v[36:39],v[208:211],a[100:103],v[36:39]\nM v[96:99],v[208:211],a[104:107],v[96:99]\nbuffer_load_dwordx4 v226,s[36:39],s67 offen offset:2048 lds\nM v[100:103],v[208:211],a[108:111],v[100:103]\nds_read_b64_tr_b16 v[208:209],v241 offset:58368\ns_waitcnt lgkmcnt(7)\n" \
"M v[40:43],v[212:215],a[96:99],v[40:43]\nds_read_b64_tr_b16 v[210:211],v241 offset:58624\nM v[44:47],v[212:215],a[100:103],v[44:47]\nM v[104:107],v[212:215],a[104:107],v[104:107]\nM v[108:111],v[212:215],a[108:111],v[108:111]\nbuffer_load_dwordx4 v227,s[36:39],s67 offen offset:3072 lds\nM v[48:51],v[216:219],a[96:99],v[48:51]\nds_read_b64_tr_b16 v[212:213],v242 offset:58368\n" \
"ds_read_b64_tr_b16 v[214:215],v242 offset:58624\nM v[52:55],v[216:219],a[100:103],v[52:55]\ns_add_u32 m0,s64,114688\ns_add_u32 s69,s68,2621440\nM v[112:115],v[216:219],a[104:107],v[112:115]\nM v[116:119],v[216:219],a[108:111],v[116:119]\nds_read_b64_tr_b16 v[216:217],v241 offset:58880\nds_read_b64_tr_b16 v[218:219],v241 offset:59136\n" \
"M v[56:59],v[220:223],a[96:99],v[56:59]\nbuffer_load_dwordx4 v243,s[40:43],s69 offen offset:0 lds\nM v[60:63],v[220:223],a[100:103],v[60:63]\nM v[120:123],v[220:223],a[104:107],v[120:123]\nM v[124:127],v[220:223],a[108:111],v[124:127]\nds_read_b64_tr_b16 v[220:221],v242 offset:58880\nds_read_b64_tr_b16 v[222:223],v242 offset:59136\nM v[0:3],v[192:195],a[112:115],v[0:3]\n" \
"buffer_load_dwordx2 v[166:167],v165,s[52:55],s80 offen sc1\nM v[4:7],v[192:195],a[116:119],v[4:7]\nbuffer_load_dwordx4 v244,s[40:43],s69 offen offset:1024 lds\nM v[64:67],v[192:195],a[120:123],v[64:67]\nM v[68:71],v[192:195],a[124:127],v[68:71]\ns_waitcnt lgkmcnt(6)\nM v[8:11],v[196:199],a[112:115],v[8:11]\nM v[12:15],v[196:199],a[116:119],v[12:15]\n" \
"M v[72:75],v[196:199],a[120:123],v[72:75]\nbuffer_load_dwordx4 v245,s[40:43],s69 offen offset:2048 lds\nM v[76:79],v[196:199],a[124:127],v[76:79]\nM v[16:19],v[200:203],a[112:115],v[16:19]\nM v[20:23],v[200:203],a[116:119],v[20:23]\nM v[80:83],v[200:203],a[120:123],v[80:83]\nM v[84:87],v[200:203],a[124:127],v[84:87]\nbuffer_load_dwordx4 v246,s[40:43],s69 offen offset:3072 lds\n" \
"M v[24:27],v[204:207],a[112:115],v[24:27]\nM v[28:31],v[204:207],a[116:119],v[28:31]\ns_add_u32 m0,s65,0\ns_add_u32 s67,s66,768\nM v[88:91],v[204:207],a[120:123],v[88:91]\ns_waitcnt vmcnt(13) lgkmcnt(0)\ns_barrier\nM v[92:95],v[204:207],a[124:127],v[92:95]\n" \
"ds_read_b128 a[128:131],v228 offset:8192\nds_read_b64_tr_b16 v[192:193],v241 offset:0\nM v[32:35],v[208:211],a[112:115],v[32:35]\nbuffer_load_dwordx4 v224,s[36:39],s67 offen offset:0 lds\nM v[36:39],v[208:211],a[116:119],v[36:39]\nds_read_b64_tr_b16 v[194:195],v241 offset:256\nds_read_b128 a[132:135],v228 offset:10240\nM v[96:99],v[208:211],a[120:123],v[96:99]\n" \
"ds_read_b128 a[136:139],v228 offset:12288\nM v[100:103],v[208:211],a[124:127],v[100:103]\nds_read_b128 a[140:143],v228 offset:14336\nM v[40:43],v[212:215],a[112:115],v[40:43]\nds_read_b64_tr_b16 v[196:197],v242 offset:0\nds_read_b64_tr_b16 v[198:199],v242 offset:256\nM v[44:47],v[212:215],a[116:119],v[44:47]\nbuffer_load_dwordx4 v225,s[36:39],s67 offen offset:1024 lds\n" \
"M v[104:107],v[212:215],a[120:123],v[104:107]\nds_read_b64_tr_b16 v[200:201],v241 offset:512\nds_read_b64_tr_b16 v[202:203],v241 offset:768\nM v[108:111],v[212:215],a[124:127],v[108:111]\nds_read_b64_tr_b16 v[204:205],v242 offset:512\nds_read_b64_tr_b16 v[206:207],v242 offset:768\nM v[48:51],v[216:219],a[112:115],v[48:51]\nds_read_b64_tr_b16 v[208:209],v241 offset:1024\n" \
"ds_read_b64_tr_b16 v[210:211],v241 offset:1280\nM v[52:55],v[216:219],a[116:119],v[52:55]\nds_read_b64_tr_b16 v[212:213],v242 offset:1024\nds_read_b64_tr_b16 v[214:215],v242 offset:1280\nM v[112:115],v[216:219],a[120:123],v[112:115]\nbuffer_load_dwordx4 v226,s[36:39],s67 offen offset:2048 lds\nM v[116:119],v[216:219],a[124:127],v[116:119]\nds_read_b64_tr_b16 v[216:217],v241 offset:1536\n" \
"ds_read_b64_tr_b16 v[218:219],v241 offset:1792\nM v[56:59],v[220:223],a[112:115],v[56:59]\nds_read_b128 a[144:147],v229 offset:8192\nds_read_b128 a[148:151],v229 offset:10240\nM v[60:63],v[220:223],a[116:119],v[60:63]\nds_read_b128 a[152:155],v229 offset:12288\nM v[120:123],v[220:223],a[120:123],v[120:123]\nds_read_b128 a[156:159],v229 offset:14336\n" \
"M v[124:127],v[220:223],a[124:127],v[124:127]\nbuffer_load_dwordx4 v227,s[36:39],s67 offen offset:3072 lds\ns_waitcnt lgkmcnt(14)\nM v[0:3],v[192:195],a[128:131],v[0:3]\nds_read_b64_tr_b16 v[220:221],v242 offset:1536\nds_read_b64_tr_b16 v[222:223],v242 offset:1792\nM v[4:7],v[192:195],a[132:135],v[4:7]\ns_add_u32 m0,s65,4096\n" \
"s_add_u32 s67,s66,33536\nM v[64:67],v[192:195],a[136:139],v[64:67]\nM v[68:71],v[192:195],a[140:143],v[68:71]\nds_read_b64_tr_b16 v[192:193],v241 offset:8192\nds_read_b64_tr_b16 v[194:195],v241 offset:8448\nM v[8:11],v[196:199],a[128:131],v[8:11]\nbuffer_load_dwordx4 v224,s[36:39],s67 offen offset:0 lds\nM v[12:15],v[196:199],a[132:135],v[12:15]\n" \
"M v[72:75],v[196:199],a[136:139],v[72:75]\nM v[76:79],v[196:199],a[140:143],v[76:79]\nds_read_b64_tr_b16 v[196:197],v242 offset:8192\ns_waitcnt lgkmcnt(9)\nM v[16:19],v[200:203],a[128:131],v[16:19]\nds_read_b64_tr_b16 v[198:199],v242 offset:8448\nM v[20:23],v[200:203],a[132:135],v[20:23]\nbuffer_load_dwordx4 v225,s[36:39],s67 offen offset:1024 lds\n" \
"M v[80:83],v[200:203],a[136:139],v[80:83]\nM v[84:87],v[200:203],a[140:143],v[84:87]\nds_read_b64_tr_b16 v[200:201],v241 offset:8704\nds_read_b64_tr_b16 v[202:203],v241 offset:8960\nM v[24:27],v[204:207],a[128:131],v[24:27]\nM v[28:31],v[204:207],a[132:135],v[28:31]\nM v[88:91],v[204:207],a[136:139],v[88:91]\nbuffer_load_dwordx4 v226,s[36:39],s67 offen offset:2048 lds\n" \
"M v[92:95],v[204:207],a[140:143],v[92:95]\nds_read_b64_tr_b16 v[204:205],v242 offset:8704\nds_read_b64_tr_b16 v[206:207],v242 offset:8960\nM v[32:35],v[208:211],a[128:131],v[32:35]\nM v[36:39],v[208:211],a[132:135],v[36:39]\nM v[96:99],v[208:211],a[136:139],v[96:99]\nM v[100:103],v[208:211],a[140:143],v[100:103]\nbuffer_load_dwordx4 v227,s[36:39],s67 offen offset:3072 lds\n" \
"M v[40:43],v[212:215],a[128:131],v[40:43]\nds_read_b64_tr_b16 v[208:209],v241 offset:9216\nds_read_b64_tr_b16 v[210:211],v241 offset:9472\nM v[44:47],v[212:215],a[132:135],v[44:47]\ns_add_u32 m0,s64,131072\ns_add_u32 s69,s68,3145728\nM v[104:107],v[212:215],a[136:139],v[104:107]\nM v[108:111],v[212:215],a[140:143],v[108:111]\n" \
"ds_read_b64_tr_b16 v[212:213],v242 offset:9216\nds_read_b64_tr_b16 v[214:215],v242 offset:9472\nM v[48:51],v[216:219],a[128:131],v[48:51]\nbuffer_load_dwordx4 v243,s[40:43],s69 offen offset:0 lds\nM v[52:55],v[216:219],a[132:135],v[52:55]\nM v[112:115],v[216:219],a[136:139],v[112:115]\nM v[116:119],v[216:219],a[140:143],v[116:119]\nds_read_b64_tr_b16 v[216:217],v241 offset:9728\n" \
"s_waitcnt lgkmcnt(7)\nM v[56:59],v[220:223],a[128:131],v[56:59]\nds_read_b64_tr_b16 v[218:219],v241 offset:9984\nM v[60:63],v[220:223],a[132:135],v[60:63]\nbuffer_load_dwordx4 v244,s[40:43],s69 offen offset:1024 lds\nM v[120:123],v[220:223],a[136:139],v[120:123]\nM v[124:127],v[220:223],a[140:143],v[124:127]\nds_read_b64_tr_b16 v[220:221],v242 offset:9728\n" \
"ds_read_b64_tr_b16 v[222:223],v242 offset:9984\nM v[0:3],v[192:195],a[144:147],v[0:3]\nM v[4:7],v[192:195],a[148:151],v[4:7]\nM v[64:67],v[192:195],a[152:155],v[64:67]\nbuffer_load_dwordx4 v245,s[40:43],s69 offen offset:2048 lds\nM v[68:71],v[192:195],a[156:159],v[68:71]\nM v[8:11],v[196:199],a[144:147],v[8:11]\ns_waitcnt vmcnt(14)\n" \
"v_xor_b32 v166,v166,v230\nM v[12:15],v[196:199],a[148:151],v[12:15]\nv_xor_b32 v167,v167,v231\nv_or_b32 v166,v166,v167\nM v[72:75],v[196:199],a[152:155],v[72:75]\nv_cmp_ne_u32 vcc,0,v166\nM v[76:79],v[196:199],a[156:159],v[76:79]\nbuffer_load_dwordx4 v246,s[40:43],s69 offen offset:3072 lds\n" \
"M v[16:19],v[200:203],a[144:147],v[16:19]\ns_cbranch_vccnz .Lhs\n.Lhb:\nv_bfe_u32 v230,v249,2,3\nM v[20:23],v[200:203],a[148:151],v[20:23]\ns_add_u32 m0,s65,8192\ns_add_u32 s67,s66,896\nM v[80:83],v[200:203],a[152:155],v[80:83]\n" \
"v_and_b32 v231,0x70,v243\nv_lshl_or_b32 v230,v230,10,v231\nM v[84:87],v[200:203],a[156:159],v[84:87]\nv_and_b32 v231,16,v249\ns_waitcnt lgkmcnt(0)\nM v[24:27],v[204:207],a[144:147],v[24:27]\nbuffer_load_dwordx4 v224,s[36:39],s67 offen offset:0 lds\nM v[28:31],v[204:207],a[148:151],v[28:31]\n" \
"v_lshlrev_b32 v231,1,v231\nv_xor_b32 v230,v230,v231\nM v[88:91],v[204:207],a[152:155],v[88:91]\ns_waitcnt vmcnt(13)\ns_barrier\nM v[92:95],v[204:207],a[156:159],v[92:95]\nds_read_b128 a[160:163],v228 offset:16384\nds_read_b64_tr_b16 v[192:193],v241 offset:16384\n" \
"M v[32:35],v[208:211],a[144:147],v[32:35]\nbuffer_load_dwordx4 v[152:155],v240,s[52:55],s71 offen sc1\nM v[36:39],v[208:211],a[148:151],v[36:39]\ns_add_u32 s71,s70,798720\nds_read_b64_tr_b16 v[194:195],v241 offset:16640\nM v[96:99],v[208:211],a[152:155],v[96:99]\nds_read_b128 a[164:167],v228 offset:18432\nds_read_b128 a[168:171],v228 offset:20480\n" \
"M v[100:103],v[208:211],a[156:159],v[100:103]\nbuffer_load_dwordx4 v[156:159],v240,s[52:55],s71 offen sc1\nM v[40:43],v[212:215],a[144:147],v[40:43]\nbuffer_load_dwordx4 v225,s[36:39],s67 offen offset:1024 lds\ns_add_u32 s71,s70,811008\nM v[44:47],v[212:215],a[148:151],v[44:47]\nbuffer_load_dwordx4 v[160:163],v240,s[52:55],s71 offen sc1\nM v[104:107],v[212:215],a[152:155],v[104:107]\n" \
"buffer_load_dwordx4 v226,s[36:39],s67 offen offset:2048 lds\ns_add_u32 s71,s70,823296\nbuffer_load_dwordx4 v[164:167],v240,s[52:55],s71 offen sc1\nM v[108:111],v[212:215],a[156:159],v[108:111]\nds_read_b128 a[172:175],v228 offset:22528\nds_read_b64_tr_b16 v[196:197],v242 offset:16384\nM v[48:51],v[216:219],a[144:147],v[48:51]\nds_read_b64_tr_b16 v[198:199],v242 offset:16640\n" \
"ds_read_b64_tr_b16 v[200:201],v241 offset:16896\nM v[52:55],v[216:219],a[148:151],v[52:55]\nds_read_b64_tr_b16 v[202:203],v241 offset:17152\nds_read_b64_tr_b16 v[204:205],v242 offset:16896\nM v[112:115],v[216:219],a[152:155],v[112:115]\nds_read_b64_tr_b16 v[206:207],v242 offset:17152\nds_read_b64_tr_b16 v[208:209],v241 offset:17408\nM v[116:119],v[216:219],a[156:159],v[116:119]\n" \
"buffer_load_dwordx4 v227,s[36:39],s67 offen offset:3072 lds\nM v[56:59],v[220:223],a[144:147],v[56:59]\nds_read_b64_tr_b16 v[210:211],v241 offset:17664\nds_read_b64_tr_b16 v[212:213],v242 offset:17408\nM v[60:63],v[220:223],a[148:151],v[60:63]\ns_add_u32 m0,s65,12288\ns_add_u32 s67,s66,33664\nM v[120:123],v[220:223],a[152:155],v[120:123]\n" \
"ds_read_b64_tr_b16 v[214:215],v242 offset:17664\nds_read_b64_tr_b16 v[216:217],v241 offset:17920\nM v[124:127],v[220:223],a[156:159],v[124:127]\nds_read_b64_tr_b16 v[218:219],v241 offset:18176\ns_waitcnt lgkmcnt(13)\nM v[0:3],v[192:195],a[160:163],v[0:3]\nds_read_b64_tr_b16 v[220:221],v242 offset:17920\nds_read_b64_tr_b16 v[222:223],v242 offset:18176\n" \
"M v[4:7],v[192:195],a[164:167],v[4:7]\nds_read_b128 a[176:179],v229 offset:16384\nds_read_b128 a[180:183],v229 offset:18432\nM v[64:67],v[192:195],a[168:171],v[64:67]\nds_read_b128 a[184:187],v229 offset:20480\ns_waitcnt lgkmcnt(15)\nM v[68:71],v[192:195],a[172:175],v[68:71]\nds_read_b64_tr_b16 v[192:193],v241 offset:24576\n" \
"s_lshl_b32 s71,s6,5\nM v[8:11],v[196:199],a[160:163],v[8:11]\nds_read_b64_tr_b16 v[194:195],v241 offset:24832\nds_read_b128 a[188:191],v229 offset:22528\nM v[12:15],v[196:199],a[164:167],v[12:15]\ns_add_u32 s71,s71,790528\nv_and_b32 v231,16,v249\nM v[72:75],v[196:199],a[168:171],v[72:75]\n" \
"s_cmp_lt_u32 s7,2\ns_cselect_b32 s70,0,1.0\nM v[76:79],v[196:199],a[172:175],v[76:79]\nds_read_b64_tr_b16 v[196:197],v242 offset:24576\ns_waitcnt lgkmcnt(12)\nM v[16:19],v[200:203],a[160:163],v[16:19]\nds_read_b64_tr_b16 v[198:199],v242 offset:24832\nM v[20:23],v[200:203],a[164:167],v[20:23]\n" \
"M v[80:83],v[200:203],a[168:171],v[80:83]\nM v[84:87],v[200:203],a[172:175],v[84:87]\nds_read_b64_tr_b16 v[200:201],v241 offset:25088\nds_read_b64_tr_b16 v[202:203],v241 offset:25344\nM v[24:27],v[204:207],a[160:163],v[24:27]\nbuffer_load_dwordx4 v224,s[36:39],s67 offen offset:0 lds\nM v[28:31],v[204:207],a[164:167],v[28:31]\nM v[88:91],v[204:207],a[168:171],v[88:91]\n" \
"M v[92:95],v[204:207],a[172:175],v[92:95]\nds_read_b64_tr_b16 v[204:205],v242 offset:25088\nds_read_b64_tr_b16 v[206:207],v242 offset:25344\nM v[32:35],v[208:211],a[160:163],v[32:35]\nbuffer_load_dwordx4 v225,s[36:39],s67 offen offset:1024 lds\nM v[36:39],v[208:211],a[164:167],v[36:39]\ns_add_u32 s69,s68,3670016\nM v[96:99],v[208:211],a[168:171],v[96:99]\n" \
"M v[100:103],v[208:211],a[172:175],v[100:103]\nds_read_b64_tr_b16 v[208:209],v241 offset:25600\ns_waitcnt lgkmcnt(7)\nM v[40:43],v[212:215],a[160:163],v[40:43]\nbuffer_load_dwordx4 v226,s[36:39],s67 offen offset:2048 lds\nM v[44:47],v[212:215],a[164:167],v[44:47]\nds_read_b64_tr_b16 v[210:211],v241 offset:25856\nM v[104:107],v[212:215],a[168:171],v[104:107]\n" \
"M v[108:111],v[212:215],a[172:175],v[108:111]\nds_read_b64_tr_b16 v[212:213],v242 offset:25600\nds_read_b64_tr_b16 v[214:215],v242 offset:25856\nM v[48:51],v[216:219],a[160:163],v[48:51]\nbuffer_load_dwordx4 v227,s[36:39],s67 offen offset:3072 lds\nM v[52:55],v[216:219],a[164:167],v[52:55]\ns_cmp_eq_u32 s62,0\ns_cselect_b32 s36,0x8000,0\n" \
"M v[112:115],v[216:219],a[168:171],v[112:115]\ns_add_u32 m0,s64,147456\ns_add_u32 s36,s36,s64\nM v[116:119],v[216:219],a[172:175],v[116:119]\nds_read_b64_tr_b16 v[216:217],v241 offset:26112\nds_read_b64_tr_b16 v[218:219],v241 offset:26368\nM v[56:59],v[220:223],a[160:163],v[56:59]\nbuffer_load_dwordx4 v243,s[40:43],s69 offen offset:0 lds\n" \
"M v[60:63],v[220:223],a[164:167],v[60:63]\nM v[120:123],v[220:223],a[168:171],v[120:123]\nM v[124:127],v[220:223],a[172:175],v[124:127]\nds_read_b64_tr_b16 v[220:221],v242 offset:26112\nds_read_b64_tr_b16 v[222:223],v242 offset:26368\nM v[0:3],v[192:195],a[176:179],v[0:3]\nbuffer_load_dwordx4 v244,s[40:43],s69 offen offset:1024 lds\nM v[4:7],v[192:195],a[180:183],v[4:7]\n" \
"M v[64:67],v[192:195],a[184:187],v[64:67]\nM v[68:71],v[192:195],a[188:191],v[68:71]\ns_waitcnt lgkmcnt(6)\nM v[8:11],v[196:199],a[176:179],v[8:11]\nbuffer_load_dwordx4 v245,s[40:43],s69 offen offset:2048 lds\nM v[12:15],v[196:199],a[180:183],v[12:15]\nM v[72:75],v[196:199],a[184:187],v[72:75]\nM v[76:79],v[196:199],a[188:191],v[76:79]\n" \
"M v[16:19],v[200:203],a[176:179],v[16:19]\nbuffer_load_dwordx4 v246,s[40:43],s69 offen offset:3072 lds\nM v[20:23],v[200:203],a[180:183],v[20:23]\ns_sub_u32 s40,s16,0x1000\ns_subb_u32 s41,s17,0\nM v[80:83],v[200:203],a[184:187],v[80:83]\ns_add_u32 m0,s64,98304\ns_add_u32 s69,s68,0\n" \
"M v[84:87],v[200:203],a[188:191],v[84:87]\ns_mov_b32 s16,0\ns_mov_b32 s17,3\nM v[24:27],v[204:207],a[176:179],v[24:27]\nbuffer_load_dword v168,v169,s[52:55],s71 offen sc1\nM v[28:31],v[204:207],a[180:183],v[28:31]\ns_lshr_b32 s71,s64,2\nv_add_u32 v171,s71,v240\n" \
"M v[88:91],v[204:207],a[184:187],v[88:91]\ns_waitcnt vmcnt(17) lgkmcnt(0)\ns_barrier\nM v[92:95],v[204:207],a[188:191],v[92:95]\nds_read_b128 a[192:195],v228 offset:0\nds_read_b64_tr_b16 v[192:193],v241 offset:32768\nM v[32:35],v[208:211],a[176:179],v[32:35]\nds_read_b64_tr_b16 v[194:195],v241 offset:33024\n" \
"ds_read_b128 a[196:199],v228 offset:2048\nM v[36:39],v[208:211],a[180:183],v[36:39]\nds_read_b128 a[200:203],v228 offset:4096\ns_lshl_b32 s71,s6,5\nM v[96:99],v[208:211],a[184:187],v[96:99]\nds_read_b128 a[204:207],v228 offset:6144\ns_and_b32 s71,s71,0xfffffe00\nM v[100:103],v[208:211],a[188:191],v[100:103]\n" \
"buffer_load_dwordx4 v243,s[40:43],s69 offen offset:0 lds\nM v[40:43],v[212:215],a[176:179],v[40:43]\nds_read_b64_tr_b16 v[196:197],v242 offset:32768\nds_read_b64_tr_b16 v[198:199],v242 offset:33024\nM v[44:47],v[212:215],a[180:183],v[44:47]\nds_read_b64_tr_b16 v[200:201],v241 offset:33280\nds_read_b64_tr_b16 v[202:203],v241 offset:33536\nM v[104:107],v[212:215],a[184:187],v[104:107]\n" \
"ds_read_b64_tr_b16 v[204:205],v242 offset:33280\nds_read_b64_tr_b16 v[206:207],v242 offset:33536\nM v[108:111],v[212:215],a[188:191],v[108:111]\nds_read_b64_tr_b16 v[208:209],v241 offset:33792\nds_read_b64_tr_b16 v[210:211],v241 offset:34048\nM v[48:51],v[216:219],a[176:179],v[48:51]\nds_read_b64_tr_b16 v[212:213],v242 offset:33792\nds_read_b64_tr_b16 v[214:215],v242 offset:34048\n" \
"M v[52:55],v[216:219],a[180:183],v[52:55]\nds_read_b128 a[208:211],v229 offset:0\nds_read_b128 a[212:215],v229 offset:2048\nM v[112:115],v[216:219],a[184:187],v[112:115]\nds_read_b128 a[216:219],v229 offset:4096\ns_waitcnt vmcnt(15)\nv_add_f32 v152,v152,v156\nM v[116:119],v[216:219],a[188:191],v[116:119]\n" \
"ds_read_b64_tr_b16 v[216:217],v241 offset:34304\nv_add_f32 v153,v153,v157\nM v[56:59],v[220:223],a[176:179],v[56:59]\nds_read_b64_tr_b16 v[218:219],v241 offset:34560\nds_read_b128 a[220:223],v229 offset:6144\nM v[60:63],v[220:223],a[180:183],v[60:63]\ns_waitcnt vmcnt(11)\nv_add_f32 v160,v160,v164\n" \
"M v[120:123],v[220:223],a[184:187],v[120:123]\nv_add_f32 v161,v161,v165\nv_add_f32 v154,v154,v158\nM v[124:127],v[220:223],a[188:191],v[124:127]\nds_read_b64_tr_b16 v[220:221],v242 offset:34304\ns_waitcnt lgkmcnt(13)\nM v[0:3],v[192:195],a[192:195],v[0:3]\nds_read_b64_tr_b16 v[222:223],v242 offset:34560\n" \
"v_add_f32 v162,v162,v166\nM v[4:7],v[192:195],a[196:199],v[4:7]\nv_add_f32 v155,v155,v159\nv_add_f32 v163,v163,v167\nM v[64:67],v[192:195],a[200:203],v[64:67]\nv_add_f32 v152,v152,v160\nv_add_f32 v153,v153,v161\nM v[68:71],v[192:195],a[204:207],v[68:71]\n" \
"ds_read_b64_tr_b16 v[192:193],v241 offset:40960\nds_read_b64_tr_b16 v[194:195],v241 offset:41216\nM v[8:11],v[196:199],a[192:195],v[8:11]\nv_add_f32 v154,v154,v162\nv_add_f32 v155,v155,v163\nM v[12:15],v[196:199],a[196:199],v[12:15]\nv_cvt_pk_bf16_f32 v152,0,v152\nv_cvt_pk_bf16_f32 v153,0,v153\n" \
"M v[72:75],v[196:199],a[200:203],v[72:75]\nv_cvt_pk_bf16_f32 v154,0,v154\nv_cvt_pk_bf16_f32 v155,0,v155\nM v[76:79],v[196:199],a[204:207],v[76:79]\nds_read_b64_tr_b16 v[196:197],v242 offset:40960\nds_read_b64_tr_b16 v[198:199],v242 offset:41216\nM v[16:19],v[200:203],a[192:195],v[16:19]\nv_add_f32 v152,s70,v152\n" \
"v_add_f32 v153,s70,v153\nM v[20:23],v[200:203],a[196:199],v[20:23]\nv_add_f32 v154,s70,v154\nv_add_f32 v155,s70,v155\nM v[80:83],v[200:203],a[200:203],v[80:83]\nv_cvt_pk_bf16_f32 v152,0,v152\nv_cvt_pk_bf16_f32 v153,0,v153\nM v[84:87],v[200:203],a[204:207],v[84:87]\n" \
"ds_read_b64_tr_b16 v[200:201],v241 offset:41472\ns_waitcnt lgkmcnt(5)\nM v[24:27],v[204:207],a[192:195],v[24:27]\nds_read_b64_tr_b16 v[202:203],v241 offset:41728\nv_cvt_pk_bf16_f32 v154,0,v154\nM v[28:31],v[204:207],a[196:199],v[28:31]\nv_cvt_pk_bf16_f32 v155,0,v155\ns_lshl_b32 s70,s7,8\n" \
"M v[88:91],v[204:207],a[200:203],v[88:91]\nds_write_b128 v171,v[152:155] offset:16384\nM v[92:95],v[204:207],a[204:207],v[92:95]\nds_read_b64_tr_b16 v[204:205],v242 offset:41472\nds_read_b64_tr_b16 v[206:207],v242 offset:41728\nM v[32:35],v[208:211],a[192:195],v[32:35]\nbuffer_load_dwordx4 v244,s[40:43],s69 offen offset:1024 lds\nM v[36:39],v[208:211],a[196:199],v[36:39]\n" \
"v_lshl_add_u32 v251,v251,5,s70\ns_xor_b32 s70,s63,16\nM v[96:99],v[208:211],a[200:203],v[96:99]\ns_lshr_b32 s70,s70,2\ns_lshl3_add_u32 s70,s62,s70\nM v[100:103],v[208:211],a[204:207],v[100:103]\nds_read_b64_tr_b16 v[208:209],v241 offset:41984\nds_read_b64_tr_b16 v[210:211],v241 offset:42240\n" \
"M v[40:43],v[212:215],a[192:195],v[40:43]\nbuffer_load_dwordx4 v245,s[40:43],s69 offen offset:2048 lds\nM v[44:47],v[212:215],a[196:199],v[44:47]\ns_lshl_b32 s70,s70,15\ns_add_u32 s70,s70,s64\nM v[104:107],v[212:215],a[200:203],v[104:107]\nM v[108:111],v[212:215],a[204:207],v[108:111]\nds_read_b64_tr_b16 v[212:213],v242 offset:41984\n" \
"ds_read_b64_tr_b16 v[214:215],v242 offset:42240\nM v[48:51],v[216:219],a[192:195],v[48:51]\nbuffer_load_dwordx4 v246,s[40:43],s69 offen offset:3072 lds\nM v[52:55],v[216:219],a[196:199],v[52:55]\ns_add_u32 m0,s64,114688\ns_add_u32 s69,s68,524288\nM v[112:115],v[216:219],a[200:203],v[112:115]\nM v[116:119],v[216:219],a[204:207],v[116:119]\n" \
"ds_read_b64_tr_b16 v[216:217],v241 offset:42496\nds_read_b64_tr_b16 v[218:219],v241 offset:42752\nM v[56:59],v[220:223],a[192:195],v[56:59]\nbuffer_store_dwordx2 v[232:233],off,s[52:55],s72 sc1\nM v[60:63],v[220:223],a[196:199],v[60:63]\ns_and_b32 s72,s6,1\ns_lshl_b32 s72,s72,8\nM v[120:123],v[220:223],a[200:203],v[120:123]\n" \
"s_add_u32 s71,s71,s72\ns_and_b32 s72,s6,14\nM v[124:127],v[220:223],a[204:207],v[124:127]\nds_read_b64_tr_b16 v[220:221],v242 offset:42496\ns_waitcnt lgkmcnt(5)\nM v[0:3],v[192:195],a[208:211],v[0:3]\nds_read_b64_tr_b16 v[222:223],v242 offset:42752\ns_lshl1_add_u32 s71,s72,s71\n" \
"M v[4:7],v[192:195],a[212:215],v[4:7]\ns_or_b32 s71,s71,s75\ns_mov_b32 s72,0x3e0293ee\nM v[64:67],v[192:195],a[216:219],v[64:67]\nbuffer_load_dwordx4 v243,s[40:43],s69 offen offset:0 lds\nM v[68:71],v[192:195],a[220:223],v[68:71]\nM v[8:11],v[196:199],a[208:211],v[8:11]\nM v[12:15],v[196:199],a[212:215],v[12:15]\n" \
"M v[72:75],v[196:199],a[216:219],v[72:75]\nglobal_load_dword v247,v253,s[12:13]\nM v[76:79],v[196:199],a[220:223],v[76:79]\nbuffer_load_dwordx4 v244,s[40:43],s69 offen offset:1024 lds\nM v[16:19],v[200:203],a[208:211],v[16:19]\nM v[20:23],v[200:203],a[212:215],v[20:23]\nM v[80:83],v[200:203],a[216:219],v[80:83]\nM v[84:87],v[200:203],a[220:223],v[84:87]\n" \
"buffer_load_dword v247,v252,s[56:59],s78 offen\nM v[24:27],v[204:207],a[208:211],v[24:27]\nbuffer_load_dwordx4 v245,s[40:43],s69 offen offset:2048 lds\nM v[28:31],v[204:207],a[212:215],v[28:31]\nv_lshrrev_b32 v253,4,v249\nM v[88:91],v[204:207],a[216:219],v[88:91]\ns_waitcnt vmcnt(10) lgkmcnt(0)\ns_barrier\n" \
"M v[92:95],v[204:207],a[220:223],v[92:95]\nds_read_b128 a[224:227],v228 offset:8192\nds_read_b64_tr_b16 v[192:193],v241 offset:49152\nM v[32:35],v[208:211],a[208:211],v[32:35]\nds_read_b64_tr_b16 v[194:195],v241 offset:49408\nds_read_b128 a[228:231],v228 offset:10240\nM v[36:39],v[208:211],a[212:215],v[36:39]\nds_read_b128 a[232:235],v228 offset:12288\n" \
"v_mov_b32 v252,0xff800000\nM v[96:99],v[208:211],a[216:219],v[96:99]\nds_read_b128 a[236:239],v228 offset:14336\nv_add_f32_dpp v168,v168,v168 quad_perm:[1,0,3,2] row_mask:0xf bank_mask:0xf\nM v[100:103],v[208:211],a[220:223],v[100:103]\nds_read_b64_tr_b16 v[196:197],v242 offset:49152\nv_add_f32_dpp v168,v168,v168 quad_perm:[2,3,0,1] row_mask:0xf bank_mask:0xf\nM v[40:43],v[212:215],a[208:211],v[40:43]\n" \
"ds_read_b64_tr_b16 v[198:199],v242 offset:49408\nds_read_b64_tr_b16 v[200:201],v241 offset:49664\nM v[44:47],v[212:215],a[212:215],v[44:47]\nds_read_b64_tr_b16 v[202:203],v241 offset:49920\nds_read_b64_tr_b16 v[204:205],v242 offset:49664\nM v[104:107],v[212:215],a[216:219],v[104:107]\nds_read_b64_tr_b16 v[206:207],v242 offset:49920\nds_read_b64_tr_b16 v[208:209],v241 offset:50176\n" \
"M v[108:111],v[212:215],a[220:223],v[108:111]\nds_read_b64_tr_b16 v[210:211],v241 offset:50432\nds_read_b64_tr_b16 v[212:213],v242 offset:50176\nM v[48:51],v[216:219],a[208:211],v[48:51]\nds_read_b64_tr_b16 v[214:215],v242 offset:50432\nds_read_b128 a[240:243],v229 offset:8192\nM v[52:55],v[216:219],a[212:215],v[52:55]\nds_read_b128 a[244:247],v229 offset:10240\n" \
"v_cvt_pk_bf16_f32 v168,0,v168\nM v[112:115],v[216:219],a[216:219],v[112:115]\nds_read_b128 a[248:251],v229 offset:12288\nv_and_b32 v228,15,v249\nM v[116:119],v[216:219],a[220:223],v[116:119]\nds_read_b64_tr_b16 v[216:217],v241 offset:50688\nv_lshlrev_b32 v228,2,v228\nM v[56:59],v[220:223],a[208:211],v[56:59]\n" \
"ds_read_b64_tr_b16 v[218:219],v241 offset:50944\nds_read_b128 a[252:255],v229 offset:14336\nM v[60:63],v[220:223],a[212:215],v[60:63]\nds_read_b128 v[176:179],v251 offset:19456\nv_and_b32 v229,16,v249\nM v[120:123],v[220:223],a[216:219],v[120:123]\nds_read_b128 v[160:163],v251 offset:17408\nv_lshl_or_b32 v228,v229,4,v228\n" \
"M v[124:127],v[220:223],a[220:223],v[124:127]\nds_read_b64_tr_b16 v[220:221],v242 offset:50688\ns_waitcnt lgkmcnt(15)\nM v[0:3],v[192:195],a[224:227],v[0:3]\nds_read_b64_tr_b16 v[222:223],v242 offset:50944\nds_read_b128 v[172:175],v251 offset:18448\nM v[4:7],v[192:195],a[228:231],v[4:7]\nbuffer_store_dword v168,v170,s[52:55],s71 offen sc1\n" \
"M v[64:67],v[192:195],a[232:235],v[64:67]\nds_read_b128 v[168:171],v251 offset:18432\nds_read_b128 v[180:183],v251 offset:19472\nM v[68:71],v[192:195],a[236:239],v[68:71]\nds_read_b64_tr_b16 v[192:193],v241 offset:57344\ns_and_b32 s71,s63,2\nM v[8:11],v[196:199],a[224:227],v[8:11]\nds_read_b64_tr_b16 v[194:195],v241 offset:57600\n" \
"ds_read_b128 v[152:155],v251 offset:16384\nM v[12:15],v[196:199],a[228:231],v[12:15]\nds_read_b128 v[156:159],v251 offset:16400\ns_lshl_b32 s71,s71,9\nM v[72:75],v[196:199],a[232:235],v[72:75]\nds_read_b128 v[164:167],v251 offset:17424\nv_lshrrev_b32 v251,5,v249\nM v[76:79],v[196:199],a[236:239],v[76:79]\n" \
"ds_read_b64_tr_b16 v[196:197],v242 offset:57344\ns_waitcnt lgkmcnt(12)\nv_mul_f32 v144,v144,v176\nM v[16:19],v[200:203],a[224:227],v[16:19]\nds_read_b64_tr_b16 v[198:199],v242 offset:57600\nv_mul_f32 v145,v145,v177\nM v[20:23],v[200:203],a[228:231],v[20:23]\nv_cvt_pk_bf16_f32 v144,0,v144\n" \
"v_cvt_pk_bf16_f32 v145,0,v145\nM v[80:83],v[200:203],a[232:235],v[80:83]\ns_waitcnt lgkmcnt(10)\nv_add_f32 v144,v144,v160\nM v[84:87],v[200:203],a[236:239],v[84:87]\nds_read_b64_tr_b16 v[200:201],v241 offset:57856\nds_read_b64_tr_b16 v[202:203],v241 offset:58112\nM v[24:27],v[204:207],a[224:227],v[24:27]\n" \
"v_add_f32 v145,v145,v161\nv_cvt_pk_bf16_f32 v144,0,v144\nM v[28:31],v[204:207],a[228:231],v[28:31]\nv_cvt_pk_bf16_f32 v145,0,v145\nv_mul_f32 v144,s72,v144\nM v[88:91],v[204:207],a[232:235],v[88:91]\nv_mul_f32 v145,s72,v145\nv_cvt_pk_bf16_f32 v236,v144,v145\n" \
"M v[92:95],v[204:207],a[236:239],v[92:95]\nds_read_b64_tr_b16 v[204:205],v242 offset:57856\nds_read_b64_tr_b16 v[206:207],v242 offset:58112\nM v[32:35],v[208:211],a[224:227],v[32:35]\nv_mul_f32 v146,v146,v178\nv_mul_f32 v147,v147,v179\nM v[36:39],v[208:211],a[228:231],v[36:39]\nv_cvt_pk_bf16_f32 v146,0,v146\n" \
"v_cvt_pk_bf16_f32 v147,0,v147\nM v[96:99],v[208:211],a[232:235],v[96:99]\nv_add_f32 v146,v146,v162\nv_add_f32 v147,v147,v163\nM v[100:103],v[208:211],a[236:239],v[100:103]\nds_read_b64_tr_b16 v[208:209],v241 offset:58368\nds_read_b64_tr_b16 v[210:211],v241 offset:58624\nM v[40:43],v[212:215],a[224:227],v[40:43]\n" \
"v_cvt_pk_bf16_f32 v146,0,v146\nv_cvt_pk_bf16_f32 v147,0,v147\nM v[44:47],v[212:215],a[228:231],v[44:47]\nv_mul_f32 v146,s72,v146\nv_mul_f32 v147,s72,v147\nM v[104:107],v[212:215],a[232:235],v[104:107]\nv_cvt_pk_bf16_f32 v237,v146,v147\ns_waitcnt lgkmcnt(4)\n" \
"v_mul_f32 v136,v136,v168\nM v[108:111],v[212:215],a[236:239],v[108:111]\nds_read_b64_tr_b16 v[212:213],v242 offset:58368\nds_read_b64_tr_b16 v[214:215],v242 offset:58624\nM v[48:51],v[216:219],a[224:227],v[48:51]\nv_mul_f32 v137,v137,v169\nv_mul_f32 v138,v138,v170\nM v[52:55],v[216:219],a[228:231],v[52:55]\n" \
"v_mul_f32 v139,v139,v171\nv_mul_f32 v140,v140,v172\nM v[112:115],v[216:219],a[232:235],v[112:115]\nv_mul_f32 v141,v141,v173\nv_mul_f32 v142,v142,v174\nM v[116:119],v[216:219],a[236:239],v[116:119]\nds_read_b64_tr_b16 v[216:217],v241 offset:58880\nds_read_b64_tr_b16 v[218:219],v241 offset:59136\n" \
"M v[56:59],v[220:223],a[224:227],v[56:59]\nv_mul_f32 v143,v143,v175\nv_mul_f32 v148,v148,v180\nM v[60:63],v[220:223],a[228:231],v[60:63]\nv_mul_f32 v149,v149,v181\nv_cvt_pk_bf16_f32 v136,0,v136\nM v[120:123],v[220:223],a[232:235],v[120:123]\nv_cvt_pk_bf16_f32 v137,0,v137\n" \
"v_cvt_pk_bf16_f32 v138,0,v138\nM v[124:127],v[220:223],a[236:239],v[124:127]\nds_read_b64_tr_b16 v[220:221],v242 offset:58880\nds_read_b64_tr_b16 v[222:223],v242 offset:59136\nM v[0:3],v[192:195],a[240:243],v[0:3]\nv_cvt_pk_bf16_f32 v139,0,v139\nv_cvt_pk_bf16_f32 v140,0,v140\nM v[4:7],v[192:195],a[244:247],v[4:7]\n" \
"v_cvt_pk_bf16_f32 v141,0,v141\nv_cvt_pk_bf16_f32 v142,0,v142\nM v[64:67],v[192:195],a[248:251],v[64:67]\nv_cvt_pk_bf16_f32 v143,0,v143\nv_cvt_pk_bf16_f32 v148,0,v148\nM v[68:71],v[192:195],a[252:255],v[68:71]\nv_cvt_pk_bf16_f32 v128,v0,v1\nv_cvt_pk_bf16_f32 v129,v2,v3\n" \
"M v[8:11],v[196:199],a[240:243],v[8:11]\nv_cvt_pk_bf16_f32 v144,v4,v5\nv_cvt_pk_bf16_f32 v145,v6,v7\nM v[12:15],v[196:199],a[244:247],v[12:15]\nv_cvt_pk_bf16_f32 v160,v64,v65\nv_cvt_pk_bf16_f32 v161,v66,v67\nM v[72:75],v[196:199],a[248:251],v[72:75]\nv_cvt_pk_bf16_f32 v176,v68,v69\n" \
"v_cvt_pk_bf16_f32 v177,v70,v71\nM v[76:79],v[196:199],a[252:255],v[76:79]\nv_cvt_pk_bf16_f32 v130,v8,v9\nv_cvt_pk_bf16_f32 v131,v10,v11\nM v[16:19],v[200:203],a[240:243],v[16:19]\nv_cvt_pk_bf16_f32 v146,v12,v13\nv_cvt_pk_bf16_f32 v147,v14,v15\nM v[20:23],v[200:203],a[244:247],v[20:23]\n" \
"v_cvt_pk_bf16_f32 v162,v72,v73\nv_cvt_pk_bf16_f32 v163,v74,v75\nM v[80:83],v[200:203],a[248:251],v[80:83]\nv_cvt_pk_bf16_f32 v149,0,v149\ns_add_u32 s70,s70,s71\nM v[84:87],v[200:203],a[252:255],v[84:87]\nv_add_f32 v136,v136,v152\ns_waitcnt lgkmcnt(0)\n" \
"M v[24:27],v[204:207],a[240:243],v[24:27]\nbuffer_load_dwordx4 v246,s[40:43],s69 offen offset:3072 lds\nM v[28:31],v[204:207],a[244:247],v[28:31]\nv_add_f32 v137,v137,v153\nv_add_f32 v138,v138,v154\nM v[88:91],v[204:207],a[248:251],v[88:91]\ns_waitcnt vmcnt(8)\ns_barrier\n" \
"M v[92:95],v[204:207],a[252:255],v[92:95]\nds_read_b64_tr_b16 v[192:193],v241 offset:0\nds_read_b64_tr_b16 v[194:195],v241 offset:256\nM v[32:35],v[208:211],a[240:243],v[32:35]\nds_read_b64_tr_b16 v[196:197],v242 offset:0\nds_read_b64_tr_b16 v[198:199],v242 offset:256\nM v[36:39],v[208:211],a[244:247],v[36:39]\nds_read_b64_tr_b16 v[200:201],v241 offset:512\n" \
"ds_read_b64_tr_b16 v[202:203],v241 offset:768\nM v[96:99],v[208:211],a[248:251],v[96:99]\nds_read_b64_tr_b16 v[204:205],v242 offset:512\nds_read_b64_tr_b16 v[206:207],v242 offset:768\nM v[100:103],v[208:211],a[252:255],v[100:103]\nds_read_b64_tr_b16 v[208:209],v241 offset:1024\nds_read_b64_tr_b16 v[210:211],v241 offset:1280\nM v[40:43],v[212:215],a[240:243],v[40:43]\n" \
"v_add_f32 v139,v139,v155\nv_add_f32 v140,v140,v156\nM v[44:47],v[212:215],a[244:247],v[44:47]\nv_add_f32 v141,v141,v157\nv_add_f32 v142,v142,v158\nM v[104:107],v[212:215],a[248:251],v[104:107]\nv_add_f32 v143,v143,v159\nv_add_f32 v148,v148,v164\n" \
"M v[108:111],v[212:215],a[252:255],v[108:111]\nv_add_f32 v149,v149,v165\nv_lshlrev_b32 v251,11,v251\nM v[48:51],v[216:219],a[240:243],v[48:51]\ns_and_b32 s71,s63,1\nv_cvt_pk_bf16_f32 v178,v76,v77\nM v[52:55],v[216:219],a[244:247],v[52:55]\nv_cvt_pk_bf16_f32 v179,v78,v79\n" \
"v_cvt_pk_bf16_f32 v136,0,v136\nM v[112:115],v[216:219],a[248:251],v[112:115]\nv_cvt_pk_bf16_f32 v137,0,v137\nv_cvt_pk_bf16_f32 v138,0,v138\nM v[116:119],v[216:219],a[252:255],v[116:119]\nv_cvt_pk_bf16_f32 v139,0,v139\nv_cvt_pk_bf16_f32 v140,0,v140\nM v[56:59],v[220:223],a[240:243],v[56:59]\n" \
"v_cvt_pk_bf16_f32 v141,0,v141\nv_cvt_pk_bf16_f32 v142,0,v142\nM v[60:63],v[220:223],a[244:247],v[60:63]\nv_cvt_pk_bf16_f32 v143,0,v143\nv_cvt_pk_bf16_f32 v148,0,v148\nM v[120:123],v[220:223],a[248:251],v[120:123]\nv_cvt_pk_bf16_f32 v149,0,v149\nv_lshl_or_b32 v251,v250,8,v251\n" \
"M v[124:127],v[220:223],a[252:255],v[124:127]\ns_lshl_b32 s71,s71,7\ns_waitcnt lgkmcnt(0)\nM v[0:3],a[0:3],v[192:195],0\nv_mul_f32 v136,s72,v136\nv_mul_f32 v137,s72,v137\nM v[4:7],a[4:7],v[192:195],0\nv_mul_f32 v138,s72,v138\n" \
"v_mul_f32 v139,s72,v139\nM v[64:67],a[8:11],v[192:195],0\nv_mul_f32 v140,s72,v140\nv_mul_f32 v141,s72,v141\nM v[68:71],a[12:15],v[192:195],0\nv_mul_f32 v142,s72,v142\nv_mul_f32 v143,s72,v143\nM v[8:11],a[0:3],v[196:199],0\n" \
"v_mul_f32 v148,s72,v148\nv_mul_f32 v149,s72,v149\nM v[12:15],a[4:7],v[196:199],0\nv_and_b32 v250,0x70,v240\ns_add_u32 s70,s70,s71\nM v[72:75],a[8:11],v[196:199],0\nv_cvt_pk_bf16_f32 v132,v16,v17\nv_cvt_pk_bf16_f32 v133,v18,v19\n" \
"M v[76:79],a[12:15],v[196:199],0\nv_cvt_pk_bf16_f32 v232,v136,v137\nv_cvt_pk_bf16_f32 v233,v138,v139\nM v[16:19],a[0:3],v[200:203],0\nv_cvt_pk_bf16_f32 v234,v140,v141\nv_cvt_pk_bf16_f32 v235,v142,v143\nv_cvt_pk_bf16_f32 v238,v148,v149\nv_or_b32 v251,v251,v250\n" \
"s_add_u32 s70,s70,1048576\nbuffer_store_dwordx4 v[232:235],v251,s[52:55],s70 offen sc1\nv_cvt_pk_bf16_f32 v148,v20,v21\nv_cvt_pk_bf16_f32 v149,v22,v23\nM v[20:23],a[4:7],v[200:203],0\nv_mul_f32 v150,v150,v182\nv_mul_f32 v151,v151,v183\nv_cvt_pk_bf16_f32 v164,v80,v81\n" \
"v_cvt_pk_bf16_f32 v165,v82,v83\nds_read_b64_tr_b16 v[212:213],v242 offset:1024\nds_read_b64_tr_b16 v[214:215],v242 offset:1280\nM v[80:83],a[8:11],v[200:203],0\nv_cvt_pk_bf16_f32 v150,0,v150\nv_cvt_pk_bf16_f32 v151,0,v151\nv_add_f32 v150,v150,v166\nv_add_f32 v151,v151,v167\n" \
"v_cvt_pk_bf16_f32 v180,v84,v85\nv_cvt_pk_bf16_f32 v181,v86,v87\nM v[84:87],a[12:15],v[200:203],0\nv_cvt_pk_bf16_f32 v150,0,v150\nv_cvt_pk_bf16_f32 v151,0,v151\nv_mul_f32 v150,s72,v150\nv_mul_f32 v151,s72,v151\nv_cvt_pk_bf16_f32 v134,v24,v25\n" \
"v_cvt_pk_bf16_f32 v135,v26,v27\nM v[24:27],a[0:3],v[204:207],0\nv_cvt_pk_bf16_f32 v239,v150,v151\ns_add_u32 s71,s70,16384\nbuffer_store_dwordx4 v[236:239],v251,s[52:55],s71 offen sc1\nv_cvt_pk_bf16_f32 v150,v28,v29\nv_cvt_pk_bf16_f32 v151,v30,v31\nM v[28:31],a[4:7],v[204:207],0\n" \
"v_cvt_pk_bf16_f32 v166,v88,v89\nv_cvt_pk_bf16_f32 v167,v90,v91\nds_read_b64_tr_b16 v[216:217],v241 offset:1536\nds_read_b64_tr_b16 v[218:219],v241 offset:1792\nM v[88:91],a[8:11],v[204:207],0\nv_cvt_pk_bf16_f32 v182,v92,v93\nv_cvt_pk_bf16_f32 v183,v94,v95\nM v[92:95],a[12:15],v[204:207],0\n" \
"v_cvt_pk_bf16_f32 v136,v32,v33\nv_cvt_pk_bf16_f32 v137,v34,v35\nM v[32:35],a[0:3],v[208:211],0\ns_lshl_b32 s70,s62,5\ns_add_u32 s70,s70,s63\nv_cvt_pk_bf16_f32 v152,v36,v37\nv_cvt_pk_bf16_f32 v153,v38,v39\nM v[36:39],a[4:7],v[208:211],0\n" \
"s_lshl2_add_u32 s70,s70,s7\ns_lshl2_add_u32 s79,s70,90112\nv_cvt_pk_bf16_f32 v168,v96,v97\nv_cvt_pk_bf16_f32 v169,v98,v99\nds_read_b64_tr_b16 v[220:221],v242 offset:1536\nds_read_b64_tr_b16 v[222:223],v242 offset:1792\nM v[96:99],a[8:11],v[208:211],0\ns_lshl_b32 s70,s60,4\n" \
"v_cvt_pk_bf16_f32 v184,v100,v101\nv_cvt_pk_bf16_f32 v185,v102,v103\ns_sub_u32 s70,s70,s62\nM v[100:103],a[12:15],v[208:211],0\ns_add_u32 s70,s70,15\nv_cvt_pk_bf16_f32 v138,v40,v41\nv_cvt_pk_bf16_f32 v139,v42,v43\ns_lshl2_add_u32 s70,s70,s7\n" \
"s_waitcnt lgkmcnt(4)\nM v[40:43],a[0:3],v[212:215],0\ns_lshl_b32 s70,s70,15\ns_lshl_b32 s71,s61,8\nv_cvt_pk_bf16_f32 v154,v44,v45\nv_cvt_pk_bf16_f32 v155,v46,v47\ns_add_u32 s70,s70,s71\nM v[44:47],a[4:7],v[212:215],0\n" \
"s_add_u32 s78,s70,54525952\nv_cvt_pk_bf16_f32 v170,v104,v105\nv_cvt_pk_bf16_f32 v171,v106,v107\ns_lshl3_add_u32 s70,s62,s7\nds_read_b64_tr_b16 v[192:193],v241 offset:8192\nds_read_b64_tr_b16 v[194:195],v241 offset:8448\nM v[104:107],a[8:11],v[212:215],0\ns_lshl_b32 s80,s70,6\n" \
"s_lshl_b32 s76,s70,15\nv_cvt_pk_bf16_f32 v186,v108,v109\nv_cvt_pk_bf16_f32 v187,v110,v111\ns_lshl_b32 s70,s63,5\nM v[108:111],a[12:15],v[212:215],0\ns_add_u32 s70,s70,s74\nv_cvt_pk_bf16_f32 v140,v48,v49\nv_cvt_pk_bf16_f32 v141,v50,v51\n" \
"s_lshl_b32 s74,s70,14\ns_waitcnt lgkmcnt(2)\nM v[48:51],a[0:3],v[216:219],0\ns_add_u32 s74,s74,3145728\nv_cvt_pk_bf16_f32 v156,v52,v53\nv_cvt_pk_bf16_f32 v157,v54,v55\nbuffer_store_dwordx4 v[128:131],v240,s[52:55],s74 offen offset:0 sc1\nM v[52:55],a[4:7],v[216:219],0\n" \
"v_cvt_pk_bf16_f32 v172,v112,v113\nv_cvt_pk_bf16_f32 v173,v114,v115\nds_read_b64_tr_b16 v[196:197],v242 offset:8192\nds_read_b64_tr_b16 v[198:199],v242 offset:8448\nM v[112:115],a[8:11],v[216:219],0\nv_cvt_pk_bf16_f32 v188,v116,v117\nv_cvt_pk_bf16_f32 v189,v118,v119\nM v[116:119],a[12:15],v[216:219],0\n" \
"v_cvt_pk_bf16_f32 v142,v56,v57\nv_cvt_pk_bf16_f32 v143,v58,v59\nM v[56:59],a[0:3],v[220:223],0\nv_cvt_pk_bf16_f32 v158,v60,v61\nv_cvt_pk_bf16_f32 v159,v62,v63\nM v[60:63],a[4:7],v[220:223],0\nv_cvt_pk_bf16_f32 v174,v120,v121\nv_cvt_pk_bf16_f32 v175,v122,v123\n" \
"ds_read_b64_tr_b16 v[200:201],v241 offset:8704\nds_read_b64_tr_b16 v[202:203],v241 offset:8960\nM v[120:123],a[8:11],v[220:223],0\ns_add_u32 m0,s64,131072\ns_add_u32 s69,s68,1048576\nv_cvt_pk_bf16_f32 v190,v124,v125\nv_cvt_pk_bf16_f32 v191,v126,v127\nM v[124:127],a[12:15],v[220:223],0\n" \
"ds_read_b64_tr_b16 v[204:205],v242 offset:8704\ns_waitcnt lgkmcnt(5)\nM v[0:3],a[16:19],v[192:195],v[0:3]\nds_read_b64_tr_b16 v[206:207],v242 offset:8960\nds_read_b64_tr_b16 v[208:209],v241 offset:9216\nM v[4:7],a[20:23],v[192:195],v[4:7]\nds_read_b64_tr_b16 v[210:211],v241 offset:9472\nds_read_b64_tr_b16 v[212:213],v242 offset:9216\n" \
"M v[64:67],a[24:27],v[192:195],v[64:67]\nds_read_b64_tr_b16 v[214:215],v242 offset:9472\nds_read_b64_tr_b16 v[216:217],v241 offset:9728\nM v[68:71],a[28:31],v[192:195],v[68:71]\nds_read_b64_tr_b16 v[218:219],v241 offset:9984\ns_waitcnt lgkmcnt(10)\nM v[8:11],a[16:19],v[196:199],v[8:11]\nds_read_b64_tr_b16 v[220:221],v242 offset:9728\n" \
"ds_read_b64_tr_b16 v[222:223],v242 offset:9984\nM v[12:15],a[20:23],v[196:199],v[12:15]\nbuffer_load_dwordx4 v243,s[40:43],s69 offen offset:0 lds\nM v[72:75],a[24:27],v[196:199],v[72:75]\ns_add_u32 s80,s80,90112\ns_add_u32 s76,s76,1048576\nM v[76:79],a[28:31],v[196:199],v[76:79]\ns_add_u32 s77,s76,131072\n" \
"s_waitcnt lgkmcnt(10)\nM v[16:19],a[16:19],v[200:203],v[16:19]\ns_lshl2_add_u32 s29,s70,81920\ns_lshl_b32 s70,s7,14\nM v[20:23],a[20:23],v[200:203],v[20:23]\nbuffer_store_dwordx4 v[132:135],v240,s[52:55],s74 offen offset:1024 sc1\nM v[80:83],a[24:27],v[200:203],v[80:83]\nv_add_u32 v250,s70,v240\n" \
"s_lshl_b32 s70,s63,19\nM v[84:87],a[28:31],v[200:203],v[84:87]\ns_lshl_b32 s71,s60,6\ns_waitcnt lgkmcnt(6)\nM v[24:27],a[16:19],v[204:207],v[24:27]\ns_add_u32 s44,s34,s70\ns_addc_u32 s45,s35,0\nM v[28:31],a[20:23],v[204:207],v[28:31]\n" \
"buffer_load_dwordx4 v244,s[40:43],s69 offen offset:1024 lds\nM v[88:91],a[24:27],v[204:207],v[88:91]\ns_waitcnt vmcnt(6) lgkmcnt(0)\ns_barrier\nM v[92:95],a[28:31],v[204:207],v[92:95]\nds_read_b64_tr_b16 v[192:193],v241 offset:16384\nds_read_b64_tr_b16 v[194:195],v241 offset:16640\nM v[32:35],a[16:19],v[208:211],v[32:35]\n" \
"ds_read_b64_tr_b16 v[196:197],v242 offset:16384\nds_read_b64_tr_b16 v[198:199],v242 offset:16640\nM v[36:39],a[20:23],v[208:211],v[36:39]\nds_read_b64_tr_b16 v[200:201],v241 offset:16896\nds_read_b64_tr_b16 v[202:203],v241 offset:17152\nM v[96:99],a[24:27],v[208:211],v[96:99]\nds_read_b64_tr_b16 v[204:205],v242 offset:16896\nds_read_b64_tr_b16 v[206:207],v242 offset:17152\n" \
"M v[100:103],a[28:31],v[208:211],v[100:103]\nds_read_b64_tr_b16 v[208:209],v241 offset:17408\nds_read_b64_tr_b16 v[210:211],v241 offset:17664\nM v[40:43],a[16:19],v[212:215],v[40:43]\nbuffer_load_dwordx4 v245,s[40:43],s69 offen offset:2048 lds\nM v[44:47],a[20:23],v[212:215],v[44:47]\ns_lshl_b32 s70,s62,15\ns_sub_u32 s46,491520,s70\n" \
"M v[104:107],a[24:27],v[212:215],v[104:107]\ns_add_u32 s96,s70,s64\ns_lshl2_add_u32 s70,s62,0\nM v[108:111],a[28:31],v[212:215],v[108:111]\nds_read_b64_tr_b16 v[212:213],v242 offset:17408\nds_read_b64_tr_b16 v[214:215],v242 offset:17664\nM v[48:51],a[16:19],v[216:219],v[48:51]\nbuffer_store_dwordx4 v[136:139],v240,s[52:55],s74 offen offset:2048 sc1\n" \
"M v[52:55],a[20:23],v[216:219],v[52:55]\nds_write_b64 v250,v[128:129] offset:0\ns_sub_u32 s70,s70,s7\nM v[112:115],a[24:27],v[216:219],v[112:115]\ns_lshl_b32 s70,s70,16\ns_add_u32 s57,s78,s70\nM v[116:119],a[28:31],v[216:219],v[116:119]\nds_read_b64_tr_b16 v[216:217],v241 offset:17920\n" \
"ds_read_b64_tr_b16 v[218:219],v241 offset:18176\nM v[56:59],a[16:19],v[220:223],v[56:59]\nbuffer_load_dwordx4 v246,s[40:43],s69 offen offset:3072 lds\nM v[60:63],a[20:23],v[220:223],v[60:63]\ns_add_u32 m0,s64,147456\ns_add_u32 s69,s68,1572864\nM v[120:123],a[24:27],v[220:223],v[120:123]\ns_lshl3_add_u32 s70,s62,s7\n" \
"s_lshl3_add_u32 s70,s70,s60\nM v[124:127],a[28:31],v[220:223],v[124:127]\nds_read_b64_tr_b16 v[220:221],v242 offset:17920\ns_waitcnt lgkmcnt(6)\nM v[0:3],a[32:35],v[192:195],v[0:3]\nds_read_b64_tr_b16 v[222:223],v242 offset:18176\ns_lshl_b32 s67,s70,15\nM v[4:7],a[36:39],v[192:195],v[4:7]\n" \
"buffer_load_dwordx4 v243,s[40:43],s69 offen offset:0 lds\nM v[64:67],a[40:43],v[192:195],v[64:67]\ns_lshl3_add_u32 s70,s62,s60\ns_lshl2_add_u32 s70,s70,s7\nM v[68:71],a[44:47],v[192:195],v[68:71]\nds_read_b64_tr_b16 v[192:193],v241 offset:24576\nds_read_b64_tr_b16 v[194:195],v241 offset:24832\nM v[8:11],a[32:35],v[196:199],v[8:11]\n" \
"ds_write_b64 v250,v[130:131] offset:8\nv_lshlrev_b32 v229,7,v249\nM v[12:15],a[36:39],v[196:199],v[12:15]\nbuffer_load_dwordx4 v244,s[40:43],s69 offen offset:1024 lds\nM v[72:75],a[40:43],v[196:199],v[72:75]\ns_add_u32 s72,s78,16384\ns_sub_u32 s96,s96,16384\nM v[76:79],a[44:47],v[196:199],v[76:79]\n" \
"ds_read_b64_tr_b16 v[196:197],v242 offset:24576\nds_read_b64_tr_b16 v[198:199],v242 offset:24832\nM v[16:19],a[32:35],v[200:203],v[16:19]\ns_cmp_eq_u32 s36,s96\ns_cselect_b32 s72,0xc000,s98\ns_add_u32 s65,s36,s72\nM v[20:23],a[36:39],v[200:203],v[20:23]\nbuffer_load_dwordx4 v245,s[40:43],s69 offen offset:2048 lds\n" \
"M v[80:83],a[40:43],v[200:203],v[80:83]\ns_cmp_eq_u32 s65,s96\ns_cselect_b32 s72,0xc000,s98\ns_add_u32 s66,s65,s72\nM v[84:87],a[44:47],v[200:203],v[84:87]\nds_read_b64_tr_b16 v[200:201],v241 offset:25088\nds_read_b64_tr_b16 v[202:203],v241 offset:25344\nM v[24:27],a[32:35],v[204:207],v[24:27]\n" \
"s_cmp_eq_u32 s66,s96\ns_cselect_b32 s72,0xc000,s98\ns_add_u32 s95,s66,s72\nM v[28:31],a[36:39],v[204:207],v[28:31]\nbuffer_store_dwordx4 v[140:143],v240,s[52:55],s74 offen offset:3072 sc1\nM v[88:91],a[40:43],v[204:207],v[88:91]\ns_add_u32 s74,s74,4096\ns_add_u32 s15,s64,s46\n" \
"M v[92:95],a[44:47],v[204:207],v[92:95]\nds_read_b64_tr_b16 v[204:205],v242 offset:25088\nds_read_b64_tr_b16 v[206:207],v242 offset:25344\nM v[32:35],a[32:35],v[208:211],v[32:35]\nds_write_b64 v250,v[132:133] offset:1024\nv_and_b32 v251,0xf0,v240\nM v[36:39],a[36:39],v[208:211],v[36:39]\nbuffer_load_dwordx4 v246,s[40:43],s69 offen offset:3072 lds\n" \
"M v[96:99],a[40:43],v[208:211],v[96:99]\ns_add_u32 m0,s64,98304\ns_add_u32 s69,s68,2097152\nM v[100:103],a[44:47],v[208:211],v[100:103]\nds_read_b64_tr_b16 v[208:209],v241 offset:25600\ns_waitcnt lgkmcnt(6)\nM v[40:43],a[32:35],v[212:215],v[40:43]\nds_read_b64_tr_b16 v[210:211],v241 offset:25856\n" \
"s_add_u32 s15,s15,32768\nM v[44:47],a[36:39],v[212:215],v[44:47]\nbuffer_load_dwordx4 v243,s[40:43],s69 offen offset:0 lds\nM v[104:107],a[40:43],v[212:215],v[104:107]\ns_add_u32 s48,s44,19922944\ns_addc_u32 s49,s45,0\nM v[108:111],a[44:47],v[212:215],v[108:111]\nds_read_b64_tr_b16 v[212:213],v242 offset:25600\n" \
"ds_read_b64_tr_b16 v[214:215],v242 offset:25856\nM v[48:51],a[32:35],v[216:219],v[48:51]\ns_cmp_lt_u32 s62,6\ns_cselect_b32 s72,65536,0\nM v[52:55],a[36:39],v[216:219],v[52:55]\nbuffer_load_dwordx4 v244,s[40:43],s69 offen offset:1024 lds\nM v[112:115],a[40:43],v[216:219],v[112:115]\nv_lshl_or_b32 v253,v253,12,v251\n" \
"s_mov_b32 s56,s15\nM v[116:119],a[44:47],v[216:219],v[116:119]\nds_read_b64_tr_b16 v[216:217],v241 offset:26112\nds_read_b64_tr_b16 v[218:219],v241 offset:26368\nM v[56:59],a[32:35],v[220:223],v[56:59]\nds_write_b64 v250,v[134:135] offset:1032\ns_mov_b32 s50,s46\nM v[60:63],a[36:39],v[220:223],v[60:63]\n" \
"buffer_store_dwordx4 v[144:147],v240,s[52:55],s74 offen offset:0 sc1\nM v[120:123],a[40:43],v[220:223],v[120:123]\ns_add_u32 s44,s44,3145728\ns_addc_u32 s45,s45,0\nM v[124:127],a[44:47],v[220:223],v[124:127]\nds_read_b64_tr_b16 v[220:221],v242 offset:26112\nds_read_b64_tr_b16 v[222:223],v242 offset:26368\nM v[0:3],a[48:51],v[192:195],v[0:3]\n" \
"s_add_u32 s97,s96,98304\ns_sub_u32 s15,s15,s72\nM v[4:7],a[52:55],v[192:195],v[4:7]\nbuffer_load_dwordx4 v245,s[40:43],s69 offen offset:2048 lds\nM v[64:67],a[56:59],v[192:195],v[64:67]\ns_sub_u32 s57,s57,1867776\ns_add_u32 s67,s67,36700160\nM v[68:71],a[60:63],v[192:195],v[68:71]\n" \
"M v[8:11],a[48:51],v[196:199],v[8:11]\nM v[12:15],a[52:55],v[196:199],v[12:15]\nbuffer_load_dwordx4 v246,s[40:43],s69 offen offset:3072 lds\nM v[72:75],a[56:59],v[196:199],v[72:75]\nM v[76:79],a[60:63],v[196:199],v[76:79]\ns_waitcnt lgkmcnt(3)\nM v[16:19],a[48:51],v[200:203],v[16:19]\nds_write_b64 v250,v[136:137] offset:2048\n" \
"M v[20:23],a[52:55],v[200:203],v[20:23]\nM v[80:83],a[56:59],v[200:203],v[80:83]\nM v[84:87],a[60:63],v[200:203],v[84:87]\nM v[24:27],a[48:51],v[204:207],v[24:27]\ns_add_u32 m0,s64,114688\ns_add_u32 s69,s68,2621440\nM v[28:31],a[52:55],v[204:207],v[28:31]\nM v[88:91],a[56:59],v[204:207],v[88:91]\n" \
"s_waitcnt vmcnt(10) lgkmcnt(1)\ns_barrier\nM v[92:95],a[60:63],v[204:207],v[92:95]\nbuffer_store_dwordx4 v[148:151],v240,s[52:55],s74 offen offset:1024 sc1\nM v[32:35],a[48:51],v[208:211],v[32:35]\nds_read_b64_tr_b16 v[192:193],v241 offset:32768\nds_read_b64_tr_b16 v[194:195],v241 offset:33024\nM v[36:39],a[52:55],v[208:211],v[36:39]\n" \
"ds_read_b64_tr_b16 v[196:197],v242 offset:32768\nds_read_b64_tr_b16 v[198:199],v242 offset:33024\nM v[96:99],a[56:59],v[208:211],v[96:99]\nds_read_b64_tr_b16 v[200:201],v241 offset:33280\nds_read_b64_tr_b16 v[202:203],v241 offset:33536\nM v[100:103],a[60:63],v[208:211],v[100:103]\nbuffer_store_dword v254,off,s[52:55],s79 sc1\nM v[40:43],a[48:51],v[212:215],v[40:43]\n" \
"ds_read_b64_tr_b16 v[204:205],v242 offset:33280\nds_read_b64_tr_b16 v[206:207],v242 offset:33536\nM v[44:47],a[52:55],v[212:215],v[44:47]\nds_read_b64_tr_b16 v[208:209],v241 offset:33792\nds_read_b64_tr_b16 v[210:211],v241 offset:34048\nM v[104:107],a[56:59],v[212:215],v[104:107]\nbuffer_load_dwordx4 v243,s[40:43],s69 offen offset:0 lds\nM v[108:111],a[60:63],v[212:215],v[108:111]\n" \
"ds_read_b64_tr_b16 v[212:213],v242 offset:33792\nds_read_b64_tr_b16 v[214:215],v242 offset:34048\nM v[48:51],a[48:51],v[216:219],v[48:51]\nds_write_b64 v250,v[138:139] offset:2056\ns_lshl4_add_u32 s79,s70,65536\nM v[52:55],a[52:55],v[216:219],v[52:55]\ns_lshr_b32 s70,s6,3\ns_lshl2_add_u32 s70,s70,s7\n" \
"M v[112:115],a[56:59],v[216:219],v[112:115]\nbuffer_load_dwordx4 v244,s[40:43],s69 offen offset:1024 lds\nM v[116:119],a[60:63],v[216:219],v[116:119]\nds_read_b64_tr_b16 v[216:217],v241 offset:34304\nds_read_b64_tr_b16 v[218:219],v241 offset:34560\nM v[56:59],a[48:51],v[220:223],v[56:59]\ns_and_b32 s70,s70,63\ns_add_u32 s70,s70,s71\n" \
"M v[60:63],a[52:55],v[220:223],v[60:63]\ns_lshl_b32 s70,s70,13\nv_add_u32 v229,s70,v229\nM v[120:123],a[56:59],v[220:223],v[120:123]\nbuffer_load_dwordx4 v245,s[40:43],s69 offen offset:2048 lds\nM v[124:127],a[60:63],v[220:223],v[124:127]\nds_read_b64_tr_b16 v[220:221],v242 offset:34304\ns_waitcnt lgkmcnt(7)\n" \
"M v[0:3],a[64:67],v[192:195],v[0:3]\nds_read_b64_tr_b16 v[222:223],v242 offset:34560\ns_lshl2_add_u32 s70,s6,s7\nM v[4:7],a[68:71],v[192:195],v[4:7]\nds_write_b64 v250,v[140:141] offset:3072\nM v[64:67],a[72:75],v[192:195],v[64:67]\nbuffer_store_dwordx4 v[152:155],v240,s[52:55],s74 offen offset:2048 sc1\nM v[68:71],a[76:79],v[192:195],v[68:71]\n" \
"ds_read_b64_tr_b16 v[192:193],v241 offset:40960\nds_read_b64_tr_b16 v[194:195],v241 offset:41216\nM v[8:11],a[64:67],v[196:199],v[8:11]\ns_cmp_ge_u32 s70,672\ns_cselect_b32 s71,672,0\nM v[12:15],a[68:71],v[196:199],v[12:15]\ns_sub_u32 s70,s70,s71\nM v[72:75],a[72:75],v[196:199],v[72:75]\n" \
"buffer_load_dwordx4 v246,s[40:43],s69 offen offset:3072 lds\nM v[76:79],a[76:79],v[196:199],v[76:79]\nds_read_b64_tr_b16 v[196:197],v242 offset:40960\nds_read_b64_tr_b16 v[198:199],v242 offset:41216\nM v[16:19],a[64:67],v[200:203],v[16:19]\ns_cmp_ge_u32 s70,224\ns_cselect_b64 s[92:93],s[24:25],s[22:23]\ns_cselect_b32 s71,224,0\n" \
"M v[20:23],a[68:71],v[200:203],v[20:23]\ns_cmp_ge_u32 s70,448\ns_cselect_b64 s[92:93],s[26:27],s[92:93]\ns_cselect_b32 s71,448,s71\nM v[80:83],a[72:75],v[200:203],v[80:83]\ns_sub_u32 s70,s70,s71\ns_lshl_b32 s70,s70,13\nM v[84:87],a[76:79],v[200:203],v[84:87]\n" \
"ds_read_b64_tr_b16 v[200:201],v241 offset:41472\nds_read_b64_tr_b16 v[202:203],v241 offset:41728\nM v[24:27],a[64:67],v[204:207],v[24:27]\nds_write_b64 v250,v[142:143] offset:3080\nM v[28:31],a[68:71],v[204:207],v[28:31]\nM v[88:91],a[72:75],v[204:207],v[88:91]\nM v[92:95],a[76:79],v[204:207],v[92:95]\nds_read_b64_tr_b16 v[204:205],v242 offset:41472\n" \
"s_waitcnt lgkmcnt(6)\nM v[32:35],a[64:67],v[208:211],v[32:35]\nds_read_b64_tr_b16 v[206:207],v242 offset:41728\nM v[36:39],a[68:71],v[208:211],v[36:39]\nM v[96:99],a[72:75],v[208:211],v[96:99]\nbuffer_store_dwordx4 v[156:159],v240,s[52:55],s74 offen offset:3072 sc1\nM v[100:103],a[76:79],v[208:211],v[100:103]\nds_read_b64_tr_b16 v[208:209],v241 offset:41984\n" \
"ds_read_b64_tr_b16 v[210:211],v241 offset:42240\nM v[40:43],a[64:67],v[212:215],v[40:43]\ns_add_u32 s74,s74,4096\nM v[44:47],a[68:71],v[212:215],v[44:47]\nM v[104:107],a[72:75],v[212:215],v[104:107]\nM v[108:111],a[76:79],v[212:215],v[108:111]\nds_read_b64_tr_b16 v[212:213],v242 offset:41984\nds_read_b64_tr_b16 v[214:215],v242 offset:42240\n" \
"M v[48:51],a[64:67],v[216:219],v[48:51]\nds_write_b64 v250,v[144:145] offset:4096\nM v[52:55],a[68:71],v[216:219],v[52:55]\nM v[112:115],a[72:75],v[216:219],v[112:115]\nM v[116:119],a[76:79],v[216:219],v[116:119]\nds_read_b64_tr_b16 v[216:217],v241 offset:42496\nds_read_b64_tr_b16 v[218:219],v241 offset:42752\nM v[56:59],a[64:67],v[220:223],v[56:59]\n" \
"M v[60:63],a[68:71],v[220:223],v[60:63]\nM v[120:123],a[72:75],v[220:223],v[120:123]\nM v[124:127],a[76:79],v[220:223],v[124:127]\nds_read_b64_tr_b16 v[220:221],v242 offset:42496\nds_read_b64_tr_b16 v[222:223],v242 offset:42752\nM v[0:3],a[80:83],v[192:195],v[0:3]\nM v[4:7],a[84:87],v[192:195],v[4:7]\nM v[64:67],a[88:91],v[192:195],v[64:67]\n" \
"M v[68:71],a[92:95],v[192:195],v[68:71]\ns_waitcnt lgkmcnt(7)\nM v[8:11],a[80:83],v[196:199],v[8:11]\nbuffer_store_dwordx4 v[160:163],v240,s[52:55],s74 offen offset:0 sc1\nM v[12:15],a[84:87],v[196:199],v[12:15]\nds_write_b64 v250,v[146:147] offset:4104\nM v[72:75],a[88:91],v[196:199],v[72:75]\nM v[76:79],a[92:95],v[196:199],v[76:79]\n" \
"M v[16:19],a[80:83],v[200:203],v[16:19]\nM v[20:23],a[84:87],v[200:203],v[20:23]\nM v[80:83],a[88:91],v[200:203],v[80:83]\nM v[84:87],a[92:95],v[200:203],v[84:87]\nM v[24:27],a[80:83],v[204:207],v[24:27]\ns_add_u32 m0,s64,131072\ns_add_u32 s69,s68,3145728\nM v[28:31],a[84:87],v[204:207],v[28:31]\n" \
"M v[88:91],a[88:91],v[204:207],v[88:91]\ns_waitcnt vmcnt(14) lgkmcnt(1)\ns_barrier\nM v[92:95],a[92:95],v[204:207],v[92:95]\nds_read_b64_tr_b16 v[192:193],v241 offset:49152\nds_read_b64_tr_b16 v[194:195],v241 offset:49408\nM v[32:35],a[80:83],v[208:211],v[32:35]\nds_read_b64_tr_b16 v[196:197],v242 offset:49152\n" \
"ds_read_b64_tr_b16 v[198:199],v242 offset:49408\nM v[36:39],a[84:87],v[208:211],v[36:39]\nds_read_b64_tr_b16 v[200:201],v241 offset:49664\nds_read_b64_tr_b16 v[202:203],v241 offset:49920\nM v[96:99],a[88:91],v[208:211],v[96:99]\nds_read_b64_tr_b16 v[204:205],v242 offset:49664\nds_read_b64_tr_b16 v[206:207],v242 offset:49920\nM v[100:103],a[92:95],v[208:211],v[100:103]\n" \
"ds_read_b64_tr_b16 v[208:209],v241 offset:50176\nds_read_b64_tr_b16 v[210:211],v241 offset:50432\nM v[40:43],a[80:83],v[212:215],v[40:43]\nbuffer_load_dwordx4 v243,s[40:43],s69 offen offset:0 lds\nM v[44:47],a[84:87],v[212:215],v[44:47]\nds_write_b64 v250,v[148:149] offset:5120\nM v[104:107],a[88:91],v[212:215],v[104:107]\nM v[108:111],a[92:95],v[212:215],v[108:111]\n" \
"ds_read_b64_tr_b16 v[212:213],v242 offset:50176\nds_read_b64_tr_b16 v[214:215],v242 offset:50432\nM v[48:51],a[80:83],v[216:219],v[48:51]\nbuffer_store_dwordx4 v[164:167],v240,s[52:55],s74 offen offset:1024 sc1\nM v[52:55],a[84:87],v[216:219],v[52:55]\nM v[112:115],a[88:91],v[216:219],v[112:115]\nM v[116:119],a[92:95],v[216:219],v[116:119]\nds_read_b64_tr_b16 v[216:217],v241 offset:50688\n" \
"ds_read_b64_tr_b16 v[218:219],v241 offset:50944\nM v[56:59],a[80:83],v[220:223],v[56:59]\nbuffer_load_dwordx4 v244,s[40:43],s69 offen offset:1024 lds\nM v[60:63],a[84:87],v[220:223],v[60:63]\nds_write_b64 v250,v[150:151] offset:5128\nM v[120:123],a[88:91],v[220:223],v[120:123]\nM v[124:127],a[92:95],v[220:223],v[124:127]\nds_read_b64_tr_b16 v[220:221],v242 offset:50688\n" \
"s_waitcnt lgkmcnt(9)\nM v[0:3],a[96:99],v[192:195],v[0:3]\nbuffer_load_dwordx4 v245,s[40:43],s69 offen offset:2048 lds\nM v[4:7],a[100:103],v[192:195],v[4:7]\nds_read_b64_tr_b16 v[222:223],v242 offset:50944\nM v[64:67],a[104:107],v[192:195],v[64:67]\nM v[68:71],a[108:111],v[192:195],v[68:71]\nds_read_b64_tr_b16 v[192:193],v241 offset:57344\n" \
"ds_read_b64_tr_b16 v[194:195],v241 offset:57600\nM v[8:11],a[96:99],v[196:199],v[8:11]\nbuffer_load_dwordx4 v246,s[40:43],s69 offen offset:3072 lds\nM v[12:15],a[100:103],v[196:199],v[12:15]\nM v[72:75],a[104:107],v[196:199],v[72:75]\nM v[76:79],a[108:111],v[196:199],v[76:79]\nds_read_b64_tr_b16 v[196:197],v242 offset:57344\nds_read_b64_tr_b16 v[198:199],v242 offset:57600\n" \
"M v[16:19],a[96:99],v[200:203],v[16:19]\nbuffer_store_dwordx4 v[168:171],v240,s[52:55],s74 offen offset:2048 sc1\nM v[20:23],a[100:103],v[200:203],v[20:23]\nds_write_b64 v250,v[152:153] offset:6144\nM v[80:83],a[104:107],v[200:203],v[80:83]\nM v[84:87],a[108:111],v[200:203],v[84:87]\nds_read_b64_tr_b16 v[200:201],v241 offset:57856\nds_read_b64_tr_b16 v[202:203],v241 offset:58112\n" \
"M v[24:27],a[96:99],v[204:207],v[24:27]\nM v[28:31],a[100:103],v[204:207],v[28:31]\nM v[88:91],a[104:107],v[204:207],v[88:91]\nM v[92:95],a[108:111],v[204:207],v[92:95]\nds_read_b64_tr_b16 v[204:205],v242 offset:57856\ns_waitcnt lgkmcnt(8)\nM v[32:35],a[96:99],v[208:211],v[32:35]\nds_read_b64_tr_b16 v[206:207],v242 offset:58112\n" \
"M v[36:39],a[100:103],v[208:211],v[36:39]\nM v[96:99],a[104:107],v[208:211],v[96:99]\nM v[100:103],a[108:111],v[208:211],v[100:103]\nds_read_b64_tr_b16 v[208:209],v241 offset:58368\nds_read_b64_tr_b16 v[210:211],v241 offset:58624\nM v[40:43],a[96:99],v[212:215],v[40:43]\nds_write_b64 v250,v[154:155] offset:6152\nM v[44:47],a[100:103],v[212:215],v[44:47]\n" \
"M v[104:107],a[104:107],v[212:215],v[104:107]\nM v[108:111],a[108:111],v[212:215],v[108:111]\nds_read_b64_tr_b16 v[212:213],v242 offset:58368\nds_read_b64_tr_b16 v[214:215],v242 offset:58624\nM v[48:51],a[96:99],v[216:219],v[48:51]\nM v[52:55],a[100:103],v[216:219],v[52:55]\nM v[112:115],a[104:107],v[216:219],v[112:115]\nbuffer_store_dwordx4 v[172:175],v240,s[52:55],s74 offen offset:3072 sc1\n" \
"M v[116:119],a[108:111],v[216:219],v[116:119]\nds_read_b64_tr_b16 v[216:217],v241 offset:58880\nds_read_b64_tr_b16 v[218:219],v241 offset:59136\nM v[56:59],a[96:99],v[220:223],v[56:59]\ns_add_u32 s74,s74,4096\nM v[60:63],a[100:103],v[220:223],v[60:63]\nM v[120:123],a[104:107],v[220:223],v[120:123]\nM v[124:127],a[108:111],v[220:223],v[124:127]\n" \
"ds_read_b64_tr_b16 v[220:221],v242 offset:58880\ns_waitcnt lgkmcnt(8)\nM v[0:3],a[112:115],v[192:195],v[0:3]\nds_write_b64 v250,v[156:157] offset:7168\nds_read_b64_tr_b16 v[222:223],v242 offset:59136\nM v[4:7],a[116:119],v[192:195],v[4:7]\nM v[64:67],a[120:123],v[192:195],v[64:67]\nM v[68:71],a[124:127],v[192:195],v[68:71]\n" \
"M v[8:11],a[112:115],v[196:199],v[8:11]\nM v[12:15],a[116:119],v[196:199],v[12:15]\nM v[72:75],a[120:123],v[196:199],v[72:75]\nM v[76:79],a[124:127],v[196:199],v[76:79]\nM v[16:19],a[112:115],v[200:203],v[16:19]\nM v[20:23],a[116:119],v[200:203],v[20:23]\nM v[80:83],a[120:123],v[200:203],v[80:83]\nM v[84:87],a[124:127],v[200:203],v[84:87]\n" \
"M v[24:27],a[112:115],v[204:207],v[24:27]\nbuffer_store_dwordx4 v[176:179],v240,s[52:55],s74 offen offset:0 sc1\nM v[28:31],a[116:119],v[204:207],v[28:31]\ns_add_u32 m0,s64,147456\ns_add_u32 s69,s68,3670016\nM v[88:91],a[120:123],v[204:207],v[88:91]\ns_waitcnt vmcnt(17) lgkmcnt(0)\ns_barrier\n" \
"M v[92:95],a[124:127],v[204:207],v[92:95]\nds_read_b64_tr_b16 v[192:193],v241 offset:0\nds_read_b64_tr_b16 v[194:195],v241 offset:256\nM v[32:35],a[112:115],v[208:211],v[32:35]\nds_read_b64_tr_b16 v[196:197],v242 offset:0\nds_read_b64_tr_b16 v[198:199],v242 offset:256\nM v[36:39],a[116:119],v[208:211],v[36:39]\nds_read_b64_tr_b16 v[200:201],v241 offset:512\n" \
"ds_read_b64_tr_b16 v[202:203],v241 offset:768\nM v[96:99],a[120:123],v[208:211],v[96:99]\nds_read_b64_tr_b16 v[204:205],v242 offset:512\nds_read_b64_tr_b16 v[206:207],v242 offset:768\nM v[100:103],a[124:127],v[208:211],v[100:103]\nds_read_b64_tr_b16 v[208:209],v241 offset:1024\nds_read_b64_tr_b16 v[210:211],v241 offset:1280\nM v[40:43],a[112:115],v[212:215],v[40:43]\n" \
"buffer_load_dwordx4 v243,s[40:43],s69 offen offset:0 lds\nM v[44:47],a[116:119],v[212:215],v[44:47]\nds_write_b64 v250,v[158:159] offset:7176\nM v[104:107],a[120:123],v[212:215],v[104:107]\nM v[108:111],a[124:127],v[212:215],v[108:111]\nds_read_b64_tr_b16 v[212:213],v242 offset:1024\nds_read_b64_tr_b16 v[214:215],v242 offset:1280\nM v[48:51],a[112:115],v[216:219],v[48:51]\n" \
"buffer_load_dwordx4 v244,s[40:43],s69 offen offset:1024 lds\nM v[52:55],a[116:119],v[216:219],v[52:55]\nds_write_b64 v250,v[160:161] offset:8192\nM v[112:115],a[120:123],v[216:219],v[112:115]\nM v[116:119],a[124:127],v[216:219],v[116:119]\nds_read_b64_tr_b16 v[216:217],v241 offset:1536\nds_read_b64_tr_b16 v[218:219],v241 offset:1792\nM v[56:59],a[112:115],v[220:223],v[56:59]\n" \
"buffer_load_dwordx4 v245,s[40:43],s69 offen offset:2048 lds\nM v[60:63],a[116:119],v[220:223],v[60:63]\nM v[120:123],a[120:123],v[220:223],v[120:123]\nM v[124:127],a[124:127],v[220:223],v[124:127]\nds_read_b64_tr_b16 v[220:221],v242 offset:1536\ns_waitcnt lgkmcnt(9)\nM v[0:3],a[128:131],v[192:195],v[0:3]\nbuffer_store_dwordx4 v[180:183],v240,s[52:55],s74 offen offset:1024 sc1\n" \
"M v[4:7],a[132:135],v[192:195],v[4:7]\nds_read_b64_tr_b16 v[222:223],v242 offset:1792\nM v[64:67],a[136:139],v[192:195],v[64:67]\nM v[68:71],a[140:143],v[192:195],v[68:71]\nds_read_b64_tr_b16 v[192:193],v241 offset:8192\nds_read_b64_tr_b16 v[194:195],v241 offset:8448\nM v[8:11],a[128:131],v[196:199],v[8:11]\nbuffer_load_dwordx4 v246,s[40:43],s69 offen offset:3072 lds\n" \
"M v[12:15],a[132:135],v[196:199],v[12:15]\nds_write_b64 v250,v[162:163] offset:8200\nM v[72:75],a[136:139],v[196:199],v[72:75]\nM v[76:79],a[140:143],v[196:199],v[76:79]\nds_read_b64_tr_b16 v[196:197],v242 offset:8192\nds_read_b64_tr_b16 v[198:199],v242 offset:8448\nM v[16:19],a[128:131],v[200:203],v[16:19]\nM v[20:23],a[132:135],v[200:203],v[20:23]\n" \
"M v[80:83],a[136:139],v[200:203],v[80:83]\nM v[84:87],a[140:143],v[200:203],v[84:87]\nds_read_b64_tr_b16 v[200:201],v241 offset:8704\nds_read_b64_tr_b16 v[202:203],v241 offset:8960\nM v[24:27],a[128:131],v[204:207],v[24:27]\nM v[28:31],a[132:135],v[204:207],v[28:31]\nM v[88:91],a[136:139],v[204:207],v[88:91]\nM v[92:95],a[140:143],v[204:207],v[92:95]\n" \
"ds_read_b64_tr_b16 v[204:205],v242 offset:8704\ns_waitcnt lgkmcnt(9)\nM v[32:35],a[128:131],v[208:211],v[32:35]\nbuffer_store_dwordx4 v[184:187],v240,s[52:55],s74 offen offset:2048 sc1\nM v[36:39],a[132:135],v[208:211],v[36:39]\nds_write_b64 v250,v[164:165] offset:9216\nds_read_b64_tr_b16 v[206:207],v242 offset:8960\nM v[96:99],a[136:139],v[208:211],v[96:99]\n" \
"M v[100:103],a[140:143],v[208:211],v[100:103]\nds_read_b64_tr_b16 v[208:209],v241 offset:9216\nds_read_b64_tr_b16 v[210:211],v241 offset:9472\nM v[40:43],a[128:131],v[212:215],v[40:43]\nM v[44:47],a[132:135],v[212:215],v[44:47]\nM v[104:107],a[136:139],v[212:215],v[104:107]\nbuffer_load_dword v248,v228,s[52:55],s80 offen sc1\nM v[108:111],a[140:143],v[212:215],v[108:111]\n" \
"ds_read_b64_tr_b16 v[212:213],v242 offset:9216\nds_read_b64_tr_b16 v[214:215],v242 offset:9472\nM v[48:51],a[128:131],v[216:219],v[48:51]\nM v[52:55],a[132:135],v[216:219],v[52:55]\nM v[112:115],a[136:139],v[216:219],v[112:115]\nM v[116:119],a[140:143],v[216:219],v[116:119]\nds_read_b64_tr_b16 v[216:217],v241 offset:9728\ns_waitcnt lgkmcnt(8)\n" \
"M v[56:59],a[128:131],v[220:223],v[56:59]\nds_write_b64 v250,v[166:167] offset:9224\nds_read_b64_tr_b16 v[218:219],v241 offset:9984\nM v[60:63],a[132:135],v[220:223],v[60:63]\nM v[120:123],a[136:139],v[220:223],v[120:123]\nM v[124:127],a[140:143],v[220:223],v[124:127]\nds_read_b64_tr_b16 v[220:221],v242 offset:9728\nds_read_b64_tr_b16 v[222:223],v242 offset:9984\n" \
"M v[0:3],a[144:147],v[192:195],v[0:3]\nM v[4:7],a[148:151],v[192:195],v[4:7]\nM v[64:67],a[152:155],v[192:195],v[64:67]\nbuffer_store_dwordx4 v[188:191],v240,s[52:55],s74 offen offset:3072 sc1\nM v[68:71],a[156:159],v[192:195],v[68:71]\ns_add_u32 s74,s74,16764928\nM v[8:11],a[144:147],v[196:199],v[8:11]\nM v[12:15],a[148:151],v[196:199],v[12:15]\n" \
"M v[72:75],a[152:155],v[196:199],v[72:75]\nM v[76:79],a[156:159],v[196:199],v[76:79]\nM v[16:19],a[144:147],v[200:203],v[16:19]\nds_write_b64 v250,v[168:169] offset:10240\nM v[20:23],a[148:151],v[200:203],v[20:23]\nM v[80:83],a[152:155],v[200:203],v[80:83]\nM v[84:87],a[156:159],v[200:203],v[84:87]\ns_waitcnt lgkmcnt(5)\n" \
"M v[24:27],a[144:147],v[204:207],v[24:27]\ns_sub_u32 s40,s12,0x1000\ns_subb_u32 s41,s13,0\nM v[28:31],a[148:151],v[204:207],v[28:31]\ns_add_u32 m0,s64,98304\ns_add_u32 s69,s68,0\nM v[88:91],a[152:155],v[204:207],v[88:91]\ns_waitcnt vmcnt(18) lgkmcnt(1)\n" \
"s_barrier\nM v[92:95],a[156:159],v[204:207],v[92:95]\nds_read_b64_tr_b16 v[192:193],v241 offset:16384\nds_read_b64_tr_b16 v[194:195],v241 offset:16640\nM v[32:35],a[144:147],v[208:211],v[32:35]\nds_read_b64_tr_b16 v[196:197],v242 offset:16384\nds_read_b64_tr_b16 v[198:199],v242 offset:16640\nM v[36:39],a[148:151],v[208:211],v[36:39]\n" \
"ds_read_b64_tr_b16 v[200:201],v241 offset:16896\nds_read_b64_tr_b16 v[202:203],v241 offset:17152\nM v[96:99],a[152:155],v[208:211],v[96:99]\nds_read_b64_tr_b16 v[204:205],v242 offset:16896\nds_read_b64_tr_b16 v[206:207],v242 offset:17152\nM v[100:103],a[156:159],v[208:211],v[100:103]\nds_read_b64_tr_b16 v[208:209],v241 offset:17408\nds_read_b64_tr_b16 v[210:211],v241 offset:17664\n" \
"M v[40:43],a[144:147],v[212:215],v[40:43]\nbuffer_load_dwordx4 v243,s[40:43],s69 offen offset:0 lds\nM v[44:47],a[148:151],v[212:215],v[44:47]\nds_write_b64 v250,v[170:171] offset:10248\ns_lshl_b32 s12,s61,13\nM v[104:107],a[152:155],v[212:215],v[104:107]\ns_cmp_eq_u32 s62,7\ns_cselect_b32 s13,s12,s12\n" \
"M v[108:111],a[156:159],v[212:215],v[108:111]\nds_read_b64_tr_b16 v[212:213],v242 offset:17408\nds_read_b64_tr_b16 v[214:215],v242 offset:17664\nM v[48:51],a[144:147],v[216:219],v[48:51]\nbuffer_load_dwordx4 v244,s[40:43],s69 offen offset:1024 lds\nM v[52:55],a[148:151],v[216:219],v[52:55]\nM v[112:115],a[152:155],v[216:219],v[112:115]\nM v[116:119],a[156:159],v[216:219],v[116:119]\n" \
"ds_read_b64_tr_b16 v[216:217],v241 offset:17920\nds_read_b64_tr_b16 v[218:219],v241 offset:18176\nM v[56:59],a[144:147],v[220:223],v[56:59]\nbuffer_load_dwordx4 v245,s[40:43],s69 offen offset:2048 lds\nM v[60:63],a[148:151],v[220:223],v[60:63]\nM v[120:123],a[152:155],v[220:223],v[120:123]\nM v[124:127],a[156:159],v[220:223],v[124:127]\nds_read_b64_tr_b16 v[220:221],v242 offset:17920\n" \
"s_waitcnt lgkmcnt(7)\nM v[0:3],a[160:163],v[192:195],v[0:3]\nbuffer_load_dwordx4 v246,s[40:43],s69 offen offset:3072 lds\nM v[4:7],a[164:167],v[192:195],v[4:7]\nds_write_b64 v250,v[172:173] offset:11264\nds_read_b64_tr_b16 v[222:223],v242 offset:18176\nM v[64:67],a[168:171],v[192:195],v[64:67]\nM v[68:71],a[172:175],v[192:195],v[68:71]\n" \
"ds_read_b64_tr_b16 v[192:193],v241 offset:24576\nds_read_b64_tr_b16 v[194:195],v241 offset:24832\nM v[8:11],a[160:163],v[196:199],v[8:11]\nM v[12:15],a[164:167],v[196:199],v[12:15]\nM v[72:75],a[168:171],v[196:199],v[72:75]\nM v[76:79],a[172:175],v[196:199],v[76:79]\nds_read_b64_tr_b16 v[196:197],v242 offset:24576\nds_read_b64_tr_b16 v[198:199],v242 offset:24832\n" \
"M v[16:19],a[160:163],v[200:203],v[16:19]\nM v[20:23],a[164:167],v[200:203],v[20:23]\nM v[80:83],a[168:171],v[200:203],v[80:83]\nM v[84:87],a[172:175],v[200:203],v[84:87]\nds_read_b64_tr_b16 v[200:201],v241 offset:25088\nds_read_b64_tr_b16 v[202:203],v241 offset:25344\nM v[24:27],a[160:163],v[204:207],v[24:27]\nds_write_b64 v250,v[174:175] offset:11272\n" \
"M v[28:31],a[164:167],v[204:207],v[28:31]\ns_waitcnt vmcnt(5)\nv_cmp_eq_u32 vcc,0,v248\nM v[88:91],a[168:171],v[204:207],v[88:91]\ns_cbranch_vccnz .Lps0\n.Lpb0:\ns_lshl_b32 s80,s63,7\nM v[92:95],a[172:175],v[204:207],v[92:95]\n" \
"ds_read_b64_tr_b16 v[204:205],v242 offset:25088\ns_waitcnt lgkmcnt(8)\nM v[32:35],a[160:163],v[208:211],v[32:35]\nds_read_b64_tr_b16 v[206:207],v242 offset:25344\ns_add_u32 s80,s80,81920\nM v[36:39],a[164:167],v[208:211],v[36:39]\nbuffer_load_dwordx4 a[0:3],v240,s[52:55],s76 offen offset:0 sc1\nM v[96:99],a[168:171],v[208:211],v[96:99]\n" \
"v_and_b32 v228,31,v249\nv_lshlrev_b32 v228,2,v228\nM v[100:103],a[172:175],v[208:211],v[100:103]\nds_read_b64_tr_b16 v[208:209],v241 offset:25600\nds_read_b64_tr_b16 v[210:211],v241 offset:25856\nM v[40:43],a[160:163],v[212:215],v[40:43]\nv_mov_b32 v251,v228\nM v[44:47],a[164:167],v[212:215],v[44:47]\n" \
"buffer_load_dwordx4 a[4:7],v240,s[52:55],s76 offen offset:1024 sc1\nM v[104:107],a[168:171],v[212:215],v[104:107]\nM v[108:111],a[172:175],v[212:215],v[108:111]\nds_read_b64_tr_b16 v[212:213],v242 offset:25600\nds_read_b64_tr_b16 v[214:215],v242 offset:25856\nM v[48:51],a[160:163],v[216:219],v[48:51]\nds_write_b64 v250,v[176:177] offset:12288\nM v[52:55],a[164:167],v[216:219],v[52:55]\n" \
"buffer_load_dwordx4 a[8:11],v240,s[52:55],s77 offen offset:0 sc1\nM v[112:115],a[168:171],v[216:219],v[112:115]\nM v[116:119],a[172:175],v[216:219],v[116:119]\nds_read_b64_tr_b16 v[216:217],v241 offset:26112\nds_read_b64_tr_b16 v[218:219],v241 offset:26368\nM v[56:59],a[160:163],v[220:223],v[56:59]\nM v[60:63],a[164:167],v[220:223],v[60:63]\nbuffer_load_dwordx4 a[12:15],v240,s[52:55],s77 offen offset:1024 sc1\n" \
"M v[120:123],a[168:171],v[220:223],v[120:123]\nM v[124:127],a[172:175],v[220:223],v[124:127]\nds_read_b64_tr_b16 v[220:221],v242 offset:26112\ns_waitcnt lgkmcnt(8)\nM v[0:3],a[176:179],v[192:195],v[0:3]\nds_read_b64_tr_b16 v[222:223],v242 offset:26368\nM v[4:7],a[180:183],v[192:195],v[4:7]\nM v[64:67],a[184:187],v[192:195],v[64:67]\n" \
"buffer_load_dwordx4 a[16:19],v240,s[52:55],s76 offen offset:2048 sc1\nM v[68:71],a[188:191],v[192:195],v[68:71]\nM v[8:11],a[176:179],v[196:199],v[8:11]\nds_write_b64 v250,v[178:179] offset:12296\nM v[12:15],a[180:183],v[196:199],v[12:15]\nM v[72:75],a[184:187],v[196:199],v[72:75]\nM v[76:79],a[188:191],v[196:199],v[76:79]\nM v[16:19],a[176:179],v[200:203],v[16:19]\n" \
"M v[20:23],a[180:183],v[200:203],v[20:23]\nM v[80:83],a[184:187],v[200:203],v[80:83]\nbuffer_load_dwordx4 a[20:23],v240,s[52:55],s76 offen offset:3072 sc1\nM v[84:87],a[188:191],v[200:203],v[84:87]\ns_add_u32 s76,s76,4096\nM v[24:27],a[176:179],v[204:207],v[24:27]\ns_add_u32 m0,s64,114688\ns_add_u32 s69,s68,524288\n" \
"M v[28:31],a[180:183],v[204:207],v[28:31]\nM v[88:91],a[184:187],v[204:207],v[88:91]\ns_waitcnt lgkmcnt(1)\ns_barrier\nM v[92:95],a[188:191],v[204:207],v[92:95]\nds_read_b64_tr_b16 v[192:193],v241 offset:32768\nds_read_b64_tr_b16 v[194:195],v241 offset:33024\nM v[32:35],a[176:179],v[208:211],v[32:35]\n" \
"ds_read_b64_tr_b16 v[196:197],v242 offset:32768\nds_read_b64_tr_b16 v[198:199],v242 offset:33024\nM v[36:39],a[180:183],v[208:211],v[36:39]\nds_read_b64_tr_b16 v[200:201],v241 offset:33280\nds_read_b64_tr_b16 v[202:203],v241 offset:33536\nM v[96:99],a[184:187],v[208:211],v[96:99]\nds_read_b64_tr_b16 v[204:205],v242 offset:33280\nds_read_b64_tr_b16 v[206:207],v242 offset:33536\n" \
"M v[100:103],a[188:191],v[208:211],v[100:103]\nds_read_b64_tr_b16 v[208:209],v241 offset:33792\nds_read_b64_tr_b16 v[210:211],v241 offset:34048\nM v[40:43],a[176:179],v[212:215],v[40:43]\nbuffer_load_dwordx4 a[24:27],v240,s[52:55],s77 offen offset:2048 sc1\nM v[44:47],a[180:183],v[212:215],v[44:47]\nds_write_b64 v250,v[180:181] offset:13312\nM v[104:107],a[184:187],v[212:215],v[104:107]\n" \
"M v[108:111],a[188:191],v[212:215],v[108:111]\nds_read_b64_tr_b16 v[212:213],v242 offset:33792\nds_read_b64_tr_b16 v[214:215],v242 offset:34048\nM v[48:51],a[176:179],v[216:219],v[48:51]\nbuffer_load_dwordx4 v243,s[40:43],s69 offen offset:0 lds\nM v[52:55],a[180:183],v[216:219],v[52:55]\nM v[112:115],a[184:187],v[216:219],v[112:115]\nM v[116:119],a[188:191],v[216:219],v[116:119]\n" \
"ds_read_b64_tr_b16 v[216:217],v241 offset:34304\nds_read_b64_tr_b16 v[218:219],v241 offset:34560\nM v[56:59],a[176:179],v[220:223],v[56:59]\nbuffer_load_dwordx4 a[28:31],v240,s[52:55],s77 offen offset:3072 sc1\nM v[60:63],a[180:183],v[220:223],v[60:63]\nds_write_b64 v250,v[182:183] offset:13320\ns_add_u32 s77,s77,4096\nM v[120:123],a[184:187],v[220:223],v[120:123]\n" \
"M v[124:127],a[188:191],v[220:223],v[124:127]\nds_read_b64_tr_b16 v[220:221],v242 offset:34304\ns_waitcnt lgkmcnt(9)\nM v[0:3],a[192:195],v[192:195],v[0:3]\nbuffer_load_dwordx4 v244,s[40:43],s69 offen offset:1024 lds\nM v[4:7],a[196:199],v[192:195],v[4:7]\nds_read_b64_tr_b16 v[222:223],v242 offset:34560\nM v[64:67],a[200:203],v[192:195],v[64:67]\n" \
"M v[68:71],a[204:207],v[192:195],v[68:71]\nds_read_b64_tr_b16 v[192:193],v241 offset:40960\nds_read_b64_tr_b16 v[194:195],v241 offset:41216\nM v[8:11],a[192:195],v[196:199],v[8:11]\nbuffer_load_dwordx4 a[32:35],v240,s[52:55],s76 offen offset:0 sc1\nM v[12:15],a[196:199],v[196:199],v[12:15]\nM v[72:75],a[200:203],v[196:199],v[72:75]\nM v[76:79],a[204:207],v[196:199],v[76:79]\n" \
"ds_read_b64_tr_b16 v[196:197],v242 offset:40960\nds_read_b64_tr_b16 v[198:199],v242 offset:41216\nM v[16:19],a[192:195],v[200:203],v[16:19]\nbuffer_load_dwordx4 v245,s[40:43],s69 offen offset:2048 lds\nM v[20:23],a[196:199],v[200:203],v[20:23]\nds_write_b64 v250,v[184:185] offset:14336\nM v[80:83],a[200:203],v[200:203],v[80:83]\nM v[84:87],a[204:207],v[200:203],v[84:87]\n" \
"ds_read_b64_tr_b16 v[200:201],v241 offset:41472\nds_read_b64_tr_b16 v[202:203],v241 offset:41728\nM v[24:27],a[192:195],v[204:207],v[24:27]\nbuffer_load_dwordx4 a[36:39],v240,s[52:55],s76 offen offset:1024 sc1\nM v[28:31],a[196:199],v[204:207],v[28:31]\nM v[88:91],a[200:203],v[204:207],v[88:91]\nM v[92:95],a[204:207],v[204:207],v[92:95]\nds_read_b64_tr_b16 v[204:205],v242 offset:41472\n" \
"s_waitcnt lgkmcnt(8)\nM v[32:35],a[192:195],v[208:211],v[32:35]\nbuffer_load_dwordx4 v246,s[40:43],s69 offen offset:3072 lds\nM v[36:39],a[196:199],v[208:211],v[36:39]\nds_read_b64_tr_b16 v[206:207],v242 offset:41728\nM v[96:99],a[200:203],v[208:211],v[96:99]\nM v[100:103],a[204:207],v[208:211],v[100:103]\nds_read_b64_tr_b16 v[208:209],v241 offset:41984\n" \
"ds_read_b64_tr_b16 v[210:211],v241 offset:42240\nM v[40:43],a[192:195],v[212:215],v[40:43]\nbuffer_load_dwordx4 a[40:43],v240,s[52:55],s77 offen offset:0 sc1\nM v[44:47],a[196:199],v[212:215],v[44:47]\nds_write_b64 v250,v[186:187] offset:14344\nM v[104:107],a[200:203],v[212:215],v[104:107]\nM v[108:111],a[204:207],v[212:215],v[108:111]\nds_read_b64_tr_b16 v[212:213],v242 offset:41984\n" \
"ds_read_b64_tr_b16 v[214:215],v242 offset:42240\nM v[48:51],a[192:195],v[216:219],v[48:51]\ns_waitcnt vmcnt(19)\nbuffer_store_dword v254,off,s[52:55],s29 sc1\nM v[52:55],a[196:199],v[216:219],v[52:55]\ns_add_u32 s29,s29,4096\nM v[112:115],a[200:203],v[216:219],v[112:115]\nbuffer_load_dwordx4 a[44:47],v240,s[52:55],s77 offen offset:1024 sc1\n" \
"M v[116:119],a[204:207],v[216:219],v[116:119]\nds_read_b64_tr_b16 v[216:217],v241 offset:42496\nds_read_b64_tr_b16 v[218:219],v241 offset:42752\nM v[56:59],a[192:195],v[220:223],v[56:59]\nM v[60:63],a[196:199],v[220:223],v[60:63]\nM v[120:123],a[200:203],v[220:223],v[120:123]\nM v[124:127],a[204:207],v[220:223],v[124:127]\nds_read_b64_tr_b16 v[220:221],v242 offset:42496\n" \
"s_waitcnt lgkmcnt(8)\nM v[0:3],a[208:211],v[192:195],v[0:3]\nds_write_b64 v250,v[188:189] offset:15360\nds_read_b64_tr_b16 v[222:223],v242 offset:42752\nM v[4:7],a[212:215],v[192:195],v[4:7]\nM v[64:67],a[216:219],v[192:195],v[64:67]\nbuffer_load_dwordx4 a[48:51],v240,s[52:55],s76 offen offset:2048 sc1\nM v[68:71],a[220:223],v[192:195],v[68:71]\n" \
"M v[8:11],a[208:211],v[196:199],v[8:11]\nM v[12:15],a[212:215],v[196:199],v[12:15]\nM v[72:75],a[216:219],v[196:199],v[72:75]\nM v[76:79],a[220:223],v[196:199],v[76:79]\nM v[16:19],a[208:211],v[200:203],v[16:19]\nM v[20:23],a[212:215],v[200:203],v[20:23]\nM v[80:83],a[216:219],v[200:203],v[80:83]\nbuffer_load_dwordx4 a[52:55],v240,s[52:55],s76 offen offset:3072 sc1\n" \
"M v[84:87],a[220:223],v[200:203],v[84:87]\ns_add_u32 s76,s76,4096\nM v[24:27],a[208:211],v[204:207],v[24:27]\nds_write_b64 v250,v[190:191] offset:15368\ns_add_u32 m0,s64,131072\nM v[28:31],a[212:215],v[204:207],v[28:31]\ns_add_u32 s69,s68,1048576\nv_add_u32 v250,65536,v250\n" \
"M v[88:91],a[216:219],v[204:207],v[88:91]\ns_waitcnt lgkmcnt(1)\ns_barrier\nM v[92:95],a[220:223],v[204:207],v[92:95]\nds_read_b64_tr_b16 v[192:193],v241 offset:49152\nds_read_b64_tr_b16 v[194:195],v241 offset:49408\nM v[32:35],a[208:211],v[208:211],v[32:35]\nds_read_b64_tr_b16 v[196:197],v242 offset:49152\n" \
"ds_read_b64_tr_b16 v[198:199],v242 offset:49408\nM v[36:39],a[212:215],v[208:211],v[36:39]\nds_read_b64_tr_b16 v[200:201],v241 offset:49664\nds_read_b64_tr_b16 v[202:203],v241 offset:49920\nM v[96:99],a[216:219],v[208:211],v[96:99]\nds_read_b64_tr_b16 v[204:205],v242 offset:49664\nds_read_b64_tr_b16 v[206:207],v242 offset:49920\nM v[100:103],a[220:223],v[208:211],v[100:103]\n" \
"ds_read_b64_tr_b16 v[208:209],v241 offset:50176\nds_read_b64_tr_b16 v[210:211],v241 offset:50432\nM v[40:43],a[208:211],v[212:215],v[40:43]\nbuffer_load_dwordx4 a[56:59],v240,s[52:55],s77 offen offset:2048 sc1\nM v[44:47],a[212:215],v[212:215],v[44:47]\nM v[104:107],a[216:219],v[212:215],v[104:107]\nM v[108:111],a[220:223],v[212:215],v[108:111]\nds_read_b64_tr_b16 v[212:213],v242 offset:50176\n" \
"ds_read_b64_tr_b16 v[214:215],v242 offset:50432\nM v[48:51],a[208:211],v[216:219],v[48:51]\nbuffer_load_dwordx4 v243,s[40:43],s69 offen offset:0 lds\nM v[52:55],a[212:215],v[216:219],v[52:55]\nM v[112:115],a[216:219],v[216:219],v[112:115]\nM v[116:119],a[220:223],v[216:219],v[116:119]\nds_read_b64_tr_b16 v[216:217],v241 offset:50688\nds_read_b64_tr_b16 v[218:219],v241 offset:50944\n" \
"M v[56:59],a[208:211],v[220:223],v[56:59]\nbuffer_load_dwordx4 a[60:63],v240,s[52:55],s77 offen offset:3072 sc1\nM v[60:63],a[212:215],v[220:223],v[60:63]\ns_add_u32 s77,s77,4096\nM v[120:123],a[216:219],v[220:223],v[120:123]\nM v[124:127],a[220:223],v[220:223],v[124:127]\nds_read_b64_tr_b16 v[220:221],v242 offset:50688\ns_waitcnt lgkmcnt(7)\n" \
"M v[0:3],a[224:227],v[192:195],v[0:3]\nbuffer_load_dwordx4 v244,s[40:43],s69 offen offset:1024 lds\nM v[4:7],a[228:231],v[192:195],v[4:7]\nds_read_b64_tr_b16 v[222:223],v242 offset:50944\nM v[64:67],a[232:235],v[192:195],v[64:67]\nM v[68:71],a[236:239],v[192:195],v[68:71]\nds_read_b64_tr_b16 v[192:193],v241 offset:57344\nds_read_b64_tr_b16 v[194:195],v241 offset:57600\n" \
"M v[8:11],a[224:227],v[196:199],v[8:11]\nbuffer_load_dwordx4 a[64:67],v240,s[52:55],s76 offen offset:0 sc1\nM v[12:15],a[228:231],v[196:199],v[12:15]\nM v[72:75],a[232:235],v[196:199],v[72:75]\nM v[76:79],a[236:239],v[196:199],v[76:79]\nds_read_b64_tr_b16 v[196:197],v242 offset:57344\nds_read_b64_tr_b16 v[198:199],v242 offset:57600\nM v[16:19],a[224:227],v[200:203],v[16:19]\n" \
"buffer_load_dwordx4 v245,s[40:43],s69 offen offset:2048 lds\nM v[20:23],a[228:231],v[200:203],v[20:23]\nM v[80:83],a[232:235],v[200:203],v[80:83]\nM v[84:87],a[236:239],v[200:203],v[84:87]\nds_read_b64_tr_b16 v[200:201],v241 offset:57856\nds_read_b64_tr_b16 v[202:203],v241 offset:58112\nM v[24:27],a[224:227],v[204:207],v[24:27]\nbuffer_load_dwordx4 a[68:71],v240,s[52:55],s76 offen offset:1024 sc1\n" \
"M v[28:31],a[228:231],v[204:207],v[28:31]\nM v[88:91],a[232:235],v[204:207],v[88:91]\nM v[92:95],a[236:239],v[204:207],v[92:95]\nds_read_b64_tr_b16 v[204:205],v242 offset:57856\ns_waitcnt lgkmcnt(7)\nM v[32:35],a[224:227],v[208:211],v[32:35]\nbuffer_load_dwordx4 v246,s[40:43],s69 offen offset:3072 lds\nM v[36:39],a[228:231],v[208:211],v[36:39]\n" \
"ds_read_b64_tr_b16 v[206:207],v242 offset:58112\nM v[96:99],a[232:235],v[208:211],v[96:99]\nM v[100:103],a[236:239],v[208:211],v[100:103]\nds_read_b64_tr_b16 v[208:209],v241 offset:58368\nds_read_b64_tr_b16 v[210:211],v241 offset:58624\nM v[40:43],a[224:227],v[212:215],v[40:43]\nbuffer_load_dwordx4 a[72:75],v240,s[52:55],s77 offen offset:0 sc1\nM v[44:47],a[228:231],v[212:215],v[44:47]\n" \
"M v[104:107],a[232:235],v[212:215],v[104:107]\nM v[108:111],a[236:239],v[212:215],v[108:111]\nds_read_b64_tr_b16 v[212:213],v242 offset:58368\nds_read_b64_tr_b16 v[214:215],v242 offset:58624\nM v[48:51],a[224:227],v[216:219],v[48:51]\nM v[52:55],a[228:231],v[216:219],v[52:55]\nM v[112:115],a[232:235],v[216:219],v[112:115]\nbuffer_load_dwordx4 a[76:79],v240,s[52:55],s77 offen offset:1024 sc1\n" \
"M v[116:119],a[236:239],v[216:219],v[116:119]\nds_read_b64_tr_b16 v[216:217],v241 offset:58880\nds_read_b64_tr_b16 v[218:219],v241 offset:59136\nM v[56:59],a[224:227],v[220:223],v[56:59]\nM v[60:63],a[228:231],v[220:223],v[60:63]\nM v[120:123],a[232:235],v[220:223],v[120:123]\nM v[124:127],a[236:239],v[220:223],v[124:127]\nds_read_b64_tr_b16 v[220:221],v242 offset:58880\n" \
"s_waitcnt lgkmcnt(8)\nM v[0:3],a[240:243],v[192:195],v[0:3]\nds_read_b64_tr_b16 v[222:223],v242 offset:59136\nM v[4:7],a[244:247],v[192:195],v[4:7]\nM v[64:67],a[248:251],v[192:195],v[64:67]\nbuffer_load_dwordx4 a[80:83],v240,s[52:55],s76 offen offset:2048 sc1\nM v[68:71],a[252:255],v[192:195],v[68:71]\nM v[8:11],a[240:243],v[196:199],v[8:11]\n" \
"M v[12:15],a[244:247],v[196:199],v[12:15]\nM v[72:75],a[248:251],v[196:199],v[72:75]\nv_cvt_pk_bf16_f32 v128,v0,v1\nv_cvt_pk_bf16_f32 v129,v2,v3\nM v[76:79],a[252:255],v[196:199],v[76:79]\nv_cvt_pk_bf16_f32 v130,v4,v5\nv_cvt_pk_bf16_f32 v131,v6,v7\nM v[16:19],a[240:243],v[200:203],v[16:19]\n" \
"v_cvt_pk_bf16_f32 v160,v64,v65\nv_cvt_pk_bf16_f32 v161,v66,v67\nM v[20:23],a[244:247],v[200:203],v[20:23]\nv_cvt_pk_bf16_f32 v162,v68,v69\nv_cvt_pk_bf16_f32 v163,v70,v71\nM v[80:83],a[248:251],v[200:203],v[80:83]\nv_cvt_pk_bf16_f32 v132,v8,v9\nv_cvt_pk_bf16_f32 v133,v10,v11\n" \
"M v[84:87],a[252:255],v[200:203],v[84:87]\nv_cvt_pk_bf16_f32 v134,v12,v13\ns_waitcnt lgkmcnt(2)\nM v[24:27],a[240:243],v[204:207],v[24:27]\nv_cvt_pk_bf16_f32 v135,v14,v15\nv_cvt_pk_bf16_f32 v164,v72,v73\nM v[28:31],a[244:247],v[204:207],v[28:31]\nv_cvt_pk_bf16_f32 v165,v74,v75\n" \
"v_cvt_pk_bf16_f32 v166,v76,v77\nM v[88:91],a[248:251],v[204:207],v[88:91]\ns_waitcnt vmcnt(23) lgkmcnt(0)\ns_barrier\nM v[92:95],a[252:255],v[204:207],v[92:95]\nds_read_b64_tr_b16 v[192:193],v241 offset:0\nds_read_b64_tr_b16 v[194:195],v241 offset:256\nM v[32:35],a[240:243],v[208:211],v[32:35]\n" \
"ds_read_b64_tr_b16 v[196:197],v242 offset:0\nds_read_b64_tr_b16 v[198:199],v242 offset:256\nM v[36:39],a[244:247],v[208:211],v[36:39]\nds_read_b64_tr_b16 v[200:201],v241 offset:512\nds_read_b64_tr_b16 v[202:203],v241 offset:768\nM v[96:99],a[248:251],v[208:211],v[96:99]\nds_read_b64_tr_b16 v[204:205],v242 offset:512\nds_read_b64_tr_b16 v[206:207],v242 offset:768\n" \
"M v[100:103],a[252:255],v[208:211],v[100:103]\nds_read_b64_tr_b16 v[208:209],v241 offset:1024\nds_read_b64_tr_b16 v[210:211],v241 offset:1280\nM v[40:43],a[240:243],v[212:215],v[40:43]\nv_cvt_pk_bf16_f32 v167,v78,v79\nv_cvt_pk_bf16_f32 v136,v16,v17\nM v[44:47],a[244:247],v[212:215],v[44:47]\nv_cvt_pk_bf16_f32 v137,v18,v19\n" \
"v_cvt_pk_bf16_f32 v138,v20,v21\nM v[104:107],a[248:251],v[212:215],v[104:107]\nv_cvt_pk_bf16_f32 v139,v22,v23\nv_cvt_pk_bf16_f32 v168,v80,v81\nM v[108:111],a[252:255],v[212:215],v[108:111]\nv_cvt_pk_bf16_f32 v169,v82,v83\nds_read_b64_tr_b16 v[212:213],v242 offset:1024\nM v[48:51],a[240:243],v[216:219],v[48:51]\n" \
"ds_read_b64_tr_b16 v[214:215],v242 offset:1280\nv_cvt_pk_bf16_f32 v170,v84,v85\nM v[52:55],a[244:247],v[216:219],v[52:55]\nv_cvt_pk_bf16_f32 v171,v86,v87\nv_cvt_pk_bf16_f32 v140,v24,v25\nM v[112:115],a[248:251],v[216:219],v[112:115]\nv_cvt_pk_bf16_f32 v141,v26,v27\nv_cvt_pk_bf16_f32 v142,v28,v29\n" \
"M v[116:119],a[252:255],v[216:219],v[116:119]\nv_cvt_pk_bf16_f32 v143,v30,v31\nv_cvt_pk_bf16_f32 v172,v88,v89\nM v[56:59],a[240:243],v[220:223],v[56:59]\nv_cvt_pk_bf16_f32 v173,v90,v91\nds_read_b64_tr_b16 v[216:217],v241 offset:1536\nM v[60:63],a[244:247],v[220:223],v[60:63]\nds_read_b64_tr_b16 v[218:219],v241 offset:1792\n" \
"v_cvt_pk_bf16_f32 v174,v92,v93\nM v[120:123],a[248:251],v[220:223],v[120:123]\nv_cvt_pk_bf16_f32 v175,v94,v95\nv_cvt_pk_bf16_f32 v144,v32,v33\nM v[124:127],a[252:255],v[220:223],v[124:127]\nv_cvt_pk_bf16_f32 v145,v34,v35\ns_waitcnt lgkmcnt(4)\nM v[0:3],v[192:195],a[0:3],0\n" \
"v_cvt_pk_bf16_f32 v146,v36,v37\nv_cvt_pk_bf16_f32 v147,v38,v39\nM v[4:7],v[192:195],a[4:7],0\nv_cvt_pk_bf16_f32 v176,v96,v97\nv_cvt_pk_bf16_f32 v177,v98,v99\nM v[64:67],v[192:195],a[8:11],0\nds_read_b64_tr_b16 v[220:221],v242 offset:1536\nds_read_b64_tr_b16 v[222:223],v242 offset:1792\n" \
"M v[68:71],v[192:195],a[12:15],0\nv_cvt_pk_bf16_f32 v178,v100,v101\nv_cvt_pk_bf16_f32 v179,v102,v103\nM v[8:11],v[196:199],a[0:3],0\nv_cvt_pk_bf16_f32 v148,v40,v41\nv_cvt_pk_bf16_f32 v149,v42,v43\nM v[12:15],v[196:199],a[4:7],0\nv_cvt_pk_bf16_f32 v150,v44,v45\n" \
"v_cvt_pk_bf16_f32 v151,v46,v47\nM v[72:75],v[196:199],a[8:11],0\nv_cvt_pk_bf16_f32 v180,v104,v105\nv_cvt_pk_bf16_f32 v181,v106,v107\nM v[76:79],v[196:199],a[12:15],0\nds_read_b64_tr_b16 v[192:193],v241 offset:8192\nds_read_b64_tr_b16 v[194:195],v241 offset:8448\nM v[16:19],v[200:203],a[0:3],0\n" \
"v_cvt_pk_bf16_f32 v182,v108,v109\nv_cvt_pk_bf16_f32 v183,v110,v111\nM v[20:23],v[200:203],a[4:7],0\nv_cvt_pk_bf16_f32 v152,v48,v49\nv_cvt_pk_bf16_f32 v153,v50,v51\nM v[80:83],v[200:203],a[8:11],0\nv_cvt_pk_bf16_f32 v154,v52,v53\nv_cvt_pk_bf16_f32 v155,v54,v55\n" \
"M v[84:87],v[200:203],a[12:15],0\nv_cvt_pk_bf16_f32 v184,v112,v113\nv_cvt_pk_bf16_f32 v185,v114,v115\nM v[24:27],v[204:207],a[0:3],0\nds_read_b64_tr_b16 v[196:197],v242 offset:8192\nds_read_b64_tr_b16 v[198:199],v242 offset:8448\nM v[28:31],v[204:207],a[4:7],0\nv_cvt_pk_bf16_f32 v186,v116,v117\n" \
"v_cvt_pk_bf16_f32 v187,v118,v119\nM v[88:91],v[204:207],a[8:11],0\nv_cvt_pk_bf16_f32 v156,v56,v57\nv_cvt_pk_bf16_f32 v157,v58,v59\nM v[92:95],v[204:207],a[12:15],0\nv_cvt_pk_bf16_f32 v158,v60,v61\nv_cvt_pk_bf16_f32 v159,v62,v63\nM v[32:35],v[208:211],a[0:3],0\n" \
"v_cvt_pk_bf16_f32 v188,v120,v121\nv_cvt_pk_bf16_f32 v189,v122,v123\nM v[36:39],v[208:211],a[4:7],0\nds_read_b64_tr_b16 v[200:201],v241 offset:8704\nds_read_b64_tr_b16 v[202:203],v241 offset:8960\nM v[96:99],v[208:211],a[8:11],0\nv_cvt_pk_bf16_f32 v190,v124,v125\nv_cvt_pk_bf16_f32 v191,v126,v127\n" \
"M v[100:103],v[208:211],a[12:15],0\nds_read_b64_tr_b16 v[204:205],v242 offset:8704\ns_waitcnt lgkmcnt(5)\nM v[40:43],v[212:215],a[0:3],0\nbuffer_store_dwordx4 v[128:131],v240,s[52:55],s74 offen offset:0 sc1\nM v[44:47],v[212:215],a[4:7],0\nds_read_b64_tr_b16 v[206:207],v242 offset:8960\nds_read_b64_tr_b16 v[208:209],v241 offset:9216\n" \
"M v[104:107],v[212:215],a[8:11],0\nds_read_b64_tr_b16 v[210:211],v241 offset:9472\ns_add_u32 m0,s64,147456\nM v[108:111],v[212:215],a[12:15],0\nds_read_b64_tr_b16 v[212:213],v242 offset:9216\nds_read_b64_tr_b16 v[214:215],v242 offset:9472\nM v[48:51],v[216:219],a[0:3],0\nbuffer_load_dwordx4 a[84:87],v240,s[52:55],s76 offen offset:3072 sc1\n" \
"M v[52:55],v[216:219],a[4:7],0\ns_add_u32 s69,s68,1572864\ns_add_u32 s76,s76,4096\nM v[112:115],v[216:219],a[8:11],0\nM v[116:119],v[216:219],a[12:15],0\nds_read_b64_tr_b16 v[216:217],v241 offset:9728\nds_read_b64_tr_b16 v[218:219],v241 offset:9984\nM v[56:59],v[220:223],a[0:3],0\n" \
"buffer_load_dwordx4 a[88:91],v240,s[52:55],s77 offen offset:2048 sc1\nM v[60:63],v[220:223],a[4:7],0\nM v[120:123],v[220:223],a[8:11],0\nM v[124:127],v[220:223],a[12:15],0\nds_read_b64_tr_b16 v[220:221],v242 offset:9728\nds_read_b64_tr_b16 v[222:223],v242 offset:9984\nM v[0:3],v[192:195],a[16:19],v[0:3]\nbuffer_load_dwordx4 a[92:95],v240,s[52:55],s77 offen offset:3072 sc1\n" \
"M v[4:7],v[192:195],a[20:23],v[4:7]\ns_add_u32 s77,s77,4096\nM v[64:67],v[192:195],a[24:27],v[64:67]\ns_waitcnt vmcnt(25)\nM v[68:71],v[192:195],a[28:31],v[68:71]\ns_waitcnt lgkmcnt(6)\nM v[8:11],v[196:199],a[16:19],v[8:11]\nbuffer_load_dwordx4 a[96:99],v240,s[52:55],s76 offen offset:0 sc1\n" \
"M v[12:15],v[196:199],a[20:23],v[12:15]\nM v[72:75],v[196:199],a[24:27],v[72:75]\nM v[76:79],v[196:199],a[28:31],v[76:79]\nM v[16:19],v[200:203],a[16:19],v[16:19]\nbuffer_load_dwordx4 a[100:103],v240,s[52:55],s76 offen offset:1024 sc1\nM v[20:23],v[200:203],a[20:23],v[20:23]\nM v[80:83],v[200:203],a[24:27],v[80:83]\nM v[84:87],v[200:203],a[28:31],v[84:87]\n" \
"M v[24:27],v[204:207],a[16:19],v[24:27]\nbuffer_load_dwordx4 a[104:107],v240,s[52:55],s77 offen offset:0 sc1\nM v[28:31],v[204:207],a[20:23],v[28:31]\nM v[88:91],v[204:207],a[24:27],v[88:91]\ns_waitcnt vmcnt(22) lgkmcnt(0)\ns_barrier\nM v[92:95],v[204:207],a[28:31],v[92:95]\nds_read_b64_tr_b16 v[192:193],v241 offset:16384\n" \
"ds_read_b64_tr_b16 v[194:195],v241 offset:16640\nM v[32:35],v[208:211],a[16:19],v[32:35]\nds_read_b64_tr_b16 v[196:197],v242 offset:16384\nds_read_b64_tr_b16 v[198:199],v242 offset:16640\nM v[36:39],v[208:211],a[20:23],v[36:39]\nds_read_b64_tr_b16 v[200:201],v241 offset:16896\nds_read_b64_tr_b16 v[202:203],v241 offset:17152\nM v[96:99],v[208:211],a[24:27],v[96:99]\n" \
"buffer_store_dwordx4 v[132:135],v240,s[52:55],s74 offen offset:1024 sc1\nM v[100:103],v[208:211],a[28:31],v[100:103]\nds_read_b64_tr_b16 v[204:205],v242 offset:16896\nds_read_b64_tr_b16 v[206:207],v242 offset:17152\nM v[40:43],v[212:215],a[16:19],v[40:43]\nds_read_b64_tr_b16 v[208:209],v241 offset:17408\nds_read_b64_tr_b16 v[210:211],v241 offset:17664\nM v[44:47],v[212:215],a[20:23],v[44:47]\n" \
"M v[104:107],v[212:215],a[24:27],v[104:107]\nbuffer_load_dwordx4 a[108:111],v240,s[52:55],s77 offen offset:1024 sc1\nM v[108:111],v[212:215],a[28:31],v[108:111]\nds_read_b64_tr_b16 v[212:213],v242 offset:17408\nds_read_b64_tr_b16 v[214:215],v242 offset:17664\nM v[48:51],v[216:219],a[16:19],v[48:51]\nM v[52:55],v[216:219],a[20:23],v[52:55]\nM v[112:115],v[216:219],a[24:27],v[112:115]\n" \
"buffer_load_dwordx4 a[112:115],v240,s[52:55],s76 offen offset:2048 sc1\nM v[116:119],v[216:219],a[28:31],v[116:119]\nds_read_b64_tr_b16 v[216:217],v241 offset:17920\nds_read_b64_tr_b16 v[218:219],v241 offset:18176\nM v[56:59],v[220:223],a[16:19],v[56:59]\nM v[60:63],v[220:223],a[20:23],v[60:63]\nM v[120:123],v[220:223],a[24:27],v[120:123]\nbuffer_load_dwordx4 v243,s[40:43],s69 offen offset:0 lds\n" \
"M v[124:127],v[220:223],a[28:31],v[124:127]\nds_read_b64_tr_b16 v[220:221],v242 offset:17920\ns_waitcnt lgkmcnt(9)\nM v[0:3],v[192:195],a[32:35],v[0:3]\nds_read_b64_tr_b16 v[222:223],v242 offset:18176\nM v[4:7],v[192:195],a[36:39],v[4:7]\nM v[64:67],v[192:195],a[40:43],v[64:67]\ns_waitcnt vmcnt(21)\n" \
"M v[68:71],v[192:195],a[44:47],v[68:71]\nds_read_b64_tr_b16 v[192:193],v241 offset:24576\nds_read_b64_tr_b16 v[194:195],v241 offset:24832\nM v[8:11],v[196:199],a[32:35],v[8:11]\nbuffer_load_dwordx4 v244,s[40:43],s69 offen offset:1024 lds\nM v[12:15],v[196:199],a[36:39],v[12:15]\nM v[72:75],v[196:199],a[40:43],v[72:75]\nM v[76:79],v[196:199],a[44:47],v[76:79]\n" \
"ds_read_b64_tr_b16 v[196:197],v242 offset:24576\nds_read_b64_tr_b16 v[198:199],v242 offset:24832\nM v[16:19],v[200:203],a[32:35],v[16:19]\nbuffer_load_dwordx4 v245,s[40:43],s69 offen offset:2048 lds\nM v[20:23],v[200:203],a[36:39],v[20:23]\nM v[80:83],v[200:203],a[40:43],v[80:83]\nM v[84:87],v[200:203],a[44:47],v[84:87]\nds_read_b64_tr_b16 v[200:201],v241 offset:25088\n" \
"s_waitcnt lgkmcnt(7)\nM v[24:27],v[204:207],a[32:35],v[24:27]\nbuffer_load_dwordx4 v246,s[40:43],s69 offen offset:3072 lds\nM v[28:31],v[204:207],a[36:39],v[28:31]\nds_read_b64_tr_b16 v[202:203],v241 offset:25344\ns_add_u32 m0,s64,98304\nM v[88:91],v[204:207],a[40:43],v[88:91]\ns_add_u32 s69,s68,2097152\n" \
"M v[92:95],v[204:207],a[44:47],v[92:95]\nds_read_b64_tr_b16 v[204:205],v242 offset:25088\nds_read_b64_tr_b16 v[206:207],v242 offset:25344\nM v[32:35],v[208:211],a[32:35],v[32:35]\nbuffer_store_dwordx4 v[136:139],v240,s[52:55],s74 offen offset:2048 sc1\nM v[36:39],v[208:211],a[36:39],v[36:39]\nM v[96:99],v[208:211],a[40:43],v[96:99]\nM v[100:103],v[208:211],a[44:47],v[100:103]\n" \
"ds_read_b64_tr_b16 v[208:209],v241 offset:25600\nds_read_b64_tr_b16 v[210:211],v241 offset:25856\nM v[40:43],v[212:215],a[32:35],v[40:43]\nbuffer_load_dwordx4 a[116:119],v240,s[52:55],s76 offen offset:3072 sc1\nM v[44:47],v[212:215],a[36:39],v[44:47]\ns_add_u32 s76,s76,4096\nM v[104:107],v[212:215],a[40:43],v[104:107]\nM v[108:111],v[212:215],a[44:47],v[108:111]\n" \
"ds_read_b64_tr_b16 v[212:213],v242 offset:25600\nds_read_b64_tr_b16 v[214:215],v242 offset:25856\nM v[48:51],v[216:219],a[32:35],v[48:51]\nbuffer_load_dwordx4 a[120:123],v240,s[52:55],s77 offen offset:2048 sc1\nM v[52:55],v[216:219],a[36:39],v[52:55]\nM v[112:115],v[216:219],a[40:43],v[112:115]\nM v[116:119],v[216:219],a[44:47],v[116:119]\nds_read_b64_tr_b16 v[216:217],v241 offset:26112\n" \
"s_waitcnt lgkmcnt(7)\nM v[56:59],v[220:223],a[32:35],v[56:59]\nbuffer_load_dwordx4 a[124:127],v240,s[52:55],s77 offen offset:3072 sc1\nM v[60:63],v[220:223],a[36:39],v[60:63]\nds_read_b64_tr_b16 v[218:219],v241 offset:26368\ns_add_u32 s77,s77,4096\nM v[120:123],v[220:223],a[40:43],v[120:123]\nM v[124:127],v[220:223],a[44:47],v[124:127]\n" \
"ds_read_b64_tr_b16 v[220:221],v242 offset:26112\nds_read_b64_tr_b16 v[222:223],v242 offset:26368\nM v[0:3],v[192:195],a[48:51],v[0:3]\nbuffer_load_dwordx4 a[128:131],v240,s[52:55],s76 offen offset:0 sc1\nM v[4:7],v[192:195],a[52:55],v[4:7]\nM v[64:67],v[192:195],a[56:59],v[64:67]\ns_waitcnt vmcnt(27)\nM v[68:71],v[192:195],a[60:63],v[68:71]\n" \
"M v[8:11],v[196:199],a[48:51],v[8:11]\nbuffer_load_dwordx4 a[132:135],v240,s[52:55],s76 offen offset:1024 sc1\nM v[12:15],v[196:199],a[52:55],v[12:15]\nM v[72:75],v[196:199],a[56:59],v[72:75]\nM v[76:79],v[196:199],a[60:63],v[76:79]\nM v[16:19],v[200:203],a[48:51],v[16:19]\nbuffer_load_dwordx4 a[136:139],v240,s[52:55],s77 offen offset:0 sc1\nM v[20:23],v[200:203],a[52:55],v[20:23]\n" \
"M v[80:83],v[200:203],a[56:59],v[80:83]\nM v[84:87],v[200:203],a[60:63],v[84:87]\ns_waitcnt lgkmcnt(3)\nM v[24:27],v[204:207],a[48:51],v[24:27]\nbuffer_load_dwordx4 a[140:143],v240,s[52:55],s77 offen offset:1024 sc1\nM v[28:31],v[204:207],a[52:55],v[28:31]\nM v[88:91],v[204:207],a[56:59],v[88:91]\ns_waitcnt vmcnt(22) lgkmcnt(0)\n" \
"s_barrier\nM v[92:95],v[204:207],a[60:63],v[92:95]\nds_read_b64_tr_b16 v[192:193],v241 offset:32768\nds_read_b64_tr_b16 v[194:195],v241 offset:33024\nM v[32:35],v[208:211],a[48:51],v[32:35]\nbuffer_store_dwordx4 v[140:143],v240,s[52:55],s74 offen offset:3072 sc1\nM v[36:39],v[208:211],a[52:55],v[36:39]\nds_read_b64_tr_b16 v[196:197],v242 offset:32768\n" \
"ds_read_b64_tr_b16 v[198:199],v242 offset:33024\nM v[96:99],v[208:211],a[56:59],v[96:99]\nds_read_b64_tr_b16 v[200:201],v241 offset:33280\nds_read_b64_tr_b16 v[202:203],v241 offset:33536\nM v[100:103],v[208:211],a[60:63],v[100:103]\nds_read_b64_tr_b16 v[204:205],v242 offset:33280\nds_read_b64_tr_b16 v[206:207],v242 offset:33536\nM v[40:43],v[212:215],a[48:51],v[40:43]\n" \
"ds_read_b64_tr_b16 v[208:209],v241 offset:33792\nds_read_b64_tr_b16 v[210:211],v241 offset:34048\nM v[44:47],v[212:215],a[52:55],v[44:47]\nbuffer_load_dwordx4 a[144:147],v240,s[52:55],s76 offen offset:2048 sc1\nM v[104:107],v[212:215],a[56:59],v[104:107]\ns_add_u32 s74,s74,4096\nM v[108:111],v[212:215],a[60:63],v[108:111]\nds_read_b64_tr_b16 v[212:213],v242 offset:33792\n" \
"ds_read_b64_tr_b16 v[214:215],v242 offset:34048\nM v[48:51],v[216:219],a[48:51],v[48:51]\nM v[52:55],v[216:219],a[52:55],v[52:55]\nbuffer_load_dwordx4 v243,s[40:43],s69 offen offset:0 lds\nM v[112:115],v[216:219],a[56:59],v[112:115]\nM v[116:119],v[216:219],a[60:63],v[116:119]\nds_read_b64_tr_b16 v[216:217],v241 offset:34304\nds_read_b64_tr_b16 v[218:219],v241 offset:34560\n" \
"M v[56:59],v[220:223],a[48:51],v[56:59]\nM v[60:63],v[220:223],a[52:55],v[60:63]\nbuffer_load_dwordx4 v244,s[40:43],s69 offen offset:1024 lds\nM v[120:123],v[220:223],a[56:59],v[120:123]\nM v[124:127],v[220:223],a[60:63],v[124:127]\nds_read_b64_tr_b16 v[220:221],v242 offset:34304\ns_waitcnt lgkmcnt(9)\nM v[0:3],v[192:195],a[64:67],v[0:3]\n" \
"ds_read_b64_tr_b16 v[222:223],v242 offset:34560\nM v[4:7],v[192:195],a[68:71],v[4:7]\nbuffer_load_dwordx4 v245,s[40:43],s69 offen offset:2048 lds\nM v[64:67],v[192:195],a[72:75],v[64:67]\nM v[68:71],v[192:195],a[76:79],v[68:71]\nds_read_b64_tr_b16 v[192:193],v241 offset:40960\nds_read_b64_tr_b16 v[194:195],v241 offset:41216\nM v[8:11],v[196:199],a[64:67],v[8:11]\n" \
"M v[12:15],v[196:199],a[68:71],v[12:15]\nbuffer_load_dwordx4 v246,s[40:43],s69 offen offset:3072 lds\nM v[72:75],v[196:199],a[72:75],v[72:75]\ns_add_u32 m0,s64,114688\ns_add_u32 s69,s68,2621440\nM v[76:79],v[196:199],a[76:79],v[76:79]\nds_read_b64_tr_b16 v[196:197],v242 offset:40960\nds_read_b64_tr_b16 v[198:199],v242 offset:41216\n" \
"M v[16:19],v[200:203],a[64:67],v[16:19]\nM v[20:23],v[200:203],a[68:71],v[20:23]\nbuffer_load_dwordx4 a[148:151],v240,s[52:55],s76 offen offset:3072 sc1\nM v[80:83],v[200:203],a[72:75],v[80:83]\ns_add_u32 s76,s76,4096\nM v[84:87],v[200:203],a[76:79],v[84:87]\nds_read_b64_tr_b16 v[200:201],v241 offset:41472\ns_waitcnt lgkmcnt(6)\n" \
"M v[24:27],v[204:207],a[64:67],v[24:27]\nds_read_b64_tr_b16 v[202:203],v241 offset:41728\nM v[28:31],v[204:207],a[68:71],v[28:31]\nbuffer_store_dwordx4 v[144:147],v240,s[52:55],s74 offen offset:0 sc1\nM v[88:91],v[204:207],a[72:75],v[88:91]\nM v[92:95],v[204:207],a[76:79],v[92:95]\nds_read_b64_tr_b16 v[204:205],v242 offset:41472\nds_read_b64_tr_b16 v[206:207],v242 offset:41728\n" \
"M v[32:35],v[208:211],a[64:67],v[32:35]\nM v[36:39],v[208:211],a[68:71],v[36:39]\nbuffer_load_dwordx4 a[152:155],v240,s[52:55],s77 offen offset:2048 sc1\nM v[96:99],v[208:211],a[72:75],v[96:99]\nM v[100:103],v[208:211],a[76:79],v[100:103]\nds_read_b64_tr_b16 v[208:209],v241 offset:41984\nds_read_b64_tr_b16 v[210:211],v241 offset:42240\nM v[40:43],v[212:215],a[64:67],v[40:43]\n" \
"M v[44:47],v[212:215],a[68:71],v[44:47]\nbuffer_load_dwordx4 a[156:159],v240,s[52:55],s77 offen offset:3072 sc1\nM v[104:107],v[212:215],a[72:75],v[104:107]\ns_add_u32 s77,s77,4096\nM v[108:111],v[212:215],a[76:79],v[108:111]\nds_read_b64_tr_b16 v[212:213],v242 offset:41984\nds_read_b64_tr_b16 v[214:215],v242 offset:42240\nM v[48:51],v[216:219],a[64:67],v[48:51]\n" \
"M v[52:55],v[216:219],a[68:71],v[52:55]\nbuffer_load_dwordx4 a[160:163],v240,s[52:55],s76 offen offset:0 sc1\nM v[112:115],v[216:219],a[72:75],v[112:115]\nM v[116:119],v[216:219],a[76:79],v[116:119]\nds_read_b64_tr_b16 v[216:217],v241 offset:42496\ns_waitcnt lgkmcnt(7)\nM v[56:59],v[220:223],a[64:67],v[56:59]\nds_read_b64_tr_b16 v[218:219],v241 offset:42752\n" \
"M v[60:63],v[220:223],a[68:71],v[60:63]\nbuffer_load_dwordx4 a[164:167],v240,s[52:55],s76 offen offset:1024 sc1\nM v[120:123],v[220:223],a[72:75],v[120:123]\nM v[124:127],v[220:223],a[76:79],v[124:127]\nds_read_b64_tr_b16 v[220:221],v242 offset:42496\nds_read_b64_tr_b16 v[222:223],v242 offset:42752\nM v[0:3],v[192:195],a[80:83],v[0:3]\ns_waitcnt vmcnt(27)\n" \
"M v[4:7],v[192:195],a[84:87],v[4:7]\nbuffer_load_dwordx4 a[168:171],v240,s[52:55],s77 offen offset:0 sc1\nM v[64:67],v[192:195],a[88:91],v[64:67]\nM v[68:71],v[192:195],a[92:95],v[68:71]\nM v[8:11],v[196:199],a[80:83],v[8:11]\nM v[12:15],v[196:199],a[84:87],v[12:15]\nbuffer_load_dwordx4 a[172:175],v240,s[52:55],s77 offen offset:1024 sc1\nM v[72:75],v[196:199],a[88:91],v[72:75]\n" \
"M v[76:79],v[196:199],a[92:95],v[76:79]\nM v[16:19],v[200:203],a[80:83],v[16:19]\nM v[20:23],v[200:203],a[84:87],v[20:23]\nbuffer_load_dwordx4 a[176:179],v240,s[52:55],s76 offen offset:2048 sc1\nM v[80:83],v[200:203],a[88:91],v[80:83]\nM v[84:87],v[200:203],a[92:95],v[84:87]\ns_waitcnt lgkmcnt(2)\nM v[24:27],v[204:207],a[80:83],v[24:27]\n" \
"M v[28:31],v[204:207],a[84:87],v[28:31]\nbuffer_store_dwordx4 v[148:151],v240,s[52:55],s74 offen offset:1024 sc1\nM v[88:91],v[204:207],a[88:91],v[88:91]\ns_waitcnt vmcnt(24) lgkmcnt(0)\ns_barrier\nM v[92:95],v[204:207],a[92:95],v[92:95]\nds_read_b64_tr_b16 v[192:193],v241 offset:49152\nds_read_b64_tr_b16 v[194:195],v241 offset:49408\n" \
"M v[32:35],v[208:211],a[80:83],v[32:35]\nds_read_b64_tr_b16 v[196:197],v242 offset:49152\nds_read_b64_tr_b16 v[198:199],v242 offset:49408\nM v[36:39],v[208:211],a[84:87],v[36:39]\nds_read_b64_tr_b16 v[200:201],v241 offset:49664\nds_read_b64_tr_b16 v[202:203],v241 offset:49920\nM v[96:99],v[208:211],a[88:91],v[96:99]\nds_read_b64_tr_b16 v[204:205],v242 offset:49664\n" \
"ds_read_b64_tr_b16 v[206:207],v242 offset:49920\nM v[100:103],v[208:211],a[92:95],v[100:103]\nds_read_b64_tr_b16 v[208:209],v241 offset:50176\nds_read_b64_tr_b16 v[210:211],v241 offset:50432\nM v[40:43],v[212:215],a[80:83],v[40:43]\nbuffer_load_dwordx4 v243,s[40:43],s69 offen offset:0 lds\nM v[44:47],v[212:215],a[84:87],v[44:47]\nM v[104:107],v[212:215],a[88:91],v[104:107]\n" \
"M v[108:111],v[212:215],a[92:95],v[108:111]\nds_read_b64_tr_b16 v[212:213],v242 offset:50176\nds_read_b64_tr_b16 v[214:215],v242 offset:50432\nM v[48:51],v[216:219],a[80:83],v[48:51]\nbuffer_load_dwordx4 v244,s[40:43],s69 offen offset:1024 lds\nM v[52:55],v[216:219],a[84:87],v[52:55]\nM v[112:115],v[216:219],a[88:91],v[112:115]\nM v[116:119],v[216:219],a[92:95],v[116:119]\n" \
"ds_read_b64_tr_b16 v[216:217],v241 offset:50688\nds_read_b64_tr_b16 v[218:219],v241 offset:50944\nM v[56:59],v[220:223],a[80:83],v[56:59]\nbuffer_load_dwordx4 v245,s[40:43],s69 offen offset:2048 lds\nM v[60:63],v[220:223],a[84:87],v[60:63]\nM v[120:123],v[220:223],a[88:91],v[120:123]\nM v[124:127],v[220:223],a[92:95],v[124:127]\nds_read_b64_tr_b16 v[220:221],v242 offset:50688\n" \
"s_waitcnt lgkmcnt(8)\nM v[0:3],v[192:195],a[96:99],v[0:3]\nbuffer_load_dwordx4 v246,s[40:43],s69 offen offset:3072 lds\nM v[4:7],v[192:195],a[100:103],v[4:7]\nds_read_b64_tr_b16 v[222:223],v242 offset:50944\ns_add_u32 m0,s64,131072\nM v[64:67],v[192:195],a[104:107],v[64:67]\ns_add_u32 s69,s68,3145728\n" \
"M v[68:71],v[192:195],a[108:111],v[68:71]\nds_read_b64_tr_b16 v[192:193],v241 offset:57344\nds_read_b64_tr_b16 v[194:195],v241 offset:57600\nM v[8:11],v[196:199],a[96:99],v[8:11]\nbuffer_load_dwordx4 a[180:183],v240,s[52:55],s76 offen offset:3072 sc1\nM v[12:15],v[196:199],a[100:103],v[12:15]\ns_add_u32 s76,s76,4096\nM v[72:75],v[196:199],a[104:107],v[72:75]\n" \
"M v[76:79],v[196:199],a[108:111],v[76:79]\nds_read_b64_tr_b16 v[196:197],v242 offset:57344\nds_read_b64_tr_b16 v[198:199],v242 offset:57600\nM v[16:19],v[200:203],a[96:99],v[16:19]\nbuffer_store_dwordx4 v[152:155],v240,s[52:55],s74 offen offset:2048 sc1\nM v[20:23],v[200:203],a[100:103],v[20:23]\nM v[80:83],v[200:203],a[104:107],v[80:83]\nM v[84:87],v[200:203],a[108:111],v[84:87]\n" \
"ds_read_b64_tr_b16 v[200:201],v241 offset:57856\ns_waitcnt lgkmcnt(6)\nM v[24:27],v[204:207],a[96:99],v[24:27]\nbuffer_load_dwordx4 a[184:187],v240,s[52:55],s77 offen offset:2048 sc1\nM v[28:31],v[204:207],a[100:103],v[28:31]\nds_read_b64_tr_b16 v[202:203],v241 offset:58112\nM v[88:91],v[204:207],a[104:107],v[88:91]\nM v[92:95],v[204:207],a[108:111],v[92:95]\n" \
"ds_read_b64_tr_b16 v[204:205],v242 offset:57856\nds_read_b64_tr_b16 v[206:207],v242 offset:58112\nM v[32:35],v[208:211],a[96:99],v[32:35]\nbuffer_load_dwordx4 a[188:191],v240,s[52:55],s77 offen offset:3072 sc1\nM v[36:39],v[208:211],a[100:103],v[36:39]\ns_add_u32 s77,s77,4096\nM v[96:99],v[208:211],a[104:107],v[96:99]\nM v[100:103],v[208:211],a[108:111],v[100:103]\n" \
"ds_read_b64_tr_b16 v[208:209],v241 offset:58368\nds_read_b64_tr_b16 v[210:211],v241 offset:58624\nM v[40:43],v[212:215],a[96:99],v[40:43]\nbuffer_load_dwordx4 a[192:195],v240,s[52:55],s76 offen offset:0 sc1\nM v[44:47],v[212:215],a[100:103],v[44:47]\nM v[104:107],v[212:215],a[104:107],v[104:107]\nM v[108:111],v[212:215],a[108:111],v[108:111]\nds_read_b64_tr_b16 v[212:213],v242 offset:58368\n" \
"ds_read_b64_tr_b16 v[214:215],v242 offset:58624\nM v[48:51],v[216:219],a[96:99],v[48:51]\nbuffer_load_dwordx4 a[196:199],v240,s[52:55],s76 offen offset:1024 sc1\nM v[52:55],v[216:219],a[100:103],v[52:55]\nM v[112:115],v[216:219],a[104:107],v[112:115]\nM v[116:119],v[216:219],a[108:111],v[116:119]\nds_read_b64_tr_b16 v[216:217],v241 offset:58880\ns_waitcnt lgkmcnt(7)\n" \
"M v[56:59],v[220:223],a[96:99],v[56:59]\nbuffer_load_dwordx4 a[200:203],v240,s[52:55],s77 offen offset:0 sc1\nM v[60:63],v[220:223],a[100:103],v[60:63]\nds_read_b64_tr_b16 v[218:219],v241 offset:59136\nM v[120:123],v[220:223],a[104:107],v[120:123]\nM v[124:127],v[220:223],a[108:111],v[124:127]\nds_read_b64_tr_b16 v[220:221],v242 offset:58880\nds_read_b64_tr_b16 v[222:223],v242 offset:59136\n" \
"M v[0:3],v[192:195],a[112:115],v[0:3]\ns_waitcnt vmcnt(27)\nM v[4:7],v[192:195],a[116:119],v[4:7]\nbuffer_load_dwordx4 a[204:207],v240,s[52:55],s77 offen offset:1024 sc1\nM v[64:67],v[192:195],a[120:123],v[64:67]\nM v[68:71],v[192:195],a[124:127],v[68:71]\nM v[8:11],v[196:199],a[112:115],v[8:11]\nM v[12:15],v[196:199],a[116:119],v[12:15]\n" \
"buffer_load_dwordx4 a[208:211],v240,s[52:55],s76 offen offset:2048 sc1\nM v[72:75],v[196:199],a[120:123],v[72:75]\nM v[76:79],v[196:199],a[124:127],v[76:79]\nM v[16:19],v[200:203],a[112:115],v[16:19]\nM v[20:23],v[200:203],a[116:119],v[20:23]\nbuffer_store_dwordx4 v[156:159],v240,s[52:55],s74 offen offset:3072 sc1\nM v[80:83],v[200:203],a[120:123],v[80:83]\ns_add_u32 s74,s74,4096\n" \
"M v[84:87],v[200:203],a[124:127],v[84:87]\ns_waitcnt lgkmcnt(3)\nM v[24:27],v[204:207],a[112:115],v[24:27]\nM v[28:31],v[204:207],a[116:119],v[28:31]\nbuffer_load_dwordx4 v243,s[40:43],s69 offen offset:0 lds\nM v[88:91],v[204:207],a[120:123],v[88:91]\ns_waitcnt vmcnt(24) lgkmcnt(0)\ns_barrier\n" \
"M v[92:95],v[204:207],a[124:127],v[92:95]\nds_read_b64_tr_b16 v[192:193],v241 offset:0\nds_read_b64_tr_b16 v[194:195],v241 offset:256\nM v[32:35],v[208:211],a[112:115],v[32:35]\nds_read_b64_tr_b16 v[196:197],v242 offset:0\nds_read_b64_tr_b16 v[198:199],v242 offset:256\nM v[36:39],v[208:211],a[116:119],v[36:39]\nds_read_b64_tr_b16 v[200:201],v241 offset:512\n" \
"ds_read_b64_tr_b16 v[202:203],v241 offset:768\nM v[96:99],v[208:211],a[120:123],v[96:99]\nds_read_b64_tr_b16 v[204:205],v242 offset:512\nds_read_b64_tr_b16 v[206:207],v242 offset:768\nM v[100:103],v[208:211],a[124:127],v[100:103]\nds_read_b64_tr_b16 v[208:209],v241 offset:1024\nds_read_b64_tr_b16 v[210:211],v241 offset:1280\nM v[40:43],v[212:215],a[112:115],v[40:43]\n" \
"buffer_load_dwordx4 v244,s[40:43],s69 offen offset:1024 lds\nM v[44:47],v[212:215],a[116:119],v[44:47]\nM v[104:107],v[212:215],a[120:123],v[104:107]\nM v[108:111],v[212:215],a[124:127],v[108:111]\nds_read_b64_tr_b16 v[212:213],v242 offset:1024\nds_read_b64_tr_b16 v[214:215],v242 offset:1280\nM v[48:51],v[216:219],a[112:115],v[48:51]\nbuffer_load_dwordx4 v245,s[40:43],s69 offen offset:2048 lds\n" \
"M v[52:55],v[216:219],a[116:119],v[52:55]\nM v[112:115],v[216:219],a[120:123],v[112:115]\nM v[116:119],v[216:219],a[124:127],v[116:119]\nds_read_b64_tr_b16 v[216:217],v241 offset:1536\nds_read_b64_tr_b16 v[218:219],v241 offset:1792\nM v[56:59],v[220:223],a[112:115],v[56:59]\nbuffer_load_dwordx4 v246,s[40:43],s69 offen offset:3072 lds\nM v[60:63],v[220:223],a[116:119],v[60:63]\n" \
"s_add_u32 m0,s64,147456\ns_add_u32 s69,s68,3670016\nM v[120:123],v[220:223],a[120:123],v[120:123]\ns_lshl_b32 s68,s60,19\ns_lshl2_add_u32 s68,s64,s68\nM v[124:127],v[220:223],a[124:127],v[124:127]\nds_read_b64_tr_b16 v[220:221],v242 offset:1536\ns_waitcnt lgkmcnt(6)\n" \
"M v[0:3],v[192:195],a[128:131],v[0:3]\nbuffer_load_dwordx4 a[212:215],v240,s[52:55],s76 offen offset:3072 sc1\nM v[4:7],v[192:195],a[132:135],v[4:7]\nds_read_b64_tr_b16 v[222:223],v242 offset:1792\ns_add_u32 s76,s76,4096\nM v[64:67],v[192:195],a[136:139],v[64:67]\nM v[68:71],v[192:195],a[140:143],v[68:71]\nds_read_b64_tr_b16 v[192:193],v241 offset:8192\n" \
"ds_read_b64_tr_b16 v[194:195],v241 offset:8448\nM v[8:11],v[196:199],a[128:131],v[8:11]\nbuffer_store_dwordx4 v[160:163],v240,s[52:55],s74 offen offset:0 sc1\nM v[12:15],v[196:199],a[132:135],v[12:15]\nM v[72:75],v[196:199],a[136:139],v[72:75]\nM v[76:79],v[196:199],a[140:143],v[76:79]\nds_read_b64_tr_b16 v[196:197],v242 offset:8192\nds_read_b64_tr_b16 v[198:199],v242 offset:8448\n" \
"M v[16:19],v[200:203],a[128:131],v[16:19]\nbuffer_load_dwordx4 a[216:219],v240,s[52:55],s77 offen offset:2048 sc1\nM v[20:23],v[200:203],a[132:135],v[20:23]\nM v[80:83],v[200:203],a[136:139],v[80:83]\nM v[84:87],v[200:203],a[140:143],v[84:87]\nds_read_b64_tr_b16 v[200:201],v241 offset:8704\nds_read_b64_tr_b16 v[202:203],v241 offset:8960\nM v[24:27],v[204:207],a[128:131],v[24:27]\n" \
"buffer_load_dwordx4 a[220:223],v240,s[52:55],s77 offen offset:3072 sc1\nM v[28:31],v[204:207],a[132:135],v[28:31]\ns_add_u32 s77,s77,4096\nM v[88:91],v[204:207],a[136:139],v[88:91]\nM v[92:95],v[204:207],a[140:143],v[92:95]\nds_read_b64_tr_b16 v[204:205],v242 offset:8704\ns_waitcnt lgkmcnt(7)\nM v[32:35],v[208:211],a[128:131],v[32:35]\n" \
"buffer_load_dwordx4 a[224:227],v240,s[52:55],s76 offen offset:0 sc1\nM v[36:39],v[208:211],a[132:135],v[36:39]\nds_read_b64_tr_b16 v[206:207],v242 offset:8960\nM v[96:99],v[208:211],a[136:139],v[96:99]\nM v[100:103],v[208:211],a[140:143],v[100:103]\nds_read_b64_tr_b16 v[208:209],v241 offset:9216\nds_read_b64_tr_b16 v[210:211],v241 offset:9472\nM v[40:43],v[212:215],a[128:131],v[40:43]\n" \
"buffer_load_dword v248,v228,s[52:55],s80 offen sc1\nM v[44:47],v[212:215],a[132:135],v[44:47]\nbuffer_load_dwordx4 a[228:231],v240,s[52:55],s76 offen offset:1024 sc1\nM v[104:107],v[212:215],a[136:139],v[104:107]\nds_read_b128 a[0:3],v240 offset:0\nM v[108:111],v[212:215],a[140:143],v[108:111]\nds_read_b64_tr_b16 v[212:213],v242 offset:9216\nds_read_b64_tr_b16 v[214:215],v242 offset:9472\n" \
"M v[48:51],v[216:219],a[128:131],v[48:51]\nM v[52:55],v[216:219],a[132:135],v[52:55]\nbuffer_load_dwordx4 a[232:235],v240,s[52:55],s77 offen offset:0 sc1\nM v[112:115],v[216:219],a[136:139],v[112:115]\nds_read_b128 a[4:7],v240 offset:1024\nM v[116:119],v[216:219],a[140:143],v[116:119]\nds_read_b64_tr_b16 v[216:217],v241 offset:9728\nds_read_b64_tr_b16 v[218:219],v241 offset:9984\n" \
"M v[56:59],v[220:223],a[128:131],v[56:59]\nM v[60:63],v[220:223],a[132:135],v[60:63]\nbuffer_load_dwordx4 a[236:239],v240,s[52:55],s77 offen offset:1024 sc1\nM v[120:123],v[220:223],a[136:139],v[120:123]\nM v[124:127],v[220:223],a[140:143],v[124:127]\nds_read_b64_tr_b16 v[220:221],v242 offset:9728\ns_waitcnt lgkmcnt(8)\nM v[0:3],v[192:195],a[144:147],v[0:3]\n" \
"ds_read_b64_tr_b16 v[222:223],v242 offset:9984\nds_read_b128 a[8:11],v240 offset:2048\nM v[4:7],v[192:195],a[148:151],v[4:7]\ns_waitcnt vmcnt(28)\nM v[64:67],v[192:195],a[152:155],v[64:67]\nbuffer_store_dwordx4 v[164:167],v240,s[52:55],s74 offen offset:1024 sc1\nM v[68:71],v[192:195],a[156:159],v[68:71]\nM v[8:11],v[196:199],a[144:147],v[8:11]\n" \
"M v[12:15],v[196:199],a[148:151],v[12:15]\nM v[72:75],v[196:199],a[152:155],v[72:75]\nbuffer_load_dwordx4 a[240:243],v240,s[52:55],s76 offen offset:2048 sc1\nM v[76:79],v[196:199],a[156:159],v[76:79]\nds_read_b128 a[12:15],v240 offset:3072\nM v[16:19],v[200:203],a[144:147],v[16:19]\nM v[20:23],v[200:203],a[148:151],v[20:23]\nM v[80:83],v[200:203],a[152:155],v[80:83]\n" \
"buffer_load_dwordx4 v243,s[40:43],s69 offen offset:0 lds\nM v[84:87],v[200:203],a[156:159],v[84:87]\nv_add_u32 v243,4096,v230\nM v[24:27],v[204:207],a[144:147],v[24:27]\nds_read_b128 a[16:19],v240 offset:4096\nM v[28:31],v[204:207],a[148:151],v[28:31]\nM v[88:91],v[204:207],a[152:155],v[88:91]\ns_waitcnt vmcnt(25) lgkmcnt(3)\n" \
"s_barrier\nM v[92:95],v[204:207],a[156:159],v[92:95]\nds_read_b64_tr_b16 v[192:193],v241 offset:16384\nds_read_b64_tr_b16 v[194:195],v241 offset:16640\nM v[32:35],v[208:211],a[144:147],v[32:35]\nds_read_b64_tr_b16 v[196:197],v242 offset:16384\nds_read_b64_tr_b16 v[198:199],v242 offset:16640\nM v[36:39],v[208:211],a[148:151],v[36:39]\n" \
"ds_read_b64_tr_b16 v[200:201],v241 offset:16896\nds_read_b64_tr_b16 v[202:203],v241 offset:17152\nM v[96:99],v[208:211],a[152:155],v[96:99]\nds_read_b64_tr_b16 v[204:205],v242 offset:16896\nds_read_b64_tr_b16 v[206:207],v242 offset:17152\nM v[100:103],v[208:211],a[156:159],v[100:103]\nds_read_b64_tr_b16 v[208:209],v241 offset:17408\nds_read_b64_tr_b16 v[210:211],v241 offset:17664\n" \
"M v[40:43],v[212:215],a[144:147],v[40:43]\nbuffer_load_dwordx4 v244,s[40:43],s69 offen offset:1024 lds\nM v[44:47],v[212:215],a[148:151],v[44:47]\nv_add_u32 v244,3200,v230\nds_read_b128 a[20:23],v240 offset:5120\nM v[104:107],v[212:215],a[152:155],v[104:107]\nM v[108:111],v[212:215],a[156:159],v[108:111]\nds_read_b64_tr_b16 v[212:213],v242 offset:17408\n" \
"ds_read_b64_tr_b16 v[214:215],v242 offset:17664\nM v[48:51],v[216:219],a[144:147],v[48:51]\nbuffer_load_dwordx4 v245,s[40:43],s69 offen offset:2048 lds\nM v[52:55],v[216:219],a[148:151],v[52:55]\nv_add_u32 v245,10240,v230\nds_read_b128 a[24:27],v240 offset:6144\nM v[112:115],v[216:219],a[152:155],v[112:115]\nM v[116:119],v[216:219],a[156:159],v[116:119]\n" \
"ds_read_b64_tr_b16 v[216:217],v241 offset:17920\nds_read_b64_tr_b16 v[218:219],v241 offset:18176\nM v[56:59],v[220:223],a[144:147],v[56:59]\nbuffer_load_dwordx4 v246,s[40:43],s69 offen offset:3072 lds\nM v[60:63],v[220:223],a[148:151],v[60:63]\nv_add_u32 v246,9344,v230\nv_mov_b32 v230,0x3f803f80\nM v[120:123],v[220:223],a[152:155],v[120:123]\n" \
"s_sub_u32 s40,s18,0x1000\ns_subb_u32 s41,s19,0\nM v[124:127],v[220:223],a[156:159],v[124:127]\nds_read_b64_tr_b16 v[220:221],v242 offset:17920\ns_waitcnt lgkmcnt(7)\nM v[0:3],v[192:195],a[160:163],v[0:3]\nbuffer_store_dwordx4 v[168:171],v240,s[52:55],s74 offen offset:2048 sc1\nM v[4:7],v[192:195],a[164:167],v[4:7]\n" \
"ds_read_b64_tr_b16 v[222:223],v242 offset:18176\nds_read_b128 a[28:31],v240 offset:7168\nM v[64:67],v[192:195],a[168:171],v[64:67]\nM v[68:71],v[192:195],a[172:175],v[68:71]\nds_read_b64_tr_b16 v[192:193],v241 offset:24576\nds_read_b64_tr_b16 v[194:195],v241 offset:24832\nM v[8:11],v[196:199],a[160:163],v[8:11]\nbuffer_load_dwordx4 a[244:247],v240,s[52:55],s76 offen offset:3072 sc1\n" \
"M v[12:15],v[196:199],a[164:167],v[12:15]\nds_read_b128 a[32:35],v240 offset:8192\nM v[72:75],v[196:199],a[168:171],v[72:75]\nM v[76:79],v[196:199],a[172:175],v[76:79]\nds_read_b64_tr_b16 v[196:197],v242 offset:24576\nds_read_b64_tr_b16 v[198:199],v242 offset:24832\nM v[16:19],v[200:203],a[160:163],v[16:19]\nbuffer_load_dwordx4 a[248:251],v240,s[52:55],s77 offen offset:2048 sc1\n" \
"M v[20:23],v[200:203],a[164:167],v[20:23]\nM v[80:83],v[200:203],a[168:171],v[80:83]\nds_read_b128 a[36:39],v240 offset:9216\nM v[84:87],v[200:203],a[172:175],v[84:87]\nds_read_b64_tr_b16 v[200:201],v241 offset:25088\nds_read_b64_tr_b16 v[202:203],v241 offset:25344\nM v[24:27],v[204:207],a[160:163],v[24:27]\nbuffer_load_dwordx4 a[252:255],v240,s[52:55],s77 offen offset:3072 sc1\n" \
"M v[28:31],v[204:207],a[164:167],v[28:31]\nM v[88:91],v[204:207],a[168:171],v[88:91]\nM v[92:95],v[204:207],a[172:175],v[92:95]\nds_read_b64_tr_b16 v[204:205],v242 offset:25088\nds_read_b64_tr_b16 v[206:207],v242 offset:25344\nM v[32:35],v[208:211],a[160:163],v[32:35]\ns_waitcnt vmcnt(13)\nv_cmp_eq_u32 vcc,0,v248\n" \
"M v[36:39],v[208:211],a[164:167],v[36:39]\ns_cbranch_vccnz .Lps1\n.Lpb1:\ns_add_u32 s80,s80,4096\nM v[96:99],v[208:211],a[168:171],v[96:99]\nds_read_b128 a[40:43],v240 offset:10240\nM v[100:103],v[208:211],a[172:175],v[100:103]\nds_read_b64_tr_b16 v[208:209],v241 offset:25600\n" \
"s_waitcnt lgkmcnt(8)\nM v[40:43],v[212:215],a[160:163],v[40:43]\nds_read_b64_tr_b16 v[210:211],v241 offset:25856\nM v[44:47],v[212:215],a[164:167],v[44:47]\nM v[104:107],v[212:215],a[168:171],v[104:107]\nds_read_b128 a[44:47],v240 offset:11264\nM v[108:111],v[212:215],a[172:175],v[108:111]\nds_read_b64_tr_b16 v[212:213],v242 offset:25600\n" \
"ds_read_b64_tr_b16 v[214:215],v242 offset:25856\nM v[48:51],v[216:219],a[160:163],v[48:51]\nM v[52:55],v[216:219],a[164:167],v[52:55]\nM v[112:115],v[216:219],a[168:171],v[112:115]\nM v[116:119],v[216:219],a[172:175],v[116:119]\nds_read_b64_tr_b16 v[216:217],v241 offset:26112\nds_read_b64_tr_b16 v[218:219],v241 offset:26368\nM v[56:59],v[220:223],a[160:163],v[56:59]\n" \
"ds_read_b128 a[48:51],v240 offset:12288\nM v[60:63],v[220:223],a[164:167],v[60:63]\nM v[120:123],v[220:223],a[168:171],v[120:123]\nbuffer_store_dwordx4 v[172:175],v240,s[52:55],s74 offen offset:3072 sc1\nM v[124:127],v[220:223],a[172:175],v[124:127]\nds_read_b64_tr_b16 v[220:221],v242 offset:26112\nds_read_b64_tr_b16 v[222:223],v242 offset:26368\nM v[0:3],v[192:195],a[176:179],v[0:3]\n" \
"s_add_u32 s74,s74,4096\nM v[4:7],v[192:195],a[180:183],v[4:7]\nM v[64:67],v[192:195],a[184:187],v[64:67]\nds_read_b128 a[52:55],v240 offset:13312\nM v[68:71],v[192:195],a[188:191],v[68:71]\ns_waitcnt lgkmcnt(9)\nM v[8:11],v[196:199],a[176:179],v[8:11]\nM v[12:15],v[196:199],a[180:183],v[12:15]\n" \
"M v[72:75],v[196:199],a[184:187],v[72:75]\nM v[76:79],v[196:199],a[188:191],v[76:79]\nM v[16:19],v[200:203],a[176:179],v[16:19]\nds_read_b128 a[56:59],v240 offset:14336\nM v[20:23],v[200:203],a[180:183],v[20:23]\nM v[80:83],v[200:203],a[184:187],v[80:83]\nM v[84:87],v[200:203],a[188:191],v[84:87]\nM v[24:27],v[204:207],a[176:179],v[24:27]\n" \
"M v[28:31],v[204:207],a[180:183],v[28:31]\nM v[88:91],v[204:207],a[184:187],v[88:91]\ns_waitcnt lgkmcnt(2)\ns_barrier\nM v[92:95],v[204:207],a[188:191],v[92:95]\nds_read_b64_tr_b16 v[192:193],v241 offset:32768\nds_read_b64_tr_b16 v[194:195],v241 offset:33024\nM v[32:35],v[208:211],a[176:179],v[32:35]\n" \
"ds_read_b64_tr_b16 v[196:197],v242 offset:32768\nds_read_b64_tr_b16 v[198:199],v242 offset:33024\nM v[36:39],v[208:211],a[180:183],v[36:39]\nds_read_b64_tr_b16 v[200:201],v241 offset:33280\nds_read_b64_tr_b16 v[202:203],v241 offset:33536\nM v[96:99],v[208:211],a[184:187],v[96:99]\nds_read_b64_tr_b16 v[204:205],v242 offset:33280\nds_read_b64_tr_b16 v[206:207],v242 offset:33536\n" \
"M v[100:103],v[208:211],a[188:191],v[100:103]\nds_read_b64_tr_b16 v[208:209],v241 offset:33792\nds_read_b64_tr_b16 v[210:211],v241 offset:34048\nM v[40:43],v[212:215],a[176:179],v[40:43]\nds_write_b64 v250,v[128:129] offset:0\nds_read_b128 a[60:63],v240 offset:15360\nM v[44:47],v[212:215],a[180:183],v[44:47]\nds_write_b64 v250,v[130:131] offset:8\n" \
"M v[104:107],v[212:215],a[184:187],v[104:107]\nds_write_b64 v250,v[132:133] offset:1024\nM v[108:111],v[212:215],a[188:191],v[108:111]\nds_read_b64_tr_b16 v[212:213],v242 offset:33792\nds_read_b64_tr_b16 v[214:215],v242 offset:34048\nM v[48:51],v[216:219],a[176:179],v[48:51]\nM v[52:55],v[216:219],a[180:183],v[52:55]\nds_write_b64 v250,v[134:135] offset:1032\n" \
"M v[112:115],v[216:219],a[184:187],v[112:115]\nM v[116:119],v[216:219],a[188:191],v[116:119]\nds_read_b64_tr_b16 v[216:217],v241 offset:34304\nds_read_b64_tr_b16 v[218:219],v241 offset:34560\nM v[56:59],v[220:223],a[176:179],v[56:59]\nbuffer_store_dwordx4 v[176:179],v240,s[52:55],s74 offen offset:0 sc1\nM v[60:63],v[220:223],a[180:183],v[60:63]\nds_write_b64 v250,v[136:137] offset:2048\n" \
"M v[120:123],v[220:223],a[184:187],v[120:123]\nM v[124:127],v[220:223],a[188:191],v[124:127]\nds_read_b64_tr_b16 v[220:221],v242 offset:34304\ns_waitcnt lgkmcnt(13)\nM v[0:3],v[192:195],a[192:195],v[0:3]\nds_write_b64 v250,v[138:139] offset:2056\nds_read_b64_tr_b16 v[222:223],v242 offset:34560\nM v[4:7],v[192:195],a[196:199],v[4:7]\n" \
"M v[64:67],v[192:195],a[200:203],v[64:67]\nds_write_b64 v250,v[140:141] offset:3072\nM v[68:71],v[192:195],a[204:207],v[68:71]\nds_read_b64_tr_b16 v[192:193],v241 offset:40960\nds_read_b64_tr_b16 v[194:195],v241 offset:41216\nM v[8:11],v[196:199],a[192:195],v[8:11]\nM v[12:15],v[196:199],a[196:199],v[12:15]\nds_write_b64 v250,v[142:143] offset:3080\n" \
"M v[72:75],v[196:199],a[200:203],v[72:75]\nM v[76:79],v[196:199],a[204:207],v[76:79]\nds_read_b64_tr_b16 v[196:197],v242 offset:40960\nds_read_b64_tr_b16 v[198:199],v242 offset:41216\nM v[16:19],v[200:203],a[192:195],v[16:19]\nds_write_b64 v250,v[144:145] offset:4096\nM v[20:23],v[200:203],a[196:199],v[20:23]\nM v[80:83],v[200:203],a[200:203],v[80:83]\n" \
"M v[84:87],v[200:203],a[204:207],v[84:87]\nds_read_b64_tr_b16 v[200:201],v241 offset:41472\nds_read_b64_tr_b16 v[202:203],v241 offset:41728\nM v[24:27],v[204:207],a[192:195],v[24:27]\nds_write_b64 v250,v[146:147] offset:4104\nM v[28:31],v[204:207],a[196:199],v[28:31]\nM v[88:91],v[204:207],a[200:203],v[88:91]\nds_write_b64 v250,v[148:149] offset:5120\n" \
"M v[92:95],v[204:207],a[204:207],v[92:95]\nds_read_b64_tr_b16 v[204:205],v242 offset:41472\ns_waitcnt lgkmcnt(12)\nM v[32:35],v[208:211],a[192:195],v[32:35]\nds_read_b64_tr_b16 v[206:207],v242 offset:41728\nM v[36:39],v[208:211],a[196:199],v[36:39]\nds_write_b64 v250,v[150:151] offset:5128\nM v[96:99],v[208:211],a[200:203],v[96:99]\n" \
"M v[100:103],v[208:211],a[204:207],v[100:103]\nds_read_b64_tr_b16 v[208:209],v241 offset:41984\nds_read_b64_tr_b16 v[210:211],v241 offset:42240\nM v[40:43],v[212:215],a[192:195],v[40:43]\nds_write_b64 v250,v[152:153] offset:6144\nM v[44:47],v[212:215],a[196:199],v[44:47]\nM v[104:107],v[212:215],a[200:203],v[104:107]\nM v[108:111],v[212:215],a[204:207],v[108:111]\n" \
"ds_read_b64_tr_b16 v[212:213],v242 offset:41984\nds_read_b64_tr_b16 v[214:215],v242 offset:42240\nM v[48:51],v[216:219],a[192:195],v[48:51]\nds_write_b64 v250,v[154:155] offset:6152\nM v[52:55],v[216:219],a[196:199],v[52:55]\nM v[112:115],v[216:219],a[200:203],v[112:115]\nbuffer_store_dwordx4 v[180:183],v240,s[52:55],s74 offen offset:1024 sc1\nM v[116:119],v[216:219],a[204:207],v[116:119]\n" \
"ds_read_b64_tr_b16 v[216:217],v241 offset:42496\nds_read_b64_tr_b16 v[218:219],v241 offset:42752\nM v[56:59],v[220:223],a[192:195],v[56:59]\nds_write_b64 v250,v[156:157] offset:7168\nM v[60:63],v[220:223],a[196:199],v[60:63]\nds_write_b64 v250,v[158:159] offset:7176\nM v[120:123],v[220:223],a[200:203],v[120:123]\nM v[124:127],v[220:223],a[204:207],v[124:127]\n" \
"ds_read_b64_tr_b16 v[220:221],v242 offset:42496\ns_waitcnt lgkmcnt(11)\nM v[0:3],v[192:195],a[208:211],v[0:3]\nglobal_load_dword v247,v229,s[18:19]\nM v[4:7],v[192:195],a[212:215],v[4:7]\nds_read_b64_tr_b16 v[222:223],v242 offset:42752\nv_lshl_add_u32 v229,v249,7,s70\nM v[64:67],v[192:195],a[216:219],v[64:67]\n" \
"ds_write_b64 v250,v[160:161] offset:8192\ns_lshl_b32 s70,s61,8\nM v[68:71],v[192:195],a[220:223],v[68:71]\nds_write_b64 v250,v[162:163] offset:8200\ns_add_u32 s68,s68,s70\nM v[8:11],v[196:199],a[208:211],v[8:11]\nM v[12:15],v[196:199],a[212:215],v[12:15]\nM v[72:75],v[196:199],a[216:219],v[72:75]\n" \
"ds_write_b64 v250,v[164:165] offset:9216\nM v[76:79],v[196:199],a[220:223],v[76:79]\nM v[16:19],v[200:203],a[208:211],v[16:19]\nM v[20:23],v[200:203],a[212:215],v[20:23]\nds_write_b64 v250,v[166:167] offset:9224\nM v[80:83],v[200:203],a[216:219],v[80:83]\nM v[84:87],v[200:203],a[220:223],v[84:87]\nM v[24:27],v[204:207],a[208:211],v[24:27]\n" \
"ds_write_b64 v250,v[168:169] offset:10240\nM v[28:31],v[204:207],a[212:215],v[28:31]\nM v[88:91],v[204:207],a[216:219],v[88:91]\ns_waitcnt vmcnt(8) lgkmcnt(5)\ns_barrier\nM v[92:95],v[204:207],a[220:223],v[92:95]\nds_read_b64_tr_b16 v[192:193],v241 offset:49152\nds_read_b64_tr_b16 v[194:195],v241 offset:49408\n" \
"M v[32:35],v[208:211],a[208:211],v[32:35]\nds_read_b64_tr_b16 v[196:197],v242 offset:49152\nds_read_b64_tr_b16 v[198:199],v242 offset:49408\nM v[36:39],v[208:211],a[212:215],v[36:39]\nds_read_b64_tr_b16 v[200:201],v241 offset:49664\nds_read_b64_tr_b16 v[202:203],v241 offset:49920\nM v[96:99],v[208:211],a[216:219],v[96:99]\nds_read_b64_tr_b16 v[204:205],v242 offset:49664\n" \
"ds_read_b64_tr_b16 v[206:207],v242 offset:49920\nM v[100:103],v[208:211],a[220:223],v[100:103]\nds_read_b64_tr_b16 v[208:209],v241 offset:50176\nds_read_b64_tr_b16 v[210:211],v241 offset:50432\nM v[40:43],v[212:215],a[208:211],v[40:43]\nds_write_b64 v250,v[170:171] offset:10248\nM v[44:47],v[212:215],a[212:215],v[44:47]\nds_write_b64 v250,v[172:173] offset:11264\n" \
"M v[104:107],v[212:215],a[216:219],v[104:107]\nds_write_b64 v250,v[174:175] offset:11272\nM v[108:111],v[212:215],a[220:223],v[108:111]\nds_read_b64_tr_b16 v[212:213],v242 offset:50176\nds_read_b64_tr_b16 v[214:215],v242 offset:50432\nM v[48:51],v[216:219],a[208:211],v[48:51]\nbuffer_store_dwordx4 v[184:187],v240,s[52:55],s74 offen offset:2048 sc1\nM v[52:55],v[216:219],a[212:215],v[52:55]\n" \
"ds_write_b64 v250,v[176:177] offset:12288\nM v[112:115],v[216:219],a[216:219],v[112:115]\nM v[116:119],v[216:219],a[220:223],v[116:119]\nds_read_b64_tr_b16 v[216:217],v241 offset:50688\nds_read_b64_tr_b16 v[218:219],v241 offset:50944\nM v[56:59],v[220:223],a[208:211],v[56:59]\nds_write_b64 v250,v[178:179] offset:12296\nM v[60:63],v[220:223],a[212:215],v[60:63]\n" \
"M v[120:123],v[220:223],a[216:219],v[120:123]\nds_write_b64 v250,v[180:181] offset:13312\nM v[124:127],v[220:223],a[220:223],v[124:127]\nds_read_b64_tr_b16 v[220:221],v242 offset:50688\ns_waitcnt lgkmcnt(13)\nM v[0:3],v[192:195],a[224:227],v[0:3]\nds_read_b64_tr_b16 v[222:223],v242 offset:50944\nM v[4:7],v[192:195],a[228:231],v[4:7]\n" \
"ds_write_b64 v250,v[182:183] offset:13320\nM v[64:67],v[192:195],a[232:235],v[64:67]\nM v[68:71],v[192:195],a[236:239],v[68:71]\nds_read_b64_tr_b16 v[192:193],v241 offset:57344\nds_read_b64_tr_b16 v[194:195],v241 offset:57600\nM v[8:11],v[196:199],a[224:227],v[8:11]\nds_write_b64 v250,v[184:185] offset:14336\nM v[12:15],v[196:199],a[228:231],v[12:15]\n" \
"M v[72:75],v[196:199],a[232:235],v[72:75]\nM v[76:79],v[196:199],a[236:239],v[76:79]\nds_read_b64_tr_b16 v[196:197],v242 offset:57344\nds_read_b64_tr_b16 v[198:199],v242 offset:57600\nM v[16:19],v[200:203],a[224:227],v[16:19]\nds_write_b64 v250,v[186:187] offset:14344\nM v[20:23],v[200:203],a[228:231],v[20:23]\nM v[80:83],v[200:203],a[232:235],v[80:83]\n" \
"ds_write_b64 v250,v[188:189] offset:15360\nM v[84:87],v[200:203],a[236:239],v[84:87]\nds_read_b64_tr_b16 v[200:201],v241 offset:57856\nds_read_b64_tr_b16 v[202:203],v241 offset:58112\nM v[24:27],v[204:207],a[224:227],v[24:27]\nM v[28:31],v[204:207],a[228:231],v[28:31]\nds_write_b64 v250,v[190:191] offset:15368\nv_add_u32 v250,114688,v240\n" \
"M v[88:91],v[204:207],a[232:235],v[88:91]\nM v[92:95],v[204:207],a[236:239],v[92:95]\nds_read_b64_tr_b16 v[204:205],v242 offset:57856\ns_waitcnt lgkmcnt(11)\nM v[32:35],v[208:211],a[224:227],v[32:35]\nds_read_b64_tr_b16 v[206:207],v242 offset:58112\nM v[36:39],v[208:211],a[228:231],v[36:39]\nM v[96:99],v[208:211],a[232:235],v[96:99]\n" \
"M v[100:103],v[208:211],a[236:239],v[100:103]\nds_read_b64_tr_b16 v[208:209],v241 offset:58368\nds_read_b64_tr_b16 v[210:211],v241 offset:58624\nM v[40:43],v[212:215],a[224:227],v[40:43]\nM v[44:47],v[212:215],a[228:231],v[44:47]\nM v[104:107],v[212:215],a[232:235],v[104:107]\nbuffer_store_dwordx4 v[188:191],v240,s[52:55],s74 offen offset:3072 sc1\nM v[108:111],v[212:215],a[236:239],v[108:111]\n" \
"ds_read_b64_tr_b16 v[212:213],v242 offset:58368\nds_read_b64_tr_b16 v[214:215],v242 offset:58624\nM v[48:51],v[216:219],a[224:227],v[48:51]\nM v[52:55],v[216:219],a[228:231],v[52:55]\nM v[112:115],v[216:219],a[232:235],v[112:115]\nM v[116:119],v[216:219],a[236:239],v[116:119]\nds_read_b64_tr_b16 v[216:217],v241 offset:58880\nds_read_b64_tr_b16 v[218:219],v241 offset:59136\n" \
"M v[56:59],v[220:223],a[224:227],v[56:59]\nglobal_load_dword v247,v229,s[92:93]\nM v[60:63],v[220:223],a[228:231],v[60:63]\nv_and_b32 v229,3,v249\nv_cmp_eq_u32 vcc,0,v229\nM v[120:123],v[220:223],a[232:235],v[120:123]\nM v[124:127],v[220:223],a[236:239],v[124:127]\nds_read_b64_tr_b16 v[220:221],v242 offset:58880\n" \
"ds_read_b64_tr_b16 v[222:223],v242 offset:59136\ns_waitcnt lgkmcnt(8)\nM v[0:3],v[192:195],a[240:243],v[0:3]\nv_cndmask_b32 v229,0,v230,vcc\nv_and_b32 v230,0xff,v241\nM v[8:11],v[196:199],a[240:243],v[8:11]\nv_lshl_or_b32 v230,v231,4,v230\nv_and_b32 v231,32,v249\n" \
"M v[16:19],v[200:203],a[240:243],v[16:19]\nv_accvgpr_write_b32 a208,v229\nv_accvgpr_write_b32 a209,v229\nM v[24:27],v[204:207],a[240:243],v[24:27]\nv_cvt_pk_bf16_f32 v0,v0,v1\ns_waitcnt lgkmcnt(4)\nM v[32:35],v[208:211],a[240:243],v[32:35]\nv_cvt_pk_bf16_f32 v1,v2,v3\n" \
"v_cvt_pk_bf16_f32 v2,v8,v9\nM v[40:43],v[212:215],a[240:243],v[40:43]\nv_cvt_pk_bf16_f32 v16,v16,v17\ns_waitcnt lgkmcnt(2)\nM v[48:51],v[216:219],a[240:243],v[48:51]\nv_cvt_pk_bf16_f32 v17,v18,v19\ns_waitcnt lgkmcnt(0)\nM v[56:59],v[220:223],a[240:243],v[56:59]\n" \
"v_cvt_pk_bf16_f32 v32,v32,v33\ns_waitcnt vmcnt(7)\nM v[4:7],v[192:195],a[244:247],v[4:7]\nv_cvt_pk_bf16_f32 v33,v34,v35\nv_cvt_pk_bf16_f32 v3,v10,v11\nM v[12:15],v[196:199],a[244:247],v[12:15]\nv_cvt_pk_bf16_f32 v48,v48,v49\nv_cvt_pk_bf16_f32 v49,v50,v51\n" \
"M v[20:23],v[200:203],a[244:247],v[20:23]\nv_cvt_pk_bf16_f32 v18,v24,v25\nv_cvt_pk_bf16_f32 v19,v26,v27\nM v[28:31],v[204:207],a[244:247],v[28:31]\nv_cvt_pk_bf16_f32 v4,v4,v5\nv_cvt_pk_bf16_f32 v5,v6,v7\nM v[36:39],v[208:211],a[244:247],v[36:39]\nv_cvt_pk_bf16_f32 v34,v40,v41\n" \
"v_cvt_pk_bf16_f32 v35,v42,v43\nM v[44:47],v[212:215],a[244:247],v[44:47]\nv_cvt_pk_bf16_f32 v20,v20,v21\nv_cvt_pk_bf16_f32 v21,v22,v23\nM v[52:55],v[216:219],a[244:247],v[52:55]\nv_cvt_pk_bf16_f32 v50,v56,v57\nv_cvt_pk_bf16_f32 v51,v58,v59\nM v[60:63],v[220:223],a[244:247],v[60:63]\n" \
"v_cvt_pk_bf16_f32 v36,v36,v37\nv_cvt_pk_bf16_f32 v37,v38,v39\nM v[64:67],v[192:195],a[248:251],v[64:67]\nv_cvt_pk_bf16_f32 v6,v12,v13\nv_cvt_pk_bf16_f32 v7,v14,v15\nM v[72:75],v[196:199],a[248:251],v[72:75]\nv_cvt_pk_bf16_f32 v52,v52,v53\nv_cvt_pk_bf16_f32 v53,v54,v55\n" \
"M v[80:83],v[200:203],a[248:251],v[80:83]\nv_cvt_pk_bf16_f32 v22,v28,v29\nv_cvt_pk_bf16_f32 v23,v30,v31\nM v[88:91],v[204:207],a[248:251],v[88:91]\nv_cvt_pk_bf16_f32 v64,v64,v65\nv_cvt_pk_bf16_f32 v65,v66,v67\nM v[96:99],v[208:211],a[248:251],v[96:99]\nv_cvt_pk_bf16_f32 v38,v44,v45\n" \
"v_cvt_pk_bf16_f32 v39,v46,v47\nM v[104:107],v[212:215],a[248:251],v[104:107]\nv_cvt_pk_bf16_f32 v80,v80,v81\nv_cvt_pk_bf16_f32 v81,v82,v83\nM v[112:115],v[216:219],a[248:251],v[112:115]\nv_cvt_pk_bf16_f32 v54,v60,v61\nv_cvt_pk_bf16_f32 v55,v62,v63\nM v[120:123],v[220:223],a[248:251],v[120:123]\n" \
"v_cvt_pk_bf16_f32 v96,v96,v97\nv_cvt_pk_bf16_f32 v97,v98,v99\nM v[68:71],v[192:195],a[252:255],v[68:71]\nv_cvt_pk_bf16_f32 v66,v72,v73\nv_cvt_pk_bf16_f32 v67,v74,v75\nM v[76:79],v[196:199],a[252:255],v[76:79]\nv_cvt_pk_bf16_f32 v112,v112,v113\nv_cvt_pk_bf16_f32 v113,v114,v115\n" \
"M v[84:87],v[200:203],a[252:255],v[84:87]\nv_cvt_pk_bf16_f32 v82,v88,v89\nv_cvt_pk_bf16_f32 v83,v90,v91\nM v[92:95],v[204:207],a[252:255],v[92:95]\nv_cvt_pk_bf16_f32 v68,v68,v69\nv_cvt_pk_bf16_f32 v69,v70,v71\nM v[100:103],v[208:211],a[252:255],v[100:103]\nv_cvt_pk_bf16_f32 v98,v104,v105\n" \
"v_cvt_pk_bf16_f32 v99,v106,v107\nM v[108:111],v[212:215],a[252:255],v[108:111]\nv_cvt_pk_bf16_f32 v84,v84,v85\nv_cvt_pk_bf16_f32 v85,v86,v87\nM v[116:119],v[216:219],a[252:255],v[116:119]\nv_cvt_pk_bf16_f32 v114,v120,v121\nv_cvt_pk_bf16_f32 v115,v122,v123\nv_cvt_pk_bf16_f32 v100,v100,v101\n" \
"v_cvt_pk_bf16_f32 v101,v102,v103\nM v[124:127],v[220:223],a[252:255],v[124:127]\nv_cvt_pk_bf16_f32 v70,v76,v77\nv_cvt_pk_bf16_f32 v71,v78,v79\nv_cvt_pk_bf16_f32 v86,v92,v93\nv_cvt_pk_bf16_f32 v116,v116,v117\nv_cvt_pk_bf16_f32 v117,v118,v119\nv_cvt_pk_bf16_f32 v87,v94,v95\n" \
"v_cvt_pk_bf16_f32 v102,v108,v109\nv_cvt_pk_bf16_f32 v103,v110,v111\nv_cvt_pk_bf16_f32 v118,v124,v125\nv_cvt_pk_bf16_f32 v119,v126,v127\ns_waitcnt vmcnt(1)\nbuffer_store_dword v254,off,s[52:55],s29 sc1\nv_accvgpr_write_b32 a210,v229\nv_accvgpr_write_b32 a211,v229\n" \
"v_accvgpr_write_b32 a244,0\nv_accvgpr_write_b32 a248,-1\nv_accvgpr_write_b32 a245,0\nv_accvgpr_write_b32 a249,-1\nv_accvgpr_write_b32 a246,0\nv_accvgpr_write_b32 a250,-1\nv_accvgpr_write_b32 a247,0\nv_accvgpr_write_b32 a251,-1\n" \
"v_lshl_or_b32 v242,v231,6,v230\nv_add_u32 v241,65536,v240\nv_accvgpr_write_b32 a128,v0\nv_accvgpr_write_b32 a129,v1\nv_accvgpr_write_b32 a130,v2\nv_accvgpr_write_b32 a131,v3\nv_accvgpr_write_b32 a132,v16\nv_accvgpr_write_b32 a133,v17\n" \
"v_accvgpr_write_b32 a134,v18\nv_accvgpr_write_b32 a135,v19\nv_accvgpr_write_b32 a136,v32\nv_accvgpr_write_b32 a137,v33\nv_accvgpr_write_b32 a138,v34\nv_accvgpr_write_b32 a139,v35\nv_accvgpr_write_b32 a140,v48\nv_accvgpr_write_b32 a141,v49\n" \
"v_accvgpr_write_b32 a142,v50\nv_accvgpr_write_b32 a143,v51\nv_accvgpr_write_b32 a144,v4\nv_accvgpr_write_b32 a145,v5\nv_accvgpr_write_b32 a146,v6\nv_accvgpr_write_b32 a147,v7\nv_accvgpr_write_b32 a148,v20\nv_accvgpr_write_b32 a149,v21\n" \
"v_accvgpr_write_b32 a150,v22\nv_accvgpr_write_b32 a151,v23\nv_accvgpr_write_b32 a152,v36\nv_accvgpr_write_b32 a153,v37\nv_accvgpr_write_b32 a154,v38\nv_accvgpr_write_b32 a155,v39\nv_accvgpr_write_b32 a156,v52\nv_accvgpr_write_b32 a157,v53\n" \
"v_accvgpr_write_b32 a158,v54\nv_accvgpr_write_b32 a159,v55\nv_accvgpr_write_b32 a160,v64\nv_accvgpr_write_b32 a161,v65\nv_accvgpr_write_b32 a162,v66\nv_accvgpr_write_b32 a163,v67\nv_accvgpr_write_b32 a164,v80\nv_accvgpr_write_b32 a165,v81\n" \
"v_accvgpr_write_b32 a166,v82\nv_accvgpr_write_b32 a167,v83\nv_accvgpr_write_b32 a168,v96\nv_accvgpr_write_b32 a169,v97\nv_accvgpr_write_b32 a170,v98\nv_accvgpr_write_b32 a171,v99\nv_accvgpr_write_b32 a172,v112\nv_accvgpr_write_b32 a173,v113\n" \
"v_accvgpr_write_b32 a174,v114\nv_accvgpr_write_b32 a175,v115\nv_accvgpr_write_b32 a176,v68\nv_accvgpr_write_b32 a177,v69\nv_accvgpr_write_b32 a178,v70\nv_accvgpr_write_b32 a179,v71\nv_accvgpr_write_b32 a180,v84\nv_accvgpr_write_b32 a181,v85\n" \
"v_accvgpr_write_b32 a182,v86\nv_accvgpr_write_b32 a183,v87\nv_accvgpr_write_b32 a184,v100\nv_accvgpr_write_b32 a185,v101\nv_accvgpr_write_b32 a186,v102\nv_accvgpr_write_b32 a187,v103\nv_accvgpr_write_b32 a188,v116\nv_accvgpr_write_b32 a189,v117\n" \
"v_accvgpr_write_b32 a190,v118\nv_accvgpr_write_b32 a191,v119\ns_waitcnt lgkmcnt(0)\n.Lattn:\ns_cmp_lt_u32 s7,2\ns_cbranch_scc0 .Lp23\ns_cmp_eq_u32 s7,0\ns_cbranch_scc0 .Lpro1\n" \
".Lpro0:\ns_barrier\nM v[64:67],a[0:3],a[128:131],0\nds_read_b128 a[32:35],v240 offset:40960\nds_read_b128 a[36:39],v240 offset:41984\nM v[64:67],a[4:7],a[132:135],v[64:67]\nds_read_b128 a[40:43],v240 offset:43008\ns_mov_b32 m0,s64\n" \
"v_mov_b32 v218,0\nM v[64:67],a[8:11],a[136:139],v[64:67]\nds_read_b128 a[44:47],v240 offset:44032\nv_mov_b32 v219,0\nM v[64:67],a[12:15],a[140:143],v[64:67]\nds_read_b128 a[48:51],v240 offset:45056\nbuffer_load_dwordx4 v240,s[44:47],s36 offen offset:0 sc1 lds\nM v[68:71],a[0:3],a[144:147],0\n" \
"ds_read_b128 a[0:3],v240 offset:32768\nM v[68:71],a[4:7],a[148:151],v[68:71]\nds_read_b128 a[4:7],v240 offset:33792\nM v[68:71],a[8:11],a[152:155],v[68:71]\nds_read_b128 a[8:11],v240 offset:34816\nv_max3_f32 v225,v64,v65,v66\nv_max_f32 v225,v225,v67\nM v[68:71],a[12:15],a[156:159],v[68:71]\n" \
"ds_read_b128 a[12:15],v240 offset:35840\nv_cndmask_b32 v225,v224,v225,s[90:91]\nv_cndmask_b32 v64,v64,v252,s[82:83]\nM v[76:79],a[16:19],a[144:147],0\nds_read_b128 a[16:19],v240 offset:36864\nv_cndmask_b32 v65,v65,v252,s[84:85]\nM v[76:79],a[20:23],a[148:151],v[76:79]\nds_read_b128 a[20:23],v240 offset:37888\n" \
"v_max3_f32 v229,v68,v69,v70\nv_cndmask_b32 v66,v66,v252,s[86:87]\nM v[76:79],a[24:27],a[152:155],v[76:79]\nds_read_b128 a[24:27],v240 offset:38912\nv_cndmask_b32 v67,v67,v252,s[88:89]\ns_lshl4_add_u32 s69,s13,s68\nM v[76:79],a[28:31],a[156:159],v[76:79]\nds_read_b128 a[28:31],v240 offset:39936\n" \
"s_waitcnt lgkmcnt(7)\nM v[136:139],a[0:3],a[160:163],0\nbuffer_load_dwordx4 v240,s[44:47],s36 offen offset:1024 sc1 lds\ns_waitcnt lgkmcnt(6)\nM v[136:139],a[4:7],a[164:167],v[136:139]\nds_read_b128 a[52:55],v240 offset:46080\ns_waitcnt lgkmcnt(6)\nM v[136:139],a[8:11],a[168:171],v[136:139]\n" \
"v_max3_f32 v229,v229,v71,v76\nv_max3_f32 v229,v229,v77,v78\ns_waitcnt lgkmcnt(5)\nM v[136:139],a[12:15],a[172:175],v[136:139]\nv_max_f32 v229,v229,v79\nv_cndmask_b32 v229,v228,v229,s[90:91]\nds_read_b128 a[56:59],v240 offset:47104\nM v[140:143],a[0:3],a[176:179],0\n" \
"v_permlane16_swap_b32 v225,v229\nv_max_f32 v225,v225,v229\nM v[140:143],a[4:7],a[180:183],v[140:143]\nv_mov_b32 v229,v225\nds_read_b128 a[60:63],v240 offset:48128\nv_max3_f32 v233,v136,v137,v138\nM v[140:143],a[8:11],a[184:187],v[140:143]\nv_permlane32_swap_b32 v225,v229\n" \
"v_max_f32 v225,v225,v229\nv_mov_b32 v229,v225\nM v[140:143],a[12:15],a[188:191],v[140:143]\nds_read_b128 a[64:67],v241 offset:0\ns_waitcnt lgkmcnt(5)\nM v[152:155],a[16:19],a[160:163],0\nv_permlane16_swap_b32 v225,v229\nv_sub_f32 v224,0,v225\n" \
"v_sub_f32 v226,0,v225\nM v[152:155],a[20:23],a[164:167],v[152:155]\nv_sub_f32 v227,0,v225\nv_sub_f32 v225,0,v225\nv_sub_f32 v228,0,v229\nM v[152:155],a[24:27],a[168:171],v[152:155]\nv_sub_f32 v230,0,v229\ns_waitcnt lgkmcnt(3)\n" \
"M v[152:155],a[28:31],a[172:175],v[152:155]\nv_sub_f32 v231,0,v229\nv_sub_f32 v229,0,v229\nv_max3_f32 v237,v140,v141,v142\nM v[156:159],a[16:19],a[176:179],0\nds_read_b128 a[68:71],v241 offset:1024\nds_read_b128 a[72:75],v241 offset:2048\nM v[156:159],a[20:23],a[180:183],v[156:159]\n" \
"ds_read_b128 a[76:79],v241 offset:3072\nv_max3_f32 v233,v233,v139,v152\nv_max3_f32 v233,v233,v153,v154\nM v[156:159],a[24:27],a[184:187],v[156:159]\nv_max_f32 v233,v233,v155\nv_cndmask_b32 v233,v232,v233,s[90:91]\nv_add_f32 v64,v64,v224\nM v[156:159],a[28:31],a[188:191],v[156:159]\n" \
"v_add_f32 v65,v65,v224\nv_add_f32 v66,v66,v224\nM v[128:131],a[0:3],a[128:131],v[224:227]\nv_add_f32 v67,v67,v224\nv_exp_f32 v64,v64\nM v[128:131],a[4:7],a[132:135],v[128:131]\nv_exp_f32 v65,v65\nv_add_f32 v68,v68,v228\n" \
"M v[128:131],a[8:11],a[136:139],v[128:131]\nv_max3_f32 v237,v237,v143,v156\nv_max3_f32 v237,v237,v157,v158\nM v[128:131],a[12:15],a[140:143],v[128:131]\nv_max_f32 v237,v237,v159\nv_cndmask_b32 v237,v236,v237,s[90:91]\nv_add_f32 v69,v69,v228\nM v[132:135],a[0:3],a[144:147],v[228:231]\n" \
"v_permlane16_swap_b32 v233,v237\nv_max_f32 v233,v233,v237\nv_mov_b32 v237,v233\nM v[132:135],a[4:7],a[148:151],v[132:135]\nv_exp_f32 v66,v66\nM v[132:135],a[8:11],a[152:155],v[132:135]\nv_permlane32_swap_b32 v233,v237\nv_max_f32 v233,v233,v237\n" \
"v_mov_b32 v237,v233\nM v[132:135],a[12:15],a[156:159],v[132:135]\nv_exp_f32 v67,v67\nM v[144:147],a[16:19],a[128:131],v[224:227]\nv_permlane16_swap_b32 v233,v237\nv_sub_f32 v232,0,v233\nv_sub_f32 v234,0,v233\nM v[144:147],a[20:23],a[132:135],v[144:147]\n" \
"v_sub_f32 v235,0,v233\nv_sub_f32 v233,0,v233\nv_sub_f32 v236,0,v237\nM v[144:147],a[24:27],a[136:139],v[144:147]\nv_sub_f32 v238,0,v237\nv_sub_f32 v239,0,v237\nM v[144:147],a[28:31],a[140:143],v[144:147]\nv_sub_f32 v237,0,v237\n" \
"v_cndmask_b32 v76,v76,v252,s[82:83]\nv_cndmask_b32 v77,v77,v252,s[84:85]\nM v[148:151],a[16:19],a[144:147],v[228:231]\nv_cndmask_b32 v78,v78,v252,s[86:87]\nv_cndmask_b32 v79,v79,v252,s[88:89]\nv_add_f32 v70,v70,v228\nM v[148:151],a[20:23],a[148:151],v[148:151]\nv_add_f32 v71,v71,v228\n" \
"v_cvt_pk_bf16_f32 v64,v64,v65\nM v[148:151],a[24:27],a[152:155],v[148:151]\nv_exp_f32 v68,v68\nds_read_b128 a[80:83],v241 offset:4096\nM v[148:151],a[28:31],a[156:159],v[148:151]\nv_exp_f32 v69,v69\nv_add_f32 v76,v76,v228\nM v[160:163],a[32:35],a[128:131],v[224:227]\n" \
"v_exp_f32 v70,v70\nM v[160:163],a[36:39],a[132:135],v[160:163]\nv_exp_f32 v71,v71\nv_add_f32 v77,v77,v228\nM v[160:163],a[40:43],a[136:139],v[160:163]\nv_add_f32 v78,v78,v228\nv_add_f32 v79,v79,v228\nM v[160:163],a[44:47],a[140:143],v[160:163]\n" \
"v_cvt_pk_bf16_f32 v65,v66,v67\nv_exp_f32 v76,v76\nM v[164:167],a[32:35],a[144:147],v[228:231]\nv_exp_f32 v77,v77\nv_mov_b32 v66,0\nM v[164:167],a[36:39],a[148:151],v[164:167]\nv_exp_f32 v78,v78\nM v[164:167],a[40:43],a[152:155],v[164:167]\n" \
"v_exp_f32 v79,v79\nv_mov_b32 v67,0\nM v[164:167],a[44:47],a[156:159],v[164:167]\nv_cvt_pk_bf16_f32 v68,v68,v69\nv_cvt_pk_bf16_f32 v69,v70,v71\nv_cvt_pk_bf16_f32 v70,v76,v77\nM v[176:179],a[48:51],a[128:131],v[224:227]\nv_cvt_pk_bf16_f32 v71,v78,v79\n" \
"ds_read_b128 a[84:87],v241 offset:5120\nM v[176:179],a[52:55],a[132:135],v[176:179]\nbuffer_load_dwordx4 v240,s[44:47],s36 offen offset:2048 sc1 lds\ns_waitcnt lgkmcnt(1)\nM v[176:179],a[56:59],a[136:139],v[176:179]\nds_read_b128 a[88:91],v241 offset:6144\nds_read_b128 a[92:95],v241 offset:7168\nv_add_f32 v136,v136,v232\n" \
"M v[176:179],a[60:63],a[140:143],v[176:179]\nds_read_b128 a[0:3],v240 offset:49152\nv_add_f32 v137,v137,v232\nM v[180:183],a[48:51],a[144:147],v[228:231]\nds_read_b128 a[4:7],v240 offset:50176\nv_exp_f32 v128,v128\nM v[180:183],a[52:55],a[148:151],v[180:183]\nbuffer_load_dwordx4 v240,s[44:47],s36 offen offset:3072 sc1 lds\n" \
"M v[180:183],a[56:59],a[152:155],v[180:183]\nds_read_b128 a[8:11],v240 offset:51200\nds_read_b128 a[12:15],v240 offset:52224\nv_add_f32 v138,v138,v232\nM v[180:183],a[60:63],a[156:159],v[180:183]\nds_read_b128 a[16:19],v240 offset:53248\nv_exp_f32 v129,v129\nM v[168:171],a[32:35],a[160:163],v[232:235]\n" \
"v_exp_f32 v130,v130\nM v[168:171],a[36:39],a[164:167],v[168:171]\nv_exp_f32 v131,v131\nds_read_b128 a[20:23],v240 offset:54272\nM v[168:171],a[40:43],a[168:171],v[168:171]\nv_cvt_pk_bf16_f32 v192,v128,v129\nv_cvt_pk_bf16_f32 v193,v130,v131\nds_read_b128 a[24:27],v240 offset:55296\n" \
"M v[168:171],a[44:47],a[172:175],v[168:171]\nds_read_b128 a[28:31],v240 offset:56320\nv_add_f32 v139,v139,v232\nM v[172:175],a[32:35],a[176:179],v[236:239]\nv_exp_f32 v132,v132\nv_add_f32 v140,v140,v236\nM v[172:175],a[36:39],a[180:183],v[172:175]\nv_exp_f32 v133,v133\n" \
"v_add_f32 v141,v141,v236\nM v[172:175],a[40:43],a[184:187],v[172:175]\nv_exp_f32 v134,v134\nM v[172:175],a[44:47],a[188:191],v[172:175]\nv_exp_f32 v135,v135\nv_cvt_pk_bf16_f32 v196,v132,v133\nM v[188:191],a[48:51],a[176:179],v[236:239]\nv_cvt_pk_bf16_f32 v197,v134,v135\n" \
"v_exp_f32 v144,v144\nM v[188:191],a[52:55],a[180:183],v[188:191]\nv_exp_f32 v145,v145\nM v[188:191],a[56:59],a[184:187],v[188:191]\nv_exp_f32 v146,v146\nv_cvt_pk_bf16_f32 v194,v144,v145\nM v[188:191],a[60:63],a[188:191],v[188:191]\nv_exp_f32 v147,v147\n" \
"M v[0:3],a[64:67],v[64:67],0\nv_cvt_pk_bf16_f32 v195,v146,v147\nv_exp_f32 v148,v148\nM v[4:7],a[64:67],v[68:71],0\nv_exp_f32 v149,v149\nv_add_f32 v142,v142,v236\nM v[8:11],a[68:71],v[64:67],0\nbuffer_load_dword v248,v251,s[52:55],s80 offen sc1\n" \
"M v[12:15],a[68:71],v[68:71],0\nv_exp_f32 v150,v150\nv_cvt_pk_bf16_f32 v198,v148,v149\nM v[16:19],a[72:75],v[64:67],0\nv_exp_f32 v151,v151\nv_add_f32 v143,v143,v236\nM v[20:23],a[72:75],v[68:71],0\nv_cvt_pk_bf16_f32 v199,v150,v151\n" \
"ds_read_b128 a[32:35],v240 offset:57344\nM v[24:27],a[76:79],v[64:67],0\nv_exp_f32 v136,v136\nv_add_f32 v152,v152,v232\nM v[28:31],a[76:79],v[68:71],0\nv_exp_f32 v137,v137\nv_add_f32 v153,v153,v232\nM v[32:35],a[80:83],v[64:67],0\n" \
"v_exp_f32 v138,v138\nM v[36:39],a[80:83],v[68:71],0\nv_exp_f32 v139,v139\ns_waitcnt lgkmcnt(1)\nM v[40:43],a[84:87],v[64:67],0\nv_cvt_pk_bf16_f32 v200,v136,v137\nv_cvt_pk_bf16_f32 v201,v138,v139\nM v[44:47],a[84:87],v[68:71],0\n" \
"v_exp_f32 v140,v140\nv_add_f32 v154,v154,v232\nM v[48:51],a[88:91],v[64:67],0\nv_exp_f32 v141,v141\nv_add_f32 v155,v155,v232\nM v[52:55],a[88:91],v[68:71],0\nv_exp_f32 v142,v142\nM v[56:59],a[92:95],v[64:67],0\n" \
"v_exp_f32 v143,v143\nv_cvt_pk_bf16_f32 v204,v140,v141\nM v[60:63],a[92:95],v[68:71],0\nv_cvt_pk_bf16_f32 v205,v142,v143\nv_exp_f32 v152,v152\nM a[192:195],a[208:211],v[64:67],0\nv_exp_f32 v153,v153\nM a[196:199],a[208:211],v[68:71],0\n" \
"v_exp_f32 v154,v154\nds_read_b128 a[36:39],v240 offset:58368\nM v[128:131],a[0:3],a[128:131],v[224:227]\nv_exp_f32 v155,v155\nv_cvt_pk_bf16_f32 v202,v152,v153\nM v[128:131],a[4:7],a[132:135],v[128:131]\nv_cvt_pk_bf16_f32 v203,v154,v155\nds_read_b128 a[40:43],v240 offset:59392\n" \
"M v[128:131],a[8:11],a[136:139],v[128:131]\nds_read_b128 a[44:47],v240 offset:60416\nv_add_f32 v156,v156,v236\nv_add_f32 v157,v157,v236\nM v[128:131],a[12:15],a[140:143],v[128:131]\nv_add_f32 v158,v158,v236\nv_add_f32 v159,v159,v236\nM v[132:135],a[0:3],a[144:147],v[228:231]\n" \
"v_exp_f32 v156,v156\nds_read_b128 a[48:51],v240 offset:61440\nM v[132:135],a[4:7],a[148:151],v[132:135]\nv_exp_f32 v157,v157\nds_read_b128 a[52:55],v240 offset:62464\nM v[132:135],a[8:11],a[152:155],v[132:135]\nv_exp_f32 v158,v158\nM v[132:135],a[12:15],a[156:159],v[132:135]\n" \
"v_exp_f32 v159,v159\nv_cvt_pk_bf16_f32 v206,v156,v157\nM v[144:147],a[16:19],a[128:131],v[224:227]\nv_cvt_pk_bf16_f32 v207,v158,v159\nv_exp_f32 v160,v160\nM v[144:147],a[20:23],a[132:135],v[144:147]\nv_exp_f32 v161,v161\nM v[144:147],a[24:27],a[136:139],v[144:147]\n" \
"v_exp_f32 v162,v162\nv_cvt_pk_bf16_f32 v208,v160,v161\nM v[144:147],a[28:31],a[140:143],v[144:147]\nv_exp_f32 v163,v163\nds_read_b128 a[56:59],v240 offset:63488\nM v[148:151],a[16:19],a[144:147],v[228:231]\nv_cvt_pk_bf16_f32 v209,v162,v163\nds_read_b128 a[60:63],v240 offset:64512\n" \
"M v[148:151],a[20:23],a[148:151],v[148:151]\nv_exp_f32 v164,v164\nv_cndmask_b32 v168,v168,v252,s[82:83]\nM v[148:151],a[24:27],a[152:155],v[148:151]\nv_exp_f32 v165,v165\nM v[148:151],a[28:31],a[156:159],v[148:151]\nv_exp_f32 v166,v166\nv_cvt_pk_bf16_f32 v212,v164,v165\n" \
"M v[136:139],a[0:3],a[160:163],v[232:235]\nv_exp_f32 v167,v167\nv_cndmask_b32 v169,v169,v252,s[84:85]\nM v[136:139],a[4:7],a[164:167],v[136:139]\nv_cvt_pk_bf16_f32 v213,v166,v167\nv_cndmask_b32 v170,v170,v252,s[86:87]\nM v[136:139],a[8:11],a[168:171],v[136:139]\nv_exp_f32 v176,v176\n" \
"v_cndmask_b32 v171,v171,v252,s[88:89]\nM v[136:139],a[12:15],a[172:175],v[136:139]\nv_exp_f32 v177,v177\nds_read_b128 a[64:67],v241 offset:32768\nM v[140:143],a[0:3],a[176:179],v[236:239]\nv_exp_f32 v178,v178\nM v[140:143],a[4:7],a[180:183],v[140:143]\nv_exp_f32 v179,v179\n" \
"v_cvt_pk_bf16_f32 v210,v176,v177\nM v[140:143],a[8:11],a[184:187],v[140:143]\nv_cvt_pk_bf16_f32 v211,v178,v179\nv_exp_f32 v180,v180\nM v[140:143],a[12:15],a[188:191],v[140:143]\nv_exp_f32 v181,v181\nM v[152:155],a[16:19],a[160:163],v[232:235]\nv_exp_f32 v182,v182\n" \
"v_cvt_pk_bf16_f32 v214,v180,v181\nM v[152:155],a[20:23],a[164:167],v[152:155]\nv_exp_f32 v183,v183\nM v[152:155],a[24:27],a[168:171],v[152:155]\nv_cvt_pk_bf16_f32 v215,v182,v183\nv_exp_f32 v168,v168\nM v[152:155],a[28:31],a[172:175],v[152:155]\nv_exp_f32 v169,v169\n" \
"ds_read_b128 a[68:71],v241 offset:33792\nM v[156:159],a[16:19],a[176:179],v[236:239]\nv_exp_f32 v170,v170\nM v[156:159],a[20:23],a[180:183],v[156:159]\nv_exp_f32 v171,v171\nv_cvt_pk_bf16_f32 v216,v168,v169\nM v[156:159],a[24:27],a[184:187],v[156:159]\nv_cvt_pk_bf16_f32 v217,v170,v171\n" \
"v_exp_f32 v172,v172\nM v[156:159],a[28:31],a[188:191],v[156:159]\nv_cndmask_b32 v188,v188,v252,s[82:83]\ns_waitcnt lgkmcnt(1)\nM v[160:163],a[32:35],a[128:131],v[224:227]\nv_exp_f32 v173,v173\nv_cndmask_b32 v189,v189,v252,s[84:85]\nM v[160:163],a[36:39],a[132:135],v[160:163]\n" \
"v_exp_f32 v174,v174\nv_cvt_pk_bf16_f32 v220,v172,v173\nM v[160:163],a[40:43],a[136:139],v[160:163]\ns_waitcnt vmcnt(1)\ns_barrier\nM v[160:163],a[44:47],a[140:143],v[160:163]\nv_exp_f32 v175,v175\nv_cndmask_b32 v190,v190,v252,s[86:87]\n" \
"M v[164:167],a[32:35],a[144:147],v[228:231]\nv_cvt_pk_bf16_f32 v221,v174,v175\nv_cndmask_b32 v191,v191,v252,s[88:89]\nds_read_b128 a[72:75],v241 offset:34816\nM v[164:167],a[36:39],a[148:151],v[164:167]\nv_exp_f32 v188,v188\nM v[164:167],a[40:43],a[152:155],v[164:167]\nv_exp_f32 v189,v189\n" \
"ds_read_b128 a[76:79],v241 offset:35840\nM v[164:167],a[44:47],a[156:159],v[164:167]\nv_exp_f32 v190,v190\nM v[176:179],a[48:51],a[128:131],v[224:227]\nv_exp_f32 v191,v191\nv_cvt_pk_bf16_f32 v222,v188,v189\nM v[176:179],a[52:55],a[132:135],v[176:179]\nv_cvt_pk_bf16_f32 v223,v190,v191\n" \
"ds_read_b128 a[80:83],v241 offset:36864\nds_read_b128 a[84:87],v241 offset:37888\nM v[176:179],a[56:59],a[136:139],v[176:179]\nds_read_b128 a[88:91],v241 offset:38912\ns_add_u32 m0,s64,131072\nM v[176:179],a[60:63],a[140:143],v[176:179]\nds_read_b128 a[92:95],v241 offset:39936\nbuffer_load_dwordx4 v243,s[40:43],s69 offen offset:0 lds\n" \
"M v[180:183],a[48:51],a[144:147],v[228:231]\nds_read_b128 a[96:99],v241 offset:40960\nv_exp_f32 v128,v128\nM v[180:183],a[52:55],a[148:151],v[180:183]\nds_read_b128 a[100:103],v241 offset:41984\ns_waitcnt vmcnt(1)\nv_cmp_eq_u32 vcc,0,v248\nM v[180:183],a[56:59],a[152:155],v[180:183]\n" \
"ds_read_b128 a[104:107],v241 offset:43008\nv_exp_f32 v129,v129\nM v[180:183],a[60:63],a[156:159],v[180:183]\nds_read_b128 a[108:111],v241 offset:44032\nbuffer_load_dwordx4 v244,s[40:43],s69 offen offset:1024 lds\nM v[168:171],a[32:35],a[160:163],v[232:235]\nds_read_b128 a[112:115],v241 offset:45056\ns_cbranch_vccnz .Lps2\n" \
".Lpb2:\nM v[168:171],a[36:39],a[164:167],v[168:171]\nds_read_b128 a[116:119],v241 offset:46080\nv_exp_f32 v130,v130\nM v[168:171],a[40:43],a[168:171],v[168:171]\nv_exp_f32 v131,v131\nM v[168:171],a[44:47],a[172:175],v[168:171]\nds_read_b128 a[120:123],v241 offset:47104\n" \
"buffer_load_dwordx4 v245,s[40:43],s69 offen offset:2048 lds\nM v[172:175],a[32:35],a[176:179],v[236:239]\nds_read_b128 a[124:127],v241 offset:48128\nv_exp_f32 v132,v132\nM v[172:175],a[36:39],a[180:183],v[172:175]\nds_read_b128 a[0:3],v240 offset:0\nds_read_b128 a[4:7],v240 offset:1024\nM v[172:175],a[40:43],a[184:187],v[172:175]\n" \
"ds_read_b128 a[8:11],v240 offset:2048\nv_exp_f32 v133,v133\nM v[172:175],a[44:47],a[188:191],v[172:175]\nbuffer_load_dwordx4 v246,s[40:43],s69 offen offset:3072 lds\nds_read_b128 a[12:15],v240 offset:3072\nM v[184:187],a[48:51],a[160:163],v[232:235]\ns_lshl4_add_u32 s69,s13,s68\nds_read_b128 a[16:19],v240 offset:4096\n" \
"M v[184:187],a[52:55],a[164:167],v[184:187]\ns_add_u32 s69,s69,0x10000\ns_add_u32 m0,s64,147456\nds_read_b128 a[20:23],v240 offset:5120\nM v[184:187],a[56:59],a[168:171],v[184:187]\nds_read_b128 a[24:27],v240 offset:6144\nv_exp_f32 v134,v134\nM v[184:187],a[60:63],a[172:175],v[184:187]\n" \
"buffer_load_dwordx4 v243,s[40:43],s69 offen offset:0 lds\nM v[188:191],a[48:51],a[176:179],v[236:239]\nds_read_b128 a[28:31],v240 offset:7168\nv_exp_f32 v135,v135\nM v[188:191],a[52:55],a[180:183],v[188:191]\nds_read_b128 a[32:35],v240 offset:8192\nds_read_b128 a[36:39],v240 offset:9216\nM v[188:191],a[56:59],a[184:187],v[188:191]\n" \
"ds_read_b128 a[40:43],v240 offset:10240\nv_exp_f32 v144,v144\nM v[188:191],a[60:63],a[188:191],v[188:191]\nbuffer_load_dwordx4 v244,s[40:43],s69 offen offset:1024 lds\nds_read_b128 a[44:47],v240 offset:11264\nM v[0:3],a[64:67],v[192:195],v[0:3]\nv_exp_f32 v145,v145\nM v[4:7],a[64:67],v[196:199],v[4:7]\n" \
"ds_read_b128 a[48:51],v240 offset:12288\nv_exp_f32 v146,v146\nM v[64:67],a[64:67],v[200:203],0\nds_read_b128 a[52:55],v240 offset:13312\nv_exp_f32 v147,v147\nM v[68:71],a[64:67],v[204:207],0\nds_read_b128 a[56:59],v240 offset:14336\ns_waitcnt lgkmcnt(8)\n" \
"M v[8:11],a[68:71],v[192:195],v[8:11]\nbuffer_load_dwordx4 v245,s[40:43],s69 offen offset:2048 lds\nds_read_b128 a[60:63],v240 offset:15360\nM v[12:15],a[68:71],v[196:199],v[12:15]\nv_exp_f32 v148,v148\nM v[72:75],a[68:71],v[200:203],0\nv_exp_f32 v149,v149\nM v[76:79],a[68:71],v[204:207],0\n" \
"v_exp_f32 v150,v150\nM v[16:19],a[72:75],v[192:195],v[16:19]\nbuffer_load_dwordx4 v246,s[40:43],s69 offen offset:3072 lds\nM v[20:23],a[72:75],v[196:199],v[20:23]\ns_add_u32 m0,s64,65536\nv_exp_f32 v151,v151\nM v[80:83],a[72:75],v[200:203],0\nv_exp_f32 v136,v136\n" \
"M v[84:87],a[72:75],v[204:207],0\nv_exp_f32 v137,v137\nM v[24:27],a[76:79],v[192:195],v[24:27]\nbuffer_load_dwordx4 v240,s[48:51],s36 offen offset:0 sc1 lds\nM v[28:31],a[76:79],v[196:199],v[28:31]\nv_exp_f32 v138,v138\nM v[88:91],a[76:79],v[200:203],0\nv_exp_f32 v139,v139\n" \
"M v[92:95],a[76:79],v[204:207],0\nv_exp_f32 v140,v140\nM v[32:35],a[80:83],v[192:195],v[32:35]\nbuffer_load_dwordx4 v240,s[48:51],s36 offen offset:1024 sc1 lds\nM v[36:39],a[80:83],v[196:199],v[36:39]\nv_exp_f32 v141,v141\nM v[96:99],a[80:83],v[200:203],0\nv_exp_f32 v142,v142\n" \
"M v[100:103],a[80:83],v[204:207],0\nv_exp_f32 v143,v143\nM v[40:43],a[84:87],v[192:195],v[40:43]\nbuffer_load_dwordx4 v240,s[48:51],s36 offen offset:2048 sc1 lds\nM v[44:47],a[84:87],v[196:199],v[44:47]\nv_exp_f32 v152,v152\nM v[104:107],a[84:87],v[200:203],0\nv_exp_f32 v153,v153\n" \
"M v[108:111],a[84:87],v[204:207],0\nv_exp_f32 v154,v154\nM v[48:51],a[88:91],v[192:195],v[48:51]\nbuffer_load_dwordx4 v240,s[48:51],s36 offen offset:3072 sc1 lds\ns_add_u32 m0,s64,16384\nM v[52:55],a[88:91],v[196:199],v[52:55]\nv_exp_f32 v155,v155\nM v[112:115],a[88:91],v[200:203],0\n" \
"v_exp_f32 v156,v156\nM v[116:119],a[88:91],v[204:207],0\nv_exp_f32 v157,v157\nM v[56:59],a[92:95],v[192:195],v[56:59]\nbuffer_load_dwordx4 v240,s[44:47],s65 offen offset:0 sc1 lds\nM v[60:63],a[92:95],v[196:199],v[60:63]\nv_exp_f32 v158,v158\nM v[120:123],a[92:95],v[200:203],0\n" \
"v_exp_f32 v159,v159\nM v[124:127],a[92:95],v[204:207],0\nv_exp_f32 v160,v160\nM a[192:195],a[208:211],v[192:195],a[192:195]\nbuffer_load_dwordx4 v240,s[44:47],s65 offen offset:1024 sc1 lds\nv_cvt_pk_bf16_f32 v192,v128,v129\nM a[196:199],a[208:211],v[196:199],a[196:199]\nv_exp_f32 v161,v161\n" \
"M a[200:203],a[208:211],v[200:203],0\nv_exp_f32 v162,v162\nv_cvt_pk_bf16_f32 v193,v130,v131\nM a[204:207],a[208:211],v[204:207],0\nv_exp_f32 v163,v163\nv_cvt_pk_bf16_f32 v194,v144,v145\nM v[0:3],a[96:99],v[208:211],v[0:3]\nbuffer_load_dwordx4 v240,s[44:47],s65 offen offset:2048 sc1 lds\n" \
"M v[4:7],a[96:99],v[212:215],v[4:7]\nv_exp_f32 v164,v164\nv_cvt_pk_bf16_f32 v195,v146,v147\nM v[64:67],a[96:99],v[216:219],v[64:67]\nv_exp_f32 v165,v165\nv_cvt_pk_bf16_f32 v196,v132,v133\nM v[68:71],a[96:99],v[220:223],v[68:71]\nv_exp_f32 v166,v166\n" \
"M v[8:11],a[100:103],v[208:211],v[8:11]\nbuffer_load_dwordx4 v240,s[44:47],s65 offen offset:3072 sc1 lds\ns_add_u32 m0,s64,81920\nM v[12:15],a[100:103],v[212:215],v[12:15]\nv_exp_f32 v167,v167\nv_cvt_pk_bf16_f32 v197,v134,v135\nM v[72:75],a[100:103],v[216:219],v[72:75]\ns_barrier\n" \
"v_cvt_pk_bf16_f32 v198,v148,v149\nM v[76:79],a[100:103],v[220:223],v[76:79]\nv_exp_f32 v176,v176\nv_cvt_pk_bf16_f32 v199,v150,v151\nM v[16:19],a[104:107],v[208:211],v[16:19]\nbuffer_load_dwordx4 v240,s[48:51],s65 offen offset:0 sc1 lds\nM v[20:23],a[104:107],v[212:215],v[20:23]\nv_exp_f32 v177,v177\n" \
"v_cvt_pk_bf16_f32 v200,v136,v137\nM v[80:83],a[104:107],v[216:219],v[80:83]\nv_exp_f32 v178,v178\nv_cvt_pk_bf16_f32 v201,v138,v139\nM v[84:87],a[104:107],v[220:223],v[84:87]\nv_exp_f32 v179,v179\nM v[24:27],a[108:111],v[208:211],v[24:27]\nbuffer_load_dwordx4 v240,s[48:51],s65 offen offset:1024 sc1 lds\n" \
"v_cvt_pk_bf16_f32 v202,v152,v153\nM v[28:31],a[108:111],v[212:215],v[28:31]\nv_exp_f32 v180,v180\nv_cvt_pk_bf16_f32 v203,v154,v155\nM v[88:91],a[108:111],v[216:219],v[88:91]\nv_exp_f32 v181,v181\nM v[92:95],a[108:111],v[220:223],v[92:95]\nv_exp_f32 v182,v182\n" \
"v_cvt_pk_bf16_f32 v204,v140,v141\nM v[32:35],a[112:115],v[208:211],v[32:35]\nbuffer_load_dwordx4 v240,s[48:51],s65 offen offset:2048 sc1 lds\nv_cvt_pk_bf16_f32 v205,v142,v143\nM v[36:39],a[112:115],v[212:215],v[36:39]\nv_exp_f32 v183,v183\nM v[96:99],a[112:115],v[216:219],v[96:99]\nv_exp_f32 v168,v168\n" \
"v_cvt_pk_bf16_f32 v206,v156,v157\nM v[100:103],a[112:115],v[220:223],v[100:103]\nv_exp_f32 v169,v169\nM v[40:43],a[116:119],v[208:211],v[40:43]\nbuffer_load_dwordx4 v240,s[48:51],s65 offen offset:3072 sc1 lds\ns_add_u32 m0,s64,32768\nM v[44:47],a[116:119],v[212:215],v[44:47]\nv_exp_f32 v170,v170\n" \
"v_cvt_pk_bf16_f32 v207,v158,v159\nM v[104:107],a[116:119],v[216:219],v[104:107]\nv_exp_f32 v171,v171\nM v[108:111],a[116:119],v[220:223],v[108:111]\nM v[48:51],a[120:123],v[208:211],v[48:51]\nbuffer_load_dwordx4 v240,s[44:47],s66 offen offset:0 sc1 lds\nM v[52:55],a[120:123],v[212:215],v[52:55]\nM v[112:115],a[120:123],v[216:219],v[112:115]\n" \
"M v[116:119],a[120:123],v[220:223],v[116:119]\nM v[56:59],a[124:127],v[208:211],v[56:59]\nbuffer_load_dwordx4 v240,s[44:47],s66 offen offset:1024 sc1 lds\nM v[60:63],a[124:127],v[212:215],v[60:63]\nM v[120:123],a[124:127],v[216:219],v[120:123]\nM v[124:127],a[124:127],v[220:223],v[124:127]\nM a[192:195],a[208:211],v[208:211],a[192:195]\nbuffer_load_dwordx4 v240,s[44:47],s66 offen offset:2048 sc1 lds\n" \
"M a[196:199],a[208:211],v[212:215],a[196:199]\nbuffer_load_dwordx4 v240,s[44:47],s66 offen offset:3072 sc1 lds\nM a[200:203],a[208:211],v[216:219],a[200:203]\ns_add_u32 m0,s64,98304\ns_nop 0\nbuffer_load_dwordx4 v240,s[48:51],s66 offen offset:0 sc1 lds\nbuffer_load_dwordx4 v240,s[48:51],s66 offen offset:1024 sc1 lds\nbuffer_load_dwordx4 v240,s[48:51],s66 offen offset:2048 sc1 lds\n" \
"buffer_load_dwordx4 v240,s[48:51],s66 offen offset:3072 sc1 lds\nM a[204:207],a[208:211],v[220:223],a[204:207]\ns_cmp_eq_u32 s62,0\ns_cbranch_scc1 .Ltrans\ns_branch .Ldual\n.Lpro1:\ns_barrier\nM v[64:67],a[0:3],a[128:131],0\n" \
"ds_read_b128 a[64:67],v241 offset:0\nds_read_b128 a[68:71],v241 offset:1024\nM v[64:67],a[4:7],a[132:135],v[64:67]\nds_read_b128 a[72:75],v241 offset:2048\ns_mov_b32 m0,s64\nv_mov_b32 v202,0\nM v[64:67],a[8:11],a[136:139],v[64:67]\nds_read_b128 a[76:79],v241 offset:3072\n" \
"buffer_load_dwordx4 v240,s[44:47],s36 offen offset:0 sc1 lds\nM v[64:67],a[12:15],a[140:143],v[64:67]\nds_read_b128 a[80:83],v241 offset:4096\nv_mov_b32 v203,0\nM v[68:71],a[0:3],a[144:147],0\nds_read_b128 a[0:3],v240 offset:32768\nM v[68:71],a[4:7],a[148:151],v[68:71]\nds_read_b128 a[4:7],v240 offset:33792\n" \
"M v[68:71],a[8:11],a[152:155],v[68:71]\nds_read_b128 a[8:11],v240 offset:34816\nbuffer_load_dwordx4 v240,s[44:47],s36 offen offset:1024 sc1 lds\nM v[68:71],a[12:15],a[156:159],v[68:71]\nds_read_b128 a[12:15],v240 offset:35840\nv_max3_f32 v225,v64,v65,v66\nM v[72:75],a[16:19],a[128:131],0\nds_read_b128 a[84:87],v241 offset:5120\n" \
"s_lshl4_add_u32 s69,s13,s68\nM v[72:75],a[20:23],a[132:135],v[72:75]\nds_read_b128 a[88:91],v241 offset:6144\nM v[72:75],a[24:27],a[136:139],v[72:75]\nv_max3_f32 v229,v68,v69,v70\nbuffer_load_dwordx4 v240,s[44:47],s36 offen offset:2048 sc1 lds\nM v[72:75],a[28:31],a[140:143],v[72:75]\nds_read_b128 a[92:95],v241 offset:7168\n" \
"ds_read_b128 a[96:99],v241 offset:8192\nM v[76:79],a[16:19],a[144:147],0\nds_read_b128 a[16:19],v240 offset:36864\nM v[76:79],a[20:23],a[148:151],v[76:79]\nds_read_b128 a[20:23],v240 offset:37888\nM v[76:79],a[24:27],a[152:155],v[76:79]\nds_read_b128 a[24:27],v240 offset:38912\nv_max3_f32 v225,v225,v67,v72\n" \
"v_max3_f32 v225,v225,v73,v74\nM v[76:79],a[28:31],a[156:159],v[76:79]\nds_read_b128 a[28:31],v240 offset:39936\nv_max_f32 v225,v225,v75\nM v[80:83],a[32:35],a[128:131],0\nv_cndmask_b32 v225,v224,v225,s[90:91]\nbuffer_load_dwordx4 v240,s[44:47],s36 offen offset:3072 sc1 lds\nM v[80:83],a[36:39],a[132:135],v[80:83]\n" \
"ds_read_b128 a[100:103],v241 offset:9216\nds_read_b128 a[104:107],v241 offset:10240\nv_max3_f32 v229,v229,v71,v76\nM v[80:83],a[40:43],a[136:139],v[80:83]\nv_max3_f32 v229,v229,v77,v78\nv_max_f32 v229,v229,v79\nM v[80:83],a[44:47],a[140:143],v[80:83]\nv_cndmask_b32 v229,v228,v229,s[90:91]\n" \
"ds_read_b128 a[108:111],v241 offset:11264\nds_read_b128 a[112:115],v241 offset:12288\nM v[84:87],a[32:35],a[144:147],0\nv_permlane16_swap_b32 v225,v229\nv_max_f32 v225,v225,v229\nv_mov_b32 v229,v225\nM v[84:87],a[36:39],a[148:151],v[84:87]\nds_read_b128 a[32:35],v240 offset:40960\n" \
"v_permlane32_swap_b32 v225,v229\nv_max_f32 v225,v225,v229\nM v[84:87],a[40:43],a[152:155],v[84:87]\nv_mov_b32 v229,v225\nds_read_b128 a[36:39],v240 offset:41984\nM v[84:87],a[44:47],a[156:159],v[84:87]\nv_permlane16_swap_b32 v225,v229\nv_sub_f32 v224,0,v225\n" \
"v_sub_f32 v226,0,v225\nM v[92:95],a[48:51],a[144:147],0\nv_sub_f32 v227,0,v225\nv_sub_f32 v225,0,v225\nv_sub_f32 v228,0,v229\nM v[92:95],a[52:55],a[148:151],v[92:95]\nv_sub_f32 v230,0,v229\nv_sub_f32 v231,0,v229\n" \
"v_sub_f32 v229,0,v229\nM v[92:95],a[56:59],a[152:155],v[92:95]\nds_read_b128 a[40:43],v240 offset:43008\nds_read_b128 a[44:47],v240 offset:44032\nM v[92:95],a[60:63],a[156:159],v[92:95]\nds_read_b128 a[48:51],v240 offset:45056\nv_add_f32 v64,v64,v224\ns_waitcnt lgkmcnt(7)\n" \
"M v[136:139],a[0:3],a[160:163],0\nds_read_b128 a[52:55],v240 offset:46080\nv_add_f32 v65,v65,v224\nv_add_f32 v66,v66,v224\nM v[136:139],a[4:7],a[164:167],v[136:139]\nds_read_b128 a[56:59],v240 offset:47104\nv_add_f32 v67,v67,v224\nv_add_f32 v68,v68,v228\n" \
"M v[136:139],a[8:11],a[168:171],v[136:139]\nds_read_b128 a[60:63],v240 offset:48128\nv_add_f32 v69,v69,v228\nM v[136:139],a[12:15],a[172:175],v[136:139]\nv_exp_f32 v64,v64\nv_add_f32 v72,v72,v224\nM v[140:143],a[0:3],a[176:179],0\nv_exp_f32 v65,v65\n" \
"v_add_f32 v73,v73,v224\nM v[140:143],a[4:7],a[180:183],v[140:143]\nv_exp_f32 v66,v66\nM v[140:143],a[8:11],a[184:187],v[140:143]\nv_exp_f32 v67,v67\nv_add_f32 v74,v74,v224\nM v[140:143],a[12:15],a[188:191],v[140:143]\nv_add_f32 v75,v75,v224\n" \
"v_add_f32 v70,v70,v228\nv_add_f32 v71,v71,v228\nM v[156:159],a[16:19],a[176:179],0\nv_exp_f32 v68,v68\nv_cvt_pk_bf16_f32 v64,v64,v65\nM v[156:159],a[20:23],a[180:183],v[156:159]\nv_exp_f32 v69,v69\nM v[156:159],a[24:27],a[184:187],v[156:159]\n" \
"v_exp_f32 v72,v72\nv_add_f32 v76,v76,v228\nM v[156:159],a[28:31],a[188:191],v[156:159]\nv_exp_f32 v73,v73\nv_add_f32 v77,v77,v228\nM v[128:131],a[0:3],a[128:131],v[224:227]\nv_exp_f32 v74,v74\nv_add_f32 v78,v78,v228\n" \
"M v[128:131],a[4:7],a[132:135],v[128:131]\nv_exp_f32 v75,v75\nM v[128:131],a[8:11],a[136:139],v[128:131]\nv_exp_f32 v70,v70\nv_add_f32 v79,v79,v228\nM v[128:131],a[12:15],a[140:143],v[128:131]\nv_exp_f32 v71,v71\nv_cvt_pk_bf16_f32 v65,v66,v67\n" \
"M v[132:135],a[0:3],a[144:147],v[228:231]\nv_exp_f32 v76,v76\nv_cvt_pk_bf16_f32 v66,v72,v73\nM v[132:135],a[4:7],a[148:151],v[132:135]\nv_exp_f32 v77,v77\nM v[132:135],a[8:11],a[152:155],v[132:135]\nv_exp_f32 v78,v78\nv_cvt_pk_bf16_f32 v67,v74,v75\n" \
"M v[132:135],a[12:15],a[156:159],v[132:135]\nv_exp_f32 v79,v79\nv_cvt_pk_bf16_f32 v68,v68,v69\nM v[144:147],a[16:19],a[128:131],v[224:227]\nv_cvt_pk_bf16_f32 v69,v70,v71\nv_cvt_pk_bf16_f32 v70,v76,v77\nM v[144:147],a[20:23],a[132:135],v[144:147]\nv_cvt_pk_bf16_f32 v71,v78,v79\n" \
"v_cndmask_b32 v80,v80,v252,s[82:83]\nv_cndmask_b32 v81,v81,v252,s[84:85]\nM v[144:147],a[24:27],a[136:139],v[144:147]\nv_cndmask_b32 v82,v82,v252,s[86:87]\nv_cndmask_b32 v83,v83,v252,s[88:89]\nv_add_f32 v80,v80,v224\nM v[144:147],a[28:31],a[140:143],v[144:147]\nv_add_f32 v81,v81,v224\n" \
"v_add_f32 v82,v82,v224\nv_add_f32 v83,v83,v224\nM v[148:151],a[16:19],a[144:147],v[228:231]\nv_exp_f32 v80,v80\nM v[148:151],a[20:23],a[148:151],v[148:151]\nv_exp_f32 v81,v81\nv_add_f32 v84,v84,v228\nM v[148:151],a[24:27],a[152:155],v[148:151]\n" \
"v_exp_f32 v82,v82\nv_add_f32 v85,v85,v228\nM v[148:151],a[28:31],a[156:159],v[148:151]\nv_exp_f32 v83,v83\ns_waitcnt lgkmcnt(0)\nM v[160:163],a[32:35],a[128:131],v[224:227]\nv_cndmask_b32 v92,v92,v252,s[82:83]\nv_cndmask_b32 v93,v93,v252,s[84:85]\n" \
"M v[160:163],a[36:39],a[132:135],v[160:163]\nv_cndmask_b32 v94,v94,v252,s[86:87]\nv_cndmask_b32 v95,v95,v252,s[88:89]\nv_add_f32 v86,v86,v228\nM v[160:163],a[40:43],a[136:139],v[160:163]\nv_add_f32 v87,v87,v228\nv_exp_f32 v84,v84\nM v[160:163],a[44:47],a[140:143],v[160:163]\n" \
"v_exp_f32 v85,v85\nv_cvt_pk_bf16_f32 v80,v80,v81\nM v[164:167],a[32:35],a[144:147],v[228:231]\nv_exp_f32 v86,v86\nM v[164:167],a[36:39],a[148:151],v[164:167]\nv_exp_f32 v87,v87\nv_add_f32 v92,v92,v228\nM v[164:167],a[40:43],a[152:155],v[164:167]\n" \
"v_add_f32 v93,v93,v228\nv_add_f32 v94,v94,v228\nv_add_f32 v95,v95,v228\nM v[164:167],a[44:47],a[156:159],v[164:167]\nv_cvt_pk_bf16_f32 v81,v82,v83\nv_mov_b32 v82,0\nM v[176:179],a[48:51],a[128:131],v[224:227]\nv_exp_f32 v92,v92\n" \
"v_mov_b32 v83,0\nM v[176:179],a[52:55],a[132:135],v[176:179]\nv_exp_f32 v93,v93\nv_cvt_pk_bf16_f32 v84,v84,v85\nM v[176:179],a[56:59],a[136:139],v[176:179]\nv_exp_f32 v94,v94\nv_cvt_pk_bf16_f32 v85,v86,v87\nM v[176:179],a[60:63],a[140:143],v[176:179]\n" \
"v_exp_f32 v95,v95\nM v[180:183],a[48:51],a[144:147],v[228:231]\nv_cvt_pk_bf16_f32 v86,v92,v93\nv_cvt_pk_bf16_f32 v87,v94,v95\nds_read_b128 a[116:119],v241 offset:13312\nM v[180:183],a[52:55],a[148:151],v[180:183]\nds_read_b128 a[120:123],v241 offset:14336\nds_read_b128 a[124:127],v241 offset:15360\n" \
"v_max3_f32 v237,v140,v141,v142\nM v[180:183],a[56:59],a[152:155],v[180:183]\nds_read_b128 a[0:3],v240 offset:49152\nv_exp_f32 v128,v128\nM v[180:183],a[60:63],a[156:159],v[180:183]\nds_read_b128 a[4:7],v240 offset:50176\nv_max3_f32 v237,v237,v143,v156\nM v[0:3],a[64:67],v[64:67],0\n" \
"ds_read_b128 a[8:11],v240 offset:51200\nv_exp_f32 v129,v129\nM v[4:7],a[64:67],v[68:71],0\nds_read_b128 a[12:15],v240 offset:52224\nv_exp_f32 v130,v130\nM v[8:11],a[68:71],v[64:67],0\nbuffer_load_dword v248,v251,s[52:55],s80 offen sc1\nds_read_b128 a[16:19],v240 offset:53248\n" \
"M v[12:15],a[68:71],v[68:71],0\nv_exp_f32 v131,v131\nM v[16:19],a[72:75],v[64:67],0\nds_read_b128 a[20:23],v240 offset:54272\nv_cvt_pk_bf16_f32 v192,v128,v129\nv_cvt_pk_bf16_f32 v193,v130,v131\nM v[20:23],a[72:75],v[68:71],0\nds_read_b128 a[24:27],v240 offset:55296\n" \
"ds_read_b128 a[28:31],v240 offset:56320\nv_max3_f32 v233,v136,v137,v138\nM v[24:27],a[76:79],v[64:67],0\nv_exp_f32 v132,v132\nM v[28:31],a[76:79],v[68:71],0\nv_exp_f32 v133,v133\nv_max3_f32 v237,v237,v157,v158\nM v[32:35],a[80:83],v[64:67],0\n" \
"v_exp_f32 v134,v134\nv_cvt_pk_bf16_f32 v196,v132,v133\nM v[36:39],a[80:83],v[68:71],0\nv_exp_f32 v135,v135\nv_max_f32 v233,v233,v139\nM v[40:43],a[84:87],v[64:67],0\nv_cvt_pk_bf16_f32 v197,v134,v135\nv_max_f32 v237,v237,v159\n" \
"M v[44:47],a[84:87],v[68:71],0\nv_exp_f32 v144,v144\nv_cndmask_b32 v233,v232,v233,s[90:91]\nM v[48:51],a[88:91],v[64:67],0\nv_exp_f32 v145,v145\nv_cndmask_b32 v237,v236,v237,s[90:91]\nM v[52:55],a[88:91],v[68:71],0\nv_exp_f32 v146,v146\n" \
"v_cvt_pk_bf16_f32 v194,v144,v145\nM v[56:59],a[92:95],v[64:67],0\nv_exp_f32 v147,v147\nM v[60:63],a[92:95],v[68:71],0\nv_cvt_pk_bf16_f32 v195,v146,v147\nv_permlane16_swap_b32 v233,v237\nv_max_f32 v233,v233,v237\nM a[192:195],a[208:211],v[64:67],0\n" \
"v_exp_f32 v148,v148\nv_mov_b32 v237,v233\nM a[196:199],a[208:211],v[68:71],0\nv_exp_f32 v149,v149\nv_permlane32_swap_b32 v233,v237\nM v[0:3],a[96:99],v[80:83],v[0:3]\nv_exp_f32 v150,v150\nM v[4:7],a[96:99],v[84:87],v[4:7]\n" \
"v_exp_f32 v151,v151\nv_cvt_pk_bf16_f32 v198,v148,v149\nM v[8:11],a[100:103],v[80:83],v[8:11]\nv_cvt_pk_bf16_f32 v199,v150,v151\nv_max_f32 v233,v233,v237\nv_mov_b32 v237,v233\nM v[12:15],a[100:103],v[84:87],v[12:15]\nv_cndmask_b32 v136,v136,v252,s[82:83]\n" \
"v_permlane16_swap_b32 v233,v237\nM v[16:19],a[104:107],v[80:83],v[16:19]\nv_cndmask_b32 v137,v137,v252,s[84:85]\nv_cndmask_b32 v138,v138,v252,s[86:87]\nv_cndmask_b32 v139,v139,v252,s[88:89]\nM v[20:23],a[104:107],v[84:87],v[20:23]\nv_sub_f32 v232,0,v233\nv_sub_f32 v234,0,v233\n" \
"v_sub_f32 v235,0,v233\nM v[24:27],a[108:111],v[80:83],v[24:27]\nv_add_f32 v136,v136,v232\nv_add_f32 v137,v137,v232\nv_add_f32 v138,v138,v232\nM v[28:31],a[108:111],v[84:87],v[28:31]\nv_add_f32 v139,v139,v232\nv_sub_f32 v233,0,v233\n" \
"M v[32:35],a[112:115],v[80:83],v[32:35]\nv_exp_f32 v136,v136\nv_sub_f32 v236,0,v237\nM v[36:39],a[112:115],v[84:87],v[36:39]\nv_exp_f32 v137,v137\ns_waitcnt lgkmcnt(0)\nM v[40:43],a[116:119],v[80:83],v[40:43]\nv_exp_f32 v138,v138\n" \
"v_cvt_pk_bf16_f32 v200,v136,v137\nM v[44:47],a[116:119],v[84:87],v[44:47]\nv_exp_f32 v139,v139\nM v[48:51],a[120:123],v[80:83],v[48:51]\nv_cvt_pk_bf16_f32 v201,v138,v139\nv_sub_f32 v238,0,v237\nv_sub_f32 v239,0,v237\nM v[52:55],a[120:123],v[84:87],v[52:55]\n" \
"v_add_f32 v140,v140,v236\nv_add_f32 v141,v141,v236\nv_add_f32 v142,v142,v236\nM v[56:59],a[124:127],v[80:83],v[56:59]\nv_add_f32 v143,v143,v236\nv_sub_f32 v237,0,v237\nds_read_b128 a[32:35],v240 offset:57344\nM v[60:63],a[124:127],v[84:87],v[60:63]\n" \
"v_exp_f32 v140,v140\nM a[192:195],a[208:211],v[80:83],a[192:195]\nv_exp_f32 v141,v141\nds_read_b128 a[36:39],v240 offset:58368\nM a[196:199],a[208:211],v[84:87],a[196:199]\nv_exp_f32 v142,v142\nv_cvt_pk_bf16_f32 v204,v140,v141\nM v[128:131],a[0:3],a[128:131],v[224:227]\n" \
"v_exp_f32 v143,v143\nM v[128:131],a[4:7],a[132:135],v[128:131]\nv_cvt_pk_bf16_f32 v205,v142,v143\nds_read_b128 a[40:43],v240 offset:59392\nds_read_b128 a[44:47],v240 offset:60416\nM v[128:131],a[8:11],a[136:139],v[128:131]\nv_cndmask_b32 v156,v156,v252,s[82:83]\nv_cndmask_b32 v157,v157,v252,s[84:85]\n" \
"v_cndmask_b32 v158,v158,v252,s[86:87]\nM v[128:131],a[12:15],a[140:143],v[128:131]\nv_cndmask_b32 v159,v159,v252,s[88:89]\nv_add_f32 v156,v156,v236\nv_add_f32 v157,v157,v236\nM v[132:135],a[0:3],a[144:147],v[228:231]\nv_add_f32 v158,v158,v236\nv_add_f32 v159,v159,v236\n" \
"M v[132:135],a[4:7],a[148:151],v[132:135]\nv_exp_f32 v156,v156\nds_read_b128 a[48:51],v240 offset:61440\nM v[132:135],a[8:11],a[152:155],v[132:135]\nv_exp_f32 v157,v157\nds_read_b128 a[52:55],v240 offset:62464\nM v[132:135],a[12:15],a[156:159],v[132:135]\nv_exp_f32 v158,v158\n" \
"v_cvt_pk_bf16_f32 v206,v156,v157\nM v[144:147],a[16:19],a[128:131],v[224:227]\nv_exp_f32 v159,v159\nM v[144:147],a[20:23],a[132:135],v[144:147]\nv_cvt_pk_bf16_f32 v207,v158,v159\nv_exp_f32 v160,v160\nM v[144:147],a[24:27],a[136:139],v[144:147]\nv_exp_f32 v161,v161\n" \
"ds_read_b128 a[56:59],v240 offset:63488\nM v[144:147],a[28:31],a[140:143],v[144:147]\nv_exp_f32 v162,v162\nv_cvt_pk_bf16_f32 v208,v160,v161\nM v[148:151],a[16:19],a[144:147],v[228:231]\nv_exp_f32 v163,v163\nM v[148:151],a[20:23],a[148:151],v[148:151]\nv_cvt_pk_bf16_f32 v209,v162,v163\n" \
"ds_read_b128 a[60:63],v240 offset:64512\nds_read_b128 a[64:67],v241 offset:32768\nM v[148:151],a[24:27],a[152:155],v[148:151]\nv_exp_f32 v164,v164\nds_read_b128 a[68:71],v241 offset:33792\nM v[148:151],a[28:31],a[156:159],v[148:151]\nv_exp_f32 v165,v165\nM v[136:139],a[0:3],a[160:163],v[232:235]\n" \
"s_waitcnt vmcnt(1)\ns_barrier\nv_cvt_pk_bf16_f32 v212,v164,v165\nM v[136:139],a[4:7],a[164:167],v[136:139]\nv_exp_f32 v166,v166\nds_read_b128 a[72:75],v241 offset:34816\nM v[136:139],a[8:11],a[168:171],v[136:139]\nv_exp_f32 v167,v167\n" \
"ds_read_b128 a[76:79],v241 offset:35840\nM v[136:139],a[12:15],a[172:175],v[136:139]\nv_cvt_pk_bf16_f32 v213,v166,v167\nds_read_b128 a[80:83],v241 offset:36864\nM v[140:143],a[0:3],a[176:179],v[236:239]\nv_exp_f32 v176,v176\nds_read_b128 a[84:87],v241 offset:37888\nM v[140:143],a[4:7],a[180:183],v[140:143]\n" \
"v_exp_f32 v177,v177\nds_read_b128 a[88:91],v241 offset:38912\nM v[140:143],a[8:11],a[184:187],v[140:143]\nv_exp_f32 v178,v178\nv_cvt_pk_bf16_f32 v210,v176,v177\nM v[140:143],a[12:15],a[188:191],v[140:143]\nv_exp_f32 v179,v179\nM v[152:155],a[16:19],a[160:163],v[232:235]\n" \
"v_cvt_pk_bf16_f32 v211,v178,v179\nv_exp_f32 v180,v180\nM v[152:155],a[20:23],a[164:167],v[152:155]\nv_exp_f32 v181,v181\nds_read_b128 a[92:95],v241 offset:39936\nM v[152:155],a[24:27],a[168:171],v[152:155]\nv_exp_f32 v182,v182\nv_cvt_pk_bf16_f32 v214,v180,v181\n" \
"M v[152:155],a[28:31],a[172:175],v[152:155]\nv_exp_f32 v183,v183\nM v[156:159],a[16:19],a[176:179],v[236:239]\nv_cvt_pk_bf16_f32 v215,v182,v183\nds_read_b128 a[96:99],v241 offset:40960\nds_read_b128 a[100:103],v241 offset:41984\nM v[156:159],a[20:23],a[180:183],v[156:159]\nds_read_b128 a[104:107],v241 offset:43008\n" \
"s_add_u32 m0,s64,131072\ns_waitcnt vmcnt(0)\nv_cmp_eq_u32 vcc,0,v248\nM v[156:159],a[24:27],a[184:187],v[156:159]\nds_read_b128 a[108:111],v241 offset:44032\ns_cbranch_vccnz .Lps3\n.Lpb3:\nM v[156:159],a[28:31],a[188:191],v[156:159]\n" \
"buffer_load_dwordx4 v243,s[40:43],s69 offen offset:0 lds\ns_waitcnt lgkmcnt(5)\nM v[160:163],a[32:35],a[128:131],v[224:227]\nds_read_b128 a[112:115],v241 offset:45056\nv_exp_f32 v128,v128\nM v[160:163],a[36:39],a[132:135],v[160:163]\nds_read_b128 a[116:119],v241 offset:46080\nv_exp_f32 v129,v129\n" \
"M v[160:163],a[40:43],a[136:139],v[160:163]\nds_read_b128 a[120:123],v241 offset:47104\nds_read_b128 a[124:127],v241 offset:48128\nM v[160:163],a[44:47],a[140:143],v[160:163]\nds_read_b128 a[0:3],v240 offset:0\nbuffer_load_dwordx4 v244,s[40:43],s69 offen offset:1024 lds\nM v[164:167],a[32:35],a[144:147],v[228:231]\nds_read_b128 a[4:7],v240 offset:1024\n" \
"v_exp_f32 v130,v130\nM v[164:167],a[36:39],a[148:151],v[164:167]\nds_read_b128 a[8:11],v240 offset:2048\nv_exp_f32 v131,v131\nM v[164:167],a[40:43],a[152:155],v[164:167]\nv_exp_f32 v132,v132\nM v[164:167],a[44:47],a[156:159],v[164:167]\nbuffer_load_dwordx4 v245,s[40:43],s69 offen offset:2048 lds\n" \
"ds_read_b128 a[12:15],v240 offset:3072\nM v[176:179],a[48:51],a[128:131],v[224:227]\nds_read_b128 a[16:19],v240 offset:4096\nv_exp_f32 v133,v133\nM v[176:179],a[52:55],a[132:135],v[176:179]\nds_read_b128 a[20:23],v240 offset:5120\nv_exp_f32 v134,v134\nM v[176:179],a[56:59],a[136:139],v[176:179]\n" \
"ds_read_b128 a[24:27],v240 offset:6144\nds_read_b128 a[28:31],v240 offset:7168\nM v[176:179],a[60:63],a[140:143],v[176:179]\nbuffer_load_dwordx4 v246,s[40:43],s69 offen offset:3072 lds\ns_lshl4_add_u32 s69,s13,s68\nM v[180:183],a[48:51],a[144:147],v[228:231]\ns_add_u32 s69,s69,0x10000\nv_exp_f32 v135,v135\n" \
"M v[180:183],a[52:55],a[148:151],v[180:183]\nv_exp_f32 v144,v144\nM v[180:183],a[56:59],a[152:155],v[180:183]\ns_add_u32 m0,s64,147456\nv_exp_f32 v145,v145\nM v[180:183],a[60:63],a[156:159],v[180:183]\nbuffer_load_dwordx4 v243,s[40:43],s69 offen offset:0 lds\nM v[168:171],a[32:35],a[160:163],v[232:235]\n" \
"v_exp_f32 v146,v146\nM v[168:171],a[36:39],a[164:167],v[168:171]\nv_exp_f32 v147,v147\nM v[168:171],a[40:43],a[168:171],v[168:171]\nv_exp_f32 v148,v148\nM v[168:171],a[44:47],a[172:175],v[168:171]\nbuffer_load_dwordx4 v244,s[40:43],s69 offen offset:1024 lds\nM v[172:175],a[32:35],a[176:179],v[236:239]\n" \
"ds_read_b128 a[32:35],v240 offset:8192\nv_exp_f32 v149,v149\nM v[172:175],a[36:39],a[180:183],v[172:175]\nv_exp_f32 v150,v150\nM v[172:175],a[40:43],a[184:187],v[172:175]\nds_read_b128 a[36:39],v240 offset:9216\nv_exp_f32 v151,v151\nM v[172:175],a[44:47],a[188:191],v[172:175]\n" \
"buffer_load_dwordx4 v245,s[40:43],s69 offen offset:2048 lds\nds_read_b128 a[40:43],v240 offset:10240\nM v[184:187],a[48:51],a[160:163],v[232:235]\nds_read_b128 a[44:47],v240 offset:11264\nv_exp_f32 v136,v136\nM v[184:187],a[52:55],a[164:167],v[184:187]\nv_exp_f32 v137,v137\nM v[184:187],a[56:59],a[168:171],v[184:187]\n" \
"v_exp_f32 v138,v138\nM v[184:187],a[60:63],a[172:175],v[184:187]\nbuffer_load_dwordx4 v246,s[40:43],s69 offen offset:3072 lds\ns_add_u32 m0,s64,65536\nM v[188:191],a[48:51],a[176:179],v[236:239]\nv_exp_f32 v139,v139\nM v[188:191],a[52:55],a[180:183],v[188:191]\nds_read_b128 a[48:51],v240 offset:12288\n" \
"v_exp_f32 v140,v140\nM v[188:191],a[56:59],a[184:187],v[188:191]\nds_read_b128 a[52:55],v240 offset:13312\nv_exp_f32 v141,v141\nM v[188:191],a[60:63],a[188:191],v[188:191]\nbuffer_load_dwordx4 v240,s[48:51],s36 offen offset:0 sc1 lds\nds_read_b128 a[56:59],v240 offset:14336\nM v[0:3],a[64:67],v[192:195],v[0:3]\n" \
"v_exp_f32 v142,v142\nM v[4:7],a[64:67],v[196:199],v[4:7]\nds_read_b128 a[60:63],v240 offset:15360\nv_exp_f32 v143,v143\nM v[64:67],a[64:67],v[200:203],0\nv_exp_f32 v152,v152\nM v[68:71],a[64:67],v[204:207],0\nbuffer_load_dwordx4 v240,s[48:51],s36 offen offset:1024 sc1 lds\n" \
"M v[8:11],a[68:71],v[192:195],v[8:11]\nv_exp_f32 v153,v153\nM v[12:15],a[68:71],v[196:199],v[12:15]\nv_exp_f32 v154,v154\nM v[72:75],a[68:71],v[200:203],0\nv_exp_f32 v155,v155\nM v[76:79],a[68:71],v[204:207],0\nbuffer_load_dwordx4 v240,s[48:51],s36 offen offset:2048 sc1 lds\n" \
"M v[16:19],a[72:75],v[192:195],v[16:19]\nv_exp_f32 v156,v156\nM v[20:23],a[72:75],v[196:199],v[20:23]\nv_exp_f32 v157,v157\nM v[80:83],a[72:75],v[200:203],0\nv_exp_f32 v158,v158\nM v[84:87],a[72:75],v[204:207],0\nbuffer_load_dwordx4 v240,s[48:51],s36 offen offset:3072 sc1 lds\n" \
"M v[24:27],a[76:79],v[192:195],v[24:27]\ns_add_u32 m0,s64,16384\nv_exp_f32 v159,v159\nM v[28:31],a[76:79],v[196:199],v[28:31]\nv_exp_f32 v160,v160\nM v[88:91],a[76:79],v[200:203],0\nv_exp_f32 v161,v161\nM v[92:95],a[76:79],v[204:207],0\n" \
"buffer_load_dwordx4 v240,s[44:47],s65 offen offset:0 sc1 lds\nM v[32:35],a[80:83],v[192:195],v[32:35]\nv_exp_f32 v162,v162\nM v[36:39],a[80:83],v[196:199],v[36:39]\nv_exp_f32 v163,v163\nM v[96:99],a[80:83],v[200:203],0\nv_exp_f32 v164,v164\nM v[100:103],a[80:83],v[204:207],0\n" \
"buffer_load_dwordx4 v240,s[44:47],s65 offen offset:1024 sc1 lds\nM v[40:43],a[84:87],v[192:195],v[40:43]\nv_exp_f32 v165,v165\nM v[44:47],a[84:87],v[196:199],v[44:47]\nv_exp_f32 v166,v166\nM v[104:107],a[84:87],v[200:203],0\nv_exp_f32 v167,v167\nM v[108:111],a[84:87],v[204:207],0\n" \
"buffer_load_dwordx4 v240,s[44:47],s65 offen offset:2048 sc1 lds\nM v[48:51],a[88:91],v[192:195],v[48:51]\ns_waitcnt lgkmcnt(0)\ns_barrier\nM v[52:55],a[88:91],v[196:199],v[52:55]\nv_exp_f32 v176,v176\nM v[112:115],a[88:91],v[200:203],0\nv_exp_f32 v177,v177\n" \
"M v[116:119],a[88:91],v[204:207],0\nbuffer_load_dwordx4 v240,s[44:47],s65 offen offset:3072 sc1 lds\ns_add_u32 m0,s64,81920\nM v[56:59],a[92:95],v[192:195],v[56:59]\nv_exp_f32 v178,v178\nM v[60:63],a[92:95],v[196:199],v[60:63]\nv_exp_f32 v179,v179\nM v[120:123],a[92:95],v[200:203],0\n" \
"v_exp_f32 v180,v180\nM v[124:127],a[92:95],v[204:207],0\nbuffer_load_dwordx4 v240,s[48:51],s65 offen offset:0 sc1 lds\nM a[192:195],a[208:211],v[192:195],a[192:195]\nv_exp_f32 v181,v181\nv_cvt_pk_bf16_f32 v192,v128,v129\nM a[196:199],a[208:211],v[196:199],a[196:199]\nv_exp_f32 v182,v182\n" \
"v_cvt_pk_bf16_f32 v193,v130,v131\nM a[200:203],a[208:211],v[200:203],0\nv_exp_f32 v183,v183\nM a[204:207],a[208:211],v[204:207],0\nbuffer_load_dwordx4 v240,s[48:51],s65 offen offset:1024 sc1 lds\nv_cvt_pk_bf16_f32 v194,v144,v145\nM v[0:3],a[96:99],v[208:211],v[0:3]\nv_exp_f32 v168,v168\n" \
"v_cvt_pk_bf16_f32 v195,v146,v147\nM v[4:7],a[96:99],v[212:215],v[4:7]\nv_exp_f32 v169,v169\nv_cvt_pk_bf16_f32 v196,v132,v133\nM v[8:11],a[100:103],v[208:211],v[8:11]\nv_exp_f32 v170,v170\nM v[12:15],a[100:103],v[212:215],v[12:15]\nbuffer_load_dwordx4 v240,s[48:51],s65 offen offset:2048 sc1 lds\n" \
"v_cvt_pk_bf16_f32 v197,v134,v135\nM v[16:19],a[104:107],v[208:211],v[16:19]\nv_exp_f32 v171,v171\nv_cvt_pk_bf16_f32 v198,v148,v149\nM v[20:23],a[104:107],v[212:215],v[20:23]\nv_cvt_pk_bf16_f32 v199,v150,v151\nv_cvt_pk_bf16_f32 v200,v136,v137\nM v[24:27],a[108:111],v[208:211],v[24:27]\n" \
"v_cvt_pk_bf16_f32 v201,v138,v139\nv_cvt_pk_bf16_f32 v202,v152,v153\nv_cvt_pk_bf16_f32 v203,v154,v155\nM v[28:31],a[108:111],v[212:215],v[28:31]\nbuffer_load_dwordx4 v240,s[48:51],s65 offen offset:3072 sc1 lds\ns_add_u32 m0,s64,32768\nM v[32:35],a[112:115],v[208:211],v[32:35]\nv_cvt_pk_bf16_f32 v204,v140,v141\n" \
"v_cvt_pk_bf16_f32 v205,v142,v143\nv_cvt_pk_bf16_f32 v206,v156,v157\nM v[36:39],a[112:115],v[212:215],v[36:39]\nv_cvt_pk_bf16_f32 v207,v158,v159\nM v[40:43],a[116:119],v[208:211],v[40:43]\nM v[44:47],a[116:119],v[212:215],v[44:47]\nbuffer_load_dwordx4 v240,s[44:47],s66 offen offset:0 sc1 lds\nM v[48:51],a[120:123],v[208:211],v[48:51]\n" \
"M v[52:55],a[120:123],v[212:215],v[52:55]\nM v[56:59],a[124:127],v[208:211],v[56:59]\nM v[60:63],a[124:127],v[212:215],v[60:63]\nbuffer_load_dwordx4 v240,s[44:47],s66 offen offset:1024 sc1 lds\nbuffer_load_dwordx4 v240,s[44:47],s66 offen offset:2048 sc1 lds\nbuffer_load_dwordx4 v240,s[44:47],s66 offen offset:3072 sc1 lds\nM a[192:195],a[208:211],v[208:211],a[192:195]\ns_add_u32 m0,s64,98304\n" \
"s_nop 0\nbuffer_load_dwordx4 v240,s[48:51],s66 offen offset:0 sc1 lds\nbuffer_load_dwordx4 v240,s[48:51],s66 offen offset:1024 sc1 lds\nbuffer_load_dwordx4 v240,s[48:51],s66 offen offset:2048 sc1 lds\nbuffer_load_dwordx4 v240,s[48:51],s66 offen offset:3072 sc1 lds\nM a[196:199],a[208:211],v[212:215],a[196:199]\ns_cmp_eq_u32 s62,0\ns_cbranch_scc1 .Ltrans\n" \
"s_branch .Ldual\n.Lp23:\ns_cmp_eq_u32 s7,2\ns_cbranch_scc0 .Lpro3\n.Lpro2:\ns_barrier\nM v[64:67],a[0:3],a[128:131],0\nds_read_b128 a[64:67],v241 offset:0\n" \
"ds_read_b128 a[68:71],v241 offset:1024\nM v[64:67],a[4:7],a[132:135],v[64:67]\nds_read_b128 a[72:75],v241 offset:2048\ns_mov_b32 m0,s64\nv_mov_b32 v184,0xff800000\nM v[64:67],a[8:11],a[136:139],v[64:67]\nds_read_b128 a[76:79],v241 offset:3072\nbuffer_load_dwordx4 v240,s[44:47],s36 offen offset:0 sc1 lds\n" \
"M v[64:67],a[12:15],a[140:143],v[64:67]\nds_read_b128 a[80:83],v241 offset:4096\nv_mov_b32 v185,0xff800000\nM v[68:71],a[0:3],a[144:147],0\nds_read_b128 a[0:3],v240 offset:16384\nv_mov_b32 v186,0xff800000\nv_mov_b32 v187,0xff800000\nM v[68:71],a[4:7],a[148:151],v[68:71]\n" \
"ds_read_b128 a[4:7],v240 offset:17408\nv_max3_f32 v225,v64,v65,v66\nM v[68:71],a[8:11],a[152:155],v[68:71]\nbuffer_load_dwordx4 v240,s[44:47],s36 offen offset:1024 sc1 lds\nM v[68:71],a[12:15],a[156:159],v[68:71]\nds_read_b128 a[8:11],v240 offset:18432\nds_read_b128 a[12:15],v240 offset:19456\nM v[72:75],a[16:19],a[128:131],0\n" \
"ds_read_b128 a[84:87],v241 offset:5120\ns_lshl4_add_u32 s69,s13,s68\nM v[72:75],a[20:23],a[132:135],v[72:75]\nds_read_b128 a[88:91],v241 offset:6144\nM v[72:75],a[24:27],a[136:139],v[72:75]\nv_max3_f32 v229,v68,v69,v70\nds_read_b128 a[92:95],v241 offset:7168\nM v[72:75],a[28:31],a[140:143],v[72:75]\n" \
"ds_read_b128 a[96:99],v241 offset:8192\nbuffer_load_dwordx4 v240,s[44:47],s36 offen offset:2048 sc1 lds\nM v[76:79],a[16:19],a[144:147],0\nds_read_b128 a[16:19],v240 offset:20480\nM v[76:79],a[20:23],a[148:151],v[76:79]\nds_read_b128 a[20:23],v240 offset:21504\nM v[76:79],a[24:27],a[152:155],v[76:79]\nds_read_b128 a[24:27],v240 offset:22528\n" \
"v_max3_f32 v225,v225,v67,v72\nv_max3_f32 v225,v225,v73,v74\nM v[76:79],a[28:31],a[156:159],v[76:79]\nv_max_f32 v225,v225,v75\nv_cndmask_b32 v225,v224,v225,s[90:91]\nds_read_b128 a[28:31],v240 offset:23552\nM v[80:83],a[32:35],a[128:131],0\nbuffer_load_dwordx4 v240,s[44:47],s36 offen offset:3072 sc1 lds\n" \
"M v[80:83],a[36:39],a[132:135],v[80:83]\nds_read_b128 a[100:103],v241 offset:9216\nds_read_b128 a[104:107],v241 offset:10240\nv_max3_f32 v229,v229,v71,v76\nM v[80:83],a[40:43],a[136:139],v[80:83]\nv_max3_f32 v229,v229,v77,v78\nv_max_f32 v229,v229,v79\nv_cndmask_b32 v229,v228,v229,s[90:91]\n" \
"M v[80:83],a[44:47],a[140:143],v[80:83]\nds_read_b128 a[108:111],v241 offset:11264\nv_permlane16_swap_b32 v225,v229\nv_max_f32 v225,v225,v229\nM v[84:87],a[32:35],a[144:147],0\nv_mov_b32 v229,v225\nds_read_b128 a[112:115],v241 offset:12288\nM v[84:87],a[36:39],a[148:151],v[84:87]\n" \
"v_permlane32_swap_b32 v225,v229\nv_max_f32 v225,v225,v229\nv_mov_b32 v229,v225\nM v[84:87],a[40:43],a[152:155],v[84:87]\nds_read_b128 a[116:119],v241 offset:13312\nv_permlane16_swap_b32 v225,v229\nv_sub_f32 v224,0,v225\nM v[84:87],a[44:47],a[156:159],v[84:87]\n" \
"v_sub_f32 v226,0,v225\nv_sub_f32 v227,0,v225\nM v[88:91],a[48:51],a[128:131],0\nv_sub_f32 v225,0,v225\nv_sub_f32 v228,0,v229\nv_sub_f32 v230,0,v229\nM v[88:91],a[52:55],a[132:135],v[88:91]\nv_sub_f32 v231,0,v229\n" \
"v_sub_f32 v229,0,v229\nv_add_f32 v64,v64,v224\nM v[88:91],a[56:59],a[136:139],v[88:91]\nv_add_f32 v65,v65,v224\nv_add_f32 v66,v66,v224\nM v[88:91],a[60:63],a[140:143],v[88:91]\nv_add_f32 v67,v67,v224\nv_exp_f32 v64,v64\n" \
"M v[92:95],a[48:51],a[144:147],0\nv_exp_f32 v65,v65\nv_add_f32 v68,v68,v228\nM v[92:95],a[52:55],a[148:151],v[92:95]\nv_exp_f32 v66,v66\nv_add_f32 v69,v69,v228\nM v[92:95],a[56:59],a[152:155],v[92:95]\nv_exp_f32 v67,v67\n" \
"M v[92:95],a[60:63],a[156:159],v[92:95]\nv_add_f32 v72,v72,v224\nv_add_f32 v73,v73,v224\ns_waitcnt lgkmcnt(0)\nM v[96:99],a[0:3],a[128:131],v[224:227]\nv_add_f32 v74,v74,v224\nv_add_f32 v75,v75,v224\nv_add_f32 v70,v70,v228\n" \
"M v[96:99],a[4:7],a[132:135],v[96:99]\nv_add_f32 v71,v71,v228\nv_cvt_pk_bf16_f32 v64,v64,v65\nM v[96:99],a[8:11],a[136:139],v[96:99]\nv_exp_f32 v68,v68\nv_add_f32 v76,v76,v228\nM v[96:99],a[12:15],a[140:143],v[96:99]\nv_exp_f32 v69,v69\n" \
"v_add_f32 v77,v77,v228\nM v[100:103],a[0:3],a[144:147],v[228:231]\nv_exp_f32 v72,v72\nM v[100:103],a[4:7],a[148:151],v[100:103]\nv_exp_f32 v73,v73\nv_add_f32 v78,v78,v228\nM v[100:103],a[8:11],a[152:155],v[100:103]\nv_exp_f32 v74,v74\n" \
"v_add_f32 v79,v79,v228\nM v[100:103],a[12:15],a[156:159],v[100:103]\nv_exp_f32 v75,v75\nv_cvt_pk_bf16_f32 v65,v66,v67\nM v[108:111],a[16:19],a[144:147],v[228:231]\nv_exp_f32 v70,v70\nM v[108:111],a[20:23],a[148:151],v[108:111]\nv_exp_f32 v71,v71\n" \
"v_cvt_pk_bf16_f32 v66,v72,v73\nM v[108:111],a[24:27],a[152:155],v[108:111]\nv_exp_f32 v76,v76\nv_exp_f32 v77,v77\nv_exp_f32 v78,v78\nv_exp_f32 v79,v79\nv_cvt_pk_bf16_f32 v67,v74,v75\nv_cvt_pk_bf16_f32 v68,v68,v69\n" \
"M v[108:111],a[28:31],a[156:159],v[108:111]\nv_cvt_pk_bf16_f32 v69,v70,v71\nv_cvt_pk_bf16_f32 v70,v76,v77\nv_cvt_pk_bf16_f32 v71,v78,v79\nM v[0:3],a[64:67],v[64:67],0\nv_add_f32 v80,v80,v224\nv_add_f32 v81,v81,v224\nv_add_f32 v82,v82,v224\n" \
"M v[4:7],a[64:67],v[68:71],0\nv_add_f32 v83,v83,v224\nv_exp_f32 v80,v80\nM v[8:11],a[68:71],v[64:67],0\nv_exp_f32 v81,v81\nM v[12:15],a[68:71],v[68:71],0\nv_exp_f32 v82,v82\nv_add_f32 v84,v84,v228\n" \
"M v[16:19],a[72:75],v[64:67],0\nv_exp_f32 v83,v83\nv_add_f32 v85,v85,v228\nM v[20:23],a[72:75],v[68:71],0\nv_add_f32 v88,v88,v224\nv_add_f32 v89,v89,v224\nv_add_f32 v90,v90,v224\nM v[24:27],a[76:79],v[64:67],0\n" \
"v_add_f32 v91,v91,v224\nv_add_f32 v86,v86,v228\nM v[28:31],a[76:79],v[68:71],0\nv_add_f32 v87,v87,v228\nv_exp_f32 v84,v84\nM v[32:35],a[80:83],v[64:67],0\nv_exp_f32 v85,v85\nv_cvt_pk_bf16_f32 v80,v80,v81\n" \
"M v[36:39],a[80:83],v[68:71],0\nv_exp_f32 v88,v88\nM v[40:43],a[84:87],v[64:67],0\nv_exp_f32 v89,v89\nv_add_f32 v92,v92,v228\nM v[44:47],a[84:87],v[68:71],0\nv_exp_f32 v90,v90\nv_add_f32 v93,v93,v228\n" \
"M v[48:51],a[88:91],v[64:67],0\nv_exp_f32 v91,v91\nM v[52:55],a[88:91],v[68:71],0\nv_exp_f32 v86,v86\nv_add_f32 v94,v94,v228\nM v[56:59],a[92:95],v[64:67],0\nv_exp_f32 v87,v87\nv_add_f32 v95,v95,v228\n" \
"M v[60:63],a[92:95],v[68:71],0\nv_cvt_pk_bf16_f32 v81,v82,v83\nv_exp_f32 v92,v92\nM a[192:195],a[208:211],v[64:67],0\nv_exp_f32 v93,v93\nv_exp_f32 v94,v94\nv_exp_f32 v95,v95\nv_cvt_pk_bf16_f32 v82,v88,v89\n" \
"v_cvt_pk_bf16_f32 v83,v90,v91\nv_cvt_pk_bf16_f32 v84,v84,v85\nM a[196:199],a[208:211],v[68:71],0\nv_cvt_pk_bf16_f32 v85,v86,v87\nv_cvt_pk_bf16_f32 v86,v92,v93\nv_cvt_pk_bf16_f32 v87,v94,v95\nM v[0:3],a[96:99],v[80:83],v[0:3]\nds_read_b128 a[120:123],v241 offset:14336\n" \
"ds_read_b128 a[124:127],v241 offset:15360\nv_cndmask_b32 v96,v96,v252,s[82:83]\nM v[4:7],a[96:99],v[84:87],v[4:7]\nds_read_b128 a[0:3],v240 offset:32768\nv_cndmask_b32 v97,v97,v252,s[84:85]\nM v[8:11],a[100:103],v[80:83],v[8:11]\nds_read_b128 a[4:7],v240 offset:33792\nv_cndmask_b32 v98,v98,v252,s[86:87]\n" \
"v_cndmask_b32 v99,v99,v252,s[88:89]\nM v[12:15],a[100:103],v[84:87],v[12:15]\nds_read_b128 a[8:11],v240 offset:34816\nv_exp_f32 v96,v96\nM v[16:19],a[104:107],v[80:83],v[16:19]\nds_read_b128 a[12:15],v240 offset:35840\nv_cndmask_b32 v108,v108,v252,s[82:83]\nM v[20:23],a[104:107],v[84:87],v[20:23]\n" \
"ds_read_b128 a[16:19],v240 offset:36864\nv_exp_f32 v97,v97\nM v[24:27],a[108:111],v[80:83],v[24:27]\nds_read_b128 a[20:23],v240 offset:37888\nv_exp_f32 v98,v98\nM v[28:31],a[108:111],v[84:87],v[28:31]\nds_read_b128 a[24:27],v240 offset:38912\nv_exp_f32 v99,v99\n" \
"M v[32:35],a[112:115],v[80:83],v[32:35]\nds_read_b128 a[28:31],v240 offset:39936\nv_cvt_pk_bf16_f32 v96,v96,v97\nM v[36:39],a[112:115],v[84:87],v[36:39]\nds_read_b128 a[32:35],v240 offset:40960\nv_exp_f32 v100,v100\nM v[40:43],a[116:119],v[80:83],v[40:43]\nds_read_b128 a[36:39],v240 offset:41984\n" \
"v_exp_f32 v101,v101\nM v[44:47],a[116:119],v[84:87],v[44:47]\nds_read_b128 a[40:43],v240 offset:43008\ns_waitcnt lgkmcnt(9)\nM v[48:51],a[120:123],v[80:83],v[48:51]\nbuffer_load_dword v248,v251,s[52:55],s80 offen sc1\nds_read_b128 a[44:47],v240 offset:44032\nM v[52:55],a[120:123],v[84:87],v[52:55]\n" \
"ds_read_b128 a[48:51],v240 offset:45056\nv_cndmask_b32 v109,v109,v252,s[84:85]\nv_cndmask_b32 v110,v110,v252,s[86:87]\nM v[56:59],a[124:127],v[80:83],v[56:59]\nds_read_b128 a[52:55],v240 offset:46080\nv_cndmask_b32 v111,v111,v252,s[88:89]\nM v[60:63],a[124:127],v[84:87],v[60:63]\nds_read_b128 a[56:59],v240 offset:47104\n" \
"v_exp_f32 v102,v102\nM a[192:195],a[208:211],v[80:83],a[192:195]\nds_read_b128 a[60:63],v240 offset:48128\nv_exp_f32 v103,v103\nM a[196:199],a[208:211],v[84:87],a[196:199]\nds_read_b128 a[64:67],v241 offset:16384\nv_cvt_pk_bf16_f32 v97,v98,v99\nv_mov_b32 v98,0\n" \
"M v[128:131],a[0:3],a[128:131],v[224:227]\nds_read_b128 a[68:71],v241 offset:17408\nv_mov_b32 v99,0\nM v[128:131],a[4:7],a[132:135],v[128:131]\nds_read_b128 a[72:75],v241 offset:18432\nv_cvt_pk_bf16_f32 v100,v100,v101\ns_waitcnt lgkmcnt(9)\nM v[128:131],a[8:11],a[136:139],v[128:131]\n" \
"ds_read_b128 a[76:79],v241 offset:19456\nv_exp_f32 v108,v108\nM v[128:131],a[12:15],a[140:143],v[128:131]\nds_read_b128 a[80:83],v241 offset:20480\nv_cvt_pk_bf16_f32 v101,v102,v103\nM v[132:135],a[0:3],a[144:147],v[228:231]\nv_exp_f32 v109,v109\nds_read_b128 a[84:87],v241 offset:21504\n" \
"M v[132:135],a[4:7],a[148:151],v[132:135]\nv_exp_f32 v110,v110\nv_cvt_pk_bf16_f32 v102,v108,v109\nM v[132:135],a[8:11],a[152:155],v[132:135]\nv_exp_f32 v111,v111\nM v[132:135],a[12:15],a[156:159],v[132:135]\nv_cvt_pk_bf16_f32 v103,v110,v111\nds_read_b128 a[88:91],v241 offset:22528\n" \
"ds_read_b128 a[92:95],v241 offset:23552\nM v[144:147],a[16:19],a[128:131],v[224:227]\nds_read_b128 a[0:3],v240 offset:49152\nv_exp_f32 v128,v128\nM v[144:147],a[20:23],a[132:135],v[144:147]\nds_read_b128 a[4:7],v240 offset:50176\nv_exp_f32 v129,v129\nM v[144:147],a[24:27],a[136:139],v[144:147]\n" \
"ds_read_b128 a[8:11],v240 offset:51200\nv_cvt_pk_bf16_f32 v192,v128,v129\nM v[144:147],a[28:31],a[140:143],v[144:147]\nds_read_b128 a[12:15],v240 offset:52224\nv_exp_f32 v130,v130\nM v[148:151],a[16:19],a[144:147],v[228:231]\nds_read_b128 a[16:19],v240 offset:53248\nv_exp_f32 v131,v131\n" \
"M v[148:151],a[20:23],a[148:151],v[148:151]\nds_read_b128 a[20:23],v240 offset:54272\nv_cvt_pk_bf16_f32 v193,v130,v131\nM v[148:151],a[24:27],a[152:155],v[148:151]\nds_read_b128 a[24:27],v240 offset:55296\nv_exp_f32 v132,v132\nM v[148:151],a[28:31],a[156:159],v[148:151]\nds_read_b128 a[28:31],v240 offset:56320\n" \
"v_exp_f32 v133,v133\nM v[160:163],a[32:35],a[128:131],v[224:227]\nv_exp_f32 v134,v134\nM v[160:163],a[36:39],a[132:135],v[160:163]\nv_exp_f32 v135,v135\ns_waitcnt lgkmcnt(7)\nM v[160:163],a[40:43],a[136:139],v[160:163]\nv_cvt_pk_bf16_f32 v196,v132,v133\n" \
"v_cvt_pk_bf16_f32 v197,v134,v135\nds_read_b128 a[96:99],v241 offset:40960\nM v[160:163],a[44:47],a[140:143],v[160:163]\nv_exp_f32 v144,v144\nds_read_b128 a[100:103],v241 offset:41984\nM v[164:167],a[32:35],a[144:147],v[228:231]\nds_read_b128 a[32:35],v240 offset:57344\nds_read_b128 a[104:107],v241 offset:43008\n" \
"M v[164:167],a[36:39],a[148:151],v[164:167]\nv_exp_f32 v145,v145\nds_read_b128 a[36:39],v240 offset:58368\nM v[164:167],a[40:43],a[152:155],v[164:167]\nv_exp_f32 v146,v146\nv_cvt_pk_bf16_f32 v194,v144,v145\nM v[164:167],a[44:47],a[156:159],v[164:167]\nv_exp_f32 v147,v147\n" \
"M v[176:179],a[48:51],a[128:131],v[224:227]\nv_cvt_pk_bf16_f32 v195,v146,v147\nds_read_b128 a[40:43],v240 offset:59392\nds_read_b128 a[44:47],v240 offset:60416\nM v[176:179],a[52:55],a[132:135],v[176:179]\nv_exp_f32 v148,v148\nds_read_b128 a[108:111],v241 offset:44032\nM v[176:179],a[56:59],a[136:139],v[176:179]\n" \
"v_exp_f32 v149,v149\nM v[176:179],a[60:63],a[140:143],v[176:179]\nv_exp_f32 v150,v150\nv_cvt_pk_bf16_f32 v198,v148,v149\nM v[180:183],a[48:51],a[144:147],v[228:231]\nv_exp_f32 v151,v151\nds_read_b128 a[48:51],v240 offset:61440\nM v[180:183],a[52:55],a[148:151],v[180:183]\n" \
"v_cvt_pk_bf16_f32 v199,v150,v151\nv_exp_f32 v160,v160\nM v[180:183],a[56:59],a[152:155],v[180:183]\nv_exp_f32 v161,v161\nM v[180:183],a[60:63],a[156:159],v[180:183]\nv_exp_f32 v162,v162\nds_read_b128 a[52:55],v240 offset:62464\nM v[0:3],a[64:67],v[96:99],v[0:3]\n" \
"v_exp_f32 v163,v163\nv_cvt_pk_bf16_f32 v208,v160,v161\nM v[4:7],a[64:67],v[100:103],v[4:7]\nv_cvt_pk_bf16_f32 v209,v162,v163\nds_read_b128 a[56:59],v240 offset:63488\nM v[8:11],a[68:71],v[96:99],v[8:11]\nds_read_b128 a[60:63],v240 offset:64512\nv_exp_f32 v164,v164\n" \
"M v[12:15],a[68:71],v[100:103],v[12:15]\nv_exp_f32 v165,v165\nds_read_b128 a[64:67],v241 offset:32768\nM v[16:19],a[72:75],v[96:99],v[16:19]\nv_exp_f32 v166,v166\nM v[20:23],a[72:75],v[100:103],v[20:23]\nv_exp_f32 v167,v167\nv_cvt_pk_bf16_f32 v212,v164,v165\n" \
"M v[24:27],a[76:79],v[96:99],v[24:27]\nv_cvt_pk_bf16_f32 v213,v166,v167\nv_exp_f32 v176,v176\nM v[28:31],a[76:79],v[100:103],v[28:31]\nv_exp_f32 v177,v177\nds_read_b128 a[68:71],v241 offset:33792\nM v[32:35],a[80:83],v[96:99],v[32:35]\nv_exp_f32 v178,v178\n" \
"M v[36:39],a[80:83],v[100:103],v[36:39]\nv_exp_f32 v179,v179\nv_cvt_pk_bf16_f32 v210,v176,v177\nM v[40:43],a[84:87],v[96:99],v[40:43]\nv_cvt_pk_bf16_f32 v211,v178,v179\nv_exp_f32 v180,v180\nM v[44:47],a[84:87],v[100:103],v[44:47]\nv_exp_f32 v181,v181\n" \
"M v[48:51],a[88:91],v[96:99],v[48:51]\ns_waitcnt vmcnt(1)\ns_barrier\nv_cvt_pk_bf16_f32 v214,v180,v181\nM v[52:55],a[88:91],v[100:103],v[52:55]\nv_exp_f32 v182,v182\nds_read_b128 a[72:75],v241 offset:34816\nM v[56:59],a[92:95],v[96:99],v[56:59]\n" \
"v_exp_f32 v183,v183\nM v[60:63],a[92:95],v[100:103],v[60:63]\nv_cvt_pk_bf16_f32 v215,v182,v183\nds_read_b128 a[76:79],v241 offset:35840\nds_read_b128 a[80:83],v241 offset:36864\nM a[192:195],a[208:211],v[96:99],a[192:195]\nds_read_b128 a[84:87],v241 offset:37888\ns_add_u32 m0,s64,131072\n" \
"s_waitcnt vmcnt(0)\nv_cmp_eq_u32 vcc,0,v248\nM a[196:199],a[208:211],v[100:103],a[196:199]\nds_read_b128 a[88:91],v241 offset:38912\nbuffer_load_dwordx4 v243,s[40:43],s69 offen offset:0 lds\nM v[64:67],a[244:247],a[244:247],0\nds_read_b128 a[92:95],v241 offset:39936\ns_cbranch_vccnz .Lps4\n" \
".Lpb4:\nM v[68:71],a[244:247],a[244:247],0\nds_read_b128 a[112:115],v241 offset:45056\nM v[72:75],a[244:247],a[244:247],0\nds_read_b128 a[116:119],v241 offset:46080\nM v[76:79],a[244:247],a[244:247],0\nbuffer_load_dwordx4 v244,s[40:43],s69 offen offset:1024 lds\nM v[80:83],a[244:247],a[244:247],0\n" \
"ds_read_b128 a[120:123],v241 offset:47104\nds_read_b128 a[124:127],v241 offset:48128\nM v[84:87],a[244:247],a[244:247],0\nM v[88:91],a[244:247],a[244:247],0\nM v[92:95],a[244:247],a[244:247],0\nbuffer_load_dwordx4 v245,s[40:43],s69 offen offset:2048 lds\nM v[96:99],a[244:247],a[244:247],0\nM v[100:103],a[244:247],a[244:247],0\n" \
"M v[104:107],a[244:247],a[244:247],0\nM v[108:111],a[244:247],a[244:247],0\nbuffer_load_dwordx4 v246,s[40:43],s69 offen offset:3072 lds\ns_lshl4_add_u32 s69,s13,s68\nM v[112:115],a[244:247],a[244:247],0\ns_add_u32 s69,s69,0x10000\ns_add_u32 m0,s64,147456\nM v[116:119],a[244:247],a[244:247],0\n" \
"M v[120:123],a[244:247],a[244:247],0\nM v[124:127],a[244:247],a[244:247],0\nbuffer_load_dwordx4 v243,s[40:43],s69 offen offset:0 lds\nM a[200:203],a[244:247],a[244:247],0\nM a[204:207],a[244:247],a[244:247],0\nM v[136:139],a[0:3],a[160:163],0\ns_waitcnt lgkmcnt(2)\nM v[136:139],a[4:7],a[164:167],v[136:139]\n" \
"buffer_load_dwordx4 v244,s[40:43],s69 offen offset:1024 lds\nM v[136:139],a[8:11],a[168:171],v[136:139]\nM v[136:139],a[12:15],a[172:175],v[136:139]\nM v[140:143],a[0:3],a[176:179],0\nM v[140:143],a[4:7],a[180:183],v[140:143]\nbuffer_load_dwordx4 v245,s[40:43],s69 offen offset:2048 lds\nM v[140:143],a[8:11],a[184:187],v[140:143]\nM v[140:143],a[12:15],a[188:191],v[140:143]\n" \
"M v[152:155],a[16:19],a[160:163],0\nM v[152:155],a[20:23],a[164:167],v[152:155]\nbuffer_load_dwordx4 v246,s[40:43],s69 offen offset:3072 lds\nv_max3_f32 v233,v136,v137,v138\nM v[152:155],a[24:27],a[168:171],v[152:155]\ns_add_u32 m0,s64,65536\nM v[152:155],a[28:31],a[172:175],v[152:155]\nM v[156:159],a[16:19],a[176:179],0\n" \
"v_max3_f32 v237,v140,v141,v142\nM v[156:159],a[20:23],a[180:183],v[156:159]\nbuffer_load_dwordx4 v240,s[48:51],s36 offen offset:0 sc1 lds\nM v[156:159],a[24:27],a[184:187],v[156:159]\nM v[156:159],a[28:31],a[188:191],v[156:159]\nM v[128:131],a[0:3],a[128:131],v[224:227]\nM v[128:131],a[4:7],a[132:135],v[128:131]\nv_max3_f32 v233,v233,v139,v152\n" \
"v_max3_f32 v233,v233,v153,v154\nv_max_f32 v233,v233,v155\nM v[128:131],a[8:11],a[136:139],v[128:131]\nbuffer_load_dwordx4 v240,s[48:51],s36 offen offset:1024 sc1 lds\nM v[128:131],a[12:15],a[140:143],v[128:131]\nv_max3_f32 v237,v237,v143,v156\nv_max3_f32 v237,v237,v157,v158\nv_max_f32 v237,v237,v159\n" \
"M v[132:135],a[0:3],a[144:147],v[228:231]\nv_cndmask_b32 v233,v232,v233,s[90:91]\nv_cndmask_b32 v237,v236,v237,s[90:91]\nds_read_b128 a[0:3],v240 offset:0\nM v[132:135],a[4:7],a[148:151],v[132:135]\nv_permlane16_swap_b32 v233,v237\nv_max_f32 v233,v233,v237\nv_mov_b32 v237,v233\n" \
"M v[132:135],a[8:11],a[152:155],v[132:135]\nds_read_b128 a[4:7],v240 offset:1024\nv_permlane32_swap_b32 v233,v237\nM v[132:135],a[12:15],a[156:159],v[132:135]\nv_max_f32 v233,v233,v237\nv_mov_b32 v237,v233\nds_read_b128 a[8:11],v240 offset:2048\nM v[144:147],a[16:19],a[128:131],v[224:227]\n" \
"v_permlane16_swap_b32 v233,v237\nv_sub_f32 v232,0,v233\nv_sub_f32 v234,0,v233\nM v[144:147],a[20:23],a[132:135],v[144:147]\nv_sub_f32 v235,0,v233\nv_sub_f32 v233,0,v233\nM v[144:147],a[24:27],a[136:139],v[144:147]\nv_sub_f32 v236,0,v237\n" \
"v_sub_f32 v238,0,v237\nv_sub_f32 v239,0,v237\nM v[144:147],a[28:31],a[140:143],v[144:147]\nv_sub_f32 v237,0,v237\nds_read_b128 a[12:15],v240 offset:3072\nv_add_f32 v136,v136,v232\nM v[148:151],a[16:19],a[144:147],v[228:231]\nds_read_b128 a[16:19],v240 offset:4096\n" \
"v_add_f32 v137,v137,v232\nM v[148:151],a[20:23],a[148:151],v[148:151]\nds_read_b128 a[20:23],v240 offset:5120\nbuffer_load_dwordx4 v240,s[48:51],s36 offen offset:2048 sc1 lds\nM v[148:151],a[24:27],a[152:155],v[148:151]\nds_read_b128 a[24:27],v240 offset:6144\nv_add_f32 v138,v138,v232\nv_add_f32 v139,v139,v232\n" \
"M v[148:151],a[28:31],a[156:159],v[148:151]\nds_read_b128 a[28:31],v240 offset:7168\nv_add_f32 v140,v140,v236\nv_add_f32 v141,v141,v236\nM v[160:163],a[32:35],a[128:131],v[224:227]\nv_add_f32 v142,v142,v236\nv_add_f32 v143,v143,v236\nM v[160:163],a[36:39],a[132:135],v[160:163]\n" \
"buffer_load_dwordx4 v240,s[48:51],s36 offen offset:3072 sc1 lds\ns_add_u32 m0,s64,16384\nM v[160:163],a[40:43],a[136:139],v[160:163]\nv_add_f32 v152,v152,v232\nv_add_f32 v153,v153,v232\nv_add_f32 v154,v154,v232\nM v[160:163],a[44:47],a[140:143],v[160:163]\nv_add_f32 v155,v155,v232\n" \
"v_add_f32 v156,v156,v236\nM v[164:167],a[32:35],a[144:147],v[228:231]\nv_add_f32 v157,v157,v236\nv_add_f32 v158,v158,v236\nv_add_f32 v159,v159,v236\nM v[164:167],a[36:39],a[148:151],v[164:167]\nbuffer_load_dwordx4 v240,s[44:47],s65 offen offset:0 sc1 lds\nM v[164:167],a[40:43],a[152:155],v[164:167]\n" \
"v_exp_f32 v136,v136\nM v[164:167],a[44:47],a[156:159],v[164:167]\nv_exp_f32 v137,v137\nM v[176:179],a[48:51],a[128:131],v[224:227]\nv_exp_f32 v138,v138\nv_cvt_pk_bf16_f32 v200,v136,v137\nM v[176:179],a[52:55],a[132:135],v[176:179]\nbuffer_load_dwordx4 v240,s[44:47],s65 offen offset:1024 sc1 lds\n" \
"M v[176:179],a[56:59],a[136:139],v[176:179]\nv_exp_f32 v139,v139\nM v[176:179],a[60:63],a[140:143],v[176:179]\nv_exp_f32 v140,v140\nv_cvt_pk_bf16_f32 v201,v138,v139\nM v[180:183],a[48:51],a[144:147],v[228:231]\nv_exp_f32 v141,v141\nM v[180:183],a[52:55],a[148:151],v[180:183]\n" \
"buffer_load_dwordx4 v240,s[44:47],s65 offen offset:2048 sc1 lds\nM v[180:183],a[56:59],a[152:155],v[180:183]\nv_exp_f32 v142,v142\nv_cvt_pk_bf16_f32 v204,v140,v141\nM v[180:183],a[60:63],a[156:159],v[180:183]\nv_exp_f32 v143,v143\nM v[168:171],a[32:35],a[160:163],v[232:235]\ns_waitcnt lgkmcnt(0)\n" \
"s_barrier\nM v[168:171],a[36:39],a[164:167],v[168:171]\nbuffer_load_dwordx4 v240,s[44:47],s65 offen offset:3072 sc1 lds\ns_add_u32 m0,s64,81920\nM v[168:171],a[40:43],a[168:171],v[168:171]\nv_exp_f32 v152,v152\nv_cvt_pk_bf16_f32 v205,v142,v143\nM v[168:171],a[44:47],a[172:175],v[168:171]\n" \
"v_exp_f32 v153,v153\nM v[172:175],a[32:35],a[176:179],v[236:239]\nds_read_b128 a[32:35],v240 offset:8192\nv_cvt_pk_bf16_f32 v202,v152,v153\nM v[172:175],a[36:39],a[180:183],v[172:175]\nds_read_b128 a[36:39],v240 offset:9216\nbuffer_load_dwordx4 v240,s[48:51],s65 offen offset:0 sc1 lds\nM v[172:175],a[40:43],a[184:187],v[172:175]\n" \
"ds_read_b128 a[40:43],v240 offset:10240\nv_exp_f32 v154,v154\nM v[172:175],a[44:47],a[188:191],v[172:175]\nds_read_b128 a[44:47],v240 offset:11264\nv_cndmask_b32 v168,v168,v252,s[82:83]\nM v[188:191],a[48:51],a[176:179],v[236:239]\nds_read_b128 a[48:51],v240 offset:12288\nv_exp_f32 v155,v155\n" \
"M v[188:191],a[52:55],a[180:183],v[188:191]\nds_read_b128 a[52:55],v240 offset:13312\nbuffer_load_dwordx4 v240,s[48:51],s65 offen offset:1024 sc1 lds\nM v[188:191],a[56:59],a[184:187],v[188:191]\nds_read_b128 a[56:59],v240 offset:14336\nv_cndmask_b32 v169,v169,v252,s[84:85]\nM v[188:191],a[60:63],a[188:191],v[188:191]\nds_read_b128 a[60:63],v240 offset:15360\n" \
"v_exp_f32 v156,v156\nM v[0:3],a[64:67],v[192:195],v[0:3]\nv_exp_f32 v157,v157\nv_cndmask_b32 v170,v170,v252,s[86:87]\nM v[4:7],a[64:67],v[196:199],v[4:7]\nbuffer_load_dwordx4 v240,s[48:51],s65 offen offset:2048 sc1 lds\nv_cndmask_b32 v171,v171,v252,s[88:89]\nM v[8:11],a[68:71],v[192:195],v[8:11]\n" \
"v_exp_f32 v158,v158\nM v[12:15],a[68:71],v[196:199],v[12:15]\nv_exp_f32 v159,v159\nv_cndmask_b32 v188,v188,v252,s[82:83]\nM v[16:19],a[72:75],v[192:195],v[16:19]\nv_exp_f32 v128,v128\nv_cndmask_b32 v189,v189,v252,s[84:85]\nM v[20:23],a[72:75],v[196:199],v[20:23]\n" \
"buffer_load_dwordx4 v240,s[48:51],s65 offen offset:3072 sc1 lds\nM v[24:27],a[76:79],v[192:195],v[24:27]\ns_add_u32 m0,s64,32768\nv_exp_f32 v129,v129\nM v[28:31],a[76:79],v[196:199],v[28:31]\nv_exp_f32 v130,v130\nv_cndmask_b32 v190,v190,v252,s[86:87]\nM v[32:35],a[80:83],v[192:195],v[32:35]\n" \
"v_exp_f32 v131,v131\nM v[36:39],a[80:83],v[196:199],v[36:39]\nbuffer_load_dwordx4 v240,s[44:47],s66 offen offset:0 sc1 lds\nv_cndmask_b32 v191,v191,v252,s[88:89]\nM v[40:43],a[84:87],v[192:195],v[40:43]\nv_exp_f32 v132,v132\nv_cvt_pk_bf16_f32 v203,v154,v155\nM v[44:47],a[84:87],v[196:199],v[44:47]\n" \
"v_exp_f32 v133,v133\nv_cvt_pk_bf16_f32 v206,v156,v157\nM v[48:51],a[88:91],v[192:195],v[48:51]\nv_exp_f32 v134,v134\nM v[52:55],a[88:91],v[196:199],v[52:55]\nbuffer_load_dwordx4 v240,s[44:47],s66 offen offset:1024 sc1 lds\nv_cvt_pk_bf16_f32 v207,v158,v159\nM v[56:59],a[92:95],v[192:195],v[56:59]\n" \
"v_exp_f32 v135,v135\nM v[60:63],a[92:95],v[196:199],v[60:63]\nv_exp_f32 v144,v144\nM a[192:195],a[208:211],v[192:195],a[192:195]\nv_exp_f32 v145,v145\nv_cvt_pk_bf16_f32 v192,v128,v129\nM a[196:199],a[208:211],v[196:199],a[196:199]\nbuffer_load_dwordx4 v240,s[44:47],s66 offen offset:2048 sc1 lds\n" \
"v_cvt_pk_bf16_f32 v193,v130,v131\nM v[0:3],a[96:99],v[208:211],v[0:3]\nv_exp_f32 v146,v146\nM v[4:7],a[96:99],v[212:215],v[4:7]\nv_exp_f32 v147,v147\nv_cvt_pk_bf16_f32 v194,v144,v145\nM v[8:11],a[100:103],v[208:211],v[8:11]\nv_exp_f32 v148,v148\n" \
"v_cvt_pk_bf16_f32 v195,v146,v147\nM v[12:15],a[100:103],v[212:215],v[12:15]\nbuffer_load_dwordx4 v240,s[44:47],s66 offen offset:3072 sc1 lds\ns_add_u32 m0,s64,98304\nM v[16:19],a[104:107],v[208:211],v[16:19]\nv_exp_f32 v149,v149\nM v[20:23],a[104:107],v[212:215],v[20:23]\nv_exp_f32 v150,v150\n" \
"v_cvt_pk_bf16_f32 v196,v132,v133\nM v[24:27],a[108:111],v[208:211],v[24:27]\nv_exp_f32 v151,v151\nv_cvt_pk_bf16_f32 v197,v134,v135\nM v[28:31],a[108:111],v[212:215],v[28:31]\nv_exp_f32 v160,v160\nM v[32:35],a[112:115],v[208:211],v[32:35]\nv_exp_f32 v161,v161\n" \
"v_cvt_pk_bf16_f32 v198,v148,v149\nM v[36:39],a[112:115],v[212:215],v[36:39]\nv_exp_f32 v162,v162\nv_cvt_pk_bf16_f32 v199,v150,v151\nM v[40:43],a[116:119],v[208:211],v[40:43]\nv_exp_f32 v163,v163\nM v[44:47],a[116:119],v[212:215],v[44:47]\nv_exp_f32 v164,v164\n" \
"M v[48:51],a[120:123],v[208:211],v[48:51]\nv_exp_f32 v165,v165\nM v[52:55],a[120:123],v[212:215],v[52:55]\nv_exp_f32 v166,v166\nM v[56:59],a[124:127],v[208:211],v[56:59]\nv_exp_f32 v167,v167\nM v[60:63],a[124:127],v[212:215],v[60:63]\nv_exp_f32 v176,v176\n" \
"M a[192:195],a[208:211],v[208:211],a[192:195]\nv_exp_f32 v177,v177\nv_exp_f32 v178,v178\nv_exp_f32 v179,v179\nv_exp_f32 v180,v180\nv_exp_f32 v181,v181\nv_exp_f32 v182,v182\nv_exp_f32 v183,v183\n" \
"v_exp_f32 v168,v168\nv_exp_f32 v169,v169\nv_exp_f32 v170,v170\nv_exp_f32 v171,v171\nbuffer_load_dwordx4 v240,s[48:51],s66 offen offset:0 sc1 lds\nbuffer_load_dwordx4 v240,s[48:51],s66 offen offset:1024 sc1 lds\nbuffer_load_dwordx4 v240,s[48:51],s66 offen offset:2048 sc1 lds\nbuffer_load_dwordx4 v240,s[48:51],s66 offen offset:3072 sc1 lds\n" \
"M a[196:199],a[208:211],v[212:215],a[196:199]\ns_cmp_eq_u32 s62,0\ns_cbranch_scc1 .Ltrans\ns_branch .Ldual\n.Lpro3:\ns_barrier\nM v[64:67],a[0:3],a[128:131],0\nds_read_b128 a[64:67],v241 offset:0\n" \
"ds_read_b128 a[68:71],v241 offset:1024\nM v[64:67],a[4:7],a[132:135],v[64:67]\nds_read_b128 a[72:75],v241 offset:2048\ns_mov_b32 m0,s64\nv_mov_b32 v168,0\nM v[64:67],a[8:11],a[136:139],v[64:67]\nds_read_b128 a[76:79],v241 offset:3072\nbuffer_load_dwordx4 v240,s[44:47],s36 offen offset:0 sc1 lds\n" \
"M v[64:67],a[12:15],a[140:143],v[64:67]\nds_read_b128 a[80:83],v241 offset:4096\nv_mov_b32 v169,0\nM v[68:71],a[0:3],a[144:147],0\nds_read_b128 a[0:3],v240 offset:16384\nv_mov_b32 v170,0\nv_mov_b32 v171,0\nM v[68:71],a[4:7],a[148:151],v[68:71]\n" \
"ds_read_b128 a[4:7],v240 offset:17408\nv_max3_f32 v225,v64,v65,v66\nv_mov_b32 v172,0xff800000\nM v[68:71],a[8:11],a[152:155],v[68:71]\nds_read_b128 a[8:11],v240 offset:18432\nv_mov_b32 v173,0xff800000\nM v[68:71],a[12:15],a[156:159],v[68:71]\nds_read_b128 a[12:15],v240 offset:19456\n" \
"buffer_load_dwordx4 v240,s[44:47],s36 offen offset:1024 sc1 lds\nM v[72:75],a[16:19],a[128:131],0\nds_read_b128 a[84:87],v241 offset:5120\ns_lshl4_add_u32 s69,s13,s68\nv_mov_b32 v174,0xff800000\nM v[72:75],a[20:23],a[132:135],v[72:75]\nds_read_b128 a[88:91],v241 offset:6144\nv_max3_f32 v229,v68,v69,v70\n" \
"v_mov_b32 v175,0xff800000\nM v[72:75],a[24:27],a[136:139],v[72:75]\nds_read_b128 a[92:95],v241 offset:7168\nv_mov_b32 v184,0xff800000\nM v[72:75],a[28:31],a[140:143],v[72:75]\nds_read_b128 a[96:99],v241 offset:8192\nbuffer_load_dwordx4 v240,s[44:47],s36 offen offset:2048 sc1 lds\nM v[76:79],a[16:19],a[144:147],0\n" \
"ds_read_b128 a[16:19],v240 offset:20480\nv_mov_b32 v185,0xff800000\nv_mov_b32 v186,0xff800000\nM v[76:79],a[20:23],a[148:151],v[76:79]\nds_read_b128 a[20:23],v240 offset:21504\nv_max3_f32 v225,v225,v67,v72\nM v[76:79],a[24:27],a[152:155],v[76:79]\nv_max3_f32 v225,v225,v73,v74\n" \
"v_max_f32 v225,v225,v75\nv_cndmask_b32 v225,v224,v225,s[90:91]\nM v[76:79],a[28:31],a[156:159],v[76:79]\nds_read_b128 a[24:27],v240 offset:22528\nds_read_b128 a[28:31],v240 offset:23552\nv_mov_b32 v187,0xff800000\nM v[80:83],a[32:35],a[128:131],0\nds_read_b128 a[100:103],v241 offset:9216\n" \
"v_mov_b32 v188,0xff800000\nM v[80:83],a[36:39],a[132:135],v[80:83]\nds_read_b128 a[104:107],v241 offset:10240\nv_max3_f32 v229,v229,v71,v76\nv_max3_f32 v229,v229,v77,v78\nM v[80:83],a[40:43],a[136:139],v[80:83]\nv_max_f32 v229,v229,v79\nv_cndmask_b32 v229,v228,v229,s[90:91]\n" \
"ds_read_b128 a[108:111],v241 offset:11264\nM v[80:83],a[44:47],a[140:143],v[80:83]\nv_permlane16_swap_b32 v225,v229\nv_max_f32 v225,v225,v229\nv_mov_b32 v229,v225\nM v[84:87],a[32:35],a[144:147],0\nds_read_b128 a[32:35],v240 offset:24576\nv_permlane32_swap_b32 v225,v229\n" \
"M v[84:87],a[36:39],a[148:151],v[84:87]\nv_max_f32 v225,v225,v229\nv_mov_b32 v229,v225\nds_read_b128 a[36:39],v240 offset:25600\nM v[84:87],a[40:43],a[152:155],v[84:87]\nv_permlane16_swap_b32 v225,v229\nv_sub_f32 v224,0,v225\nv_sub_f32 v226,0,v225\n" \
"M v[84:87],a[44:47],a[156:159],v[84:87]\nv_sub_f32 v227,0,v225\nv_sub_f32 v225,0,v225\nM v[88:91],a[48:51],a[128:131],0\nv_sub_f32 v228,0,v229\nv_sub_f32 v230,0,v229\nv_sub_f32 v231,0,v229\nM v[88:91],a[52:55],a[132:135],v[88:91]\n" \
"v_sub_f32 v229,0,v229\nds_read_b128 a[40:43],v240 offset:26624\nds_read_b128 a[44:47],v240 offset:27648\nM v[88:91],a[56:59],a[136:139],v[88:91]\nv_add_f32 v64,v64,v224\nv_add_f32 v65,v65,v224\nv_add_f32 v66,v66,v224\nM v[88:91],a[60:63],a[140:143],v[88:91]\n" \
"v_add_f32 v67,v67,v224\nv_add_f32 v68,v68,v228\nM v[92:95],a[48:51],a[144:147],0\nds_read_b128 a[48:51],v240 offset:28672\nv_exp_f32 v64,v64\nM v[92:95],a[52:55],a[148:151],v[92:95]\nds_read_b128 a[52:55],v240 offset:29696\nv_exp_f32 v65,v65\n" \
"M v[92:95],a[56:59],a[152:155],v[92:95]\nds_read_b128 a[56:59],v240 offset:30720\nv_add_f32 v69,v69,v228\nM v[92:95],a[60:63],a[156:159],v[92:95]\nds_read_b128 a[60:63],v240 offset:31744\nv_add_f32 v72,v72,v224\ns_waitcnt lgkmcnt(6)\nM v[96:99],a[0:3],a[128:131],v[224:227]\n" \
"v_exp_f32 v66,v66\nv_add_f32 v73,v73,v224\nM v[96:99],a[4:7],a[132:135],v[96:99]\nv_exp_f32 v67,v67\nM v[96:99],a[8:11],a[136:139],v[96:99]\nv_add_f32 v74,v74,v224\nv_add_f32 v75,v75,v224\nv_add_f32 v70,v70,v228\n" \
"M v[96:99],a[12:15],a[140:143],v[96:99]\nv_add_f32 v71,v71,v228\nv_exp_f32 v68,v68\nM v[100:103],a[0:3],a[144:147],v[228:231]\nv_exp_f32 v69,v69\nv_cvt_pk_bf16_f32 v64,v64,v65\nM v[100:103],a[4:7],a[148:151],v[100:103]\nv_exp_f32 v72,v72\n" \
"M v[100:103],a[8:11],a[152:155],v[100:103]\nv_exp_f32 v73,v73\nv_add_f32 v76,v76,v228\nM v[100:103],a[12:15],a[156:159],v[100:103]\nv_exp_f32 v74,v74\nv_add_f32 v77,v77,v228\nM v[104:107],a[16:19],a[128:131],v[224:227]\nv_exp_f32 v75,v75\n" \
"M v[104:107],a[20:23],a[132:135],v[104:107]\nv_exp_f32 v70,v70\nv_add_f32 v78,v78,v228\nM v[104:107],a[24:27],a[136:139],v[104:107]\nv_exp_f32 v71,v71\nv_add_f32 v79,v79,v228\nM v[104:107],a[28:31],a[140:143],v[104:107]\nv_cvt_pk_bf16_f32 v65,v66,v67\n" \
"v_cvt_pk_bf16_f32 v66,v72,v73\nM v[108:111],a[16:19],a[144:147],v[228:231]\nv_exp_f32 v76,v76\nv_cvt_pk_bf16_f32 v67,v74,v75\nM v[108:111],a[20:23],a[148:151],v[108:111]\nv_exp_f32 v77,v77\nv_cvt_pk_bf16_f32 v68,v68,v69\nM v[108:111],a[24:27],a[152:155],v[108:111]\n" \
"v_exp_f32 v78,v78\nv_cvt_pk_bf16_f32 v69,v70,v71\nM v[108:111],a[28:31],a[156:159],v[108:111]\nv_exp_f32 v79,v79\nM v[112:115],a[32:35],a[128:131],v[224:227]\nv_cvt_pk_bf16_f32 v70,v76,v77\nv_cvt_pk_bf16_f32 v71,v78,v79\nv_add_f32 v80,v80,v224\n" \
"M v[112:115],a[36:39],a[132:135],v[112:115]\nbuffer_load_dwordx4 v240,s[44:47],s36 offen offset:3072 sc1 lds\ns_waitcnt lgkmcnt(0)\nM v[112:115],a[40:43],a[136:139],v[112:115]\nv_add_f32 v81,v81,v224\nv_add_f32 v82,v82,v224\nM v[112:115],a[44:47],a[140:143],v[112:115]\nv_add_f32 v83,v83,v224\n" \
"v_exp_f32 v80,v80\nM v[116:119],a[32:35],a[144:147],v[228:231]\nv_exp_f32 v81,v81\nv_add_f32 v84,v84,v228\nM v[116:119],a[36:39],a[148:151],v[116:119]\nv_exp_f32 v82,v82\nv_add_f32 v85,v85,v228\nM v[116:119],a[40:43],a[152:155],v[116:119]\n" \
"v_exp_f32 v83,v83\nM v[116:119],a[44:47],a[156:159],v[116:119]\nv_add_f32 v88,v88,v224\nv_add_f32 v89,v89,v224\nv_add_f32 v90,v90,v224\nM v[124:127],a[48:51],a[144:147],v[228:231]\nv_add_f32 v91,v91,v224\nv_add_f32 v86,v86,v228\n" \
"v_add_f32 v87,v87,v228\nM v[124:127],a[52:55],a[148:151],v[124:127]\nv_exp_f32 v84,v84\nM v[124:127],a[56:59],a[152:155],v[124:127]\nv_exp_f32 v85,v85\nv_cvt_pk_bf16_f32 v80,v80,v81\nM v[124:127],a[60:63],a[156:159],v[124:127]\nv_exp_f32 v88,v88\n" \
"ds_read_b128 a[112:115],v241 offset:12288\nM v[0:3],a[64:67],v[64:67],0\nv_exp_f32 v89,v89\nM v[4:7],a[64:67],v[68:71],0\nv_exp_f32 v90,v90\nv_add_f32 v92,v92,v228\nM v[8:11],a[68:71],v[64:67],0\nv_exp_f32 v91,v91\n" \
"v_add_f32 v93,v93,v228\nM v[12:15],a[68:71],v[68:71],0\nv_exp_f32 v86,v86\nv_add_f32 v94,v94,v228\nM v[16:19],a[72:75],v[64:67],0\nv_exp_f32 v87,v87\nM v[20:23],a[72:75],v[68:71],0\nv_add_f32 v95,v95,v228\n" \
"v_cvt_pk_bf16_f32 v81,v82,v83\nv_cvt_pk_bf16_f32 v82,v88,v89\nM v[24:27],a[76:79],v[64:67],0\nv_exp_f32 v92,v92\nv_cvt_pk_bf16_f32 v83,v90,v91\nM v[28:31],a[76:79],v[68:71],0\nv_exp_f32 v93,v93\nM v[32:35],a[80:83],v[64:67],0\n" \
"v_exp_f32 v94,v94\nv_cvt_pk_bf16_f32 v84,v84,v85\nM v[36:39],a[80:83],v[68:71],0\nv_exp_f32 v95,v95\nv_cvt_pk_bf16_f32 v85,v86,v87\nM v[40:43],a[84:87],v[64:67],0\nv_cvt_pk_bf16_f32 v86,v92,v93\nv_cvt_pk_bf16_f32 v87,v94,v95\n" \
"M v[44:47],a[84:87],v[68:71],0\nds_read_b128 a[116:119],v241 offset:13312\nds_read_b128 a[120:123],v241 offset:14336\nv_cndmask_b32 v112,v112,v252,s[82:83]\nM v[48:51],a[88:91],v[64:67],0\nds_read_b128 a[124:127],v241 offset:15360\nv_exp_f32 v96,v96\nM v[52:55],a[88:91],v[68:71],0\n" \
"ds_read_b128 a[0:3],v240 offset:32768\nv_exp_f32 v97,v97\nM v[56:59],a[92:95],v[64:67],0\nbuffer_load_dword v248,v251,s[52:55],s80 offen sc1\nM v[60:63],a[92:95],v[68:71],0\nds_read_b128 a[4:7],v240 offset:33792\nds_read_b128 a[8:11],v240 offset:34816\nv_cvt_pk_bf16_f32 v96,v96,v97\n" \
"M a[192:195],a[208:211],v[64:67],0\nds_read_b128 a[12:15],v240 offset:35840\nv_exp_f32 v98,v98\nM a[196:199],a[208:211],v[68:71],0\nds_read_b128 a[16:19],v240 offset:36864\nv_cndmask_b32 v113,v113,v252,s[84:85]\nM v[0:3],a[96:99],v[80:83],v[0:3]\nds_read_b128 a[20:23],v240 offset:37888\n" \
"v_exp_f32 v99,v99\nM v[4:7],a[96:99],v[84:87],v[4:7]\nds_read_b128 a[24:27],v240 offset:38912\nv_exp_f32 v100,v100\nM v[8:11],a[100:103],v[80:83],v[8:11]\nds_read_b128 a[28:31],v240 offset:39936\nv_exp_f32 v101,v101\nM v[12:15],a[100:103],v[84:87],v[12:15]\n" \
"ds_read_b128 a[32:35],v240 offset:40960\nv_cvt_pk_bf16_f32 v97,v98,v99\nM v[16:19],a[104:107],v[80:83],v[16:19]\nds_read_b128 a[36:39],v240 offset:41984\nv_exp_f32 v104,v104\nM v[20:23],a[104:107],v[84:87],v[20:23]\nds_read_b128 a[40:43],v240 offset:43008\nv_exp_f32 v105,v105\n" \
"M v[24:27],a[108:111],v[80:83],v[24:27]\nds_read_b128 a[44:47],v240 offset:44032\nv_cvt_pk_bf16_f32 v98,v104,v105\nM v[28:31],a[108:111],v[84:87],v[28:31]\nds_read_b128 a[48:51],v240 offset:45056\nv_cvt_pk_bf16_f32 v100,v100,v101\ns_waitcnt lgkmcnt(9)\nM v[32:35],a[112:115],v[80:83],v[32:35]\n" \
"ds_read_b128 a[52:55],v240 offset:46080\nv_exp_f32 v106,v106\nM v[36:39],a[112:115],v[84:87],v[36:39]\nds_read_b128 a[56:59],v240 offset:47104\nv_cndmask_b32 v114,v114,v252,s[86:87]\nM v[40:43],a[116:119],v[80:83],v[40:43]\nds_read_b128 a[60:63],v240 offset:48128\nv_exp_f32 v107,v107\n" \
"M v[44:47],a[116:119],v[84:87],v[44:47]\nds_read_b128 a[64:67],v241 offset:16384\nv_exp_f32 v102,v102\nM v[48:51],a[120:123],v[80:83],v[48:51]\nds_read_b128 a[68:71],v241 offset:17408\nv_exp_f32 v103,v103\nM v[52:55],a[120:123],v[84:87],v[52:55]\nds_read_b128 a[72:75],v241 offset:18432\n" \
"v_cvt_pk_bf16_f32 v99,v106,v107\nM v[56:59],a[124:127],v[80:83],v[56:59]\nds_read_b128 a[76:79],v241 offset:19456\nv_exp_f32 v108,v108\nM v[60:63],a[124:127],v[84:87],v[60:63]\nds_read_b128 a[80:83],v241 offset:20480\nv_exp_f32 v109,v109\nM a[192:195],a[208:211],v[80:83],a[192:195]\n" \
"ds_read_b128 a[84:87],v241 offset:21504\nv_cvt_pk_bf16_f32 v101,v102,v103\nM a[196:199],a[208:211],v[84:87],a[196:199]\nds_read_b128 a[88:91],v241 offset:22528\nv_exp_f32 v110,v110\nM v[128:131],a[0:3],a[128:131],v[224:227]\nds_read_b128 a[92:95],v241 offset:23552\nv_exp_f32 v111,v111\n" \
"M v[128:131],a[4:7],a[132:135],v[128:131]\nds_read_b128 a[96:99],v241 offset:24576\nv_cvt_pk_bf16_f32 v102,v108,v109\nv_cvt_pk_bf16_f32 v103,v110,v111\nM v[128:131],a[8:11],a[136:139],v[128:131]\nds_read_b128 a[100:103],v241 offset:25600\nv_cndmask_b32 v115,v115,v252,s[88:89]\nM v[128:131],a[12:15],a[140:143],v[128:131]\n" \
"ds_read_b128 a[104:107],v241 offset:26624\nv_exp_f32 v112,v112\nM v[132:135],a[0:3],a[144:147],v[228:231]\nds_read_b128 a[108:111],v241 offset:27648\nv_exp_f32 v113,v113\nM v[132:135],a[4:7],a[148:151],v[132:135]\nds_read_b128 a[112:115],v241 offset:28672\nv_cvt_pk_bf16_f32 v112,v112,v113\n" \
"M v[132:135],a[8:11],a[152:155],v[132:135]\nds_read_b128 a[116:119],v241 offset:29696\nv_exp_f32 v114,v114\nM v[132:135],a[12:15],a[156:159],v[132:135]\nds_read_b128 a[120:123],v241 offset:30720\nv_cndmask_b32 v124,v124,v252,s[82:83]\ns_waitcnt lgkmcnt(9)\nM v[144:147],a[16:19],a[128:131],v[224:227]\n" \
"ds_read_b128 a[124:127],v241 offset:31744\nv_cndmask_b32 v125,v125,v252,s[84:85]\nM v[144:147],a[20:23],a[132:135],v[144:147]\nv_exp_f32 v115,v115\nv_cndmask_b32 v126,v126,v252,s[86:87]\nM v[144:147],a[24:27],a[136:139],v[144:147]\nv_exp_f32 v116,v116\nv_cndmask_b32 v127,v127,v252,s[88:89]\n" \
"M v[144:147],a[28:31],a[140:143],v[144:147]\nv_exp_f32 v117,v117\nv_cvt_pk_bf16_f32 v113,v114,v115\nM v[148:151],a[16:19],a[144:147],v[228:231]\nv_exp_f32 v118,v118\nM v[148:151],a[20:23],a[148:151],v[148:151]\nv_exp_f32 v119,v119\nv_mov_b32 v114,0\n" \
"M v[148:151],a[24:27],a[152:155],v[148:151]\nv_exp_f32 v124,v124\nv_mov_b32 v115,0\nM v[148:151],a[28:31],a[156:159],v[148:151]\nv_exp_f32 v125,v125\nM v[160:163],a[32:35],a[128:131],v[224:227]\nv_exp_f32 v126,v126\nv_cvt_pk_bf16_f32 v116,v116,v117\n" \
"M v[160:163],a[36:39],a[132:135],v[160:163]\nv_exp_f32 v127,v127\nv_cvt_pk_bf16_f32 v117,v118,v119\nM v[160:163],a[40:43],a[136:139],v[160:163]\nv_cvt_pk_bf16_f32 v118,v124,v125\nv_cvt_pk_bf16_f32 v119,v126,v127\nM v[160:163],a[44:47],a[140:143],v[160:163]\nds_read_b128 a[0:3],v240 offset:49152\n" \
"ds_read_b128 a[4:7],v240 offset:50176\ns_add_u32 m0,s64,131072\nM v[164:167],a[32:35],a[144:147],v[228:231]\nds_read_b128 a[8:11],v240 offset:51200\nv_exp_f32 v128,v128\nM v[164:167],a[36:39],a[148:151],v[164:167]\nds_read_b128 a[12:15],v240 offset:52224\nv_exp_f32 v129,v129\n" \
"M v[164:167],a[40:43],a[152:155],v[164:167]\nds_read_b128 a[16:19],v240 offset:53248\nv_cvt_pk_bf16_f32 v192,v128,v129\nM v[164:167],a[44:47],a[156:159],v[164:167]\nds_read_b128 a[20:23],v240 offset:54272\nv_exp_f32 v130,v130\nM v[176:179],a[48:51],a[128:131],v[224:227]\nds_read_b128 a[24:27],v240 offset:55296\n" \
"v_exp_f32 v131,v131\nM v[176:179],a[52:55],a[132:135],v[176:179]\nds_read_b128 a[28:31],v240 offset:56320\nv_cvt_pk_bf16_f32 v193,v130,v131\nM v[176:179],a[56:59],a[136:139],v[176:179]\nds_read_b128 a[32:35],v240 offset:57344\nv_exp_f32 v132,v132\nM v[176:179],a[60:63],a[140:143],v[176:179]\n" \
"ds_read_b128 a[36:39],v240 offset:58368\nv_exp_f32 v133,v133\nM v[180:183],a[48:51],a[144:147],v[228:231]\ns_waitcnt vmcnt(1) lgkmcnt(8)\ns_barrier\nds_read_b128 a[40:43],v240 offset:59392\nM v[180:183],a[52:55],a[148:151],v[180:183]\nds_read_b128 a[44:47],v240 offset:60416\n" \
"v_cvt_pk_bf16_f32 v196,v132,v133\nM v[180:183],a[56:59],a[152:155],v[180:183]\nds_read_b128 a[48:51],v240 offset:61440\nv_exp_f32 v134,v134\nM v[180:183],a[60:63],a[156:159],v[180:183]\nds_read_b128 a[52:55],v240 offset:62464\nv_exp_f32 v135,v135\nM v[0:3],a[64:67],v[96:99],v[0:3]\n" \
"ds_read_b128 a[56:59],v240 offset:63488\nv_cvt_pk_bf16_f32 v197,v134,v135\nM v[4:7],a[64:67],v[100:103],v[4:7]\nds_read_b128 a[60:63],v240 offset:64512\nv_exp_f32 v144,v144\nM v[8:11],a[68:71],v[96:99],v[8:11]\nds_read_b128 a[64:67],v241 offset:32768\nv_exp_f32 v145,v145\n" \
"M v[12:15],a[68:71],v[100:103],v[12:15]\nds_read_b128 a[68:71],v241 offset:33792\nv_cvt_pk_bf16_f32 v194,v144,v145\nM v[16:19],a[72:75],v[96:99],v[16:19]\nv_exp_f32 v146,v146\ns_waitcnt vmcnt(0)\nv_cmp_eq_u32 vcc,0,v248\nM v[20:23],a[72:75],v[100:103],v[20:23]\n" \
"ds_read_b128 a[72:75],v241 offset:34816\nv_exp_f32 v147,v147\nM v[24:27],a[76:79],v[96:99],v[24:27]\nv_cvt_pk_bf16_f32 v195,v146,v147\nv_exp_f32 v148,v148\nM v[28:31],a[76:79],v[100:103],v[28:31]\nds_read_b128 a[76:79],v241 offset:35840\ns_cbranch_vccnz .Lps5\n" \
".Lpb5:\nM v[32:35],a[80:83],v[96:99],v[32:35]\nv_exp_f32 v149,v149\nv_mov_b32 v189,0xff800000\nM v[36:39],a[80:83],v[100:103],v[36:39]\nds_read_b128 a[80:83],v241 offset:36864\nv_exp_f32 v150,v150\nM v[40:43],a[84:87],v[96:99],v[40:43]\n" \
"v_exp_f32 v151,v151\nM v[44:47],a[84:87],v[100:103],v[44:47]\nds_read_b128 a[84:87],v241 offset:37888\nv_cvt_pk_bf16_f32 v198,v148,v149\nv_cvt_pk_bf16_f32 v199,v150,v151\nM v[48:51],a[88:91],v[96:99],v[48:51]\nv_exp_f32 v160,v160\nv_mov_b32 v190,0xff800000\n" \
"M v[52:55],a[88:91],v[100:103],v[52:55]\nds_read_b128 a[88:91],v241 offset:38912\nv_mov_b32 v191,0xff800000\nM v[56:59],a[92:95],v[96:99],v[56:59]\nv_exp_f32 v161,v161\nv_mov_b32 v202,0\nM v[60:63],a[92:95],v[100:103],v[60:63]\nds_read_b128 a[92:95],v241 offset:39936\n" \
"v_exp_f32 v162,v162\nM a[192:195],a[208:211],v[96:99],a[192:195]\nv_exp_f32 v163,v163\nv_cvt_pk_bf16_f32 v208,v160,v161\nM a[196:199],a[208:211],v[100:103],a[196:199]\nv_cvt_pk_bf16_f32 v209,v162,v163\nv_mov_b32 v203,0\nM v[0:3],a[96:99],v[112:115],v[0:3]\n" \
"v_exp_f32 v164,v164\nM v[4:7],a[96:99],v[116:119],v[4:7]\nds_read_b128 a[96:99],v241 offset:40960\nv_exp_f32 v165,v165\nM v[8:11],a[100:103],v[112:115],v[8:11]\nv_exp_f32 v166,v166\nM v[12:15],a[100:103],v[116:119],v[12:15]\nds_read_b128 a[100:103],v241 offset:41984\n" \
"v_exp_f32 v167,v167\nM v[16:19],a[104:107],v[112:115],v[16:19]\nv_cvt_pk_bf16_f32 v212,v164,v165\nv_exp_f32 v176,v176\nM v[20:23],a[104:107],v[116:119],v[20:23]\nds_read_b128 a[104:107],v241 offset:43008\nv_exp_f32 v177,v177\nM v[24:27],a[108:111],v[112:115],v[24:27]\n" \
"v_cvt_pk_bf16_f32 v213,v166,v167\nv_cvt_pk_bf16_f32 v210,v176,v177\nM v[28:31],a[108:111],v[116:119],v[28:31]\nds_read_b128 a[108:111],v241 offset:44032\nv_exp_f32 v178,v178\nM v[32:35],a[112:115],v[112:115],v[32:35]\nv_exp_f32 v179,v179\nM v[36:39],a[112:115],v[116:119],v[36:39]\n" \
"ds_read_b128 a[112:115],v241 offset:45056\nv_cvt_pk_bf16_f32 v211,v178,v179\nM v[40:43],a[116:119],v[112:115],v[40:43]\nv_exp_f32 v180,v180\nM v[44:47],a[116:119],v[116:119],v[44:47]\nds_read_b128 a[116:119],v241 offset:46080\nv_exp_f32 v181,v181\nM v[48:51],a[120:123],v[112:115],v[48:51]\n" \
"v_exp_f32 v182,v182\nM v[52:55],a[120:123],v[116:119],v[52:55]\nds_read_b128 a[120:123],v241 offset:47104\nv_exp_f32 v183,v183\nM v[56:59],a[124:127],v[112:115],v[56:59]\nv_cvt_pk_bf16_f32 v214,v180,v181\nbuffer_load_dwordx4 v243,s[40:43],s69 offen offset:0 lds\nM v[60:63],a[124:127],v[116:119],v[60:63]\n" \
"ds_read_b128 a[124:127],v241 offset:48128\nv_cvt_pk_bf16_f32 v215,v182,v183\nM a[192:195],a[208:211],v[112:115],a[192:195]\nM a[196:199],a[208:211],v[116:119],a[196:199]\nM v[64:67],a[244:247],a[244:247],0\nbuffer_load_dwordx4 v244,s[40:43],s69 offen offset:1024 lds\nM v[68:71],a[244:247],a[244:247],0\nM v[72:75],a[244:247],a[244:247],0\n" \
"M v[76:79],a[244:247],a[244:247],0\nM v[80:83],a[244:247],a[244:247],0\nbuffer_load_dwordx4 v245,s[40:43],s69 offen offset:2048 lds\nM v[84:87],a[244:247],a[244:247],0\nM v[88:91],a[244:247],a[244:247],0\nM v[92:95],a[244:247],a[244:247],0\nM v[96:99],a[244:247],a[244:247],0\nbuffer_load_dwordx4 v246,s[40:43],s69 offen offset:3072 lds\n" \
"M v[100:103],a[244:247],a[244:247],0\ns_lshl4_add_u32 s69,s13,s68\ns_add_u32 s69,s69,0x10000\ns_add_u32 m0,s64,147456\nM v[104:107],a[244:247],a[244:247],0\nM v[108:111],a[244:247],a[244:247],0\nM v[112:115],a[244:247],a[244:247],0\nbuffer_load_dwordx4 v243,s[40:43],s69 offen offset:0 lds\n" \
"M v[116:119],a[244:247],a[244:247],0\nM v[120:123],a[244:247],a[244:247],0\nM v[124:127],a[244:247],a[244:247],0\nM a[200:203],a[244:247],a[244:247],0\nbuffer_load_dwordx4 v244,s[40:43],s69 offen offset:1024 lds\nM a[204:207],a[244:247],a[244:247],0\nM v[136:139],a[0:3],a[160:163],0\nM v[136:139],a[4:7],a[164:167],v[136:139]\n" \
"s_waitcnt lgkmcnt(0)\nM v[136:139],a[8:11],a[168:171],v[136:139]\nbuffer_load_dwordx4 v245,s[40:43],s69 offen offset:2048 lds\nM v[136:139],a[12:15],a[172:175],v[136:139]\nM v[140:143],a[0:3],a[176:179],0\nM v[140:143],a[4:7],a[180:183],v[140:143]\nM v[140:143],a[8:11],a[184:187],v[140:143]\nbuffer_load_dwordx4 v246,s[40:43],s69 offen offset:3072 lds\n" \
"s_add_u32 m0,s64,65536\nM v[140:143],a[12:15],a[188:191],v[140:143]\nM v[156:159],a[16:19],a[176:179],0\nM v[156:159],a[20:23],a[180:183],v[156:159]\nv_max3_f32 v233,v136,v137,v138\nv_max_f32 v233,v233,v139\nv_cndmask_b32 v233,v232,v233,s[90:91]\nM v[156:159],a[24:27],a[184:187],v[156:159]\n" \
"buffer_load_dwordx4 v240,s[48:51],s36 offen offset:0 sc1 lds\nM v[156:159],a[28:31],a[188:191],v[156:159]\nv_max3_f32 v237,v140,v141,v142\nv_cndmask_b32 v136,v136,v252,s[82:83]\nv_cndmask_b32 v137,v137,v252,s[84:85]\nM v[128:131],a[0:3],a[128:131],v[224:227]\nv_cndmask_b32 v138,v138,v252,s[86:87]\nv_cndmask_b32 v139,v139,v252,s[88:89]\n" \
"M v[128:131],a[4:7],a[132:135],v[128:131]\nM v[128:131],a[8:11],a[136:139],v[128:131]\ns_barrier\nv_max3_f32 v237,v237,v143,v156\nM v[128:131],a[12:15],a[140:143],v[128:131]\nv_max3_f32 v237,v237,v157,v158\nv_max_f32 v237,v237,v159\nv_cndmask_b32 v237,v236,v237,s[90:91]\n" \
"M v[132:135],a[0:3],a[144:147],v[228:231]\nds_read_b128 a[0:3],v240 offset:0\nbuffer_load_dwordx4 v240,s[48:51],s36 offen offset:1024 sc1 lds\nM v[132:135],a[4:7],a[148:151],v[132:135]\nds_read_b128 a[4:7],v240 offset:1024\nv_permlane16_swap_b32 v233,v237\nM v[132:135],a[8:11],a[152:155],v[132:135]\nds_read_b128 a[8:11],v240 offset:2048\n" \
"v_max_f32 v233,v233,v237\nv_mov_b32 v237,v233\nM v[132:135],a[12:15],a[156:159],v[132:135]\nds_read_b128 a[12:15],v240 offset:3072\nv_permlane32_swap_b32 v233,v237\nv_max_f32 v233,v233,v237\nM v[144:147],a[16:19],a[128:131],v[224:227]\nbuffer_load_dwordx4 v240,s[48:51],s36 offen offset:2048 sc1 lds\n" \
"M v[144:147],a[20:23],a[132:135],v[144:147]\nv_mov_b32 v237,v233\nv_cndmask_b32 v156,v156,v252,s[82:83]\nv_cndmask_b32 v157,v157,v252,s[84:85]\nM v[144:147],a[24:27],a[136:139],v[144:147]\nv_permlane16_swap_b32 v233,v237\nv_cndmask_b32 v158,v158,v252,s[86:87]\nv_cndmask_b32 v159,v159,v252,s[88:89]\n" \
"M v[144:147],a[28:31],a[140:143],v[144:147]\nv_sub_f32 v232,0,v233\nv_sub_f32 v236,0,v237\nv_add_f32 v136,v136,v232\nM v[148:151],a[16:19],a[144:147],v[228:231]\nds_read_b128 a[16:19],v240 offset:4096\nv_add_f32 v137,v137,v232\nM v[148:151],a[20:23],a[148:151],v[148:151]\n" \
"ds_read_b128 a[20:23],v240 offset:5120\nbuffer_load_dwordx4 v240,s[48:51],s36 offen offset:3072 sc1 lds\nM v[148:151],a[24:27],a[152:155],v[148:151]\nds_read_b128 a[24:27],v240 offset:6144\ns_add_u32 m0,s64,16384\nv_add_f32 v138,v138,v232\nM v[148:151],a[28:31],a[156:159],v[148:151]\nds_read_b128 a[28:31],v240 offset:7168\n" \
"v_add_f32 v139,v139,v232\nM v[160:163],a[32:35],a[128:131],v[224:227]\nv_add_f32 v140,v140,v236\nv_add_f32 v141,v141,v236\nv_add_f32 v142,v142,v236\nM v[160:163],a[36:39],a[132:135],v[160:163]\nbuffer_load_dwordx4 v240,s[44:47],s65 offen offset:0 sc1 lds\nv_add_f32 v143,v143,v236\n" \
"M v[160:163],a[40:43],a[136:139],v[160:163]\nv_add_f32 v156,v156,v236\nv_add_f32 v157,v157,v236\nM v[160:163],a[44:47],a[140:143],v[160:163]\nv_add_f32 v158,v158,v236\nv_add_f32 v159,v159,v236\nv_sub_f32 v234,0,v233\nM v[164:167],a[32:35],a[144:147],v[228:231]\n" \
"ds_read_b128 a[32:35],v240 offset:8192\nv_exp_f32 v136,v136\nM v[164:167],a[36:39],a[148:151],v[164:167]\nds_read_b128 a[36:39],v240 offset:9216\nbuffer_load_dwordx4 v240,s[44:47],s65 offen offset:1024 sc1 lds\nM v[164:167],a[40:43],a[152:155],v[164:167]\nds_read_b128 a[40:43],v240 offset:10240\nv_sub_f32 v235,0,v233\n" \
"M v[164:167],a[44:47],a[156:159],v[164:167]\nds_read_b128 a[44:47],v240 offset:11264\nv_exp_f32 v137,v137\nM v[176:179],a[48:51],a[128:131],v[224:227]\nv_exp_f32 v138,v138\nv_sub_f32 v238,0,v237\nM v[176:179],a[52:55],a[132:135],v[176:179]\nbuffer_load_dwordx4 v240,s[44:47],s65 offen offset:2048 sc1 lds\n" \
"M v[176:179],a[56:59],a[136:139],v[176:179]\nv_exp_f32 v139,v139\nv_sub_f32 v239,0,v237\nM v[176:179],a[60:63],a[140:143],v[176:179]\nv_exp_f32 v140,v140\nv_sub_f32 v233,0,v233\nM v[180:183],a[48:51],a[144:147],v[228:231]\nds_read_b128 a[48:51],v240 offset:12288\n" \
"v_exp_f32 v141,v141\nM v[180:183],a[52:55],a[148:151],v[180:183]\nbuffer_load_dwordx4 v240,s[44:47],s65 offen offset:3072 sc1 lds\nM v[180:183],a[56:59],a[152:155],v[180:183]\nds_read_b128 a[52:55],v240 offset:13312\ns_add_u32 m0,s64,81920\nds_read_b128 a[56:59],v240 offset:14336\nM v[180:183],a[60:63],a[156:159],v[180:183]\n" \
"ds_read_b128 a[60:63],v240 offset:15360\nv_exp_f32 v142,v142\nM v[0:3],a[64:67],v[192:195],v[0:3]\nv_exp_f32 v143,v143\nM v[4:7],a[64:67],v[196:199],v[4:7]\nbuffer_load_dwordx4 v240,s[48:51],s65 offen offset:0 sc1 lds\nv_sub_f32 v237,0,v237\nM v[8:11],a[68:71],v[192:195],v[8:11]\n" \
"v_exp_f32 v156,v156\nv_cvt_pk_bf16_f32 v200,v136,v137\nM v[12:15],a[68:71],v[196:199],v[12:15]\nv_exp_f32 v157,v157\nM v[16:19],a[72:75],v[192:195],v[16:19]\nv_exp_f32 v158,v158\nv_cvt_pk_bf16_f32 v201,v138,v139\nM v[20:23],a[72:75],v[196:199],v[20:23]\n" \
"buffer_load_dwordx4 v240,s[48:51],s65 offen offset:1024 sc1 lds\nv_cvt_pk_bf16_f32 v204,v140,v141\nM v[24:27],a[76:79],v[192:195],v[24:27]\nv_exp_f32 v159,v159\nv_cvt_pk_bf16_f32 v205,v142,v143\nM v[28:31],a[76:79],v[196:199],v[28:31]\nv_exp_f32 v128,v128\nM v[32:35],a[80:83],v[192:195],v[32:35]\n" \
"v_exp_f32 v129,v129\nv_cvt_pk_bf16_f32 v206,v156,v157\nM v[36:39],a[80:83],v[196:199],v[36:39]\nbuffer_load_dwordx4 v240,s[48:51],s65 offen offset:2048 sc1 lds\nv_cvt_pk_bf16_f32 v207,v158,v159\nM v[40:43],a[84:87],v[192:195],v[40:43]\nv_exp_f32 v130,v130\nM v[44:47],a[84:87],v[196:199],v[44:47]\n" \
"v_exp_f32 v131,v131\nM v[48:51],a[88:91],v[192:195],v[48:51]\nv_exp_f32 v132,v132\nM v[52:55],a[88:91],v[196:199],v[52:55]\nbuffer_load_dwordx4 v240,s[48:51],s65 offen offset:3072 sc1 lds\nM v[56:59],a[92:95],v[192:195],v[56:59]\ns_add_u32 m0,s64,32768\nv_exp_f32 v133,v133\n" \
"M v[60:63],a[92:95],v[196:199],v[60:63]\nv_exp_f32 v134,v134\nM a[192:195],a[208:211],v[192:195],a[192:195]\nv_exp_f32 v135,v135\nv_cvt_pk_bf16_f32 v192,v128,v129\nM a[196:199],a[208:211],v[196:199],a[196:199]\nbuffer_load_dwordx4 v240,s[44:47],s66 offen offset:0 sc1 lds\nM v[0:3],a[96:99],v[208:211],v[0:3]\n" \
"v_exp_f32 v144,v144\nv_cvt_pk_bf16_f32 v193,v130,v131\nM v[4:7],a[96:99],v[212:215],v[4:7]\nv_exp_f32 v145,v145\nv_cvt_pk_bf16_f32 v196,v132,v133\nM v[8:11],a[100:103],v[208:211],v[8:11]\nv_exp_f32 v146,v146\nM v[12:15],a[100:103],v[212:215],v[12:15]\n" \
"buffer_load_dwordx4 v240,s[44:47],s66 offen offset:1024 sc1 lds\nv_cvt_pk_bf16_f32 v194,v144,v145\nM v[16:19],a[104:107],v[208:211],v[16:19]\nv_exp_f32 v147,v147\nv_cvt_pk_bf16_f32 v197,v134,v135\nM v[20:23],a[104:107],v[212:215],v[20:23]\nv_exp_f32 v148,v148\nv_cvt_pk_bf16_f32 v195,v146,v147\n" \
"M v[24:27],a[108:111],v[208:211],v[24:27]\nv_exp_f32 v149,v149\nM v[28:31],a[108:111],v[212:215],v[28:31]\nbuffer_load_dwordx4 v240,s[44:47],s66 offen offset:2048 sc1 lds\nv_cvt_pk_bf16_f32 v198,v148,v149\nM v[32:35],a[112:115],v[208:211],v[32:35]\nv_exp_f32 v150,v150\nM v[36:39],a[112:115],v[212:215],v[36:39]\n" \
"v_exp_f32 v151,v151\nM v[40:43],a[116:119],v[208:211],v[40:43]\nv_exp_f32 v160,v160\nv_cvt_pk_bf16_f32 v199,v150,v151\nM v[44:47],a[116:119],v[212:215],v[44:47]\nbuffer_load_dwordx4 v240,s[44:47],s66 offen offset:3072 sc1 lds\ns_add_u32 m0,s64,98304\nM v[48:51],a[120:123],v[208:211],v[48:51]\n" \
"v_exp_f32 v161,v161\nM v[52:55],a[120:123],v[212:215],v[52:55]\nv_exp_f32 v162,v162\nM v[56:59],a[124:127],v[208:211],v[56:59]\nv_exp_f32 v163,v163\nM v[60:63],a[124:127],v[212:215],v[60:63]\nv_exp_f32 v164,v164\nM a[192:195],a[208:211],v[208:211],a[192:195]\n" \
"v_exp_f32 v165,v165\nv_exp_f32 v166,v166\nv_exp_f32 v167,v167\nv_exp_f32 v176,v176\nv_exp_f32 v177,v177\nv_exp_f32 v178,v178\nv_exp_f32 v179,v179\nv_exp_f32 v180,v180\n" \
"v_exp_f32 v181,v181\nv_exp_f32 v182,v182\nv_exp_f32 v183,v183\nbuffer_load_dwordx4 v240,s[48:51],s66 offen offset:0 sc1 lds\nbuffer_load_dwordx4 v240,s[48:51],s66 offen offset:1024 sc1 lds\nbuffer_load_dwordx4 v240,s[48:51],s66 offen offset:2048 sc1 lds\nbuffer_load_dwordx4 v240,s[48:51],s66 offen offset:3072 sc1 lds\nM a[196:199],a[208:211],v[212:215],a[196:199]\n" \
"s_cmp_eq_u32 s62,0\ns_cbranch_scc1 .Ltrans\n.Ldual:\ns_waitcnt lgkmcnt(4)\nM v[128:131],a[0:3],a[128:131],v[224:227]\ns_add_u32 m0,s94,s64\nv_cvt_pk_bf16_f32 v208,v160,v161\nM v[128:131],a[4:7],a[132:135],v[128:131]\n" \
"ds_read_b128 a[64:67],v250 offset:0\nds_read_b128 a[68:71],v250 offset:1024\nM v[128:131],a[8:11],a[136:139],v[128:131]\nv_cvt_pk_bf16_f32 v209,v162,v163\nv_cvt_pk_bf16_f32 v212,v164,v165\nM v[128:131],a[12:15],a[140:143],v[128:131]\nv_cvt_pk_bf16_f32 v213,v166,v167\nv_cvt_pk_bf16_f32 v216,v168,v169\n" \
"M v[132:135],a[0:3],a[144:147],v[228:231]\nbuffer_load_dwordx4 v240,s[44:47],s95 offen offset:0 sc1 lds\nM v[132:135],a[4:7],a[148:151],v[132:135]\nv_cvt_pk_bf16_f32 v217,v170,v171\nv_cvt_pk_bf16_f32 v210,v176,v177\nM v[132:135],a[8:11],a[152:155],v[132:135]\nv_exp_f32 v172,v172\nM v[132:135],a[12:15],a[156:159],v[132:135]\n" \
"ds_read_b128 a[72:75],v250 offset:2048\nds_read_b128 a[76:79],v250 offset:3072\nM v[136:139],a[0:3],a[160:163],v[232:235]\nv_exp_f32 v173,v173\nM v[136:139],a[4:7],a[164:167],v[136:139]\nv_exp_f32 v174,v174\nM v[136:139],a[8:11],a[168:171],v[136:139]\nv_exp_f32 v175,v175\n" \
"M v[136:139],a[12:15],a[172:175],v[136:139]\nv_cvt_pk_bf16_f32 v220,v172,v173\nv_cvt_pk_bf16_f32 v221,v174,v175\nM v[140:143],a[0:3],a[176:179],v[236:239]\nv_cvt_pk_bf16_f32 v211,v178,v179\nv_cvt_pk_bf16_f32 v214,v180,v181\nM v[140:143],a[4:7],a[180:183],v[140:143]\nds_read_b128 a[80:83],v250 offset:4096\n" \
"ds_read_b128 a[84:87],v250 offset:5120\nM v[140:143],a[8:11],a[184:187],v[140:143]\nv_cvt_pk_bf16_f32 v215,v182,v183\ns_add_u32 s94,s94,0x4000\nM v[140:143],a[12:15],a[188:191],v[140:143]\nv_exp_f32 v184,v184\nM v[144:147],a[16:19],a[128:131],v[224:227]\nv_exp_f32 v185,v185\n" \
"M v[144:147],a[20:23],a[132:135],v[144:147]\nv_exp_f32 v186,v186\nM v[144:147],a[24:27],a[136:139],v[144:147]\nv_exp_f32 v187,v187\nM v[144:147],a[28:31],a[140:143],v[144:147]\nds_read_b128 a[88:91],v250 offset:6144\nds_read_b128 a[92:95],v250 offset:7168\nM v[148:151],a[16:19],a[144:147],v[228:231]\n" \
"v_cvt_pk_bf16_f32 v218,v184,v185\nv_cvt_pk_bf16_f32 v219,v186,v187\nM v[148:151],a[20:23],a[148:151],v[148:151]\nv_exp_f32 v188,v188\nM v[148:151],a[24:27],a[152:155],v[148:151]\nbuffer_load_dwordx4 v240,s[44:47],s95 offen offset:1024 sc1 lds\nM v[148:151],a[28:31],a[156:159],v[148:151]\nv_exp_f32 v189,v189\n" \
"M v[152:155],a[16:19],a[160:163],v[232:235]\nv_exp_f32 v190,v190\nM v[152:155],a[20:23],a[164:167],v[152:155]\nds_read_b128 a[96:99],v250 offset:8192\nds_read_b128 a[100:103],v250 offset:9216\nM v[152:155],a[24:27],a[168:171],v[152:155]\nv_exp_f32 v191,v191\nM v[152:155],a[28:31],a[172:175],v[152:155]\n" \
"v_cvt_pk_bf16_f32 v222,v188,v189\nv_cvt_pk_bf16_f32 v223,v190,v191\nM v[156:159],a[16:19],a[176:179],v[236:239]\nv_exp_f32 v128,v128\nM v[156:159],a[20:23],a[180:183],v[156:159]\nv_exp_f32 v129,v129\nM v[156:159],a[24:27],a[184:187],v[156:159]\nv_exp_f32 v130,v130\n" \
"M v[156:159],a[28:31],a[188:191],v[156:159]\nds_read_b128 a[104:107],v250 offset:10240\nds_read_b128 a[108:111],v250 offset:11264\nM v[160:163],a[32:35],a[128:131],v[224:227]\nv_exp_f32 v131,v131\nM v[160:163],a[36:39],a[132:135],v[160:163]\nv_exp_f32 v144,v144\nM v[160:163],a[40:43],a[136:139],v[160:163]\n" \
"v_exp_f32 v145,v145\nM v[160:163],a[44:47],a[140:143],v[160:163]\nv_exp_f32 v146,v146\nM v[164:167],a[32:35],a[144:147],v[228:231]\nv_exp_f32 v147,v147\nM v[164:167],a[36:39],a[148:151],v[164:167]\nds_read_b128 a[112:115],v250 offset:12288\nds_read_b128 a[116:119],v250 offset:13312\n" \
"M v[164:167],a[40:43],a[152:155],v[164:167]\nv_exp_f32 v132,v132\nM v[164:167],a[44:47],a[156:159],v[164:167]\nv_exp_f32 v133,v133\nM v[168:171],a[32:35],a[160:163],v[232:235]\nbuffer_load_dwordx4 v240,s[44:47],s95 offen offset:2048 sc1 lds\nM v[168:171],a[36:39],a[164:167],v[168:171]\nv_exp_f32 v134,v134\n" \
"M v[168:171],a[40:43],a[168:171],v[168:171]\nv_exp_f32 v135,v135\nM v[168:171],a[44:47],a[172:175],v[168:171]\nds_read_b128 a[120:123],v250 offset:14336\nds_read_b128 a[124:127],v250 offset:15360\nM v[172:175],a[32:35],a[176:179],v[236:239]\nv_xor_b32 v250,0x18000,v250\ns_and_b32 s94,s94,0xffff\n" \
"M v[172:175],a[36:39],a[180:183],v[172:175]\nv_exp_f32 v148,v148\nM v[172:175],a[40:43],a[184:187],v[172:175]\nv_exp_f32 v149,v149\nM v[172:175],a[44:47],a[188:191],v[172:175]\ns_waitcnt lgkmcnt(4)\nM v[176:179],a[48:51],a[128:131],v[224:227]\nv_exp_f32 v150,v150\n" \
"M v[176:179],a[52:55],a[132:135],v[176:179]\nv_exp_f32 v151,v151\nM v[176:179],a[56:59],a[136:139],v[176:179]\nv_exp_f32 v136,v136\nM v[176:179],a[60:63],a[140:143],v[176:179]\nv_exp_f32 v137,v137\nM v[180:183],a[48:51],a[144:147],v[228:231]\nv_exp_f32 v138,v138\n" \
"M v[180:183],a[52:55],a[148:151],v[180:183]\nv_exp_f32 v139,v139\nM v[180:183],a[56:59],a[152:155],v[180:183]\nv_exp_f32 v152,v152\nM v[180:183],a[60:63],a[156:159],v[180:183]\nv_exp_f32 v153,v153\nM v[184:187],a[48:51],a[160:163],v[232:235]\nv_exp_f32 v154,v154\n" \
"M v[184:187],a[52:55],a[164:167],v[184:187]\nv_exp_f32 v155,v155\nM v[184:187],a[56:59],a[168:171],v[184:187]\nbuffer_load_dwordx4 v240,s[44:47],s95 offen offset:3072 sc1 lds\nM v[184:187],a[60:63],a[172:175],v[184:187]\nv_exp_f32 v140,v140\nM v[188:191],a[48:51],a[176:179],v[236:239]\nv_exp_f32 v141,v141\n" \
"M v[188:191],a[52:55],a[180:183],v[188:191]\nv_exp_f32 v142,v142\nM v[188:191],a[56:59],a[184:187],v[188:191]\nv_exp_f32 v143,v143\nM v[188:191],a[60:63],a[188:191],v[188:191]\nv_exp_f32 v156,v156\nM v[0:3],a[64:67],v[192:195],v[0:3]\ns_waitcnt vmcnt(16) lgkmcnt(0)\n" \
"s_barrier\nM v[4:7],a[64:67],v[196:199],v[4:7]\nds_read_b128 a[0:3],v250 offset:0\nds_read_b128 a[4:7],v250 offset:1024\nM v[64:67],a[64:67],v[200:203],v[64:67]\nv_exp_f32 v157,v157\nM v[68:71],a[64:67],v[204:207],v[68:71]\nv_exp_f32 v158,v158\n" \
"M v[8:11],a[68:71],v[192:195],v[8:11]\ns_add_u32 m0,m0,0x10000\nM v[12:15],a[68:71],v[196:199],v[12:15]\nbuffer_load_dwordx4 v240,s[48:51],s95 offen offset:0 sc1 lds\nM v[72:75],a[68:71],v[200:203],v[72:75]\nv_exp_f32 v159,v159\nM v[76:79],a[68:71],v[204:207],v[76:79]\nv_exp_f32 v160,v160\n" \
"M v[16:19],a[72:75],v[192:195],v[16:19]\nv_exp_f32 v161,v161\nM v[20:23],a[72:75],v[196:199],v[20:23]\nds_read_b128 a[8:11],v250 offset:2048\nds_read_b128 a[12:15],v250 offset:3072\nM v[80:83],a[72:75],v[200:203],v[80:83]\nv_exp_f32 v162,v162\nM v[84:87],a[72:75],v[204:207],v[84:87]\n" \
"v_exp_f32 v163,v163\nM v[24:27],a[76:79],v[192:195],v[24:27]\nv_exp_f32 v176,v176\nM v[28:31],a[76:79],v[196:199],v[28:31]\nv_exp_f32 v177,v177\nM v[88:91],a[76:79],v[200:203],v[88:91]\nv_exp_f32 v178,v178\nM v[92:95],a[76:79],v[204:207],v[92:95]\n" \
"v_exp_f32 v179,v179\nM v[32:35],a[80:83],v[192:195],v[32:35]\nv_exp_f32 v164,v164\nM v[36:39],a[80:83],v[196:199],v[36:39]\nds_read_b128 a[16:19],v250 offset:4096\nds_read_b128 a[20:23],v250 offset:5120\nM v[96:99],a[80:83],v[200:203],v[96:99]\nv_exp_f32 v165,v165\n" \
"M v[100:103],a[80:83],v[204:207],v[100:103]\nv_exp_f32 v166,v166\nM v[40:43],a[84:87],v[192:195],v[40:43]\nv_exp_f32 v167,v167\nM v[44:47],a[84:87],v[196:199],v[44:47]\nbuffer_load_dwordx4 v240,s[48:51],s95 offen offset:1024 sc1 lds\nM v[104:107],a[84:87],v[200:203],v[104:107]\nv_exp_f32 v180,v180\n" \
"M v[108:111],a[84:87],v[204:207],v[108:111]\nv_exp_f32 v181,v181\nM v[48:51],a[88:91],v[192:195],v[48:51]\nv_exp_f32 v182,v182\nM v[52:55],a[88:91],v[196:199],v[52:55]\nds_read_b128 a[24:27],v250 offset:6144\nds_read_b128 a[28:31],v250 offset:7168\nM v[112:115],a[88:91],v[200:203],v[112:115]\n" \
"v_exp_f32 v183,v183\nM v[116:119],a[88:91],v[204:207],v[116:119]\nv_exp_f32 v168,v168\nM v[56:59],a[92:95],v[192:195],v[56:59]\nv_exp_f32 v169,v169\nM v[60:63],a[92:95],v[196:199],v[60:63]\nv_exp_f32 v170,v170\nM v[120:123],a[92:95],v[200:203],v[120:123]\n" \
"v_exp_f32 v171,v171\nM v[124:127],a[92:95],v[204:207],v[124:127]\nM a[192:195],a[208:211],v[192:195],a[192:195]\nv_cvt_pk_bf16_f32 v192,v128,v129\nv_cvt_pk_bf16_f32 v193,v130,v131\nM a[196:199],a[208:211],v[196:199],a[196:199]\nds_read_b128 a[32:35],v250 offset:8192\nds_read_b128 a[36:39],v250 offset:9216\n" \
"M a[200:203],a[208:211],v[200:203],a[200:203]\nv_cvt_pk_bf16_f32 v194,v144,v145\nv_cvt_pk_bf16_f32 v195,v146,v147\nM a[204:207],a[208:211],v[204:207],a[204:207]\nv_cvt_pk_bf16_f32 v196,v132,v133\nv_cvt_pk_bf16_f32 v197,v134,v135\nM v[0:3],a[96:99],v[208:211],v[0:3]\nv_cvt_pk_bf16_f32 v198,v148,v149\n" \
"v_cvt_pk_bf16_f32 v199,v150,v151\nM v[4:7],a[96:99],v[212:215],v[4:7]\nbuffer_load_dwordx4 v240,s[48:51],s95 offen offset:2048 sc1 lds\nM v[64:67],a[96:99],v[216:219],v[64:67]\nv_cvt_pk_bf16_f32 v200,v136,v137\nv_cvt_pk_bf16_f32 v201,v138,v139\nM v[68:71],a[96:99],v[220:223],v[68:71]\nv_cvt_pk_bf16_f32 v202,v152,v153\n" \
"v_cvt_pk_bf16_f32 v203,v154,v155\nM v[8:11],a[100:103],v[208:211],v[8:11]\nv_cvt_pk_bf16_f32 v204,v140,v141\nv_cvt_pk_bf16_f32 v205,v142,v143\nM v[12:15],a[100:103],v[212:215],v[12:15]\nds_read_b128 a[40:43],v250 offset:10240\nds_read_b128 a[44:47],v250 offset:11264\nM v[72:75],a[100:103],v[216:219],v[72:75]\n" \
"v_cvt_pk_bf16_f32 v206,v156,v157\nv_cvt_pk_bf16_f32 v207,v158,v159\nM v[76:79],a[100:103],v[220:223],v[76:79]\nM v[16:19],a[104:107],v[208:211],v[16:19]\nM v[20:23],a[104:107],v[212:215],v[20:23]\nM v[80:83],a[104:107],v[216:219],v[80:83]\nM v[84:87],a[104:107],v[220:223],v[84:87]\nM v[24:27],a[108:111],v[208:211],v[24:27]\n" \
"M v[28:31],a[108:111],v[212:215],v[28:31]\nds_read_b128 a[48:51],v250 offset:12288\nds_read_b128 a[52:55],v250 offset:13312\nM v[88:91],a[108:111],v[216:219],v[88:91]\nM v[92:95],a[108:111],v[220:223],v[92:95]\nM v[32:35],a[112:115],v[208:211],v[32:35]\nM v[36:39],a[112:115],v[212:215],v[36:39]\nbuffer_load_dwordx4 v240,s[48:51],s95 offen offset:3072 sc1 lds\n" \
"M v[96:99],a[112:115],v[216:219],v[96:99]\ns_cmp_eq_u32 s95,s96\ns_cselect_b32 s72,0xc000,s98\ns_add_u32 s95,s95,s72\nM v[100:103],a[112:115],v[220:223],v[100:103]\nM v[40:43],a[116:119],v[208:211],v[40:43]\nM v[44:47],a[116:119],v[212:215],v[44:47]\nds_read_b128 a[56:59],v250 offset:14336\n" \
"ds_read_b128 a[60:63],v250 offset:15360\nM v[104:107],a[116:119],v[216:219],v[104:107]\nv_add_u32 v250,s94,v241\nM v[108:111],a[116:119],v[220:223],v[108:111]\nM v[48:51],a[120:123],v[208:211],v[48:51]\nM v[52:55],a[120:123],v[212:215],v[52:55]\nM v[112:115],a[120:123],v[216:219],v[112:115]\nM v[116:119],a[120:123],v[220:223],v[116:119]\n" \
"M v[56:59],a[124:127],v[208:211],v[56:59]\nM v[60:63],a[124:127],v[212:215],v[60:63]\nM v[120:123],a[124:127],v[216:219],v[120:123]\nM v[124:127],a[124:127],v[220:223],v[124:127]\nM a[192:195],a[208:211],v[208:211],a[192:195]\nM a[196:199],a[208:211],v[212:215],a[196:199]\nM a[200:203],a[208:211],v[216:219],a[200:203]\nM a[204:207],a[208:211],v[220:223],a[204:207]\n" \
"s_cmp_lg_u32 s95,s97\ns_cbranch_scc1 .Ldual\ns_cmp_eq_u32 s62,7\ns_cbranch_scc1 .Lod1x\n.Ltrans:\ns_waitcnt lgkmcnt(4)\nM v[128:131],a[0:3],a[128:131],v[224:227]\ns_add_u32 m0,s94,s64\n" \
"v_cvt_pk_bf16_f32 v208,v160,v161\nM v[128:131],a[4:7],a[132:135],v[128:131]\nds_read_b128 a[64:67],v250 offset:0\nds_read_b128 a[68:71],v250 offset:1024\nM v[128:131],a[8:11],a[136:139],v[128:131]\nbuffer_load_dwordx4 v240,s[44:47],s95 offen offset:0 sc1 lds\nM v[128:131],a[12:15],a[140:143],v[128:131]\nv_cvt_pk_bf16_f32 v209,v162,v163\n" \
"v_cvt_pk_bf16_f32 v212,v164,v165\nM v[132:135],a[0:3],a[144:147],v[228:231]\nds_read_b128 a[72:75],v250 offset:2048\nds_read_b128 a[76:79],v250 offset:3072\nM v[132:135],a[4:7],a[148:151],v[132:135]\nbuffer_store_dwordx4 a[248:251],v253,s[52:55],s57 offen offset:0 sc1\nM v[132:135],a[8:11],a[152:155],v[132:135]\nv_cvt_pk_bf16_f32 v213,v166,v167\n" \
"v_cvt_pk_bf16_f32 v210,v176,v177\nM v[132:135],a[12:15],a[156:159],v[132:135]\nds_read_b128 a[80:83],v250 offset:4096\nds_read_b128 a[84:87],v250 offset:5120\nM v[144:147],a[16:19],a[128:131],v[224:227]\nv_cvt_pk_bf16_f32 v211,v178,v179\nv_cvt_pk_bf16_f32 v214,v180,v181\nM v[144:147],a[20:23],a[132:135],v[144:147]\n" \
"v_cvt_pk_bf16_f32 v215,v182,v183\ns_add_u32 s71,s57,16384\nM v[144:147],a[24:27],a[136:139],v[144:147]\nds_read_b128 a[88:91],v250 offset:6144\nds_read_b128 a[92:95],v250 offset:7168\nM v[144:147],a[28:31],a[140:143],v[144:147]\nbuffer_load_dwordx4 v240,s[44:47],s95 offen offset:1024 sc1 lds\nM v[148:151],a[16:19],a[144:147],v[228:231]\n" \
"v_exp_f32 v184,v184\nM v[148:151],a[20:23],a[148:151],v[148:151]\nds_read_b128 a[96:99],v250 offset:8192\nds_read_b128 a[100:103],v250 offset:9216\nM v[148:151],a[24:27],a[152:155],v[148:151]\nbuffer_store_dwordx4 a[248:251],v253,s[52:55],s57 offen offset:1024 sc1\nM v[148:151],a[28:31],a[156:159],v[148:151]\nv_exp_f32 v185,v185\n" \
"M v[160:163],a[32:35],a[128:131],v[224:227]\nds_read_b128 a[104:107],v250 offset:10240\nds_read_b128 a[108:111],v250 offset:11264\nM v[160:163],a[36:39],a[132:135],v[160:163]\nv_exp_f32 v186,v186\nM v[160:163],a[40:43],a[136:139],v[160:163]\nv_exp_f32 v187,v187\nM v[160:163],a[44:47],a[140:143],v[160:163]\n" \
"ds_read_b128 a[112:115],v250 offset:12288\nds_read_b128 a[116:119],v250 offset:13312\nM v[164:167],a[32:35],a[144:147],v[228:231]\nbuffer_load_dwordx4 v240,s[44:47],s95 offen offset:2048 sc1 lds\nM v[164:167],a[36:39],a[148:151],v[164:167]\nv_exp_f32 v172,v172\nM v[164:167],a[40:43],a[152:155],v[164:167]\nds_read_b128 a[120:123],v250 offset:14336\n" \
"ds_read_b128 a[124:127],v250 offset:15360\nM v[164:167],a[44:47],a[156:159],v[164:167]\nbuffer_store_dwordx4 a[248:251],v253,s[52:55],s57 offen offset:2048 sc1\ns_waitcnt lgkmcnt(6)\nM v[176:179],a[48:51],a[128:131],v[224:227]\nv_xor_b32 v250,0x18000,v250\nv_cvt_pk_bf16_f32 v216,v168,v169\nM v[176:179],a[52:55],a[132:135],v[176:179]\n" \
"v_exp_f32 v173,v173\nM v[176:179],a[56:59],a[136:139],v[176:179]\nv_exp_f32 v174,v174\nM v[176:179],a[60:63],a[140:143],v[176:179]\nv_exp_f32 v175,v175\nM v[180:183],a[48:51],a[144:147],v[228:231]\nv_exp_f32 v188,v188\nM v[180:183],a[52:55],a[148:151],v[180:183]\n" \
"buffer_load_dwordx4 v240,s[44:47],s95 offen offset:3072 sc1 lds\nM v[180:183],a[56:59],a[152:155],v[180:183]\ns_add_u32 m0,m0,0x10000\nv_cvt_pk_bf16_f32 v217,v170,v171\nM v[180:183],a[60:63],a[156:159],v[180:183]\nv_exp_f32 v189,v189\nM v[0:3],a[64:67],v[192:195],v[0:3]\nbuffer_store_dwordx4 a[248:251],v253,s[52:55],s57 offen offset:3072 sc1\n" \
"M v[4:7],a[64:67],v[196:199],v[4:7]\nv_exp_f32 v190,v190\nM v[64:67],a[64:67],v[200:203],v[64:67]\ns_waitcnt vmcnt(16) lgkmcnt(0)\ns_barrier\nM v[68:71],a[64:67],v[204:207],v[68:71]\nds_read_b128 a[0:3],v250 offset:0\nds_read_b128 a[4:7],v250 offset:1024\n" \
"M v[8:11],a[68:71],v[192:195],v[8:11]\nv_exp_f32 v191,v191\nM v[12:15],a[68:71],v[196:199],v[12:15]\nbuffer_store_dwordx4 a[248:251],v253,s[52:55],s71 offen offset:0 sc1\nM v[72:75],a[68:71],v[200:203],v[72:75]\nv_cvt_pk_bf16_f32 v218,v184,v185\nv_cvt_pk_bf16_f32 v219,v186,v187\nM v[76:79],a[68:71],v[204:207],v[76:79]\n" \
"buffer_load_dwordx4 v240,s[48:51],s95 offen offset:0 sc1 lds\nM v[16:19],a[72:75],v[192:195],v[16:19]\nv_cvt_pk_bf16_f32 v220,v172,v173\nv_cvt_pk_bf16_f32 v221,v174,v175\nM v[20:23],a[72:75],v[196:199],v[20:23]\nbuffer_store_dwordx4 a[248:251],v253,s[52:55],s71 offen offset:1024 sc1\nM v[80:83],a[72:75],v[200:203],v[80:83]\nv_cvt_pk_bf16_f32 v222,v188,v189\n" \
"v_cvt_pk_bf16_f32 v223,v190,v191\nM v[84:87],a[72:75],v[204:207],v[84:87]\nds_read_b128 a[8:11],v250 offset:2048\nds_read_b128 a[12:15],v250 offset:3072\nM v[24:27],a[76:79],v[192:195],v[24:27]\ns_add_u32 s70,s78,16384\ns_add_u32 s94,s94,0x4000\nM v[28:31],a[76:79],v[196:199],v[28:31]\n" \
"buffer_store_dwordx4 a[248:251],v253,s[52:55],s71 offen offset:2048 sc1\nM v[88:91],a[76:79],v[200:203],v[88:91]\nv_exp_f32 v128,v128\nM v[92:95],a[76:79],v[204:207],v[92:95]\nv_exp_f32 v129,v129\nM v[32:35],a[80:83],v[192:195],v[32:35]\nv_exp_f32 v130,v130\nM v[36:39],a[80:83],v[196:199],v[36:39]\n" \
"buffer_store_dwordx4 a[248:251],v253,s[52:55],s71 offen offset:3072 sc1\nM v[96:99],a[80:83],v[200:203],v[96:99]\nv_exp_f32 v131,v131\nM v[100:103],a[80:83],v[204:207],v[100:103]\nds_read_b128 a[16:19],v250 offset:4096\nds_read_b128 a[20:23],v250 offset:5120\nM v[40:43],a[84:87],v[192:195],v[40:43]\nv_exp_f32 v144,v144\n" \
"M v[44:47],a[84:87],v[196:199],v[44:47]\nv_exp_f32 v145,v145\nM v[104:107],a[84:87],v[200:203],v[104:107]\nv_exp_f32 v146,v146\nM v[108:111],a[84:87],v[204:207],v[108:111]\nbuffer_load_dwordx4 v240,s[48:51],s95 offen offset:1024 sc1 lds\nM v[48:51],a[88:91],v[192:195],v[48:51]\nv_exp_f32 v147,v147\n" \
"M v[52:55],a[88:91],v[196:199],v[52:55]\nbuffer_store_dwordx4 a[248:251],v253,s[52:55],s78 offen offset:0 sc1\nM v[112:115],a[88:91],v[200:203],v[112:115]\nv_exp_f32 v132,v132\nM v[116:119],a[88:91],v[204:207],v[116:119]\nds_read_b128 a[24:27],v250 offset:6144\nds_read_b128 a[28:31],v250 offset:7168\nM v[56:59],a[92:95],v[192:195],v[56:59]\n" \
"v_exp_f32 v133,v133\nM v[60:63],a[92:95],v[196:199],v[60:63]\nbuffer_store_dwordx4 a[248:251],v253,s[52:55],s78 offen offset:1024 sc1\nM v[120:123],a[92:95],v[200:203],v[120:123]\nv_exp_f32 v134,v134\nM v[124:127],a[92:95],v[204:207],v[124:127]\nv_exp_f32 v135,v135\nM a[192:195],a[208:211],v[192:195],a[192:195]\n" \
"v_exp_f32 v148,v148\nM a[196:199],a[208:211],v[196:199],a[196:199]\nbuffer_store_dwordx4 a[248:251],v253,s[52:55],s78 offen offset:2048 sc1\nM a[200:203],a[208:211],v[200:203],a[200:203]\nv_exp_f32 v149,v149\nM a[204:207],a[208:211],v[204:207],a[204:207]\nds_read_b128 a[32:35],v250 offset:8192\nds_read_b128 a[36:39],v250 offset:9216\n" \
"M v[0:3],a[96:99],v[208:211],v[0:3]\nv_exp_f32 v150,v150\nM v[4:7],a[96:99],v[212:215],v[4:7]\nv_exp_f32 v151,v151\nM v[64:67],a[96:99],v[216:219],v[64:67]\ns_and_b32 s94,s94,0xffff\nv_cvt_pk_bf16_f32 v192,v128,v129\nM v[68:71],a[96:99],v[220:223],v[68:71]\n" \
"buffer_load_dwordx4 v240,s[48:51],s95 offen offset:2048 sc1 lds\nM v[8:11],a[100:103],v[208:211],v[8:11]\nv_cvt_pk_bf16_f32 v193,v130,v131\nv_cvt_pk_bf16_f32 v194,v144,v145\nM v[12:15],a[100:103],v[212:215],v[12:15]\nbuffer_store_dwordx4 a[248:251],v253,s[52:55],s78 offen offset:3072 sc1\nM v[72:75],a[100:103],v[216:219],v[72:75]\nv_cvt_pk_bf16_f32 v195,v146,v147\n" \
"v_cvt_pk_bf16_f32 v196,v132,v133\nM v[76:79],a[100:103],v[220:223],v[76:79]\nds_read_b128 a[40:43],v250 offset:10240\nds_read_b128 a[44:47],v250 offset:11264\nM v[16:19],a[104:107],v[208:211],v[16:19]\nv_cvt_pk_bf16_f32 v197,v134,v135\nv_cvt_pk_bf16_f32 v198,v148,v149\nM v[20:23],a[104:107],v[212:215],v[20:23]\n" \
"buffer_store_dwordx4 a[248:251],v253,s[52:55],s70 offen offset:0 sc1\nM v[80:83],a[104:107],v[216:219],v[80:83]\nv_exp_f32 v160,v160\nM v[84:87],a[104:107],v[220:223],v[84:87]\nv_exp_f32 v161,v161\nM v[24:27],a[108:111],v[208:211],v[24:27]\nv_exp_f32 v162,v162\nM v[28:31],a[108:111],v[212:215],v[28:31]\n" \
"buffer_store_dwordx4 a[248:251],v253,s[52:55],s70 offen offset:1024 sc1\nM v[88:91],a[108:111],v[216:219],v[88:91]\nv_exp_f32 v163,v163\nM v[92:95],a[108:111],v[220:223],v[92:95]\nds_read_b128 a[48:51],v250 offset:12288\nds_read_b128 a[52:55],v250 offset:13312\nM v[32:35],a[112:115],v[208:211],v[32:35]\nv_exp_f32 v176,v176\n" \
"M v[36:39],a[112:115],v[212:215],v[36:39]\nv_exp_f32 v177,v177\nM v[96:99],a[112:115],v[216:219],v[96:99]\nv_exp_f32 v178,v178\nM v[100:103],a[112:115],v[220:223],v[100:103]\nbuffer_load_dwordx4 v240,s[48:51],s95 offen offset:3072 sc1 lds\nM v[40:43],a[116:119],v[208:211],v[40:43]\nv_cvt_pk_bf16_f32 v199,v150,v151\n" \
"s_add_u32 s95,s95,0x4000\nM v[44:47],a[116:119],v[212:215],v[44:47]\nbuffer_store_dwordx4 a[248:251],v253,s[52:55],s70 offen offset:2048 sc1\nM v[104:107],a[116:119],v[216:219],v[104:107]\nv_exp_f32 v179,v179\nM v[108:111],a[116:119],v[220:223],v[108:111]\nds_read_b128 a[56:59],v250 offset:14336\nds_read_b128 a[60:63],v250 offset:15360\n" \
"M v[48:51],a[120:123],v[208:211],v[48:51]\nv_add_u32 v250,s94,v241\nM v[52:55],a[120:123],v[212:215],v[52:55]\nbuffer_store_dwordx4 a[248:251],v253,s[52:55],s70 offen offset:3072 sc1\nM v[112:115],a[120:123],v[216:219],v[112:115]\nM v[116:119],a[120:123],v[220:223],v[116:119]\nM v[56:59],a[124:127],v[208:211],v[56:59]\nM v[60:63],a[124:127],v[212:215],v[60:63]\n" \
"M v[120:123],a[124:127],v[216:219],v[120:123]\nM v[124:127],a[124:127],v[220:223],v[124:127]\nM a[192:195],a[208:211],v[208:211],a[192:195]\nM a[196:199],a[208:211],v[212:215],a[196:199]\nM a[200:203],a[208:211],v[216:219],a[200:203]\nM a[204:207],a[208:211],v[220:223],a[204:207]\ns_waitcnt lgkmcnt(4)\nM v[128:131],a[0:3],a[128:131],v[224:227]\n" \
"s_add_u32 m0,s94,s64\nv_cvt_pk_bf16_f32 v208,v160,v161\nv_cvt_pk_bf16_f32 v209,v162,v163\nv_cvt_pk_bf16_f32 v210,v176,v177\nM v[128:131],a[4:7],a[132:135],v[128:131]\nds_read_b128 a[64:67],v250 offset:0\nds_read_b128 a[68:71],v250 offset:1024\nv_exp_f32 v164,v164\n" \
"M v[128:131],a[8:11],a[136:139],v[128:131]\nbuffer_load_dwordx4 v240,s[44:47],s95 offen offset:0 sc1 lds\nv_exp_f32 v165,v165\nM v[128:131],a[12:15],a[140:143],v[128:131]\nv_exp_f32 v166,v166\nv_exp_f32 v167,v167\nM v[132:135],a[0:3],a[144:147],v[228:231]\nds_read_b128 a[72:75],v250 offset:2048\n" \
"ds_read_b128 a[76:79],v250 offset:3072\nv_cvt_pk_bf16_f32 v212,v164,v165\nv_cvt_pk_bf16_f32 v213,v166,v167\nv_cvt_pk_bf16_f32 v211,v178,v179\nM v[132:135],a[4:7],a[148:151],v[132:135]\nv_exp_f32 v180,v180\nv_exp_f32 v181,v181\nM v[132:135],a[8:11],a[152:155],v[132:135]\n" \
"v_exp_f32 v182,v182\nv_exp_f32 v183,v183\nM v[132:135],a[12:15],a[156:159],v[132:135]\nds_read_b128 a[80:83],v250 offset:4096\nds_read_b128 a[84:87],v250 offset:5120\nv_cvt_pk_bf16_f32 v214,v180,v181\nv_cvt_pk_bf16_f32 v215,v182,v183\nM v[144:147],a[16:19],a[128:131],v[224:227]\n" \
"v_accvgpr_read_b32 v232,a200\nv_accvgpr_read_b32 v236,a204\nv_add_f32 v233,v232,v236\nv_rcp_f32 v232,v232\nM v[144:147],a[20:23],a[132:135],v[144:147]\nv_rcp_f32 v236,v236\nv_add_f32 v233,v233,v232\nv_add_f32 v233,v233,v236\n" \
"M v[144:147],a[24:27],a[136:139],v[144:147]\nds_read_b128 a[88:91],v250 offset:6144\nds_read_b128 a[92:95],v250 offset:7168\nv_cmp_ngt_f32 vcc,s28,v233\ns_add_u32 s37,s12,s67\nM v[144:147],a[28:31],a[140:143],v[144:147]\nbuffer_load_dwordx4 v240,s[44:47],s95 offen offset:1024 sc1 lds\nv_mul_f32 v64,v64,v232\n" \
"v_mul_f32 v65,v65,v232\nM v[148:151],a[16:19],a[144:147],v[228:231]\ns_and_b64 vcc,vcc,s[90:91]\ns_cselect_b32 s39,0x8000000,s16\ns_cselect_b32 s16,s16,0x8000000\ns_cselect_b32 s71,2,1\ns_addc_u32 s99,s99,0\nM v[148:151],a[20:23],a[148:151],v[148:151]\n" \
"ds_read_b128 a[96:99],v250 offset:8192\nds_read_b128 a[100:103],v250 offset:9216\nv_mul_f32 v68,v68,v236\nv_mul_f32 v69,v69,v236\nM v[148:151],a[24:27],a[152:155],v[148:151]\nv_mul_f32 v80,v80,v232\nv_mul_f32 v81,v81,v232\nv_mul_f32 v84,v84,v236\n" \
"v_mul_f32 v85,v85,v236\nM v[148:151],a[28:31],a[156:159],v[148:151]\nv_mul_f32 v96,v96,v232\nv_mul_f32 v97,v97,v232\nv_mul_f32 v100,v100,v236\nv_mul_f32 v101,v101,v236\nM v[160:163],a[32:35],a[128:131],v[224:227]\nds_read_b128 a[104:107],v250 offset:10240\n" \
"ds_read_b128 a[108:111],v250 offset:11264\nv_mul_f32 v112,v112,v232\nv_mul_f32 v113,v113,v232\nM v[160:163],a[36:39],a[132:135],v[160:163]\nv_mul_f32 v116,v116,v236\nv_mul_f32 v117,v117,v236\ns_add_u32 s37,s37,1048576\nv_mul_f32 v66,v66,v232\n" \
"v_mul_f32 v67,v67,v232\nM v[160:163],a[40:43],a[136:139],v[160:163]\nv_cvt_pk_bf16_f32 v64,v64,v65\nv_mul_f32 v70,v70,v236\nv_mul_f32 v71,v71,v236\nv_cvt_pk_bf16_f32 v68,v68,v69\nM v[160:163],a[44:47],a[140:143],v[160:163]\nds_read_b128 a[112:115],v250 offset:12288\n" \
"ds_read_b128 a[116:119],v250 offset:13312\nv_mul_f32 v82,v82,v232\nv_mul_f32 v83,v83,v232\nM v[164:167],a[32:35],a[144:147],v[228:231]\nbuffer_load_dwordx4 v240,s[44:47],s95 offen offset:2048 sc1 lds\nv_cvt_pk_bf16_f32 v80,v80,v81\nv_mul_f32 v86,v86,v236\nM v[164:167],a[36:39],a[148:151],v[164:167]\n" \
"v_mul_f32 v87,v87,v236\nv_cvt_pk_bf16_f32 v84,v84,v85\nv_mul_f32 v98,v98,v232\nv_mul_f32 v99,v99,v232\nv_cvt_pk_bf16_f32 v96,v96,v97\nM v[164:167],a[40:43],a[152:155],v[164:167]\nds_read_b128 a[120:123],v250 offset:14336\nds_read_b128 a[124:127],v250 offset:15360\n" \
"v_xor_b32 v250,0x18000,v250\nv_mul_f32 v102,v102,v236\nM v[164:167],a[44:47],a[156:159],v[164:167]\nv_mul_f32 v103,v103,v236\nv_cvt_pk_bf16_f32 v100,v100,v101\nv_mul_f32 v114,v114,v232\ns_waitcnt lgkmcnt(4)\nM v[176:179],a[48:51],a[128:131],v[224:227]\n" \
"v_mul_f32 v115,v115,v232\nv_cvt_pk_bf16_f32 v112,v112,v113\nv_mul_f32 v118,v118,v236\nv_mul_f32 v119,v119,v236\nM v[176:179],a[52:55],a[132:135],v[176:179]\nv_cvt_pk_bf16_f32 v116,v116,v117\ns_or_b32 s37,s37,s39\nv_mul_f32 v72,v72,v232\n" \
"v_mul_f32 v73,v73,v232\nv_mul_f32 v74,v74,v232\nM v[176:179],a[56:59],a[136:139],v[176:179]\nv_mul_f32 v75,v75,v232\nv_cvt_pk_bf16_f32 v65,v66,v67\nv_mul_f32 v76,v76,v236\nv_mul_f32 v77,v77,v236\nM v[176:179],a[60:63],a[140:143],v[176:179]\n" \
"v_mul_f32 v78,v78,v236\nv_mul_f32 v79,v79,v236\nv_cvt_pk_bf16_f32 v69,v70,v71\nv_mul_f32 v88,v88,v232\nM v[180:183],a[48:51],a[144:147],v[228:231]\nv_mul_f32 v89,v89,v232\nv_mul_f32 v90,v90,v232\nv_mul_f32 v91,v91,v232\n" \
"v_cvt_pk_bf16_f32 v81,v82,v83\nM v[180:183],a[52:55],a[148:151],v[180:183]\nbuffer_load_dwordx4 v240,s[44:47],s95 offen offset:3072 sc1 lds\nv_mul_f32 v92,v92,v236\nv_mul_f32 v93,v93,v236\nM v[180:183],a[56:59],a[152:155],v[180:183]\nv_mul_f32 v94,v94,v236\nv_mul_f32 v95,v95,v236\n" \
"v_cvt_pk_bf16_f32 v85,v86,v87\nv_mul_f32 v104,v104,v232\nv_mul_f32 v105,v105,v232\nM v[180:183],a[60:63],a[156:159],v[180:183]\nv_mul_f32 v106,v106,v232\nv_mul_f32 v107,v107,v232\nv_cvt_pk_bf16_f32 v97,v98,v99\nv_mul_f32 v108,v108,v236\n" \
"M v[0:3],a[64:67],v[192:195],v[0:3]\nv_mul_f32 v109,v109,v236\nv_mul_f32 v110,v110,v236\nv_mul_f32 v111,v111,v236\nv_cvt_pk_bf16_f32 v101,v102,v103\nM v[4:7],a[64:67],v[196:199],v[4:7]\nv_mul_f32 v120,v120,v232\nv_mul_f32 v121,v121,v232\n" \
"v_mul_f32 v122,v122,v232\nv_mul_f32 v123,v123,v232\nM v[8:11],a[68:71],v[192:195],v[8:11]\ns_waitcnt vmcnt(16) lgkmcnt(0)\ns_barrier\nv_cvt_pk_bf16_f32 v113,v114,v115\nv_mul_f32 v124,v124,v236\nv_mul_f32 v125,v125,v236\n" \
"M v[12:15],a[68:71],v[196:199],v[12:15]\nds_read_b128 a[0:3],v250 offset:0\nds_read_b128 a[4:7],v250 offset:1024\nv_mul_f32 v126,v126,v236\nv_mul_f32 v127,v127,v236\nM v[16:19],a[72:75],v[192:195],v[16:19]\nv_cvt_pk_bf16_f32 v117,v118,v119\ns_add_u32 s38,s37,4096\n" \
"v_cvt_pk_bf16_f32 v66,v72,v73\nv_cvt_pk_bf16_f32 v67,v74,v75\nM v[20:23],a[72:75],v[196:199],v[20:23]\nv_cvt_pk_bf16_f32 v70,v76,v77\nv_cvt_pk_bf16_f32 v71,v78,v79\nv_cvt_pk_bf16_f32 v82,v88,v89\nv_cvt_pk_bf16_f32 v83,v90,v91\nv_cvt_pk_bf16_f32 v86,v92,v93\n" \
"v_cvt_pk_bf16_f32 v87,v94,v95\nv_cvt_pk_bf16_f32 v98,v104,v105\nv_cvt_pk_bf16_f32 v99,v106,v107\nv_cvt_pk_bf16_f32 v102,v108,v109\nv_cvt_pk_bf16_f32 v103,v110,v111\nv_cvt_pk_bf16_f32 v114,v120,v121\nv_cvt_pk_bf16_f32 v115,v122,v123\nv_cvt_pk_bf16_f32 v118,v124,v125\n" \
"v_cvt_pk_bf16_f32 v119,v126,v127\ns_add_u32 m0,m0,0x10000\nbuffer_store_dwordx4 v[64:67],v240,s[52:55],s37 offen offset:0 sc1\nbuffer_store_dwordx4 v[68:71],v240,s[52:55],s37 offen offset:1024 sc1\nbuffer_store_dwordx4 v[80:83],v240,s[52:55],s37 offen offset:2048 sc1\nbuffer_store_dwordx4 v[84:87],v240,s[52:55],s37 offen offset:3072 sc1\nbuffer_store_dwordx4 v[96:99],v240,s[52:55],s38 offen offset:0 sc1\nbuffer_store_dwordx4 v[100:103],v240,s[52:55],s38 offen offset:1024 sc1\n" \
"buffer_store_dwordx4 v[112:115],v240,s[52:55],s38 offen offset:2048 sc1\nbuffer_store_dwordx4 v[116:119],v240,s[52:55],s38 offen offset:3072 sc1\nbuffer_load_dwordx4 v240,s[48:51],s95 offen offset:0 sc1 lds\nM v[24:27],a[76:79],v[192:195],v[24:27]\ns_lshl2_add_u32 s77,s61,s79\nv_exp_f32 v128,v128\nv_exp_f32 v129,v129\nM v[28:31],a[76:79],v[196:199],v[28:31]\n" \
"ds_read_b128 a[8:11],v250 offset:2048\nds_read_b128 a[12:15],v250 offset:3072\nv_exp_f32 v130,v130\nM v[32:35],a[80:83],v[192:195],v[32:35]\nv_exp_f32 v131,v131\nv_exp_f32 v144,v144\nM v[36:39],a[80:83],v[196:199],v[36:39]\nv_exp_f32 v145,v145\n" \
"v_exp_f32 v146,v146\nM v[40:43],a[84:87],v[192:195],v[40:43]\nv_exp_f32 v147,v147\nv_exp_f32 v132,v132\nM v[44:47],a[84:87],v[196:199],v[44:47]\nds_read_b128 a[16:19],v250 offset:4096\nds_read_b128 a[20:23],v250 offset:5120\nv_exp_f32 v133,v133\n" \
"s_add_u32 s94,s94,0x4000\nM v[48:51],a[88:91],v[192:195],v[48:51]\nv_exp_f32 v134,v134\nv_exp_f32 v135,v135\nM v[52:55],a[88:91],v[196:199],v[52:55]\nbuffer_load_dwordx4 v240,s[48:51],s95 offen offset:1024 sc1 lds\nv_exp_f32 v148,v148\nM v[56:59],a[92:95],v[192:195],v[56:59]\n" \
"v_exp_f32 v149,v149\nv_exp_f32 v150,v150\nM v[60:63],a[92:95],v[196:199],v[60:63]\nds_read_b128 a[24:27],v250 offset:6144\nds_read_b128 a[28:31],v250 offset:7168\nv_exp_f32 v151,v151\ns_add_u32 s77,s77,4096\nM a[192:195],a[208:211],v[192:195],a[192:195]\n" \
"v_mov_b32 v233,s71\ns_and_b32 s94,s94,0xffff\nv_cvt_pk_bf16_f32 v192,v128,v129\nv_cvt_pk_bf16_f32 v193,v130,v131\nM a[196:199],a[208:211],v[196:199],a[196:199]\nv_cvt_pk_bf16_f32 v194,v144,v145\nv_cvt_pk_bf16_f32 v195,v146,v147\nv_cvt_pk_bf16_f32 v196,v132,v133\n" \
"v_cvt_pk_bf16_f32 v197,v134,v135\nM v[0:3],a[96:99],v[208:211],v[0:3]\nv_cvt_pk_bf16_f32 v198,v148,v149\nv_cvt_pk_bf16_f32 v199,v150,v151\nv_exp_f32 v160,v160\nM v[4:7],a[96:99],v[212:215],v[4:7]\nds_read_b128 a[32:35],v250 offset:8192\nds_read_b128 a[36:39],v250 offset:9216\n" \
"v_exp_f32 v161,v161\nM v[8:11],a[100:103],v[208:211],v[8:11]\nv_exp_f32 v162,v162\nv_exp_f32 v163,v163\nM v[12:15],a[100:103],v[212:215],v[12:15]\nbuffer_load_dwordx4 v240,s[48:51],s95 offen offset:2048 sc1 lds\nv_exp_f32 v176,v176\nM v[16:19],a[104:107],v[208:211],v[16:19]\n" \
"v_exp_f32 v177,v177\nv_exp_f32 v178,v178\nM v[20:23],a[104:107],v[212:215],v[20:23]\nds_read_b128 a[40:43],v250 offset:10240\nds_read_b128 a[44:47],v250 offset:11264\nv_exp_f32 v179,v179\nM v[24:27],a[108:111],v[208:211],v[24:27]\nM v[28:31],a[108:111],v[212:215],v[28:31]\n" \
"M v[32:35],a[112:115],v[208:211],v[32:35]\nM v[36:39],a[112:115],v[212:215],v[36:39]\nds_read_b128 a[48:51],v250 offset:12288\nds_read_b128 a[52:55],v250 offset:13312\nM v[40:43],a[116:119],v[208:211],v[40:43]\nM v[44:47],a[116:119],v[212:215],v[44:47]\nbuffer_load_dwordx4 v240,s[48:51],s95 offen offset:3072 sc1 lds\ns_add_u32 s95,s95,0x4000\n" \
"M v[48:51],a[120:123],v[208:211],v[48:51]\nM v[52:55],a[120:123],v[212:215],v[52:55]\nds_read_b128 a[56:59],v250 offset:14336\nds_read_b128 a[60:63],v250 offset:15360\nv_add_u32 v250,s94,v241\nM v[56:59],a[124:127],v[208:211],v[56:59]\nM v[60:63],a[124:127],v[212:215],v[60:63]\nM a[192:195],a[208:211],v[208:211],a[192:195]\n" \
"M a[196:199],a[208:211],v[212:215],a[196:199]\ns_waitcnt vmcnt(4)\nbuffer_store_dword v233,off,s[52:55],s77 sc1\n.Lsingle:\ns_waitcnt lgkmcnt(4)\nM v[128:131],a[0:3],a[128:131],v[224:227]\ns_add_u32 m0,s94,s64\nv_cvt_pk_bf16_f32 v208,v160,v161\n" \
"M v[128:131],a[4:7],a[132:135],v[128:131]\nds_read_b128 a[64:67],v250 offset:0\nds_read_b128 a[68:71],v250 offset:1024\nM v[128:131],a[8:11],a[136:139],v[128:131]\nbuffer_load_dwordx4 v240,s[44:47],s95 offen offset:0 sc1 lds\nM v[128:131],a[12:15],a[140:143],v[128:131]\nv_cvt_pk_bf16_f32 v209,v162,v163\nv_cvt_pk_bf16_f32 v210,v176,v177\n" \
"M v[132:135],a[0:3],a[144:147],v[228:231]\nds_read_b128 a[72:75],v250 offset:2048\nds_read_b128 a[76:79],v250 offset:3072\nM v[132:135],a[4:7],a[148:151],v[132:135]\nv_exp_f32 v164,v164\nM v[132:135],a[8:11],a[152:155],v[132:135]\nv_exp_f32 v165,v165\nM v[132:135],a[12:15],a[156:159],v[132:135]\n" \
"ds_read_b128 a[80:83],v250 offset:4096\nds_read_b128 a[84:87],v250 offset:5120\nM v[144:147],a[16:19],a[128:131],v[224:227]\nv_exp_f32 v166,v166\nM v[144:147],a[20:23],a[132:135],v[144:147]\nv_exp_f32 v167,v167\nM v[144:147],a[24:27],a[136:139],v[144:147]\nds_read_b128 a[88:91],v250 offset:6144\n" \
"ds_read_b128 a[92:95],v250 offset:7168\nM v[144:147],a[28:31],a[140:143],v[144:147]\nbuffer_load_dwordx4 v240,s[44:47],s95 offen offset:1024 sc1 lds\nM v[148:151],a[16:19],a[144:147],v[228:231]\nv_cvt_pk_bf16_f32 v212,v164,v165\nv_cvt_pk_bf16_f32 v213,v166,v167\nM v[148:151],a[20:23],a[148:151],v[148:151]\nds_read_b128 a[96:99],v250 offset:8192\n" \
"ds_read_b128 a[100:103],v250 offset:9216\nM v[148:151],a[24:27],a[152:155],v[148:151]\nv_cvt_pk_bf16_f32 v211,v178,v179\ns_add_u32 s94,s94,0x4000\nM v[148:151],a[28:31],a[156:159],v[148:151]\nv_exp_f32 v180,v180\nM v[160:163],a[32:35],a[128:131],v[224:227]\nds_read_b128 a[104:107],v250 offset:10240\n" \
"ds_read_b128 a[108:111],v250 offset:11264\nM v[160:163],a[36:39],a[132:135],v[160:163]\nv_exp_f32 v181,v181\nM v[160:163],a[40:43],a[136:139],v[160:163]\nv_exp_f32 v182,v182\nM v[160:163],a[44:47],a[140:143],v[160:163]\nds_read_b128 a[112:115],v250 offset:12288\nds_read_b128 a[116:119],v250 offset:13312\n" \
"M v[164:167],a[32:35],a[144:147],v[228:231]\nbuffer_load_dwordx4 v240,s[44:47],s95 offen offset:2048 sc1 lds\nM v[164:167],a[36:39],a[148:151],v[164:167]\nv_exp_f32 v183,v183\nM v[164:167],a[40:43],a[152:155],v[164:167]\nds_read_b128 a[120:123],v250 offset:14336\nds_read_b128 a[124:127],v250 offset:15360\nM v[164:167],a[44:47],a[156:159],v[164:167]\n" \
"v_cvt_pk_bf16_f32 v214,v180,v181\ns_waitcnt lgkmcnt(7)\nM v[176:179],a[48:51],a[128:131],v[224:227]\nv_cvt_pk_bf16_f32 v215,v182,v183\nv_xor_b32 v250,0x18000,v250\nM v[176:179],a[52:55],a[132:135],v[176:179]\nv_exp_f32 v128,v128\nM v[176:179],a[56:59],a[136:139],v[176:179]\n" \
"v_exp_f32 v129,v129\nM v[176:179],a[60:63],a[140:143],v[176:179]\nv_exp_f32 v130,v130\nM v[180:183],a[48:51],a[144:147],v[228:231]\nv_exp_f32 v131,v131\nM v[180:183],a[52:55],a[148:151],v[180:183]\nbuffer_load_dwordx4 v240,s[44:47],s95 offen offset:3072 sc1 lds\nM v[180:183],a[56:59],a[152:155],v[180:183]\n" \
"s_add_u32 m0,m0,0x10000\ns_and_b32 s94,s94,0xffff\nM v[180:183],a[60:63],a[156:159],v[180:183]\nv_exp_f32 v144,v144\nM v[0:3],a[64:67],v[192:195],v[0:3]\nv_exp_f32 v145,v145\nM v[4:7],a[64:67],v[196:199],v[4:7]\nv_exp_f32 v146,v146\n" \
"M v[8:11],a[68:71],v[192:195],v[8:11]\ns_waitcnt vmcnt(16) lgkmcnt(0)\ns_barrier\nM v[12:15],a[68:71],v[196:199],v[12:15]\nds_read_b128 a[0:3],v250 offset:0\nds_read_b128 a[4:7],v250 offset:1024\nM v[16:19],a[72:75],v[192:195],v[16:19]\nv_exp_f32 v147,v147\n" \
"M v[20:23],a[72:75],v[196:199],v[20:23]\nbuffer_load_dwordx4 v240,s[48:51],s95 offen offset:0 sc1 lds\nM v[24:27],a[76:79],v[192:195],v[24:27]\nv_exp_f32 v132,v132\nM v[28:31],a[76:79],v[196:199],v[28:31]\nds_read_b128 a[8:11],v250 offset:2048\nds_read_b128 a[12:15],v250 offset:3072\nM v[32:35],a[80:83],v[192:195],v[32:35]\n" \
"v_exp_f32 v133,v133\nM v[36:39],a[80:83],v[196:199],v[36:39]\nv_exp_f32 v134,v134\nM v[40:43],a[84:87],v[192:195],v[40:43]\nv_exp_f32 v135,v135\nM v[44:47],a[84:87],v[196:199],v[44:47]\nds_read_b128 a[16:19],v250 offset:4096\nds_read_b128 a[20:23],v250 offset:5120\n" \
"M v[48:51],a[88:91],v[192:195],v[48:51]\nv_exp_f32 v148,v148\nM v[52:55],a[88:91],v[196:199],v[52:55]\nbuffer_load_dwordx4 v240,s[48:51],s95 offen offset:1024 sc1 lds\nM v[56:59],a[92:95],v[192:195],v[56:59]\nv_exp_f32 v149,v149\nM v[60:63],a[92:95],v[196:199],v[60:63]\nds_read_b128 a[24:27],v250 offset:6144\n" \
"ds_read_b128 a[28:31],v250 offset:7168\nM a[192:195],a[208:211],v[192:195],a[192:195]\nv_exp_f32 v150,v150\nM a[196:199],a[208:211],v[196:199],a[196:199]\nv_exp_f32 v151,v151\nM v[0:3],a[96:99],v[208:211],v[0:3]\nv_cvt_pk_bf16_f32 v192,v128,v129\nv_cvt_pk_bf16_f32 v193,v130,v131\n" \
"M v[4:7],a[96:99],v[212:215],v[4:7]\nds_read_b128 a[32:35],v250 offset:8192\nds_read_b128 a[36:39],v250 offset:9216\nM v[8:11],a[100:103],v[208:211],v[8:11]\nv_cvt_pk_bf16_f32 v194,v144,v145\nv_cvt_pk_bf16_f32 v195,v146,v147\nM v[12:15],a[100:103],v[212:215],v[12:15]\nbuffer_load_dwordx4 v240,s[48:51],s95 offen offset:2048 sc1 lds\n" \
"M v[16:19],a[104:107],v[208:211],v[16:19]\nv_cvt_pk_bf16_f32 v196,v132,v133\nv_cvt_pk_bf16_f32 v197,v134,v135\nM v[20:23],a[104:107],v[212:215],v[20:23]\nds_read_b128 a[40:43],v250 offset:10240\nds_read_b128 a[44:47],v250 offset:11264\nM v[24:27],a[108:111],v[208:211],v[24:27]\nv_cvt_pk_bf16_f32 v198,v148,v149\n" \
"v_cvt_pk_bf16_f32 v199,v150,v151\nM v[28:31],a[108:111],v[212:215],v[28:31]\nv_exp_f32 v160,v160\nM v[32:35],a[112:115],v[208:211],v[32:35]\nv_exp_f32 v161,v161\nM v[36:39],a[112:115],v[212:215],v[36:39]\nds_read_b128 a[48:51],v250 offset:12288\nds_read_b128 a[52:55],v250 offset:13312\n" \
"M v[40:43],a[116:119],v[208:211],v[40:43]\nv_exp_f32 v162,v162\nM v[44:47],a[116:119],v[212:215],v[44:47]\nbuffer_load_dwordx4 v240,s[48:51],s95 offen offset:3072 sc1 lds\nM v[48:51],a[120:123],v[208:211],v[48:51]\nv_exp_f32 v163,v163\nM v[52:55],a[120:123],v[212:215],v[52:55]\nds_read_b128 a[56:59],v250 offset:14336\n" \
"ds_read_b128 a[60:63],v250 offset:15360\nM v[56:59],a[124:127],v[208:211],v[56:59]\nv_exp_f32 v176,v176\nM v[60:63],a[124:127],v[212:215],v[60:63]\nv_exp_f32 v177,v177\nM a[192:195],a[208:211],v[208:211],a[192:195]\nv_exp_f32 v178,v178\nv_exp_f32 v179,v179\n" \
"v_add_u32 v250,s94,v241\ns_add_u32 s95,s95,0x4000\nM a[196:199],a[208:211],v[212:215],a[196:199]\ns_cmp_lg_u32 s95,s15\ns_cbranch_scc1 .Lsingle\ns_cmp_lg_u32 s15,s56\ns_cbranch_scc1 .Ltouch\ns_waitcnt lgkmcnt(2)\n" \
"M v[128:131],a[0:3],a[128:131],v[224:227]\nv_and_b32 v251,3,v249\nv_lshlrev_b32 v251,2,v251\nM v[128:131],a[4:7],a[132:135],v[128:131]\nds_read_b128 a[64:67],v250 offset:0\nds_read_b128 a[68:71],v250 offset:1024\nM v[128:131],a[8:11],a[136:139],v[128:131]\ns_add_u32 s80,s79,4096\n" \
"buffer_load_dword v248,v251,s[52:55],s80 offen sc1\nM v[128:131],a[12:15],a[140:143],v[128:131]\nv_cvt_pk_bf16_f32 v208,v160,v161\nv_cvt_pk_bf16_f32 v209,v162,v163\nv_cvt_pk_bf16_f32 v210,v176,v177\nM v[132:135],a[0:3],a[144:147],v[228:231]\nds_read_b128 a[72:75],v250 offset:2048\nds_read_b128 a[76:79],v250 offset:3072\n" \
"M v[132:135],a[4:7],a[148:151],v[132:135]\nv_exp_f32 v164,v164\nM v[132:135],a[8:11],a[152:155],v[132:135]\nv_exp_f32 v165,v165\nM v[132:135],a[12:15],a[156:159],v[132:135]\nds_read_b128 a[80:83],v250 offset:4096\nds_read_b128 a[84:87],v250 offset:5120\nv_cvt_pk_bf16_f32 v212,v164,v165\n" \
"M v[144:147],a[16:19],a[128:131],v[224:227]\nv_exp_f32 v166,v166\nM v[144:147],a[20:23],a[132:135],v[144:147]\nv_exp_f32 v167,v167\nM v[144:147],a[24:27],a[136:139],v[144:147]\nds_read_b128 a[88:91],v250 offset:6144\nds_read_b128 a[92:95],v250 offset:7168\nM v[144:147],a[28:31],a[140:143],v[144:147]\n" \
"v_cvt_pk_bf16_f32 v213,v166,v167\nv_cvt_pk_bf16_f32 v211,v178,v179\ns_lshl2_add_u32 s39,s62,s7\nM v[148:151],a[16:19],a[144:147],v[228:231]\nv_exp_f32 v180,v180\nM v[148:151],a[20:23],a[148:151],v[148:151]\nds_read_b128 a[96:99],v250 offset:8192\nds_read_b128 a[100:103],v250 offset:9216\n" \
"M v[148:151],a[24:27],a[152:155],v[148:151]\nv_exp_f32 v181,v181\nM v[148:151],a[28:31],a[156:159],v[148:151]\nv_exp_f32 v182,v182\nv_cvt_pk_bf16_f32 v214,v180,v181\nM v[160:163],a[32:35],a[128:131],v[224:227]\nds_read_b128 a[104:107],v250 offset:10240\nds_read_b128 a[108:111],v250 offset:11264\n" \
"M v[160:163],a[36:39],a[132:135],v[160:163]\nv_exp_f32 v183,v183\nM v[160:163],a[40:43],a[136:139],v[160:163]\nv_cvt_pk_bf16_f32 v215,v182,v183\ns_lshl_b32 s39,s39,5\nM v[160:163],a[44:47],a[140:143],v[160:163]\nds_read_b128 a[112:115],v250 offset:12288\nds_read_b128 a[116:119],v250 offset:13312\n" \
"s_add_u32 s39,s39,s63\nM v[164:167],a[32:35],a[144:147],v[228:231]\ns_lshl2_add_u32 s39,s39,73728\ns_add_u32 s94,s94,0x4000\nM v[164:167],a[36:39],a[148:151],v[164:167]\ns_and_b32 s94,s94,0xffff\ns_add_u32 s65,s10,s67\nM v[164:167],a[40:43],a[152:155],v[164:167]\n" \
"ds_read_b128 a[120:123],v250 offset:14336\nds_read_b128 a[124:127],v250 offset:15360\nM v[164:167],a[44:47],a[156:159],v[164:167]\nv_add_u32 v250,s94,v241\nv_exp_f32 v128,v128\nM v[176:179],a[48:51],a[128:131],v[224:227]\nv_exp_f32 v129,v129\nM v[176:179],a[52:55],a[132:135],v[176:179]\n" \
"s_add_u32 s65,s65,1048576\ns_waitcnt lgkmcnt(4)\nM v[176:179],a[56:59],a[136:139],v[176:179]\nv_exp_f32 v130,v130\nM v[176:179],a[60:63],a[140:143],v[176:179]\nv_exp_f32 v131,v131\nv_add_u32 v226,131072,v242\nM v[180:183],a[48:51],a[144:147],v[228:231]\n" \
"v_exp_f32 v144,v144\nM v[180:183],a[52:55],a[148:151],v[180:183]\nv_exp_f32 v145,v145\nM v[180:183],a[56:59],a[152:155],v[180:183]\ns_waitcnt vmcnt(1)\nbuffer_store_dword v254,off,s[52:55],s39 sc1\nM v[180:183],a[60:63],a[156:159],v[180:183]\ns_lshl4_add_u32 s69,s10,s68\n" \
"s_add_u32 m0,s64,0\ns_waitcnt vmcnt(1)\nv_and_b32 v248,s17,v248\nM v[0:3],a[64:67],v[192:195],v[0:3]\nv_exp_f32 v146,v146\nM v[4:7],a[64:67],v[196:199],v[4:7]\nv_exp_f32 v147,v147\nM v[8:11],a[68:71],v[192:195],v[8:11]\n" \
"s_waitcnt vmcnt(12) lgkmcnt(0)\ns_barrier\nM v[12:15],a[68:71],v[196:199],v[12:15]\nds_read_b128 a[64:67],v250 offset:0\nds_read_b128 a[68:71],v250 offset:1024\nv_cmp_eq_u32 vcc,0,v248\nM v[16:19],a[72:75],v[192:195],v[16:19]\nbuffer_load_dwordx4 v243,s[40:43],s69 offen offset:0 lds\n" \
"M v[20:23],a[72:75],v[196:199],v[20:23]\nds_read_b128 a[72:75],v250 offset:2048\ns_cbranch_vccnz .Lps6\n.Lpb6:\nM v[24:27],a[76:79],v[192:195],v[24:27]\nv_exp_f32 v132,v132\nM v[28:31],a[76:79],v[196:199],v[28:31]\nds_read_b128 a[76:79],v250 offset:3072\n" \
"v_exp_f32 v133,v133\nM v[32:35],a[80:83],v[192:195],v[32:35]\nbuffer_load_dwordx4 v244,s[40:43],s69 offen offset:1024 lds\nM v[36:39],a[80:83],v[196:199],v[36:39]\nds_read_b128 a[80:83],v250 offset:4096\nds_read_b64_tr_b16 a[212:213],v226 offset:0\nM v[40:43],a[84:87],v[192:195],v[40:43]\nv_exp_f32 v134,v134\n" \
"M v[44:47],a[84:87],v[196:199],v[44:47]\nv_exp_f32 v135,v135\nds_read_b128 a[84:87],v250 offset:5120\nM v[48:51],a[88:91],v[192:195],v[48:51]\nbuffer_load_dwordx4 v245,s[40:43],s69 offen offset:2048 lds\nM v[52:55],a[88:91],v[196:199],v[52:55]\nv_exp_f32 v148,v148\nM v[56:59],a[92:95],v[192:195],v[56:59]\n" \
"v_exp_f32 v149,v149\nM v[60:63],a[92:95],v[196:199],v[60:63]\nv_exp_f32 v150,v150\nds_read_b128 a[88:91],v250 offset:6144\nM a[192:195],a[208:211],v[192:195],a[192:195]\nbuffer_load_dwordx4 v246,s[40:43],s69 offen offset:3072 lds\nM a[196:199],a[208:211],v[196:199],a[196:199]\ns_lshl4_add_u32 s69,s10,s68\n" \
"s_add_u32 s69,s69,0x10000\nM v[0:3],a[96:99],v[208:211],v[0:3]\ns_add_u32 m0,s64,16384\nv_cvt_pk_bf16_f32 v192,v128,v129\nM v[4:7],a[96:99],v[212:215],v[4:7]\nv_exp_f32 v151,v151\nv_cvt_pk_bf16_f32 v193,v130,v131\nM v[8:11],a[100:103],v[208:211],v[8:11]\n" \
"buffer_load_dwordx4 v243,s[40:43],s69 offen offset:0 lds\nM v[12:15],a[100:103],v[212:215],v[12:15]\nv_cvt_pk_bf16_f32 v194,v144,v145\nv_cvt_pk_bf16_f32 v195,v146,v147\nM v[16:19],a[104:107],v[208:211],v[16:19]\nv_cvt_pk_bf16_f32 v196,v132,v133\nv_cvt_pk_bf16_f32 v197,v134,v135\nM v[20:23],a[104:107],v[212:215],v[20:23]\n" \
"v_cvt_pk_bf16_f32 v198,v148,v149\nv_cvt_pk_bf16_f32 v199,v150,v151\nds_read_b128 a[92:95],v250 offset:7168\nM v[24:27],a[108:111],v[208:211],v[24:27]\nbuffer_load_dwordx4 v244,s[40:43],s69 offen offset:1024 lds\nM v[28:31],a[108:111],v[212:215],v[28:31]\nds_read_b128 a[96:99],v250 offset:8192\nds_read_b128 a[100:103],v250 offset:9216\n" \
"M v[32:35],a[112:115],v[208:211],v[32:35]\nds_read_b128 a[104:107],v250 offset:10240\nv_xor_b32 v227,32,v226\nM v[36:39],a[112:115],v[212:215],v[36:39]\nds_read_b128 a[108:111],v250 offset:11264\nv_exp_f32 v160,v160\nM v[40:43],a[116:119],v[208:211],v[40:43]\nbuffer_load_dwordx4 v245,s[40:43],s69 offen offset:2048 lds\n" \
"M v[44:47],a[116:119],v[212:215],v[44:47]\nv_exp_f32 v161,v161\nM v[48:51],a[120:123],v[208:211],v[48:51]\nv_exp_f32 v162,v162\nM v[52:55],a[120:123],v[212:215],v[52:55]\nv_exp_f32 v163,v163\nds_read_b128 a[112:115],v250 offset:12288\nM v[56:59],a[124:127],v[208:211],v[56:59]\n" \
"buffer_load_dwordx4 v246,s[40:43],s69 offen offset:3072 lds\nM v[60:63],a[124:127],v[212:215],v[60:63]\nv_exp_f32 v176,v176\nM a[192:195],a[208:211],v[208:211],a[192:195]\nv_exp_f32 v177,v177\nM a[196:199],a[208:211],v[212:215],a[196:199]\nv_exp_f32 v178,v178\ns_waitcnt lgkmcnt(1)\n" \
"M v[0:3],a[64:67],v[192:195],v[0:3]\nbuffer_load_dwordx4 v[72:75],v240,s[52:55],s65 offen offset:0 sc1\nM v[4:7],a[64:67],v[196:199],v[4:7]\nv_exp_f32 v179,v179\nM v[8:11],a[68:71],v[192:195],v[8:11]\nv_exp_f32 v164,v164\nM v[12:15],a[68:71],v[196:199],v[12:15]\nv_exp_f32 v165,v165\n" \
"v_cvt_pk_bf16_f32 v208,v160,v161\nM v[16:19],a[72:75],v[192:195],v[16:19]\nv_exp_f32 v166,v166\nM v[20:23],a[72:75],v[196:199],v[20:23]\nbuffer_load_dwordx4 v[76:79],v240,s[52:55],s65 offen offset:1024 sc1\nM v[24:27],a[76:79],v[192:195],v[24:27]\nv_exp_f32 v167,v167\nM v[28:31],a[76:79],v[196:199],v[28:31]\n" \
"v_exp_f32 v180,v180\nv_cvt_pk_bf16_f32 v209,v162,v163\nM v[32:35],a[80:83],v[192:195],v[32:35]\nv_exp_f32 v181,v181\nM v[36:39],a[80:83],v[196:199],v[36:39]\nv_exp_f32 v182,v182\nM v[40:43],a[84:87],v[192:195],v[40:43]\nbuffer_load_dwordx4 v[88:91],v240,s[52:55],s65 offen offset:2048 sc1\n" \
"M v[44:47],a[84:87],v[196:199],v[44:47]\nv_exp_f32 v183,v183\nv_cvt_pk_bf16_f32 v210,v176,v177\nM v[48:51],a[88:91],v[192:195],v[48:51]\nv_cvt_pk_bf16_f32 v211,v178,v179\nv_cvt_pk_bf16_f32 v212,v164,v165\nM v[52:55],a[88:91],v[196:199],v[52:55]\nv_cvt_pk_bf16_f32 v213,v166,v167\n" \
"v_cvt_pk_bf16_f32 v214,v180,v181\nM v[56:59],a[92:95],v[192:195],v[56:59]\nv_cvt_pk_bf16_f32 v215,v182,v183\nds_read_b128 a[116:119],v250 offset:13312\nM v[60:63],a[92:95],v[196:199],v[60:63]\nbuffer_load_dwordx4 v[92:95],v240,s[52:55],s65 offen offset:3072 sc1\nds_read_b128 a[120:123],v250 offset:14336\nM a[192:195],a[208:211],v[192:195],a[192:195]\n" \
"s_add_u32 s65,s65,4096\nds_read_b128 a[124:127],v250 offset:15360\nM a[196:199],a[208:211],v[196:199],a[196:199]\nds_read_b64_tr_b16 a[214:215],v226 offset:4096\nds_read_b64_tr_b16 a[216:217],v227 offset:0\nM v[0:3],a[96:99],v[208:211],v[0:3]\nds_read_b64_tr_b16 a[218:219],v227 offset:4096\nds_read_b64_tr_b16 a[220:221],v226 offset:512\n" \
"M v[4:7],a[96:99],v[212:215],v[4:7]\nds_read_b64_tr_b16 a[222:223],v226 offset:4608\nds_read_b64_tr_b16 a[224:225],v227 offset:512\nds_read_b64_tr_b16 a[226:227],v227 offset:4608\nM v[8:11],a[100:103],v[208:211],v[8:11]\nbuffer_load_dwordx4 v[104:107],v240,s[52:55],s65 offen offset:0 sc1\nM v[12:15],a[100:103],v[212:215],v[12:15]\nds_read_b64_tr_b16 a[228:229],v226 offset:1024\n" \
"ds_read_b64_tr_b16 a[230:231],v226 offset:5120\nM v[16:19],a[104:107],v[208:211],v[16:19]\nds_read_b64_tr_b16 a[232:233],v227 offset:1024\nds_read_b64_tr_b16 a[234:235],v227 offset:5120\nM v[20:23],a[104:107],v[212:215],v[20:23]\nds_read_b64_tr_b16 a[236:237],v226 offset:1536\nds_read_b64_tr_b16 a[238:239],v226 offset:5632\nds_read_b64_tr_b16 a[240:241],v227 offset:1536\n" \
"M v[24:27],a[108:111],v[208:211],v[24:27]\nds_read_b64_tr_b16 a[242:243],v227 offset:5632\ns_add_u32 s37,s12,s67\nM v[28:31],a[108:111],v[212:215],v[28:31]\nbuffer_load_dwordx4 v[108:111],v240,s[52:55],s65 offen offset:1024 sc1\ns_waitcnt lgkmcnt(11)\nM v[32:35],a[112:115],v[208:211],v[32:35]\nv_cmp_eq_u32 vcc,2,v248\n" \
"v_xor_b32 v250,0x8000,v250\nM v[36:39],a[112:115],v[212:215],v[36:39]\ns_cmp_lg_u64 vcc,0\ns_addc_u32 s99,s99,0\nv_add_u32 v229,s64,v250\nM v[40:43],a[116:119],v[208:211],v[40:43]\ns_add_u32 s38,s37,4096\ns_lshr_b32 s70,s60,1\n" \
"M v[44:47],a[116:119],v[212:215],v[44:47]\ns_lshl_b32 s70,s70,7\ns_mul_i32 s71,s60,28\nM v[48:51],a[120:123],v[208:211],v[48:51]\nbuffer_load_dwordx4 v[120:123],v240,s[52:55],s65 offen offset:2048 sc1\nM v[52:55],a[120:123],v[212:215],v[52:55]\ns_lshl2_add_u32 s76,s61,s79\ns_lshl_b32 s80,s62,9\n" \
"s_add_u32 s80,s80,73728\nM v[56:59],a[124:127],v[208:211],v[56:59]\nv_and_b32 v236,15,v249\ns_lshl2_add_u32 s74,s63,s7\nM v[60:63],a[124:127],v[212:215],v[60:63]\ns_lshl_b32 s72,s62,7\nv_lshrrev_b32 v235,4,v249\nM a[192:195],a[208:211],v[208:211],a[192:195]\n" \
"s_lshl4_add_u32 s69,s11,s68\ns_add_u32 m0,s64,32768\nM a[196:199],a[208:211],v[212:215],a[196:199]\nbuffer_load_dwordx4 v[124:127],v240,s[52:55],s65 offen offset:3072 sc1\ns_add_u32 s65,s11,s67\nM v[128:131],v[64:67],a[212:215],0\ns_add_u32 s65,s65,1048576\nv_lshlrev_b32 v236,1,v236\n" \
"v_accvgpr_read_b32 v224,a192\nM v[132:135],v[68:71],a[212:215],0\nds_read_b64_tr_b16 a[212:213],v226 offset:8192\nds_read_b64_tr_b16 a[214:215],v226 offset:12288\nv_accvgpr_read_b32 v228,a196\nM v[136:139],v[64:67],a[216:219],0\nv_add_f32 v225,v224,v228\nv_rcp_f32 v224,v224\n" \
"M v[140:143],v[68:71],a[216:219],0\nds_read_b64_tr_b16 a[216:217],v227 offset:8192\nds_read_b64_tr_b16 a[218:219],v227 offset:12288\ns_waitcnt lgkmcnt(4)\nM v[144:147],v[64:67],a[220:223],0\nbuffer_load_dwordx4 v243,s[40:43],s69 offen offset:0 lds\nv_mul_f32 v0,v0,v224\nM v[148:151],v[68:71],a[220:223],0\n" \
"ds_read_b64_tr_b16 a[220:221],v226 offset:8704\nds_read_b64_tr_b16 a[222:223],v226 offset:12800\nv_mul_f32 v1,v1,v224\nM v[152:155],v[64:67],a[224:227],0\nv_mul_f32 v2,v2,v224\nv_mul_f32 v3,v3,v224\nv_cvt_pk_bf16_f32 v0,v0,v1\nM v[156:159],v[68:71],a[224:227],0\n" \
"ds_read_b64_tr_b16 a[224:225],v227 offset:8704\nds_read_b64_tr_b16 a[226:227],v227 offset:12800\nv_mul_f32 v8,v8,v224\nM v[160:163],v[64:67],a[228:231],0\nbuffer_load_dwordx4 v244,s[40:43],s69 offen offset:1024 lds\nv_mul_f32 v9,v9,v224\nM v[164:167],v[68:71],a[228:231],0\nds_read_b64_tr_b16 a[228:229],v226 offset:9216\n" \
"ds_read_b64_tr_b16 a[230:231],v226 offset:13312\nv_mul_f32 v10,v10,v224\nM v[168:171],v[64:67],a[232:235],0\nv_mul_f32 v11,v11,v224\nv_cvt_pk_bf16_f32 v1,v2,v3\nv_cvt_pk_bf16_f32 v2,v8,v9\nM v[172:175],v[68:71],a[232:235],0\nds_read_b64_tr_b16 a[232:233],v227 offset:9216\n" \
"ds_read_b64_tr_b16 a[234:235],v227 offset:13312\nv_cvt_pk_bf16_f32 v3,v10,v11\nM v[176:179],v[64:67],a[236:239],0\nbuffer_load_dwordx4 v245,s[40:43],s69 offen offset:2048 lds\nv_add_f32 v225,v225,v224\nM v[180:183],v[68:71],a[236:239],0\nds_read_b64_tr_b16 a[236:237],v226 offset:9728\nds_read_b64_tr_b16 a[238:239],v226 offset:13824\n" \
"v_mul_f32 v16,v16,v224\nM v[184:187],v[64:67],a[240:243],0\nv_rcp_f32 v228,v228\nv_mul_f32 v17,v17,v224\nM v[188:191],v[68:71],a[240:243],0\nds_read_b64_tr_b16 a[240:241],v227 offset:9728\nds_read_b64_tr_b16 a[242:243],v227 offset:13824\ns_waitcnt lgkmcnt(6)\n" \
"M v[128:131],v[80:83],a[212:215],v[128:131]\nbuffer_load_dwordx4 v246,s[40:43],s69 offen offset:3072 lds\ns_lshl4_add_u32 s69,s11,s68\nM v[132:135],v[84:87],a[212:215],v[132:135]\ns_add_u32 s69,s69,0x10000\ns_add_u32 m0,s64,49152\nds_read_b64_tr_b16 a[212:213],v226 offset:16384\nM v[136:139],v[80:83],a[216:219],v[136:139]\n" \
"ds_read_b64_tr_b16 a[214:215],v226 offset:20480\nv_mul_f32 v4,v4,v228\nv_mul_f32 v5,v5,v228\nM v[140:143],v[84:87],a[216:219],v[140:143]\nds_read_b64_tr_b16 a[216:217],v227 offset:16384\nds_read_b64_tr_b16 a[218:219],v227 offset:20480\nv_mul_f32 v6,v6,v228\nM v[144:147],v[80:83],a[220:223],v[144:147]\n" \
"buffer_load_dwordx4 v243,s[40:43],s69 offen offset:0 lds\nv_mul_f32 v7,v7,v228\nM v[148:151],v[84:87],a[220:223],v[148:151]\nds_read_b64_tr_b16 a[220:221],v226 offset:16896\nds_read_b64_tr_b16 a[222:223],v226 offset:20992\nv_cvt_pk_bf16_f32 v4,v4,v5\nM v[152:155],v[80:83],a[224:227],v[152:155]\nbuffer_store_dwordx4 v[0:3],v240,s[52:55],s37 offen offset:0 sc1\n" \
"v_mul_f32 v12,v12,v228\nM v[156:159],v[84:87],a[224:227],v[156:159]\nds_read_b64_tr_b16 a[224:225],v227 offset:16896\nds_read_b64_tr_b16 a[226:227],v227 offset:20992\nv_mul_f32 v13,v13,v228\nM v[160:163],v[80:83],a[228:231],v[160:163]\nbuffer_load_dwordx4 v244,s[40:43],s69 offen offset:1024 lds\nv_mul_f32 v14,v14,v228\n" \
"M v[164:167],v[84:87],a[228:231],v[164:167]\nds_read_b64_tr_b16 a[228:229],v226 offset:17408\nds_read_b64_tr_b16 a[230:231],v226 offset:21504\ns_waitcnt lgkmcnt(6)\nM v[168:171],v[80:83],a[232:235],v[168:171]\nv_mul_f32 v15,v15,v228\nv_cvt_pk_bf16_f32 v5,v6,v7\nv_cvt_pk_bf16_f32 v6,v12,v13\n" \
"M v[172:175],v[84:87],a[232:235],v[172:175]\nds_read_b64_tr_b16 a[232:233],v227 offset:17408\nds_read_b64_tr_b16 a[234:235],v227 offset:21504\nv_cvt_pk_bf16_f32 v7,v14,v15\nM v[176:179],v[80:83],a[236:239],v[176:179]\nbuffer_load_dwordx4 v245,s[40:43],s69 offen offset:2048 lds\nv_add_f32 v225,v225,v228\nM v[180:183],v[84:87],a[236:239],v[180:183]\n" \
"ds_read_b64_tr_b16 a[236:237],v226 offset:17920\nds_read_b64_tr_b16 a[238:239],v226 offset:22016\nv_cmp_ngt_f32 vcc,s28,v225\nM v[184:187],v[80:83],a[240:243],v[184:187]\ns_and_b64 vcc,vcc,s[90:91]\ns_addc_u32 s99,s99,0\nv_mov_b32 v230,s99\nM v[188:191],v[84:87],a[240:243],v[188:191]\n" \
"ds_read_b64_tr_b16 a[240:241],v227 offset:17920\nds_read_b64_tr_b16 a[242:243],v227 offset:22016\nv_mul_f32 v18,v18,v224\nM v[128:131],v[96:99],a[212:215],v[128:131]\nbuffer_store_dwordx4 v[4:7],v240,s[52:55],s37 offen offset:1024 sc1\nbuffer_load_dwordx4 v246,s[40:43],s69 offen offset:3072 lds\nM v[132:135],v[100:103],a[212:215],v[132:135]\nds_write_b32 v229,v230\n" \
"ds_read_b64_tr_b16 a[212:213],v226 offset:24576\nds_read_b64_tr_b16 a[214:215],v226 offset:28672\nM v[136:139],v[96:99],a[216:219],v[136:139]\nv_mul_f32 v19,v19,v224\nv_cvt_pk_bf16_f32 v16,v16,v17\nv_mul_f32 v24,v24,v224\nM v[140:143],v[100:103],a[216:219],v[140:143]\nds_read_b64_tr_b16 a[216:217],v227 offset:24576\n" \
"ds_read_b64_tr_b16 a[218:219],v227 offset:28672\ns_waitcnt lgkmcnt(7)\nM v[144:147],v[96:99],a[220:223],v[144:147]\nv_mul_f32 v25,v25,v224\nv_mul_f32 v26,v26,v224\nv_mul_f32 v27,v27,v224\nM v[148:151],v[100:103],a[220:223],v[148:151]\nv_cvt_pk_bf16_f32 v17,v18,v19\n" \
"ds_read_b64_tr_b16 a[220:221],v226 offset:25088\nds_read_b64_tr_b16 a[222:223],v226 offset:29184\nM v[152:155],v[96:99],a[224:227],v[152:155]\nbuffer_load_dwordx4 a[0:3],v240,s[52:55],s65 offen offset:0 sc1\nv_cvt_pk_bf16_f32 v18,v24,v25\nM v[156:159],v[100:103],a[224:227],v[156:159]\nv_cvt_pk_bf16_f32 v19,v26,v27\nds_read_b64_tr_b16 a[224:225],v227 offset:25088\n" \
"ds_read_b64_tr_b16 a[226:227],v227 offset:29184\nM v[160:163],v[96:99],a[228:231],v[160:163]\nv_lshl_add_u32 v229,v251,10,v250\nv_mul_f32 v20,v20,v228\nv_mul_f32 v21,v21,v228\nM v[164:167],v[100:103],a[228:231],v[164:167]\nds_read_b64_tr_b16 a[228:229],v226 offset:25600\nds_read_b64_tr_b16 a[230:231],v226 offset:29696\n" \
"v_mul_f32 v22,v22,v228\nM v[168:171],v[96:99],a[232:235],v[168:171]\nbuffer_store_dwordx4 v[16:19],v240,s[52:55],s37 offen offset:2048 sc1\nbuffer_load_dwordx4 a[4:7],v240,s[52:55],s65 offen offset:1024 sc1\nM v[172:175],v[100:103],a[232:235],v[172:175]\nds_read_b64_tr_b16 a[232:233],v227 offset:25600\nds_read_b64_tr_b16 a[234:235],v227 offset:29696\nv_mul_f32 v23,v23,v228\n" \
"M v[176:179],v[96:99],a[236:239],v[176:179]\nv_cvt_pk_bf16_f32 v20,v20,v21\nv_mul_f32 v28,v28,v228\nv_mul_f32 v29,v29,v228\nM v[180:183],v[100:103],a[236:239],v[180:183]\nds_read_b64_tr_b16 a[236:237],v226 offset:26112\nds_read_b64_tr_b16 a[238:239],v226 offset:30208\ns_waitcnt lgkmcnt(6)\n" \
"M v[184:187],v[96:99],a[240:243],v[184:187]\ns_waitcnt vmcnt(14)\ns_barrier\nbuffer_load_dwordx4 a[8:11],v240,s[52:55],s65 offen offset:2048 sc1\nM v[188:191],v[100:103],a[240:243],v[188:191]\nds_read_b32 v230,v229\nv_mul_f32 v30,v30,v228\nv_mul_f32 v31,v31,v228\n" \
"M v[128:131],v[112:115],a[212:215],v[128:131]\nv_cvt_pk_bf16_f32 v21,v22,v23\nds_read_b64_tr_b16 a[240:241],v227 offset:26112\nds_read_b64_tr_b16 a[242:243],v227 offset:30208\nM v[132:135],v[116:119],a[212:215],v[132:135]\nv_cvt_pk_bf16_f32 v22,v28,v29\nv_cvt_pk_bf16_f32 v23,v30,v31\nv_add_u32 v226,0,v242\n" \
"M v[136:139],v[112:115],a[216:219],v[136:139]\nbuffer_load_dwordx4 a[12:15],v240,s[52:55],s65 offen offset:3072 sc1\nds_read_b64_tr_b16 a[212:213],v226 offset:0\nM v[140:143],v[116:119],a[216:219],v[140:143]\nds_read_b64_tr_b16 a[214:215],v226 offset:4096\ns_add_u32 s65,s65,4096\nv_xor_b32 v227,32,v226\nM v[144:147],v[112:115],a[220:223],v[144:147]\n" \
"buffer_store_dwordx4 v[20:23],v240,s[52:55],s37 offen offset:3072 sc1\nds_read_b64_tr_b16 a[216:217],v227 offset:0\nM v[148:151],v[116:119],a[220:223],v[148:151]\nds_read_b64_tr_b16 a[218:219],v227 offset:4096\nds_read_b64_tr_b16 a[220:221],v226 offset:512\nds_read_b64_tr_b16 a[222:223],v226 offset:4608\nM v[152:155],v[112:115],a[224:227],v[152:155]\nbuffer_load_dwordx4 a[16:19],v240,s[52:55],s65 offen offset:0 sc1\n" \
"v_mul_f32 v32,v32,v224\nM v[156:159],v[116:119],a[224:227],v[156:159]\nds_read_b64_tr_b16 a[224:225],v227 offset:512\nds_read_b64_tr_b16 a[226:227],v227 offset:4608\ns_waitcnt lgkmcnt(8)\nM v[160:163],v[112:115],a[228:231],v[160:163]\nv_mul_f32 v33,v33,v224\nv_mul_f32 v34,v34,v224\n" \
"v_mul_f32 v35,v35,v224\nM v[164:167],v[116:119],a[228:231],v[164:167]\nv_cmp_ne_u32 vcc,0,v230\nv_cvt_pk_bf16_f32 v32,v32,v33\ns_cbranch_vccnz .Lredo\nM v[168:171],v[112:115],a[232:235],v[168:171]\nbuffer_load_dwordx4 a[20:23],v240,s[52:55],s65 offen offset:1024 sc1\nv_mul_f32 v40,v40,v224\n" \
"M v[172:175],v[116:119],a[232:235],v[172:175]\nv_mul_f32 v41,v41,v224\nv_mul_f32 v42,v42,v224\nv_mul_f32 v43,v43,v224\nM v[176:179],v[112:115],a[236:239],v[176:179]\nv_cvt_pk_bf16_f32 v33,v34,v35\nds_read_b64_tr_b16 a[228:229],v226 offset:1024\nds_read_b64_tr_b16 a[230:231],v226 offset:5120\n" \
"M v[180:183],v[116:119],a[236:239],v[180:183]\nv_cvt_pk_bf16_f32 v34,v40,v41\nv_cvt_pk_bf16_f32 v35,v42,v43\nds_read_b64_tr_b16 a[232:233],v227 offset:1024\nM v[184:187],v[112:115],a[240:243],v[184:187]\nbuffer_store_dwordx4 v[32:35],v240,s[52:55],s38 offen offset:0 sc1\nbuffer_load_dwordx4 a[24:27],v240,s[52:55],s65 offen offset:2048 sc1\nM v[188:191],v[116:119],a[240:243],v[188:191]\n" \
"ds_read_b64_tr_b16 a[234:235],v227 offset:5120\nds_read_b64_tr_b16 a[236:237],v226 offset:1536\ns_waitcnt lgkmcnt(5)\nM v[128:131],v[72:75],a[212:215],v[128:131]\nds_read_b64_tr_b16 a[238:239],v226 offset:5632\nds_read_b64_tr_b16 a[240:241],v227 offset:1536\nds_read_b64_tr_b16 a[242:243],v227 offset:5632\nM v[132:135],v[76:79],a[212:215],v[132:135]\n" \
"ds_read_b64_tr_b16 a[212:213],v226 offset:8192\nds_read_b64_tr_b16 a[214:215],v226 offset:12288\nv_mul_f32 v36,v36,v228\nM v[136:139],v[72:75],a[216:219],v[136:139]\nbuffer_load_dwordx4 a[28:31],v240,s[52:55],s65 offen offset:3072 sc1\nv_mul_f32 v37,v37,v228\nM v[140:143],v[76:79],a[216:219],v[140:143]\nv_mul_f32 v38,v38,v228\n" \
"v_mul_f32 v39,v39,v228\nv_cvt_pk_bf16_f32 v36,v36,v37\nM v[144:147],v[72:75],a[220:223],v[144:147]\nv_mul_f32 v44,v44,v228\nv_mul_f32 v45,v45,v228\nv_mul_f32 v46,v46,v228\nM v[148:151],v[76:79],a[220:223],v[148:151]\nv_mul_f32 v47,v47,v228\n" \
"v_cvt_pk_bf16_f32 v37,v38,v39\nds_read_b64_tr_b16 a[216:217],v227 offset:8192\nM v[152:155],v[72:75],a[224:227],v[152:155]\nds_read_b64_tr_b16 a[218:219],v227 offset:12288\nv_cvt_pk_bf16_f32 v38,v44,v45\nv_cvt_pk_bf16_f32 v39,v46,v47\nM v[156:159],v[76:79],a[224:227],v[156:159]\ns_lshl4_add_u32 s69,s14,s68\n" \
"s_add_u32 m0,s64,65536\ns_waitcnt lgkmcnt(2)\nM v[160:163],v[72:75],a[228:231],v[160:163]\nbuffer_store_dwordx4 v[36:39],v240,s[52:55],s38 offen offset:1024 sc1\nbuffer_load_dwordx4 v243,s[40:43],s69 offen offset:0 lds\nM v[164:167],v[76:79],a[228:231],v[164:167]\nds_read_b64_tr_b16 a[220:221],v226 offset:8704\nds_read_b64_tr_b16 a[222:223],v226 offset:12800\n" \
"ds_read_b64_tr_b16 a[224:225],v227 offset:8704\nM v[168:171],v[72:75],a[232:235],v[168:171]\nds_read_b64_tr_b16 a[226:227],v227 offset:12800\nds_read_b64_tr_b16 a[228:229],v226 offset:9216\nv_mul_f32 v48,v48,v224\nM v[172:175],v[76:79],a[232:235],v[172:175]\nds_read_b64_tr_b16 a[230:231],v226 offset:13312\nds_read_b64_tr_b16 a[232:233],v227 offset:9216\n" \
"v_mul_f32 v49,v49,v224\nM v[176:179],v[72:75],a[236:239],v[176:179]\nbuffer_load_dwordx4 v244,s[40:43],s69 offen offset:1024 lds\nds_read_b64_tr_b16 a[234:235],v227 offset:13312\nM v[180:183],v[76:79],a[236:239],v[180:183]\nv_mul_f32 v50,v50,v224\nv_mul_f32 v51,v51,v224\nv_cvt_pk_bf16_f32 v48,v48,v49\n" \
"M v[184:187],v[72:75],a[240:243],v[184:187]\nv_mul_f32 v56,v56,v224\nv_mul_f32 v57,v57,v224\nv_mul_f32 v58,v58,v224\nM v[188:191],v[76:79],a[240:243],v[188:191]\nv_mul_f32 v59,v59,v224\nv_cvt_pk_bf16_f32 v49,v50,v51\nds_read_b64_tr_b16 a[236:237],v226 offset:9728\n" \
"M v[128:131],v[88:91],a[212:215],v[128:131]\nbuffer_load_dwordx4 v245,s[40:43],s69 offen offset:2048 lds\nds_read_b64_tr_b16 a[238:239],v226 offset:13824\nM v[132:135],v[92:95],a[212:215],v[132:135]\nv_cvt_pk_bf16_f32 v50,v56,v57\nv_cvt_pk_bf16_f32 v51,v58,v59\ns_waitcnt lgkmcnt(3)\nM v[136:139],v[88:91],a[216:219],v[136:139]\n" \
"buffer_store_dwordx4 v[48:51],v240,s[52:55],s38 offen offset:2048 sc1\nds_read_b64_tr_b16 a[240:241],v227 offset:9728\nM v[140:143],v[92:95],a[216:219],v[140:143]\nds_read_b64_tr_b16 a[242:243],v227 offset:13824\nds_read_b64_tr_b16 a[212:213],v226 offset:16384\nds_read_b64_tr_b16 a[214:215],v226 offset:20480\nM v[144:147],v[88:91],a[220:223],v[144:147]\nbuffer_load_dwordx4 v246,s[40:43],s69 offen offset:3072 lds\n" \
"s_lshl4_add_u32 s69,s14,s68\nM v[148:151],v[92:95],a[220:223],v[148:151]\nds_read_b64_tr_b16 a[216:217],v227 offset:16384\nds_read_b64_tr_b16 a[218:219],v227 offset:20480\ns_add_u32 s69,s69,0x10000\nM v[152:155],v[88:91],a[224:227],v[152:155]\ns_add_u32 m0,s64,98304\nds_read_b64_tr_b16 a[220:221],v226 offset:16896\n" \
"ds_read_b64_tr_b16 a[222:223],v226 offset:20992\nM v[156:159],v[92:95],a[224:227],v[156:159]\nv_mul_f32 v52,v52,v228\nv_mul_f32 v53,v53,v228\nv_mul_f32 v54,v54,v228\nM v[160:163],v[88:91],a[228:231],v[160:163]\nbuffer_load_dwordx4 v243,s[40:43],s69 offen offset:0 lds\nv_mul_f32 v55,v55,v228\n" \
"M v[164:167],v[92:95],a[228:231],v[164:167]\nv_cvt_pk_bf16_f32 v52,v52,v53\nv_mul_f32 v60,v60,v228\ns_waitcnt lgkmcnt(4)\nM v[168:171],v[88:91],a[232:235],v[168:171]\nv_mul_f32 v61,v61,v228\nv_mul_f32 v62,v62,v228\nv_mul_f32 v63,v63,v228\n" \
"M v[172:175],v[92:95],a[232:235],v[172:175]\nv_cvt_pk_bf16_f32 v53,v54,v55\nds_read_b64_tr_b16 a[224:225],v227 offset:16896\nds_read_b64_tr_b16 a[226:227],v227 offset:20992\nM v[176:179],v[88:91],a[236:239],v[176:179]\nv_cvt_pk_bf16_f32 v54,v60,v61\nv_cvt_pk_bf16_f32 v55,v62,v63\nbuffer_store_dwordx4 v[52:55],v240,s[52:55],s38 offen offset:3072 sc1\n" \
"buffer_load_dwordx4 v244,s[40:43],s69 offen offset:1024 lds\nM v[180:183],v[92:95],a[236:239],v[180:183]\nds_read_b64_tr_b16 a[228:229],v226 offset:17408\nds_read_b64_tr_b16 a[230:231],v226 offset:21504\nds_read_b64_tr_b16 a[232:233],v227 offset:17408\nM v[184:187],v[88:91],a[240:243],v[184:187]\nds_read_b64_tr_b16 a[234:235],v227 offset:21504\nds_read_b64_tr_b16 a[236:237],v226 offset:17920\n" \
"s_add_u32 s65,s14,s67\nM v[188:191],v[92:95],a[240:243],v[188:191]\nds_read_b64_tr_b16 a[238:239],v226 offset:22016\nds_read_b64_tr_b16 a[240:241],v227 offset:17920\ns_add_u32 s65,s65,1048576\nM v[128:131],v[104:107],a[212:215],v[128:131]\nbuffer_load_dwordx4 v245,s[40:43],s69 offen offset:2048 lds\nds_read_b64_tr_b16 a[242:243],v227 offset:22016\n" \
"M v[132:135],v[108:111],a[212:215],v[132:135]\nds_read_b64_tr_b16 a[212:213],v226 offset:24576\nds_read_b64_tr_b16 a[214:215],v226 offset:28672\ns_waitcnt lgkmcnt(10)\nM v[136:139],v[104:107],a[216:219],v[136:139]\nv_and_b32 v228,31,v249\nv_lshl_add_u32 v228,v228,2,s70\nv_and_b32 v229,32,v249\n" \
"M v[140:143],v[108:111],a[216:219],v[140:143]\nds_read_b64_tr_b16 a[216:217],v227 offset:24576\nds_read_b64_tr_b16 a[218:219],v227 offset:28672\nv_mul_u32_u24 v229,12,v229\nM v[144:147],v[104:107],a[220:223],v[144:147]\nbuffer_load_dwordx4 v246,s[40:43],s69 offen offset:3072 lds\nM v[148:151],v[108:111],a[220:223],v[148:151]\nds_read_b64_tr_b16 a[220:221],v226 offset:25088\n" \
"ds_read_b64_tr_b16 a[222:223],v226 offset:29184\nM v[152:155],v[104:107],a[224:227],v[152:155]\ns_lshr_b32 s70,s6,3\nv_xor_b32 v228,v228,v229\nM v[156:159],v[108:111],a[224:227],v[156:159]\nds_read_b64_tr_b16 a[224:225],v227 offset:25088\ns_waitcnt lgkmcnt(7)\nM v[160:163],v[104:107],a[228:231],v[160:163]\n" \
"ds_read_b64_tr_b16 a[226:227],v227 offset:29184\ns_min_u32 s70,s70,27\nM v[164:167],v[108:111],a[228:231],v[164:167]\nds_read_b64_tr_b16 a[228:229],v226 offset:25600\nds_read_b64_tr_b16 a[230:231],v226 offset:29696\nM v[168:171],v[104:107],a[232:235],v[168:171]\nbuffer_load_dwordx4 a[32:35],v240,s[52:55],s65 offen offset:0 sc1\nM v[172:175],v[108:111],a[232:235],v[172:175]\n" \
"ds_read_b64_tr_b16 a[232:233],v227 offset:25600\nds_read_b64_tr_b16 a[234:235],v227 offset:29696\nM v[176:179],v[104:107],a[236:239],v[176:179]\ns_add_u32 s70,s70,s71\nv_and_b32 v229,3,v249\nM v[180:183],v[108:111],a[236:239],v[180:183]\nds_read_b64_tr_b16 a[236:237],v226 offset:26112\nds_read_b64_tr_b16 a[238:239],v226 offset:30208\n" \
"M v[184:187],v[104:107],a[240:243],v[184:187]\ns_waitcnt vmcnt(22)\ns_barrier\nM v[188:191],v[108:111],a[240:243],v[188:191]\nds_read_b64_tr_b16 a[240:241],v227 offset:26112\ns_waitcnt lgkmcnt(7)\nM v[128:131],v[120:123],a[212:215],v[128:131]\nbuffer_load_dwordx4 a[36:39],v240,s[52:55],s65 offen offset:1024 sc1\n" \
"M v[132:135],v[124:127],a[212:215],v[132:135]\nds_read_b64_tr_b16 a[242:243],v227 offset:30208\nds_read_b64_tr_b16 a[212:213],v226 offset:32768\nM v[136:139],v[120:123],a[216:219],v[136:139]\nds_read_b64_tr_b16 a[214:215],v226 offset:36864\nv_lshrrev_b32 v230,2,v249\nM v[140:143],v[124:127],a[216:219],v[140:143]\nds_read_b64_tr_b16 a[216:217],v227 offset:32768\n" \
"ds_read_b64_tr_b16 a[218:219],v227 offset:36864\nM v[144:147],v[120:123],a[220:223],v[144:147]\nv_lshl_add_u32 v229,v229,5,v230\ns_mul_i32 s71,s7,458752\nM v[148:151],v[124:127],a[220:223],v[148:151]\nds_read_b64_tr_b16 a[220:221],v226 offset:33280\nds_read_b64_tr_b16 a[222:223],v226 offset:37376\nM v[152:155],v[120:123],a[224:227],v[152:155]\n" \
"buffer_load_dwordx4 a[40:43],v240,s[52:55],s65 offen offset:2048 sc1\nM v[156:159],v[124:127],a[224:227],v[156:159]\nds_read_b64_tr_b16 a[224:225],v227 offset:33280\ns_waitcnt lgkmcnt(8)\nM v[160:163],v[120:123],a[228:231],v[160:163]\nds_read_b64_tr_b16 a[226:227],v227 offset:37376\ns_lshl4_add_u32 s71,s70,s71\nM v[164:167],v[124:127],a[228:231],v[164:167]\n" \
"ds_read_b64_tr_b16 a[228:229],v226 offset:33792\nds_read_b64_tr_b16 a[230:231],v226 offset:37888\nM v[168:171],v[120:123],a[232:235],v[168:171]\nv_mul_u32_u24 v229,3584,v229\nv_add_u32 v229,s71,v229\nM v[172:175],v[124:127],a[232:235],v[172:175]\nds_read_b64_tr_b16 a[232:233],v227 offset:33792\nds_read_b64_tr_b16 a[234:235],v227 offset:37888\n" \
"M v[176:179],v[120:123],a[236:239],v[176:179]\nbuffer_load_dwordx4 a[44:47],v240,s[52:55],s65 offen offset:3072 sc1\nM v[180:183],v[124:127],a[236:239],v[180:183]\nds_read_b64_tr_b16 a[236:237],v226 offset:34304\ns_waitcnt lgkmcnt(7)\nM v[184:187],v[120:123],a[240:243],v[184:187]\nds_read_b64_tr_b16 a[238:239],v226 offset:38400\ns_add_u32 s65,s65,4096\n" \
"M v[188:191],v[124:127],a[240:243],v[188:191]\nds_read_b64_tr_b16 a[240:241],v227 offset:34304\nds_read_b64_tr_b16 a[242:243],v227 offset:38400\nM v[128:131],a[0:3],a[212:215],v[128:131]\nv_add_u32 v230,57344,v229\ns_waitcnt vmcnt(21)\nM v[132:135],a[4:7],a[212:215],v[132:135]\nds_read_b64_tr_b16 a[212:213],v226 offset:40960\n" \
"ds_read_b64_tr_b16 a[214:215],v226 offset:45056\nM v[136:139],a[0:3],a[216:219],v[136:139]\nbuffer_load_dwordx4 a[48:51],v240,s[52:55],s65 offen offset:0 sc1\nglobal_load_dwordx4 a[160:163],v229,s[22:23]\nM v[140:143],a[4:7],a[216:219],v[140:143]\nds_read_b64_tr_b16 a[216:217],v227 offset:40960\nds_read_b64_tr_b16 a[218:219],v227 offset:45056\nM v[144:147],a[0:3],a[220:223],v[144:147]\n" \
"v_mov_b32 v250,81920\nds_write_b64 v250,a[244:245]\nM v[148:151],a[4:7],a[220:223],v[148:151]\nds_read_b64_tr_b16 a[220:221],v226 offset:41472\ns_waitcnt lgkmcnt(8)\nM v[152:155],a[0:3],a[224:227],v[152:155]\nds_read_b64_tr_b16 a[222:223],v226 offset:45568\ns_lshl_b32 s70,s70,13\n" \
"M v[156:159],a[4:7],a[224:227],v[156:159]\nds_read_b64_tr_b16 a[224:225],v227 offset:41472\nds_read_b64_tr_b16 a[226:227],v227 offset:45568\nM v[160:163],a[0:3],a[228:231],v[160:163]\nbuffer_load_dwordx4 a[52:55],v240,s[52:55],s65 offen offset:1024 sc1\nM v[164:167],a[4:7],a[228:231],v[164:167]\nds_read_b64_tr_b16 a[228:229],v226 offset:41984\nds_read_b64_tr_b16 a[230:231],v226 offset:46080\n" \
"M v[168:171],a[0:3],a[232:235],v[168:171]\ns_lshl_b32 s71,s7,8\ns_add_u32 s70,s70,s71\nM v[172:175],a[4:7],a[232:235],v[172:175]\nds_read_b64_tr_b16 a[232:233],v227 offset:41984\nds_read_b64_tr_b16 a[234:235],v227 offset:46080\nM v[176:179],a[0:3],a[236:239],v[176:179]\nglobal_load_dwordx4 a[164:167],v230,s[22:23]\n" \
"M v[180:183],a[4:7],a[236:239],v[180:183]\nds_read_b64_tr_b16 a[236:237],v226 offset:42496\ns_waitcnt lgkmcnt(7)\nM v[184:187],a[0:3],a[240:243],v[184:187]\nbuffer_load_dwordx4 a[56:59],v240,s[52:55],s65 offen offset:2048 sc1\nM v[188:191],a[4:7],a[240:243],v[188:191]\nds_read_b64_tr_b16 a[238:239],v226 offset:46592\nds_read_b64_tr_b16 a[240:241],v227 offset:42496\n" \
"M v[128:131],a[8:11],a[212:215],v[128:131]\nds_read_b64_tr_b16 a[242:243],v227 offset:46592\ns_add_u32 s70,s70,4096\nM v[132:135],a[12:15],a[212:215],v[132:135]\nds_read_b64_tr_b16 a[212:213],v226 offset:49152\nds_read_b64_tr_b16 a[214:215],v226 offset:53248\nM v[136:139],a[8:11],a[216:219],v[136:139]\nv_lshl_add_u32 v231,v249,2,s70\n" \
"s_add_u32 s95,s74,s72\nM v[140:143],a[12:15],a[216:219],v[140:143]\nds_read_b64_tr_b16 a[216:217],v227 offset:49152\nds_read_b64_tr_b16 a[218:219],v227 offset:53248\nM v[144:147],a[8:11],a[220:223],v[144:147]\nbuffer_load_dwordx4 a[60:63],v240,s[52:55],s65 offen offset:3072 sc1\nM v[148:151],a[12:15],a[220:223],v[148:151]\nds_read_b64_tr_b16 a[220:221],v226 offset:49664\n" \
"s_waitcnt lgkmcnt(8)\nM v[152:155],a[8:11],a[224:227],v[152:155]\nglobal_load_dwordx4 a[168:171],v229,s[24:25]\nM v[156:159],a[12:15],a[224:227],v[156:159]\nds_read_b64_tr_b16 a[222:223],v226 offset:53760\nds_read_b64_tr_b16 a[224:225],v227 offset:49664\nM v[160:163],a[8:11],a[228:231],v[160:163]\nds_read_b64_tr_b16 a[226:227],v227 offset:53760\n" \
"v_lshl_or_b32 v235,v235,8,v236\nM v[164:167],a[12:15],a[228:231],v[164:167]\nds_read_b64_tr_b16 a[228:229],v226 offset:50176\nds_read_b64_tr_b16 a[230:231],v226 offset:54272\nM v[168:171],a[8:11],a[232:235],v[168:171]\ns_lshl_b32 s95,s95,10\nv_add_u32 v236,s95,v235\nM v[172:175],a[12:15],a[232:235],v[172:175]\n" \
"ds_read_b64_tr_b16 a[232:233],v227 offset:50176\ns_waitcnt lgkmcnt(7)\nM v[176:179],a[8:11],a[236:239],v[176:179]\ns_waitcnt vmcnt(14)\nbuffer_store_dword v254,off,s[52:55],s76 sc1\nM v[180:183],a[12:15],a[236:239],v[180:183]\nds_read_b64_tr_b16 a[234:235],v227 offset:54272\nds_read_b64_tr_b16 a[236:237],v226 offset:50688\n" \
"M v[184:187],a[8:11],a[240:243],v[184:187]\nds_read_b64_tr_b16 a[238:239],v226 offset:54784\ns_sub_u32 s74,s74,s72\nM v[188:191],a[12:15],a[240:243],v[188:191]\nds_read_b64_tr_b16 a[240:241],v227 offset:50688\nds_read_b64_tr_b16 a[242:243],v227 offset:54784\nM v[128:131],a[16:19],a[212:215],v[128:131]\nglobal_load_dwordx4 a[172:175],v230,s[24:25]\n" \
"M v[132:135],a[20:23],a[212:215],v[132:135]\nds_read_b64_tr_b16 a[212:213],v226 offset:57344\nds_read_b64_tr_b16 a[214:215],v226 offset:61440\nM v[136:139],a[16:19],a[216:219],v[136:139]\ns_add_u32 s74,s74,1920\ns_lshl_b32 s74,s74,10\nM v[140:143],a[20:23],a[216:219],v[140:143]\nds_read_b64_tr_b16 a[216:217],v227 offset:57344\n" \
"s_waitcnt lgkmcnt(8)\nM v[144:147],a[16:19],a[220:223],v[144:147]\nds_read_b64_tr_b16 a[218:219],v227 offset:61440\nv_add_u32 v237,s74,v235\nM v[148:151],a[20:23],a[220:223],v[148:151]\nds_read_b64_tr_b16 a[220:221],v226 offset:57856\nds_read_b64_tr_b16 a[222:223],v226 offset:61952\nM v[152:155],a[16:19],a[224:227],v[152:155]\n" \
"s_lshl_b32 s72,s7,9\nv_lshl_add_u32 v239,v249,3,s72\nM v[156:159],a[20:23],a[224:227],v[156:159]\nds_read_b64_tr_b16 a[224:225],v227 offset:57856\nds_read_b64_tr_b16 a[226:227],v227 offset:61952\nM v[160:163],a[16:19],a[228:231],v[160:163]\nbuffer_load_dword v248,v228,s[52:55],s80 offen sc1\nglobal_load_dword a176,v231,s[26:27] offset:-4096\n" \
"M v[164:167],a[20:23],a[228:231],v[164:167]\nds_read_b64_tr_b16 a[228:229],v226 offset:58368\ns_waitcnt lgkmcnt(7)\nM v[168:171],a[16:19],a[232:235],v[168:171]\nglobal_load_dword a177,v231,s[26:27] offset:-3072\nM v[172:175],a[20:23],a[232:235],v[172:175]\nglobal_load_ushort v64,v236,s[8:9] offset:0\nM v[176:179],a[16:19],a[236:239],v[176:179]\n" \
"global_load_dword a178,v231,s[26:27] offset:-2048\nM v[180:183],a[20:23],a[236:239],v[180:183]\nds_read_b64_tr_b16 a[230:231],v226 offset:62464\nglobal_load_ushort v80,v237,s[8:9] offset:0\nM v[184:187],a[16:19],a[240:243],v[184:187]\ns_waitcnt vmcnt(17)\ns_barrier\nglobal_load_dword a179,v231,s[26:27] offset:-1024\n" \
"M v[188:191],a[20:23],a[240:243],v[188:191]\nds_read_b64_tr_b16 a[232:233],v227 offset:58368\nds_read_b64_tr_b16 a[234:235],v227 offset:62464\nglobal_load_ushort v96,v235,s[20:21] offset:0\nM v[128:131],a[24:27],a[212:215],v[128:131]\nglobal_load_dword a180,v231,s[26:27] offset:0\nM v[132:135],a[28:31],a[212:215],v[132:135]\nds_read_b64_tr_b16 a[236:237],v226 offset:58880\n" \
"ds_read_b64_tr_b16 a[238:239],v226 offset:62976\nglobal_load_ushort v65,v236,s[8:9] offset:32\ns_waitcnt lgkmcnt(5)\nM v[136:139],a[24:27],a[216:219],v[136:139]\nglobal_load_dword a181,v231,s[26:27] offset:1024\nM v[140:143],a[28:31],a[216:219],v[140:143]\nds_read_b64_tr_b16 a[240:241],v227 offset:58880\nds_read_b64_tr_b16 a[242:243],v227 offset:62976\n" \
"global_load_ushort v81,v237,s[8:9] offset:32\nM v[144:147],a[24:27],a[220:223],v[144:147]\nglobal_load_dword a182,v231,s[26:27] offset:2048\nM v[148:151],a[28:31],a[220:223],v[148:151]\nv_add_u32 v226,65536,v242\nds_read_b64_tr_b16 a[212:213],v226 offset:0\nds_read_b64_tr_b16 a[214:215],v226 offset:4096\nglobal_load_ushort v97,v235,s[20:21] offset:32\n" \
"M v[152:155],a[24:27],a[224:227],v[152:155]\nglobal_load_dword a183,v231,s[26:27] offset:3072\nM v[156:159],a[28:31],a[224:227],v[156:159]\nv_xor_b32 v227,32,v226\nds_read_b64_tr_b16 a[216:217],v227 offset:0\nds_read_b64_tr_b16 a[218:219],v227 offset:4096\nglobal_load_ushort v66,v236,s[8:9] offset:64\ns_waitcnt lgkmcnt(6)\n" \
"M v[160:163],a[24:27],a[228:231],v[160:163]\nds_read_b64_tr_b16 a[220:221],v226 offset:512\nds_read_b64_tr_b16 a[222:223],v226 offset:4608\nM v[164:167],a[28:31],a[228:231],v[164:167]\nglobal_load_ushort v82,v237,s[8:9] offset:64\nM v[168:171],a[24:27],a[232:235],v[168:171]\nds_read_b64_tr_b16 a[224:225],v227 offset:512\nds_read_b64_tr_b16 a[226:227],v227 offset:4608\n" \
"M v[172:175],a[28:31],a[232:235],v[172:175]\nglobal_load_ushort v98,v235,s[20:21] offset:64\nM v[176:179],a[24:27],a[236:239],v[176:179]\nds_read_b64_tr_b16 a[228:229],v226 offset:1024\nds_read_b64_tr_b16 a[230:231],v226 offset:5120\nM v[180:183],a[28:31],a[236:239],v[180:183]\nglobal_load_ushort v67,v236,s[8:9] offset:96\ns_waitcnt lgkmcnt(7)\n" \
"M v[184:187],a[24:27],a[240:243],v[184:187]\nds_read_b64_tr_b16 a[232:233],v227 offset:1024\nds_read_b64_tr_b16 a[234:235],v227 offset:5120\nM v[188:191],a[28:31],a[240:243],v[188:191]\nglobal_load_ushort v83,v237,s[8:9] offset:96\nM v[128:131],a[32:35],a[212:215],v[128:131]\nds_read_b64_tr_b16 a[236:237],v226 offset:1536\nds_read_b64_tr_b16 a[238:239],v226 offset:5632\n" \
"M v[132:135],a[36:39],a[212:215],v[132:135]\nglobal_load_ushort v99,v235,s[20:21] offset:96\ns_waitcnt lgkmcnt(6)\nM v[136:139],a[32:35],a[216:219],v[136:139]\nds_read_b64_tr_b16 a[240:241],v227 offset:1536\nds_read_b64_tr_b16 a[242:243],v227 offset:5632\nM v[140:143],a[36:39],a[216:219],v[140:143]\nglobal_load_ushort v68,v236,s[8:9] offset:128\n" \
"M v[144:147],a[32:35],a[220:223],v[144:147]\nds_read_b64_tr_b16 a[212:213],v226 offset:8192\nds_read_b64_tr_b16 a[214:215],v226 offset:12288\nM v[148:151],a[36:39],a[220:223],v[148:151]\nglobal_load_ushort v84,v237,s[8:9] offset:128\nM v[152:155],a[32:35],a[224:227],v[152:155]\nds_read_b64_tr_b16 a[216:217],v227 offset:8192\nds_read_b64_tr_b16 a[218:219],v227 offset:12288\n" \
"M v[156:159],a[36:39],a[224:227],v[156:159]\nglobal_load_ushort v100,v235,s[20:21] offset:128\ns_waitcnt lgkmcnt(6)\nM v[160:163],a[32:35],a[228:231],v[160:163]\nds_read_b64_tr_b16 a[220:221],v226 offset:8704\nds_read_b64_tr_b16 a[222:223],v226 offset:12800\nM v[164:167],a[36:39],a[228:231],v[164:167]\nglobal_load_ushort v69,v236,s[8:9] offset:160\n" \
"M v[168:171],a[32:35],a[232:235],v[168:171]\nds_read_b64_tr_b16 a[224:225],v227 offset:8704\nds_read_b64_tr_b16 a[226:227],v227 offset:12800\nM v[172:175],a[36:39],a[232:235],v[172:175]\nglobal_load_ushort v85,v237,s[8:9] offset:160\nM v[176:179],a[32:35],a[236:239],v[176:179]\nds_read_b64_tr_b16 a[228:229],v226 offset:9216\nds_read_b64_tr_b16 a[230:231],v226 offset:13312\n" \
"M v[180:183],a[36:39],a[236:239],v[180:183]\nglobal_load_ushort v101,v235,s[20:21] offset:160\ns_waitcnt lgkmcnt(6)\nM v[184:187],a[32:35],a[240:243],v[184:187]\nds_read_b64_tr_b16 a[232:233],v227 offset:9216\nds_read_b64_tr_b16 a[234:235],v227 offset:13312\nM v[188:191],a[36:39],a[240:243],v[188:191]\nglobal_load_ushort v70,v236,s[8:9] offset:192\n" \
"s_waitcnt vmcnt(30)\nM v[128:131],a[40:43],a[212:215],v[128:131]\nds_read_b64_tr_b16 a[236:237],v226 offset:9728\nds_read_b64_tr_b16 a[238:239],v226 offset:13824\nM v[132:135],a[44:47],a[212:215],v[132:135]\nglobal_load_ushort v86,v237,s[8:9] offset:192\nM v[136:139],a[40:43],a[216:219],v[136:139]\ns_waitcnt vmcnt(28)\n" \
"v_cmp_eq_u32 vcc,0,v248\nM v[140:143],a[44:47],a[216:219],v[140:143]\nds_read_b64_tr_b16 a[240:241],v227 offset:9728\nds_read_b64_tr_b16 a[242:243],v227 offset:13824\nglobal_load_ushort v102,v235,s[20:21] offset:192\ns_waitcnt lgkmcnt(6)\nM v[144:147],a[40:43],a[220:223],v[144:147]\ns_cbranch_vccnz .Lps7\n" \
".Lpb7:\nbuffer_load_dword v248,v251,s[52:55],s79 offen sc1\nM v[148:151],a[44:47],a[220:223],v[148:151]\nds_read_b64_tr_b16 a[212:213],v226 offset:32768\nds_read_b64_tr_b16 a[214:215],v226 offset:36864\nglobal_load_ushort v71,v236,s[8:9] offset:224\nM v[152:155],a[40:43],a[224:227],v[152:155]\nds_read_b64_tr_b16 a[216:217],v227 offset:32768\n" \
"ds_read_b64_tr_b16 a[218:219],v227 offset:36864\nM v[156:159],a[44:47],a[224:227],v[156:159]\nglobal_load_ushort v87,v237,s[8:9] offset:224\nM v[160:163],a[40:43],a[228:231],v[160:163]\nds_read_b64_tr_b16 a[220:221],v226 offset:33280\nds_read_b64_tr_b16 a[222:223],v226 offset:37376\nM v[164:167],a[44:47],a[228:231],v[164:167]\nglobal_load_ushort v103,v235,s[20:21] offset:224\n" \
"s_waitcnt lgkmcnt(6)\nM v[168:171],a[40:43],a[232:235],v[168:171]\nds_read_b64_tr_b16 a[224:225],v227 offset:33280\nds_read_b64_tr_b16 a[226:227],v227 offset:37376\nM v[172:175],a[44:47],a[232:235],v[172:175]\nbuffer_load_dwordx2 v[112:113],v239,s[52:55],0 offen offset:0 sc1\nM v[176:179],a[40:43],a[236:239],v[176:179]\nds_read_b64_tr_b16 a[228:229],v226 offset:33792\n" \
"ds_read_b64_tr_b16 a[230:231],v226 offset:37888\nM v[180:183],a[44:47],a[236:239],v[180:183]\nbuffer_load_dwordx2 v[114:115],v239,s[52:55],0 offen offset:2048 sc1\nM v[184:187],a[40:43],a[240:243],v[184:187]\ns_mov_b32 s97,4096\nds_read_b64_tr_b16 a[232:233],v227 offset:33792\nM v[188:191],a[44:47],a[240:243],v[188:191]\nds_read_b64_tr_b16 a[234:235],v227 offset:37888\n" \
"buffer_load_dwordx2 v[116:117],v239,s[52:55],s97 offen offset:0 sc1\ns_waitcnt lgkmcnt(6)\nM v[128:131],a[48:51],a[212:215],v[128:131]\nds_read_b64_tr_b16 a[236:237],v226 offset:34304\nds_read_b64_tr_b16 a[238:239],v226 offset:38400\nM v[132:135],a[52:55],a[212:215],v[132:135]\nbuffer_load_dwordx2 v[118:119],v239,s[52:55],s97 offen offset:2048 sc1\nM v[136:139],a[48:51],a[216:219],v[136:139]\n" \
"ds_read_b64_tr_b16 a[240:241],v227 offset:34304\nds_read_b64_tr_b16 a[242:243],v227 offset:38400\nM v[140:143],a[52:55],a[216:219],v[140:143]\nds_read_b64_tr_b16 a[212:213],v226 offset:40960\nds_read_b64_tr_b16 a[214:215],v226 offset:45056\nM v[144:147],a[48:51],a[220:223],v[144:147]\nds_read_b64_tr_b16 a[216:217],v227 offset:40960\nds_read_b64_tr_b16 a[218:219],v227 offset:45056\n" \
"M v[148:151],a[52:55],a[220:223],v[148:151]\nds_read_b64_tr_b16 a[220:221],v226 offset:41472\ns_waitcnt lgkmcnt(10)\nM v[152:155],a[48:51],a[224:227],v[152:155]\nds_read_b64_tr_b16 a[222:223],v226 offset:45568\ns_add_u32 s65,s10,s67\nM v[156:159],a[52:55],a[224:227],v[156:159]\nds_read_b64_tr_b16 a[224:225],v227 offset:41472\n" \
"ds_read_b64_tr_b16 a[226:227],v227 offset:45568\nM v[160:163],a[48:51],a[228:231],v[160:163]\ns_add_u32 s38,s57,16384\nv_lshlrev_b32 v238,1,v240\nM v[164:167],a[52:55],a[228:231],v[164:167]\nds_read_b64_tr_b16 a[228:229],v226 offset:41984\ns_waitcnt lgkmcnt(10)\nM v[168:171],a[48:51],a[232:235],v[168:171]\n" \
"ds_read_b64_tr_b16 a[230:231],v226 offset:46080\nv_bfe_u32 v234,v249,2,2\nM v[172:175],a[52:55],a[232:235],v[172:175]\nds_read_b64_tr_b16 a[232:233],v227 offset:41984\nds_read_b64_tr_b16 a[234:235],v227 offset:46080\nM v[176:179],a[48:51],a[236:239],v[176:179]\nv_lshrrev_b32 v235,4,v249\nv_accvgpr_write_b32 a194,0\n" \
"M v[180:183],a[52:55],a[236:239],v[180:183]\nds_read_b64_tr_b16 a[236:237],v226 offset:42496\ns_waitcnt lgkmcnt(7)\nM v[184:187],a[48:51],a[240:243],v[184:187]\nds_read_b64_tr_b16 a[238:239],v226 offset:46592\nv_add_u32 v226,131072,v242\nM v[188:191],a[52:55],a[240:243],v[188:191]\nds_read_b64_tr_b16 a[240:241],v227 offset:42496\n" \
"ds_read_b64_tr_b16 a[242:243],v227 offset:46592\nM v[128:131],a[56:59],a[212:215],v[128:131]\nv_xor_b32 v227,32,v226\nv_accvgpr_write_b32 a195,0\nM v[132:135],a[60:63],a[212:215],v[132:135]\nds_read_b64_tr_b16 a[212:213],v226 offset:0\nds_read_b64_tr_b16 a[214:215],v226 offset:4096\nM v[136:139],a[56:59],a[216:219],v[136:139]\n" \
"v_accvgpr_write_b32 a196,0\nv_accvgpr_write_b32 a197,0\nM v[140:143],a[60:63],a[216:219],v[140:143]\nds_read_b64_tr_b16 a[216:217],v227 offset:0\nds_read_b64_tr_b16 a[218:219],v227 offset:4096\nM v[144:147],a[56:59],a[220:223],v[144:147]\ns_load_dwordx2 s[44:45],s[4:5],0x68\ns_mov_b32 s46,2097152\n" \
"M v[148:151],a[60:63],a[220:223],v[148:151]\nds_read_b64_tr_b16 a[220:221],v226 offset:512\ns_waitcnt lgkmcnt(7)\nM v[152:155],a[56:59],a[224:227],v[152:155]\ns_waitcnt vmcnt(7)\nv_cmp_eq_u32 vcc,0,v248\nM v[156:159],a[60:63],a[224:227],v[156:159]\ns_cbranch_vccnz .Lps8\n" \
".Lpb8:\nds_read_b64_tr_b16 a[222:223],v226 offset:4608\nM v[160:163],a[56:59],a[228:231],v[160:163]\nbuffer_load_dwordx4 a[64:67],v240,s[52:55],s65 offen offset:0 sc1\nM v[164:167],a[60:63],a[228:231],v[164:167]\nds_read_b64_tr_b16 a[224:225],v227 offset:512\nds_read_b64_tr_b16 a[226:227],v227 offset:4608\nM v[168:171],a[56:59],a[232:235],v[168:171]\n" \
"ds_read_b64_tr_b16 a[228:229],v226 offset:1024\nds_read_b64_tr_b16 a[230:231],v226 offset:5120\nM v[172:175],a[60:63],a[232:235],v[172:175]\nds_read_b64_tr_b16 a[232:233],v227 offset:1024\nds_read_b64_tr_b16 a[234:235],v227 offset:5120\nM v[176:179],a[56:59],a[236:239],v[176:179]\nbuffer_load_dwordx4 a[68:71],v240,s[52:55],s65 offen offset:1024 sc1\nM v[180:183],a[60:63],a[236:239],v[180:183]\n" \
"ds_read_b64_tr_b16 a[236:237],v226 offset:1536\ns_waitcnt lgkmcnt(8)\nM v[184:187],a[56:59],a[240:243],v[184:187]\nds_read_b64_tr_b16 a[238:239],v226 offset:5632\nv_cvt_pk_bf16_f32 v192,v128,v136\nM v[188:191],a[60:63],a[240:243],v[188:191]\nds_read_b64_tr_b16 a[240:241],v227 offset:1536\nds_read_b64_tr_b16 a[242:243],v227 offset:5632\n" \
"M v[8:11],v[0:3],a[212:215],0\nbuffer_load_dwordx4 a[72:75],v240,s[52:55],s65 offen offset:2048 sc1\nM v[12:15],v[4:7],a[212:215],0\nds_read_b64_tr_b16 a[212:213],v226 offset:8192\nds_read_b64_tr_b16 a[214:215],v226 offset:12288\nM v[24:27],v[0:3],a[216:219],0\nv_cvt_pk_bf16_f32 v193,v144,v152\nv_cvt_pk_bf16_f32 v194,v160,v168\n" \
"M v[28:31],v[4:7],a[216:219],0\nds_read_b64_tr_b16 a[216:217],v227 offset:8192\ns_waitcnt lgkmcnt(7)\nM v[40:43],v[0:3],a[220:223],0\nbuffer_load_dwordx4 a[76:79],v240,s[52:55],s65 offen offset:3072 sc1\nM v[44:47],v[4:7],a[220:223],0\nds_read_b64_tr_b16 a[218:219],v227 offset:12288\ns_add_u32 s65,s65,4096\n" \
"M v[56:59],v[0:3],a[224:227],0\nds_read_b64_tr_b16 a[220:221],v226 offset:8704\nds_read_b64_tr_b16 a[222:223],v226 offset:12800\nM v[60:63],v[4:7],a[224:227],0\nds_read_b64_tr_b16 a[224:225],v227 offset:8704\nds_read_b64_tr_b16 a[226:227],v227 offset:12800\nM v[72:75],v[0:3],a[228:231],0\nbuffer_load_dwordx4 a[80:83],v240,s[52:55],s65 offen offset:0 sc1\n" \
"M v[76:79],v[4:7],a[228:231],0\nds_read_b64_tr_b16 a[228:229],v226 offset:9216\nds_read_b64_tr_b16 a[230:231],v226 offset:13312\nM v[88:91],v[0:3],a[232:235],0\nv_cvt_pk_bf16_f32 v195,v176,v184\nv_min_u32 v192,-2,v192\nM v[92:95],v[4:7],a[232:235],0\nv_min_u32 v193,-2,v193\n" \
"s_waitcnt lgkmcnt(7)\nM v[104:107],v[0:3],a[236:239],0\nbuffer_load_dwordx4 a[84:87],v240,s[52:55],s65 offen offset:1024 sc1\nM v[108:111],v[4:7],a[236:239],0\nv_min_u32 v194,-2,v194\nv_min_u32 v195,-2,v195\nM v[120:123],v[0:3],a[240:243],0\nbuffer_store_dwordx4 v[192:195],v253,s[52:55],s57 offen offset:0 sc1\n" \
"M v[124:127],v[4:7],a[240:243],0\nds_read_b64_tr_b16 a[232:233],v227 offset:9216\nds_read_b64_tr_b16 a[234:235],v227 offset:13312\nM v[8:11],v[16:19],a[212:215],v[8:11]\nbuffer_load_dwordx4 a[88:91],v240,s[52:55],s65 offen offset:2048 sc1\nM v[12:15],v[20:23],a[212:215],v[12:15]\nds_read_b64_tr_b16 a[236:237],v226 offset:9728\nds_read_b64_tr_b16 a[238:239],v226 offset:13824\n" \
"s_waitcnt lgkmcnt(4)\nM v[24:27],v[16:19],a[216:219],v[24:27]\nds_read_b64_tr_b16 a[240:241],v227 offset:9728\nds_read_b64_tr_b16 a[242:243],v227 offset:13824\nM v[28:31],v[20:23],a[216:219],v[28:31]\nds_read_b64_tr_b16 a[212:213],v226 offset:16384\nds_read_b64_tr_b16 a[214:215],v226 offset:20480\nM v[40:43],v[16:19],a[220:223],v[40:43]\n" \
"buffer_load_dwordx4 a[92:95],v240,s[52:55],s65 offen offset:3072 sc1\nM v[44:47],v[20:23],a[220:223],v[44:47]\nds_read_b64_tr_b16 a[216:217],v227 offset:16384\nds_read_b64_tr_b16 a[218:219],v227 offset:20480\nM v[56:59],v[16:19],a[224:227],v[56:59]\ns_add_u32 s65,s11,s67\nds_read_b64_tr_b16 a[220:221],v226 offset:16896\nM v[60:63],v[20:23],a[224:227],v[60:63]\n" \
"ds_read_b64_tr_b16 a[222:223],v226 offset:20992\nds_read_b64_tr_b16 a[224:225],v227 offset:16896\nM v[72:75],v[16:19],a[228:231],v[72:75]\nbuffer_load_dwordx4 a[96:99],v240,s[52:55],s65 offen offset:0 sc1\nM v[76:79],v[20:23],a[228:231],v[76:79]\nds_read_b64_tr_b16 a[226:227],v227 offset:20992\ns_waitcnt lgkmcnt(9)\nM v[88:91],v[16:19],a[232:235],v[88:91]\n" \
"ds_read_b64_tr_b16 a[228:229],v226 offset:17408\nds_read_b64_tr_b16 a[230:231],v226 offset:21504\nM v[92:95],v[20:23],a[232:235],v[92:95]\nv_cvt_pk_bf16_f32 v196,v129,v137\nv_cvt_pk_bf16_f32 v197,v145,v153\nM v[104:107],v[16:19],a[236:239],v[104:107]\nbuffer_load_dwordx4 a[100:103],v240,s[52:55],s65 offen offset:1024 sc1\nM v[108:111],v[20:23],a[236:239],v[108:111]\n" \
"v_cvt_pk_bf16_f32 v198,v161,v169\nv_cvt_pk_bf16_f32 v199,v177,v185\ns_waitcnt lgkmcnt(5)\nM v[120:123],v[16:19],a[240:243],v[120:123]\nv_min_u32 v196,-2,v196\nv_min_u32 v197,-2,v197\nv_min_u32 v198,-2,v198\nv_min_u32 v199,-2,v199\n" \
"buffer_store_dwordx4 v[196:199],v253,s[52:55],s57 offen offset:1024 sc1\nM v[124:127],v[20:23],a[240:243],v[124:127]\nds_read_b64_tr_b16 a[232:233],v227 offset:17408\nds_read_b64_tr_b16 a[234:235],v227 offset:21504\nM v[8:11],v[32:35],a[212:215],v[8:11]\nbuffer_load_dwordx4 a[104:107],v240,s[52:55],s65 offen offset:2048 sc1\nM v[12:15],v[36:39],a[212:215],v[12:15]\nds_read_b64_tr_b16 a[236:237],v226 offset:17920\n" \
"ds_read_b64_tr_b16 a[238:239],v226 offset:22016\nM v[24:27],v[32:35],a[216:219],v[24:27]\nds_read_b64_tr_b16 a[240:241],v227 offset:17920\nds_read_b64_tr_b16 a[242:243],v227 offset:22016\nM v[28:31],v[36:39],a[216:219],v[28:31]\nds_read_b64_tr_b16 a[212:213],v226 offset:24576\ns_waitcnt lgkmcnt(7)\nM v[40:43],v[32:35],a[220:223],v[40:43]\n" \
"buffer_load_dwordx4 a[108:111],v240,s[52:55],s65 offen offset:3072 sc1\nM v[44:47],v[36:39],a[220:223],v[44:47]\nds_read_b64_tr_b16 a[214:215],v226 offset:28672\nds_read_b64_tr_b16 a[216:217],v227 offset:24576\nM v[56:59],v[32:35],a[224:227],v[56:59]\nds_read_b64_tr_b16 a[218:219],v227 offset:28672\ns_add_u32 s65,s65,4096\nM v[60:63],v[36:39],a[224:227],v[60:63]\n" \
"ds_read_b64_tr_b16 a[220:221],v226 offset:25088\nds_read_b64_tr_b16 a[222:223],v226 offset:29184\nM v[72:75],v[32:35],a[228:231],v[72:75]\nbuffer_load_dwordx4 a[112:115],v240,s[52:55],s65 offen offset:0 sc1\nM v[76:79],v[36:39],a[228:231],v[76:79]\nds_read_b64_tr_b16 a[224:225],v227 offset:25088\ns_waitcnt lgkmcnt(8)\nM v[88:91],v[32:35],a[232:235],v[88:91]\n" \
"ds_read_b64_tr_b16 a[226:227],v227 offset:29184\nds_read_b64_tr_b16 a[228:229],v226 offset:25600\nM v[92:95],v[36:39],a[232:235],v[92:95]\nds_read_b64_tr_b16 a[230:231],v226 offset:29696\nv_cvt_pk_bf16_f32 v200,v130,v138\nM v[104:107],v[32:35],a[236:239],v[104:107]\nbuffer_load_dwordx4 a[116:119],v240,s[52:55],s65 offen offset:1024 sc1\nM v[108:111],v[36:39],a[236:239],v[108:111]\n" \
"v_cvt_pk_bf16_f32 v201,v146,v154\nv_cvt_pk_bf16_f32 v202,v162,v170\nv_cvt_pk_bf16_f32 v203,v178,v186\ns_waitcnt lgkmcnt(6)\nM v[120:123],v[32:35],a[240:243],v[120:123]\nv_min_u32 v200,-2,v200\nv_min_u32 v201,-2,v201\nv_min_u32 v202,-2,v202\n" \
"v_min_u32 v203,-2,v203\nbuffer_store_dwordx4 v[200:203],v253,s[52:55],s57 offen offset:2048 sc1\nM v[124:127],v[36:39],a[240:243],v[124:127]\nds_read_b64_tr_b16 a[232:233],v227 offset:25600\nds_read_b64_tr_b16 a[234:235],v227 offset:29696\nM v[8:11],v[48:51],a[212:215],v[8:11]\nbuffer_load_dwordx4 a[120:123],v240,s[52:55],s65 offen offset:2048 sc1\nM v[12:15],v[52:55],a[212:215],v[12:15]\n" \
"ds_read_b64_tr_b16 a[236:237],v226 offset:26112\nds_read_b64_tr_b16 a[238:239],v226 offset:30208\nM v[24:27],v[48:51],a[216:219],v[24:27]\nds_read_b64_tr_b16 a[240:241],v227 offset:26112\nds_read_b64_tr_b16 a[242:243],v227 offset:30208\nM v[28:31],v[52:55],a[216:219],v[28:31]\nv_add_u32 v226,0,v242\ns_waitcnt lgkmcnt(6)\n" \
"M v[40:43],v[48:51],a[220:223],v[40:43]\nbuffer_load_dwordx4 a[124:127],v240,s[52:55],s65 offen offset:3072 sc1\nM v[44:47],v[52:55],a[220:223],v[44:47]\nds_read_b64_tr_b16 a[212:213],v226 offset:0\nds_read_b64_tr_b16 a[214:215],v226 offset:4096\nM v[56:59],v[48:51],a[224:227],v[56:59]\nv_xor_b32 v227,32,v226\nds_read_b64_tr_b16 a[216:217],v227 offset:0\n" \
"M v[60:63],v[52:55],a[224:227],v[60:63]\nds_read_b64_tr_b16 a[218:219],v227 offset:4096\ns_add_u32 s65,s14,s67\nM v[72:75],v[48:51],a[228:231],v[72:75]\nbuffer_load_dwordx4 a[128:131],v240,s[52:55],s65 offen offset:0 sc1\nM v[76:79],v[52:55],a[228:231],v[76:79]\nds_read_b64_tr_b16 a[220:221],v226 offset:512\nds_read_b64_tr_b16 a[222:223],v226 offset:4608\n" \
"s_waitcnt lgkmcnt(6)\nM v[88:91],v[48:51],a[232:235],v[88:91]\nds_read_b64_tr_b16 a[224:225],v227 offset:512\nds_read_b64_tr_b16 a[226:227],v227 offset:4608\nM v[92:95],v[52:55],a[232:235],v[92:95]\nv_cvt_pk_bf16_f32 v204,v131,v139\nv_cvt_pk_bf16_f32 v205,v147,v155\nM v[104:107],v[48:51],a[236:239],v[104:107]\n" \
"buffer_load_dwordx4 a[132:135],v240,s[52:55],s65 offen offset:1024 sc1\nM v[108:111],v[52:55],a[236:239],v[108:111]\nv_cvt_pk_bf16_f32 v206,v163,v171\nv_cvt_pk_bf16_f32 v207,v179,v187\nds_read_b64_tr_b16 a[228:229],v226 offset:1024\nds_read_b64_tr_b16 a[230:231],v226 offset:5120\nM v[120:123],v[48:51],a[240:243],v[120:123]\nv_min_u32 v204,-2,v204\n" \
"v_min_u32 v205,-2,v205\nv_min_u32 v206,-2,v206\nv_min_u32 v207,-2,v207\nbuffer_store_dwordx4 v[204:207],v253,s[52:55],s57 offen offset:3072 sc1\nM v[124:127],v[52:55],a[240:243],v[124:127]\nds_read_b64_tr_b16 a[232:233],v227 offset:1024\nds_read_b64_tr_b16 a[234:235],v227 offset:5120\ns_waitcnt vmcnt(18) lgkmcnt(6)\n" \
"M v[8:11],a[64:67],a[212:215],v[8:11]\nbuffer_load_dwordx4 a[136:139],v240,s[52:55],s65 offen offset:2048 sc1\nM v[12:15],a[68:71],a[212:215],v[12:15]\nds_read_b64_tr_b16 a[236:237],v226 offset:1536\nds_read_b64_tr_b16 a[238:239],v226 offset:5632\nM v[24:27],a[64:67],a[216:219],v[24:27]\nds_read_b64_tr_b16 a[240:241],v227 offset:1536\nds_read_b64_tr_b16 a[242:243],v227 offset:5632\n" \
"M v[28:31],a[68:71],a[216:219],v[28:31]\nds_read_b64_tr_b16 a[212:213],v226 offset:8192\nds_read_b64_tr_b16 a[214:215],v226 offset:12288\nM v[40:43],a[64:67],a[220:223],v[40:43]\nbuffer_load_dwordx4 a[140:143],v240,s[52:55],s65 offen offset:3072 sc1\nM v[44:47],a[68:71],a[220:223],v[44:47]\nds_read_b64_tr_b16 a[216:217],v227 offset:8192\ns_waitcnt lgkmcnt(9)\n" \
"M v[56:59],a[64:67],a[224:227],v[56:59]\nds_read_b64_tr_b16 a[218:219],v227 offset:12288\ns_add_u32 s65,s65,4096\nM v[60:63],a[68:71],a[224:227],v[60:63]\nds_read_b64_tr_b16 a[220:221],v226 offset:8704\nds_read_b64_tr_b16 a[222:223],v226 offset:12800\nM v[72:75],a[64:67],a[228:231],v[72:75]\nbuffer_load_dwordx4 a[144:147],v240,s[52:55],s65 offen offset:0 sc1\n" \
"M v[76:79],a[68:71],a[228:231],v[76:79]\nds_read_b64_tr_b16 a[224:225],v227 offset:8704\ns_waitcnt lgkmcnt(8)\nM v[88:91],a[64:67],a[232:235],v[88:91]\nds_read_b64_tr_b16 a[226:227],v227 offset:12800\nv_cvt_pk_bf16_f32 v208,v132,v140\nM v[92:95],a[68:71],a[232:235],v[92:95]\nv_cvt_pk_bf16_f32 v209,v148,v156\n" \
"v_cvt_pk_bf16_f32 v210,v164,v172\nM v[104:107],a[64:67],a[236:239],v[104:107]\nbuffer_load_dwordx4 a[148:151],v240,s[52:55],s65 offen offset:1024 sc1\nM v[108:111],a[68:71],a[236:239],v[108:111]\nv_cvt_pk_bf16_f32 v211,v180,v188\nds_read_b64_tr_b16 a[228:229],v226 offset:9216\nds_read_b64_tr_b16 a[230:231],v226 offset:13312\ns_waitcnt lgkmcnt(6)\n" \
"M v[120:123],a[64:67],a[240:243],v[120:123]\nv_min_u32 v208,-2,v208\nv_min_u32 v209,-2,v209\nv_min_u32 v210,-2,v210\nv_min_u32 v211,-2,v211\nbuffer_store_dwordx4 v[208:211],v253,s[52:55],s38 offen offset:0 sc1\nM v[124:127],a[68:71],a[240:243],v[124:127]\nds_read_b64_tr_b16 a[232:233],v227 offset:9216\n" \
"ds_read_b64_tr_b16 a[234:235],v227 offset:13312\nM v[8:11],a[72:75],a[212:215],v[8:11]\nbuffer_load_dwordx4 a[152:155],v240,s[52:55],s65 offen offset:2048 sc1\nM v[12:15],a[76:79],a[212:215],v[12:15]\nds_read_b64_tr_b16 a[236:237],v226 offset:9728\nds_read_b64_tr_b16 a[238:239],v226 offset:13824\nM v[24:27],a[72:75],a[216:219],v[24:27]\nds_read_b64_tr_b16 a[240:241],v227 offset:9728\n" \
"ds_read_b64_tr_b16 a[242:243],v227 offset:13824\nM v[28:31],a[76:79],a[216:219],v[28:31]\nds_read_b64_tr_b16 a[212:213],v226 offset:16384\ns_waitcnt lgkmcnt(7)\nM v[40:43],a[72:75],a[220:223],v[40:43]\nbuffer_load_dwordx4 a[156:159],v240,s[52:55],s65 offen offset:3072 sc1\nM v[44:47],a[76:79],a[220:223],v[44:47]\nds_read_b64_tr_b16 a[214:215],v226 offset:20480\n" \
"ds_read_b64_tr_b16 a[216:217],v227 offset:16384\nM v[56:59],a[72:75],a[224:227],v[56:59]\nds_read_b64_tr_b16 a[218:219],v227 offset:20480\nds_read_b64_tr_b16 a[220:221],v226 offset:16896\nM v[60:63],a[76:79],a[224:227],v[60:63]\nds_read_b64_tr_b16 a[222:223],v226 offset:20992\nds_read_b64_tr_b16 a[224:225],v227 offset:16896\nM v[72:75],a[72:75],a[228:231],v[72:75]\n" \
"ds_read_b64_tr_b16 a[226:227],v227 offset:20992\nv_cvt_pk_bf16_f32 v212,v133,v141\nM v[76:79],a[76:79],a[228:231],v[76:79]\nv_cvt_pk_bf16_f32 v213,v149,v157\ns_waitcnt lgkmcnt(9)\nM v[88:91],a[72:75],a[232:235],v[88:91]\nv_cvt_pk_bf16_f32 v214,v165,v173\nv_cvt_pk_bf16_f32 v215,v181,v189\n" \
"M v[92:95],a[76:79],a[232:235],v[92:95]\nds_read_b64_tr_b16 a[228:229],v226 offset:17408\nds_read_b64_tr_b16 a[230:231],v226 offset:21504\nM v[104:107],a[72:75],a[236:239],v[104:107]\nv_min_u32 v212,-2,v212\nv_min_u32 v213,-2,v213\nM v[108:111],a[76:79],a[236:239],v[108:111]\nv_min_u32 v214,-2,v214\n" \
"s_waitcnt lgkmcnt(6)\nM v[120:123],a[72:75],a[240:243],v[120:123]\nv_min_u32 v215,-2,v215\nbuffer_store_dwordx4 v[212:215],v253,s[52:55],s38 offen offset:1024 sc1\nM v[124:127],a[76:79],a[240:243],v[124:127]\nds_read_b64_tr_b16 a[232:233],v227 offset:17408\nds_read_b64_tr_b16 a[234:235],v227 offset:21504\ns_waitcnt vmcnt(24)\n" \
"M v[8:11],a[80:83],a[212:215],v[8:11]\nds_read_b64_tr_b16 a[236:237],v226 offset:17920\nds_read_b64_tr_b16 a[238:239],v226 offset:22016\nM v[12:15],a[84:87],a[212:215],v[12:15]\nds_read_b64_tr_b16 a[240:241],v227 offset:17920\nds_read_b64_tr_b16 a[242:243],v227 offset:22016\nM v[24:27],a[80:83],a[216:219],v[24:27]\nds_read_b64_tr_b16 a[212:213],v226 offset:24576\n" \
"ds_read_b64_tr_b16 a[214:215],v226 offset:28672\nM v[28:31],a[84:87],a[216:219],v[28:31]\nds_read_b64_tr_b16 a[216:217],v227 offset:24576\ns_waitcnt lgkmcnt(9)\nM v[40:43],a[80:83],a[220:223],v[40:43]\nds_read_b64_tr_b16 a[218:219],v227 offset:28672\nv_cvt_pk_bf16_f32 v216,v134,v142\nM v[44:47],a[84:87],a[220:223],v[44:47]\n" \
"ds_read_b64_tr_b16 a[220:221],v226 offset:25088\nds_read_b64_tr_b16 a[222:223],v226 offset:29184\nM v[56:59],a[80:83],a[224:227],v[56:59]\nv_cvt_pk_bf16_f32 v217,v150,v158\nv_cvt_pk_bf16_f32 v218,v166,v174\nM v[60:63],a[84:87],a[224:227],v[60:63]\nds_read_b64_tr_b16 a[224:225],v227 offset:25088\nds_read_b64_tr_b16 a[226:227],v227 offset:29184\n" \
"M v[72:75],a[80:83],a[228:231],v[72:75]\nv_cvt_pk_bf16_f32 v219,v182,v190\nv_min_u32 v216,-2,v216\nM v[76:79],a[84:87],a[228:231],v[76:79]\nds_read_b64_tr_b16 a[228:229],v226 offset:25600\ns_waitcnt lgkmcnt(8)\nM v[88:91],a[80:83],a[232:235],v[88:91]\nds_read_b64_tr_b16 a[230:231],v226 offset:29696\n" \
"v_min_u32 v217,-2,v217\nM v[92:95],a[84:87],a[232:235],v[92:95]\nv_min_u32 v218,-2,v218\nv_min_u32 v219,-2,v219\nM v[104:107],a[80:83],a[236:239],v[104:107]\nds_read_b64_tr_b16 a[232:233],v227 offset:25600\nds_read_b64_tr_b16 a[234:235],v227 offset:29696\nM v[108:111],a[84:87],a[236:239],v[108:111]\n" \
"ds_read_b64_tr_b16 a[236:237],v226 offset:26112\nds_read_b64_tr_b16 a[238:239],v226 offset:30208\nM v[120:123],a[80:83],a[240:243],v[120:123]\nbuffer_store_dwordx4 v[216:219],v253,s[52:55],s38 offen offset:2048 sc1\nM v[124:127],a[84:87],a[240:243],v[124:127]\nds_read_b64_tr_b16 a[240:241],v227 offset:26112\ns_waitcnt vmcnt(20) lgkmcnt(7)\nM v[8:11],a[88:91],a[212:215],v[8:11]\n" \
"ds_read_b64_tr_b16 a[242:243],v227 offset:30208\nv_cvt_pk_bf16_f32 v220,v135,v143\nM v[12:15],a[92:95],a[212:215],v[12:15]\nds_read_b64_tr_b16 a[212:213],v226 offset:32768\nds_read_b64_tr_b16 a[214:215],v226 offset:36864\nM v[24:27],a[88:91],a[216:219],v[24:27]\nv_cvt_pk_bf16_f32 v221,v151,v159\nv_cvt_pk_bf16_f32 v222,v167,v175\n" \
"M v[28:31],a[92:95],a[216:219],v[28:31]\nds_read_b64_tr_b16 a[216:217],v227 offset:32768\nds_read_b64_tr_b16 a[218:219],v227 offset:36864\nM v[40:43],a[88:91],a[220:223],v[40:43]\nv_cvt_pk_bf16_f32 v223,v183,v191\nv_min_u32 v220,-2,v220\nM v[44:47],a[92:95],a[220:223],v[44:47]\nds_read_b64_tr_b16 a[220:221],v226 offset:33280\n" \
"ds_read_b64_tr_b16 a[222:223],v226 offset:37376\nM v[56:59],a[88:91],a[224:227],v[56:59]\nv_min_u32 v221,-2,v221\nv_min_u32 v222,-2,v222\nM v[60:63],a[92:95],a[224:227],v[60:63]\nds_read_b64_tr_b16 a[224:225],v227 offset:33280\ns_waitcnt lgkmcnt(7)\nM v[72:75],a[88:91],a[228:231],v[72:75]\n" \
"ds_read_b64_tr_b16 a[226:227],v227 offset:37376\nv_min_u32 v223,-2,v223\nM v[76:79],a[92:95],a[228:231],v[76:79]\nds_read_b64_tr_b16 a[228:229],v226 offset:33792\nds_read_b64_tr_b16 a[230:231],v226 offset:37888\nM v[88:91],a[88:91],a[232:235],v[88:91]\nv_cmp_eq_u32 vcc,v234,v235\nv_and_b32 v234,1,v249\n" \
"M v[92:95],a[92:95],a[232:235],v[92:95]\nds_read_b64_tr_b16 a[232:233],v227 offset:33792\nds_read_b64_tr_b16 a[234:235],v227 offset:37888\nM v[104:107],a[88:91],a[236:239],v[104:107]\nv_lshlrev_b32 v234,4,v234\nv_mov_b32 v235,0x3f80\nM v[108:111],a[92:95],a[236:239],v[108:111]\nds_read_b64_tr_b16 a[236:237],v226 offset:34304\n" \
"ds_read_b64_tr_b16 a[238:239],v226 offset:38400\nM v[120:123],a[88:91],a[240:243],v[120:123]\nbuffer_store_dwordx4 v[220:223],v253,s[52:55],s38 offen offset:3072 sc1\nM v[124:127],a[92:95],a[240:243],v[124:127]\nds_read_b64_tr_b16 a[240:241],v227 offset:34304\ns_waitcnt lgkmcnt(7)\nM v[8:11],a[96:99],a[212:215],v[8:11]\nds_read_b64_tr_b16 a[242:243],v227 offset:38400\n" \
"v_lshlrev_b32 v234,v234,v235\nM v[12:15],a[100:103],a[212:215],v[12:15]\nds_read_b64_tr_b16 a[212:213],v226 offset:40960\nds_read_b64_tr_b16 a[214:215],v226 offset:45056\nM v[24:27],a[96:99],a[216:219],v[24:27]\nv_cndmask_b32 v234,0,v234,vcc\nv_and_b32 v235,2,v249\nM v[28:31],a[100:103],a[216:219],v[28:31]\n" \
"ds_read_b64_tr_b16 a[216:217],v227 offset:40960\nds_read_b64_tr_b16 a[218:219],v227 offset:45056\nM v[40:43],a[96:99],a[220:223],v[40:43]\nv_cmp_eq_u32 vcc,0,v235\ns_add_u32 s38,s78,16384\nM v[44:47],a[100:103],a[220:223],v[44:47]\nds_read_b64_tr_b16 a[220:221],v226 offset:41472\nds_read_b64_tr_b16 a[222:223],v226 offset:45568\n" \
"M v[56:59],a[96:99],a[224:227],v[56:59]\nbuffer_load_dwordx4 v[0:3],v238,s[52:55],0 offen offset:0 sc1\nM v[60:63],a[100:103],a[224:227],v[60:63]\nds_read_b64_tr_b16 a[224:225],v227 offset:41472\ns_waitcnt lgkmcnt(7)\nM v[72:75],a[96:99],a[228:231],v[72:75]\nds_read_b64_tr_b16 a[226:227],v227 offset:45568\nv_cndmask_b32 v235,0,v234,vcc\n" \
"M v[76:79],a[100:103],a[228:231],v[76:79]\nds_read_b64_tr_b16 a[228:229],v226 offset:41984\nds_read_b64_tr_b16 a[230:231],v226 offset:46080\nM v[88:91],a[96:99],a[232:235],v[88:91]\nv_xor_b32 v234,v234,v235\nv_lshlrev_b32 v64,16,v64\nM v[92:95],a[100:103],a[232:235],v[92:95]\nds_read_b64_tr_b16 a[232:233],v227 offset:41984\n" \
"ds_read_b64_tr_b16 a[234:235],v227 offset:46080\nM v[104:107],a[96:99],a[236:239],v[104:107]\nbuffer_load_dwordx4 v[4:7],v238,s[52:55],0 offen offset:16 sc1\nM v[108:111],a[100:103],a[236:239],v[108:111]\nds_read_b64_tr_b16 a[236:237],v226 offset:42496\nds_read_b64_tr_b16 a[238:239],v226 offset:46592\nM v[120:123],a[96:99],a[240:243],v[120:123]\nv_lshlrev_b32 v65,16,v65\n" \
"v_lshlrev_b32 v66,16,v66\nM v[124:127],a[100:103],a[240:243],v[124:127]\nds_read_b64_tr_b16 a[240:241],v227 offset:42496\ns_waitcnt vmcnt(18) lgkmcnt(7)\nM v[8:11],a[104:107],a[212:215],v[8:11]\nds_read_b64_tr_b16 a[242:243],v227 offset:46592\nv_lshlrev_b32 v67,16,v67\nM v[12:15],a[108:111],a[212:215],v[12:15]\n" \
"ds_read_b64_tr_b16 a[212:213],v226 offset:49152\nds_read_b64_tr_b16 a[214:215],v226 offset:53248\nM v[24:27],a[104:107],a[216:219],v[24:27]\nbuffer_load_dwordx4 v[16:19],v238,s[52:55],0 offen offset:2048 sc1\nM v[28:31],a[108:111],a[216:219],v[28:31]\nds_read_b64_tr_b16 a[216:217],v227 offset:49152\nds_read_b64_tr_b16 a[218:219],v227 offset:53248\nM v[40:43],a[104:107],a[220:223],v[40:43]\n" \
"v_lshlrev_b32 v68,16,v68\nv_lshlrev_b32 v69,16,v69\nM v[44:47],a[108:111],a[220:223],v[44:47]\nds_read_b64_tr_b16 a[220:221],v226 offset:49664\nds_read_b64_tr_b16 a[222:223],v226 offset:53760\nM v[56:59],a[104:107],a[224:227],v[56:59]\nv_lshlrev_b32 v70,16,v70\nv_lshlrev_b32 v71,16,v71\n" \
"M v[60:63],a[108:111],a[224:227],v[60:63]\nds_read_b64_tr_b16 a[224:225],v227 offset:49664\ns_waitcnt lgkmcnt(7)\nM v[72:75],a[104:107],a[228:231],v[72:75]\nbuffer_load_dwordx4 v[20:23],v238,s[52:55],0 offen offset:2064 sc1\nM v[76:79],a[108:111],a[228:231],v[76:79]\nds_read_b64_tr_b16 a[226:227],v227 offset:53760\nds_read_b64_tr_b16 a[228:229],v226 offset:50176\n" \
"M v[88:91],a[104:107],a[232:235],v[88:91]\nds_read_b64_tr_b16 a[230:231],v226 offset:54272\nv_lshlrev_b32 v80,16,v80\nM v[92:95],a[108:111],a[232:235],v[92:95]\nds_read_b64_tr_b16 a[232:233],v227 offset:50176\nds_read_b64_tr_b16 a[234:235],v227 offset:54272\nM v[104:107],a[104:107],a[236:239],v[104:107]\nv_lshlrev_b32 v81,16,v81\n" \
"v_lshlrev_b32 v82,16,v82\nM v[108:111],a[108:111],a[236:239],v[108:111]\nds_read_b64_tr_b16 a[236:237],v226 offset:50688\nds_read_b64_tr_b16 a[238:239],v226 offset:54784\nM v[120:123],a[104:107],a[240:243],v[120:123]\nbuffer_load_dwordx4 v[32:35],v238,s[52:55],s97 offen offset:0 sc1\nM v[124:127],a[108:111],a[240:243],v[124:127]\nds_read_b64_tr_b16 a[240:241],v227 offset:50688\n" \
"s_waitcnt lgkmcnt(8)\nM v[8:11],a[112:115],a[212:215],v[8:11]\nds_read_b64_tr_b16 a[242:243],v227 offset:54784\nv_lshlrev_b32 v83,16,v83\nM v[12:15],a[116:119],a[212:215],v[12:15]\nds_read_b64_tr_b16 a[212:213],v226 offset:57344\nds_read_b64_tr_b16 a[214:215],v226 offset:61440\nM v[24:27],a[112:115],a[216:219],v[24:27]\n" \
"v_lshlrev_b32 v84,16,v84\nv_lshlrev_b32 v85,16,v85\nM v[28:31],a[116:119],a[216:219],v[28:31]\nds_read_b64_tr_b16 a[216:217],v227 offset:57344\nds_read_b64_tr_b16 a[218:219],v227 offset:61440\nM v[40:43],a[112:115],a[220:223],v[40:43]\nbuffer_load_dwordx4 v[36:39],v238,s[52:55],s97 offen offset:16 sc1\nM v[44:47],a[116:119],a[220:223],v[44:47]\n" \
"ds_read_b64_tr_b16 a[220:221],v226 offset:57856\ns_waitcnt lgkmcnt(7)\nM v[56:59],a[112:115],a[224:227],v[56:59]\nds_read_b64_tr_b16 a[222:223],v226 offset:61952\nv_lshlrev_b32 v86,16,v86\nM v[60:63],a[116:119],a[224:227],v[60:63]\nds_read_b64_tr_b16 a[224:225],v227 offset:57856\nds_read_b64_tr_b16 a[226:227],v227 offset:61952\n" \
"M v[72:75],a[112:115],a[228:231],v[72:75]\nv_lshlrev_b32 v87,16,v87\nv_lshlrev_b32 v96,16,v96\nM v[76:79],a[116:119],a[228:231],v[76:79]\nds_read_b64_tr_b16 a[228:229],v226 offset:58368\nds_read_b64_tr_b16 a[230:231],v226 offset:62464\nM v[88:91],a[112:115],a[232:235],v[88:91]\nbuffer_load_dwordx4 v[48:51],v238,s[52:55],s97 offen offset:2048 sc1\n" \
"M v[92:95],a[116:119],a[232:235],v[92:95]\nds_read_b64_tr_b16 a[232:233],v227 offset:58368\nds_read_b64_tr_b16 a[234:235],v227 offset:62464\nM v[104:107],a[112:115],a[236:239],v[104:107]\nv_lshlrev_b32 v97,16,v97\nv_lshlrev_b32 v98,16,v98\nM v[108:111],a[116:119],a[236:239],v[108:111]\nds_read_b64_tr_b16 a[236:237],v226 offset:58880\n" \
"s_waitcnt lgkmcnt(7)\nM v[120:123],a[112:115],a[240:243],v[120:123]\nds_read_b64_tr_b16 a[238:239],v226 offset:62976\nv_add_u32 v226,65536,v242\nM v[124:127],a[116:119],a[240:243],v[124:127]\nds_read_b64_tr_b16 a[240:241],v227 offset:58880\ns_waitcnt vmcnt(18)\nM v[8:11],a[120:123],a[212:215],v[8:11]\n" \
"buffer_load_dwordx4 v[52:55],v238,s[52:55],s97 offen offset:2064 sc1\nM v[12:15],a[124:127],a[212:215],v[12:15]\nds_read_b64_tr_b16 a[242:243],v227 offset:62976\nds_read_b64_tr_b16 a[212:213],v226 offset:0\nM v[24:27],a[120:123],a[216:219],v[24:27]\nds_read_b64_tr_b16 a[214:215],v226 offset:4096\nv_xor_b32 v227,32,v226\nM v[28:31],a[124:127],a[216:219],v[28:31]\n" \
"ds_read_b64_tr_b16 a[216:217],v227 offset:0\nds_read_b64_tr_b16 a[218:219],v227 offset:4096\nM v[40:43],a[120:123],a[220:223],v[40:43]\nv_lshlrev_b32 v99,16,v99\nv_lshlrev_b32 v100,16,v100\nM v[44:47],a[124:127],a[220:223],v[44:47]\nds_read_b64_tr_b16 a[220:221],v226 offset:512\ns_waitcnt lgkmcnt(7)\n" \
"M v[56:59],a[120:123],a[224:227],v[56:59]\nds_read_b64_tr_b16 a[222:223],v226 offset:4608\nv_lshlrev_b32 v101,16,v101\nM v[60:63],a[124:127],a[224:227],v[60:63]\nds_read_b64_tr_b16 a[224:225],v227 offset:512\nds_read_b64_tr_b16 a[226:227],v227 offset:4608\nM v[72:75],a[120:123],a[228:231],v[72:75]\nv_lshlrev_b32 v102,16,v102\n" \
"v_lshlrev_b32 v103,16,v103\nM v[76:79],a[124:127],a[228:231],v[76:79]\nds_read_b64_tr_b16 a[228:229],v226 offset:1024\nds_read_b64_tr_b16 a[230:231],v226 offset:5120\nM v[88:91],a[120:123],a[232:235],v[88:91]\nv_accvgpr_write_b32 a192,v235\nv_accvgpr_write_b32 a193,v234\nM v[92:95],a[124:127],a[232:235],v[92:95]\n" \
"ds_read_b64_tr_b16 a[232:233],v227 offset:1024\nds_read_b64_tr_b16 a[234:235],v227 offset:5120\nM v[104:107],a[120:123],a[236:239],v[104:107]\nv_accvgpr_write_b32 a198,v235\nv_accvgpr_write_b32 a199,v234\nM v[108:111],a[124:127],a[236:239],v[108:111]\nds_read_b64_tr_b16 a[236:237],v226 offset:1536\ns_waitcnt lgkmcnt(7)\n" \
"M v[120:123],a[120:123],a[240:243],v[120:123]\nds_read_b64_tr_b16 a[238:239],v226 offset:5632\ns_add_u32 s95,s95,54525952\nM v[124:127],a[124:127],a[240:243],v[124:127]\nds_read_b64_tr_b16 a[240:241],v227 offset:1536\nds_read_b64_tr_b16 a[242:243],v227 offset:5632\nM v[8:11],a[128:131],a[212:215],v[8:11]\ns_add_u32 s74,s74,54525952\n" \
"M v[12:15],a[132:135],a[212:215],v[12:15]\nds_read_b64_tr_b16 a[212:213],v226 offset:8192\nds_read_b64_tr_b16 a[214:215],v226 offset:12288\nM v[24:27],a[128:131],a[216:219],v[24:27]\nM v[28:31],a[132:135],a[216:219],v[28:31]\nds_read_b64_tr_b16 a[216:217],v227 offset:8192\nds_read_b64_tr_b16 a[218:219],v227 offset:12288\nM v[40:43],a[128:131],a[220:223],v[40:43]\n" \
"M v[44:47],a[132:135],a[220:223],v[44:47]\nds_read_b64_tr_b16 a[220:221],v226 offset:8704\ns_waitcnt lgkmcnt(9)\nM v[56:59],a[128:131],a[224:227],v[56:59]\nds_read_b64_tr_b16 a[222:223],v226 offset:12800\nM v[60:63],a[132:135],a[224:227],v[60:63]\nds_read_b64_tr_b16 a[224:225],v227 offset:8704\nds_read_b64_tr_b16 a[226:227],v227 offset:12800\n" \
"M v[72:75],a[128:131],a[228:231],v[72:75]\nM v[76:79],a[132:135],a[228:231],v[76:79]\nds_read_b64_tr_b16 a[228:229],v226 offset:9216\nds_read_b64_tr_b16 a[230:231],v226 offset:13312\nM v[88:91],a[128:131],a[232:235],v[88:91]\nM v[92:95],a[132:135],a[232:235],v[92:95]\nds_read_b64_tr_b16 a[232:233],v227 offset:9216\ns_waitcnt lgkmcnt(9)\n" \
"M v[104:107],a[128:131],a[236:239],v[104:107]\nds_read_b64_tr_b16 a[234:235],v227 offset:13312\nM v[108:111],a[132:135],a[236:239],v[108:111]\nds_read_b64_tr_b16 a[236:237],v226 offset:9728\nds_read_b64_tr_b16 a[238:239],v226 offset:13824\nM v[120:123],a[128:131],a[240:243],v[120:123]\nM v[124:127],a[132:135],a[240:243],v[124:127]\nds_read_b64_tr_b16 a[240:241],v227 offset:9728\n" \
"s_waitcnt vmcnt(14)\nM v[8:11],a[136:139],a[212:215],v[8:11]\nds_read_b64_tr_b16 a[242:243],v227 offset:13824\nM v[12:15],a[140:143],a[212:215],v[12:15]\nds_read_b64_tr_b16 a[212:213],v226 offset:32768\ns_waitcnt lgkmcnt(9)\nM v[24:27],a[136:139],a[216:219],v[24:27]\nds_read_b64_tr_b16 a[214:215],v226 offset:36864\n" \
"M v[28:31],a[140:143],a[216:219],v[28:31]\nds_read_b64_tr_b16 a[216:217],v227 offset:32768\nds_read_b64_tr_b16 a[218:219],v227 offset:36864\nM v[40:43],a[136:139],a[220:223],v[40:43]\nM v[44:47],a[140:143],a[220:223],v[44:47]\nds_read_b64_tr_b16 a[220:221],v226 offset:33280\nds_read_b64_tr_b16 a[222:223],v226 offset:37376\nM v[56:59],a[136:139],a[224:227],v[56:59]\n" \
"M v[60:63],a[140:143],a[224:227],v[60:63]\nds_read_b64_tr_b16 a[224:225],v227 offset:33280\ns_waitcnt lgkmcnt(9)\nM v[72:75],a[136:139],a[228:231],v[72:75]\nds_read_b64_tr_b16 a[226:227],v227 offset:37376\nM v[76:79],a[140:143],a[228:231],v[76:79]\nds_read_b64_tr_b16 a[228:229],v226 offset:33792\nds_read_b64_tr_b16 a[230:231],v226 offset:37888\n" \
"M v[88:91],a[136:139],a[232:235],v[88:91]\nM v[92:95],a[140:143],a[232:235],v[92:95]\nds_read_b64_tr_b16 a[232:233],v227 offset:33792\nds_read_b64_tr_b16 a[234:235],v227 offset:37888\nM v[104:107],a[136:139],a[236:239],v[104:107]\nM v[108:111],a[140:143],a[236:239],v[108:111]\nds_read_b64_tr_b16 a[236:237],v226 offset:34304\ns_waitcnt lgkmcnt(9)\n" \
"M v[120:123],a[136:139],a[240:243],v[120:123]\nds_read_b64_tr_b16 a[238:239],v226 offset:38400\nM v[124:127],a[140:143],a[240:243],v[124:127]\nds_read_b64_tr_b16 a[240:241],v227 offset:34304\nds_read_b64_tr_b16 a[242:243],v227 offset:38400\nM v[8:11],a[144:147],a[212:215],v[8:11]\nM v[12:15],a[148:151],a[212:215],v[12:15]\nds_read_b64_tr_b16 a[212:213],v226 offset:40960\n" \
"ds_read_b64_tr_b16 a[214:215],v226 offset:45056\nM v[24:27],a[144:147],a[216:219],v[24:27]\nM v[28:31],a[148:151],a[216:219],v[28:31]\nds_read_b64_tr_b16 a[216:217],v227 offset:40960\ns_waitcnt lgkmcnt(9)\nM v[40:43],a[144:147],a[220:223],v[40:43]\nds_read_b64_tr_b16 a[218:219],v227 offset:45056\nM v[44:47],a[148:151],a[220:223],v[44:47]\n" \
"ds_read_b64_tr_b16 a[220:221],v226 offset:41472\nds_read_b64_tr_b16 a[222:223],v226 offset:45568\nM v[56:59],a[144:147],a[224:227],v[56:59]\nM v[60:63],a[148:151],a[224:227],v[60:63]\nds_read_b64_tr_b16 a[224:225],v227 offset:41472\nds_read_b64_tr_b16 a[226:227],v227 offset:45568\nM v[72:75],a[144:147],a[228:231],v[72:75]\nM v[76:79],a[148:151],a[228:231],v[76:79]\n" \
"ds_read_b64_tr_b16 a[228:229],v226 offset:41984\ns_waitcnt lgkmcnt(9)\nM v[88:91],a[144:147],a[232:235],v[88:91]\nds_read_b64_tr_b16 a[230:231],v226 offset:46080\nM v[92:95],a[148:151],a[232:235],v[92:95]\nds_read_b64_tr_b16 a[232:233],v227 offset:41984\nds_read_b64_tr_b16 a[234:235],v227 offset:46080\nM v[104:107],a[144:147],a[236:239],v[104:107]\n" \
"M v[108:111],a[148:151],a[236:239],v[108:111]\nds_read_b64_tr_b16 a[236:237],v226 offset:42496\nds_read_b64_tr_b16 a[238:239],v226 offset:46592\nM v[120:123],a[144:147],a[240:243],v[120:123]\nM v[124:127],a[148:151],a[240:243],v[124:127]\nds_read_b64_tr_b16 a[240:241],v227 offset:42496\ns_waitcnt vmcnt(11) lgkmcnt(9)\nM v[8:11],a[152:155],a[212:215],v[8:11]\n" \
"ds_read_b64_tr_b16 a[242:243],v227 offset:46592\nM v[12:15],a[156:159],a[212:215],v[12:15]\nM v[24:27],a[152:155],a[216:219],v[24:27]\nM v[28:31],a[156:159],a[216:219],v[28:31]\nM v[40:43],a[152:155],a[220:223],v[40:43]\nM v[44:47],a[156:159],a[220:223],v[44:47]\ns_waitcnt lgkmcnt(6)\nM v[56:59],a[152:155],a[224:227],v[56:59]\n" \
"M v[60:63],a[156:159],a[224:227],v[60:63]\nM v[72:75],a[152:155],a[228:231],v[72:75]\nM v[76:79],a[156:159],a[228:231],v[76:79]\nv_cvt_pk_bf16_f32 v192,v8,v24\ns_waitcnt lgkmcnt(2)\nM v[88:91],a[152:155],a[232:235],v[88:91]\nv_cvt_pk_bf16_f32 v196,v9,v25\nv_cvt_pk_bf16_f32 v200,v10,v26\n" \
"M v[92:95],a[156:159],a[232:235],v[92:95]\nv_cvt_pk_bf16_f32 v193,v40,v56\nv_cvt_pk_bf16_f32 v197,v41,v57\nM v[104:107],a[152:155],a[236:239],v[104:107]\nv_cvt_pk_bf16_f32 v201,v42,v58\nv_cvt_pk_bf16_f32 v204,v11,v27\nM v[108:111],a[156:159],a[236:239],v[108:111]\nv_cvt_pk_bf16_f32 v194,v72,v88\n" \
"s_waitcnt lgkmcnt(0)\nM v[120:123],a[152:155],a[240:243],v[120:123]\nv_cvt_pk_bf16_f32 v198,v73,v89\nv_cvt_pk_bf16_f32 v202,v74,v90\nv_cvt_pk_bf16_f32 v205,v43,v59\nv_cvt_pk_bf16_f32 v206,v75,v91\nv_cvt_pk_bf16_f32 v208,v12,v28\nv_cvt_pk_bf16_f32 v209,v44,v60\n" \
"v_cvt_pk_bf16_f32 v210,v76,v92\nv_cvt_pk_bf16_f32 v212,v13,v29\nv_cvt_pk_bf16_f32 v195,v104,v120\nv_cvt_pk_bf16_f32 v199,v105,v121\nv_cvt_pk_bf16_f32 v203,v106,v122\nv_cvt_pk_bf16_f32 v207,v107,v123\nv_cvt_pk_bf16_f32 v213,v45,v61\nv_cvt_pk_bf16_f32 v214,v77,v93\n" \
"v_cvt_pk_bf16_f32 v216,v14,v30\nv_cvt_pk_bf16_f32 v217,v46,v62\nv_cvt_pk_bf16_f32 v218,v78,v94\nv_cvt_pk_bf16_f32 v220,v15,v31\nv_cvt_pk_bf16_f32 v221,v47,v63\nv_cvt_pk_bf16_f32 v222,v79,v95\nM v[124:127],a[156:159],a[240:243],v[124:127]\nv_min_u32 v192,-2,v192\n" \
"v_min_u32 v193,-2,v193\nv_min_u32 v194,-2,v194\nv_min_u32 v195,-2,v195\nv_min_u32 v196,-2,v196\nv_min_u32 v197,-2,v197\nv_min_u32 v198,-2,v198\nv_min_u32 v199,-2,v199\nv_cvt_pk_bf16_f32 v211,v108,v124\n" \
"v_cvt_pk_bf16_f32 v215,v109,v125\nv_cvt_pk_bf16_f32 v219,v110,v126\nv_cvt_pk_bf16_f32 v223,v111,v127\nv_min_u32 v200,-2,v200\nv_min_u32 v201,-2,v201\nv_min_u32 v202,-2,v202\nv_min_u32 v203,-2,v203\nv_min_u32 v204,-2,v204\n" \
"v_min_u32 v205,-2,v205\nv_min_u32 v206,-2,v206\nv_min_u32 v207,-2,v207\nv_min_u32 v208,-2,v208\nv_min_u32 v209,-2,v209\nv_min_u32 v210,-2,v210\nv_min_u32 v211,-2,v211\nv_min_u32 v212,-2,v212\n" \
"v_min_u32 v213,-2,v213\nv_min_u32 v214,-2,v214\nv_min_u32 v215,-2,v215\nv_min_u32 v216,-2,v216\nv_min_u32 v217,-2,v217\nv_min_u32 v218,-2,v218\nv_min_u32 v219,-2,v219\nv_min_u32 v220,-2,v220\n" \
"v_min_u32 v221,-2,v221\nv_min_u32 v222,-2,v222\nv_min_u32 v223,-2,v223\nbuffer_store_dwordx4 v[192:195],v253,s[52:55],s78 offen offset:0 sc1\nbuffer_store_dwordx4 v[196:199],v253,s[52:55],s78 offen offset:1024 sc1\nbuffer_store_dwordx4 v[200:203],v253,s[52:55],s78 offen offset:2048 sc1\nbuffer_store_dwordx4 v[204:207],v253,s[52:55],s78 offen offset:3072 sc1\nbuffer_store_dwordx4 v[208:211],v253,s[52:55],s38 offen offset:0 sc1\n" \
"buffer_store_dwordx4 v[212:215],v253,s[52:55],s38 offen offset:1024 sc1\nbuffer_store_dwordx4 v[216:219],v253,s[52:55],s38 offen offset:2048 sc1\nbuffer_store_dwordx4 v[220:223],v253,s[52:55],s38 offen offset:3072 sc1\ns_branch .Ltail\n.Lod1x:\nds_read_b128 a[64:67],v250 offset:0\nds_read_b128 a[68:71],v250 offset:1024\nds_read_b128 a[72:75],v250 offset:2048\n" \
"s_waitcnt lgkmcnt(2)\nM v[0:3],a[64:67],v[192:195],v[0:3]\ns_lshl4_add_u32 s69,s10,s68\ns_add_u32 m0,s64,0\nM v[4:7],a[64:67],v[196:199],v[4:7]\nds_read_b128 a[76:79],v250 offset:3072\nv_cvt_pk_bf16_f32 v216,v168,v169\nM v[64:67],a[64:67],v[200:203],v[64:67]\n" \
"buffer_store_dwordx4 a[248:251],v253,s[52:55],s57 offen offset:0 sc1\nM v[68:71],a[64:67],v[204:207],v[68:71]\nds_read_b128 a[80:83],v250 offset:4096\ns_waitcnt lgkmcnt(3)\nM v[8:11],a[68:71],v[192:195],v[8:11]\nds_read_b128 a[84:87],v250 offset:5120\nds_read_b128 a[88:91],v250 offset:6144\nM v[12:15],a[68:71],v[196:199],v[12:15]\n" \
"buffer_store_dwordx4 a[248:251],v253,s[52:55],s57 offen offset:1024 sc1\nM v[72:75],a[68:71],v[200:203],v[72:75]\nbuffer_load_dwordx4 v243,s[40:43],s69 offen offset:0 lds\nM v[76:79],a[68:71],v[204:207],v[76:79]\nds_read_b128 a[92:95],v250 offset:7168\ns_waitcnt lgkmcnt(5)\nM v[16:19],a[72:75],v[192:195],v[16:19]\nbuffer_store_dwordx4 a[248:251],v253,s[52:55],s57 offen offset:2048 sc1\n" \
"M v[20:23],a[72:75],v[196:199],v[20:23]\nds_read_b128 a[96:99],v250 offset:8192\nds_read_b128 a[100:103],v250 offset:9216\nM v[80:83],a[72:75],v[200:203],v[80:83]\nbuffer_load_dwordx4 v244,s[40:43],s69 offen offset:1024 lds\nM v[84:87],a[72:75],v[204:207],v[84:87]\nbuffer_store_dwordx4 a[248:251],v253,s[52:55],s57 offen offset:3072 sc1\ns_waitcnt lgkmcnt(3)\n" \
"M v[24:27],a[76:79],v[192:195],v[24:27]\nds_read_b128 a[104:107],v250 offset:10240\nds_read_b128 a[108:111],v250 offset:11264\nM v[28:31],a[76:79],v[196:199],v[28:31]\nv_exp_f32 v184,v184\nM v[88:91],a[76:79],v[200:203],v[88:91]\nbuffer_load_dwordx4 v245,s[40:43],s69 offen offset:2048 lds\nM v[92:95],a[76:79],v[204:207],v[92:95]\n" \
"v_exp_f32 v185,v185\nM v[32:35],a[80:83],v[192:195],v[32:35]\nv_exp_f32 v186,v186\nM v[36:39],a[80:83],v[196:199],v[36:39]\nv_exp_f32 v187,v187\nM v[96:99],a[80:83],v[200:203],v[96:99]\nbuffer_load_dwordx4 v246,s[40:43],s69 offen offset:3072 lds\nM v[100:103],a[80:83],v[204:207],v[100:103]\n" \
"s_lshl4_add_u32 s69,s10,s68\ns_add_u32 s69,s69,0x10000\nM v[40:43],a[84:87],v[192:195],v[40:43]\ns_add_u32 m0,s64,16384\nds_read_b128 a[112:115],v250 offset:12288\nM v[44:47],a[84:87],v[196:199],v[44:47]\nv_exp_f32 v172,v172\nM v[104:107],a[84:87],v[200:203],v[104:107]\n" \
"buffer_load_dwordx4 v243,s[40:43],s69 offen offset:0 lds\nM v[108:111],a[84:87],v[204:207],v[108:111]\nv_exp_f32 v173,v173\nM v[48:51],a[88:91],v[192:195],v[48:51]\nv_exp_f32 v174,v174\nM v[52:55],a[88:91],v[196:199],v[52:55]\nv_exp_f32 v175,v175\nM v[112:115],a[88:91],v[200:203],v[112:115]\n" \
"buffer_store_dwordx4 a[248:251],v253,s[52:55],s78 offen offset:0 sc1\nbuffer_load_dwordx4 v244,s[40:43],s69 offen offset:1024 lds\nM v[116:119],a[88:91],v[204:207],v[116:119]\nv_cvt_pk_bf16_f32 v217,v170,v171\ns_waitcnt lgkmcnt(0)\nM v[56:59],a[92:95],v[192:195],v[56:59]\nv_exp_f32 v188,v188\nM v[60:63],a[92:95],v[196:199],v[60:63]\n" \
"buffer_store_dwordx4 a[248:251],v253,s[52:55],s78 offen offset:1024 sc1\nM v[120:123],a[92:95],v[200:203],v[120:123]\nbuffer_load_dwordx4 v245,s[40:43],s69 offen offset:2048 lds\nM v[124:127],a[92:95],v[204:207],v[124:127]\nv_exp_f32 v189,v189\nM a[192:195],a[208:211],v[192:195],a[192:195]\nbuffer_store_dwordx4 a[248:251],v253,s[52:55],s78 offen offset:2048 sc1\nM a[196:199],a[208:211],v[196:199],a[196:199]\n" \
"v_exp_f32 v190,v190\nM a[200:203],a[208:211],v[200:203],a[200:203]\nv_exp_f32 v191,v191\nv_cvt_pk_bf16_f32 v218,v184,v185\nv_cvt_pk_bf16_f32 v219,v186,v187\nbuffer_load_dwordx4 v246,s[40:43],s69 offen offset:3072 lds\nM a[204:207],a[208:211],v[204:207],a[204:207]\nv_cvt_pk_bf16_f32 v220,v172,v173\n" \
"v_cvt_pk_bf16_f32 v221,v174,v175\nv_cvt_pk_bf16_f32 v222,v188,v189\nv_cvt_pk_bf16_f32 v223,v190,v191\nds_read_b128 a[116:119],v250 offset:13312\nbuffer_store_dwordx4 a[248:251],v253,s[52:55],s78 offen offset:3072 sc1\nM v[64:67],a[96:99],v[216:219],v[64:67]\nds_read_b128 a[120:123],v250 offset:14336\ns_add_u32 s70,s78,16384\n" \
"M v[68:71],a[96:99],v[220:223],v[68:71]\nds_read_b128 a[124:127],v250 offset:15360\nv_cvt_pk_bf16_f32 v208,v160,v161\nM v[72:75],a[100:103],v[216:219],v[72:75]\nbuffer_store_dwordx4 a[248:251],v253,s[52:55],s70 offen offset:0 sc1\nM v[76:79],a[100:103],v[220:223],v[76:79]\nv_cvt_pk_bf16_f32 v209,v162,v163\nv_cvt_pk_bf16_f32 v210,v176,v177\n" \
"M v[80:83],a[104:107],v[216:219],v[80:83]\nv_cvt_pk_bf16_f32 v211,v178,v179\nv_cvt_pk_bf16_f32 v212,v164,v165\nM v[84:87],a[104:107],v[220:223],v[84:87]\nbuffer_store_dwordx4 a[248:251],v253,s[52:55],s70 offen offset:1024 sc1\nM v[88:91],a[108:111],v[216:219],v[88:91]\nv_cvt_pk_bf16_f32 v213,v166,v167\nv_cvt_pk_bf16_f32 v214,v180,v181\n" \
"M v[92:95],a[108:111],v[220:223],v[92:95]\nv_cvt_pk_bf16_f32 v215,v182,v183\nv_add_u32 v226,131072,v242\nM v[96:99],a[112:115],v[216:219],v[96:99]\nbuffer_store_dwordx4 a[248:251],v253,s[52:55],s70 offen offset:2048 sc1\nM v[100:103],a[112:115],v[220:223],v[100:103]\nds_read_b64_tr_b16 a[212:213],v226 offset:0\ns_waitcnt lgkmcnt(1)\n" \
"M v[104:107],a[116:119],v[216:219],v[104:107]\nds_read_b64_tr_b16 a[214:215],v226 offset:4096\nv_xor_b32 v227,32,v226\nM v[108:111],a[116:119],v[220:223],v[108:111]\nbuffer_store_dwordx4 a[248:251],v253,s[52:55],s70 offen offset:3072 sc1\nM v[112:115],a[120:123],v[216:219],v[112:115]\nds_read_b64_tr_b16 a[216:217],v227 offset:0\nds_read_b64_tr_b16 a[218:219],v227 offset:4096\n" \
"M v[116:119],a[120:123],v[220:223],v[116:119]\nds_read_b64_tr_b16 a[220:221],v226 offset:512\nds_read_b64_tr_b16 a[222:223],v226 offset:4608\nM v[120:123],a[124:127],v[216:219],v[120:123]\nds_read_b64_tr_b16 a[224:225],v227 offset:512\nds_read_b64_tr_b16 a[226:227],v227 offset:4608\nds_read_b64_tr_b16 a[228:229],v226 offset:1024\nM v[124:127],a[124:127],v[220:223],v[124:127]\n" \
"ds_read_b64_tr_b16 a[230:231],v226 offset:5120\ns_add_u32 s37,s12,s67\ns_add_u32 s37,s37,1048576\nM a[200:203],a[208:211],v[216:219],a[200:203]\nds_read_b64_tr_b16 a[232:233],v227 offset:1024\nds_read_b64_tr_b16 a[234:235],v227 offset:5120\nds_read_b64_tr_b16 a[236:237],v226 offset:1536\ns_lshl2_add_u32 s77,s61,s79\n" \
"M a[204:207],a[208:211],v[220:223],a[204:207]\nds_read_b64_tr_b16 a[238:239],v226 offset:5632\nds_read_b64_tr_b16 a[240:241],v227 offset:1536\ns_add_u32 s77,s77,4096\nv_accvgpr_read_b32 v232,a200\nM v[0:3],a[96:99],v[208:211],v[0:3]\nds_read_b64_tr_b16 a[242:243],v227 offset:5632\nv_and_b32 v251,3,v249\n" \
"v_lshlrev_b32 v251,2,v251\nv_accvgpr_read_b32 v236,a204\nv_add_f32 v233,v232,v236\nv_rcp_f32 v232,v232\nv_rcp_f32 v236,v236\nv_add_f32 v233,v233,v232\nv_add_f32 v233,v233,v236\nv_cmp_ngt_f32 vcc,s28,v233\n" \
"M v[4:7],a[96:99],v[212:215],v[4:7]\ns_and_b64 vcc,vcc,s[90:91]\ns_cselect_b32 s39,0x8000000,s16\ns_cselect_b32 s16,s16,0x8000000\ns_cselect_b32 s71,2,1\ns_addc_u32 s99,s99,0\nv_mov_b32 v233,s71\ns_add_u32 s71,s57,16384\n" \
"buffer_store_dwordx4 a[248:251],v253,s[52:55],s71 offen offset:0 sc1\nbuffer_store_dwordx4 a[248:251],v253,s[52:55],s71 offen offset:1024 sc1\nbuffer_store_dwordx4 a[248:251],v253,s[52:55],s71 offen offset:2048 sc1\nbuffer_store_dwordx4 a[248:251],v253,s[52:55],s71 offen offset:3072 sc1\nM v[8:11],a[100:103],v[208:211],v[8:11]\nv_mul_f32 v64,v64,v232\nv_mul_f32 v65,v65,v232\nv_mul_f32 v66,v66,v232\n" \
"M v[12:15],a[100:103],v[212:215],v[12:15]\nv_mul_f32 v67,v67,v232\nv_cvt_pk_bf16_f32 v64,v64,v65\nv_mul_f32 v72,v72,v232\nv_mul_f32 v73,v73,v232\nM v[16:19],a[104:107],v[208:211],v[16:19]\nv_mul_f32 v74,v74,v232\nv_mul_f32 v75,v75,v232\n" \
"v_cvt_pk_bf16_f32 v65,v66,v67\nM v[20:23],a[104:107],v[212:215],v[20:23]\nv_mul_f32 v68,v68,v236\nv_mul_f32 v69,v69,v236\nv_cvt_pk_bf16_f32 v66,v72,v73\nv_cvt_pk_bf16_f32 v67,v74,v75\nM v[24:27],a[108:111],v[208:211],v[24:27]\nv_mul_f32 v70,v70,v236\n" \
"v_mul_f32 v71,v71,v236\nv_cvt_pk_bf16_f32 v68,v68,v69\nM v[28:31],a[108:111],v[212:215],v[28:31]\nv_mul_f32 v80,v80,v232\nv_mul_f32 v81,v81,v232\nv_mul_f32 v84,v84,v236\nM v[32:35],a[112:115],v[208:211],v[32:35]\nv_mul_f32 v85,v85,v236\n" \
"v_mul_f32 v96,v96,v232\nv_mul_f32 v97,v97,v232\nv_mul_f32 v100,v100,v236\nM v[36:39],a[112:115],v[212:215],v[36:39]\nv_mul_f32 v101,v101,v236\nv_mul_f32 v112,v112,v232\nv_mul_f32 v113,v113,v232\nM v[40:43],a[116:119],v[208:211],v[40:43]\n" \
"v_mul_f32 v116,v116,v236\nv_mul_f32 v117,v117,v236\nv_mul_f32 v76,v76,v236\nv_mul_f32 v77,v77,v236\nM v[44:47],a[116:119],v[212:215],v[44:47]\nv_mul_f32 v78,v78,v236\nv_mul_f32 v79,v79,v236\nv_cvt_pk_bf16_f32 v69,v70,v71\n" \
"M v[48:51],a[120:123],v[208:211],v[48:51]\nv_mul_f32 v82,v82,v232\nv_mul_f32 v83,v83,v232\nv_cvt_pk_bf16_f32 v80,v80,v81\nM v[52:55],a[120:123],v[212:215],v[52:55]\nv_mul_f32 v86,v86,v236\nv_mul_f32 v87,v87,v236\nv_cvt_pk_bf16_f32 v84,v84,v85\n" \
"v_mul_f32 v98,v98,v232\nM v[56:59],a[124:127],v[208:211],v[56:59]\nv_mul_f32 v99,v99,v232\nv_cvt_pk_bf16_f32 v96,v96,v97\nv_mul_f32 v102,v102,v236\nM v[60:63],a[124:127],v[212:215],v[60:63]\nv_mul_f32 v103,v103,v236\nv_cvt_pk_bf16_f32 v100,v100,v101\n" \
"v_mul_f32 v114,v114,v232\nv_mul_f32 v115,v115,v232\nM a[192:195],a[208:211],v[208:211],a[192:195]\nv_cvt_pk_bf16_f32 v112,v112,v113\nv_mul_f32 v118,v118,v236\nv_mul_f32 v119,v119,v236\nM a[196:199],a[208:211],v[212:215],a[196:199]\nv_cvt_pk_bf16_f32 v116,v116,v117\n" \
"s_or_b32 s37,s37,s39\nv_cvt_pk_bf16_f32 v70,v76,v77\nv_cvt_pk_bf16_f32 v71,v78,v79\nv_mul_f32 v88,v88,v232\nv_mul_f32 v89,v89,v232\nv_mul_f32 v90,v90,v232\nv_mul_f32 v91,v91,v232\nv_cvt_pk_bf16_f32 v81,v82,v83\n" \
"v_mul_f32 v92,v92,v236\nv_mul_f32 v93,v93,v236\nv_mul_f32 v94,v94,v236\nv_mul_f32 v95,v95,v236\nv_cvt_pk_bf16_f32 v85,v86,v87\nv_mul_f32 v104,v104,v232\nv_mul_f32 v105,v105,v232\nv_mul_f32 v106,v106,v232\n" \
"v_mul_f32 v107,v107,v232\nv_cvt_pk_bf16_f32 v97,v98,v99\nv_mul_f32 v108,v108,v236\nv_mul_f32 v109,v109,v236\nv_mul_f32 v110,v110,v236\nv_mul_f32 v111,v111,v236\nv_cvt_pk_bf16_f32 v101,v102,v103\nv_mul_f32 v120,v120,v232\n" \
"v_mul_f32 v121,v121,v232\nv_mul_f32 v122,v122,v232\nv_mul_f32 v123,v123,v232\nv_cvt_pk_bf16_f32 v113,v114,v115\nv_mul_f32 v124,v124,v236\nv_mul_f32 v125,v125,v236\nv_mul_f32 v126,v126,v236\nv_mul_f32 v127,v127,v236\n" \
"v_cvt_pk_bf16_f32 v117,v118,v119\ns_waitcnt lgkmcnt(0)\nM v[128:131],v[64:67],a[212:215],0\ns_add_u32 s38,s37,4096\nv_cvt_pk_bf16_f32 v82,v88,v89\nv_cvt_pk_bf16_f32 v83,v90,v91\nv_cvt_pk_bf16_f32 v86,v92,v93\nv_cvt_pk_bf16_f32 v87,v94,v95\n" \
"v_cvt_pk_bf16_f32 v98,v104,v105\nv_cvt_pk_bf16_f32 v99,v106,v107\nv_cvt_pk_bf16_f32 v102,v108,v109\nv_cvt_pk_bf16_f32 v103,v110,v111\nv_cvt_pk_bf16_f32 v114,v120,v121\nv_cvt_pk_bf16_f32 v115,v122,v123\nv_cvt_pk_bf16_f32 v118,v124,v125\nv_cvt_pk_bf16_f32 v119,v126,v127\n" \
"buffer_store_dwordx4 v[64:67],v240,s[52:55],s37 offen offset:0 sc1\nbuffer_store_dwordx4 v[68:71],v240,s[52:55],s37 offen offset:1024 sc1\nbuffer_store_dwordx4 v[80:83],v240,s[52:55],s37 offen offset:2048 sc1\nbuffer_store_dwordx4 v[84:87],v240,s[52:55],s37 offen offset:3072 sc1\nbuffer_store_dwordx4 v[96:99],v240,s[52:55],s38 offen offset:0 sc1\nbuffer_store_dwordx4 v[100:103],v240,s[52:55],s38 offen offset:1024 sc1\nbuffer_store_dwordx4 v[112:115],v240,s[52:55],s38 offen offset:2048 sc1\nbuffer_store_dwordx4 v[116:119],v240,s[52:55],s38 offen offset:3072 sc1\n" \
"M v[132:135],v[68:71],a[212:215],0\nds_read_b64_tr_b16 a[212:213],v226 offset:8192\nds_read_b64_tr_b16 a[214:215],v226 offset:12288\nv_accvgpr_read_b32 v224,a192\nM v[136:139],v[64:67],a[216:219],0\nv_accvgpr_read_b32 v228,a196\nv_add_f32 v225,v224,v228\nv_rcp_f32 v224,v224\n" \
"M v[140:143],v[68:71],a[216:219],0\nds_read_b64_tr_b16 a[216:217],v227 offset:8192\nds_read_b64_tr_b16 a[218:219],v227 offset:12288\nv_mul_f32 v0,v0,v224\nM v[144:147],v[64:67],a[220:223],0\nv_mul_f32 v1,v1,v224\nv_mul_f32 v2,v2,v224\nv_mul_f32 v3,v3,v224\n" \
"M v[148:151],v[68:71],a[220:223],0\nds_read_b64_tr_b16 a[220:221],v226 offset:8704\nds_read_b64_tr_b16 a[222:223],v226 offset:12800\nv_cvt_pk_bf16_f32 v0,v0,v1\nv_mul_f32 v8,v8,v224\nM v[152:155],v[64:67],a[224:227],0\nv_mul_f32 v9,v9,v224\nv_mul_f32 v10,v10,v224\n" \
"v_mul_f32 v11,v11,v224\nM v[156:159],v[68:71],a[224:227],0\nds_read_b64_tr_b16 a[224:225],v227 offset:8704\nds_read_b64_tr_b16 a[226:227],v227 offset:12800\nv_cvt_pk_bf16_f32 v1,v2,v3\ns_add_u32 s37,s12,s67\nM v[160:163],v[64:67],a[228:231],0\nv_cvt_pk_bf16_f32 v2,v8,v9\n" \
"v_cvt_pk_bf16_f32 v3,v10,v11\nv_mul_f32 v16,v16,v224\nM v[164:167],v[68:71],a[228:231],0\nds_read_b64_tr_b16 a[228:229],v226 offset:9216\nds_read_b64_tr_b16 a[230:231],v226 offset:13312\nv_mul_f32 v17,v17,v224\nM v[168:171],v[64:67],a[232:235],0\nv_rcp_f32 v228,v228\n" \
"v_mul_f32 v18,v18,v224\nv_mul_f32 v4,v4,v228\nM v[172:175],v[68:71],a[232:235],0\nds_read_b64_tr_b16 a[232:233],v227 offset:9216\nds_read_b64_tr_b16 a[234:235],v227 offset:13312\nv_mul_f32 v5,v5,v228\nM v[176:179],v[64:67],a[236:239],0\nv_mul_f32 v6,v6,v228\n" \
"v_mul_f32 v7,v7,v228\nv_cvt_pk_bf16_f32 v4,v4,v5\nv_mul_f32 v12,v12,v228\nM v[180:183],v[68:71],a[236:239],0\nds_read_b64_tr_b16 a[236:237],v226 offset:9728\nds_read_b64_tr_b16 a[238:239],v226 offset:13824\nv_mul_f32 v13,v13,v228\nM v[184:187],v[64:67],a[240:243],0\n" \
"buffer_store_dwordx4 v[0:3],v240,s[52:55],s37 offen offset:0 sc1\nv_mul_f32 v14,v14,v228\nM v[188:191],v[68:71],a[240:243],0\nv_mul_f32 v15,v15,v228\nv_cvt_pk_bf16_f32 v5,v6,v7\nds_read_b64_tr_b16 a[240:241],v227 offset:9728\ns_waitcnt lgkmcnt(5)\nM v[128:131],v[80:83],a[212:215],v[128:131]\n" \
"ds_read_b64_tr_b16 a[242:243],v227 offset:13824\nv_cvt_pk_bf16_f32 v6,v12,v13\nv_cvt_pk_bf16_f32 v7,v14,v15\nM v[132:135],v[84:87],a[212:215],v[132:135]\nds_read_b64_tr_b16 a[212:213],v226 offset:16384\nds_read_b64_tr_b16 a[214:215],v226 offset:20480\nv_mul_f32 v19,v19,v224\nv_cvt_pk_bf16_f32 v16,v16,v17\n" \
"M v[136:139],v[80:83],a[216:219],v[136:139]\nv_mul_f32 v24,v24,v224\nv_mul_f32 v25,v25,v224\nv_mul_f32 v26,v26,v224\nM v[140:143],v[84:87],a[216:219],v[140:143]\nds_read_b64_tr_b16 a[216:217],v227 offset:16384\nds_read_b64_tr_b16 a[218:219],v227 offset:20480\nv_mul_f32 v27,v27,v224\n" \
"M v[144:147],v[80:83],a[220:223],v[144:147]\nbuffer_store_dwordx4 v[4:7],v240,s[52:55],s37 offen offset:1024 sc1\nv_cvt_pk_bf16_f32 v17,v18,v19\nv_cvt_pk_bf16_f32 v18,v24,v25\nM v[148:151],v[84:87],a[220:223],v[148:151]\nds_read_b64_tr_b16 a[220:221],v226 offset:16896\nds_read_b64_tr_b16 a[222:223],v226 offset:20992\nv_cvt_pk_bf16_f32 v19,v26,v27\n" \
"M v[152:155],v[80:83],a[224:227],v[152:155]\ns_lshl4_add_u32 s69,s11,s68\ns_add_u32 m0,s64,32768\nv_mul_f32 v20,v20,v228\nv_mul_f32 v21,v21,v228\nM v[156:159],v[84:87],a[224:227],v[156:159]\nds_read_b64_tr_b16 a[224:225],v227 offset:16896\nds_read_b64_tr_b16 a[226:227],v227 offset:20992\n" \
"v_mul_f32 v22,v22,v228\nM v[160:163],v[80:83],a[228:231],v[160:163]\nv_mul_f32 v23,v23,v228\nv_cvt_pk_bf16_f32 v20,v20,v21\nv_mul_f32 v28,v28,v228\nM v[164:167],v[84:87],a[228:231],v[164:167]\nds_read_b64_tr_b16 a[228:229],v226 offset:17408\nds_read_b64_tr_b16 a[230:231],v226 offset:21504\n" \
"v_mul_f32 v29,v29,v228\ns_waitcnt lgkmcnt(6)\nM v[168:171],v[80:83],a[232:235],v[168:171]\nbuffer_store_dwordx4 v[16:19],v240,s[52:55],s37 offen offset:2048 sc1\nv_mul_f32 v30,v30,v228\nM v[172:175],v[84:87],a[232:235],v[172:175]\nv_mul_f32 v31,v31,v228\nv_cvt_pk_bf16_f32 v21,v22,v23\n" \
"ds_read_b64_tr_b16 a[232:233],v227 offset:17408\nds_read_b64_tr_b16 a[234:235],v227 offset:21504\nM v[176:179],v[80:83],a[236:239],v[176:179]\nbuffer_load_dwordx4 v243,s[40:43],s69 offen offset:0 lds\nv_cvt_pk_bf16_f32 v22,v28,v29\nM v[180:183],v[84:87],a[236:239],v[180:183]\nv_cvt_pk_bf16_f32 v23,v30,v31\nds_read_b64_tr_b16 a[236:237],v226 offset:17920\n" \
"ds_read_b64_tr_b16 a[238:239],v226 offset:22016\nM v[184:187],v[80:83],a[240:243],v[184:187]\nv_mul_f32 v32,v32,v224\nv_mul_f32 v33,v33,v224\nv_mul_f32 v34,v34,v224\nv_mul_f32 v35,v35,v224\nM v[188:191],v[84:87],a[240:243],v[188:191]\nds_read_b64_tr_b16 a[240:241],v227 offset:17920\n" \
"ds_read_b64_tr_b16 a[242:243],v227 offset:22016\nv_cvt_pk_bf16_f32 v32,v32,v33\nM v[128:131],v[96:99],a[212:215],v[128:131]\nbuffer_store_dwordx4 v[20:23],v240,s[52:55],s37 offen offset:3072 sc1\nbuffer_load_dwordx4 v244,s[40:43],s69 offen offset:1024 lds\nM v[132:135],v[100:103],a[212:215],v[132:135]\nv_mul_f32 v40,v40,v224\nv_mul_f32 v41,v41,v224\n" \
"v_mul_f32 v42,v42,v224\nM v[136:139],v[96:99],a[216:219],v[136:139]\nv_mul_f32 v43,v43,v224\nv_cvt_pk_bf16_f32 v33,v34,v35\nds_read_b64_tr_b16 a[212:213],v226 offset:24576\nM v[140:143],v[100:103],a[216:219],v[140:143]\nds_read_b64_tr_b16 a[214:215],v226 offset:28672\ns_add_u32 s38,s37,4096\n" \
"v_cvt_pk_bf16_f32 v34,v40,v41\ns_waitcnt lgkmcnt(4)\nM v[144:147],v[96:99],a[220:223],v[144:147]\nbuffer_load_dwordx4 v245,s[40:43],s69 offen offset:2048 lds\nv_cvt_pk_bf16_f32 v35,v42,v43\nM v[148:151],v[100:103],a[220:223],v[148:151]\nds_read_b64_tr_b16 a[216:217],v227 offset:24576\nds_read_b64_tr_b16 a[218:219],v227 offset:28672\n" \
"ds_read_b64_tr_b16 a[220:221],v226 offset:25088\nv_mul_f32 v36,v36,v228\nM v[152:155],v[96:99],a[224:227],v[152:155]\nbuffer_store_dwordx4 v[32:35],v240,s[52:55],s38 offen offset:0 sc1\nds_read_b64_tr_b16 a[222:223],v226 offset:29184\nM v[156:159],v[100:103],a[224:227],v[156:159]\nv_mul_f32 v37,v37,v228\nv_mul_f32 v38,v38,v228\n" \
"v_mul_f32 v39,v39,v228\nM v[160:163],v[96:99],a[228:231],v[160:163]\ns_waitcnt vmcnt(8)\nbuffer_store_dword v233,off,s[52:55],s77 sc1\nbuffer_load_dwordx4 v246,s[40:43],s69 offen offset:3072 lds\nM v[164:167],v[100:103],a[228:231],v[164:167]\nv_cvt_pk_bf16_f32 v36,v36,v37\nv_mul_f32 v44,v44,v228\n" \
"v_mul_f32 v45,v45,v228\nM v[168:171],v[96:99],a[232:235],v[168:171]\nv_mul_f32 v46,v46,v228\nv_mul_f32 v47,v47,v228\nv_cvt_pk_bf16_f32 v37,v38,v39\ns_lshl4_add_u32 s69,s11,s68\nM v[172:175],v[100:103],a[232:235],v[172:175]\nds_read_b64_tr_b16 a[224:225],v227 offset:25088\n" \
"ds_read_b64_tr_b16 a[226:227],v227 offset:29184\nv_cvt_pk_bf16_f32 v38,v44,v45\nM v[176:179],v[96:99],a[236:239],v[176:179]\nv_cvt_pk_bf16_f32 v39,v46,v47\ns_add_u32 s69,s69,0x10000\ns_add_u32 m0,s64,49152\nbuffer_store_dwordx4 v[36:39],v240,s[52:55],s38 offen offset:1024 sc1\ns_lshl2_add_u32 s39,s62,s7\n" \
"buffer_load_dwordx4 v243,s[40:43],s69 offen offset:0 lds\nM v[180:183],v[100:103],a[236:239],v[180:183]\ns_lshl_b32 s39,s39,5\ns_add_u32 s39,s39,s63\nds_read_b64_tr_b16 a[228:229],v226 offset:25600\nds_read_b64_tr_b16 a[230:231],v226 offset:29696\ns_waitcnt lgkmcnt(4)\nM v[184:187],v[96:99],a[240:243],v[184:187]\n" \
"s_lshl2_add_u32 s39,s39,73728\nbuffer_store_dword v254,off,s[52:55],s39 sc1\nM v[188:191],v[100:103],a[240:243],v[188:191]\nds_read_b64_tr_b16 a[232:233],v227 offset:25600\nds_read_b64_tr_b16 a[234:235],v227 offset:29696\nv_mul_f32 v48,v48,v224\nv_mul_f32 v49,v49,v224\nM v[128:131],v[112:115],a[212:215],v[128:131]\n" \
"buffer_load_dwordx4 v244,s[40:43],s69 offen offset:1024 lds\nv_mul_f32 v50,v50,v224\nM v[132:135],v[116:119],a[212:215],v[132:135]\nv_mul_f32 v51,v51,v224\nv_cvt_pk_bf16_f32 v48,v48,v49\nv_mul_f32 v56,v56,v224\nv_mul_f32 v57,v57,v224\nv_mul_f32 v58,v58,v224\n" \
"v_mul_f32 v59,v59,v224\nv_cvt_pk_bf16_f32 v49,v50,v51\nds_read_b64_tr_b16 a[236:237],v226 offset:26112\nds_read_b64_tr_b16 a[238:239],v226 offset:30208\nM v[136:139],v[112:115],a[216:219],v[136:139]\nv_cvt_pk_bf16_f32 v50,v56,v57\nv_cvt_pk_bf16_f32 v51,v58,v59\nbuffer_store_dwordx4 v[48:51],v240,s[52:55],s38 offen offset:2048 sc1\n" \
"M v[140:143],v[116:119],a[216:219],v[140:143]\nds_read_b64_tr_b16 a[240:241],v227 offset:26112\nds_read_b64_tr_b16 a[242:243],v227 offset:30208\nds_read_b64_tr_b16 a[212:213],v226 offset:0\nM v[144:147],v[112:115],a[220:223],v[144:147]\nbuffer_load_dwordx4 v245,s[40:43],s69 offen offset:2048 lds\nds_read_b64_tr_b16 a[214:215],v226 offset:4096\nv_mul_f32 v52,v52,v228\n" \
"M v[148:151],v[116:119],a[220:223],v[148:151]\nv_mul_f32 v53,v53,v228\nv_mul_f32 v54,v54,v228\ns_waitcnt lgkmcnt(6)\nM v[152:155],v[112:115],a[224:227],v[152:155]\nv_mul_f32 v55,v55,v228\nv_cvt_pk_bf16_f32 v52,v52,v53\nv_mul_f32 v60,v60,v228\n" \
"M v[156:159],v[116:119],a[224:227],v[156:159]\nv_mul_f32 v61,v61,v228\nv_mul_f32 v62,v62,v228\nv_mul_f32 v63,v63,v228\nv_cvt_pk_bf16_f32 v53,v54,v55\nds_read_b64_tr_b16 a[216:217],v227 offset:0\nds_read_b64_tr_b16 a[218:219],v227 offset:4096\nM v[160:163],v[112:115],a[228:231],v[160:163]\n" \
"v_cvt_pk_bf16_f32 v54,v60,v61\nv_cvt_pk_bf16_f32 v55,v62,v63\nbuffer_store_dwordx4 v[52:55],v240,s[52:55],s38 offen offset:3072 sc1\nbuffer_load_dwordx4 v246,s[40:43],s69 offen offset:3072 lds\nM v[164:167],v[116:119],a[228:231],v[164:167]\nds_read_b64_tr_b16 a[220:221],v226 offset:512\nds_read_b64_tr_b16 a[222:223],v226 offset:4608\nds_read_b64_tr_b16 a[224:225],v227 offset:512\n" \
"s_add_u32 s80,s79,4096\nM v[168:171],v[112:115],a[232:235],v[168:171]\nds_read_b64_tr_b16 a[226:227],v227 offset:4608\nds_read_b64_tr_b16 a[228:229],v226 offset:1024\nv_add_f32 v225,v225,v224\nM v[172:175],v[116:119],a[232:235],v[172:175]\nds_read_b64_tr_b16 a[230:231],v226 offset:5120\nds_read_b64_tr_b16 a[232:233],v227 offset:1024\n" \
"s_waitcnt lgkmcnt(9)\nM v[176:179],v[112:115],a[236:239],v[176:179]\nds_read_b64_tr_b16 a[234:235],v227 offset:5120\nv_add_f32 v225,v225,v228\nv_cmp_ngt_f32 vcc,s28,v225\ns_add_u32 s65,s10,s67\nM v[180:183],v[116:119],a[236:239],v[180:183]\nds_read_b64_tr_b16 a[236:237],v226 offset:1536\n" \
"ds_read_b64_tr_b16 a[238:239],v226 offset:5632\ns_add_u32 s65,s65,1048576\nM v[184:187],v[112:115],a[240:243],v[184:187]\nbuffer_load_dword v248,v251,s[52:55],s80 offen sc1\ns_and_b64 vcc,vcc,s[90:91]\ns_addc_u32 s99,s99,0\nM v[188:191],v[116:119],a[240:243],v[188:191]\nds_read_b64_tr_b16 a[240:241],v227 offset:1536\n" \
"ds_read_b64_tr_b16 a[242:243],v227 offset:5632\nv_xor_b32 v250,0x8000,v250\nM v[8:11],v[0:3],a[212:215],0\nv_add_u32 v229,s64,v250\ns_lshl2_add_u32 s76,s61,s79\ns_lshr_b32 s70,s60,1\nM v[12:15],v[4:7],a[212:215],0\nds_read_b64_tr_b16 a[212:213],v226 offset:8192\n" \
"ds_read_b64_tr_b16 a[214:215],v226 offset:12288\ns_lshl_b32 s70,s70,7\ns_waitcnt lgkmcnt(6)\nM v[24:27],v[0:3],a[216:219],0\nv_and_b32 v228,31,v249\nv_lshl_add_u32 v228,v228,2,s70\nv_and_b32 v236,15,v249\nM v[28:31],v[4:7],a[216:219],0\n" \
"ds_read_b64_tr_b16 a[216:217],v227 offset:8192\nds_read_b64_tr_b16 a[218:219],v227 offset:12288\ns_lshl2_add_u32 s74,s63,s7\ns_lshl_b32 s72,s62,7\nM v[40:43],v[0:3],a[220:223],0\nv_lshrrev_b32 v235,4,v249\nv_lshlrev_b32 v236,1,v236\ns_add_u32 s95,s74,s72\n" \
"M v[44:47],v[4:7],a[220:223],0\nds_read_b64_tr_b16 a[220:221],v226 offset:8704\nds_read_b64_tr_b16 a[222:223],v226 offset:12800\nv_lshl_or_b32 v235,v235,8,v236\nM v[56:59],v[0:3],a[224:227],0\ns_lshl_b32 s95,s95,10\nv_add_u32 v236,s95,v235\ns_sub_u32 s74,s74,s72\n" \
"s_add_u32 s74,s74,1920\nM v[60:63],v[4:7],a[224:227],0\nds_read_b64_tr_b16 a[224:225],v227 offset:8704\nds_read_b64_tr_b16 a[226:227],v227 offset:12800\ns_lshl_b32 s74,s74,10\nM v[72:75],v[0:3],a[228:231],0\nv_add_u32 v237,s74,v235\ns_lshl_b32 s72,s7,9\n" \
"v_lshl_add_u32 v239,v249,3,s72\ns_mov_b32 s97,4096\nM v[76:79],v[4:7],a[228:231],0\nds_read_b64_tr_b16 a[228:229],v226 offset:9216\nds_read_b64_tr_b16 a[230:231],v226 offset:13312\ns_add_u32 s38,s57,16384\nM v[88:91],v[0:3],a[232:235],0\nv_lshlrev_b32 v238,1,v240\n" \
"v_bfe_u32 v234,v249,2,2\ns_add_u32 s95,s95,54525952\nM v[92:95],v[4:7],a[232:235],0\nds_read_b64_tr_b16 a[232:233],v227 offset:9216\nds_read_b64_tr_b16 a[234:235],v227 offset:13312\ns_add_u32 s74,s74,54525952\ns_waitcnt lgkmcnt(6)\nM v[104:107],v[0:3],a[236:239],0\n" \
"M v[108:111],v[4:7],a[236:239],0\nds_read_b64_tr_b16 a[236:237],v226 offset:9728\nds_read_b64_tr_b16 a[238:239],v226 offset:13824\nM v[120:123],v[0:3],a[240:243],0\nM v[124:127],v[4:7],a[240:243],0\nds_read_b64_tr_b16 a[240:241],v227 offset:9728\nds_read_b64_tr_b16 a[242:243],v227 offset:13824\nM v[8:11],v[16:19],a[212:215],v[8:11]\n" \
"M v[12:15],v[20:23],a[212:215],v[12:15]\nds_read_b64_tr_b16 a[212:213],v226 offset:16384\nds_read_b64_tr_b16 a[214:215],v226 offset:20480\nM v[24:27],v[16:19],a[216:219],v[24:27]\nM v[28:31],v[20:23],a[216:219],v[28:31]\nds_read_b64_tr_b16 a[216:217],v227 offset:16384\nds_read_b64_tr_b16 a[218:219],v227 offset:20480\nM v[40:43],v[16:19],a[220:223],v[40:43]\n" \
"M v[44:47],v[20:23],a[220:223],v[44:47]\nds_read_b64_tr_b16 a[220:221],v226 offset:16896\nds_read_b64_tr_b16 a[222:223],v226 offset:20992\ns_waitcnt lgkmcnt(10)\nM v[56:59],v[16:19],a[224:227],v[56:59]\ns_waitcnt vmcnt(0)\nv_and_b32 v248,s17,v248\nv_cmp_eq_u32 vcc,0,v248\n" \
"M v[60:63],v[20:23],a[224:227],v[60:63]\ns_cbranch_vccnz .Lps9\n.Lpb9:\nds_read_b64_tr_b16 a[224:225],v227 offset:16896\nds_read_b64_tr_b16 a[226:227],v227 offset:20992\nv_cmp_eq_u32 vcc,2,v248\nM v[72:75],v[16:19],a[228:231],v[72:75]\nbuffer_load_dwordx4 a[0:3],v240,s[52:55],s65 offen offset:0 sc1\n" \
"s_lshl_b32 s80,s62,9\nM v[76:79],v[20:23],a[228:231],v[76:79]\nds_read_b64_tr_b16 a[228:229],v226 offset:17408\nds_read_b64_tr_b16 a[230:231],v226 offset:21504\ns_add_u32 s80,s80,73728\nM v[88:91],v[16:19],a[232:235],v[88:91]\ns_cmp_lg_u64 vcc,0\ns_addc_u32 s99,s99,0\n" \
"M v[92:95],v[20:23],a[232:235],v[92:95]\nbuffer_load_dwordx4 a[4:7],v240,s[52:55],s65 offen offset:1024 sc1\ns_waitcnt lgkmcnt(4)\nM v[104:107],v[16:19],a[236:239],v[104:107]\nds_read_b64_tr_b16 a[232:233],v227 offset:17408\nds_read_b64_tr_b16 a[234:235],v227 offset:21504\nM v[108:111],v[20:23],a[236:239],v[108:111]\nds_read_b64_tr_b16 a[236:237],v226 offset:17920\n" \
"ds_read_b64_tr_b16 a[238:239],v226 offset:22016\nM v[120:123],v[16:19],a[240:243],v[120:123]\nbuffer_load_dwordx4 a[8:11],v240,s[52:55],s65 offen offset:2048 sc1\nM v[124:127],v[20:23],a[240:243],v[124:127]\nv_mov_b32 v230,s99\nds_read_b64_tr_b16 a[240:241],v227 offset:17920\nM v[8:11],v[32:35],a[212:215],v[8:11]\nds_write_b32 v229,v230\n" \
"ds_read_b64_tr_b16 a[242:243],v227 offset:22016\nM v[12:15],v[36:39],a[212:215],v[12:15]\nbuffer_load_dwordx4 a[12:15],v240,s[52:55],s65 offen offset:3072 sc1\nM v[24:27],v[32:35],a[216:219],v[24:27]\ns_add_u32 s65,s65,4096\nds_read_b64_tr_b16 a[212:213],v226 offset:24576\nM v[28:31],v[36:39],a[216:219],v[28:31]\nds_read_b64_tr_b16 a[214:215],v226 offset:28672\n" \
"ds_read_b64_tr_b16 a[216:217],v227 offset:24576\nM v[40:43],v[32:35],a[220:223],v[40:43]\nbuffer_load_dwordx4 a[16:19],v240,s[52:55],s65 offen offset:0 sc1\nM v[44:47],v[36:39],a[220:223],v[44:47]\nds_read_b64_tr_b16 a[218:219],v227 offset:28672\ns_waitcnt lgkmcnt(7)\nM v[56:59],v[32:35],a[224:227],v[56:59]\nds_read_b64_tr_b16 a[220:221],v226 offset:25088\n" \
"ds_read_b64_tr_b16 a[222:223],v226 offset:29184\nM v[60:63],v[36:39],a[224:227],v[60:63]\nbuffer_load_dwordx4 a[20:23],v240,s[52:55],s65 offen offset:1024 sc1\nM v[72:75],v[32:35],a[228:231],v[72:75]\nds_read_b64_tr_b16 a[224:225],v227 offset:25088\nds_read_b64_tr_b16 a[226:227],v227 offset:29184\nM v[76:79],v[36:39],a[228:231],v[76:79]\nv_lshl_add_u32 v229,v251,10,v250\n" \
"ds_read_b64_tr_b16 a[228:229],v226 offset:25600\nM v[88:91],v[32:35],a[232:235],v[88:91]\nbuffer_load_dwordx4 a[24:27],v240,s[52:55],s65 offen offset:2048 sc1\nM v[92:95],v[36:39],a[232:235],v[92:95]\nds_read_b64_tr_b16 a[230:231],v226 offset:29696\nds_read_b64_tr_b16 a[232:233],v227 offset:25600\nM v[104:107],v[32:35],a[236:239],v[104:107]\nds_read_b64_tr_b16 a[234:235],v227 offset:29696\n" \
"M v[108:111],v[36:39],a[236:239],v[108:111]\nbuffer_load_dwordx4 a[28:31],v240,s[52:55],s65 offen offset:3072 sc1\ns_waitcnt lgkmcnt(7)\nM v[120:123],v[32:35],a[240:243],v[120:123]\ns_barrier\nds_read_b32 v230,v229\nM v[124:127],v[36:39],a[240:243],v[124:127]\ns_add_u32 s65,s11,s67\n" \
"s_add_u32 s65,s65,1048576\nM v[8:11],v[48:51],a[212:215],v[8:11]\nbuffer_load_dwordx4 a[32:35],v240,s[52:55],s65 offen offset:0 sc1\nM v[12:15],v[52:55],a[212:215],v[12:15]\nds_read_b64_tr_b16 a[236:237],v226 offset:26112\nds_read_b64_tr_b16 a[238:239],v226 offset:30208\nM v[24:27],v[48:51],a[216:219],v[24:27]\nds_read_b64_tr_b16 a[240:241],v227 offset:26112\n" \
"ds_read_b64_tr_b16 a[242:243],v227 offset:30208\nM v[28:31],v[52:55],a[216:219],v[28:31]\nbuffer_load_dwordx4 a[36:39],v240,s[52:55],s65 offen offset:1024 sc1\ns_waitcnt lgkmcnt(5)\nM v[40:43],v[48:51],a[220:223],v[40:43]\nv_add_u32 v226,0,v242\nds_read_b64_tr_b16 a[212:213],v226 offset:0\nM v[44:47],v[52:55],a[220:223],v[44:47]\n" \
"ds_read_b64_tr_b16 a[214:215],v226 offset:4096\nv_xor_b32 v227,32,v226\nM v[56:59],v[48:51],a[224:227],v[56:59]\nbuffer_load_dwordx4 a[40:43],v240,s[52:55],s65 offen offset:2048 sc1\nM v[60:63],v[52:55],a[224:227],v[60:63]\ns_waitcnt lgkmcnt(6)\nv_cmp_ne_u32 vcc,0,v230\nds_read_b64_tr_b16 a[216:217],v227 offset:0\n" \
"ds_read_b64_tr_b16 a[218:219],v227 offset:4096\nM v[72:75],v[48:51],a[228:231],v[72:75]\ns_cbranch_vccnz .Lredo\nbuffer_store_dword v254,off,s[52:55],s76 sc1\nM v[76:79],v[52:55],a[228:231],v[76:79]\nbuffer_load_dwordx4 a[44:47],v240,s[52:55],s65 offen offset:3072 sc1\nds_read_b64_tr_b16 a[220:221],v226 offset:512\nds_read_b64_tr_b16 a[222:223],v226 offset:4608\n" \
"M v[88:91],v[48:51],a[232:235],v[88:91]\nds_read_b64_tr_b16 a[224:225],v227 offset:512\nds_read_b64_tr_b16 a[226:227],v227 offset:4608\nM v[92:95],v[52:55],a[232:235],v[92:95]\ns_add_u32 s65,s65,4096\ns_waitcnt lgkmcnt(6)\nM v[104:107],v[48:51],a[236:239],v[104:107]\nbuffer_load_dwordx4 a[48:51],v240,s[52:55],s65 offen offset:0 sc1\n" \
"M v[108:111],v[52:55],a[236:239],v[108:111]\nds_read_b64_tr_b16 a[228:229],v226 offset:1024\nds_read_b64_tr_b16 a[230:231],v226 offset:5120\nM v[120:123],v[48:51],a[240:243],v[120:123]\nds_read_b64_tr_b16 a[232:233],v227 offset:1024\nds_read_b64_tr_b16 a[234:235],v227 offset:5120\nM v[124:127],v[52:55],a[240:243],v[124:127]\nbuffer_load_dwordx4 a[52:55],v240,s[52:55],s65 offen offset:1024 sc1\n" \
"s_waitcnt vmcnt(14)\nM v[128:131],a[0:3],a[212:215],v[128:131]\ns_lshl4_add_u32 s69,s14,s68\ns_add_u32 m0,s64,65536\ns_waitcnt vmcnt(13)\nM v[132:135],a[4:7],a[212:215],v[132:135]\nbuffer_load_dwordx4 v243,s[40:43],s69 offen offset:0 lds\nds_read_b64_tr_b16 a[236:237],v226 offset:1536\n" \
"ds_read_b64_tr_b16 a[238:239],v226 offset:5632\ns_waitcnt lgkmcnt(6)\nM v[136:139],a[0:3],a[216:219],v[136:139]\nbuffer_load_dwordx4 a[56:59],v240,s[52:55],s65 offen offset:2048 sc1\nM v[140:143],a[4:7],a[216:219],v[140:143]\nds_read_b64_tr_b16 a[240:241],v227 offset:1536\nds_read_b64_tr_b16 a[242:243],v227 offset:5632\nM v[144:147],a[0:3],a[220:223],v[144:147]\n" \
"buffer_load_dwordx4 v244,s[40:43],s69 offen offset:1024 lds\nM v[148:151],a[4:7],a[220:223],v[148:151]\nbuffer_load_dwordx4 a[60:63],v240,s[52:55],s65 offen offset:3072 sc1\nds_read_b64_tr_b16 a[212:213],v226 offset:8192\nds_read_b64_tr_b16 a[214:215],v226 offset:12288\nM v[152:155],a[0:3],a[224:227],v[152:155]\ns_add_u32 s65,s14,s67\nds_read_b64_tr_b16 a[216:217],v227 offset:8192\n" \
"M v[156:159],a[4:7],a[224:227],v[156:159]\nds_read_b64_tr_b16 a[218:219],v227 offset:12288\ns_waitcnt lgkmcnt(8)\nM v[160:163],a[0:3],a[228:231],v[160:163]\ns_add_u32 s65,s65,1048576\nbuffer_load_dwordx4 v245,s[40:43],s69 offen offset:2048 lds\nbuffer_load_dwordx4 a[160:163],v240,s[52:55],s65 offen offset:0 sc1\nM v[164:167],a[4:7],a[228:231],v[164:167]\n" \
"ds_read_b64_tr_b16 a[220:221],v226 offset:8704\nds_read_b64_tr_b16 a[222:223],v226 offset:12800\nM v[168:171],a[0:3],a[232:235],v[168:171]\nds_read_b64_tr_b16 a[224:225],v227 offset:8704\nds_read_b64_tr_b16 a[226:227],v227 offset:12800\nM v[172:175],a[4:7],a[232:235],v[172:175]\nbuffer_load_dwordx4 a[164:167],v240,s[52:55],s65 offen offset:1024 sc1\ns_waitcnt lgkmcnt(7)\n" \
"M v[176:179],a[0:3],a[236:239],v[176:179]\nbuffer_load_dwordx4 v246,s[40:43],s69 offen offset:3072 lds\nM v[180:183],a[4:7],a[236:239],v[180:183]\nds_read_b64_tr_b16 a[228:229],v226 offset:9216\nds_read_b64_tr_b16 a[230:231],v226 offset:13312\nM v[184:187],a[0:3],a[240:243],v[184:187]\nbuffer_load_dwordx4 a[168:171],v240,s[52:55],s65 offen offset:2048 sc1\nM v[188:191],a[4:7],a[240:243],v[188:191]\n" \
"s_lshl4_add_u32 s69,s14,s68\nds_read_b64_tr_b16 a[232:233],v227 offset:9216\nds_read_b64_tr_b16 a[234:235],v227 offset:13312\ns_waitcnt vmcnt(21) lgkmcnt(8)\nM v[128:131],a[8:11],a[212:215],v[128:131]\ns_add_u32 s69,s69,0x10000\ns_add_u32 m0,s64,98304\ns_waitcnt vmcnt(20)\n" \
"M v[132:135],a[12:15],a[212:215],v[132:135]\nbuffer_load_dwordx4 v243,s[40:43],s69 offen offset:0 lds\nbuffer_load_dwordx4 a[172:175],v240,s[52:55],s65 offen offset:3072 sc1\nds_read_b64_tr_b16 a[236:237],v226 offset:9728\nds_read_b64_tr_b16 a[238:239],v226 offset:13824\nM v[136:139],a[8:11],a[216:219],v[136:139]\nds_read_b64_tr_b16 a[240:241],v227 offset:9728\nds_read_b64_tr_b16 a[242:243],v227 offset:13824\n" \
"M v[140:143],a[12:15],a[216:219],v[140:143]\ns_add_u32 s65,s65,4096\ns_waitcnt lgkmcnt(6)\nM v[144:147],a[8:11],a[220:223],v[144:147]\nbuffer_load_dwordx4 v244,s[40:43],s69 offen offset:1024 lds\nbuffer_load_dwordx4 a[176:179],v240,s[52:55],s65 offen offset:0 sc1\nM v[148:151],a[12:15],a[220:223],v[148:151]\nds_read_b64_tr_b16 a[212:213],v226 offset:16384\n" \
"ds_read_b64_tr_b16 a[214:215],v226 offset:20480\nM v[152:155],a[8:11],a[224:227],v[152:155]\nds_read_b64_tr_b16 a[216:217],v227 offset:16384\nds_read_b64_tr_b16 a[218:219],v227 offset:20480\nM v[156:159],a[12:15],a[224:227],v[156:159]\nbuffer_load_dwordx4 a[180:183],v240,s[52:55],s65 offen offset:1024 sc1\nM v[160:163],a[8:11],a[228:231],v[160:163]\nbuffer_load_dwordx4 v245,s[40:43],s69 offen offset:2048 lds\n" \
"M v[164:167],a[12:15],a[228:231],v[164:167]\nds_read_b64_tr_b16 a[220:221],v226 offset:16896\nds_read_b64_tr_b16 a[222:223],v226 offset:20992\ns_waitcnt lgkmcnt(6)\nM v[168:171],a[8:11],a[232:235],v[168:171]\nbuffer_load_dwordx4 a[184:187],v240,s[52:55],s65 offen offset:2048 sc1\nM v[172:175],a[12:15],a[232:235],v[172:175]\nds_read_b64_tr_b16 a[224:225],v227 offset:16896\n" \
"ds_read_b64_tr_b16 a[226:227],v227 offset:20992\nM v[176:179],a[8:11],a[236:239],v[176:179]\nbuffer_load_dwordx4 v246,s[40:43],s69 offen offset:3072 lds\nM v[180:183],a[12:15],a[236:239],v[180:183]\nbuffer_load_dwordx4 a[188:191],v240,s[52:55],s65 offen offset:3072 sc1\nds_read_b64_tr_b16 a[228:229],v226 offset:17408\nds_read_b64_tr_b16 a[230:231],v226 offset:21504\nM v[184:187],a[8:11],a[240:243],v[184:187]\n" \
"ds_read_b64_tr_b16 a[232:233],v227 offset:17408\nds_read_b64_tr_b16 a[234:235],v227 offset:21504\nM v[188:191],a[12:15],a[240:243],v[188:191]\nds_read_b64_tr_b16 a[236:237],v226 offset:17920\ns_waitcnt vmcnt(26) lgkmcnt(9)\nM v[128:131],a[16:19],a[212:215],v[128:131]\nds_read_b64_tr_b16 a[238:239],v226 offset:22016\nds_read_b64_tr_b16 a[240:241],v227 offset:17920\n" \
"M v[132:135],a[20:23],a[212:215],v[132:135]\nds_read_b64_tr_b16 a[242:243],v227 offset:22016\nds_read_b64_tr_b16 a[212:213],v226 offset:24576\nM v[136:139],a[16:19],a[216:219],v[136:139]\nds_read_b64_tr_b16 a[214:215],v226 offset:28672\nv_and_b32 v229,32,v249\nM v[140:143],a[20:23],a[216:219],v[140:143]\nv_mul_u32_u24 v229,12,v229\n" \
"s_waitcnt lgkmcnt(10)\nM v[144:147],a[16:19],a[220:223],v[144:147]\nds_read_b64_tr_b16 a[216:217],v227 offset:24576\nds_read_b64_tr_b16 a[218:219],v227 offset:28672\nM v[148:151],a[20:23],a[220:223],v[148:151]\nv_xor_b32 v228,v228,v229\nds_read_b64_tr_b16 a[220:221],v226 offset:25088\nM v[152:155],a[16:19],a[224:227],v[152:155]\n" \
"ds_read_b64_tr_b16 a[222:223],v226 offset:29184\nv_mov_b32 v250,81920\nM v[156:159],a[20:23],a[224:227],v[156:159]\nds_read_b64_tr_b16 a[224:225],v227 offset:25088\ns_waitcnt lgkmcnt(9)\nM v[160:163],a[16:19],a[228:231],v[160:163]\nbuffer_load_dword v248,v228,s[52:55],s80 offen sc1\nM v[164:167],a[20:23],a[228:231],v[164:167]\n" \
"ds_read_b64_tr_b16 a[226:227],v227 offset:29184\nds_read_b64_tr_b16 a[228:229],v226 offset:25600\nM v[168:171],a[16:19],a[232:235],v[168:171]\nds_read_b64_tr_b16 a[230:231],v226 offset:29696\ns_add_u32 s65,s10,s67\nM v[172:175],a[20:23],a[232:235],v[172:175]\nds_read_b64_tr_b16 a[232:233],v227 offset:25600\nds_read_b64_tr_b16 a[234:235],v227 offset:29696\n" \
"M v[176:179],a[16:19],a[236:239],v[176:179]\nv_accvgpr_write_b32 a194,0\nv_accvgpr_write_b32 a195,0\nM v[180:183],a[20:23],a[236:239],v[180:183]\nds_read_b64_tr_b16 a[236:237],v226 offset:26112\ns_waitcnt lgkmcnt(7)\nM v[184:187],a[16:19],a[240:243],v[184:187]\ns_barrier\n" \
"ds_read_b64_tr_b16 a[238:239],v226 offset:30208\nM v[188:191],a[20:23],a[240:243],v[188:191]\nds_read_b64_tr_b16 a[240:241],v227 offset:26112\nds_read_b64_tr_b16 a[242:243],v227 offset:30208\nM v[128:131],a[24:27],a[212:215],v[128:131]\nds_write_b64 v250,a[244:245]\ns_waitcnt vmcnt(23)\nM v[132:135],a[28:31],a[212:215],v[132:135]\n" \
"ds_read_b64_tr_b16 a[212:213],v226 offset:32768\nds_read_b64_tr_b16 a[214:215],v226 offset:36864\nM v[136:139],a[24:27],a[216:219],v[136:139]\nv_accvgpr_write_b32 a196,0\nv_accvgpr_write_b32 a197,0\nM v[140:143],a[28:31],a[216:219],v[140:143]\nds_read_b64_tr_b16 a[216:217],v227 offset:32768\nds_read_b64_tr_b16 a[218:219],v227 offset:36864\n" \
"M v[144:147],a[24:27],a[220:223],v[144:147]\ns_load_dwordx2 s[44:45],s[4:5],0x68\ns_mov_b32 s46,2097152\nM v[148:151],a[28:31],a[220:223],v[148:151]\nds_read_b64_tr_b16 a[220:221],v226 offset:33280\ns_waitcnt lgkmcnt(9)\nM v[152:155],a[24:27],a[224:227],v[152:155]\nds_read_b64_tr_b16 a[222:223],v226 offset:37376\n" \
"M v[156:159],a[28:31],a[224:227],v[156:159]\nds_read_b64_tr_b16 a[224:225],v227 offset:33280\nds_read_b64_tr_b16 a[226:227],v227 offset:37376\nM v[160:163],a[24:27],a[228:231],v[160:163]\nM v[164:167],a[28:31],a[228:231],v[164:167]\nds_read_b64_tr_b16 a[228:229],v226 offset:33792\nds_read_b64_tr_b16 a[230:231],v226 offset:37888\nM v[168:171],a[24:27],a[232:235],v[168:171]\n" \
"M v[172:175],a[28:31],a[232:235],v[172:175]\nds_read_b64_tr_b16 a[232:233],v227 offset:33792\ns_waitcnt lgkmcnt(9)\nM v[176:179],a[24:27],a[236:239],v[176:179]\nds_read_b64_tr_b16 a[234:235],v227 offset:37888\nM v[180:183],a[28:31],a[236:239],v[180:183]\nds_read_b64_tr_b16 a[236:237],v226 offset:34304\nds_read_b64_tr_b16 a[238:239],v226 offset:38400\n" \
"M v[184:187],a[24:27],a[240:243],v[184:187]\nM v[188:191],a[28:31],a[240:243],v[188:191]\nds_read_b64_tr_b16 a[240:241],v227 offset:34304\nds_read_b64_tr_b16 a[242:243],v227 offset:38400\nM v[128:131],a[32:35],a[212:215],v[128:131]\nM v[132:135],a[36:39],a[212:215],v[132:135]\nds_read_b64_tr_b16 a[212:213],v226 offset:40960\ns_waitcnt lgkmcnt(9)\n" \
"M v[136:139],a[32:35],a[216:219],v[136:139]\nds_read_b64_tr_b16 a[214:215],v226 offset:45056\nM v[140:143],a[36:39],a[216:219],v[140:143]\nds_read_b64_tr_b16 a[216:217],v227 offset:40960\nds_read_b64_tr_b16 a[218:219],v227 offset:45056\nM v[144:147],a[32:35],a[220:223],v[144:147]\nM v[148:151],a[36:39],a[220:223],v[148:151]\nglobal_load_ushort v64,v236,s[8:9] offset:0\n" \
"M v[152:155],a[32:35],a[224:227],v[152:155]\nds_read_b64_tr_b16 a[220:221],v226 offset:41472\nds_read_b64_tr_b16 a[222:223],v226 offset:45568\nM v[156:159],a[36:39],a[224:227],v[156:159]\nglobal_load_ushort v80,v237,s[8:9] offset:0\ns_waitcnt lgkmcnt(7)\nM v[160:163],a[32:35],a[228:231],v[160:163]\nds_read_b64_tr_b16 a[224:225],v227 offset:41472\n" \
"ds_read_b64_tr_b16 a[226:227],v227 offset:45568\nM v[164:167],a[36:39],a[228:231],v[164:167]\nglobal_load_ushort v96,v235,s[20:21] offset:0\nM v[168:171],a[32:35],a[232:235],v[168:171]\nds_read_b64_tr_b16 a[228:229],v226 offset:41984\nds_read_b64_tr_b16 a[230:231],v226 offset:46080\nM v[172:175],a[36:39],a[232:235],v[172:175]\nglobal_load_ushort v65,v236,s[8:9] offset:32\n" \
"M v[176:179],a[32:35],a[236:239],v[176:179]\nds_read_b64_tr_b16 a[232:233],v227 offset:41984\nds_read_b64_tr_b16 a[234:235],v227 offset:46080\nM v[180:183],a[36:39],a[236:239],v[180:183]\nglobal_load_ushort v81,v237,s[8:9] offset:32\ns_waitcnt lgkmcnt(6)\nM v[184:187],a[32:35],a[240:243],v[184:187]\nds_read_b64_tr_b16 a[236:237],v226 offset:42496\n" \
"ds_read_b64_tr_b16 a[238:239],v226 offset:46592\nM v[188:191],a[36:39],a[240:243],v[188:191]\nglobal_load_ushort v97,v235,s[20:21] offset:32\nM v[128:131],a[40:43],a[212:215],v[128:131]\ns_waitcnt vmcnt(6)\nv_cmp_eq_u32 vcc,0,v248\nM v[132:135],a[44:47],a[212:215],v[132:135]\nglobal_load_ushort v66,v236,s[8:9] offset:64\n" \
"M v[136:139],a[40:43],a[216:219],v[136:139]\nds_read_b64_tr_b16 a[240:241],v227 offset:42496\nds_read_b64_tr_b16 a[242:243],v227 offset:46592\nM v[140:143],a[44:47],a[216:219],v[140:143]\nglobal_load_ushort v82,v237,s[8:9] offset:64\nM v[144:147],a[40:43],a[220:223],v[144:147]\ns_cbranch_vccnz .Lps10\n.Lpb10:\n" \
"buffer_load_dword v248,v251,s[52:55],s79 offen sc1\nM v[148:151],a[44:47],a[220:223],v[148:151]\nds_read_b64_tr_b16 a[212:213],v226 offset:49152\nds_read_b64_tr_b16 a[214:215],v226 offset:53248\nglobal_load_ushort v98,v235,s[20:21] offset:64\ns_waitcnt lgkmcnt(4)\nM v[152:155],a[40:43],a[224:227],v[152:155]\nds_read_b64_tr_b16 a[216:217],v227 offset:49152\n" \
"ds_read_b64_tr_b16 a[218:219],v227 offset:53248\nM v[156:159],a[44:47],a[224:227],v[156:159]\nglobal_load_ushort v67,v236,s[8:9] offset:96\nM v[160:163],a[40:43],a[228:231],v[160:163]\nds_read_b64_tr_b16 a[220:221],v226 offset:49664\nds_read_b64_tr_b16 a[222:223],v226 offset:53760\nM v[164:167],a[44:47],a[228:231],v[164:167]\nglobal_load_ushort v83,v237,s[8:9] offset:96\n" \
"M v[168:171],a[40:43],a[232:235],v[168:171]\nds_read_b64_tr_b16 a[224:225],v227 offset:49664\nds_read_b64_tr_b16 a[226:227],v227 offset:53760\nM v[172:175],a[44:47],a[232:235],v[172:175]\nglobal_load_ushort v99,v235,s[20:21] offset:96\nM v[176:179],a[40:43],a[236:239],v[176:179]\nds_read_b64_tr_b16 a[228:229],v226 offset:50176\nds_read_b64_tr_b16 a[230:231],v226 offset:54272\n" \
"M v[180:183],a[44:47],a[236:239],v[180:183]\nglobal_load_ushort v68,v236,s[8:9] offset:128\ns_waitcnt lgkmcnt(6)\nM v[184:187],a[40:43],a[240:243],v[184:187]\nds_read_b64_tr_b16 a[232:233],v227 offset:50176\nds_read_b64_tr_b16 a[234:235],v227 offset:54272\nM v[188:191],a[44:47],a[240:243],v[188:191]\nglobal_load_ushort v84,v237,s[8:9] offset:128\n" \
"M v[128:131],a[48:51],a[212:215],v[128:131]\nds_read_b64_tr_b16 a[236:237],v226 offset:50688\nds_read_b64_tr_b16 a[238:239],v226 offset:54784\nM v[132:135],a[52:55],a[212:215],v[132:135]\nglobal_load_ushort v100,v235,s[20:21] offset:128\nM v[136:139],a[48:51],a[216:219],v[136:139]\nds_read_b64_tr_b16 a[240:241],v227 offset:50688\nds_read_b64_tr_b16 a[242:243],v227 offset:54784\n" \
"M v[140:143],a[52:55],a[216:219],v[140:143]\nglobal_load_ushort v69,v236,s[8:9] offset:160\ns_waitcnt lgkmcnt(6)\nM v[144:147],a[48:51],a[220:223],v[144:147]\nds_read_b64_tr_b16 a[212:213],v226 offset:57344\nds_read_b64_tr_b16 a[214:215],v226 offset:61440\nM v[148:151],a[52:55],a[220:223],v[148:151]\nglobal_load_ushort v85,v237,s[8:9] offset:160\n" \
"M v[152:155],a[48:51],a[224:227],v[152:155]\nds_read_b64_tr_b16 a[216:217],v227 offset:57344\nds_read_b64_tr_b16 a[218:219],v227 offset:61440\nM v[156:159],a[52:55],a[224:227],v[156:159]\nglobal_load_ushort v101,v235,s[20:21] offset:160\nM v[160:163],a[48:51],a[228:231],v[160:163]\nds_read_b64_tr_b16 a[220:221],v226 offset:57856\nds_read_b64_tr_b16 a[222:223],v226 offset:61952\n" \
"M v[164:167],a[52:55],a[228:231],v[164:167]\nglobal_load_ushort v70,v236,s[8:9] offset:192\ns_waitcnt lgkmcnt(6)\nM v[168:171],a[48:51],a[232:235],v[168:171]\nds_read_b64_tr_b16 a[224:225],v227 offset:57856\nds_read_b64_tr_b16 a[226:227],v227 offset:61952\nM v[172:175],a[52:55],a[232:235],v[172:175]\nglobal_load_ushort v86,v237,s[8:9] offset:192\n" \
"M v[176:179],a[48:51],a[236:239],v[176:179]\nds_read_b64_tr_b16 a[228:229],v226 offset:58368\nds_read_b64_tr_b16 a[230:231],v226 offset:62464\nM v[180:183],a[52:55],a[236:239],v[180:183]\nglobal_load_ushort v102,v235,s[20:21] offset:192\nM v[184:187],a[48:51],a[240:243],v[184:187]\ns_barrier\nds_read_b64_tr_b16 a[232:233],v227 offset:58368\n" \
"M v[188:191],a[52:55],a[240:243],v[188:191]\nds_read_b64_tr_b16 a[234:235],v227 offset:62464\nglobal_load_ushort v71,v236,s[8:9] offset:224\ns_waitcnt lgkmcnt(6)\nM v[128:131],a[56:59],a[212:215],v[128:131]\nds_read_b64_tr_b16 a[236:237],v226 offset:58880\nds_read_b64_tr_b16 a[238:239],v226 offset:62976\nM v[132:135],a[60:63],a[212:215],v[132:135]\n" \
"global_load_ushort v87,v237,s[8:9] offset:224\nM v[136:139],a[56:59],a[216:219],v[136:139]\nds_read_b64_tr_b16 a[240:241],v227 offset:58880\nds_read_b64_tr_b16 a[242:243],v227 offset:62976\nM v[140:143],a[60:63],a[216:219],v[140:143]\nglobal_load_ushort v103,v235,s[20:21] offset:224\nM v[144:147],a[56:59],a[220:223],v[144:147]\nv_add_u32 v226,65536,v242\n" \
"ds_read_b64_tr_b16 a[212:213],v226 offset:0\nM v[148:151],a[60:63],a[220:223],v[148:151]\nds_read_b64_tr_b16 a[214:215],v226 offset:4096\nbuffer_load_dwordx2 v[112:113],v239,s[52:55],0 offen offset:0 sc1\ns_waitcnt lgkmcnt(6)\nM v[152:155],a[56:59],a[224:227],v[152:155]\nv_xor_b32 v227,32,v226\ns_waitcnt vmcnt(17)\n" \
"v_cmp_eq_u32 vcc,0,v248\nM v[156:159],a[60:63],a[224:227],v[156:159]\nds_read_b64_tr_b16 a[216:217],v227 offset:0\nds_read_b64_tr_b16 a[218:219],v227 offset:4096\nbuffer_load_dwordx2 v[114:115],v239,s[52:55],0 offen offset:2048 sc1\nM v[160:163],a[56:59],a[228:231],v[160:163]\ns_cbranch_vccnz .Lps11\n.Lpb11:\n" \
"buffer_load_dwordx4 a[64:67],v240,s[52:55],s65 offen offset:0 sc1\nM v[164:167],a[60:63],a[228:231],v[164:167]\nds_read_b64_tr_b16 a[220:221],v226 offset:512\nds_read_b64_tr_b16 a[222:223],v226 offset:4608\nbuffer_load_dwordx2 v[116:117],v239,s[52:55],s97 offen offset:0 sc1\nM v[168:171],a[56:59],a[232:235],v[168:171]\nds_read_b64_tr_b16 a[224:225],v227 offset:512\nds_read_b64_tr_b16 a[226:227],v227 offset:4608\n" \
"M v[172:175],a[60:63],a[232:235],v[172:175]\nbuffer_load_dwordx2 v[118:119],v239,s[52:55],s97 offen offset:2048 sc1\ns_waitcnt lgkmcnt(6)\nM v[176:179],a[56:59],a[236:239],v[176:179]\nbuffer_load_dwordx4 a[68:71],v240,s[52:55],s65 offen offset:1024 sc1\nM v[180:183],a[60:63],a[236:239],v[180:183]\nds_read_b64_tr_b16 a[228:229],v226 offset:1024\nds_read_b64_tr_b16 a[230:231],v226 offset:5120\n" \
"M v[184:187],a[56:59],a[240:243],v[184:187]\nds_read_b64_tr_b16 a[232:233],v227 offset:1024\nds_read_b64_tr_b16 a[234:235],v227 offset:5120\nM v[188:191],a[60:63],a[240:243],v[188:191]\nds_read_b64_tr_b16 a[236:237],v226 offset:1536\nds_read_b64_tr_b16 a[238:239],v226 offset:5632\nM v[128:131],a[160:163],a[212:215],v[128:131]\nbuffer_load_dwordx4 a[72:75],v240,s[52:55],s65 offen offset:2048 sc1\n" \
"M v[132:135],a[164:167],a[212:215],v[132:135]\nds_read_b64_tr_b16 a[240:241],v227 offset:1536\ns_waitcnt lgkmcnt(7)\nM v[136:139],a[160:163],a[216:219],v[136:139]\nds_read_b64_tr_b16 a[242:243],v227 offset:5632\nds_read_b64_tr_b16 a[212:213],v226 offset:8192\nM v[140:143],a[164:167],a[216:219],v[140:143]\nds_read_b64_tr_b16 a[214:215],v226 offset:12288\n" \
"ds_read_b64_tr_b16 a[216:217],v227 offset:8192\nM v[144:147],a[160:163],a[220:223],v[144:147]\nbuffer_load_dwordx4 a[76:79],v240,s[52:55],s65 offen offset:3072 sc1\nM v[148:151],a[164:167],a[220:223],v[148:151]\nds_read_b64_tr_b16 a[218:219],v227 offset:12288\ns_add_u32 s65,s65,4096\nM v[152:155],a[160:163],a[224:227],v[152:155]\nds_read_b64_tr_b16 a[220:221],v226 offset:8704\n" \
"ds_read_b64_tr_b16 a[222:223],v226 offset:12800\nM v[156:159],a[164:167],a[224:227],v[156:159]\nds_read_b64_tr_b16 a[224:225],v227 offset:8704\ns_waitcnt lgkmcnt(9)\nM v[160:163],a[160:163],a[228:231],v[160:163]\nbuffer_load_dwordx4 a[80:83],v240,s[52:55],s65 offen offset:0 sc1\nM v[164:167],a[164:167],a[228:231],v[164:167]\nds_read_b64_tr_b16 a[226:227],v227 offset:12800\n" \
"ds_read_b64_tr_b16 a[228:229],v226 offset:9216\nM v[168:171],a[160:163],a[232:235],v[168:171]\nds_read_b64_tr_b16 a[230:231],v226 offset:13312\nv_lshrrev_b32 v235,4,v249\nM v[172:175],a[164:167],a[232:235],v[172:175]\nds_read_b64_tr_b16 a[232:233],v227 offset:9216\nds_read_b64_tr_b16 a[234:235],v227 offset:13312\nM v[176:179],a[160:163],a[236:239],v[176:179]\n" \
"buffer_load_dwordx4 a[84:87],v240,s[52:55],s65 offen offset:1024 sc1\nM v[180:183],a[164:167],a[236:239],v[180:183]\nds_read_b64_tr_b16 a[236:237],v226 offset:9728\ns_waitcnt lgkmcnt(8)\nM v[184:187],a[160:163],a[240:243],v[184:187]\nds_read_b64_tr_b16 a[238:239],v226 offset:13824\nv_cmp_eq_u32 vcc,v234,v235\nM v[188:191],a[164:167],a[240:243],v[188:191]\n" \
"ds_read_b64_tr_b16 a[240:241],v227 offset:9728\nds_read_b64_tr_b16 a[242:243],v227 offset:13824\nM v[128:131],a[168:171],a[212:215],v[128:131]\nbuffer_load_dwordx4 a[88:91],v240,s[52:55],s65 offen offset:2048 sc1\nM v[132:135],a[172:175],a[212:215],v[132:135]\nds_read_b64_tr_b16 a[212:213],v226 offset:32768\nds_read_b64_tr_b16 a[214:215],v226 offset:36864\nM v[136:139],a[168:171],a[216:219],v[136:139]\n" \
"v_and_b32 v234,1,v249\nv_lshlrev_b32 v234,4,v234\nM v[140:143],a[172:175],a[216:219],v[140:143]\nds_read_b64_tr_b16 a[216:217],v227 offset:32768\ns_waitcnt lgkmcnt(7)\nM v[144:147],a[168:171],a[220:223],v[144:147]\nbuffer_load_dwordx4 a[92:95],v240,s[52:55],s65 offen offset:3072 sc1\nM v[148:151],a[172:175],a[220:223],v[148:151]\n" \
"ds_read_b64_tr_b16 a[218:219],v227 offset:36864\ns_add_u32 s65,s11,s67\nM v[152:155],a[168:171],a[224:227],v[152:155]\nds_read_b64_tr_b16 a[220:221],v226 offset:33280\nds_read_b64_tr_b16 a[222:223],v226 offset:37376\nM v[156:159],a[172:175],a[224:227],v[156:159]\nds_read_b64_tr_b16 a[224:225],v227 offset:33280\nds_read_b64_tr_b16 a[226:227],v227 offset:37376\n" \
"M v[160:163],a[168:171],a[228:231],v[160:163]\nbuffer_load_dwordx4 a[96:99],v240,s[52:55],s65 offen offset:0 sc1\nM v[164:167],a[172:175],a[228:231],v[164:167]\nds_read_b64_tr_b16 a[228:229],v226 offset:33792\nds_read_b64_tr_b16 a[230:231],v226 offset:37888\nM v[168:171],a[168:171],a[232:235],v[168:171]\nv_mov_b32 v235,0x3f80\nv_lshlrev_b32 v234,v234,v235\n" \
"M v[172:175],a[172:175],a[232:235],v[172:175]\nds_read_b64_tr_b16 a[232:233],v227 offset:33792\ns_waitcnt lgkmcnt(8)\nM v[176:179],a[168:171],a[236:239],v[176:179]\nbuffer_load_dwordx4 a[100:103],v240,s[52:55],s65 offen offset:1024 sc1\nM v[180:183],a[172:175],a[236:239],v[180:183]\nds_read_b64_tr_b16 a[234:235],v227 offset:37888\nds_read_b64_tr_b16 a[236:237],v226 offset:34304\n" \
"M v[184:187],a[168:171],a[240:243],v[184:187]\nds_read_b64_tr_b16 a[238:239],v226 offset:38400\nv_cndmask_b32 v234,0,v234,vcc\nM v[188:191],a[172:175],a[240:243],v[188:191]\nds_read_b64_tr_b16 a[240:241],v227 offset:34304\nds_read_b64_tr_b16 a[242:243],v227 offset:38400\nM v[128:131],a[176:179],a[212:215],v[128:131]\nbuffer_load_dwordx4 a[104:107],v240,s[52:55],s65 offen offset:2048 sc1\n" \
"M v[132:135],a[180:183],a[212:215],v[132:135]\nds_read_b64_tr_b16 a[212:213],v226 offset:40960\ns_waitcnt lgkmcnt(7)\nM v[136:139],a[176:179],a[216:219],v[136:139]\nds_read_b64_tr_b16 a[214:215],v226 offset:45056\nv_and_b32 v235,2,v249\nM v[140:143],a[180:183],a[216:219],v[140:143]\nds_read_b64_tr_b16 a[216:217],v227 offset:40960\n" \
"ds_read_b64_tr_b16 a[218:219],v227 offset:45056\nM v[144:147],a[176:179],a[220:223],v[144:147]\nbuffer_load_dwordx4 a[108:111],v240,s[52:55],s65 offen offset:3072 sc1\nM v[148:151],a[180:183],a[220:223],v[148:151]\ns_add_u32 s65,s65,4096\nds_read_b64_tr_b16 a[220:221],v226 offset:41472\nM v[152:155],a[176:179],a[224:227],v[152:155]\nds_read_b64_tr_b16 a[222:223],v226 offset:45568\n" \
"v_cmp_eq_u32 vcc,0,v235\nM v[156:159],a[180:183],a[224:227],v[156:159]\nds_read_b64_tr_b16 a[224:225],v227 offset:41472\nds_read_b64_tr_b16 a[226:227],v227 offset:45568\nM v[160:163],a[176:179],a[228:231],v[160:163]\nbuffer_load_dwordx4 a[112:115],v240,s[52:55],s65 offen offset:0 sc1\nM v[164:167],a[180:183],a[228:231],v[164:167]\nds_read_b64_tr_b16 a[228:229],v226 offset:41984\n" \
"s_waitcnt lgkmcnt(7)\nM v[168:171],a[176:179],a[232:235],v[168:171]\nds_read_b64_tr_b16 a[230:231],v226 offset:46080\nv_cndmask_b32 v235,0,v234,vcc\nM v[172:175],a[180:183],a[232:235],v[172:175]\nds_read_b64_tr_b16 a[232:233],v227 offset:41984\nds_read_b64_tr_b16 a[234:235],v227 offset:46080\nM v[176:179],a[176:179],a[236:239],v[176:179]\n" \
"buffer_load_dwordx4 a[116:119],v240,s[52:55],s65 offen offset:1024 sc1\nM v[180:183],a[180:183],a[236:239],v[180:183]\nds_read_b64_tr_b16 a[236:237],v226 offset:42496\nds_read_b64_tr_b16 a[238:239],v226 offset:46592\nM v[184:187],a[176:179],a[240:243],v[184:187]\nv_add_u32 v226,0,v242\nv_xor_b32 v234,v234,v235\nM v[188:191],a[180:183],a[240:243],v[188:191]\n" \
"ds_read_b64_tr_b16 a[240:241],v227 offset:42496\nds_read_b64_tr_b16 a[242:243],v227 offset:46592\nM v[128:131],a[184:187],a[212:215],v[128:131]\nbuffer_load_dwordx4 a[120:123],v240,s[52:55],s65 offen offset:2048 sc1\nM v[132:135],a[188:191],a[212:215],v[132:135]\nds_read_b64_tr_b16 a[212:213],v226 offset:0\ns_waitcnt lgkmcnt(7)\nM v[136:139],a[184:187],a[216:219],v[136:139]\n" \
"ds_read_b64_tr_b16 a[214:215],v226 offset:4096\nv_xor_b32 v227,32,v226\nM v[140:143],a[188:191],a[216:219],v[140:143]\nds_read_b64_tr_b16 a[216:217],v227 offset:0\nds_read_b64_tr_b16 a[218:219],v227 offset:4096\nM v[144:147],a[184:187],a[220:223],v[144:147]\nbuffer_load_dwordx4 a[124:127],v240,s[52:55],s65 offen offset:3072 sc1\nM v[148:151],a[188:191],a[220:223],v[148:151]\n" \
"s_add_u32 s65,s14,s67\nds_read_b64_tr_b16 a[220:221],v226 offset:512\nM v[152:155],a[184:187],a[224:227],v[152:155]\nds_read_b64_tr_b16 a[222:223],v226 offset:4608\nv_cvt_pk_bf16_f32 v192,v128,v136\nM v[156:159],a[188:191],a[224:227],v[156:159]\nds_read_b64_tr_b16 a[224:225],v227 offset:512\nds_read_b64_tr_b16 a[226:227],v227 offset:4608\n" \
"M v[160:163],a[184:187],a[228:231],v[160:163]\nbuffer_load_dwordx4 a[128:131],v240,s[52:55],s65 offen offset:0 sc1\nM v[164:167],a[188:191],a[228:231],v[164:167]\nds_read_b64_tr_b16 a[228:229],v226 offset:1024\ns_waitcnt lgkmcnt(7)\nM v[168:171],a[184:187],a[232:235],v[168:171]\nds_read_b64_tr_b16 a[230:231],v226 offset:5120\nv_cvt_pk_bf16_f32 v193,v144,v152\n" \
"M v[172:175],a[188:191],a[232:235],v[172:175]\nds_read_b64_tr_b16 a[232:233],v227 offset:1024\nds_read_b64_tr_b16 a[234:235],v227 offset:5120\nM v[176:179],a[184:187],a[236:239],v[176:179]\nbuffer_load_dwordx4 a[132:135],v240,s[52:55],s65 offen offset:1024 sc1\nM v[180:183],a[188:191],a[236:239],v[180:183]\nds_read_b64_tr_b16 a[236:237],v226 offset:1536\nds_read_b64_tr_b16 a[238:239],v226 offset:5632\n" \
"M v[184:187],a[184:187],a[240:243],v[184:187]\nv_cvt_pk_bf16_f32 v194,v160,v168\nv_min_u32 v192,-2,v192\nM v[188:191],a[188:191],a[240:243],v[188:191]\nds_read_b64_tr_b16 a[240:241],v227 offset:1536\ns_waitcnt vmcnt(15)\nM v[8:11],a[64:67],a[212:215],v[8:11]\nbuffer_load_dwordx4 a[136:139],v240,s[52:55],s65 offen offset:2048 sc1\n" \
"M v[12:15],a[68:71],a[212:215],v[12:15]\nds_read_b64_tr_b16 a[242:243],v227 offset:5632\ns_waitcnt lgkmcnt(6)\nM v[24:27],a[64:67],a[216:219],v[24:27]\nds_read_b64_tr_b16 a[212:213],v226 offset:8192\nds_read_b64_tr_b16 a[214:215],v226 offset:12288\nM v[28:31],a[68:71],a[216:219],v[28:31]\nds_read_b64_tr_b16 a[216:217],v227 offset:8192\n" \
"ds_read_b64_tr_b16 a[218:219],v227 offset:12288\nM v[40:43],a[64:67],a[220:223],v[40:43]\nbuffer_load_dwordx4 a[140:143],v240,s[52:55],s65 offen offset:3072 sc1\nM v[44:47],a[68:71],a[220:223],v[44:47]\ns_add_u32 s65,s65,4096\nds_read_b64_tr_b16 a[220:221],v226 offset:8704\nM v[56:59],a[64:67],a[224:227],v[56:59]\nds_read_b64_tr_b16 a[222:223],v226 offset:12800\n" \
"v_cvt_pk_bf16_f32 v195,v176,v184\nM v[60:63],a[68:71],a[224:227],v[60:63]\nds_read_b64_tr_b16 a[224:225],v227 offset:8704\nds_read_b64_tr_b16 a[226:227],v227 offset:12800\nM v[72:75],a[64:67],a[228:231],v[72:75]\nbuffer_load_dwordx4 a[144:147],v240,s[52:55],s65 offen offset:0 sc1\nM v[76:79],a[68:71],a[228:231],v[76:79]\nds_read_b64_tr_b16 a[228:229],v226 offset:9216\n" \
"s_waitcnt lgkmcnt(8)\nM v[88:91],a[64:67],a[232:235],v[88:91]\nds_read_b64_tr_b16 a[230:231],v226 offset:13312\nv_min_u32 v193,-2,v193\nM v[92:95],a[68:71],a[232:235],v[92:95]\nv_min_u32 v194,-2,v194\nv_min_u32 v195,-2,v195\nM v[104:107],a[64:67],a[236:239],v[104:107]\n" \
"buffer_load_dwordx4 a[148:151],v240,s[52:55],s65 offen offset:1024 sc1\nM v[108:111],a[68:71],a[236:239],v[108:111]\nds_read_b64_tr_b16 a[232:233],v227 offset:9216\nds_read_b64_tr_b16 a[234:235],v227 offset:13312\nM v[120:123],a[64:67],a[240:243],v[120:123]\nbuffer_store_dwordx4 v[192:195],v253,s[52:55],s57 offen offset:0 sc1\nM v[124:127],a[68:71],a[240:243],v[124:127]\nds_read_b64_tr_b16 a[236:237],v226 offset:9728\n" \
"s_waitcnt lgkmcnt(5)\nM v[8:11],a[72:75],a[212:215],v[8:11]\nbuffer_load_dwordx4 a[152:155],v240,s[52:55],s65 offen offset:2048 sc1\ns_waitcnt vmcnt(16)\nM v[12:15],a[76:79],a[212:215],v[12:15]\nds_read_b64_tr_b16 a[238:239],v226 offset:13824\nds_read_b64_tr_b16 a[240:241],v227 offset:9728\nM v[24:27],a[72:75],a[216:219],v[24:27]\n" \
"ds_read_b64_tr_b16 a[242:243],v227 offset:13824\nds_read_b64_tr_b16 a[212:213],v226 offset:16384\nM v[28:31],a[76:79],a[216:219],v[28:31]\nds_read_b64_tr_b16 a[214:215],v226 offset:20480\nds_read_b64_tr_b16 a[216:217],v227 offset:16384\nM v[40:43],a[72:75],a[220:223],v[40:43]\nbuffer_load_dwordx4 a[156:159],v240,s[52:55],s65 offen offset:3072 sc1\nM v[44:47],a[76:79],a[220:223],v[44:47]\n" \
"ds_read_b64_tr_b16 a[218:219],v227 offset:20480\nv_cvt_pk_bf16_f32 v196,v129,v137\nM v[56:59],a[72:75],a[224:227],v[56:59]\nv_cvt_pk_bf16_f32 v197,v145,v153\nv_cvt_pk_bf16_f32 v198,v161,v169\nM v[60:63],a[76:79],a[224:227],v[60:63]\nv_cvt_pk_bf16_f32 v199,v177,v185\ns_waitcnt lgkmcnt(7)\n" \
"M v[72:75],a[72:75],a[228:231],v[72:75]\nds_read_b64_tr_b16 a[220:221],v226 offset:16896\nds_read_b64_tr_b16 a[222:223],v226 offset:20992\nM v[76:79],a[76:79],a[228:231],v[76:79]\nv_min_u32 v196,-2,v196\nv_min_u32 v197,-2,v197\nM v[88:91],a[72:75],a[232:235],v[88:91]\nv_min_u32 v198,-2,v198\n" \
"v_min_u32 v199,-2,v199\nbuffer_store_dwordx4 v[196:199],v253,s[52:55],s57 offen offset:1024 sc1\nM v[92:95],a[76:79],a[232:235],v[92:95]\nds_read_b64_tr_b16 a[224:225],v227 offset:16896\nds_read_b64_tr_b16 a[226:227],v227 offset:20992\ns_waitcnt lgkmcnt(5)\nM v[104:107],a[72:75],a[236:239],v[104:107]\nds_read_b64_tr_b16 a[228:229],v226 offset:17408\n" \
"ds_read_b64_tr_b16 a[230:231],v226 offset:21504\nM v[108:111],a[76:79],a[236:239],v[108:111]\nds_read_b64_tr_b16 a[232:233],v227 offset:17408\nds_read_b64_tr_b16 a[234:235],v227 offset:21504\nM v[120:123],a[72:75],a[240:243],v[120:123]\nds_read_b64_tr_b16 a[236:237],v226 offset:17920\nds_read_b64_tr_b16 a[238:239],v226 offset:22016\nM v[124:127],a[76:79],a[240:243],v[124:127]\n" \
"ds_read_b64_tr_b16 a[240:241],v227 offset:17920\nds_read_b64_tr_b16 a[242:243],v227 offset:22016\nM v[8:11],a[80:83],a[212:215],v[8:11]\nv_cvt_pk_bf16_f32 v200,v130,v138\nv_cvt_pk_bf16_f32 v201,v146,v154\nM v[12:15],a[84:87],a[212:215],v[12:15]\nv_cvt_pk_bf16_f32 v202,v162,v170\ns_waitcnt lgkmcnt(10)\n" \
"M v[24:27],a[80:83],a[216:219],v[24:27]\nv_cvt_pk_bf16_f32 v203,v178,v186\nds_read_b64_tr_b16 a[212:213],v226 offset:24576\nM v[28:31],a[84:87],a[216:219],v[28:31]\nds_read_b64_tr_b16 a[214:215],v226 offset:28672\nv_min_u32 v200,-2,v200\nM v[40:43],a[80:83],a[220:223],v[40:43]\nv_min_u32 v201,-2,v201\n" \
"v_min_u32 v202,-2,v202\nM v[44:47],a[84:87],a[220:223],v[44:47]\nv_min_u32 v203,-2,v203\ns_waitcnt lgkmcnt(5)\nM v[56:59],a[80:83],a[224:227],v[56:59]\nbuffer_store_dwordx4 v[200:203],v253,s[52:55],s57 offen offset:2048 sc1\nM v[60:63],a[84:87],a[224:227],v[60:63]\nds_read_b64_tr_b16 a[216:217],v227 offset:24576\n" \
"ds_read_b64_tr_b16 a[218:219],v227 offset:28672\nM v[72:75],a[80:83],a[228:231],v[72:75]\nds_read_b64_tr_b16 a[220:221],v226 offset:25088\nds_read_b64_tr_b16 a[222:223],v226 offset:29184\nM v[76:79],a[84:87],a[228:231],v[76:79]\nds_read_b64_tr_b16 a[224:225],v227 offset:25088\nds_read_b64_tr_b16 a[226:227],v227 offset:29184\nM v[88:91],a[80:83],a[232:235],v[88:91]\n" \
"ds_read_b64_tr_b16 a[228:229],v226 offset:25600\nds_read_b64_tr_b16 a[230:231],v226 offset:29696\nM v[92:95],a[84:87],a[232:235],v[92:95]\nds_read_b64_tr_b16 a[232:233],v227 offset:25600\ns_waitcnt lgkmcnt(9)\nM v[104:107],a[80:83],a[236:239],v[104:107]\nds_read_b64_tr_b16 a[234:235],v227 offset:29696\nv_cvt_pk_bf16_f32 v204,v131,v139\n" \
"M v[108:111],a[84:87],a[236:239],v[108:111]\nv_cvt_pk_bf16_f32 v205,v147,v155\nv_cvt_pk_bf16_f32 v206,v163,v171\nM v[120:123],a[80:83],a[240:243],v[120:123]\nv_cvt_pk_bf16_f32 v207,v179,v187\nds_read_b64_tr_b16 a[236:237],v226 offset:26112\nM v[124:127],a[84:87],a[240:243],v[124:127]\nds_read_b64_tr_b16 a[238:239],v226 offset:30208\n" \
"v_min_u32 v204,-2,v204\nM v[8:11],a[88:91],a[212:215],v[8:11]\nv_min_u32 v205,-2,v205\nv_min_u32 v206,-2,v206\nM v[12:15],a[92:95],a[212:215],v[12:15]\nv_min_u32 v207,-2,v207\ns_waitcnt lgkmcnt(5)\nM v[24:27],a[88:91],a[216:219],v[24:27]\n" \
"buffer_store_dwordx4 v[204:207],v253,s[52:55],s57 offen offset:3072 sc1\nM v[28:31],a[92:95],a[216:219],v[28:31]\nds_read_b64_tr_b16 a[240:241],v227 offset:26112\nds_read_b64_tr_b16 a[242:243],v227 offset:30208\nM v[40:43],a[88:91],a[220:223],v[40:43]\nds_read_b64_tr_b16 a[212:213],v226 offset:32768\nds_read_b64_tr_b16 a[214:215],v226 offset:36864\nM v[44:47],a[92:95],a[220:223],v[44:47]\n" \
"ds_read_b64_tr_b16 a[216:217],v227 offset:32768\nds_read_b64_tr_b16 a[218:219],v227 offset:36864\nM v[56:59],a[88:91],a[224:227],v[56:59]\nds_read_b64_tr_b16 a[220:221],v226 offset:33280\nds_read_b64_tr_b16 a[222:223],v226 offset:37376\nM v[60:63],a[92:95],a[224:227],v[60:63]\nds_read_b64_tr_b16 a[224:225],v227 offset:33280\ns_waitcnt lgkmcnt(9)\n" \
"M v[72:75],a[88:91],a[228:231],v[72:75]\nds_read_b64_tr_b16 a[226:227],v227 offset:37376\nv_cvt_pk_bf16_f32 v208,v132,v140\nM v[76:79],a[92:95],a[228:231],v[76:79]\nv_cvt_pk_bf16_f32 v209,v148,v156\nv_cvt_pk_bf16_f32 v210,v164,v172\nM v[88:91],a[88:91],a[232:235],v[88:91]\nv_cvt_pk_bf16_f32 v211,v180,v188\n" \
"ds_read_b64_tr_b16 a[228:229],v226 offset:33792\nM v[92:95],a[92:95],a[232:235],v[92:95]\nds_read_b64_tr_b16 a[230:231],v226 offset:37888\nv_min_u32 v208,-2,v208\nM v[104:107],a[88:91],a[236:239],v[104:107]\nv_min_u32 v209,-2,v209\nv_min_u32 v210,-2,v210\nM v[108:111],a[92:95],a[236:239],v[108:111]\n" \
"v_min_u32 v211,-2,v211\ns_waitcnt lgkmcnt(5)\nM v[120:123],a[88:91],a[240:243],v[120:123]\nbuffer_store_dwordx4 v[208:211],v253,s[52:55],s38 offen offset:0 sc1\nM v[124:127],a[92:95],a[240:243],v[124:127]\nds_read_b64_tr_b16 a[232:233],v227 offset:33792\nds_read_b64_tr_b16 a[234:235],v227 offset:37888\ns_waitcnt vmcnt(9)\n" \
"M v[8:11],a[96:99],a[212:215],v[8:11]\nds_read_b64_tr_b16 a[236:237],v226 offset:34304\nds_read_b64_tr_b16 a[238:239],v226 offset:38400\nM v[12:15],a[100:103],a[212:215],v[12:15]\nds_read_b64_tr_b16 a[240:241],v227 offset:34304\nds_read_b64_tr_b16 a[242:243],v227 offset:38400\nM v[24:27],a[96:99],a[216:219],v[24:27]\nds_read_b64_tr_b16 a[212:213],v226 offset:40960\n" \
"ds_read_b64_tr_b16 a[214:215],v226 offset:45056\nM v[28:31],a[100:103],a[216:219],v[28:31]\nds_read_b64_tr_b16 a[216:217],v227 offset:40960\ns_waitcnt lgkmcnt(9)\nM v[40:43],a[96:99],a[220:223],v[40:43]\nds_read_b64_tr_b16 a[218:219],v227 offset:45056\nv_cvt_pk_bf16_f32 v212,v133,v141\nM v[44:47],a[100:103],a[220:223],v[44:47]\n" \
"v_cvt_pk_bf16_f32 v213,v149,v157\nv_cvt_pk_bf16_f32 v214,v165,v173\nM v[56:59],a[96:99],a[224:227],v[56:59]\nbuffer_load_dwordx4 v[0:3],v238,s[52:55],0 offen offset:0 sc1\nM v[60:63],a[100:103],a[224:227],v[60:63]\nv_cvt_pk_bf16_f32 v215,v181,v189\nds_read_b64_tr_b16 a[220:221],v226 offset:41472\nM v[72:75],a[96:99],a[228:231],v[72:75]\n" \
"ds_read_b64_tr_b16 a[222:223],v226 offset:45568\nv_min_u32 v212,-2,v212\nM v[76:79],a[100:103],a[228:231],v[76:79]\nv_min_u32 v213,-2,v213\ns_waitcnt lgkmcnt(5)\nM v[88:91],a[96:99],a[232:235],v[88:91]\nv_min_u32 v214,-2,v214\nv_min_u32 v215,-2,v215\n" \
"buffer_store_dwordx4 v[212:215],v253,s[52:55],s38 offen offset:1024 sc1\nM v[92:95],a[100:103],a[232:235],v[92:95]\nds_read_b64_tr_b16 a[224:225],v227 offset:41472\nds_read_b64_tr_b16 a[226:227],v227 offset:45568\nM v[104:107],a[96:99],a[236:239],v[104:107]\nbuffer_load_dwordx4 v[4:7],v238,s[52:55],0 offen offset:16 sc1\nM v[108:111],a[100:103],a[236:239],v[108:111]\nds_read_b64_tr_b16 a[228:229],v226 offset:41984\n" \
"ds_read_b64_tr_b16 a[230:231],v226 offset:46080\nM v[120:123],a[96:99],a[240:243],v[120:123]\nds_read_b64_tr_b16 a[232:233],v227 offset:41984\nds_read_b64_tr_b16 a[234:235],v227 offset:46080\nM v[124:127],a[100:103],a[240:243],v[124:127]\nds_read_b64_tr_b16 a[236:237],v226 offset:42496\ns_waitcnt lgkmcnt(7)\nM v[8:11],a[104:107],a[212:215],v[8:11]\n" \
"ds_read_b64_tr_b16 a[238:239],v226 offset:46592\nds_read_b64_tr_b16 a[240:241],v227 offset:42496\nM v[12:15],a[108:111],a[212:215],v[12:15]\nds_read_b64_tr_b16 a[242:243],v227 offset:46592\nv_cvt_pk_bf16_f32 v216,v134,v142\nM v[24:27],a[104:107],a[216:219],v[24:27]\nbuffer_load_dwordx4 v[16:19],v238,s[52:55],0 offen offset:2048 sc1\nM v[28:31],a[108:111],a[216:219],v[28:31]\n" \
"v_cvt_pk_bf16_f32 v217,v150,v158\nv_cvt_pk_bf16_f32 v218,v166,v174\nM v[40:43],a[104:107],a[220:223],v[40:43]\nv_cvt_pk_bf16_f32 v219,v182,v190\nds_read_b64_tr_b16 a[212:213],v226 offset:49152\nM v[44:47],a[108:111],a[220:223],v[44:47]\nds_read_b64_tr_b16 a[214:215],v226 offset:53248\ns_waitcnt lgkmcnt(7)\n" \
"M v[56:59],a[104:107],a[224:227],v[56:59]\nv_min_u32 v216,-2,v216\nv_min_u32 v217,-2,v217\nv_min_u32 v218,-2,v218\nv_min_u32 v219,-2,v219\nbuffer_store_dwordx4 v[216:219],v253,s[52:55],s38 offen offset:2048 sc1\nM v[60:63],a[108:111],a[224:227],v[60:63]\nds_read_b64_tr_b16 a[216:217],v227 offset:49152\n" \
"ds_read_b64_tr_b16 a[218:219],v227 offset:53248\nM v[72:75],a[104:107],a[228:231],v[72:75]\nbuffer_load_dwordx4 v[20:23],v238,s[52:55],0 offen offset:2064 sc1\nM v[76:79],a[108:111],a[228:231],v[76:79]\nds_read_b64_tr_b16 a[220:221],v226 offset:49664\nds_read_b64_tr_b16 a[222:223],v226 offset:53760\ns_waitcnt lgkmcnt(6)\nM v[88:91],a[104:107],a[232:235],v[88:91]\n" \
"ds_read_b64_tr_b16 a[224:225],v227 offset:49664\nds_read_b64_tr_b16 a[226:227],v227 offset:53760\nM v[92:95],a[108:111],a[232:235],v[92:95]\nds_read_b64_tr_b16 a[228:229],v226 offset:50176\nds_read_b64_tr_b16 a[230:231],v226 offset:54272\nM v[104:107],a[104:107],a[236:239],v[104:107]\nds_read_b64_tr_b16 a[232:233],v227 offset:50176\nds_read_b64_tr_b16 a[234:235],v227 offset:54272\n" \
"M v[108:111],a[108:111],a[236:239],v[108:111]\nv_cvt_pk_bf16_f32 v220,v135,v143\nv_cvt_pk_bf16_f32 v221,v151,v159\nM v[120:123],a[104:107],a[240:243],v[120:123]\nbuffer_load_dwordx4 v[32:35],v238,s[52:55],s97 offen offset:0 sc1\nM v[124:127],a[108:111],a[240:243],v[124:127]\nv_cvt_pk_bf16_f32 v222,v167,v175\ns_waitcnt lgkmcnt(8)\n" \
"M v[8:11],a[112:115],a[212:215],v[8:11]\nv_cvt_pk_bf16_f32 v223,v183,v191\nds_read_b64_tr_b16 a[236:237],v226 offset:50688\nM v[12:15],a[116:119],a[212:215],v[12:15]\nds_read_b64_tr_b16 a[238:239],v226 offset:54784\nv_min_u32 v220,-2,v220\nM v[24:27],a[112:115],a[216:219],v[24:27]\nv_min_u32 v221,-2,v221\n" \
"v_min_u32 v222,-2,v222\nv_min_u32 v223,-2,v223\nbuffer_store_dwordx4 v[220:223],v253,s[52:55],s38 offen offset:3072 sc1\nM v[28:31],a[116:119],a[216:219],v[28:31]\nds_read_b64_tr_b16 a[240:241],v227 offset:50688\nds_read_b64_tr_b16 a[242:243],v227 offset:54784\ns_waitcnt lgkmcnt(4)\nM v[40:43],a[112:115],a[220:223],v[40:43]\n" \
"buffer_load_dwordx4 v[36:39],v238,s[52:55],s97 offen offset:16 sc1\nM v[44:47],a[116:119],a[220:223],v[44:47]\nds_read_b64_tr_b16 a[212:213],v226 offset:57344\nds_read_b64_tr_b16 a[214:215],v226 offset:61440\nM v[56:59],a[112:115],a[224:227],v[56:59]\nds_read_b64_tr_b16 a[216:217],v227 offset:57344\nds_read_b64_tr_b16 a[218:219],v227 offset:61440\nM v[60:63],a[116:119],a[224:227],v[60:63]\n" \
"ds_read_b64_tr_b16 a[220:221],v226 offset:57856\nds_read_b64_tr_b16 a[222:223],v226 offset:61952\nM v[72:75],a[112:115],a[228:231],v[72:75]\nds_read_b64_tr_b16 a[224:225],v227 offset:57856\nds_read_b64_tr_b16 a[226:227],v227 offset:61952\nM v[76:79],a[116:119],a[228:231],v[76:79]\nds_read_b64_tr_b16 a[228:229],v226 offset:58368\nds_read_b64_tr_b16 a[230:231],v226 offset:62464\n" \
"M v[88:91],a[112:115],a[232:235],v[88:91]\nbuffer_load_dwordx4 v[48:51],v238,s[52:55],s97 offen offset:2048 sc1\nM v[92:95],a[116:119],a[232:235],v[92:95]\nds_read_b64_tr_b16 a[232:233],v227 offset:58368\ns_waitcnt lgkmcnt(11)\nM v[104:107],a[112:115],a[236:239],v[104:107]\nds_read_b64_tr_b16 a[234:235],v227 offset:62464\ns_add_u32 s38,s78,16384\n" \
"M v[108:111],a[116:119],a[236:239],v[108:111]\nds_read_b64_tr_b16 a[236:237],v226 offset:58880\nds_read_b64_tr_b16 a[238:239],v226 offset:62976\nM v[120:123],a[112:115],a[240:243],v[120:123]\nv_add_u32 v226,65536,v242\nv_lshlrev_b32 v64,16,v64\nM v[124:127],a[116:119],a[240:243],v[124:127]\nds_read_b64_tr_b16 a[240:241],v227 offset:58880\n" \
"s_waitcnt lgkmcnt(8)\nM v[8:11],a[120:123],a[212:215],v[8:11]\nbuffer_load_dwordx4 v[52:55],v238,s[52:55],s97 offen offset:2064 sc1\nM v[12:15],a[124:127],a[212:215],v[12:15]\nds_read_b64_tr_b16 a[242:243],v227 offset:62976\nds_read_b64_tr_b16 a[212:213],v226 offset:0\nM v[24:27],a[120:123],a[216:219],v[24:27]\nds_read_b64_tr_b16 a[214:215],v226 offset:4096\n" \
"v_xor_b32 v227,32,v226\nM v[28:31],a[124:127],a[216:219],v[28:31]\nds_read_b64_tr_b16 a[216:217],v227 offset:0\nds_read_b64_tr_b16 a[218:219],v227 offset:4096\nM v[40:43],a[120:123],a[220:223],v[40:43]\nv_lshlrev_b32 v65,16,v65\nv_lshlrev_b32 v66,16,v66\nM v[44:47],a[124:127],a[220:223],v[44:47]\n" \
"ds_read_b64_tr_b16 a[220:221],v226 offset:512\ns_waitcnt lgkmcnt(7)\nM v[56:59],a[120:123],a[224:227],v[56:59]\nds_read_b64_tr_b16 a[222:223],v226 offset:4608\nv_lshlrev_b32 v67,16,v67\nM v[60:63],a[124:127],a[224:227],v[60:63]\nds_read_b64_tr_b16 a[224:225],v227 offset:512\nds_read_b64_tr_b16 a[226:227],v227 offset:4608\n" \
"M v[72:75],a[120:123],a[228:231],v[72:75]\nv_lshlrev_b32 v68,16,v68\nv_lshlrev_b32 v69,16,v69\nM v[76:79],a[124:127],a[228:231],v[76:79]\nds_read_b64_tr_b16 a[228:229],v226 offset:1024\nds_read_b64_tr_b16 a[230:231],v226 offset:5120\nM v[88:91],a[120:123],a[232:235],v[88:91]\nv_lshlrev_b32 v70,16,v70\n" \
"v_lshlrev_b32 v71,16,v71\nM v[92:95],a[124:127],a[232:235],v[92:95]\nds_read_b64_tr_b16 a[232:233],v227 offset:1024\nds_read_b64_tr_b16 a[234:235],v227 offset:5120\nM v[104:107],a[120:123],a[236:239],v[104:107]\nv_lshlrev_b32 v80,16,v80\nv_lshlrev_b32 v81,16,v81\nM v[108:111],a[124:127],a[236:239],v[108:111]\n" \
"ds_read_b64_tr_b16 a[236:237],v226 offset:1536\ns_waitcnt lgkmcnt(7)\nM v[120:123],a[120:123],a[240:243],v[120:123]\nds_read_b64_tr_b16 a[238:239],v226 offset:5632\nv_lshlrev_b32 v82,16,v82\nM v[124:127],a[124:127],a[240:243],v[124:127]\nds_read_b64_tr_b16 a[240:241],v227 offset:1536\nds_read_b64_tr_b16 a[242:243],v227 offset:5632\n" \
"M v[8:11],a[128:131],a[212:215],v[8:11]\nv_lshlrev_b32 v83,16,v83\nv_lshlrev_b32 v84,16,v84\nM v[12:15],a[132:135],a[212:215],v[12:15]\nds_read_b64_tr_b16 a[212:213],v226 offset:8192\nds_read_b64_tr_b16 a[214:215],v226 offset:12288\nM v[24:27],a[128:131],a[216:219],v[24:27]\nv_lshlrev_b32 v85,16,v85\n" \
"v_lshlrev_b32 v86,16,v86\nM v[28:31],a[132:135],a[216:219],v[28:31]\nds_read_b64_tr_b16 a[216:217],v227 offset:8192\nds_read_b64_tr_b16 a[218:219],v227 offset:12288\nM v[40:43],a[128:131],a[220:223],v[40:43]\nv_lshlrev_b32 v87,16,v87\nv_lshlrev_b32 v96,16,v96\nM v[44:47],a[132:135],a[220:223],v[44:47]\n" \
"ds_read_b64_tr_b16 a[220:221],v226 offset:8704\ns_waitcnt lgkmcnt(7)\nM v[56:59],a[128:131],a[224:227],v[56:59]\nds_read_b64_tr_b16 a[222:223],v226 offset:12800\nv_lshlrev_b32 v97,16,v97\nM v[60:63],a[132:135],a[224:227],v[60:63]\nds_read_b64_tr_b16 a[224:225],v227 offset:8704\nds_read_b64_tr_b16 a[226:227],v227 offset:12800\n" \
"M v[72:75],a[128:131],a[228:231],v[72:75]\nv_lshlrev_b32 v98,16,v98\nv_lshlrev_b32 v99,16,v99\nM v[76:79],a[132:135],a[228:231],v[76:79]\nds_read_b64_tr_b16 a[228:229],v226 offset:9216\nds_read_b64_tr_b16 a[230:231],v226 offset:13312\nM v[88:91],a[128:131],a[232:235],v[88:91]\nv_lshlrev_b32 v100,16,v100\n" \
"v_lshlrev_b32 v101,16,v101\nM v[92:95],a[132:135],a[232:235],v[92:95]\nds_read_b64_tr_b16 a[232:233],v227 offset:9216\nds_read_b64_tr_b16 a[234:235],v227 offset:13312\nM v[104:107],a[128:131],a[236:239],v[104:107]\nv_lshlrev_b32 v102,16,v102\nv_lshlrev_b32 v103,16,v103\nM v[108:111],a[132:135],a[236:239],v[108:111]\n" \
"ds_read_b64_tr_b16 a[236:237],v226 offset:9728\ns_waitcnt lgkmcnt(7)\nM v[120:123],a[128:131],a[240:243],v[120:123]\nds_read_b64_tr_b16 a[238:239],v226 offset:13824\nv_accvgpr_write_b32 a192,v235\nM v[124:127],a[132:135],a[240:243],v[124:127]\nds_read_b64_tr_b16 a[240:241],v227 offset:9728\nds_read_b64_tr_b16 a[242:243],v227 offset:13824\n" \
"M v[8:11],a[136:139],a[212:215],v[8:11]\nv_accvgpr_write_b32 a193,v234\nv_accvgpr_write_b32 a198,v235\nM v[12:15],a[140:143],a[212:215],v[12:15]\nds_read_b64_tr_b16 a[212:213],v226 offset:32768\nds_read_b64_tr_b16 a[214:215],v226 offset:36864\nM v[24:27],a[136:139],a[216:219],v[24:27]\nv_accvgpr_write_b32 a199,v234\n" \
"M v[28:31],a[140:143],a[216:219],v[28:31]\nds_read_b64_tr_b16 a[216:217],v227 offset:32768\nds_read_b64_tr_b16 a[218:219],v227 offset:36864\nM v[40:43],a[136:139],a[220:223],v[40:43]\nM v[44:47],a[140:143],a[220:223],v[44:47]\nds_read_b64_tr_b16 a[220:221],v226 offset:33280\ns_waitcnt lgkmcnt(8)\nM v[56:59],a[136:139],a[224:227],v[56:59]\n" \
"ds_read_b64_tr_b16 a[222:223],v226 offset:37376\nM v[60:63],a[140:143],a[224:227],v[60:63]\nds_read_b64_tr_b16 a[224:225],v227 offset:33280\nds_read_b64_tr_b16 a[226:227],v227 offset:37376\nM v[72:75],a[136:139],a[228:231],v[72:75]\nM v[76:79],a[140:143],a[228:231],v[76:79]\nds_read_b64_tr_b16 a[228:229],v226 offset:33792\nds_read_b64_tr_b16 a[230:231],v226 offset:37888\n" \
"M v[88:91],a[136:139],a[232:235],v[88:91]\nM v[92:95],a[140:143],a[232:235],v[92:95]\nds_read_b64_tr_b16 a[232:233],v227 offset:33792\ns_waitcnt lgkmcnt(9)\nM v[104:107],a[136:139],a[236:239],v[104:107]\nds_read_b64_tr_b16 a[234:235],v227 offset:37888\nM v[108:111],a[140:143],a[236:239],v[108:111]\nds_read_b64_tr_b16 a[236:237],v226 offset:34304\n" \
"ds_read_b64_tr_b16 a[238:239],v226 offset:38400\nM v[120:123],a[136:139],a[240:243],v[120:123]\nM v[124:127],a[140:143],a[240:243],v[124:127]\nds_read_b64_tr_b16 a[240:241],v227 offset:34304\ns_waitcnt vmcnt(18)\nM v[8:11],a[144:147],a[212:215],v[8:11]\nds_read_b64_tr_b16 a[242:243],v227 offset:38400\nM v[12:15],a[148:151],a[212:215],v[12:15]\n" \
"ds_read_b64_tr_b16 a[212:213],v226 offset:40960\ns_waitcnt lgkmcnt(9)\nM v[24:27],a[144:147],a[216:219],v[24:27]\nds_read_b64_tr_b16 a[214:215],v226 offset:45056\nM v[28:31],a[148:151],a[216:219],v[28:31]\nds_read_b64_tr_b16 a[216:217],v227 offset:40960\nds_read_b64_tr_b16 a[218:219],v227 offset:45056\nM v[40:43],a[144:147],a[220:223],v[40:43]\n" \
"M v[44:47],a[148:151],a[220:223],v[44:47]\nds_read_b64_tr_b16 a[220:221],v226 offset:41472\nds_read_b64_tr_b16 a[222:223],v226 offset:45568\nM v[56:59],a[144:147],a[224:227],v[56:59]\nM v[60:63],a[148:151],a[224:227],v[60:63]\nds_read_b64_tr_b16 a[224:225],v227 offset:41472\ns_waitcnt lgkmcnt(9)\nM v[72:75],a[144:147],a[228:231],v[72:75]\n" \
"ds_read_b64_tr_b16 a[226:227],v227 offset:45568\nM v[76:79],a[148:151],a[228:231],v[76:79]\nds_read_b64_tr_b16 a[228:229],v226 offset:41984\nds_read_b64_tr_b16 a[230:231],v226 offset:46080\nM v[88:91],a[144:147],a[232:235],v[88:91]\nM v[92:95],a[148:151],a[232:235],v[92:95]\nds_read_b64_tr_b16 a[232:233],v227 offset:41984\nds_read_b64_tr_b16 a[234:235],v227 offset:46080\n" \
"M v[104:107],a[144:147],a[236:239],v[104:107]\nM v[108:111],a[148:151],a[236:239],v[108:111]\nds_read_b64_tr_b16 a[236:237],v226 offset:42496\ns_waitcnt lgkmcnt(9)\nM v[120:123],a[144:147],a[240:243],v[120:123]\nds_read_b64_tr_b16 a[238:239],v226 offset:46592\nM v[124:127],a[148:151],a[240:243],v[124:127]\nds_read_b64_tr_b16 a[240:241],v227 offset:42496\n" \
"s_waitcnt vmcnt(15)\nM v[8:11],a[152:155],a[212:215],v[8:11]\nds_read_b64_tr_b16 a[242:243],v227 offset:46592\nM v[12:15],a[156:159],a[212:215],v[12:15]\nM v[24:27],a[152:155],a[216:219],v[24:27]\nM v[28:31],a[156:159],a[216:219],v[28:31]\ns_waitcnt lgkmcnt(6)\nM v[40:43],a[152:155],a[220:223],v[40:43]\n" \
"M v[44:47],a[156:159],a[220:223],v[44:47]\nM v[56:59],a[152:155],a[224:227],v[56:59]\nM v[60:63],a[156:159],a[224:227],v[60:63]\nM v[72:75],a[152:155],a[228:231],v[72:75]\nM v[76:79],a[156:159],a[228:231],v[76:79]\nv_cvt_pk_bf16_f32 v192,v8,v24\ns_waitcnt lgkmcnt(3)\nM v[88:91],a[152:155],a[232:235],v[88:91]\n" \
"v_cvt_pk_bf16_f32 v196,v9,v25\nv_cvt_pk_bf16_f32 v200,v10,v26\nM v[92:95],a[156:159],a[232:235],v[92:95]\nv_cvt_pk_bf16_f32 v193,v40,v56\ns_waitcnt lgkmcnt(0)\nM v[104:107],a[152:155],a[236:239],v[104:107]\nv_cvt_pk_bf16_f32 v197,v41,v57\nv_cvt_pk_bf16_f32 v201,v42,v58\n" \
"M v[108:111],a[156:159],a[236:239],v[108:111]\nv_cvt_pk_bf16_f32 v194,v72,v88\nv_cvt_pk_bf16_f32 v198,v73,v89\nM v[120:123],a[152:155],a[240:243],v[120:123]\nv_cvt_pk_bf16_f32 v202,v74,v90\nv_cvt_pk_bf16_f32 v204,v11,v27\nv_cvt_pk_bf16_f32 v205,v43,v59\nv_cvt_pk_bf16_f32 v206,v75,v91\n" \
"v_cvt_pk_bf16_f32 v208,v12,v28\nv_cvt_pk_bf16_f32 v209,v44,v60\nv_cvt_pk_bf16_f32 v210,v76,v92\nv_cvt_pk_bf16_f32 v212,v13,v29\nv_cvt_pk_bf16_f32 v195,v104,v120\nv_cvt_pk_bf16_f32 v199,v105,v121\nv_cvt_pk_bf16_f32 v203,v106,v122\nv_cvt_pk_bf16_f32 v207,v107,v123\n" \
"v_cvt_pk_bf16_f32 v213,v45,v61\nv_cvt_pk_bf16_f32 v214,v77,v93\nv_cvt_pk_bf16_f32 v216,v14,v30\nv_cvt_pk_bf16_f32 v217,v46,v62\nv_cvt_pk_bf16_f32 v218,v78,v94\nv_cvt_pk_bf16_f32 v220,v15,v31\nv_cvt_pk_bf16_f32 v221,v47,v63\nv_cvt_pk_bf16_f32 v222,v79,v95\n" \
"M v[124:127],a[156:159],a[240:243],v[124:127]\nv_min_u32 v192,-2,v192\nv_min_u32 v193,-2,v193\nv_min_u32 v194,-2,v194\nv_min_u32 v195,-2,v195\nv_min_u32 v196,-2,v196\nv_min_u32 v197,-2,v197\nv_min_u32 v198,-2,v198\n" \
"v_min_u32 v199,-2,v199\nv_cvt_pk_bf16_f32 v211,v108,v124\nv_cvt_pk_bf16_f32 v215,v109,v125\nv_cvt_pk_bf16_f32 v219,v110,v126\nv_cvt_pk_bf16_f32 v223,v111,v127\nv_min_u32 v200,-2,v200\nv_min_u32 v201,-2,v201\nv_min_u32 v202,-2,v202\n" \
"v_min_u32 v203,-2,v203\nv_min_u32 v204,-2,v204\nv_min_u32 v205,-2,v205\nv_min_u32 v206,-2,v206\nv_min_u32 v207,-2,v207\nv_min_u32 v208,-2,v208\nv_min_u32 v209,-2,v209\nv_min_u32 v210,-2,v210\n" \
"v_min_u32 v211,-2,v211\nv_min_u32 v212,-2,v212\nv_min_u32 v213,-2,v213\nv_min_u32 v214,-2,v214\nv_min_u32 v215,-2,v215\nv_min_u32 v216,-2,v216\nv_min_u32 v217,-2,v217\nv_min_u32 v218,-2,v218\n" \
"v_min_u32 v219,-2,v219\nv_min_u32 v220,-2,v220\nv_min_u32 v221,-2,v221\nv_min_u32 v222,-2,v222\nv_min_u32 v223,-2,v223\nbuffer_store_dwordx4 v[192:195],v253,s[52:55],s78 offen offset:0 sc1\nbuffer_store_dwordx4 v[196:199],v253,s[52:55],s78 offen offset:1024 sc1\nbuffer_store_dwordx4 v[200:203],v253,s[52:55],s78 offen offset:2048 sc1\n" \
"buffer_store_dwordx4 v[204:207],v253,s[52:55],s78 offen offset:3072 sc1\nbuffer_store_dwordx4 v[208:211],v253,s[52:55],s38 offen offset:0 sc1\nbuffer_store_dwordx4 v[212:215],v253,s[52:55],s38 offen offset:1024 sc1\nbuffer_store_dwordx4 v[216:219],v253,s[52:55],s38 offen offset:2048 sc1\nbuffer_store_dwordx4 v[220:223],v253,s[52:55],s38 offen offset:3072 sc1\n.Ltail:\ns_add_u32 s72,s95,0\nbuffer_load_dwordx4 v[160:163],v240,s[52:55],s72 offen sc1\n" \
"s_add_u32 s72,s95,2097152\nbuffer_load_dwordx4 v[164:167],v240,s[52:55],s72 offen sc1\ns_add_u32 s72,s95,4194304\nbuffer_load_dwordx4 v[168:171],v240,s[52:55],s72 offen sc1\ns_add_u32 s72,s95,6291456\nbuffer_load_dwordx4 v[172:175],v240,s[52:55],s72 offen sc1\ns_add_u32 s72,s95,8388608\nbuffer_load_dwordx4 v[176:179],v240,s[52:55],s72 offen sc1\n" \
"s_add_u32 s72,s95,10485760\nbuffer_load_dwordx4 v[180:183],v240,s[52:55],s72 offen sc1\ns_add_u32 s72,s95,12582912\nbuffer_load_dwordx4 v[184:187],v240,s[52:55],s72 offen sc1\ns_add_u32 s72,s95,14680064\nbuffer_load_dwordx4 v[188:191],v240,s[52:55],s72 offen sc1\ns_add_u32 s72,s74,0\nbuffer_load_dwordx4 v[128:131],v240,s[52:55],s72 offen sc1\n" \
"s_add_u32 s72,s74,2097152\nbuffer_load_dwordx4 v[132:135],v240,s[52:55],s72 offen sc1\ns_add_u32 s72,s74,4194304\nbuffer_load_dwordx4 v[136:139],v240,s[52:55],s72 offen sc1\ns_add_u32 s72,s74,6291456\nbuffer_load_dwordx4 v[140:143],v240,s[52:55],s72 offen sc1\ns_add_u32 s72,s74,8388608\nbuffer_load_dwordx4 v[144:147],v240,s[52:55],s72 offen sc1\n" \
"s_add_u32 s72,s74,10485760\nbuffer_load_dwordx4 v[148:151],v240,s[52:55],s72 offen sc1\ns_add_u32 s72,s74,12582912\nbuffer_load_dwordx4 v[152:155],v240,s[52:55],s72 offen sc1\ns_add_u32 s72,s74,14680064\nbuffer_load_dwordx4 v[156:159],v240,s[52:55],s72 offen sc1\ns_waitcnt vmcnt(24)\nv_accvgpr_read_b32 v192,a160\n" \
"v_accvgpr_read_b32 v193,a161\nv_accvgpr_read_b32 v194,a162\nv_accvgpr_read_b32 v195,a163\nv_accvgpr_read_b32 v196,a164\nv_accvgpr_read_b32 v197,a165\nv_accvgpr_read_b32 v198,a166\nv_accvgpr_read_b32 v199,a167\nv_accvgpr_read_b32 v200,a168\n" \
"v_accvgpr_read_b32 v201,a169\nv_accvgpr_read_b32 v202,a170\nv_accvgpr_read_b32 v203,a171\nv_accvgpr_read_b32 v204,a172\nv_accvgpr_read_b32 v205,a173\nv_accvgpr_read_b32 v206,a174\nv_accvgpr_read_b32 v207,a175\nv_accvgpr_read_b32 v208,a176\n" \
"v_accvgpr_read_b32 v209,a177\nv_accvgpr_read_b32 v210,a178\nv_accvgpr_read_b32 v211,a179\nv_accvgpr_read_b32 v212,a180\nv_accvgpr_read_b32 v213,a181\nv_accvgpr_read_b32 v214,a182\nv_accvgpr_read_b32 v215,a183\nv_and_b32 v228,15,v249\n" \
"v_and_b32 v229,0x300,v240\nv_lshl_or_b32 v228,v228,1,v229\ns_add_u32 s70,s64,82944\nv_add_u32 v228,s70,v228\nv_add_u32 v229,s70,v240\nv_lshlrev_b32 v230,11,v249\nv_cmp_lt_u32 vcc,7,v249\nv_mov_b32 v231,0x40000000\n" \
"s_lshl3_add_u32 s37,s6,s75\nv_cndmask_b32 v230,v230,v231,vcc\ns_add_u32 s37,s37,557056\n.Lrow1:\ns_waitcnt vmcnt(15)\nM v[72:75],a[192:195],v[160:163],0\nM v[76:79],a[196:199],v[160:163],0\nv_add_f32 v104,1.0,v32\n" \
"v_add_f32 v105,1.0,v33\nv_add_f32 v106,1.0,v34\nv_add_f32 v107,1.0,v35\nv_add_f32 v108,1.0,v36\nv_add_f32 v109,1.0,v37\nv_add_f32 v110,1.0,v38\nv_add_f32 v111,1.0,v39\nv_cvt_pk_bf16_f32 v232,0,v104\n" \
"v_cvt_pk_bf16_f32 v233,0,v105\nv_cvt_pk_bf16_f32 v234,0,v106\nv_cvt_pk_bf16_f32 v235,0,v107\nv_cvt_pk_bf16_f32 v236,0,v108\nv_cvt_pk_bf16_f32 v237,0,v109\nv_cvt_pk_bf16_f32 v238,0,v110\nv_cvt_pk_bf16_f32 v239,0,v111\ns_waitcnt vmcnt(14)\n" \
"M v[72:75],a[192:195],v[164:167],v[72:75]\nM v[76:79],a[196:199],v[164:167],v[76:79]\ns_waitcnt vmcnt(13)\nM v[72:75],a[192:195],v[168:171],v[72:75]\nM v[76:79],a[196:199],v[168:171],v[76:79]\ns_waitcnt vmcnt(12)\nM v[72:75],a[192:195],v[172:175],v[72:75]\nM v[76:79],a[196:199],v[172:175],v[76:79]\n" \
"s_waitcnt vmcnt(11)\nM v[72:75],a[192:195],v[176:179],v[72:75]\nM v[76:79],a[196:199],v[176:179],v[76:79]\ns_waitcnt vmcnt(10)\nM v[72:75],a[192:195],v[180:183],v[72:75]\nM v[76:79],a[196:199],v[180:183],v[76:79]\ns_waitcnt vmcnt(9)\nM v[72:75],a[192:195],v[184:187],v[72:75]\n" \
"M v[76:79],a[196:199],v[184:187],v[76:79]\ns_waitcnt vmcnt(8)\nM v[72:75],a[192:195],v[188:191],v[72:75]\nM v[76:79],a[196:199],v[188:191],v[76:79]\ns_sub_u32 s38,56622112,s95\ns_lshr_b32 s38,s38,5\ns_nop 4\nv_cvt_pk_bf16_f32 v72,0,v72\n" \
"v_cvt_pk_bf16_f32 v73,0,v73\nv_cvt_pk_bf16_f32 v74,0,v74\nv_cvt_pk_bf16_f32 v75,0,v75\nv_cvt_pk_bf16_f32 v76,0,v76\nv_cvt_pk_bf16_f32 v77,0,v77\nv_cvt_pk_bf16_f32 v78,0,v78\nv_cvt_pk_bf16_f32 v79,0,v79\nv_pk_mul_f32 v[72:73],v[72:73],v[0:1]\n" \
"v_pk_mul_f32 v[74:75],v[74:75],v[2:3]\nv_pk_mul_f32 v[76:77],v[76:77],v[4:5]\nv_pk_mul_f32 v[78:79],v[78:79],v[6:7]\nv_cvt_pk_bf16_f32 v72,0,v72\nv_cvt_pk_bf16_f32 v73,0,v73\nv_cvt_pk_bf16_f32 v74,0,v74\nv_cvt_pk_bf16_f32 v75,0,v75\nv_cvt_pk_bf16_f32 v76,0,v76\n" \
"v_cvt_pk_bf16_f32 v77,0,v77\nv_cvt_pk_bf16_f32 v78,0,v78\nv_cvt_pk_bf16_f32 v79,0,v79\nv_pk_add_f32 v[72:73],v[72:73],v[64:65]\nv_pk_add_f32 v[74:75],v[74:75],v[66:67]\nv_pk_add_f32 v[76:77],v[76:77],v[68:69]\nv_pk_add_f32 v[78:79],v[78:79],v[70:71]\nv_cvt_pk_bf16_f32 v72,0,v72\n" \
"v_cvt_pk_bf16_f32 v73,0,v73\nv_cvt_pk_bf16_f32 v74,0,v74\nv_cvt_pk_bf16_f32 v75,0,v75\nv_cvt_pk_bf16_f32 v76,0,v76\nv_cvt_pk_bf16_f32 v77,0,v77\nv_cvt_pk_bf16_f32 v78,0,v78\nv_cvt_pk_bf16_f32 v79,0,v79\nv_readfirstlane_b32 s70,v72\n" \
"s_nop 1\nv_subrev_f32 v104,s70,v72\nv_subrev_f32 v105,s70,v73\nv_subrev_f32 v106,s70,v74\nv_subrev_f32 v107,s70,v75\nv_subrev_f32 v108,s70,v76\nv_subrev_f32 v109,s70,v77\nv_subrev_f32 v110,s70,v78\n" \
"v_subrev_f32 v111,s70,v79\nv_pk_mul_f32 v[124:125],v[104:105],v[104:105]\nv_pk_add_f32 v[120:121],v[104:105],v[106:107]\nv_pk_fma_f32 v[124:125],v[106:107],v[106:107],v[124:125]\nv_pk_fma_f32 v[124:125],v[108:109],v[108:109],v[124:125]\nv_pk_fma_f32 v[124:125],v[110:111],v[110:111],v[124:125]\nv_pk_add_f32 v[108:109],v[108:109],v[110:111]\nv_pk_add_f32 v[120:121],v[120:121],v[108:109]\n" \
"v_add_f32 v120,v120,v121\nv_add_f32 v121,v124,v125\ns_nop 1\nv_permlane16_swap_b32 v120,v121\nv_add_f32 v120,v120,v121\nv_mov_b32 v121,v120\ns_nop 1\nv_permlane32_swap_b32 v120,v121\n" \
"v_add_f32 v120,v120,v121\ns_nop 1\nv_add_f32_dpp v120,v120,v120 quad_perm:[1,0,3,2] row_mask:0xf bank_mask:0xf\ns_nop 1\nv_add_f32_dpp v120,v120,v120 quad_perm:[2,3,0,1] row_mask:0xf bank_mask:0xf\ns_nop 1\nv_add_f32_dpp v120,v120,v120 row_half_mirror row_mask:0xf bank_mask:0xf\ns_nop 1\n" \
"v_add_f32_dpp v120,v120,v120 row_mirror row_mask:0xf bank_mask:0xf\nv_mul_f32 v120,0x3b000000,v120\nv_mov_b32 v121,v120\nv_cmp_u_f32 vcc,v120,v120\ns_nop 0\nv_permlane16_swap_b32 v120,v121\ns_cbranch_vccnz .Lrs1\n.Lrb1:\n" \
"v_fma_f32 v121,-v120,v120,v121\nv_add_f32 v122,s70,v120\nv_max_f32 v121,0,v121\nv_cvt_pk_bf16_f32 v122,0,v122\nv_cvt_pk_bf16_f32 v121,0,v121\nv_add_f32 v121,0x3727c5ac,v121\nv_cvt_pk_bf16_f32 v121,0,v121\nv_sqrt_f32 v121,v121\n" \
"v_pk_add_f32 v[88:89],v[72:73],v[122:123] op_sel_hi:[1,0] neg_lo:[0,1] neg_hi:[0,1]\nv_cvt_pk_bf16_f32 v121,0,v121\nv_rcp_f32 v123,v121\nv_cvt_pk_bf16_f32 v88,0,v88\nv_pk_add_f32 v[90:91],v[74:75],v[122:123] op_sel_hi:[1,0] neg_lo:[0,1] neg_hi:[0,1]\nv_pk_add_f32 v[92:93],v[76:77],v[122:123] op_sel_hi:[1,0] neg_lo:[0,1] neg_hi:[0,1]\nv_pk_add_f32 v[94:95],v[78:79],v[122:123] op_sel_hi:[1,0] neg_lo:[0,1] neg_hi:[0,1]\nv_cvt_pk_bf16_f32 v89,0,v89\n" \
"v_pk_mul_f32 v[88:89],v[88:89],v[122:123] op_sel:[0,1]\nv_cvt_pk_bf16_f32 v88,0,v88\nv_cvt_pk_bf16_f32 v89,0,v89\nv_pk_mul_f32 v[88:89],v[88:89],v[232:233]\nv_cvt_pk_bf16_f32 v88,0,v88\nv_cvt_pk_bf16_f32 v89,0,v89\nv_pk_add_f32 v[88:89],v[88:89],v[16:17]\nv_cvt_pk_bf16_f32 v88,0,v88\n" \
"v_cvt_pk_bf16_f32 v89,0,v89\nv_pk_mul_f32 v[124:125],v[88:89],v[96:97]\nv_cvt_pk_bf16_f32 v90,0,v90\nv_cvt_pk_bf16_f32 v91,0,v91\nv_pk_mul_f32 v[90:91],v[90:91],v[122:123] op_sel:[0,1]\nv_cvt_pk_bf16_f32 v90,0,v90\nv_cvt_pk_bf16_f32 v91,0,v91\nv_pk_mul_f32 v[90:91],v[90:91],v[234:235]\n" \
"v_cvt_pk_bf16_f32 v90,0,v90\nv_cvt_pk_bf16_f32 v91,0,v91\nv_pk_add_f32 v[90:91],v[90:91],v[18:19]\nv_cvt_pk_bf16_f32 v90,0,v90\nv_cvt_pk_bf16_f32 v91,0,v91\nv_pk_fma_f32 v[124:125],v[90:91],v[98:99],v[124:125]\nv_cvt_pk_bf16_f32 v92,0,v92\nv_cvt_pk_bf16_f32 v93,0,v93\n" \
"v_pk_mul_f32 v[92:93],v[92:93],v[122:123] op_sel:[0,1]\nv_cvt_pk_bf16_f32 v92,0,v92\nv_cvt_pk_bf16_f32 v93,0,v93\nv_pk_mul_f32 v[92:93],v[92:93],v[236:237]\nv_cvt_pk_bf16_f32 v92,0,v92\nv_cvt_pk_bf16_f32 v93,0,v93\nv_pk_add_f32 v[92:93],v[92:93],v[20:21]\nv_cvt_pk_bf16_f32 v92,0,v92\n" \
"v_cvt_pk_bf16_f32 v93,0,v93\nv_pk_fma_f32 v[124:125],v[92:93],v[100:101],v[124:125]\nv_cvt_pk_bf16_f32 v94,0,v94\nv_cvt_pk_bf16_f32 v95,0,v95\nv_pk_mul_f32 v[94:95],v[94:95],v[122:123] op_sel:[0,1]\nv_cvt_pk_bf16_f32 v94,0,v94\nv_cvt_pk_bf16_f32 v95,0,v95\nv_pk_mul_f32 v[94:95],v[94:95],v[238:239]\n" \
"v_cvt_pk_bf16_f32 v94,0,v94\nv_cvt_pk_bf16_f32 v95,0,v95\nv_pk_add_f32 v[94:95],v[94:95],v[22:23]\nv_cvt_pk_bf16_f32 v94,0,v94\nv_cvt_pk_bf16_f32 v95,0,v95\nv_pk_fma_f32 v[124:125],v[94:95],v[102:103],v[124:125]\nv_add_f32 v124,v124,v125\nv_mov_b32 v125,v124\n" \
"v_lshrrev_b32 v104,16,v121\nv_or_b32 v104,v104,v122\nv_permlane16_swap_b32 v124,v125\nv_add_f32 v124,v124,v125\nv_mov_b32 v125,v124\nv_pk_mul_f32 v[88:89],v[88:89],v[48:49]\nv_pk_mul_f32 v[90:91],v[90:91],v[50:51]\nv_permlane32_swap_b32 v124,v125\n" \
"v_add_f32 v124,v124,v125\nv_pk_mul_f32 v[92:93],v[92:93],v[52:53]\nv_pk_mul_f32 v[94:95],v[94:95],v[54:55]\nv_add_f32_dpp v124,v124,v124 quad_perm:[1,0,3,2] row_mask:0xf bank_mask:0xf\nv_cvt_pk_bf16_f32 v88,0,v88\nv_cvt_pk_bf16_f32 v89,0,v89\nv_add_f32_dpp v124,v124,v124 quad_perm:[2,3,0,1] row_mask:0xf bank_mask:0xf\nv_cvt_pk_bf16_f32 v90,0,v90\n" \
"v_cvt_pk_bf16_f32 v91,0,v91\nv_add_f32_dpp v124,v124,v124 row_half_mirror row_mask:0xf bank_mask:0xf\nv_cvt_pk_bf16_f32 v92,0,v92\nv_cvt_pk_bf16_f32 v93,0,v93\nv_add_f32_dpp v124,v124,v124 row_mirror row_mask:0xf bank_mask:0xf\nv_cvt_pk_bf16_f32 v124,0,v124\nv_cvt_pk_bf16_f32 v94,0,v94\nv_readfirstlane_b32 s77,v124\n" \
"v_cvt_pk_bf16_f32 v95,0,v95\ns_ashr_i32 s71,s77,31\ns_and_b32 s72,s77,0x7fffffff\ns_or_b32 s71,s71,0x80000000\ns_xor_b32 s77,s77,s71\ns_and_b32 s77,s77,0xffff0000\ns_cmp_gt_u32 s72,0x7f800000\ns_cselect_b32 s77,0,s77\n" \
"s_or_b32 s77,s77,s38\nv_mov_b32 v105,s77\nds_max_u64 v250,v[104:105]\nv_pk_add_f32 v[88:89],v[88:89],v[72:73]\nv_pk_add_f32 v[90:91],v[90:91],v[74:75]\nv_pk_add_f32 v[92:93],v[92:93],v[76:77]\nv_pk_add_f32 v[94:95],v[94:95],v[78:79]\nv_cvt_pk_bf16_f32 v88,0,v88\n" \
"ds_write_b16_d16_hi v228,v88 offset:0\nv_cvt_pk_bf16_f32 v89,0,v89\nds_write_b16_d16_hi v228,v89 offset:32\nv_cvt_pk_bf16_f32 v90,0,v90\nds_write_b16_d16_hi v228,v90 offset:64\nv_cvt_pk_bf16_f32 v91,0,v91\nds_write_b16_d16_hi v228,v91 offset:96\nv_cvt_pk_bf16_f32 v92,0,v92\n" \
"ds_write_b16_d16_hi v228,v92 offset:128\nv_cvt_pk_bf16_f32 v93,0,v93\nds_write_b16_d16_hi v228,v93 offset:160\nv_cvt_pk_bf16_f32 v94,0,v94\nds_write_b16_d16_hi v228,v94 offset:192\nv_cvt_pk_bf16_f32 v95,0,v95\nds_write_b16_d16_hi v228,v95 offset:224\nds_read_b128 v[216:219],v229 offset:0\n" \
".Lrow0:\ns_waitcnt vmcnt(7)\nM v[8:11],a[192:195],v[128:131],0\nM v[12:15],a[196:199],v[128:131],0\ns_waitcnt vmcnt(6)\nM v[8:11],a[192:195],v[132:135],v[8:11]\nM v[12:15],a[196:199],v[132:135],v[12:15]\ns_waitcnt vmcnt(5)\n" \
"M v[8:11],a[192:195],v[136:139],v[8:11]\nM v[12:15],a[196:199],v[136:139],v[12:15]\ns_waitcnt vmcnt(4)\nM v[8:11],a[192:195],v[140:143],v[8:11]\nM v[12:15],a[196:199],v[140:143],v[12:15]\ns_waitcnt vmcnt(3)\nM v[8:11],a[192:195],v[144:147],v[8:11]\nM v[12:15],a[196:199],v[144:147],v[12:15]\n" \
"s_waitcnt vmcnt(2)\nM v[8:11],a[192:195],v[148:151],v[8:11]\nM v[12:15],a[196:199],v[148:151],v[12:15]\ns_waitcnt vmcnt(1)\nM v[8:11],a[192:195],v[152:155],v[8:11]\nM v[12:15],a[196:199],v[152:155],v[12:15]\ns_waitcnt vmcnt(0)\nM v[8:11],a[192:195],v[156:159],v[8:11]\n" \
"M v[12:15],a[196:199],v[156:159],v[12:15]\ns_sub_u32 s39,56622112,s74\ns_lshr_b32 s39,s39,5\ns_nop 4\nv_cvt_pk_bf16_f32 v8,0,v8\nv_cvt_pk_bf16_f32 v9,0,v9\nv_cvt_pk_bf16_f32 v10,0,v10\nv_cvt_pk_bf16_f32 v11,0,v11\n" \
"v_cvt_pk_bf16_f32 v12,0,v12\nv_cvt_pk_bf16_f32 v13,0,v13\nv_cvt_pk_bf16_f32 v14,0,v14\nv_cvt_pk_bf16_f32 v15,0,v15\nv_pk_mul_f32 v[8:9],v[8:9],v[0:1]\nv_pk_mul_f32 v[10:11],v[10:11],v[2:3]\nv_pk_mul_f32 v[12:13],v[12:13],v[4:5]\nv_pk_mul_f32 v[14:15],v[14:15],v[6:7]\n" \
"v_cvt_pk_bf16_f32 v8,0,v8\nv_cvt_pk_bf16_f32 v9,0,v9\nv_cvt_pk_bf16_f32 v10,0,v10\nv_cvt_pk_bf16_f32 v11,0,v11\nv_cvt_pk_bf16_f32 v12,0,v12\nv_cvt_pk_bf16_f32 v13,0,v13\nv_cvt_pk_bf16_f32 v14,0,v14\nv_cvt_pk_bf16_f32 v15,0,v15\n" \
"v_pk_add_f32 v[8:9],v[8:9],v[80:81]\nv_pk_add_f32 v[10:11],v[10:11],v[82:83]\nv_pk_add_f32 v[12:13],v[12:13],v[84:85]\nv_pk_add_f32 v[14:15],v[14:15],v[86:87]\nv_cvt_pk_bf16_f32 v8,0,v8\nv_cvt_pk_bf16_f32 v9,0,v9\nv_cvt_pk_bf16_f32 v10,0,v10\nv_cvt_pk_bf16_f32 v11,0,v11\n" \
"v_cvt_pk_bf16_f32 v12,0,v12\nv_cvt_pk_bf16_f32 v13,0,v13\nv_cvt_pk_bf16_f32 v14,0,v14\nv_cvt_pk_bf16_f32 v15,0,v15\nv_readfirstlane_b32 s70,v8\ns_nop 1\nv_subrev_f32 v40,s70,v8\nv_subrev_f32 v41,s70,v9\n" \
"v_subrev_f32 v42,s70,v10\nv_subrev_f32 v43,s70,v11\nv_subrev_f32 v44,s70,v12\nv_subrev_f32 v45,s70,v13\nv_subrev_f32 v46,s70,v14\nv_subrev_f32 v47,s70,v15\nv_pk_mul_f32 v[60:61],v[40:41],v[40:41]\nv_pk_add_f32 v[56:57],v[40:41],v[42:43]\n" \
"v_pk_fma_f32 v[60:61],v[42:43],v[42:43],v[60:61]\nv_pk_fma_f32 v[60:61],v[44:45],v[44:45],v[60:61]\nv_pk_fma_f32 v[60:61],v[46:47],v[46:47],v[60:61]\nv_pk_add_f32 v[44:45],v[44:45],v[46:47]\nv_pk_add_f32 v[56:57],v[56:57],v[44:45]\nv_add_f32 v56,v56,v57\nv_add_f32 v57,v60,v61\ns_nop 1\n" \
"v_permlane16_swap_b32 v56,v57\nv_add_f32 v56,v56,v57\nv_mov_b32 v57,v56\ns_nop 1\nv_permlane32_swap_b32 v56,v57\nv_add_f32 v56,v56,v57\ns_nop 1\nv_add_f32_dpp v56,v56,v56 quad_perm:[1,0,3,2] row_mask:0xf bank_mask:0xf\n" \
"s_nop 1\nv_add_f32_dpp v56,v56,v56 quad_perm:[2,3,0,1] row_mask:0xf bank_mask:0xf\ns_nop 1\nv_add_f32_dpp v56,v56,v56 row_half_mirror row_mask:0xf bank_mask:0xf\ns_nop 1\nv_add_f32_dpp v56,v56,v56 row_mirror row_mask:0xf bank_mask:0xf\nv_mul_f32 v56,0x3b000000,v56\nv_mov_b32 v57,v56\n" \
"v_cmp_u_f32 vcc,v56,v56\ns_nop 0\nv_permlane16_swap_b32 v56,v57\ns_cbranch_vccnz .Lrs0\n.Lrb0:\nv_fma_f32 v57,-v56,v56,v57\nv_add_f32 v58,s70,v56\nv_max_f32 v57,0,v57\n" \
"v_cvt_pk_bf16_f32 v58,0,v58\nv_cvt_pk_bf16_f32 v57,0,v57\nv_add_f32 v57,0x3727c5ac,v57\nv_cvt_pk_bf16_f32 v57,0,v57\nv_sqrt_f32 v57,v57\nv_pk_add_f32 v[24:25],v[8:9],v[58:59] op_sel_hi:[1,0] neg_lo:[0,1] neg_hi:[0,1]\nv_cvt_pk_bf16_f32 v57,0,v57\nv_rcp_f32 v59,v57\n" \
"v_cvt_pk_bf16_f32 v24,0,v24\nv_pk_add_f32 v[26:27],v[10:11],v[58:59] op_sel_hi:[1,0] neg_lo:[0,1] neg_hi:[0,1]\nv_pk_add_f32 v[28:29],v[12:13],v[58:59] op_sel_hi:[1,0] neg_lo:[0,1] neg_hi:[0,1]\nv_pk_add_f32 v[30:31],v[14:15],v[58:59] op_sel_hi:[1,0] neg_lo:[0,1] neg_hi:[0,1]\nv_cvt_pk_bf16_f32 v25,0,v25\nv_pk_mul_f32 v[24:25],v[24:25],v[58:59] op_sel:[0,1]\nv_cvt_pk_bf16_f32 v24,0,v24\nv_cvt_pk_bf16_f32 v25,0,v25\n" \
"v_pk_mul_f32 v[24:25],v[24:25],v[232:233]\nv_cvt_pk_bf16_f32 v24,0,v24\nv_cvt_pk_bf16_f32 v25,0,v25\nv_pk_add_f32 v[24:25],v[24:25],v[16:17]\nv_cvt_pk_bf16_f32 v24,0,v24\nv_cvt_pk_bf16_f32 v25,0,v25\nv_pk_mul_f32 v[60:61],v[24:25],v[96:97]\nv_cvt_pk_bf16_f32 v26,0,v26\n" \
"v_cvt_pk_bf16_f32 v27,0,v27\nv_pk_mul_f32 v[26:27],v[26:27],v[58:59] op_sel:[0,1]\nv_cvt_pk_bf16_f32 v26,0,v26\nv_cvt_pk_bf16_f32 v27,0,v27\nv_pk_mul_f32 v[26:27],v[26:27],v[234:235]\nv_cvt_pk_bf16_f32 v26,0,v26\nv_cvt_pk_bf16_f32 v27,0,v27\nv_pk_add_f32 v[26:27],v[26:27],v[18:19]\n" \
"v_cvt_pk_bf16_f32 v26,0,v26\nv_cvt_pk_bf16_f32 v27,0,v27\nv_pk_fma_f32 v[60:61],v[26:27],v[98:99],v[60:61]\nv_cvt_pk_bf16_f32 v28,0,v28\nv_cvt_pk_bf16_f32 v29,0,v29\nv_pk_mul_f32 v[28:29],v[28:29],v[58:59] op_sel:[0,1]\nv_cvt_pk_bf16_f32 v28,0,v28\nv_cvt_pk_bf16_f32 v29,0,v29\n" \
"v_pk_mul_f32 v[28:29],v[28:29],v[236:237]\nv_cvt_pk_bf16_f32 v28,0,v28\nv_cvt_pk_bf16_f32 v29,0,v29\nv_pk_add_f32 v[28:29],v[28:29],v[20:21]\nv_cvt_pk_bf16_f32 v28,0,v28\nv_cvt_pk_bf16_f32 v29,0,v29\nv_pk_fma_f32 v[60:61],v[28:29],v[100:101],v[60:61]\nv_cvt_pk_bf16_f32 v30,0,v30\n" \
"v_cvt_pk_bf16_f32 v31,0,v31\nv_pk_mul_f32 v[30:31],v[30:31],v[58:59] op_sel:[0,1]\nv_cvt_pk_bf16_f32 v30,0,v30\nv_cvt_pk_bf16_f32 v31,0,v31\nv_pk_mul_f32 v[30:31],v[30:31],v[238:239]\nv_cvt_pk_bf16_f32 v30,0,v30\nv_cvt_pk_bf16_f32 v31,0,v31\nv_pk_add_f32 v[30:31],v[30:31],v[22:23]\n" \
"v_cvt_pk_bf16_f32 v30,0,v30\nv_cvt_pk_bf16_f32 v31,0,v31\nv_pk_fma_f32 v[60:61],v[30:31],v[102:103],v[60:61]\nv_add_f32 v60,v60,v61\nv_mov_b32 v61,v60\nv_lshrrev_b32 v40,16,v57\nv_or_b32 v40,v40,v58\nv_permlane16_swap_b32 v60,v61\n" \
"v_add_f32 v60,v60,v61\nv_mov_b32 v61,v60\nv_pk_mul_f32 v[24:25],v[24:25],v[48:49]\nv_pk_mul_f32 v[26:27],v[26:27],v[50:51]\nv_permlane32_swap_b32 v60,v61\nv_add_f32 v60,v60,v61\nv_pk_mul_f32 v[28:29],v[28:29],v[52:53]\nv_pk_mul_f32 v[30:31],v[30:31],v[54:55]\n" \
"v_add_f32_dpp v60,v60,v60 quad_perm:[1,0,3,2] row_mask:0xf bank_mask:0xf\nv_cvt_pk_bf16_f32 v24,0,v24\nv_cvt_pk_bf16_f32 v25,0,v25\nv_add_f32_dpp v60,v60,v60 quad_perm:[2,3,0,1] row_mask:0xf bank_mask:0xf\nv_cvt_pk_bf16_f32 v26,0,v26\nv_cvt_pk_bf16_f32 v27,0,v27\nv_add_f32_dpp v60,v60,v60 row_half_mirror row_mask:0xf bank_mask:0xf\nv_cvt_pk_bf16_f32 v28,0,v28\n" \
"v_cvt_pk_bf16_f32 v29,0,v29\nv_add_f32_dpp v60,v60,v60 row_mirror row_mask:0xf bank_mask:0xf\nv_cvt_pk_bf16_f32 v60,0,v60\nv_cvt_pk_bf16_f32 v30,0,v30\nv_readfirstlane_b32 s76,v60\nv_cvt_pk_bf16_f32 v31,0,v31\ns_ashr_i32 s71,s76,31\ns_and_b32 s72,s76,0x7fffffff\n" \
"s_or_b32 s71,s71,0x80000000\ns_xor_b32 s76,s76,s71\ns_and_b32 s76,s76,0xffff0000\ns_cmp_gt_u32 s72,0x7f800000\ns_cselect_b32 s76,0,s76\ns_or_b32 s76,s76,s39\nv_mov_b32 v41,s76\nds_max_u64 v250,v[40:41]\n" \
"s_waitcnt lgkmcnt(0)\ns_barrier\nds_read_b64 v[224:225],v250\ns_waitcnt lgkmcnt(0)\nbuffer_store_dwordx2 v[224:225],v230,s[52:55],s37 offen sc1\nv_pk_add_f32 v[24:25],v[24:25],v[8:9]\nv_pk_add_f32 v[26:27],v[26:27],v[10:11]\nv_pk_add_f32 v[28:29],v[28:29],v[12:13]\n" \
"v_pk_add_f32 v[30:31],v[30:31],v[14:15]\nv_cvt_pk_bf16_f32 v24,0,v24\nds_write_b16_d16_hi v228,v24 offset:1024\nv_cvt_pk_bf16_f32 v25,0,v25\nds_write_b16_d16_hi v228,v25 offset:1056\nv_cvt_pk_bf16_f32 v26,0,v26\nds_write_b16_d16_hi v228,v26 offset:1088\nv_cvt_pk_bf16_f32 v27,0,v27\n" \
"ds_write_b16_d16_hi v228,v27 offset:1120\nv_cvt_pk_bf16_f32 v28,0,v28\nds_write_b16_d16_hi v228,v28 offset:1152\nv_cvt_pk_bf16_f32 v29,0,v29\nds_write_b16_d16_hi v228,v29 offset:1184\nv_cvt_pk_bf16_f32 v30,0,v30\nds_write_b16_d16_hi v228,v30 offset:1216\nv_cvt_pk_bf16_f32 v31,0,v31\n" \
"ds_write_b16_d16_hi v228,v31 offset:1248\nds_read_b128 v[220:223],v229 offset:1024\nv_readfirstlane_b32 s70,v225\nv_mov_b32 v226,s77\nv_mov_b32 v227,s76\ns_sub_u32 s95,s95,54525952\ns_sub_u32 s74,s74,54525952\ns_cmp_eq_u32 s70,s77\n" \
"s_cselect_b32 s95,0x40000000,s95\ns_cmp_eq_u32 s70,s76\ns_cselect_b32 s74,0x40000000,s74\ns_waitcnt lgkmcnt(0)\nbuffer_store_dwordx4 v[216:219],v240,s[44:47],s95 offen sc1\nbuffer_store_dwordx4 v[220:223],v240,s[44:47],s74 offen sc1\ns_waitcnt vmcnt(3)\ns_branch .Lfin\n" \
".Lhs:\ns_sub_u32 s73,s73,1\ns_cbranch_scc1 .Lhb\ns_sleep 2\nbuffer_load_dwordx2 v[166:167],v165,s[52:55],s80 offen sc1\ns_waitcnt vmcnt(0)\nv_xor_b32 v166,v166,v230\nv_xor_b32 v167,v167,v231\n" \
"v_or_b32 v166,v166,v167\nv_cmp_ne_u32 vcc,0,v166\ns_nop 0\ns_cbranch_vccnz .Lhs\ns_branch .Lhb\n.Lps0:\ns_sub_u32 s73,s73,1\ns_cbranch_scc1 .Lpb0\n" \
"s_sleep 1\nbuffer_load_dword v248,v228,s[52:55],s80 offen sc1\ns_waitcnt vmcnt(0)\nv_cmp_eq_u32 vcc,0,v248\ns_nop 0\ns_cbranch_vccnz .Lps0\ns_branch .Lpb0\n.Lps1:\n" \
"s_sub_u32 s73,s73,1\ns_cbranch_scc1 .Lpb1\ns_sleep 1\nbuffer_load_dword v248,v228,s[52:55],s80 offen sc1\ns_waitcnt vmcnt(0)\nv_cmp_eq_u32 vcc,0,v248\ns_nop 0\ns_cbranch_vccnz .Lps1\n" \
"s_branch .Lpb1\n.Lps2:\ns_sub_u32 s73,s73,1\ns_cbranch_scc1 .Lpb2\ns_sleep 1\nbuffer_load_dword v248,v251,s[52:55],s80 offen sc1\ns_waitcnt vmcnt(0)\nv_cmp_eq_u32 vcc,0,v248\n" \
"s_nop 0\ns_cbranch_vccnz .Lps2\ns_branch .Lpb2\n.Lps3:\ns_sub_u32 s73,s73,1\ns_cbranch_scc1 .Lpb3\ns_sleep 1\nbuffer_load_dword v248,v251,s[52:55],s80 offen sc1\n" \
"s_waitcnt vmcnt(0)\nv_cmp_eq_u32 vcc,0,v248\ns_nop 0\ns_cbranch_vccnz .Lps3\ns_branch .Lpb3\n.Lps4:\ns_sub_u32 s73,s73,1\ns_cbranch_scc1 .Lpb4\n" \
"s_sleep 1\nbuffer_load_dword v248,v251,s[52:55],s80 offen sc1\ns_waitcnt vmcnt(0)\nv_cmp_eq_u32 vcc,0,v248\ns_nop 0\ns_cbranch_vccnz .Lps4\ns_branch .Lpb4\n.Lps5:\n" \
"s_sub_u32 s73,s73,1\ns_cbranch_scc1 .Lpb5\ns_sleep 1\nbuffer_load_dword v248,v251,s[52:55],s80 offen sc1\ns_waitcnt vmcnt(0)\nv_cmp_eq_u32 vcc,0,v248\ns_nop 0\ns_cbranch_vccnz .Lps5\n" \
"s_branch .Lpb5\n.Ltouch:\nv_lshlrev_b32 v236,7,v249\ns_add_u32 s70,s12,s67\ns_add_u32 s70,s70,1048576\nbuffer_load_dword v237,v236,s[52:55],s70 offen sc1\ns_lshr_b32 s70,s6,3\ns_lshl2_add_u32 s70,s70,s7\n" \
"s_and_b32 s70,s70,63\ns_lshl_b32 s71,s60,6\ns_add_u32 s70,s70,s71\ns_lshl_b32 s70,s70,13\ns_add_u32 s70,s70,4096\nbuffer_load_dword v238,v236,s[40:43],s70 offen\ns_mov_b32 s15,s56\ns_lshr_b32 s70,s6,3\n" \
"s_lshl2_add_u32 s70,s70,s7\ns_cmp_lt_u32 s70,64\ns_cbranch_scc0 .Ltd\ns_cmp_lt_u32 s70,32\ns_cselect_b64 vcc,s[22:23],s[24:25]\ns_and_b32 s70,s70,31\ns_mul_i32 s70,s70,57344\ns_mul_i32 s71,s60,448\n" \
"s_and_b32 s71,s71,0xffffff80\ns_add_u32 s70,s70,s71\nv_lshrrev_b32 v239,2,v249\nv_mul_u32_u24 v239,3584,v239\nv_and_b32 v236,0x180,v236\nv_add3_u32 v236,v236,v239,s70\nglobal_load_dword v239,v236,vcc\ns_branch .Lsingle\n" \
".Ltd:\ns_min_u32 s70,s70,91\ns_mul_i32 s71,s60,28\ns_add_u32 s70,s70,s71\ns_sub_u32 s70,s70,64\ns_lshl_b32 s70,s70,13\nv_add_u32 v236,s70,v236\nglobal_load_dword v239,v236,s[26:27]\n" \
"s_branch .Lsingle\n.Lps6:\ns_sub_u32 s73,s73,1\ns_cbranch_scc1 .Lpb6\ns_sleep 1\nbuffer_load_dword v248,v251,s[52:55],s80 offen sc1\ns_waitcnt vmcnt(0)\nv_and_b32 v248,s17,v248\n" \
"v_cmp_eq_u32 vcc,0,v248\ns_nop 0\ns_cbranch_vccnz .Lps6\ns_branch .Lpb6\n.Lps7:\ns_sub_u32 s73,s73,1\ns_cbranch_scc1 .Lpb7\ns_sleep 1\n" \
"buffer_load_dword v248,v228,s[52:55],s80 offen sc1\ns_waitcnt vmcnt(0)\nv_cmp_eq_u32 vcc,0,v248\ns_nop 0\ns_cbranch_vccnz .Lps7\ns_branch .Lpb7\n.Lps8:\ns_sub_u32 s73,s73,1\n" \
"s_cbranch_scc1 .Lpb8\ns_sleep 1\nbuffer_load_dword v248,v251,s[52:55],s79 offen sc1\ns_waitcnt vmcnt(0)\nv_cmp_eq_u32 vcc,0,v248\ns_nop 0\ns_cbranch_vccnz .Lps8\ns_branch .Lpb8\n" \
".Lps9:\ns_sub_u32 s73,s73,1\ns_cbranch_scc1 .Lpb9\ns_sleep 1\nbuffer_load_dword v248,v251,s[52:55],s80 offen sc1\ns_waitcnt vmcnt(0)\nv_and_b32 v248,s17,v248\nv_cmp_eq_u32 vcc,0,v248\n" \
"s_nop 0\ns_cbranch_vccnz .Lps9\ns_branch .Lpb9\n.Lps10:\ns_sub_u32 s73,s73,1\ns_cbranch_scc1 .Lpb10\ns_sleep 1\nbuffer_load_dword v248,v228,s[52:55],s80 offen sc1\n" \
"s_waitcnt vmcnt(0)\nv_cmp_eq_u32 vcc,0,v248\ns_nop 0\ns_cbranch_vccnz .Lps10\ns_branch .Lpb10\n.Lps11:\ns_sub_u32 s73,s73,1\ns_cbranch_scc1 .Lpb11\n" \
"s_sleep 1\nbuffer_load_dword v248,v251,s[52:55],s79 offen sc1\ns_waitcnt vmcnt(0)\nv_cmp_eq_u32 vcc,0,v248\ns_nop 0\ns_cbranch_vccnz .Lps11\ns_branch .Lpb11\n.Lrs1:\n" \
"s_mov_b32 s71,0\n.Lrl1:\ns_mov_b32 s97,0\nv_max3_u32 v124,v160,v161,v162\nv_max3_u32 v124,v124,v163,v163\nv_cmp_eq_u32 vcc,-1,v124\ns_add_u32 s72,s95,0\ns_cbranch_vccz .Lrz10\n" \
"s_mov_b64 exec,vcc\ns_mov_b32 s97,1\nbuffer_load_dwordx4 v[160:163],v240,s[52:55],s72 offen sc1\ns_mov_b64 exec,-1\n.Lrz10:\nv_max3_u32 v124,v164,v165,v166\nv_max3_u32 v124,v124,v167,v167\nv_cmp_eq_u32 vcc,-1,v124\n" \
"s_add_u32 s72,s95,2097152\ns_cbranch_vccz .Lrz11\ns_mov_b64 exec,vcc\ns_mov_b32 s97,1\nbuffer_load_dwordx4 v[164:167],v240,s[52:55],s72 offen sc1\ns_mov_b64 exec,-1\n.Lrz11:\nv_max3_u32 v124,v168,v169,v170\n" \
"v_max3_u32 v124,v124,v171,v171\nv_cmp_eq_u32 vcc,-1,v124\ns_add_u32 s72,s95,4194304\ns_cbranch_vccz .Lrz12\ns_mov_b64 exec,vcc\ns_mov_b32 s97,1\nbuffer_load_dwordx4 v[168:171],v240,s[52:55],s72 offen sc1\ns_mov_b64 exec,-1\n" \
".Lrz12:\nv_max3_u32 v124,v172,v173,v174\nv_max3_u32 v124,v124,v175,v175\nv_cmp_eq_u32 vcc,-1,v124\ns_add_u32 s72,s95,6291456\ns_cbranch_vccz .Lrz13\ns_mov_b64 exec,vcc\ns_mov_b32 s97,1\n" \
"buffer_load_dwordx4 v[172:175],v240,s[52:55],s72 offen sc1\ns_mov_b64 exec,-1\n.Lrz13:\nv_max3_u32 v124,v176,v177,v178\nv_max3_u32 v124,v124,v179,v179\nv_cmp_eq_u32 vcc,-1,v124\ns_add_u32 s72,s95,8388608\ns_cbranch_vccz .Lrz14\n" \
"s_mov_b64 exec,vcc\ns_mov_b32 s97,1\nbuffer_load_dwordx4 v[176:179],v240,s[52:55],s72 offen sc1\ns_mov_b64 exec,-1\n.Lrz14:\nv_max3_u32 v124,v180,v181,v182\nv_max3_u32 v124,v124,v183,v183\nv_cmp_eq_u32 vcc,-1,v124\n" \
"s_add_u32 s72,s95,10485760\ns_cbranch_vccz .Lrz15\ns_mov_b64 exec,vcc\ns_mov_b32 s97,1\nbuffer_load_dwordx4 v[180:183],v240,s[52:55],s72 offen sc1\ns_mov_b64 exec,-1\n.Lrz15:\nv_max3_u32 v124,v184,v185,v186\n" \
"v_max3_u32 v124,v124,v187,v187\nv_cmp_eq_u32 vcc,-1,v124\ns_add_u32 s72,s95,12582912\ns_cbranch_vccz .Lrz16\ns_mov_b64 exec,vcc\ns_mov_b32 s97,1\nbuffer_load_dwordx4 v[184:187],v240,s[52:55],s72 offen sc1\ns_mov_b64 exec,-1\n" \
".Lrz16:\nv_max3_u32 v124,v188,v189,v190\nv_max3_u32 v124,v124,v191,v191\nv_cmp_eq_u32 vcc,-1,v124\ns_add_u32 s72,s95,14680064\ns_cbranch_vccz .Lrz17\ns_mov_b64 exec,vcc\ns_mov_b32 s97,1\n" \
"buffer_load_dwordx4 v[188:191],v240,s[52:55],s72 offen sc1\ns_mov_b64 exec,-1\n.Lrz17:\ns_cmp_lg_u32 s97,0\ns_cbranch_scc1 .Lrm1\ns_cmp_eq_u32 s71,0\ns_cbranch_scc1 .Lrb1\ns_branch .Lrow1\n" \
".Lrm1:\ns_mov_b32 s71,1\ns_waitcnt vmcnt(0)\ns_sub_u32 s73,s73,1\ns_cbranch_scc1 .Lrb1\ns_sleep 2\ns_branch .Lrl1\n.Lrs0:\n" \
"s_mov_b32 s71,0\n.Lrl0:\ns_mov_b32 s97,0\nv_max3_u32 v60,v128,v129,v130\nv_max3_u32 v60,v60,v131,v131\nv_cmp_eq_u32 vcc,-1,v60\ns_add_u32 s72,s74,0\ns_cbranch_vccz .Lrz00\n" \
"s_mov_b64 exec,vcc\ns_mov_b32 s97,1\nbuffer_load_dwordx4 v[128:131],v240,s[52:55],s72 offen sc1\ns_mov_b64 exec,-1\n.Lrz00:\nv_max3_u32 v60,v132,v133,v134\nv_max3_u32 v60,v60,v135,v135\nv_cmp_eq_u32 vcc,-1,v60\n" \
"s_add_u32 s72,s74,2097152\ns_cbranch_vccz .Lrz01\ns_mov_b64 exec,vcc\ns_mov_b32 s97,1\nbuffer_load_dwordx4 v[132:135],v240,s[52:55],s72 offen sc1\ns_mov_b64 exec,-1\n.Lrz01:\nv_max3_u32 v60,v136,v137,v138\n" \
"v_max3_u32 v60,v60,v139,v139\nv_cmp_eq_u32 vcc,-1,v60\ns_add_u32 s72,s74,4194304\ns_cbranch_vccz .Lrz02\ns_mov_b64 exec,vcc\ns_mov_b32 s97,1\nbuffer_load_dwordx4 v[136:139],v240,s[52:55],s72 offen sc1\ns_mov_b64 exec,-1\n" \
".Lrz02:\nv_max3_u32 v60,v140,v141,v142\nv_max3_u32 v60,v60,v143,v143\nv_cmp_eq_u32 vcc,-1,v60\ns_add_u32 s72,s74,6291456\ns_cbranch_vccz .Lrz03\ns_mov_b64 exec,vcc\ns_mov_b32 s97,1\n" \
"buffer_load_dwordx4 v[140:143],v240,s[52:55],s72 offen sc1\ns_mov_b64 exec,-1\n.Lrz03:\nv_max3_u32 v60,v144,v145,v146\nv_max3_u32 v60,v60,v147,v147\nv_cmp_eq_u32 vcc,-1,v60\ns_add_u32 s72,s74,8388608\ns_cbranch_vccz .Lrz04\n" \
"s_mov_b64 exec,vcc\ns_mov_b32 s97,1\nbuffer_load_dwordx4 v[144:147],v240,s[52:55],s72 offen sc1\ns_mov_b64 exec,-1\n.Lrz04:\nv_max3_u32 v60,v148,v149,v150\nv_max3_u32 v60,v60,v151,v151\nv_cmp_eq_u32 vcc,-1,v60\n" \
"s_add_u32 s72,s74,10485760\ns_cbranch_vccz .Lrz05\ns_mov_b64 exec,vcc\ns_mov_b32 s97,1\nbuffer_load_dwordx4 v[148:151],v240,s[52:55],s72 offen sc1\ns_mov_b64 exec,-1\n.Lrz05:\nv_max3_u32 v60,v152,v153,v154\n" \
"v_max3_u32 v60,v60,v155,v155\nv_cmp_eq_u32 vcc,-1,v60\ns_add_u32 s72,s74,12582912\ns_cbranch_vccz .Lrz06\ns_mov_b64 exec,vcc\ns_mov_b32 s97,1\nbuffer_load_dwordx4 v[152:155],v240,s[52:55],s72 offen sc1\ns_mov_b64 exec,-1\n" \
".Lrz06:\nv_max3_u32 v60,v156,v157,v158\nv_max3_u32 v60,v60,v159,v159\nv_cmp_eq_u32 vcc,-1,v60\ns_add_u32 s72,s74,14680064\ns_cbranch_vccz .Lrz07\ns_mov_b64 exec,vcc\ns_mov_b32 s97,1\n" \
"buffer_load_dwordx4 v[156:159],v240,s[52:55],s72 offen sc1\ns_mov_b64 exec,-1\n.Lrz07:\ns_cmp_lg_u32 s97,0\ns_cbranch_scc1 .Lrm0\ns_cmp_eq_u32 s71,0\ns_cbranch_scc1 .Lrb0\ns_branch .Lrow0\n" \
".Lrm0:\ns_mov_b32 s71,1\ns_waitcnt vmcnt(0)\ns_sub_u32 s73,s73,1\ns_cbranch_scc1 .Lrb0\ns_sleep 2\ns_branch .Lrl0\n.Lredo:\n" \
"s_waitcnt vmcnt(0) lgkmcnt(0)\ns_barrier\ns_mov_b64 s[90:91],0\ns_mov_b32 s99,0\ns_mov_b32 s17,1\ns_lshl_b32 s70,s63,19\ns_add_u32 s37,s70,3145728\ns_lshl_b32 s71,s62,15\n" \
"s_add_u32 s72,s37,s64\ns_add_u32 s72,s72,491520\ns_sub_u32 s72,s72,s71\ns_add_u32 m0,s64,0\ns_nop 0\nbuffer_load_dwordx4 v240,s[52:55],s72 offen offset:0 sc1 lds\nbuffer_load_dwordx4 v240,s[52:55],s72 offen offset:1024 sc1 lds\nbuffer_load_dwordx4 v240,s[52:55],s72 offen offset:2048 sc1 lds\n" \
"buffer_load_dwordx4 v240,s[52:55],s72 offen offset:3072 sc1 lds\ns_add_u32 m0,s64,65536\ns_add_u32 s72,s72,16777216\nbuffer_load_dwordx4 v240,s[52:55],s72 offen offset:0 sc1 lds\nbuffer_load_dwordx4 v240,s[52:55],s72 offen offset:1024 sc1 lds\nbuffer_load_dwordx4 v240,s[52:55],s72 offen offset:2048 sc1 lds\nbuffer_load_dwordx4 v240,s[52:55],s72 offen offset:3072 sc1 lds\ns_add_u32 s72,s37,s64\n" \
"s_add_u32 s72,s72,507904\ns_sub_u32 s72,s72,s71\ns_add_u32 m0,s64,16384\ns_nop 0\nbuffer_load_dwordx4 v240,s[52:55],s72 offen offset:0 sc1 lds\nbuffer_load_dwordx4 v240,s[52:55],s72 offen offset:1024 sc1 lds\nbuffer_load_dwordx4 v240,s[52:55],s72 offen offset:2048 sc1 lds\nbuffer_load_dwordx4 v240,s[52:55],s72 offen offset:3072 sc1 lds\n" \
"s_add_u32 m0,s64,81920\ns_add_u32 s72,s72,16777216\nbuffer_load_dwordx4 v240,s[52:55],s72 offen offset:0 sc1 lds\nbuffer_load_dwordx4 v240,s[52:55],s72 offen offset:1024 sc1 lds\nbuffer_load_dwordx4 v240,s[52:55],s72 offen offset:2048 sc1 lds\nbuffer_load_dwordx4 v240,s[52:55],s72 offen offset:3072 sc1 lds\ns_add_u32 s72,s37,s64\ns_add_u32 s72,s72,16384\n" \
"s_add_u32 s72,s72,s71\ns_add_u32 m0,s64,32768\ns_nop 0\nbuffer_load_dwordx4 v240,s[52:55],s72 offen offset:0 sc1 lds\nbuffer_load_dwordx4 v240,s[52:55],s72 offen offset:1024 sc1 lds\nbuffer_load_dwordx4 v240,s[52:55],s72 offen offset:2048 sc1 lds\nbuffer_load_dwordx4 v240,s[52:55],s72 offen offset:3072 sc1 lds\ns_add_u32 m0,s64,98304\n" \
"s_add_u32 s72,s72,16777216\nbuffer_load_dwordx4 v240,s[52:55],s72 offen offset:0 sc1 lds\nbuffer_load_dwordx4 v240,s[52:55],s72 offen offset:1024 sc1 lds\nbuffer_load_dwordx4 v240,s[52:55],s72 offen offset:2048 sc1 lds\nbuffer_load_dwordx4 v240,s[52:55],s72 offen offset:3072 sc1 lds\ns_add_u32 s72,s37,s64\ns_add_u32 s72,s72,0\ns_add_u32 s72,s72,s71\n" \
"s_add_u32 m0,s64,49152\ns_nop 0\nbuffer_load_dwordx4 v240,s[52:55],s72 offen offset:0 sc1 lds\nbuffer_load_dwordx4 v240,s[52:55],s72 offen offset:1024 sc1 lds\nbuffer_load_dwordx4 v240,s[52:55],s72 offen offset:2048 sc1 lds\nbuffer_load_dwordx4 v240,s[52:55],s72 offen offset:3072 sc1 lds\ns_add_u32 m0,s64,114688\ns_add_u32 s72,s72,16777216\n" \
"buffer_load_dwordx4 v240,s[52:55],s72 offen offset:0 sc1 lds\nbuffer_load_dwordx4 v240,s[52:55],s72 offen offset:1024 sc1 lds\nbuffer_load_dwordx4 v240,s[52:55],s72 offen offset:2048 sc1 lds\nbuffer_load_dwordx4 v240,s[52:55],s72 offen offset:3072 sc1 lds\nv_and_b32 v251,15,v249\nv_lshrrev_b32 v250,4,v249\nv_lshlrev_b32 v250,2,v250\nv_sub_u32 v251,v251,v250\n" \
"s_lshl_b32 s70,s62,7\ns_lshl_b32 s72,s7,5\ns_sub_u32 s38,s72,s70\ns_add_u32 s38,s38,1920\ns_sub_u32 s39,s70,s72\ns_add_u32 s39,s39,96\nv_add_u32 v225,s38,v251\nv_add_u32 v225,0,v225\n" \
"v_mov_b32 v224,0xff800000\nv_add_u32 v229,s38,v251\nv_add_u32 v229,16,v229\nv_mov_b32 v228,0xff800000\nv_add_u32 v233,s39,v251\nv_add_u32 v233,0,v233\nv_mov_b32 v232,0xff800000\nv_add_u32 v237,s39,v251\n" \
"v_add_u32 v237,16,v237\nv_mov_b32 v236,0xff800000\ns_sub_u32 s38,524288,s71\ns_add_u32 s38,s38,s37\n.Lrl:\nbuffer_load_dwordx4 a[0:3],v240,s[52:55],s37 offen offset:0 sc1\nbuffer_load_dwordx4 a[4:7],v240,s[52:55],s37 offen offset:1024 sc1\nbuffer_load_dwordx4 a[8:11],v240,s[52:55],s37 offen offset:2048 sc1\n" \
"buffer_load_dwordx4 a[12:15],v240,s[52:55],s37 offen offset:3072 sc1\ns_add_u32 s37,s37,4096\nbuffer_load_dwordx4 a[16:19],v240,s[52:55],s37 offen offset:0 sc1\nbuffer_load_dwordx4 a[20:23],v240,s[52:55],s37 offen offset:1024 sc1\nbuffer_load_dwordx4 a[24:27],v240,s[52:55],s37 offen offset:2048 sc1\nbuffer_load_dwordx4 a[28:31],v240,s[52:55],s37 offen offset:3072 sc1\ns_add_u32 s37,s37,4096\nbuffer_load_dwordx4 a[32:35],v240,s[52:55],s37 offen offset:0 sc1\n" \
"buffer_load_dwordx4 a[36:39],v240,s[52:55],s37 offen offset:1024 sc1\nbuffer_load_dwordx4 a[40:43],v240,s[52:55],s37 offen offset:2048 sc1\nbuffer_load_dwordx4 a[44:47],v240,s[52:55],s37 offen offset:3072 sc1\ns_add_u32 s37,s37,4096\nbuffer_load_dwordx4 a[48:51],v240,s[52:55],s37 offen offset:0 sc1\nbuffer_load_dwordx4 a[52:55],v240,s[52:55],s37 offen offset:1024 sc1\nbuffer_load_dwordx4 a[56:59],v240,s[52:55],s37 offen offset:2048 sc1\nbuffer_load_dwordx4 a[60:63],v240,s[52:55],s37 offen offset:3072 sc1\n" \
"s_add_u32 s37,s37,4096\ns_waitcnt vmcnt(15)\nM v[128:131],a[0:3],a[128:131],0\ns_waitcnt vmcnt(14)\nM v[128:131],a[4:7],a[132:135],v[128:131]\ns_waitcnt vmcnt(13)\nM v[128:131],a[8:11],a[136:139],v[128:131]\ns_waitcnt vmcnt(12)\n" \
"M v[128:131],a[12:15],a[140:143],v[128:131]\nM v[132:135],a[0:3],a[144:147],0\nM v[132:135],a[4:7],a[148:151],v[132:135]\nM v[132:135],a[8:11],a[152:155],v[132:135]\nM v[132:135],a[12:15],a[156:159],v[132:135]\nM v[136:139],a[0:3],a[160:163],0\nM v[136:139],a[4:7],a[164:167],v[136:139]\nM v[136:139],a[8:11],a[168:171],v[136:139]\n" \
"M v[136:139],a[12:15],a[172:175],v[136:139]\nM v[140:143],a[0:3],a[176:179],0\nM v[140:143],a[4:7],a[180:183],v[140:143]\nM v[140:143],a[8:11],a[184:187],v[140:143]\nM v[140:143],a[12:15],a[188:191],v[140:143]\ns_waitcnt vmcnt(11)\nM v[144:147],a[16:19],a[128:131],0\ns_waitcnt vmcnt(10)\n" \
"M v[144:147],a[20:23],a[132:135],v[144:147]\ns_waitcnt vmcnt(9)\nM v[144:147],a[24:27],a[136:139],v[144:147]\ns_waitcnt vmcnt(8)\nM v[144:147],a[28:31],a[140:143],v[144:147]\nM v[148:151],a[16:19],a[144:147],0\nM v[148:151],a[20:23],a[148:151],v[148:151]\nM v[148:151],a[24:27],a[152:155],v[148:151]\n" \
"M v[148:151],a[28:31],a[156:159],v[148:151]\nM v[152:155],a[16:19],a[160:163],0\nM v[152:155],a[20:23],a[164:167],v[152:155]\nM v[152:155],a[24:27],a[168:171],v[152:155]\nM v[152:155],a[28:31],a[172:175],v[152:155]\nM v[156:159],a[16:19],a[176:179],0\nM v[156:159],a[20:23],a[180:183],v[156:159]\nM v[156:159],a[24:27],a[184:187],v[156:159]\n" \
"M v[156:159],a[28:31],a[188:191],v[156:159]\ns_waitcnt vmcnt(7)\nM v[160:163],a[32:35],a[128:131],0\ns_waitcnt vmcnt(6)\nM v[160:163],a[36:39],a[132:135],v[160:163]\ns_waitcnt vmcnt(5)\nM v[160:163],a[40:43],a[136:139],v[160:163]\ns_waitcnt vmcnt(4)\n" \
"M v[160:163],a[44:47],a[140:143],v[160:163]\nM v[164:167],a[32:35],a[144:147],0\nM v[164:167],a[36:39],a[148:151],v[164:167]\nM v[164:167],a[40:43],a[152:155],v[164:167]\nM v[164:167],a[44:47],a[156:159],v[164:167]\nM v[168:171],a[32:35],a[160:163],0\nM v[168:171],a[36:39],a[164:167],v[168:171]\nM v[168:171],a[40:43],a[168:171],v[168:171]\n" \
"M v[168:171],a[44:47],a[172:175],v[168:171]\nM v[172:175],a[32:35],a[176:179],0\nM v[172:175],a[36:39],a[180:183],v[172:175]\nM v[172:175],a[40:43],a[184:187],v[172:175]\nM v[172:175],a[44:47],a[188:191],v[172:175]\ns_waitcnt vmcnt(3)\nM v[176:179],a[48:51],a[128:131],0\ns_waitcnt vmcnt(2)\n" \
"M v[176:179],a[52:55],a[132:135],v[176:179]\ns_waitcnt vmcnt(1)\nM v[176:179],a[56:59],a[136:139],v[176:179]\ns_waitcnt vmcnt(0)\nM v[176:179],a[60:63],a[140:143],v[176:179]\nM v[180:183],a[48:51],a[144:147],0\nM v[180:183],a[52:55],a[148:151],v[180:183]\nM v[180:183],a[56:59],a[152:155],v[180:183]\n" \
"M v[180:183],a[60:63],a[156:159],v[180:183]\nM v[184:187],a[48:51],a[160:163],0\nM v[184:187],a[52:55],a[164:167],v[184:187]\nM v[184:187],a[56:59],a[168:171],v[184:187]\nM v[184:187],a[60:63],a[172:175],v[184:187]\nM v[188:191],a[48:51],a[176:179],0\nM v[188:191],a[52:55],a[180:183],v[188:191]\nM v[188:191],a[56:59],a[184:187],v[188:191]\n" \
"M v[188:191],a[60:63],a[188:191],v[188:191]\nv_cmp_le_i32 vcc,0,v225\ns_nop 1\nv_cndmask_b32 v128,v252,v128,vcc\nv_max_f32 v224,v224,v128\nv_cmp_le_i32 vcc,1,v225\ns_nop 1\nv_cndmask_b32 v129,v252,v129,vcc\n" \
"v_max_f32 v224,v224,v129\nv_cmp_le_i32 vcc,2,v225\ns_nop 1\nv_cndmask_b32 v130,v252,v130,vcc\nv_max_f32 v224,v224,v130\nv_cmp_le_i32 vcc,3,v225\ns_nop 1\nv_cndmask_b32 v131,v252,v131,vcc\n" \
"v_max_f32 v224,v224,v131\nv_cmp_le_i32 vcc,0,v229\ns_nop 1\nv_cndmask_b32 v132,v252,v132,vcc\nv_max_f32 v228,v228,v132\nv_cmp_le_i32 vcc,1,v229\ns_nop 1\nv_cndmask_b32 v133,v252,v133,vcc\n" \
"v_max_f32 v228,v228,v133\nv_cmp_le_i32 vcc,2,v229\ns_nop 1\nv_cndmask_b32 v134,v252,v134,vcc\nv_max_f32 v228,v228,v134\nv_cmp_le_i32 vcc,3,v229\ns_nop 1\nv_cndmask_b32 v135,v252,v135,vcc\n" \
"v_max_f32 v228,v228,v135\nv_cmp_le_i32 vcc,0,v233\ns_nop 1\nv_cndmask_b32 v136,v252,v136,vcc\nv_max_f32 v232,v232,v136\nv_cmp_le_i32 vcc,1,v233\ns_nop 1\nv_cndmask_b32 v137,v252,v137,vcc\n" \
"v_max_f32 v232,v232,v137\nv_cmp_le_i32 vcc,2,v233\ns_nop 1\nv_cndmask_b32 v138,v252,v138,vcc\nv_max_f32 v232,v232,v138\nv_cmp_le_i32 vcc,3,v233\ns_nop 1\nv_cndmask_b32 v139,v252,v139,vcc\n" \
"v_max_f32 v232,v232,v139\nv_cmp_le_i32 vcc,0,v237\ns_nop 1\nv_cndmask_b32 v140,v252,v140,vcc\nv_max_f32 v236,v236,v140\nv_cmp_le_i32 vcc,1,v237\ns_nop 1\nv_cndmask_b32 v141,v252,v141,vcc\n" \
"v_max_f32 v236,v236,v141\nv_cmp_le_i32 vcc,2,v237\ns_nop 1\nv_cndmask_b32 v142,v252,v142,vcc\nv_max_f32 v236,v236,v142\nv_cmp_le_i32 vcc,3,v237\ns_nop 1\nv_cndmask_b32 v143,v252,v143,vcc\n" \
"v_max_f32 v236,v236,v143\nv_cmp_le_i32 vcc,16,v225\ns_nop 1\nv_cndmask_b32 v144,v252,v144,vcc\nv_max_f32 v224,v224,v144\nv_cmp_le_i32 vcc,17,v225\ns_nop 1\nv_cndmask_b32 v145,v252,v145,vcc\n" \
"v_max_f32 v224,v224,v145\nv_cmp_le_i32 vcc,18,v225\ns_nop 1\nv_cndmask_b32 v146,v252,v146,vcc\nv_max_f32 v224,v224,v146\nv_cmp_le_i32 vcc,19,v225\ns_nop 1\nv_cndmask_b32 v147,v252,v147,vcc\n" \
"v_max_f32 v224,v224,v147\nv_cmp_le_i32 vcc,16,v229\ns_nop 1\nv_cndmask_b32 v148,v252,v148,vcc\nv_max_f32 v228,v228,v148\nv_cmp_le_i32 vcc,17,v229\ns_nop 1\nv_cndmask_b32 v149,v252,v149,vcc\n" \
"v_max_f32 v228,v228,v149\nv_cmp_le_i32 vcc,18,v229\ns_nop 1\nv_cndmask_b32 v150,v252,v150,vcc\nv_max_f32 v228,v228,v150\nv_cmp_le_i32 vcc,19,v229\ns_nop 1\nv_cndmask_b32 v151,v252,v151,vcc\n" \
"v_max_f32 v228,v228,v151\nv_cmp_le_i32 vcc,16,v233\ns_nop 1\nv_cndmask_b32 v152,v252,v152,vcc\nv_max_f32 v232,v232,v152\nv_cmp_le_i32 vcc,17,v233\ns_nop 1\nv_cndmask_b32 v153,v252,v153,vcc\n" \
"v_max_f32 v232,v232,v153\nv_cmp_le_i32 vcc,18,v233\ns_nop 1\nv_cndmask_b32 v154,v252,v154,vcc\nv_max_f32 v232,v232,v154\nv_cmp_le_i32 vcc,19,v233\ns_nop 1\nv_cndmask_b32 v155,v252,v155,vcc\n" \
"v_max_f32 v232,v232,v155\nv_cmp_le_i32 vcc,16,v237\ns_nop 1\nv_cndmask_b32 v156,v252,v156,vcc\nv_max_f32 v236,v236,v156\nv_cmp_le_i32 vcc,17,v237\ns_nop 1\nv_cndmask_b32 v157,v252,v157,vcc\n" \
"v_max_f32 v236,v236,v157\nv_cmp_le_i32 vcc,18,v237\ns_nop 1\nv_cndmask_b32 v158,v252,v158,vcc\nv_max_f32 v236,v236,v158\nv_cmp_le_i32 vcc,19,v237\ns_nop 1\nv_cndmask_b32 v159,v252,v159,vcc\n" \
"v_max_f32 v236,v236,v159\nv_cmp_le_i32 vcc,32,v225\ns_nop 1\nv_cndmask_b32 v160,v252,v160,vcc\nv_max_f32 v224,v224,v160\nv_cmp_le_i32 vcc,33,v225\ns_nop 1\nv_cndmask_b32 v161,v252,v161,vcc\n" \
"v_max_f32 v224,v224,v161\nv_cmp_le_i32 vcc,34,v225\ns_nop 1\nv_cndmask_b32 v162,v252,v162,vcc\nv_max_f32 v224,v224,v162\nv_cmp_le_i32 vcc,35,v225\ns_nop 1\nv_cndmask_b32 v163,v252,v163,vcc\n" \
"v_max_f32 v224,v224,v163\nv_cmp_le_i32 vcc,32,v229\ns_nop 1\nv_cndmask_b32 v164,v252,v164,vcc\nv_max_f32 v228,v228,v164\nv_cmp_le_i32 vcc,33,v229\ns_nop 1\nv_cndmask_b32 v165,v252,v165,vcc\n" \
"v_max_f32 v228,v228,v165\nv_cmp_le_i32 vcc,34,v229\ns_nop 1\nv_cndmask_b32 v166,v252,v166,vcc\nv_max_f32 v228,v228,v166\nv_cmp_le_i32 vcc,35,v229\ns_nop 1\nv_cndmask_b32 v167,v252,v167,vcc\n" \
"v_max_f32 v228,v228,v167\nv_cmp_le_i32 vcc,32,v233\ns_nop 1\nv_cndmask_b32 v168,v252,v168,vcc\nv_max_f32 v232,v232,v168\nv_cmp_le_i32 vcc,33,v233\ns_nop 1\nv_cndmask_b32 v169,v252,v169,vcc\n" \
"v_max_f32 v232,v232,v169\nv_cmp_le_i32 vcc,34,v233\ns_nop 1\nv_cndmask_b32 v170,v252,v170,vcc\nv_max_f32 v232,v232,v170\nv_cmp_le_i32 vcc,35,v233\ns_nop 1\nv_cndmask_b32 v171,v252,v171,vcc\n" \
"v_max_f32 v232,v232,v171\nv_cmp_le_i32 vcc,32,v237\ns_nop 1\nv_cndmask_b32 v172,v252,v172,vcc\nv_max_f32 v236,v236,v172\nv_cmp_le_i32 vcc,33,v237\ns_nop 1\nv_cndmask_b32 v173,v252,v173,vcc\n" \
"v_max_f32 v236,v236,v173\nv_cmp_le_i32 vcc,34,v237\ns_nop 1\nv_cndmask_b32 v174,v252,v174,vcc\nv_max_f32 v236,v236,v174\nv_cmp_le_i32 vcc,35,v237\ns_nop 1\nv_cndmask_b32 v175,v252,v175,vcc\n" \
"v_max_f32 v236,v236,v175\nv_cmp_le_i32 vcc,48,v225\ns_nop 1\nv_cndmask_b32 v176,v252,v176,vcc\nv_max_f32 v224,v224,v176\nv_cmp_le_i32 vcc,49,v225\ns_nop 1\nv_cndmask_b32 v177,v252,v177,vcc\n" \
"v_max_f32 v224,v224,v177\nv_cmp_le_i32 vcc,50,v225\ns_nop 1\nv_cndmask_b32 v178,v252,v178,vcc\nv_max_f32 v224,v224,v178\nv_cmp_le_i32 vcc,51,v225\ns_nop 1\nv_cndmask_b32 v179,v252,v179,vcc\n" \
"v_max_f32 v224,v224,v179\nv_cmp_le_i32 vcc,48,v229\ns_nop 1\nv_cndmask_b32 v180,v252,v180,vcc\nv_max_f32 v228,v228,v180\nv_cmp_le_i32 vcc,49,v229\ns_nop 1\nv_cndmask_b32 v181,v252,v181,vcc\n" \
"v_max_f32 v228,v228,v181\nv_cmp_le_i32 vcc,50,v229\ns_nop 1\nv_cndmask_b32 v182,v252,v182,vcc\nv_max_f32 v228,v228,v182\nv_cmp_le_i32 vcc,51,v229\ns_nop 1\nv_cndmask_b32 v183,v252,v183,vcc\n" \
"v_max_f32 v228,v228,v183\nv_cmp_le_i32 vcc,48,v233\ns_nop 1\nv_cndmask_b32 v184,v252,v184,vcc\nv_max_f32 v232,v232,v184\nv_cmp_le_i32 vcc,49,v233\ns_nop 1\nv_cndmask_b32 v185,v252,v185,vcc\n" \
"v_max_f32 v232,v232,v185\nv_cmp_le_i32 vcc,50,v233\ns_nop 1\nv_cndmask_b32 v186,v252,v186,vcc\nv_max_f32 v232,v232,v186\nv_cmp_le_i32 vcc,51,v233\ns_nop 1\nv_cndmask_b32 v187,v252,v187,vcc\n" \
"v_max_f32 v232,v232,v187\nv_cmp_le_i32 vcc,48,v237\ns_nop 1\nv_cndmask_b32 v188,v252,v188,vcc\nv_max_f32 v236,v236,v188\nv_cmp_le_i32 vcc,49,v237\ns_nop 1\nv_cndmask_b32 v189,v252,v189,vcc\n" \
"v_max_f32 v236,v236,v189\nv_cmp_le_i32 vcc,50,v237\ns_nop 1\nv_cndmask_b32 v190,v252,v190,vcc\nv_max_f32 v236,v236,v190\nv_cmp_le_i32 vcc,51,v237\ns_nop 1\nv_cndmask_b32 v191,v252,v191,vcc\n" \
"v_max_f32 v236,v236,v191\nv_subrev_u32 v225,64,v225\nv_subrev_u32 v229,64,v229\nv_subrev_u32 v233,64,v233\nv_subrev_u32 v237,64,v237\ns_cmp_lt_u32 s37,s38\ns_cbranch_scc1 .Lrl\ns_waitcnt vmcnt(0)\n" \
"s_barrier\nds_read_b128 a[0:3],v240 offset:0\nds_read_b128 a[4:7],v240 offset:1024\nds_read_b128 a[8:11],v240 offset:2048\nds_read_b128 a[12:15],v240 offset:3072\nds_read_b128 a[16:19],v240 offset:4096\nds_read_b128 a[20:23],v240 offset:5120\nds_read_b128 a[24:27],v240 offset:6144\n" \
"ds_read_b128 a[28:31],v240 offset:7168\nds_read_b128 a[32:35],v240 offset:8192\nds_read_b128 a[36:39],v240 offset:9216\nds_read_b128 a[40:43],v240 offset:10240\nds_read_b128 a[44:47],v240 offset:11264\nds_read_b128 a[48:51],v240 offset:12288\nds_read_b128 a[52:55],v240 offset:13312\nds_read_b128 a[56:59],v240 offset:14336\n" \
"ds_read_b128 a[60:63],v240 offset:15360\ns_cmp_eq_u32 s62,0\ns_cselect_b32 s36,0x8000,0\ns_add_u32 s36,s36,s64\ns_cmp_eq_u32 s36,s96\ns_cselect_b32 s72,0xc000,s98\ns_add_u32 s65,s36,s72\ns_cmp_eq_u32 s65,s96\n" \
"s_cselect_b32 s72,0xc000,s98\ns_add_u32 s66,s65,s72\ns_cmp_eq_u32 s66,s96\ns_cselect_b32 s72,0xc000,s98\ns_add_u32 s95,s66,s72\ns_add_u32 s97,s96,98304\ns_add_u32 s15,s64,s46\ns_add_u32 s15,s15,32768\n" \
"s_mov_b32 s94,0xc000\ns_mov_b32 s56,s15\ns_cmp_lt_u32 s62,6\ns_cselect_b32 s72,65536,0\ns_sub_u32 s15,s15,s72\nv_and_b32 v251,31,v249\nv_lshlrev_b32 v251,2,v251\ns_lshl_b32 s80,s63,7\n" \
"s_add_u32 s80,s80,86016\nv_add_u32 v250,0xc000,v241\ns_waitcnt lgkmcnt(0)\ns_branch .Lattn\n.Lfin:\n" \
""
typedef __attribute__((address_space(1))) unsigned gu32;
typedef __attribute__((address_space(1))) unsigned long long gu64;
#define RLX_AGENT __ATOMIC_RELAXED, __HIP_MEMORY_SCOPE_AGENT
constexpr size_t WS_HO = 65536, WS_DBG = (size_t)51 << 20, WS_PART = (size_t)52 << 20;
constexpr unsigned HO_FP = 8192, HO_P2 = 32768, HO_KR = HO_P2 + 224 * DM * 4, LDS_BYTES = 163840, WX = 131072, SPIN_CAP = 1u << 20;
#define PSLEEP 4
struct Args {
    const u16 *x, *ckv, *w_q, *w_uk, *w_uv, *w_o, *w_r, *wg, *wu, *wd, *cvec, *w_mod;
    char* ws; u16* out;
};
typedef const __attribute__((address_space(4))) Args* kargs_t;
DI void st16_dev(void* p, const u32x4& v) { asm volatile("global_store_dwordx4 %0, %1, off sc1\n\ts_nop 1" :: "v"(p), "v"(v) : "memory"); }
DI void ld16_dev(u32x4& v, const void* p) { asm volatile("global_load_dwordx4 %0, %1, off sc1" : "=v"(v) : "v"(p) : "memory"); }
DI void sum8(const unsigned* w, float& lo, float& hi) {
    lo = ((lo2f(w[0]) + lo2f(w[1])) + (lo2f(w[2]) + lo2f(w[3]))) + ((lo2f(w[4]) + lo2f(w[5])) + (lo2f(w[6]) + lo2f(w[7])));
    hi = ((hi2f(w[0]) + hi2f(w[1])) + (hi2f(w[2]) + hi2f(w[3]))) + ((hi2f(w[4]) + hi2f(w[5])) + (hi2f(w[6]) + hi2f(w[7])));
}
DI unsigned ord16(float r) { const unsigned u = __float_as_uint(r) >> 16; return (r != r) ? 0u : ((u & 0x8000u) ? (~u & 0xffffu) : (u | 0x8000u)); }
DI float ord16_val(unsigned o) { return __uint_as_float(((o & 0x8000u) ? (o & 0x7fffu) : (~o & 0xffffu)) << 16); }
#define STAMPD(i) do {} while (0)
#define C8(p, a) p #a "0", p #a "1", p #a "2", p #a "3", p #a "4", p #a "5", p #a "6", p #a "7", p #a "8", p #a "9"
#define C100(p, a) C8(p, a##0), C8(p, a##1), C8(p, a##2), C8(p, a##3), C8(p, a##4), C8(p, a##5), C8(p, a##6), C8(p, a##7), C8(p, a##8), C8(p, a##9)
#define C10_99(p) C8(p, 1), C8(p, 2), C8(p, 3), C8(p, 4), C8(p, 5), C8(p, 6), C8(p, 7), C8(p, 8), C8(p, 9)
#define C200_249(p) C8(p, 20), C8(p, 21), C8(p, 22), C8(p, 23), C8(p, 24)
#define BODY_CLOB "memory", "vcc", "scc", "s8", "s9", C8("s", 1), C8("s", 2), "s30", "s31", "s34", "s35", "s36", "s37", "s38", "s39", C8("s", 4), C8("s", 5), C8("s", 6), C8("s", 7), C8("s", 8), C8("s", 9), \
    C8("v", ), C10_99("v"), C8("v", 10), "v110", "v111", C8("v", 12), C8("v", 13), C8("v", 14), C8("v", 15), C8("v", 16), C8("v", 17), C8("v", 18), "v190", "v191", "v228", "v229", C8("v", 23), C8("v", 24), "v250", "v251", "v252", "v253", "v254", \
    C8("a", ), C10_99("a"), C100("a", 1), C200_249("a"), "a250", "a251", "a252", "a253", "a254", "a255"
typedef unsigned u32x8 __attribute__((ext_vector_type(8)));

__global__ __launch_bounds__(256, 1) void k_all(const Args a_) {
    extern __shared__ __attribute__((aligned(16))) char lds[];
    kargs_t ap = (kargs_t)__builtin_amdgcn_kernarg_segment_ptr(); unsigned bid = blockIdx.x, wid = rfl(threadIdx.x >> 6);
    u32x16 FW; u32x8 fd, MPR; u32x4 ooL, ooH, KY; unsigned sv_;
    asm volatile(BODY_ASM : "+{s[4:5]}"(ap), "+{s6}"(bid), "+{s7}"(wid), "={v[192:207]}"(FW), "={v[208:215]}"(fd), "={v[216:219]}"(ooL), "={v[220:223]}"(ooH), "={v[224:227]}"(KY), "={v[112:119]}"(MPR), "={v255}"(sv_) :: BODY_CLOB);
    bid = rfl(bid); wid = rfl(wid);
    char* const ws = ap->ws; char* const ho = ws + WS_HO; const unsigned lane = lane_id();
    const unsigned g = bid & 7u, idx = bid >> 3, j = idx & 3u, xx = idx >> 2, head = 4u * g + j, tid = wid * 64u + lane;
    const u16* __restrict__ x = ap->x; u16* const out = ap->out; const char* const part = ws + WS_PART;
    const unsigned rowL = xx * 128u + 4u * head + wid, rowH = (15u - xx) * 128u + 4u * head + wid;
    const bool ffn = idx < 28u; const unsigned fb = g * 28u + (ffn ? idx : 0u);
    const unsigned c0 = wid * 128u + (lane & 3u) * 32u + (lane >> 2);
    const unsigned kwl = KY[0], kwg = KY[1], kL = KY[2], kH = KY[3];
    u32x2* sk = (u32x2*)lds;
    float* red = (float*)(lds + 1024);
    float* red3 = (float*)(lds + 2048);
    float* fin = (float*)(lds + 4096);
    if (wid == 0) {
        unsigned kh = 0, kl = 0;
        for (unsigned spins = 0; spins < SPIN_CAP; ++spins) {
            u32x4 ka, kb; ld16_dev(ka, ho + HO_KR + g * 2048u + lane * 32u); ld16_dev(kb, ho + HO_KR + g * 2048u + lane * 32u + 16u);
            asm volatile("s_waitcnt vmcnt(0)" : "+v"(ka), "+v"(kb) :: "memory");
            if (lane == (bid >> 2)) { const unsigned o = bid & 3u;
                if (o == 0) { ka[0] = kwl; ka[1] = kwg; } if (o == 1) { ka[2] = kwl; ka[3] = kwg; } if (o == 2) { kb[0] = kwl; kb[1] = kwg; } if (o == 3) { kb[2] = kwl; kb[3] = kwg; } }
            const u32x2 p = ka[1] > ka[3] ? (u32x2){ka[0], ka[1]} : (u32x2){ka[2], ka[3]}, q = kb[1] > kb[3] ? (u32x2){kb[0], kb[1]} : (u32x2){kb[2], kb[3]};
            kh = p[1] > q[1] ? p[1] : q[1]; kl = p[1] > q[1] ? p[0] : q[0];
            if (__all(ka[1] != 0u && ka[3] != 0u && kb[1] != 0u && kb[3] != 0u)) break;
            __builtin_amdgcn_s_sleep(PSLEEP);
        }
        const unsigned mh = wave_umax(kh);
        if (kh == mh) sk[4] = (u32x2){kl, kh};
    }
    LDSBAR();
    const u32x2 key = sk[4];
    const unsigned sel = (2047u - ((key[1] >> 5) & 2047u)) & 2047u;
    STAMPD(14);
    unsigned h2w;
    {   unsigned p[8];
#pragma unroll
        for (int zz = 0; zz < 8; ++zz) p[zz] = __hip_atomic_load((gu32*)(part + ((size_t)zz * SEQ + sel) * (DM * 2) + 4u * tid), RLX_AGENT);
        const unsigned xa = x[(size_t)sel * DM + c0], xb = x[(size_t)sel * DM + c0 + 16u];
        if (key[1] != kwg) {
            if (kL == kwg) st16_dev(out + (size_t)rowL * DM + lane * 8, ooL);
            if (kH == kwg) st16_dev(out + (size_t)rowH * DM + lane * 8, ooH); }
        float a0, a1; sum8(p, a0, a1);
        const float m = hi2f(key[0]), rden = __builtin_amdgcn_rcpf(lo2f(key[0]));
#define MP(i) __uint_as_float(MPR[i])
        const float x10 = rb(lo2f(xa) + rb(MP(0) * rb(a0))), x11 = rb(lo2f(xb) + rb(MP(1) * rb(a1)));
        h2w = cvt2(rb(rb(rb(x10 - m) * rden) * rb(1.f + MP(4))) + MP(2), rb(rb(rb(x11 - m) * rden) * rb(1.f + MP(5))) + MP(3));
        if (bid < 64u) { fin[c0] = x10; fin[c0 + 16u] = x11; fin[512u + c0] = lo2f(h2w); fin[528u + c0] = hi2f(h2w); fin[1024u + c0] = MP(6); fin[1040u + c0] = MP(7); } }
    if (ffn) {
        float v[16];
#pragma unroll
        for (int mtx = 0; mtx < 2; ++mtx)
#pragma unroll
            for (int e = 0; e < 4; ++e) { const unsigned w0 = FW[8 * mtx + e], w1 = FW[8 * mtx + 4 + e];
                v[8 * mtx + 2 * e] = dot2bf(__builtin_amdgcn_perm(w1, w0, 0x05040100u), h2w, 0.f); v[8 * mtx + 2 * e + 1] = dot2bf(__builtin_amdgcn_perm(w1, w0, 0x07060302u), h2w, 0.f); }
        float v8[8], v4[4];
#pragma unroll
        for (int i = 0; i < 8; ++i) v8[i] = swap32_sum(v[i], v[8 + i]);
#pragma unroll
        for (int i = 0; i < 4; ++i) v4[i] = row16_sum(swap16_sum(v8[i], v8[4 + i]));
        if ((lane & 15u) == 0) *reinterpret_cast<f32x4*>(red + wid * 16u + (lane >> 4) * 4u) = (f32x4){v4[0], v4[1], v4[2], v4[3]};
        LDSBAR();
        const unsigned f8 = lane & 7u;
        const float gg = rb((red[f8] + red[16 + f8]) + (red[32 + f8] + red[48 + f8])), uu = rb((red[8 + f8] + red[24 + f8]) + (red[40 + f8] + red[56 + f8]));
        const float gu = rb(rb(gg / (1.f + __expf(-gg))) * uu);
        float a0 = 0.f, a1 = 0.f;
#pragma unroll
        for (int i = 0; i < 8; ++i) { const float gv = __builtin_bit_cast(float, __builtin_amdgcn_readlane(__builtin_bit_cast(int, gu), i)); a0 = fmaf(gv, lo2f(fd[i]), a0); a1 = fmaf(gv, hi2f(fd[i]), a1); }
        const unsigned long long o = ((unsigned long long)min(__float_as_uint(a1), 0xfffffffeu) << 32) | min(__float_as_uint(a0), 0xfffffffeu);
        __hip_atomic_store((gu64*)(ho + HO_P2) + fb * 256u + tid, o, RLX_AGENT);
    }
    STAMPD(15);
    if (bid < 64u) {
        const unsigned cg = g * 8u + idx;
        const char* gp = ho + HO_P2 + (tid >> 1) * 2048u + cg * 32u + (tid & 1u) * 16u;
        u32x4 ga_ = {}, gb_ = {};
        for (unsigned spins = 0; spins < SPIN_CAP; ++spins) {
            ld16_dev(ga_, gp); ld16_dev(gb_, gp + (tid < 192u ? 128u * 2048u : 0u));
            asm volatile("s_waitcnt vmcnt(0)" : "+v"(ga_), "+v"(gb_) :: "memory");
            const unsigned mx = max(max(max(ga_[0], ga_[1]), max(ga_[2], ga_[3])), max(max(gb_[0], gb_[1]), max(gb_[2], gb_[3])));
            if (__all(mx != 0xffffffffu)) break;
            __builtin_amdgcn_s_sleep(PSLEEP);
        }
        float s4[4];
#pragma unroll
        for (int i = 0; i < 4; ++i) { float s = __uint_as_float(ga_[i]) + (tid < 192u ? __uint_as_float(gb_[i]) : 0.f);
            s += DPPF(s, 0x4E); s += DPPF(s, 0x124); s += DPPF(s, 0x128);
            s = swap16_sum(s, s); s4[i] = swap32_sum(s, s); }
        if (lane < 2) *reinterpret_cast<f32x4*>(red3 + wid * 8u + lane * 4u) = (f32x4){s4[0], s4[1], s4[2], s4[3]};
        LDSBAR();
        if (tid < 4u) { const unsigned t = tid, cc = cg * 8u + 2u * t;
            const float s0 = (red3[2 * t] + red3[8 + 2 * t]) + (red3[16 + 2 * t] + red3[24 + 2 * t]), s1 = (red3[2 * t + 1] + red3[9 + 2 * t]) + (red3[17 + 2 * t] + red3[25 + 2 * t]);
            const float rsel = rb(1.f / (1.f + __expf(-ord16_val(key[1] >> 16))));
            const float y0 = rb(rb(s0) * rsel), y1 = rb(rb(s1) * rsel);
            const unsigned ow = cvt2(fin[cc] + rb(fin[1024u + cc] * rb(fin[512u + cc] + y0)), fin[cc + 1u] + rb(fin[1025u + cc] * rb(fin[513u + cc] + y1)));
            __hip_atomic_store((gu32*)(out + (size_t)sel * DM + cc), ow, RLX_AGENT);
        }
    }
    asm volatile(".Lcode_e:");
}
extern "C" void kernel_launch(void* const* d_in, const int* in_sizes, int n_in, void* d_out, int out_size, void* d_ws, size_t ws_size, hipStream_t stream) {
    Args a;
    a.x = (const u16*)d_in[0]; a.ckv = (const u16*)d_in[1]; a.w_q = (const u16*)d_in[2]; a.w_uk = (const u16*)d_in[3]; a.w_uv = (const u16*)d_in[4]; a.w_o = (const u16*)d_in[5];
    a.w_r = (const u16*)d_in[7]; a.wg = (const u16*)d_in[8]; a.wu = (const u16*)d_in[9]; a.wd = (const u16*)d_in[10]; a.cvec = (const u16*)d_in[11]; a.w_mod = (const u16*)d_in[12];
    a.ws = (char*)d_ws; a.out = (u16*)d_out;
    (void)hipFuncSetAttribute((const void*)k_all, hipFuncAttributeMaxDynamicSharedMemorySize, LDS_BYTES);
    k_all<<<256, 256, LDS_BYTES, stream>>>(a);
}
```

```cpp
#include <hip/hip_runtime.h>
#include <hip/hip_bf16.h>
#include <stdint.h>
typedef unsigned short u16;
typedef float f32x4 __attribute__((ext_vector_type(4)));
typedef float f32x2 __attribute__((ext_vector_type(2)));
typedef unsigned u32x4 __attribute__((ext_vector_type(4)));
typedef unsigned u32x2 __attribute__((ext_vector_type(2)));
typedef unsigned u32x16 __attribute__((ext_vector_type(16)));
typedef __bf16 bf16x2_t __attribute__((ext_vector_type(2)));
constexpr int SEQ = 2048, DM = 512, FF = 1792;
#define DI __device__ __forceinline__
#define LDSBAR() do { asm volatile("s_waitcnt lgkmcnt(0)" ::: "memory"); __builtin_amdgcn_s_barrier(); asm volatile("" ::: "memory"); } while (0)
DI unsigned lane_id() { unsigned l; asm volatile("v_mbcnt_lo_u32_b32 %0, -1, 0\n\tv_mbcnt_hi_u32_b32 %0, -1, %0" : "=v"(l)); return l; }
DI unsigned rfl(unsigned x) { return __builtin_amdgcn_readfirstlane(x); }
DI unsigned cvt2(float lo, float hi) { f32x2 v = {lo, hi}; bf16x2_t b = __builtin_convertvector(v, bf16x2_t); return __builtin_bit_cast(unsigned, b); }
DI float rb(float x) { return __uint_as_float(cvt2(0.f, x)); }
DI float dot2bf(unsigned a, unsigned b, float c) { return __builtin_amdgcn_fdot2_f32_bf16(__builtin_bit_cast(bf16x2_t, a), __builtin_bit_cast(bf16x2_t, b), c, false); }
DI float lo2f(unsigned w) { return __uint_as_float(w << 16); }
DI float hi2f(unsigned w) { return __uint_as_float(w & 0xffff0000u); }
#define DPPF(v, ctl) __builtin_bit_cast(float, __builtin_amdgcn_update_dpp(0, __builtin_bit_cast(int, v), ctl, 0xF, 0xF, true))
DI float row16_sum(float v) { v += DPPF(v, 0xB1); v += DPPF(v, 0x4E); v += DPPF(v, 0x141); v += DPPF(v, 0x140); return v; }
DI float swap16_sum(float a, float b) { auto r = __builtin_amdgcn_permlane16_swap(__float_as_uint(a), __float_as_uint(b), false, false); return __uint_as_float(r[0]) + __uint_as_float(r[1]); }
DI float swap32_sum(float a, float b) { auto r = __builtin_amdgcn_permlane32_swap(__float_as_uint(a), __float_as_uint(b), false, false); return __uint_as_float(r[0]) + __uint_as_float(r[1]); }
DI float wave_sum2(float v) { v = row16_sum(v); v = swap16_sum(v, v); return swap32_sum(v, v); }
DI unsigned wave_umax(unsigned v) {
    v = max(v, (unsigned)__builtin_amdgcn_update_dpp(0, (int)v, 0xB1, 0xF, 0xF, true)); v = max(v, (unsigned)__builtin_amdgcn_update_dpp(0, (int)v, 0x4E, 0xF, 0xF, true));
    v = max(v, (unsigned)__builtin_amdgcn_update_dpp(0, (int)v, 0x141, 0xF, 0xF, true)); v = max(v, (unsigned)__builtin_amdgcn_update_dpp(0, (int)v, 0x140, 0xF, 0xF, true));
    { auto r = __builtin_amdgcn_permlane16_swap(v, v, false, false); v = max((unsigned)r[0], (unsigned)r[1]); }
    { auto r = __builtin_amdgcn_permlane32_swap(v, v, false, false); v = max((unsigned)r[0], (unsigned)r[1]); }
    return v;
}
#define BODY_ASM ".macro M a,b,c,d\nv_mfma_f32_16x16x32_bf16 \\a,\\b,\\c,\\d\n.endm\n" \
".Lcode_b:\ns_load_dwordx16 s[8:23],s[4:5],0x0\ns_load_dwordx8 s[24:31],s[4:5],0x40\ns_load_dwordx2 s[34:35],s[4:5],0x60\nv_mbcnt_lo_u32_b32 v249,-1,0\nv_mbcnt_hi_u32_b32 v249,-1,v249\ns_getpc_b64 s[56:57]\n.Lpc0:\n" \
"s_and_b32 s57,s57,0xffff\ns_mov_b32 s58,.Lcode_e-.Lpc0\ns_mov_b32 s59,0x20000\ns_bfe_u32 s61,s6,0x20003\nv_lshlrev_b32 v251,7,v249\ns_lshl_b32 s64,s7,12\ns_lshl_b32 s70,s7,13\ns_cmp_eq_u32 s61,0\n" \
"s_cselect_b32 s70,s70,0x8000000\nbuffer_load_dword v247,v251,s[56:59],s70 offen\ns_and_b32 s60,s6,7\ns_lshr_b32 s62,s6,5\ns_lshl2_add_u32 s63,s60,s61\nv_lshlrev_b32 v240,4,v249\ns_cmp_lt_u32 s6,192\ns_cselect_b32 s70,s6,0\n" \
"s_mul_i32 s71,s70,171\ns_lshr_b32 s71,s71,13\ns_mul_i32 s72,s71,48\ns_sub_u32 s70,s70,s72\ns_lshl2_add_u32 s72,s71,s7\ns_lshl_b32 s72,s72,5\nv_lshrrev_b32 v0,3,v249\nv_lshl_add_u32 v1,v0,2,s72\n" \
"v_and_b32 v2,7,v249\nv_mul_u32_u24 v3,6144,v1\nv_lshl_add_u32 v3,v2,4,v3\ns_lshl_b32 s72,s70,7\ns_add_u32 s72,s72,3072\nv_add_u32 v3,s72,v3\nv_add_u32 v4,12288,v3\nv_lshlrev_b32 v1,1,v1\n" \
"s_mul_i32 s71,s71,12288\ns_lshl_b32 s70,s70,8\ns_add_u32 s79,s70,s71\ns_add_u32 s79,s79,786432\ns_cmp_eq_u32 s7,0\ns_cselect_b32 s75,0,0x8000000\ns_cmp_lt_u32 s6,192\ns_cselect_b32 s70,s75,0x8000000\n" \
"s_or_b32 s79,s79,s70\ns_lshl_b32 s70,s62,7\ns_xor_b32 s71,s63,12\ns_lshl3_add_u32 s71,s71,s70\ns_lshl_b32 s72,s63,3\ns_sub_u32 s72,s72,s70\ns_add_u32 s72,s72,1792\ns_cmp_lt_u32 s63,16\n" \
"s_cselect_b32 s71,s71,s72\ns_lshl_b32 s71,s71,10\ns_lshl_b32 s72,s7,7\ns_add_u32 s71,s71,s72\nv_lshlrev_b32 v5,10,v2\nv_lshl_add_u32 v5,v0,4,v5\nv_add_u32 v5,s71,v5\nv_bfe_u32 v6,v249,2,3\n" \
"v_lshrrev_b32 v7,5,v249\nv_and_b32 v8,48,v240\nv_lshlrev_b32 v6,13,v6\nv_lshl_or_b32 v7,v7,6,v8\nv_or_b32 v6,0x1000,v6\nv_or_b32 v243,v6,v7\nv_add_u32 v244,0xfffffc80,v243\nv_xor_b32 v6,32,v243\n" \
"v_add_u32 v245,63488,v6\nv_add_u32 v246,62592,v6\nv_lshrrev_b32 v6,4,v249\nv_xor_b32 v7,v6,v2\nv_lshlrev_b32 v7,4,v7\nv_lshl_or_b32 v224,v0,10,v7\nv_xor_b32 v7,64,v224\nv_add_u32 v225,7168,v7\n" \
"v_add_u32 v226,14336,v224\nv_add_u32 v227,21504,v7\ns_lshl_b32 s70,s62,1\ns_sub_u32 s71,s7,s70\ns_add_u32 s71,s71,30\ns_sub_u32 s72,s70,s7\ns_add_u32 s72,s72,3\ns_cmp_lt_u32 s7,2\n" \
"s_cselect_b32 s74,s71,s72\ns_lshl_b32 s66,s74,16\ns_mul_i32 s65,s7,24576\ns_lshl_b32 s68,s7,17\ns_lshl_b32 s70,s63,8\ns_add_u32 s68,s68,s70\ns_mov_b32 s38,2097152\ns_mov_b32 s39,0x20000\n" \
"s_mov_b32 s42,4198400\ns_mov_b32 s43,0x20000\ns_mov_b32 s54,71303168\ns_mov_b32 s55,0x20000\ns_mov_b32 s70,94208\ns_waitcnt lgkmcnt(0)\ns_mov_b64 s[52:53],s[34:35]\ns_mov_b64 s[36:37],s[10:11]\n" \
"s_sub_u32 s40,s14,0x1000\ns_subb_u32 s41,s15,0\nbuffer_load_dwordx2 v[230:231],off,s[52:55],s70 sc1\nglobal_load_dwordx2 v[10:11],v1,s[28:29]\nglobal_load_dwordx4 v[12:15],v3,s[30:31] offset:-3072\nglobal_load_dwordx4 v[16:19],v3,s[30:31] offset:3072\nglobal_load_dwordx4 v[20:23],v4,s[30:31] offset:-3072\nglobal_load_dwordx4 v[24:27],v4,s[30:31] offset:3072\n" \
"global_load_dwordx4 v[128:131],v5,s[8:9]\nglobal_load_dwordx4 v[132:135],v5,s[8:9] offset:512\ns_add_u32 m0,s64,98304\ns_add_u32 s69,s68,0\nbuffer_load_dwordx4 v243,s[40:43],s69 offen offset:0 lds\nbuffer_load_dwordx4 v244,s[40:43],s69 offen offset:1024 lds\nbuffer_load_dwordx4 v245,s[40:43],s69 offen offset:2048 lds\nbuffer_load_dwordx4 v246,s[40:43],s69 offen offset:3072 lds\n" \
"s_add_u32 m0,s65,0\ns_add_u32 s67,s66,0\nbuffer_load_dwordx4 v224,s[36:39],s67 offen offset:0 lds\nbuffer_load_dwordx4 v225,s[36:39],s67 offen offset:1024 lds\nbuffer_load_dwordx4 v226,s[36:39],s67 offen offset:2048 lds\nbuffer_load_dwordx4 v227,s[36:39],s67 offen offset:3072 lds\ns_add_u32 m0,s65,4096\ns_add_u32 s67,s66,32768\n" \
"buffer_load_dwordx4 v224,s[36:39],s67 offen offset:0 lds\nbuffer_load_dwordx4 v225,s[36:39],s67 offen offset:1024 lds\nbuffer_load_dwordx4 v226,s[36:39],s67 offen offset:2048 lds\nbuffer_load_dwordx4 v227,s[36:39],s67 offen offset:3072 lds\ns_min_u32 s70,s6,223\ns_lshl_b32 s70,s70,11\ns_lshl_b32 s71,s7,9\ns_add_u32 s70,s70,s71\n" \
"s_add_u32 s70,s70,98304\nv_mov_b32 v28,-1\nv_mov_b32 v29,-1\nv_lshlrev_b32 v30,3,v249\nbuffer_store_dwordx2 v[28:29],v30,s[52:55],s70 offen sc1\ns_lshl_b32 s70,s6,7\ns_cmp_lt_u32 s6,224\ns_cselect_b32 s71,65536,0x40000000\n" \
"s_cmp_lt_u32 s6,128\ns_cselect_b32 s72,556928,0x40000000\nv_mov_b32 v31,s71\nv_mov_b32 v32,s72\nv_cmp_lt_u32 vcc,31,v249\ns_or_b32 s70,s70,s75\nv_mov_b32 v33,0\nv_cndmask_b32 v31,v31,v32,vcc\n" \
"v_lshl_add_u32 v31,v249,2,v31\nbuffer_store_dword v33,v31,s[52:55],s70 offen sc1\nv_and_b32 v34,15,v249\nv_lshrrev_b32 v35,1,v34\nv_xor_b32 v35,v35,v6\nv_lshlrev_b32 v35,4,v35\nv_lshl_or_b32 v35,v34,7,v35\nv_add_u32 v228,s65,v35\n" \
"v_xor_b32 v229,64,v228\nv_bfe_u32 v34,v249,2,2\nv_and_b32 v35,3,v249\nv_lshlrev_b32 v35,3,v35\nv_lshl_or_b32 v35,v34,6,v35\nv_lshl_or_b32 v35,v6,11,v35\nv_and_b32 v34,16,v249\nv_lshl_or_b32 v35,v34,1,v35\n" \
"v_add_u32 v241,98304,v35\nv_xor_b32 v242,32,v241\ns_mov_b32 s73,0x400000\nv_mov_b32 v254,1\ns_mov_b32 s70,0x05040100\ns_mov_b32 s71,0x07060302\ns_waitcnt vmcnt(20)\nv_lshlrev_b32 v36,16,v10\n" \
"v_and_b32 v37,0xffff0000,v10\nv_lshlrev_b32 v38,16,v11\nv_and_b32 v39,0xffff0000,v11\nv_mul_f32 v40,0xbfb8aa3b,v36\nv_mul_f32 v41,0xbfb8aa3b,v37\nv_mul_f32 v42,0xbfb8aa3b,v38\nv_mul_f32 v43,0xbfb8aa3b,v39\nv_exp_f32 v40,v40\n" \
"v_exp_f32 v41,v41\nv_exp_f32 v42,v42\nv_exp_f32 v43,v43\nv_add_f32 v40,1.0,v40\nv_add_f32 v41,1.0,v41\nv_add_f32 v42,1.0,v42\nv_add_f32 v43,1.0,v43\nv_rcp_f32 v40,v40\n" \
"v_rcp_f32 v41,v41\nv_rcp_f32 v42,v42\nv_rcp_f32 v43,v43\nv_mul_f32 v36,v36,v40\nv_mul_f32 v37,v37,v41\nv_mul_f32 v38,v38,v42\nv_mul_f32 v39,v39,v43\nv_cvt_pk_bf16_f32 v36,v36,v37\n" \
"v_cvt_pk_bf16_f32 v37,v38,v39\ns_waitcnt vmcnt(18)\nv_perm_b32 v48,v16,v12,s70\nv_perm_b32 v49,v16,v12,s71\nv_perm_b32 v50,v17,v13,s70\nv_perm_b32 v51,v17,v13,s71\nv_perm_b32 v52,v18,v14,s70\nv_perm_b32 v53,v18,v14,s71\n" \
"v_perm_b32 v54,v19,v15,s70\nv_perm_b32 v55,v19,v15,s71\nv_dot2_f32_bf16 v40,v48,v36,0\nv_dot2_f32_bf16 v41,v49,v36,0\nv_dot2_f32_bf16 v42,v50,v36,0\nv_dot2_f32_bf16 v43,v51,v36,0\nv_dot2_f32_bf16 v44,v52,v36,0\nv_dot2_f32_bf16 v45,v53,v36,0\n" \
"v_dot2_f32_bf16 v46,v54,v36,0\nv_dot2_f32_bf16 v47,v55,v36,0\ns_waitcnt vmcnt(16)\nv_perm_b32 v48,v24,v20,s70\nv_perm_b32 v49,v24,v20,s71\nv_perm_b32 v50,v25,v21,s70\nv_perm_b32 v51,v25,v21,s71\nv_perm_b32 v52,v26,v22,s70\n" \
"v_perm_b32 v53,v26,v22,s71\nv_perm_b32 v54,v27,v23,s70\nv_perm_b32 v55,v27,v23,s71\nv_dot2_f32_bf16 v40,v48,v37,v40\nv_dot2_f32_bf16 v41,v49,v37,v41\nv_dot2_f32_bf16 v42,v50,v37,v42\nv_dot2_f32_bf16 v43,v51,v37,v43\nv_dot2_f32_bf16 v44,v52,v37,v44\n" \
"v_dot2_f32_bf16 v45,v53,v37,v45\nv_dot2_f32_bf16 v46,v54,v37,v46\nv_dot2_f32_bf16 v47,v55,v37,v47\ns_waitcnt vmcnt(15)\nv_lshlrev_b32 v136,16,v128\nv_and_b32 v137,0xffff0000,v128\nv_lshlrev_b32 v138,16,v129\nv_and_b32 v139,0xffff0000,v129\n" \
"v_lshlrev_b32 v140,16,v130\nv_and_b32 v141,0xffff0000,v130\nv_lshlrev_b32 v142,16,v131\nv_and_b32 v143,0xffff0000,v131\ns_waitcnt vmcnt(14)\nv_lshlrev_b32 v144,16,v132\nv_and_b32 v145,0xffff0000,v132\nv_lshlrev_b32 v146,16,v133\n" \
"v_and_b32 v147,0xffff0000,v133\nv_lshlrev_b32 v148,16,v134\nv_and_b32 v149,0xffff0000,v134\nv_lshlrev_b32 v150,16,v135\nv_and_b32 v151,0xffff0000,v135\nv_permlane32_swap_b32 v40,v44\nv_permlane32_swap_b32 v41,v45\nv_permlane32_swap_b32 v42,v46\n" \
"v_permlane32_swap_b32 v43,v47\nv_add_f32 v40,v40,v44\nv_add_f32 v41,v41,v45\nv_add_f32 v42,v42,v46\nv_add_f32 v43,v43,v47\nv_add_f32 v48,v136,v140\nv_add_f32 v49,v137,v141\nv_add_f32 v50,v138,v142\n" \
"v_add_f32 v51,v139,v143\nv_permlane16_swap_b32 v40,v42\nv_permlane16_swap_b32 v41,v43\nv_add_f32 v52,v144,v148\nv_add_f32 v53,v145,v149\nv_add_f32 v54,v146,v150\nv_add_f32 v55,v147,v151\nv_add_f32 v40,v40,v42\n" \
"v_add_f32 v41,v41,v43\nv_add_f32 v48,v48,v52\nv_add_f32 v49,v49,v53\nv_add_f32 v50,v50,v54\nv_add_f32 v51,v51,v55\nv_add_f32_dpp v40,v40,v40 row_ror:8 row_mask:0xf bank_mask:0xf\nv_add_f32_dpp v41,v41,v41 row_ror:8 row_mask:0xf bank_mask:0xf\nv_add_f32 v48,v48,v49\n" \
"v_add_f32 v50,v50,v51\nv_lshlrev_b32 v42,5,v2\nv_add_f32 v48,v48,v50\nv_lshl_or_b32 v42,v6,3,v42\ns_lshl_b32 s72,s7,8\ns_add_u32 s72,s72,147456\nv_add_u32 v42,s72,v42\nds_write_b64 v42,v[40:41]\n" \
"v_add_f32_dpp v48,v48,v48 row_ror:8 row_mask:0xf bank_mask:0xf\nv_mov_b32 v50,v48\ns_nop 1\nv_permlane16_swap_b32 v48,v50\nv_add_f32 v48,v48,v50\nv_mov_b32 v50,v48\ns_nop 1\nv_permlane32_swap_b32 v48,v50\n" \
"v_add_f32 v48,v48,v50\nv_mul_f32 v48,0x3c000000,v48\nv_sub_f32 v56,v136,v48\nv_sub_f32 v57,v137,v48\nv_sub_f32 v58,v138,v48\nv_sub_f32 v59,v139,v48\nv_sub_f32 v60,v140,v48\nv_sub_f32 v61,v141,v48\n" \
"v_sub_f32 v62,v142,v48\nv_sub_f32 v63,v143,v48\nv_sub_f32 v64,v144,v48\nv_sub_f32 v65,v145,v48\nv_sub_f32 v66,v146,v48\nv_sub_f32 v67,v147,v48\nv_sub_f32 v68,v148,v48\nv_sub_f32 v69,v149,v48\n" \
"v_sub_f32 v70,v150,v48\nv_sub_f32 v71,v151,v48\nv_mul_f32 v52,v56,v56\nv_mul_f32 v53,v57,v57\nv_mul_f32 v54,v58,v58\nv_mul_f32 v55,v59,v59\nv_fmac_f32 v52,v60,v60\nv_fmac_f32 v53,v61,v61\n" \
"v_fmac_f32 v54,v62,v62\nv_fmac_f32 v55,v63,v63\nv_fmac_f32 v52,v64,v64\nv_fmac_f32 v53,v65,v65\nv_fmac_f32 v54,v66,v66\nv_fmac_f32 v55,v67,v67\nv_fmac_f32 v52,v68,v68\nv_fmac_f32 v53,v69,v69\n" \
"v_fmac_f32 v54,v70,v70\nv_fmac_f32 v55,v71,v71\nv_add_f32 v52,v52,v53\nv_add_f32 v54,v54,v55\ns_lshl_b32 s72,s7,3\nv_add_f32 v49,v52,v54\ns_add_u32 s72,s72,148480\nv_lshl_add_u32 v42,v2,5,s72\n" \
"v_add_f32_dpp v49,v49,v49 row_ror:8 row_mask:0xf bank_mask:0xf\nv_mov_b32 v50,v49\ns_nop 1\nv_permlane16_swap_b32 v49,v50\nv_add_f32 v49,v49,v50\nv_mov_b32 v50,v49\ns_nop 1\nv_permlane32_swap_b32 v49,v50\n" \
"v_add_f32 v49,v49,v50\nds_write_b64 v42,v[48:49]\nv_lshlrev_b32 v251,5,v2\nv_add_u32 v251,148480,v251\nv_lshlrev_b32 v250,2,v249\nv_add_u32 v250,147456,v250\ns_add_u32 m0,s64,114688\ns_add_u32 s69,s68,524288\n" \
"buffer_load_dwordx4 v243,s[40:43],s69 offen offset:0 lds\nbuffer_load_dwordx4 v244,s[40:43],s69 offen offset:1024 lds\nbuffer_load_dwordx4 v245,s[40:43],s69 offen offset:2048 lds\nbuffer_load_dwordx4 v246,s[40:43],s69 offen offset:3072 lds\ns_add_u32 m0,s65,8192\ns_add_u32 s67,s66,128\nbuffer_load_dwordx4 v224,s[36:39],s67 offen offset:0 lds\nbuffer_load_dwordx4 v225,s[36:39],s67 offen offset:1024 lds\n" \
"buffer_load_dwordx4 v226,s[36:39],s67 offen offset:2048 lds\nbuffer_load_dwordx4 v227,s[36:39],s67 offen offset:3072 lds\ns_add_u32 m0,s65,12288\ns_add_u32 s67,s66,32896\nbuffer_load_dwordx4 v224,s[36:39],s67 offen offset:0 lds\nbuffer_load_dwordx4 v225,s[36:39],s67 offen offset:1024 lds\nbuffer_load_dwordx4 v226,s[36:39],s67 offen offset:2048 lds\nbuffer_load_dwordx4 v227,s[36:39],s67 offen offset:3072 lds\n" \
"s_waitcnt lgkmcnt(0)\ns_waitcnt vmcnt(14)\ns_barrier\ns_add_u32 m0,s64,131072\ns_add_u32 s69,s68,1048576\nds_read_b128 a[0:3],v228 offset:0\nds_read_b128 a[4:7],v228 offset:2048\nds_read_b128 a[8:11],v228 offset:4096\n" \
"ds_read_b128 a[12:15],v228 offset:6144\nds_read_b128 a[16:19],v229 offset:0\nds_read_b128 a[20:23],v229 offset:2048\nds_read_b128 a[24:27],v229 offset:4096\nds_read_b128 a[28:31],v229 offset:6144\nds_read_b32 v152,v250 offset:0\nds_read_b32 v153,v250 offset:256\nds_read_b32 v154,v250 offset:512\n" \
"ds_read_b32 v155,v250 offset:768\ns_waitcnt lgkmcnt(2)\nv_add_f32 v152,v152,v153\ns_waitcnt lgkmcnt(0)\nv_add_f32 v154,v154,v155\nv_lshlrev_b32 v153,2,v249\nv_add_f32 v152,v152,v154\nds_read_b64_tr_b16 v[192:193],v241 offset:0\n" \
"ds_read_b64_tr_b16 v[194:195],v241 offset:256\nds_read_b64_tr_b16 v[196:197],v242 offset:0\nds_read_b64_tr_b16 v[198:199],v242 offset:256\nds_read_b64_tr_b16 v[200:201],v241 offset:512\nds_read_b64_tr_b16 v[202:203],v241 offset:768\nds_read_b64_tr_b16 v[204:205],v242 offset:512\nds_read_b64_tr_b16 v[206:207],v242 offset:768\nds_read_b64_tr_b16 v[208:209],v241 offset:1024\n" \
"ds_read_b64_tr_b16 v[210:211],v241 offset:1280\nds_read_b64_tr_b16 v[212:213],v242 offset:1024\nds_read_b64_tr_b16 v[214:215],v242 offset:1280\nds_read_b64_tr_b16 v[216:217],v241 offset:1536\nds_read_b64_tr_b16 v[218:219],v241 offset:1792\nds_read_b64_tr_b16 v[220:221],v242 offset:1536\nds_read_b64_tr_b16 v[222:223],v242 offset:1792\nbuffer_store_dword v152,v153,s[52:55],s79 offen sc1\n" \
"ds_read_b128 v[156:159],v251\nds_read_b128 v[160:163],v251 offset:16\ns_waitcnt lgkmcnt(15)\nM v[0:3],v[192:195],a[0:3],0\nbuffer_load_dwordx4 v243,s[40:43],s69 offen offset:0 lds\nM v[4:7],v[192:195],a[4:7],0\nv_bfe_u32 v253,v249,1,4\nv_and_b32 v252,1,v249\n" \
"M v[64:67],v[192:195],a[8:11],0\nv_lshlrev_b32 v253,13,v253\ns_lshl_b32 s78,s62,19\nM v[68:71],v[192:195],a[12:15],0\nds_read_b64_tr_b16 v[192:193],v241 offset:8192\nds_read_b64_tr_b16 v[194:195],v241 offset:8448\ns_waitcnt lgkmcnt(15)\nM v[8:11],v[196:199],a[0:3],0\n" \
"v_lshl_or_b32 v253,v252,7,v253\ns_add_u32 s78,s78,s68\nM v[12:15],v[196:199],a[4:7],0\nbuffer_load_dwordx4 v244,s[40:43],s69 offen offset:1024 lds\nM v[72:75],v[196:199],a[8:11],0\nv_add_u32 v253,s78,v253\ns_lshl3_add_u32 s72,s6,s75\nM v[76:79],v[196:199],a[12:15],0\n" \
"ds_read_b64_tr_b16 v[196:197],v242 offset:8192\nds_read_b64_tr_b16 v[198:199],v242 offset:8448\ns_waitcnt lgkmcnt(8)\nM v[16:19],v[200:203],a[0:3],0\nglobal_load_dword v247,v253,s[16:17]\nM v[20:23],v[200:203],a[4:7],0\nv_readfirstlane_b32 s92,v230\nv_readfirstlane_b32 s93,v231\n" \
"M v[80:83],v[200:203],a[8:11],0\nbuffer_load_dwordx4 v245,s[40:43],s69 offen offset:2048 lds\nM v[84:87],v[200:203],a[12:15],0\nds_read_b64_tr_b16 v[200:201],v241 offset:8704\nds_read_b64_tr_b16 v[202:203],v241 offset:8960\nM v[24:27],v[204:207],a[0:3],0\ns_add_u32 s70,s92,1\ns_addc_u32 s71,s93,0\n" \
"M v[28:31],v[204:207],a[4:7],0\ns_waitcnt lgkmcnt(4)\nv_add_f32 v164,v156,v158\nM v[88:91],v[204:207],a[8:11],0\nv_add_f32 v165,v160,v162\nv_add_f32 v164,v164,v165\nM v[92:95],v[204:207],a[12:15],0\nbuffer_load_dwordx4 v246,s[40:43],s69 offen offset:3072 lds\n" \
"ds_read_b64_tr_b16 v[204:205],v242 offset:8704\nds_read_b64_tr_b16 v[206:207],v242 offset:8960\nM v[32:35],v[208:211],a[0:3],0\ns_add_u32 s92,s92,0x7f4a7c15\ns_addc_u32 s93,s93,0x9e3779b9\nM v[36:39],v[208:211],a[4:7],0\ns_add_u32 m0,s65,16384\ns_add_u32 s67,s66,256\n" \
"M v[96:99],v[208:211],a[8:11],0\nv_and_b32 v165,7,v249\nv_mov_b32 v230,s92\nM v[100:103],v[208:211],a[12:15],0\nds_read_b64_tr_b16 v[208:209],v241 offset:9216\nds_read_b64_tr_b16 v[210:211],v241 offset:9472\nM v[40:43],v[212:215],a[0:3],0\nbuffer_load_dwordx4 v224,s[36:39],s67 offen offset:0 lds\n" \
"M v[44:47],v[212:215],a[4:7],0\nv_mov_b32 v231,s93\nv_lshlrev_b32 v165,11,v165\nM v[104:107],v[212:215],a[8:11],0\ns_add_u32 s72,s72,573440\ns_lshl2_add_u32 s80,s60,s7\nM v[108:111],v[212:215],a[12:15],0\nds_read_b64_tr_b16 v[212:213],v242 offset:9216\n" \
"ds_read_b64_tr_b16 v[214:215],v242 offset:9472\nM v[48:51],v[216:219],a[0:3],0\ns_lshl_b32 s80,s80,9\ns_add_u32 s80,s80,573440\nM v[52:55],v[216:219],a[4:7],0\nbuffer_load_dwordx4 v225,s[36:39],s67 offen offset:1024 lds\nM v[112:115],v[216:219],a[8:11],0\nv_mul_f32 v164,0x3e800000,v164\n" \
"v_sub_f32 v156,v156,v164\nM v[116:119],v[216:219],a[12:15],0\nds_read_b64_tr_b16 v[216:217],v241 offset:9728\nds_read_b64_tr_b16 v[218:219],v241 offset:9984\nM v[56:59],v[220:223],a[0:3],0\nv_sub_f32 v158,v158,v164\nv_mul_f32 v156,v156,v156\nM v[60:63],v[220:223],a[4:7],0\n" \
"v_sub_f32 v160,v160,v164\nv_fmac_f32 v156,v158,v158\nM v[120:123],v[220:223],a[8:11],0\nbuffer_load_dwordx4 v226,s[36:39],s67 offen offset:2048 lds\nM v[124:127],v[220:223],a[12:15],0\nds_read_b64_tr_b16 v[220:221],v242 offset:9728\nds_read_b64_tr_b16 v[222:223],v242 offset:9984\nM v[0:3],v[192:195],a[16:19],v[0:3]\n" \
"v_add_f32 v157,v157,v159\nv_add_f32 v161,v161,v163\nM v[4:7],v[192:195],a[20:23],v[4:7]\nv_sub_f32 v162,v162,v164\nv_fmac_f32 v156,v160,v160\nM v[64:67],v[192:195],a[24:27],v[64:67]\nv_add_f32 v157,v157,v161\nv_fmac_f32 v156,v162,v162\n" \
"M v[68:71],v[192:195],a[28:31],v[68:71]\nbuffer_load_dwordx4 v227,s[36:39],s67 offen offset:3072 lds\ns_waitcnt lgkmcnt(4)\nM v[8:11],v[196:199],a[16:19],v[8:11]\nv_fmac_f32 v157,0x43000000,v156\nv_mul_f32 v157,0x3b000000,v157\nM v[12:15],v[196:199],a[20:23],v[12:15]\ns_add_u32 m0,s65,20480\n" \
"s_add_u32 s67,s66,33024\nM v[72:75],v[196:199],a[24:27],v[72:75]\nv_cvt_pk_bf16_f32 v157,0,v157\nv_add_f32 v157,0x3727c5ac,v157\nM v[76:79],v[196:199],a[28:31],v[76:79]\nv_cvt_pk_bf16_f32 v157,0,v157\nv_mov_b32 v232,s70\nM v[16:19],v[200:203],a[16:19],v[16:19]\n" \
"buffer_load_dwordx4 v224,s[36:39],s67 offen offset:0 lds\nM v[20:23],v[200:203],a[20:23],v[20:23]\nv_sqrt_f32 v157,v157\nM v[80:83],v[200:203],a[24:27],v[80:83]\nv_mov_b32 v233,s71\ns_lshr_b32 s70,s64,2\nM v[84:87],v[200:203],a[28:31],v[84:87]\nv_cvt_pk_bf16_f32 v164,0,v164\n" \
"v_cvt_pk_bf16_f32 v157,0,v157\nM v[24:27],v[204:207],a[16:19],v[24:27]\ns_add_u32 s71,s70,786432\nv_sub_f32 v136,v136,v164\nM v[28:31],v[204:207],a[20:23],v[28:31]\nbuffer_load_dwordx4 v225,s[36:39],s67 offen offset:1024 lds\nM v[88:91],v[204:207],a[24:27],v[88:91]\ns_waitcnt vmcnt(11) lgkmcnt(0)\n" \
"s_barrier\nds_read_b128 a[32:35],v228 offset:8192\nds_read_b128 a[36:39],v228 offset:10240\nds_read_b128 a[40:43],v228 offset:12288\nds_read_b128 a[44:47],v228 offset:14336\nds_read_b128 a[48:51],v229 offset:8192\nds_read_b128 a[52:55],v229 offset:10240\nds_read_b128 a[56:59],v229 offset:12288\n" \
"ds_read_b128 a[60:63],v229 offset:14336\nds_read_b64_tr_b16 v[192:193],v241 offset:16384\nds_read_b64_tr_b16 v[194:195],v241 offset:16640\nds_read_b64_tr_b16 v[196:197],v242 offset:16384\nds_read_b64_tr_b16 v[198:199],v242 offset:16640\nds_read_b64_tr_b16 v[200:201],v241 offset:16896\nds_read_b64_tr_b16 v[202:203],v241 offset:17152\nM v[92:95],v[204:207],a[28:31],v[92:95]\n" \
"ds_read_b64_tr_b16 v[204:205],v242 offset:16896\nds_read_b64_tr_b16 v[206:207],v242 offset:17152\nM v[32:35],v[208:211],a[16:19],v[32:35]\nbuffer_store_dwordx2 v[230:231],v165,s[52:55],s72 offen sc1\nM v[36:39],v[208:211],a[20:23],v[36:39]\nv_lshlrev_b32 v165,3,v249\nv_sub_f32 v137,v137,v164\nM v[96:99],v[208:211],a[24:27],v[96:99]\n" \
"buffer_load_dwordx4 v226,s[36:39],s67 offen offset:2048 lds\nM v[100:103],v[208:211],a[28:31],v[100:103]\nds_read_b64_tr_b16 v[208:209],v241 offset:17408\nds_read_b64_tr_b16 v[210:211],v241 offset:17664\nM v[40:43],v[212:215],a[16:19],v[40:43]\nv_rcp_f32 v157,v157\nM v[44:47],v[212:215],a[20:23],v[44:47]\nv_sub_f32 v138,v138,v164\n" \
"v_sub_f32 v139,v139,v164\nM v[104:107],v[212:215],a[24:27],v[104:107]\nv_sub_f32 v140,v140,v164\nv_sub_f32 v141,v141,v164\nM v[108:111],v[212:215],a[28:31],v[108:111]\nbuffer_load_dwordx4 v227,s[36:39],s67 offen offset:3072 lds\nds_read_b64_tr_b16 v[212:213],v242 offset:17408\nds_read_b64_tr_b16 v[214:215],v242 offset:17664\n" \
"M v[48:51],v[216:219],a[16:19],v[48:51]\nv_sub_f32 v142,v142,v164\nv_sub_f32 v143,v143,v164\nM v[52:55],v[216:219],a[20:23],v[52:55]\ns_add_u32 m0,s65,0\ns_add_u32 s67,s66,384\nM v[112:115],v[216:219],a[24:27],v[112:115]\nv_sub_f32 v144,v144,v164\n" \
"v_sub_f32 v145,v145,v164\nM v[116:119],v[216:219],a[28:31],v[116:119]\nds_read_b64_tr_b16 v[216:217],v241 offset:17920\nds_read_b64_tr_b16 v[218:219],v241 offset:18176\nM v[56:59],v[220:223],a[16:19],v[56:59]\nbuffer_load_dwordx4 v224,s[36:39],s67 offen offset:0 lds\nM v[60:63],v[220:223],a[20:23],v[60:63]\nv_sub_f32 v146,v146,v164\n" \
"v_sub_f32 v147,v147,v164\nM v[120:123],v[220:223],a[24:27],v[120:123]\nv_sub_f32 v148,v148,v164\nv_sub_f32 v149,v149,v164\nM v[124:127],v[220:223],a[28:31],v[124:127]\nds_read_b64_tr_b16 v[220:221],v242 offset:17920\nds_read_b64_tr_b16 v[222:223],v242 offset:18176\ns_waitcnt lgkmcnt(6)\n" \
"M v[0:3],v[192:195],a[32:35],v[0:3]\nv_sub_f32 v150,v150,v164\nv_sub_f32 v151,v151,v164\nM v[4:7],v[192:195],a[36:39],v[4:7]\nbuffer_load_dwordx4 v225,s[36:39],s67 offen offset:1024 lds\nM v[64:67],v[192:195],a[40:43],v[64:67]\nv_cvt_pk_bf16_f32 v136,0,v136\nv_cvt_pk_bf16_f32 v137,0,v137\n" \
"M v[68:71],v[192:195],a[44:47],v[68:71]\nds_read_b64_tr_b16 v[192:193],v241 offset:24576\nds_read_b64_tr_b16 v[194:195],v241 offset:24832\nM v[8:11],v[196:199],a[32:35],v[8:11]\nv_cvt_pk_bf16_f32 v138,0,v138\nv_cvt_pk_bf16_f32 v139,0,v139\nM v[12:15],v[196:199],a[36:39],v[12:15]\nv_cvt_pk_bf16_f32 v140,0,v140\n" \
"v_cvt_pk_bf16_f32 v141,0,v141\nM v[72:75],v[196:199],a[40:43],v[72:75]\nbuffer_load_dwordx4 v226,s[36:39],s67 offen offset:2048 lds\nM v[76:79],v[196:199],a[44:47],v[76:79]\nds_read_b64_tr_b16 v[196:197],v242 offset:24576\nds_read_b64_tr_b16 v[198:199],v242 offset:24832\nM v[16:19],v[200:203],a[32:35],v[16:19]\nv_cvt_pk_bf16_f32 v142,0,v142\n" \
"v_cvt_pk_bf16_f32 v143,0,v143\nM v[20:23],v[200:203],a[36:39],v[20:23]\nv_cvt_pk_bf16_f32 v144,0,v144\nv_cvt_pk_bf16_f32 v145,0,v145\nM v[80:83],v[200:203],a[40:43],v[80:83]\nv_cvt_pk_bf16_f32 v146,0,v146\nv_cvt_pk_bf16_f32 v147,0,v147\nM v[84:87],v[200:203],a[44:47],v[84:87]\n" \
"buffer_load_dwordx4 v227,s[36:39],s67 offen offset:3072 lds\nds_read_b64_tr_b16 v[200:201],v241 offset:25088\nds_read_b64_tr_b16 v[202:203],v241 offset:25344\nM v[24:27],v[204:207],a[32:35],v[24:27]\nv_cvt_pk_bf16_f32 v148,0,v148\nv_cvt_pk_bf16_f32 v149,0,v149\nM v[28:31],v[204:207],a[36:39],v[28:31]\ns_add_u32 m0,s65,4096\n" \
"s_add_u32 s67,s66,33152\nM v[88:91],v[204:207],a[40:43],v[88:91]\nv_cvt_pk_bf16_f32 v150,0,v150\nv_cvt_pk_bf16_f32 v151,0,v151\nM v[92:95],v[204:207],a[44:47],v[92:95]\nds_read_b64_tr_b16 v[204:205],v242 offset:25088\nds_read_b64_tr_b16 v[206:207],v242 offset:25344\nM v[32:35],v[208:211],a[32:35],v[32:35]\n" \
"buffer_load_dwordx4 v224,s[36:39],s67 offen offset:0 lds\nM v[36:39],v[208:211],a[36:39],v[36:39]\nv_mul_f32 v136,v136,v157\nv_mul_f32 v137,v137,v157\nM v[96:99],v[208:211],a[40:43],v[96:99]\nv_mul_f32 v138,v138,v157\nv_mul_f32 v139,v139,v157\nM v[100:103],v[208:211],a[44:47],v[100:103]\n" \
"ds_read_b64_tr_b16 v[208:209],v241 offset:25600\nds_read_b64_tr_b16 v[210:211],v241 offset:25856\ns_waitcnt lgkmcnt(6)\nM v[40:43],v[212:215],a[32:35],v[40:43]\nv_mul_f32 v140,v140,v157\nv_mul_f32 v141,v141,v157\nM v[44:47],v[212:215],a[36:39],v[44:47]\nbuffer_load_dwordx4 v225,s[36:39],s67 offen offset:1024 lds\n" \
"M v[104:107],v[212:215],a[40:43],v[104:107]\nv_mul_f32 v142,v142,v157\nv_mul_f32 v143,v143,v157\nM v[108:111],v[212:215],a[44:47],v[108:111]\nds_read_b64_tr_b16 v[212:213],v242 offset:25600\nds_read_b64_tr_b16 v[214:215],v242 offset:25856\nM v[48:51],v[216:219],a[32:35],v[48:51]\nv_mul_f32 v144,v144,v157\n" \
"v_mul_f32 v145,v145,v157\nM v[52:55],v[216:219],a[36:39],v[52:55]\nv_mul_f32 v146,v146,v157\nv_mul_f32 v147,v147,v157\nM v[112:115],v[216:219],a[40:43],v[112:115]\nbuffer_load_dwordx4 v226,s[36:39],s67 offen offset:2048 lds\nM v[116:119],v[216:219],a[44:47],v[116:119]\nds_read_b64_tr_b16 v[216:217],v241 offset:26112\n" \
"ds_read_b64_tr_b16 v[218:219],v241 offset:26368\nM v[56:59],v[220:223],a[32:35],v[56:59]\nv_mul_f32 v148,v148,v157\nv_mul_f32 v149,v149,v157\nM v[60:63],v[220:223],a[36:39],v[60:63]\nv_mul_f32 v150,v150,v157\nv_mul_f32 v151,v151,v157\nM v[120:123],v[220:223],a[40:43],v[120:123]\n" \
"v_and_b32 v169,3,v249\nv_mul_u32_u24 v169,12288,v169\nM v[124:127],v[220:223],a[44:47],v[124:127]\nbuffer_load_dwordx4 v227,s[36:39],s67 offen offset:3072 lds\nds_read_b64_tr_b16 v[220:221],v242 offset:26112\nds_read_b64_tr_b16 v[222:223],v242 offset:26368\nM v[0:3],v[192:195],a[48:51],v[0:3]\nv_and_b32 v170,28,v249\n" \
"v_add_u32 v169,v169,v170\nM v[4:7],v[192:195],a[52:55],v[4:7]\ns_add_u32 m0,s64,147456\ns_add_u32 s69,s68,1572864\nM v[64:67],v[192:195],a[56:59],v[64:67]\ns_and_b32 s78,s62,1\ns_lshl2_add_u32 s78,s78,s7\nM v[68:71],v[192:195],a[60:63],v[68:71]\n" \
"s_lshl_b32 s78,s78,13\nv_lshlrev_b32 v252,7,v249\nM v[8:11],v[196:199],a[48:51],v[8:11]\nbuffer_load_dwordx4 v243,s[40:43],s69 offen offset:0 lds\nM v[12:15],v[196:199],a[52:55],v[12:15]\ns_add_u32 s78,s78,.Lattn-.Lpc0\nv_lshrrev_b32 v251,3,v249\nM v[72:75],v[196:199],a[56:59],v[72:75]\n" \
"s_cmp_eq_u32 s6,0\ns_cselect_b32 s72,s75,0x8000000\nM v[76:79],v[196:199],a[60:63],v[76:79]\ns_add_u32 s72,s72,94208\ns_waitcnt lgkmcnt(2)\nM v[16:19],v[200:203],a[48:51],v[16:19]\nv_lshlrev_b32 v170,3,v170\nv_cvt_pk_bf16_f32 v144,0,v144\n" \
"M v[20:23],v[200:203],a[52:55],v[20:23]\nbuffer_load_dwordx4 v244,s[40:43],s69 offen offset:1024 lds\nM v[80:83],v[200:203],a[56:59],v[80:83]\nv_cvt_pk_bf16_f32 v145,0,v145\nv_cvt_pk_bf16_f32 v146,0,v146\nM v[84:87],v[200:203],a[60:63],v[84:87]\nv_cvt_pk_bf16_f32 v147,0,v147\nv_cvt_pk_bf16_f32 v136,0,v136\n" \
"M v[24:27],v[204:207],a[48:51],v[24:27]\nv_cvt_pk_bf16_f32 v137,0,v137\nv_cvt_pk_bf16_f32 v138,0,v138\nM v[28:31],v[204:207],a[52:55],v[28:31]\nv_cvt_pk_bf16_f32 v139,0,v139\nv_cvt_pk_bf16_f32 v140,0,v140\nM v[88:91],v[204:207],a[56:59],v[88:91]\nbuffer_load_dwordx4 v245,s[40:43],s69 offen offset:2048 lds\n" \
"s_waitcnt vmcnt(11) lgkmcnt(0)\ns_barrier\nds_read_b128 a[64:67],v228 offset:16384\nds_read_b128 a[68:71],v228 offset:18432\nds_read_b128 a[72:75],v228 offset:20480\nds_read_b128 a[76:79],v228 offset:22528\nds_read_b128 a[80:83],v229 offset:16384\nds_read_b128 a[84:87],v229 offset:18432\n" \
"ds_read_b128 a[88:91],v229 offset:20480\nds_read_b128 a[92:95],v229 offset:22528\nM v[92:95],v[204:207],a[60:63],v[92:95]\nv_cvt_pk_bf16_f32 v141,0,v141\nv_cvt_pk_bf16_f32 v142,0,v142\nM v[32:35],v[208:211],a[48:51],v[32:35]\nv_cvt_pk_bf16_f32 v143,0,v143\nv_cvt_pk_bf16_f32 v148,0,v148\n" \
"M v[36:39],v[208:211],a[52:55],v[36:39]\nv_cvt_pk_bf16_f32 v149,0,v149\nv_bfe_u32 v250,v249,3,2\nM v[96:99],v[208:211],a[56:59],v[96:99]\nv_cvt_pk_bf16_f32 v150,0,v150\nv_cvt_pk_bf16_f32 v151,0,v151\nM v[100:103],v[208:211],a[60:63],v[100:103]\nbuffer_load_dwordx4 v246,s[40:43],s69 offen offset:3072 lds\n" \
"M v[40:43],v[212:215],a[48:51],v[40:43]\ns_mov_b32 s98,0x4000\ns_add_u32 s10,s61,1\nM v[44:47],v[212:215],a[52:55],v[44:47]\ns_add_u32 m0,s65,8192\ns_add_u32 s67,s66,512\nds_read_b64_tr_b16 v[192:193],v241 offset:32768\nds_read_b64_tr_b16 v[194:195],v241 offset:33024\n" \
"M v[104:107],v[212:215],a[56:59],v[104:107]\nds_read_b64_tr_b16 v[196:197],v242 offset:32768\ns_add_u32 s11,s61,2\nM v[108:111],v[212:215],a[60:63],v[108:111]\nds_read_b64_tr_b16 v[198:199],v242 offset:33024\nds_read_b64_tr_b16 v[200:201],v241 offset:33280\nM v[48:51],v[216:219],a[48:51],v[48:51]\nbuffer_load_dwordx4 v224,s[36:39],s67 offen offset:0 lds\n" \
"M v[52:55],v[216:219],a[52:55],v[52:55]\nds_read_b64_tr_b16 v[202:203],v241 offset:33536\nds_read_b64_tr_b16 v[204:205],v242 offset:33280\nM v[112:115],v[216:219],a[56:59],v[112:115]\nds_read_b64_tr_b16 v[206:207],v242 offset:33536\nds_read_b64_tr_b16 v[208:209],v241 offset:33792\nM v[116:119],v[216:219],a[60:63],v[116:119]\nds_read_b64_tr_b16 v[210:211],v241 offset:34048\n" \
"ds_read_b64_tr_b16 v[212:213],v242 offset:33792\nM v[56:59],v[220:223],a[48:51],v[56:59]\nds_read_b64_tr_b16 v[214:215],v242 offset:34048\nds_read_b64_tr_b16 v[216:217],v241 offset:34304\nM v[60:63],v[220:223],a[52:55],v[60:63]\nbuffer_load_dwordx4 v225,s[36:39],s67 offen offset:1024 lds\nM v[120:123],v[220:223],a[56:59],v[120:123]\nds_read_b64_tr_b16 v[218:219],v241 offset:34560\n" \
"s_add_u32 s14,s61,3\nM v[124:127],v[220:223],a[60:63],v[124:127]\nds_read_b64_tr_b16 v[220:221],v242 offset:34304\ns_waitcnt lgkmcnt(13)\nM v[0:3],v[192:195],a[64:67],v[0:3]\nds_read_b64_tr_b16 v[222:223],v242 offset:34560\ns_and_b32 s10,s10,3\nM v[4:7],v[192:195],a[68:71],v[4:7]\n" \
"s_and_b32 s11,s11,3\ns_and_b32 s14,s14,3\nM v[64:67],v[192:195],a[72:75],v[64:67]\nbuffer_load_dwordx4 v226,s[36:39],s67 offen offset:2048 lds\nM v[68:71],v[192:195],a[76:79],v[68:71]\nds_read_b64_tr_b16 v[192:193],v241 offset:40960\ns_waitcnt lgkmcnt(11)\nM v[8:11],v[196:199],a[64:67],v[8:11]\n" \
"ds_read_b64_tr_b16 v[194:195],v241 offset:41216\ns_mov_b32 s47,0x20000\nM v[12:15],v[196:199],a[68:71],v[12:15]\ns_mov_b32 s51,0x20000\ns_mov_b32 s94,0xc000\nM v[72:75],v[196:199],a[72:75],v[72:75]\ns_lshl_b32 s10,s10,13\ns_lshl_b32 s11,s11,13\n" \
"M v[76:79],v[196:199],a[76:79],v[76:79]\nbuffer_load_dwordx4 v227,s[36:39],s67 offen offset:3072 lds\nM v[16:19],v[200:203],a[64:67],v[16:19]\nds_read_b64_tr_b16 v[196:197],v242 offset:40960\nds_read_b64_tr_b16 v[198:199],v242 offset:41216\nM v[20:23],v[200:203],a[68:71],v[20:23]\ns_add_u32 m0,s65,12288\ns_add_u32 s67,s66,33280\n" \
"M v[80:83],v[200:203],a[72:75],v[80:83]\ns_lshl_b32 s14,s14,13\ns_mov_b32 s82,0xf0000\nM v[84:87],v[200:203],a[76:79],v[84:87]\nds_read_b64_tr_b16 v[200:201],v241 offset:41472\ns_waitcnt lgkmcnt(5)\nM v[24:27],v[204:207],a[64:67],v[24:27]\nbuffer_load_dwordx4 v224,s[36:39],s67 offen offset:0 lds\n" \
"M v[28:31],v[204:207],a[68:71],v[28:31]\nds_read_b64_tr_b16 v[202:203],v241 offset:41728\ns_mov_b32 s83,0xfff00ff\nM v[88:91],v[204:207],a[72:75],v[88:91]\ns_mov_b32 s84,0x1f0001\ns_mov_b32 s85,0x1fff01ff\nM v[92:95],v[204:207],a[76:79],v[92:95]\nds_read_b64_tr_b16 v[204:205],v242 offset:41472\n" \
"ds_read_b64_tr_b16 v[206:207],v242 offset:41728\nM v[32:35],v[208:211],a[64:67],v[32:35]\ns_mov_b32 s86,0x3f0003\ns_mov_b32 s87,0x3fff03ff\nM v[36:39],v[208:211],a[68:71],v[36:39]\nbuffer_load_dwordx4 v225,s[36:39],s67 offen offset:1024 lds\nM v[96:99],v[208:211],a[72:75],v[96:99]\ns_mov_b32 s88,0x7f0007\n" \
"s_mov_b32 s89,0x7fff07ff\nM v[100:103],v[208:211],a[76:79],v[100:103]\nds_read_b64_tr_b16 v[208:209],v241 offset:41984\nds_read_b64_tr_b16 v[210:211],v241 offset:42240\nM v[40:43],v[212:215],a[64:67],v[40:43]\ns_mov_b64 s[90:91],-1\ns_mov_b32 s28,0x5f800000\nM v[44:47],v[212:215],a[68:71],v[44:47]\n" \
"s_mov_b32 s99,0\nM v[104:107],v[212:215],a[72:75],v[104:107]\nbuffer_load_dwordx4 v226,s[36:39],s67 offen offset:2048 lds\nM v[108:111],v[212:215],a[76:79],v[108:111]\nds_read_b64_tr_b16 v[212:213],v242 offset:41984\nds_read_b64_tr_b16 v[214:215],v242 offset:42240\nM v[48:51],v[216:219],a[64:67],v[48:51]\nM v[52:55],v[216:219],a[68:71],v[52:55]\n" \
"M v[112:115],v[216:219],a[72:75],v[112:115]\nM v[116:119],v[216:219],a[76:79],v[116:119]\nbuffer_load_dwordx4 v227,s[36:39],s67 offen offset:3072 lds\nM v[56:59],v[220:223],a[64:67],v[56:59]\nds_read_b64_tr_b16 v[216:217],v241 offset:42496\nds_read_b64_tr_b16 v[218:219],v241 offset:42752\nM v[60:63],v[220:223],a[68:71],v[60:63]\ns_add_u32 m0,s64,98304\n" \
"s_add_u32 s69,s68,2097152\nM v[120:123],v[220:223],a[72:75],v[120:123]\nM v[124:127],v[220:223],a[76:79],v[124:127]\nds_read_b64_tr_b16 v[220:221],v242 offset:42496\ns_waitcnt lgkmcnt(7)\nM v[0:3],v[192:195],a[80:83],v[0:3]\nbuffer_load_dwordx4 v243,s[40:43],s69 offen offset:0 lds\nM v[4:7],v[192:195],a[84:87],v[4:7]\n" \
"ds_read_b64_tr_b16 v[222:223],v242 offset:42752\nM v[64:67],v[192:195],a[88:91],v[64:67]\nM v[68:71],v[192:195],a[92:95],v[68:71]\nM v[8:11],v[196:199],a[80:83],v[8:11]\nM v[12:15],v[196:199],a[84:87],v[12:15]\nbuffer_load_dwordx4 v244,s[40:43],s69 offen offset:1024 lds\nM v[72:75],v[196:199],a[88:91],v[72:75]\nM v[76:79],v[196:199],a[92:95],v[76:79]\n" \
"M v[16:19],v[200:203],a[80:83],v[16:19]\nM v[20:23],v[200:203],a[84:87],v[20:23]\nM v[80:83],v[200:203],a[88:91],v[80:83]\nbuffer_load_dwordx4 v245,s[40:43],s69 offen offset:2048 lds\nM v[84:87],v[200:203],a[92:95],v[84:87]\nM v[24:27],v[204:207],a[80:83],v[24:27]\nM v[28:31],v[204:207],a[84:87],v[28:31]\nM v[88:91],v[204:207],a[88:91],v[88:91]\n" \
"s_waitcnt vmcnt(11) lgkmcnt(0)\ns_barrier\nds_read_b128 a[96:99],v228 offset:0\nds_read_b128 a[100:103],v228 offset:2048\nds_read_b128 a[104:107],v228 offset:4096\nds_read_b128 a[108:111],v228 offset:6144\nds_read_b128 a[112:115],v229 offset:0\nds_read_b128 a[116:119],v229 offset:2048\n" \
"ds_read_b128 a[120:123],v229 offset:4096\nds_read_b128 a[124:127],v229 offset:6144\nM v[92:95],v[204:207],a[92:95],v[92:95]\nbuffer_load_dwordx4 v246,s[40:43],s69 offen offset:3072 lds\nM v[32:35],v[208:211],a[80:83],v[32:35]\nM v[36:39],v[208:211],a[84:87],v[36:39]\ns_add_u32 m0,s65,16384\ns_add_u32 s67,s66,640\n" \
"M v[96:99],v[208:211],a[88:91],v[96:99]\nM v[100:103],v[208:211],a[92:95],v[100:103]\nM v[40:43],v[212:215],a[80:83],v[40:43]\nbuffer_load_dwordx4 v224,s[36:39],s67 offen offset:0 lds\nM v[44:47],v[212:215],a[84:87],v[44:47]\nds_read_b64_tr_b16 v[192:193],v241 offset:49152\nds_read_b64_tr_b16 v[194:195],v241 offset:49408\nM v[104:107],v[212:215],a[88:91],v[104:107]\n" \
"ds_read_b64_tr_b16 v[196:197],v242 offset:49152\nM v[108:111],v[212:215],a[92:95],v[108:111]\nds_read_b64_tr_b16 v[198:199],v242 offset:49408\nds_read_b64_tr_b16 v[200:201],v241 offset:49664\nM v[48:51],v[216:219],a[80:83],v[48:51]\nds_read_b64_tr_b16 v[202:203],v241 offset:49920\nds_read_b64_tr_b16 v[204:205],v242 offset:49664\nM v[52:55],v[216:219],a[84:87],v[52:55]\n" \
"buffer_load_dwordx4 v225,s[36:39],s67 offen offset:1024 lds\nM v[112:115],v[216:219],a[88:91],v[112:115]\nds_read_b64_tr_b16 v[206:207],v242 offset:49920\nds_read_b64_tr_b16 v[208:209],v241 offset:50176\nM v[116:119],v[216:219],a[92:95],v[116:119]\nds_read_b64_tr_b16 v[210:211],v241 offset:50432\nds_read_b64_tr_b16 v[212:213],v242 offset:50176\nM v[56:59],v[220:223],a[80:83],v[56:59]\n" \
"ds_read_b64_tr_b16 v[214:215],v242 offset:50432\nds_read_b64_tr_b16 v[216:217],v241 offset:50688\nM v[60:63],v[220:223],a[84:87],v[60:63]\nds_read_b64_tr_b16 v[218:219],v241 offset:50944\nM v[120:123],v[220:223],a[88:91],v[120:123]\nbuffer_load_dwordx4 v226,s[36:39],s67 offen offset:2048 lds\nM v[124:127],v[220:223],a[92:95],v[124:127]\nds_read_b64_tr_b16 v[220:221],v242 offset:50688\n" \
"s_waitcnt lgkmcnt(13)\nM v[0:3],v[192:195],a[96:99],v[0:3]\nds_read_b64_tr_b16 v[222:223],v242 offset:50944\nM v[4:7],v[192:195],a[100:103],v[4:7]\nM v[64:67],v[192:195],a[104:107],v[64:67]\nM v[68:71],v[192:195],a[108:111],v[68:71]\nbuffer_load_dwordx4 v227,s[36:39],s67 offen offset:3072 lds\ns_waitcnt lgkmcnt(11)\n" \
"M v[8:11],v[196:199],a[96:99],v[8:11]\nds_read_b64_tr_b16 v[192:193],v241 offset:57344\nds_read_b64_tr_b16 v[194:195],v241 offset:57600\nM v[12:15],v[196:199],a[100:103],v[12:15]\ns_add_u32 m0,s65,20480\ns_add_u32 s67,s66,33408\nM v[72:75],v[196:199],a[104:107],v[72:75]\nM v[76:79],v[196:199],a[108:111],v[76:79]\n" \
"ds_read_b64_tr_b16 v[196:197],v242 offset:57344\ns_waitcnt lgkmcnt(8)\nM v[16:19],v[200:203],a[96:99],v[16:19]\nbuffer_load_dwordx4 v224,s[36:39],s67 offen offset:0 lds\nM v[20:23],v[200:203],a[100:103],v[20:23]\nds_read_b64_tr_b16 v[198:199],v242 offset:57600\nM v[80:83],v[200:203],a[104:107],v[80:83]\nM v[84:87],v[200:203],a[108:111],v[84:87]\n" \
"ds_read_b64_tr_b16 v[200:201],v241 offset:57856\nds_read_b64_tr_b16 v[202:203],v241 offset:58112\nM v[24:27],v[204:207],a[96:99],v[24:27]\nM v[28:31],v[204:207],a[100:103],v[28:31]\nbuffer_load_dwordx4 v225,s[36:39],s67 offen offset:1024 lds\nM v[88:91],v[204:207],a[104:107],v[88:91]\nM v[92:95],v[204:207],a[108:111],v[92:95]\nds_read_b64_tr_b16 v[204:205],v242 offset:57856\n" \
"ds_read_b64_tr_b16 v[206:207],v242 offset:58112\nM v[32:35],v[208:211],a[96:99],v[32:35]\nM v[36:39],v[208:211],a[100:103],v[36:39]\nM v[96:99],v[208:211],a[104:107],v[96:99]\nbuffer_load_dwordx4 v226,s[36:39],s67 offen offset:2048 lds\nM v[100:103],v[208:211],a[108:111],v[100:103]\nds_read_b64_tr_b16 v[208:209],v241 offset:58368\ns_waitcnt lgkmcnt(7)\n" \
"M v[40:43],v[212:215],a[96:99],v[40:43]\nds_read_b64_tr_b16 v[210:211],v241 offset:58624\nM v[44:47],v[212:215],a[100:103],v[44:47]\nM v[104:107],v[212:215],a[104:107],v[104:107]\nM v[108:111],v[212:215],a[108:111],v[108:111]\nbuffer_load_dwordx4 v227,s[36:39],s67 offen offset:3072 lds\nM v[48:51],v[216:219],a[96:99],v[48:51]\nds_read_b64_tr_b16 v[212:213],v242 offset:58368\n" \
"ds_read_b64_tr_b16 v[214:215],v242 offset:58624\nM v[52:55],v[216:219],a[100:103],v[52:55]\ns_add_u32 m0,s64,114688\ns_add_u32 s69,s68,2621440\nM v[112:115],v[216:219],a[104:107],v[112:115]\nM v[116:119],v[216:219],a[108:111],v[116:119]\nds_read_b64_tr_b16 v[216:217],v241 offset:58880\nds_read_b64_tr_b16 v[218:219],v241 offset:59136\n" \
"M v[56:59],v[220:223],a[96:99],v[56:59]\nbuffer_load_dwordx4 v243,s[40:43],s69 offen offset:0 lds\nM v[60:63],v[220:223],a[100:103],v[60:63]\nM v[120:123],v[220:223],a[104:107],v[120:123]\nM v[124:127],v[220:223],a[108:111],v[124:127]\nds_read_b64_tr_b16 v[220:221],v242 offset:58880\nds_read_b64_tr_b16 v[222:223],v242 offset:59136\nM v[0:3],v[192:195],a[112:115],v[0:3]\n" \
"buffer_load_dwordx2 v[166:167],v165,s[52:55],s80 offen sc1\nM v[4:7],v[192:195],a[116:119],v[4:7]\nbuffer_load_dwordx4 v244,s[40:43],s69 offen offset:1024 lds\nM v[64:67],v[192:195],a[120:123],v[64:67]\nM v[68:71],v[192:195],a[124:127],v[68:71]\ns_waitcnt lgkmcnt(6)\nM v[8:11],v[196:199],a[112:115],v[8:11]\nM v[12:15],v[196:199],a[116:119],v[12:15]\n" \
"M v[72:75],v[196:199],a[120:123],v[72:75]\nbuffer_load_dwordx4 v245,s[40:43],s69 offen offset:2048 lds\nM v[76:79],v[196:199],a[124:127],v[76:79]\nM v[16:19],v[200:203],a[112:115],v[16:19]\nM v[20:23],v[200:203],a[116:119],v[20:23]\nM v[80:83],v[200:203],a[120:123],v[80:83]\nM v[84:87],v[200:203],a[124:127],v[84:87]\nbuffer_load_dwordx4 v246,s[40:43],s69 offen offset:3072 lds\n" \
"M v[24:27],v[204:207],a[112:115],v[24:27]\nM v[28:31],v[204:207],a[116:119],v[28:31]\ns_add_u32 m0,s65,0\ns_add_u32 s67,s66,768\nM v[88:91],v[204:207],a[120:123],v[88:91]\ns_waitcnt vmcnt(13) lgkmcnt(0)\ns_barrier\nM v[92:95],v[204:207],a[124:127],v[92:95]\n" \
"ds_read_b128 a[128:131],v228 offset:8192\nds_read_b64_tr_b16 v[192:193],v241 offset:0\nM v[32:35],v[208:211],a[112:115],v[32:35]\nbuffer_load_dwordx4 v224,s[36:39],s67 offen offset:0 lds\nM v[36:39],v[208:211],a[116:119],v[36:39]\nds_read_b64_tr_b16 v[194:195],v241 offset:256\nds_read_b128 a[132:135],v228 offset:10240\nM v[96:99],v[208:211],a[120:123],v[96:99]\n" \
"ds_read_b128 a[136:139],v228 offset:12288\nM v[100:103],v[208:211],a[124:127],v[100:103]\nds_read_b128 a[140:143],v228 offset:14336\nM v[40:43],v[212:215],a[112:115],v[40:43]\nds_read_b64_tr_b16 v[196:197],v242 offset:0\nds_read_b64_tr_b16 v[198:199],v242 offset:256\nM v[44:47],v[212:215],a[116:119],v[44:47]\nbuffer_load_dwordx4 v225,s[36:39],s67 offen offset:1024 lds\n" \
"M v[104:107],v[212:215],a[120:123],v[104:107]\nds_read_b64_tr_b16 v[200:201],v241 offset:512\nds_read_b64_tr_b16 v[202:203],v241 offset:768\nM v[108:111],v[212:215],a[124:127],v[108:111]\nds_read_b64_tr_b16 v[204:205],v242 offset:512\nds_read_b64_tr_b16 v[206:207],v242 offset:768\nM v[48:51],v[216:219],a[112:115],v[48:51]\nds_read_b64_tr_b16 v[208:209],v241 offset:1024\n" \
"ds_read_b64_tr_b16 v[210:211],v241 offset:1280\nM v[52:55],v[216:219],a[116:119],v[52:55]\nds_read_b64_tr_b16 v[212:213],v242 offset:1024\nds_read_b64_tr_b16 v[214:215],v242 offset:1280\nM v[112:115],v[216:219],a[120:123],v[112:115]\nbuffer_load_dwordx4 v226,s[36:39],s67 offen offset:2048 lds\nM v[116:119],v[216:219],a[124:127],v[116:119]\nds_read_b64_tr_b16 v[216:217],v241 offset:1536\n" \
"ds_read_b64_tr_b16 v[218:219],v241 offset:1792\nM v[56:59],v[220:223],a[112:115],v[56:59]\nds_read_b128 a[144:147],v229 offset:8192\nds_read_b128 a[148:151],v229 offset:10240\nM v[60:63],v[220:223],a[116:119],v[60:63]\nds_read_b128 a[152:155],v229 offset:12288\nM v[120:123],v[220:223],a[120:123],v[120:123]\nds_read_b128 a[156:159],v229 offset:14336\n" \
"M v[124:127],v[220:223],a[124:127],v[124:127]\nbuffer_load_dwordx4 v227,s[36:39],s67 offen offset:3072 lds\ns_waitcnt lgkmcnt(14)\nM v[0:3],v[192:195],a[128:131],v[0:3]\nds_read_b64_tr_b16 v[220:221],v242 offset:1536\nds_read_b64_tr_b16 v[222:223],v242 offset:1792\nM v[4:7],v[192:195],a[132:135],v[4:7]\ns_add_u32 m0,s65,4096\n" \
"s_add_u32 s67,s66,33536\nM v[64:67],v[192:195],a[136:139],v[64:67]\nM v[68:71],v[192:195],a[140:143],v[68:71]\nds_read_b64_tr_b16 v[192:193],v241 offset:8192\nds_read_b64_tr_b16 v[194:195],v241 offset:8448\nM v[8:11],v[196:199],a[128:131],v[8:11]\nbuffer_load_dwordx4 v224,s[36:39],s67 offen offset:0 lds\nM v[12:15],v[196:199],a[132:135],v[12:15]\n" \
"M v[72:75],v[196:199],a[136:139],v[72:75]\nM v[76:79],v[196:199],a[140:143],v[76:79]\nds_read_b64_tr_b16 v[196:197],v242 offset:8192\ns_waitcnt lgkmcnt(9)\nM v[16:19],v[200:203],a[128:131],v[16:19]\nds_read_b64_tr_b16 v[198:199],v242 offset:8448\nM v[20:23],v[200:203],a[132:135],v[20:23]\nbuffer_load_dwordx4 v225,s[36:39],s67 offen offset:1024 lds\n" \
"M v[80:83],v[200:203],a[136:139],v[80:83]\nM v[84:87],v[200:203],a[140:143],v[84:87]\nds_read_b64_tr_b16 v[200:201],v241 offset:8704\nds_read_b64_tr_b16 v[202:203],v241 offset:8960\nM v[24:27],v[204:207],a[128:131],v[24:27]\nM v[28:31],v[204:207],a[132:135],v[28:31]\nM v[88:91],v[204:207],a[136:139],v[88:91]\nbuffer_load_dwordx4 v226,s[36:39],s67 offen offset:2048 lds\n" \
"M v[92:95],v[204:207],a[140:143],v[92:95]\nds_read_b64_tr_b16 v[204:205],v242 offset:8704\nds_read_b64_tr_b16 v[206:207],v242 offset:8960\nM v[32:35],v[208:211],a[128:131],v[32:35]\nM v[36:39],v[208:211],a[132:135],v[36:39]\nM v[96:99],v[208:211],a[136:139],v[96:99]\nM v[100:103],v[208:211],a[140:143],v[100:103]\nbuffer_load_dwordx4 v227,s[36:39],s67 offen offset:3072 lds\n" \
"M v[40:43],v[212:215],a[128:131],v[40:43]\nds_read_b64_tr_b16 v[208:209],v241 offset:9216\nds_read_b64_tr_b16 v[210:211],v241 offset:9472\nM v[44:47],v[212:215],a[132:135],v[44:47]\ns_add_u32 m0,s64,131072\ns_add_u32 s69,s68,3145728\nM v[104:107],v[212:215],a[136:139],v[104:107]\nM v[108:111],v[212:215],a[140:143],v[108:111]\n" \
"ds_read_b64_tr_b16 v[212:213],v242 offset:9216\nds_read_b64_tr_b16 v[214:215],v242 offset:9472\nM v[48:51],v[216:219],a[128:131],v[48:51]\nbuffer_load_dwordx4 v243,s[40:43],s69 offen offset:0 lds\nM v[52:55],v[216:219],a[132:135],v[52:55]\nM v[112:115],v[216:219],a[136:139],v[112:115]\nM v[116:119],v[216:219],a[140:143],v[116:119]\nds_read_b64_tr_b16 v[216:217],v241 offset:9728\n" \
"s_waitcnt lgkmcnt(7)\nM v[56:59],v[220:223],a[128:131],v[56:59]\nds_read_b64_tr_b16 v[218:219],v241 offset:9984\nM v[60:63],v[220:223],a[132:135],v[60:63]\nbuffer_load_dwordx4 v244,s[40:43],s69 offen offset:1024 lds\nM v[120:123],v[220:223],a[136:139],v[120:123]\nM v[124:127],v[220:223],a[140:143],v[124:127]\nds_read_b64_tr_b16 v[220:221],v242 offset:9728\n" \
"ds_read_b64_tr_b16 v[222:223],v242 offset:9984\nM v[0:3],v[192:195],a[144:147],v[0:3]\nM v[4:7],v[192:195],a[148:151],v[4:7]\nM v[64:67],v[192:195],a[152:155],v[64:67]\nbuffer_load_dwordx4 v245,s[40:43],s69 offen offset:2048 lds\nM v[68:71],v[192:195],a[156:159],v[68:71]\nM v[8:11],v[196:199],a[144:147],v[8:11]\ns_waitcnt vmcnt(14)\n" \
"v_xor_b32 v166,v166,v230\nM v[12:15],v[196:199],a[148:151],v[12:15]\nv_xor_b32 v167,v167,v231\nv_or_b32 v166,v166,v167\nM v[72:75],v[196:199],a[152:155],v[72:75]\nv_cmp_ne_u32 vcc,0,v166\nM v[76:79],v[196:199],a[156:159],v[76:79]\nbuffer_load_dwordx4 v246,s[40:43],s69 offen offset:3072 lds\n" \
"M v[16:19],v[200:203],a[144:147],v[16:19]\ns_cbranch_vccnz .Lhs\n.Lhb:\nv_bfe_u32 v230,v249,2,3\nM v[20:23],v[200:203],a[148:151],v[20:23]\ns_add_u32 m0,s65,8192\ns_add_u32 s67,s66,896\nM v[80:83],v[200:203],a[152:155],v[80:83]\n" \
"v_and_b32 v231,0x70,v243\nv_lshl_or_b32 v230,v230,10,v231\nM v[84:87],v[200:203],a[156:159],v[84:87]\nv_and_b32 v231,16,v249\ns_waitcnt lgkmcnt(0)\nM v[24:27],v[204:207],a[144:147],v[24:27]\nbuffer_load_dwordx4 v224,s[36:39],s67 offen offset:0 lds\nM v[28:31],v[204:207],a[148:151],v[28:31]\n" \
"v_lshlrev_b32 v231,1,v231\nv_xor_b32 v230,v230,v231\nM v[88:91],v[204:207],a[152:155],v[88:91]\ns_waitcnt vmcnt(13)\ns_barrier\nM v[92:95],v[204:207],a[156:159],v[92:95]\nds_read_b128 a[160:163],v228 offset:16384\nds_read_b64_tr_b16 v[192:193],v241 offset:16384\n" \
"M v[32:35],v[208:211],a[144:147],v[32:35]\nbuffer_load_dwordx4 v[152:155],v240,s[52:55],s71 offen sc1\nM v[36:39],v[208:211],a[148:151],v[36:39]\ns_add_u32 s71,s70,798720\nds_read_b64_tr_b16 v[194:195],v241 offset:16640\nM v[96:99],v[208:211],a[152:155],v[96:99]\nds_read_b128 a[164:167],v228 offset:18432\nds_read_b128 a[168:171],v228 offset:20480\n" \
"M v[100:103],v[208:211],a[156:159],v[100:103]\nbuffer_load_dwordx4 v[156:159],v240,s[52:55],s71 offen sc1\nM v[40:43],v[212:215],a[144:147],v[40:43]\nbuffer_load_dwordx4 v225,s[36:39],s67 offen offset:1024 lds\ns_add_u32 s71,s70,811008\nM v[44:47],v[212:215],a[148:151],v[44:47]\nbuffer_load_dwordx4 v[160:163],v240,s[52:55],s71 offen sc1\nM v[104:107],v[212:215],a[152:155],v[104:107]\n" \
"buffer_load_dwordx4 v226,s[36:39],s67 offen offset:2048 lds\ns_add_u32 s71,s70,823296\nbuffer_load_dwordx4 v[164:167],v240,s[52:55],s71 offen sc1\nM v[108:111],v[212:215],a[156:159],v[108:111]\nds_read_b128 a[172:175],v228 offset:22528\nds_read_b64_tr_b16 v[196:197],v242 offset:16384\nM v[48:51],v[216:219],a[144:147],v[48:51]\nds_read_b64_tr_b16 v[198:199],v242 offset:16640\n" \
"ds_read_b64_tr_b16 v[200:201],v241 offset:16896\nM v[52:55],v[216:219],a[148:151],v[52:55]\nds_read_b64_tr_b16 v[202:203],v241 offset:17152\nds_read_b64_tr_b16 v[204:205],v242 offset:16896\nM v[112:115],v[216:219],a[152:155],v[112:115]\nds_read_b64_tr_b16 v[206:207],v242 offset:17152\nds_read_b64_tr_b16 v[208:209],v241 offset:17408\nM v[116:119],v[216:219],a[156:159],v[116:119]\n" \
"buffer_load_dwordx4 v227,s[36:39],s67 offen offset:3072 lds\nM v[56:59],v[220:223],a[144:147],v[56:59]\nds_read_b64_tr_b16 v[210:211],v241 offset:17664\nds_read_b64_tr_b16 v[212:213],v242 offset:17408\nM v[60:63],v[220:223],a[148:151],v[60:63]\ns_add_u32 m0,s65,12288\ns_add_u32 s67,s66,33664\nM v[120:123],v[220:223],a[152:155],v[120:123]\n" \
"ds_read_b64_tr_b16 v[214:215],v242 offset:17664\nds_read_b64_tr_b16 v[216:217],v241 offset:17920\nM v[124:127],v[220:223],a[156:159],v[124:127]\nds_read_b64_tr_b16 v[218:219],v241 offset:18176\ns_waitcnt lgkmcnt(13)\nM v[0:3],v[192:195],a[160:163],v[0:3]\nds_read_b64_tr_b16 v[220:221],v242 offset:17920\nds_read_b64_tr_b16 v[222:223],v242 offset:18176\n" \
"M v[4:7],v[192:195],a[164:167],v[4:7]\nds_read_b128 a[176:179],v229 offset:16384\nds_read_b128 a[180:183],v229 offset:18432\nM v[64:67],v[192:195],a[168:171],v[64:67]\nds_read_b128 a[184:187],v229 offset:20480\ns_waitcnt lgkmcnt(15)\nM v[68:71],v[192:195],a[172:175],v[68:71]\nds_read_b64_tr_b16 v[192:193],v241 offset:24576\n" \
"s_lshl_b32 s71,s6,5\nM v[8:11],v[196:199],a[160:163],v[8:11]\nds_read_b64_tr_b16 v[194:195],v241 offset:24832\nds_read_b128 a[188:191],v229 offset:22528\nM v[12:15],v[196:199],a[164:167],v[12:15]\ns_add_u32 s71,s71,790528\nv_and_b32 v231,16,v249\nM v[72:75],v[196:199],a[168:171],v[72:75]\n" \
"s_cmp_lt_u32 s7,2\ns_cselect_b32 s70,0,1.0\nM v[76:79],v[196:199],a[172:175],v[76:79]\nds_read_b64_tr_b16 v[196:197],v242 offset:24576\ns_waitcnt lgkmcnt(12)\nM v[16:19],v[200:203],a[160:163],v[16:19]\nds_read_b64_tr_b16 v[198:199],v242 offset:24832\nM v[20:23],v[200:203],a[164:167],v[20:23]\n" \
"M v[80:83],v[200:203],a[168:171],v[80:83]\nM v[84:87],v[200:203],a[172:175],v[84:87]\nds_read_b64_tr_b16 v[200:201],v241 offset:25088\nds_read_b64_tr_b16 v[202:203],v241 offset:25344\nM v[24:27],v[204:207],a[160:163],v[24:27]\nbuffer_load_dwordx4 v224,s[36:39],s67 offen offset:0 lds\nM v[28:31],v[204:207],a[164:167],v[28:31]\nM v[88:91],v[204:207],a[168:171],v[88:91]\n" \
"M v[92:95],v[204:207],a[172:175],v[92:95]\nds_read_b64_tr_b16 v[204:205],v242 offset:25088\nds_read_b64_tr_b16 v[206:207],v242 offset:25344\nM v[32:35],v[208:211],a[160:163],v[32:35]\nbuffer_load_dwordx4 v225,s[36:39],s67 offen offset:1024 lds\nM v[36:39],v[208:211],a[164:167],v[36:39]\ns_add_u32 s69,s68,3670016\nM v[96:99],v[208:211],a[168:171],v[96:99]\n" \
"M v[100:103],v[208:211],a[172:175],v[100:103]\nds_read_b64_tr_b16 v[208:209],v241 offset:25600\ns_waitcnt lgkmcnt(7)\nM v[40:43],v[212:215],a[160:163],v[40:43]\nbuffer_load_dwordx4 v226,s[36:39],s67 offen offset:2048 lds\nM v[44:47],v[212:215],a[164:167],v[44:47]\nds_read_b64_tr_b16 v[210:211],v241 offset:25856\nM v[104:107],v[212:215],a[168:171],v[104:107]\n" \
"M v[108:111],v[212:215],a[172:175],v[108:111]\nds_read_b64_tr_b16 v[212:213],v242 offset:25600\nds_read_b64_tr_b16 v[214:215],v242 offset:25856\nM v[48:51],v[216:219],a[160:163],v[48:51]\nbuffer_load_dwordx4 v227,s[36:39],s67 offen offset:3072 lds\nM v[52:55],v[216:219],a[164:167],v[52:55]\ns_cmp_eq_u32 s62,0\ns_cselect_b32 s36,0x8000,0\n" \
"M v[112:115],v[216:219],a[168:171],v[112:115]\ns_add_u32 m0,s64,147456\ns_add_u32 s36,s36,s64\nM v[116:119],v[216:219],a[172:175],v[116:119]\nds_read_b64_tr_b16 v[216:217],v241 offset:26112\nds_read_b64_tr_b16 v[218:219],v241 offset:26368\nM v[56:59],v[220:223],a[160:163],v[56:59]\nbuffer_load_dwordx4 v243,s[40:43],s69 offen offset:0 lds\n" \
"M v[60:63],v[220:223],a[164:167],v[60:63]\nM v[120:123],v[220:223],a[168:171],v[120:123]\nM v[124:127],v[220:223],a[172:175],v[124:127]\nds_read_b64_tr_b16 v[220:221],v242 offset:26112\nds_read_b64_tr_b16 v[222:223],v242 offset:26368\nM v[0:3],v[192:195],a[176:179],v[0:3]\nbuffer_load_dwordx4 v244,s[40:43],s69 offen offset:1024 lds\nM v[4:7],v[192:195],a[180:183],v[4:7]\n" \
"M v[64:67],v[192:195],a[184:187],v[64:67]\nM v[68:71],v[192:195],a[188:191],v[68:71]\ns_waitcnt lgkmcnt(6)\nM v[8:11],v[196:199],a[176:179],v[8:11]\nbuffer_load_dwordx4 v245,s[40:43],s69 offen offset:2048 lds\nM v[12:15],v[196:199],a[180:183],v[12:15]\nM v[72:75],v[196:199],a[184:187],v[72:75]\nM v[76:79],v[196:199],a[188:191],v[76:79]\n" \
"M v[16:19],v[200:203],a[176:179],v[16:19]\nbuffer_load_dwordx4 v246,s[40:43],s69 offen offset:3072 lds\nM v[20:23],v[200:203],a[180:183],v[20:23]\ns_sub_u32 s40,s16,0x1000\ns_subb_u32 s41,s17,0\nM v[80:83],v[200:203],a[184:187],v[80:83]\ns_add_u32 m0,s64,98304\ns_add_u32 s69,s68,0\n" \
"M v[84:87],v[200:203],a[188:191],v[84:87]\ns_mov_b32 s16,0\ns_mov_b32 s17,3\nM v[24:27],v[204:207],a[176:179],v[24:27]\nbuffer_load_dword v168,v169,s[52:55],s71 offen sc1\nM v[28:31],v[204:207],a[180:183],v[28:31]\ns_lshr_b32 s71,s64,2\nv_add_u32 v171,s71,v240\n" \
"M v[88:91],v[204:207],a[184:187],v[88:91]\ns_waitcnt vmcnt(17) lgkmcnt(0)\ns_barrier\nM v[92:95],v[204:207],a[188:191],v[92:95]\nds_read_b128 a[192:195],v228 offset:0\nds_read_b64_tr_b16 v[192:193],v241 offset:32768\nM v[32:35],v[208:211],a[176:179],v[32:35]\nds_read_b64_tr_b16 v[194:195],v241 offset:33024\n" \
"ds_read_b128 a[196:199],v228 offset:2048\nM v[36:39],v[208:211],a[180:183],v[36:39]\nds_read_b128 a[200:203],v228 offset:4096\ns_lshl_b32 s71,s6,5\nM v[96:99],v[208:211],a[184:187],v[96:99]\nds_read_b128 a[204:207],v228 offset:6144\ns_and_b32 s71,s71,0xfffffe00\nM v[100:103],v[208:211],a[188:191],v[100:103]\n" \
"buffer_load_dwordx4 v243,s[40:43],s69 offen offset:0 lds\nM v[40:43],v[212:215],a[176:179],v[40:43]\nds_read_b64_tr_b16 v[196:197],v242 offset:32768\nds_read_b64_tr_b16 v[198:199],v242 offset:33024\nM v[44:47],v[212:215],a[180:183],v[44:47]\nds_read_b64_tr_b16 v[200:201],v241 offset:33280\nds_read_b64_tr_b16 v[202:203],v241 offset:33536\nM v[104:107],v[212:215],a[184:187],v[104:107]\n" \
"ds_read_b64_tr_b16 v[204:205],v242 offset:33280\nds_read_b64_tr_b16 v[206:207],v242 offset:33536\nM v[108:111],v[212:215],a[188:191],v[108:111]\nds_read_b64_tr_b16 v[208:209],v241 offset:33792\nds_read_b64_tr_b16 v[210:211],v241 offset:34048\nM v[48:51],v[216:219],a[176:179],v[48:51]\nds_read_b64_tr_b16 v[212:213],v242 offset:33792\nds_read_b64_tr_b16 v[214:215],v242 offset:34048\n" \
"M v[52:55],v[216:219],a[180:183],v[52:55]\nds_read_b128 a[208:211],v229 offset:0\nds_read_b128 a[212:215],v229 offset:2048\nM v[112:115],v[216:219],a[184:187],v[112:115]\nds_read_b128 a[216:219],v229 offset:4096\ns_waitcnt vmcnt(15)\nv_add_f32 v152,v152,v156\nM v[116:119],v[216:219],a[188:191],v[116:119]\n" \
"ds_read_b64_tr_b16 v[216:217],v241 offset:34304\nv_add_f32 v153,v153,v157\nM v[56:59],v[220:223],a[176:179],v[56:59]\nds_read_b64_tr_b16 v[218:219],v241 offset:34560\nds_read_b128 a[220:223],v229 offset:6144\nM v[60:63],v[220:223],a[180:183],v[60:63]\ns_waitcnt vmcnt(11)\nv_add_f32 v160,v160,v164\n" \
"M v[120:123],v[220:223],a[184:187],v[120:123]\nv_add_f32 v161,v161,v165\nv_add_f32 v154,v154,v158\nM v[124:127],v[220:223],a[188:191],v[124:127]\nds_read_b64_tr_b16 v[220:221],v242 offset:34304\ns_waitcnt lgkmcnt(13)\nM v[0:3],v[192:195],a[192:195],v[0:3]\nds_read_b64_tr_b16 v[222:223],v242 offset:34560\n" \
"v_add_f32 v162,v162,v166\nM v[4:7],v[192:195],a[196:199],v[4:7]\nv_add_f32 v155,v155,v159\nv_add_f32 v163,v163,v167\nM v[64:67],v[192:195],a[200:203],v[64:67]\nv_add_f32 v152,v152,v160\nv_add_f32 v153,v153,v161\nM v[68:71],v[192:195],a[204:207],v[68:71]\n" \
"ds_read_b64_tr_b16 v[192:193],v241 offset:40960\nds_read_b64_tr_b16 v[194:195],v241 offset:41216\nM v[8:11],v[196:199],a[192:195],v[8:11]\nv_add_f32 v154,v154,v162\nv_add_f32 v155,v155,v163\nM v[12:15],v[196:199],a[196:199],v[12:15]\nv_cvt_pk_bf16_f32 v152,0,v152\nv_cvt_pk_bf16_f32 v153,0,v153\n" \
"M v[72:75],v[196:199],a[200:203],v[72:75]\nv_cvt_pk_bf16_f32 v154,0,v154\nv_cvt_pk_bf16_f32 v155,0,v155\nM v[76:79],v[196:199],a[204:207],v[76:79]\nds_read_b64_tr_b16 v[196:197],v242 offset:40960\nds_read_b64_tr_b16 v[198:199],v242 offset:41216\nM v[16:19],v[200:203],a[192:195],v[16:19]\nv_add_f32 v152,s70,v152\n" \
"v_add_f32 v153,s70,v153\nM v[20:23],v[200:203],a[196:199],v[20:23]\nv_add_f32 v154,s70,v154\nv_add_f32 v155,s70,v155\nM v[80:83],v[200:203],a[200:203],v[80:83]\nv_cvt_pk_bf16_f32 v152,0,v152\nv_cvt_pk_bf16_f32 v153,0,v153\nM v[84:87],v[200:203],a[204:207],v[84:87]\n" \
"ds_read_b64_tr_b16 v[200:201],v241 offset:41472\ns_waitcnt lgkmcnt(5)\nM v[24:27],v[204:207],a[192:195],v[24:27]\nds_read_b64_tr_b16 v[202:203],v241 offset:41728\nv_cvt_pk_bf16_f32 v154,0,v154\nM v[28:31],v[204:207],a[196:199],v[28:31]\nv_cvt_pk_bf16_f32 v155,0,v155\ns_lshl_b32 s70,s7,8\n" \
"M v[88:91],v[204:207],a[200:203],v[88:91]\nds_write_b128 v171,v[152:155] offset:16384\nM v[92:95],v[204:207],a[204:207],v[92:95]\nds_read_b64_tr_b16 v[204:205],v242 offset:41472\nds_read_b64_tr_b16 v[206:207],v242 offset:41728\nM v[32:35],v[208:211],a[192:195],v[32:35]\nbuffer_load_dwordx4 v244,s[40:43],s69 offen offset:1024 lds\nM v[36:39],v[208:211],a[196:199],v[36:39]\n" \
"v_lshl_add_u32 v251,v251,5,s70\ns_xor_b32 s70,s63,16\nM v[96:99],v[208:211],a[200:203],v[96:99]\ns_lshr_b32 s70,s70,2\ns_lshl3_add_u32 s70,s62,s70\nM v[100:103],v[208:211],a[204:207],v[100:103]\nds_read_b64_tr_b16 v[208:209],v241 offset:41984\nds_read_b64_tr_b16 v[210:211],v241 offset:42240\n" \
"M v[40:43],v[212:215],a[192:195],v[40:43]\nbuffer_load_dwordx4 v245,s[40:43],s69 offen offset:2048 lds\nM v[44:47],v[212:215],a[196:199],v[44:47]\ns_lshl_b32 s70,s70,15\ns_add_u32 s70,s70,s64\nM v[104:107],v[212:215],a[200:203],v[104:107]\nM v[108:111],v[212:215],a[204:207],v[108:111]\nds_read_b64_tr_b16 v[212:213],v242 offset:41984\n" \
"ds_read_b64_tr_b16 v[214:215],v242 offset:42240\nM v[48:51],v[216:219],a[192:195],v[48:51]\nbuffer_load_dwordx4 v246,s[40:43],s69 offen offset:3072 lds\nM v[52:55],v[216:219],a[196:199],v[52:55]\ns_add_u32 m0,s64,114688\ns_add_u32 s69,s68,524288\nM v[112:115],v[216:219],a[200:203],v[112:115]\nM v[116:119],v[216:219],a[204:207],v[116:119]\n" \
"ds_read_b64_tr_b16 v[216:217],v241 offset:42496\nds_read_b64_tr_b16 v[218:219],v241 offset:42752\nM v[56:59],v[220:223],a[192:195],v[56:59]\nbuffer_store_dwordx2 v[232:233],off,s[52:55],s72 sc1\nM v[60:63],v[220:223],a[196:199],v[60:63]\ns_and_b32 s72,s6,1\ns_lshl_b32 s72,s72,8\nM v[120:123],v[220:223],a[200:203],v[120:123]\n" \
"s_add_u32 s71,s71,s72\ns_and_b32 s72,s6,14\nM v[124:127],v[220:223],a[204:207],v[124:127]\nds_read_b64_tr_b16 v[220:221],v242 offset:42496\ns_waitcnt lgkmcnt(5)\nM v[0:3],v[192:195],a[208:211],v[0:3]\nds_read_b64_tr_b16 v[222:223],v242 offset:42752\ns_lshl1_add_u32 s71,s72,s71\n" \
"M v[4:7],v[192:195],a[212:215],v[4:7]\ns_or_b32 s71,s71,s75\ns_mov_b32 s72,0x3e0293ee\nM v[64:67],v[192:195],a[216:219],v[64:67]\nbuffer_load_dwordx4 v243,s[40:43],s69 offen offset:0 lds\nM v[68:71],v[192:195],a[220:223],v[68:71]\nM v[8:11],v[196:199],a[208:211],v[8:11]\nM v[12:15],v[196:199],a[212:215],v[12:15]\n" \
"M v[72:75],v[196:199],a[216:219],v[72:75]\nglobal_load_dword v247,v253,s[12:13]\nM v[76:79],v[196:199],a[220:223],v[76:79]\nbuffer_load_dwordx4 v244,s[40:43],s69 offen offset:1024 lds\nM v[16:19],v[200:203],a[208:211],v[16:19]\nM v[20:23],v[200:203],a[212:215],v[20:23]\nM v[80:83],v[200:203],a[216:219],v[80:83]\nM v[84:87],v[200:203],a[220:223],v[84:87]\n" \
"buffer_load_dword v247,v252,s[56:59],s78 offen\nM v[24:27],v[204:207],a[208:211],v[24:27]\nbuffer_load_dwordx4 v245,s[40:43],s69 offen offset:2048 lds\nM v[28:31],v[204:207],a[212:215],v[28:31]\nv_lshrrev_b32 v253,4,v249\nM v[88:91],v[204:207],a[216:219],v[88:91]\ns_waitcnt vmcnt(10) lgkmcnt(0)\ns_barrier\n" \
"M v[92:95],v[204:207],a[220:223],v[92:95]\nds_read_b128 a[224:227],v228 offset:8192\nds_read_b64_tr_b16 v[192:193],v241 offset:49152\nM v[32:35],v[208:211],a[208:211],v[32:35]\nds_read_b64_tr_b16 v[194:195],v241 offset:49408\nds_read_b128 a[228:231],v228 offset:10240\nM v[36:39],v[208:211],a[212:215],v[36:39]\nds_read_b128 a[232:235],v228 offset:12288\n" \
"v_mov_b32 v252,0xff800000\nM v[96:99],v[208:211],a[216:219],v[96:99]\nds_read_b128 a[236:239],v228 offset:14336\nv_add_f32_dpp v168,v168,v168 quad_perm:[1,0,3,2] row_mask:0xf bank_mask:0xf\nM v[100:103],v[208:211],a[220:223],v[100:103]\nds_read_b64_tr_b16 v[196:197],v242 offset:49152\nv_add_f32_dpp v168,v168,v168 quad_perm:[2,3,0,1] row_mask:0xf bank_mask:0xf\nM v[40:43],v[212:215],a[208:211],v[40:43]\n" \
"ds_read_b64_tr_b16 v[198:199],v242 offset:49408\nds_read_b64_tr_b16 v[200:201],v241 offset:49664\nM v[44:47],v[212:215],a[212:215],v[44:47]\nds_read_b64_tr_b16 v[202:203],v241 offset:49920\nds_read_b64_tr_b16 v[204:205],v242 offset:49664\nM v[104:107],v[212:215],a[216:219],v[104:107]\nds_read_b64_tr_b16 v[206:207],v242 offset:49920\nds_read_b64_tr_b16 v[208:209],v241 offset:50176\n" \
"M v[108:111],v[212:215],a[220:223],v[108:111]\nds_read_b64_tr_b16 v[210:211],v241 offset:50432\nds_read_b64_tr_b16 v[212:213],v242 offset:50176\nM v[48:51],v[216:219],a[208:211],v[48:51]\nds_read_b64_tr_b16 v[214:215],v242 offset:50432\nds_read_b128 a[240:243],v229 offset:8192\nM v[52:55],v[216:219],a[212:215],v[52:55]\nds_read_b128 a[244:247],v229 offset:10240\n" \
"v_cvt_pk_bf16_f32 v168,0,v168\nM v[112:115],v[216:219],a[216:219],v[112:115]\nds_read_b128 a[248:251],v229 offset:12288\nv_and_b32 v228,15,v249\nM v[116:119],v[216:219],a[220:223],v[116:119]\nds_read_b64_tr_b16 v[216:217],v241 offset:50688\nv_lshlrev_b32 v228,2,v228\nM v[56:59],v[220:223],a[208:211],v[56:59]\n" \
"ds_read_b64_tr_b16 v[218:219],v241 offset:50944\nds_read_b128 a[252:255],v229 offset:14336\nM v[60:63],v[220:223],a[212:215],v[60:63]\nds_read_b128 v[176:179],v251 offset:19456\nv_and_b32 v229,16,v249\nM v[120:123],v[220:223],a[216:219],v[120:123]\nds_read_b128 v[160:163],v251 offset:17408\nv_lshl_or_b32 v228,v229,4,v228\n" \
"M v[124:127],v[220:223],a[220:223],v[124:127]\nds_read_b64_tr_b16 v[220:221],v242 offset:50688\ns_waitcnt lgkmcnt(15)\nM v[0:3],v[192:195],a[224:227],v[0:3]\nds_read_b64_tr_b16 v[222:223],v242 offset:50944\nds_read_b128 v[172:175],v251 offset:18448\nM v[4:7],v[192:195],a[228:231],v[4:7]\nbuffer_store_dword v168,v170,s[52:55],s71 offen sc1\n" \
"M v[64:67],v[192:195],a[232:235],v[64:67]\nds_read_b128 v[168:171],v251 offset:18432\nds_read_b128 v[180:183],v251 offset:19472\nM v[68:71],v[192:195],a[236:239],v[68:71]\nds_read_b64_tr_b16 v[192:193],v241 offset:57344\ns_and_b32 s71,s63,2\nM v[8:11],v[196:199],a[224:227],v[8:11]\nds_read_b64_tr_b16 v[194:195],v241 offset:57600\n" \
"ds_read_b128 v[152:155],v251 offset:16384\nM v[12:15],v[196:199],a[228:231],v[12:15]\nds_read_b128 v[156:159],v251 offset:16400\ns_lshl_b32 s71,s71,9\nM v[72:75],v[196:199],a[232:235],v[72:75]\nds_read_b128 v[164:167],v251 offset:17424\nv_lshrrev_b32 v251,5,v249\nM v[76:79],v[196:199],a[236:239],v[76:79]\n" \
"ds_read_b64_tr_b16 v[196:197],v242 offset:57344\ns_waitcnt lgkmcnt(12)\nv_mul_f32 v144,v144,v176\nM v[16:19],v[200:203],a[224:227],v[16:19]\nds_read_b64_tr_b16 v[198:199],v242 offset:57600\nv_mul_f32 v145,v145,v177\nM v[20:23],v[200:203],a[228:231],v[20:23]\nv_cvt_pk_bf16_f32 v144,0,v144\n" \
"v_cvt_pk_bf16_f32 v145,0,v145\nM v[80:83],v[200:203],a[232:235],v[80:83]\ns_waitcnt lgkmcnt(10)\nv_add_f32 v144,v144,v160\nM v[84:87],v[200:203],a[236:239],v[84:87]\nds_read_b64_tr_b16 v[200:201],v241 offset:57856\nds_read_b64_tr_b16 v[202:203],v241 offset:58112\nM v[24:27],v[204:207],a[224:227],v[24:27]\n" \
"v_add_f32 v145,v145,v161\nv_cvt_pk_bf16_f32 v144,0,v144\nM v[28:31],v[204:207],a[228:231],v[28:31]\nv_cvt_pk_bf16_f32 v145,0,v145\nv_mul_f32 v144,s72,v144\nM v[88:91],v[204:207],a[232:235],v[88:91]\nv_mul_f32 v145,s72,v145\nv_cvt_pk_bf16_f32 v236,v144,v145\n" \
"M v[92:95],v[204:207],a[236:239],v[92:95]\nds_read_b64_tr_b16 v[204:205],v242 offset:57856\nds_read_b64_tr_b16 v[206:207],v242 offset:58112\nM v[32:35],v[208:211],a[224:227],v[32:35]\nv_mul_f32 v146,v146,v178\nv_mul_f32 v147,v147,v179\nM v[36:39],v[208:211],a[228:231],v[36:39]\nv_cvt_pk_bf16_f32 v146,0,v146\n" \
"v_cvt_pk_bf16_f32 v147,0,v147\nM v[96:99],v[208:211],a[232:235],v[96:99]\nv_add_f32 v146,v146,v162\nv_add_f32 v147,v147,v163\nM v[100:103],v[208:211],a[236:239],v[100:103]\nds_read_b64_tr_b16 v[208:209],v241 offset:58368\nds_read_b64_tr_b16 v[210:211],v241 offset:58624\nM v[40:43],v[212:215],a[224:227],v[40:43]\n" \
"v_cvt_pk_bf16_f32 v146,0,v146\nv_cvt_pk_bf16_f32 v147,0,v147\nM v[44:47],v[212:215],a[228:231],v[44:47]\nv_mul_f32 v146,s72,v146\nv_mul_f32 v147,s72,v147\nM v[104:107],v[212:215],a[232:235],v[104:107]\nv_cvt_pk_bf16_f32 v237,v146,v147\ns_waitcnt lgkmcnt(4)\n" \
"v_mul_f32 v136,v136,v168\nM v[108:111],v[212:215],a[236:239],v[108:111]\nds_read_b64_tr_b16 v[212:213],v242 offset:58368\nds_read_b64_tr_b16 v[214:215],v242 offset:58624\nM v[48:51],v[216:219],a[224:227],v[48:51]\nv_mul_f32 v137,v137,v169\nv_mul_f32 v138,v138,v170\nM v[52:55],v[216:219],a[228:231],v[52:55]\n" \
"v_mul_f32 v139,v139,v171\nv_mul_f32 v140,v140,v172\nM v[112:115],v[216:219],a[232:235],v[112:115]\nv_mul_f32 v141,v141,v173\nv_mul_f32 v142,v142,v174\nM v[116:119],v[216:219],a[236:239],v[116:119]\nds_read_b64_tr_b16 v[216:217],v241 offset:58880\nds_read_b64_tr_b16 v[218:219],v241 offset:59136\n" \
"M v[56:59],v[220:223],a[224:227],v[56:59]\nv_mul_f32 v143,v143,v175\nv_mul_f32 v148,v148,v180\nM v[60:63],v[220:223],a[228:231],v[60:63]\nv_mul_f32 v149,v149,v181\nv_cvt_pk_bf16_f32 v136,0,v136\nM v[120:123],v[220:223],a[232:235],v[120:123]\nv_cvt_pk_bf16_f32 v137,0,v137\n" \
"v_cvt_pk_bf16_f32 v138,0,v138\nM v[124:127],v[220:223],a[236:239],v[124:127]\nds_read_b64_tr_b16 v[220:221],v242 offset:58880\nds_read_b64_tr_b16 v[222:223],v242 offset:59136\nM v[0:3],v[192:195],a[240:243],v[0:3]\nv_cvt_pk_bf16_f32 v139,0,v139\nv_cvt_pk_bf16_f32 v140,0,v140\nM v[4:7],v[192:195],a[244:247],v[4:7]\n" \
"v_cvt_pk_bf16_f32 v141,0,v141\nv_cvt_pk_bf16_f32 v142,0,v142\nM v[64:67],v[192:195],a[248:251],v[64:67]\nv_cvt_pk_bf16_f32 v143,0,v143\nv_cvt_pk_bf16_f32 v148,0,v148\nM v[68:71],v[192:195],a[252:255],v[68:71]\nv_cvt_pk_bf16_f32 v128,v0,v1\nv_cvt_pk_bf16_f32 v129,v2,v3\n" \
"M v[8:11],v[196:199],a[240:243],v[8:11]\nv_cvt_pk_bf16_f32 v144,v4,v5\nv_cvt_pk_bf16_f32 v145,v6,v7\nM v[12:15],v[196:199],a[244:247],v[12:15]\nv_cvt_pk_bf16_f32 v160,v64,v65\nv_cvt_pk_bf16_f32 v161,v66,v67\nM v[72:75],v[196:199],a[248:251],v[72:75]\nv_cvt_pk_bf16_f32 v176,v68,v69\n" \
"v_cvt_pk_bf16_f32 v177,v70,v71\nM v[76:79],v[196:199],a[252:255],v[76:79]\nv_cvt_pk_bf16_f32 v130,v8,v9\nv_cvt_pk_bf16_f32 v131,v10,v11\nM v[16:19],v[200:203],a[240:243],v[16:19]\nv_cvt_pk_bf16_f32 v146,v12,v13\nv_cvt_pk_bf16_f32 v147,v14,v15\nM v[20:23],v[200:203],a[244:247],v[20:23]\n" \
"v_cvt_pk_bf16_f32 v162,v72,v73\nv_cvt_pk_bf16_f32 v163,v74,v75\nM v[80:83],v[200:203],a[248:251],v[80:83]\nv_cvt_pk_bf16_f32 v149,0,v149\ns_add_u32 s70,s70,s71\nM v[84:87],v[200:203],a[252:255],v[84:87]\nv_add_f32 v136,v136,v152\ns_waitcnt lgkmcnt(0)\n" \
"M v[24:27],v[204:207],a[240:243],v[24:27]\nbuffer_load_dwordx4 v246,s[40:43],s69 offen offset:3072 lds\nM v[28:31],v[204:207],a[244:247],v[28:31]\nv_add_f32 v137,v137,v153\nv_add_f32 v138,v138,v154\nM v[88:91],v[204:207],a[248:251],v[88:91]\ns_waitcnt vmcnt(8)\ns_barrier\n" \
"M v[92:95],v[204:207],a[252:255],v[92:95]\nds_read_b64_tr_b16 v[192:193],v241 offset:0\nds_read_b64_tr_b16 v[194:195],v241 offset:256\nM v[32:35],v[208:211],a[240:243],v[32:35]\nds_read_b64_tr_b16 v[196:197],v242 offset:0\nds_read_b64_tr_b16 v[198:199],v242 offset:256\nM v[36:39],v[208:211],a[244:247],v[36:39]\nds_read_b64_tr_b16 v[200:201],v241 offset:512\n" \
"ds_read_b64_tr_b16 v[202:203],v241 offset:768\nM v[96:99],v[208:211],a[248:251],v[96:99]\nds_read_b64_tr_b16 v[204:205],v242 offset:512\nds_read_b64_tr_b16 v[206:207],v242 offset:768\nM v[100:103],v[208:211],a[252:255],v[100:103]\nds_read_b64_tr_b16 v[208:209],v241 offset:1024\nds_read_b64_tr_b16 v[210:211],v241 offset:1280\nM v[40:43],v[212:215],a[240:243],v[40:43]\n" \
"v_add_f32 v139,v139,v155\nv_add_f32 v140,v140,v156\nM v[44:47],v[212:215],a[244:247],v[44:47]\nv_add_f32 v141,v141,v157\nv_add_f32 v142,v142,v158\nM v[104:107],v[212:215],a[248:251],v[104:107]\nv_add_f32 v143,v143,v159\nv_add_f32 v148,v148,v164\n" \
"M v[108:111],v[212:215],a[252:255],v[108:111]\nv_add_f32 v149,v149,v165\nv_lshlrev_b32 v251,11,v251\nM v[48:51],v[216:219],a[240:243],v[48:51]\ns_and_b32 s71,s63,1\nv_cvt_pk_bf16_f32 v178,v76,v77\nM v[52:55],v[216:219],a[244:247],v[52:55]\nv_cvt_pk_bf16_f32 v179,v78,v79\n" \
"v_cvt_pk_bf16_f32 v136,0,v136\nM v[112:115],v[216:219],a[248:251],v[112:115]\nv_cvt_pk_bf16_f32 v137,0,v137\nv_cvt_pk_bf16_f32 v138,0,v138\nM v[116:119],v[216:219],a[252:255],v[116:119]\nv_cvt_pk_bf16_f32 v139,0,v139\nv_cvt_pk_bf16_f32 v140,0,v140\nM v[56:59],v[220:223],a[240:243],v[56:59]\n" \
"v_cvt_pk_bf16_f32 v141,0,v141\nv_cvt_pk_bf16_f32 v142,0,v142\nM v[60:63],v[220:223],a[244:247],v[60:63]\nv_cvt_pk_bf16_f32 v143,0,v143\nv_cvt_pk_bf16_f32 v148,0,v148\nM v[120:123],v[220:223],a[248:251],v[120:123]\nv_cvt_pk_bf16_f32 v149,0,v149\nv_lshl_or_b32 v251,v250,8,v251\n" \
"M v[124:127],v[220:223],a[252:255],v[124:127]\ns_lshl_b32 s71,s71,7\ns_waitcnt lgkmcnt(0)\nM v[0:3],a[0:3],v[192:195],0\nv_mul_f32 v136,s72,v136\nv_mul_f32 v137,s72,v137\nM v[4:7],a[4:7],v[192:195],0\nv_mul_f32 v138,s72,v138\n" \
"v_mul_f32 v139,s72,v139\nM v[64:67],a[8:11],v[192:195],0\nv_mul_f32 v140,s72,v140\nv_mul_f32 v141,s72,v141\nM v[68:71],a[12:15],v[192:195],0\nv_mul_f32 v142,s72,v142\nv_mul_f32 v143,s72,v143\nM v[8:11],a[0:3],v[196:199],0\n" \
"v_mul_f32 v148,s72,v148\nv_mul_f32 v149,s72,v149\nM v[12:15],a[4:7],v[196:199],0\nv_and_b32 v250,0x70,v240\ns_add_u32 s70,s70,s71\nM v[72:75],a[8:11],v[196:199],0\nv_cvt_pk_bf16_f32 v132,v16,v17\nv_cvt_pk_bf16_f32 v133,v18,v19\n" \
"M v[76:79],a[12:15],v[196:199],0\nv_cvt_pk_bf16_f32 v232,v136,v137\nv_cvt_pk_bf16_f32 v233,v138,v139\nM v[16:19],a[0:3],v[200:203],0\nv_cvt_pk_bf16_f32 v234,v140,v141\nv_cvt_pk_bf16_f32 v235,v142,v143\nv_cvt_pk_bf16_f32 v238,v148,v149\nv_or_b32 v251,v251,v250\n" \
"s_add_u32 s70,s70,1048576\nbuffer_store_dwordx4 v[232:235],v251,s[52:55],s70 offen sc1\nv_cvt_pk_bf16_f32 v148,v20,v21\nv_cvt_pk_bf16_f32 v149,v22,v23\nM v[20:23],a[4:7],v[200:203],0\nv_mul_f32 v150,v150,v182\nv_mul_f32 v151,v151,v183\nv_cvt_pk_bf16_f32 v164,v80,v81\n" \
"v_cvt_pk_bf16_f32 v165,v82,v83\nds_read_b64_tr_b16 v[212:213],v242 offset:1024\nds_read_b64_tr_b16 v[214:215],v242 offset:1280\nM v[80:83],a[8:11],v[200:203],0\nv_cvt_pk_bf16_f32 v150,0,v150\nv_cvt_pk_bf16_f32 v151,0,v151\nv_add_f32 v150,v150,v166\nv_add_f32 v151,v151,v167\n" \
"v_cvt_pk_bf16_f32 v180,v84,v85\nv_cvt_pk_bf16_f32 v181,v86,v87\nM v[84:87],a[12:15],v[200:203],0\nv_cvt_pk_bf16_f32 v150,0,v150\nv_cvt_pk_bf16_f32 v151,0,v151\nv_mul_f32 v150,s72,v150\nv_mul_f32 v151,s72,v151\nv_cvt_pk_bf16_f32 v134,v24,v25\n" \
"v_cvt_pk_bf16_f32 v135,v26,v27\nM v[24:27],a[0:3],v[204:207],0\nv_cvt_pk_bf16_f32 v239,v150,v151\ns_add_u32 s71,s70,16384\nbuffer_store_dwordx4 v[236:239],v251,s[52:55],s71 offen sc1\nv_cvt_pk_bf16_f32 v150,v28,v29\nv_cvt_pk_bf16_f32 v151,v30,v31\nM v[28:31],a[4:7],v[204:207],0\n" \
"v_cvt_pk_bf16_f32 v166,v88,v89\nv_cvt_pk_bf16_f32 v167,v90,v91\nds_read_b64_tr_b16 v[216:217],v241 offset:1536\nds_read_b64_tr_b16 v[218:219],v241 offset:1792\nM v[88:91],a[8:11],v[204:207],0\nv_cvt_pk_bf16_f32 v182,v92,v93\nv_cvt_pk_bf16_f32 v183,v94,v95\nM v[92:95],a[12:15],v[204:207],0\n" \
"v_cvt_pk_bf16_f32 v136,v32,v33\nv_cvt_pk_bf16_f32 v137,v34,v35\nM v[32:35],a[0:3],v[208:211],0\ns_lshl_b32 s70,s62,5\ns_add_u32 s70,s70,s63\nv_cvt_pk_bf16_f32 v152,v36,v37\nv_cvt_pk_bf16_f32 v153,v38,v39\nM v[36:39],a[4:7],v[208:211],0\n" \
"s_lshl2_add_u32 s70,s70,s7\ns_lshl2_add_u32 s79,s70,90112\nv_cvt_pk_bf16_f32 v168,v96,v97\nv_cvt_pk_bf16_f32 v169,v98,v99\nds_read_b64_tr_b16 v[220:221],v242 offset:1536\nds_read_b64_tr_b16 v[222:223],v242 offset:1792\nM v[96:99],a[8:11],v[208:211],0\ns_lshl_b32 s70,s60,4\n" \
"v_cvt_pk_bf16_f32 v184,v100,v101\nv_cvt_pk_bf16_f32 v185,v102,v103\ns_sub_u32 s70,s70,s62\nM v[100:103],a[12:15],v[208:211],0\ns_add_u32 s70,s70,15\nv_cvt_pk_bf16_f32 v138,v40,v41\nv_cvt_pk_bf16_f32 v139,v42,v43\ns_lshl2_add_u32 s70,s70,s7\n" \
"s_waitcnt lgkmcnt(4)\nM v[40:43],a[0:3],v[212:215],0\ns_lshl_b32 s70,s70,15\ns_lshl_b32 s71,s61,8\nv_cvt_pk_bf16_f32 v154,v44,v45\nv_cvt_pk_bf16_f32 v155,v46,v47\ns_add_u32 s70,s70,s71\nM v[44:47],a[4:7],v[212:215],0\n" \
"s_add_u32 s78,s70,54525952\nv_cvt_pk_bf16_f32 v170,v104,v105\nv_cvt_pk_bf16_f32 v171,v106,v107\ns_lshl3_add_u32 s70,s62,s7\nds_read_b64_tr_b16 v[192:193],v241 offset:8192\nds_read_b64_tr_b16 v[194:195],v241 offset:8448\nM v[104:107],a[8:11],v[212:215],0\ns_lshl_b32 s80,s70,6\n" \
"s_lshl_b32 s76,s70,15\nv_cvt_pk_bf16_f32 v186,v108,v109\nv_cvt_pk_bf16_f32 v187,v110,v111\ns_lshl_b32 s70,s63,5\nM v[108:111],a[12:15],v[212:215],0\ns_add_u32 s70,s70,s74\nv_cvt_pk_bf16_f32 v140,v48,v49\nv_cvt_pk_bf16_f32 v141,v50,v51\n" \
"s_lshl_b32 s74,s70,14\ns_waitcnt lgkmcnt(2)\nM v[48:51],a[0:3],v[216:219],0\ns_add_u32 s74,s74,3145728\nv_cvt_pk_bf16_f32 v156,v52,v53\nv_cvt_pk_bf16_f32 v157,v54,v55\nbuffer_store_dwordx4 v[128:131],v240,s[52:55],s74 offen offset:0 sc1\nM v[52:55],a[4:7],v[216:219],0\n" \
"v_cvt_pk_bf16_f32 v172,v112,v113\nv_cvt_pk_bf16_f32 v173,v114,v115\nds_read_b64_tr_b16 v[196:197],v242 offset:8192\nds_read_b64_tr_b16 v[198:199],v242 offset:8448\nM v[112:115],a[8:11],v[216:219],0\nv_cvt_pk_bf16_f32 v188,v116,v117\nv_cvt_pk_bf16_f32 v189,v118,v119\nM v[116:119],a[12:15],v[216:219],0\n" \
"v_cvt_pk_bf16_f32 v142,v56,v57\nv_cvt_pk_bf16_f32 v143,v58,v59\nM v[56:59],a[0:3],v[220:223],0\nv_cvt_pk_bf16_f32 v158,v60,v61\nv_cvt_pk_bf16_f32 v159,v62,v63\nM v[60:63],a[4:7],v[220:223],0\nv_cvt_pk_bf16_f32 v174,v120,v121\nv_cvt_pk_bf16_f32 v175,v122,v123\n" \
"ds_read_b64_tr_b16 v[200:201],v241 offset:8704\nds_read_b64_tr_b16 v[202:203],v241 offset:8960\nM v[120:123],a[8:11],v[220:223],0\ns_add_u32 m0,s64,131072\ns_add_u32 s69,s68,1048576\nv_cvt_pk_bf16_f32 v190,v124,v125\nv_cvt_pk_bf16_f32 v191,v126,v127\nM v[124:127],a[12:15],v[220:223],0\n" \
"ds_read_b64_tr_b16 v[204:205],v242 offset:8704\ns_waitcnt lgkmcnt(5)\nM v[0:3],a[16:19],v[192:195],v[0:3]\nds_read_b64_tr_b16 v[206:207],v242 offset:8960\nds_read_b64_tr_b16 v[208:209],v241 offset:9216\nM v[4:7],a[20:23],v[192:195],v[4:7]\nds_read_b64_tr_b16 v[210:211],v241 offset:9472\nds_read_b64_tr_b16 v[212:213],v242 offset:9216\n" \
"M v[64:67],a[24:27],v[192:195],v[64:67]\nds_read_b64_tr_b16 v[214:215],v242 offset:9472\nds_read_b64_tr_b16 v[216:217],v241 offset:9728\nM v[68:71],a[28:31],v[192:195],v[68:71]\nds_read_b64_tr_b16 v[218:219],v241 offset:9984\ns_waitcnt lgkmcnt(10)\nM v[8:11],a[16:19],v[196:199],v[8:11]\nds_read_b64_tr_b16 v[220:221],v242 offset:9728\n" \
"ds_read_b64_tr_b16 v[222:223],v242 offset:9984\nM v[12:15],a[20:23],v[196:199],v[12:15]\nbuffer_load_dwordx4 v243,s[40:43],s69 offen offset:0 lds\nM v[72:75],a[24:27],v[196:199],v[72:75]\ns_add_u32 s80,s80,90112\ns_add_u32 s76,s76,1048576\nM v[76:79],a[28:31],v[196:199],v[76:79]\ns_add_u32 s77,s76,131072\n" \
"s_waitcnt lgkmcnt(10)\nM v[16:19],a[16:19],v[200:203],v[16:19]\ns_lshl2_add_u32 s29,s70,81920\ns_lshl_b32 s70,s7,14\nM v[20:23],a[20:23],v[200:203],v[20:23]\nbuffer_store_dwordx4 v[132:135],v240,s[52:55],s74 offen offset:1024 sc1\nM v[80:83],a[24:27],v[200:203],v[80:83]\nv_add_u32 v250,s70,v240\n" \
"s_lshl_b32 s70,s63,19\nM v[84:87],a[28:31],v[200:203],v[84:87]\ns_lshl_b32 s71,s60,6\ns_waitcnt lgkmcnt(6)\nM v[24:27],a[16:19],v[204:207],v[24:27]\ns_add_u32 s44,s34,s70\ns_addc_u32 s45,s35,0\nM v[28:31],a[20:23],v[204:207],v[28:31]\n" \
"buffer_load_dwordx4 v244,s[40:43],s69 offen offset:1024 lds\nM v[88:91],a[24:27],v[204:207],v[88:91]\ns_waitcnt vmcnt(6) lgkmcnt(0)\ns_barrier\nM v[92:95],a[28:31],v[204:207],v[92:95]\nds_read_b64_tr_b16 v[192:193],v241 offset:16384\nds_read_b64_tr_b16 v[194:195],v241 offset:16640\nM v[32:35],a[16:19],v[208:211],v[32:35]\n" \
"ds_read_b64_tr_b16 v[196:197],v242 offset:16384\nds_read_b64_tr_b16 v[198:199],v242 offset:16640\nM v[36:39],a[20:23],v[208:211],v[36:39]\nds_read_b64_tr_b16 v[200:201],v241 offset:16896\nds_read_b64_tr_b16 v[202:203],v241 offset:17152\nM v[96:99],a[24:27],v[208:211],v[96:99]\nds_read_b64_tr_b16 v[204:205],v242 offset:16896\nds_read_b64_tr_b16 v[206:207],v242 offset:17152\n" \
"M v[100:103],a[28:31],v[208:211],v[100:103]\nds_read_b64_tr_b16 v[208:209],v241 offset:17408\nds_read_b64_tr_b16 v[210:211],v241 offset:17664\nM v[40:43],a[16:19],v[212:215],v[40:43]\nbuffer_load_dwordx4 v245,s[40:43],s69 offen offset:2048 lds\nM v[44:47],a[20:23],v[212:215],v[44:47]\ns_lshl_b32 s70,s62,15\ns_sub_u32 s46,491520,s70\n" \
"M v[104:107],a[24:27],v[212:215],v[104:107]\ns_add_u32 s96,s70,s64\ns_lshl2_add_u32 s70,s62,0\nM v[108:111],a[28:31],v[212:215],v[108:111]\nds_read_b64_tr_b16 v[212:213],v242 offset:17408\nds_read_b64_tr_b16 v[214:215],v242 offset:17664\nM v[48:51],a[16:19],v[216:219],v[48:51]\nbuffer_store_dwordx4 v[136:139],v240,s[52:55],s74 offen offset:2048 sc1\n" \
"M v[52:55],a[20:23],v[216:219],v[52:55]\nds_write_b64 v250,v[128:129] offset:0\ns_sub_u32 s70,s70,s7\nM v[112:115],a[24:27],v[216:219],v[112:115]\ns_lshl_b32 s70,s70,16\ns_add_u32 s57,s78,s70\nM v[116:119],a[28:31],v[216:219],v[116:119]\nds_read_b64_tr_b16 v[216:217],v241 offset:17920\n" \
"ds_read_b64_tr_b16 v[218:219],v241 offset:18176\nM v[56:59],a[16:19],v[220:223],v[56:59]\nbuffer_load_dwordx4 v246,s[40:43],s69 offen offset:3072 lds\nM v[60:63],a[20:23],v[220:223],v[60:63]\ns_add_u32 m0,s64,147456\ns_add_u32 s69,s68,1572864\nM v[120:123],a[24:27],v[220:223],v[120:123]\ns_lshl3_add_u32 s70,s62,s7\n" \
"s_lshl3_add_u32 s70,s70,s60\nM v[124:127],a[28:31],v[220:223],v[124:127]\nds_read_b64_tr_b16 v[220:221],v242 offset:17920\ns_waitcnt lgkmcnt(6)\nM v[0:3],a[32:35],v[192:195],v[0:3]\nds_read_b64_tr_b16 v[222:223],v242 offset:18176\ns_lshl_b32 s67,s70,15\nM v[4:7],a[36:39],v[192:195],v[4:7]\n" \
"buffer_load_dwordx4 v243,s[40:43],s69 offen offset:0 lds\nM v[64:67],a[40:43],v[192:195],v[64:67]\ns_lshl3_add_u32 s70,s62,s60\ns_lshl2_add_u32 s70,s70,s7\nM v[68:71],a[44:47],v[192:195],v[68:71]\nds_read_b64_tr_b16 v[192:193],v241 offset:24576\nds_read_b64_tr_b16 v[194:195],v241 offset:24832\nM v[8:11],a[32:35],v[196:199],v[8:11]\n" \
"ds_write_b64 v250,v[130:131] offset:8\nv_lshlrev_b32 v229,7,v249\nM v[12:15],a[36:39],v[196:199],v[12:15]\nbuffer_load_dwordx4 v244,s[40:43],s69 offen offset:1024 lds\nM v[72:75],a[40:43],v[196:199],v[72:75]\ns_add_u32 s72,s78,16384\ns_sub_u32 s96,s96,16384\nM v[76:79],a[44:47],v[196:199],v[76:79]\n" \
"ds_read_b64_tr_b16 v[196:197],v242 offset:24576\nds_read_b64_tr_b16 v[198:199],v242 offset:24832\nM v[16:19],a[32:35],v[200:203],v[16:19]\ns_cmp_eq_u32 s36,s96\ns_cselect_b32 s72,0xc000,s98\ns_add_u32 s65,s36,s72\nM v[20:23],a[36:39],v[200:203],v[20:23]\nbuffer_load_dwordx4 v245,s[40:43],s69 offen offset:2048 lds\n" \
"M v[80:83],a[40:43],v[200:203],v[80:83]\ns_cmp_eq_u32 s65,s96\ns_cselect_b32 s72,0xc000,s98\ns_add_u32 s66,s65,s72\nM v[84:87],a[44:47],v[200:203],v[84:87]\nds_read_b64_tr_b16 v[200:201],v241 offset:25088\nds_read_b64_tr_b16 v[202:203],v241 offset:25344\nM v[24:27],a[32:35],v[204:207],v[24:27]\n" \
"v_and_b32 v251,0xf0,v240\nv_lshl_or_b32 v253,v253,12,v251\nM v[28:31],a[36:39],v[204:207],v[28:31]\nbuffer_store_dwordx4 v[140:143],v240,s[52:55],s74 offen offset:3072 sc1\nM v[88:91],a[40:43],v[204:207],v[88:91]\ns_add_u32 s74,s74,4096\ns_add_u32 s15,s64,s46\nM v[92:95],a[44:47],v[204:207],v[92:95]\n" \
"ds_read_b64_tr_b16 v[204:205],v242 offset:25088\nds_read_b64_tr_b16 v[206:207],v242 offset:25344\nM v[32:35],a[32:35],v[208:211],v[32:35]\nds_write_b64 v250,v[132:133] offset:1024\ns_mov_b32 s50,s46\nM v[36:39],a[36:39],v[208:211],v[36:39]\nbuffer_load_dwordx4 v246,s[40:43],s69 offen offset:3072 lds\nM v[96:99],a[40:43],v[208:211],v[96:99]\n" \
"s_add_u32 m0,s64,98304\ns_add_u32 s69,s68,2097152\nM v[100:103],a[44:47],v[208:211],v[100:103]\nds_read_b64_tr_b16 v[208:209],v241 offset:25600\ns_waitcnt lgkmcnt(6)\nM v[40:43],a[32:35],v[212:215],v[40:43]\nds_read_b64_tr_b16 v[210:211],v241 offset:25856\ns_add_u32 s97,s96,98304\n" \
"M v[44:47],a[36:39],v[212:215],v[44:47]\nbuffer_load_dwordx4 v243,s[40:43],s69 offen offset:0 lds\nM v[104:107],a[40:43],v[212:215],v[104:107]\ns_add_u32 s48,s44,19922944\ns_addc_u32 s49,s45,0\nM v[108:111],a[44:47],v[212:215],v[108:111]\nds_read_b64_tr_b16 v[212:213],v242 offset:25600\nds_read_b64_tr_b16 v[214:215],v242 offset:25856\n" \
"M v[48:51],a[32:35],v[216:219],v[48:51]\ns_add_u32 s44,s44,3145728\ns_addc_u32 s45,s45,0\nM v[52:55],a[36:39],v[216:219],v[52:55]\nbuffer_load_dwordx4 v244,s[40:43],s69 offen offset:1024 lds\nM v[112:115],a[40:43],v[216:219],v[112:115]\ns_cmp_eq_u32 s66,s96\ns_cselect_b32 s72,0xc000,s98\n" \
"s_add_u32 s95,s66,s72\nM v[116:119],a[44:47],v[216:219],v[116:119]\nds_read_b64_tr_b16 v[216:217],v241 offset:26112\nds_read_b64_tr_b16 v[218:219],v241 offset:26368\nM v[56:59],a[32:35],v[220:223],v[56:59]\nds_write_b64 v250,v[134:135] offset:1032\ns_add_u32 s15,s15,32768\nM v[60:63],a[36:39],v[220:223],v[60:63]\n" \
"buffer_store_dwordx4 v[144:147],v240,s[52:55],s74 offen offset:0 sc1\nM v[120:123],a[40:43],v[220:223],v[120:123]\ns_sub_u32 s57,s57,1867776\ns_add_u32 s67,s67,36700160\nM v[124:127],a[44:47],v[220:223],v[124:127]\nds_read_b64_tr_b16 v[220:221],v242 offset:26112\nds_read_b64_tr_b16 v[222:223],v242 offset:26368\nM v[0:3],a[48:51],v[192:195],v[0:3]\n" \
"M v[4:7],a[52:55],v[192:195],v[4:7]\nbuffer_load_dwordx4 v245,s[40:43],s69 offen offset:2048 lds\nM v[64:67],a[56:59],v[192:195],v[64:67]\nM v[68:71],a[60:63],v[192:195],v[68:71]\nM v[8:11],a[48:51],v[196:199],v[8:11]\nM v[12:15],a[52:55],v[196:199],v[12:15]\nbuffer_load_dwordx4 v246,s[40:43],s69 offen offset:3072 lds\nM v[72:75],a[56:59],v[196:199],v[72:75]\n" \
"M v[76:79],a[60:63],v[196:199],v[76:79]\ns_waitcnt lgkmcnt(5)\nM v[16:19],a[48:51],v[200:203],v[16:19]\nds_write_b64 v250,v[136:137] offset:2048\nM v[20:23],a[52:55],v[200:203],v[20:23]\nM v[80:83],a[56:59],v[200:203],v[80:83]\nM v[84:87],a[60:63],v[200:203],v[84:87]\nM v[24:27],a[48:51],v[204:207],v[24:27]\n" \
"s_add_u32 m0,s64,114688\ns_add_u32 s69,s68,2621440\nM v[28:31],a[52:55],v[204:207],v[28:31]\nM v[88:91],a[56:59],v[204:207],v[88:91]\ns_waitcnt vmcnt(10) lgkmcnt(1)\ns_barrier\nM v[92:95],a[60:63],v[204:207],v[92:95]\nbuffer_store_dwordx4 v[148:151],v240,s[52:55],s74 offen offset:1024 sc1\n" \
"M v[32:35],a[48:51],v[208:211],v[32:35]\nds_read_b64_tr_b16 v[192:193],v241 offset:32768\nds_read_b64_tr_b16 v[194:195],v241 offset:33024\nM v[36:39],a[52:55],v[208:211],v[36:39]\nds_read_b64_tr_b16 v[196:197],v242 offset:32768\nds_read_b64_tr_b16 v[198:199],v242 offset:33024\nM v[96:99],a[56:59],v[208:211],v[96:99]\nds_read_b64_tr_b16 v[200:201],v241 offset:33280\n" \
"ds_read_b64_tr_b16 v[202:203],v241 offset:33536\nM v[100:103],a[60:63],v[208:211],v[100:103]\nbuffer_store_dword v254,off,s[52:55],s79 sc1\nM v[40:43],a[48:51],v[212:215],v[40:43]\nds_read_b64_tr_b16 v[204:205],v242 offset:33280\nds_read_b64_tr_b16 v[206:207],v242 offset:33536\nM v[44:47],a[52:55],v[212:215],v[44:47]\nds_read_b64_tr_b16 v[208:209],v241 offset:33792\n" \
"ds_read_b64_tr_b16 v[210:211],v241 offset:34048\nM v[104:107],a[56:59],v[212:215],v[104:107]\nbuffer_load_dwordx4 v243,s[40:43],s69 offen offset:0 lds\nM v[108:111],a[60:63],v[212:215],v[108:111]\nds_read_b64_tr_b16 v[212:213],v242 offset:33792\nds_read_b64_tr_b16 v[214:215],v242 offset:34048\nM v[48:51],a[48:51],v[216:219],v[48:51]\nds_write_b64 v250,v[138:139] offset:2056\n" \
"s_lshl4_add_u32 s79,s70,65536\nM v[52:55],a[52:55],v[216:219],v[52:55]\ns_lshr_b32 s70,s6,3\ns_lshl2_add_u32 s70,s70,s7\nM v[112:115],a[56:59],v[216:219],v[112:115]\nbuffer_load_dwordx4 v244,s[40:43],s69 offen offset:1024 lds\nM v[116:119],a[60:63],v[216:219],v[116:119]\nds_read_b64_tr_b16 v[216:217],v241 offset:34304\n" \
"ds_read_b64_tr_b16 v[218:219],v241 offset:34560\nM v[56:59],a[48:51],v[220:223],v[56:59]\ns_and_b32 s70,s70,63\ns_add_u32 s70,s70,s71\nM v[60:63],a[52:55],v[220:223],v[60:63]\ns_lshl_b32 s70,s70,13\nv_add_u32 v229,s70,v229\nM v[120:123],a[56:59],v[220:223],v[120:123]\n" \
"buffer_load_dwordx4 v245,s[40:43],s69 offen offset:2048 lds\nM v[124:127],a[60:63],v[220:223],v[124:127]\nds_read_b64_tr_b16 v[220:221],v242 offset:34304\ns_waitcnt lgkmcnt(7)\nM v[0:3],a[64:67],v[192:195],v[0:3]\nds_read_b64_tr_b16 v[222:223],v242 offset:34560\ns_lshl2_add_u32 s70,s6,s7\nM v[4:7],a[68:71],v[192:195],v[4:7]\n" \
"ds_write_b64 v250,v[140:141] offset:3072\nM v[64:67],a[72:75],v[192:195],v[64:67]\nbuffer_store_dwordx4 v[152:155],v240,s[52:55],s74 offen offset:2048 sc1\nM v[68:71],a[76:79],v[192:195],v[68:71]\nds_read_b64_tr_b16 v[192:193],v241 offset:40960\nds_read_b64_tr_b16 v[194:195],v241 offset:41216\nM v[8:11],a[64:67],v[196:199],v[8:11]\ns_cmp_ge_u32 s70,672\n" \
"s_cselect_b32 s71,672,0\nM v[12:15],a[68:71],v[196:199],v[12:15]\ns_sub_u32 s70,s70,s71\nM v[72:75],a[72:75],v[196:199],v[72:75]\nbuffer_load_dwordx4 v246,s[40:43],s69 offen offset:3072 lds\nM v[76:79],a[76:79],v[196:199],v[76:79]\nds_read_b64_tr_b16 v[196:197],v242 offset:40960\nds_read_b64_tr_b16 v[198:199],v242 offset:41216\n" \
"M v[16:19],a[64:67],v[200:203],v[16:19]\ns_cmp_ge_u32 s70,224\ns_cselect_b64 s[92:93],s[24:25],s[22:23]\ns_cselect_b32 s71,224,0\nM v[20:23],a[68:71],v[200:203],v[20:23]\ns_cmp_ge_u32 s70,448\ns_cselect_b64 s[92:93],s[26:27],s[92:93]\ns_cselect_b32 s71,448,s71\n" \
"M v[80:83],a[72:75],v[200:203],v[80:83]\ns_sub_u32 s70,s70,s71\ns_lshl_b32 s70,s70,13\nM v[84:87],a[76:79],v[200:203],v[84:87]\nds_read_b64_tr_b16 v[200:201],v241 offset:41472\nds_read_b64_tr_b16 v[202:203],v241 offset:41728\nM v[24:27],a[64:67],v[204:207],v[24:27]\nds_write_b64 v250,v[142:143] offset:3080\n" \
"M v[28:31],a[68:71],v[204:207],v[28:31]\nM v[88:91],a[72:75],v[204:207],v[88:91]\nM v[92:95],a[76:79],v[204:207],v[92:95]\nds_read_b64_tr_b16 v[204:205],v242 offset:41472\ns_waitcnt lgkmcnt(6)\nM v[32:35],a[64:67],v[208:211],v[32:35]\nds_read_b64_tr_b16 v[206:207],v242 offset:41728\nM v[36:39],a[68:71],v[208:211],v[36:39]\n" \
"M v[96:99],a[72:75],v[208:211],v[96:99]\nbuffer_store_dwordx4 v[156:159],v240,s[52:55],s74 offen offset:3072 sc1\nM v[100:103],a[76:79],v[208:211],v[100:103]\nds_read_b64_tr_b16 v[208:209],v241 offset:41984\nds_read_b64_tr_b16 v[210:211],v241 offset:42240\nM v[40:43],a[64:67],v[212:215],v[40:43]\ns_add_u32 s74,s74,4096\nM v[44:47],a[68:71],v[212:215],v[44:47]\n" \
"M v[104:107],a[72:75],v[212:215],v[104:107]\nM v[108:111],a[76:79],v[212:215],v[108:111]\nds_read_b64_tr_b16 v[212:213],v242 offset:41984\nds_read_b64_tr_b16 v[214:215],v242 offset:42240\nM v[48:51],a[64:67],v[216:219],v[48:51]\nds_write_b64 v250,v[144:145] offset:4096\nM v[52:55],a[68:71],v[216:219],v[52:55]\nM v[112:115],a[72:75],v[216:219],v[112:115]\n" \
"M v[116:119],a[76:79],v[216:219],v[116:119]\nds_read_b64_tr_b16 v[216:217],v241 offset:42496\nds_read_b64_tr_b16 v[218:219],v241 offset:42752\nM v[56:59],a[64:67],v[220:223],v[56:59]\nM v[60:63],a[68:71],v[220:223],v[60:63]\nM v[120:123],a[72:75],v[220:223],v[120:123]\nM v[124:127],a[76:79],v[220:223],v[124:127]\nds_read_b64_tr_b16 v[220:221],v242 offset:42496\n" \
"ds_read_b64_tr_b16 v[222:223],v242 offset:42752\nM v[0:3],a[80:83],v[192:195],v[0:3]\nM v[4:7],a[84:87],v[192:195],v[4:7]\nM v[64:67],a[88:91],v[192:195],v[64:67]\nM v[68:71],a[92:95],v[192:195],v[68:71]\ns_waitcnt lgkmcnt(7)\nM v[8:11],a[80:83],v[196:199],v[8:11]\nbuffer_store_dwordx4 v[160:163],v240,s[52:55],s74 offen offset:0 sc1\n" \
"M v[12:15],a[84:87],v[196:199],v[12:15]\nds_write_b64 v250,v[146:147] offset:4104\nM v[72:75],a[88:91],v[196:199],v[72:75]\nM v[76:79],a[92:95],v[196:199],v[76:79]\nM v[16:19],a[80:83],v[200:203],v[16:19]\nM v[20:23],a[84:87],v[200:203],v[20:23]\nM v[80:83],a[88:91],v[200:203],v[80:83]\nM v[84:87],a[92:95],v[200:203],v[84:87]\n" \
"M v[24:27],a[80:83],v[204:207],v[24:27]\ns_add_u32 m0,s64,131072\ns_add_u32 s69,s68,3145728\nM v[28:31],a[84:87],v[204:207],v[28:31]\nM v[88:91],a[88:91],v[204:207],v[88:91]\ns_waitcnt vmcnt(14) lgkmcnt(1)\ns_barrier\nM v[92:95],a[92:95],v[204:207],v[92:95]\n" \
"ds_read_b64_tr_b16 v[192:193],v241 offset:49152\nds_read_b64_tr_b16 v[194:195],v241 offset:49408\nM v[32:35],a[80:83],v[208:211],v[32:35]\nds_read_b64_tr_b16 v[196:197],v242 offset:49152\nds_read_b64_tr_b16 v[198:199],v242 offset:49408\nM v[36:39],a[84:87],v[208:211],v[36:39]\nds_read_b64_tr_b16 v[200:201],v241 offset:49664\nds_read_b64_tr_b16 v[202:203],v241 offset:49920\n" \
"M v[96:99],a[88:91],v[208:211],v[96:99]\nds_read_b64_tr_b16 v[204:205],v242 offset:49664\nds_read_b64_tr_b16 v[206:207],v242 offset:49920\nM v[100:103],a[92:95],v[208:211],v[100:103]\nds_read_b64_tr_b16 v[208:209],v241 offset:50176\nds_read_b64_tr_b16 v[210:211],v241 offset:50432\nM v[40:43],a[80:83],v[212:215],v[40:43]\nbuffer_load_dwordx4 v243,s[40:43],s69 offen offset:0 lds\n" \
"M v[44:47],a[84:87],v[212:215],v[44:47]\nds_write_b64 v250,v[148:149] offset:5120\nM v[104:107],a[88:91],v[212:215],v[104:107]\nM v[108:111],a[92:95],v[212:215],v[108:111]\nds_read_b64_tr_b16 v[212:213],v242 offset:50176\nds_read_b64_tr_b16 v[214:215],v242 offset:50432\nM v[48:51],a[80:83],v[216:219],v[48:51]\nbuffer_store_dwordx4 v[164:167],v240,s[52:55],s74 offen offset:1024 sc1\n" \
"M v[52:55],a[84:87],v[216:219],v[52:55]\nM v[112:115],a[88:91],v[216:219],v[112:115]\nM v[116:119],a[92:95],v[216:219],v[116:119]\nds_read_b64_tr_b16 v[216:217],v241 offset:50688\nds_read_b64_tr_b16 v[218:219],v241 offset:50944\nM v[56:59],a[80:83],v[220:223],v[56:59]\nbuffer_load_dwordx4 v244,s[40:43],s69 offen offset:1024 lds\nM v[60:63],a[84:87],v[220:223],v[60:63]\n" \
"ds_write_b64 v250,v[150:151] offset:5128\nM v[120:123],a[88:91],v[220:223],v[120:123]\nM v[124:127],a[92:95],v[220:223],v[124:127]\nds_read_b64_tr_b16 v[220:221],v242 offset:50688\ns_waitcnt lgkmcnt(9)\nM v[0:3],a[96:99],v[192:195],v[0:3]\nbuffer_load_dwordx4 v245,s[40:43],s69 offen offset:2048 lds\nM v[4:7],a[100:103],v[192:195],v[4:7]\n" \
"ds_read_b64_tr_b16 v[222:223],v242 offset:50944\nM v[64:67],a[104:107],v[192:195],v[64:67]\nM v[68:71],a[108:111],v[192:195],v[68:71]\nds_read_b64_tr_b16 v[192:193],v241 offset:57344\nds_read_b64_tr_b16 v[194:195],v241 offset:57600\nM v[8:11],a[96:99],v[196:199],v[8:11]\nbuffer_load_dwordx4 v246,s[40:43],s69 offen offset:3072 lds\nM v[12:15],a[100:103],v[196:199],v[12:15]\n" \
"M v[72:75],a[104:107],v[196:199],v[72:75]\nM v[76:79],a[108:111],v[196:199],v[76:79]\nds_read_b64_tr_b16 v[196:197],v242 offset:57344\nds_read_b64_tr_b16 v[198:199],v242 offset:57600\nM v[16:19],a[96:99],v[200:203],v[16:19]\nbuffer_store_dwordx4 v[168:171],v240,s[52:55],s74 offen offset:2048 sc1\nM v[20:23],a[100:103],v[200:203],v[20:23]\nds_write_b64 v250,v[152:153] offset:6144\n" \
"M v[80:83],a[104:107],v[200:203],v[80:83]\nM v[84:87],a[108:111],v[200:203],v[84:87]\nds_read_b64_tr_b16 v[200:201],v241 offset:57856\nds_read_b64_tr_b16 v[202:203],v241 offset:58112\nM v[24:27],a[96:99],v[204:207],v[24:27]\nM v[28:31],a[100:103],v[204:207],v[28:31]\nM v[88:91],a[104:107],v[204:207],v[88:91]\nM v[92:95],a[108:111],v[204:207],v[92:95]\n" \
"ds_read_b64_tr_b16 v[204:205],v242 offset:57856\ns_waitcnt lgkmcnt(8)\nM v[32:35],a[96:99],v[208:211],v[32:35]\nds_read_b64_tr_b16 v[206:207],v242 offset:58112\nM v[36:39],a[100:103],v[208:211],v[36:39]\nM v[96:99],a[104:107],v[208:211],v[96:99]\nM v[100:103],a[108:111],v[208:211],v[100:103]\nds_read_b64_tr_b16 v[208:209],v241 offset:58368\n" \
"ds_read_b64_tr_b16 v[210:211],v241 offset:58624\nM v[40:43],a[96:99],v[212:215],v[40:43]\nds_write_b64 v250,v[154:155] offset:6152\nM v[44:47],a[100:103],v[212:215],v[44:47]\nM v[104:107],a[104:107],v[212:215],v[104:107]\nM v[108:111],a[108:111],v[212:215],v[108:111]\nds_read_b64_tr_b16 v[212:213],v242 offset:58368\nds_read_b64_tr_b16 v[214:215],v242 offset:58624\n" \
"M v[48:51],a[96:99],v[216:219],v[48:51]\nM v[52:55],a[100:103],v[216:219],v[52:55]\nM v[112:115],a[104:107],v[216:219],v[112:115]\nbuffer_store_dwordx4 v[172:175],v240,s[52:55],s74 offen offset:3072 sc1\nM v[116:119],a[108:111],v[216:219],v[116:119]\nds_read_b64_tr_b16 v[216:217],v241 offset:58880\nds_read_b64_tr_b16 v[218:219],v241 offset:59136\nM v[56:59],a[96:99],v[220:223],v[56:59]\n" \
"s_add_u32 s74,s74,4096\nM v[60:63],a[100:103],v[220:223],v[60:63]\nM v[120:123],a[104:107],v[220:223],v[120:123]\nM v[124:127],a[108:111],v[220:223],v[124:127]\nds_read_b64_tr_b16 v[220:221],v242 offset:58880\ns_waitcnt lgkmcnt(8)\nM v[0:3],a[112:115],v[192:195],v[0:3]\nds_write_b64 v250,v[156:157] offset:7168\n" \
"ds_read_b64_tr_b16 v[222:223],v242 offset:59136\nM v[4:7],a[116:119],v[192:195],v[4:7]\nM v[64:67],a[120:123],v[192:195],v[64:67]\nM v[68:71],a[124:127],v[192:195],v[68:71]\nM v[8:11],a[112:115],v[196:199],v[8:11]\nM v[12:15],a[116:119],v[196:199],v[12:15]\nM v[72:75],a[120:123],v[196:199],v[72:75]\nM v[76:79],a[124:127],v[196:199],v[76:79]\n" \
"M v[16:19],a[112:115],v[200:203],v[16:19]\nM v[20:23],a[116:119],v[200:203],v[20:23]\nM v[80:83],a[120:123],v[200:203],v[80:83]\nM v[84:87],a[124:127],v[200:203],v[84:87]\nM v[24:27],a[112:115],v[204:207],v[24:27]\nbuffer_store_dwordx4 v[176:179],v240,s[52:55],s74 offen offset:0 sc1\nM v[28:31],a[116:119],v[204:207],v[28:31]\ns_add_u32 m0,s64,147456\n" \
"s_add_u32 s69,s68,3670016\nM v[88:91],a[120:123],v[204:207],v[88:91]\ns_waitcnt vmcnt(17) lgkmcnt(0)\ns_barrier\nM v[92:95],a[124:127],v[204:207],v[92:95]\nds_read_b64_tr_b16 v[192:193],v241 offset:0\nds_read_b64_tr_b16 v[194:195],v241 offset:256\nM v[32:35],a[112:115],v[208:211],v[32:35]\n" \
"ds_read_b64_tr_b16 v[196:197],v242 offset:0\nds_read_b64_tr_b16 v[198:199],v242 offset:256\nM v[36:39],a[116:119],v[208:211],v[36:39]\nds_read_b64_tr_b16 v[200:201],v241 offset:512\nds_read_b64_tr_b16 v[202:203],v241 offset:768\nM v[96:99],a[120:123],v[208:211],v[96:99]\nds_read_b64_tr_b16 v[204:205],v242 offset:512\nds_read_b64_tr_b16 v[206:207],v242 offset:768\n" \
"M v[100:103],a[124:127],v[208:211],v[100:103]\nds_read_b64_tr_b16 v[208:209],v241 offset:1024\nds_read_b64_tr_b16 v[210:211],v241 offset:1280\nM v[40:43],a[112:115],v[212:215],v[40:43]\nbuffer_load_dwordx4 v243,s[40:43],s69 offen offset:0 lds\nM v[44:47],a[116:119],v[212:215],v[44:47]\nds_write_b64 v250,v[158:159] offset:7176\nM v[104:107],a[120:123],v[212:215],v[104:107]\n" \
"M v[108:111],a[124:127],v[212:215],v[108:111]\nds_read_b64_tr_b16 v[212:213],v242 offset:1024\nds_read_b64_tr_b16 v[214:215],v242 offset:1280\nM v[48:51],a[112:115],v[216:219],v[48:51]\nbuffer_load_dwordx4 v244,s[40:43],s69 offen offset:1024 lds\nM v[52:55],a[116:119],v[216:219],v[52:55]\nds_write_b64 v250,v[160:161] offset:8192\nM v[112:115],a[120:123],v[216:219],v[112:115]\n" \
"M v[116:119],a[124:127],v[216:219],v[116:119]\nds_read_b64_tr_b16 v[216:217],v241 offset:1536\nds_read_b64_tr_b16 v[218:219],v241 offset:1792\nM v[56:59],a[112:115],v[220:223],v[56:59]\nbuffer_load_dwordx4 v245,s[40:43],s69 offen offset:2048 lds\nM v[60:63],a[116:119],v[220:223],v[60:63]\nM v[120:123],a[120:123],v[220:223],v[120:123]\nM v[124:127],a[124:127],v[220:223],v[124:127]\n" \
"ds_read_b64_tr_b16 v[220:221],v242 offset:1536\ns_waitcnt lgkmcnt(9)\nM v[0:3],a[128:131],v[192:195],v[0:3]\nbuffer_store_dwordx4 v[180:183],v240,s[52:55],s74 offen offset:1024 sc1\nM v[4:7],a[132:135],v[192:195],v[4:7]\nds_read_b64_tr_b16 v[222:223],v242 offset:1792\nM v[64:67],a[136:139],v[192:195],v[64:67]\nM v[68:71],a[140:143],v[192:195],v[68:71]\n" \
"ds_read_b64_tr_b16 v[192:193],v241 offset:8192\nds_read_b64_tr_b16 v[194:195],v241 offset:8448\nM v[8:11],a[128:131],v[196:199],v[8:11]\nbuffer_load_dwordx4 v246,s[40:43],s69 offen offset:3072 lds\nM v[12:15],a[132:135],v[196:199],v[12:15]\nds_write_b64 v250,v[162:163] offset:8200\nM v[72:75],a[136:139],v[196:199],v[72:75]\nM v[76:79],a[140:143],v[196:199],v[76:79]\n" \
"ds_read_b64_tr_b16 v[196:197],v242 offset:8192\nds_read_b64_tr_b16 v[198:199],v242 offset:8448\nM v[16:19],a[128:131],v[200:203],v[16:19]\nM v[20:23],a[132:135],v[200:203],v[20:23]\nM v[80:83],a[136:139],v[200:203],v[80:83]\nM v[84:87],a[140:143],v[200:203],v[84:87]\nds_read_b64_tr_b16 v[200:201],v241 offset:8704\nds_read_b64_tr_b16 v[202:203],v241 offset:8960\n" \
"M v[24:27],a[128:131],v[204:207],v[24:27]\nM v[28:31],a[132:135],v[204:207],v[28:31]\nM v[88:91],a[136:139],v[204:207],v[88:91]\nM v[92:95],a[140:143],v[204:207],v[92:95]\nds_read_b64_tr_b16 v[204:205],v242 offset:8704\ns_waitcnt lgkmcnt(9)\nM v[32:35],a[128:131],v[208:211],v[32:35]\nbuffer_store_dwordx4 v[184:187],v240,s[52:55],s74 offen offset:2048 sc1\n" \
"M v[36:39],a[132:135],v[208:211],v[36:39]\nds_write_b64 v250,v[164:165] offset:9216\nds_read_b64_tr_b16 v[206:207],v242 offset:8960\nM v[96:99],a[136:139],v[208:211],v[96:99]\nM v[100:103],a[140:143],v[208:211],v[100:103]\nds_read_b64_tr_b16 v[208:209],v241 offset:9216\nds_read_b64_tr_b16 v[210:211],v241 offset:9472\nM v[40:43],a[128:131],v[212:215],v[40:43]\n" \
"M v[44:47],a[132:135],v[212:215],v[44:47]\nM v[104:107],a[136:139],v[212:215],v[104:107]\nbuffer_load_dword v248,v228,s[52:55],s80 offen sc1\nM v[108:111],a[140:143],v[212:215],v[108:111]\nds_read_b64_tr_b16 v[212:213],v242 offset:9216\nds_read_b64_tr_b16 v[214:215],v242 offset:9472\nM v[48:51],a[128:131],v[216:219],v[48:51]\nM v[52:55],a[132:135],v[216:219],v[52:55]\n" \
"M v[112:115],a[136:139],v[216:219],v[112:115]\nM v[116:119],a[140:143],v[216:219],v[116:119]\nds_read_b64_tr_b16 v[216:217],v241 offset:9728\ns_waitcnt lgkmcnt(8)\nM v[56:59],a[128:131],v[220:223],v[56:59]\nds_write_b64 v250,v[166:167] offset:9224\nds_read_b64_tr_b16 v[218:219],v241 offset:9984\nM v[60:63],a[132:135],v[220:223],v[60:63]\n" \
"M v[120:123],a[136:139],v[220:223],v[120:123]\nM v[124:127],a[140:143],v[220:223],v[124:127]\nds_read_b64_tr_b16 v[220:221],v242 offset:9728\nds_read_b64_tr_b16 v[222:223],v242 offset:9984\nM v[0:3],a[144:147],v[192:195],v[0:3]\nM v[4:7],a[148:151],v[192:195],v[4:7]\nM v[64:67],a[152:155],v[192:195],v[64:67]\nbuffer_store_dwordx4 v[188:191],v240,s[52:55],s74 offen offset:3072 sc1\n" \
"M v[68:71],a[156:159],v[192:195],v[68:71]\ns_add_u32 s74,s74,16764928\nM v[8:11],a[144:147],v[196:199],v[8:11]\nM v[12:15],a[148:151],v[196:199],v[12:15]\nM v[72:75],a[152:155],v[196:199],v[72:75]\nM v[76:79],a[156:159],v[196:199],v[76:79]\nM v[16:19],a[144:147],v[200:203],v[16:19]\nds_write_b64 v250,v[168:169] offset:10240\n" \
"M v[20:23],a[148:151],v[200:203],v[20:23]\nM v[80:83],a[152:155],v[200:203],v[80:83]\nM v[84:87],a[156:159],v[200:203],v[84:87]\ns_waitcnt lgkmcnt(5)\nM v[24:27],a[144:147],v[204:207],v[24:27]\ns_sub_u32 s40,s12,0x1000\ns_subb_u32 s41,s13,0\nM v[28:31],a[148:151],v[204:207],v[28:31]\n" \
"s_add_u32 m0,s64,98304\ns_add_u32 s69,s68,0\nM v[88:91],a[152:155],v[204:207],v[88:91]\ns_waitcnt vmcnt(18) lgkmcnt(1)\ns_barrier\nM v[92:95],a[156:159],v[204:207],v[92:95]\nds_read_b64_tr_b16 v[192:193],v241 offset:16384\nds_read_b64_tr_b16 v[194:195],v241 offset:16640\n" \
"M v[32:35],a[144:147],v[208:211],v[32:35]\nds_read_b64_tr_b16 v[196:197],v242 offset:16384\nds_read_b64_tr_b16 v[198:199],v242 offset:16640\nM v[36:39],a[148:151],v[208:211],v[36:39]\nds_read_b64_tr_b16 v[200:201],v241 offset:16896\nds_read_b64_tr_b16 v[202:203],v241 offset:17152\nM v[96:99],a[152:155],v[208:211],v[96:99]\nds_read_b64_tr_b16 v[204:205],v242 offset:16896\n" \
"ds_read_b64_tr_b16 v[206:207],v242 offset:17152\nM v[100:103],a[156:159],v[208:211],v[100:103]\nds_read_b64_tr_b16 v[208:209],v241 offset:17408\nds_read_b64_tr_b16 v[210:211],v241 offset:17664\nM v[40:43],a[144:147],v[212:215],v[40:43]\nbuffer_load_dwordx4 v243,s[40:43],s69 offen offset:0 lds\nM v[44:47],a[148:151],v[212:215],v[44:47]\nds_write_b64 v250,v[170:171] offset:10248\n" \
"s_lshl_b32 s12,s61,13\nM v[104:107],a[152:155],v[212:215],v[104:107]\ns_cmp_eq_u32 s62,7\ns_cselect_b32 s13,s12,s12\nM v[108:111],a[156:159],v[212:215],v[108:111]\nds_read_b64_tr_b16 v[212:213],v242 offset:17408\nds_read_b64_tr_b16 v[214:215],v242 offset:17664\nM v[48:51],a[144:147],v[216:219],v[48:51]\n" \
"buffer_load_dwordx4 v244,s[40:43],s69 offen offset:1024 lds\nM v[52:55],a[148:151],v[216:219],v[52:55]\nM v[112:115],a[152:155],v[216:219],v[112:115]\nM v[116:119],a[156:159],v[216:219],v[116:119]\nds_read_b64_tr_b16 v[216:217],v241 offset:17920\nds_read_b64_tr_b16 v[218:219],v241 offset:18176\nM v[56:59],a[144:147],v[220:223],v[56:59]\nbuffer_load_dwordx4 v245,s[40:43],s69 offen offset:2048 lds\n" \
"M v[60:63],a[148:151],v[220:223],v[60:63]\nM v[120:123],a[152:155],v[220:223],v[120:123]\nM v[124:127],a[156:159],v[220:223],v[124:127]\nds_read_b64_tr_b16 v[220:221],v242 offset:17920\ns_waitcnt lgkmcnt(7)\nM v[0:3],a[160:163],v[192:195],v[0:3]\nbuffer_load_dwordx4 v246,s[40:43],s69 offen offset:3072 lds\nM v[4:7],a[164:167],v[192:195],v[4:7]\n" \
"ds_write_b64 v250,v[172:173] offset:11264\nds_read_b64_tr_b16 v[222:223],v242 offset:18176\nM v[64:67],a[168:171],v[192:195],v[64:67]\nM v[68:71],a[172:175],v[192:195],v[68:71]\nds_read_b64_tr_b16 v[192:193],v241 offset:24576\nds_read_b64_tr_b16 v[194:195],v241 offset:24832\nM v[8:11],a[160:163],v[196:199],v[8:11]\nM v[12:15],a[164:167],v[196:199],v[12:15]\n" \
"M v[72:75],a[168:171],v[196:199],v[72:75]\nM v[76:79],a[172:175],v[196:199],v[76:79]\nds_read_b64_tr_b16 v[196:197],v242 offset:24576\nds_read_b64_tr_b16 v[198:199],v242 offset:24832\nM v[16:19],a[160:163],v[200:203],v[16:19]\nM v[20:23],a[164:167],v[200:203],v[20:23]\nM v[80:83],a[168:171],v[200:203],v[80:83]\nM v[84:87],a[172:175],v[200:203],v[84:87]\n" \
"ds_read_b64_tr_b16 v[200:201],v241 offset:25088\nds_read_b64_tr_b16 v[202:203],v241 offset:25344\nM v[24:27],a[160:163],v[204:207],v[24:27]\nds_write_b64 v250,v[174:175] offset:11272\nM v[28:31],a[164:167],v[204:207],v[28:31]\ns_waitcnt vmcnt(5)\nv_cmp_eq_u32 vcc,0,v248\nM v[88:91],a[168:171],v[204:207],v[88:91]\n" \
"s_cbranch_vccnz .Lps0\n.Lpb0:\ns_lshl_b32 s80,s63,7\nM v[92:95],a[172:175],v[204:207],v[92:95]\nds_read_b64_tr_b16 v[204:205],v242 offset:25088\ns_waitcnt lgkmcnt(8)\nM v[32:35],a[160:163],v[208:211],v[32:35]\nds_read_b64_tr_b16 v[206:207],v242 offset:25344\n" \
"s_add_u32 s80,s80,81920\nM v[36:39],a[164:167],v[208:211],v[36:39]\nbuffer_load_dwordx4 a[0:3],v240,s[52:55],s76 offen offset:0 sc1\nM v[96:99],a[168:171],v[208:211],v[96:99]\nv_and_b32 v228,31,v249\nv_lshlrev_b32 v228,2,v228\nM v[100:103],a[172:175],v[208:211],v[100:103]\nds_read_b64_tr_b16 v[208:209],v241 offset:25600\n" \
"ds_read_b64_tr_b16 v[210:211],v241 offset:25856\nM v[40:43],a[160:163],v[212:215],v[40:43]\nv_mov_b32 v251,v228\nM v[44:47],a[164:167],v[212:215],v[44:47]\nbuffer_load_dwordx4 a[4:7],v240,s[52:55],s76 offen offset:1024 sc1\nM v[104:107],a[168:171],v[212:215],v[104:107]\nM v[108:111],a[172:175],v[212:215],v[108:111]\nds_read_b64_tr_b16 v[212:213],v242 offset:25600\n" \
"ds_read_b64_tr_b16 v[214:215],v242 offset:25856\nM v[48:51],a[160:163],v[216:219],v[48:51]\nds_write_b64 v250,v[176:177] offset:12288\nM v[52:55],a[164:167],v[216:219],v[52:55]\nbuffer_load_dwordx4 a[8:11],v240,s[52:55],s77 offen offset:0 sc1\nM v[112:115],a[168:171],v[216:219],v[112:115]\nM v[116:119],a[172:175],v[216:219],v[116:119]\nds_read_b64_tr_b16 v[216:217],v241 offset:26112\n" \
"ds_read_b64_tr_b16 v[218:219],v241 offset:26368\nM v[56:59],a[160:163],v[220:223],v[56:59]\nM v[60:63],a[164:167],v[220:223],v[60:63]\nbuffer_load_dwordx4 a[12:15],v240,s[52:55],s77 offen offset:1024 sc1\nM v[120:123],a[168:171],v[220:223],v[120:123]\nM v[124:127],a[172:175],v[220:223],v[124:127]\nds_read_b64_tr_b16 v[220:221],v242 offset:26112\ns_waitcnt lgkmcnt(8)\n" \
"M v[0:3],a[176:179],v[192:195],v[0:3]\nds_read_b64_tr_b16 v[222:223],v242 offset:26368\nM v[4:7],a[180:183],v[192:195],v[4:7]\nM v[64:67],a[184:187],v[192:195],v[64:67]\nbuffer_load_dwordx4 a[16:19],v240,s[52:55],s76 offen offset:2048 sc1\nM v[68:71],a[188:191],v[192:195],v[68:71]\nM v[8:11],a[176:179],v[196:199],v[8:11]\nds_write_b64 v250,v[178:179] offset:12296\n" \
"M v[12:15],a[180:183],v[196:199],v[12:15]\nM v[72:75],a[184:187],v[196:199],v[72:75]\nM v[76:79],a[188:191],v[196:199],v[76:79]\nM v[16:19],a[176:179],v[200:203],v[16:19]\nM v[20:23],a[180:183],v[200:203],v[20:23]\nM v[80:83],a[184:187],v[200:203],v[80:83]\nbuffer_load_dwordx4 a[20:23],v240,s[52:55],s76 offen offset:3072 sc1\nM v[84:87],a[188:191],v[200:203],v[84:87]\n" \
"s_add_u32 s76,s76,4096\nM v[24:27],a[176:179],v[204:207],v[24:27]\ns_add_u32 m0,s64,114688\ns_add_u32 s69,s68,524288\nM v[28:31],a[180:183],v[204:207],v[28:31]\nM v[88:91],a[184:187],v[204:207],v[88:91]\ns_waitcnt lgkmcnt(1)\ns_barrier\n" \
"M v[92:95],a[188:191],v[204:207],v[92:95]\nds_read_b64_tr_b16 v[192:193],v241 offset:32768\nds_read_b64_tr_b16 v[194:195],v241 offset:33024\nM v[32:35],a[176:179],v[208:211],v[32:35]\nds_read_b64_tr_b16 v[196:197],v242 offset:32768\nds_read_b64_tr_b16 v[198:199],v242 offset:33024\nM v[36:39],a[180:183],v[208:211],v[36:39]\nds_read_b64_tr_b16 v[200:201],v241 offset:33280\n" \
"ds_read_b64_tr_b16 v[202:203],v241 offset:33536\nM v[96:99],a[184:187],v[208:211],v[96:99]\nds_read_b64_tr_b16 v[204:205],v242 offset:33280\nds_read_b64_tr_b16 v[206:207],v242 offset:33536\nM v[100:103],a[188:191],v[208:211],v[100:103]\nds_read_b64_tr_b16 v[208:209],v241 offset:33792\nds_read_b64_tr_b16 v[210:211],v241 offset:34048\nM v[40:43],a[176:179],v[212:215],v[40:43]\n" \
"buffer_load_dwordx4 a[24:27],v240,s[52:55],s77 offen offset:2048 sc1\nM v[44:47],a[180:183],v[212:215],v[44:47]\nds_write_b64 v250,v[180:181] offset:13312\nM v[104:107],a[184:187],v[212:215],v[104:107]\nM v[108:111],a[188:191],v[212:215],v[108:111]\nds_read_b64_tr_b16 v[212:213],v242 offset:33792\nds_read_b64_tr_b16 v[214:215],v242 offset:34048\nM v[48:51],a[176:179],v[216:219],v[48:51]\n" \
"buffer_load_dwordx4 v243,s[40:43],s69 offen offset:0 lds\nM v[52:55],a[180:183],v[216:219],v[52:55]\nM v[112:115],a[184:187],v[216:219],v[112:115]\nM v[116:119],a[188:191],v[216:219],v[116:119]\nds_read_b64_tr_b16 v[216:217],v241 offset:34304\nds_read_b64_tr_b16 v[218:219],v241 offset:34560\nM v[56:59],a[176:179],v[220:223],v[56:59]\nbuffer_load_dwordx4 a[28:31],v240,s[52:55],s77 offen offset:3072 sc1\n" \
"M v[60:63],a[180:183],v[220:223],v[60:63]\nds_write_b64 v250,v[182:183] offset:13320\ns_add_u32 s77,s77,4096\nM v[120:123],a[184:187],v[220:223],v[120:123]\nM v[124:127],a[188:191],v[220:223],v[124:127]\nds_read_b64_tr_b16 v[220:221],v242 offset:34304\ns_waitcnt lgkmcnt(9)\nM v[0:3],a[192:195],v[192:195],v[0:3]\n" \
"buffer_load_dwordx4 v244,s[40:43],s69 offen offset:1024 lds\nM v[4:7],a[196:199],v[192:195],v[4:7]\nds_read_b64_tr_b16 v[222:223],v242 offset:34560\nM v[64:67],a[200:203],v[192:195],v[64:67]\nM v[68:71],a[204:207],v[192:195],v[68:71]\nds_read_b64_tr_b16 v[192:193],v241 offset:40960\nds_read_b64_tr_b16 v[194:195],v241 offset:41216\nM v[8:11],a[192:195],v[196:199],v[8:11]\n" \
"buffer_load_dwordx4 a[32:35],v240,s[52:55],s76 offen offset:0 sc1\nM v[12:15],a[196:199],v[196:199],v[12:15]\nM v[72:75],a[200:203],v[196:199],v[72:75]\nM v[76:79],a[204:207],v[196:199],v[76:79]\nds_read_b64_tr_b16 v[196:197],v242 offset:40960\nds_read_b64_tr_b16 v[198:199],v242 offset:41216\nM v[16:19],a[192:195],v[200:203],v[16:19]\nbuffer_load_dwordx4 v245,s[40:43],s69 offen offset:2048 lds\n" \
"M v[20:23],a[196:199],v[200:203],v[20:23]\nds_write_b64 v250,v[184:185] offset:14336\nM v[80:83],a[200:203],v[200:203],v[80:83]\nM v[84:87],a[204:207],v[200:203],v[84:87]\nds_read_b64_tr_b16 v[200:201],v241 offset:41472\nds_read_b64_tr_b16 v[202:203],v241 offset:41728\nM v[24:27],a[192:195],v[204:207],v[24:27]\nbuffer_load_dwordx4 a[36:39],v240,s[52:55],s76 offen offset:1024 sc1\n" \
"M v[28:31],a[196:199],v[204:207],v[28:31]\nM v[88:91],a[200:203],v[204:207],v[88:91]\nM v[92:95],a[204:207],v[204:207],v[92:95]\nds_read_b64_tr_b16 v[204:205],v242 offset:41472\ns_waitcnt lgkmcnt(8)\nM v[32:35],a[192:195],v[208:211],v[32:35]\nbuffer_load_dwordx4 v246,s[40:43],s69 offen offset:3072 lds\nM v[36:39],a[196:199],v[208:211],v[36:39]\n" \
"ds_read_b64_tr_b16 v[206:207],v242 offset:41728\nM v[96:99],a[200:203],v[208:211],v[96:99]\nM v[100:103],a[204:207],v[208:211],v[100:103]\nds_read_b64_tr_b16 v[208:209],v241 offset:41984\nds_read_b64_tr_b16 v[210:211],v241 offset:42240\nM v[40:43],a[192:195],v[212:215],v[40:43]\nbuffer_load_dwordx4 a[40:43],v240,s[52:55],s77 offen offset:0 sc1\nM v[44:47],a[196:199],v[212:215],v[44:47]\n" \
"ds_write_b64 v250,v[186:187] offset:14344\nM v[104:107],a[200:203],v[212:215],v[104:107]\nM v[108:111],a[204:207],v[212:215],v[108:111]\nds_read_b64_tr_b16 v[212:213],v242 offset:41984\nds_read_b64_tr_b16 v[214:215],v242 offset:42240\nM v[48:51],a[192:195],v[216:219],v[48:51]\ns_waitcnt vmcnt(19)\nbuffer_store_dword v254,off,s[52:55],s29 sc1\n" \
"M v[52:55],a[196:199],v[216:219],v[52:55]\ns_add_u32 s29,s29,4096\nM v[112:115],a[200:203],v[216:219],v[112:115]\nbuffer_load_dwordx4 a[44:47],v240,s[52:55],s77 offen offset:1024 sc1\nM v[116:119],a[204:207],v[216:219],v[116:119]\nds_read_b64_tr_b16 v[216:217],v241 offset:42496\nds_read_b64_tr_b16 v[218:219],v241 offset:42752\nM v[56:59],a[192:195],v[220:223],v[56:59]\n" \
"M v[60:63],a[196:199],v[220:223],v[60:63]\nM v[120:123],a[200:203],v[220:223],v[120:123]\nM v[124:127],a[204:207],v[220:223],v[124:127]\nds_read_b64_tr_b16 v[220:221],v242 offset:42496\ns_waitcnt lgkmcnt(8)\nM v[0:3],a[208:211],v[192:195],v[0:3]\nds_write_b64 v250,v[188:189] offset:15360\nds_read_b64_tr_b16 v[222:223],v242 offset:42752\n" \
"M v[4:7],a[212:215],v[192:195],v[4:7]\nM v[64:67],a[216:219],v[192:195],v[64:67]\nbuffer_load_dwordx4 a[48:51],v240,s[52:55],s76 offen offset:2048 sc1\nM v[68:71],a[220:223],v[192:195],v[68:71]\nM v[8:11],a[208:211],v[196:199],v[8:11]\nM v[12:15],a[212:215],v[196:199],v[12:15]\nM v[72:75],a[216:219],v[196:199],v[72:75]\nM v[76:79],a[220:223],v[196:199],v[76:79]\n" \
"M v[16:19],a[208:211],v[200:203],v[16:19]\nM v[20:23],a[212:215],v[200:203],v[20:23]\nM v[80:83],a[216:219],v[200:203],v[80:83]\nbuffer_load_dwordx4 a[52:55],v240,s[52:55],s76 offen offset:3072 sc1\nM v[84:87],a[220:223],v[200:203],v[84:87]\ns_add_u32 s76,s76,4096\nM v[24:27],a[208:211],v[204:207],v[24:27]\nds_write_b64 v250,v[190:191] offset:15368\n" \
"s_add_u32 m0,s64,131072\nM v[28:31],a[212:215],v[204:207],v[28:31]\ns_add_u32 s69,s68,1048576\nv_add_u32 v250,65536,v250\nM v[88:91],a[216:219],v[204:207],v[88:91]\ns_waitcnt lgkmcnt(1)\ns_barrier\nM v[92:95],a[220:223],v[204:207],v[92:95]\n" \
"ds_read_b64_tr_b16 v[192:193],v241 offset:49152\nds_read_b64_tr_b16 v[194:195],v241 offset:49408\nM v[32:35],a[208:211],v[208:211],v[32:35]\nds_read_b64_tr_b16 v[196:197],v242 offset:49152\nds_read_b64_tr_b16 v[198:199],v242 offset:49408\nM v[36:39],a[212:215],v[208:211],v[36:39]\nds_read_b64_tr_b16 v[200:201],v241 offset:49664\nds_read_b64_tr_b16 v[202:203],v241 offset:49920\n" \
"M v[96:99],a[216:219],v[208:211],v[96:99]\nds_read_b64_tr_b16 v[204:205],v242 offset:49664\nds_read_b64_tr_b16 v[206:207],v242 offset:49920\nM v[100:103],a[220:223],v[208:211],v[100:103]\nds_read_b64_tr_b16 v[208:209],v241 offset:50176\nds_read_b64_tr_b16 v[210:211],v241 offset:50432\nM v[40:43],a[208:211],v[212:215],v[40:43]\nbuffer_load_dwordx4 a[56:59],v240,s[52:55],s77 offen offset:2048 sc1\n" \
"M v[44:47],a[212:215],v[212:215],v[44:47]\nM v[104:107],a[216:219],v[212:215],v[104:107]\nM v[108:111],a[220:223],v[212:215],v[108:111]\nds_read_b64_tr_b16 v[212:213],v242 offset:50176\nds_read_b64_tr_b16 v[214:215],v242 offset:50432\nM v[48:51],a[208:211],v[216:219],v[48:51]\nbuffer_load_dwordx4 v243,s[40:43],s69 offen offset:0 lds\nM v[52:55],a[212:215],v[216:219],v[52:55]\n" \
"M v[112:115],a[216:219],v[216:219],v[112:115]\nM v[116:119],a[220:223],v[216:219],v[116:119]\nds_read_b64_tr_b16 v[216:217],v241 offset:50688\nds_read_b64_tr_b16 v[218:219],v241 offset:50944\nM v[56:59],a[208:211],v[220:223],v[56:59]\nbuffer_load_dwordx4 a[60:63],v240,s[52:55],s77 offen offset:3072 sc1\nM v[60:63],a[212:215],v[220:223],v[60:63]\ns_add_u32 s77,s77,4096\n" \
"M v[120:123],a[216:219],v[220:223],v[120:123]\nM v[124:127],a[220:223],v[220:223],v[124:127]\nds_read_b64_tr_b16 v[220:221],v242 offset:50688\ns_waitcnt lgkmcnt(7)\nM v[0:3],a[224:227],v[192:195],v[0:3]\nbuffer_load_dwordx4 v244,s[40:43],s69 offen offset:1024 lds\nM v[4:7],a[228:231],v[192:195],v[4:7]\nds_read_b64_tr_b16 v[222:223],v242 offset:50944\n" \
"M v[64:67],a[232:235],v[192:195],v[64:67]\nM v[68:71],a[236:239],v[192:195],v[68:71]\nds_read_b64_tr_b16 v[192:193],v241 offset:57344\nds_read_b64_tr_b16 v[194:195],v241 offset:57600\nM v[8:11],a[224:227],v[196:199],v[8:11]\nbuffer_load_dwordx4 a[64:67],v240,s[52:55],s76 offen offset:0 sc1\nM v[12:15],a[228:231],v[196:199],v[12:15]\nM v[72:75],a[232:235],v[196:199],v[72:75]\n" \
"M v[76:79],a[236:239],v[196:199],v[76:79]\nds_read_b64_tr_b16 v[196:197],v242 offset:57344\nds_read_b64_tr_b16 v[198:199],v242 offset:57600\nM v[16:19],a[224:227],v[200:203],v[16:19]\nbuffer_load_dwordx4 v245,s[40:43],s69 offen offset:2048 lds\nM v[20:23],a[228:231],v[200:203],v[20:23]\nM v[80:83],a[232:235],v[200:203],v[80:83]\nM v[84:87],a[236:239],v[200:203],v[84:87]\n" \
"ds_read_b64_tr_b16 v[200:201],v241 offset:57856\nds_read_b64_tr_b16 v[202:203],v241 offset:58112\nM v[24:27],a[224:227],v[204:207],v[24:27]\nbuffer_load_dwordx4 a[68:71],v240,s[52:55],s76 offen offset:1024 sc1\nM v[28:31],a[228:231],v[204:207],v[28:31]\nM v[88:91],a[232:235],v[204:207],v[88:91]\nM v[92:95],a[236:239],v[204:207],v[92:95]\nds_read_b64_tr_b16 v[204:205],v242 offset:57856\n" \
"s_waitcnt lgkmcnt(7)\nM v[32:35],a[224:227],v[208:211],v[32:35]\nbuffer_load_dwordx4 v246,s[40:43],s69 offen offset:3072 lds\nM v[36:39],a[228:231],v[208:211],v[36:39]\nds_read_b64_tr_b16 v[206:207],v242 offset:58112\nM v[96:99],a[232:235],v[208:211],v[96:99]\nM v[100:103],a[236:239],v[208:211],v[100:103]\nds_read_b64_tr_b16 v[208:209],v241 offset:58368\n" \
"ds_read_b64_tr_b16 v[210:211],v241 offset:58624\nM v[40:43],a[224:227],v[212:215],v[40:43]\nbuffer_load_dwordx4 a[72:75],v240,s[52:55],s77 offen offset:0 sc1\nM v[44:47],a[228:231],v[212:215],v[44:47]\nM v[104:107],a[232:235],v[212:215],v[104:107]\nM v[108:111],a[236:239],v[212:215],v[108:111]\nds_read_b64_tr_b16 v[212:213],v242 offset:58368\nds_read_b64_tr_b16 v[214:215],v242 offset:58624\n" \
"M v[48:51],a[224:227],v[216:219],v[48:51]\nM v[52:55],a[228:231],v[216:219],v[52:55]\nM v[112:115],a[232:235],v[216:219],v[112:115]\nbuffer_load_dwordx4 a[76:79],v240,s[52:55],s77 offen offset:1024 sc1\nM v[116:119],a[236:239],v[216:219],v[116:119]\nds_read_b64_tr_b16 v[216:217],v241 offset:58880\nds_read_b64_tr_b16 v[218:219],v241 offset:59136\nM v[56:59],a[224:227],v[220:223],v[56:59]\n" \
"M v[60:63],a[228:231],v[220:223],v[60:63]\nM v[120:123],a[232:235],v[220:223],v[120:123]\nM v[124:127],a[236:239],v[220:223],v[124:127]\nds_read_b64_tr_b16 v[220:221],v242 offset:58880\ns_waitcnt lgkmcnt(8)\nM v[0:3],a[240:243],v[192:195],v[0:3]\nds_read_b64_tr_b16 v[222:223],v242 offset:59136\nM v[4:7],a[244:247],v[192:195],v[4:7]\n" \
"M v[64:67],a[248:251],v[192:195],v[64:67]\nbuffer_load_dwordx4 a[80:83],v240,s[52:55],s76 offen offset:2048 sc1\nM v[68:71],a[252:255],v[192:195],v[68:71]\nM v[8:11],a[240:243],v[196:199],v[8:11]\nM v[12:15],a[244:247],v[196:199],v[12:15]\nM v[72:75],a[248:251],v[196:199],v[72:75]\nv_cvt_pk_bf16_f32 v128,v0,v1\nv_cvt_pk_bf16_f32 v129,v2,v3\n" \
"M v[76:79],a[252:255],v[196:199],v[76:79]\nv_cvt_pk_bf16_f32 v130,v4,v5\nv_cvt_pk_bf16_f32 v131,v6,v7\nM v[16:19],a[240:243],v[200:203],v[16:19]\nv_cvt_pk_bf16_f32 v160,v64,v65\nv_cvt_pk_bf16_f32 v161,v66,v67\nM v[20:23],a[244:247],v[200:203],v[20:23]\nv_cvt_pk_bf16_f32 v162,v68,v69\n" \
"v_cvt_pk_bf16_f32 v163,v70,v71\nM v[80:83],a[248:251],v[200:203],v[80:83]\nv_cvt_pk_bf16_f32 v132,v8,v9\nv_cvt_pk_bf16_f32 v133,v10,v11\nM v[84:87],a[252:255],v[200:203],v[84:87]\nv_cvt_pk_bf16_f32 v134,v12,v13\ns_waitcnt lgkmcnt(2)\nM v[24:27],a[240:243],v[204:207],v[24:27]\n" \
"v_cvt_pk_bf16_f32 v135,v14,v15\nv_cvt_pk_bf16_f32 v164,v72,v73\nM v[28:31],a[244:247],v[204:207],v[28:31]\nv_cvt_pk_bf16_f32 v165,v74,v75\nv_cvt_pk_bf16_f32 v166,v76,v77\nM v[88:91],a[248:251],v[204:207],v[88:91]\ns_waitcnt vmcnt(23) lgkmcnt(0)\ns_barrier\n" \
"M v[92:95],a[252:255],v[204:207],v[92:95]\nds_read_b64_tr_b16 v[192:193],v241 offset:0\nds_read_b64_tr_b16 v[194:195],v241 offset:256\nM v[32:35],a[240:243],v[208:211],v[32:35]\nds_read_b64_tr_b16 v[196:197],v242 offset:0\nds_read_b64_tr_b16 v[198:199],v242 offset:256\nM v[36:39],a[244:247],v[208:211],v[36:39]\nds_read_b64_tr_b16 v[200:201],v241 offset:512\n" \
"ds_read_b64_tr_b16 v[202:203],v241 offset:768\nM v[96:99],a[248:251],v[208:211],v[96:99]\nds_read_b64_tr_b16 v[204:205],v242 offset:512\nds_read_b64_tr_b16 v[206:207],v242 offset:768\nM v[100:103],a[252:255],v[208:211],v[100:103]\nds_read_b64_tr_b16 v[208:209],v241 offset:1024\nds_read_b64_tr_b16 v[210:211],v241 offset:1280\nM v[40:43],a[240:243],v[212:215],v[40:43]\n" \
"v_cvt_pk_bf16_f32 v167,v78,v79\nv_cvt_pk_bf16_f32 v136,v16,v17\nM v[44:47],a[244:247],v[212:215],v[44:47]\nv_cvt_pk_bf16_f32 v137,v18,v19\nv_cvt_pk_bf16_f32 v138,v20,v21\nM v[104:107],a[248:251],v[212:215],v[104:107]\nv_cvt_pk_bf16_f32 v139,v22,v23\nv_cvt_pk_bf16_f32 v168,v80,v81\n" \
"M v[108:111],a[252:255],v[212:215],v[108:111]\nv_cvt_pk_bf16_f32 v169,v82,v83\nds_read_b64_tr_b16 v[212:213],v242 offset:1024\nM v[48:51],a[240:243],v[216:219],v[48:51]\nds_read_b64_tr_b16 v[214:215],v242 offset:1280\nv_cvt_pk_bf16_f32 v170,v84,v85\nM v[52:55],a[244:247],v[216:219],v[52:55]\nv_cvt_pk_bf16_f32 v171,v86,v87\n" \
"v_cvt_pk_bf16_f32 v140,v24,v25\nM v[112:115],a[248:251],v[216:219],v[112:115]\nv_cvt_pk_bf16_f32 v141,v26,v27\nv_cvt_pk_bf16_f32 v142,v28,v29\nM v[116:119],a[252:255],v[216:219],v[116:119]\nv_cvt_pk_bf16_f32 v143,v30,v31\nv_cvt_pk_bf16_f32 v172,v88,v89\nM v[56:59],a[240:243],v[220:223],v[56:59]\n" \
"v_cvt_pk_bf16_f32 v173,v90,v91\nds_read_b64_tr_b16 v[216:217],v241 offset:1536\nM v[60:63],a[244:247],v[220:223],v[60:63]\nds_read_b64_tr_b16 v[218:219],v241 offset:1792\nv_cvt_pk_bf16_f32 v174,v92,v93\nM v[120:123],a[248:251],v[220:223],v[120:123]\nv_cvt_pk_bf16_f32 v175,v94,v95\nv_cvt_pk_bf16_f32 v144,v32,v33\n" \
"M v[124:127],a[252:255],v[220:223],v[124:127]\nv_cvt_pk_bf16_f32 v145,v34,v35\ns_waitcnt lgkmcnt(4)\nM v[0:3],v[192:195],a[0:3],0\nv_cvt_pk_bf16_f32 v146,v36,v37\nv_cvt_pk_bf16_f32 v147,v38,v39\nM v[4:7],v[192:195],a[4:7],0\nv_cvt_pk_bf16_f32 v176,v96,v97\n" \
"v_cvt_pk_bf16_f32 v177,v98,v99\nM v[64:67],v[192:195],a[8:11],0\nds_read_b64_tr_b16 v[220:221],v242 offset:1536\nds_read_b64_tr_b16 v[222:223],v242 offset:1792\nM v[68:71],v[192:195],a[12:15],0\nv_cvt_pk_bf16_f32 v178,v100,v101\nv_cvt_pk_bf16_f32 v179,v102,v103\nM v[8:11],v[196:199],a[0:3],0\n" \
"v_cvt_pk_bf16_f32 v148,v40,v41\nv_cvt_pk_bf16_f32 v149,v42,v43\nM v[12:15],v[196:199],a[4:7],0\nv_cvt_pk_bf16_f32 v150,v44,v45\nv_cvt_pk_bf16_f32 v151,v46,v47\nM v[72:75],v[196:199],a[8:11],0\nv_cvt_pk_bf16_f32 v180,v104,v105\nv_cvt_pk_bf16_f32 v181,v106,v107\n" \
"M v[76:79],v[196:199],a[12:15],0\nds_read_b64_tr_b16 v[192:193],v241 offset:8192\nds_read_b64_tr_b16 v[194:195],v241 offset:8448\nM v[16:19],v[200:203],a[0:3],0\nv_cvt_pk_bf16_f32 v182,v108,v109\nv_cvt_pk_bf16_f32 v183,v110,v111\nM v[20:23],v[200:203],a[4:7],0\nv_cvt_pk_bf16_f32 v152,v48,v49\n" \
"v_cvt_pk_bf16_f32 v153,v50,v51\nM v[80:83],v[200:203],a[8:11],0\nv_cvt_pk_bf16_f32 v154,v52,v53\nv_cvt_pk_bf16_f32 v155,v54,v55\nM v[84:87],v[200:203],a[12:15],0\nv_cvt_pk_bf16_f32 v184,v112,v113\nv_cvt_pk_bf16_f32 v185,v114,v115\nM v[24:27],v[204:207],a[0:3],0\n" \
"ds_read_b64_tr_b16 v[196:197],v242 offset:8192\nds_read_b64_tr_b16 v[198:199],v242 offset:8448\nM v[28:31],v[204:207],a[4:7],0\nv_cvt_pk_bf16_f32 v186,v116,v117\nv_cvt_pk_bf16_f32 v187,v118,v119\nM v[88:91],v[204:207],a[8:11],0\nv_cvt_pk_bf16_f32 v156,v56,v57\nv_cvt_pk_bf16_f32 v157,v58,v59\n" \
"M v[92:95],v[204:207],a[12:15],0\nv_cvt_pk_bf16_f32 v158,v60,v61\nv_cvt_pk_bf16_f32 v159,v62,v63\nM v[32:35],v[208:211],a[0:3],0\nv_cvt_pk_bf16_f32 v188,v120,v121\nv_cvt_pk_bf16_f32 v189,v122,v123\nM v[36:39],v[208:211],a[4:7],0\nds_read_b64_tr_b16 v[200:201],v241 offset:8704\n" \
"ds_read_b64_tr_b16 v[202:203],v241 offset:8960\nM v[96:99],v[208:211],a[8:11],0\nv_cvt_pk_bf16_f32 v190,v124,v125\nv_cvt_pk_bf16_f32 v191,v126,v127\nM v[100:103],v[208:211],a[12:15],0\nds_read_b64_tr_b16 v[204:205],v242 offset:8704\ns_waitcnt lgkmcnt(5)\nM v[40:43],v[212:215],a[0:3],0\n" \
"buffer_store_dwordx4 v[128:131],v240,s[52:55],s74 offen offset:0 sc1\nM v[44:47],v[212:215],a[4:7],0\nds_read_b64_tr_b16 v[206:207],v242 offset:8960\nds_read_b64_tr_b16 v[208:209],v241 offset:9216\nM v[104:107],v[212:215],a[8:11],0\nds_read_b64_tr_b16 v[210:211],v241 offset:9472\ns_add_u32 m0,s64,147456\nM v[108:111],v[212:215],a[12:15],0\n" \
"ds_read_b64_tr_b16 v[212:213],v242 offset:9216\nds_read_b64_tr_b16 v[214:215],v242 offset:9472\nM v[48:51],v[216:219],a[0:3],0\nbuffer_load_dwordx4 a[84:87],v240,s[52:55],s76 offen offset:3072 sc1\nM v[52:55],v[216:219],a[4:7],0\ns_add_u32 s69,s68,1572864\ns_add_u32 s76,s76,4096\nM v[112:115],v[216:219],a[8:11],0\n" \
"M v[116:119],v[216:219],a[12:15],0\nds_read_b64_tr_b16 v[216:217],v241 offset:9728\nds_read_b64_tr_b16 v[218:219],v241 offset:9984\nM v[56:59],v[220:223],a[0:3],0\nbuffer_load_dwordx4 a[88:91],v240,s[52:55],s77 offen offset:2048 sc1\nM v[60:63],v[220:223],a[4:7],0\nM v[120:123],v[220:223],a[8:11],0\nM v[124:127],v[220:223],a[12:15],0\n" \
"ds_read_b64_tr_b16 v[220:221],v242 offset:9728\nds_read_b64_tr_b16 v[222:223],v242 offset:9984\nM v[0:3],v[192:195],a[16:19],v[0:3]\nbuffer_load_dwordx4 a[92:95],v240,s[52:55],s77 offen offset:3072 sc1\nM v[4:7],v[192:195],a[20:23],v[4:7]\ns_add_u32 s77,s77,4096\nM v[64:67],v[192:195],a[24:27],v[64:67]\ns_waitcnt vmcnt(25)\n" \
"M v[68:71],v[192:195],a[28:31],v[68:71]\ns_waitcnt lgkmcnt(6)\nM v[8:11],v[196:199],a[16:19],v[8:11]\nbuffer_load_dwordx4 a[96:99],v240,s[52:55],s76 offen offset:0 sc1\nM v[12:15],v[196:199],a[20:23],v[12:15]\nM v[72:75],v[196:199],a[24:27],v[72:75]\nM v[76:79],v[196:199],a[28:31],v[76:79]\nM v[16:19],v[200:203],a[16:19],v[16:19]\n" \
"buffer_load_dwordx4 a[100:103],v240,s[52:55],s76 offen offset:1024 sc1\nM v[20:23],v[200:203],a[20:23],v[20:23]\nM v[80:83],v[200:203],a[24:27],v[80:83]\nM v[84:87],v[200:203],a[28:31],v[84:87]\nM v[24:27],v[204:207],a[16:19],v[24:27]\nbuffer_load_dwordx4 a[104:107],v240,s[52:55],s77 offen offset:0 sc1\nM v[28:31],v[204:207],a[20:23],v[28:31]\nM v[88:91],v[204:207],a[24:27],v[88:91]\n" \
"s_waitcnt vmcnt(22) lgkmcnt(0)\ns_barrier\nM v[92:95],v[204:207],a[28:31],v[92:95]\nds_read_b64_tr_b16 v[192:193],v241 offset:16384\nds_read_b64_tr_b16 v[194:195],v241 offset:16640\nM v[32:35],v[208:211],a[16:19],v[32:35]\nds_read_b64_tr_b16 v[196:197],v242 offset:16384\nds_read_b64_tr_b16 v[198:199],v242 offset:16640\n" \
"M v[36:39],v[208:211],a[20:23],v[36:39]\nds_read_b64_tr_b16 v[200:201],v241 offset:16896\nds_read_b64_tr_b16 v[202:203],v241 offset:17152\nM v[96:99],v[208:211],a[24:27],v[96:99]\nbuffer_store_dwordx4 v[132:135],v240,s[52:55],s74 offen offset:1024 sc1\nM v[100:103],v[208:211],a[28:31],v[100:103]\nds_read_b64_tr_b16 v[204:205],v242 offset:16896\nds_read_b64_tr_b16 v[206:207],v242 offset:17152\n" \
"M v[40:43],v[212:215],a[16:19],v[40:43]\nds_read_b64_tr_b16 v[208:209],v241 offset:17408\nds_read_b64_tr_b16 v[210:211],v241 offset:17664\nM v[44:47],v[212:215],a[20:23],v[44:47]\nM v[104:107],v[212:215],a[24:27],v[104:107]\nbuffer_load_dwordx4 a[108:111],v240,s[52:55],s77 offen offset:1024 sc1\nM v[108:111],v[212:215],a[28:31],v[108:111]\nds_read_b64_tr_b16 v[212:213],v242 offset:17408\n" \
"ds_read_b64_tr_b16 v[214:215],v242 offset:17664\nM v[48:51],v[216:219],a[16:19],v[48:51]\nM v[52:55],v[216:219],a[20:23],v[52:55]\nM v[112:115],v[216:219],a[24:27],v[112:115]\nbuffer_load_dwordx4 a[112:115],v240,s[52:55],s76 offen offset:2048 sc1\nM v[116:119],v[216:219],a[28:31],v[116:119]\nds_read_b64_tr_b16 v[216:217],v241 offset:17920\nds_read_b64_tr_b16 v[218:219],v241 offset:18176\n" \
"M v[56:59],v[220:223],a[16:19],v[56:59]\nM v[60:63],v[220:223],a[20:23],v[60:63]\nM v[120:123],v[220:223],a[24:27],v[120:123]\nbuffer_load_dwordx4 v243,s[40:43],s69 offen offset:0 lds\nM v[124:127],v[220:223],a[28:31],v[124:127]\nds_read_b64_tr_b16 v[220:221],v242 offset:17920\ns_waitcnt lgkmcnt(9)\nM v[0:3],v[192:195],a[32:35],v[0:3]\n" \
"ds_read_b64_tr_b16 v[222:223],v242 offset:18176\nM v[4:7],v[192:195],a[36:39],v[4:7]\nM v[64:67],v[192:195],a[40:43],v[64:67]\ns_waitcnt vmcnt(21)\nM v[68:71],v[192:195],a[44:47],v[68:71]\nds_read_b64_tr_b16 v[192:193],v241 offset:24576\nds_read_b64_tr_b16 v[194:195],v241 offset:24832\nM v[8:11],v[196:199],a[32:35],v[8:11]\n" \
"buffer_load_dwordx4 v244,s[40:43],s69 offen offset:1024 lds\nM v[12:15],v[196:199],a[36:39],v[12:15]\nM v[72:75],v[196:199],a[40:43],v[72:75]\nM v[76:79],v[196:199],a[44:47],v[76:79]\nds_read_b64_tr_b16 v[196:197],v242 offset:24576\nds_read_b64_tr_b16 v[198:199],v242 offset:24832\nM v[16:19],v[200:203],a[32:35],v[16:19]\nbuffer_load_dwordx4 v245,s[40:43],s69 offen offset:2048 lds\n" \
"M v[20:23],v[200:203],a[36:39],v[20:23]\nM v[80:83],v[200:203],a[40:43],v[80:83]\nM v[84:87],v[200:203],a[44:47],v[84:87]\nds_read_b64_tr_b16 v[200:201],v241 offset:25088\ns_waitcnt lgkmcnt(7)\nM v[24:27],v[204:207],a[32:35],v[24:27]\nbuffer_load_dwordx4 v246,s[40:43],s69 offen offset:3072 lds\nM v[28:31],v[204:207],a[36:39],v[28:31]\n" \
"ds_read_b64_tr_b16 v[202:203],v241 offset:25344\ns_add_u32 m0,s64,98304\nM v[88:91],v[204:207],a[40:43],v[88:91]\ns_add_u32 s69,s68,2097152\nM v[92:95],v[204:207],a[44:47],v[92:95]\nds_read_b64_tr_b16 v[204:205],v242 offset:25088\nds_read_b64_tr_b16 v[206:207],v242 offset:25344\nM v[32:35],v[208:211],a[32:35],v[32:35]\n" \
"buffer_store_dwordx4 v[136:139],v240,s[52:55],s74 offen offset:2048 sc1\nM v[36:39],v[208:211],a[36:39],v[36:39]\nM v[96:99],v[208:211],a[40:43],v[96:99]\nM v[100:103],v[208:211],a[44:47],v[100:103]\nds_read_b64_tr_b16 v[208:209],v241 offset:25600\nds_read_b64_tr_b16 v[210:211],v241 offset:25856\nM v[40:43],v[212:215],a[32:35],v[40:43]\nbuffer_load_dwordx4 a[116:119],v240,s[52:55],s76 offen offset:3072 sc1\n" \
"M v[44:47],v[212:215],a[36:39],v[44:47]\ns_add_u32 s76,s76,4096\nM v[104:107],v[212:215],a[40:43],v[104:107]\nM v[108:111],v[212:215],a[44:47],v[108:111]\nds_read_b64_tr_b16 v[212:213],v242 offset:25600\nds_read_b64_tr_b16 v[214:215],v242 offset:25856\nM v[48:51],v[216:219],a[32:35],v[48:51]\nbuffer_load_dwordx4 a[120:123],v240,s[52:55],s77 offen offset:2048 sc1\n" \
"M v[52:55],v[216:219],a[36:39],v[52:55]\nM v[112:115],v[216:219],a[40:43],v[112:115]\nM v[116:119],v[216:219],a[44:47],v[116:119]\nds_read_b64_tr_b16 v[216:217],v241 offset:26112\ns_waitcnt lgkmcnt(7)\nM v[56:59],v[220:223],a[32:35],v[56:59]\nbuffer_load_dwordx4 a[124:127],v240,s[52:55],s77 offen offset:3072 sc1\nM v[60:63],v[220:223],a[36:39],v[60:63]\n" \
"ds_read_b64_tr_b16 v[218:219],v241 offset:26368\ns_add_u32 s77,s77,4096\nM v[120:123],v[220:223],a[40:43],v[120:123]\nM v[124:127],v[220:223],a[44:47],v[124:127]\nds_read_b64_tr_b16 v[220:221],v242 offset:26112\nds_read_b64_tr_b16 v[222:223],v242 offset:26368\nM v[0:3],v[192:195],a[48:51],v[0:3]\nbuffer_load_dwordx4 a[128:131],v240,s[52:55],s76 offen offset:0 sc1\n" \
"M v[4:7],v[192:195],a[52:55],v[4:7]\nM v[64:67],v[192:195],a[56:59],v[64:67]\ns_waitcnt vmcnt(27)\nM v[68:71],v[192:195],a[60:63],v[68:71]\nM v[8:11],v[196:199],a[48:51],v[8:11]\nbuffer_load_dwordx4 a[132:135],v240,s[52:55],s76 offen offset:1024 sc1\nM v[12:15],v[196:199],a[52:55],v[12:15]\nM v[72:75],v[196:199],a[56:59],v[72:75]\n" \
"M v[76:79],v[196:199],a[60:63],v[76:79]\nM v[16:19],v[200:203],a[48:51],v[16:19]\nbuffer_load_dwordx4 a[136:139],v240,s[52:55],s77 offen offset:0 sc1\nM v[20:23],v[200:203],a[52:55],v[20:23]\nM v[80:83],v[200:203],a[56:59],v[80:83]\nM v[84:87],v[200:203],a[60:63],v[84:87]\ns_waitcnt lgkmcnt(3)\nM v[24:27],v[204:207],a[48:51],v[24:27]\n" \
"buffer_load_dwordx4 a[140:143],v240,s[52:55],s77 offen offset:1024 sc1\nM v[28:31],v[204:207],a[52:55],v[28:31]\nM v[88:91],v[204:207],a[56:59],v[88:91]\ns_waitcnt vmcnt(22) lgkmcnt(0)\ns_barrier\nM v[92:95],v[204:207],a[60:63],v[92:95]\nds_read_b64_tr_b16 v[192:193],v241 offset:32768\nds_read_b64_tr_b16 v[194:195],v241 offset:33024\n" \
"M v[32:35],v[208:211],a[48:51],v[32:35]\nbuffer_store_dwordx4 v[140:143],v240,s[52:55],s74 offen offset:3072 sc1\nM v[36:39],v[208:211],a[52:55],v[36:39]\nds_read_b64_tr_b16 v[196:197],v242 offset:32768\nds_read_b64_tr_b16 v[198:199],v242 offset:33024\nM v[96:99],v[208:211],a[56:59],v[96:99]\nds_read_b64_tr_b16 v[200:201],v241 offset:33280\nds_read_b64_tr_b16 v[202:203],v241 offset:33536\n" \
"M v[100:103],v[208:211],a[60:63],v[100:103]\nds_read_b64_tr_b16 v[204:205],v242 offset:33280\nds_read_b64_tr_b16 v[206:207],v242 offset:33536\nM v[40:43],v[212:215],a[48:51],v[40:43]\nds_read_b64_tr_b16 v[208:209],v241 offset:33792\nds_read_b64_tr_b16 v[210:211],v241 offset:34048\nM v[44:47],v[212:215],a[52:55],v[44:47]\nbuffer_load_dwordx4 a[144:147],v240,s[52:55],s76 offen offset:2048 sc1\n" \
"M v[104:107],v[212:215],a[56:59],v[104:107]\ns_add_u32 s74,s74,4096\nM v[108:111],v[212:215],a[60:63],v[108:111]\nds_read_b64_tr_b16 v[212:213],v242 offset:33792\nds_read_b64_tr_b16 v[214:215],v242 offset:34048\nM v[48:51],v[216:219],a[48:51],v[48:51]\nM v[52:55],v[216:219],a[52:55],v[52:55]\nbuffer_load_dwordx4 v243,s[40:43],s69 offen offset:0 lds\n" \
"M v[112:115],v[216:219],a[56:59],v[112:115]\nM v[116:119],v[216:219],a[60:63],v[116:119]\nds_read_b64_tr_b16 v[216:217],v241 offset:34304\nds_read_b64_tr_b16 v[218:219],v241 offset:34560\nM v[56:59],v[220:223],a[48:51],v[56:59]\nM v[60:63],v[220:223],a[52:55],v[60:63]\nbuffer_load_dwordx4 v244,s[40:43],s69 offen offset:1024 lds\nM v[120:123],v[220:223],a[56:59],v[120:123]\n" \
"M v[124:127],v[220:223],a[60:63],v[124:127]\nds_read_b64_tr_b16 v[220:221],v242 offset:34304\ns_waitcnt lgkmcnt(9)\nM v[0:3],v[192:195],a[64:67],v[0:3]\nds_read_b64_tr_b16 v[222:223],v242 offset:34560\nM v[4:7],v[192:195],a[68:71],v[4:7]\nbuffer_load_dwordx4 v245,s[40:43],s69 offen offset:2048 lds\nM v[64:67],v[192:195],a[72:75],v[64:67]\n" \
"M v[68:71],v[192:195],a[76:79],v[68:71]\nds_read_b64_tr_b16 v[192:193],v241 offset:40960\nds_read_b64_tr_b16 v[194:195],v241 offset:41216\nM v[8:11],v[196:199],a[64:67],v[8:11]\nM v[12:15],v[196:199],a[68:71],v[12:15]\nbuffer_load_dwordx4 v246,s[40:43],s69 offen offset:3072 lds\nM v[72:75],v[196:199],a[72:75],v[72:75]\ns_add_u32 m0,s64,114688\n" \
"s_add_u32 s69,s68,2621440\nM v[76:79],v[196:199],a[76:79],v[76:79]\nds_read_b64_tr_b16 v[196:197],v242 offset:40960\nds_read_b64_tr_b16 v[198:199],v242 offset:41216\nM v[16:19],v[200:203],a[64:67],v[16:19]\nM v[20:23],v[200:203],a[68:71],v[20:23]\nbuffer_load_dwordx4 a[148:151],v240,s[52:55],s76 offen offset:3072 sc1\nM v[80:83],v[200:203],a[72:75],v[80:83]\n" \
"s_add_u32 s76,s76,4096\nM v[84:87],v[200:203],a[76:79],v[84:87]\nds_read_b64_tr_b16 v[200:201],v241 offset:41472\ns_waitcnt lgkmcnt(6)\nM v[24:27],v[204:207],a[64:67],v[24:27]\nds_read_b64_tr_b16 v[202:203],v241 offset:41728\nM v[28:31],v[204:207],a[68:71],v[28:31]\nbuffer_store_dwordx4 v[144:147],v240,s[52:55],s74 offen offset:0 sc1\n" \
"M v[88:91],v[204:207],a[72:75],v[88:91]\nM v[92:95],v[204:207],a[76:79],v[92:95]\nds_read_b64_tr_b16 v[204:205],v242 offset:41472\nds_read_b64_tr_b16 v[206:207],v242 offset:41728\nM v[32:35],v[208:211],a[64:67],v[32:35]\nM v[36:39],v[208:211],a[68:71],v[36:39]\nbuffer_load_dwordx4 a[152:155],v240,s[52:55],s77 offen offset:2048 sc1\nM v[96:99],v[208:211],a[72:75],v[96:99]\n" \
"M v[100:103],v[208:211],a[76:79],v[100:103]\nds_read_b64_tr_b16 v[208:209],v241 offset:41984\nds_read_b64_tr_b16 v[210:211],v241 offset:42240\nM v[40:43],v[212:215],a[64:67],v[40:43]\nM v[44:47],v[212:215],a[68:71],v[44:47]\nbuffer_load_dwordx4 a[156:159],v240,s[52:55],s77 offen offset:3072 sc1\nM v[104:107],v[212:215],a[72:75],v[104:107]\ns_add_u32 s77,s77,4096\n" \
"M v[108:111],v[212:215],a[76:79],v[108:111]\nds_read_b64_tr_b16 v[212:213],v242 offset:41984\nds_read_b64_tr_b16 v[214:215],v242 offset:42240\nM v[48:51],v[216:219],a[64:67],v[48:51]\nM v[52:55],v[216:219],a[68:71],v[52:55]\nbuffer_load_dwordx4 a[160:163],v240,s[52:55],s76 offen offset:0 sc1\nM v[112:115],v[216:219],a[72:75],v[112:115]\nM v[116:119],v[216:219],a[76:79],v[116:119]\n" \
"ds_read_b64_tr_b16 v[216:217],v241 offset:42496\ns_waitcnt lgkmcnt(7)\nM v[56:59],v[220:223],a[64:67],v[56:59]\nds_read_b64_tr_b16 v[218:219],v241 offset:42752\nM v[60:63],v[220:223],a[68:71],v[60:63]\nbuffer_load_dwordx4 a[164:167],v240,s[52:55],s76 offen offset:1024 sc1\nM v[120:123],v[220:223],a[72:75],v[120:123]\nM v[124:127],v[220:223],a[76:79],v[124:127]\n" \
"ds_read_b64_tr_b16 v[220:221],v242 offset:42496\nds_read_b64_tr_b16 v[222:223],v242 offset:42752\nM v[0:3],v[192:195],a[80:83],v[0:3]\ns_waitcnt vmcnt(27)\nM v[4:7],v[192:195],a[84:87],v[4:7]\nbuffer_load_dwordx4 a[168:171],v240,s[52:55],s77 offen offset:0 sc1\nM v[64:67],v[192:195],a[88:91],v[64:67]\nM v[68:71],v[192:195],a[92:95],v[68:71]\n" \
"M v[8:11],v[196:199],a[80:83],v[8:11]\nM v[12:15],v[196:199],a[84:87],v[12:15]\nbuffer_load_dwordx4 a[172:175],v240,s[52:55],s77 offen offset:1024 sc1\nM v[72:75],v[196:199],a[88:91],v[72:75]\nM v[76:79],v[196:199],a[92:95],v[76:79]\nM v[16:19],v[200:203],a[80:83],v[16:19]\nM v[20:23],v[200:203],a[84:87],v[20:23]\nbuffer_load_dwordx4 a[176:179],v240,s[52:55],s76 offen offset:2048 sc1\n" \
"M v[80:83],v[200:203],a[88:91],v[80:83]\nM v[84:87],v[200:203],a[92:95],v[84:87]\ns_waitcnt lgkmcnt(2)\nM v[24:27],v[204:207],a[80:83],v[24:27]\nM v[28:31],v[204:207],a[84:87],v[28:31]\nbuffer_store_dwordx4 v[148:151],v240,s[52:55],s74 offen offset:1024 sc1\nM v[88:91],v[204:207],a[88:91],v[88:91]\ns_waitcnt vmcnt(24) lgkmcnt(0)\n" \
"s_barrier\nM v[92:95],v[204:207],a[92:95],v[92:95]\nds_read_b64_tr_b16 v[192:193],v241 offset:49152\nds_read_b64_tr_b16 v[194:195],v241 offset:49408\nM v[32:35],v[208:211],a[80:83],v[32:35]\nds_read_b64_tr_b16 v[196:197],v242 offset:49152\nds_read_b64_tr_b16 v[198:199],v242 offset:49408\nM v[36:39],v[208:211],a[84:87],v[36:39]\n" \
"ds_read_b64_tr_b16 v[200:201],v241 offset:49664\nds_read_b64_tr_b16 v[202:203],v241 offset:49920\nM v[96:99],v[208:211],a[88:91],v[96:99]\nds_read_b64_tr_b16 v[204:205],v242 offset:49664\nds_read_b64_tr_b16 v[206:207],v242 offset:49920\nM v[100:103],v[208:211],a[92:95],v[100:103]\nds_read_b64_tr_b16 v[208:209],v241 offset:50176\nds_read_b64_tr_b16 v[210:211],v241 offset:50432\n" \
"M v[40:43],v[212:215],a[80:83],v[40:43]\nbuffer_load_dwordx4 v243,s[40:43],s69 offen offset:0 lds\nM v[44:47],v[212:215],a[84:87],v[44:47]\nM v[104:107],v[212:215],a[88:91],v[104:107]\nM v[108:111],v[212:215],a[92:95],v[108:111]\nds_read_b64_tr_b16 v[212:213],v242 offset:50176\nds_read_b64_tr_b16 v[214:215],v242 offset:50432\nM v[48:51],v[216:219],a[80:83],v[48:51]\n" \
"buffer_load_dwordx4 v244,s[40:43],s69 offen offset:1024 lds\nM v[52:55],v[216:219],a[84:87],v[52:55]\nM v[112:115],v[216:219],a[88:91],v[112:115]\nM v[116:119],v[216:219],a[92:95],v[116:119]\nds_read_b64_tr_b16 v[216:217],v241 offset:50688\nds_read_b64_tr_b16 v[218:219],v241 offset:50944\nM v[56:59],v[220:223],a[80:83],v[56:59]\nbuffer_load_dwordx4 v245,s[40:43],s69 offen offset:2048 lds\n" \
"M v[60:63],v[220:223],a[84:87],v[60:63]\nM v[120:123],v[220:223],a[88:91],v[120:123]\nM v[124:127],v[220:223],a[92:95],v[124:127]\nds_read_b64_tr_b16 v[220:221],v242 offset:50688\ns_waitcnt lgkmcnt(8)\nM v[0:3],v[192:195],a[96:99],v[0:3]\nbuffer_load_dwordx4 v246,s[40:43],s69 offen offset:3072 lds\nM v[4:7],v[192:195],a[100:103],v[4:7]\n" \
"ds_read_b64_tr_b16 v[222:223],v242 offset:50944\ns_add_u32 m0,s64,131072\nM v[64:67],v[192:195],a[104:107],v[64:67]\ns_add_u32 s69,s68,3145728\nM v[68:71],v[192:195],a[108:111],v[68:71]\nds_read_b64_tr_b16 v[192:193],v241 offset:57344\nds_read_b64_tr_b16 v[194:195],v241 offset:57600\nM v[8:11],v[196:199],a[96:99],v[8:11]\n" \
"buffer_load_dwordx4 a[180:183],v240,s[52:55],s76 offen offset:3072 sc1\nM v[12:15],v[196:199],a[100:103],v[12:15]\ns_add_u32 s76,s76,4096\nM v[72:75],v[196:199],a[104:107],v[72:75]\nM v[76:79],v[196:199],a[108:111],v[76:79]\nds_read_b64_tr_b16 v[196:197],v242 offset:57344\nds_read_b64_tr_b16 v[198:199],v242 offset:57600\nM v[16:19],v[200:203],a[96:99],v[16:19]\n" \
"buffer_store_dwordx4 v[152:155],v240,s[52:55],s74 offen offset:2048 sc1\nM v[20:23],v[200:203],a[100:103],v[20:23]\nM v[80:83],v[200:203],a[104:107],v[80:83]\nM v[84:87],v[200:203],a[108:111],v[84:87]\nds_read_b64_tr_b16 v[200:201],v241 offset:57856\ns_waitcnt lgkmcnt(6)\nM v[24:27],v[204:207],a[96:99],v[24:27]\nbuffer_load_dwordx4 a[184:187],v240,s[52:55],s77 offen offset:2048 sc1\n" \
"M v[28:31],v[204:207],a[100:103],v[28:31]\nds_read_b64_tr_b16 v[202:203],v241 offset:58112\nM v[88:91],v[204:207],a[104:107],v[88:91]\nM v[92:95],v[204:207],a[108:111],v[92:95]\nds_read_b64_tr_b16 v[204:205],v242 offset:57856\nds_read_b64_tr_b16 v[206:207],v242 offset:58112\nM v[32:35],v[208:211],a[96:99],v[32:35]\nbuffer_load_dwordx4 a[188:191],v240,s[52:55],s77 offen offset:3072 sc1\n" \
"M v[36:39],v[208:211],a[100:103],v[36:39]\ns_add_u32 s77,s77,4096\nM v[96:99],v[208:211],a[104:107],v[96:99]\nM v[100:103],v[208:211],a[108:111],v[100:103]\nds_read_b64_tr_b16 v[208:209],v241 offset:58368\nds_read_b64_tr_b16 v[210:211],v241 offset:58624\nM v[40:43],v[212:215],a[96:99],v[40:43]\nbuffer_load_dwordx4 a[192:195],v240,s[52:55],s76 offen offset:0 sc1\n" \
"M v[44:47],v[212:215],a[100:103],v[44:47]\nM v[104:107],v[212:215],a[104:107],v[104:107]\nM v[108:111],v[212:215],a[108:111],v[108:111]\nds_read_b64_tr_b16 v[212:213],v242 offset:58368\nds_read_b64_tr_b16 v[214:215],v242 offset:58624\nM v[48:51],v[216:219],a[96:99],v[48:51]\nbuffer_load_dwordx4 a[196:199],v240,s[52:55],s76 offen offset:1024 sc1\nM v[52:55],v[216:219],a[100:103],v[52:55]\n" \
"M v[112:115],v[216:219],a[104:107],v[112:115]\nM v[116:119],v[216:219],a[108:111],v[116:119]\nds_read_b64_tr_b16 v[216:217],v241 offset:58880\ns_waitcnt lgkmcnt(7)\nM v[56:59],v[220:223],a[96:99],v[56:59]\nbuffer_load_dwordx4 a[200:203],v240,s[52:55],s77 offen offset:0 sc1\nM v[60:63],v[220:223],a[100:103],v[60:63]\nds_read_b64_tr_b16 v[218:219],v241 offset:59136\n" \
"M v[120:123],v[220:223],a[104:107],v[120:123]\nM v[124:127],v[220:223],a[108:111],v[124:127]\nds_read_b64_tr_b16 v[220:221],v242 offset:58880\nds_read_b64_tr_b16 v[222:223],v242 offset:59136\nM v[0:3],v[192:195],a[112:115],v[0:3]\ns_waitcnt vmcnt(27)\nM v[4:7],v[192:195],a[116:119],v[4:7]\nbuffer_load_dwordx4 a[204:207],v240,s[52:55],s77 offen offset:1024 sc1\n" \
"M v[64:67],v[192:195],a[120:123],v[64:67]\nM v[68:71],v[192:195],a[124:127],v[68:71]\nM v[8:11],v[196:199],a[112:115],v[8:11]\nM v[12:15],v[196:199],a[116:119],v[12:15]\nbuffer_load_dwordx4 a[208:211],v240,s[52:55],s76 offen offset:2048 sc1\nM v[72:75],v[196:199],a[120:123],v[72:75]\nM v[76:79],v[196:199],a[124:127],v[76:79]\nM v[16:19],v[200:203],a[112:115],v[16:19]\n" \
"M v[20:23],v[200:203],a[116:119],v[20:23]\nbuffer_store_dwordx4 v[156:159],v240,s[52:55],s74 offen offset:3072 sc1\nM v[80:83],v[200:203],a[120:123],v[80:83]\ns_add_u32 s74,s74,4096\nM v[84:87],v[200:203],a[124:127],v[84:87]\ns_waitcnt lgkmcnt(3)\nM v[24:27],v[204:207],a[112:115],v[24:27]\nM v[28:31],v[204:207],a[116:119],v[28:31]\n" \
"buffer_load_dwordx4 v243,s[40:43],s69 offen offset:0 lds\nM v[88:91],v[204:207],a[120:123],v[88:91]\ns_waitcnt vmcnt(24) lgkmcnt(0)\ns_barrier\nM v[92:95],v[204:207],a[124:127],v[92:95]\nds_read_b64_tr_b16 v[192:193],v241 offset:0\nds_read_b64_tr_b16 v[194:195],v241 offset:256\nM v[32:35],v[208:211],a[112:115],v[32:35]\n" \
"ds_read_b64_tr_b16 v[196:197],v242 offset:0\nds_read_b64_tr_b16 v[198:199],v242 offset:256\nM v[36:39],v[208:211],a[116:119],v[36:39]\nds_read_b64_tr_b16 v[200:201],v241 offset:512\nds_read_b64_tr_b16 v[202:203],v241 offset:768\nM v[96:99],v[208:211],a[120:123],v[96:99]\nds_read_b64_tr_b16 v[204:205],v242 offset:512\nds_read_b64_tr_b16 v[206:207],v242 offset:768\n" \
"M v[100:103],v[208:211],a[124:127],v[100:103]\nds_read_b64_tr_b16 v[208:209],v241 offset:1024\nds_read_b64_tr_b16 v[210:211],v241 offset:1280\nM v[40:43],v[212:215],a[112:115],v[40:43]\nbuffer_load_dwordx4 v244,s[40:43],s69 offen offset:1024 lds\nM v[44:47],v[212:215],a[116:119],v[44:47]\nM v[104:107],v[212:215],a[120:123],v[104:107]\nM v[108:111],v[212:215],a[124:127],v[108:111]\n" \
"ds_read_b64_tr_b16 v[212:213],v242 offset:1024\nds_read_b64_tr_b16 v[214:215],v242 offset:1280\nM v[48:51],v[216:219],a[112:115],v[48:51]\nbuffer_load_dwordx4 v245,s[40:43],s69 offen offset:2048 lds\nM v[52:55],v[216:219],a[116:119],v[52:55]\nM v[112:115],v[216:219],a[120:123],v[112:115]\nM v[116:119],v[216:219],a[124:127],v[116:119]\nds_read_b64_tr_b16 v[216:217],v241 offset:1536\n" \
"ds_read_b64_tr_b16 v[218:219],v241 offset:1792\nM v[56:59],v[220:223],a[112:115],v[56:59]\nbuffer_load_dwordx4 v246,s[40:43],s69 offen offset:3072 lds\nM v[60:63],v[220:223],a[116:119],v[60:63]\ns_add_u32 m0,s64,147456\ns_add_u32 s69,s68,3670016\nM v[120:123],v[220:223],a[120:123],v[120:123]\ns_lshl_b32 s68,s60,19\n" \
"s_lshl2_add_u32 s68,s64,s68\nM v[124:127],v[220:223],a[124:127],v[124:127]\nds_read_b64_tr_b16 v[220:221],v242 offset:1536\ns_waitcnt lgkmcnt(6)\nM v[0:3],v[192:195],a[128:131],v[0:3]\nbuffer_load_dwordx4 a[212:215],v240,s[52:55],s76 offen offset:3072 sc1\nM v[4:7],v[192:195],a[132:135],v[4:7]\nds_read_b64_tr_b16 v[222:223],v242 offset:1792\n" \
"s_add_u32 s76,s76,4096\nM v[64:67],v[192:195],a[136:139],v[64:67]\nM v[68:71],v[192:195],a[140:143],v[68:71]\nds_read_b64_tr_b16 v[192:193],v241 offset:8192\nds_read_b64_tr_b16 v[194:195],v241 offset:8448\nM v[8:11],v[196:199],a[128:131],v[8:11]\nbuffer_store_dwordx4 v[160:163],v240,s[52:55],s74 offen offset:0 sc1\nM v[12:15],v[196:199],a[132:135],v[12:15]\n" \
"M v[72:75],v[196:199],a[136:139],v[72:75]\nM v[76:79],v[196:199],a[140:143],v[76:79]\nds_read_b64_tr_b16 v[196:197],v242 offset:8192\nds_read_b64_tr_b16 v[198:199],v242 offset:8448\nM v[16:19],v[200:203],a[128:131],v[16:19]\nbuffer_load_dwordx4 a[216:219],v240,s[52:55],s77 offen offset:2048 sc1\nM v[20:23],v[200:203],a[132:135],v[20:23]\nM v[80:83],v[200:203],a[136:139],v[80:83]\n" \
"M v[84:87],v[200:203],a[140:143],v[84:87]\nds_read_b64_tr_b16 v[200:201],v241 offset:8704\nds_read_b64_tr_b16 v[202:203],v241 offset:8960\nM v[24:27],v[204:207],a[128:131],v[24:27]\nbuffer_load_dwordx4 a[220:223],v240,s[52:55],s77 offen offset:3072 sc1\nM v[28:31],v[204:207],a[132:135],v[28:31]\ns_add_u32 s77,s77,4096\nM v[88:91],v[204:207],a[136:139],v[88:91]\n" \
"M v[92:95],v[204:207],a[140:143],v[92:95]\nds_read_b64_tr_b16 v[204:205],v242 offset:8704\ns_waitcnt lgkmcnt(7)\nM v[32:35],v[208:211],a[128:131],v[32:35]\nbuffer_load_dwordx4 a[224:227],v240,s[52:55],s76 offen offset:0 sc1\nM v[36:39],v[208:211],a[132:135],v[36:39]\nds_read_b64_tr_b16 v[206:207],v242 offset:8960\nM v[96:99],v[208:211],a[136:139],v[96:99]\n" \
"M v[100:103],v[208:211],a[140:143],v[100:103]\nds_read_b64_tr_b16 v[208:209],v241 offset:9216\nds_read_b64_tr_b16 v[210:211],v241 offset:9472\nM v[40:43],v[212:215],a[128:131],v[40:43]\nbuffer_load_dword v248,v228,s[52:55],s80 offen sc1\nM v[44:47],v[212:215],a[132:135],v[44:47]\nbuffer_load_dwordx4 a[228:231],v240,s[52:55],s76 offen offset:1024 sc1\nM v[104:107],v[212:215],a[136:139],v[104:107]\n" \
"ds_read_b128 a[0:3],v240 offset:0\nM v[108:111],v[212:215],a[140:143],v[108:111]\nds_read_b64_tr_b16 v[212:213],v242 offset:9216\nds_read_b64_tr_b16 v[214:215],v242 offset:9472\nM v[48:51],v[216:219],a[128:131],v[48:51]\nM v[52:55],v[216:219],a[132:135],v[52:55]\nbuffer_load_dwordx4 a[232:235],v240,s[52:55],s77 offen offset:0 sc1\nM v[112:115],v[216:219],a[136:139],v[112:115]\n" \
"ds_read_b128 a[4:7],v240 offset:1024\nM v[116:119],v[216:219],a[140:143],v[116:119]\nds_read_b64_tr_b16 v[216:217],v241 offset:9728\nds_read_b64_tr_b16 v[218:219],v241 offset:9984\nM v[56:59],v[220:223],a[128:131],v[56:59]\nM v[60:63],v[220:223],a[132:135],v[60:63]\nbuffer_load_dwordx4 a[236:239],v240,s[52:55],s77 offen offset:1024 sc1\nM v[120:123],v[220:223],a[136:139],v[120:123]\n" \
"M v[124:127],v[220:223],a[140:143],v[124:127]\nds_read_b64_tr_b16 v[220:221],v242 offset:9728\ns_waitcnt lgkmcnt(8)\nM v[0:3],v[192:195],a[144:147],v[0:3]\nds_read_b64_tr_b16 v[222:223],v242 offset:9984\nds_read_b128 a[8:11],v240 offset:2048\nM v[4:7],v[192:195],a[148:151],v[4:7]\ns_waitcnt vmcnt(28)\n" \
"M v[64:67],v[192:195],a[152:155],v[64:67]\nbuffer_store_dwordx4 v[164:167],v240,s[52:55],s74 offen offset:1024 sc1\nM v[68:71],v[192:195],a[156:159],v[68:71]\nM v[8:11],v[196:199],a[144:147],v[8:11]\nM v[12:15],v[196:199],a[148:151],v[12:15]\nM v[72:75],v[196:199],a[152:155],v[72:75]\nbuffer_load_dwordx4 a[240:243],v240,s[52:55],s76 offen offset:2048 sc1\nM v[76:79],v[196:199],a[156:159],v[76:79]\n" \
"ds_read_b128 a[12:15],v240 offset:3072\nM v[16:19],v[200:203],a[144:147],v[16:19]\nM v[20:23],v[200:203],a[148:151],v[20:23]\nM v[80:83],v[200:203],a[152:155],v[80:83]\nbuffer_load_dwordx4 v243,s[40:43],s69 offen offset:0 lds\nM v[84:87],v[200:203],a[156:159],v[84:87]\nv_add_u32 v243,4096,v230\nM v[24:27],v[204:207],a[144:147],v[24:27]\n" \
"ds_read_b128 a[16:19],v240 offset:4096\nM v[28:31],v[204:207],a[148:151],v[28:31]\nM v[88:91],v[204:207],a[152:155],v[88:91]\ns_waitcnt vmcnt(25) lgkmcnt(3)\ns_barrier\nM v[92:95],v[204:207],a[156:159],v[92:95]\nds_read_b64_tr_b16 v[192:193],v241 offset:16384\nds_read_b64_tr_b16 v[194:195],v241 offset:16640\n" \
"M v[32:35],v[208:211],a[144:147],v[32:35]\nds_read_b64_tr_b16 v[196:197],v242 offset:16384\nds_read_b64_tr_b16 v[198:199],v242 offset:16640\nM v[36:39],v[208:211],a[148:151],v[36:39]\nds_read_b64_tr_b16 v[200:201],v241 offset:16896\nds_read_b64_tr_b16 v[202:203],v241 offset:17152\nM v[96:99],v[208:211],a[152:155],v[96:99]\nds_read_b64_tr_b16 v[204:205],v242 offset:16896\n" \
"ds_read_b64_tr_b16 v[206:207],v242 offset:17152\nM v[100:103],v[208:211],a[156:159],v[100:103]\nds_read_b64_tr_b16 v[208:209],v241 offset:17408\nds_read_b64_tr_b16 v[210:211],v241 offset:17664\nM v[40:43],v[212:215],a[144:147],v[40:43]\nbuffer_load_dwordx4 v244,s[40:43],s69 offen offset:1024 lds\nM v[44:47],v[212:215],a[148:151],v[44:47]\nv_add_u32 v244,3200,v230\n" \
"ds_read_b128 a[20:23],v240 offset:5120\nM v[104:107],v[212:215],a[152:155],v[104:107]\nM v[108:111],v[212:215],a[156:159],v[108:111]\nds_read_b64_tr_b16 v[212:213],v242 offset:17408\nds_read_b64_tr_b16 v[214:215],v242 offset:17664\nM v[48:51],v[216:219],a[144:147],v[48:51]\nbuffer_load_dwordx4 v245,s[40:43],s69 offen offset:2048 lds\nM v[52:55],v[216:219],a[148:151],v[52:55]\n" \
"v_add_u32 v245,10240,v230\nds_read_b128 a[24:27],v240 offset:6144\nM v[112:115],v[216:219],a[152:155],v[112:115]\nM v[116:119],v[216:219],a[156:159],v[116:119]\nds_read_b64_tr_b16 v[216:217],v241 offset:17920\nds_read_b64_tr_b16 v[218:219],v241 offset:18176\nM v[56:59],v[220:223],a[144:147],v[56:59]\nbuffer_load_dwordx4 v246,s[40:43],s69 offen offset:3072 lds\n" \
"M v[60:63],v[220:223],a[148:151],v[60:63]\nv_add_u32 v246,9344,v230\nv_mov_b32 v230,0x3f803f80\nM v[120:123],v[220:223],a[152:155],v[120:123]\ns_sub_u32 s40,s18,0x1000\ns_subb_u32 s41,s19,0\nM v[124:127],v[220:223],a[156:159],v[124:127]\nds_read_b64_tr_b16 v[220:221],v242 offset:17920\n" \
"s_waitcnt lgkmcnt(7)\nM v[0:3],v[192:195],a[160:163],v[0:3]\nbuffer_store_dwordx4 v[168:171],v240,s[52:55],s74 offen offset:2048 sc1\nM v[4:7],v[192:195],a[164:167],v[4:7]\nds_read_b64_tr_b16 v[222:223],v242 offset:18176\nds_read_b128 a[28:31],v240 offset:7168\nM v[64:67],v[192:195],a[168:171],v[64:67]\nM v[68:71],v[192:195],a[172:175],v[68:71]\n" \
"ds_read_b64_tr_b16 v[192:193],v241 offset:24576\nds_read_b64_tr_b16 v[194:195],v241 offset:24832\nM v[8:11],v[196:199],a[160:163],v[8:11]\nbuffer_load_dwordx4 a[244:247],v240,s[52:55],s76 offen offset:3072 sc1\nM v[12:15],v[196:199],a[164:167],v[12:15]\nds_read_b128 a[32:35],v240 offset:8192\nM v[72:75],v[196:199],a[168:171],v[72:75]\nM v[76:79],v[196:199],a[172:175],v[76:79]\n" \
"ds_read_b64_tr_b16 v[196:197],v242 offset:24576\nds_read_b64_tr_b16 v[198:199],v242 offset:24832\nM v[16:19],v[200:203],a[160:163],v[16:19]\nbuffer_load_dwordx4 a[248:251],v240,s[52:55],s77 offen offset:2048 sc1\nM v[20:23],v[200:203],a[164:167],v[20:23]\nM v[80:83],v[200:203],a[168:171],v[80:83]\nds_read_b128 a[36:39],v240 offset:9216\nM v[84:87],v[200:203],a[172:175],v[84:87]\n" \
"ds_read_b64_tr_b16 v[200:201],v241 offset:25088\nds_read_b64_tr_b16 v[202:203],v241 offset:25344\nM v[24:27],v[204:207],a[160:163],v[24:27]\nbuffer_load_dwordx4 a[252:255],v240,s[52:55],s77 offen offset:3072 sc1\nM v[28:31],v[204:207],a[164:167],v[28:31]\nM v[88:91],v[204:207],a[168:171],v[88:91]\nM v[92:95],v[204:207],a[172:175],v[92:95]\nds_read_b64_tr_b16 v[204:205],v242 offset:25088\n" \
"ds_read_b64_tr_b16 v[206:207],v242 offset:25344\nM v[32:35],v[208:211],a[160:163],v[32:35]\ns_waitcnt vmcnt(13)\nv_cmp_eq_u32 vcc,0,v248\nM v[36:39],v[208:211],a[164:167],v[36:39]\ns_cbranch_vccnz .Lps1\n.Lpb1:\ns_add_u32 s80,s80,4096\n" \
"M v[96:99],v[208:211],a[168:171],v[96:99]\nds_read_b128 a[40:43],v240 offset:10240\nM v[100:103],v[208:211],a[172:175],v[100:103]\nds_read_b64_tr_b16 v[208:209],v241 offset:25600\ns_waitcnt lgkmcnt(8)\nM v[40:43],v[212:215],a[160:163],v[40:43]\nds_read_b64_tr_b16 v[210:211],v241 offset:25856\nM v[44:47],v[212:215],a[164:167],v[44:47]\n" \
"M v[104:107],v[212:215],a[168:171],v[104:107]\nds_read_b128 a[44:47],v240 offset:11264\nM v[108:111],v[212:215],a[172:175],v[108:111]\nds_read_b64_tr_b16 v[212:213],v242 offset:25600\nds_read_b64_tr_b16 v[214:215],v242 offset:25856\nM v[48:51],v[216:219],a[160:163],v[48:51]\nM v[52:55],v[216:219],a[164:167],v[52:55]\nM v[112:115],v[216:219],a[168:171],v[112:115]\n" \
"M v[116:119],v[216:219],a[172:175],v[116:119]\nds_read_b64_tr_b16 v[216:217],v241 offset:26112\nds_read_b64_tr_b16 v[218:219],v241 offset:26368\nM v[56:59],v[220:223],a[160:163],v[56:59]\nds_read_b128 a[48:51],v240 offset:12288\nM v[60:63],v[220:223],a[164:167],v[60:63]\nM v[120:123],v[220:223],a[168:171],v[120:123]\nbuffer_store_dwordx4 v[172:175],v240,s[52:55],s74 offen offset:3072 sc1\n" \
"M v[124:127],v[220:223],a[172:175],v[124:127]\nds_read_b64_tr_b16 v[220:221],v242 offset:26112\nds_read_b64_tr_b16 v[222:223],v242 offset:26368\nM v[0:3],v[192:195],a[176:179],v[0:3]\ns_add_u32 s74,s74,4096\nM v[4:7],v[192:195],a[180:183],v[4:7]\nM v[64:67],v[192:195],a[184:187],v[64:67]\nds_read_b128 a[52:55],v240 offset:13312\n" \
"M v[68:71],v[192:195],a[188:191],v[68:71]\ns_waitcnt lgkmcnt(9)\nM v[8:11],v[196:199],a[176:179],v[8:11]\nM v[12:15],v[196:199],a[180:183],v[12:15]\nM v[72:75],v[196:199],a[184:187],v[72:75]\nM v[76:79],v[196:199],a[188:191],v[76:79]\nM v[16:19],v[200:203],a[176:179],v[16:19]\nds_read_b128 a[56:59],v240 offset:14336\n" \
"M v[20:23],v[200:203],a[180:183],v[20:23]\nM v[80:83],v[200:203],a[184:187],v[80:83]\nM v[84:87],v[200:203],a[188:191],v[84:87]\nM v[24:27],v[204:207],a[176:179],v[24:27]\nM v[28:31],v[204:207],a[180:183],v[28:31]\nM v[88:91],v[204:207],a[184:187],v[88:91]\ns_waitcnt lgkmcnt(2)\ns_barrier\n" \
"M v[92:95],v[204:207],a[188:191],v[92:95]\nds_read_b64_tr_b16 v[192:193],v241 offset:32768\nds_read_b64_tr_b16 v[194:195],v241 offset:33024\nM v[32:35],v[208:211],a[176:179],v[32:35]\nds_read_b64_tr_b16 v[196:197],v242 offset:32768\nds_read_b64_tr_b16 v[198:199],v242 offset:33024\nM v[36:39],v[208:211],a[180:183],v[36:39]\nds_read_b64_tr_b16 v[200:201],v241 offset:33280\n" \
"ds_read_b64_tr_b16 v[202:203],v241 offset:33536\nM v[96:99],v[208:211],a[184:187],v[96:99]\nds_read_b64_tr_b16 v[204:205],v242 offset:33280\nds_read_b64_tr_b16 v[206:207],v242 offset:33536\nM v[100:103],v[208:211],a[188:191],v[100:103]\nds_read_b64_tr_b16 v[208:209],v241 offset:33792\nds_read_b64_tr_b16 v[210:211],v241 offset:34048\nM v[40:43],v[212:215],a[176:179],v[40:43]\n" \
"ds_write_b64 v250,v[128:129] offset:0\nds_read_b128 a[60:63],v240 offset:15360\nM v[44:47],v[212:215],a[180:183],v[44:47]\nds_write_b64 v250,v[130:131] offset:8\nM v[104:107],v[212:215],a[184:187],v[104:107]\nds_write_b64 v250,v[132:133] offset:1024\nM v[108:111],v[212:215],a[188:191],v[108:111]\nds_read_b64_tr_b16 v[212:213],v242 offset:33792\n" \
"ds_read_b64_tr_b16 v[214:215],v242 offset:34048\nM v[48:51],v[216:219],a[176:179],v[48:51]\nM v[52:55],v[216:219],a[180:183],v[52:55]\nds_write_b64 v250,v[134:135] offset:1032\nM v[112:115],v[216:219],a[184:187],v[112:115]\nM v[116:119],v[216:219],a[188:191],v[116:119]\nds_read_b64_tr_b16 v[216:217],v241 offset:34304\nds_read_b64_tr_b16 v[218:219],v241 offset:34560\n" \
"M v[56:59],v[220:223],a[176:179],v[56:59]\nbuffer_store_dwordx4 v[176:179],v240,s[52:55],s74 offen offset:0 sc1\nM v[60:63],v[220:223],a[180:183],v[60:63]\nds_write_b64 v250,v[136:137] offset:2048\nM v[120:123],v[220:223],a[184:187],v[120:123]\nM v[124:127],v[220:223],a[188:191],v[124:127]\nds_read_b64_tr_b16 v[220:221],v242 offset:34304\ns_waitcnt lgkmcnt(13)\n" \
"M v[0:3],v[192:195],a[192:195],v[0:3]\nds_write_b64 v250,v[138:139] offset:2056\nds_read_b64_tr_b16 v[222:223],v242 offset:34560\nM v[4:7],v[192:195],a[196:199],v[4:7]\nM v[64:67],v[192:195],a[200:203],v[64:67]\nds_write_b64 v250,v[140:141] offset:3072\nM v[68:71],v[192:195],a[204:207],v[68:71]\nds_read_b64_tr_b16 v[192:193],v241 offset:40960\n" \
"ds_read_b64_tr_b16 v[194:195],v241 offset:41216\nM v[8:11],v[196:199],a[192:195],v[8:11]\nM v[12:15],v[196:199],a[196:199],v[12:15]\nds_write_b64 v250,v[142:143] offset:3080\nM v[72:75],v[196:199],a[200:203],v[72:75]\nM v[76:79],v[196:199],a[204:207],v[76:79]\nds_read_b64_tr_b16 v[196:197],v242 offset:40960\nds_read_b64_tr_b16 v[198:199],v242 offset:41216\n" \
"M v[16:19],v[200:203],a[192:195],v[16:19]\nds_write_b64 v250,v[144:145] offset:4096\nM v[20:23],v[200:203],a[196:199],v[20:23]\nM v[80:83],v[200:203],a[200:203],v[80:83]\nM v[84:87],v[200:203],a[204:207],v[84:87]\nds_read_b64_tr_b16 v[200:201],v241 offset:41472\nds_read_b64_tr_b16 v[202:203],v241 offset:41728\nM v[24:27],v[204:207],a[192:195],v[24:27]\n" \
"ds_write_b64 v250,v[146:147] offset:4104\nM v[28:31],v[204:207],a[196:199],v[28:31]\nM v[88:91],v[204:207],a[200:203],v[88:91]\nds_write_b64 v250,v[148:149] offset:5120\nM v[92:95],v[204:207],a[204:207],v[92:95]\nds_read_b64_tr_b16 v[204:205],v242 offset:41472\ns_waitcnt lgkmcnt(12)\nM v[32:35],v[208:211],a[192:195],v[32:35]\n" \
"ds_read_b64_tr_b16 v[206:207],v242 offset:41728\nM v[36:39],v[208:211],a[196:199],v[36:39]\nds_write_b64 v250,v[150:151] offset:5128\nM v[96:99],v[208:211],a[200:203],v[96:99]\nM v[100:103],v[208:211],a[204:207],v[100:103]\nds_read_b64_tr_b16 v[208:209],v241 offset:41984\nds_read_b64_tr_b16 v[210:211],v241 offset:42240\nM v[40:43],v[212:215],a[192:195],v[40:43]\n" \
"ds_write_b64 v250,v[152:153] offset:6144\nM v[44:47],v[212:215],a[196:199],v[44:47]\nM v[104:107],v[212:215],a[200:203],v[104:107]\nM v[108:111],v[212:215],a[204:207],v[108:111]\nds_read_b64_tr_b16 v[212:213],v242 offset:41984\nds_read_b64_tr_b16 v[214:215],v242 offset:42240\nM v[48:51],v[216:219],a[192:195],v[48:51]\nds_write_b64 v250,v[154:155] offset:6152\n" \
"M v[52:55],v[216:219],a[196:199],v[52:55]\nM v[112:115],v[216:219],a[200:203],v[112:115]\nbuffer_store_dwordx4 v[180:183],v240,s[52:55],s74 offen offset:1024 sc1\nM v[116:119],v[216:219],a[204:207],v[116:119]\nds_read_b64_tr_b16 v[216:217],v241 offset:42496\nds_read_b64_tr_b16 v[218:219],v241 offset:42752\nM v[56:59],v[220:223],a[192:195],v[56:59]\nds_write_b64 v250,v[156:157] offset:7168\n" \
"M v[60:63],v[220:223],a[196:199],v[60:63]\nds_write_b64 v250,v[158:159] offset:7176\nM v[120:123],v[220:223],a[200:203],v[120:123]\nM v[124:127],v[220:223],a[204:207],v[124:127]\nds_read_b64_tr_b16 v[220:221],v242 offset:42496\ns_waitcnt lgkmcnt(11)\nM v[0:3],v[192:195],a[208:211],v[0:3]\nglobal_load_dword v247,v229,s[18:19]\n" \
"M v[4:7],v[192:195],a[212:215],v[4:7]\nds_read_b64_tr_b16 v[222:223],v242 offset:42752\nv_lshl_add_u32 v229,v249,7,s70\nM v[64:67],v[192:195],a[216:219],v[64:67]\nds_write_b64 v250,v[160:161] offset:8192\ns_lshl_b32 s70,s61,8\nM v[68:71],v[192:195],a[220:223],v[68:71]\nds_write_b64 v250,v[162:163] offset:8200\n" \
"s_add_u32 s68,s68,s70\nM v[8:11],v[196:199],a[208:211],v[8:11]\nM v[12:15],v[196:199],a[212:215],v[12:15]\nM v[72:75],v[196:199],a[216:219],v[72:75]\nds_write_b64 v250,v[164:165] offset:9216\nM v[76:79],v[196:199],a[220:223],v[76:79]\nM v[16:19],v[200:203],a[208:211],v[16:19]\nM v[20:23],v[200:203],a[212:215],v[20:23]\n" \
"ds_write_b64 v250,v[166:167] offset:9224\nM v[80:83],v[200:203],a[216:219],v[80:83]\nM v[84:87],v[200:203],a[220:223],v[84:87]\nM v[24:27],v[204:207],a[208:211],v[24:27]\nds_write_b64 v250,v[168:169] offset:10240\nM v[28:31],v[204:207],a[212:215],v[28:31]\nM v[88:91],v[204:207],a[216:219],v[88:91]\ns_waitcnt vmcnt(8) lgkmcnt(5)\n" \
"s_barrier\nM v[92:95],v[204:207],a[220:223],v[92:95]\nds_read_b64_tr_b16 v[192:193],v241 offset:49152\nds_read_b64_tr_b16 v[194:195],v241 offset:49408\nM v[32:35],v[208:211],a[208:211],v[32:35]\nds_read_b64_tr_b16 v[196:197],v242 offset:49152\nds_read_b64_tr_b16 v[198:199],v242 offset:49408\nM v[36:39],v[208:211],a[212:215],v[36:39]\n" \
"ds_read_b64_tr_b16 v[200:201],v241 offset:49664\nds_read_b64_tr_b16 v[202:203],v241 offset:49920\nM v[96:99],v[208:211],a[216:219],v[96:99]\nds_read_b64_tr_b16 v[204:205],v242 offset:49664\nds_read_b64_tr_b16 v[206:207],v242 offset:49920\nM v[100:103],v[208:211],a[220:223],v[100:103]\nds_read_b64_tr_b16 v[208:209],v241 offset:50176\nds_read_b64_tr_b16 v[210:211],v241 offset:50432\n" \
"M v[40:43],v[212:215],a[208:211],v[40:43]\nds_write_b64 v250,v[170:171] offset:10248\nM v[44:47],v[212:215],a[212:215],v[44:47]\nds_write_b64 v250,v[172:173] offset:11264\nM v[104:107],v[212:215],a[216:219],v[104:107]\nds_write_b64 v250,v[174:175] offset:11272\nM v[108:111],v[212:215],a[220:223],v[108:111]\nds_read_b64_tr_b16 v[212:213],v242 offset:50176\n" \
"ds_read_b64_tr_b16 v[214:215],v242 offset:50432\nM v[48:51],v[216:219],a[208:211],v[48:51]\nbuffer_store_dwordx4 v[184:187],v240,s[52:55],s74 offen offset:2048 sc1\nM v[52:55],v[216:219],a[212:215],v[52:55]\nds_write_b64 v250,v[176:177] offset:12288\nM v[112:115],v[216:219],a[216:219],v[112:115]\nM v[116:119],v[216:219],a[220:223],v[116:119]\nds_read_b64_tr_b16 v[216:217],v241 offset:50688\n" \
"ds_read_b64_tr_b16 v[218:219],v241 offset:50944\nM v[56:59],v[220:223],a[208:211],v[56:59]\nds_write_b64 v250,v[178:179] offset:12296\nM v[60:63],v[220:223],a[212:215],v[60:63]\nM v[120:123],v[220:223],a[216:219],v[120:123]\nds_write_b64 v250,v[180:181] offset:13312\nM v[124:127],v[220:223],a[220:223],v[124:127]\nds_read_b64_tr_b16 v[220:221],v242 offset:50688\n" \
"s_waitcnt lgkmcnt(13)\nM v[0:3],v[192:195],a[224:227],v[0:3]\nds_read_b64_tr_b16 v[222:223],v242 offset:50944\nM v[4:7],v[192:195],a[228:231],v[4:7]\nds_write_b64 v250,v[182:183] offset:13320\nM v[64:67],v[192:195],a[232:235],v[64:67]\nM v[68:71],v[192:195],a[236:239],v[68:71]\nds_read_b64_tr_b16 v[192:193],v241 offset:57344\n" \
"ds_read_b64_tr_b16 v[194:195],v241 offset:57600\nM v[8:11],v[196:199],a[224:227],v[8:11]\nds_write_b64 v250,v[184:185] offset:14336\nM v[12:15],v[196:199],a[228:231],v[12:15]\nM v[72:75],v[196:199],a[232:235],v[72:75]\nM v[76:79],v[196:199],a[236:239],v[76:79]\nds_read_b64_tr_b16 v[196:197],v242 offset:57344\nds_read_b64_tr_b16 v[198:199],v242 offset:57600\n" \
"M v[16:19],v[200:203],a[224:227],v[16:19]\nds_write_b64 v250,v[186:187] offset:14344\nM v[20:23],v[200:203],a[228:231],v[20:23]\nM v[80:83],v[200:203],a[232:235],v[80:83]\nds_write_b64 v250,v[188:189] offset:15360\nM v[84:87],v[200:203],a[236:239],v[84:87]\nds_read_b64_tr_b16 v[200:201],v241 offset:57856\nds_read_b64_tr_b16 v[202:203],v241 offset:58112\n" \
"M v[24:27],v[204:207],a[224:227],v[24:27]\nM v[28:31],v[204:207],a[228:231],v[28:31]\nds_write_b64 v250,v[190:191] offset:15368\nv_add_u32 v250,114688,v240\nM v[88:91],v[204:207],a[232:235],v[88:91]\nM v[92:95],v[204:207],a[236:239],v[92:95]\nds_read_b64_tr_b16 v[204:205],v242 offset:57856\ns_waitcnt lgkmcnt(11)\n" \
"M v[32:35],v[208:211],a[224:227],v[32:35]\nds_read_b64_tr_b16 v[206:207],v242 offset:58112\nM v[36:39],v[208:211],a[228:231],v[36:39]\nM v[96:99],v[208:211],a[232:235],v[96:99]\nM v[100:103],v[208:211],a[236:239],v[100:103]\nds_read_b64_tr_b16 v[208:209],v241 offset:58368\nds_read_b64_tr_b16 v[210:211],v241 offset:58624\nM v[40:43],v[212:215],a[224:227],v[40:43]\n" \
"M v[44:47],v[212:215],a[228:231],v[44:47]\nM v[104:107],v[212:215],a[232:235],v[104:107]\nbuffer_store_dwordx4 v[188:191],v240,s[52:55],s74 offen offset:3072 sc1\nM v[108:111],v[212:215],a[236:239],v[108:111]\nds_read_b64_tr_b16 v[212:213],v242 offset:58368\nds_read_b64_tr_b16 v[214:215],v242 offset:58624\nM v[48:51],v[216:219],a[224:227],v[48:51]\nM v[52:55],v[216:219],a[228:231],v[52:55]\n" \
"M v[112:115],v[216:219],a[232:235],v[112:115]\nM v[116:119],v[216:219],a[236:239],v[116:119]\nds_read_b64_tr_b16 v[216:217],v241 offset:58880\nds_read_b64_tr_b16 v[218:219],v241 offset:59136\nM v[56:59],v[220:223],a[224:227],v[56:59]\nglobal_load_dword v247,v229,s[92:93]\nM v[60:63],v[220:223],a[228:231],v[60:63]\nv_and_b32 v229,3,v249\n" \
"v_cmp_eq_u32 vcc,0,v229\nM v[120:123],v[220:223],a[232:235],v[120:123]\nM v[124:127],v[220:223],a[236:239],v[124:127]\nds_read_b64_tr_b16 v[220:221],v242 offset:58880\nds_read_b64_tr_b16 v[222:223],v242 offset:59136\ns_waitcnt lgkmcnt(8)\nM v[0:3],v[192:195],a[240:243],v[0:3]\nv_cndmask_b32 v229,0,v230,vcc\n" \
"v_and_b32 v230,0xff,v241\nM v[8:11],v[196:199],a[240:243],v[8:11]\nv_lshl_or_b32 v230,v231,4,v230\nv_and_b32 v231,32,v249\nM v[16:19],v[200:203],a[240:243],v[16:19]\nv_accvgpr_write_b32 a208,v229\nv_accvgpr_write_b32 a209,v229\nM v[24:27],v[204:207],a[240:243],v[24:27]\n" \
"v_cvt_pk_bf16_f32 v0,v0,v1\ns_waitcnt lgkmcnt(4)\nM v[32:35],v[208:211],a[240:243],v[32:35]\nv_cvt_pk_bf16_f32 v1,v2,v3\nv_cvt_pk_bf16_f32 v2,v8,v9\nM v[40:43],v[212:215],a[240:243],v[40:43]\nv_cvt_pk_bf16_f32 v16,v16,v17\ns_waitcnt lgkmcnt(2)\n" \
"M v[48:51],v[216:219],a[240:243],v[48:51]\nv_cvt_pk_bf16_f32 v17,v18,v19\ns_waitcnt lgkmcnt(0)\nM v[56:59],v[220:223],a[240:243],v[56:59]\nv_cvt_pk_bf16_f32 v32,v32,v33\ns_waitcnt vmcnt(7)\nM v[4:7],v[192:195],a[244:247],v[4:7]\nv_cvt_pk_bf16_f32 v33,v34,v35\n" \
"v_cvt_pk_bf16_f32 v3,v10,v11\nM v[12:15],v[196:199],a[244:247],v[12:15]\nv_cvt_pk_bf16_f32 v48,v48,v49\nv_cvt_pk_bf16_f32 v49,v50,v51\nM v[20:23],v[200:203],a[244:247],v[20:23]\nv_cvt_pk_bf16_f32 v18,v24,v25\nv_cvt_pk_bf16_f32 v19,v26,v27\nM v[28:31],v[204:207],a[244:247],v[28:31]\n" \
"v_cvt_pk_bf16_f32 v4,v4,v5\nv_cvt_pk_bf16_f32 v5,v6,v7\nM v[36:39],v[208:211],a[244:247],v[36:39]\nv_cvt_pk_bf16_f32 v34,v40,v41\nv_cvt_pk_bf16_f32 v35,v42,v43\nM v[44:47],v[212:215],a[244:247],v[44:47]\nv_cvt_pk_bf16_f32 v20,v20,v21\nv_cvt_pk_bf16_f32 v21,v22,v23\n" \
"M v[52:55],v[216:219],a[244:247],v[52:55]\nv_cvt_pk_bf16_f32 v50,v56,v57\nv_cvt_pk_bf16_f32 v51,v58,v59\nM v[60:63],v[220:223],a[244:247],v[60:63]\nv_cvt_pk_bf16_f32 v36,v36,v37\nv_cvt_pk_bf16_f32 v37,v38,v39\nM v[64:67],v[192:195],a[248:251],v[64:67]\nv_cvt_pk_bf16_f32 v6,v12,v13\n" \
"v_cvt_pk_bf16_f32 v7,v14,v15\nM v[72:75],v[196:199],a[248:251],v[72:75]\nv_cvt_pk_bf16_f32 v52,v52,v53\nv_cvt_pk_bf16_f32 v53,v54,v55\nM v[80:83],v[200:203],a[248:251],v[80:83]\nv_cvt_pk_bf16_f32 v22,v28,v29\nv_cvt_pk_bf16_f32 v23,v30,v31\nM v[88:91],v[204:207],a[248:251],v[88:91]\n" \
"v_cvt_pk_bf16_f32 v64,v64,v65\nv_cvt_pk_bf16_f32 v65,v66,v67\nM v[96:99],v[208:211],a[248:251],v[96:99]\nv_cvt_pk_bf16_f32 v38,v44,v45\nv_cvt_pk_bf16_f32 v39,v46,v47\nM v[104:107],v[212:215],a[248:251],v[104:107]\nv_cvt_pk_bf16_f32 v80,v80,v81\nv_cvt_pk_bf16_f32 v81,v82,v83\n" \
"M v[112:115],v[216:219],a[248:251],v[112:115]\nv_cvt_pk_bf16_f32 v54,v60,v61\nv_cvt_pk_bf16_f32 v55,v62,v63\nM v[120:123],v[220:223],a[248:251],v[120:123]\nv_cvt_pk_bf16_f32 v96,v96,v97\nv_cvt_pk_bf16_f32 v97,v98,v99\nM v[68:71],v[192:195],a[252:255],v[68:71]\nv_cvt_pk_bf16_f32 v66,v72,v73\n" \
"v_cvt_pk_bf16_f32 v67,v74,v75\nM v[76:79],v[196:199],a[252:255],v[76:79]\nv_cvt_pk_bf16_f32 v112,v112,v113\nv_cvt_pk_bf16_f32 v113,v114,v115\nM v[84:87],v[200:203],a[252:255],v[84:87]\nv_cvt_pk_bf16_f32 v82,v88,v89\nv_cvt_pk_bf16_f32 v83,v90,v91\nM v[92:95],v[204:207],a[252:255],v[92:95]\n" \
"v_cvt_pk_bf16_f32 v68,v68,v69\nv_cvt_pk_bf16_f32 v69,v70,v71\nM v[100:103],v[208:211],a[252:255],v[100:103]\nv_cvt_pk_bf16_f32 v98,v104,v105\nv_cvt_pk_bf16_f32 v99,v106,v107\nM v[108:111],v[212:215],a[252:255],v[108:111]\nv_cvt_pk_bf16_f32 v84,v84,v85\nv_cvt_pk_bf16_f32 v85,v86,v87\n" \
"M v[116:119],v[216:219],a[252:255],v[116:119]\nv_cvt_pk_bf16_f32 v114,v120,v121\nv_cvt_pk_bf16_f32 v115,v122,v123\nv_cvt_pk_bf16_f32 v100,v100,v101\nv_cvt_pk_bf16_f32 v101,v102,v103\nM v[124:127],v[220:223],a[252:255],v[124:127]\nv_cvt_pk_bf16_f32 v70,v76,v77\nv_cvt_pk_bf16_f32 v71,v78,v79\n" \
"v_cvt_pk_bf16_f32 v86,v92,v93\nv_cvt_pk_bf16_f32 v116,v116,v117\nv_cvt_pk_bf16_f32 v117,v118,v119\nv_cvt_pk_bf16_f32 v87,v94,v95\nv_cvt_pk_bf16_f32 v102,v108,v109\nv_cvt_pk_bf16_f32 v103,v110,v111\nv_cvt_pk_bf16_f32 v118,v124,v125\nv_cvt_pk_bf16_f32 v119,v126,v127\n" \
"s_waitcnt vmcnt(1)\nbuffer_store_dword v254,off,s[52:55],s29 sc1\nv_accvgpr_write_b32 a210,v229\nv_accvgpr_write_b32 a211,v229\nv_accvgpr_write_b32 a244,0\nv_accvgpr_write_b32 a248,-1\nv_accvgpr_write_b32 a245,0\nv_accvgpr_write_b32 a249,-1\n" \
"v_accvgpr_write_b32 a246,0\nv_accvgpr_write_b32 a250,-1\nv_accvgpr_write_b32 a247,0\nv_accvgpr_write_b32 a251,-1\nv_lshl_or_b32 v242,v231,6,v230\nv_add_u32 v241,65536,v240\nv_accvgpr_write_b32 a128,v0\nv_accvgpr_write_b32 a129,v1\n" \
"v_accvgpr_write_b32 a130,v2\nv_accvgpr_write_b32 a131,v3\nv_accvgpr_write_b32 a132,v16\nv_accvgpr_write_b32 a133,v17\nv_accvgpr_write_b32 a134,v18\nv_accvgpr_write_b32 a135,v19\nv_accvgpr_write_b32 a136,v32\nv_accvgpr_write_b32 a137,v33\n" \
"v_accvgpr_write_b32 a138,v34\nv_accvgpr_write_b32 a139,v35\nv_accvgpr_write_b32 a140,v48\nv_accvgpr_write_b32 a141,v49\nv_accvgpr_write_b32 a142,v50\nv_accvgpr_write_b32 a143,v51\nv_accvgpr_write_b32 a144,v4\nv_accvgpr_write_b32 a145,v5\n" \
"v_accvgpr_write_b32 a146,v6\nv_accvgpr_write_b32 a147,v7\nv_accvgpr_write_b32 a148,v20\nv_accvgpr_write_b32 a149,v21\nv_accvgpr_write_b32 a150,v22\nv_accvgpr_write_b32 a151,v23\nv_accvgpr_write_b32 a152,v36\nv_accvgpr_write_b32 a153,v37\n" \
"v_accvgpr_write_b32 a154,v38\nv_accvgpr_write_b32 a155,v39\nv_accvgpr_write_b32 a156,v52\nv_accvgpr_write_b32 a157,v53\nv_accvgpr_write_b32 a158,v54\nv_accvgpr_write_b32 a159,v55\nv_accvgpr_write_b32 a160,v64\nv_accvgpr_write_b32 a161,v65\n" \
"v_accvgpr_write_b32 a162,v66\nv_accvgpr_write_b32 a163,v67\nv_accvgpr_write_b32 a164,v80\nv_accvgpr_write_b32 a165,v81\nv_accvgpr_write_b32 a166,v82\nv_accvgpr_write_b32 a167,v83\nv_accvgpr_write_b32 a168,v96\nv_accvgpr_write_b32 a169,v97\n" \
"v_accvgpr_write_b32 a170,v98\nv_accvgpr_write_b32 a171,v99\nv_accvgpr_write_b32 a172,v112\nv_accvgpr_write_b32 a173,v113\nv_accvgpr_write_b32 a174,v114\nv_accvgpr_write_b32 a175,v115\nv_accvgpr_write_b32 a176,v68\nv_accvgpr_write_b32 a177,v69\n" \
"v_accvgpr_write_b32 a178,v70\nv_accvgpr_write_b32 a179,v71\nv_accvgpr_write_b32 a180,v84\nv_accvgpr_write_b32 a181,v85\nv_accvgpr_write_b32 a182,v86\nv_accvgpr_write_b32 a183,v87\nv_accvgpr_write_b32 a184,v100\nv_accvgpr_write_b32 a185,v101\n" \
"v_accvgpr_write_b32 a186,v102\nv_accvgpr_write_b32 a187,v103\nv_accvgpr_write_b32 a188,v116\nv_accvgpr_write_b32 a189,v117\nv_accvgpr_write_b32 a190,v118\nv_accvgpr_write_b32 a191,v119\ns_waitcnt lgkmcnt(0)\n.Lattn:\n" \
"s_cmp_lt_u32 s7,2\ns_cbranch_scc0 .Lp23\ns_cmp_eq_u32 s7,0\ns_cbranch_scc0 .Lpro1\n.Lpro0:\ns_barrier\nM v[64:67],a[0:3],a[128:131],0\nds_read_b128 a[32:35],v240 offset:40960\n" \
"ds_read_b128 a[36:39],v240 offset:41984\nM v[64:67],a[4:7],a[132:135],v[64:67]\nds_read_b128 a[40:43],v240 offset:43008\ns_mov_b32 m0,s64\nv_mov_b32 v218,0\nM v[64:67],a[8:11],a[136:139],v[64:67]\nds_read_b128 a[44:47],v240 offset:44032\nv_mov_b32 v219,0\n" \
"M v[64:67],a[12:15],a[140:143],v[64:67]\nds_read_b128 a[48:51],v240 offset:45056\nbuffer_load_dwordx4 v240,s[44:47],s36 offen offset:0 sc1 lds\nM v[68:71],a[0:3],a[144:147],0\nds_read_b128 a[0:3],v240 offset:32768\nM v[68:71],a[4:7],a[148:151],v[68:71]\nds_read_b128 a[4:7],v240 offset:33792\nM v[68:71],a[8:11],a[152:155],v[68:71]\n" \
"ds_read_b128 a[8:11],v240 offset:34816\nv_max3_f32 v225,v64,v65,v66\nv_max_f32 v225,v225,v67\nM v[68:71],a[12:15],a[156:159],v[68:71]\nds_read_b128 a[12:15],v240 offset:35840\nv_cndmask_b32 v225,v224,v225,s[90:91]\nv_cndmask_b32 v64,v64,v252,s[82:83]\nM v[76:79],a[16:19],a[144:147],0\n" \
"ds_read_b128 a[16:19],v240 offset:36864\nv_cndmask_b32 v65,v65,v252,s[84:85]\nM v[76:79],a[20:23],a[148:151],v[76:79]\nds_read_b128 a[20:23],v240 offset:37888\nv_max3_f32 v229,v68,v69,v70\nv_cndmask_b32 v66,v66,v252,s[86:87]\nM v[76:79],a[24:27],a[152:155],v[76:79]\nds_read_b128 a[24:27],v240 offset:38912\n" \
"v_cndmask_b32 v67,v67,v252,s[88:89]\ns_lshl4_add_u32 s69,s13,s68\nM v[76:79],a[28:31],a[156:159],v[76:79]\nds_read_b128 a[28:31],v240 offset:39936\ns_waitcnt lgkmcnt(7)\nM v[136:139],a[0:3],a[160:163],0\nbuffer_load_dwordx4 v240,s[44:47],s36 offen offset:1024 sc1 lds\ns_waitcnt lgkmcnt(6)\n" \
"M v[136:139],a[4:7],a[164:167],v[136:139]\nds_read_b128 a[52:55],v240 offset:46080\ns_waitcnt lgkmcnt(6)\nM v[136:139],a[8:11],a[168:171],v[136:139]\nv_max3_f32 v229,v229,v71,v76\nv_max3_f32 v229,v229,v77,v78\ns_waitcnt lgkmcnt(5)\nM v[136:139],a[12:15],a[172:175],v[136:139]\n" \
"v_max_f32 v229,v229,v79\nv_cndmask_b32 v229,v228,v229,s[90:91]\nds_read_b128 a[56:59],v240 offset:47104\nM v[140:143],a[0:3],a[176:179],0\nv_permlane16_swap_b32 v225,v229\nv_max_f32 v225,v225,v229\nM v[140:143],a[4:7],a[180:183],v[140:143]\nv_mov_b32 v229,v225\n" \
"ds_read_b128 a[60:63],v240 offset:48128\nv_max3_f32 v233,v136,v137,v138\nM v[140:143],a[8:11],a[184:187],v[140:143]\nv_permlane32_swap_b32 v225,v229\nv_max_f32 v225,v225,v229\nv_mov_b32 v229,v225\nM v[140:143],a[12:15],a[188:191],v[140:143]\nds_read_b128 a[64:67],v241 offset:0\n" \
"s_waitcnt lgkmcnt(5)\nM v[152:155],a[16:19],a[160:163],0\nv_permlane16_swap_b32 v225,v229\nv_sub_f32 v224,0,v225\nv_sub_f32 v226,0,v225\nM v[152:155],a[20:23],a[164:167],v[152:155]\nv_sub_f32 v227,0,v225\nv_sub_f32 v225,0,v225\n" \
"v_sub_f32 v228,0,v229\nM v[152:155],a[24:27],a[168:171],v[152:155]\nv_sub_f32 v230,0,v229\ns_waitcnt lgkmcnt(3)\nM v[152:155],a[28:31],a[172:175],v[152:155]\nv_sub_f32 v231,0,v229\nv_sub_f32 v229,0,v229\nv_max3_f32 v237,v140,v141,v142\n" \
"M v[156:159],a[16:19],a[176:179],0\nds_read_b128 a[68:71],v241 offset:1024\nds_read_b128 a[72:75],v241 offset:2048\nM v[156:159],a[20:23],a[180:183],v[156:159]\nds_read_b128 a[76:79],v241 offset:3072\nv_max3_f32 v233,v233,v139,v152\nv_max3_f32 v233,v233,v153,v154\nM v[156:159],a[24:27],a[184:187],v[156:159]\n" \
"v_max_f32 v233,v233,v155\nv_cndmask_b32 v233,v232,v233,s[90:91]\nv_add_f32 v64,v64,v224\nM v[156:159],a[28:31],a[188:191],v[156:159]\nv_add_f32 v65,v65,v224\nv_add_f32 v66,v66,v224\nM v[128:131],a[0:3],a[128:131],v[224:227]\nv_add_f32 v67,v67,v224\n" \
"v_exp_f32 v64,v64\nM v[128:131],a[4:7],a[132:135],v[128:131]\nv_exp_f32 v65,v65\nv_add_f32 v68,v68,v228\nM v[128:131],a[8:11],a[136:139],v[128:131]\nv_max3_f32 v237,v237,v143,v156\nv_max3_f32 v237,v237,v157,v158\nM v[128:131],a[12:15],a[140:143],v[128:131]\n" \
"v_max_f32 v237,v237,v159\nv_cndmask_b32 v237,v236,v237,s[90:91]\nv_add_f32 v69,v69,v228\nM v[132:135],a[0:3],a[144:147],v[228:231]\nv_permlane16_swap_b32 v233,v237\nv_max_f32 v233,v233,v237\nv_mov_b32 v237,v233\nM v[132:135],a[4:7],a[148:151],v[132:135]\n" \
"v_exp_f32 v66,v66\nM v[132:135],a[8:11],a[152:155],v[132:135]\nv_permlane32_swap_b32 v233,v237\nv_max_f32 v233,v233,v237\nv_mov_b32 v237,v233\nM v[132:135],a[12:15],a[156:159],v[132:135]\nv_exp_f32 v67,v67\nM v[144:147],a[16:19],a[128:131],v[224:227]\n" \
"v_permlane16_swap_b32 v233,v237\nv_sub_f32 v232,0,v233\nv_sub_f32 v234,0,v233\nM v[144:147],a[20:23],a[132:135],v[144:147]\nv_sub_f32 v235,0,v233\nv_sub_f32 v233,0,v233\nv_sub_f32 v236,0,v237\nM v[144:147],a[24:27],a[136:139],v[144:147]\n" \
"v_sub_f32 v238,0,v237\nv_sub_f32 v239,0,v237\nM v[144:147],a[28:31],a[140:143],v[144:147]\nv_sub_f32 v237,0,v237\nv_cndmask_b32 v76,v76,v252,s[82:83]\nv_cndmask_b32 v77,v77,v252,s[84:85]\nM v[148:151],a[16:19],a[144:147],v[228:231]\nv_cndmask_b32 v78,v78,v252,s[86:87]\n" \
"v_cndmask_b32 v79,v79,v252,s[88:89]\nv_add_f32 v70,v70,v228\nM v[148:151],a[20:23],a[148:151],v[148:151]\nv_add_f32 v71,v71,v228\nv_cvt_pk_bf16_f32 v64,v64,v65\nM v[148:151],a[24:27],a[152:155],v[148:151]\nv_exp_f32 v68,v68\nds_read_b128 a[80:83],v241 offset:4096\n" \
"M v[148:151],a[28:31],a[156:159],v[148:151]\nv_exp_f32 v69,v69\nv_add_f32 v76,v76,v228\nM v[160:163],a[32:35],a[128:131],v[224:227]\nv_exp_f32 v70,v70\nM v[160:163],a[36:39],a[132:135],v[160:163]\nv_exp_f32 v71,v71\nv_add_f32 v77,v77,v228\n" \
"M v[160:163],a[40:43],a[136:139],v[160:163]\nv_add_f32 v78,v78,v228\nv_add_f32 v79,v79,v228\nM v[160:163],a[44:47],a[140:143],v[160:163]\nv_cvt_pk_bf16_f32 v65,v66,v67\nv_exp_f32 v76,v76\nM v[164:167],a[32:35],a[144:147],v[228:231]\nv_exp_f32 v77,v77\n" \
"v_mov_b32 v66,0\nM v[164:167],a[36:39],a[148:151],v[164:167]\nv_exp_f32 v78,v78\nM v[164:167],a[40:43],a[152:155],v[164:167]\nv_exp_f32 v79,v79\nv_mov_b32 v67,0\nM v[164:167],a[44:47],a[156:159],v[164:167]\nv_cvt_pk_bf16_f32 v68,v68,v69\n" \
"v_cvt_pk_bf16_f32 v69,v70,v71\nv_cvt_pk_bf16_f32 v70,v76,v77\nM v[176:179],a[48:51],a[128:131],v[224:227]\nv_cvt_pk_bf16_f32 v71,v78,v79\nds_read_b128 a[84:87],v241 offset:5120\nM v[176:179],a[52:55],a[132:135],v[176:179]\nbuffer_load_dwordx4 v240,s[44:47],s36 offen offset:2048 sc1 lds\ns_waitcnt lgkmcnt(1)\n" \
"M v[176:179],a[56:59],a[136:139],v[176:179]\nds_read_b128 a[88:91],v241 offset:6144\nds_read_b128 a[92:95],v241 offset:7168\nv_add_f32 v136,v136,v232\nM v[176:179],a[60:63],a[140:143],v[176:179]\nds_read_b128 a[0:3],v240 offset:49152\nv_add_f32 v137,v137,v232\nM v[180:183],a[48:51],a[144:147],v[228:231]\n" \
"ds_read_b128 a[4:7],v240 offset:50176\nv_exp_f32 v128,v128\nM v[180:183],a[52:55],a[148:151],v[180:183]\nbuffer_load_dwordx4 v240,s[44:47],s36 offen offset:3072 sc1 lds\nM v[180:183],a[56:59],a[152:155],v[180:183]\nds_read_b128 a[8:11],v240 offset:51200\nds_read_b128 a[12:15],v240 offset:52224\nv_add_f32 v138,v138,v232\n" \
"M v[180:183],a[60:63],a[156:159],v[180:183]\nds_read_b128 a[16:19],v240 offset:53248\nv_exp_f32 v129,v129\nM v[168:171],a[32:35],a[160:163],v[232:235]\nv_exp_f32 v130,v130\nM v[168:171],a[36:39],a[164:167],v[168:171]\nv_exp_f32 v131,v131\nds_read_b128 a[20:23],v240 offset:54272\n" \
"M v[168:171],a[40:43],a[168:171],v[168:171]\nv_cvt_pk_bf16_f32 v192,v128,v129\nv_cvt_pk_bf16_f32 v193,v130,v131\nds_read_b128 a[24:27],v240 offset:55296\nM v[168:171],a[44:47],a[172:175],v[168:171]\nds_read_b128 a[28:31],v240 offset:56320\nv_add_f32 v139,v139,v232\nM v[172:175],a[32:35],a[176:179],v[236:239]\n" \
"v_exp_f32 v132,v132\nv_add_f32 v140,v140,v236\nM v[172:175],a[36:39],a[180:183],v[172:175]\nv_exp_f32 v133,v133\nv_add_f32 v141,v141,v236\nM v[172:175],a[40:43],a[184:187],v[172:175]\nv_exp_f32 v134,v134\nM v[172:175],a[44:47],a[188:191],v[172:175]\n" \
"v_exp_f32 v135,v135\nv_cvt_pk_bf16_f32 v196,v132,v133\nM v[188:191],a[48:51],a[176:179],v[236:239]\nv_cvt_pk_bf16_f32 v197,v134,v135\nv_exp_f32 v144,v144\nM v[188:191],a[52:55],a[180:183],v[188:191]\nv_exp_f32 v145,v145\nM v[188:191],a[56:59],a[184:187],v[188:191]\n" \
"v_exp_f32 v146,v146\nv_cvt_pk_bf16_f32 v194,v144,v145\nM v[188:191],a[60:63],a[188:191],v[188:191]\nv_exp_f32 v147,v147\nM v[0:3],a[64:67],v[64:67],0\nv_cvt_pk_bf16_f32 v195,v146,v147\nv_exp_f32 v148,v148\nM v[4:7],a[64:67],v[68:71],0\n" \
"v_exp_f32 v149,v149\nv_add_f32 v142,v142,v236\nM v[8:11],a[68:71],v[64:67],0\nbuffer_load_dword v248,v251,s[52:55],s80 offen sc1\nM v[12:15],a[68:71],v[68:71],0\nv_exp_f32 v150,v150\nv_cvt_pk_bf16_f32 v198,v148,v149\nM v[16:19],a[72:75],v[64:67],0\n" \
"v_exp_f32 v151,v151\nv_add_f32 v143,v143,v236\nM v[20:23],a[72:75],v[68:71],0\nv_cvt_pk_bf16_f32 v199,v150,v151\nds_read_b128 a[32:35],v240 offset:57344\nM v[24:27],a[76:79],v[64:67],0\nv_exp_f32 v136,v136\nv_add_f32 v152,v152,v232\n" \
"M v[28:31],a[76:79],v[68:71],0\nv_exp_f32 v137,v137\nv_add_f32 v153,v153,v232\nM v[32:35],a[80:83],v[64:67],0\nv_exp_f32 v138,v138\nM v[36:39],a[80:83],v[68:71],0\nv_exp_f32 v139,v139\ns_waitcnt lgkmcnt(1)\n" \
"M v[40:43],a[84:87],v[64:67],0\nv_cvt_pk_bf16_f32 v200,v136,v137\nv_cvt_pk_bf16_f32 v201,v138,v139\nM v[44:47],a[84:87],v[68:71],0\nv_exp_f32 v140,v140\nv_add_f32 v154,v154,v232\nM v[48:51],a[88:91],v[64:67],0\nv_exp_f32 v141,v141\n" \
"v_add_f32 v155,v155,v232\nM v[52:55],a[88:91],v[68:71],0\nv_exp_f32 v142,v142\nM v[56:59],a[92:95],v[64:67],0\nv_exp_f32 v143,v143\nv_cvt_pk_bf16_f32 v204,v140,v141\nM v[60:63],a[92:95],v[68:71],0\nv_cvt_pk_bf16_f32 v205,v142,v143\n" \
"v_exp_f32 v152,v152\nM a[192:195],a[208:211],v[64:67],0\nv_exp_f32 v153,v153\nM a[196:199],a[208:211],v[68:71],0\nv_exp_f32 v154,v154\nds_read_b128 a[36:39],v240 offset:58368\nM v[128:131],a[0:3],a[128:131],v[224:227]\nv_exp_f32 v155,v155\n" \
"v_cvt_pk_bf16_f32 v202,v152,v153\nM v[128:131],a[4:7],a[132:135],v[128:131]\nv_cvt_pk_bf16_f32 v203,v154,v155\nds_read_b128 a[40:43],v240 offset:59392\nM v[128:131],a[8:11],a[136:139],v[128:131]\nds_read_b128 a[44:47],v240 offset:60416\nv_add_f32 v156,v156,v236\nv_add_f32 v157,v157,v236\n" \
"M v[128:131],a[12:15],a[140:143],v[128:131]\nv_add_f32 v158,v158,v236\nv_add_f32 v159,v159,v236\nM v[132:135],a[0:3],a[144:147],v[228:231]\nv_exp_f32 v156,v156\nds_read_b128 a[48:51],v240 offset:61440\nM v[132:135],a[4:7],a[148:151],v[132:135]\nv_exp_f32 v157,v157\n" \
"ds_read_b128 a[52:55],v240 offset:62464\nM v[132:135],a[8:11],a[152:155],v[132:135]\nv_exp_f32 v158,v158\nM v[132:135],a[12:15],a[156:159],v[132:135]\nv_exp_f32 v159,v159\nv_cvt_pk_bf16_f32 v206,v156,v157\nM v[144:147],a[16:19],a[128:131],v[224:227]\nv_cvt_pk_bf16_f32 v207,v158,v159\n" \
"v_exp_f32 v160,v160\nM v[144:147],a[20:23],a[132:135],v[144:147]\nv_exp_f32 v161,v161\nM v[144:147],a[24:27],a[136:139],v[144:147]\nv_exp_f32 v162,v162\nv_cvt_pk_bf16_f32 v208,v160,v161\nM v[144:147],a[28:31],a[140:143],v[144:147]\nv_exp_f32 v163,v163\n" \
"ds_read_b128 a[56:59],v240 offset:63488\nM v[148:151],a[16:19],a[144:147],v[228:231]\nv_cvt_pk_bf16_f32 v209,v162,v163\nds_read_b128 a[60:63],v240 offset:64512\nM v[148:151],a[20:23],a[148:151],v[148:151]\nv_exp_f32 v164,v164\nv_cndmask_b32 v168,v168,v252,s[82:83]\nM v[148:151],a[24:27],a[152:155],v[148:151]\n" \
"v_exp_f32 v165,v165\nM v[148:151],a[28:31],a[156:159],v[148:151]\nv_exp_f32 v166,v166\nv_cvt_pk_bf16_f32 v212,v164,v165\nM v[136:139],a[0:3],a[160:163],v[232:235]\nv_exp_f32 v167,v167\nv_cndmask_b32 v169,v169,v252,s[84:85]\nM v[136:139],a[4:7],a[164:167],v[136:139]\n" \
"v_cvt_pk_bf16_f32 v213,v166,v167\nv_cndmask_b32 v170,v170,v252,s[86:87]\nM v[136:139],a[8:11],a[168:171],v[136:139]\nv_exp_f32 v176,v176\nv_cndmask_b32 v171,v171,v252,s[88:89]\nM v[136:139],a[12:15],a[172:175],v[136:139]\nv_exp_f32 v177,v177\nds_read_b128 a[64:67],v241 offset:32768\n" \
"M v[140:143],a[0:3],a[176:179],v[236:239]\nv_exp_f32 v178,v178\nM v[140:143],a[4:7],a[180:183],v[140:143]\nv_exp_f32 v179,v179\nv_cvt_pk_bf16_f32 v210,v176,v177\nM v[140:143],a[8:11],a[184:187],v[140:143]\nv_cvt_pk_bf16_f32 v211,v178,v179\nv_exp_f32 v180,v180\n" \
"M v[140:143],a[12:15],a[188:191],v[140:143]\nv_exp_f32 v181,v181\nM v[152:155],a[16:19],a[160:163],v[232:235]\nv_exp_f32 v182,v182\nv_cvt_pk_bf16_f32 v214,v180,v181\nM v[152:155],a[20:23],a[164:167],v[152:155]\nv_exp_f32 v183,v183\nM v[152:155],a[24:27],a[168:171],v[152:155]\n" \
"v_cvt_pk_bf16_f32 v215,v182,v183\nv_exp_f32 v168,v168\nM v[152:155],a[28:31],a[172:175],v[152:155]\nv_exp_f32 v169,v169\nds_read_b128 a[68:71],v241 offset:33792\nM v[156:159],a[16:19],a[176:179],v[236:239]\nv_exp_f32 v170,v170\nM v[156:159],a[20:23],a[180:183],v[156:159]\n" \
"v_exp_f32 v171,v171\nv_cvt_pk_bf16_f32 v216,v168,v169\nM v[156:159],a[24:27],a[184:187],v[156:159]\nv_cvt_pk_bf16_f32 v217,v170,v171\nv_exp_f32 v172,v172\nM v[156:159],a[28:31],a[188:191],v[156:159]\nv_cndmask_b32 v188,v188,v252,s[82:83]\ns_waitcnt lgkmcnt(1)\n" \
"M v[160:163],a[32:35],a[128:131],v[224:227]\nv_exp_f32 v173,v173\nv_cndmask_b32 v189,v189,v252,s[84:85]\nM v[160:163],a[36:39],a[132:135],v[160:163]\nv_exp_f32 v174,v174\nv_cvt_pk_bf16_f32 v220,v172,v173\nM v[160:163],a[40:43],a[136:139],v[160:163]\ns_waitcnt vmcnt(1)\n" \
"s_barrier\nM v[160:163],a[44:47],a[140:143],v[160:163]\nv_exp_f32 v175,v175\nv_cndmask_b32 v190,v190,v252,s[86:87]\nM v[164:167],a[32:35],a[144:147],v[228:231]\nv_cvt_pk_bf16_f32 v221,v174,v175\nv_cndmask_b32 v191,v191,v252,s[88:89]\nds_read_b128 a[72:75],v241 offset:34816\n" \
"M v[164:167],a[36:39],a[148:151],v[164:167]\nv_exp_f32 v188,v188\nM v[164:167],a[40:43],a[152:155],v[164:167]\nv_exp_f32 v189,v189\nds_read_b128 a[76:79],v241 offset:35840\nM v[164:167],a[44:47],a[156:159],v[164:167]\nv_exp_f32 v190,v190\nM v[176:179],a[48:51],a[128:131],v[224:227]\n" \
"v_exp_f32 v191,v191\nv_cvt_pk_bf16_f32 v222,v188,v189\nM v[176:179],a[52:55],a[132:135],v[176:179]\nv_cvt_pk_bf16_f32 v223,v190,v191\nds_read_b128 a[80:83],v241 offset:36864\nds_read_b128 a[84:87],v241 offset:37888\nM v[176:179],a[56:59],a[136:139],v[176:179]\nds_read_b128 a[88:91],v241 offset:38912\n" \
"s_add_u32 m0,s64,131072\nM v[176:179],a[60:63],a[140:143],v[176:179]\nds_read_b128 a[92:95],v241 offset:39936\nbuffer_load_dwordx4 v243,s[40:43],s69 offen offset:0 lds\nM v[180:183],a[48:51],a[144:147],v[228:231]\nds_read_b128 a[96:99],v241 offset:40960\nv_exp_f32 v128,v128\nM v[180:183],a[52:55],a[148:151],v[180:183]\n" \
"ds_read_b128 a[100:103],v241 offset:41984\ns_waitcnt vmcnt(1)\nv_cmp_eq_u32 vcc,0,v248\nM v[180:183],a[56:59],a[152:155],v[180:183]\nds_read_b128 a[104:107],v241 offset:43008\nv_exp_f32 v129,v129\nM v[180:183],a[60:63],a[156:159],v[180:183]\nds_read_b128 a[108:111],v241 offset:44032\n" \
"buffer_load_dwordx4 v244,s[40:43],s69 offen offset:1024 lds\nM v[168:171],a[32:35],a[160:163],v[232:235]\nds_read_b128 a[112:115],v241 offset:45056\ns_cbranch_vccnz .Lps2\n.Lpb2:\nM v[168:171],a[36:39],a[164:167],v[168:171]\nds_read_b128 a[116:119],v241 offset:46080\nv_exp_f32 v130,v130\n" \
"M v[168:171],a[40:43],a[168:171],v[168:171]\nv_exp_f32 v131,v131\nM v[168:171],a[44:47],a[172:175],v[168:171]\nds_read_b128 a[120:123],v241 offset:47104\nbuffer_load_dwordx4 v245,s[40:43],s69 offen offset:2048 lds\nM v[172:175],a[32:35],a[176:179],v[236:239]\nds_read_b128 a[124:127],v241 offset:48128\nv_exp_f32 v132,v132\n" \
"M v[172:175],a[36:39],a[180:183],v[172:175]\nds_read_b128 a[0:3],v240 offset:0\nds_read_b128 a[4:7],v240 offset:1024\nM v[172:175],a[40:43],a[184:187],v[172:175]\nds_read_b128 a[8:11],v240 offset:2048\nv_exp_f32 v133,v133\nM v[172:175],a[44:47],a[188:191],v[172:175]\nbuffer_load_dwordx4 v246,s[40:43],s69 offen offset:3072 lds\n" \
"ds_read_b128 a[12:15],v240 offset:3072\nM v[184:187],a[48:51],a[160:163],v[232:235]\ns_lshl4_add_u32 s69,s13,s68\nds_read_b128 a[16:19],v240 offset:4096\nM v[184:187],a[52:55],a[164:167],v[184:187]\ns_add_u32 s69,s69,0x10000\ns_add_u32 m0,s64,147456\nds_read_b128 a[20:23],v240 offset:5120\n" \
"M v[184:187],a[56:59],a[168:171],v[184:187]\nds_read_b128 a[24:27],v240 offset:6144\nv_exp_f32 v134,v134\nM v[184:187],a[60:63],a[172:175],v[184:187]\nbuffer_load_dwordx4 v243,s[40:43],s69 offen offset:0 lds\nM v[188:191],a[48:51],a[176:179],v[236:239]\nds_read_b128 a[28:31],v240 offset:7168\nv_exp_f32 v135,v135\n" \
"M v[188:191],a[52:55],a[180:183],v[188:191]\nds_read_b128 a[32:35],v240 offset:8192\nds_read_b128 a[36:39],v240 offset:9216\nM v[188:191],a[56:59],a[184:187],v[188:191]\nds_read_b128 a[40:43],v240 offset:10240\nv_exp_f32 v144,v144\nM v[188:191],a[60:63],a[188:191],v[188:191]\nbuffer_load_dwordx4 v244,s[40:43],s69 offen offset:1024 lds\n" \
"ds_read_b128 a[44:47],v240 offset:11264\nM v[0:3],a[64:67],v[192:195],v[0:3]\nv_exp_f32 v145,v145\nM v[4:7],a[64:67],v[196:199],v[4:7]\nds_read_b128 a[48:51],v240 offset:12288\nv_exp_f32 v146,v146\nM v[64:67],a[64:67],v[200:203],0\nds_read_b128 a[52:55],v240 offset:13312\n" \
"v_exp_f32 v147,v147\nM v[68:71],a[64:67],v[204:207],0\nds_read_b128 a[56:59],v240 offset:14336\ns_waitcnt lgkmcnt(8)\nM v[8:11],a[68:71],v[192:195],v[8:11]\nbuffer_load_dwordx4 v245,s[40:43],s69 offen offset:2048 lds\nds_read_b128 a[60:63],v240 offset:15360\nM v[12:15],a[68:71],v[196:199],v[12:15]\n" \
"v_exp_f32 v148,v148\nM v[72:75],a[68:71],v[200:203],0\nv_exp_f32 v149,v149\nM v[76:79],a[68:71],v[204:207],0\nv_exp_f32 v150,v150\nM v[16:19],a[72:75],v[192:195],v[16:19]\nbuffer_load_dwordx4 v246,s[40:43],s69 offen offset:3072 lds\nM v[20:23],a[72:75],v[196:199],v[20:23]\n" \
"s_add_u32 m0,s64,65536\nv_exp_f32 v151,v151\nM v[80:83],a[72:75],v[200:203],0\nv_exp_f32 v136,v136\nM v[84:87],a[72:75],v[204:207],0\nv_exp_f32 v137,v137\nM v[24:27],a[76:79],v[192:195],v[24:27]\nbuffer_load_dwordx4 v240,s[48:51],s36 offen offset:0 sc1 lds\n" \
"M v[28:31],a[76:79],v[196:199],v[28:31]\nv_exp_f32 v138,v138\nM v[88:91],a[76:79],v[200:203],0\nv_exp_f32 v139,v139\nM v[92:95],a[76:79],v[204:207],0\nv_exp_f32 v140,v140\nM v[32:35],a[80:83],v[192:195],v[32:35]\nbuffer_load_dwordx4 v240,s[48:51],s36 offen offset:1024 sc1 lds\n" \
"M v[36:39],a[80:83],v[196:199],v[36:39]\nv_exp_f32 v141,v141\nM v[96:99],a[80:83],v[200:203],0\nv_exp_f32 v142,v142\nM v[100:103],a[80:83],v[204:207],0\nv_exp_f32 v143,v143\nM v[40:43],a[84:87],v[192:195],v[40:43]\nbuffer_load_dwordx4 v240,s[48:51],s36 offen offset:2048 sc1 lds\n" \
"M v[44:47],a[84:87],v[196:199],v[44:47]\nv_exp_f32 v152,v152\nM v[104:107],a[84:87],v[200:203],0\nv_exp_f32 v153,v153\nM v[108:111],a[84:87],v[204:207],0\nv_exp_f32 v154,v154\nM v[48:51],a[88:91],v[192:195],v[48:51]\nbuffer_load_dwordx4 v240,s[48:51],s36 offen offset:3072 sc1 lds\n" \
"s_add_u32 m0,s64,16384\nM v[52:55],a[88:91],v[196:199],v[52:55]\nv_exp_f32 v155,v155\nM v[112:115],a[88:91],v[200:203],0\nv_exp_f32 v156,v156\nM v[116:119],a[88:91],v[204:207],0\nv_exp_f32 v157,v157\nM v[56:59],a[92:95],v[192:195],v[56:59]\n" \
"buffer_load_dwordx4 v240,s[44:47],s65 offen offset:0 sc1 lds\nM v[60:63],a[92:95],v[196:199],v[60:63]\nv_exp_f32 v158,v158\nM v[120:123],a[92:95],v[200:203],0\nv_exp_f32 v159,v159\nM v[124:127],a[92:95],v[204:207],0\nv_exp_f32 v160,v160\nM a[192:195],a[208:211],v[192:195],a[192:195]\n" \
"buffer_load_dwordx4 v240,s[44:47],s65 offen offset:1024 sc1 lds\nv_cvt_pk_bf16_f32 v192,v128,v129\nM a[196:199],a[208:211],v[196:199],a[196:199]\nv_exp_f32 v161,v161\nM a[200:203],a[208:211],v[200:203],0\nv_exp_f32 v162,v162\nv_cvt_pk_bf16_f32 v193,v130,v131\nM a[204:207],a[208:211],v[204:207],0\n" \
"v_exp_f32 v163,v163\nv_cvt_pk_bf16_f32 v194,v144,v145\nM v[0:3],a[96:99],v[208:211],v[0:3]\nbuffer_load_dwordx4 v240,s[44:47],s65 offen offset:2048 sc1 lds\nM v[4:7],a[96:99],v[212:215],v[4:7]\nv_exp_f32 v164,v164\nv_cvt_pk_bf16_f32 v195,v146,v147\nM v[64:67],a[96:99],v[216:219],v[64:67]\n" \
"v_exp_f32 v165,v165\nv_cvt_pk_bf16_f32 v196,v132,v133\nM v[68:71],a[96:99],v[220:223],v[68:71]\nv_exp_f32 v166,v166\nM v[8:11],a[100:103],v[208:211],v[8:11]\nbuffer_load_dwordx4 v240,s[44:47],s65 offen offset:3072 sc1 lds\ns_add_u32 m0,s64,81920\nM v[12:15],a[100:103],v[212:215],v[12:15]\n" \
"v_exp_f32 v167,v167\nv_cvt_pk_bf16_f32 v197,v134,v135\nM v[72:75],a[100:103],v[216:219],v[72:75]\ns_barrier\nv_cvt_pk_bf16_f32 v198,v148,v149\nM v[76:79],a[100:103],v[220:223],v[76:79]\nv_exp_f32 v176,v176\nv_cvt_pk_bf16_f32 v199,v150,v151\n" \
"M v[16:19],a[104:107],v[208:211],v[16:19]\nbuffer_load_dwordx4 v240,s[48:51],s65 offen offset:0 sc1 lds\nM v[20:23],a[104:107],v[212:215],v[20:23]\nv_exp_f32 v177,v177\nv_cvt_pk_bf16_f32 v200,v136,v137\nM v[80:83],a[104:107],v[216:219],v[80:83]\nv_exp_f32 v178,v178\nv_cvt_pk_bf16_f32 v201,v138,v139\n" \
"M v[84:87],a[104:107],v[220:223],v[84:87]\nv_exp_f32 v179,v179\nM v[24:27],a[108:111],v[208:211],v[24:27]\nbuffer_load_dwordx4 v240,s[48:51],s65 offen offset:1024 sc1 lds\nv_cvt_pk_bf16_f32 v202,v152,v153\nM v[28:31],a[108:111],v[212:215],v[28:31]\nv_exp_f32 v180,v180\nv_cvt_pk_bf16_f32 v203,v154,v155\n" \
"M v[88:91],a[108:111],v[216:219],v[88:91]\nv_exp_f32 v181,v181\nM v[92:95],a[108:111],v[220:223],v[92:95]\nv_exp_f32 v182,v182\nv_cvt_pk_bf16_f32 v204,v140,v141\nM v[32:35],a[112:115],v[208:211],v[32:35]\nbuffer_load_dwordx4 v240,s[48:51],s65 offen offset:2048 sc1 lds\nv_cvt_pk_bf16_f32 v205,v142,v143\n" \
"M v[36:39],a[112:115],v[212:215],v[36:39]\nv_exp_f32 v183,v183\nM v[96:99],a[112:115],v[216:219],v[96:99]\nv_exp_f32 v168,v168\nv_cvt_pk_bf16_f32 v206,v156,v157\nM v[100:103],a[112:115],v[220:223],v[100:103]\nv_exp_f32 v169,v169\nM v[40:43],a[116:119],v[208:211],v[40:43]\n" \
"buffer_load_dwordx4 v240,s[48:51],s65 offen offset:3072 sc1 lds\ns_add_u32 m0,s64,32768\nM v[44:47],a[116:119],v[212:215],v[44:47]\nv_exp_f32 v170,v170\nv_cvt_pk_bf16_f32 v207,v158,v159\nM v[104:107],a[116:119],v[216:219],v[104:107]\nv_exp_f32 v171,v171\nM v[108:111],a[116:119],v[220:223],v[108:111]\n" \
"M v[48:51],a[120:123],v[208:211],v[48:51]\nbuffer_load_dwordx4 v240,s[44:47],s66 offen offset:0 sc1 lds\nM v[52:55],a[120:123],v[212:215],v[52:55]\nM v[112:115],a[120:123],v[216:219],v[112:115]\nM v[116:119],a[120:123],v[220:223],v[116:119]\nM v[56:59],a[124:127],v[208:211],v[56:59]\nbuffer_load_dwordx4 v240,s[44:47],s66 offen offset:1024 sc1 lds\nM v[60:63],a[124:127],v[212:215],v[60:63]\n" \
"M v[120:123],a[124:127],v[216:219],v[120:123]\nM v[124:127],a[124:127],v[220:223],v[124:127]\nM a[192:195],a[208:211],v[208:211],a[192:195]\nbuffer_load_dwordx4 v240,s[44:47],s66 offen offset:2048 sc1 lds\nM a[196:199],a[208:211],v[212:215],a[196:199]\nbuffer_load_dwordx4 v240,s[44:47],s66 offen offset:3072 sc1 lds\nM a[200:203],a[208:211],v[216:219],a[200:203]\ns_add_u32 m0,s64,98304\n" \
"s_nop 0\nbuffer_load_dwordx4 v240,s[48:51],s66 offen offset:0 sc1 lds\nbuffer_load_dwordx4 v240,s[48:51],s66 offen offset:1024 sc1 lds\nbuffer_load_dwordx4 v240,s[48:51],s66 offen offset:2048 sc1 lds\nbuffer_load_dwordx4 v240,s[48:51],s66 offen offset:3072 sc1 lds\nM a[204:207],a[208:211],v[220:223],a[204:207]\ns_cmp_eq_u32 s62,0\ns_cbranch_scc1 .Ltrans\n" \
"s_branch .Ldual\n.Lpro1:\ns_barrier\nM v[64:67],a[0:3],a[128:131],0\nds_read_b128 a[64:67],v241 offset:0\nds_read_b128 a[68:71],v241 offset:1024\nM v[64:67],a[4:7],a[132:135],v[64:67]\nds_read_b128 a[72:75],v241 offset:2048\n" \
"s_mov_b32 m0,s64\nv_mov_b32 v202,0\nM v[64:67],a[8:11],a[136:139],v[64:67]\nds_read_b128 a[76:79],v241 offset:3072\nbuffer_load_dwordx4 v240,s[44:47],s36 offen offset:0 sc1 lds\nM v[64:67],a[12:15],a[140:143],v[64:67]\nds_read_b128 a[80:83],v241 offset:4096\nv_mov_b32 v203,0\n" \
"M v[68:71],a[0:3],a[144:147],0\nds_read_b128 a[0:3],v240 offset:32768\nM v[68:71],a[4:7],a[148:151],v[68:71]\nds_read_b128 a[4:7],v240 offset:33792\nM v[68:71],a[8:11],a[152:155],v[68:71]\nds_read_b128 a[8:11],v240 offset:34816\nbuffer_load_dwordx4 v240,s[44:47],s36 offen offset:1024 sc1 lds\nM v[68:71],a[12:15],a[156:159],v[68:71]\n" \
"ds_read_b128 a[12:15],v240 offset:35840\nv_max3_f32 v225,v64,v65,v66\nM v[72:75],a[16:19],a[128:131],0\nds_read_b128 a[84:87],v241 offset:5120\ns_lshl4_add_u32 s69,s13,s68\nM v[72:75],a[20:23],a[132:135],v[72:75]\nds_read_b128 a[88:91],v241 offset:6144\nM v[72:75],a[24:27],a[136:139],v[72:75]\n" \
"v_max3_f32 v229,v68,v69,v70\nbuffer_load_dwordx4 v240,s[44:47],s36 offen offset:2048 sc1 lds\nM v[72:75],a[28:31],a[140:143],v[72:75]\nds_read_b128 a[92:95],v241 offset:7168\nds_read_b128 a[96:99],v241 offset:8192\nM v[76:79],a[16:19],a[144:147],0\nds_read_b128 a[16:19],v240 offset:36864\nM v[76:79],a[20:23],a[148:151],v[76:79]\n" \
"ds_read_b128 a[20:23],v240 offset:37888\nM v[76:79],a[24:27],a[152:155],v[76:79]\nds_read_b128 a[24:27],v240 offset:38912\nv_max3_f32 v225,v225,v67,v72\nv_max3_f32 v225,v225,v73,v74\nM v[76:79],a[28:31],a[156:159],v[76:79]\nds_read_b128 a[28:31],v240 offset:39936\nv_max_f32 v225,v225,v75\n" \
"M v[80:83],a[32:35],a[128:131],0\nv_cndmask_b32 v225,v224,v225,s[90:91]\nbuffer_load_dwordx4 v240,s[44:47],s36 offen offset:3072 sc1 lds\nM v[80:83],a[36:39],a[132:135],v[80:83]\nds_read_b128 a[100:103],v241 offset:9216\nds_read_b128 a[104:107],v241 offset:10240\nv_max3_f32 v229,v229,v71,v76\nM v[80:83],a[40:43],a[136:139],v[80:83]\n" \
"v_max3_f32 v229,v229,v77,v78\nv_max_f32 v229,v229,v79\nM v[80:83],a[44:47],a[140:143],v[80:83]\nv_cndmask_b32 v229,v228,v229,s[90:91]\nds_read_b128 a[108:111],v241 offset:11264\nds_read_b128 a[112:115],v241 offset:12288\nM v[84:87],a[32:35],a[144:147],0\nv_permlane16_swap_b32 v225,v229\n" \
"v_max_f32 v225,v225,v229\nv_mov_b32 v229,v225\nM v[84:87],a[36:39],a[148:151],v[84:87]\nds_read_b128 a[32:35],v240 offset:40960\nv_permlane32_swap_b32 v225,v229\nv_max_f32 v225,v225,v229\nM v[84:87],a[40:43],a[152:155],v[84:87]\nv_mov_b32 v229,v225\n" \
"ds_read_b128 a[36:39],v240 offset:41984\nM v[84:87],a[44:47],a[156:159],v[84:87]\nv_permlane16_swap_b32 v225,v229\nv_sub_f32 v224,0,v225\nv_sub_f32 v226,0,v225\nM v[92:95],a[48:51],a[144:147],0\nv_sub_f32 v227,0,v225\nv_sub_f32 v225,0,v225\n" \
"v_sub_f32 v228,0,v229\nM v[92:95],a[52:55],a[148:151],v[92:95]\nv_sub_f32 v230,0,v229\nv_sub_f32 v231,0,v229\nv_sub_f32 v229,0,v229\nM v[92:95],a[56:59],a[152:155],v[92:95]\nds_read_b128 a[40:43],v240 offset:43008\nds_read_b128 a[44:47],v240 offset:44032\n" \
"M v[92:95],a[60:63],a[156:159],v[92:95]\nds_read_b128 a[48:51],v240 offset:45056\nv_add_f32 v64,v64,v224\ns_waitcnt lgkmcnt(7)\nM v[136:139],a[0:3],a[160:163],0\nds_read_b128 a[52:55],v240 offset:46080\nv_add_f32 v65,v65,v224\nv_add_f32 v66,v66,v224\n" \
"M v[136:139],a[4:7],a[164:167],v[136:139]\nds_read_b128 a[56:59],v240 offset:47104\nv_add_f32 v67,v67,v224\nv_add_f32 v68,v68,v228\nM v[136:139],a[8:11],a[168:171],v[136:139]\nds_read_b128 a[60:63],v240 offset:48128\nv_add_f32 v69,v69,v228\nM v[136:139],a[12:15],a[172:175],v[136:139]\n" \
"v_exp_f32 v64,v64\nv_add_f32 v72,v72,v224\nM v[140:143],a[0:3],a[176:179],0\nv_exp_f32 v65,v65\nv_add_f32 v73,v73,v224\nM v[140:143],a[4:7],a[180:183],v[140:143]\nv_exp_f32 v66,v66\nM v[140:143],a[8:11],a[184:187],v[140:143]\n" \
"v_exp_f32 v67,v67\nv_add_f32 v74,v74,v224\nM v[140:143],a[12:15],a[188:191],v[140:143]\nv_add_f32 v75,v75,v224\nv_add_f32 v70,v70,v228\nv_add_f32 v71,v71,v228\nM v[156:159],a[16:19],a[176:179],0\nv_exp_f32 v68,v68\n" \
"v_cvt_pk_bf16_f32 v64,v64,v65\nM v[156:159],a[20:23],a[180:183],v[156:159]\nv_exp_f32 v69,v69\nM v[156:159],a[24:27],a[184:187],v[156:159]\nv_exp_f32 v72,v72\nv_add_f32 v76,v76,v228\nM v[156:159],a[28:31],a[188:191],v[156:159]\nv_exp_f32 v73,v73\n" \
"v_add_f32 v77,v77,v228\nM v[128:131],a[0:3],a[128:131],v[224:227]\nv_exp_f32 v74,v74\nv_add_f32 v78,v78,v228\nM v[128:131],a[4:7],a[132:135],v[128:131]\nv_exp_f32 v75,v75\nM v[128:131],a[8:11],a[136:139],v[128:131]\nv_exp_f32 v70,v70\n" \
"v_add_f32 v79,v79,v228\nM v[128:131],a[12:15],a[140:143],v[128:131]\nv_exp_f32 v71,v71\nv_cvt_pk_bf16_f32 v65,v66,v67\nM v[132:135],a[0:3],a[144:147],v[228:231]\nv_exp_f32 v76,v76\nv_cvt_pk_bf16_f32 v66,v72,v73\nM v[132:135],a[4:7],a[148:151],v[132:135]\n" \
"v_exp_f32 v77,v77\nM v[132:135],a[8:11],a[152:155],v[132:135]\nv_exp_f32 v78,v78\nv_cvt_pk_bf16_f32 v67,v74,v75\nM v[132:135],a[12:15],a[156:159],v[132:135]\nv_exp_f32 v79,v79\nv_cvt_pk_bf16_f32 v68,v68,v69\nM v[144:147],a[16:19],a[128:131],v[224:227]\n" \
"v_cvt_pk_bf16_f32 v69,v70,v71\nv_cvt_pk_bf16_f32 v70,v76,v77\nM v[144:147],a[20:23],a[132:135],v[144:147]\nv_cvt_pk_bf16_f32 v71,v78,v79\nv_cndmask_b32 v80,v80,v252,s[82:83]\nv_cndmask_b32 v81,v81,v252,s[84:85]\nM v[144:147],a[24:27],a[136:139],v[144:147]\nv_cndmask_b32 v82,v82,v252,s[86:87]\n" \
"v_cndmask_b32 v83,v83,v252,s[88:89]\nv_add_f32 v80,v80,v224\nM v[144:147],a[28:31],a[140:143],v[144:147]\nv_add_f32 v81,v81,v224\nv_add_f32 v82,v82,v224\nv_add_f32 v83,v83,v224\nM v[148:151],a[16:19],a[144:147],v[228:231]\nv_exp_f32 v80,v80\n" \
"M v[148:151],a[20:23],a[148:151],v[148:151]\nv_exp_f32 v81,v81\nv_add_f32 v84,v84,v228\nM v[148:151],a[24:27],a[152:155],v[148:151]\nv_exp_f32 v82,v82\nv_add_f32 v85,v85,v228\nM v[148:151],a[28:31],a[156:159],v[148:151]\nv_exp_f32 v83,v83\n" \
"s_waitcnt lgkmcnt(0)\nM v[160:163],a[32:35],a[128:131],v[224:227]\nv_cndmask_b32 v92,v92,v252,s[82:83]\nv_cndmask_b32 v93,v93,v252,s[84:85]\nM v[160:163],a[36:39],a[132:135],v[160:163]\nv_cndmask_b32 v94,v94,v252,s[86:87]\nv_cndmask_b32 v95,v95,v252,s[88:89]\nv_add_f32 v86,v86,v228\n" \
"M v[160:163],a[40:43],a[136:139],v[160:163]\nv_add_f32 v87,v87,v228\nv_exp_f32 v84,v84\nM v[160:163],a[44:47],a[140:143],v[160:163]\nv_exp_f32 v85,v85\nv_cvt_pk_bf16_f32 v80,v80,v81\nM v[164:167],a[32:35],a[144:147],v[228:231]\nv_exp_f32 v86,v86\n" \
"M v[164:167],a[36:39],a[148:151],v[164:167]\nv_exp_f32 v87,v87\nv_add_f32 v92,v92,v228\nM v[164:167],a[40:43],a[152:155],v[164:167]\nv_add_f32 v93,v93,v228\nv_add_f32 v94,v94,v228\nv_add_f32 v95,v95,v228\nM v[164:167],a[44:47],a[156:159],v[164:167]\n" \
"v_cvt_pk_bf16_f32 v81,v82,v83\nv_mov_b32 v82,0\nM v[176:179],a[48:51],a[128:131],v[224:227]\nv_exp_f32 v92,v92\nv_mov_b32 v83,0\nM v[176:179],a[52:55],a[132:135],v[176:179]\nv_exp_f32 v93,v93\nv_cvt_pk_bf16_f32 v84,v84,v85\n" \
"M v[176:179],a[56:59],a[136:139],v[176:179]\nv_exp_f32 v94,v94\nv_cvt_pk_bf16_f32 v85,v86,v87\nM v[176:179],a[60:63],a[140:143],v[176:179]\nv_exp_f32 v95,v95\nM v[180:183],a[48:51],a[144:147],v[228:231]\nv_cvt_pk_bf16_f32 v86,v92,v93\nv_cvt_pk_bf16_f32 v87,v94,v95\n" \
"ds_read_b128 a[116:119],v241 offset:13312\nM v[180:183],a[52:55],a[148:151],v[180:183]\nds_read_b128 a[120:123],v241 offset:14336\nds_read_b128 a[124:127],v241 offset:15360\nv_max3_f32 v237,v140,v141,v142\nM v[180:183],a[56:59],a[152:155],v[180:183]\nds_read_b128 a[0:3],v240 offset:49152\nv_exp_f32 v128,v128\n" \
"M v[180:183],a[60:63],a[156:159],v[180:183]\nds_read_b128 a[4:7],v240 offset:50176\nv_max3_f32 v237,v237,v143,v156\nM v[0:3],a[64:67],v[64:67],0\nds_read_b128 a[8:11],v240 offset:51200\nv_exp_f32 v129,v129\nM v[4:7],a[64:67],v[68:71],0\nds_read_b128 a[12:15],v240 offset:52224\n" \
"v_exp_f32 v130,v130\nM v[8:11],a[68:71],v[64:67],0\nbuffer_load_dword v248,v251,s[52:55],s80 offen sc1\nds_read_b128 a[16:19],v240 offset:53248\nM v[12:15],a[68:71],v[68:71],0\nv_exp_f32 v131,v131\nM v[16:19],a[72:75],v[64:67],0\nds_read_b128 a[20:23],v240 offset:54272\n" \
"v_cvt_pk_bf16_f32 v192,v128,v129\nv_cvt_pk_bf16_f32 v193,v130,v131\nM v[20:23],a[72:75],v[68:71],0\nds_read_b128 a[24:27],v240 offset:55296\nds_read_b128 a[28:31],v240 offset:56320\nv_max3_f32 v233,v136,v137,v138\nM v[24:27],a[76:79],v[64:67],0\nv_exp_f32 v132,v132\n" \
"M v[28:31],a[76:79],v[68:71],0\nv_exp_f32 v133,v133\nv_max3_f32 v237,v237,v157,v158\nM v[32:35],a[80:83],v[64:67],0\nv_exp_f32 v134,v134\nv_cvt_pk_bf16_f32 v196,v132,v133\nM v[36:39],a[80:83],v[68:71],0\nv_exp_f32 v135,v135\n" \
"v_max_f32 v233,v233,v139\nM v[40:43],a[84:87],v[64:67],0\nv_cvt_pk_bf16_f32 v197,v134,v135\nv_max_f32 v237,v237,v159\nM v[44:47],a[84:87],v[68:71],0\nv_exp_f32 v144,v144\nv_cndmask_b32 v233,v232,v233,s[90:91]\nM v[48:51],a[88:91],v[64:67],0\n" \
"v_exp_f32 v145,v145\nv_cndmask_b32 v237,v236,v237,s[90:91]\nM v[52:55],a[88:91],v[68:71],0\nv_exp_f32 v146,v146\nv_cvt_pk_bf16_f32 v194,v144,v145\nM v[56:59],a[92:95],v[64:67],0\nv_exp_f32 v147,v147\nM v[60:63],a[92:95],v[68:71],0\n" \
"v_cvt_pk_bf16_f32 v195,v146,v147\nv_permlane16_swap_b32 v233,v237\nv_max_f32 v233,v233,v237\nM a[192:195],a[208:211],v[64:67],0\nv_exp_f32 v148,v148\nv_mov_b32 v237,v233\nM a[196:199],a[208:211],v[68:71],0\nv_exp_f32 v149,v149\n" \
"v_permlane32_swap_b32 v233,v237\nM v[0:3],a[96:99],v[80:83],v[0:3]\nv_exp_f32 v150,v150\nM v[4:7],a[96:99],v[84:87],v[4:7]\nv_exp_f32 v151,v151\nv_cvt_pk_bf16_f32 v198,v148,v149\nM v[8:11],a[100:103],v[80:83],v[8:11]\nv_cvt_pk_bf16_f32 v199,v150,v151\n" \
"v_max_f32 v233,v233,v237\nv_mov_b32 v237,v233\nM v[12:15],a[100:103],v[84:87],v[12:15]\nv_cndmask_b32 v136,v136,v252,s[82:83]\nv_permlane16_swap_b32 v233,v237\nM v[16:19],a[104:107],v[80:83],v[16:19]\nv_cndmask_b32 v137,v137,v252,s[84:85]\nv_cndmask_b32 v138,v138,v252,s[86:87]\n" \
"v_cndmask_b32 v139,v139,v252,s[88:89]\nM v[20:23],a[104:107],v[84:87],v[20:23]\nv_sub_f32 v232,0,v233\nv_sub_f32 v234,0,v233\nv_sub_f32 v235,0,v233\nM v[24:27],a[108:111],v[80:83],v[24:27]\nv_add_f32 v136,v136,v232\nv_add_f32 v137,v137,v232\n" \
"v_add_f32 v138,v138,v232\nM v[28:31],a[108:111],v[84:87],v[28:31]\nv_add_f32 v139,v139,v232\nv_sub_f32 v233,0,v233\nM v[32:35],a[112:115],v[80:83],v[32:35]\nv_exp_f32 v136,v136\nv_sub_f32 v236,0,v237\nM v[36:39],a[112:115],v[84:87],v[36:39]\n" \
"v_exp_f32 v137,v137\ns_waitcnt lgkmcnt(0)\nM v[40:43],a[116:119],v[80:83],v[40:43]\nv_exp_f32 v138,v138\nv_cvt_pk_bf16_f32 v200,v136,v137\nM v[44:47],a[116:119],v[84:87],v[44:47]\nv_exp_f32 v139,v139\nM v[48:51],a[120:123],v[80:83],v[48:51]\n" \
"v_cvt_pk_bf16_f32 v201,v138,v139\nv_sub_f32 v238,0,v237\nv_sub_f32 v239,0,v237\nM v[52:55],a[120:123],v[84:87],v[52:55]\nv_add_f32 v140,v140,v236\nv_add_f32 v141,v141,v236\nv_add_f32 v142,v142,v236\nM v[56:59],a[124:127],v[80:83],v[56:59]\n" \
"v_add_f32 v143,v143,v236\nv_sub_f32 v237,0,v237\nds_read_b128 a[32:35],v240 offset:57344\nM v[60:63],a[124:127],v[84:87],v[60:63]\nv_exp_f32 v140,v140\nM a[192:195],a[208:211],v[80:83],a[192:195]\nv_exp_f32 v141,v141\nds_read_b128 a[36:39],v240 offset:58368\n" \
"M a[196:199],a[208:211],v[84:87],a[196:199]\nv_exp_f32 v142,v142\nv_cvt_pk_bf16_f32 v204,v140,v141\nM v[128:131],a[0:3],a[128:131],v[224:227]\nv_exp_f32 v143,v143\nM v[128:131],a[4:7],a[132:135],v[128:131]\nv_cvt_pk_bf16_f32 v205,v142,v143\nds_read_b128 a[40:43],v240 offset:59392\n" \
"ds_read_b128 a[44:47],v240 offset:60416\nM v[128:131],a[8:11],a[136:139],v[128:131]\nv_cndmask_b32 v156,v156,v252,s[82:83]\nv_cndmask_b32 v157,v157,v252,s[84:85]\nv_cndmask_b32 v158,v158,v252,s[86:87]\nM v[128:131],a[12:15],a[140:143],v[128:131]\nv_cndmask_b32 v159,v159,v252,s[88:89]\nv_add_f32 v156,v156,v236\n" \
"v_add_f32 v157,v157,v236\nM v[132:135],a[0:3],a[144:147],v[228:231]\nv_add_f32 v158,v158,v236\nv_add_f32 v159,v159,v236\nM v[132:135],a[4:7],a[148:151],v[132:135]\nv_exp_f32 v156,v156\nds_read_b128 a[48:51],v240 offset:61440\nM v[132:135],a[8:11],a[152:155],v[132:135]\n" \
"v_exp_f32 v157,v157\nds_read_b128 a[52:55],v240 offset:62464\nM v[132:135],a[12:15],a[156:159],v[132:135]\nv_exp_f32 v158,v158\nv_cvt_pk_bf16_f32 v206,v156,v157\nM v[144:147],a[16:19],a[128:131],v[224:227]\nv_exp_f32 v159,v159\nM v[144:147],a[20:23],a[132:135],v[144:147]\n" \
"v_cvt_pk_bf16_f32 v207,v158,v159\nv_exp_f32 v160,v160\nM v[144:147],a[24:27],a[136:139],v[144:147]\nv_exp_f32 v161,v161\nds_read_b128 a[56:59],v240 offset:63488\nM v[144:147],a[28:31],a[140:143],v[144:147]\nv_exp_f32 v162,v162\nv_cvt_pk_bf16_f32 v208,v160,v161\n" \
"M v[148:151],a[16:19],a[144:147],v[228:231]\nv_exp_f32 v163,v163\nM v[148:151],a[20:23],a[148:151],v[148:151]\nv_cvt_pk_bf16_f32 v209,v162,v163\nds_read_b128 a[60:63],v240 offset:64512\nds_read_b128 a[64:67],v241 offset:32768\nM v[148:151],a[24:27],a[152:155],v[148:151]\nv_exp_f32 v164,v164\n" \
"ds_read_b128 a[68:71],v241 offset:33792\nM v[148:151],a[28:31],a[156:159],v[148:151]\nv_exp_f32 v165,v165\nM v[136:139],a[0:3],a[160:163],v[232:235]\ns_waitcnt vmcnt(1)\ns_barrier\nv_cvt_pk_bf16_f32 v212,v164,v165\nM v[136:139],a[4:7],a[164:167],v[136:139]\n" \
"v_exp_f32 v166,v166\nds_read_b128 a[72:75],v241 offset:34816\nM v[136:139],a[8:11],a[168:171],v[136:139]\nv_exp_f32 v167,v167\nds_read_b128 a[76:79],v241 offset:35840\nM v[136:139],a[12:15],a[172:175],v[136:139]\nv_cvt_pk_bf16_f32 v213,v166,v167\nds_read_b128 a[80:83],v241 offset:36864\n" \
"M v[140:143],a[0:3],a[176:179],v[236:239]\nv_exp_f32 v176,v176\nds_read_b128 a[84:87],v241 offset:37888\nM v[140:143],a[4:7],a[180:183],v[140:143]\nv_exp_f32 v177,v177\nds_read_b128 a[88:91],v241 offset:38912\nM v[140:143],a[8:11],a[184:187],v[140:143]\nv_exp_f32 v178,v178\n" \
"v_cvt_pk_bf16_f32 v210,v176,v177\nM v[140:143],a[12:15],a[188:191],v[140:143]\nv_exp_f32 v179,v179\nM v[152:155],a[16:19],a[160:163],v[232:235]\nv_cvt_pk_bf16_f32 v211,v178,v179\nv_exp_f32 v180,v180\nM v[152:155],a[20:23],a[164:167],v[152:155]\nv_exp_f32 v181,v181\n" \
"ds_read_b128 a[92:95],v241 offset:39936\nM v[152:155],a[24:27],a[168:171],v[152:155]\nv_exp_f32 v182,v182\nv_cvt_pk_bf16_f32 v214,v180,v181\nM v[152:155],a[28:31],a[172:175],v[152:155]\nv_exp_f32 v183,v183\nM v[156:159],a[16:19],a[176:179],v[236:239]\nv_cvt_pk_bf16_f32 v215,v182,v183\n" \
"ds_read_b128 a[96:99],v241 offset:40960\nds_read_b128 a[100:103],v241 offset:41984\nM v[156:159],a[20:23],a[180:183],v[156:159]\nds_read_b128 a[104:107],v241 offset:43008\ns_add_u32 m0,s64,131072\ns_waitcnt vmcnt(0)\nv_cmp_eq_u32 vcc,0,v248\nM v[156:159],a[24:27],a[184:187],v[156:159]\n" \
"ds_read_b128 a[108:111],v241 offset:44032\ns_cbranch_vccnz .Lps3\n.Lpb3:\nM v[156:159],a[28:31],a[188:191],v[156:159]\nbuffer_load_dwordx4 v243,s[40:43],s69 offen offset:0 lds\ns_waitcnt lgkmcnt(5)\nM v[160:163],a[32:35],a[128:131],v[224:227]\nds_read_b128 a[112:115],v241 offset:45056\n" \
"v_exp_f32 v128,v128\nM v[160:163],a[36:39],a[132:135],v[160:163]\nds_read_b128 a[116:119],v241 offset:46080\nv_exp_f32 v129,v129\nM v[160:163],a[40:43],a[136:139],v[160:163]\nds_read_b128 a[120:123],v241 offset:47104\nds_read_b128 a[124:127],v241 offset:48128\nM v[160:163],a[44:47],a[140:143],v[160:163]\n" \
"ds_read_b128 a[0:3],v240 offset:0\nbuffer_load_dwordx4 v244,s[40:43],s69 offen offset:1024 lds\nM v[164:167],a[32:35],a[144:147],v[228:231]\nds_read_b128 a[4:7],v240 offset:1024\nv_exp_f32 v130,v130\nM v[164:167],a[36:39],a[148:151],v[164:167]\nds_read_b128 a[8:11],v240 offset:2048\nv_exp_f32 v131,v131\n" \
"M v[164:167],a[40:43],a[152:155],v[164:167]\nv_exp_f32 v132,v132\nM v[164:167],a[44:47],a[156:159],v[164:167]\nbuffer_load_dwordx4 v245,s[40:43],s69 offen offset:2048 lds\nds_read_b128 a[12:15],v240 offset:3072\nM v[176:179],a[48:51],a[128:131],v[224:227]\nds_read_b128 a[16:19],v240 offset:4096\nv_exp_f32 v133,v133\n" \
"M v[176:179],a[52:55],a[132:135],v[176:179]\nds_read_b128 a[20:23],v240 offset:5120\nv_exp_f32 v134,v134\nM v[176:179],a[56:59],a[136:139],v[176:179]\nds_read_b128 a[24:27],v240 offset:6144\nds_read_b128 a[28:31],v240 offset:7168\nM v[176:179],a[60:63],a[140:143],v[176:179]\nbuffer_load_dwordx4 v246,s[40:43],s69 offen offset:3072 lds\n" \
"s_lshl4_add_u32 s69,s13,s68\nM v[180:183],a[48:51],a[144:147],v[228:231]\ns_add_u32 s69,s69,0x10000\nv_exp_f32 v135,v135\nM v[180:183],a[52:55],a[148:151],v[180:183]\nv_exp_f32 v144,v144\nM v[180:183],a[56:59],a[152:155],v[180:183]\ns_add_u32 m0,s64,147456\n" \
"v_exp_f32 v145,v145\nM v[180:183],a[60:63],a[156:159],v[180:183]\nbuffer_load_dwordx4 v243,s[40:43],s69 offen offset:0 lds\nM v[168:171],a[32:35],a[160:163],v[232:235]\nv_exp_f32 v146,v146\nM v[168:171],a[36:39],a[164:167],v[168:171]\nv_exp_f32 v147,v147\nM v[168:171],a[40:43],a[168:171],v[168:171]\n" \
"v_exp_f32 v148,v148\nM v[168:171],a[44:47],a[172:175],v[168:171]\nbuffer_load_dwordx4 v244,s[40:43],s69 offen offset:1024 lds\nM v[172:175],a[32:35],a[176:179],v[236:239]\nds_read_b128 a[32:35],v240 offset:8192\nv_exp_f32 v149,v149\nM v[172:175],a[36:39],a[180:183],v[172:175]\nv_exp_f32 v150,v150\n" \
"M v[172:175],a[40:43],a[184:187],v[172:175]\nds_read_b128 a[36:39],v240 offset:9216\nv_exp_f32 v151,v151\nM v[172:175],a[44:47],a[188:191],v[172:175]\nbuffer_load_dwordx4 v245,s[40:43],s69 offen offset:2048 lds\nds_read_b128 a[40:43],v240 offset:10240\nM v[184:187],a[48:51],a[160:163],v[232:235]\nds_read_b128 a[44:47],v240 offset:11264\n" \
"v_exp_f32 v136,v136\nM v[184:187],a[52:55],a[164:167],v[184:187]\nv_exp_f32 v137,v137\nM v[184:187],a[56:59],a[168:171],v[184:187]\nv_exp_f32 v138,v138\nM v[184:187],a[60:63],a[172:175],v[184:187]\nbuffer_load_dwordx4 v246,s[40:43],s69 offen offset:3072 lds\ns_add_u32 m0,s64,65536\n" \
"M v[188:191],a[48:51],a[176:179],v[236:239]\nv_exp_f32 v139,v139\nM v[188:191],a[52:55],a[180:183],v[188:191]\nds_read_b128 a[48:51],v240 offset:12288\nv_exp_f32 v140,v140\nM v[188:191],a[56:59],a[184:187],v[188:191]\nds_read_b128 a[52:55],v240 offset:13312\nv_exp_f32 v141,v141\n" \
"M v[188:191],a[60:63],a[188:191],v[188:191]\nbuffer_load_dwordx4 v240,s[48:51],s36 offen offset:0 sc1 lds\nds_read_b128 a[56:59],v240 offset:14336\nM v[0:3],a[64:67],v[192:195],v[0:3]\nv_exp_f32 v142,v142\nM v[4:7],a[64:67],v[196:199],v[4:7]\nds_read_b128 a[60:63],v240 offset:15360\nv_exp_f32 v143,v143\n" \
"M v[64:67],a[64:67],v[200:203],0\nv_exp_f32 v152,v152\nM v[68:71],a[64:67],v[204:207],0\nbuffer_load_dwordx4 v240,s[48:51],s36 offen offset:1024 sc1 lds\nM v[8:11],a[68:71],v[192:195],v[8:11]\nv_exp_f32 v153,v153\nM v[12:15],a[68:71],v[196:199],v[12:15]\nv_exp_f32 v154,v154\n" \
"M v[72:75],a[68:71],v[200:203],0\nv_exp_f32 v155,v155\nM v[76:79],a[68:71],v[204:207],0\nbuffer_load_dwordx4 v240,s[48:51],s36 offen offset:2048 sc1 lds\nM v[16:19],a[72:75],v[192:195],v[16:19]\nv_exp_f32 v156,v156\nM v[20:23],a[72:75],v[196:199],v[20:23]\nv_exp_f32 v157,v157\n" \
"M v[80:83],a[72:75],v[200:203],0\nv_exp_f32 v158,v158\nM v[84:87],a[72:75],v[204:207],0\nbuffer_load_dwordx4 v240,s[48:51],s36 offen offset:3072 sc1 lds\nM v[24:27],a[76:79],v[192:195],v[24:27]\ns_add_u32 m0,s64,16384\nv_exp_f32 v159,v159\nM v[28:31],a[76:79],v[196:199],v[28:31]\n" \
"v_exp_f32 v160,v160\nM v[88:91],a[76:79],v[200:203],0\nv_exp_f32 v161,v161\nM v[92:95],a[76:79],v[204:207],0\nbuffer_load_dwordx4 v240,s[44:47],s65 offen offset:0 sc1 lds\nM v[32:35],a[80:83],v[192:195],v[32:35]\nv_exp_f32 v162,v162\nM v[36:39],a[80:83],v[196:199],v[36:39]\n" \
"v_exp_f32 v163,v163\nM v[96:99],a[80:83],v[200:203],0\nv_exp_f32 v164,v164\nM v[100:103],a[80:83],v[204:207],0\nbuffer_load_dwordx4 v240,s[44:47],s65 offen offset:1024 sc1 lds\nM v[40:43],a[84:87],v[192:195],v[40:43]\nv_exp_f32 v165,v165\nM v[44:47],a[84:87],v[196:199],v[44:47]\n" \
"v_exp_f32 v166,v166\nM v[104:107],a[84:87],v[200:203],0\nv_exp_f32 v167,v167\nM v[108:111],a[84:87],v[204:207],0\nbuffer_load_dwordx4 v240,s[44:47],s65 offen offset:2048 sc1 lds\nM v[48:51],a[88:91],v[192:195],v[48:51]\ns_waitcnt lgkmcnt(0)\ns_barrier\n" \
"M v[52:55],a[88:91],v[196:199],v[52:55]\nv_exp_f32 v176,v176\nM v[112:115],a[88:91],v[200:203],0\nv_exp_f32 v177,v177\nM v[116:119],a[88:91],v[204:207],0\nbuffer_load_dwordx4 v240,s[44:47],s65 offen offset:3072 sc1 lds\ns_add_u32 m0,s64,81920\nM v[56:59],a[92:95],v[192:195],v[56:59]\n" \
"v_exp_f32 v178,v178\nM v[60:63],a[92:95],v[196:199],v[60:63]\nv_exp_f32 v179,v179\nM v[120:123],a[92:95],v[200:203],0\nv_exp_f32 v180,v180\nM v[124:127],a[92:95],v[204:207],0\nbuffer_load_dwordx4 v240,s[48:51],s65 offen offset:0 sc1 lds\nM a[192:195],a[208:211],v[192:195],a[192:195]\n" \
"v_exp_f32 v181,v181\nv_cvt_pk_bf16_f32 v192,v128,v129\nM a[196:199],a[208:211],v[196:199],a[196:199]\nv_exp_f32 v182,v182\nv_cvt_pk_bf16_f32 v193,v130,v131\nM a[200:203],a[208:211],v[200:203],0\nv_exp_f32 v183,v183\nM a[204:207],a[208:211],v[204:207],0\n" \
"buffer_load_dwordx4 v240,s[48:51],s65 offen offset:1024 sc1 lds\nv_cvt_pk_bf16_f32 v194,v144,v145\nM v[0:3],a[96:99],v[208:211],v[0:3]\nv_exp_f32 v168,v168\nv_cvt_pk_bf16_f32 v195,v146,v147\nM v[4:7],a[96:99],v[212:215],v[4:7]\nv_exp_f32 v169,v169\nv_cvt_pk_bf16_f32 v196,v132,v133\n" \
"M v[8:11],a[100:103],v[208:211],v[8:11]\nv_exp_f32 v170,v170\nM v[12:15],a[100:103],v[212:215],v[12:15]\nbuffer_load_dwordx4 v240,s[48:51],s65 offen offset:2048 sc1 lds\nv_cvt_pk_bf16_f32 v197,v134,v135\nM v[16:19],a[104:107],v[208:211],v[16:19]\nv_exp_f32 v171,v171\nv_cvt_pk_bf16_f32 v198,v148,v149\n" \
"M v[20:23],a[104:107],v[212:215],v[20:23]\nv_cvt_pk_bf16_f32 v199,v150,v151\nv_cvt_pk_bf16_f32 v200,v136,v137\nM v[24:27],a[108:111],v[208:211],v[24:27]\nv_cvt_pk_bf16_f32 v201,v138,v139\nv_cvt_pk_bf16_f32 v202,v152,v153\nv_cvt_pk_bf16_f32 v203,v154,v155\nM v[28:31],a[108:111],v[212:215],v[28:31]\n" \
"buffer_load_dwordx4 v240,s[48:51],s65 offen offset:3072 sc1 lds\ns_add_u32 m0,s64,32768\nM v[32:35],a[112:115],v[208:211],v[32:35]\nv_cvt_pk_bf16_f32 v204,v140,v141\nv_cvt_pk_bf16_f32 v205,v142,v143\nv_cvt_pk_bf16_f32 v206,v156,v157\nM v[36:39],a[112:115],v[212:215],v[36:39]\nv_cvt_pk_bf16_f32 v207,v158,v159\n" \
"M v[40:43],a[116:119],v[208:211],v[40:43]\nM v[44:47],a[116:119],v[212:215],v[44:47]\nbuffer_load_dwordx4 v240,s[44:47],s66 offen offset:0 sc1 lds\nM v[48:51],a[120:123],v[208:211],v[48:51]\nM v[52:55],a[120:123],v[212:215],v[52:55]\nM v[56:59],a[124:127],v[208:211],v[56:59]\nM v[60:63],a[124:127],v[212:215],v[60:63]\nbuffer_load_dwordx4 v240,s[44:47],s66 offen offset:1024 sc1 lds\n" \
"buffer_load_dwordx4 v240,s[44:47],s66 offen offset:2048 sc1 lds\nbuffer_load_dwordx4 v240,s[44:47],s66 offen offset:3072 sc1 lds\nM a[192:195],a[208:211],v[208:211],a[192:195]\ns_add_u32 m0,s64,98304\ns_nop 0\nbuffer_load_dwordx4 v240,s[48:51],s66 offen offset:0 sc1 lds\nbuffer_load_dwordx4 v240,s[48:51],s66 offen offset:1024 sc1 lds\nbuffer_load_dwordx4 v240,s[48:51],s66 offen offset:2048 sc1 lds\n" \
"buffer_load_dwordx4 v240,s[48:51],s66 offen offset:3072 sc1 lds\nM a[196:199],a[208:211],v[212:215],a[196:199]\ns_cmp_eq_u32 s62,0\ns_cbranch_scc1 .Ltrans\ns_branch .Ldual\n.Lp23:\ns_cmp_eq_u32 s7,2\ns_cbranch_scc0 .Lpro3\n" \
".Lpro2:\ns_barrier\nM v[64:67],a[0:3],a[128:131],0\nds_read_b128 a[64:67],v241 offset:0\nds_read_b128 a[68:71],v241 offset:1024\nM v[64:67],a[4:7],a[132:135],v[64:67]\nds_read_b128 a[72:75],v241 offset:2048\ns_mov_b32 m0,s64\n" \
"v_mov_b32 v184,0xff800000\nM v[64:67],a[8:11],a[136:139],v[64:67]\nds_read_b128 a[76:79],v241 offset:3072\nbuffer_load_dwordx4 v240,s[44:47],s36 offen offset:0 sc1 lds\nM v[64:67],a[12:15],a[140:143],v[64:67]\nds_read_b128 a[80:83],v241 offset:4096\nv_mov_b32 v185,0xff800000\nM v[68:71],a[0:3],a[144:147],0\n" \
"ds_read_b128 a[0:3],v240 offset:16384\nv_mov_b32 v186,0xff800000\nv_mov_b32 v187,0xff800000\nM v[68:71],a[4:7],a[148:151],v[68:71]\nds_read_b128 a[4:7],v240 offset:17408\nv_max3_f32 v225,v64,v65,v66\nM v[68:71],a[8:11],a[152:155],v[68:71]\nbuffer_load_dwordx4 v240,s[44:47],s36 offen offset:1024 sc1 lds\n" \
"M v[68:71],a[12:15],a[156:159],v[68:71]\nds_read_b128 a[8:11],v240 offset:18432\nds_read_b128 a[12:15],v240 offset:19456\nM v[72:75],a[16:19],a[128:131],0\nds_read_b128 a[84:87],v241 offset:5120\ns_lshl4_add_u32 s69,s13,s68\nM v[72:75],a[20:23],a[132:135],v[72:75]\nds_read_b128 a[88:91],v241 offset:6144\n" \
"M v[72:75],a[24:27],a[136:139],v[72:75]\nv_max3_f32 v229,v68,v69,v70\nds_read_b128 a[92:95],v241 offset:7168\nM v[72:75],a[28:31],a[140:143],v[72:75]\nds_read_b128 a[96:99],v241 offset:8192\nbuffer_load_dwordx4 v240,s[44:47],s36 offen offset:2048 sc1 lds\nM v[76:79],a[16:19],a[144:147],0\nds_read_b128 a[16:19],v240 offset:20480\n" \
"M v[76:79],a[20:23],a[148:151],v[76:79]\nds_read_b128 a[20:23],v240 offset:21504\nM v[76:79],a[24:27],a[152:155],v[76:79]\nds_read_b128 a[24:27],v240 offset:22528\nv_max3_f32 v225,v225,v67,v72\nv_max3_f32 v225,v225,v73,v74\nM v[76:79],a[28:31],a[156:159],v[76:79]\nv_max_f32 v225,v225,v75\n" \
"v_cndmask_b32 v225,v224,v225,s[90:91]\nds_read_b128 a[28:31],v240 offset:23552\nM v[80:83],a[32:35],a[128:131],0\nbuffer_load_dwordx4 v240,s[44:47],s36 offen offset:3072 sc1 lds\nM v[80:83],a[36:39],a[132:135],v[80:83]\nds_read_b128 a[100:103],v241 offset:9216\nds_read_b128 a[104:107],v241 offset:10240\nv_max3_f32 v229,v229,v71,v76\n" \
"M v[80:83],a[40:43],a[136:139],v[80:83]\nv_max3_f32 v229,v229,v77,v78\nv_max_f32 v229,v229,v79\nv_cndmask_b32 v229,v228,v229,s[90:91]\nM v[80:83],a[44:47],a[140:143],v[80:83]\nds_read_b128 a[108:111],v241 offset:11264\nv_permlane16_swap_b32 v225,v229\nv_max_f32 v225,v225,v229\n" \
"M v[84:87],a[32:35],a[144:147],0\nv_mov_b32 v229,v225\nds_read_b128 a[112:115],v241 offset:12288\nM v[84:87],a[36:39],a[148:151],v[84:87]\nv_permlane32_swap_b32 v225,v229\nv_max_f32 v225,v225,v229\nv_mov_b32 v229,v225\nM v[84:87],a[40:43],a[152:155],v[84:87]\n" \
"ds_read_b128 a[116:119],v241 offset:13312\nv_permlane16_swap_b32 v225,v229\nv_sub_f32 v224,0,v225\nM v[84:87],a[44:47],a[156:159],v[84:87]\nv_sub_f32 v226,0,v225\nv_sub_f32 v227,0,v225\nM v[88:91],a[48:51],a[128:131],0\nv_sub_f32 v225,0,v225\n" \
"v_sub_f32 v228,0,v229\nv_sub_f32 v230,0,v229\nM v[88:91],a[52:55],a[132:135],v[88:91]\nv_sub_f32 v231,0,v229\nv_sub_f32 v229,0,v229\nv_add_f32 v64,v64,v224\nM v[88:91],a[56:59],a[136:139],v[88:91]\nv_add_f32 v65,v65,v224\n" \
"v_add_f32 v66,v66,v224\nM v[88:91],a[60:63],a[140:143],v[88:91]\nv_add_f32 v67,v67,v224\nv_exp_f32 v64,v64\nM v[92:95],a[48:51],a[144:147],0\nv_exp_f32 v65,v65\nv_add_f32 v68,v68,v228\nM v[92:95],a[52:55],a[148:151],v[92:95]\n" \
"v_exp_f32 v66,v66\nv_add_f32 v69,v69,v228\nM v[92:95],a[56:59],a[152:155],v[92:95]\nv_exp_f32 v67,v67\nM v[92:95],a[60:63],a[156:159],v[92:95]\nv_add_f32 v72,v72,v224\nv_add_f32 v73,v73,v224\ns_waitcnt lgkmcnt(0)\n" \
"M v[96:99],a[0:3],a[128:131],v[224:227]\nv_add_f32 v74,v74,v224\nv_add_f32 v75,v75,v224\nv_add_f32 v70,v70,v228\nM v[96:99],a[4:7],a[132:135],v[96:99]\nv_add_f32 v71,v71,v228\nv_cvt_pk_bf16_f32 v64,v64,v65\nM v[96:99],a[8:11],a[136:139],v[96:99]\n" \
"v_exp_f32 v68,v68\nv_add_f32 v76,v76,v228\nM v[96:99],a[12:15],a[140:143],v[96:99]\nv_exp_f32 v69,v69\nv_add_f32 v77,v77,v228\nM v[100:103],a[0:3],a[144:147],v[228:231]\nv_exp_f32 v72,v72\nM v[100:103],a[4:7],a[148:151],v[100:103]\n" \
"v_exp_f32 v73,v73\nv_add_f32 v78,v78,v228\nM v[100:103],a[8:11],a[152:155],v[100:103]\nv_exp_f32 v74,v74\nv_add_f32 v79,v79,v228\nM v[100:103],a[12:15],a[156:159],v[100:103]\nv_exp_f32 v75,v75\nv_cvt_pk_bf16_f32 v65,v66,v67\n" \
"M v[108:111],a[16:19],a[144:147],v[228:231]\nv_exp_f32 v70,v70\nM v[108:111],a[20:23],a[148:151],v[108:111]\nv_exp_f32 v71,v71\nv_cvt_pk_bf16_f32 v66,v72,v73\nM v[108:111],a[24:27],a[152:155],v[108:111]\nv_exp_f32 v76,v76\nv_exp_f32 v77,v77\n" \
"v_exp_f32 v78,v78\nv_exp_f32 v79,v79\nv_cvt_pk_bf16_f32 v67,v74,v75\nv_cvt_pk_bf16_f32 v68,v68,v69\nM v[108:111],a[28:31],a[156:159],v[108:111]\nv_cvt_pk_bf16_f32 v69,v70,v71\nv_cvt_pk_bf16_f32 v70,v76,v77\nv_cvt_pk_bf16_f32 v71,v78,v79\n" \
"M v[0:3],a[64:67],v[64:67],0\nv_add_f32 v80,v80,v224\nv_add_f32 v81,v81,v224\nv_add_f32 v82,v82,v224\nM v[4:7],a[64:67],v[68:71],0\nv_add_f32 v83,v83,v224\nv_exp_f32 v80,v80\nM v[8:11],a[68:71],v[64:67],0\n" \
"v_exp_f32 v81,v81\nM v[12:15],a[68:71],v[68:71],0\nv_exp_f32 v82,v82\nv_add_f32 v84,v84,v228\nM v[16:19],a[72:75],v[64:67],0\nv_exp_f32 v83,v83\nv_add_f32 v85,v85,v228\nM v[20:23],a[72:75],v[68:71],0\n" \
"v_add_f32 v88,v88,v224\nv_add_f32 v89,v89,v224\nv_add_f32 v90,v90,v224\nM v[24:27],a[76:79],v[64:67],0\nv_add_f32 v91,v91,v224\nv_add_f32 v86,v86,v228\nM v[28:31],a[76:79],v[68:71],0\nv_add_f32 v87,v87,v228\n" \
"v_exp_f32 v84,v84\nM v[32:35],a[80:83],v[64:67],0\nv_exp_f32 v85,v85\nv_cvt_pk_bf16_f32 v80,v80,v81\nM v[36:39],a[80:83],v[68:71],0\nv_exp_f32 v88,v88\nM v[40:43],a[84:87],v[64:67],0\nv_exp_f32 v89,v89\n" \
"v_add_f32 v92,v92,v228\nM v[44:47],a[84:87],v[68:71],0\nv_exp_f32 v90,v90\nv_add_f32 v93,v93,v228\nM v[48:51],a[88:91],v[64:67],0\nv_exp_f32 v91,v91\nM v[52:55],a[88:91],v[68:71],0\nv_exp_f32 v86,v86\n" \
"v_add_f32 v94,v94,v228\nM v[56:59],a[92:95],v[64:67],0\nv_exp_f32 v87,v87\nv_add_f32 v95,v95,v228\nM v[60:63],a[92:95],v[68:71],0\nv_cvt_pk_bf16_f32 v81,v82,v83\nv_exp_f32 v92,v92\nM a[192:195],a[208:211],v[64:67],0\n" \
"v_exp_f32 v93,v93\nv_exp_f32 v94,v94\nv_exp_f32 v95,v95\nv_cvt_pk_bf16_f32 v82,v88,v89\nv_cvt_pk_bf16_f32 v83,v90,v91\nv_cvt_pk_bf16_f32 v84,v84,v85\nM a[196:199],a[208:211],v[68:71],0\nv_cvt_pk_bf16_f32 v85,v86,v87\n" \
"v_cvt_pk_bf16_f32 v86,v92,v93\nv_cvt_pk_bf16_f32 v87,v94,v95\nM v[0:3],a[96:99],v[80:83],v[0:3]\nds_read_b128 a[120:123],v241 offset:14336\nds_read_b128 a[124:127],v241 offset:15360\nv_cndmask_b32 v96,v96,v252,s[82:83]\nM v[4:7],a[96:99],v[84:87],v[4:7]\nds_read_b128 a[0:3],v240 offset:32768\n" \
"v_cndmask_b32 v97,v97,v252,s[84:85]\nM v[8:11],a[100:103],v[80:83],v[8:11]\nds_read_b128 a[4:7],v240 offset:33792\nv_cndmask_b32 v98,v98,v252,s[86:87]\nv_cndmask_b32 v99,v99,v252,s[88:89]\nM v[12:15],a[100:103],v[84:87],v[12:15]\nds_read_b128 a[8:11],v240 offset:34816\nv_exp_f32 v96,v96\n" \
"M v[16:19],a[104:107],v[80:83],v[16:19]\nds_read_b128 a[12:15],v240 offset:35840\nv_cndmask_b32 v108,v108,v252,s[82:83]\nM v[20:23],a[104:107],v[84:87],v[20:23]\nds_read_b128 a[16:19],v240 offset:36864\nv_exp_f32 v97,v97\nM v[24:27],a[108:111],v[80:83],v[24:27]\nds_read_b128 a[20:23],v240 offset:37888\n" \
"v_exp_f32 v98,v98\nM v[28:31],a[108:111],v[84:87],v[28:31]\nds_read_b128 a[24:27],v240 offset:38912\nv_exp_f32 v99,v99\nM v[32:35],a[112:115],v[80:83],v[32:35]\nds_read_b128 a[28:31],v240 offset:39936\nv_cvt_pk_bf16_f32 v96,v96,v97\nM v[36:39],a[112:115],v[84:87],v[36:39]\n" \
"ds_read_b128 a[32:35],v240 offset:40960\nv_exp_f32 v100,v100\nM v[40:43],a[116:119],v[80:83],v[40:43]\nds_read_b128 a[36:39],v240 offset:41984\nv_exp_f32 v101,v101\nM v[44:47],a[116:119],v[84:87],v[44:47]\nds_read_b128 a[40:43],v240 offset:43008\ns_waitcnt lgkmcnt(9)\n" \
"M v[48:51],a[120:123],v[80:83],v[48:51]\nbuffer_load_dword v248,v251,s[52:55],s80 offen sc1\nds_read_b128 a[44:47],v240 offset:44032\nM v[52:55],a[120:123],v[84:87],v[52:55]\nds_read_b128 a[48:51],v240 offset:45056\nv_cndmask_b32 v109,v109,v252,s[84:85]\nv_cndmask_b32 v110,v110,v252,s[86:87]\nM v[56:59],a[124:127],v[80:83],v[56:59]\n" \
"ds_read_b128 a[52:55],v240 offset:46080\nv_cndmask_b32 v111,v111,v252,s[88:89]\nM v[60:63],a[124:127],v[84:87],v[60:63]\nds_read_b128 a[56:59],v240 offset:47104\nv_exp_f32 v102,v102\nM a[192:195],a[208:211],v[80:83],a[192:195]\nds_read_b128 a[60:63],v240 offset:48128\nv_exp_f32 v103,v103\n" \
"M a[196:199],a[208:211],v[84:87],a[196:199]\nds_read_b128 a[64:67],v241 offset:16384\nv_cvt_pk_bf16_f32 v97,v98,v99\nv_mov_b32 v98,0\nM v[128:131],a[0:3],a[128:131],v[224:227]\nds_read_b128 a[68:71],v241 offset:17408\nv_mov_b32 v99,0\nM v[128:131],a[4:7],a[132:135],v[128:131]\n" \
"ds_read_b128 a[72:75],v241 offset:18432\nv_cvt_pk_bf16_f32 v100,v100,v101\ns_waitcnt lgkmcnt(9)\nM v[128:131],a[8:11],a[136:139],v[128:131]\nds_read_b128 a[76:79],v241 offset:19456\nv_exp_f32 v108,v108\nM v[128:131],a[12:15],a[140:143],v[128:131]\nds_read_b128 a[80:83],v241 offset:20480\n" \
"v_cvt_pk_bf16_f32 v101,v102,v103\nM v[132:135],a[0:3],a[144:147],v[228:231]\nv_exp_f32 v109,v109\nds_read_b128 a[84:87],v241 offset:21504\nM v[132:135],a[4:7],a[148:151],v[132:135]\nv_exp_f32 v110,v110\nv_cvt_pk_bf16_f32 v102,v108,v109\nM v[132:135],a[8:11],a[152:155],v[132:135]\n" \
"v_exp_f32 v111,v111\nM v[132:135],a[12:15],a[156:159],v[132:135]\nv_cvt_pk_bf16_f32 v103,v110,v111\nds_read_b128 a[88:91],v241 offset:22528\nds_read_b128 a[92:95],v241 offset:23552\nM v[144:147],a[16:19],a[128:131],v[224:227]\nds_read_b128 a[0:3],v240 offset:49152\nv_exp_f32 v128,v128\n" \
"M v[144:147],a[20:23],a[132:135],v[144:147]\nds_read_b128 a[4:7],v240 offset:50176\nv_exp_f32 v129,v129\nM v[144:147],a[24:27],a[136:139],v[144:147]\nds_read_b128 a[8:11],v240 offset:51200\nv_cvt_pk_bf16_f32 v192,v128,v129\nM v[144:147],a[28:31],a[140:143],v[144:147]\nds_read_b128 a[12:15],v240 offset:52224\n" \
"v_exp_f32 v130,v130\nM v[148:151],a[16:19],a[144:147],v[228:231]\nds_read_b128 a[16:19],v240 offset:53248\nv_exp_f32 v131,v131\nM v[148:151],a[20:23],a[148:151],v[148:151]\nds_read_b128 a[20:23],v240 offset:54272\nv_cvt_pk_bf16_f32 v193,v130,v131\nM v[148:151],a[24:27],a[152:155],v[148:151]\n" \
"ds_read_b128 a[24:27],v240 offset:55296\nv_exp_f32 v132,v132\nM v[148:151],a[28:31],a[156:159],v[148:151]\nds_read_b128 a[28:31],v240 offset:56320\nv_exp_f32 v133,v133\nM v[160:163],a[32:35],a[128:131],v[224:227]\nv_exp_f32 v134,v134\nM v[160:163],a[36:39],a[132:135],v[160:163]\n" \
"v_exp_f32 v135,v135\ns_waitcnt lgkmcnt(7)\nM v[160:163],a[40:43],a[136:139],v[160:163]\nv_cvt_pk_bf16_f32 v196,v132,v133\nv_cvt_pk_bf16_f32 v197,v134,v135\nds_read_b128 a[96:99],v241 offset:40960\nM v[160:163],a[44:47],a[140:143],v[160:163]\nv_exp_f32 v144,v144\n" \
"ds_read_b128 a[100:103],v241 offset:41984\nM v[164:167],a[32:35],a[144:147],v[228:231]\nds_read_b128 a[32:35],v240 offset:57344\nds_read_b128 a[104:107],v241 offset:43008\nM v[164:167],a[36:39],a[148:151],v[164:167]\nv_exp_f32 v145,v145\nds_read_b128 a[36:39],v240 offset:58368\nM v[164:167],a[40:43],a[152:155],v[164:167]\n" \
"v_exp_f32 v146,v146\nv_cvt_pk_bf16_f32 v194,v144,v145\nM v[164:167],a[44:47],a[156:159],v[164:167]\nv_exp_f32 v147,v147\nM v[176:179],a[48:51],a[128:131],v[224:227]\nv_cvt_pk_bf16_f32 v195,v146,v147\nds_read_b128 a[40:43],v240 offset:59392\nds_read_b128 a[44:47],v240 offset:60416\n" \
"M v[176:179],a[52:55],a[132:135],v[176:179]\nv_exp_f32 v148,v148\nds_read_b128 a[108:111],v241 offset:44032\nM v[176:179],a[56:59],a[136:139],v[176:179]\nv_exp_f32 v149,v149\nM v[176:179],a[60:63],a[140:143],v[176:179]\nv_exp_f32 v150,v150\nv_cvt_pk_bf16_f32 v198,v148,v149\n" \
"M v[180:183],a[48:51],a[144:147],v[228:231]\nv_exp_f32 v151,v151\nds_read_b128 a[48:51],v240 offset:61440\nM v[180:183],a[52:55],a[148:151],v[180:183]\nv_cvt_pk_bf16_f32 v199,v150,v151\nv_exp_f32 v160,v160\nM v[180:183],a[56:59],a[152:155],v[180:183]\nv_exp_f32 v161,v161\n" \
"M v[180:183],a[60:63],a[156:159],v[180:183]\nv_exp_f32 v162,v162\nds_read_b128 a[52:55],v240 offset:62464\nM v[0:3],a[64:67],v[96:99],v[0:3]\nv_exp_f32 v163,v163\nv_cvt_pk_bf16_f32 v208,v160,v161\nM v[4:7],a[64:67],v[100:103],v[4:7]\nv_cvt_pk_bf16_f32 v209,v162,v163\n" \
"ds_read_b128 a[56:59],v240 offset:63488\nM v[8:11],a[68:71],v[96:99],v[8:11]\nds_read_b128 a[60:63],v240 offset:64512\nv_exp_f32 v164,v164\nM v[12:15],a[68:71],v[100:103],v[12:15]\nv_exp_f32 v165,v165\nds_read_b128 a[64:67],v241 offset:32768\nM v[16:19],a[72:75],v[96:99],v[16:19]\n" \
"v_exp_f32 v166,v166\nM v[20:23],a[72:75],v[100:103],v[20:23]\nv_exp_f32 v167,v167\nv_cvt_pk_bf16_f32 v212,v164,v165\nM v[24:27],a[76:79],v[96:99],v[24:27]\nv_cvt_pk_bf16_f32 v213,v166,v167\nv_exp_f32 v176,v176\nM v[28:31],a[76:79],v[100:103],v[28:31]\n" \
"v_exp_f32 v177,v177\nds_read_b128 a[68:71],v241 offset:33792\nM v[32:35],a[80:83],v[96:99],v[32:35]\nv_exp_f32 v178,v178\nM v[36:39],a[80:83],v[100:103],v[36:39]\nv_exp_f32 v179,v179\nv_cvt_pk_bf16_f32 v210,v176,v177\nM v[40:43],a[84:87],v[96:99],v[40:43]\n" \
"v_cvt_pk_bf16_f32 v211,v178,v179\nv_exp_f32 v180,v180\nM v[44:47],a[84:87],v[100:103],v[44:47]\nv_exp_f32 v181,v181\nM v[48:51],a[88:91],v[96:99],v[48:51]\ns_waitcnt vmcnt(1)\ns_barrier\nv_cvt_pk_bf16_f32 v214,v180,v181\n" \
"M v[52:55],a[88:91],v[100:103],v[52:55]\nv_exp_f32 v182,v182\nds_read_b128 a[72:75],v241 offset:34816\nM v[56:59],a[92:95],v[96:99],v[56:59]\nv_exp_f32 v183,v183\nM v[60:63],a[92:95],v[100:103],v[60:63]\nv_cvt_pk_bf16_f32 v215,v182,v183\nds_read_b128 a[76:79],v241 offset:35840\n" \
"ds_read_b128 a[80:83],v241 offset:36864\nM a[192:195],a[208:211],v[96:99],a[192:195]\nds_read_b128 a[84:87],v241 offset:37888\ns_add_u32 m0,s64,131072\ns_waitcnt vmcnt(0)\nv_cmp_eq_u32 vcc,0,v248\nM a[196:199],a[208:211],v[100:103],a[196:199]\nds_read_b128 a[88:91],v241 offset:38912\n" \
"buffer_load_dwordx4 v243,s[40:43],s69 offen offset:0 lds\nM v[64:67],a[244:247],a[244:247],0\nds_read_b128 a[92:95],v241 offset:39936\ns_cbranch_vccnz .Lps4\n.Lpb4:\nM v[68:71],a[244:247],a[244:247],0\nds_read_b128 a[112:115],v241 offset:45056\nM v[72:75],a[244:247],a[244:247],0\n" \
"ds_read_b128 a[116:119],v241 offset:46080\nM v[76:79],a[244:247],a[244:247],0\nbuffer_load_dwordx4 v244,s[40:43],s69 offen offset:1024 lds\nM v[80:83],a[244:247],a[244:247],0\nds_read_b128 a[120:123],v241 offset:47104\nds_read_b128 a[124:127],v241 offset:48128\nM v[84:87],a[244:247],a[244:247],0\nM v[88:91],a[244:247],a[244:247],0\n" \
"M v[92:95],a[244:247],a[244:247],0\nbuffer_load_dwordx4 v245,s[40:43],s69 offen offset:2048 lds\nM v[96:99],a[244:247],a[244:247],0\nM v[100:103],a[244:247],a[244:247],0\nM v[104:107],a[244:247],a[244:247],0\nM v[108:111],a[244:247],a[244:247],0\nbuffer_load_dwordx4 v246,s[40:43],s69 offen offset:3072 lds\ns_lshl4_add_u32 s69,s13,s68\n" \
"M v[112:115],a[244:247],a[244:247],0\ns_add_u32 s69,s69,0x10000\ns_add_u32 m0,s64,147456\nM v[116:119],a[244:247],a[244:247],0\nM v[120:123],a[244:247],a[244:247],0\nM v[124:127],a[244:247],a[244:247],0\nbuffer_load_dwordx4 v243,s[40:43],s69 offen offset:0 lds\nM a[200:203],a[244:247],a[244:247],0\n" \
"M a[204:207],a[244:247],a[244:247],0\nM v[136:139],a[0:3],a[160:163],0\ns_waitcnt lgkmcnt(2)\nM v[136:139],a[4:7],a[164:167],v[136:139]\nbuffer_load_dwordx4 v244,s[40:43],s69 offen offset:1024 lds\nM v[136:139],a[8:11],a[168:171],v[136:139]\nM v[136:139],a[12:15],a[172:175],v[136:139]\nM v[140:143],a[0:3],a[176:179],0\n" \
"M v[140:143],a[4:7],a[180:183],v[140:143]\nbuffer_load_dwordx4 v245,s[40:43],s69 offen offset:2048 lds\nM v[140:143],a[8:11],a[184:187],v[140:143]\nM v[140:143],a[12:15],a[188:191],v[140:143]\nM v[152:155],a[16:19],a[160:163],0\nM v[152:155],a[20:23],a[164:167],v[152:155]\nbuffer_load_dwordx4 v246,s[40:43],s69 offen offset:3072 lds\nv_max3_f32 v233,v136,v137,v138\n" \
"M v[152:155],a[24:27],a[168:171],v[152:155]\ns_add_u32 m0,s64,65536\nM v[152:155],a[28:31],a[172:175],v[152:155]\nM v[156:159],a[16:19],a[176:179],0\nv_max3_f32 v237,v140,v141,v142\nM v[156:159],a[20:23],a[180:183],v[156:159]\nbuffer_load_dwordx4 v240,s[48:51],s36 offen offset:0 sc1 lds\nM v[156:159],a[24:27],a[184:187],v[156:159]\n" \
"M v[156:159],a[28:31],a[188:191],v[156:159]\nM v[128:131],a[0:3],a[128:131],v[224:227]\nM v[128:131],a[4:7],a[132:135],v[128:131]\nv_max3_f32 v233,v233,v139,v152\nv_max3_f32 v233,v233,v153,v154\nv_max_f32 v233,v233,v155\nM v[128:131],a[8:11],a[136:139],v[128:131]\nbuffer_load_dwordx4 v240,s[48:51],s36 offen offset:1024 sc1 lds\n" \
"M v[128:131],a[12:15],a[140:143],v[128:131]\nv_max3_f32 v237,v237,v143,v156\nv_max3_f32 v237,v237,v157,v158\nv_max_f32 v237,v237,v159\nM v[132:135],a[0:3],a[144:147],v[228:231]\nv_cndmask_b32 v233,v232,v233,s[90:91]\nv_cndmask_b32 v237,v236,v237,s[90:91]\nds_read_b128 a[0:3],v240 offset:0\n" \
"M v[132:135],a[4:7],a[148:151],v[132:135]\nv_permlane16_swap_b32 v233,v237\nv_max_f32 v233,v233,v237\nv_mov_b32 v237,v233\nM v[132:135],a[8:11],a[152:155],v[132:135]\nds_read_b128 a[4:7],v240 offset:1024\nv_permlane32_swap_b32 v233,v237\nM v[132:135],a[12:15],a[156:159],v[132:135]\n" \
"v_max_f32 v233,v233,v237\nv_mov_b32 v237,v233\nds_read_b128 a[8:11],v240 offset:2048\nM v[144:147],a[16:19],a[128:131],v[224:227]\nv_permlane16_swap_b32 v233,v237\nv_sub_f32 v232,0,v233\nv_sub_f32 v234,0,v233\nM v[144:147],a[20:23],a[132:135],v[144:147]\n" \
"v_sub_f32 v235,0,v233\nv_sub_f32 v233,0,v233\nM v[144:147],a[24:27],a[136:139],v[144:147]\nv_sub_f32 v236,0,v237\nv_sub_f32 v238,0,v237\nv_sub_f32 v239,0,v237\nM v[144:147],a[28:31],a[140:143],v[144:147]\nv_sub_f32 v237,0,v237\n" \
"ds_read_b128 a[12:15],v240 offset:3072\nv_add_f32 v136,v136,v232\nM v[148:151],a[16:19],a[144:147],v[228:231]\nds_read_b128 a[16:19],v240 offset:4096\nv_add_f32 v137,v137,v232\nM v[148:151],a[20:23],a[148:151],v[148:151]\nds_read_b128 a[20:23],v240 offset:5120\nbuffer_load_dwordx4 v240,s[48:51],s36 offen offset:2048 sc1 lds\n" \
"M v[148:151],a[24:27],a[152:155],v[148:151]\nds_read_b128 a[24:27],v240 offset:6144\nv_add_f32 v138,v138,v232\nv_add_f32 v139,v139,v232\nM v[148:151],a[28:31],a[156:159],v[148:151]\nds_read_b128 a[28:31],v240 offset:7168\nv_add_f32 v140,v140,v236\nv_add_f32 v141,v141,v236\n" \
"M v[160:163],a[32:35],a[128:131],v[224:227]\nv_add_f32 v142,v142,v236\nv_add_f32 v143,v143,v236\nM v[160:163],a[36:39],a[132:135],v[160:163]\nbuffer_load_dwordx4 v240,s[48:51],s36 offen offset:3072 sc1 lds\ns_add_u32 m0,s64,16384\nM v[160:163],a[40:43],a[136:139],v[160:163]\nv_add_f32 v152,v152,v232\n" \
"v_add_f32 v153,v153,v232\nv_add_f32 v154,v154,v232\nM v[160:163],a[44:47],a[140:143],v[160:163]\nv_add_f32 v155,v155,v232\nv_add_f32 v156,v156,v236\nM v[164:167],a[32:35],a[144:147],v[228:231]\nv_add_f32 v157,v157,v236\nv_add_f32 v158,v158,v236\n" \
"v_add_f32 v159,v159,v236\nM v[164:167],a[36:39],a[148:151],v[164:167]\nbuffer_load_dwordx4 v240,s[44:47],s65 offen offset:0 sc1 lds\nM v[164:167],a[40:43],a[152:155],v[164:167]\nv_exp_f32 v136,v136\nM v[164:167],a[44:47],a[156:159],v[164:167]\nv_exp_f32 v137,v137\nM v[176:179],a[48:51],a[128:131],v[224:227]\n" \
"v_exp_f32 v138,v138\nv_cvt_pk_bf16_f32 v200,v136,v137\nM v[176:179],a[52:55],a[132:135],v[176:179]\nbuffer_load_dwordx4 v240,s[44:47],s65 offen offset:1024 sc1 lds\nM v[176:179],a[56:59],a[136:139],v[176:179]\nv_exp_f32 v139,v139\nM v[176:179],a[60:63],a[140:143],v[176:179]\nv_exp_f32 v140,v140\n" \
"v_cvt_pk_bf16_f32 v201,v138,v139\nM v[180:183],a[48:51],a[144:147],v[228:231]\nv_exp_f32 v141,v141\nM v[180:183],a[52:55],a[148:151],v[180:183]\nbuffer_load_dwordx4 v240,s[44:47],s65 offen offset:2048 sc1 lds\nM v[180:183],a[56:59],a[152:155],v[180:183]\nv_exp_f32 v142,v142\nv_cvt_pk_bf16_f32 v204,v140,v141\n" \
"M v[180:183],a[60:63],a[156:159],v[180:183]\nv_exp_f32 v143,v143\nM v[168:171],a[32:35],a[160:163],v[232:235]\ns_waitcnt lgkmcnt(0)\ns_barrier\nM v[168:171],a[36:39],a[164:167],v[168:171]\nbuffer_load_dwordx4 v240,s[44:47],s65 offen offset:3072 sc1 lds\ns_add_u32 m0,s64,81920\n" \
"M v[168:171],a[40:43],a[168:171],v[168:171]\nv_exp_f32 v152,v152\nv_cvt_pk_bf16_f32 v205,v142,v143\nM v[168:171],a[44:47],a[172:175],v[168:171]\nv_exp_f32 v153,v153\nM v[172:175],a[32:35],a[176:179],v[236:239]\nds_read_b128 a[32:35],v240 offset:8192\nv_cvt_pk_bf16_f32 v202,v152,v153\n" \
"M v[172:175],a[36:39],a[180:183],v[172:175]\nds_read_b128 a[36:39],v240 offset:9216\nbuffer_load_dwordx4 v240,s[48:51],s65 offen offset:0 sc1 lds\nM v[172:175],a[40:43],a[184:187],v[172:175]\nds_read_b128 a[40:43],v240 offset:10240\nv_exp_f32 v154,v154\nM v[172:175],a[44:47],a[188:191],v[172:175]\nds_read_b128 a[44:47],v240 offset:11264\n" \
"v_cndmask_b32 v168,v168,v252,s[82:83]\nM v[188:191],a[48:51],a[176:179],v[236:239]\nds_read_b128 a[48:51],v240 offset:12288\nv_exp_f32 v155,v155\nM v[188:191],a[52:55],a[180:183],v[188:191]\nds_read_b128 a[52:55],v240 offset:13312\nbuffer_load_dwordx4 v240,s[48:51],s65 offen offset:1024 sc1 lds\nM v[188:191],a[56:59],a[184:187],v[188:191]\n" \
"ds_read_b128 a[56:59],v240 offset:14336\nv_cndmask_b32 v169,v169,v252,s[84:85]\nM v[188:191],a[60:63],a[188:191],v[188:191]\nds_read_b128 a[60:63],v240 offset:15360\nv_exp_f32 v156,v156\nM v[0:3],a[64:67],v[192:195],v[0:3]\nv_exp_f32 v157,v157\nv_cndmask_b32 v170,v170,v252,s[86:87]\n" \
"M v[4:7],a[64:67],v[196:199],v[4:7]\nbuffer_load_dwordx4 v240,s[48:51],s65 offen offset:2048 sc1 lds\nv_cndmask_b32 v171,v171,v252,s[88:89]\nM v[8:11],a[68:71],v[192:195],v[8:11]\nv_exp_f32 v158,v158\nM v[12:15],a[68:71],v[196:199],v[12:15]\nv_exp_f32 v159,v159\nv_cndmask_b32 v188,v188,v252,s[82:83]\n" \
"M v[16:19],a[72:75],v[192:195],v[16:19]\nv_exp_f32 v128,v128\nv_cndmask_b32 v189,v189,v252,s[84:85]\nM v[20:23],a[72:75],v[196:199],v[20:23]\nbuffer_load_dwordx4 v240,s[48:51],s65 offen offset:3072 sc1 lds\nM v[24:27],a[76:79],v[192:195],v[24:27]\ns_add_u32 m0,s64,32768\nv_exp_f32 v129,v129\n" \
"M v[28:31],a[76:79],v[196:199],v[28:31]\nv_exp_f32 v130,v130\nv_cndmask_b32 v190,v190,v252,s[86:87]\nM v[32:35],a[80:83],v[192:195],v[32:35]\nv_exp_f32 v131,v131\nM v[36:39],a[80:83],v[196:199],v[36:39]\nbuffer_load_dwordx4 v240,s[44:47],s66 offen offset:0 sc1 lds\nv_cndmask_b32 v191,v191,v252,s[88:89]\n" \
"M v[40:43],a[84:87],v[192:195],v[40:43]\nv_exp_f32 v132,v132\nv_cvt_pk_bf16_f32 v203,v154,v155\nM v[44:47],a[84:87],v[196:199],v[44:47]\nv_exp_f32 v133,v133\nv_cvt_pk_bf16_f32 v206,v156,v157\nM v[48:51],a[88:91],v[192:195],v[48:51]\nv_exp_f32 v134,v134\n" \
"M v[52:55],a[88:91],v[196:199],v[52:55]\nbuffer_load_dwordx4 v240,s[44:47],s66 offen offset:1024 sc1 lds\nv_cvt_pk_bf16_f32 v207,v158,v159\nM v[56:59],a[92:95],v[192:195],v[56:59]\nv_exp_f32 v135,v135\nM v[60:63],a[92:95],v[196:199],v[60:63]\nv_exp_f32 v144,v144\nM a[192:195],a[208:211],v[192:195],a[192:195]\n" \
"v_exp_f32 v145,v145\nv_cvt_pk_bf16_f32 v192,v128,v129\nM a[196:199],a[208:211],v[196:199],a[196:199]\nbuffer_load_dwordx4 v240,s[44:47],s66 offen offset:2048 sc1 lds\nv_cvt_pk_bf16_f32 v193,v130,v131\nM v[0:3],a[96:99],v[208:211],v[0:3]\nv_exp_f32 v146,v146\nM v[4:7],a[96:99],v[212:215],v[4:7]\n" \
"v_exp_f32 v147,v147\nv_cvt_pk_bf16_f32 v194,v144,v145\nM v[8:11],a[100:103],v[208:211],v[8:11]\nv_exp_f32 v148,v148\nv_cvt_pk_bf16_f32 v195,v146,v147\nM v[12:15],a[100:103],v[212:215],v[12:15]\nbuffer_load_dwordx4 v240,s[44:47],s66 offen offset:3072 sc1 lds\ns_add_u32 m0,s64,98304\n" \
"M v[16:19],a[104:107],v[208:211],v[16:19]\nv_exp_f32 v149,v149\nM v[20:23],a[104:107],v[212:215],v[20:23]\nv_exp_f32 v150,v150\nv_cvt_pk_bf16_f32 v196,v132,v133\nM v[24:27],a[108:111],v[208:211],v[24:27]\nv_exp_f32 v151,v151\nv_cvt_pk_bf16_f32 v197,v134,v135\n" \
"M v[28:31],a[108:111],v[212:215],v[28:31]\nv_exp_f32 v160,v160\nM v[32:35],a[112:115],v[208:211],v[32:35]\nv_exp_f32 v161,v161\nv_cvt_pk_bf16_f32 v198,v148,v149\nM v[36:39],a[112:115],v[212:215],v[36:39]\nv_exp_f32 v162,v162\nv_cvt_pk_bf16_f32 v199,v150,v151\n" \
"M v[40:43],a[116:119],v[208:211],v[40:43]\nv_exp_f32 v163,v163\nM v[44:47],a[116:119],v[212:215],v[44:47]\nv_exp_f32 v164,v164\nM v[48:51],a[120:123],v[208:211],v[48:51]\nv_exp_f32 v165,v165\nM v[52:55],a[120:123],v[212:215],v[52:55]\nv_exp_f32 v166,v166\n" \
"M v[56:59],a[124:127],v[208:211],v[56:59]\nv_exp_f32 v167,v167\nM v[60:63],a[124:127],v[212:215],v[60:63]\nv_exp_f32 v176,v176\nM a[192:195],a[208:211],v[208:211],a[192:195]\nv_exp_f32 v177,v177\nv_exp_f32 v178,v178\nv_exp_f32 v179,v179\n" \
"v_exp_f32 v180,v180\nv_exp_f32 v181,v181\nv_exp_f32 v182,v182\nv_exp_f32 v183,v183\nv_exp_f32 v168,v168\nv_exp_f32 v169,v169\nv_exp_f32 v170,v170\nv_exp_f32 v171,v171\n" \
"buffer_load_dwordx4 v240,s[48:51],s66 offen offset:0 sc1 lds\nbuffer_load_dwordx4 v240,s[48:51],s66 offen offset:1024 sc1 lds\nbuffer_load_dwordx4 v240,s[48:51],s66 offen offset:2048 sc1 lds\nbuffer_load_dwordx4 v240,s[48:51],s66 offen offset:3072 sc1 lds\nM a[196:199],a[208:211],v[212:215],a[196:199]\ns_cmp_eq_u32 s62,0\ns_cbranch_scc1 .Ltrans\ns_branch .Ldual\n" \
".Lpro3:\ns_barrier\nM v[64:67],a[0:3],a[128:131],0\nds_read_b128 a[64:67],v241 offset:0\nds_read_b128 a[68:71],v241 offset:1024\nM v[64:67],a[4:7],a[132:135],v[64:67]\nds_read_b128 a[72:75],v241 offset:2048\ns_mov_b32 m0,s64\n" \
"v_mov_b32 v168,0\nM v[64:67],a[8:11],a[136:139],v[64:67]\nds_read_b128 a[76:79],v241 offset:3072\nbuffer_load_dwordx4 v240,s[44:47],s36 offen offset:0 sc1 lds\nM v[64:67],a[12:15],a[140:143],v[64:67]\nds_read_b128 a[80:83],v241 offset:4096\nv_mov_b32 v169,0\nM v[68:71],a[0:3],a[144:147],0\n" \
"ds_read_b128 a[0:3],v240 offset:16384\nv_mov_b32 v170,0\nv_mov_b32 v171,0\nM v[68:71],a[4:7],a[148:151],v[68:71]\nds_read_b128 a[4:7],v240 offset:17408\nv_max3_f32 v225,v64,v65,v66\nv_mov_b32 v172,0xff800000\nM v[68:71],a[8:11],a[152:155],v[68:71]\n" \
"ds_read_b128 a[8:11],v240 offset:18432\nv_mov_b32 v173,0xff800000\nM v[68:71],a[12:15],a[156:159],v[68:71]\nds_read_b128 a[12:15],v240 offset:19456\nbuffer_load_dwordx4 v240,s[44:47],s36 offen offset:1024 sc1 lds\nM v[72:75],a[16:19],a[128:131],0\nds_read_b128 a[84:87],v241 offset:5120\ns_lshl4_add_u32 s69,s13,s68\n" \
"v_mov_b32 v174,0xff800000\nM v[72:75],a[20:23],a[132:135],v[72:75]\nds_read_b128 a[88:91],v241 offset:6144\nv_max3_f32 v229,v68,v69,v70\nv_mov_b32 v175,0xff800000\nM v[72:75],a[24:27],a[136:139],v[72:75]\nds_read_b128 a[92:95],v241 offset:7168\nv_mov_b32 v184,0xff800000\n" \
"M v[72:75],a[28:31],a[140:143],v[72:75]\nds_read_b128 a[96:99],v241 offset:8192\nbuffer_load_dwordx4 v240,s[44:47],s36 offen offset:2048 sc1 lds\nM v[76:79],a[16:19],a[144:147],0\nds_read_b128 a[16:19],v240 offset:20480\nv_mov_b32 v185,0xff800000\nv_mov_b32 v186,0xff800000\nM v[76:79],a[20:23],a[148:151],v[76:79]\n" \
"ds_read_b128 a[20:23],v240 offset:21504\nv_max3_f32 v225,v225,v67,v72\nM v[76:79],a[24:27],a[152:155],v[76:79]\nv_max3_f32 v225,v225,v73,v74\nv_max_f32 v225,v225,v75\nv_cndmask_b32 v225,v224,v225,s[90:91]\nM v[76:79],a[28:31],a[156:159],v[76:79]\nds_read_b128 a[24:27],v240 offset:22528\n" \
"ds_read_b128 a[28:31],v240 offset:23552\nv_mov_b32 v187,0xff800000\nM v[80:83],a[32:35],a[128:131],0\nds_read_b128 a[100:103],v241 offset:9216\nv_mov_b32 v188,0xff800000\nM v[80:83],a[36:39],a[132:135],v[80:83]\nds_read_b128 a[104:107],v241 offset:10240\nv_max3_f32 v229,v229,v71,v76\n" \
"v_max3_f32 v229,v229,v77,v78\nM v[80:83],a[40:43],a[136:139],v[80:83]\nv_max_f32 v229,v229,v79\nv_cndmask_b32 v229,v228,v229,s[90:91]\nds_read_b128 a[108:111],v241 offset:11264\nM v[80:83],a[44:47],a[140:143],v[80:83]\nv_permlane16_swap_b32 v225,v229\nv_max_f32 v225,v225,v229\n" \
"v_mov_b32 v229,v225\nM v[84:87],a[32:35],a[144:147],0\nds_read_b128 a[32:35],v240 offset:24576\nv_permlane32_swap_b32 v225,v229\nM v[84:87],a[36:39],a[148:151],v[84:87]\nv_max_f32 v225,v225,v229\nv_mov_b32 v229,v225\nds_read_b128 a[36:39],v240 offset:25600\n" \
"M v[84:87],a[40:43],a[152:155],v[84:87]\nv_permlane16_swap_b32 v225,v229\nv_sub_f32 v224,0,v225\nv_sub_f32 v226,0,v225\nM v[84:87],a[44:47],a[156:159],v[84:87]\nv_sub_f32 v227,0,v225\nv_sub_f32 v225,0,v225\nM v[88:91],a[48:51],a[128:131],0\n" \
"v_sub_f32 v228,0,v229\nv_sub_f32 v230,0,v229\nv_sub_f32 v231,0,v229\nM v[88:91],a[52:55],a[132:135],v[88:91]\nv_sub_f32 v229,0,v229\nds_read_b128 a[40:43],v240 offset:26624\nds_read_b128 a[44:47],v240 offset:27648\nM v[88:91],a[56:59],a[136:139],v[88:91]\n" \
"v_add_f32 v64,v64,v224\nv_add_f32 v65,v65,v224\nv_add_f32 v66,v66,v224\nM v[88:91],a[60:63],a[140:143],v[88:91]\nv_add_f32 v67,v67,v224\nv_add_f32 v68,v68,v228\nM v[92:95],a[48:51],a[144:147],0\nds_read_b128 a[48:51],v240 offset:28672\n" \
"v_exp_f32 v64,v64\nM v[92:95],a[52:55],a[148:151],v[92:95]\nds_read_b128 a[52:55],v240 offset:29696\nv_exp_f32 v65,v65\nM v[92:95],a[56:59],a[152:155],v[92:95]\nds_read_b128 a[56:59],v240 offset:30720\nv_add_f32 v69,v69,v228\nM v[92:95],a[60:63],a[156:159],v[92:95]\n" \
"ds_read_b128 a[60:63],v240 offset:31744\nv_add_f32 v72,v72,v224\ns_waitcnt lgkmcnt(6)\nM v[96:99],a[0:3],a[128:131],v[224:227]\nv_exp_f32 v66,v66\nv_add_f32 v73,v73,v224\nM v[96:99],a[4:7],a[132:135],v[96:99]\nv_exp_f32 v67,v67\n" \
"M v[96:99],a[8:11],a[136:139],v[96:99]\nv_add_f32 v74,v74,v224\nv_add_f32 v75,v75,v224\nv_add_f32 v70,v70,v228\nM v[96:99],a[12:15],a[140:143],v[96:99]\nv_add_f32 v71,v71,v228\nv_exp_f32 v68,v68\nM v[100:103],a[0:3],a[144:147],v[228:231]\n" \
"v_exp_f32 v69,v69\nv_cvt_pk_bf16_f32 v64,v64,v65\nM v[100:103],a[4:7],a[148:151],v[100:103]\nv_exp_f32 v72,v72\nM v[100:103],a[8:11],a[152:155],v[100:103]\nv_exp_f32 v73,v73\nv_add_f32 v76,v76,v228\nM v[100:103],a[12:15],a[156:159],v[100:103]\n" \
"v_exp_f32 v74,v74\nv_add_f32 v77,v77,v228\nM v[104:107],a[16:19],a[128:131],v[224:227]\nv_exp_f32 v75,v75\nM v[104:107],a[20:23],a[132:135],v[104:107]\nv_exp_f32 v70,v70\nv_add_f32 v78,v78,v228\nM v[104:107],a[24:27],a[136:139],v[104:107]\n" \
"v_exp_f32 v71,v71\nv_add_f32 v79,v79,v228\nM v[104:107],a[28:31],a[140:143],v[104:107]\nv_cvt_pk_bf16_f32 v65,v66,v67\nv_cvt_pk_bf16_f32 v66,v72,v73\nM v[108:111],a[16:19],a[144:147],v[228:231]\nv_exp_f32 v76,v76\nv_cvt_pk_bf16_f32 v67,v74,v75\n" \
"M v[108:111],a[20:23],a[148:151],v[108:111]\nv_exp_f32 v77,v77\nv_cvt_pk_bf16_f32 v68,v68,v69\nM v[108:111],a[24:27],a[152:155],v[108:111]\nv_exp_f32 v78,v78\nv_cvt_pk_bf16_f32 v69,v70,v71\nM v[108:111],a[28:31],a[156:159],v[108:111]\nv_exp_f32 v79,v79\n" \
"M v[112:115],a[32:35],a[128:131],v[224:227]\nv_cvt_pk_bf16_f32 v70,v76,v77\nv_cvt_pk_bf16_f32 v71,v78,v79\nv_add_f32 v80,v80,v224\nM v[112:115],a[36:39],a[132:135],v[112:115]\nbuffer_load_dwordx4 v240,s[44:47],s36 offen offset:3072 sc1 lds\ns_waitcnt lgkmcnt(0)\nM v[112:115],a[40:43],a[136:139],v[112:115]\n" \
"v_add_f32 v81,v81,v224\nv_add_f32 v82,v82,v224\nM v[112:115],a[44:47],a[140:143],v[112:115]\nv_add_f32 v83,v83,v224\nv_exp_f32 v80,v80\nM v[116:119],a[32:35],a[144:147],v[228:231]\nv_exp_f32 v81,v81\nv_add_f32 v84,v84,v228\n" \
"M v[116:119],a[36:39],a[148:151],v[116:119]\nv_exp_f32 v82,v82\nv_add_f32 v85,v85,v228\nM v[116:119],a[40:43],a[152:155],v[116:119]\nv_exp_f32 v83,v83\nM v[116:119],a[44:47],a[156:159],v[116:119]\nv_add_f32 v88,v88,v224\nv_add_f32 v89,v89,v224\n" \
"v_add_f32 v90,v90,v224\nM v[124:127],a[48:51],a[144:147],v[228:231]\nv_add_f32 v91,v91,v224\nv_add_f32 v86,v86,v228\nv_add_f32 v87,v87,v228\nM v[124:127],a[52:55],a[148:151],v[124:127]\nv_exp_f32 v84,v84\nM v[124:127],a[56:59],a[152:155],v[124:127]\n" \
"v_exp_f32 v85,v85\nv_cvt_pk_bf16_f32 v80,v80,v81\nM v[124:127],a[60:63],a[156:159],v[124:127]\nv_exp_f32 v88,v88\nds_read_b128 a[112:115],v241 offset:12288\nM v[0:3],a[64:67],v[64:67],0\nv_exp_f32 v89,v89\nM v[4:7],a[64:67],v[68:71],0\n" \
"v_exp_f32 v90,v90\nv_add_f32 v92,v92,v228\nM v[8:11],a[68:71],v[64:67],0\nv_exp_f32 v91,v91\nv_add_f32 v93,v93,v228\nM v[12:15],a[68:71],v[68:71],0\nv_exp_f32 v86,v86\nv_add_f32 v94,v94,v228\n" \
"M v[16:19],a[72:75],v[64:67],0\nv_exp_f32 v87,v87\nM v[20:23],a[72:75],v[68:71],0\nv_add_f32 v95,v95,v228\nv_cvt_pk_bf16_f32 v81,v82,v83\nv_cvt_pk_bf16_f32 v82,v88,v89\nM v[24:27],a[76:79],v[64:67],0\nv_exp_f32 v92,v92\n" \
"v_cvt_pk_bf16_f32 v83,v90,v91\nM v[28:31],a[76:79],v[68:71],0\nv_exp_f32 v93,v93\nM v[32:35],a[80:83],v[64:67],0\nv_exp_f32 v94,v94\nv_cvt_pk_bf16_f32 v84,v84,v85\nM v[36:39],a[80:83],v[68:71],0\nv_exp_f32 v95,v95\n" \
"v_cvt_pk_bf16_f32 v85,v86,v87\nM v[40:43],a[84:87],v[64:67],0\nv_cvt_pk_bf16_f32 v86,v92,v93\nv_cvt_pk_bf16_f32 v87,v94,v95\nM v[44:47],a[84:87],v[68:71],0\nds_read_b128 a[116:119],v241 offset:13312\nds_read_b128 a[120:123],v241 offset:14336\nv_cndmask_b32 v112,v112,v252,s[82:83]\n" \
"M v[48:51],a[88:91],v[64:67],0\nds_read_b128 a[124:127],v241 offset:15360\nv_exp_f32 v96,v96\nM v[52:55],a[88:91],v[68:71],0\nds_read_b128 a[0:3],v240 offset:32768\nv_exp_f32 v97,v97\nM v[56:59],a[92:95],v[64:67],0\nbuffer_load_dword v248,v251,s[52:55],s80 offen sc1\n" \
"M v[60:63],a[92:95],v[68:71],0\nds_read_b128 a[4:7],v240 offset:33792\nds_read_b128 a[8:11],v240 offset:34816\nv_cvt_pk_bf16_f32 v96,v96,v97\nM a[192:195],a[208:211],v[64:67],0\nds_read_b128 a[12:15],v240 offset:35840\nv_exp_f32 v98,v98\nM a[196:199],a[208:211],v[68:71],0\n" \
"ds_read_b128 a[16:19],v240 offset:36864\nv_cndmask_b32 v113,v113,v252,s[84:85]\nM v[0:3],a[96:99],v[80:83],v[0:3]\nds_read_b128 a[20:23],v240 offset:37888\nv_exp_f32 v99,v99\nM v[4:7],a[96:99],v[84:87],v[4:7]\nds_read_b128 a[24:27],v240 offset:38912\nv_exp_f32 v100,v100\n" \
"M v[8:11],a[100:103],v[80:83],v[8:11]\nds_read_b128 a[28:31],v240 offset:39936\nv_exp_f32 v101,v101\nM v[12:15],a[100:103],v[84:87],v[12:15]\nds_read_b128 a[32:35],v240 offset:40960\nv_cvt_pk_bf16_f32 v97,v98,v99\nM v[16:19],a[104:107],v[80:83],v[16:19]\nds_read_b128 a[36:39],v240 offset:41984\n" \
"v_exp_f32 v104,v104\nM v[20:23],a[104:107],v[84:87],v[20:23]\nds_read_b128 a[40:43],v240 offset:43008\nv_exp_f32 v105,v105\nM v[24:27],a[108:111],v[80:83],v[24:27]\nds_read_b128 a[44:47],v240 offset:44032\nv_cvt_pk_bf16_f32 v98,v104,v105\nM v[28:31],a[108:111],v[84:87],v[28:31]\n" \
"ds_read_b128 a[48:51],v240 offset:45056\nv_cvt_pk_bf16_f32 v100,v100,v101\ns_waitcnt lgkmcnt(9)\nM v[32:35],a[112:115],v[80:83],v[32:35]\nds_read_b128 a[52:55],v240 offset:46080\nv_exp_f32 v106,v106\nM v[36:39],a[112:115],v[84:87],v[36:39]\nds_read_b128 a[56:59],v240 offset:47104\n" \
"v_cndmask_b32 v114,v114,v252,s[86:87]\nM v[40:43],a[116:119],v[80:83],v[40:43]\nds_read_b128 a[60:63],v240 offset:48128\nv_exp_f32 v107,v107\nM v[44:47],a[116:119],v[84:87],v[44:47]\nds_read_b128 a[64:67],v241 offset:16384\nv_exp_f32 v102,v102\nM v[48:51],a[120:123],v[80:83],v[48:51]\n" \
"ds_read_b128 a[68:71],v241 offset:17408\nv_exp_f32 v103,v103\nM v[52:55],a[120:123],v[84:87],v[52:55]\nds_read_b128 a[72:75],v241 offset:18432\nv_cvt_pk_bf16_f32 v99,v106,v107\nM v[56:59],a[124:127],v[80:83],v[56:59]\nds_read_b128 a[76:79],v241 offset:19456\nv_exp_f32 v108,v108\n" \
"M v[60:63],a[124:127],v[84:87],v[60:63]\nds_read_b128 a[80:83],v241 offset:20480\nv_exp_f32 v109,v109\nM a[192:195],a[208:211],v[80:83],a[192:195]\nds_read_b128 a[84:87],v241 offset:21504\nv_cvt_pk_bf16_f32 v101,v102,v103\nM a[196:199],a[208:211],v[84:87],a[196:199]\nds_read_b128 a[88:91],v241 offset:22528\n" \
"v_exp_f32 v110,v110\nM v[128:131],a[0:3],a[128:131],v[224:227]\nds_read_b128 a[92:95],v241 offset:23552\nv_exp_f32 v111,v111\nM v[128:131],a[4:7],a[132:135],v[128:131]\nds_read_b128 a[96:99],v241 offset:24576\nv_cvt_pk_bf16_f32 v102,v108,v109\nv_cvt_pk_bf16_f32 v103,v110,v111\n" \
"M v[128:131],a[8:11],a[136:139],v[128:131]\nds_read_b128 a[100:103],v241 offset:25600\nv_cndmask_b32 v115,v115,v252,s[88:89]\nM v[128:131],a[12:15],a[140:143],v[128:131]\nds_read_b128 a[104:107],v241 offset:26624\nv_exp_f32 v112,v112\nM v[132:135],a[0:3],a[144:147],v[228:231]\nds_read_b128 a[108:111],v241 offset:27648\n" \
"v_exp_f32 v113,v113\nM v[132:135],a[4:7],a[148:151],v[132:135]\nds_read_b128 a[112:115],v241 offset:28672\nv_cvt_pk_bf16_f32 v112,v112,v113\nM v[132:135],a[8:11],a[152:155],v[132:135]\nds_read_b128 a[116:119],v241 offset:29696\nv_exp_f32 v114,v114\nM v[132:135],a[12:15],a[156:159],v[132:135]\n" \
"ds_read_b128 a[120:123],v241 offset:30720\nv_cndmask_b32 v124,v124,v252,s[82:83]\ns_waitcnt lgkmcnt(9)\nM v[144:147],a[16:19],a[128:131],v[224:227]\nds_read_b128 a[124:127],v241 offset:31744\nv_cndmask_b32 v125,v125,v252,s[84:85]\nM v[144:147],a[20:23],a[132:135],v[144:147]\nv_exp_f32 v115,v115\n" \
"v_cndmask_b32 v126,v126,v252,s[86:87]\nM v[144:147],a[24:27],a[136:139],v[144:147]\nv_exp_f32 v116,v116\nv_cndmask_b32 v127,v127,v252,s[88:89]\nM v[144:147],a[28:31],a[140:143],v[144:147]\nv_exp_f32 v117,v117\nv_cvt_pk_bf16_f32 v113,v114,v115\nM v[148:151],a[16:19],a[144:147],v[228:231]\n" \
"v_exp_f32 v118,v118\nM v[148:151],a[20:23],a[148:151],v[148:151]\nv_exp_f32 v119,v119\nv_mov_b32 v114,0\nM v[148:151],a[24:27],a[152:155],v[148:151]\nv_exp_f32 v124,v124\nv_mov_b32 v115,0\nM v[148:151],a[28:31],a[156:159],v[148:151]\n" \
"v_exp_f32 v125,v125\nM v[160:163],a[32:35],a[128:131],v[224:227]\nv_exp_f32 v126,v126\nv_cvt_pk_bf16_f32 v116,v116,v117\nM v[160:163],a[36:39],a[132:135],v[160:163]\nv_exp_f32 v127,v127\nv_cvt_pk_bf16_f32 v117,v118,v119\nM v[160:163],a[40:43],a[136:139],v[160:163]\n" \
"v_cvt_pk_bf16_f32 v118,v124,v125\nv_cvt_pk_bf16_f32 v119,v126,v127\nM v[160:163],a[44:47],a[140:143],v[160:163]\nds_read_b128 a[0:3],v240 offset:49152\nds_read_b128 a[4:7],v240 offset:50176\ns_add_u32 m0,s64,131072\nM v[164:167],a[32:35],a[144:147],v[228:231]\nds_read_b128 a[8:11],v240 offset:51200\n" \
"v_exp_f32 v128,v128\nM v[164:167],a[36:39],a[148:151],v[164:167]\nds_read_b128 a[12:15],v240 offset:52224\nv_exp_f32 v129,v129\nM v[164:167],a[40:43],a[152:155],v[164:167]\nds_read_b128 a[16:19],v240 offset:53248\nv_cvt_pk_bf16_f32 v192,v128,v129\nM v[164:167],a[44:47],a[156:159],v[164:167]\n" \
"ds_read_b128 a[20:23],v240 offset:54272\nv_exp_f32 v130,v130\nM v[176:179],a[48:51],a[128:131],v[224:227]\nds_read_b128 a[24:27],v240 offset:55296\nv_exp_f32 v131,v131\nM v[176:179],a[52:55],a[132:135],v[176:179]\nds_read_b128 a[28:31],v240 offset:56320\nv_cvt_pk_bf16_f32 v193,v130,v131\n" \
"M v[176:179],a[56:59],a[136:139],v[176:179]\nds_read_b128 a[32:35],v240 offset:57344\nv_exp_f32 v132,v132\nM v[176:179],a[60:63],a[140:143],v[176:179]\nds_read_b128 a[36:39],v240 offset:58368\nv_exp_f32 v133,v133\nM v[180:183],a[48:51],a[144:147],v[228:231]\ns_waitcnt vmcnt(1) lgkmcnt(8)\n" \
"s_barrier\nds_read_b128 a[40:43],v240 offset:59392\nM v[180:183],a[52:55],a[148:151],v[180:183]\nds_read_b128 a[44:47],v240 offset:60416\nv_cvt_pk_bf16_f32 v196,v132,v133\nM v[180:183],a[56:59],a[152:155],v[180:183]\nds_read_b128 a[48:51],v240 offset:61440\nv_exp_f32 v134,v134\n" \
"M v[180:183],a[60:63],a[156:159],v[180:183]\nds_read_b128 a[52:55],v240 offset:62464\nv_exp_f32 v135,v135\nM v[0:3],a[64:67],v[96:99],v[0:3]\nds_read_b128 a[56:59],v240 offset:63488\nv_cvt_pk_bf16_f32 v197,v134,v135\nM v[4:7],a[64:67],v[100:103],v[4:7]\nds_read_b128 a[60:63],v240 offset:64512\n" \
"v_exp_f32 v144,v144\nM v[8:11],a[68:71],v[96:99],v[8:11]\nds_read_b128 a[64:67],v241 offset:32768\nv_exp_f32 v145,v145\nM v[12:15],a[68:71],v[100:103],v[12:15]\nds_read_b128 a[68:71],v241 offset:33792\nv_cvt_pk_bf16_f32 v194,v144,v145\nM v[16:19],a[72:75],v[96:99],v[16:19]\n" \
"v_exp_f32 v146,v146\ns_waitcnt vmcnt(0)\nv_cmp_eq_u32 vcc,0,v248\nM v[20:23],a[72:75],v[100:103],v[20:23]\nds_read_b128 a[72:75],v241 offset:34816\nv_exp_f32 v147,v147\nM v[24:27],a[76:79],v[96:99],v[24:27]\nv_cvt_pk_bf16_f32 v195,v146,v147\n" \
"v_exp_f32 v148,v148\nM v[28:31],a[76:79],v[100:103],v[28:31]\nds_read_b128 a[76:79],v241 offset:35840\ns_cbranch_vccnz .Lps5\n.Lpb5:\nM v[32:35],a[80:83],v[96:99],v[32:35]\nv_exp_f32 v149,v149\nv_mov_b32 v189,0xff800000\n" \
"M v[36:39],a[80:83],v[100:103],v[36:39]\nds_read_b128 a[80:83],v241 offset:36864\nv_exp_f32 v150,v150\nM v[40:43],a[84:87],v[96:99],v[40:43]\nv_exp_f32 v151,v151\nM v[44:47],a[84:87],v[100:103],v[44:47]\nds_read_b128 a[84:87],v241 offset:37888\nv_cvt_pk_bf16_f32 v198,v148,v149\n" \
"v_cvt_pk_bf16_f32 v199,v150,v151\nM v[48:51],a[88:91],v[96:99],v[48:51]\nv_exp_f32 v160,v160\nv_mov_b32 v190,0xff800000\nM v[52:55],a[88:91],v[100:103],v[52:55]\nds_read_b128 a[88:91],v241 offset:38912\nv_mov_b32 v191,0xff800000\nM v[56:59],a[92:95],v[96:99],v[56:59]\n" \
"v_exp_f32 v161,v161\nv_mov_b32 v202,0\nM v[60:63],a[92:95],v[100:103],v[60:63]\nds_read_b128 a[92:95],v241 offset:39936\nv_exp_f32 v162,v162\nM a[192:195],a[208:211],v[96:99],a[192:195]\nv_exp_f32 v163,v163\nv_cvt_pk_bf16_f32 v208,v160,v161\n" \
"M a[196:199],a[208:211],v[100:103],a[196:199]\nv_cvt_pk_bf16_f32 v209,v162,v163\nv_mov_b32 v203,0\nM v[0:3],a[96:99],v[112:115],v[0:3]\nv_exp_f32 v164,v164\nM v[4:7],a[96:99],v[116:119],v[4:7]\nds_read_b128 a[96:99],v241 offset:40960\nv_exp_f32 v165,v165\n" \
"M v[8:11],a[100:103],v[112:115],v[8:11]\nv_exp_f32 v166,v166\nM v[12:15],a[100:103],v[116:119],v[12:15]\nds_read_b128 a[100:103],v241 offset:41984\nv_exp_f32 v167,v167\nM v[16:19],a[104:107],v[112:115],v[16:19]\nv_cvt_pk_bf16_f32 v212,v164,v165\nv_exp_f32 v176,v176\n" \
"M v[20:23],a[104:107],v[116:119],v[20:23]\nds_read_b128 a[104:107],v241 offset:43008\nv_exp_f32 v177,v177\nM v[24:27],a[108:111],v[112:115],v[24:27]\nv_cvt_pk_bf16_f32 v213,v166,v167\nv_cvt_pk_bf16_f32 v210,v176,v177\nM v[28:31],a[108:111],v[116:119],v[28:31]\nds_read_b128 a[108:111],v241 offset:44032\n" \
"v_exp_f32 v178,v178\nM v[32:35],a[112:115],v[112:115],v[32:35]\nv_exp_f32 v179,v179\nM v[36:39],a[112:115],v[116:119],v[36:39]\nds_read_b128 a[112:115],v241 offset:45056\nv_cvt_pk_bf16_f32 v211,v178,v179\nM v[40:43],a[116:119],v[112:115],v[40:43]\nv_exp_f32 v180,v180\n" \
"M v[44:47],a[116:119],v[116:119],v[44:47]\nds_read_b128 a[116:119],v241 offset:46080\nv_exp_f32 v181,v181\nM v[48:51],a[120:123],v[112:115],v[48:51]\nv_exp_f32 v182,v182\nM v[52:55],a[120:123],v[116:119],v[52:55]\nds_read_b128 a[120:123],v241 offset:47104\nv_exp_f32 v183,v183\n" \
"M v[56:59],a[124:127],v[112:115],v[56:59]\nv_cvt_pk_bf16_f32 v214,v180,v181\nbuffer_load_dwordx4 v243,s[40:43],s69 offen offset:0 lds\nM v[60:63],a[124:127],v[116:119],v[60:63]\nds_read_b128 a[124:127],v241 offset:48128\nv_cvt_pk_bf16_f32 v215,v182,v183\nM a[192:195],a[208:211],v[112:115],a[192:195]\nM a[196:199],a[208:211],v[116:119],a[196:199]\n" \
"M v[64:67],a[244:247],a[244:247],0\nbuffer_load_dwordx4 v244,s[40:43],s69 offen offset:1024 lds\nM v[68:71],a[244:247],a[244:247],0\nM v[72:75],a[244:247],a[244:247],0\nM v[76:79],a[244:247],a[244:247],0\nM v[80:83],a[244:247],a[244:247],0\nbuffer_load_dwordx4 v245,s[40:43],s69 offen offset:2048 lds\nM v[84:87],a[244:247],a[244:247],0\n" \
"M v[88:91],a[244:247],a[244:247],0\nM v[92:95],a[244:247],a[244:247],0\nM v[96:99],a[244:247],a[244:247],0\nbuffer_load_dwordx4 v246,s[40:43],s69 offen offset:3072 lds\nM v[100:103],a[244:247],a[244:247],0\ns_lshl4_add_u32 s69,s13,s68\ns_add_u32 s69,s69,0x10000\ns_add_u32 m0,s64,147456\n" \
"M v[104:107],a[244:247],a[244:247],0\nM v[108:111],a[244:247],a[244:247],0\nM v[112:115],a[244:247],a[244:247],0\nbuffer_load_dwordx4 v243,s[40:43],s69 offen offset:0 lds\nM v[116:119],a[244:247],a[244:247],0\nM v[120:123],a[244:247],a[244:247],0\nM v[124:127],a[244:247],a[244:247],0\nM a[200:203],a[244:247],a[244:247],0\n" \
"buffer_load_dwordx4 v244,s[40:43],s69 offen offset:1024 lds\nM a[204:207],a[244:247],a[244:247],0\nM v[136:139],a[0:3],a[160:163],0\nM v[136:139],a[4:7],a[164:167],v[136:139]\ns_waitcnt lgkmcnt(0)\nM v[136:139],a[8:11],a[168:171],v[136:139]\nbuffer_load_dwordx4 v245,s[40:43],s69 offen offset:2048 lds\nM v[136:139],a[12:15],a[172:175],v[136:139]\n" \
"M v[140:143],a[0:3],a[176:179],0\nM v[140:143],a[4:7],a[180:183],v[140:143]\nM v[140:143],a[8:11],a[184:187],v[140:143]\nbuffer_load_dwordx4 v246,s[40:43],s69 offen offset:3072 lds\ns_add_u32 m0,s64,65536\nM v[140:143],a[12:15],a[188:191],v[140:143]\nM v[156:159],a[16:19],a[176:179],0\nM v[156:159],a[20:23],a[180:183],v[156:159]\n" \
"v_max3_f32 v233,v136,v137,v138\nv_max_f32 v233,v233,v139\nv_cndmask_b32 v233,v232,v233,s[90:91]\nM v[156:159],a[24:27],a[184:187],v[156:159]\nbuffer_load_dwordx4 v240,s[48:51],s36 offen offset:0 sc1 lds\nM v[156:159],a[28:31],a[188:191],v[156:159]\nv_max3_f32 v237,v140,v141,v142\nv_cndmask_b32 v136,v136,v252,s[82:83]\n" \
"v_cndmask_b32 v137,v137,v252,s[84:85]\nM v[128:131],a[0:3],a[128:131],v[224:227]\nv_cndmask_b32 v138,v138,v252,s[86:87]\nv_cndmask_b32 v139,v139,v252,s[88:89]\nM v[128:131],a[4:7],a[132:135],v[128:131]\nM v[128:131],a[8:11],a[136:139],v[128:131]\ns_barrier\nv_max3_f32 v237,v237,v143,v156\n" \
"M v[128:131],a[12:15],a[140:143],v[128:131]\nv_max3_f32 v237,v237,v157,v158\nv_max_f32 v237,v237,v159\nv_cndmask_b32 v237,v236,v237,s[90:91]\nM v[132:135],a[0:3],a[144:147],v[228:231]\nds_read_b128 a[0:3],v240 offset:0\nbuffer_load_dwordx4 v240,s[48:51],s36 offen offset:1024 sc1 lds\nM v[132:135],a[4:7],a[148:151],v[132:135]\n" \
"ds_read_b128 a[4:7],v240 offset:1024\nv_permlane16_swap_b32 v233,v237\nM v[132:135],a[8:11],a[152:155],v[132:135]\nds_read_b128 a[8:11],v240 offset:2048\nv_max_f32 v233,v233,v237\nv_mov_b32 v237,v233\nM v[132:135],a[12:15],a[156:159],v[132:135]\nds_read_b128 a[12:15],v240 offset:3072\n" \
"v_permlane32_swap_b32 v233,v237\nv_max_f32 v233,v233,v237\nM v[144:147],a[16:19],a[128:131],v[224:227]\nbuffer_load_dwordx4 v240,s[48:51],s36 offen offset:2048 sc1 lds\nM v[144:147],a[20:23],a[132:135],v[144:147]\nv_mov_b32 v237,v233\nv_cndmask_b32 v156,v156,v252,s[82:83]\nv_cndmask_b32 v157,v157,v252,s[84:85]\n" \
"M v[144:147],a[24:27],a[136:139],v[144:147]\nv_permlane16_swap_b32 v233,v237\nv_cndmask_b32 v158,v158,v252,s[86:87]\nv_cndmask_b32 v159,v159,v252,s[88:89]\nM v[144:147],a[28:31],a[140:143],v[144:147]\nv_sub_f32 v232,0,v233\nv_sub_f32 v236,0,v237\nv_add_f32 v136,v136,v232\n" \
"M v[148:151],a[16:19],a[144:147],v[228:231]\nds_read_b128 a[16:19],v240 offset:4096\nv_add_f32 v137,v137,v232\nM v[148:151],a[20:23],a[148:151],v[148:151]\nds_read_b128 a[20:23],v240 offset:5120\nbuffer_load_dwordx4 v240,s[48:51],s36 offen offset:3072 sc1 lds\nM v[148:151],a[24:27],a[152:155],v[148:151]\nds_read_b128 a[24:27],v240 offset:6144\n" \
"s_add_u32 m0,s64,16384\nv_add_f32 v138,v138,v232\nM v[148:151],a[28:31],a[156:159],v[148:151]\nds_read_b128 a[28:31],v240 offset:7168\nv_add_f32 v139,v139,v232\nM v[160:163],a[32:35],a[128:131],v[224:227]\nv_add_f32 v140,v140,v236\nv_add_f32 v141,v141,v236\n" \
"v_add_f32 v142,v142,v236\nM v[160:163],a[36:39],a[132:135],v[160:163]\nbuffer_load_dwordx4 v240,s[44:47],s65 offen offset:0 sc1 lds\nv_add_f32 v143,v143,v236\nM v[160:163],a[40:43],a[136:139],v[160:163]\nv_add_f32 v156,v156,v236\nv_add_f32 v157,v157,v236\nM v[160:163],a[44:47],a[140:143],v[160:163]\n" \
"v_add_f32 v158,v158,v236\nv_add_f32 v159,v159,v236\nv_sub_f32 v234,0,v233\nM v[164:167],a[32:35],a[144:147],v[228:231]\nds_read_b128 a[32:35],v240 offset:8192\nv_exp_f32 v136,v136\nM v[164:167],a[36:39],a[148:151],v[164:167]\nds_read_b128 a[36:39],v240 offset:9216\n" \
"buffer_load_dwordx4 v240,s[44:47],s65 offen offset:1024 sc1 lds\nM v[164:167],a[40:43],a[152:155],v[164:167]\nds_read_b128 a[40:43],v240 offset:10240\nv_sub_f32 v235,0,v233\nM v[164:167],a[44:47],a[156:159],v[164:167]\nds_read_b128 a[44:47],v240 offset:11264\nv_exp_f32 v137,v137\nM v[176:179],a[48:51],a[128:131],v[224:227]\n" \
"v_exp_f32 v138,v138\nv_sub_f32 v238,0,v237\nM v[176:179],a[52:55],a[132:135],v[176:179]\nbuffer_load_dwordx4 v240,s[44:47],s65 offen offset:2048 sc1 lds\nM v[176:179],a[56:59],a[136:139],v[176:179]\nv_exp_f32 v139,v139\nv_sub_f32 v239,0,v237\nM v[176:179],a[60:63],a[140:143],v[176:179]\n" \
"v_exp_f32 v140,v140\nv_sub_f32 v233,0,v233\nM v[180:183],a[48:51],a[144:147],v[228:231]\nds_read_b128 a[48:51],v240 offset:12288\nv_exp_f32 v141,v141\nM v[180:183],a[52:55],a[148:151],v[180:183]\nbuffer_load_dwordx4 v240,s[44:47],s65 offen offset:3072 sc1 lds\nM v[180:183],a[56:59],a[152:155],v[180:183]\n" \
"ds_read_b128 a[52:55],v240 offset:13312\ns_add_u32 m0,s64,81920\nds_read_b128 a[56:59],v240 offset:14336\nM v[180:183],a[60:63],a[156:159],v[180:183]\nds_read_b128 a[60:63],v240 offset:15360\nv_exp_f32 v142,v142\nM v[0:3],a[64:67],v[192:195],v[0:3]\nv_exp_f32 v143,v143\n" \
"M v[4:7],a[64:67],v[196:199],v[4:7]\nbuffer_load_dwordx4 v240,s[48:51],s65 offen offset:0 sc1 lds\nv_sub_f32 v237,0,v237\nM v[8:11],a[68:71],v[192:195],v[8:11]\nv_exp_f32 v156,v156\nv_cvt_pk_bf16_f32 v200,v136,v137\nM v[12:15],a[68:71],v[196:199],v[12:15]\nv_exp_f32 v157,v157\n" \
"M v[16:19],a[72:75],v[192:195],v[16:19]\nv_exp_f32 v158,v158\nv_cvt_pk_bf16_f32 v201,v138,v139\nM v[20:23],a[72:75],v[196:199],v[20:23]\nbuffer_load_dwordx4 v240,s[48:51],s65 offen offset:1024 sc1 lds\nv_cvt_pk_bf16_f32 v204,v140,v141\nM v[24:27],a[76:79],v[192:195],v[24:27]\nv_exp_f32 v159,v159\n" \
"v_cvt_pk_bf16_f32 v205,v142,v143\nM v[28:31],a[76:79],v[196:199],v[28:31]\nv_exp_f32 v128,v128\nM v[32:35],a[80:83],v[192:195],v[32:35]\nv_exp_f32 v129,v129\nv_cvt_pk_bf16_f32 v206,v156,v157\nM v[36:39],a[80:83],v[196:199],v[36:39]\nbuffer_load_dwordx4 v240,s[48:51],s65 offen offset:2048 sc1 lds\n" \
"v_cvt_pk_bf16_f32 v207,v158,v159\nM v[40:43],a[84:87],v[192:195],v[40:43]\nv_exp_f32 v130,v130\nM v[44:47],a[84:87],v[196:199],v[44:47]\nv_exp_f32 v131,v131\nM v[48:51],a[88:91],v[192:195],v[48:51]\nv_exp_f32 v132,v132\nM v[52:55],a[88:91],v[196:199],v[52:55]\n" \
"buffer_load_dwordx4 v240,s[48:51],s65 offen offset:3072 sc1 lds\nM v[56:59],a[92:95],v[192:195],v[56:59]\ns_add_u32 m0,s64,32768\nv_exp_f32 v133,v133\nM v[60:63],a[92:95],v[196:199],v[60:63]\nv_exp_f32 v134,v134\nM a[192:195],a[208:211],v[192:195],a[192:195]\nv_exp_f32 v135,v135\n" \
"v_cvt_pk_bf16_f32 v192,v128,v129\nM a[196:199],a[208:211],v[196:199],a[196:199]\nbuffer_load_dwordx4 v240,s[44:47],s66 offen offset:0 sc1 lds\nM v[0:3],a[96:99],v[208:211],v[0:3]\nv_exp_f32 v144,v144\nv_cvt_pk_bf16_f32 v193,v130,v131\nM v[4:7],a[96:99],v[212:215],v[4:7]\nv_exp_f32 v145,v145\n" \
"v_cvt_pk_bf16_f32 v196,v132,v133\nM v[8:11],a[100:103],v[208:211],v[8:11]\nv_exp_f32 v146,v146\nM v[12:15],a[100:103],v[212:215],v[12:15]\nbuffer_load_dwordx4 v240,s[44:47],s66 offen offset:1024 sc1 lds\nv_cvt_pk_bf16_f32 v194,v144,v145\nM v[16:19],a[104:107],v[208:211],v[16:19]\nv_exp_f32 v147,v147\n" \
"v_cvt_pk_bf16_f32 v197,v134,v135\nM v[20:23],a[104:107],v[212:215],v[20:23]\nv_exp_f32 v148,v148\nv_cvt_pk_bf16_f32 v195,v146,v147\nM v[24:27],a[108:111],v[208:211],v[24:27]\nv_exp_f32 v149,v149\nM v[28:31],a[108:111],v[212:215],v[28:31]\nbuffer_load_dwordx4 v240,s[44:47],s66 offen offset:2048 sc1 lds\n" \
"v_cvt_pk_bf16_f32 v198,v148,v149\nM v[32:35],a[112:115],v[208:211],v[32:35]\nv_exp_f32 v150,v150\nM v[36:39],a[112:115],v[212:215],v[36:39]\nv_exp_f32 v151,v151\nM v[40:43],a[116:119],v[208:211],v[40:43]\nv_exp_f32 v160,v160\nv_cvt_pk_bf16_f32 v199,v150,v151\n" \
"M v[44:47],a[116:119],v[212:215],v[44:47]\nbuffer_load_dwordx4 v240,s[44:47],s66 offen offset:3072 sc1 lds\ns_add_u32 m0,s64,98304\nM v[48:51],a[120:123],v[208:211],v[48:51]\nv_exp_f32 v161,v161\nM v[52:55],a[120:123],v[212:215],v[52:55]\nv_exp_f32 v162,v162\nM v[56:59],a[124:127],v[208:211],v[56:59]\n" \
"v_exp_f32 v163,v163\nM v[60:63],a[124:127],v[212:215],v[60:63]\nv_exp_f32 v164,v164\nM a[192:195],a[208:211],v[208:211],a[192:195]\nv_exp_f32 v165,v165\nv_exp_f32 v166,v166\nv_exp_f32 v167,v167\nv_exp_f32 v176,v176\n" \
"v_exp_f32 v177,v177\nv_exp_f32 v178,v178\nv_exp_f32 v179,v179\nv_exp_f32 v180,v180\nv_exp_f32 v181,v181\nv_exp_f32 v182,v182\nv_exp_f32 v183,v183\nbuffer_load_dwordx4 v240,s[48:51],s66 offen offset:0 sc1 lds\n" \
"buffer_load_dwordx4 v240,s[48:51],s66 offen offset:1024 sc1 lds\nbuffer_load_dwordx4 v240,s[48:51],s66 offen offset:2048 sc1 lds\nbuffer_load_dwordx4 v240,s[48:51],s66 offen offset:3072 sc1 lds\nM a[196:199],a[208:211],v[212:215],a[196:199]\ns_cmp_eq_u32 s62,0\ns_cbranch_scc1 .Ltrans\n.Ldual:\ns_waitcnt lgkmcnt(4)\n" \
"M v[128:131],a[0:3],a[128:131],v[224:227]\ns_add_u32 m0,s94,s64\nv_cvt_pk_bf16_f32 v208,v160,v161\nM v[128:131],a[4:7],a[132:135],v[128:131]\nds_read_b128 a[64:67],v250 offset:0\nds_read_b128 a[68:71],v250 offset:1024\nM v[128:131],a[8:11],a[136:139],v[128:131]\nv_cvt_pk_bf16_f32 v209,v162,v163\n" \
"v_cvt_pk_bf16_f32 v212,v164,v165\nM v[128:131],a[12:15],a[140:143],v[128:131]\nv_cvt_pk_bf16_f32 v213,v166,v167\nv_cvt_pk_bf16_f32 v216,v168,v169\nM v[132:135],a[0:3],a[144:147],v[228:231]\nbuffer_load_dwordx4 v240,s[44:47],s95 offen offset:0 sc1 lds\nM v[132:135],a[4:7],a[148:151],v[132:135]\nv_cvt_pk_bf16_f32 v217,v170,v171\n" \
"v_cvt_pk_bf16_f32 v210,v176,v177\nM v[132:135],a[8:11],a[152:155],v[132:135]\nv_exp_f32 v172,v172\nM v[132:135],a[12:15],a[156:159],v[132:135]\nds_read_b128 a[72:75],v250 offset:2048\nds_read_b128 a[76:79],v250 offset:3072\nM v[136:139],a[0:3],a[160:163],v[232:235]\nv_exp_f32 v173,v173\n" \
"M v[136:139],a[4:7],a[164:167],v[136:139]\nv_exp_f32 v174,v174\nM v[136:139],a[8:11],a[168:171],v[136:139]\nv_exp_f32 v175,v175\nM v[136:139],a[12:15],a[172:175],v[136:139]\nv_cvt_pk_bf16_f32 v220,v172,v173\nv_cvt_pk_bf16_f32 v221,v174,v175\nM v[140:143],a[0:3],a[176:179],v[236:239]\n" \
"v_cvt_pk_bf16_f32 v211,v178,v179\nv_cvt_pk_bf16_f32 v214,v180,v181\nM v[140:143],a[4:7],a[180:183],v[140:143]\nds_read_b128 a[80:83],v250 offset:4096\nds_read_b128 a[84:87],v250 offset:5120\nM v[140:143],a[8:11],a[184:187],v[140:143]\nv_cvt_pk_bf16_f32 v215,v182,v183\ns_add_u32 s94,s94,0x4000\n" \
"M v[140:143],a[12:15],a[188:191],v[140:143]\nv_exp_f32 v184,v184\nM v[144:147],a[16:19],a[128:131],v[224:227]\nv_exp_f32 v185,v185\nM v[144:147],a[20:23],a[132:135],v[144:147]\nv_exp_f32 v186,v186\nM v[144:147],a[24:27],a[136:139],v[144:147]\nv_exp_f32 v187,v187\n" \
"M v[144:147],a[28:31],a[140:143],v[144:147]\nds_read_b128 a[88:91],v250 offset:6144\nds_read_b128 a[92:95],v250 offset:7168\nM v[148:151],a[16:19],a[144:147],v[228:231]\nv_cvt_pk_bf16_f32 v218,v184,v185\nv_cvt_pk_bf16_f32 v219,v186,v187\nM v[148:151],a[20:23],a[148:151],v[148:151]\nv_exp_f32 v188,v188\n" \
"M v[148:151],a[24:27],a[152:155],v[148:151]\nbuffer_load_dwordx4 v240,s[44:47],s95 offen offset:1024 sc1 lds\nM v[148:151],a[28:31],a[156:159],v[148:151]\nv_exp_f32 v189,v189\nM v[152:155],a[16:19],a[160:163],v[232:235]\nv_exp_f32 v190,v190\nM v[152:155],a[20:23],a[164:167],v[152:155]\nds_read_b128 a[96:99],v250 offset:8192\n" \
"ds_read_b128 a[100:103],v250 offset:9216\nM v[152:155],a[24:27],a[168:171],v[152:155]\nv_exp_f32 v191,v191\nM v[152:155],a[28:31],a[172:175],v[152:155]\nv_cvt_pk_bf16_f32 v222,v188,v189\nv_cvt_pk_bf16_f32 v223,v190,v191\nM v[156:159],a[16:19],a[176:179],v[236:239]\nv_exp_f32 v128,v128\n" \
"M v[156:159],a[20:23],a[180:183],v[156:159]\nv_exp_f32 v129,v129\nM v[156:159],a[24:27],a[184:187],v[156:159]\nv_exp_f32 v130,v130\nM v[156:159],a[28:31],a[188:191],v[156:159]\nds_read_b128 a[104:107],v250 offset:10240\nds_read_b128 a[108:111],v250 offset:11264\nM v[160:163],a[32:35],a[128:131],v[224:227]\n" \
"v_exp_f32 v131,v131\nM v[160:163],a[36:39],a[132:135],v[160:163]\nv_exp_f32 v144,v144\nM v[160:163],a[40:43],a[136:139],v[160:163]\nv_exp_f32 v145,v145\nM v[160:163],a[44:47],a[140:143],v[160:163]\nv_exp_f32 v146,v146\nM v[164:167],a[32:35],a[144:147],v[228:231]\n" \
"v_exp_f32 v147,v147\nM v[164:167],a[36:39],a[148:151],v[164:167]\nds_read_b128 a[112:115],v250 offset:12288\nds_read_b128 a[116:119],v250 offset:13312\nM v[164:167],a[40:43],a[152:155],v[164:167]\nv_exp_f32 v132,v132\nM v[164:167],a[44:47],a[156:159],v[164:167]\nv_exp_f32 v133,v133\n" \
"M v[168:171],a[32:35],a[160:163],v[232:235]\nbuffer_load_dwordx4 v240,s[44:47],s95 offen offset:2048 sc1 lds\nM v[168:171],a[36:39],a[164:167],v[168:171]\nv_exp_f32 v134,v134\nM v[168:171],a[40:43],a[168:171],v[168:171]\nv_exp_f32 v135,v135\nM v[168:171],a[44:47],a[172:175],v[168:171]\nds_read_b128 a[120:123],v250 offset:14336\n" \
"ds_read_b128 a[124:127],v250 offset:15360\nM v[172:175],a[32:35],a[176:179],v[236:239]\nv_xor_b32 v250,0x18000,v250\ns_and_b32 s94,s94,0xffff\nM v[172:175],a[36:39],a[180:183],v[172:175]\nv_exp_f32 v148,v148\nM v[172:175],a[40:43],a[184:187],v[172:175]\nv_exp_f32 v149,v149\n" \
"M v[172:175],a[44:47],a[188:191],v[172:175]\ns_waitcnt lgkmcnt(4)\nM v[176:179],a[48:51],a[128:131],v[224:227]\nv_exp_f32 v150,v150\nM v[176:179],a[52:55],a[132:135],v[176:179]\nv_exp_f32 v151,v151\nM v[176:179],a[56:59],a[136:139],v[176:179]\nv_exp_f32 v136,v136\n" \
"M v[176:179],a[60:63],a[140:143],v[176:179]\nv_exp_f32 v137,v137\nM v[180:183],a[48:51],a[144:147],v[228:231]\nv_exp_f32 v138,v138\nM v[180:183],a[52:55],a[148:151],v[180:183]\nv_exp_f32 v139,v139\nM v[180:183],a[56:59],a[152:155],v[180:183]\nv_exp_f32 v152,v152\n" \
"M v[180:183],a[60:63],a[156:159],v[180:183]\nv_exp_f32 v153,v153\nM v[184:187],a[48:51],a[160:163],v[232:235]\nv_exp_f32 v154,v154\nM v[184:187],a[52:55],a[164:167],v[184:187]\nv_exp_f32 v155,v155\nM v[184:187],a[56:59],a[168:171],v[184:187]\nbuffer_load_dwordx4 v240,s[44:47],s95 offen offset:3072 sc1 lds\n" \
"M v[184:187],a[60:63],a[172:175],v[184:187]\nv_exp_f32 v140,v140\nM v[188:191],a[48:51],a[176:179],v[236:239]\nv_exp_f32 v141,v141\nM v[188:191],a[52:55],a[180:183],v[188:191]\nv_exp_f32 v142,v142\nM v[188:191],a[56:59],a[184:187],v[188:191]\nv_exp_f32 v143,v143\n" \
"M v[188:191],a[60:63],a[188:191],v[188:191]\nv_exp_f32 v156,v156\nM v[0:3],a[64:67],v[192:195],v[0:3]\ns_waitcnt vmcnt(16) lgkmcnt(0)\ns_barrier\nM v[4:7],a[64:67],v[196:199],v[4:7]\nds_read_b128 a[0:3],v250 offset:0\nds_read_b128 a[4:7],v250 offset:1024\n" \
"M v[64:67],a[64:67],v[200:203],v[64:67]\nv_exp_f32 v157,v157\nM v[68:71],a[64:67],v[204:207],v[68:71]\nv_exp_f32 v158,v158\nM v[8:11],a[68:71],v[192:195],v[8:11]\ns_add_u32 m0,m0,0x10000\nM v[12:15],a[68:71],v[196:199],v[12:15]\nbuffer_load_dwordx4 v240,s[48:51],s95 offen offset:0 sc1 lds\n" \
"M v[72:75],a[68:71],v[200:203],v[72:75]\nv_exp_f32 v159,v159\nM v[76:79],a[68:71],v[204:207],v[76:79]\nv_exp_f32 v160,v160\nM v[16:19],a[72:75],v[192:195],v[16:19]\nv_exp_f32 v161,v161\nM v[20:23],a[72:75],v[196:199],v[20:23]\nds_read_b128 a[8:11],v250 offset:2048\n" \
"ds_read_b128 a[12:15],v250 offset:3072\nM v[80:83],a[72:75],v[200:203],v[80:83]\nv_exp_f32 v162,v162\nM v[84:87],a[72:75],v[204:207],v[84:87]\nv_exp_f32 v163,v163\nM v[24:27],a[76:79],v[192:195],v[24:27]\nv_exp_f32 v176,v176\nM v[28:31],a[76:79],v[196:199],v[28:31]\n" \
"v_exp_f32 v177,v177\nM v[88:91],a[76:79],v[200:203],v[88:91]\nv_exp_f32 v178,v178\nM v[92:95],a[76:79],v[204:207],v[92:95]\nv_exp_f32 v179,v179\nM v[32:35],a[80:83],v[192:195],v[32:35]\nv_exp_f32 v164,v164\nM v[36:39],a[80:83],v[196:199],v[36:39]\n" \
"ds_read_b128 a[16:19],v250 offset:4096\nds_read_b128 a[20:23],v250 offset:5120\nM v[96:99],a[80:83],v[200:203],v[96:99]\nv_exp_f32 v165,v165\nM v[100:103],a[80:83],v[204:207],v[100:103]\nv_exp_f32 v166,v166\nM v[40:43],a[84:87],v[192:195],v[40:43]\nv_exp_f32 v167,v167\n" \
"M v[44:47],a[84:87],v[196:199],v[44:47]\nbuffer_load_dwordx4 v240,s[48:51],s95 offen offset:1024 sc1 lds\nM v[104:107],a[84:87],v[200:203],v[104:107]\nv_exp_f32 v180,v180\nM v[108:111],a[84:87],v[204:207],v[108:111]\nv_exp_f32 v181,v181\nM v[48:51],a[88:91],v[192:195],v[48:51]\nv_exp_f32 v182,v182\n" \
"M v[52:55],a[88:91],v[196:199],v[52:55]\nds_read_b128 a[24:27],v250 offset:6144\nds_read_b128 a[28:31],v250 offset:7168\nM v[112:115],a[88:91],v[200:203],v[112:115]\nv_exp_f32 v183,v183\nM v[116:119],a[88:91],v[204:207],v[116:119]\nv_exp_f32 v168,v168\nM v[56:59],a[92:95],v[192:195],v[56:59]\n" \
"v_exp_f32 v169,v169\nM v[60:63],a[92:95],v[196:199],v[60:63]\nv_exp_f32 v170,v170\nM v[120:123],a[92:95],v[200:203],v[120:123]\nv_exp_f32 v171,v171\nM v[124:127],a[92:95],v[204:207],v[124:127]\nM a[192:195],a[208:211],v[192:195],a[192:195]\nv_cvt_pk_bf16_f32 v192,v128,v129\n" \
"v_cvt_pk_bf16_f32 v193,v130,v131\nM a[196:199],a[208:211],v[196:199],a[196:199]\nds_read_b128 a[32:35],v250 offset:8192\nds_read_b128 a[36:39],v250 offset:9216\nM a[200:203],a[208:211],v[200:203],a[200:203]\nv_cvt_pk_bf16_f32 v194,v144,v145\nv_cvt_pk_bf16_f32 v195,v146,v147\nM a[204:207],a[208:211],v[204:207],a[204:207]\n" \
"v_cvt_pk_bf16_f32 v196,v132,v133\nv_cvt_pk_bf16_f32 v197,v134,v135\nM v[0:3],a[96:99],v[208:211],v[0:3]\nv_cvt_pk_bf16_f32 v198,v148,v149\nv_cvt_pk_bf16_f32 v199,v150,v151\nM v[4:7],a[96:99],v[212:215],v[4:7]\nbuffer_load_dwordx4 v240,s[48:51],s95 offen offset:2048 sc1 lds\nM v[64:67],a[96:99],v[216:219],v[64:67]\n" \
"v_cvt_pk_bf16_f32 v200,v136,v137\nv_cvt_pk_bf16_f32 v201,v138,v139\nM v[68:71],a[96:99],v[220:223],v[68:71]\nv_cvt_pk_bf16_f32 v202,v152,v153\nv_cvt_pk_bf16_f32 v203,v154,v155\nM v[8:11],a[100:103],v[208:211],v[8:11]\nv_cvt_pk_bf16_f32 v204,v140,v141\nv_cvt_pk_bf16_f32 v205,v142,v143\n" \
"M v[12:15],a[100:103],v[212:215],v[12:15]\nds_read_b128 a[40:43],v250 offset:10240\nds_read_b128 a[44:47],v250 offset:11264\nM v[72:75],a[100:103],v[216:219],v[72:75]\nv_cvt_pk_bf16_f32 v206,v156,v157\nv_cvt_pk_bf16_f32 v207,v158,v159\nM v[76:79],a[100:103],v[220:223],v[76:79]\nM v[16:19],a[104:107],v[208:211],v[16:19]\n" \
"M v[20:23],a[104:107],v[212:215],v[20:23]\nM v[80:83],a[104:107],v[216:219],v[80:83]\nM v[84:87],a[104:107],v[220:223],v[84:87]\nM v[24:27],a[108:111],v[208:211],v[24:27]\nM v[28:31],a[108:111],v[212:215],v[28:31]\nds_read_b128 a[48:51],v250 offset:12288\nds_read_b128 a[52:55],v250 offset:13312\nM v[88:91],a[108:111],v[216:219],v[88:91]\n" \
"M v[92:95],a[108:111],v[220:223],v[92:95]\nM v[32:35],a[112:115],v[208:211],v[32:35]\nM v[36:39],a[112:115],v[212:215],v[36:39]\nbuffer_load_dwordx4 v240,s[48:51],s95 offen offset:3072 sc1 lds\nM v[96:99],a[112:115],v[216:219],v[96:99]\ns_cmp_eq_u32 s95,s96\ns_cselect_b32 s72,0xc000,s98\ns_add_u32 s95,s95,s72\n" \
"M v[100:103],a[112:115],v[220:223],v[100:103]\nM v[40:43],a[116:119],v[208:211],v[40:43]\nM v[44:47],a[116:119],v[212:215],v[44:47]\nds_read_b128 a[56:59],v250 offset:14336\nds_read_b128 a[60:63],v250 offset:15360\nM v[104:107],a[116:119],v[216:219],v[104:107]\nv_add_u32 v250,s94,v241\nM v[108:111],a[116:119],v[220:223],v[108:111]\n" \
"M v[48:51],a[120:123],v[208:211],v[48:51]\nM v[52:55],a[120:123],v[212:215],v[52:55]\nM v[112:115],a[120:123],v[216:219],v[112:115]\nM v[116:119],a[120:123],v[220:223],v[116:119]\nM v[56:59],a[124:127],v[208:211],v[56:59]\nM v[60:63],a[124:127],v[212:215],v[60:63]\nM v[120:123],a[124:127],v[216:219],v[120:123]\nM v[124:127],a[124:127],v[220:223],v[124:127]\n" \
"M a[192:195],a[208:211],v[208:211],a[192:195]\nM a[196:199],a[208:211],v[212:215],a[196:199]\nM a[200:203],a[208:211],v[216:219],a[200:203]\nM a[204:207],a[208:211],v[220:223],a[204:207]\ns_cmp_lg_u32 s95,s97\ns_cbranch_scc1 .Ldual\ns_cmp_eq_u32 s62,7\ns_cbranch_scc1 .Lod1x\n" \
".Ltrans:\ns_waitcnt lgkmcnt(4)\nM v[128:131],a[0:3],a[128:131],v[224:227]\ns_add_u32 m0,s94,s64\nv_cvt_pk_bf16_f32 v208,v160,v161\nM v[128:131],a[4:7],a[132:135],v[128:131]\nds_read_b128 a[64:67],v250 offset:0\nds_read_b128 a[68:71],v250 offset:1024\n" \
"M v[128:131],a[8:11],a[136:139],v[128:131]\nbuffer_load_dwordx4 v240,s[44:47],s95 offen offset:0 sc1 lds\nM v[128:131],a[12:15],a[140:143],v[128:131]\nv_cvt_pk_bf16_f32 v209,v162,v163\nv_cvt_pk_bf16_f32 v212,v164,v165\nM v[132:135],a[0:3],a[144:147],v[228:231]\nds_read_b128 a[72:75],v250 offset:2048\nds_read_b128 a[76:79],v250 offset:3072\n" \
"M v[132:135],a[4:7],a[148:151],v[132:135]\nbuffer_store_dwordx4 a[248:251],v253,s[52:55],s57 offen offset:0 sc1\nM v[132:135],a[8:11],a[152:155],v[132:135]\nv_cvt_pk_bf16_f32 v213,v166,v167\nv_cvt_pk_bf16_f32 v210,v176,v177\nM v[132:135],a[12:15],a[156:159],v[132:135]\nds_read_b128 a[80:83],v250 offset:4096\nds_read_b128 a[84:87],v250 offset:5120\n" \
"M v[144:147],a[16:19],a[128:131],v[224:227]\nv_cvt_pk_bf16_f32 v211,v178,v179\nv_cvt_pk_bf16_f32 v214,v180,v181\nM v[144:147],a[20:23],a[132:135],v[144:147]\nv_cvt_pk_bf16_f32 v215,v182,v183\ns_add_u32 s71,s57,16384\nM v[144:147],a[24:27],a[136:139],v[144:147]\nds_read_b128 a[88:91],v250 offset:6144\n" \
"ds_read_b128 a[92:95],v250 offset:7168\nM v[144:147],a[28:31],a[140:143],v[144:147]\nbuffer_load_dwordx4 v240,s[44:47],s95 offen offset:1024 sc1 lds\nM v[148:151],a[16:19],a[144:147],v[228:231]\nv_exp_f32 v184,v184\nM v[148:151],a[20:23],a[148:151],v[148:151]\nds_read_b128 a[96:99],v250 offset:8192\nds_read_b128 a[100:103],v250 offset:9216\n" \
"M v[148:151],a[24:27],a[152:155],v[148:151]\nbuffer_store_dwordx4 a[248:251],v253,s[52:55],s57 offen offset:1024 sc1\nM v[148:151],a[28:31],a[156:159],v[148:151]\nv_exp_f32 v185,v185\nM v[160:163],a[32:35],a[128:131],v[224:227]\nds_read_b128 a[104:107],v250 offset:10240\nds_read_b128 a[108:111],v250 offset:11264\nM v[160:163],a[36:39],a[132:135],v[160:163]\n" \
"v_exp_f32 v186,v186\nM v[160:163],a[40:43],a[136:139],v[160:163]\nv_exp_f32 v187,v187\nM v[160:163],a[44:47],a[140:143],v[160:163]\nds_read_b128 a[112:115],v250 offset:12288\nds_read_b128 a[116:119],v250 offset:13312\nM v[164:167],a[32:35],a[144:147],v[228:231]\nbuffer_load_dwordx4 v240,s[44:47],s95 offen offset:2048 sc1 lds\n" \
"M v[164:167],a[36:39],a[148:151],v[164:167]\nv_exp_f32 v172,v172\nM v[164:167],a[40:43],a[152:155],v[164:167]\nds_read_b128 a[120:123],v250 offset:14336\nds_read_b128 a[124:127],v250 offset:15360\nM v[164:167],a[44:47],a[156:159],v[164:167]\nbuffer_store_dwordx4 a[248:251],v253,s[52:55],s57 offen offset:2048 sc1\ns_waitcnt lgkmcnt(6)\n" \
"M v[176:179],a[48:51],a[128:131],v[224:227]\nv_xor_b32 v250,0x18000,v250\nv_cvt_pk_bf16_f32 v216,v168,v169\nM v[176:179],a[52:55],a[132:135],v[176:179]\nv_exp_f32 v173,v173\nM v[176:179],a[56:59],a[136:139],v[176:179]\nv_exp_f32 v174,v174\nM v[176:179],a[60:63],a[140:143],v[176:179]\n" \
"v_exp_f32 v175,v175\nM v[180:183],a[48:51],a[144:147],v[228:231]\nv_exp_f32 v188,v188\nM v[180:183],a[52:55],a[148:151],v[180:183]\nbuffer_load_dwordx4 v240,s[44:47],s95 offen offset:3072 sc1 lds\nM v[180:183],a[56:59],a[152:155],v[180:183]\ns_add_u32 m0,m0,0x10000\nv_cvt_pk_bf16_f32 v217,v170,v171\n" \
"M v[180:183],a[60:63],a[156:159],v[180:183]\nv_exp_f32 v189,v189\nM v[0:3],a[64:67],v[192:195],v[0:3]\nbuffer_store_dwordx4 a[248:251],v253,s[52:55],s57 offen offset:3072 sc1\nM v[4:7],a[64:67],v[196:199],v[4:7]\nv_exp_f32 v190,v190\nM v[64:67],a[64:67],v[200:203],v[64:67]\ns_waitcnt vmcnt(16) lgkmcnt(0)\n" \
"s_barrier\nM v[68:71],a[64:67],v[204:207],v[68:71]\nds_read_b128 a[0:3],v250 offset:0\nds_read_b128 a[4:7],v250 offset:1024\nM v[8:11],a[68:71],v[192:195],v[8:11]\nv_exp_f32 v191,v191\nM v[12:15],a[68:71],v[196:199],v[12:15]\nbuffer_store_dwordx4 a[248:251],v253,s[52:55],s71 offen offset:0 sc1\n" \
"M v[72:75],a[68:71],v[200:203],v[72:75]\nv_cvt_pk_bf16_f32 v218,v184,v185\nv_cvt_pk_bf16_f32 v219,v186,v187\nM v[76:79],a[68:71],v[204:207],v[76:79]\nbuffer_load_dwordx4 v240,s[48:51],s95 offen offset:0 sc1 lds\nM v[16:19],a[72:75],v[192:195],v[16:19]\nv_cvt_pk_bf16_f32 v220,v172,v173\nv_cvt_pk_bf16_f32 v221,v174,v175\n" \
"M v[20:23],a[72:75],v[196:199],v[20:23]\nbuffer_store_dwordx4 a[248:251],v253,s[52:55],s71 offen offset:1024 sc1\nM v[80:83],a[72:75],v[200:203],v[80:83]\nv_cvt_pk_bf16_f32 v222,v188,v189\nv_cvt_pk_bf16_f32 v223,v190,v191\nM v[84:87],a[72:75],v[204:207],v[84:87]\nds_read_b128 a[8:11],v250 offset:2048\nds_read_b128 a[12:15],v250 offset:3072\n" \
"M v[24:27],a[76:79],v[192:195],v[24:27]\ns_add_u32 s70,s78,16384\ns_add_u32 s94,s94,0x4000\nM v[28:31],a[76:79],v[196:199],v[28:31]\nbuffer_store_dwordx4 a[248:251],v253,s[52:55],s71 offen offset:2048 sc1\nM v[88:91],a[76:79],v[200:203],v[88:91]\nv_exp_f32 v128,v128\nM v[92:95],a[76:79],v[204:207],v[92:95]\n" \
"v_exp_f32 v129,v129\nM v[32:35],a[80:83],v[192:195],v[32:35]\nv_exp_f32 v130,v130\nM v[36:39],a[80:83],v[196:199],v[36:39]\nbuffer_store_dwordx4 a[248:251],v253,s[52:55],s71 offen offset:3072 sc1\nM v[96:99],a[80:83],v[200:203],v[96:99]\nv_exp_f32 v131,v131\nM v[100:103],a[80:83],v[204:207],v[100:103]\n" \
"ds_read_b128 a[16:19],v250 offset:4096\nds_read_b128 a[20:23],v250 offset:5120\nM v[40:43],a[84:87],v[192:195],v[40:43]\nv_exp_f32 v144,v144\nM v[44:47],a[84:87],v[196:199],v[44:47]\nv_exp_f32 v145,v145\nM v[104:107],a[84:87],v[200:203],v[104:107]\nv_exp_f32 v146,v146\n" \
"M v[108:111],a[84:87],v[204:207],v[108:111]\nbuffer_load_dwordx4 v240,s[48:51],s95 offen offset:1024 sc1 lds\nM v[48:51],a[88:91],v[192:195],v[48:51]\nv_exp_f32 v147,v147\nM v[52:55],a[88:91],v[196:199],v[52:55]\nbuffer_store_dwordx4 a[248:251],v253,s[52:55],s78 offen offset:0 sc1\nM v[112:115],a[88:91],v[200:203],v[112:115]\nv_exp_f32 v132,v132\n" \
"M v[116:119],a[88:91],v[204:207],v[116:119]\nds_read_b128 a[24:27],v250 offset:6144\nds_read_b128 a[28:31],v250 offset:7168\nM v[56:59],a[92:95],v[192:195],v[56:59]\nv_exp_f32 v133,v133\nM v[60:63],a[92:95],v[196:199],v[60:63]\nbuffer_store_dwordx4 a[248:251],v253,s[52:55],s78 offen offset:1024 sc1\nM v[120:123],a[92:95],v[200:203],v[120:123]\n" \
"v_exp_f32 v134,v134\nM v[124:127],a[92:95],v[204:207],v[124:127]\nv_exp_f32 v135,v135\nM a[192:195],a[208:211],v[192:195],a[192:195]\nv_exp_f32 v148,v148\nM a[196:199],a[208:211],v[196:199],a[196:199]\nbuffer_store_dwordx4 a[248:251],v253,s[52:55],s78 offen offset:2048 sc1\nM a[200:203],a[208:211],v[200:203],a[200:203]\n" \
"v_exp_f32 v149,v149\nM a[204:207],a[208:211],v[204:207],a[204:207]\nds_read_b128 a[32:35],v250 offset:8192\nds_read_b128 a[36:39],v250 offset:9216\nM v[0:3],a[96:99],v[208:211],v[0:3]\nv_exp_f32 v150,v150\nM v[4:7],a[96:99],v[212:215],v[4:7]\nv_exp_f32 v151,v151\n" \
"M v[64:67],a[96:99],v[216:219],v[64:67]\ns_and_b32 s94,s94,0xffff\nv_cvt_pk_bf16_f32 v192,v128,v129\nM v[68:71],a[96:99],v[220:223],v[68:71]\nbuffer_load_dwordx4 v240,s[48:51],s95 offen offset:2048 sc1 lds\nM v[8:11],a[100:103],v[208:211],v[8:11]\nv_cvt_pk_bf16_f32 v193,v130,v131\nv_cvt_pk_bf16_f32 v194,v144,v145\n" \
"M v[12:15],a[100:103],v[212:215],v[12:15]\nbuffer_store_dwordx4 a[248:251],v253,s[52:55],s78 offen offset:3072 sc1\nM v[72:75],a[100:103],v[216:219],v[72:75]\nv_cvt_pk_bf16_f32 v195,v146,v147\nv_cvt_pk_bf16_f32 v196,v132,v133\nM v[76:79],a[100:103],v[220:223],v[76:79]\nds_read_b128 a[40:43],v250 offset:10240\nds_read_b128 a[44:47],v250 offset:11264\n" \
"M v[16:19],a[104:107],v[208:211],v[16:19]\nv_cvt_pk_bf16_f32 v197,v134,v135\nv_cvt_pk_bf16_f32 v198,v148,v149\nM v[20:23],a[104:107],v[212:215],v[20:23]\nbuffer_store_dwordx4 a[248:251],v253,s[52:55],s70 offen offset:0 sc1\nM v[80:83],a[104:107],v[216:219],v[80:83]\nv_exp_f32 v160,v160\nM v[84:87],a[104:107],v[220:223],v[84:87]\n" \
"v_exp_f32 v161,v161\nM v[24:27],a[108:111],v[208:211],v[24:27]\nv_exp_f32 v162,v162\nM v[28:31],a[108:111],v[212:215],v[28:31]\nbuffer_store_dwordx4 a[248:251],v253,s[52:55],s70 offen offset:1024 sc1\nM v[88:91],a[108:111],v[216:219],v[88:91]\nv_exp_f32 v163,v163\nM v[92:95],a[108:111],v[220:223],v[92:95]\n" \
"ds_read_b128 a[48:51],v250 offset:12288\nds_read_b128 a[52:55],v250 offset:13312\nM v[32:35],a[112:115],v[208:211],v[32:35]\nv_exp_f32 v176,v176\nM v[36:39],a[112:115],v[212:215],v[36:39]\nv_exp_f32 v177,v177\nM v[96:99],a[112:115],v[216:219],v[96:99]\nv_exp_f32 v178,v178\n" \
"M v[100:103],a[112:115],v[220:223],v[100:103]\nbuffer_load_dwordx4 v240,s[48:51],s95 offen offset:3072 sc1 lds\nM v[40:43],a[116:119],v[208:211],v[40:43]\nv_cvt_pk_bf16_f32 v199,v150,v151\ns_add_u32 s95,s95,0x4000\nM v[44:47],a[116:119],v[212:215],v[44:47]\nbuffer_store_dwordx4 a[248:251],v253,s[52:55],s70 offen offset:2048 sc1\nM v[104:107],a[116:119],v[216:219],v[104:107]\n" \
"v_exp_f32 v179,v179\nM v[108:111],a[116:119],v[220:223],v[108:111]\nds_read_b128 a[56:59],v250 offset:14336\nds_read_b128 a[60:63],v250 offset:15360\nM v[48:51],a[120:123],v[208:211],v[48:51]\nv_add_u32 v250,s94,v241\nM v[52:55],a[120:123],v[212:215],v[52:55]\nbuffer_store_dwordx4 a[248:251],v253,s[52:55],s70 offen offset:3072 sc1\n" \
"M v[112:115],a[120:123],v[216:219],v[112:115]\nM v[116:119],a[120:123],v[220:223],v[116:119]\nM v[56:59],a[124:127],v[208:211],v[56:59]\nM v[60:63],a[124:127],v[212:215],v[60:63]\nM v[120:123],a[124:127],v[216:219],v[120:123]\nM v[124:127],a[124:127],v[220:223],v[124:127]\nM a[192:195],a[208:211],v[208:211],a[192:195]\nM a[196:199],a[208:211],v[212:215],a[196:199]\n" \
"M a[200:203],a[208:211],v[216:219],a[200:203]\nM a[204:207],a[208:211],v[220:223],a[204:207]\ns_waitcnt lgkmcnt(4)\nM v[128:131],a[0:3],a[128:131],v[224:227]\ns_add_u32 m0,s94,s64\nv_cvt_pk_bf16_f32 v208,v160,v161\nv_cvt_pk_bf16_f32 v209,v162,v163\nv_cvt_pk_bf16_f32 v210,v176,v177\n" \
"M v[128:131],a[4:7],a[132:135],v[128:131]\nds_read_b128 a[64:67],v250 offset:0\nds_read_b128 a[68:71],v250 offset:1024\nv_exp_f32 v164,v164\nM v[128:131],a[8:11],a[136:139],v[128:131]\nbuffer_load_dwordx4 v240,s[44:47],s95 offen offset:0 sc1 lds\nv_exp_f32 v165,v165\nM v[128:131],a[12:15],a[140:143],v[128:131]\n" \
"v_exp_f32 v166,v166\nv_exp_f32 v167,v167\nM v[132:135],a[0:3],a[144:147],v[228:231]\nds_read_b128 a[72:75],v250 offset:2048\nds_read_b128 a[76:79],v250 offset:3072\nv_cvt_pk_bf16_f32 v212,v164,v165\nv_cvt_pk_bf16_f32 v213,v166,v167\nv_cvt_pk_bf16_f32 v211,v178,v179\n" \
"M v[132:135],a[4:7],a[148:151],v[132:135]\nv_exp_f32 v180,v180\nv_exp_f32 v181,v181\nM v[132:135],a[8:11],a[152:155],v[132:135]\nv_exp_f32 v182,v182\nv_exp_f32 v183,v183\nM v[132:135],a[12:15],a[156:159],v[132:135]\nds_read_b128 a[80:83],v250 offset:4096\n" \
"ds_read_b128 a[84:87],v250 offset:5120\nv_cvt_pk_bf16_f32 v214,v180,v181\nv_cvt_pk_bf16_f32 v215,v182,v183\nM v[144:147],a[16:19],a[128:131],v[224:227]\nv_accvgpr_read_b32 v232,a200\nv_accvgpr_read_b32 v236,a204\nv_add_f32 v233,v232,v236\nv_rcp_f32 v232,v232\n" \
"M v[144:147],a[20:23],a[132:135],v[144:147]\nv_rcp_f32 v236,v236\nv_add_f32 v233,v233,v232\nv_add_f32 v233,v233,v236\nM v[144:147],a[24:27],a[136:139],v[144:147]\nds_read_b128 a[88:91],v250 offset:6144\nds_read_b128 a[92:95],v250 offset:7168\nv_cmp_ngt_f32 vcc,s28,v233\n" \
"s_add_u32 s37,s12,s67\nM v[144:147],a[28:31],a[140:143],v[144:147]\nbuffer_load_dwordx4 v240,s[44:47],s95 offen offset:1024 sc1 lds\nv_mul_f32 v64,v64,v232\nv_mul_f32 v65,v65,v232\nM v[148:151],a[16:19],a[144:147],v[228:231]\ns_and_b64 vcc,vcc,s[90:91]\ns_cselect_b32 s39,0x8000000,s16\n" \
"s_cselect_b32 s16,s16,0x8000000\ns_cselect_b32 s71,2,1\ns_addc_u32 s99,s99,0\nM v[148:151],a[20:23],a[148:151],v[148:151]\nds_read_b128 a[96:99],v250 offset:8192\nds_read_b128 a[100:103],v250 offset:9216\nv_mul_f32 v68,v68,v236\nv_mul_f32 v69,v69,v236\n" \
"M v[148:151],a[24:27],a[152:155],v[148:151]\nv_mul_f32 v80,v80,v232\nv_mul_f32 v81,v81,v232\nv_mul_f32 v84,v84,v236\nv_mul_f32 v85,v85,v236\nM v[148:151],a[28:31],a[156:159],v[148:151]\nv_mul_f32 v96,v96,v232\nv_mul_f32 v97,v97,v232\n" \
"v_mul_f32 v100,v100,v236\nv_mul_f32 v101,v101,v236\nM v[160:163],a[32:35],a[128:131],v[224:227]\nds_read_b128 a[104:107],v250 offset:10240\nds_read_b128 a[108:111],v250 offset:11264\nv_mul_f32 v112,v112,v232\nv_mul_f32 v113,v113,v232\nM v[160:163],a[36:39],a[132:135],v[160:163]\n" \
"v_mul_f32 v116,v116,v236\nv_mul_f32 v117,v117,v236\ns_add_u32 s37,s37,1048576\nv_mul_f32 v66,v66,v232\nv_mul_f32 v67,v67,v232\nM v[160:163],a[40:43],a[136:139],v[160:163]\nv_cvt_pk_bf16_f32 v64,v64,v65\nv_mul_f32 v70,v70,v236\n" \
"v_mul_f32 v71,v71,v236\nv_cvt_pk_bf16_f32 v68,v68,v69\nM v[160:163],a[44:47],a[140:143],v[160:163]\nds_read_b128 a[112:115],v250 offset:12288\nds_read_b128 a[116:119],v250 offset:13312\nv_mul_f32 v82,v82,v232\nv_mul_f32 v83,v83,v232\nM v[164:167],a[32:35],a[144:147],v[228:231]\n" \
"buffer_load_dwordx4 v240,s[44:47],s95 offen offset:2048 sc1 lds\nv_cvt_pk_bf16_f32 v80,v80,v81\nv_mul_f32 v86,v86,v236\nM v[164:167],a[36:39],a[148:151],v[164:167]\nv_mul_f32 v87,v87,v236\nv_cvt_pk_bf16_f32 v84,v84,v85\nv_mul_f32 v98,v98,v232\nv_mul_f32 v99,v99,v232\n" \
"v_cvt_pk_bf16_f32 v96,v96,v97\nM v[164:167],a[40:43],a[152:155],v[164:167]\nds_read_b128 a[120:123],v250 offset:14336\nds_read_b128 a[124:127],v250 offset:15360\nv_xor_b32 v250,0x18000,v250\nv_mul_f32 v102,v102,v236\nM v[164:167],a[44:47],a[156:159],v[164:167]\nv_mul_f32 v103,v103,v236\n" \
"v_cvt_pk_bf16_f32 v100,v100,v101\nv_mul_f32 v114,v114,v232\ns_waitcnt lgkmcnt(4)\nM v[176:179],a[48:51],a[128:131],v[224:227]\nv_mul_f32 v115,v115,v232\nv_cvt_pk_bf16_f32 v112,v112,v113\nv_mul_f32 v118,v118,v236\nv_mul_f32 v119,v119,v236\n" \
"M v[176:179],a[52:55],a[132:135],v[176:179]\nv_cvt_pk_bf16_f32 v116,v116,v117\ns_or_b32 s37,s37,s39\nv_mul_f32 v72,v72,v232\nv_mul_f32 v73,v73,v232\nv_mul_f32 v74,v74,v232\nM v[176:179],a[56:59],a[136:139],v[176:179]\nv_mul_f32 v75,v75,v232\n" \
"v_cvt_pk_bf16_f32 v65,v66,v67\nv_mul_f32 v76,v76,v236\nv_mul_f32 v77,v77,v236\nM v[176:179],a[60:63],a[140:143],v[176:179]\nv_mul_f32 v78,v78,v236\nv_mul_f32 v79,v79,v236\nv_cvt_pk_bf16_f32 v69,v70,v71\nv_mul_f32 v88,v88,v232\n" \
"M v[180:183],a[48:51],a[144:147],v[228:231]\nv_mul_f32 v89,v89,v232\nv_mul_f32 v90,v90,v232\nv_mul_f32 v91,v91,v232\nv_cvt_pk_bf16_f32 v81,v82,v83\nM v[180:183],a[52:55],a[148:151],v[180:183]\nbuffer_load_dwordx4 v240,s[44:47],s95 offen offset:3072 sc1 lds\nv_mul_f32 v92,v92,v236\n" \
"v_mul_f32 v93,v93,v236\nM v[180:183],a[56:59],a[152:155],v[180:183]\nv_mul_f32 v94,v94,v236\nv_mul_f32 v95,v95,v236\nv_cvt_pk_bf16_f32 v85,v86,v87\nv_mul_f32 v104,v104,v232\nv_mul_f32 v105,v105,v232\nM v[180:183],a[60:63],a[156:159],v[180:183]\n" \
"v_mul_f32 v106,v106,v232\nv_mul_f32 v107,v107,v232\nv_cvt_pk_bf16_f32 v97,v98,v99\nv_mul_f32 v108,v108,v236\nM v[0:3],a[64:67],v[192:195],v[0:3]\nv_mul_f32 v109,v109,v236\nv_mul_f32 v110,v110,v236\nv_mul_f32 v111,v111,v236\n" \
"v_cvt_pk_bf16_f32 v101,v102,v103\nM v[4:7],a[64:67],v[196:199],v[4:7]\nv_mul_f32 v120,v120,v232\nv_mul_f32 v121,v121,v232\nv_mul_f32 v122,v122,v232\nv_mul_f32 v123,v123,v232\nM v[8:11],a[68:71],v[192:195],v[8:11]\ns_waitcnt vmcnt(16) lgkmcnt(0)\n" \
"s_barrier\nv_cvt_pk_bf16_f32 v113,v114,v115\nv_mul_f32 v124,v124,v236\nv_mul_f32 v125,v125,v236\nM v[12:15],a[68:71],v[196:199],v[12:15]\nds_read_b128 a[0:3],v250 offset:0\nds_read_b128 a[4:7],v250 offset:1024\nv_mul_f32 v126,v126,v236\n" \
"v_mul_f32 v127,v127,v236\nM v[16:19],a[72:75],v[192:195],v[16:19]\nv_cvt_pk_bf16_f32 v117,v118,v119\ns_add_u32 s38,s37,4096\nv_cvt_pk_bf16_f32 v66,v72,v73\nv_cvt_pk_bf16_f32 v67,v74,v75\nM v[20:23],a[72:75],v[196:199],v[20:23]\nv_cvt_pk_bf16_f32 v70,v76,v77\n" \
"v_cvt_pk_bf16_f32 v71,v78,v79\nv_cvt_pk_bf16_f32 v82,v88,v89\nv_cvt_pk_bf16_f32 v83,v90,v91\nv_cvt_pk_bf16_f32 v86,v92,v93\nv_cvt_pk_bf16_f32 v87,v94,v95\nv_cvt_pk_bf16_f32 v98,v104,v105\nv_cvt_pk_bf16_f32 v99,v106,v107\nv_cvt_pk_bf16_f32 v102,v108,v109\n" \
"v_cvt_pk_bf16_f32 v103,v110,v111\nv_cvt_pk_bf16_f32 v114,v120,v121\nv_cvt_pk_bf16_f32 v115,v122,v123\nv_cvt_pk_bf16_f32 v118,v124,v125\nv_cvt_pk_bf16_f32 v119,v126,v127\ns_add_u32 m0,m0,0x10000\nbuffer_store_dwordx4 v[64:67],v240,s[52:55],s37 offen offset:0 sc1\nbuffer_store_dwordx4 v[68:71],v240,s[52:55],s37 offen offset:1024 sc1\n" \
"buffer_store_dwordx4 v[80:83],v240,s[52:55],s37 offen offset:2048 sc1\nbuffer_store_dwordx4 v[84:87],v240,s[52:55],s37 offen offset:3072 sc1\nbuffer_store_dwordx4 v[96:99],v240,s[52:55],s38 offen offset:0 sc1\nbuffer_store_dwordx4 v[100:103],v240,s[52:55],s38 offen offset:1024 sc1\nbuffer_store_dwordx4 v[112:115],v240,s[52:55],s38 offen offset:2048 sc1\nbuffer_store_dwordx4 v[116:119],v240,s[52:55],s38 offen offset:3072 sc1\nbuffer_load_dwordx4 v240,s[48:51],s95 offen offset:0 sc1 lds\nM v[24:27],a[76:79],v[192:195],v[24:27]\n" \
"s_lshl2_add_u32 s77,s61,s79\nv_exp_f32 v128,v128\nv_exp_f32 v129,v129\nM v[28:31],a[76:79],v[196:199],v[28:31]\nds_read_b128 a[8:11],v250 offset:2048\nds_read_b128 a[12:15],v250 offset:3072\nv_exp_f32 v130,v130\nM v[32:35],a[80:83],v[192:195],v[32:35]\n" \
"v_exp_f32 v131,v131\nv_exp_f32 v144,v144\nM v[36:39],a[80:83],v[196:199],v[36:39]\nv_exp_f32 v145,v145\nv_exp_f32 v146,v146\nM v[40:43],a[84:87],v[192:195],v[40:43]\nv_exp_f32 v147,v147\nv_exp_f32 v132,v132\n" \
"M v[44:47],a[84:87],v[196:199],v[44:47]\nds_read_b128 a[16:19],v250 offset:4096\nds_read_b128 a[20:23],v250 offset:5120\nv_exp_f32 v133,v133\ns_add_u32 s94,s94,0x4000\nM v[48:51],a[88:91],v[192:195],v[48:51]\nv_exp_f32 v134,v134\nv_exp_f32 v135,v135\n" \
"M v[52:55],a[88:91],v[196:199],v[52:55]\nbuffer_load_dwordx4 v240,s[48:51],s95 offen offset:1024 sc1 lds\nv_exp_f32 v148,v148\nM v[56:59],a[92:95],v[192:195],v[56:59]\nv_exp_f32 v149,v149\nv_exp_f32 v150,v150\nM v[60:63],a[92:95],v[196:199],v[60:63]\nds_read_b128 a[24:27],v250 offset:6144\n" \
"ds_read_b128 a[28:31],v250 offset:7168\nv_exp_f32 v151,v151\ns_add_u32 s77,s77,4096\nM a[192:195],a[208:211],v[192:195],a[192:195]\nv_mov_b32 v233,s71\ns_and_b32 s94,s94,0xffff\nv_cvt_pk_bf16_f32 v192,v128,v129\nv_cvt_pk_bf16_f32 v193,v130,v131\n" \
"M a[196:199],a[208:211],v[196:199],a[196:199]\nv_cvt_pk_bf16_f32 v194,v144,v145\nv_cvt_pk_bf16_f32 v195,v146,v147\nv_cvt_pk_bf16_f32 v196,v132,v133\nv_cvt_pk_bf16_f32 v197,v134,v135\nM v[0:3],a[96:99],v[208:211],v[0:3]\nv_cvt_pk_bf16_f32 v198,v148,v149\nv_cvt_pk_bf16_f32 v199,v150,v151\n" \
"v_exp_f32 v160,v160\nM v[4:7],a[96:99],v[212:215],v[4:7]\nds_read_b128 a[32:35],v250 offset:8192\nds_read_b128 a[36:39],v250 offset:9216\nv_exp_f32 v161,v161\nM v[8:11],a[100:103],v[208:211],v[8:11]\nv_exp_f32 v162,v162\nv_exp_f32 v163,v163\n" \
"M v[12:15],a[100:103],v[212:215],v[12:15]\nbuffer_load_dwordx4 v240,s[48:51],s95 offen offset:2048 sc1 lds\nv_exp_f32 v176,v176\nM v[16:19],a[104:107],v[208:211],v[16:19]\nv_exp_f32 v177,v177\nv_exp_f32 v178,v178\nM v[20:23],a[104:107],v[212:215],v[20:23]\nds_read_b128 a[40:43],v250 offset:10240\n" \
"ds_read_b128 a[44:47],v250 offset:11264\nv_exp_f32 v179,v179\nM v[24:27],a[108:111],v[208:211],v[24:27]\nM v[28:31],a[108:111],v[212:215],v[28:31]\nM v[32:35],a[112:115],v[208:211],v[32:35]\nM v[36:39],a[112:115],v[212:215],v[36:39]\nds_read_b128 a[48:51],v250 offset:12288\nds_read_b128 a[52:55],v250 offset:13312\n" \
"M v[40:43],a[116:119],v[208:211],v[40:43]\nM v[44:47],a[116:119],v[212:215],v[44:47]\nbuffer_load_dwordx4 v240,s[48:51],s95 offen offset:3072 sc1 lds\ns_add_u32 s95,s95,0x4000\nM v[48:51],a[120:123],v[208:211],v[48:51]\nM v[52:55],a[120:123],v[212:215],v[52:55]\nds_read_b128 a[56:59],v250 offset:14336\nds_read_b128 a[60:63],v250 offset:15360\n" \
"v_add_u32 v250,s94,v241\nM v[56:59],a[124:127],v[208:211],v[56:59]\nM v[60:63],a[124:127],v[212:215],v[60:63]\nM a[192:195],a[208:211],v[208:211],a[192:195]\nM a[196:199],a[208:211],v[212:215],a[196:199]\ns_waitcnt vmcnt(4)\nbuffer_store_dword v233,off,s[52:55],s77 sc1\n.Lsingle:\n" \
"s_waitcnt lgkmcnt(4)\nM v[128:131],a[0:3],a[128:131],v[224:227]\ns_add_u32 m0,s94,s64\nv_cvt_pk_bf16_f32 v208,v160,v161\nM v[128:131],a[4:7],a[132:135],v[128:131]\nds_read_b128 a[64:67],v250 offset:0\nds_read_b128 a[68:71],v250 offset:1024\nM v[128:131],a[8:11],a[136:139],v[128:131]\n" \
"buffer_load_dwordx4 v240,s[44:47],s95 offen offset:0 sc1 lds\nM v[128:131],a[12:15],a[140:143],v[128:131]\nv_cvt_pk_bf16_f32 v209,v162,v163\nv_cvt_pk_bf16_f32 v210,v176,v177\nM v[132:135],a[0:3],a[144:147],v[228:231]\nds_read_b128 a[72:75],v250 offset:2048\nds_read_b128 a[76:79],v250 offset:3072\nM v[132:135],a[4:7],a[148:151],v[132:135]\n" \
"v_exp_f32 v164,v164\nM v[132:135],a[8:11],a[152:155],v[132:135]\nv_exp_f32 v165,v165\nM v[132:135],a[12:15],a[156:159],v[132:135]\nds_read_b128 a[80:83],v250 offset:4096\nds_read_b128 a[84:87],v250 offset:5120\nM v[144:147],a[16:19],a[128:131],v[224:227]\nv_exp_f32 v166,v166\n" \
"M v[144:147],a[20:23],a[132:135],v[144:147]\nv_exp_f32 v167,v167\nM v[144:147],a[24:27],a[136:139],v[144:147]\nds_read_b128 a[88:91],v250 offset:6144\nds_read_b128 a[92:95],v250 offset:7168\nM v[144:147],a[28:31],a[140:143],v[144:147]\nbuffer_load_dwordx4 v240,s[44:47],s95 offen offset:1024 sc1 lds\nM v[148:151],a[16:19],a[144:147],v[228:231]\n" \
"v_cvt_pk_bf16_f32 v212,v164,v165\nv_cvt_pk_bf16_f32 v213,v166,v167\nM v[148:151],a[20:23],a[148:151],v[148:151]\nds_read_b128 a[96:99],v250 offset:8192\nds_read_b128 a[100:103],v250 offset:9216\nM v[148:151],a[24:27],a[152:155],v[148:151]\nv_cvt_pk_bf16_f32 v211,v178,v179\ns_add_u32 s94,s94,0x4000\n" \
"M v[148:151],a[28:31],a[156:159],v[148:151]\nv_exp_f32 v180,v180\nM v[160:163],a[32:35],a[128:131],v[224:227]\nds_read_b128 a[104:107],v250 offset:10240\nds_read_b128 a[108:111],v250 offset:11264\nM v[160:163],a[36:39],a[132:135],v[160:163]\nv_exp_f32 v181,v181\nM v[160:163],a[40:43],a[136:139],v[160:163]\n" \
"v_exp_f32 v182,v182\nM v[160:163],a[44:47],a[140:143],v[160:163]\nds_read_b128 a[112:115],v250 offset:12288\nds_read_b128 a[116:119],v250 offset:13312\nM v[164:167],a[32:35],a[144:147],v[228:231]\nbuffer_load_dwordx4 v240,s[44:47],s95 offen offset:2048 sc1 lds\nM v[164:167],a[36:39],a[148:151],v[164:167]\nv_exp_f32 v183,v183\n" \
"M v[164:167],a[40:43],a[152:155],v[164:167]\nds_read_b128 a[120:123],v250 offset:14336\nds_read_b128 a[124:127],v250 offset:15360\nM v[164:167],a[44:47],a[156:159],v[164:167]\nv_cvt_pk_bf16_f32 v214,v180,v181\ns_waitcnt lgkmcnt(7)\nM v[176:179],a[48:51],a[128:131],v[224:227]\nv_cvt_pk_bf16_f32 v215,v182,v183\n" \
"v_xor_b32 v250,0x18000,v250\nM v[176:179],a[52:55],a[132:135],v[176:179]\nv_exp_f32 v128,v128\nM v[176:179],a[56:59],a[136:139],v[176:179]\nv_exp_f32 v129,v129\nM v[176:179],a[60:63],a[140:143],v[176:179]\nv_exp_f32 v130,v130\nM v[180:183],a[48:51],a[144:147],v[228:231]\n" \
"v_exp_f32 v131,v131\nM v[180:183],a[52:55],a[148:151],v[180:183]\nbuffer_load_dwordx4 v240,s[44:47],s95 offen offset:3072 sc1 lds\nM v[180:183],a[56:59],a[152:155],v[180:183]\ns_add_u32 m0,m0,0x10000\ns_and_b32 s94,s94,0xffff\nM v[180:183],a[60:63],a[156:159],v[180:183]\nv_exp_f32 v144,v144\n" \
"M v[0:3],a[64:67],v[192:195],v[0:3]\nv_exp_f32 v145,v145\nM v[4:7],a[64:67],v[196:199],v[4:7]\nv_exp_f32 v146,v146\nM v[8:11],a[68:71],v[192:195],v[8:11]\ns_waitcnt vmcnt(16) lgkmcnt(0)\ns_barrier\nM v[12:15],a[68:71],v[196:199],v[12:15]\n" \
"ds_read_b128 a[0:3],v250 offset:0\nds_read_b128 a[4:7],v250 offset:1024\nM v[16:19],a[72:75],v[192:195],v[16:19]\nv_exp_f32 v147,v147\nM v[20:23],a[72:75],v[196:199],v[20:23]\nbuffer_load_dwordx4 v240,s[48:51],s95 offen offset:0 sc1 lds\nM v[24:27],a[76:79],v[192:195],v[24:27]\nv_exp_f32 v132,v132\n" \
"M v[28:31],a[76:79],v[196:199],v[28:31]\nds_read_b128 a[8:11],v250 offset:2048\nds_read_b128 a[12:15],v250 offset:3072\nM v[32:35],a[80:83],v[192:195],v[32:35]\nv_exp_f32 v133,v133\nM v[36:39],a[80:83],v[196:199],v[36:39]\nv_exp_f32 v134,v134\nM v[40:43],a[84:87],v[192:195],v[40:43]\n" \
"v_exp_f32 v135,v135\nM v[44:47],a[84:87],v[196:199],v[44:47]\nds_read_b128 a[16:19],v250 offset:4096\nds_read_b128 a[20:23],v250 offset:5120\nM v[48:51],a[88:91],v[192:195],v[48:51]\nv_exp_f32 v148,v148\nM v[52:55],a[88:91],v[196:199],v[52:55]\nbuffer_load_dwordx4 v240,s[48:51],s95 offen offset:1024 sc1 lds\n" \
"M v[56:59],a[92:95],v[192:195],v[56:59]\nv_exp_f32 v149,v149\nM v[60:63],a[92:95],v[196:199],v[60:63]\nds_read_b128 a[24:27],v250 offset:6144\nds_read_b128 a[28:31],v250 offset:7168\nM a[192:195],a[208:211],v[192:195],a[192:195]\nv_exp_f32 v150,v150\nM a[196:199],a[208:211],v[196:199],a[196:199]\n" \
"v_exp_f32 v151,v151\nM v[0:3],a[96:99],v[208:211],v[0:3]\nv_cvt_pk_bf16_f32 v192,v128,v129\nv_cvt_pk_bf16_f32 v193,v130,v131\nM v[4:7],a[96:99],v[212:215],v[4:7]\nds_read_b128 a[32:35],v250 offset:8192\nds_read_b128 a[36:39],v250 offset:9216\nM v[8:11],a[100:103],v[208:211],v[8:11]\n" \
"v_cvt_pk_bf16_f32 v194,v144,v145\nv_cvt_pk_bf16_f32 v195,v146,v147\nM v[12:15],a[100:103],v[212:215],v[12:15]\nbuffer_load_dwordx4 v240,s[48:51],s95 offen offset:2048 sc1 lds\nM v[16:19],a[104:107],v[208:211],v[16:19]\nv_cvt_pk_bf16_f32 v196,v132,v133\nv_cvt_pk_bf16_f32 v197,v134,v135\nM v[20:23],a[104:107],v[212:215],v[20:23]\n" \
"ds_read_b128 a[40:43],v250 offset:10240\nds_read_b128 a[44:47],v250 offset:11264\nM v[24:27],a[108:111],v[208:211],v[24:27]\nv_cvt_pk_bf16_f32 v198,v148,v149\nv_cvt_pk_bf16_f32 v199,v150,v151\nM v[28:31],a[108:111],v[212:215],v[28:31]\nv_exp_f32 v160,v160\nM v[32:35],a[112:115],v[208:211],v[32:35]\n" \
"v_exp_f32 v161,v161\nM v[36:39],a[112:115],v[212:215],v[36:39]\nds_read_b128 a[48:51],v250 offset:12288\nds_read_b128 a[52:55],v250 offset:13312\nM v[40:43],a[116:119],v[208:211],v[40:43]\nv_exp_f32 v162,v162\nM v[44:47],a[116:119],v[212:215],v[44:47]\nbuffer_load_dwordx4 v240,s[48:51],s95 offen offset:3072 sc1 lds\n" \
"M v[48:51],a[120:123],v[208:211],v[48:51]\nv_exp_f32 v163,v163\nM v[52:55],a[120:123],v[212:215],v[52:55]\nds_read_b128 a[56:59],v250 offset:14336\nds_read_b128 a[60:63],v250 offset:15360\nM v[56:59],a[124:127],v[208:211],v[56:59]\nv_exp_f32 v176,v176\nM v[60:63],a[124:127],v[212:215],v[60:63]\n" \
"v_exp_f32 v177,v177\nM a[192:195],a[208:211],v[208:211],a[192:195]\nv_exp_f32 v178,v178\nv_exp_f32 v179,v179\nv_add_u32 v250,s94,v241\ns_add_u32 s95,s95,0x4000\nM a[196:199],a[208:211],v[212:215],a[196:199]\ns_cmp_lg_u32 s95,s15\n" \
"s_cbranch_scc1 .Lsingle\ns_waitcnt lgkmcnt(2)\nM v[128:131],a[0:3],a[128:131],v[224:227]\nv_and_b32 v251,3,v249\nv_lshlrev_b32 v251,2,v251\nM v[128:131],a[4:7],a[132:135],v[128:131]\nds_read_b128 a[64:67],v250 offset:0\nds_read_b128 a[68:71],v250 offset:1024\n" \
"M v[128:131],a[8:11],a[136:139],v[128:131]\ns_add_u32 s80,s79,4096\nbuffer_load_dword v248,v251,s[52:55],s80 offen sc1\nM v[128:131],a[12:15],a[140:143],v[128:131]\nv_cvt_pk_bf16_f32 v208,v160,v161\nv_cvt_pk_bf16_f32 v209,v162,v163\nv_cvt_pk_bf16_f32 v210,v176,v177\nM v[132:135],a[0:3],a[144:147],v[228:231]\n" \
"ds_read_b128 a[72:75],v250 offset:2048\nds_read_b128 a[76:79],v250 offset:3072\nM v[132:135],a[4:7],a[148:151],v[132:135]\nv_exp_f32 v164,v164\nM v[132:135],a[8:11],a[152:155],v[132:135]\nv_exp_f32 v165,v165\nM v[132:135],a[12:15],a[156:159],v[132:135]\nds_read_b128 a[80:83],v250 offset:4096\n" \
"ds_read_b128 a[84:87],v250 offset:5120\nv_cvt_pk_bf16_f32 v212,v164,v165\nM v[144:147],a[16:19],a[128:131],v[224:227]\nv_exp_f32 v166,v166\nM v[144:147],a[20:23],a[132:135],v[144:147]\nv_exp_f32 v167,v167\nM v[144:147],a[24:27],a[136:139],v[144:147]\nds_read_b128 a[88:91],v250 offset:6144\n" \
"ds_read_b128 a[92:95],v250 offset:7168\nM v[144:147],a[28:31],a[140:143],v[144:147]\nv_cvt_pk_bf16_f32 v213,v166,v167\nv_cvt_pk_bf16_f32 v211,v178,v179\ns_lshl2_add_u32 s39,s62,s7\nM v[148:151],a[16:19],a[144:147],v[228:231]\nv_exp_f32 v180,v180\nM v[148:151],a[20:23],a[148:151],v[148:151]\n" \
"ds_read_b128 a[96:99],v250 offset:8192\nds_read_b128 a[100:103],v250 offset:9216\nM v[148:151],a[24:27],a[152:155],v[148:151]\nv_exp_f32 v181,v181\nM v[148:151],a[28:31],a[156:159],v[148:151]\nv_exp_f32 v182,v182\nv_cvt_pk_bf16_f32 v214,v180,v181\nM v[160:163],a[32:35],a[128:131],v[224:227]\n" \
"ds_read_b128 a[104:107],v250 offset:10240\nds_read_b128 a[108:111],v250 offset:11264\nM v[160:163],a[36:39],a[132:135],v[160:163]\nv_exp_f32 v183,v183\nM v[160:163],a[40:43],a[136:139],v[160:163]\nv_cvt_pk_bf16_f32 v215,v182,v183\ns_lshl_b32 s39,s39,5\nM v[160:163],a[44:47],a[140:143],v[160:163]\n" \
"ds_read_b128 a[112:115],v250 offset:12288\nds_read_b128 a[116:119],v250 offset:13312\ns_add_u32 s39,s39,s63\nM v[164:167],a[32:35],a[144:147],v[228:231]\ns_lshl2_add_u32 s39,s39,73728\ns_add_u32 s94,s94,0x4000\nM v[164:167],a[36:39],a[148:151],v[164:167]\ns_and_b32 s94,s94,0xffff\n" \
"s_add_u32 s65,s10,s67\nM v[164:167],a[40:43],a[152:155],v[164:167]\nds_read_b128 a[120:123],v250 offset:14336\nds_read_b128 a[124:127],v250 offset:15360\nM v[164:167],a[44:47],a[156:159],v[164:167]\nv_add_u32 v250,s94,v241\nv_exp_f32 v128,v128\nM v[176:179],a[48:51],a[128:131],v[224:227]\n" \
"v_exp_f32 v129,v129\nM v[176:179],a[52:55],a[132:135],v[176:179]\ns_add_u32 s65,s65,1048576\ns_waitcnt lgkmcnt(4)\nM v[176:179],a[56:59],a[136:139],v[176:179]\nv_exp_f32 v130,v130\nM v[176:179],a[60:63],a[140:143],v[176:179]\nv_exp_f32 v131,v131\n" \
"v_add_u32 v226,131072,v242\nM v[180:183],a[48:51],a[144:147],v[228:231]\nv_exp_f32 v144,v144\nM v[180:183],a[52:55],a[148:151],v[180:183]\nv_exp_f32 v145,v145\nM v[180:183],a[56:59],a[152:155],v[180:183]\ns_waitcnt vmcnt(1)\nbuffer_store_dword v254,off,s[52:55],s39 sc1\n" \
"M v[180:183],a[60:63],a[156:159],v[180:183]\ns_lshl4_add_u32 s69,s10,s68\ns_add_u32 m0,s64,0\ns_waitcnt vmcnt(1)\nv_and_b32 v248,s17,v248\nM v[0:3],a[64:67],v[192:195],v[0:3]\nv_exp_f32 v146,v146\nM v[4:7],a[64:67],v[196:199],v[4:7]\n" \
"v_exp_f32 v147,v147\nM v[8:11],a[68:71],v[192:195],v[8:11]\ns_waitcnt vmcnt(12) lgkmcnt(0)\ns_barrier\nM v[12:15],a[68:71],v[196:199],v[12:15]\nds_read_b128 a[64:67],v250 offset:0\nds_read_b128 a[68:71],v250 offset:1024\nv_cmp_eq_u32 vcc,0,v248\n" \
"M v[16:19],a[72:75],v[192:195],v[16:19]\nbuffer_load_dwordx4 v243,s[40:43],s69 offen offset:0 lds\nM v[20:23],a[72:75],v[196:199],v[20:23]\nds_read_b128 a[72:75],v250 offset:2048\ns_cbranch_vccnz .Lps6\n.Lpb6:\nM v[24:27],a[76:79],v[192:195],v[24:27]\nv_exp_f32 v132,v132\n" \
"M v[28:31],a[76:79],v[196:199],v[28:31]\nds_read_b128 a[76:79],v250 offset:3072\nv_exp_f32 v133,v133\nM v[32:35],a[80:83],v[192:195],v[32:35]\nbuffer_load_dwordx4 v244,s[40:43],s69 offen offset:1024 lds\nM v[36:39],a[80:83],v[196:199],v[36:39]\nds_read_b128 a[80:83],v250 offset:4096\nds_read_b64_tr_b16 a[212:213],v226 offset:0\n" \
"M v[40:43],a[84:87],v[192:195],v[40:43]\nv_exp_f32 v134,v134\nM v[44:47],a[84:87],v[196:199],v[44:47]\nv_exp_f32 v135,v135\nds_read_b128 a[84:87],v250 offset:5120\nM v[48:51],a[88:91],v[192:195],v[48:51]\nbuffer_load_dwordx4 v245,s[40:43],s69 offen offset:2048 lds\nM v[52:55],a[88:91],v[196:199],v[52:55]\n" \
"v_exp_f32 v148,v148\nM v[56:59],a[92:95],v[192:195],v[56:59]\nv_exp_f32 v149,v149\nM v[60:63],a[92:95],v[196:199],v[60:63]\nv_exp_f32 v150,v150\nds_read_b128 a[88:91],v250 offset:6144\nM a[192:195],a[208:211],v[192:195],a[192:195]\nbuffer_load_dwordx4 v246,s[40:43],s69 offen offset:3072 lds\n" \
"M a[196:199],a[208:211],v[196:199],a[196:199]\ns_lshl4_add_u32 s69,s10,s68\ns_add_u32 s69,s69,0x10000\nM v[0:3],a[96:99],v[208:211],v[0:3]\ns_add_u32 m0,s64,16384\nv_cvt_pk_bf16_f32 v192,v128,v129\nM v[4:7],a[96:99],v[212:215],v[4:7]\nv_exp_f32 v151,v151\n" \
"v_cvt_pk_bf16_f32 v193,v130,v131\nM v[8:11],a[100:103],v[208:211],v[8:11]\nbuffer_load_dwordx4 v243,s[40:43],s69 offen offset:0 lds\nM v[12:15],a[100:103],v[212:215],v[12:15]\nv_cvt_pk_bf16_f32 v194,v144,v145\nv_cvt_pk_bf16_f32 v195,v146,v147\nM v[16:19],a[104:107],v[208:211],v[16:19]\nv_cvt_pk_bf16_f32 v196,v132,v133\n" \
"v_cvt_pk_bf16_f32 v197,v134,v135\nM v[20:23],a[104:107],v[212:215],v[20:23]\nv_cvt_pk_bf16_f32 v198,v148,v149\nv_cvt_pk_bf16_f32 v199,v150,v151\nds_read_b128 a[92:95],v250 offset:7168\nM v[24:27],a[108:111],v[208:211],v[24:27]\nbuffer_load_dwordx4 v244,s[40:43],s69 offen offset:1024 lds\nM v[28:31],a[108:111],v[212:215],v[28:31]\n" \
"ds_read_b128 a[96:99],v250 offset:8192\nds_read_b128 a[100:103],v250 offset:9216\nM v[32:35],a[112:115],v[208:211],v[32:35]\nds_read_b128 a[104:107],v250 offset:10240\nv_xor_b32 v227,32,v226\nM v[36:39],a[112:115],v[212:215],v[36:39]\nds_read_b128 a[108:111],v250 offset:11264\nv_exp_f32 v160,v160\n" \
"M v[40:43],a[116:119],v[208:211],v[40:43]\nbuffer_load_dwordx4 v245,s[40:43],s69 offen offset:2048 lds\nM v[44:47],a[116:119],v[212:215],v[44:47]\nv_exp_f32 v161,v161\nM v[48:51],a[120:123],v[208:211],v[48:51]\nv_exp_f32 v162,v162\nM v[52:55],a[120:123],v[212:215],v[52:55]\nv_exp_f32 v163,v163\n" \
"ds_read_b128 a[112:115],v250 offset:12288\nM v[56:59],a[124:127],v[208:211],v[56:59]\nbuffer_load_dwordx4 v246,s[40:43],s69 offen offset:3072 lds\nM v[60:63],a[124:127],v[212:215],v[60:63]\nv_exp_f32 v176,v176\nM a[192:195],a[208:211],v[208:211],a[192:195]\nv_exp_f32 v177,v177\nM a[196:199],a[208:211],v[212:215],a[196:199]\n" \
"v_exp_f32 v178,v178\ns_waitcnt lgkmcnt(1)\nM v[0:3],a[64:67],v[192:195],v[0:3]\nbuffer_load_dwordx4 v[72:75],v240,s[52:55],s65 offen offset:0 sc1\nM v[4:7],a[64:67],v[196:199],v[4:7]\nv_exp_f32 v179,v179\nM v[8:11],a[68:71],v[192:195],v[8:11]\nv_exp_f32 v164,v164\n" \
"M v[12:15],a[68:71],v[196:199],v[12:15]\nv_exp_f32 v165,v165\nv_cvt_pk_bf16_f32 v208,v160,v161\nM v[16:19],a[72:75],v[192:195],v[16:19]\nv_exp_f32 v166,v166\nM v[20:23],a[72:75],v[196:199],v[20:23]\nbuffer_load_dwordx4 v[76:79],v240,s[52:55],s65 offen offset:1024 sc1\nM v[24:27],a[76:79],v[192:195],v[24:27]\n" \
"v_exp_f32 v167,v167\nM v[28:31],a[76:79],v[196:199],v[28:31]\nv_exp_f32 v180,v180\nv_cvt_pk_bf16_f32 v209,v162,v163\nM v[32:35],a[80:83],v[192:195],v[32:35]\nv_exp_f32 v181,v181\nM v[36:39],a[80:83],v[196:199],v[36:39]\nv_exp_f32 v182,v182\n" \
"M v[40:43],a[84:87],v[192:195],v[40:43]\nbuffer_load_dwordx4 v[88:91],v240,s[52:55],s65 offen offset:2048 sc1\nM v[44:47],a[84:87],v[196:199],v[44:47]\nv_exp_f32 v183,v183\nv_cvt_pk_bf16_f32 v210,v176,v177\nM v[48:51],a[88:91],v[192:195],v[48:51]\nv_cvt_pk_bf16_f32 v211,v178,v179\nv_cvt_pk_bf16_f32 v212,v164,v165\n" \
"M v[52:55],a[88:91],v[196:199],v[52:55]\nv_cvt_pk_bf16_f32 v213,v166,v167\nv_cvt_pk_bf16_f32 v214,v180,v181\nM v[56:59],a[92:95],v[192:195],v[56:59]\nv_cvt_pk_bf16_f32 v215,v182,v183\nds_read_b128 a[116:119],v250 offset:13312\nM v[60:63],a[92:95],v[196:199],v[60:63]\nbuffer_load_dwordx4 v[92:95],v240,s[52:55],s65 offen offset:3072 sc1\n" \
"ds_read_b128 a[120:123],v250 offset:14336\nM a[192:195],a[208:211],v[192:195],a[192:195]\ns_add_u32 s65,s65,4096\nds_read_b128 a[124:127],v250 offset:15360\nM a[196:199],a[208:211],v[196:199],a[196:199]\nds_read_b64_tr_b16 a[214:215],v226 offset:4096\nds_read_b64_tr_b16 a[216:217],v227 offset:0\nM v[0:3],a[96:99],v[208:211],v[0:3]\n" \
"ds_read_b64_tr_b16 a[218:219],v227 offset:4096\nds_read_b64_tr_b16 a[220:221],v226 offset:512\nM v[4:7],a[96:99],v[212:215],v[4:7]\nds_read_b64_tr_b16 a[222:223],v226 offset:4608\nds_read_b64_tr_b16 a[224:225],v227 offset:512\nds_read_b64_tr_b16 a[226:227],v227 offset:4608\nM v[8:11],a[100:103],v[208:211],v[8:11]\nbuffer_load_dwordx4 v[104:107],v240,s[52:55],s65 offen offset:0 sc1\n" \
"M v[12:15],a[100:103],v[212:215],v[12:15]\nds_read_b64_tr_b16 a[228:229],v226 offset:1024\nds_read_b64_tr_b16 a[230:231],v226 offset:5120\nM v[16:19],a[104:107],v[208:211],v[16:19]\nds_read_b64_tr_b16 a[232:233],v227 offset:1024\nds_read_b64_tr_b16 a[234:235],v227 offset:5120\nM v[20:23],a[104:107],v[212:215],v[20:23]\nds_read_b64_tr_b16 a[236:237],v226 offset:1536\n" \
"ds_read_b64_tr_b16 a[238:239],v226 offset:5632\nds_read_b64_tr_b16 a[240:241],v227 offset:1536\nM v[24:27],a[108:111],v[208:211],v[24:27]\nds_read_b64_tr_b16 a[242:243],v227 offset:5632\ns_add_u32 s37,s12,s67\nM v[28:31],a[108:111],v[212:215],v[28:31]\nbuffer_load_dwordx4 v[108:111],v240,s[52:55],s65 offen offset:1024 sc1\ns_waitcnt lgkmcnt(11)\n" \
"M v[32:35],a[112:115],v[208:211],v[32:35]\nv_cmp_eq_u32 vcc,2,v248\nv_xor_b32 v250,0x8000,v250\nM v[36:39],a[112:115],v[212:215],v[36:39]\ns_cmp_lg_u64 vcc,0\ns_addc_u32 s99,s99,0\nv_add_u32 v229,s64,v250\nM v[40:43],a[116:119],v[208:211],v[40:43]\n" \
"s_add_u32 s38,s37,4096\ns_lshr_b32 s70,s60,1\nM v[44:47],a[116:119],v[212:215],v[44:47]\ns_lshl_b32 s70,s70,7\ns_mul_i32 s71,s60,28\nM v[48:51],a[120:123],v[208:211],v[48:51]\nbuffer_load_dwordx4 v[120:123],v240,s[52:55],s65 offen offset:2048 sc1\nM v[52:55],a[120:123],v[212:215],v[52:55]\n" \
"s_lshl2_add_u32 s76,s61,s79\ns_lshl_b32 s80,s62,9\ns_add_u32 s80,s80,73728\nM v[56:59],a[124:127],v[208:211],v[56:59]\nv_and_b32 v236,15,v249\ns_lshl2_add_u32 s74,s63,s7\nM v[60:63],a[124:127],v[212:215],v[60:63]\ns_lshl_b32 s72,s62,7\n" \
"v_lshrrev_b32 v235,4,v249\nM a[192:195],a[208:211],v[208:211],a[192:195]\ns_lshl4_add_u32 s69,s11,s68\ns_add_u32 m0,s64,32768\nM a[196:199],a[208:211],v[212:215],a[196:199]\nbuffer_load_dwordx4 v[124:127],v240,s[52:55],s65 offen offset:3072 sc1\ns_add_u32 s65,s11,s67\nM v[128:131],v[64:67],a[212:215],0\n" \
"s_add_u32 s65,s65,1048576\nv_lshlrev_b32 v236,1,v236\nv_accvgpr_read_b32 v224,a192\nM v[132:135],v[68:71],a[212:215],0\nds_read_b64_tr_b16 a[212:213],v226 offset:8192\nds_read_b64_tr_b16 a[214:215],v226 offset:12288\nv_accvgpr_read_b32 v228,a196\nM v[136:139],v[64:67],a[216:219],0\n" \
"v_add_f32 v225,v224,v228\nv_rcp_f32 v224,v224\nM v[140:143],v[68:71],a[216:219],0\nds_read_b64_tr_b16 a[216:217],v227 offset:8192\nds_read_b64_tr_b16 a[218:219],v227 offset:12288\ns_waitcnt lgkmcnt(4)\nM v[144:147],v[64:67],a[220:223],0\nbuffer_load_dwordx4 v243,s[40:43],s69 offen offset:0 lds\n" \
"v_mul_f32 v0,v0,v224\nM v[148:151],v[68:71],a[220:223],0\nds_read_b64_tr_b16 a[220:221],v226 offset:8704\nds_read_b64_tr_b16 a[222:223],v226 offset:12800\nv_mul_f32 v1,v1,v224\nM v[152:155],v[64:67],a[224:227],0\nv_mul_f32 v2,v2,v224\nv_mul_f32 v3,v3,v224\n" \
"v_cvt_pk_bf16_f32 v0,v0,v1\nM v[156:159],v[68:71],a[224:227],0\nds_read_b64_tr_b16 a[224:225],v227 offset:8704\nds_read_b64_tr_b16 a[226:227],v227 offset:12800\nv_mul_f32 v8,v8,v224\nM v[160:163],v[64:67],a[228:231],0\nbuffer_load_dwordx4 v244,s[40:43],s69 offen offset:1024 lds\nv_mul_f32 v9,v9,v224\n" \
"M v[164:167],v[68:71],a[228:231],0\nds_read_b64_tr_b16 a[228:229],v226 offset:9216\nds_read_b64_tr_b16 a[230:231],v226 offset:13312\nv_mul_f32 v10,v10,v224\nM v[168:171],v[64:67],a[232:235],0\nv_mul_f32 v11,v11,v224\nv_cvt_pk_bf16_f32 v1,v2,v3\nv_cvt_pk_bf16_f32 v2,v8,v9\n" \
"M v[172:175],v[68:71],a[232:235],0\nds_read_b64_tr_b16 a[232:233],v227 offset:9216\nds_read_b64_tr_b16 a[234:235],v227 offset:13312\nv_cvt_pk_bf16_f32 v3,v10,v11\nM v[176:179],v[64:67],a[236:239],0\nbuffer_load_dwordx4 v245,s[40:43],s69 offen offset:2048 lds\nv_add_f32 v225,v225,v224\nM v[180:183],v[68:71],a[236:239],0\n" \
"ds_read_b64_tr_b16 a[236:237],v226 offset:9728\nds_read_b64_tr_b16 a[238:239],v226 offset:13824\nv_mul_f32 v16,v16,v224\nM v[184:187],v[64:67],a[240:243],0\nv_rcp_f32 v228,v228\nv_mul_f32 v17,v17,v224\nM v[188:191],v[68:71],a[240:243],0\nds_read_b64_tr_b16 a[240:241],v227 offset:9728\n" \
"ds_read_b64_tr_b16 a[242:243],v227 offset:13824\ns_waitcnt lgkmcnt(6)\nM v[128:131],v[80:83],a[212:215],v[128:131]\nbuffer_load_dwordx4 v246,s[40:43],s69 offen offset:3072 lds\ns_lshl4_add_u32 s69,s11,s68\nM v[132:135],v[84:87],a[212:215],v[132:135]\ns_add_u32 s69,s69,0x10000\ns_add_u32 m0,s64,49152\n" \
"ds_read_b64_tr_b16 a[212:213],v226 offset:16384\nM v[136:139],v[80:83],a[216:219],v[136:139]\nds_read_b64_tr_b16 a[214:215],v226 offset:20480\nv_mul_f32 v4,v4,v228\nv_mul_f32 v5,v5,v228\nM v[140:143],v[84:87],a[216:219],v[140:143]\nds_read_b64_tr_b16 a[216:217],v227 offset:16384\nds_read_b64_tr_b16 a[218:219],v227 offset:20480\n" \
"v_mul_f32 v6,v6,v228\nM v[144:147],v[80:83],a[220:223],v[144:147]\nbuffer_load_dwordx4 v243,s[40:43],s69 offen offset:0 lds\nv_mul_f32 v7,v7,v228\nM v[148:151],v[84:87],a[220:223],v[148:151]\nds_read_b64_tr_b16 a[220:221],v226 offset:16896\nds_read_b64_tr_b16 a[222:223],v226 offset:20992\nv_cvt_pk_bf16_f32 v4,v4,v5\n" \
"M v[152:155],v[80:83],a[224:227],v[152:155]\nbuffer_store_dwordx4 v[0:3],v240,s[52:55],s37 offen offset:0 sc1\nv_mul_f32 v12,v12,v228\nM v[156:159],v[84:87],a[224:227],v[156:159]\nds_read_b64_tr_b16 a[224:225],v227 offset:16896\nds_read_b64_tr_b16 a[226:227],v227 offset:20992\nv_mul_f32 v13,v13,v228\nM v[160:163],v[80:83],a[228:231],v[160:163]\n" \
"buffer_load_dwordx4 v244,s[40:43],s69 offen offset:1024 lds\nv_mul_f32 v14,v14,v228\nM v[164:167],v[84:87],a[228:231],v[164:167]\nds_read_b64_tr_b16 a[228:229],v226 offset:17408\nds_read_b64_tr_b16 a[230:231],v226 offset:21504\ns_waitcnt lgkmcnt(6)\nM v[168:171],v[80:83],a[232:235],v[168:171]\nv_mul_f32 v15,v15,v228\n" \
"v_cvt_pk_bf16_f32 v5,v6,v7\nv_cvt_pk_bf16_f32 v6,v12,v13\nM v[172:175],v[84:87],a[232:235],v[172:175]\nds_read_b64_tr_b16 a[232:233],v227 offset:17408\nds_read_b64_tr_b16 a[234:235],v227 offset:21504\nv_cvt_pk_bf16_f32 v7,v14,v15\nM v[176:179],v[80:83],a[236:239],v[176:179]\nbuffer_load_dwordx4 v245,s[40:43],s69 offen offset:2048 lds\n" \
"v_add_f32 v225,v225,v228\nM v[180:183],v[84:87],a[236:239],v[180:183]\nds_read_b64_tr_b16 a[236:237],v226 offset:17920\nds_read_b64_tr_b16 a[238:239],v226 offset:22016\nv_cmp_ngt_f32 vcc,s28,v225\nM v[184:187],v[80:83],a[240:243],v[184:187]\ns_and_b64 vcc,vcc,s[90:91]\ns_addc_u32 s99,s99,0\n" \
"v_mov_b32 v230,s99\nM v[188:191],v[84:87],a[240:243],v[188:191]\nds_read_b64_tr_b16 a[240:241],v227 offset:17920\nds_read_b64_tr_b16 a[242:243],v227 offset:22016\nv_mul_f32 v18,v18,v224\nM v[128:131],v[96:99],a[212:215],v[128:131]\nbuffer_store_dwordx4 v[4:7],v240,s[52:55],s37 offen offset:1024 sc1\nbuffer_load_dwordx4 v246,s[40:43],s69 offen offset:3072 lds\n" \
"M v[132:135],v[100:103],a[212:215],v[132:135]\nds_write_b32 v229,v230\nds_read_b64_tr_b16 a[212:213],v226 offset:24576\nds_read_b64_tr_b16 a[214:215],v226 offset:28672\nM v[136:139],v[96:99],a[216:219],v[136:139]\nv_mul_f32 v19,v19,v224\nv_cvt_pk_bf16_f32 v16,v16,v17\nv_mul_f32 v24,v24,v224\n" \
"M v[140:143],v[100:103],a[216:219],v[140:143]\nds_read_b64_tr_b16 a[216:217],v227 offset:24576\nds_read_b64_tr_b16 a[218:219],v227 offset:28672\ns_waitcnt lgkmcnt(7)\nM v[144:147],v[96:99],a[220:223],v[144:147]\nv_mul_f32 v25,v25,v224\nv_mul_f32 v26,v26,v224\nv_mul_f32 v27,v27,v224\n" \
"M v[148:151],v[100:103],a[220:223],v[148:151]\nv_cvt_pk_bf16_f32 v17,v18,v19\nds_read_b64_tr_b16 a[220:221],v226 offset:25088\nds_read_b64_tr_b16 a[222:223],v226 offset:29184\nM v[152:155],v[96:99],a[224:227],v[152:155]\nbuffer_load_dwordx4 a[0:3],v240,s[52:55],s65 offen offset:0 sc1\nv_cvt_pk_bf16_f32 v18,v24,v25\nM v[156:159],v[100:103],a[224:227],v[156:159]\n" \
"v_cvt_pk_bf16_f32 v19,v26,v27\nds_read_b64_tr_b16 a[224:225],v227 offset:25088\nds_read_b64_tr_b16 a[226:227],v227 offset:29184\nM v[160:163],v[96:99],a[228:231],v[160:163]\nv_lshl_add_u32 v229,v251,10,v250\nv_mul_f32 v20,v20,v228\nv_mul_f32 v21,v21,v228\nM v[164:167],v[100:103],a[228:231],v[164:167]\n" \
"ds_read_b64_tr_b16 a[228:229],v226 offset:25600\nds_read_b64_tr_b16 a[230:231],v226 offset:29696\nv_mul_f32 v22,v22,v228\nM v[168:171],v[96:99],a[232:235],v[168:171]\nbuffer_store_dwordx4 v[16:19],v240,s[52:55],s37 offen offset:2048 sc1\nbuffer_load_dwordx4 a[4:7],v240,s[52:55],s65 offen offset:1024 sc1\nM v[172:175],v[100:103],a[232:235],v[172:175]\nds_read_b64_tr_b16 a[232:233],v227 offset:25600\n" \
"ds_read_b64_tr_b16 a[234:235],v227 offset:29696\nv_mul_f32 v23,v23,v228\nM v[176:179],v[96:99],a[236:239],v[176:179]\nv_cvt_pk_bf16_f32 v20,v20,v21\nv_mul_f32 v28,v28,v228\nv_mul_f32 v29,v29,v228\nM v[180:183],v[100:103],a[236:239],v[180:183]\nds_read_b64_tr_b16 a[236:237],v226 offset:26112\n" \
"ds_read_b64_tr_b16 a[238:239],v226 offset:30208\ns_waitcnt lgkmcnt(6)\nM v[184:187],v[96:99],a[240:243],v[184:187]\ns_waitcnt vmcnt(14)\ns_barrier\nbuffer_load_dwordx4 a[8:11],v240,s[52:55],s65 offen offset:2048 sc1\nM v[188:191],v[100:103],a[240:243],v[188:191]\nds_read_b32 v230,v229\n" \
"v_mul_f32 v30,v30,v228\nv_mul_f32 v31,v31,v228\nM v[128:131],v[112:115],a[212:215],v[128:131]\nv_cvt_pk_bf16_f32 v21,v22,v23\nds_read_b64_tr_b16 a[240:241],v227 offset:26112\nds_read_b64_tr_b16 a[242:243],v227 offset:30208\nM v[132:135],v[116:119],a[212:215],v[132:135]\nv_cvt_pk_bf16_f32 v22,v28,v29\n" \
"v_cvt_pk_bf16_f32 v23,v30,v31\nv_add_u32 v226,0,v242\nM v[136:139],v[112:115],a[216:219],v[136:139]\nbuffer_load_dwordx4 a[12:15],v240,s[52:55],s65 offen offset:3072 sc1\nds_read_b64_tr_b16 a[212:213],v226 offset:0\nM v[140:143],v[116:119],a[216:219],v[140:143]\nds_read_b64_tr_b16 a[214:215],v226 offset:4096\ns_add_u32 s65,s65,4096\n" \
"v_xor_b32 v227,32,v226\nM v[144:147],v[112:115],a[220:223],v[144:147]\nbuffer_store_dwordx4 v[20:23],v240,s[52:55],s37 offen offset:3072 sc1\nds_read_b64_tr_b16 a[216:217],v227 offset:0\nM v[148:151],v[116:119],a[220:223],v[148:151]\nds_read_b64_tr_b16 a[218:219],v227 offset:4096\nds_read_b64_tr_b16 a[220:221],v226 offset:512\nds_read_b64_tr_b16 a[222:223],v226 offset:4608\n" \
"M v[152:155],v[112:115],a[224:227],v[152:155]\nbuffer_load_dwordx4 a[16:19],v240,s[52:55],s65 offen offset:0 sc1\nv_mul_f32 v32,v32,v224\nM v[156:159],v[116:119],a[224:227],v[156:159]\nds_read_b64_tr_b16 a[224:225],v227 offset:512\nds_read_b64_tr_b16 a[226:227],v227 offset:4608\ns_waitcnt lgkmcnt(8)\nM v[160:163],v[112:115],a[228:231],v[160:163]\n" \
"v_mul_f32 v33,v33,v224\nv_mul_f32 v34,v34,v224\nv_mul_f32 v35,v35,v224\nM v[164:167],v[116:119],a[228:231],v[164:167]\nv_cmp_ne_u32 vcc,0,v230\nv_cvt_pk_bf16_f32 v32,v32,v33\ns_cbranch_vccnz .Lredo\nM v[168:171],v[112:115],a[232:235],v[168:171]\n" \
"buffer_load_dwordx4 a[20:23],v240,s[52:55],s65 offen offset:1024 sc1\nv_mul_f32 v40,v40,v224\nM v[172:175],v[116:119],a[232:235],v[172:175]\nv_mul_f32 v41,v41,v224\nv_mul_f32 v42,v42,v224\nv_mul_f32 v43,v43,v224\nM v[176:179],v[112:115],a[236:239],v[176:179]\nv_cvt_pk_bf16_f32 v33,v34,v35\n" \
"ds_read_b64_tr_b16 a[228:229],v226 offset:1024\nds_read_b64_tr_b16 a[230:231],v226 offset:5120\nM v[180:183],v[116:119],a[236:239],v[180:183]\nv_cvt_pk_bf16_f32 v34,v40,v41\nv_cvt_pk_bf16_f32 v35,v42,v43\nds_read_b64_tr_b16 a[232:233],v227 offset:1024\nM v[184:187],v[112:115],a[240:243],v[184:187]\nbuffer_store_dwordx4 v[32:35],v240,s[52:55],s38 offen offset:0 sc1\n" \
"buffer_load_dwordx4 a[24:27],v240,s[52:55],s65 offen offset:2048 sc1\nM v[188:191],v[116:119],a[240:243],v[188:191]\nds_read_b64_tr_b16 a[234:235],v227 offset:5120\nds_read_b64_tr_b16 a[236:237],v226 offset:1536\ns_waitcnt lgkmcnt(5)\nM v[128:131],v[72:75],a[212:215],v[128:131]\nds_read_b64_tr_b16 a[238:239],v226 offset:5632\nds_read_b64_tr_b16 a[240:241],v227 offset:1536\n" \
"ds_read_b64_tr_b16 a[242:243],v227 offset:5632\nM v[132:135],v[76:79],a[212:215],v[132:135]\nds_read_b64_tr_b16 a[212:213],v226 offset:8192\nds_read_b64_tr_b16 a[214:215],v226 offset:12288\nv_mul_f32 v36,v36,v228\nM v[136:139],v[72:75],a[216:219],v[136:139]\nbuffer_load_dwordx4 a[28:31],v240,s[52:55],s65 offen offset:3072 sc1\nv_mul_f32 v37,v37,v228\n" \
"M v[140:143],v[76:79],a[216:219],v[140:143]\nv_mul_f32 v38,v38,v228\nv_mul_f32 v39,v39,v228\nv_cvt_pk_bf16_f32 v36,v36,v37\nM v[144:147],v[72:75],a[220:223],v[144:147]\nv_mul_f32 v44,v44,v228\nv_mul_f32 v45,v45,v228\nv_mul_f32 v46,v46,v228\n" \
"M v[148:151],v[76:79],a[220:223],v[148:151]\nv_mul_f32 v47,v47,v228\nv_cvt_pk_bf16_f32 v37,v38,v39\nds_read_b64_tr_b16 a[216:217],v227 offset:8192\nM v[152:155],v[72:75],a[224:227],v[152:155]\nds_read_b64_tr_b16 a[218:219],v227 offset:12288\nv_cvt_pk_bf16_f32 v38,v44,v45\nv_cvt_pk_bf16_f32 v39,v46,v47\n" \
"M v[156:159],v[76:79],a[224:227],v[156:159]\ns_lshl4_add_u32 s69,s14,s68\ns_add_u32 m0,s64,65536\ns_waitcnt lgkmcnt(2)\nM v[160:163],v[72:75],a[228:231],v[160:163]\nbuffer_store_dwordx4 v[36:39],v240,s[52:55],s38 offen offset:1024 sc1\nbuffer_load_dwordx4 v243,s[40:43],s69 offen offset:0 lds\nM v[164:167],v[76:79],a[228:231],v[164:167]\n" \
"ds_read_b64_tr_b16 a[220:221],v226 offset:8704\nds_read_b64_tr_b16 a[222:223],v226 offset:12800\nds_read_b64_tr_b16 a[224:225],v227 offset:8704\nM v[168:171],v[72:75],a[232:235],v[168:171]\nds_read_b64_tr_b16 a[226:227],v227 offset:12800\nds_read_b64_tr_b16 a[228:229],v226 offset:9216\nv_mul_f32 v48,v48,v224\nM v[172:175],v[76:79],a[232:235],v[172:175]\n" \
"ds_read_b64_tr_b16 a[230:231],v226 offset:13312\nds_read_b64_tr_b16 a[232:233],v227 offset:9216\nv_mul_f32 v49,v49,v224\nM v[176:179],v[72:75],a[236:239],v[176:179]\nbuffer_load_dwordx4 v244,s[40:43],s69 offen offset:1024 lds\nds_read_b64_tr_b16 a[234:235],v227 offset:13312\nM v[180:183],v[76:79],a[236:239],v[180:183]\nv_mul_f32 v50,v50,v224\n" \
"v_mul_f32 v51,v51,v224\nv_cvt_pk_bf16_f32 v48,v48,v49\nM v[184:187],v[72:75],a[240:243],v[184:187]\nv_mul_f32 v56,v56,v224\nv_mul_f32 v57,v57,v224\nv_mul_f32 v58,v58,v224\nM v[188:191],v[76:79],a[240:243],v[188:191]\nv_mul_f32 v59,v59,v224\n" \
"v_cvt_pk_bf16_f32 v49,v50,v51\nds_read_b64_tr_b16 a[236:237],v226 offset:9728\nM v[128:131],v[88:91],a[212:215],v[128:131]\nbuffer_load_dwordx4 v245,s[40:43],s69 offen offset:2048 lds\nds_read_b64_tr_b16 a[238:239],v226 offset:13824\nM v[132:135],v[92:95],a[212:215],v[132:135]\nv_cvt_pk_bf16_f32 v50,v56,v57\nv_cvt_pk_bf16_f32 v51,v58,v59\n" \
"s_waitcnt lgkmcnt(3)\nM v[136:139],v[88:91],a[216:219],v[136:139]\nbuffer_store_dwordx4 v[48:51],v240,s[52:55],s38 offen offset:2048 sc1\nds_read_b64_tr_b16 a[240:241],v227 offset:9728\nM v[140:143],v[92:95],a[216:219],v[140:143]\nds_read_b64_tr_b16 a[242:243],v227 offset:13824\nds_read_b64_tr_b16 a[212:213],v226 offset:16384\nds_read_b64_tr_b16 a[214:215],v226 offset:20480\n" \
"M v[144:147],v[88:91],a[220:223],v[144:147]\nbuffer_load_dwordx4 v246,s[40:43],s69 offen offset:3072 lds\ns_lshl4_add_u32 s69,s14,s68\nM v[148:151],v[92:95],a[220:223],v[148:151]\nds_read_b64_tr_b16 a[216:217],v227 offset:16384\nds_read_b64_tr_b16 a[218:219],v227 offset:20480\ns_add_u32 s69,s69,0x10000\nM v[152:155],v[88:91],a[224:227],v[152:155]\n" \
"s_add_u32 m0,s64,98304\nds_read_b64_tr_b16 a[220:221],v226 offset:16896\nds_read_b64_tr_b16 a[222:223],v226 offset:20992\nM v[156:159],v[92:95],a[224:227],v[156:159]\nv_mul_f32 v52,v52,v228\nv_mul_f32 v53,v53,v228\nv_mul_f32 v54,v54,v228\nM v[160:163],v[88:91],a[228:231],v[160:163]\n" \
"buffer_load_dwordx4 v243,s[40:43],s69 offen offset:0 lds\nv_mul_f32 v55,v55,v228\nM v[164:167],v[92:95],a[228:231],v[164:167]\nv_cvt_pk_bf16_f32 v52,v52,v53\nv_mul_f32 v60,v60,v228\ns_waitcnt lgkmcnt(4)\nM v[168:171],v[88:91],a[232:235],v[168:171]\nv_mul_f32 v61,v61,v228\n" \
"v_mul_f32 v62,v62,v228\nv_mul_f32 v63,v63,v228\nM v[172:175],v[92:95],a[232:235],v[172:175]\nv_cvt_pk_bf16_f32 v53,v54,v55\nds_read_b64_tr_b16 a[224:225],v227 offset:16896\nds_read_b64_tr_b16 a[226:227],v227 offset:20992\nM v[176:179],v[88:91],a[236:239],v[176:179]\nv_cvt_pk_bf16_f32 v54,v60,v61\n" \
"v_cvt_pk_bf16_f32 v55,v62,v63\nbuffer_store_dwordx4 v[52:55],v240,s[52:55],s38 offen offset:3072 sc1\nbuffer_load_dwordx4 v244,s[40:43],s69 offen offset:1024 lds\nM v[180:183],v[92:95],a[236:239],v[180:183]\nds_read_b64_tr_b16 a[228:229],v226 offset:17408\nds_read_b64_tr_b16 a[230:231],v226 offset:21504\nds_read_b64_tr_b16 a[232:233],v227 offset:17408\nM v[184:187],v[88:91],a[240:243],v[184:187]\n" \
"ds_read_b64_tr_b16 a[234:235],v227 offset:21504\nds_read_b64_tr_b16 a[236:237],v226 offset:17920\ns_add_u32 s65,s14,s67\nM v[188:191],v[92:95],a[240:243],v[188:191]\nds_read_b64_tr_b16 a[238:239],v226 offset:22016\nds_read_b64_tr_b16 a[240:241],v227 offset:17920\ns_add_u32 s65,s65,1048576\nM v[128:131],v[104:107],a[212:215],v[128:131]\n" \
"buffer_load_dwordx4 v245,s[40:43],s69 offen offset:2048 lds\nds_read_b64_tr_b16 a[242:243],v227 offset:22016\nM v[132:135],v[108:111],a[212:215],v[132:135]\nds_read_b64_tr_b16 a[212:213],v226 offset:24576\nds_read_b64_tr_b16 a[214:215],v226 offset:28672\ns_waitcnt lgkmcnt(10)\nM v[136:139],v[104:107],a[216:219],v[136:139]\nv_and_b32 v228,31,v249\n" \
"v_lshl_add_u32 v228,v228,2,s70\nv_and_b32 v229,32,v249\nM v[140:143],v[108:111],a[216:219],v[140:143]\nds_read_b64_tr_b16 a[216:217],v227 offset:24576\nds_read_b64_tr_b16 a[218:219],v227 offset:28672\nv_mul_u32_u24 v229,12,v229\nM v[144:147],v[104:107],a[220:223],v[144:147]\nbuffer_load_dwordx4 v246,s[40:43],s69 offen offset:3072 lds\n" \
"M v[148:151],v[108:111],a[220:223],v[148:151]\nds_read_b64_tr_b16 a[220:221],v226 offset:25088\nds_read_b64_tr_b16 a[222:223],v226 offset:29184\nM v[152:155],v[104:107],a[224:227],v[152:155]\ns_lshr_b32 s70,s6,3\nv_xor_b32 v228,v228,v229\nM v[156:159],v[108:111],a[224:227],v[156:159]\nds_read_b64_tr_b16 a[224:225],v227 offset:25088\n" \
"s_waitcnt lgkmcnt(7)\nM v[160:163],v[104:107],a[228:231],v[160:163]\nds_read_b64_tr_b16 a[226:227],v227 offset:29184\ns_min_u32 s70,s70,27\nM v[164:167],v[108:111],a[228:231],v[164:167]\nds_read_b64_tr_b16 a[228:229],v226 offset:25600\nds_read_b64_tr_b16 a[230:231],v226 offset:29696\nM v[168:171],v[104:107],a[232:235],v[168:171]\n" \
"buffer_load_dwordx4 a[32:35],v240,s[52:55],s65 offen offset:0 sc1\nM v[172:175],v[108:111],a[232:235],v[172:175]\nds_read_b64_tr_b16 a[232:233],v227 offset:25600\nds_read_b64_tr_b16 a[234:235],v227 offset:29696\nM v[176:179],v[104:107],a[236:239],v[176:179]\ns_add_u32 s70,s70,s71\nv_and_b32 v229,3,v249\nM v[180:183],v[108:111],a[236:239],v[180:183]\n" \
"ds_read_b64_tr_b16 a[236:237],v226 offset:26112\nds_read_b64_tr_b16 a[238:239],v226 offset:30208\nM v[184:187],v[104:107],a[240:243],v[184:187]\ns_waitcnt vmcnt(22)\ns_barrier\nM v[188:191],v[108:111],a[240:243],v[188:191]\nds_read_b64_tr_b16 a[240:241],v227 offset:26112\ns_waitcnt lgkmcnt(7)\n" \
"M v[128:131],v[120:123],a[212:215],v[128:131]\nbuffer_load_dwordx4 a[36:39],v240,s[52:55],s65 offen offset:1024 sc1\nM v[132:135],v[124:127],a[212:215],v[132:135]\nds_read_b64_tr_b16 a[242:243],v227 offset:30208\nds_read_b64_tr_b16 a[212:213],v226 offset:32768\nM v[136:139],v[120:123],a[216:219],v[136:139]\nds_read_b64_tr_b16 a[214:215],v226 offset:36864\nv_lshrrev_b32 v230,2,v249\n" \
"M v[140:143],v[124:127],a[216:219],v[140:143]\nds_read_b64_tr_b16 a[216:217],v227 offset:32768\nds_read_b64_tr_b16 a[218:219],v227 offset:36864\nM v[144:147],v[120:123],a[220:223],v[144:147]\nv_lshl_add_u32 v229,v229,5,v230\ns_mul_i32 s71,s7,458752\nM v[148:151],v[124:127],a[220:223],v[148:151]\nds_read_b64_tr_b16 a[220:221],v226 offset:33280\n" \
"ds_read_b64_tr_b16 a[222:223],v226 offset:37376\nM v[152:155],v[120:123],a[224:227],v[152:155]\nbuffer_load_dwordx4 a[40:43],v240,s[52:55],s65 offen offset:2048 sc1\nM v[156:159],v[124:127],a[224:227],v[156:159]\nds_read_b64_tr_b16 a[224:225],v227 offset:33280\ns_waitcnt lgkmcnt(8)\nM v[160:163],v[120:123],a[228:231],v[160:163]\nds_read_b64_tr_b16 a[226:227],v227 offset:37376\n" \
"s_lshl4_add_u32 s71,s70,s71\nM v[164:167],v[124:127],a[228:231],v[164:167]\nds_read_b64_tr_b16 a[228:229],v226 offset:33792\nds_read_b64_tr_b16 a[230:231],v226 offset:37888\nM v[168:171],v[120:123],a[232:235],v[168:171]\nv_mul_u32_u24 v229,3584,v229\nv_add_u32 v229,s71,v229\nM v[172:175],v[124:127],a[232:235],v[172:175]\n" \
"ds_read_b64_tr_b16 a[232:233],v227 offset:33792\nds_read_b64_tr_b16 a[234:235],v227 offset:37888\nM v[176:179],v[120:123],a[236:239],v[176:179]\nbuffer_load_dwordx4 a[44:47],v240,s[52:55],s65 offen offset:3072 sc1\nM v[180:183],v[124:127],a[236:239],v[180:183]\nds_read_b64_tr_b16 a[236:237],v226 offset:34304\ns_waitcnt lgkmcnt(7)\nM v[184:187],v[120:123],a[240:243],v[184:187]\n" \
"ds_read_b64_tr_b16 a[238:239],v226 offset:38400\ns_add_u32 s65,s65,4096\nM v[188:191],v[124:127],a[240:243],v[188:191]\nds_read_b64_tr_b16 a[240:241],v227 offset:34304\nds_read_b64_tr_b16 a[242:243],v227 offset:38400\nM v[128:131],a[0:3],a[212:215],v[128:131]\nv_add_u32 v230,57344,v229\ns_waitcnt vmcnt(21)\n" \
"M v[132:135],a[4:7],a[212:215],v[132:135]\nds_read_b64_tr_b16 a[212:213],v226 offset:40960\nds_read_b64_tr_b16 a[214:215],v226 offset:45056\nM v[136:139],a[0:3],a[216:219],v[136:139]\nbuffer_load_dwordx4 a[48:51],v240,s[52:55],s65 offen offset:0 sc1\nglobal_load_dwordx4 a[160:163],v229,s[22:23]\nM v[140:143],a[4:7],a[216:219],v[140:143]\nds_read_b64_tr_b16 a[216:217],v227 offset:40960\n" \
"ds_read_b64_tr_b16 a[218:219],v227 offset:45056\nM v[144:147],a[0:3],a[220:223],v[144:147]\nv_mov_b32 v250,81920\nds_write_b64 v250,a[244:245]\nM v[148:151],a[4:7],a[220:223],v[148:151]\nds_read_b64_tr_b16 a[220:221],v226 offset:41472\ns_waitcnt lgkmcnt(8)\nM v[152:155],a[0:3],a[224:227],v[152:155]\n" \
"ds_read_b64_tr_b16 a[222:223],v226 offset:45568\ns_lshl_b32 s70,s70,13\nM v[156:159],a[4:7],a[224:227],v[156:159]\nds_read_b64_tr_b16 a[224:225],v227 offset:41472\nds_read_b64_tr_b16 a[226:227],v227 offset:45568\nM v[160:163],a[0:3],a[228:231],v[160:163]\nbuffer_load_dwordx4 a[52:55],v240,s[52:55],s65 offen offset:1024 sc1\nM v[164:167],a[4:7],a[228:231],v[164:167]\n" \
"ds_read_b64_tr_b16 a[228:229],v226 offset:41984\nds_read_b64_tr_b16 a[230:231],v226 offset:46080\nM v[168:171],a[0:3],a[232:235],v[168:171]\ns_lshl_b32 s71,s7,8\ns_add_u32 s70,s70,s71\nM v[172:175],a[4:7],a[232:235],v[172:175]\nds_read_b64_tr_b16 a[232:233],v227 offset:41984\nds_read_b64_tr_b16 a[234:235],v227 offset:46080\n" \
"M v[176:179],a[0:3],a[236:239],v[176:179]\nglobal_load_dwordx4 a[164:167],v230,s[22:23]\nM v[180:183],a[4:7],a[236:239],v[180:183]\nds_read_b64_tr_b16 a[236:237],v226 offset:42496\ns_waitcnt lgkmcnt(7)\nM v[184:187],a[0:3],a[240:243],v[184:187]\nbuffer_load_dwordx4 a[56:59],v240,s[52:55],s65 offen offset:2048 sc1\nM v[188:191],a[4:7],a[240:243],v[188:191]\n" \
"ds_read_b64_tr_b16 a[238:239],v226 offset:46592\nds_read_b64_tr_b16 a[240:241],v227 offset:42496\nM v[128:131],a[8:11],a[212:215],v[128:131]\nds_read_b64_tr_b16 a[242:243],v227 offset:46592\ns_add_u32 s70,s70,4096\nM v[132:135],a[12:15],a[212:215],v[132:135]\nds_read_b64_tr_b16 a[212:213],v226 offset:49152\nds_read_b64_tr_b16 a[214:215],v226 offset:53248\n" \
"M v[136:139],a[8:11],a[216:219],v[136:139]\nv_lshl_add_u32 v231,v249,2,s70\ns_add_u32 s95,s74,s72\nM v[140:143],a[12:15],a[216:219],v[140:143]\nds_read_b64_tr_b16 a[216:217],v227 offset:49152\nds_read_b64_tr_b16 a[218:219],v227 offset:53248\nM v[144:147],a[8:11],a[220:223],v[144:147]\nbuffer_load_dwordx4 a[60:63],v240,s[52:55],s65 offen offset:3072 sc1\n" \
"M v[148:151],a[12:15],a[220:223],v[148:151]\nds_read_b64_tr_b16 a[220:221],v226 offset:49664\ns_waitcnt lgkmcnt(8)\nM v[152:155],a[8:11],a[224:227],v[152:155]\nglobal_load_dwordx4 a[168:171],v229,s[24:25]\nM v[156:159],a[12:15],a[224:227],v[156:159]\nds_read_b64_tr_b16 a[222:223],v226 offset:53760\nds_read_b64_tr_b16 a[224:225],v227 offset:49664\n" \
"M v[160:163],a[8:11],a[228:231],v[160:163]\nds_read_b64_tr_b16 a[226:227],v227 offset:53760\nv_lshl_or_b32 v235,v235,8,v236\nM v[164:167],a[12:15],a[228:231],v[164:167]\nds_read_b64_tr_b16 a[228:229],v226 offset:50176\nds_read_b64_tr_b16 a[230:231],v226 offset:54272\nM v[168:171],a[8:11],a[232:235],v[168:171]\ns_lshl_b32 s95,s95,10\n" \
"v_add_u32 v236,s95,v235\nM v[172:175],a[12:15],a[232:235],v[172:175]\nds_read_b64_tr_b16 a[232:233],v227 offset:50176\ns_waitcnt lgkmcnt(7)\nM v[176:179],a[8:11],a[236:239],v[176:179]\ns_waitcnt vmcnt(14)\nbuffer_store_dword v254,off,s[52:55],s76 sc1\nM v[180:183],a[12:15],a[236:239],v[180:183]\n" \
"ds_read_b64_tr_b16 a[234:235],v227 offset:54272\nds_read_b64_tr_b16 a[236:237],v226 offset:50688\nM v[184:187],a[8:11],a[240:243],v[184:187]\nds_read_b64_tr_b16 a[238:239],v226 offset:54784\ns_sub_u32 s74,s74,s72\nM v[188:191],a[12:15],a[240:243],v[188:191]\nds_read_b64_tr_b16 a[240:241],v227 offset:50688\nds_read_b64_tr_b16 a[242:243],v227 offset:54784\n" \
"M v[128:131],a[16:19],a[212:215],v[128:131]\nglobal_load_dwordx4 a[172:175],v230,s[24:25]\nM v[132:135],a[20:23],a[212:215],v[132:135]\nds_read_b64_tr_b16 a[212:213],v226 offset:57344\nds_read_b64_tr_b16 a[214:215],v226 offset:61440\nM v[136:139],a[16:19],a[216:219],v[136:139]\ns_add_u32 s74,s74,1920\ns_lshl_b32 s74,s74,10\n" \
"M v[140:143],a[20:23],a[216:219],v[140:143]\nds_read_b64_tr_b16 a[216:217],v227 offset:57344\ns_waitcnt lgkmcnt(8)\nM v[144:147],a[16:19],a[220:223],v[144:147]\nds_read_b64_tr_b16 a[218:219],v227 offset:61440\nv_add_u32 v237,s74,v235\nM v[148:151],a[20:23],a[220:223],v[148:151]\nds_read_b64_tr_b16 a[220:221],v226 offset:57856\n" \
"ds_read_b64_tr_b16 a[222:223],v226 offset:61952\nM v[152:155],a[16:19],a[224:227],v[152:155]\ns_lshl_b32 s72,s7,9\nv_lshl_add_u32 v239,v249,3,s72\nM v[156:159],a[20:23],a[224:227],v[156:159]\nds_read_b64_tr_b16 a[224:225],v227 offset:57856\nds_read_b64_tr_b16 a[226:227],v227 offset:61952\nM v[160:163],a[16:19],a[228:231],v[160:163]\n" \
"buffer_load_dword v248,v228,s[52:55],s80 offen sc1\nglobal_load_dword a176,v231,s[26:27] offset:-4096\nM v[164:167],a[20:23],a[228:231],v[164:167]\nds_read_b64_tr_b16 a[228:229],v226 offset:58368\ns_waitcnt lgkmcnt(7)\nM v[168:171],a[16:19],a[232:235],v[168:171]\nglobal_load_dword a177,v231,s[26:27] offset:-3072\nM v[172:175],a[20:23],a[232:235],v[172:175]\n" \
"global_load_ushort v64,v236,s[8:9] offset:0\nM v[176:179],a[16:19],a[236:239],v[176:179]\nglobal_load_dword a178,v231,s[26:27] offset:-2048\nM v[180:183],a[20:23],a[236:239],v[180:183]\nds_read_b64_tr_b16 a[230:231],v226 offset:62464\nglobal_load_ushort v80,v237,s[8:9] offset:0\nM v[184:187],a[16:19],a[240:243],v[184:187]\ns_waitcnt vmcnt(17)\n" \
"s_barrier\nglobal_load_dword a179,v231,s[26:27] offset:-1024\nM v[188:191],a[20:23],a[240:243],v[188:191]\nds_read_b64_tr_b16 a[232:233],v227 offset:58368\nds_read_b64_tr_b16 a[234:235],v227 offset:62464\nglobal_load_ushort v96,v235,s[20:21] offset:0\nM v[128:131],a[24:27],a[212:215],v[128:131]\nglobal_load_dword a180,v231,s[26:27] offset:0\n" \
"M v[132:135],a[28:31],a[212:215],v[132:135]\nds_read_b64_tr_b16 a[236:237],v226 offset:58880\nds_read_b64_tr_b16 a[238:239],v226 offset:62976\nglobal_load_ushort v65,v236,s[8:9] offset:32\ns_waitcnt lgkmcnt(5)\nM v[136:139],a[24:27],a[216:219],v[136:139]\nglobal_load_dword a181,v231,s[26:27] offset:1024\nM v[140:143],a[28:31],a[216:219],v[140:143]\n" \
"ds_read_b64_tr_b16 a[240:241],v227 offset:58880\nds_read_b64_tr_b16 a[242:243],v227 offset:62976\nglobal_load_ushort v81,v237,s[8:9] offset:32\nM v[144:147],a[24:27],a[220:223],v[144:147]\nglobal_load_dword a182,v231,s[26:27] offset:2048\nM v[148:151],a[28:31],a[220:223],v[148:151]\nv_add_u32 v226,65536,v242\nds_read_b64_tr_b16 a[212:213],v226 offset:0\n" \
"ds_read_b64_tr_b16 a[214:215],v226 offset:4096\nglobal_load_ushort v97,v235,s[20:21] offset:32\nM v[152:155],a[24:27],a[224:227],v[152:155]\nglobal_load_dword a183,v231,s[26:27] offset:3072\nM v[156:159],a[28:31],a[224:227],v[156:159]\nv_xor_b32 v227,32,v226\nds_read_b64_tr_b16 a[216:217],v227 offset:0\nds_read_b64_tr_b16 a[218:219],v227 offset:4096\n" \
"global_load_ushort v66,v236,s[8:9] offset:64\ns_waitcnt lgkmcnt(6)\nM v[160:163],a[24:27],a[228:231],v[160:163]\nds_read_b64_tr_b16 a[220:221],v226 offset:512\nds_read_b64_tr_b16 a[222:223],v226 offset:4608\nM v[164:167],a[28:31],a[228:231],v[164:167]\nglobal_load_ushort v82,v237,s[8:9] offset:64\nM v[168:171],a[24:27],a[232:235],v[168:171]\n" \
"ds_read_b64_tr_b16 a[224:225],v227 offset:512\nds_read_b64_tr_b16 a[226:227],v227 offset:4608\nM v[172:175],a[28:31],a[232:235],v[172:175]\nglobal_load_ushort v98,v235,s[20:21] offset:64\nM v[176:179],a[24:27],a[236:239],v[176:179]\nds_read_b64_tr_b16 a[228:229],v226 offset:1024\nds_read_b64_tr_b16 a[230:231],v226 offset:5120\nM v[180:183],a[28:31],a[236:239],v[180:183]\n" \
"global_load_ushort v67,v236,s[8:9] offset:96\ns_waitcnt lgkmcnt(7)\nM v[184:187],a[24:27],a[240:243],v[184:187]\nds_read_b64_tr_b16 a[232:233],v227 offset:1024\nds_read_b64_tr_b16 a[234:235],v227 offset:5120\nM v[188:191],a[28:31],a[240:243],v[188:191]\nglobal_load_ushort v83,v237,s[8:9] offset:96\nM v[128:131],a[32:35],a[212:215],v[128:131]\n" \
"ds_read_b64_tr_b16 a[236:237],v226 offset:1536\nds_read_b64_tr_b16 a[238:239],v226 offset:5632\nM v[132:135],a[36:39],a[212:215],v[132:135]\nglobal_load_ushort v99,v235,s[20:21] offset:96\ns_waitcnt lgkmcnt(6)\nM v[136:139],a[32:35],a[216:219],v[136:139]\nds_read_b64_tr_b16 a[240:241],v227 offset:1536\nds_read_b64_tr_b16 a[242:243],v227 offset:5632\n" \
"M v[140:143],a[36:39],a[216:219],v[140:143]\nglobal_load_ushort v68,v236,s[8:9] offset:128\nM v[144:147],a[32:35],a[220:223],v[144:147]\nds_read_b64_tr_b16 a[212:213],v226 offset:8192\nds_read_b64_tr_b16 a[214:215],v226 offset:12288\nM v[148:151],a[36:39],a[220:223],v[148:151]\nglobal_load_ushort v84,v237,s[8:9] offset:128\nM v[152:155],a[32:35],a[224:227],v[152:155]\n" \
"ds_read_b64_tr_b16 a[216:217],v227 offset:8192\nds_read_b64_tr_b16 a[218:219],v227 offset:12288\nM v[156:159],a[36:39],a[224:227],v[156:159]\nglobal_load_ushort v100,v235,s[20:21] offset:128\ns_waitcnt lgkmcnt(6)\nM v[160:163],a[32:35],a[228:231],v[160:163]\nds_read_b64_tr_b16 a[220:221],v226 offset:8704\nds_read_b64_tr_b16 a[222:223],v226 offset:12800\n" \
"M v[164:167],a[36:39],a[228:231],v[164:167]\nglobal_load_ushort v69,v236,s[8:9] offset:160\nM v[168:171],a[32:35],a[232:235],v[168:171]\nds_read_b64_tr_b16 a[224:225],v227 offset:8704\nds_read_b64_tr_b16 a[226:227],v227 offset:12800\nM v[172:175],a[36:39],a[232:235],v[172:175]\nglobal_load_ushort v85,v237,s[8:9] offset:160\nM v[176:179],a[32:35],a[236:239],v[176:179]\n" \
"ds_read_b64_tr_b16 a[228:229],v226 offset:9216\nds_read_b64_tr_b16 a[230:231],v226 offset:13312\nM v[180:183],a[36:39],a[236:239],v[180:183]\nglobal_load_ushort v101,v235,s[20:21] offset:160\ns_waitcnt lgkmcnt(6)\nM v[184:187],a[32:35],a[240:243],v[184:187]\nds_read_b64_tr_b16 a[232:233],v227 offset:9216\nds_read_b64_tr_b16 a[234:235],v227 offset:13312\n" \
"M v[188:191],a[36:39],a[240:243],v[188:191]\nglobal_load_ushort v70,v236,s[8:9] offset:192\ns_waitcnt vmcnt(30)\nM v[128:131],a[40:43],a[212:215],v[128:131]\nds_read_b64_tr_b16 a[236:237],v226 offset:9728\nds_read_b64_tr_b16 a[238:239],v226 offset:13824\nM v[132:135],a[44:47],a[212:215],v[132:135]\nglobal_load_ushort v86,v237,s[8:9] offset:192\n" \
"M v[136:139],a[40:43],a[216:219],v[136:139]\ns_waitcnt vmcnt(28)\nv_cmp_eq_u32 vcc,0,v248\nM v[140:143],a[44:47],a[216:219],v[140:143]\nds_read_b64_tr_b16 a[240:241],v227 offset:9728\nds_read_b64_tr_b16 a[242:243],v227 offset:13824\nglobal_load_ushort v102,v235,s[20:21] offset:192\ns_waitcnt lgkmcnt(6)\n" \
"M v[144:147],a[40:43],a[220:223],v[144:147]\ns_cbranch_vccnz .Lps7\n.Lpb7:\nbuffer_load_dword v248,v251,s[52:55],s79 offen sc1\nM v[148:151],a[44:47],a[220:223],v[148:151]\nds_read_b64_tr_b16 a[212:213],v226 offset:32768\nds_read_b64_tr_b16 a[214:215],v226 offset:36864\nglobal_load_ushort v71,v236,s[8:9] offset:224\n" \
"M v[152:155],a[40:43],a[224:227],v[152:155]\nds_read_b64_tr_b16 a[216:217],v227 offset:32768\nds_read_b64_tr_b16 a[218:219],v227 offset:36864\nM v[156:159],a[44:47],a[224:227],v[156:159]\nglobal_load_ushort v87,v237,s[8:9] offset:224\nM v[160:163],a[40:43],a[228:231],v[160:163]\nds_read_b64_tr_b16 a[220:221],v226 offset:33280\nds_read_b64_tr_b16 a[222:223],v226 offset:37376\n" \
"M v[164:167],a[44:47],a[228:231],v[164:167]\nglobal_load_ushort v103,v235,s[20:21] offset:224\ns_waitcnt lgkmcnt(6)\nM v[168:171],a[40:43],a[232:235],v[168:171]\nds_read_b64_tr_b16 a[224:225],v227 offset:33280\nds_read_b64_tr_b16 a[226:227],v227 offset:37376\nM v[172:175],a[44:47],a[232:235],v[172:175]\nbuffer_load_dwordx2 v[112:113],v239,s[52:55],0 offen offset:0 sc1\n" \
"M v[176:179],a[40:43],a[236:239],v[176:179]\nds_read_b64_tr_b16 a[228:229],v226 offset:33792\nds_read_b64_tr_b16 a[230:231],v226 offset:37888\nM v[180:183],a[44:47],a[236:239],v[180:183]\nbuffer_load_dwordx2 v[114:115],v239,s[52:55],0 offen offset:2048 sc1\nM v[184:187],a[40:43],a[240:243],v[184:187]\ns_mov_b32 s97,4096\nds_read_b64_tr_b16 a[232:233],v227 offset:33792\n" \
"M v[188:191],a[44:47],a[240:243],v[188:191]\nds_read_b64_tr_b16 a[234:235],v227 offset:37888\nbuffer_load_dwordx2 v[116:117],v239,s[52:55],s97 offen offset:0 sc1\ns_waitcnt lgkmcnt(6)\nM v[128:131],a[48:51],a[212:215],v[128:131]\nds_read_b64_tr_b16 a[236:237],v226 offset:34304\nds_read_b64_tr_b16 a[238:239],v226 offset:38400\nM v[132:135],a[52:55],a[212:215],v[132:135]\n" \
"buffer_load_dwordx2 v[118:119],v239,s[52:55],s97 offen offset:2048 sc1\nM v[136:139],a[48:51],a[216:219],v[136:139]\nds_read_b64_tr_b16 a[240:241],v227 offset:34304\nds_read_b64_tr_b16 a[242:243],v227 offset:38400\nM v[140:143],a[52:55],a[216:219],v[140:143]\nds_read_b64_tr_b16 a[212:213],v226 offset:40960\nds_read_b64_tr_b16 a[214:215],v226 offset:45056\nM v[144:147],a[48:51],a[220:223],v[144:147]\n" \
"ds_read_b64_tr_b16 a[216:217],v227 offset:40960\nds_read_b64_tr_b16 a[218:219],v227 offset:45056\nM v[148:151],a[52:55],a[220:223],v[148:151]\nds_read_b64_tr_b16 a[220:221],v226 offset:41472\ns_waitcnt lgkmcnt(10)\nM v[152:155],a[48:51],a[224:227],v[152:155]\nds_read_b64_tr_b16 a[222:223],v226 offset:45568\ns_add_u32 s65,s10,s67\n" \
"M v[156:159],a[52:55],a[224:227],v[156:159]\nds_read_b64_tr_b16 a[224:225],v227 offset:41472\nds_read_b64_tr_b16 a[226:227],v227 offset:45568\nM v[160:163],a[48:51],a[228:231],v[160:163]\ns_add_u32 s38,s57,16384\nv_lshlrev_b32 v238,1,v240\nM v[164:167],a[52:55],a[228:231],v[164:167]\nds_read_b64_tr_b16 a[228:229],v226 offset:41984\n" \
"s_waitcnt lgkmcnt(10)\nM v[168:171],a[48:51],a[232:235],v[168:171]\nds_read_b64_tr_b16 a[230:231],v226 offset:46080\nv_bfe_u32 v234,v249,2,2\nM v[172:175],a[52:55],a[232:235],v[172:175]\nds_read_b64_tr_b16 a[232:233],v227 offset:41984\nds_read_b64_tr_b16 a[234:235],v227 offset:46080\nM v[176:179],a[48:51],a[236:239],v[176:179]\n" \
"v_lshrrev_b32 v235,4,v249\nv_accvgpr_write_b32 a194,0\nM v[180:183],a[52:55],a[236:239],v[180:183]\nds_read_b64_tr_b16 a[236:237],v226 offset:42496\ns_waitcnt lgkmcnt(7)\nM v[184:187],a[48:51],a[240:243],v[184:187]\nds_read_b64_tr_b16 a[238:239],v226 offset:46592\nv_add_u32 v226,131072,v242\n" \
"M v[188:191],a[52:55],a[240:243],v[188:191]\nds_read_b64_tr_b16 a[240:241],v227 offset:42496\nds_read_b64_tr_b16 a[242:243],v227 offset:46592\nM v[128:131],a[56:59],a[212:215],v[128:131]\nv_xor_b32 v227,32,v226\nv_accvgpr_write_b32 a195,0\nM v[132:135],a[60:63],a[212:215],v[132:135]\nds_read_b64_tr_b16 a[212:213],v226 offset:0\n" \
"ds_read_b64_tr_b16 a[214:215],v226 offset:4096\nM v[136:139],a[56:59],a[216:219],v[136:139]\nv_accvgpr_write_b32 a196,0\nv_accvgpr_write_b32 a197,0\nM v[140:143],a[60:63],a[216:219],v[140:143]\nds_read_b64_tr_b16 a[216:217],v227 offset:0\nds_read_b64_tr_b16 a[218:219],v227 offset:4096\nM v[144:147],a[56:59],a[220:223],v[144:147]\n" \
"s_load_dwordx2 s[44:45],s[4:5],0x68\ns_mov_b32 s46,2097152\nM v[148:151],a[60:63],a[220:223],v[148:151]\nds_read_b64_tr_b16 a[220:221],v226 offset:512\ns_waitcnt lgkmcnt(7)\nM v[152:155],a[56:59],a[224:227],v[152:155]\ns_waitcnt vmcnt(7)\nv_cmp_eq_u32 vcc,0,v248\n" \
"M v[156:159],a[60:63],a[224:227],v[156:159]\ns_cbranch_vccnz .Lps8\n.Lpb8:\nds_read_b64_tr_b16 a[222:223],v226 offset:4608\nM v[160:163],a[56:59],a[228:231],v[160:163]\nbuffer_load_dwordx4 a[64:67],v240,s[52:55],s65 offen offset:0 sc1\nM v[164:167],a[60:63],a[228:231],v[164:167]\nds_read_b64_tr_b16 a[224:225],v227 offset:512\n" \
"ds_read_b64_tr_b16 a[226:227],v227 offset:4608\nM v[168:171],a[56:59],a[232:235],v[168:171]\nds_read_b64_tr_b16 a[228:229],v226 offset:1024\nds_read_b64_tr_b16 a[230:231],v226 offset:5120\nM v[172:175],a[60:63],a[232:235],v[172:175]\nds_read_b64_tr_b16 a[232:233],v227 offset:1024\nds_read_b64_tr_b16 a[234:235],v227 offset:5120\nM v[176:179],a[56:59],a[236:239],v[176:179]\n" \
"buffer_load_dwordx4 a[68:71],v240,s[52:55],s65 offen offset:1024 sc1\nM v[180:183],a[60:63],a[236:239],v[180:183]\nds_read_b64_tr_b16 a[236:237],v226 offset:1536\ns_waitcnt lgkmcnt(8)\nM v[184:187],a[56:59],a[240:243],v[184:187]\nds_read_b64_tr_b16 a[238:239],v226 offset:5632\nv_cvt_pk_bf16_f32 v192,v128,v136\nM v[188:191],a[60:63],a[240:243],v[188:191]\n" \
"ds_read_b64_tr_b16 a[240:241],v227 offset:1536\nds_read_b64_tr_b16 a[242:243],v227 offset:5632\nM v[8:11],v[0:3],a[212:215],0\nbuffer_load_dwordx4 a[72:75],v240,s[52:55],s65 offen offset:2048 sc1\nM v[12:15],v[4:7],a[212:215],0\nds_read_b64_tr_b16 a[212:213],v226 offset:8192\nds_read_b64_tr_b16 a[214:215],v226 offset:12288\nM v[24:27],v[0:3],a[216:219],0\n" \
"v_cvt_pk_bf16_f32 v193,v144,v152\nv_cvt_pk_bf16_f32 v194,v160,v168\nM v[28:31],v[4:7],a[216:219],0\nds_read_b64_tr_b16 a[216:217],v227 offset:8192\ns_waitcnt lgkmcnt(7)\nM v[40:43],v[0:3],a[220:223],0\nbuffer_load_dwordx4 a[76:79],v240,s[52:55],s65 offen offset:3072 sc1\nM v[44:47],v[4:7],a[220:223],0\n" \
"ds_read_b64_tr_b16 a[218:219],v227 offset:12288\ns_add_u32 s65,s65,4096\nM v[56:59],v[0:3],a[224:227],0\nds_read_b64_tr_b16 a[220:221],v226 offset:8704\nds_read_b64_tr_b16 a[222:223],v226 offset:12800\nM v[60:63],v[4:7],a[224:227],0\nds_read_b64_tr_b16 a[224:225],v227 offset:8704\nds_read_b64_tr_b16 a[226:227],v227 offset:12800\n" \
"M v[72:75],v[0:3],a[228:231],0\nbuffer_load_dwordx4 a[80:83],v240,s[52:55],s65 offen offset:0 sc1\nM v[76:79],v[4:7],a[228:231],0\nds_read_b64_tr_b16 a[228:229],v226 offset:9216\nds_read_b64_tr_b16 a[230:231],v226 offset:13312\nM v[88:91],v[0:3],a[232:235],0\nv_cvt_pk_bf16_f32 v195,v176,v184\nv_min_u32 v192,-2,v192\n" \
"M v[92:95],v[4:7],a[232:235],0\nv_min_u32 v193,-2,v193\ns_waitcnt lgkmcnt(7)\nM v[104:107],v[0:3],a[236:239],0\nbuffer_load_dwordx4 a[84:87],v240,s[52:55],s65 offen offset:1024 sc1\nM v[108:111],v[4:7],a[236:239],0\nv_min_u32 v194,-2,v194\nv_min_u32 v195,-2,v195\n" \
"M v[120:123],v[0:3],a[240:243],0\nbuffer_store_dwordx4 v[192:195],v253,s[52:55],s57 offen offset:0 sc1\nM v[124:127],v[4:7],a[240:243],0\nds_read_b64_tr_b16 a[232:233],v227 offset:9216\nds_read_b64_tr_b16 a[234:235],v227 offset:13312\nM v[8:11],v[16:19],a[212:215],v[8:11]\nbuffer_load_dwordx4 a[88:91],v240,s[52:55],s65 offen offset:2048 sc1\nM v[12:15],v[20:23],a[212:215],v[12:15]\n" \
"ds_read_b64_tr_b16 a[236:237],v226 offset:9728\nds_read_b64_tr_b16 a[238:239],v226 offset:13824\ns_waitcnt lgkmcnt(4)\nM v[24:27],v[16:19],a[216:219],v[24:27]\nds_read_b64_tr_b16 a[240:241],v227 offset:9728\nds_read_b64_tr_b16 a[242:243],v227 offset:13824\nM v[28:31],v[20:23],a[216:219],v[28:31]\nds_read_b64_tr_b16 a[212:213],v226 offset:16384\n" \
"ds_read_b64_tr_b16 a[214:215],v226 offset:20480\nM v[40:43],v[16:19],a[220:223],v[40:43]\nbuffer_load_dwordx4 a[92:95],v240,s[52:55],s65 offen offset:3072 sc1\nM v[44:47],v[20:23],a[220:223],v[44:47]\nds_read_b64_tr_b16 a[216:217],v227 offset:16384\nds_read_b64_tr_b16 a[218:219],v227 offset:20480\nM v[56:59],v[16:19],a[224:227],v[56:59]\ns_add_u32 s65,s11,s67\n" \
"ds_read_b64_tr_b16 a[220:221],v226 offset:16896\nM v[60:63],v[20:23],a[224:227],v[60:63]\nds_read_b64_tr_b16 a[222:223],v226 offset:20992\nds_read_b64_tr_b16 a[224:225],v227 offset:16896\nM v[72:75],v[16:19],a[228:231],v[72:75]\nbuffer_load_dwordx4 a[96:99],v240,s[52:55],s65 offen offset:0 sc1\nM v[76:79],v[20:23],a[228:231],v[76:79]\nds_read_b64_tr_b16 a[226:227],v227 offset:20992\n" \
"s_waitcnt lgkmcnt(9)\nM v[88:91],v[16:19],a[232:235],v[88:91]\nds_read_b64_tr_b16 a[228:229],v226 offset:17408\nds_read_b64_tr_b16 a[230:231],v226 offset:21504\nM v[92:95],v[20:23],a[232:235],v[92:95]\nv_cvt_pk_bf16_f32 v196,v129,v137\nv_cvt_pk_bf16_f32 v197,v145,v153\nM v[104:107],v[16:19],a[236:239],v[104:107]\n" \
"buffer_load_dwordx4 a[100:103],v240,s[52:55],s65 offen offset:1024 sc1\nM v[108:111],v[20:23],a[236:239],v[108:111]\nv_cvt_pk_bf16_f32 v198,v161,v169\nv_cvt_pk_bf16_f32 v199,v177,v185\ns_waitcnt lgkmcnt(5)\nM v[120:123],v[16:19],a[240:243],v[120:123]\nv_min_u32 v196,-2,v196\nv_min_u32 v197,-2,v197\n" \
"v_min_u32 v198,-2,v198\nv_min_u32 v199,-2,v199\nbuffer_store_dwordx4 v[196:199],v253,s[52:55],s57 offen offset:1024 sc1\nM v[124:127],v[20:23],a[240:243],v[124:127]\nds_read_b64_tr_b16 a[232:233],v227 offset:17408\nds_read_b64_tr_b16 a[234:235],v227 offset:21504\nM v[8:11],v[32:35],a[212:215],v[8:11]\nbuffer_load_dwordx4 a[104:107],v240,s[52:55],s65 offen offset:2048 sc1\n" \
"M v[12:15],v[36:39],a[212:215],v[12:15]\nds_read_b64_tr_b16 a[236:237],v226 offset:17920\nds_read_b64_tr_b16 a[238:239],v226 offset:22016\nM v[24:27],v[32:35],a[216:219],v[24:27]\nds_read_b64_tr_b16 a[240:241],v227 offset:17920\nds_read_b64_tr_b16 a[242:243],v227 offset:22016\nM v[28:31],v[36:39],a[216:219],v[28:31]\nds_read_b64_tr_b16 a[212:213],v226 offset:24576\n" \
"s_waitcnt lgkmcnt(7)\nM v[40:43],v[32:35],a[220:223],v[40:43]\nbuffer_load_dwordx4 a[108:111],v240,s[52:55],s65 offen offset:3072 sc1\nM v[44:47],v[36:39],a[220:223],v[44:47]\nds_read_b64_tr_b16 a[214:215],v226 offset:28672\nds_read_b64_tr_b16 a[216:217],v227 offset:24576\nM v[56:59],v[32:35],a[224:227],v[56:59]\nds_read_b64_tr_b16 a[218:219],v227 offset:28672\n" \
"s_add_u32 s65,s65,4096\nM v[60:63],v[36:39],a[224:227],v[60:63]\nds_read_b64_tr_b16 a[220:221],v226 offset:25088\nds_read_b64_tr_b16 a[222:223],v226 offset:29184\nM v[72:75],v[32:35],a[228:231],v[72:75]\nbuffer_load_dwordx4 a[112:115],v240,s[52:55],s65 offen offset:0 sc1\nM v[76:79],v[36:39],a[228:231],v[76:79]\nds_read_b64_tr_b16 a[224:225],v227 offset:25088\n" \
"s_waitcnt lgkmcnt(8)\nM v[88:91],v[32:35],a[232:235],v[88:91]\nds_read_b64_tr_b16 a[226:227],v227 offset:29184\nds_read_b64_tr_b16 a[228:229],v226 offset:25600\nM v[92:95],v[36:39],a[232:235],v[92:95]\nds_read_b64_tr_b16 a[230:231],v226 offset:29696\nv_cvt_pk_bf16_f32 v200,v130,v138\nM v[104:107],v[32:35],a[236:239],v[104:107]\n" \
"buffer_load_dwordx4 a[116:119],v240,s[52:55],s65 offen offset:1024 sc1\nM v[108:111],v[36:39],a[236:239],v[108:111]\nv_cvt_pk_bf16_f32 v201,v146,v154\nv_cvt_pk_bf16_f32 v202,v162,v170\nv_cvt_pk_bf16_f32 v203,v178,v186\ns_waitcnt lgkmcnt(6)\nM v[120:123],v[32:35],a[240:243],v[120:123]\nv_min_u32 v200,-2,v200\n" \
"v_min_u32 v201,-2,v201\nv_min_u32 v202,-2,v202\nv_min_u32 v203,-2,v203\nbuffer_store_dwordx4 v[200:203],v253,s[52:55],s57 offen offset:2048 sc1\nM v[124:127],v[36:39],a[240:243],v[124:127]\nds_read_b64_tr_b16 a[232:233],v227 offset:25600\nds_read_b64_tr_b16 a[234:235],v227 offset:29696\nM v[8:11],v[48:51],a[212:215],v[8:11]\n" \
"buffer_load_dwordx4 a[120:123],v240,s[52:55],s65 offen offset:2048 sc1\nM v[12:15],v[52:55],a[212:215],v[12:15]\nds_read_b64_tr_b16 a[236:237],v226 offset:26112\nds_read_b64_tr_b16 a[238:239],v226 offset:30208\nM v[24:27],v[48:51],a[216:219],v[24:27]\nds_read_b64_tr_b16 a[240:241],v227 offset:26112\nds_read_b64_tr_b16 a[242:243],v227 offset:30208\nM v[28:31],v[52:55],a[216:219],v[28:31]\n" \
"v_add_u32 v226,0,v242\ns_waitcnt lgkmcnt(6)\nM v[40:43],v[48:51],a[220:223],v[40:43]\nbuffer_load_dwordx4 a[124:127],v240,s[52:55],s65 offen offset:3072 sc1\nM v[44:47],v[52:55],a[220:223],v[44:47]\nds_read_b64_tr_b16 a[212:213],v226 offset:0\nds_read_b64_tr_b16 a[214:215],v226 offset:4096\nM v[56:59],v[48:51],a[224:227],v[56:59]\n" \
"v_xor_b32 v227,32,v226\nds_read_b64_tr_b16 a[216:217],v227 offset:0\nM v[60:63],v[52:55],a[224:227],v[60:63]\nds_read_b64_tr_b16 a[218:219],v227 offset:4096\ns_add_u32 s65,s14,s67\nM v[72:75],v[48:51],a[228:231],v[72:75]\nbuffer_load_dwordx4 a[128:131],v240,s[52:55],s65 offen offset:0 sc1\nM v[76:79],v[52:55],a[228:231],v[76:79]\n" \
"ds_read_b64_tr_b16 a[220:221],v226 offset:512\nds_read_b64_tr_b16 a[222:223],v226 offset:4608\ns_waitcnt lgkmcnt(6)\nM v[88:91],v[48:51],a[232:235],v[88:91]\nds_read_b64_tr_b16 a[224:225],v227 offset:512\nds_read_b64_tr_b16 a[226:227],v227 offset:4608\nM v[92:95],v[52:55],a[232:235],v[92:95]\nv_cvt_pk_bf16_f32 v204,v131,v139\n" \
"v_cvt_pk_bf16_f32 v205,v147,v155\nM v[104:107],v[48:51],a[236:239],v[104:107]\nbuffer_load_dwordx4 a[132:135],v240,s[52:55],s65 offen offset:1024 sc1\nM v[108:111],v[52:55],a[236:239],v[108:111]\nv_cvt_pk_bf16_f32 v206,v163,v171\nv_cvt_pk_bf16_f32 v207,v179,v187\nds_read_b64_tr_b16 a[228:229],v226 offset:1024\nds_read_b64_tr_b16 a[230:231],v226 offset:5120\n" \
"M v[120:123],v[48:51],a[240:243],v[120:123]\nv_min_u32 v204,-2,v204\nv_min_u32 v205,-2,v205\nv_min_u32 v206,-2,v206\nv_min_u32 v207,-2,v207\nbuffer_store_dwordx4 v[204:207],v253,s[52:55],s57 offen offset:3072 sc1\nM v[124:127],v[52:55],a[240:243],v[124:127]\nds_read_b64_tr_b16 a[232:233],v227 offset:1024\n" \
"ds_read_b64_tr_b16 a[234:235],v227 offset:5120\ns_waitcnt vmcnt(18) lgkmcnt(6)\nM v[8:11],a[64:67],a[212:215],v[8:11]\nbuffer_load_dwordx4 a[136:139],v240,s[52:55],s65 offen offset:2048 sc1\nM v[12:15],a[68:71],a[212:215],v[12:15]\nds_read_b64_tr_b16 a[236:237],v226 offset:1536\nds_read_b64_tr_b16 a[238:239],v226 offset:5632\nM v[24:27],a[64:67],a[216:219],v[24:27]\n" \
"ds_read_b64_tr_b16 a[240:241],v227 offset:1536\nds_read_b64_tr_b16 a[242:243],v227 offset:5632\nM v[28:31],a[68:71],a[216:219],v[28:31]\nds_read_b64_tr_b16 a[212:213],v226 offset:8192\nds_read_b64_tr_b16 a[214:215],v226 offset:12288\nM v[40:43],a[64:67],a[220:223],v[40:43]\nbuffer_load_dwordx4 a[140:143],v240,s[52:55],s65 offen offset:3072 sc1\nM v[44:47],a[68:71],a[220:223],v[44:47]\n" \
"ds_read_b64_tr_b16 a[216:217],v227 offset:8192\ns_waitcnt lgkmcnt(9)\nM v[56:59],a[64:67],a[224:227],v[56:59]\nds_read_b64_tr_b16 a[218:219],v227 offset:12288\ns_add_u32 s65,s65,4096\nM v[60:63],a[68:71],a[224:227],v[60:63]\nds_read_b64_tr_b16 a[220:221],v226 offset:8704\nds_read_b64_tr_b16 a[222:223],v226 offset:12800\n" \
"M v[72:75],a[64:67],a[228:231],v[72:75]\nbuffer_load_dwordx4 a[144:147],v240,s[52:55],s65 offen offset:0 sc1\nM v[76:79],a[68:71],a[228:231],v[76:79]\nds_read_b64_tr_b16 a[224:225],v227 offset:8704\ns_waitcnt lgkmcnt(8)\nM v[88:91],a[64:67],a[232:235],v[88:91]\nds_read_b64_tr_b16 a[226:227],v227 offset:12800\nv_cvt_pk_bf16_f32 v208,v132,v140\n" \
"M v[92:95],a[68:71],a[232:235],v[92:95]\nv_cvt_pk_bf16_f32 v209,v148,v156\nv_cvt_pk_bf16_f32 v210,v164,v172\nM v[104:107],a[64:67],a[236:239],v[104:107]\nbuffer_load_dwordx4 a[148:151],v240,s[52:55],s65 offen offset:1024 sc1\nM v[108:111],a[68:71],a[236:239],v[108:111]\nv_cvt_pk_bf16_f32 v211,v180,v188\nds_read_b64_tr_b16 a[228:229],v226 offset:9216\n" \
"ds_read_b64_tr_b16 a[230:231],v226 offset:13312\ns_waitcnt lgkmcnt(6)\nM v[120:123],a[64:67],a[240:243],v[120:123]\nv_min_u32 v208,-2,v208\nv_min_u32 v209,-2,v209\nv_min_u32 v210,-2,v210\nv_min_u32 v211,-2,v211\nbuffer_store_dwordx4 v[208:211],v253,s[52:55],s38 offen offset:0 sc1\n" \
"M v[124:127],a[68:71],a[240:243],v[124:127]\nds_read_b64_tr_b16 a[232:233],v227 offset:9216\nds_read_b64_tr_b16 a[234:235],v227 offset:13312\nM v[8:11],a[72:75],a[212:215],v[8:11]\nbuffer_load_dwordx4 a[152:155],v240,s[52:55],s65 offen offset:2048 sc1\nM v[12:15],a[76:79],a[212:215],v[12:15]\nds_read_b64_tr_b16 a[236:237],v226 offset:9728\nds_read_b64_tr_b16 a[238:239],v226 offset:13824\n" \
"M v[24:27],a[72:75],a[216:219],v[24:27]\nds_read_b64_tr_b16 a[240:241],v227 offset:9728\nds_read_b64_tr_b16 a[242:243],v227 offset:13824\nM v[28:31],a[76:79],a[216:219],v[28:31]\nds_read_b64_tr_b16 a[212:213],v226 offset:16384\ns_waitcnt lgkmcnt(7)\nM v[40:43],a[72:75],a[220:223],v[40:43]\nbuffer_load_dwordx4 a[156:159],v240,s[52:55],s65 offen offset:3072 sc1\n" \
"M v[44:47],a[76:79],a[220:223],v[44:47]\nds_read_b64_tr_b16 a[214:215],v226 offset:20480\nds_read_b64_tr_b16 a[216:217],v227 offset:16384\nM v[56:59],a[72:75],a[224:227],v[56:59]\nds_read_b64_tr_b16 a[218:219],v227 offset:20480\nds_read_b64_tr_b16 a[220:221],v226 offset:16896\nM v[60:63],a[76:79],a[224:227],v[60:63]\nds_read_b64_tr_b16 a[222:223],v226 offset:20992\n" \
"ds_read_b64_tr_b16 a[224:225],v227 offset:16896\nM v[72:75],a[72:75],a[228:231],v[72:75]\nds_read_b64_tr_b16 a[226:227],v227 offset:20992\nv_cvt_pk_bf16_f32 v212,v133,v141\nM v[76:79],a[76:79],a[228:231],v[76:79]\nv_cvt_pk_bf16_f32 v213,v149,v157\ns_waitcnt lgkmcnt(9)\nM v[88:91],a[72:75],a[232:235],v[88:91]\n" \
"v_cvt_pk_bf16_f32 v214,v165,v173\nv_cvt_pk_bf16_f32 v215,v181,v189\nM v[92:95],a[76:79],a[232:235],v[92:95]\nds_read_b64_tr_b16 a[228:229],v226 offset:17408\nds_read_b64_tr_b16 a[230:231],v226 offset:21504\nM v[104:107],a[72:75],a[236:239],v[104:107]\nv_min_u32 v212,-2,v212\nv_min_u32 v213,-2,v213\n" \
"M v[108:111],a[76:79],a[236:239],v[108:111]\nv_min_u32 v214,-2,v214\ns_waitcnt lgkmcnt(6)\nM v[120:123],a[72:75],a[240:243],v[120:123]\nv_min_u32 v215,-2,v215\nbuffer_store_dwordx4 v[212:215],v253,s[52:55],s38 offen offset:1024 sc1\nM v[124:127],a[76:79],a[240:243],v[124:127]\nds_read_b64_tr_b16 a[232:233],v227 offset:17408\n" \
"ds_read_b64_tr_b16 a[234:235],v227 offset:21504\ns_waitcnt vmcnt(24)\nM v[8:11],a[80:83],a[212:215],v[8:11]\nds_read_b64_tr_b16 a[236:237],v226 offset:17920\nds_read_b64_tr_b16 a[238:239],v226 offset:22016\nM v[12:15],a[84:87],a[212:215],v[12:15]\nds_read_b64_tr_b16 a[240:241],v227 offset:17920\nds_read_b64_tr_b16 a[242:243],v227 offset:22016\n" \
"M v[24:27],a[80:83],a[216:219],v[24:27]\nds_read_b64_tr_b16 a[212:213],v226 offset:24576\nds_read_b64_tr_b16 a[214:215],v226 offset:28672\nM v[28:31],a[84:87],a[216:219],v[28:31]\nds_read_b64_tr_b16 a[216:217],v227 offset:24576\ns_waitcnt lgkmcnt(9)\nM v[40:43],a[80:83],a[220:223],v[40:43]\nds_read_b64_tr_b16 a[218:219],v227 offset:28672\n" \
"v_cvt_pk_bf16_f32 v216,v134,v142\nM v[44:47],a[84:87],a[220:223],v[44:47]\nds_read_b64_tr_b16 a[220:221],v226 offset:25088\nds_read_b64_tr_b16 a[222:223],v226 offset:29184\nM v[56:59],a[80:83],a[224:227],v[56:59]\nv_cvt_pk_bf16_f32 v217,v150,v158\nv_cvt_pk_bf16_f32 v218,v166,v174\nM v[60:63],a[84:87],a[224:227],v[60:63]\n" \
"ds_read_b64_tr_b16 a[224:225],v227 offset:25088\nds_read_b64_tr_b16 a[226:227],v227 offset:29184\nM v[72:75],a[80:83],a[228:231],v[72:75]\nv_cvt_pk_bf16_f32 v219,v182,v190\nv_min_u32 v216,-2,v216\nM v[76:79],a[84:87],a[228:231],v[76:79]\nds_read_b64_tr_b16 a[228:229],v226 offset:25600\ns_waitcnt lgkmcnt(8)\n" \
"M v[88:91],a[80:83],a[232:235],v[88:91]\nds_read_b64_tr_b16 a[230:231],v226 offset:29696\nv_min_u32 v217,-2,v217\nM v[92:95],a[84:87],a[232:235],v[92:95]\nv_min_u32 v218,-2,v218\nv_min_u32 v219,-2,v219\nM v[104:107],a[80:83],a[236:239],v[104:107]\nds_read_b64_tr_b16 a[232:233],v227 offset:25600\n" \
"ds_read_b64_tr_b16 a[234:235],v227 offset:29696\nM v[108:111],a[84:87],a[236:239],v[108:111]\nds_read_b64_tr_b16 a[236:237],v226 offset:26112\nds_read_b64_tr_b16 a[238:239],v226 offset:30208\nM v[120:123],a[80:83],a[240:243],v[120:123]\nbuffer_store_dwordx4 v[216:219],v253,s[52:55],s38 offen offset:2048 sc1\nM v[124:127],a[84:87],a[240:243],v[124:127]\nds_read_b64_tr_b16 a[240:241],v227 offset:26112\n" \
"s_waitcnt vmcnt(20) lgkmcnt(7)\nM v[8:11],a[88:91],a[212:215],v[8:11]\nds_read_b64_tr_b16 a[242:243],v227 offset:30208\nv_cvt_pk_bf16_f32 v220,v135,v143\nM v[12:15],a[92:95],a[212:215],v[12:15]\nds_read_b64_tr_b16 a[212:213],v226 offset:32768\nds_read_b64_tr_b16 a[214:215],v226 offset:36864\nM v[24:27],a[88:91],a[216:219],v[24:27]\n" \
"v_cvt_pk_bf16_f32 v221,v151,v159\nv_cvt_pk_bf16_f32 v222,v167,v175\nM v[28:31],a[92:95],a[216:219],v[28:31]\nds_read_b64_tr_b16 a[216:217],v227 offset:32768\nds_read_b64_tr_b16 a[218:219],v227 offset:36864\nM v[40:43],a[88:91],a[220:223],v[40:43]\nv_cvt_pk_bf16_f32 v223,v183,v191\nv_min_u32 v220,-2,v220\n" \
"M v[44:47],a[92:95],a[220:223],v[44:47]\nds_read_b64_tr_b16 a[220:221],v226 offset:33280\nds_read_b64_tr_b16 a[222:223],v226 offset:37376\nM v[56:59],a[88:91],a[224:227],v[56:59]\nv_min_u32 v221,-2,v221\nv_min_u32 v222,-2,v222\nM v[60:63],a[92:95],a[224:227],v[60:63]\nds_read_b64_tr_b16 a[224:225],v227 offset:33280\n" \
"s_waitcnt lgkmcnt(7)\nM v[72:75],a[88:91],a[228:231],v[72:75]\nds_read_b64_tr_b16 a[226:227],v227 offset:37376\nv_min_u32 v223,-2,v223\nM v[76:79],a[92:95],a[228:231],v[76:79]\nds_read_b64_tr_b16 a[228:229],v226 offset:33792\nds_read_b64_tr_b16 a[230:231],v226 offset:37888\nM v[88:91],a[88:91],a[232:235],v[88:91]\n" \
"v_cmp_eq_u32 vcc,v234,v235\nv_and_b32 v234,1,v249\nM v[92:95],a[92:95],a[232:235],v[92:95]\nds_read_b64_tr_b16 a[232:233],v227 offset:33792\nds_read_b64_tr_b16 a[234:235],v227 offset:37888\nM v[104:107],a[88:91],a[236:239],v[104:107]\nv_lshlrev_b32 v234,4,v234\nv_mov_b32 v235,0x3f80\n" \
"M v[108:111],a[92:95],a[236:239],v[108:111]\nds_read_b64_tr_b16 a[236:237],v226 offset:34304\nds_read_b64_tr_b16 a[238:239],v226 offset:38400\nM v[120:123],a[88:91],a[240:243],v[120:123]\nbuffer_store_dwordx4 v[220:223],v253,s[52:55],s38 offen offset:3072 sc1\nM v[124:127],a[92:95],a[240:243],v[124:127]\nds_read_b64_tr_b16 a[240:241],v227 offset:34304\ns_waitcnt lgkmcnt(7)\n" \
"M v[8:11],a[96:99],a[212:215],v[8:11]\nds_read_b64_tr_b16 a[242:243],v227 offset:38400\nv_lshlrev_b32 v234,v234,v235\nM v[12:15],a[100:103],a[212:215],v[12:15]\nds_read_b64_tr_b16 a[212:213],v226 offset:40960\nds_read_b64_tr_b16 a[214:215],v226 offset:45056\nM v[24:27],a[96:99],a[216:219],v[24:27]\nv_cndmask_b32 v234,0,v234,vcc\n" \
"v_and_b32 v235,2,v249\nM v[28:31],a[100:103],a[216:219],v[28:31]\nds_read_b64_tr_b16 a[216:217],v227 offset:40960\nds_read_b64_tr_b16 a[218:219],v227 offset:45056\nM v[40:43],a[96:99],a[220:223],v[40:43]\nv_cmp_eq_u32 vcc,0,v235\ns_add_u32 s38,s78,16384\nM v[44:47],a[100:103],a[220:223],v[44:47]\n" \
"ds_read_b64_tr_b16 a[220:221],v226 offset:41472\nds_read_b64_tr_b16 a[222:223],v226 offset:45568\nM v[56:59],a[96:99],a[224:227],v[56:59]\nbuffer_load_dwordx4 v[0:3],v238,s[52:55],0 offen offset:0 sc1\nM v[60:63],a[100:103],a[224:227],v[60:63]\nds_read_b64_tr_b16 a[224:225],v227 offset:41472\ns_waitcnt lgkmcnt(7)\nM v[72:75],a[96:99],a[228:231],v[72:75]\n" \
"ds_read_b64_tr_b16 a[226:227],v227 offset:45568\nv_cndmask_b32 v235,0,v234,vcc\nM v[76:79],a[100:103],a[228:231],v[76:79]\nds_read_b64_tr_b16 a[228:229],v226 offset:41984\nds_read_b64_tr_b16 a[230:231],v226 offset:46080\nM v[88:91],a[96:99],a[232:235],v[88:91]\nv_xor_b32 v234,v234,v235\nv_lshlrev_b32 v64,16,v64\n" \
"M v[92:95],a[100:103],a[232:235],v[92:95]\nds_read_b64_tr_b16 a[232:233],v227 offset:41984\nds_read_b64_tr_b16 a[234:235],v227 offset:46080\nM v[104:107],a[96:99],a[236:239],v[104:107]\nbuffer_load_dwordx4 v[4:7],v238,s[52:55],0 offen offset:16 sc1\nM v[108:111],a[100:103],a[236:239],v[108:111]\nds_read_b64_tr_b16 a[236:237],v226 offset:42496\nds_read_b64_tr_b16 a[238:239],v226 offset:46592\n" \
"M v[120:123],a[96:99],a[240:243],v[120:123]\nv_lshlrev_b32 v65,16,v65\nv_lshlrev_b32 v66,16,v66\nM v[124:127],a[100:103],a[240:243],v[124:127]\nds_read_b64_tr_b16 a[240:241],v227 offset:42496\ns_waitcnt vmcnt(18) lgkmcnt(7)\nM v[8:11],a[104:107],a[212:215],v[8:11]\nds_read_b64_tr_b16 a[242:243],v227 offset:46592\n" \
"v_lshlrev_b32 v67,16,v67\nM v[12:15],a[108:111],a[212:215],v[12:15]\nds_read_b64_tr_b16 a[212:213],v226 offset:49152\nds_read_b64_tr_b16 a[214:215],v226 offset:53248\nM v[24:27],a[104:107],a[216:219],v[24:27]\nbuffer_load_dwordx4 v[16:19],v238,s[52:55],0 offen offset:2048 sc1\nM v[28:31],a[108:111],a[216:219],v[28:31]\nds_read_b64_tr_b16 a[216:217],v227 offset:49152\n" \
"ds_read_b64_tr_b16 a[218:219],v227 offset:53248\nM v[40:43],a[104:107],a[220:223],v[40:43]\nv_lshlrev_b32 v68,16,v68\nv_lshlrev_b32 v69,16,v69\nM v[44:47],a[108:111],a[220:223],v[44:47]\nds_read_b64_tr_b16 a[220:221],v226 offset:49664\nds_read_b64_tr_b16 a[222:223],v226 offset:53760\nM v[56:59],a[104:107],a[224:227],v[56:59]\n" \
"v_lshlrev_b32 v70,16,v70\nv_lshlrev_b32 v71,16,v71\nM v[60:63],a[108:111],a[224:227],v[60:63]\nds_read_b64_tr_b16 a[224:225],v227 offset:49664\ns_waitcnt lgkmcnt(7)\nM v[72:75],a[104:107],a[228:231],v[72:75]\nbuffer_load_dwordx4 v[20:23],v238,s[52:55],0 offen offset:2064 sc1\nM v[76:79],a[108:111],a[228:231],v[76:79]\n" \
"ds_read_b64_tr_b16 a[226:227],v227 offset:53760\nds_read_b64_tr_b16 a[228:229],v226 offset:50176\nM v[88:91],a[104:107],a[232:235],v[88:91]\nds_read_b64_tr_b16 a[230:231],v226 offset:54272\nv_lshlrev_b32 v80,16,v80\nM v[92:95],a[108:111],a[232:235],v[92:95]\nds_read_b64_tr_b16 a[232:233],v227 offset:50176\nds_read_b64_tr_b16 a[234:235],v227 offset:54272\n" \
"M v[104:107],a[104:107],a[236:239],v[104:107]\nv_lshlrev_b32 v81,16,v81\nv_lshlrev_b32 v82,16,v82\nM v[108:111],a[108:111],a[236:239],v[108:111]\nds_read_b64_tr_b16 a[236:237],v226 offset:50688\nds_read_b64_tr_b16 a[238:239],v226 offset:54784\nM v[120:123],a[104:107],a[240:243],v[120:123]\nbuffer_load_dwordx4 v[32:35],v238,s[52:55],s97 offen offset:0 sc1\n" \
"M v[124:127],a[108:111],a[240:243],v[124:127]\nds_read_b64_tr_b16 a[240:241],v227 offset:50688\ns_waitcnt lgkmcnt(8)\nM v[8:11],a[112:115],a[212:215],v[8:11]\nds_read_b64_tr_b16 a[242:243],v227 offset:54784\nv_lshlrev_b32 v83,16,v83\nM v[12:15],a[116:119],a[212:215],v[12:15]\nds_read_b64_tr_b16 a[212:213],v226 offset:57344\n" \
"ds_read_b64_tr_b16 a[214:215],v226 offset:61440\nM v[24:27],a[112:115],a[216:219],v[24:27]\nv_lshlrev_b32 v84,16,v84\nv_lshlrev_b32 v85,16,v85\nM v[28:31],a[116:119],a[216:219],v[28:31]\nds_read_b64_tr_b16 a[216:217],v227 offset:57344\nds_read_b64_tr_b16 a[218:219],v227 offset:61440\nM v[40:43],a[112:115],a[220:223],v[40:43]\n" \
"buffer_load_dwordx4 v[36:39],v238,s[52:55],s97 offen offset:16 sc1\nM v[44:47],a[116:119],a[220:223],v[44:47]\nds_read_b64_tr_b16 a[220:221],v226 offset:57856\ns_waitcnt lgkmcnt(7)\nM v[56:59],a[112:115],a[224:227],v[56:59]\nds_read_b64_tr_b16 a[222:223],v226 offset:61952\nv_lshlrev_b32 v86,16,v86\nM v[60:63],a[116:119],a[224:227],v[60:63]\n" \
"ds_read_b64_tr_b16 a[224:225],v227 offset:57856\nds_read_b64_tr_b16 a[226:227],v227 offset:61952\nM v[72:75],a[112:115],a[228:231],v[72:75]\nv_lshlrev_b32 v87,16,v87\nv_lshlrev_b32 v96,16,v96\nM v[76:79],a[116:119],a[228:231],v[76:79]\nds_read_b64_tr_b16 a[228:229],v226 offset:58368\nds_read_b64_tr_b16 a[230:231],v226 offset:62464\n" \
"M v[88:91],a[112:115],a[232:235],v[88:91]\nbuffer_load_dwordx4 v[48:51],v238,s[52:55],s97 offen offset:2048 sc1\nM v[92:95],a[116:119],a[232:235],v[92:95]\nds_read_b64_tr_b16 a[232:233],v227 offset:58368\nds_read_b64_tr_b16 a[234:235],v227 offset:62464\nM v[104:107],a[112:115],a[236:239],v[104:107]\nv_lshlrev_b32 v97,16,v97\nv_lshlrev_b32 v98,16,v98\n" \
"M v[108:111],a[116:119],a[236:239],v[108:111]\nds_read_b64_tr_b16 a[236:237],v226 offset:58880\ns_waitcnt lgkmcnt(7)\nM v[120:123],a[112:115],a[240:243],v[120:123]\nds_read_b64_tr_b16 a[238:239],v226 offset:62976\nv_add_u32 v226,65536,v242\nM v[124:127],a[116:119],a[240:243],v[124:127]\nds_read_b64_tr_b16 a[240:241],v227 offset:58880\n" \
"s_waitcnt vmcnt(18)\nM v[8:11],a[120:123],a[212:215],v[8:11]\nbuffer_load_dwordx4 v[52:55],v238,s[52:55],s97 offen offset:2064 sc1\nM v[12:15],a[124:127],a[212:215],v[12:15]\nds_read_b64_tr_b16 a[242:243],v227 offset:62976\nds_read_b64_tr_b16 a[212:213],v226 offset:0\nM v[24:27],a[120:123],a[216:219],v[24:27]\nds_read_b64_tr_b16 a[214:215],v226 offset:4096\n" \
"v_xor_b32 v227,32,v226\nM v[28:31],a[124:127],a[216:219],v[28:31]\nds_read_b64_tr_b16 a[216:217],v227 offset:0\nds_read_b64_tr_b16 a[218:219],v227 offset:4096\nM v[40:43],a[120:123],a[220:223],v[40:43]\nv_lshlrev_b32 v99,16,v99\nv_lshlrev_b32 v100,16,v100\nM v[44:47],a[124:127],a[220:223],v[44:47]\n" \
"ds_read_b64_tr_b16 a[220:221],v226 offset:512\ns_waitcnt lgkmcnt(7)\nM v[56:59],a[120:123],a[224:227],v[56:59]\nds_read_b64_tr_b16 a[222:223],v226 offset:4608\nv_lshlrev_b32 v101,16,v101\nM v[60:63],a[124:127],a[224:227],v[60:63]\nds_read_b64_tr_b16 a[224:225],v227 offset:512\nds_read_b64_tr_b16 a[226:227],v227 offset:4608\n" \
"M v[72:75],a[120:123],a[228:231],v[72:75]\nv_lshlrev_b32 v102,16,v102\nv_lshlrev_b32 v103,16,v103\nM v[76:79],a[124:127],a[228:231],v[76:79]\nds_read_b64_tr_b16 a[228:229],v226 offset:1024\nds_read_b64_tr_b16 a[230:231],v226 offset:5120\nM v[88:91],a[120:123],a[232:235],v[88:91]\nv_accvgpr_write_b32 a192,v235\n" \
"v_accvgpr_write_b32 a193,v234\nM v[92:95],a[124:127],a[232:235],v[92:95]\nds_read_b64_tr_b16 a[232:233],v227 offset:1024\nds_read_b64_tr_b16 a[234:235],v227 offset:5120\nM v[104:107],a[120:123],a[236:239],v[104:107]\nv_accvgpr_write_b32 a198,v235\nv_accvgpr_write_b32 a199,v234\nM v[108:111],a[124:127],a[236:239],v[108:111]\n" \
"ds_read_b64_tr_b16 a[236:237],v226 offset:1536\ns_waitcnt lgkmcnt(7)\nM v[120:123],a[120:123],a[240:243],v[120:123]\nds_read_b64_tr_b16 a[238:239],v226 offset:5632\ns_add_u32 s95,s95,54525952\nM v[124:127],a[124:127],a[240:243],v[124:127]\nds_read_b64_tr_b16 a[240:241],v227 offset:1536\nds_read_b64_tr_b16 a[242:243],v227 offset:5632\n" \
"M v[8:11],a[128:131],a[212:215],v[8:11]\ns_add_u32 s74,s74,54525952\nM v[12:15],a[132:135],a[212:215],v[12:15]\nds_read_b64_tr_b16 a[212:213],v226 offset:8192\nds_read_b64_tr_b16 a[214:215],v226 offset:12288\nM v[24:27],a[128:131],a[216:219],v[24:27]\nM v[28:31],a[132:135],a[216:219],v[28:31]\nds_read_b64_tr_b16 a[216:217],v227 offset:8192\n" \
"ds_read_b64_tr_b16 a[218:219],v227 offset:12288\nM v[40:43],a[128:131],a[220:223],v[40:43]\nM v[44:47],a[132:135],a[220:223],v[44:47]\nds_read_b64_tr_b16 a[220:221],v226 offset:8704\ns_waitcnt lgkmcnt(9)\nM v[56:59],a[128:131],a[224:227],v[56:59]\nds_read_b64_tr_b16 a[222:223],v226 offset:12800\nM v[60:63],a[132:135],a[224:227],v[60:63]\n" \
"ds_read_b64_tr_b16 a[224:225],v227 offset:8704\nds_read_b64_tr_b16 a[226:227],v227 offset:12800\nM v[72:75],a[128:131],a[228:231],v[72:75]\nM v[76:79],a[132:135],a[228:231],v[76:79]\nds_read_b64_tr_b16 a[228:229],v226 offset:9216\nds_read_b64_tr_b16 a[230:231],v226 offset:13312\nM v[88:91],a[128:131],a[232:235],v[88:91]\nM v[92:95],a[132:135],a[232:235],v[92:95]\n" \
"ds_read_b64_tr_b16 a[232:233],v227 offset:9216\ns_waitcnt lgkmcnt(9)\nM v[104:107],a[128:131],a[236:239],v[104:107]\nds_read_b64_tr_b16 a[234:235],v227 offset:13312\nM v[108:111],a[132:135],a[236:239],v[108:111]\nds_read_b64_tr_b16 a[236:237],v226 offset:9728\nds_read_b64_tr_b16 a[238:239],v226 offset:13824\nM v[120:123],a[128:131],a[240:243],v[120:123]\n" \
"M v[124:127],a[132:135],a[240:243],v[124:127]\nds_read_b64_tr_b16 a[240:241],v227 offset:9728\ns_waitcnt vmcnt(14)\nM v[8:11],a[136:139],a[212:215],v[8:11]\nds_read_b64_tr_b16 a[242:243],v227 offset:13824\nM v[12:15],a[140:143],a[212:215],v[12:15]\nds_read_b64_tr_b16 a[212:213],v226 offset:32768\ns_waitcnt lgkmcnt(9)\n" \
"M v[24:27],a[136:139],a[216:219],v[24:27]\nds_read_b64_tr_b16 a[214:215],v226 offset:36864\nM v[28:31],a[140:143],a[216:219],v[28:31]\nds_read_b64_tr_b16 a[216:217],v227 offset:32768\nds_read_b64_tr_b16 a[218:219],v227 offset:36864\nM v[40:43],a[136:139],a[220:223],v[40:43]\nM v[44:47],a[140:143],a[220:223],v[44:47]\nds_read_b64_tr_b16 a[220:221],v226 offset:33280\n" \
"ds_read_b64_tr_b16 a[222:223],v226 offset:37376\nM v[56:59],a[136:139],a[224:227],v[56:59]\nM v[60:63],a[140:143],a[224:227],v[60:63]\nds_read_b64_tr_b16 a[224:225],v227 offset:33280\ns_waitcnt lgkmcnt(9)\nM v[72:75],a[136:139],a[228:231],v[72:75]\nds_read_b64_tr_b16 a[226:227],v227 offset:37376\nM v[76:79],a[140:143],a[228:231],v[76:79]\n" \
"ds_read_b64_tr_b16 a[228:229],v226 offset:33792\nds_read_b64_tr_b16 a[230:231],v226 offset:37888\nM v[88:91],a[136:139],a[232:235],v[88:91]\nM v[92:95],a[140:143],a[232:235],v[92:95]\nds_read_b64_tr_b16 a[232:233],v227 offset:33792\nds_read_b64_tr_b16 a[234:235],v227 offset:37888\nM v[104:107],a[136:139],a[236:239],v[104:107]\nM v[108:111],a[140:143],a[236:239],v[108:111]\n" \
"ds_read_b64_tr_b16 a[236:237],v226 offset:34304\ns_waitcnt lgkmcnt(9)\nM v[120:123],a[136:139],a[240:243],v[120:123]\nds_read_b64_tr_b16 a[238:239],v226 offset:38400\nM v[124:127],a[140:143],a[240:243],v[124:127]\nds_read_b64_tr_b16 a[240:241],v227 offset:34304\nds_read_b64_tr_b16 a[242:243],v227 offset:38400\nM v[8:11],a[144:147],a[212:215],v[8:11]\n" \
"M v[12:15],a[148:151],a[212:215],v[12:15]\nds_read_b64_tr_b16 a[212:213],v226 offset:40960\nds_read_b64_tr_b16 a[214:215],v226 offset:45056\nM v[24:27],a[144:147],a[216:219],v[24:27]\nM v[28:31],a[148:151],a[216:219],v[28:31]\nds_read_b64_tr_b16 a[216:217],v227 offset:40960\ns_waitcnt lgkmcnt(9)\nM v[40:43],a[144:147],a[220:223],v[40:43]\n" \
"ds_read_b64_tr_b16 a[218:219],v227 offset:45056\nM v[44:47],a[148:151],a[220:223],v[44:47]\nds_read_b64_tr_b16 a[220:221],v226 offset:41472\nds_read_b64_tr_b16 a[222:223],v226 offset:45568\nM v[56:59],a[144:147],a[224:227],v[56:59]\nM v[60:63],a[148:151],a[224:227],v[60:63]\nds_read_b64_tr_b16 a[224:225],v227 offset:41472\nds_read_b64_tr_b16 a[226:227],v227 offset:45568\n" \
"M v[72:75],a[144:147],a[228:231],v[72:75]\nM v[76:79],a[148:151],a[228:231],v[76:79]\nds_read_b64_tr_b16 a[228:229],v226 offset:41984\ns_waitcnt lgkmcnt(9)\nM v[88:91],a[144:147],a[232:235],v[88:91]\nds_read_b64_tr_b16 a[230:231],v226 offset:46080\nM v[92:95],a[148:151],a[232:235],v[92:95]\nds_read_b64_tr_b16 a[232:233],v227 offset:41984\n" \
"ds_read_b64_tr_b16 a[234:235],v227 offset:46080\nM v[104:107],a[144:147],a[236:239],v[104:107]\nM v[108:111],a[148:151],a[236:239],v[108:111]\nds_read_b64_tr_b16 a[236:237],v226 offset:42496\nds_read_b64_tr_b16 a[238:239],v226 offset:46592\nM v[120:123],a[144:147],a[240:243],v[120:123]\nM v[124:127],a[148:151],a[240:243],v[124:127]\nds_read_b64_tr_b16 a[240:241],v227 offset:42496\n" \
"s_waitcnt vmcnt(11) lgkmcnt(9)\nM v[8:11],a[152:155],a[212:215],v[8:11]\nds_read_b64_tr_b16 a[242:243],v227 offset:46592\nM v[12:15],a[156:159],a[212:215],v[12:15]\nM v[24:27],a[152:155],a[216:219],v[24:27]\nM v[28:31],a[156:159],a[216:219],v[28:31]\nM v[40:43],a[152:155],a[220:223],v[40:43]\nM v[44:47],a[156:159],a[220:223],v[44:47]\n" \
"s_waitcnt lgkmcnt(6)\nM v[56:59],a[152:155],a[224:227],v[56:59]\nM v[60:63],a[156:159],a[224:227],v[60:63]\nM v[72:75],a[152:155],a[228:231],v[72:75]\nM v[76:79],a[156:159],a[228:231],v[76:79]\nv_cvt_pk_bf16_f32 v192,v8,v24\ns_waitcnt lgkmcnt(2)\nM v[88:91],a[152:155],a[232:235],v[88:91]\n" \
"v_cvt_pk_bf16_f32 v196,v9,v25\nv_cvt_pk_bf16_f32 v200,v10,v26\nM v[92:95],a[156:159],a[232:235],v[92:95]\nv_cvt_pk_bf16_f32 v193,v40,v56\nv_cvt_pk_bf16_f32 v197,v41,v57\nM v[104:107],a[152:155],a[236:239],v[104:107]\nv_cvt_pk_bf16_f32 v201,v42,v58\nv_cvt_pk_bf16_f32 v204,v11,v27\n" \
"M v[108:111],a[156:159],a[236:239],v[108:111]\nv_cvt_pk_bf16_f32 v194,v72,v88\ns_waitcnt lgkmcnt(0)\nM v[120:123],a[152:155],a[240:243],v[120:123]\nv_cvt_pk_bf16_f32 v198,v73,v89\nv_cvt_pk_bf16_f32 v202,v74,v90\nv_cvt_pk_bf16_f32 v205,v43,v59\nv_cvt_pk_bf16_f32 v206,v75,v91\n" \
"v_cvt_pk_bf16_f32 v208,v12,v28\nv_cvt_pk_bf16_f32 v209,v44,v60\nv_cvt_pk_bf16_f32 v210,v76,v92\nv_cvt_pk_bf16_f32 v212,v13,v29\nv_cvt_pk_bf16_f32 v195,v104,v120\nv_cvt_pk_bf16_f32 v199,v105,v121\nv_cvt_pk_bf16_f32 v203,v106,v122\nv_cvt_pk_bf16_f32 v207,v107,v123\n" \
"v_cvt_pk_bf16_f32 v213,v45,v61\nv_cvt_pk_bf16_f32 v214,v77,v93\nv_cvt_pk_bf16_f32 v216,v14,v30\nv_cvt_pk_bf16_f32 v217,v46,v62\nv_cvt_pk_bf16_f32 v218,v78,v94\nv_cvt_pk_bf16_f32 v220,v15,v31\nv_cvt_pk_bf16_f32 v221,v47,v63\nv_cvt_pk_bf16_f32 v222,v79,v95\n" \
"M v[124:127],a[156:159],a[240:243],v[124:127]\nv_min_u32 v192,-2,v192\nv_min_u32 v193,-2,v193\nv_min_u32 v194,-2,v194\nv_min_u32 v195,-2,v195\nv_min_u32 v196,-2,v196\nv_min_u32 v197,-2,v197\nv_min_u32 v198,-2,v198\n" \
"v_min_u32 v199,-2,v199\nv_cvt_pk_bf16_f32 v211,v108,v124\nv_cvt_pk_bf16_f32 v215,v109,v125\nv_cvt_pk_bf16_f32 v219,v110,v126\nv_cvt_pk_bf16_f32 v223,v111,v127\nv_min_u32 v200,-2,v200\nv_min_u32 v201,-2,v201\nv_min_u32 v202,-2,v202\n" \
"v_min_u32 v203,-2,v203\nv_min_u32 v204,-2,v204\nv_min_u32 v205,-2,v205\nv_min_u32 v206,-2,v206\nv_min_u32 v207,-2,v207\nv_min_u32 v208,-2,v208\nv_min_u32 v209,-2,v209\nv_min_u32 v210,-2,v210\n" \
"v_min_u32 v211,-2,v211\nv_min_u32 v212,-2,v212\nv_min_u32 v213,-2,v213\nv_min_u32 v214,-2,v214\nv_min_u32 v215,-2,v215\nv_min_u32 v216,-2,v216\nv_min_u32 v217,-2,v217\nv_min_u32 v218,-2,v218\n" \
"v_min_u32 v219,-2,v219\nv_min_u32 v220,-2,v220\nv_min_u32 v221,-2,v221\nv_min_u32 v222,-2,v222\nv_min_u32 v223,-2,v223\nbuffer_store_dwordx4 v[192:195],v253,s[52:55],s78 offen offset:0 sc1\nbuffer_store_dwordx4 v[196:199],v253,s[52:55],s78 offen offset:1024 sc1\nbuffer_store_dwordx4 v[200:203],v253,s[52:55],s78 offen offset:2048 sc1\n" \
"buffer_store_dwordx4 v[204:207],v253,s[52:55],s78 offen offset:3072 sc1\nbuffer_store_dwordx4 v[208:211],v253,s[52:55],s38 offen offset:0 sc1\nbuffer_store_dwordx4 v[212:215],v253,s[52:55],s38 offen offset:1024 sc1\nbuffer_store_dwordx4 v[216:219],v253,s[52:55],s38 offen offset:2048 sc1\nbuffer_store_dwordx4 v[220:223],v253,s[52:55],s38 offen offset:3072 sc1\ns_branch .Ltail\n.Lod1x:\nds_read_b128 a[64:67],v250 offset:0\n" \
"ds_read_b128 a[68:71],v250 offset:1024\nds_read_b128 a[72:75],v250 offset:2048\ns_waitcnt lgkmcnt(2)\nM v[0:3],a[64:67],v[192:195],v[0:3]\ns_lshl4_add_u32 s69,s10,s68\ns_add_u32 m0,s64,0\nM v[4:7],a[64:67],v[196:199],v[4:7]\nds_read_b128 a[76:79],v250 offset:3072\n" \
"v_cvt_pk_bf16_f32 v216,v168,v169\nM v[64:67],a[64:67],v[200:203],v[64:67]\nbuffer_store_dwordx4 a[248:251],v253,s[52:55],s57 offen offset:0 sc1\nM v[68:71],a[64:67],v[204:207],v[68:71]\nds_read_b128 a[80:83],v250 offset:4096\ns_waitcnt lgkmcnt(3)\nM v[8:11],a[68:71],v[192:195],v[8:11]\nds_read_b128 a[84:87],v250 offset:5120\n" \
"ds_read_b128 a[88:91],v250 offset:6144\nM v[12:15],a[68:71],v[196:199],v[12:15]\nbuffer_store_dwordx4 a[248:251],v253,s[52:55],s57 offen offset:1024 sc1\nM v[72:75],a[68:71],v[200:203],v[72:75]\nbuffer_load_dwordx4 v243,s[40:43],s69 offen offset:0 lds\nM v[76:79],a[68:71],v[204:207],v[76:79]\nds_read_b128 a[92:95],v250 offset:7168\ns_waitcnt lgkmcnt(5)\n" \
"M v[16:19],a[72:75],v[192:195],v[16:19]\nbuffer_store_dwordx4 a[248:251],v253,s[52:55],s57 offen offset:2048 sc1\nM v[20:23],a[72:75],v[196:199],v[20:23]\nds_read_b128 a[96:99],v250 offset:8192\nds_read_b128 a[100:103],v250 offset:9216\nM v[80:83],a[72:75],v[200:203],v[80:83]\nbuffer_load_dwordx4 v244,s[40:43],s69 offen offset:1024 lds\nM v[84:87],a[72:75],v[204:207],v[84:87]\n" \
"buffer_store_dwordx4 a[248:251],v253,s[52:55],s57 offen offset:3072 sc1\ns_waitcnt lgkmcnt(3)\nM v[24:27],a[76:79],v[192:195],v[24:27]\nds_read_b128 a[104:107],v250 offset:10240\nds_read_b128 a[108:111],v250 offset:11264\nM v[28:31],a[76:79],v[196:199],v[28:31]\nv_exp_f32 v184,v184\nM v[88:91],a[76:79],v[200:203],v[88:91]\n" \
"buffer_load_dwordx4 v245,s[40:43],s69 offen offset:2048 lds\nM v[92:95],a[76:79],v[204:207],v[92:95]\nv_exp_f32 v185,v185\nM v[32:35],a[80:83],v[192:195],v[32:35]\nv_exp_f32 v186,v186\nM v[36:39],a[80:83],v[196:199],v[36:39]\nv_exp_f32 v187,v187\nM v[96:99],a[80:83],v[200:203],v[96:99]\n" \
"buffer_load_dwordx4 v246,s[40:43],s69 offen offset:3072 lds\nM v[100:103],a[80:83],v[204:207],v[100:103]\ns_lshl4_add_u32 s69,s10,s68\ns_add_u32 s69,s69,0x10000\nM v[40:43],a[84:87],v[192:195],v[40:43]\ns_add_u32 m0,s64,16384\nds_read_b128 a[112:115],v250 offset:12288\nM v[44:47],a[84:87],v[196:199],v[44:47]\n" \
"v_exp_f32 v172,v172\nM v[104:107],a[84:87],v[200:203],v[104:107]\nbuffer_load_dwordx4 v243,s[40:43],s69 offen offset:0 lds\nM v[108:111],a[84:87],v[204:207],v[108:111]\nv_exp_f32 v173,v173\nM v[48:51],a[88:91],v[192:195],v[48:51]\nv_exp_f32 v174,v174\nM v[52:55],a[88:91],v[196:199],v[52:55]\n" \
"v_exp_f32 v175,v175\nM v[112:115],a[88:91],v[200:203],v[112:115]\nbuffer_store_dwordx4 a[248:251],v253,s[52:55],s78 offen offset:0 sc1\nbuffer_load_dwordx4 v244,s[40:43],s69 offen offset:1024 lds\nM v[116:119],a[88:91],v[204:207],v[116:119]\nv_cvt_pk_bf16_f32 v217,v170,v171\ns_waitcnt lgkmcnt(0)\nM v[56:59],a[92:95],v[192:195],v[56:59]\n" \
"v_exp_f32 v188,v188\nM v[60:63],a[92:95],v[196:199],v[60:63]\nbuffer_store_dwordx4 a[248:251],v253,s[52:55],s78 offen offset:1024 sc1\nM v[120:123],a[92:95],v[200:203],v[120:123]\nbuffer_load_dwordx4 v245,s[40:43],s69 offen offset:2048 lds\nM v[124:127],a[92:95],v[204:207],v[124:127]\nv_exp_f32 v189,v189\nM a[192:195],a[208:211],v[192:195],a[192:195]\n" \
"buffer_store_dwordx4 a[248:251],v253,s[52:55],s78 offen offset:2048 sc1\nM a[196:199],a[208:211],v[196:199],a[196:199]\nv_exp_f32 v190,v190\nM a[200:203],a[208:211],v[200:203],a[200:203]\nv_exp_f32 v191,v191\nv_cvt_pk_bf16_f32 v218,v184,v185\nv_cvt_pk_bf16_f32 v219,v186,v187\nbuffer_load_dwordx4 v246,s[40:43],s69 offen offset:3072 lds\n" \
"M a[204:207],a[208:211],v[204:207],a[204:207]\nv_cvt_pk_bf16_f32 v220,v172,v173\nv_cvt_pk_bf16_f32 v221,v174,v175\nv_cvt_pk_bf16_f32 v222,v188,v189\nv_cvt_pk_bf16_f32 v223,v190,v191\nds_read_b128 a[116:119],v250 offset:13312\nbuffer_store_dwordx4 a[248:251],v253,s[52:55],s78 offen offset:3072 sc1\nM v[64:67],a[96:99],v[216:219],v[64:67]\n" \
"ds_read_b128 a[120:123],v250 offset:14336\ns_add_u32 s70,s78,16384\nM v[68:71],a[96:99],v[220:223],v[68:71]\nds_read_b128 a[124:127],v250 offset:15360\nv_cvt_pk_bf16_f32 v208,v160,v161\nM v[72:75],a[100:103],v[216:219],v[72:75]\nbuffer_store_dwordx4 a[248:251],v253,s[52:55],s70 offen offset:0 sc1\nM v[76:79],a[100:103],v[220:223],v[76:79]\n" \
"v_cvt_pk_bf16_f32 v209,v162,v163\nv_cvt_pk_bf16_f32 v210,v176,v177\nM v[80:83],a[104:107],v[216:219],v[80:83]\nv_cvt_pk_bf16_f32 v211,v178,v179\nv_cvt_pk_bf16_f32 v212,v164,v165\nM v[84:87],a[104:107],v[220:223],v[84:87]\nbuffer_store_dwordx4 a[248:251],v253,s[52:55],s70 offen offset:1024 sc1\nM v[88:91],a[108:111],v[216:219],v[88:91]\n" \
"v_cvt_pk_bf16_f32 v213,v166,v167\nv_cvt_pk_bf16_f32 v214,v180,v181\nM v[92:95],a[108:111],v[220:223],v[92:95]\nv_cvt_pk_bf16_f32 v215,v182,v183\nv_add_u32 v226,131072,v242\nM v[96:99],a[112:115],v[216:219],v[96:99]\nbuffer_store_dwordx4 a[248:251],v253,s[52:55],s70 offen offset:2048 sc1\nM v[100:103],a[112:115],v[220:223],v[100:103]\n" \
"ds_read_b64_tr_b16 a[212:213],v226 offset:0\ns_waitcnt lgkmcnt(1)\nM v[104:107],a[116:119],v[216:219],v[104:107]\nds_read_b64_tr_b16 a[214:215],v226 offset:4096\nv_xor_b32 v227,32,v226\nM v[108:111],a[116:119],v[220:223],v[108:111]\nbuffer_store_dwordx4 a[248:251],v253,s[52:55],s70 offen offset:3072 sc1\nM v[112:115],a[120:123],v[216:219],v[112:115]\n" \
"ds_read_b64_tr_b16 a[216:217],v227 offset:0\nds_read_b64_tr_b16 a[218:219],v227 offset:4096\nM v[116:119],a[120:123],v[220:223],v[116:119]\nds_read_b64_tr_b16 a[220:221],v226 offset:512\nds_read_b64_tr_b16 a[222:223],v226 offset:4608\nM v[120:123],a[124:127],v[216:219],v[120:123]\nds_read_b64_tr_b16 a[224:225],v227 offset:512\nds_read_b64_tr_b16 a[226:227],v227 offset:4608\n" \
"ds_read_b64_tr_b16 a[228:229],v226 offset:1024\nM v[124:127],a[124:127],v[220:223],v[124:127]\nds_read_b64_tr_b16 a[230:231],v226 offset:5120\ns_add_u32 s37,s12,s67\ns_add_u32 s37,s37,1048576\nM a[200:203],a[208:211],v[216:219],a[200:203]\nds_read_b64_tr_b16 a[232:233],v227 offset:1024\nds_read_b64_tr_b16 a[234:235],v227 offset:5120\n" \
"ds_read_b64_tr_b16 a[236:237],v226 offset:1536\ns_lshl2_add_u32 s77,s61,s79\nM a[204:207],a[208:211],v[220:223],a[204:207]\nds_read_b64_tr_b16 a[238:239],v226 offset:5632\nds_read_b64_tr_b16 a[240:241],v227 offset:1536\ns_add_u32 s77,s77,4096\nv_accvgpr_read_b32 v232,a200\nM v[0:3],a[96:99],v[208:211],v[0:3]\n" \
"ds_read_b64_tr_b16 a[242:243],v227 offset:5632\nv_and_b32 v251,3,v249\nv_lshlrev_b32 v251,2,v251\nv_accvgpr_read_b32 v236,a204\nv_add_f32 v233,v232,v236\nv_rcp_f32 v232,v232\nv_rcp_f32 v236,v236\nv_add_f32 v233,v233,v232\n" \
"v_add_f32 v233,v233,v236\nv_cmp_ngt_f32 vcc,s28,v233\nM v[4:7],a[96:99],v[212:215],v[4:7]\ns_and_b64 vcc,vcc,s[90:91]\ns_cselect_b32 s39,0x8000000,s16\ns_cselect_b32 s16,s16,0x8000000\ns_cselect_b32 s71,2,1\ns_addc_u32 s99,s99,0\n" \
"v_mov_b32 v233,s71\ns_add_u32 s71,s57,16384\nbuffer_store_dwordx4 a[248:251],v253,s[52:55],s71 offen offset:0 sc1\nbuffer_store_dwordx4 a[248:251],v253,s[52:55],s71 offen offset:1024 sc1\nbuffer_store_dwordx4 a[248:251],v253,s[52:55],s71 offen offset:2048 sc1\nbuffer_store_dwordx4 a[248:251],v253,s[52:55],s71 offen offset:3072 sc1\nM v[8:11],a[100:103],v[208:211],v[8:11]\nv_mul_f32 v64,v64,v232\n" \
"v_mul_f32 v65,v65,v232\nv_mul_f32 v66,v66,v232\nM v[12:15],a[100:103],v[212:215],v[12:15]\nv_mul_f32 v67,v67,v232\nv_cvt_pk_bf16_f32 v64,v64,v65\nv_mul_f32 v72,v72,v232\nv_mul_f32 v73,v73,v232\nM v[16:19],a[104:107],v[208:211],v[16:19]\n" \
"v_mul_f32 v74,v74,v232\nv_mul_f32 v75,v75,v232\nv_cvt_pk_bf16_f32 v65,v66,v67\nM v[20:23],a[104:107],v[212:215],v[20:23]\nv_mul_f32 v68,v68,v236\nv_mul_f32 v69,v69,v236\nv_cvt_pk_bf16_f32 v66,v72,v73\nv_cvt_pk_bf16_f32 v67,v74,v75\n" \
"M v[24:27],a[108:111],v[208:211],v[24:27]\nv_mul_f32 v70,v70,v236\nv_mul_f32 v71,v71,v236\nv_cvt_pk_bf16_f32 v68,v68,v69\nM v[28:31],a[108:111],v[212:215],v[28:31]\nv_mul_f32 v80,v80,v232\nv_mul_f32 v81,v81,v232\nv_mul_f32 v84,v84,v236\n" \
"M v[32:35],a[112:115],v[208:211],v[32:35]\nv_mul_f32 v85,v85,v236\nv_mul_f32 v96,v96,v232\nv_mul_f32 v97,v97,v232\nv_mul_f32 v100,v100,v236\nM v[36:39],a[112:115],v[212:215],v[36:39]\nv_mul_f32 v101,v101,v236\nv_mul_f32 v112,v112,v232\n" \
"v_mul_f32 v113,v113,v232\nM v[40:43],a[116:119],v[208:211],v[40:43]\nv_mul_f32 v116,v116,v236\nv_mul_f32 v117,v117,v236\nv_mul_f32 v76,v76,v236\nv_mul_f32 v77,v77,v236\nM v[44:47],a[116:119],v[212:215],v[44:47]\nv_mul_f32 v78,v78,v236\n" \
"v_mul_f32 v79,v79,v236\nv_cvt_pk_bf16_f32 v69,v70,v71\nM v[48:51],a[120:123],v[208:211],v[48:51]\nv_mul_f32 v82,v82,v232\nv_mul_f32 v83,v83,v232\nv_cvt_pk_bf16_f32 v80,v80,v81\nM v[52:55],a[120:123],v[212:215],v[52:55]\nv_mul_f32 v86,v86,v236\n" \
"v_mul_f32 v87,v87,v236\nv_cvt_pk_bf16_f32 v84,v84,v85\nv_mul_f32 v98,v98,v232\nM v[56:59],a[124:127],v[208:211],v[56:59]\nv_mul_f32 v99,v99,v232\nv_cvt_pk_bf16_f32 v96,v96,v97\nv_mul_f32 v102,v102,v236\nM v[60:63],a[124:127],v[212:215],v[60:63]\n" \
"v_mul_f32 v103,v103,v236\nv_cvt_pk_bf16_f32 v100,v100,v101\nv_mul_f32 v114,v114,v232\nv_mul_f32 v115,v115,v232\nM a[192:195],a[208:211],v[208:211],a[192:195]\nv_cvt_pk_bf16_f32 v112,v112,v113\nv_mul_f32 v118,v118,v236\nv_mul_f32 v119,v119,v236\n" \
"M a[196:199],a[208:211],v[212:215],a[196:199]\nv_cvt_pk_bf16_f32 v116,v116,v117\ns_or_b32 s37,s37,s39\nv_cvt_pk_bf16_f32 v70,v76,v77\nv_cvt_pk_bf16_f32 v71,v78,v79\nv_mul_f32 v88,v88,v232\nv_mul_f32 v89,v89,v232\nv_mul_f32 v90,v90,v232\n" \
"v_mul_f32 v91,v91,v232\nv_cvt_pk_bf16_f32 v81,v82,v83\nv_mul_f32 v92,v92,v236\nv_mul_f32 v93,v93,v236\nv_mul_f32 v94,v94,v236\nv_mul_f32 v95,v95,v236\nv_cvt_pk_bf16_f32 v85,v86,v87\nv_mul_f32 v104,v104,v232\n" \
"v_mul_f32 v105,v105,v232\nv_mul_f32 v106,v106,v232\nv_mul_f32 v107,v107,v232\nv_cvt_pk_bf16_f32 v97,v98,v99\nv_mul_f32 v108,v108,v236\nv_mul_f32 v109,v109,v236\nv_mul_f32 v110,v110,v236\nv_mul_f32 v111,v111,v236\n" \
"v_cvt_pk_bf16_f32 v101,v102,v103\nv_mul_f32 v120,v120,v232\nv_mul_f32 v121,v121,v232\nv_mul_f32 v122,v122,v232\nv_mul_f32 v123,v123,v232\nv_cvt_pk_bf16_f32 v113,v114,v115\nv_mul_f32 v124,v124,v236\nv_mul_f32 v125,v125,v236\n" \
"v_mul_f32 v126,v126,v236\nv_mul_f32 v127,v127,v236\nv_cvt_pk_bf16_f32 v117,v118,v119\ns_waitcnt lgkmcnt(0)\nM v[128:131],v[64:67],a[212:215],0\ns_add_u32 s38,s37,4096\nv_cvt_pk_bf16_f32 v82,v88,v89\nv_cvt_pk_bf16_f32 v83,v90,v91\n" \
"v_cvt_pk_bf16_f32 v86,v92,v93\nv_cvt_pk_bf16_f32 v87,v94,v95\nv_cvt_pk_bf16_f32 v98,v104,v105\nv_cvt_pk_bf16_f32 v99,v106,v107\nv_cvt_pk_bf16_f32 v102,v108,v109\nv_cvt_pk_bf16_f32 v103,v110,v111\nv_cvt_pk_bf16_f32 v114,v120,v121\nv_cvt_pk_bf16_f32 v115,v122,v123\n" \
"v_cvt_pk_bf16_f32 v118,v124,v125\nv_cvt_pk_bf16_f32 v119,v126,v127\nbuffer_store_dwordx4 v[64:67],v240,s[52:55],s37 offen offset:0 sc1\nbuffer_store_dwordx4 v[68:71],v240,s[52:55],s37 offen offset:1024 sc1\nbuffer_store_dwordx4 v[80:83],v240,s[52:55],s37 offen offset:2048 sc1\nbuffer_store_dwordx4 v[84:87],v240,s[52:55],s37 offen offset:3072 sc1\nbuffer_store_dwordx4 v[96:99],v240,s[52:55],s38 offen offset:0 sc1\nbuffer_store_dwordx4 v[100:103],v240,s[52:55],s38 offen offset:1024 sc1\n" \
"buffer_store_dwordx4 v[112:115],v240,s[52:55],s38 offen offset:2048 sc1\nbuffer_store_dwordx4 v[116:119],v240,s[52:55],s38 offen offset:3072 sc1\nM v[132:135],v[68:71],a[212:215],0\nds_read_b64_tr_b16 a[212:213],v226 offset:8192\nds_read_b64_tr_b16 a[214:215],v226 offset:12288\nv_accvgpr_read_b32 v224,a192\nM v[136:139],v[64:67],a[216:219],0\nv_accvgpr_read_b32 v228,a196\n" \
"v_add_f32 v225,v224,v228\nv_rcp_f32 v224,v224\nM v[140:143],v[68:71],a[216:219],0\nds_read_b64_tr_b16 a[216:217],v227 offset:8192\nds_read_b64_tr_b16 a[218:219],v227 offset:12288\nv_mul_f32 v0,v0,v224\nM v[144:147],v[64:67],a[220:223],0\nv_mul_f32 v1,v1,v224\n" \
"v_mul_f32 v2,v2,v224\nv_mul_f32 v3,v3,v224\nM v[148:151],v[68:71],a[220:223],0\nds_read_b64_tr_b16 a[220:221],v226 offset:8704\nds_read_b64_tr_b16 a[222:223],v226 offset:12800\nv_cvt_pk_bf16_f32 v0,v0,v1\nv_mul_f32 v8,v8,v224\nM v[152:155],v[64:67],a[224:227],0\n" \
"v_mul_f32 v9,v9,v224\nv_mul_f32 v10,v10,v224\nv_mul_f32 v11,v11,v224\nM v[156:159],v[68:71],a[224:227],0\nds_read_b64_tr_b16 a[224:225],v227 offset:8704\nds_read_b64_tr_b16 a[226:227],v227 offset:12800\nv_cvt_pk_bf16_f32 v1,v2,v3\ns_add_u32 s37,s12,s67\n" \
"M v[160:163],v[64:67],a[228:231],0\nv_cvt_pk_bf16_f32 v2,v8,v9\nv_cvt_pk_bf16_f32 v3,v10,v11\nv_mul_f32 v16,v16,v224\nM v[164:167],v[68:71],a[228:231],0\nds_read_b64_tr_b16 a[228:229],v226 offset:9216\nds_read_b64_tr_b16 a[230:231],v226 offset:13312\nv_mul_f32 v17,v17,v224\n" \
"M v[168:171],v[64:67],a[232:235],0\nv_rcp_f32 v228,v228\nv_mul_f32 v18,v18,v224\nv_mul_f32 v4,v4,v228\nM v[172:175],v[68:71],a[232:235],0\nds_read_b64_tr_b16 a[232:233],v227 offset:9216\nds_read_b64_tr_b16 a[234:235],v227 offset:13312\nv_mul_f32 v5,v5,v228\n" \
"M v[176:179],v[64:67],a[236:239],0\nv_mul_f32 v6,v6,v228\nv_mul_f32 v7,v7,v228\nv_cvt_pk_bf16_f32 v4,v4,v5\nv_mul_f32 v12,v12,v228\nM v[180:183],v[68:71],a[236:239],0\nds_read_b64_tr_b16 a[236:237],v226 offset:9728\nds_read_b64_tr_b16 a[238:239],v226 offset:13824\n" \
"v_mul_f32 v13,v13,v228\nM v[184:187],v[64:67],a[240:243],0\nbuffer_store_dwordx4 v[0:3],v240,s[52:55],s37 offen offset:0 sc1\nv_mul_f32 v14,v14,v228\nM v[188:191],v[68:71],a[240:243],0\nv_mul_f32 v15,v15,v228\nv_cvt_pk_bf16_f32 v5,v6,v7\nds_read_b64_tr_b16 a[240:241],v227 offset:9728\n" \
"s_waitcnt lgkmcnt(5)\nM v[128:131],v[80:83],a[212:215],v[128:131]\nds_read_b64_tr_b16 a[242:243],v227 offset:13824\nv_cvt_pk_bf16_f32 v6,v12,v13\nv_cvt_pk_bf16_f32 v7,v14,v15\nM v[132:135],v[84:87],a[212:215],v[132:135]\nds_read_b64_tr_b16 a[212:213],v226 offset:16384\nds_read_b64_tr_b16 a[214:215],v226 offset:20480\n" \
"v_mul_f32 v19,v19,v224\nv_cvt_pk_bf16_f32 v16,v16,v17\nM v[136:139],v[80:83],a[216:219],v[136:139]\nv_mul_f32 v24,v24,v224\nv_mul_f32 v25,v25,v224\nv_mul_f32 v26,v26,v224\nM v[140:143],v[84:87],a[216:219],v[140:143]\nds_read_b64_tr_b16 a[216:217],v227 offset:16384\n" \
"ds_read_b64_tr_b16 a[218:219],v227 offset:20480\nv_mul_f32 v27,v27,v224\nM v[144:147],v[80:83],a[220:223],v[144:147]\nbuffer_store_dwordx4 v[4:7],v240,s[52:55],s37 offen offset:1024 sc1\nv_cvt_pk_bf16_f32 v17,v18,v19\nv_cvt_pk_bf16_f32 v18,v24,v25\nM v[148:151],v[84:87],a[220:223],v[148:151]\nds_read_b64_tr_b16 a[220:221],v226 offset:16896\n" \
"ds_read_b64_tr_b16 a[222:223],v226 offset:20992\nv_cvt_pk_bf16_f32 v19,v26,v27\nM v[152:155],v[80:83],a[224:227],v[152:155]\ns_lshl4_add_u32 s69,s11,s68\ns_add_u32 m0,s64,32768\nv_mul_f32 v20,v20,v228\nv_mul_f32 v21,v21,v228\nM v[156:159],v[84:87],a[224:227],v[156:159]\n" \
"ds_read_b64_tr_b16 a[224:225],v227 offset:16896\nds_read_b64_tr_b16 a[226:227],v227 offset:20992\nv_mul_f32 v22,v22,v228\nM v[160:163],v[80:83],a[228:231],v[160:163]\nv_mul_f32 v23,v23,v228\nv_cvt_pk_bf16_f32 v20,v20,v21\nv_mul_f32 v28,v28,v228\nM v[164:167],v[84:87],a[228:231],v[164:167]\n" \
"ds_read_b64_tr_b16 a[228:229],v226 offset:17408\nds_read_b64_tr_b16 a[230:231],v226 offset:21504\nv_mul_f32 v29,v29,v228\ns_waitcnt lgkmcnt(6)\nM v[168:171],v[80:83],a[232:235],v[168:171]\nbuffer_store_dwordx4 v[16:19],v240,s[52:55],s37 offen offset:2048 sc1\nv_mul_f32 v30,v30,v228\nM v[172:175],v[84:87],a[232:235],v[172:175]\n" \
"v_mul_f32 v31,v31,v228\nv_cvt_pk_bf16_f32 v21,v22,v23\nds_read_b64_tr_b16 a[232:233],v227 offset:17408\nds_read_b64_tr_b16 a[234:235],v227 offset:21504\nM v[176:179],v[80:83],a[236:239],v[176:179]\nbuffer_load_dwordx4 v243,s[40:43],s69 offen offset:0 lds\nv_cvt_pk_bf16_f32 v22,v28,v29\nM v[180:183],v[84:87],a[236:239],v[180:183]\n" \
"v_cvt_pk_bf16_f32 v23,v30,v31\nds_read_b64_tr_b16 a[236:237],v226 offset:17920\nds_read_b64_tr_b16 a[238:239],v226 offset:22016\nM v[184:187],v[80:83],a[240:243],v[184:187]\nv_mul_f32 v32,v32,v224\nv_mul_f32 v33,v33,v224\nv_mul_f32 v34,v34,v224\nv_mul_f32 v35,v35,v224\n" \
"M v[188:191],v[84:87],a[240:243],v[188:191]\nds_read_b64_tr_b16 a[240:241],v227 offset:17920\nds_read_b64_tr_b16 a[242:243],v227 offset:22016\nv_cvt_pk_bf16_f32 v32,v32,v33\nM v[128:131],v[96:99],a[212:215],v[128:131]\nbuffer_store_dwordx4 v[20:23],v240,s[52:55],s37 offen offset:3072 sc1\nbuffer_load_dwordx4 v244,s[40:43],s69 offen offset:1024 lds\nM v[132:135],v[100:103],a[212:215],v[132:135]\n" \
"v_mul_f32 v40,v40,v224\nv_mul_f32 v41,v41,v224\nv_mul_f32 v42,v42,v224\nM v[136:139],v[96:99],a[216:219],v[136:139]\nv_mul_f32 v43,v43,v224\nv_cvt_pk_bf16_f32 v33,v34,v35\nds_read_b64_tr_b16 a[212:213],v226 offset:24576\nM v[140:143],v[100:103],a[216:219],v[140:143]\n" \
"ds_read_b64_tr_b16 a[214:215],v226 offset:28672\ns_add_u32 s38,s37,4096\nv_cvt_pk_bf16_f32 v34,v40,v41\ns_waitcnt lgkmcnt(4)\nM v[144:147],v[96:99],a[220:223],v[144:147]\nbuffer_load_dwordx4 v245,s[40:43],s69 offen offset:2048 lds\nv_cvt_pk_bf16_f32 v35,v42,v43\nM v[148:151],v[100:103],a[220:223],v[148:151]\n" \
"ds_read_b64_tr_b16 a[216:217],v227 offset:24576\nds_read_b64_tr_b16 a[218:219],v227 offset:28672\nds_read_b64_tr_b16 a[220:221],v226 offset:25088\nv_mul_f32 v36,v36,v228\nM v[152:155],v[96:99],a[224:227],v[152:155]\nbuffer_store_dwordx4 v[32:35],v240,s[52:55],s38 offen offset:0 sc1\nds_read_b64_tr_b16 a[222:223],v226 offset:29184\nM v[156:159],v[100:103],a[224:227],v[156:159]\n" \
"v_mul_f32 v37,v37,v228\nv_mul_f32 v38,v38,v228\nv_mul_f32 v39,v39,v228\nM v[160:163],v[96:99],a[228:231],v[160:163]\ns_waitcnt vmcnt(8)\nbuffer_store_dword v233,off,s[52:55],s77 sc1\nbuffer_load_dwordx4 v246,s[40:43],s69 offen offset:3072 lds\nM v[164:167],v[100:103],a[228:231],v[164:167]\n" \
"v_cvt_pk_bf16_f32 v36,v36,v37\nv_mul_f32 v44,v44,v228\nv_mul_f32 v45,v45,v228\nM v[168:171],v[96:99],a[232:235],v[168:171]\nv_mul_f32 v46,v46,v228\nv_mul_f32 v47,v47,v228\nv_cvt_pk_bf16_f32 v37,v38,v39\ns_lshl4_add_u32 s69,s11,s68\n" \
"M v[172:175],v[100:103],a[232:235],v[172:175]\nds_read_b64_tr_b16 a[224:225],v227 offset:25088\nds_read_b64_tr_b16 a[226:227],v227 offset:29184\nv_cvt_pk_bf16_f32 v38,v44,v45\nM v[176:179],v[96:99],a[236:239],v[176:179]\nv_cvt_pk_bf16_f32 v39,v46,v47\ns_add_u32 s69,s69,0x10000\ns_add_u32 m0,s64,49152\n" \
"buffer_store_dwordx4 v[36:39],v240,s[52:55],s38 offen offset:1024 sc1\ns_lshl2_add_u32 s39,s62,s7\nbuffer_load_dwordx4 v243,s[40:43],s69 offen offset:0 lds\nM v[180:183],v[100:103],a[236:239],v[180:183]\ns_lshl_b32 s39,s39,5\ns_add_u32 s39,s39,s63\nds_read_b64_tr_b16 a[228:229],v226 offset:25600\nds_read_b64_tr_b16 a[230:231],v226 offset:29696\n" \
"s_waitcnt lgkmcnt(4)\nM v[184:187],v[96:99],a[240:243],v[184:187]\ns_lshl2_add_u32 s39,s39,73728\nbuffer_store_dword v254,off,s[52:55],s39 sc1\nM v[188:191],v[100:103],a[240:243],v[188:191]\nds_read_b64_tr_b16 a[232:233],v227 offset:25600\nds_read_b64_tr_b16 a[234:235],v227 offset:29696\nv_mul_f32 v48,v48,v224\n" \
"v_mul_f32 v49,v49,v224\nM v[128:131],v[112:115],a[212:215],v[128:131]\nbuffer_load_dwordx4 v244,s[40:43],s69 offen offset:1024 lds\nv_mul_f32 v50,v50,v224\nM v[132:135],v[116:119],a[212:215],v[132:135]\nv_mul_f32 v51,v51,v224\nv_cvt_pk_bf16_f32 v48,v48,v49\nv_mul_f32 v56,v56,v224\n" \
"v_mul_f32 v57,v57,v224\nv_mul_f32 v58,v58,v224\nv_mul_f32 v59,v59,v224\nv_cvt_pk_bf16_f32 v49,v50,v51\nds_read_b64_tr_b16 a[236:237],v226 offset:26112\nds_read_b64_tr_b16 a[238:239],v226 offset:30208\nM v[136:139],v[112:115],a[216:219],v[136:139]\nv_cvt_pk_bf16_f32 v50,v56,v57\n" \
"v_cvt_pk_bf16_f32 v51,v58,v59\nbuffer_store_dwordx4 v[48:51],v240,s[52:55],s38 offen offset:2048 sc1\nM v[140:143],v[116:119],a[216:219],v[140:143]\nds_read_b64_tr_b16 a[240:241],v227 offset:26112\nds_read_b64_tr_b16 a[242:243],v227 offset:30208\nds_read_b64_tr_b16 a[212:213],v226 offset:0\nM v[144:147],v[112:115],a[220:223],v[144:147]\nbuffer_load_dwordx4 v245,s[40:43],s69 offen offset:2048 lds\n" \
"ds_read_b64_tr_b16 a[214:215],v226 offset:4096\nv_mul_f32 v52,v52,v228\nM v[148:151],v[116:119],a[220:223],v[148:151]\nv_mul_f32 v53,v53,v228\nv_mul_f32 v54,v54,v228\ns_waitcnt lgkmcnt(6)\nM v[152:155],v[112:115],a[224:227],v[152:155]\nv_mul_f32 v55,v55,v228\n" \
"v_cvt_pk_bf16_f32 v52,v52,v53\nv_mul_f32 v60,v60,v228\nM v[156:159],v[116:119],a[224:227],v[156:159]\nv_mul_f32 v61,v61,v228\nv_mul_f32 v62,v62,v228\nv_mul_f32 v63,v63,v228\nv_cvt_pk_bf16_f32 v53,v54,v55\nds_read_b64_tr_b16 a[216:217],v227 offset:0\n" \
"ds_read_b64_tr_b16 a[218:219],v227 offset:4096\nM v[160:163],v[112:115],a[228:231],v[160:163]\nv_cvt_pk_bf16_f32 v54,v60,v61\nv_cvt_pk_bf16_f32 v55,v62,v63\nbuffer_store_dwordx4 v[52:55],v240,s[52:55],s38 offen offset:3072 sc1\nbuffer_load_dwordx4 v246,s[40:43],s69 offen offset:3072 lds\nM v[164:167],v[116:119],a[228:231],v[164:167]\nds_read_b64_tr_b16 a[220:221],v226 offset:512\n" \
"ds_read_b64_tr_b16 a[222:223],v226 offset:4608\nds_read_b64_tr_b16 a[224:225],v227 offset:512\ns_add_u32 s80,s79,4096\nM v[168:171],v[112:115],a[232:235],v[168:171]\nds_read_b64_tr_b16 a[226:227],v227 offset:4608\nds_read_b64_tr_b16 a[228:229],v226 offset:1024\nv_add_f32 v225,v225,v224\nM v[172:175],v[116:119],a[232:235],v[172:175]\n" \
"ds_read_b64_tr_b16 a[230:231],v226 offset:5120\nds_read_b64_tr_b16 a[232:233],v227 offset:1024\ns_waitcnt lgkmcnt(9)\nM v[176:179],v[112:115],a[236:239],v[176:179]\nds_read_b64_tr_b16 a[234:235],v227 offset:5120\nv_add_f32 v225,v225,v228\nv_cmp_ngt_f32 vcc,s28,v225\ns_add_u32 s65,s10,s67\n" \
"M v[180:183],v[116:119],a[236:239],v[180:183]\nds_read_b64_tr_b16 a[236:237],v226 offset:1536\nds_read_b64_tr_b16 a[238:239],v226 offset:5632\ns_add_u32 s65,s65,1048576\nM v[184:187],v[112:115],a[240:243],v[184:187]\nbuffer_load_dword v248,v251,s[52:55],s80 offen sc1\ns_and_b64 vcc,vcc,s[90:91]\ns_addc_u32 s99,s99,0\n" \
"M v[188:191],v[116:119],a[240:243],v[188:191]\nds_read_b64_tr_b16 a[240:241],v227 offset:1536\nds_read_b64_tr_b16 a[242:243],v227 offset:5632\nv_xor_b32 v250,0x8000,v250\nM v[8:11],v[0:3],a[212:215],0\nv_add_u32 v229,s64,v250\ns_lshl2_add_u32 s76,s61,s79\ns_lshr_b32 s70,s60,1\n" \
"M v[12:15],v[4:7],a[212:215],0\nds_read_b64_tr_b16 a[212:213],v226 offset:8192\nds_read_b64_tr_b16 a[214:215],v226 offset:12288\ns_lshl_b32 s70,s70,7\ns_waitcnt lgkmcnt(6)\nM v[24:27],v[0:3],a[216:219],0\nv_and_b32 v228,31,v249\nv_lshl_add_u32 v228,v228,2,s70\n" \
"v_and_b32 v236,15,v249\nM v[28:31],v[4:7],a[216:219],0\nds_read_b64_tr_b16 a[216:217],v227 offset:8192\nds_read_b64_tr_b16 a[218:219],v227 offset:12288\ns_lshl2_add_u32 s74,s63,s7\ns_lshl_b32 s72,s62,7\nM v[40:43],v[0:3],a[220:223],0\nv_lshrrev_b32 v235,4,v249\n" \
"v_lshlrev_b32 v236,1,v236\ns_add_u32 s95,s74,s72\nM v[44:47],v[4:7],a[220:223],0\nds_read_b64_tr_b16 a[220:221],v226 offset:8704\nds_read_b64_tr_b16 a[222:223],v226 offset:12800\nv_lshl_or_b32 v235,v235,8,v236\nM v[56:59],v[0:3],a[224:227],0\ns_lshl_b32 s95,s95,10\n" \
"v_add_u32 v236,s95,v235\ns_sub_u32 s74,s74,s72\ns_add_u32 s74,s74,1920\nM v[60:63],v[4:7],a[224:227],0\nds_read_b64_tr_b16 a[224:225],v227 offset:8704\nds_read_b64_tr_b16 a[226:227],v227 offset:12800\ns_lshl_b32 s74,s74,10\nM v[72:75],v[0:3],a[228:231],0\n" \
"v_add_u32 v237,s74,v235\ns_lshl_b32 s72,s7,9\nv_lshl_add_u32 v239,v249,3,s72\ns_mov_b32 s97,4096\nM v[76:79],v[4:7],a[228:231],0\nds_read_b64_tr_b16 a[228:229],v226 offset:9216\nds_read_b64_tr_b16 a[230:231],v226 offset:13312\ns_add_u32 s38,s57,16384\n" \
"M v[88:91],v[0:3],a[232:235],0\nv_lshlrev_b32 v238,1,v240\nv_bfe_u32 v234,v249,2,2\ns_add_u32 s95,s95,54525952\nM v[92:95],v[4:7],a[232:235],0\nds_read_b64_tr_b16 a[232:233],v227 offset:9216\nds_read_b64_tr_b16 a[234:235],v227 offset:13312\ns_add_u32 s74,s74,54525952\n" \
"s_waitcnt lgkmcnt(6)\nM v[104:107],v[0:3],a[236:239],0\nM v[108:111],v[4:7],a[236:239],0\nds_read_b64_tr_b16 a[236:237],v226 offset:9728\nds_read_b64_tr_b16 a[238:239],v226 offset:13824\nM v[120:123],v[0:3],a[240:243],0\nM v[124:127],v[4:7],a[240:243],0\nds_read_b64_tr_b16 a[240:241],v227 offset:9728\n" \
"ds_read_b64_tr_b16 a[242:243],v227 offset:13824\nM v[8:11],v[16:19],a[212:215],v[8:11]\nM v[12:15],v[20:23],a[212:215],v[12:15]\nds_read_b64_tr_b16 a[212:213],v226 offset:16384\nds_read_b64_tr_b16 a[214:215],v226 offset:20480\nM v[24:27],v[16:19],a[216:219],v[24:27]\nM v[28:31],v[20:23],a[216:219],v[28:31]\nds_read_b64_tr_b16 a[216:217],v227 offset:16384\n" \
"ds_read_b64_tr_b16 a[218:219],v227 offset:20480\nM v[40:43],v[16:19],a[220:223],v[40:43]\nM v[44:47],v[20:23],a[220:223],v[44:47]\nds_read_b64_tr_b16 a[220:221],v226 offset:16896\nds_read_b64_tr_b16 a[222:223],v226 offset:20992\ns_waitcnt lgkmcnt(10)\nM v[56:59],v[16:19],a[224:227],v[56:59]\ns_waitcnt vmcnt(0)\n" \
"v_and_b32 v248,s17,v248\nv_cmp_eq_u32 vcc,0,v248\nM v[60:63],v[20:23],a[224:227],v[60:63]\ns_cbranch_vccnz .Lps9\n.Lpb9:\nds_read_b64_tr_b16 a[224:225],v227 offset:16896\nds_read_b64_tr_b16 a[226:227],v227 offset:20992\nv_cmp_eq_u32 vcc,2,v248\n" \
"M v[72:75],v[16:19],a[228:231],v[72:75]\nbuffer_load_dwordx4 a[0:3],v240,s[52:55],s65 offen offset:0 sc1\ns_lshl_b32 s80,s62,9\nM v[76:79],v[20:23],a[228:231],v[76:79]\nds_read_b64_tr_b16 a[228:229],v226 offset:17408\nds_read_b64_tr_b16 a[230:231],v226 offset:21504\ns_add_u32 s80,s80,73728\nM v[88:91],v[16:19],a[232:235],v[88:91]\n" \
"s_cmp_lg_u64 vcc,0\ns_addc_u32 s99,s99,0\nM v[92:95],v[20:23],a[232:235],v[92:95]\nbuffer_load_dwordx4 a[4:7],v240,s[52:55],s65 offen offset:1024 sc1\ns_waitcnt lgkmcnt(4)\nM v[104:107],v[16:19],a[236:239],v[104:107]\nds_read_b64_tr_b16 a[232:233],v227 offset:17408\nds_read_b64_tr_b16 a[234:235],v227 offset:21504\n" \
"M v[108:111],v[20:23],a[236:239],v[108:111]\nds_read_b64_tr_b16 a[236:237],v226 offset:17920\nds_read_b64_tr_b16 a[238:239],v226 offset:22016\nM v[120:123],v[16:19],a[240:243],v[120:123]\nbuffer_load_dwordx4 a[8:11],v240,s[52:55],s65 offen offset:2048 sc1\nM v[124:127],v[20:23],a[240:243],v[124:127]\nv_mov_b32 v230,s99\nds_read_b64_tr_b16 a[240:241],v227 offset:17920\n" \
"M v[8:11],v[32:35],a[212:215],v[8:11]\nds_write_b32 v229,v230\nds_read_b64_tr_b16 a[242:243],v227 offset:22016\nM v[12:15],v[36:39],a[212:215],v[12:15]\nbuffer_load_dwordx4 a[12:15],v240,s[52:55],s65 offen offset:3072 sc1\nM v[24:27],v[32:35],a[216:219],v[24:27]\ns_add_u32 s65,s65,4096\nds_read_b64_tr_b16 a[212:213],v226 offset:24576\n" \
"M v[28:31],v[36:39],a[216:219],v[28:31]\nds_read_b64_tr_b16 a[214:215],v226 offset:28672\nds_read_b64_tr_b16 a[216:217],v227 offset:24576\nM v[40:43],v[32:35],a[220:223],v[40:43]\nbuffer_load_dwordx4 a[16:19],v240,s[52:55],s65 offen offset:0 sc1\nM v[44:47],v[36:39],a[220:223],v[44:47]\nds_read_b64_tr_b16 a[218:219],v227 offset:28672\ns_waitcnt lgkmcnt(7)\n" \
"M v[56:59],v[32:35],a[224:227],v[56:59]\nds_read_b64_tr_b16 a[220:221],v226 offset:25088\nds_read_b64_tr_b16 a[222:223],v226 offset:29184\nM v[60:63],v[36:39],a[224:227],v[60:63]\nbuffer_load_dwordx4 a[20:23],v240,s[52:55],s65 offen offset:1024 sc1\nM v[72:75],v[32:35],a[228:231],v[72:75]\nds_read_b64_tr_b16 a[224:225],v227 offset:25088\nds_read_b64_tr_b16 a[226:227],v227 offset:29184\n" \
"M v[76:79],v[36:39],a[228:231],v[76:79]\nv_lshl_add_u32 v229,v251,10,v250\nds_read_b64_tr_b16 a[228:229],v226 offset:25600\nM v[88:91],v[32:35],a[232:235],v[88:91]\nbuffer_load_dwordx4 a[24:27],v240,s[52:55],s65 offen offset:2048 sc1\nM v[92:95],v[36:39],a[232:235],v[92:95]\nds_read_b64_tr_b16 a[230:231],v226 offset:29696\nds_read_b64_tr_b16 a[232:233],v227 offset:25600\n" \
"M v[104:107],v[32:35],a[236:239],v[104:107]\nds_read_b64_tr_b16 a[234:235],v227 offset:29696\nM v[108:111],v[36:39],a[236:239],v[108:111]\nbuffer_load_dwordx4 a[28:31],v240,s[52:55],s65 offen offset:3072 sc1\ns_waitcnt lgkmcnt(7)\nM v[120:123],v[32:35],a[240:243],v[120:123]\ns_barrier\nds_read_b32 v230,v229\n" \
"M v[124:127],v[36:39],a[240:243],v[124:127]\ns_add_u32 s65,s11,s67\ns_add_u32 s65,s65,1048576\nM v[8:11],v[48:51],a[212:215],v[8:11]\nbuffer_load_dwordx4 a[32:35],v240,s[52:55],s65 offen offset:0 sc1\nM v[12:15],v[52:55],a[212:215],v[12:15]\nds_read_b64_tr_b16 a[236:237],v226 offset:26112\nds_read_b64_tr_b16 a[238:239],v226 offset:30208\n" \
"M v[24:27],v[48:51],a[216:219],v[24:27]\nds_read_b64_tr_b16 a[240:241],v227 offset:26112\nds_read_b64_tr_b16 a[242:243],v227 offset:30208\nM v[28:31],v[52:55],a[216:219],v[28:31]\nbuffer_load_dwordx4 a[36:39],v240,s[52:55],s65 offen offset:1024 sc1\ns_waitcnt lgkmcnt(5)\nM v[40:43],v[48:51],a[220:223],v[40:43]\nv_add_u32 v226,0,v242\n" \
"ds_read_b64_tr_b16 a[212:213],v226 offset:0\nM v[44:47],v[52:55],a[220:223],v[44:47]\nds_read_b64_tr_b16 a[214:215],v226 offset:4096\nv_xor_b32 v227,32,v226\nM v[56:59],v[48:51],a[224:227],v[56:59]\nbuffer_load_dwordx4 a[40:43],v240,s[52:55],s65 offen offset:2048 sc1\nM v[60:63],v[52:55],a[224:227],v[60:63]\ns_waitcnt lgkmcnt(6)\n" \
"v_cmp_ne_u32 vcc,0,v230\nds_read_b64_tr_b16 a[216:217],v227 offset:0\nds_read_b64_tr_b16 a[218:219],v227 offset:4096\nM v[72:75],v[48:51],a[228:231],v[72:75]\ns_cbranch_vccnz .Lredo\nbuffer_store_dword v254,off,s[52:55],s76 sc1\nM v[76:79],v[52:55],a[228:231],v[76:79]\nbuffer_load_dwordx4 a[44:47],v240,s[52:55],s65 offen offset:3072 sc1\n" \
"ds_read_b64_tr_b16 a[220:221],v226 offset:512\nds_read_b64_tr_b16 a[222:223],v226 offset:4608\nM v[88:91],v[48:51],a[232:235],v[88:91]\nds_read_b64_tr_b16 a[224:225],v227 offset:512\nds_read_b64_tr_b16 a[226:227],v227 offset:4608\nM v[92:95],v[52:55],a[232:235],v[92:95]\ns_add_u32 s65,s65,4096\ns_waitcnt lgkmcnt(6)\n" \
"M v[104:107],v[48:51],a[236:239],v[104:107]\nbuffer_load_dwordx4 a[48:51],v240,s[52:55],s65 offen offset:0 sc1\nM v[108:111],v[52:55],a[236:239],v[108:111]\nds_read_b64_tr_b16 a[228:229],v226 offset:1024\nds_read_b64_tr_b16 a[230:231],v226 offset:5120\nM v[120:123],v[48:51],a[240:243],v[120:123]\nds_read_b64_tr_b16 a[232:233],v227 offset:1024\nds_read_b64_tr_b16 a[234:235],v227 offset:5120\n" \
"M v[124:127],v[52:55],a[240:243],v[124:127]\nbuffer_load_dwordx4 a[52:55],v240,s[52:55],s65 offen offset:1024 sc1\ns_waitcnt vmcnt(14)\nM v[128:131],a[0:3],a[212:215],v[128:131]\ns_lshl4_add_u32 s69,s14,s68\ns_add_u32 m0,s64,65536\ns_waitcnt vmcnt(13)\nM v[132:135],a[4:7],a[212:215],v[132:135]\n" \
"buffer_load_dwordx4 v243,s[40:43],s69 offen offset:0 lds\nds_read_b64_tr_b16 a[236:237],v226 offset:1536\nds_read_b64_tr_b16 a[238:239],v226 offset:5632\ns_waitcnt lgkmcnt(6)\nM v[136:139],a[0:3],a[216:219],v[136:139]\nbuffer_load_dwordx4 a[56:59],v240,s[52:55],s65 offen offset:2048 sc1\nM v[140:143],a[4:7],a[216:219],v[140:143]\nds_read_b64_tr_b16 a[240:241],v227 offset:1536\n" \
"ds_read_b64_tr_b16 a[242:243],v227 offset:5632\nM v[144:147],a[0:3],a[220:223],v[144:147]\nbuffer_load_dwordx4 v244,s[40:43],s69 offen offset:1024 lds\nM v[148:151],a[4:7],a[220:223],v[148:151]\nbuffer_load_dwordx4 a[60:63],v240,s[52:55],s65 offen offset:3072 sc1\nds_read_b64_tr_b16 a[212:213],v226 offset:8192\nds_read_b64_tr_b16 a[214:215],v226 offset:12288\nM v[152:155],a[0:3],a[224:227],v[152:155]\n" \
"s_add_u32 s65,s14,s67\nds_read_b64_tr_b16 a[216:217],v227 offset:8192\nM v[156:159],a[4:7],a[224:227],v[156:159]\nds_read_b64_tr_b16 a[218:219],v227 offset:12288\ns_waitcnt lgkmcnt(8)\nM v[160:163],a[0:3],a[228:231],v[160:163]\ns_add_u32 s65,s65,1048576\nbuffer_load_dwordx4 v245,s[40:43],s69 offen offset:2048 lds\n" \
"buffer_load_dwordx4 a[160:163],v240,s[52:55],s65 offen offset:0 sc1\nM v[164:167],a[4:7],a[228:231],v[164:167]\nds_read_b64_tr_b16 a[220:221],v226 offset:8704\nds_read_b64_tr_b16 a[222:223],v226 offset:12800\nM v[168:171],a[0:3],a[232:235],v[168:171]\nds_read_b64_tr_b16 a[224:225],v227 offset:8704\nds_read_b64_tr_b16 a[226:227],v227 offset:12800\nM v[172:175],a[4:7],a[232:235],v[172:175]\n" \
"buffer_load_dwordx4 a[164:167],v240,s[52:55],s65 offen offset:1024 sc1\ns_waitcnt lgkmcnt(7)\nM v[176:179],a[0:3],a[236:239],v[176:179]\nbuffer_load_dwordx4 v246,s[40:43],s69 offen offset:3072 lds\nM v[180:183],a[4:7],a[236:239],v[180:183]\nds_read_b64_tr_b16 a[228:229],v226 offset:9216\nds_read_b64_tr_b16 a[230:231],v226 offset:13312\nM v[184:187],a[0:3],a[240:243],v[184:187]\n" \
"buffer_load_dwordx4 a[168:171],v240,s[52:55],s65 offen offset:2048 sc1\nM v[188:191],a[4:7],a[240:243],v[188:191]\ns_lshl4_add_u32 s69,s14,s68\nds_read_b64_tr_b16 a[232:233],v227 offset:9216\nds_read_b64_tr_b16 a[234:235],v227 offset:13312\ns_waitcnt vmcnt(21) lgkmcnt(8)\nM v[128:131],a[8:11],a[212:215],v[128:131]\ns_add_u32 s69,s69,0x10000\n" \
"s_add_u32 m0,s64,98304\ns_waitcnt vmcnt(20)\nM v[132:135],a[12:15],a[212:215],v[132:135]\nbuffer_load_dwordx4 v243,s[40:43],s69 offen offset:0 lds\nbuffer_load_dwordx4 a[172:175],v240,s[52:55],s65 offen offset:3072 sc1\nds_read_b64_tr_b16 a[236:237],v226 offset:9728\nds_read_b64_tr_b16 a[238:239],v226 offset:13824\nM v[136:139],a[8:11],a[216:219],v[136:139]\n" \
"ds_read_b64_tr_b16 a[240:241],v227 offset:9728\nds_read_b64_tr_b16 a[242:243],v227 offset:13824\nM v[140:143],a[12:15],a[216:219],v[140:143]\ns_add_u32 s65,s65,4096\ns_waitcnt lgkmcnt(6)\nM v[144:147],a[8:11],a[220:223],v[144:147]\nbuffer_load_dwordx4 v244,s[40:43],s69 offen offset:1024 lds\nbuffer_load_dwordx4 a[176:179],v240,s[52:55],s65 offen offset:0 sc1\n" \
"M v[148:151],a[12:15],a[220:223],v[148:151]\nds_read_b64_tr_b16 a[212:213],v226 offset:16384\nds_read_b64_tr_b16 a[214:215],v226 offset:20480\nM v[152:155],a[8:11],a[224:227],v[152:155]\nds_read_b64_tr_b16 a[216:217],v227 offset:16384\nds_read_b64_tr_b16 a[218:219],v227 offset:20480\nM v[156:159],a[12:15],a[224:227],v[156:159]\nbuffer_load_dwordx4 a[180:183],v240,s[52:55],s65 offen offset:1024 sc1\n" \
"M v[160:163],a[8:11],a[228:231],v[160:163]\nbuffer_load_dwordx4 v245,s[40:43],s69 offen offset:2048 lds\nM v[164:167],a[12:15],a[228:231],v[164:167]\nds_read_b64_tr_b16 a[220:221],v226 offset:16896\nds_read_b64_tr_b16 a[222:223],v226 offset:20992\ns_waitcnt lgkmcnt(6)\nM v[168:171],a[8:11],a[232:235],v[168:171]\nbuffer_load_dwordx4 a[184:187],v240,s[52:55],s65 offen offset:2048 sc1\n" \
"M v[172:175],a[12:15],a[232:235],v[172:175]\nds_read_b64_tr_b16 a[224:225],v227 offset:16896\nds_read_b64_tr_b16 a[226:227],v227 offset:20992\nM v[176:179],a[8:11],a[236:239],v[176:179]\nbuffer_load_dwordx4 v246,s[40:43],s69 offen offset:3072 lds\nM v[180:183],a[12:15],a[236:239],v[180:183]\nbuffer_load_dwordx4 a[188:191],v240,s[52:55],s65 offen offset:3072 sc1\nds_read_b64_tr_b16 a[228:229],v226 offset:17408\n" \
"ds_read_b64_tr_b16 a[230:231],v226 offset:21504\nM v[184:187],a[8:11],a[240:243],v[184:187]\nds_read_b64_tr_b16 a[232:233],v227 offset:17408\nds_read_b64_tr_b16 a[234:235],v227 offset:21504\nM v[188:191],a[12:15],a[240:243],v[188:191]\nds_read_b64_tr_b16 a[236:237],v226 offset:17920\ns_waitcnt vmcnt(26) lgkmcnt(9)\nM v[128:131],a[16:19],a[212:215],v[128:131]\n" \
"ds_read_b64_tr_b16 a[238:239],v226 offset:22016\nds_read_b64_tr_b16 a[240:241],v227 offset:17920\nM v[132:135],a[20:23],a[212:215],v[132:135]\nds_read_b64_tr_b16 a[242:243],v227 offset:22016\nds_read_b64_tr_b16 a[212:213],v226 offset:24576\nM v[136:139],a[16:19],a[216:219],v[136:139]\nds_read_b64_tr_b16 a[214:215],v226 offset:28672\nv_and_b32 v229,32,v249\n" \
"M v[140:143],a[20:23],a[216:219],v[140:143]\nv_mul_u32_u24 v229,12,v229\ns_waitcnt lgkmcnt(10)\nM v[144:147],a[16:19],a[220:223],v[144:147]\nds_read_b64_tr_b16 a[216:217],v227 offset:24576\nds_read_b64_tr_b16 a[218:219],v227 offset:28672\nM v[148:151],a[20:23],a[220:223],v[148:151]\nv_xor_b32 v228,v228,v229\n" \
"ds_read_b64_tr_b16 a[220:221],v226 offset:25088\nM v[152:155],a[16:19],a[224:227],v[152:155]\nds_read_b64_tr_b16 a[222:223],v226 offset:29184\nv_mov_b32 v250,81920\nM v[156:159],a[20:23],a[224:227],v[156:159]\nds_read_b64_tr_b16 a[224:225],v227 offset:25088\ns_waitcnt lgkmcnt(9)\nM v[160:163],a[16:19],a[228:231],v[160:163]\n" \
"buffer_load_dword v248,v228,s[52:55],s80 offen sc1\nM v[164:167],a[20:23],a[228:231],v[164:167]\nds_read_b64_tr_b16 a[226:227],v227 offset:29184\nds_read_b64_tr_b16 a[228:229],v226 offset:25600\nM v[168:171],a[16:19],a[232:235],v[168:171]\nds_read_b64_tr_b16 a[230:231],v226 offset:29696\ns_add_u32 s65,s10,s67\nM v[172:175],a[20:23],a[232:235],v[172:175]\n" \
"ds_read_b64_tr_b16 a[232:233],v227 offset:25600\nds_read_b64_tr_b16 a[234:235],v227 offset:29696\nM v[176:179],a[16:19],a[236:239],v[176:179]\nv_accvgpr_write_b32 a194,0\nv_accvgpr_write_b32 a195,0\nM v[180:183],a[20:23],a[236:239],v[180:183]\nds_read_b64_tr_b16 a[236:237],v226 offset:26112\ns_waitcnt lgkmcnt(7)\n" \
"M v[184:187],a[16:19],a[240:243],v[184:187]\ns_barrier\nds_read_b64_tr_b16 a[238:239],v226 offset:30208\nM v[188:191],a[20:23],a[240:243],v[188:191]\nds_read_b64_tr_b16 a[240:241],v227 offset:26112\nds_read_b64_tr_b16 a[242:243],v227 offset:30208\nM v[128:131],a[24:27],a[212:215],v[128:131]\nds_write_b64 v250,a[244:245]\n" \
"s_waitcnt vmcnt(23)\nM v[132:135],a[28:31],a[212:215],v[132:135]\nds_read_b64_tr_b16 a[212:213],v226 offset:32768\nds_read_b64_tr_b16 a[214:215],v226 offset:36864\nM v[136:139],a[24:27],a[216:219],v[136:139]\nv_accvgpr_write_b32 a196,0\nv_accvgpr_write_b32 a197,0\nM v[140:143],a[28:31],a[216:219],v[140:143]\n" \
"ds_read_b64_tr_b16 a[216:217],v227 offset:32768\nds_read_b64_tr_b16 a[218:219],v227 offset:36864\nM v[144:147],a[24:27],a[220:223],v[144:147]\ns_load_dwordx2 s[44:45],s[4:5],0x68\ns_mov_b32 s46,2097152\nM v[148:151],a[28:31],a[220:223],v[148:151]\nds_read_b64_tr_b16 a[220:221],v226 offset:33280\ns_waitcnt lgkmcnt(9)\n" \
"M v[152:155],a[24:27],a[224:227],v[152:155]\nds_read_b64_tr_b16 a[222:223],v226 offset:37376\nM v[156:159],a[28:31],a[224:227],v[156:159]\nds_read_b64_tr_b16 a[224:225],v227 offset:33280\nds_read_b64_tr_b16 a[226:227],v227 offset:37376\nM v[160:163],a[24:27],a[228:231],v[160:163]\nM v[164:167],a[28:31],a[228:231],v[164:167]\nds_read_b64_tr_b16 a[228:229],v226 offset:33792\n" \
"ds_read_b64_tr_b16 a[230:231],v226 offset:37888\nM v[168:171],a[24:27],a[232:235],v[168:171]\nM v[172:175],a[28:31],a[232:235],v[172:175]\nds_read_b64_tr_b16 a[232:233],v227 offset:33792\ns_waitcnt lgkmcnt(9)\nM v[176:179],a[24:27],a[236:239],v[176:179]\nds_read_b64_tr_b16 a[234:235],v227 offset:37888\nM v[180:183],a[28:31],a[236:239],v[180:183]\n" \
"ds_read_b64_tr_b16 a[236:237],v226 offset:34304\nds_read_b64_tr_b16 a[238:239],v226 offset:38400\nM v[184:187],a[24:27],a[240:243],v[184:187]\nM v[188:191],a[28:31],a[240:243],v[188:191]\nds_read_b64_tr_b16 a[240:241],v227 offset:34304\nds_read_b64_tr_b16 a[242:243],v227 offset:38400\nM v[128:131],a[32:35],a[212:215],v[128:131]\nM v[132:135],a[36:39],a[212:215],v[132:135]\n" \
"ds_read_b64_tr_b16 a[212:213],v226 offset:40960\ns_waitcnt lgkmcnt(9)\nM v[136:139],a[32:35],a[216:219],v[136:139]\nds_read_b64_tr_b16 a[214:215],v226 offset:45056\nM v[140:143],a[36:39],a[216:219],v[140:143]\nds_read_b64_tr_b16 a[216:217],v227 offset:40960\nds_read_b64_tr_b16 a[218:219],v227 offset:45056\nM v[144:147],a[32:35],a[220:223],v[144:147]\n" \
"M v[148:151],a[36:39],a[220:223],v[148:151]\nglobal_load_ushort v64,v236,s[8:9] offset:0\nM v[152:155],a[32:35],a[224:227],v[152:155]\nds_read_b64_tr_b16 a[220:221],v226 offset:41472\nds_read_b64_tr_b16 a[222:223],v226 offset:45568\nM v[156:159],a[36:39],a[224:227],v[156:159]\nglobal_load_ushort v80,v237,s[8:9] offset:0\ns_waitcnt lgkmcnt(7)\n" \
"M v[160:163],a[32:35],a[228:231],v[160:163]\nds_read_b64_tr_b16 a[224:225],v227 offset:41472\nds_read_b64_tr_b16 a[226:227],v227 offset:45568\nM v[164:167],a[36:39],a[228:231],v[164:167]\nglobal_load_ushort v96,v235,s[20:21] offset:0\nM v[168:171],a[32:35],a[232:235],v[168:171]\nds_read_b64_tr_b16 a[228:229],v226 offset:41984\nds_read_b64_tr_b16 a[230:231],v226 offset:46080\n" \
"M v[172:175],a[36:39],a[232:235],v[172:175]\nglobal_load_ushort v65,v236,s[8:9] offset:32\nM v[176:179],a[32:35],a[236:239],v[176:179]\nds_read_b64_tr_b16 a[232:233],v227 offset:41984\nds_read_b64_tr_b16 a[234:235],v227 offset:46080\nM v[180:183],a[36:39],a[236:239],v[180:183]\nglobal_load_ushort v81,v237,s[8:9] offset:32\ns_waitcnt lgkmcnt(6)\n" \
"M v[184:187],a[32:35],a[240:243],v[184:187]\nds_read_b64_tr_b16 a[236:237],v226 offset:42496\nds_read_b64_tr_b16 a[238:239],v226 offset:46592\nM v[188:191],a[36:39],a[240:243],v[188:191]\nglobal_load_ushort v97,v235,s[20:21] offset:32\nM v[128:131],a[40:43],a[212:215],v[128:131]\ns_waitcnt vmcnt(6)\nv_cmp_eq_u32 vcc,0,v248\n" \
"M v[132:135],a[44:47],a[212:215],v[132:135]\nglobal_load_ushort v66,v236,s[8:9] offset:64\nM v[136:139],a[40:43],a[216:219],v[136:139]\nds_read_b64_tr_b16 a[240:241],v227 offset:42496\nds_read_b64_tr_b16 a[242:243],v227 offset:46592\nM v[140:143],a[44:47],a[216:219],v[140:143]\nglobal_load_ushort v82,v237,s[8:9] offset:64\nM v[144:147],a[40:43],a[220:223],v[144:147]\n" \
"s_cbranch_vccnz .Lps10\n.Lpb10:\nbuffer_load_dword v248,v251,s[52:55],s79 offen sc1\nM v[148:151],a[44:47],a[220:223],v[148:151]\nds_read_b64_tr_b16 a[212:213],v226 offset:49152\nds_read_b64_tr_b16 a[214:215],v226 offset:53248\nglobal_load_ushort v98,v235,s[20:21] offset:64\ns_waitcnt lgkmcnt(4)\n" \
"M v[152:155],a[40:43],a[224:227],v[152:155]\nds_read_b64_tr_b16 a[216:217],v227 offset:49152\nds_read_b64_tr_b16 a[218:219],v227 offset:53248\nM v[156:159],a[44:47],a[224:227],v[156:159]\nglobal_load_ushort v67,v236,s[8:9] offset:96\nM v[160:163],a[40:43],a[228:231],v[160:163]\nds_read_b64_tr_b16 a[220:221],v226 offset:49664\nds_read_b64_tr_b16 a[222:223],v226 offset:53760\n" \
"M v[164:167],a[44:47],a[228:231],v[164:167]\nglobal_load_ushort v83,v237,s[8:9] offset:96\nM v[168:171],a[40:43],a[232:235],v[168:171]\nds_read_b64_tr_b16 a[224:225],v227 offset:49664\nds_read_b64_tr_b16 a[226:227],v227 offset:53760\nM v[172:175],a[44:47],a[232:235],v[172:175]\nglobal_load_ushort v99,v235,s[20:21] offset:96\nM v[176:179],a[40:43],a[236:239],v[176:179]\n" \
"ds_read_b64_tr_b16 a[228:229],v226 offset:50176\nds_read_b64_tr_b16 a[230:231],v226 offset:54272\nM v[180:183],a[44:47],a[236:239],v[180:183]\nglobal_load_ushort v68,v236,s[8:9] offset:128\ns_waitcnt lgkmcnt(6)\nM v[184:187],a[40:43],a[240:243],v[184:187]\nds_read_b64_tr_b16 a[232:233],v227 offset:50176\nds_read_b64_tr_b16 a[234:235],v227 offset:54272\n" \
"M v[188:191],a[44:47],a[240:243],v[188:191]\nglobal_load_ushort v84,v237,s[8:9] offset:128\nM v[128:131],a[48:51],a[212:215],v[128:131]\nds_read_b64_tr_b16 a[236:237],v226 offset:50688\nds_read_b64_tr_b16 a[238:239],v226 offset:54784\nM v[132:135],a[52:55],a[212:215],v[132:135]\nglobal_load_ushort v100,v235,s[20:21] offset:128\nM v[136:139],a[48:51],a[216:219],v[136:139]\n" \
"ds_read_b64_tr_b16 a[240:241],v227 offset:50688\nds_read_b64_tr_b16 a[242:243],v227 offset:54784\nM v[140:143],a[52:55],a[216:219],v[140:143]\nglobal_load_ushort v69,v236,s[8:9] offset:160\ns_waitcnt lgkmcnt(6)\nM v[144:147],a[48:51],a[220:223],v[144:147]\nds_read_b64_tr_b16 a[212:213],v226 offset:57344\nds_read_b64_tr_b16 a[214:215],v226 offset:61440\n" \
"M v[148:151],a[52:55],a[220:223],v[148:151]\nglobal_load_ushort v85,v237,s[8:9] offset:160\nM v[152:155],a[48:51],a[224:227],v[152:155]\nds_read_b64_tr_b16 a[216:217],v227 offset:57344\nds_read_b64_tr_b16 a[218:219],v227 offset:61440\nM v[156:159],a[52:55],a[224:227],v[156:159]\nglobal_load_ushort v101,v235,s[20:21] offset:160\nM v[160:163],a[48:51],a[228:231],v[160:163]\n" \
"ds_read_b64_tr_b16 a[220:221],v226 offset:57856\nds_read_b64_tr_b16 a[222:223],v226 offset:61952\nM v[164:167],a[52:55],a[228:231],v[164:167]\nglobal_load_ushort v70,v236,s[8:9] offset:192\ns_waitcnt lgkmcnt(6)\nM v[168:171],a[48:51],a[232:235],v[168:171]\nds_read_b64_tr_b16 a[224:225],v227 offset:57856\nds_read_b64_tr_b16 a[226:227],v227 offset:61952\n" \
"M v[172:175],a[52:55],a[232:235],v[172:175]\nglobal_load_ushort v86,v237,s[8:9] offset:192\nM v[176:179],a[48:51],a[236:239],v[176:179]\nds_read_b64_tr_b16 a[228:229],v226 offset:58368\nds_read_b64_tr_b16 a[230:231],v226 offset:62464\nM v[180:183],a[52:55],a[236:239],v[180:183]\nglobal_load_ushort v102,v235,s[20:21] offset:192\nM v[184:187],a[48:51],a[240:243],v[184:187]\n" \
"s_barrier\nds_read_b64_tr_b16 a[232:233],v227 offset:58368\nM v[188:191],a[52:55],a[240:243],v[188:191]\nds_read_b64_tr_b16 a[234:235],v227 offset:62464\nglobal_load_ushort v71,v236,s[8:9] offset:224\ns_waitcnt lgkmcnt(6)\nM v[128:131],a[56:59],a[212:215],v[128:131]\nds_read_b64_tr_b16 a[236:237],v226 offset:58880\n" \
"ds_read_b64_tr_b16 a[238:239],v226 offset:62976\nM v[132:135],a[60:63],a[212:215],v[132:135]\nglobal_load_ushort v87,v237,s[8:9] offset:224\nM v[136:139],a[56:59],a[216:219],v[136:139]\nds_read_b64_tr_b16 a[240:241],v227 offset:58880\nds_read_b64_tr_b16 a[242:243],v227 offset:62976\nM v[140:143],a[60:63],a[216:219],v[140:143]\nglobal_load_ushort v103,v235,s[20:21] offset:224\n" \
"M v[144:147],a[56:59],a[220:223],v[144:147]\nv_add_u32 v226,65536,v242\nds_read_b64_tr_b16 a[212:213],v226 offset:0\nM v[148:151],a[60:63],a[220:223],v[148:151]\nds_read_b64_tr_b16 a[214:215],v226 offset:4096\nbuffer_load_dwordx2 v[112:113],v239,s[52:55],0 offen offset:0 sc1\ns_waitcnt lgkmcnt(6)\nM v[152:155],a[56:59],a[224:227],v[152:155]\n" \
"v_xor_b32 v227,32,v226\ns_waitcnt vmcnt(17)\nv_cmp_eq_u32 vcc,0,v248\nM v[156:159],a[60:63],a[224:227],v[156:159]\nds_read_b64_tr_b16 a[216:217],v227 offset:0\nds_read_b64_tr_b16 a[218:219],v227 offset:4096\nbuffer_load_dwordx2 v[114:115],v239,s[52:55],0 offen offset:2048 sc1\nM v[160:163],a[56:59],a[228:231],v[160:163]\n" \
"s_cbranch_vccnz .Lps11\n.Lpb11:\nbuffer_load_dwordx4 a[64:67],v240,s[52:55],s65 offen offset:0 sc1\nM v[164:167],a[60:63],a[228:231],v[164:167]\nds_read_b64_tr_b16 a[220:221],v226 offset:512\nds_read_b64_tr_b16 a[222:223],v226 offset:4608\nbuffer_load_dwordx2 v[116:117],v239,s[52:55],s97 offen offset:0 sc1\nM v[168:171],a[56:59],a[232:235],v[168:171]\n" \
"ds_read_b64_tr_b16 a[224:225],v227 offset:512\nds_read_b64_tr_b16 a[226:227],v227 offset:4608\nM v[172:175],a[60:63],a[232:235],v[172:175]\nbuffer_load_dwordx2 v[118:119],v239,s[52:55],s97 offen offset:2048 sc1\ns_waitcnt lgkmcnt(6)\nM v[176:179],a[56:59],a[236:239],v[176:179]\nbuffer_load_dwordx4 a[68:71],v240,s[52:55],s65 offen offset:1024 sc1\nM v[180:183],a[60:63],a[236:239],v[180:183]\n" \
"ds_read_b64_tr_b16 a[228:229],v226 offset:1024\nds_read_b64_tr_b16 a[230:231],v226 offset:5120\nM v[184:187],a[56:59],a[240:243],v[184:187]\nds_read_b64_tr_b16 a[232:233],v227 offset:1024\nds_read_b64_tr_b16 a[234:235],v227 offset:5120\nM v[188:191],a[60:63],a[240:243],v[188:191]\nds_read_b64_tr_b16 a[236:237],v226 offset:1536\nds_read_b64_tr_b16 a[238:239],v226 offset:5632\n" \
"M v[128:131],a[160:163],a[212:215],v[128:131]\nbuffer_load_dwordx4 a[72:75],v240,s[52:55],s65 offen offset:2048 sc1\nM v[132:135],a[164:167],a[212:215],v[132:135]\nds_read_b64_tr_b16 a[240:241],v227 offset:1536\ns_waitcnt lgkmcnt(7)\nM v[136:139],a[160:163],a[216:219],v[136:139]\nds_read_b64_tr_b16 a[242:243],v227 offset:5632\nds_read_b64_tr_b16 a[212:213],v226 offset:8192\n" \
"M v[140:143],a[164:167],a[216:219],v[140:143]\nds_read_b64_tr_b16 a[214:215],v226 offset:12288\nds_read_b64_tr_b16 a[216:217],v227 offset:8192\nM v[144:147],a[160:163],a[220:223],v[144:147]\nbuffer_load_dwordx4 a[76:79],v240,s[52:55],s65 offen offset:3072 sc1\nM v[148:151],a[164:167],a[220:223],v[148:151]\nds_read_b64_tr_b16 a[218:219],v227 offset:12288\ns_add_u32 s65,s65,4096\n" \
"M v[152:155],a[160:163],a[224:227],v[152:155]\nds_read_b64_tr_b16 a[220:221],v226 offset:8704\nds_read_b64_tr_b16 a[222:223],v226 offset:12800\nM v[156:159],a[164:167],a[224:227],v[156:159]\nds_read_b64_tr_b16 a[224:225],v227 offset:8704\ns_waitcnt lgkmcnt(9)\nM v[160:163],a[160:163],a[228:231],v[160:163]\nbuffer_load_dwordx4 a[80:83],v240,s[52:55],s65 offen offset:0 sc1\n" \
"M v[164:167],a[164:167],a[228:231],v[164:167]\nds_read_b64_tr_b16 a[226:227],v227 offset:12800\nds_read_b64_tr_b16 a[228:229],v226 offset:9216\nM v[168:171],a[160:163],a[232:235],v[168:171]\nds_read_b64_tr_b16 a[230:231],v226 offset:13312\nv_lshrrev_b32 v235,4,v249\nM v[172:175],a[164:167],a[232:235],v[172:175]\nds_read_b64_tr_b16 a[232:233],v227 offset:9216\n" \
"ds_read_b64_tr_b16 a[234:235],v227 offset:13312\nM v[176:179],a[160:163],a[236:239],v[176:179]\nbuffer_load_dwordx4 a[84:87],v240,s[52:55],s65 offen offset:1024 sc1\nM v[180:183],a[164:167],a[236:239],v[180:183]\nds_read_b64_tr_b16 a[236:237],v226 offset:9728\ns_waitcnt lgkmcnt(8)\nM v[184:187],a[160:163],a[240:243],v[184:187]\nds_read_b64_tr_b16 a[238:239],v226 offset:13824\n" \
"v_cmp_eq_u32 vcc,v234,v235\nM v[188:191],a[164:167],a[240:243],v[188:191]\nds_read_b64_tr_b16 a[240:241],v227 offset:9728\nds_read_b64_tr_b16 a[242:243],v227 offset:13824\nM v[128:131],a[168:171],a[212:215],v[128:131]\nbuffer_load_dwordx4 a[88:91],v240,s[52:55],s65 offen offset:2048 sc1\nM v[132:135],a[172:175],a[212:215],v[132:135]\nds_read_b64_tr_b16 a[212:213],v226 offset:32768\n" \
"ds_read_b64_tr_b16 a[214:215],v226 offset:36864\nM v[136:139],a[168:171],a[216:219],v[136:139]\nv_and_b32 v234,1,v249\nv_lshlrev_b32 v234,4,v234\nM v[140:143],a[172:175],a[216:219],v[140:143]\nds_read_b64_tr_b16 a[216:217],v227 offset:32768\ns_waitcnt lgkmcnt(7)\nM v[144:147],a[168:171],a[220:223],v[144:147]\n" \
"buffer_load_dwordx4 a[92:95],v240,s[52:55],s65 offen offset:3072 sc1\nM v[148:151],a[172:175],a[220:223],v[148:151]\nds_read_b64_tr_b16 a[218:219],v227 offset:36864\ns_add_u32 s65,s11,s67\nM v[152:155],a[168:171],a[224:227],v[152:155]\nds_read_b64_tr_b16 a[220:221],v226 offset:33280\nds_read_b64_tr_b16 a[222:223],v226 offset:37376\nM v[156:159],a[172:175],a[224:227],v[156:159]\n" \
"ds_read_b64_tr_b16 a[224:225],v227 offset:33280\nds_read_b64_tr_b16 a[226:227],v227 offset:37376\nM v[160:163],a[168:171],a[228:231],v[160:163]\nbuffer_load_dwordx4 a[96:99],v240,s[52:55],s65 offen offset:0 sc1\nM v[164:167],a[172:175],a[228:231],v[164:167]\nds_read_b64_tr_b16 a[228:229],v226 offset:33792\nds_read_b64_tr_b16 a[230:231],v226 offset:37888\nM v[168:171],a[168:171],a[232:235],v[168:171]\n" \
"v_mov_b32 v235,0x3f80\nv_lshlrev_b32 v234,v234,v235\nM v[172:175],a[172:175],a[232:235],v[172:175]\nds_read_b64_tr_b16 a[232:233],v227 offset:33792\ns_waitcnt lgkmcnt(8)\nM v[176:179],a[168:171],a[236:239],v[176:179]\nbuffer_load_dwordx4 a[100:103],v240,s[52:55],s65 offen offset:1024 sc1\nM v[180:183],a[172:175],a[236:239],v[180:183]\n" \
"ds_read_b64_tr_b16 a[234:235],v227 offset:37888\nds_read_b64_tr_b16 a[236:237],v226 offset:34304\nM v[184:187],a[168:171],a[240:243],v[184:187]\nds_read_b64_tr_b16 a[238:239],v226 offset:38400\nv_cndmask_b32 v234,0,v234,vcc\nM v[188:191],a[172:175],a[240:243],v[188:191]\nds_read_b64_tr_b16 a[240:241],v227 offset:34304\nds_read_b64_tr_b16 a[242:243],v227 offset:38400\n" \
"M v[128:131],a[176:179],a[212:215],v[128:131]\nbuffer_load_dwordx4 a[104:107],v240,s[52:55],s65 offen offset:2048 sc1\nM v[132:135],a[180:183],a[212:215],v[132:135]\nds_read_b64_tr_b16 a[212:213],v226 offset:40960\ns_waitcnt lgkmcnt(7)\nM v[136:139],a[176:179],a[216:219],v[136:139]\nds_read_b64_tr_b16 a[214:215],v226 offset:45056\nv_and_b32 v235,2,v249\n" \
"M v[140:143],a[180:183],a[216:219],v[140:143]\nds_read_b64_tr_b16 a[216:217],v227 offset:40960\nds_read_b64_tr_b16 a[218:219],v227 offset:45056\nM v[144:147],a[176:179],a[220:223],v[144:147]\nbuffer_load_dwordx4 a[108:111],v240,s[52:55],s65 offen offset:3072 sc1\nM v[148:151],a[180:183],a[220:223],v[148:151]\ns_add_u32 s65,s65,4096\nds_read_b64_tr_b16 a[220:221],v226 offset:41472\n" \
"M v[152:155],a[176:179],a[224:227],v[152:155]\nds_read_b64_tr_b16 a[222:223],v226 offset:45568\nv_cmp_eq_u32 vcc,0,v235\nM v[156:159],a[180:183],a[224:227],v[156:159]\nds_read_b64_tr_b16 a[224:225],v227 offset:41472\nds_read_b64_tr_b16 a[226:227],v227 offset:45568\nM v[160:163],a[176:179],a[228:231],v[160:163]\nbuffer_load_dwordx4 a[112:115],v240,s[52:55],s65 offen offset:0 sc1\n" \
"M v[164:167],a[180:183],a[228:231],v[164:167]\nds_read_b64_tr_b16 a[228:229],v226 offset:41984\ns_waitcnt lgkmcnt(7)\nM v[168:171],a[176:179],a[232:235],v[168:171]\nds_read_b64_tr_b16 a[230:231],v226 offset:46080\nv_cndmask_b32 v235,0,v234,vcc\nM v[172:175],a[180:183],a[232:235],v[172:175]\nds_read_b64_tr_b16 a[232:233],v227 offset:41984\n" \
"ds_read_b64_tr_b16 a[234:235],v227 offset:46080\nM v[176:179],a[176:179],a[236:239],v[176:179]\nbuffer_load_dwordx4 a[116:119],v240,s[52:55],s65 offen offset:1024 sc1\nM v[180:183],a[180:183],a[236:239],v[180:183]\nds_read_b64_tr_b16 a[236:237],v226 offset:42496\nds_read_b64_tr_b16 a[238:239],v226 offset:46592\nM v[184:187],a[176:179],a[240:243],v[184:187]\nv_add_u32 v226,0,v242\n" \
"v_xor_b32 v234,v234,v235\nM v[188:191],a[180:183],a[240:243],v[188:191]\nds_read_b64_tr_b16 a[240:241],v227 offset:42496\nds_read_b64_tr_b16 a[242:243],v227 offset:46592\nM v[128:131],a[184:187],a[212:215],v[128:131]\nbuffer_load_dwordx4 a[120:123],v240,s[52:55],s65 offen offset:2048 sc1\nM v[132:135],a[188:191],a[212:215],v[132:135]\nds_read_b64_tr_b16 a[212:213],v226 offset:0\n" \
"s_waitcnt lgkmcnt(7)\nM v[136:139],a[184:187],a[216:219],v[136:139]\nds_read_b64_tr_b16 a[214:215],v226 offset:4096\nv_xor_b32 v227,32,v226\nM v[140:143],a[188:191],a[216:219],v[140:143]\nds_read_b64_tr_b16 a[216:217],v227 offset:0\nds_read_b64_tr_b16 a[218:219],v227 offset:4096\nM v[144:147],a[184:187],a[220:223],v[144:147]\n" \
"buffer_load_dwordx4 a[124:127],v240,s[52:55],s65 offen offset:3072 sc1\nM v[148:151],a[188:191],a[220:223],v[148:151]\ns_add_u32 s65,s14,s67\nds_read_b64_tr_b16 a[220:221],v226 offset:512\nM v[152:155],a[184:187],a[224:227],v[152:155]\nds_read_b64_tr_b16 a[222:223],v226 offset:4608\nv_cvt_pk_bf16_f32 v192,v128,v136\nM v[156:159],a[188:191],a[224:227],v[156:159]\n" \
"ds_read_b64_tr_b16 a[224:225],v227 offset:512\nds_read_b64_tr_b16 a[226:227],v227 offset:4608\nM v[160:163],a[184:187],a[228:231],v[160:163]\nbuffer_load_dwordx4 a[128:131],v240,s[52:55],s65 offen offset:0 sc1\nM v[164:167],a[188:191],a[228:231],v[164:167]\nds_read_b64_tr_b16 a[228:229],v226 offset:1024\ns_waitcnt lgkmcnt(7)\nM v[168:171],a[184:187],a[232:235],v[168:171]\n" \
"ds_read_b64_tr_b16 a[230:231],v226 offset:5120\nv_cvt_pk_bf16_f32 v193,v144,v152\nM v[172:175],a[188:191],a[232:235],v[172:175]\nds_read_b64_tr_b16 a[232:233],v227 offset:1024\nds_read_b64_tr_b16 a[234:235],v227 offset:5120\nM v[176:179],a[184:187],a[236:239],v[176:179]\nbuffer_load_dwordx4 a[132:135],v240,s[52:55],s65 offen offset:1024 sc1\nM v[180:183],a[188:191],a[236:239],v[180:183]\n" \
"ds_read_b64_tr_b16 a[236:237],v226 offset:1536\nds_read_b64_tr_b16 a[238:239],v226 offset:5632\nM v[184:187],a[184:187],a[240:243],v[184:187]\nv_cvt_pk_bf16_f32 v194,v160,v168\nv_min_u32 v192,-2,v192\nM v[188:191],a[188:191],a[240:243],v[188:191]\nds_read_b64_tr_b16 a[240:241],v227 offset:1536\ns_waitcnt vmcnt(15)\n" \
"M v[8:11],a[64:67],a[212:215],v[8:11]\nbuffer_load_dwordx4 a[136:139],v240,s[52:55],s65 offen offset:2048 sc1\nM v[12:15],a[68:71],a[212:215],v[12:15]\nds_read_b64_tr_b16 a[242:243],v227 offset:5632\ns_waitcnt lgkmcnt(6)\nM v[24:27],a[64:67],a[216:219],v[24:27]\nds_read_b64_tr_b16 a[212:213],v226 offset:8192\nds_read_b64_tr_b16 a[214:215],v226 offset:12288\n" \
"M v[28:31],a[68:71],a[216:219],v[28:31]\nds_read_b64_tr_b16 a[216:217],v227 offset:8192\nds_read_b64_tr_b16 a[218:219],v227 offset:12288\nM v[40:43],a[64:67],a[220:223],v[40:43]\nbuffer_load_dwordx4 a[140:143],v240,s[52:55],s65 offen offset:3072 sc1\nM v[44:47],a[68:71],a[220:223],v[44:47]\ns_add_u32 s65,s65,4096\nds_read_b64_tr_b16 a[220:221],v226 offset:8704\n" \
"M v[56:59],a[64:67],a[224:227],v[56:59]\nds_read_b64_tr_b16 a[222:223],v226 offset:12800\nv_cvt_pk_bf16_f32 v195,v176,v184\nM v[60:63],a[68:71],a[224:227],v[60:63]\nds_read_b64_tr_b16 a[224:225],v227 offset:8704\nds_read_b64_tr_b16 a[226:227],v227 offset:12800\nM v[72:75],a[64:67],a[228:231],v[72:75]\nbuffer_load_dwordx4 a[144:147],v240,s[52:55],s65 offen offset:0 sc1\n" \
"M v[76:79],a[68:71],a[228:231],v[76:79]\nds_read_b64_tr_b16 a[228:229],v226 offset:9216\ns_waitcnt lgkmcnt(8)\nM v[88:91],a[64:67],a[232:235],v[88:91]\nds_read_b64_tr_b16 a[230:231],v226 offset:13312\nv_min_u32 v193,-2,v193\nM v[92:95],a[68:71],a[232:235],v[92:95]\nv_min_u32 v194,-2,v194\n" \
"v_min_u32 v195,-2,v195\nM v[104:107],a[64:67],a[236:239],v[104:107]\nbuffer_load_dwordx4 a[148:151],v240,s[52:55],s65 offen offset:1024 sc1\nM v[108:111],a[68:71],a[236:239],v[108:111]\nds_read_b64_tr_b16 a[232:233],v227 offset:9216\nds_read_b64_tr_b16 a[234:235],v227 offset:13312\nM v[120:123],a[64:67],a[240:243],v[120:123]\nbuffer_store_dwordx4 v[192:195],v253,s[52:55],s57 offen offset:0 sc1\n" \
"M v[124:127],a[68:71],a[240:243],v[124:127]\nds_read_b64_tr_b16 a[236:237],v226 offset:9728\ns_waitcnt lgkmcnt(5)\nM v[8:11],a[72:75],a[212:215],v[8:11]\nbuffer_load_dwordx4 a[152:155],v240,s[52:55],s65 offen offset:2048 sc1\ns_waitcnt vmcnt(16)\nM v[12:15],a[76:79],a[212:215],v[12:15]\nds_read_b64_tr_b16 a[238:239],v226 offset:13824\n" \
"ds_read_b64_tr_b16 a[240:241],v227 offset:9728\nM v[24:27],a[72:75],a[216:219],v[24:27]\nds_read_b64_tr_b16 a[242:243],v227 offset:13824\nds_read_b64_tr_b16 a[212:213],v226 offset:16384\nM v[28:31],a[76:79],a[216:219],v[28:31]\nds_read_b64_tr_b16 a[214:215],v226 offset:20480\nds_read_b64_tr_b16 a[216:217],v227 offset:16384\nM v[40:43],a[72:75],a[220:223],v[40:43]\n" \
"buffer_load_dwordx4 a[156:159],v240,s[52:55],s65 offen offset:3072 sc1\nM v[44:47],a[76:79],a[220:223],v[44:47]\nds_read_b64_tr_b16 a[218:219],v227 offset:20480\nv_cvt_pk_bf16_f32 v196,v129,v137\nM v[56:59],a[72:75],a[224:227],v[56:59]\nv_cvt_pk_bf16_f32 v197,v145,v153\nv_cvt_pk_bf16_f32 v198,v161,v169\nM v[60:63],a[76:79],a[224:227],v[60:63]\n" \
"v_cvt_pk_bf16_f32 v199,v177,v185\ns_waitcnt lgkmcnt(7)\nM v[72:75],a[72:75],a[228:231],v[72:75]\nds_read_b64_tr_b16 a[220:221],v226 offset:16896\nds_read_b64_tr_b16 a[222:223],v226 offset:20992\nM v[76:79],a[76:79],a[228:231],v[76:79]\nv_min_u32 v196,-2,v196\nv_min_u32 v197,-2,v197\n" \
"M v[88:91],a[72:75],a[232:235],v[88:91]\nv_min_u32 v198,-2,v198\nv_min_u32 v199,-2,v199\nbuffer_store_dwordx4 v[196:199],v253,s[52:55],s57 offen offset:1024 sc1\nM v[92:95],a[76:79],a[232:235],v[92:95]\nds_read_b64_tr_b16 a[224:225],v227 offset:16896\nds_read_b64_tr_b16 a[226:227],v227 offset:20992\ns_waitcnt lgkmcnt(5)\n" \
"M v[104:107],a[72:75],a[236:239],v[104:107]\nds_read_b64_tr_b16 a[228:229],v226 offset:17408\nds_read_b64_tr_b16 a[230:231],v226 offset:21504\nM v[108:111],a[76:79],a[236:239],v[108:111]\nds_read_b64_tr_b16 a[232:233],v227 offset:17408\nds_read_b64_tr_b16 a[234:235],v227 offset:21504\nM v[120:123],a[72:75],a[240:243],v[120:123]\nds_read_b64_tr_b16 a[236:237],v226 offset:17920\n" \
"ds_read_b64_tr_b16 a[238:239],v226 offset:22016\nM v[124:127],a[76:79],a[240:243],v[124:127]\nds_read_b64_tr_b16 a[240:241],v227 offset:17920\nds_read_b64_tr_b16 a[242:243],v227 offset:22016\nM v[8:11],a[80:83],a[212:215],v[8:11]\nv_cvt_pk_bf16_f32 v200,v130,v138\nv_cvt_pk_bf16_f32 v201,v146,v154\nM v[12:15],a[84:87],a[212:215],v[12:15]\n" \
"v_cvt_pk_bf16_f32 v202,v162,v170\ns_waitcnt lgkmcnt(10)\nM v[24:27],a[80:83],a[216:219],v[24:27]\nv_cvt_pk_bf16_f32 v203,v178,v186\nds_read_b64_tr_b16 a[212:213],v226 offset:24576\nM v[28:31],a[84:87],a[216:219],v[28:31]\nds_read_b64_tr_b16 a[214:215],v226 offset:28672\nv_min_u32 v200,-2,v200\n" \
"M v[40:43],a[80:83],a[220:223],v[40:43]\nv_min_u32 v201,-2,v201\nv_min_u32 v202,-2,v202\nM v[44:47],a[84:87],a[220:223],v[44:47]\nv_min_u32 v203,-2,v203\ns_waitcnt lgkmcnt(5)\nM v[56:59],a[80:83],a[224:227],v[56:59]\nbuffer_store_dwordx4 v[200:203],v253,s[52:55],s57 offen offset:2048 sc1\n" \
"M v[60:63],a[84:87],a[224:227],v[60:63]\nds_read_b64_tr_b16 a[216:217],v227 offset:24576\nds_read_b64_tr_b16 a[218:219],v227 offset:28672\nM v[72:75],a[80:83],a[228:231],v[72:75]\nds_read_b64_tr_b16 a[220:221],v226 offset:25088\nds_read_b64_tr_b16 a[222:223],v226 offset:29184\nM v[76:79],a[84:87],a[228:231],v[76:79]\nds_read_b64_tr_b16 a[224:225],v227 offset:25088\n" \
"ds_read_b64_tr_b16 a[226:227],v227 offset:29184\nM v[88:91],a[80:83],a[232:235],v[88:91]\nds_read_b64_tr_b16 a[228:229],v226 offset:25600\nds_read_b64_tr_b16 a[230:231],v226 offset:29696\nM v[92:95],a[84:87],a[232:235],v[92:95]\nds_read_b64_tr_b16 a[232:233],v227 offset:25600\ns_waitcnt lgkmcnt(9)\nM v[104:107],a[80:83],a[236:239],v[104:107]\n" \
"ds_read_b64_tr_b16 a[234:235],v227 offset:29696\nv_cvt_pk_bf16_f32 v204,v131,v139\nM v[108:111],a[84:87],a[236:239],v[108:111]\nv_cvt_pk_bf16_f32 v205,v147,v155\nv_cvt_pk_bf16_f32 v206,v163,v171\nM v[120:123],a[80:83],a[240:243],v[120:123]\nv_cvt_pk_bf16_f32 v207,v179,v187\nds_read_b64_tr_b16 a[236:237],v226 offset:26112\n" \
"M v[124:127],a[84:87],a[240:243],v[124:127]\nds_read_b64_tr_b16 a[238:239],v226 offset:30208\nv_min_u32 v204,-2,v204\nM v[8:11],a[88:91],a[212:215],v[8:11]\nv_min_u32 v205,-2,v205\nv_min_u32 v206,-2,v206\nM v[12:15],a[92:95],a[212:215],v[12:15]\nv_min_u32 v207,-2,v207\n" \
"s_waitcnt lgkmcnt(5)\nM v[24:27],a[88:91],a[216:219],v[24:27]\nbuffer_store_dwordx4 v[204:207],v253,s[52:55],s57 offen offset:3072 sc1\nM v[28:31],a[92:95],a[216:219],v[28:31]\nds_read_b64_tr_b16 a[240:241],v227 offset:26112\nds_read_b64_tr_b16 a[242:243],v227 offset:30208\nM v[40:43],a[88:91],a[220:223],v[40:43]\nds_read_b64_tr_b16 a[212:213],v226 offset:32768\n" \
"ds_read_b64_tr_b16 a[214:215],v226 offset:36864\nM v[44:47],a[92:95],a[220:223],v[44:47]\nds_read_b64_tr_b16 a[216:217],v227 offset:32768\nds_read_b64_tr_b16 a[218:219],v227 offset:36864\nM v[56:59],a[88:91],a[224:227],v[56:59]\nds_read_b64_tr_b16 a[220:221],v226 offset:33280\nds_read_b64_tr_b16 a[222:223],v226 offset:37376\nM v[60:63],a[92:95],a[224:227],v[60:63]\n" \
"ds_read_b64_tr_b16 a[224:225],v227 offset:33280\ns_waitcnt lgkmcnt(9)\nM v[72:75],a[88:91],a[228:231],v[72:75]\nds_read_b64_tr_b16 a[226:227],v227 offset:37376\nv_cvt_pk_bf16_f32 v208,v132,v140\nM v[76:79],a[92:95],a[228:231],v[76:79]\nv_cvt_pk_bf16_f32 v209,v148,v156\nv_cvt_pk_bf16_f32 v210,v164,v172\n" \
"M v[88:91],a[88:91],a[232:235],v[88:91]\nv_cvt_pk_bf16_f32 v211,v180,v188\nds_read_b64_tr_b16 a[228:229],v226 offset:33792\nM v[92:95],a[92:95],a[232:235],v[92:95]\nds_read_b64_tr_b16 a[230:231],v226 offset:37888\nv_min_u32 v208,-2,v208\nM v[104:107],a[88:91],a[236:239],v[104:107]\nv_min_u32 v209,-2,v209\n" \
"v_min_u32 v210,-2,v210\nM v[108:111],a[92:95],a[236:239],v[108:111]\nv_min_u32 v211,-2,v211\ns_waitcnt lgkmcnt(5)\nM v[120:123],a[88:91],a[240:243],v[120:123]\nbuffer_store_dwordx4 v[208:211],v253,s[52:55],s38 offen offset:0 sc1\nM v[124:127],a[92:95],a[240:243],v[124:127]\nds_read_b64_tr_b16 a[232:233],v227 offset:33792\n" \
"ds_read_b64_tr_b16 a[234:235],v227 offset:37888\ns_waitcnt vmcnt(9)\nM v[8:11],a[96:99],a[212:215],v[8:11]\nds_read_b64_tr_b16 a[236:237],v226 offset:34304\nds_read_b64_tr_b16 a[238:239],v226 offset:38400\nM v[12:15],a[100:103],a[212:215],v[12:15]\nds_read_b64_tr_b16 a[240:241],v227 offset:34304\nds_read_b64_tr_b16 a[242:243],v227 offset:38400\n" \
"M v[24:27],a[96:99],a[216:219],v[24:27]\nds_read_b64_tr_b16 a[212:213],v226 offset:40960\nds_read_b64_tr_b16 a[214:215],v226 offset:45056\nM v[28:31],a[100:103],a[216:219],v[28:31]\nds_read_b64_tr_b16 a[216:217],v227 offset:40960\ns_waitcnt lgkmcnt(9)\nM v[40:43],a[96:99],a[220:223],v[40:43]\nds_read_b64_tr_b16 a[218:219],v227 offset:45056\n" \
"v_cvt_pk_bf16_f32 v212,v133,v141\nM v[44:47],a[100:103],a[220:223],v[44:47]\nv_cvt_pk_bf16_f32 v213,v149,v157\nv_cvt_pk_bf16_f32 v214,v165,v173\nM v[56:59],a[96:99],a[224:227],v[56:59]\nbuffer_load_dwordx4 v[0:3],v238,s[52:55],0 offen offset:0 sc1\nM v[60:63],a[100:103],a[224:227],v[60:63]\nv_cvt_pk_bf16_f32 v215,v181,v189\n" \
"ds_read_b64_tr_b16 a[220:221],v226 offset:41472\nM v[72:75],a[96:99],a[228:231],v[72:75]\nds_read_b64_tr_b16 a[222:223],v226 offset:45568\nv_min_u32 v212,-2,v212\nM v[76:79],a[100:103],a[228:231],v[76:79]\nv_min_u32 v213,-2,v213\ns_waitcnt lgkmcnt(5)\nM v[88:91],a[96:99],a[232:235],v[88:91]\n" \
"v_min_u32 v214,-2,v214\nv_min_u32 v215,-2,v215\nbuffer_store_dwordx4 v[212:215],v253,s[52:55],s38 offen offset:1024 sc1\nM v[92:95],a[100:103],a[232:235],v[92:95]\nds_read_b64_tr_b16 a[224:225],v227 offset:41472\nds_read_b64_tr_b16 a[226:227],v227 offset:45568\nM v[104:107],a[96:99],a[236:239],v[104:107]\nbuffer_load_dwordx4 v[4:7],v238,s[52:55],0 offen offset:16 sc1\n" \
"M v[108:111],a[100:103],a[236:239],v[108:111]\nds_read_b64_tr_b16 a[228:229],v226 offset:41984\nds_read_b64_tr_b16 a[230:231],v226 offset:46080\nM v[120:123],a[96:99],a[240:243],v[120:123]\nds_read_b64_tr_b16 a[232:233],v227 offset:41984\nds_read_b64_tr_b16 a[234:235],v227 offset:46080\nM v[124:127],a[100:103],a[240:243],v[124:127]\nds_read_b64_tr_b16 a[236:237],v226 offset:42496\n" \
"s_waitcnt lgkmcnt(7)\nM v[8:11],a[104:107],a[212:215],v[8:11]\nds_read_b64_tr_b16 a[238:239],v226 offset:46592\nds_read_b64_tr_b16 a[240:241],v227 offset:42496\nM v[12:15],a[108:111],a[212:215],v[12:15]\nds_read_b64_tr_b16 a[242:243],v227 offset:46592\nv_cvt_pk_bf16_f32 v216,v134,v142\nM v[24:27],a[104:107],a[216:219],v[24:27]\n" \
"buffer_load_dwordx4 v[16:19],v238,s[52:55],0 offen offset:2048 sc1\nM v[28:31],a[108:111],a[216:219],v[28:31]\nv_cvt_pk_bf16_f32 v217,v150,v158\nv_cvt_pk_bf16_f32 v218,v166,v174\nM v[40:43],a[104:107],a[220:223],v[40:43]\nv_cvt_pk_bf16_f32 v219,v182,v190\nds_read_b64_tr_b16 a[212:213],v226 offset:49152\nM v[44:47],a[108:111],a[220:223],v[44:47]\n" \
"ds_read_b64_tr_b16 a[214:215],v226 offset:53248\ns_waitcnt lgkmcnt(7)\nM v[56:59],a[104:107],a[224:227],v[56:59]\nv_min_u32 v216,-2,v216\nv_min_u32 v217,-2,v217\nv_min_u32 v218,-2,v218\nv_min_u32 v219,-2,v219\nbuffer_store_dwordx4 v[216:219],v253,s[52:55],s38 offen offset:2048 sc1\n" \
"M v[60:63],a[108:111],a[224:227],v[60:63]\nds_read_b64_tr_b16 a[216:217],v227 offset:49152\nds_read_b64_tr_b16 a[218:219],v227 offset:53248\nM v[72:75],a[104:107],a[228:231],v[72:75]\nbuffer_load_dwordx4 v[20:23],v238,s[52:55],0 offen offset:2064 sc1\nM v[76:79],a[108:111],a[228:231],v[76:79]\nds_read_b64_tr_b16 a[220:221],v226 offset:49664\nds_read_b64_tr_b16 a[222:223],v226 offset:53760\n" \
"s_waitcnt lgkmcnt(6)\nM v[88:91],a[104:107],a[232:235],v[88:91]\nds_read_b64_tr_b16 a[224:225],v227 offset:49664\nds_read_b64_tr_b16 a[226:227],v227 offset:53760\nM v[92:95],a[108:111],a[232:235],v[92:95]\nds_read_b64_tr_b16 a[228:229],v226 offset:50176\nds_read_b64_tr_b16 a[230:231],v226 offset:54272\nM v[104:107],a[104:107],a[236:239],v[104:107]\n" \
"ds_read_b64_tr_b16 a[232:233],v227 offset:50176\nds_read_b64_tr_b16 a[234:235],v227 offset:54272\nM v[108:111],a[108:111],a[236:239],v[108:111]\nv_cvt_pk_bf16_f32 v220,v135,v143\nv_cvt_pk_bf16_f32 v221,v151,v159\nM v[120:123],a[104:107],a[240:243],v[120:123]\nbuffer_load_dwordx4 v[32:35],v238,s[52:55],s97 offen offset:0 sc1\nM v[124:127],a[108:111],a[240:243],v[124:127]\n" \
"v_cvt_pk_bf16_f32 v222,v167,v175\ns_waitcnt lgkmcnt(8)\nM v[8:11],a[112:115],a[212:215],v[8:11]\nv_cvt_pk_bf16_f32 v223,v183,v191\nds_read_b64_tr_b16 a[236:237],v226 offset:50688\nM v[12:15],a[116:119],a[212:215],v[12:15]\nds_read_b64_tr_b16 a[238:239],v226 offset:54784\nv_min_u32 v220,-2,v220\n" \
"M v[24:27],a[112:115],a[216:219],v[24:27]\nv_min_u32 v221,-2,v221\nv_min_u32 v222,-2,v222\nv_min_u32 v223,-2,v223\nbuffer_store_dwordx4 v[220:223],v253,s[52:55],s38 offen offset:3072 sc1\nM v[28:31],a[116:119],a[216:219],v[28:31]\nds_read_b64_tr_b16 a[240:241],v227 offset:50688\nds_read_b64_tr_b16 a[242:243],v227 offset:54784\n" \
"s_waitcnt lgkmcnt(4)\nM v[40:43],a[112:115],a[220:223],v[40:43]\nbuffer_load_dwordx4 v[36:39],v238,s[52:55],s97 offen offset:16 sc1\nM v[44:47],a[116:119],a[220:223],v[44:47]\nds_read_b64_tr_b16 a[212:213],v226 offset:57344\nds_read_b64_tr_b16 a[214:215],v226 offset:61440\nM v[56:59],a[112:115],a[224:227],v[56:59]\nds_read_b64_tr_b16 a[216:217],v227 offset:57344\n" \
"ds_read_b64_tr_b16 a[218:219],v227 offset:61440\nM v[60:63],a[116:119],a[224:227],v[60:63]\nds_read_b64_tr_b16 a[220:221],v226 offset:57856\nds_read_b64_tr_b16 a[222:223],v226 offset:61952\nM v[72:75],a[112:115],a[228:231],v[72:75]\nds_read_b64_tr_b16 a[224:225],v227 offset:57856\nds_read_b64_tr_b16 a[226:227],v227 offset:61952\nM v[76:79],a[116:119],a[228:231],v[76:79]\n" \
"ds_read_b64_tr_b16 a[228:229],v226 offset:58368\nds_read_b64_tr_b16 a[230:231],v226 offset:62464\nM v[88:91],a[112:115],a[232:235],v[88:91]\nbuffer_load_dwordx4 v[48:51],v238,s[52:55],s97 offen offset:2048 sc1\nM v[92:95],a[116:119],a[232:235],v[92:95]\nds_read_b64_tr_b16 a[232:233],v227 offset:58368\ns_waitcnt lgkmcnt(11)\nM v[104:107],a[112:115],a[236:239],v[104:107]\n" \
"ds_read_b64_tr_b16 a[234:235],v227 offset:62464\ns_add_u32 s38,s78,16384\nM v[108:111],a[116:119],a[236:239],v[108:111]\nds_read_b64_tr_b16 a[236:237],v226 offset:58880\nds_read_b64_tr_b16 a[238:239],v226 offset:62976\nM v[120:123],a[112:115],a[240:243],v[120:123]\nv_add_u32 v226,65536,v242\nv_lshlrev_b32 v64,16,v64\n" \
"M v[124:127],a[116:119],a[240:243],v[124:127]\nds_read_b64_tr_b16 a[240:241],v227 offset:58880\ns_waitcnt lgkmcnt(8)\nM v[8:11],a[120:123],a[212:215],v[8:11]\nbuffer_load_dwordx4 v[52:55],v238,s[52:55],s97 offen offset:2064 sc1\nM v[12:15],a[124:127],a[212:215],v[12:15]\nds_read_b64_tr_b16 a[242:243],v227 offset:62976\nds_read_b64_tr_b16 a[212:213],v226 offset:0\n" \
"M v[24:27],a[120:123],a[216:219],v[24:27]\nds_read_b64_tr_b16 a[214:215],v226 offset:4096\nv_xor_b32 v227,32,v226\nM v[28:31],a[124:127],a[216:219],v[28:31]\nds_read_b64_tr_b16 a[216:217],v227 offset:0\nds_read_b64_tr_b16 a[218:219],v227 offset:4096\nM v[40:43],a[120:123],a[220:223],v[40:43]\nv_lshlrev_b32 v65,16,v65\n" \
"v_lshlrev_b32 v66,16,v66\nM v[44:47],a[124:127],a[220:223],v[44:47]\nds_read_b64_tr_b16 a[220:221],v226 offset:512\ns_waitcnt lgkmcnt(7)\nM v[56:59],a[120:123],a[224:227],v[56:59]\nds_read_b64_tr_b16 a[222:223],v226 offset:4608\nv_lshlrev_b32 v67,16,v67\nM v[60:63],a[124:127],a[224:227],v[60:63]\n" \
"ds_read_b64_tr_b16 a[224:225],v227 offset:512\nds_read_b64_tr_b16 a[226:227],v227 offset:4608\nM v[72:75],a[120:123],a[228:231],v[72:75]\nv_lshlrev_b32 v68,16,v68\nv_lshlrev_b32 v69,16,v69\nM v[76:79],a[124:127],a[228:231],v[76:79]\nds_read_b64_tr_b16 a[228:229],v226 offset:1024\nds_read_b64_tr_b16 a[230:231],v226 offset:5120\n" \
"M v[88:91],a[120:123],a[232:235],v[88:91]\nv_lshlrev_b32 v70,16,v70\nv_lshlrev_b32 v71,16,v71\nM v[92:95],a[124:127],a[232:235],v[92:95]\nds_read_b64_tr_b16 a[232:233],v227 offset:1024\nds_read_b64_tr_b16 a[234:235],v227 offset:5120\nM v[104:107],a[120:123],a[236:239],v[104:107]\nv_lshlrev_b32 v80,16,v80\n" \
"v_lshlrev_b32 v81,16,v81\nM v[108:111],a[124:127],a[236:239],v[108:111]\nds_read_b64_tr_b16 a[236:237],v226 offset:1536\ns_waitcnt lgkmcnt(7)\nM v[120:123],a[120:123],a[240:243],v[120:123]\nds_read_b64_tr_b16 a[238:239],v226 offset:5632\nv_lshlrev_b32 v82,16,v82\nM v[124:127],a[124:127],a[240:243],v[124:127]\n" \
"ds_read_b64_tr_b16 a[240:241],v227 offset:1536\nds_read_b64_tr_b16 a[242:243],v227 offset:5632\nM v[8:11],a[128:131],a[212:215],v[8:11]\nv_lshlrev_b32 v83,16,v83\nv_lshlrev_b32 v84,16,v84\nM v[12:15],a[132:135],a[212:215],v[12:15]\nds_read_b64_tr_b16 a[212:213],v226 offset:8192\nds_read_b64_tr_b16 a[214:215],v226 offset:12288\n" \
"M v[24:27],a[128:131],a[216:219],v[24:27]\nv_lshlrev_b32 v85,16,v85\nv_lshlrev_b32 v86,16,v86\nM v[28:31],a[132:135],a[216:219],v[28:31]\nds_read_b64_tr_b16 a[216:217],v227 offset:8192\nds_read_b64_tr_b16 a[218:219],v227 offset:12288\nM v[40:43],a[128:131],a[220:223],v[40:43]\nv_lshlrev_b32 v87,16,v87\n" \
"v_lshlrev_b32 v96,16,v96\nM v[44:47],a[132:135],a[220:223],v[44:47]\nds_read_b64_tr_b16 a[220:221],v226 offset:8704\ns_waitcnt lgkmcnt(7)\nM v[56:59],a[128:131],a[224:227],v[56:59]\nds_read_b64_tr_b16 a[222:223],v226 offset:12800\nv_lshlrev_b32 v97,16,v97\nM v[60:63],a[132:135],a[224:227],v[60:63]\n" \
"ds_read_b64_tr_b16 a[224:225],v227 offset:8704\nds_read_b64_tr_b16 a[226:227],v227 offset:12800\nM v[72:75],a[128:131],a[228:231],v[72:75]\nv_lshlrev_b32 v98,16,v98\nv_lshlrev_b32 v99,16,v99\nM v[76:79],a[132:135],a[228:231],v[76:79]\nds_read_b64_tr_b16 a[228:229],v226 offset:9216\nds_read_b64_tr_b16 a[230:231],v226 offset:13312\n" \
"M v[88:91],a[128:131],a[232:235],v[88:91]\nv_lshlrev_b32 v100,16,v100\nv_lshlrev_b32 v101,16,v101\nM v[92:95],a[132:135],a[232:235],v[92:95]\nds_read_b64_tr_b16 a[232:233],v227 offset:9216\nds_read_b64_tr_b16 a[234:235],v227 offset:13312\nM v[104:107],a[128:131],a[236:239],v[104:107]\nv_lshlrev_b32 v102,16,v102\n" \
"v_lshlrev_b32 v103,16,v103\nM v[108:111],a[132:135],a[236:239],v[108:111]\nds_read_b64_tr_b16 a[236:237],v226 offset:9728\ns_waitcnt lgkmcnt(7)\nM v[120:123],a[128:131],a[240:243],v[120:123]\nds_read_b64_tr_b16 a[238:239],v226 offset:13824\nv_accvgpr_write_b32 a192,v235\nM v[124:127],a[132:135],a[240:243],v[124:127]\n" \
"ds_read_b64_tr_b16 a[240:241],v227 offset:9728\nds_read_b64_tr_b16 a[242:243],v227 offset:13824\nM v[8:11],a[136:139],a[212:215],v[8:11]\nv_accvgpr_write_b32 a193,v234\nv_accvgpr_write_b32 a198,v235\nM v[12:15],a[140:143],a[212:215],v[12:15]\nds_read_b64_tr_b16 a[212:213],v226 offset:32768\nds_read_b64_tr_b16 a[214:215],v226 offset:36864\n" \
"M v[24:27],a[136:139],a[216:219],v[24:27]\nv_accvgpr_write_b32 a199,v234\nM v[28:31],a[140:143],a[216:219],v[28:31]\nds_read_b64_tr_b16 a[216:217],v227 offset:32768\nds_read_b64_tr_b16 a[218:219],v227 offset:36864\nM v[40:43],a[136:139],a[220:223],v[40:43]\nM v[44:47],a[140:143],a[220:223],v[44:47]\nds_read_b64_tr_b16 a[220:221],v226 offset:33280\n" \
"s_waitcnt lgkmcnt(8)\nM v[56:59],a[136:139],a[224:227],v[56:59]\nds_read_b64_tr_b16 a[222:223],v226 offset:37376\nM v[60:63],a[140:143],a[224:227],v[60:63]\nds_read_b64_tr_b16 a[224:225],v227 offset:33280\nds_read_b64_tr_b16 a[226:227],v227 offset:37376\nM v[72:75],a[136:139],a[228:231],v[72:75]\nM v[76:79],a[140:143],a[228:231],v[76:79]\n" \
"ds_read_b64_tr_b16 a[228:229],v226 offset:33792\nds_read_b64_tr_b16 a[230:231],v226 offset:37888\nM v[88:91],a[136:139],a[232:235],v[88:91]\nM v[92:95],a[140:143],a[232:235],v[92:95]\nds_read_b64_tr_b16 a[232:233],v227 offset:33792\ns_waitcnt lgkmcnt(9)\nM v[104:107],a[136:139],a[236:239],v[104:107]\nds_read_b64_tr_b16 a[234:235],v227 offset:37888\n" \
"M v[108:111],a[140:143],a[236:239],v[108:111]\nds_read_b64_tr_b16 a[236:237],v226 offset:34304\nds_read_b64_tr_b16 a[238:239],v226 offset:38400\nM v[120:123],a[136:139],a[240:243],v[120:123]\nM v[124:127],a[140:143],a[240:243],v[124:127]\nds_read_b64_tr_b16 a[240:241],v227 offset:34304\ns_waitcnt vmcnt(18)\nM v[8:11],a[144:147],a[212:215],v[8:11]\n" \
"ds_read_b64_tr_b16 a[242:243],v227 offset:38400\nM v[12:15],a[148:151],a[212:215],v[12:15]\nds_read_b64_tr_b16 a[212:213],v226 offset:40960\ns_waitcnt lgkmcnt(9)\nM v[24:27],a[144:147],a[216:219],v[24:27]\nds_read_b64_tr_b16 a[214:215],v226 offset:45056\nM v[28:31],a[148:151],a[216:219],v[28:31]\nds_read_b64_tr_b16 a[216:217],v227 offset:40960\n" \
"ds_read_b64_tr_b16 a[218:219],v227 offset:45056\nM v[40:43],a[144:147],a[220:223],v[40:43]\nM v[44:47],a[148:151],a[220:223],v[44:47]\nds_read_b64_tr_b16 a[220:221],v226 offset:41472\nds_read_b64_tr_b16 a[222:223],v226 offset:45568\nM v[56:59],a[144:147],a[224:227],v[56:59]\nM v[60:63],a[148:151],a[224:227],v[60:63]\nds_read_b64_tr_b16 a[224:225],v227 offset:41472\n" \
"s_waitcnt lgkmcnt(9)\nM v[72:75],a[144:147],a[228:231],v[72:75]\nds_read_b64_tr_b16 a[226:227],v227 offset:45568\nM v[76:79],a[148:151],a[228:231],v[76:79]\nds_read_b64_tr_b16 a[228:229],v226 offset:41984\nds_read_b64_tr_b16 a[230:231],v226 offset:46080\nM v[88:91],a[144:147],a[232:235],v[88:91]\nM v[92:95],a[148:151],a[232:235],v[92:95]\n" \
"ds_read_b64_tr_b16 a[232:233],v227 offset:41984\nds_read_b64_tr_b16 a[234:235],v227 offset:46080\nM v[104:107],a[144:147],a[236:239],v[104:107]\nM v[108:111],a[148:151],a[236:239],v[108:111]\nds_read_b64_tr_b16 a[236:237],v226 offset:42496\ns_waitcnt lgkmcnt(9)\nM v[120:123],a[144:147],a[240:243],v[120:123]\nds_read_b64_tr_b16 a[238:239],v226 offset:46592\n" \
"M v[124:127],a[148:151],a[240:243],v[124:127]\nds_read_b64_tr_b16 a[240:241],v227 offset:42496\ns_waitcnt vmcnt(15)\nM v[8:11],a[152:155],a[212:215],v[8:11]\nds_read_b64_tr_b16 a[242:243],v227 offset:46592\nM v[12:15],a[156:159],a[212:215],v[12:15]\nM v[24:27],a[152:155],a[216:219],v[24:27]\nM v[28:31],a[156:159],a[216:219],v[28:31]\n" \
"s_waitcnt lgkmcnt(6)\nM v[40:43],a[152:155],a[220:223],v[40:43]\nM v[44:47],a[156:159],a[220:223],v[44:47]\nM v[56:59],a[152:155],a[224:227],v[56:59]\nM v[60:63],a[156:159],a[224:227],v[60:63]\nM v[72:75],a[152:155],a[228:231],v[72:75]\nM v[76:79],a[156:159],a[228:231],v[76:79]\nv_cvt_pk_bf16_f32 v192,v8,v24\n" \
"s_waitcnt lgkmcnt(3)\nM v[88:91],a[152:155],a[232:235],v[88:91]\nv_cvt_pk_bf16_f32 v196,v9,v25\nv_cvt_pk_bf16_f32 v200,v10,v26\nM v[92:95],a[156:159],a[232:235],v[92:95]\nv_cvt_pk_bf16_f32 v193,v40,v56\ns_waitcnt lgkmcnt(0)\nM v[104:107],a[152:155],a[236:239],v[104:107]\n" \
"v_cvt_pk_bf16_f32 v197,v41,v57\nv_cvt_pk_bf16_f32 v201,v42,v58\nM v[108:111],a[156:159],a[236:239],v[108:111]\nv_cvt_pk_bf16_f32 v194,v72,v88\nv_cvt_pk_bf16_f32 v198,v73,v89\nM v[120:123],a[152:155],a[240:243],v[120:123]\nv_cvt_pk_bf16_f32 v202,v74,v90\nv_cvt_pk_bf16_f32 v204,v11,v27\n" \
"v_cvt_pk_bf16_f32 v205,v43,v59\nv_cvt_pk_bf16_f32 v206,v75,v91\nv_cvt_pk_bf16_f32 v208,v12,v28\nv_cvt_pk_bf16_f32 v209,v44,v60\nv_cvt_pk_bf16_f32 v210,v76,v92\nv_cvt_pk_bf16_f32 v212,v13,v29\nv_cvt_pk_bf16_f32 v195,v104,v120\nv_cvt_pk_bf16_f32 v199,v105,v121\n" \
"v_cvt_pk_bf16_f32 v203,v106,v122\nv_cvt_pk_bf16_f32 v207,v107,v123\nv_cvt_pk_bf16_f32 v213,v45,v61\nv_cvt_pk_bf16_f32 v214,v77,v93\nv_cvt_pk_bf16_f32 v216,v14,v30\nv_cvt_pk_bf16_f32 v217,v46,v62\nv_cvt_pk_bf16_f32 v218,v78,v94\nv_cvt_pk_bf16_f32 v220,v15,v31\n" \
"v_cvt_pk_bf16_f32 v221,v47,v63\nv_cvt_pk_bf16_f32 v222,v79,v95\nM v[124:127],a[156:159],a[240:243],v[124:127]\nv_min_u32 v192,-2,v192\nv_min_u32 v193,-2,v193\nv_min_u32 v194,-2,v194\nv_min_u32 v195,-2,v195\nv_min_u32 v196,-2,v196\n" \
"v_min_u32 v197,-2,v197\nv_min_u32 v198,-2,v198\nv_min_u32 v199,-2,v199\nv_cvt_pk_bf16_f32 v211,v108,v124\nv_cvt_pk_bf16_f32 v215,v109,v125\nv_cvt_pk_bf16_f32 v219,v110,v126\nv_cvt_pk_bf16_f32 v223,v111,v127\nv_min_u32 v200,-2,v200\n" \
"v_min_u32 v201,-2,v201\nv_min_u32 v202,-2,v202\nv_min_u32 v203,-2,v203\nv_min_u32 v204,-2,v204\nv_min_u32 v205,-2,v205\nv_min_u32 v206,-2,v206\nv_min_u32 v207,-2,v207\nv_min_u32 v208,-2,v208\n" \
"v_min_u32 v209,-2,v209\nv_min_u32 v210,-2,v210\nv_min_u32 v211,-2,v211\nv_min_u32 v212,-2,v212\nv_min_u32 v213,-2,v213\nv_min_u32 v214,-2,v214\nv_min_u32 v215,-2,v215\nv_min_u32 v216,-2,v216\n" \
"v_min_u32 v217,-2,v217\nv_min_u32 v218,-2,v218\nv_min_u32 v219,-2,v219\nv_min_u32 v220,-2,v220\nv_min_u32 v221,-2,v221\nv_min_u32 v222,-2,v222\nv_min_u32 v223,-2,v223\nbuffer_store_dwordx4 v[192:195],v253,s[52:55],s78 offen offset:0 sc1\n" \
"buffer_store_dwordx4 v[196:199],v253,s[52:55],s78 offen offset:1024 sc1\nbuffer_store_dwordx4 v[200:203],v253,s[52:55],s78 offen offset:2048 sc1\nbuffer_store_dwordx4 v[204:207],v253,s[52:55],s78 offen offset:3072 sc1\nbuffer_store_dwordx4 v[208:211],v253,s[52:55],s38 offen offset:0 sc1\nbuffer_store_dwordx4 v[212:215],v253,s[52:55],s38 offen offset:1024 sc1\nbuffer_store_dwordx4 v[216:219],v253,s[52:55],s38 offen offset:2048 sc1\nbuffer_store_dwordx4 v[220:223],v253,s[52:55],s38 offen offset:3072 sc1\n.Ltail:\n" \
"s_add_u32 s72,s95,0\nbuffer_load_dwordx4 v[160:163],v240,s[52:55],s72 offen sc1\ns_add_u32 s72,s95,2097152\nbuffer_load_dwordx4 v[164:167],v240,s[52:55],s72 offen sc1\ns_add_u32 s72,s95,4194304\nbuffer_load_dwordx4 v[168:171],v240,s[52:55],s72 offen sc1\ns_add_u32 s72,s95,6291456\nbuffer_load_dwordx4 v[172:175],v240,s[52:55],s72 offen sc1\n" \
"s_add_u32 s72,s95,8388608\nbuffer_load_dwordx4 v[176:179],v240,s[52:55],s72 offen sc1\ns_add_u32 s72,s95,10485760\nbuffer_load_dwordx4 v[180:183],v240,s[52:55],s72 offen sc1\ns_add_u32 s72,s95,12582912\nbuffer_load_dwordx4 v[184:187],v240,s[52:55],s72 offen sc1\ns_add_u32 s72,s95,14680064\nbuffer_load_dwordx4 v[188:191],v240,s[52:55],s72 offen sc1\n" \
"s_add_u32 s72,s74,0\nbuffer_load_dwordx4 v[128:131],v240,s[52:55],s72 offen sc1\ns_add_u32 s72,s74,2097152\nbuffer_load_dwordx4 v[132:135],v240,s[52:55],s72 offen sc1\ns_add_u32 s72,s74,4194304\nbuffer_load_dwordx4 v[136:139],v240,s[52:55],s72 offen sc1\ns_add_u32 s72,s74,6291456\nbuffer_load_dwordx4 v[140:143],v240,s[52:55],s72 offen sc1\n" \
"s_add_u32 s72,s74,8388608\nbuffer_load_dwordx4 v[144:147],v240,s[52:55],s72 offen sc1\ns_add_u32 s72,s74,10485760\nbuffer_load_dwordx4 v[148:151],v240,s[52:55],s72 offen sc1\ns_add_u32 s72,s74,12582912\nbuffer_load_dwordx4 v[152:155],v240,s[52:55],s72 offen sc1\ns_add_u32 s72,s74,14680064\nbuffer_load_dwordx4 v[156:159],v240,s[52:55],s72 offen sc1\n" \
"s_waitcnt vmcnt(24)\nv_accvgpr_read_b32 v192,a160\nv_accvgpr_read_b32 v193,a161\nv_accvgpr_read_b32 v194,a162\nv_accvgpr_read_b32 v195,a163\nv_accvgpr_read_b32 v196,a164\nv_accvgpr_read_b32 v197,a165\nv_accvgpr_read_b32 v198,a166\n" \
"v_accvgpr_read_b32 v199,a167\nv_accvgpr_read_b32 v200,a168\nv_accvgpr_read_b32 v201,a169\nv_accvgpr_read_b32 v202,a170\nv_accvgpr_read_b32 v203,a171\nv_accvgpr_read_b32 v204,a172\nv_accvgpr_read_b32 v205,a173\nv_accvgpr_read_b32 v206,a174\n" \
"v_accvgpr_read_b32 v207,a175\nv_accvgpr_read_b32 v208,a176\nv_accvgpr_read_b32 v209,a177\nv_accvgpr_read_b32 v210,a178\nv_accvgpr_read_b32 v211,a179\nv_accvgpr_read_b32 v212,a180\nv_accvgpr_read_b32 v213,a181\nv_accvgpr_read_b32 v214,a182\n" \
"v_accvgpr_read_b32 v215,a183\nv_and_b32 v228,15,v249\nv_and_b32 v229,0x300,v240\nv_lshl_or_b32 v228,v228,1,v229\ns_add_u32 s70,s64,82944\nv_add_u32 v228,s70,v228\nv_add_u32 v229,s70,v240\nv_lshlrev_b32 v230,11,v249\n" \
"v_cmp_lt_u32 vcc,7,v249\nv_mov_b32 v231,0x40000000\ns_lshl3_add_u32 s37,s6,s75\nv_cndmask_b32 v230,v230,v231,vcc\ns_add_u32 s37,s37,557056\n.Lrow1:\ns_waitcnt vmcnt(15)\nM v[72:75],a[192:195],v[160:163],0\n" \
"M v[76:79],a[196:199],v[160:163],0\nv_add_f32 v104,1.0,v32\nv_add_f32 v105,1.0,v33\nv_add_f32 v106,1.0,v34\nv_add_f32 v107,1.0,v35\nv_add_f32 v108,1.0,v36\nv_add_f32 v109,1.0,v37\nv_add_f32 v110,1.0,v38\n" \
"v_add_f32 v111,1.0,v39\nv_cvt_pk_bf16_f32 v232,0,v104\nv_cvt_pk_bf16_f32 v233,0,v105\nv_cvt_pk_bf16_f32 v234,0,v106\nv_cvt_pk_bf16_f32 v235,0,v107\nv_cvt_pk_bf16_f32 v236,0,v108\nv_cvt_pk_bf16_f32 v237,0,v109\nv_cvt_pk_bf16_f32 v238,0,v110\n" \
"v_cvt_pk_bf16_f32 v239,0,v111\ns_waitcnt vmcnt(14)\nM v[72:75],a[192:195],v[164:167],v[72:75]\nM v[76:79],a[196:199],v[164:167],v[76:79]\ns_waitcnt vmcnt(13)\nM v[72:75],a[192:195],v[168:171],v[72:75]\nM v[76:79],a[196:199],v[168:171],v[76:79]\ns_waitcnt vmcnt(12)\n" \
"M v[72:75],a[192:195],v[172:175],v[72:75]\nM v[76:79],a[196:199],v[172:175],v[76:79]\ns_waitcnt vmcnt(11)\nM v[72:75],a[192:195],v[176:179],v[72:75]\nM v[76:79],a[196:199],v[176:179],v[76:79]\ns_waitcnt vmcnt(10)\nM v[72:75],a[192:195],v[180:183],v[72:75]\nM v[76:79],a[196:199],v[180:183],v[76:79]\n" \
"s_waitcnt vmcnt(9)\nM v[72:75],a[192:195],v[184:187],v[72:75]\nM v[76:79],a[196:199],v[184:187],v[76:79]\ns_waitcnt vmcnt(8)\nM v[72:75],a[192:195],v[188:191],v[72:75]\nM v[76:79],a[196:199],v[188:191],v[76:79]\ns_sub_u32 s38,56622112,s95\ns_lshr_b32 s38,s38,5\n" \
"s_nop 4\nv_cvt_pk_bf16_f32 v72,0,v72\nv_cvt_pk_bf16_f32 v73,0,v73\nv_cvt_pk_bf16_f32 v74,0,v74\nv_cvt_pk_bf16_f32 v75,0,v75\nv_cvt_pk_bf16_f32 v76,0,v76\nv_cvt_pk_bf16_f32 v77,0,v77\nv_cvt_pk_bf16_f32 v78,0,v78\n" \
"v_cvt_pk_bf16_f32 v79,0,v79\nv_pk_mul_f32 v[72:73],v[72:73],v[0:1]\nv_pk_mul_f32 v[74:75],v[74:75],v[2:3]\nv_pk_mul_f32 v[76:77],v[76:77],v[4:5]\nv_pk_mul_f32 v[78:79],v[78:79],v[6:7]\nv_cvt_pk_bf16_f32 v72,0,v72\nv_cvt_pk_bf16_f32 v73,0,v73\nv_cvt_pk_bf16_f32 v74,0,v74\n" \
"v_cvt_pk_bf16_f32 v75,0,v75\nv_cvt_pk_bf16_f32 v76,0,v76\nv_cvt_pk_bf16_f32 v77,0,v77\nv_cvt_pk_bf16_f32 v78,0,v78\nv_cvt_pk_bf16_f32 v79,0,v79\nv_pk_add_f32 v[72:73],v[72:73],v[64:65]\nv_pk_add_f32 v[74:75],v[74:75],v[66:67]\nv_pk_add_f32 v[76:77],v[76:77],v[68:69]\n" \
"v_pk_add_f32 v[78:79],v[78:79],v[70:71]\nv_cvt_pk_bf16_f32 v72,0,v72\nv_cvt_pk_bf16_f32 v73,0,v73\nv_cvt_pk_bf16_f32 v74,0,v74\nv_cvt_pk_bf16_f32 v75,0,v75\nv_cvt_pk_bf16_f32 v76,0,v76\nv_cvt_pk_bf16_f32 v77,0,v77\nv_cvt_pk_bf16_f32 v78,0,v78\n" \
"v_cvt_pk_bf16_f32 v79,0,v79\nv_readfirstlane_b32 s70,v72\ns_nop 1\nv_subrev_f32 v104,s70,v72\nv_subrev_f32 v105,s70,v73\nv_subrev_f32 v106,s70,v74\nv_subrev_f32 v107,s70,v75\nv_subrev_f32 v108,s70,v76\n" \
"v_subrev_f32 v109,s70,v77\nv_subrev_f32 v110,s70,v78\nv_subrev_f32 v111,s70,v79\nv_pk_mul_f32 v[124:125],v[104:105],v[104:105]\nv_pk_add_f32 v[120:121],v[104:105],v[106:107]\nv_pk_fma_f32 v[124:125],v[106:107],v[106:107],v[124:125]\nv_pk_fma_f32 v[124:125],v[108:109],v[108:109],v[124:125]\nv_pk_fma_f32 v[124:125],v[110:111],v[110:111],v[124:125]\n" \
"v_pk_add_f32 v[108:109],v[108:109],v[110:111]\nv_pk_add_f32 v[120:121],v[120:121],v[108:109]\nv_add_f32 v120,v120,v121\nv_add_f32 v121,v124,v125\ns_nop 1\nv_permlane16_swap_b32 v120,v121\nv_add_f32 v120,v120,v121\nv_mov_b32 v121,v120\n" \
"s_nop 1\nv_permlane32_swap_b32 v120,v121\nv_add_f32 v120,v120,v121\ns_nop 1\nv_add_f32_dpp v120,v120,v120 quad_perm:[1,0,3,2] row_mask:0xf bank_mask:0xf\ns_nop 1\nv_add_f32_dpp v120,v120,v120 quad_perm:[2,3,0,1] row_mask:0xf bank_mask:0xf\ns_nop 1\n" \
"v_add_f32_dpp v120,v120,v120 row_half_mirror row_mask:0xf bank_mask:0xf\ns_nop 1\nv_add_f32_dpp v120,v120,v120 row_mirror row_mask:0xf bank_mask:0xf\nv_mul_f32 v120,0x3b000000,v120\nv_mov_b32 v121,v120\nv_cmp_u_f32 vcc,v120,v120\ns_nop 0\nv_permlane16_swap_b32 v120,v121\n" \
"s_cbranch_vccnz .Lrs1\n.Lrb1:\nv_fma_f32 v121,-v120,v120,v121\nv_add_f32 v122,s70,v120\nv_max_f32 v121,0,v121\nv_cvt_pk_bf16_f32 v122,0,v122\nv_cvt_pk_bf16_f32 v121,0,v121\nv_add_f32 v121,0x3727c5ac,v121\n" \
"v_cvt_pk_bf16_f32 v121,0,v121\nv_sqrt_f32 v121,v121\nv_pk_add_f32 v[88:89],v[72:73],v[122:123] op_sel_hi:[1,0] neg_lo:[0,1] neg_hi:[0,1]\nv_cvt_pk_bf16_f32 v121,0,v121\nv_rcp_f32 v123,v121\nv_cvt_pk_bf16_f32 v88,0,v88\nv_pk_add_f32 v[90:91],v[74:75],v[122:123] op_sel_hi:[1,0] neg_lo:[0,1] neg_hi:[0,1]\nv_pk_add_f32 v[92:93],v[76:77],v[122:123] op_sel_hi:[1,0] neg_lo:[0,1] neg_hi:[0,1]\n" \
"v_pk_add_f32 v[94:95],v[78:79],v[122:123] op_sel_hi:[1,0] neg_lo:[0,1] neg_hi:[0,1]\nv_cvt_pk_bf16_f32 v89,0,v89\nv_pk_mul_f32 v[88:89],v[88:89],v[122:123] op_sel:[0,1]\nv_cvt_pk_bf16_f32 v88,0,v88\nv_cvt_pk_bf16_f32 v89,0,v89\nv_pk_mul_f32 v[88:89],v[88:89],v[232:233]\nv_cvt_pk_bf16_f32 v88,0,v88\nv_cvt_pk_bf16_f32 v89,0,v89\n" \
"v_pk_add_f32 v[88:89],v[88:89],v[16:17]\nv_cvt_pk_bf16_f32 v88,0,v88\nv_cvt_pk_bf16_f32 v89,0,v89\nv_pk_mul_f32 v[124:125],v[88:89],v[96:97]\nv_cvt_pk_bf16_f32 v90,0,v90\nv_cvt_pk_bf16_f32 v91,0,v91\nv_pk_mul_f32 v[90:91],v[90:91],v[122:123] op_sel:[0,1]\nv_cvt_pk_bf16_f32 v90,0,v90\n" \
"v_cvt_pk_bf16_f32 v91,0,v91\nv_pk_mul_f32 v[90:91],v[90:91],v[234:235]\nv_cvt_pk_bf16_f32 v90,0,v90\nv_cvt_pk_bf16_f32 v91,0,v91\nv_pk_add_f32 v[90:91],v[90:91],v[18:19]\nv_cvt_pk_bf16_f32 v90,0,v90\nv_cvt_pk_bf16_f32 v91,0,v91\nv_pk_fma_f32 v[124:125],v[90:91],v[98:99],v[124:125]\n" \
"v_cvt_pk_bf16_f32 v92,0,v92\nv_cvt_pk_bf16_f32 v93,0,v93\nv_pk_mul_f32 v[92:93],v[92:93],v[122:123] op_sel:[0,1]\nv_cvt_pk_bf16_f32 v92,0,v92\nv_cvt_pk_bf16_f32 v93,0,v93\nv_pk_mul_f32 v[92:93],v[92:93],v[236:237]\nv_cvt_pk_bf16_f32 v92,0,v92\nv_cvt_pk_bf16_f32 v93,0,v93\n" \
"v_pk_add_f32 v[92:93],v[92:93],v[20:21]\nv_cvt_pk_bf16_f32 v92,0,v92\nv_cvt_pk_bf16_f32 v93,0,v93\nv_pk_fma_f32 v[124:125],v[92:93],v[100:101],v[124:125]\nv_cvt_pk_bf16_f32 v94,0,v94\nv_cvt_pk_bf16_f32 v95,0,v95\nv_pk_mul_f32 v[94:95],v[94:95],v[122:123] op_sel:[0,1]\nv_cvt_pk_bf16_f32 v94,0,v94\n" \
"v_cvt_pk_bf16_f32 v95,0,v95\nv_pk_mul_f32 v[94:95],v[94:95],v[238:239]\nv_cvt_pk_bf16_f32 v94,0,v94\nv_cvt_pk_bf16_f32 v95,0,v95\nv_pk_add_f32 v[94:95],v[94:95],v[22:23]\nv_cvt_pk_bf16_f32 v94,0,v94\nv_cvt_pk_bf16_f32 v95,0,v95\nv_pk_fma_f32 v[124:125],v[94:95],v[102:103],v[124:125]\n" \
"v_add_f32 v124,v124,v125\nv_mov_b32 v125,v124\nv_lshrrev_b32 v104,16,v121\nv_or_b32 v104,v104,v122\nv_permlane16_swap_b32 v124,v125\nv_add_f32 v124,v124,v125\nv_mov_b32 v125,v124\nv_pk_mul_f32 v[88:89],v[88:89],v[48:49]\n" \
"v_pk_mul_f32 v[90:91],v[90:91],v[50:51]\nv_permlane32_swap_b32 v124,v125\nv_add_f32 v124,v124,v125\nv_pk_mul_f32 v[92:93],v[92:93],v[52:53]\nv_pk_mul_f32 v[94:95],v[94:95],v[54:55]\nv_add_f32_dpp v124,v124,v124 quad_perm:[1,0,3,2] row_mask:0xf bank_mask:0xf\nv_cvt_pk_bf16_f32 v88,0,v88\nv_cvt_pk_bf16_f32 v89,0,v89\n" \
"v_add_f32_dpp v124,v124,v124 quad_perm:[2,3,0,1] row_mask:0xf bank_mask:0xf\nv_cvt_pk_bf16_f32 v90,0,v90\nv_cvt_pk_bf16_f32 v91,0,v91\nv_add_f32_dpp v124,v124,v124 row_half_mirror row_mask:0xf bank_mask:0xf\nv_cvt_pk_bf16_f32 v92,0,v92\nv_cvt_pk_bf16_f32 v93,0,v93\nv_add_f32_dpp v124,v124,v124 row_mirror row_mask:0xf bank_mask:0xf\nv_cvt_pk_bf16_f32 v124,0,v124\n" \
"v_cvt_pk_bf16_f32 v94,0,v94\nv_readfirstlane_b32 s77,v124\nv_cvt_pk_bf16_f32 v95,0,v95\ns_ashr_i32 s71,s77,31\ns_and_b32 s72,s77,0x7fffffff\ns_or_b32 s71,s71,0x80000000\ns_xor_b32 s77,s77,s71\ns_and_b32 s77,s77,0xffff0000\n" \
"s_cmp_gt_u32 s72,0x7f800000\ns_cselect_b32 s77,0,s77\ns_or_b32 s77,s77,s38\nv_mov_b32 v105,s77\nds_max_u64 v250,v[104:105]\nv_pk_add_f32 v[88:89],v[88:89],v[72:73]\nv_pk_add_f32 v[90:91],v[90:91],v[74:75]\nv_pk_add_f32 v[92:93],v[92:93],v[76:77]\n" \
"v_pk_add_f32 v[94:95],v[94:95],v[78:79]\nv_cvt_pk_bf16_f32 v88,0,v88\nds_write_b16_d16_hi v228,v88 offset:0\nv_cvt_pk_bf16_f32 v89,0,v89\nds_write_b16_d16_hi v228,v89 offset:32\nv_cvt_pk_bf16_f32 v90,0,v90\nds_write_b16_d16_hi v228,v90 offset:64\nv_cvt_pk_bf16_f32 v91,0,v91\n" \
"ds_write_b16_d16_hi v228,v91 offset:96\nv_cvt_pk_bf16_f32 v92,0,v92\nds_write_b16_d16_hi v228,v92 offset:128\nv_cvt_pk_bf16_f32 v93,0,v93\nds_write_b16_d16_hi v228,v93 offset:160\nv_cvt_pk_bf16_f32 v94,0,v94\nds_write_b16_d16_hi v228,v94 offset:192\nv_cvt_pk_bf16_f32 v95,0,v95\n" \
"ds_write_b16_d16_hi v228,v95 offset:224\nds_read_b128 v[216:219],v229 offset:0\n.Lrow0:\ns_waitcnt vmcnt(7)\nM v[8:11],a[192:195],v[128:131],0\nM v[12:15],a[196:199],v[128:131],0\ns_waitcnt vmcnt(6)\nM v[8:11],a[192:195],v[132:135],v[8:11]\n" \
"M v[12:15],a[196:199],v[132:135],v[12:15]\ns_waitcnt vmcnt(5)\nM v[8:11],a[192:195],v[136:139],v[8:11]\nM v[12:15],a[196:199],v[136:139],v[12:15]\ns_waitcnt vmcnt(4)\nM v[8:11],a[192:195],v[140:143],v[8:11]\nM v[12:15],a[196:199],v[140:143],v[12:15]\ns_waitcnt vmcnt(3)\n" \
"M v[8:11],a[192:195],v[144:147],v[8:11]\nM v[12:15],a[196:199],v[144:147],v[12:15]\ns_waitcnt vmcnt(2)\nM v[8:11],a[192:195],v[148:151],v[8:11]\nM v[12:15],a[196:199],v[148:151],v[12:15]\ns_waitcnt vmcnt(1)\nM v[8:11],a[192:195],v[152:155],v[8:11]\nM v[12:15],a[196:199],v[152:155],v[12:15]\n" \
"s_waitcnt vmcnt(0)\nM v[8:11],a[192:195],v[156:159],v[8:11]\nM v[12:15],a[196:199],v[156:159],v[12:15]\ns_sub_u32 s39,56622112,s74\ns_lshr_b32 s39,s39,5\ns_nop 4\nv_cvt_pk_bf16_f32 v8,0,v8\nv_cvt_pk_bf16_f32 v9,0,v9\n" \
"v_cvt_pk_bf16_f32 v10,0,v10\nv_cvt_pk_bf16_f32 v11,0,v11\nv_cvt_pk_bf16_f32 v12,0,v12\nv_cvt_pk_bf16_f32 v13,0,v13\nv_cvt_pk_bf16_f32 v14,0,v14\nv_cvt_pk_bf16_f32 v15,0,v15\nv_pk_mul_f32 v[8:9],v[8:9],v[0:1]\nv_pk_mul_f32 v[10:11],v[10:11],v[2:3]\n" \
"v_pk_mul_f32 v[12:13],v[12:13],v[4:5]\nv_pk_mul_f32 v[14:15],v[14:15],v[6:7]\nv_cvt_pk_bf16_f32 v8,0,v8\nv_cvt_pk_bf16_f32 v9,0,v9\nv_cvt_pk_bf16_f32 v10,0,v10\nv_cvt_pk_bf16_f32 v11,0,v11\nv_cvt_pk_bf16_f32 v12,0,v12\nv_cvt_pk_bf16_f32 v13,0,v13\n" \
"v_cvt_pk_bf16_f32 v14,0,v14\nv_cvt_pk_bf16_f32 v15,0,v15\nv_pk_add_f32 v[8:9],v[8:9],v[80:81]\nv_pk_add_f32 v[10:11],v[10:11],v[82:83]\nv_pk_add_f32 v[12:13],v[12:13],v[84:85]\nv_pk_add_f32 v[14:15],v[14:15],v[86:87]\nv_cvt_pk_bf16_f32 v8,0,v8\nv_cvt_pk_bf16_f32 v9,0,v9\n" \
"v_cvt_pk_bf16_f32 v10,0,v10\nv_cvt_pk_bf16_f32 v11,0,v11\nv_cvt_pk_bf16_f32 v12,0,v12\nv_cvt_pk_bf16_f32 v13,0,v13\nv_cvt_pk_bf16_f32 v14,0,v14\nv_cvt_pk_bf16_f32 v15,0,v15\nv_readfirstlane_b32 s70,v8\ns_nop 1\n" \
"v_subrev_f32 v40,s70,v8\nv_subrev_f32 v41,s70,v9\nv_subrev_f32 v42,s70,v10\nv_subrev_f32 v43,s70,v11\nv_subrev_f32 v44,s70,v12\nv_subrev_f32 v45,s70,v13\nv_subrev_f32 v46,s70,v14\nv_subrev_f32 v47,s70,v15\n" \
"v_pk_mul_f32 v[60:61],v[40:41],v[40:41]\nv_pk_add_f32 v[56:57],v[40:41],v[42:43]\nv_pk_fma_f32 v[60:61],v[42:43],v[42:43],v[60:61]\nv_pk_fma_f32 v[60:61],v[44:45],v[44:45],v[60:61]\nv_pk_fma_f32 v[60:61],v[46:47],v[46:47],v[60:61]\nv_pk_add_f32 v[44:45],v[44:45],v[46:47]\nv_pk_add_f32 v[56:57],v[56:57],v[44:45]\nv_add_f32 v56,v56,v57\n" \
"v_add_f32 v57,v60,v61\ns_nop 1\nv_permlane16_swap_b32 v56,v57\nv_add_f32 v56,v56,v57\nv_mov_b32 v57,v56\ns_nop 1\nv_permlane32_swap_b32 v56,v57\nv_add_f32 v56,v56,v57\n" \
"s_nop 1\nv_add_f32_dpp v56,v56,v56 quad_perm:[1,0,3,2] row_mask:0xf bank_mask:0xf\ns_nop 1\nv_add_f32_dpp v56,v56,v56 quad_perm:[2,3,0,1] row_mask:0xf bank_mask:0xf\ns_nop 1\nv_add_f32_dpp v56,v56,v56 row_half_mirror row_mask:0xf bank_mask:0xf\ns_nop 1\nv_add_f32_dpp v56,v56,v56 row_mirror row_mask:0xf bank_mask:0xf\n" \
"v_mul_f32 v56,0x3b000000,v56\nv_mov_b32 v57,v56\nv_cmp_u_f32 vcc,v56,v56\ns_nop 0\nv_permlane16_swap_b32 v56,v57\ns_cbranch_vccnz .Lrs0\n.Lrb0:\nv_fma_f32 v57,-v56,v56,v57\n" \
"v_add_f32 v58,s70,v56\nv_max_f32 v57,0,v57\nv_cvt_pk_bf16_f32 v58,0,v58\nv_cvt_pk_bf16_f32 v57,0,v57\nv_add_f32 v57,0x3727c5ac,v57\nv_cvt_pk_bf16_f32 v57,0,v57\nv_sqrt_f32 v57,v57\nv_pk_add_f32 v[24:25],v[8:9],v[58:59] op_sel_hi:[1,0] neg_lo:[0,1] neg_hi:[0,1]\n" \
"v_cvt_pk_bf16_f32 v57,0,v57\nv_rcp_f32 v59,v57\nv_cvt_pk_bf16_f32 v24,0,v24\nv_pk_add_f32 v[26:27],v[10:11],v[58:59] op_sel_hi:[1,0] neg_lo:[0,1] neg_hi:[0,1]\nv_pk_add_f32 v[28:29],v[12:13],v[58:59] op_sel_hi:[1,0] neg_lo:[0,1] neg_hi:[0,1]\nv_pk_add_f32 v[30:31],v[14:15],v[58:59] op_sel_hi:[1,0] neg_lo:[0,1] neg_hi:[0,1]\nv_cvt_pk_bf16_f32 v25,0,v25\nv_pk_mul_f32 v[24:25],v[24:25],v[58:59] op_sel:[0,1]\n" \
"v_cvt_pk_bf16_f32 v24,0,v24\nv_cvt_pk_bf16_f32 v25,0,v25\nv_pk_mul_f32 v[24:25],v[24:25],v[232:233]\nv_cvt_pk_bf16_f32 v24,0,v24\nv_cvt_pk_bf16_f32 v25,0,v25\nv_pk_add_f32 v[24:25],v[24:25],v[16:17]\nv_cvt_pk_bf16_f32 v24,0,v24\nv_cvt_pk_bf16_f32 v25,0,v25\n" \
"v_pk_mul_f32 v[60:61],v[24:25],v[96:97]\nv_cvt_pk_bf16_f32 v26,0,v26\nv_cvt_pk_bf16_f32 v27,0,v27\nv_pk_mul_f32 v[26:27],v[26:27],v[58:59] op_sel:[0,1]\nv_cvt_pk_bf16_f32 v26,0,v26\nv_cvt_pk_bf16_f32 v27,0,v27\nv_pk_mul_f32 v[26:27],v[26:27],v[234:235]\nv_cvt_pk_bf16_f32 v26,0,v26\n" \
"v_cvt_pk_bf16_f32 v27,0,v27\nv_pk_add_f32 v[26:27],v[26:27],v[18:19]\nv_cvt_pk_bf16_f32 v26,0,v26\nv_cvt_pk_bf16_f32 v27,0,v27\nv_pk_fma_f32 v[60:61],v[26:27],v[98:99],v[60:61]\nv_cvt_pk_bf16_f32 v28,0,v28\nv_cvt_pk_bf16_f32 v29,0,v29\nv_pk_mul_f32 v[28:29],v[28:29],v[58:59] op_sel:[0,1]\n" \
"v_cvt_pk_bf16_f32 v28,0,v28\nv_cvt_pk_bf16_f32 v29,0,v29\nv_pk_mul_f32 v[28:29],v[28:29],v[236:237]\nv_cvt_pk_bf16_f32 v28,0,v28\nv_cvt_pk_bf16_f32 v29,0,v29\nv_pk_add_f32 v[28:29],v[28:29],v[20:21]\nv_cvt_pk_bf16_f32 v28,0,v28\nv_cvt_pk_bf16_f32 v29,0,v29\n" \
"v_pk_fma_f32 v[60:61],v[28:29],v[100:101],v[60:61]\nv_cvt_pk_bf16_f32 v30,0,v30\nv_cvt_pk_bf16_f32 v31,0,v31\nv_pk_mul_f32 v[30:31],v[30:31],v[58:59] op_sel:[0,1]\nv_cvt_pk_bf16_f32 v30,0,v30\nv_cvt_pk_bf16_f32 v31,0,v31\nv_pk_mul_f32 v[30:31],v[30:31],v[238:239]\nv_cvt_pk_bf16_f32 v30,0,v30\n" \
"v_cvt_pk_bf16_f32 v31,0,v31\nv_pk_add_f32 v[30:31],v[30:31],v[22:23]\nv_cvt_pk_bf16_f32 v30,0,v30\nv_cvt_pk_bf16_f32 v31,0,v31\nv_pk_fma_f32 v[60:61],v[30:31],v[102:103],v[60:61]\nv_add_f32 v60,v60,v61\nv_mov_b32 v61,v60\nv_lshrrev_b32 v40,16,v57\n" \
"v_or_b32 v40,v40,v58\nv_permlane16_swap_b32 v60,v61\nv_add_f32 v60,v60,v61\nv_mov_b32 v61,v60\nv_pk_mul_f32 v[24:25],v[24:25],v[48:49]\nv_pk_mul_f32 v[26:27],v[26:27],v[50:51]\nv_permlane32_swap_b32 v60,v61\nv_add_f32 v60,v60,v61\n" \
"v_pk_mul_f32 v[28:29],v[28:29],v[52:53]\nv_pk_mul_f32 v[30:31],v[30:31],v[54:55]\nv_add_f32_dpp v60,v60,v60 quad_perm:[1,0,3,2] row_mask:0xf bank_mask:0xf\nv_cvt_pk_bf16_f32 v24,0,v24\nv_cvt_pk_bf16_f32 v25,0,v25\nv_add_f32_dpp v60,v60,v60 quad_perm:[2,3,0,1] row_mask:0xf bank_mask:0xf\nv_cvt_pk_bf16_f32 v26,0,v26\nv_cvt_pk_bf16_f32 v27,0,v27\n" \
"v_add_f32_dpp v60,v60,v60 row_half_mirror row_mask:0xf bank_mask:0xf\nv_cvt_pk_bf16_f32 v28,0,v28\nv_cvt_pk_bf16_f32 v29,0,v29\nv_add_f32_dpp v60,v60,v60 row_mirror row_mask:0xf bank_mask:0xf\nv_cvt_pk_bf16_f32 v60,0,v60\nv_cvt_pk_bf16_f32 v30,0,v30\nv_readfirstlane_b32 s76,v60\nv_cvt_pk_bf16_f32 v31,0,v31\n" \
"s_ashr_i32 s71,s76,31\ns_and_b32 s72,s76,0x7fffffff\ns_or_b32 s71,s71,0x80000000\ns_xor_b32 s76,s76,s71\ns_and_b32 s76,s76,0xffff0000\ns_cmp_gt_u32 s72,0x7f800000\ns_cselect_b32 s76,0,s76\ns_or_b32 s76,s76,s39\n" \
"v_mov_b32 v41,s76\nds_max_u64 v250,v[40:41]\ns_waitcnt lgkmcnt(0)\ns_barrier\nds_read_b64 v[224:225],v250\ns_waitcnt lgkmcnt(0)\nbuffer_store_dwordx2 v[224:225],v230,s[52:55],s37 offen sc1\nv_pk_add_f32 v[24:25],v[24:25],v[8:9]\n" \
"v_pk_add_f32 v[26:27],v[26:27],v[10:11]\nv_pk_add_f32 v[28:29],v[28:29],v[12:13]\nv_pk_add_f32 v[30:31],v[30:31],v[14:15]\nv_cvt_pk_bf16_f32 v24,0,v24\nds_write_b16_d16_hi v228,v24 offset:1024\nv_cvt_pk_bf16_f32 v25,0,v25\nds_write_b16_d16_hi v228,v25 offset:1056\nv_cvt_pk_bf16_f32 v26,0,v26\n" \
"ds_write_b16_d16_hi v228,v26 offset:1088\nv_cvt_pk_bf16_f32 v27,0,v27\nds_write_b16_d16_hi v228,v27 offset:1120\nv_cvt_pk_bf16_f32 v28,0,v28\nds_write_b16_d16_hi v228,v28 offset:1152\nv_cvt_pk_bf16_f32 v29,0,v29\nds_write_b16_d16_hi v228,v29 offset:1184\nv_cvt_pk_bf16_f32 v30,0,v30\n" \
"ds_write_b16_d16_hi v228,v30 offset:1216\nv_cvt_pk_bf16_f32 v31,0,v31\nds_write_b16_d16_hi v228,v31 offset:1248\nds_read_b128 v[220:223],v229 offset:1024\nv_readfirstlane_b32 s70,v225\nv_mov_b32 v226,s77\nv_mov_b32 v227,s76\ns_sub_u32 s95,s95,54525952\n" \
"s_sub_u32 s74,s74,54525952\ns_cmp_eq_u32 s70,s77\ns_cselect_b32 s95,0x40000000,s95\ns_cmp_eq_u32 s70,s76\ns_cselect_b32 s74,0x40000000,s74\ns_waitcnt lgkmcnt(0)\nbuffer_store_dwordx4 v[216:219],v240,s[44:47],s95 offen sc1\nbuffer_store_dwordx4 v[220:223],v240,s[44:47],s74 offen sc1\n" \
"s_waitcnt vmcnt(3)\ns_branch .Lfin\n.Lhs:\ns_sub_u32 s73,s73,1\ns_cbranch_scc1 .Lhb\ns_sleep 2\nbuffer_load_dwordx2 v[166:167],v165,s[52:55],s80 offen sc1\ns_waitcnt vmcnt(0)\n" \
"v_xor_b32 v166,v166,v230\nv_xor_b32 v167,v167,v231\nv_or_b32 v166,v166,v167\nv_cmp_ne_u32 vcc,0,v166\ns_nop 0\ns_cbranch_vccnz .Lhs\ns_branch .Lhb\n.Lps0:\n" \
"s_sub_u32 s73,s73,1\ns_cbranch_scc1 .Lpb0\ns_sleep 1\nbuffer_load_dword v248,v228,s[52:55],s80 offen sc1\ns_waitcnt vmcnt(0)\nv_cmp_eq_u32 vcc,0,v248\ns_nop 0\ns_cbranch_vccnz .Lps0\n" \
"s_branch .Lpb0\n.Lps1:\ns_sub_u32 s73,s73,1\ns_cbranch_scc1 .Lpb1\ns_sleep 1\nbuffer_load_dword v248,v228,s[52:55],s80 offen sc1\ns_waitcnt vmcnt(0)\nv_cmp_eq_u32 vcc,0,v248\n" \
"s_nop 0\ns_cbranch_vccnz .Lps1\ns_branch .Lpb1\n.Lps2:\ns_sub_u32 s73,s73,1\ns_cbranch_scc1 .Lpb2\ns_sleep 1\nbuffer_load_dword v248,v251,s[52:55],s80 offen sc1\n" \
"s_waitcnt vmcnt(0)\nv_cmp_eq_u32 vcc,0,v248\ns_nop 0\ns_cbranch_vccnz .Lps2\ns_branch .Lpb2\n.Lps3:\ns_sub_u32 s73,s73,1\ns_cbranch_scc1 .Lpb3\n" \
"s_sleep 1\nbuffer_load_dword v248,v251,s[52:55],s80 offen sc1\ns_waitcnt vmcnt(0)\nv_cmp_eq_u32 vcc,0,v248\ns_nop 0\ns_cbranch_vccnz .Lps3\ns_branch .Lpb3\n.Lps4:\n" \
"s_sub_u32 s73,s73,1\ns_cbranch_scc1 .Lpb4\ns_sleep 1\nbuffer_load_dword v248,v251,s[52:55],s80 offen sc1\ns_waitcnt vmcnt(0)\nv_cmp_eq_u32 vcc,0,v248\ns_nop 0\ns_cbranch_vccnz .Lps4\n" \
"s_branch .Lpb4\n.Lps5:\ns_sub_u32 s73,s73,1\ns_cbranch_scc1 .Lpb5\ns_sleep 1\nbuffer_load_dword v248,v251,s[52:55],s80 offen sc1\ns_waitcnt vmcnt(0)\nv_cmp_eq_u32 vcc,0,v248\n" \
"s_nop 0\ns_cbranch_vccnz .Lps5\ns_branch .Lpb5\n.Lps6:\ns_sub_u32 s73,s73,1\ns_cbranch_scc1 .Lpb6\ns_sleep 1\nbuffer_load_dword v248,v251,s[52:55],s80 offen sc1\n" \
"s_waitcnt vmcnt(0)\nv_and_b32 v248,s17,v248\nv_cmp_eq_u32 vcc,0,v248\ns_nop 0\ns_cbranch_vccnz .Lps6\ns_branch .Lpb6\n.Lps7:\ns_sub_u32 s73,s73,1\n" \
"s_cbranch_scc1 .Lpb7\ns_sleep 1\nbuffer_load_dword v248,v228,s[52:55],s80 offen sc1\ns_waitcnt vmcnt(0)\nv_cmp_eq_u32 vcc,0,v248\ns_nop 0\ns_cbranch_vccnz .Lps7\ns_branch .Lpb7\n" \
".Lps8:\ns_sub_u32 s73,s73,1\ns_cbranch_scc1 .Lpb8\ns_sleep 1\nbuffer_load_dword v248,v251,s[52:55],s79 offen sc1\ns_waitcnt vmcnt(0)\nv_cmp_eq_u32 vcc,0,v248\ns_nop 0\n" \
"s_cbranch_vccnz .Lps8\ns_branch .Lpb8\n.Lps9:\ns_sub_u32 s73,s73,1\ns_cbranch_scc1 .Lpb9\ns_sleep 1\nbuffer_load_dword v248,v251,s[52:55],s80 offen sc1\ns_waitcnt vmcnt(0)\n" \
"v_and_b32 v248,s17,v248\nv_cmp_eq_u32 vcc,0,v248\ns_nop 0\ns_cbranch_vccnz .Lps9\ns_branch .Lpb9\n.Lps10:\ns_sub_u32 s73,s73,1\ns_cbranch_scc1 .Lpb10\n" \
"s_sleep 1\nbuffer_load_dword v248,v228,s[52:55],s80 offen sc1\ns_waitcnt vmcnt(0)\nv_cmp_eq_u32 vcc,0,v248\ns_nop 0\ns_cbranch_vccnz .Lps10\ns_branch .Lpb10\n.Lps11:\n" \
"s_sub_u32 s73,s73,1\ns_cbranch_scc1 .Lpb11\ns_sleep 1\nbuffer_load_dword v248,v251,s[52:55],s79 offen sc1\ns_waitcnt vmcnt(0)\nv_cmp_eq_u32 vcc,0,v248\ns_nop 0\ns_cbranch_vccnz .Lps11\n" \
"s_branch .Lpb11\n.Lrs1:\ns_mov_b32 s71,0\n.Lrl1:\ns_mov_b32 s97,0\nv_max3_u32 v124,v160,v161,v162\nv_max3_u32 v124,v124,v163,v163\nv_cmp_eq_u32 vcc,-1,v124\n" \
"s_add_u32 s72,s95,0\ns_cbranch_vccz .Lrz10\ns_mov_b64 exec,vcc\ns_mov_b32 s97,1\nbuffer_load_dwordx4 v[160:163],v240,s[52:55],s72 offen sc1\ns_mov_b64 exec,-1\n.Lrz10:\nv_max3_u32 v124,v164,v165,v166\n" \
"v_max3_u32 v124,v124,v167,v167\nv_cmp_eq_u32 vcc,-1,v124\ns_add_u32 s72,s95,2097152\ns_cbranch_vccz .Lrz11\ns_mov_b64 exec,vcc\ns_mov_b32 s97,1\nbuffer_load_dwordx4 v[164:167],v240,s[52:55],s72 offen sc1\ns_mov_b64 exec,-1\n" \
".Lrz11:\nv_max3_u32 v124,v168,v169,v170\nv_max3_u32 v124,v124,v171,v171\nv_cmp_eq_u32 vcc,-1,v124\ns_add_u32 s72,s95,4194304\ns_cbranch_vccz .Lrz12\ns_mov_b64 exec,vcc\ns_mov_b32 s97,1\n" \
"buffer_load_dwordx4 v[168:171],v240,s[52:55],s72 offen sc1\ns_mov_b64 exec,-1\n.Lrz12:\nv_max3_u32 v124,v172,v173,v174\nv_max3_u32 v124,v124,v175,v175\nv_cmp_eq_u32 vcc,-1,v124\ns_add_u32 s72,s95,6291456\ns_cbranch_vccz .Lrz13\n" \
"s_mov_b64 exec,vcc\ns_mov_b32 s97,1\nbuffer_load_dwordx4 v[172:175],v240,s[52:55],s72 offen sc1\ns_mov_b64 exec,-1\n.Lrz13:\nv_max3_u32 v124,v176,v177,v178\nv_max3_u32 v124,v124,v179,v179\nv_cmp_eq_u32 vcc,-1,v124\n" \
"s_add_u32 s72,s95,8388608\ns_cbranch_vccz .Lrz14\ns_mov_b64 exec,vcc\ns_mov_b32 s97,1\nbuffer_load_dwordx4 v[176:179],v240,s[52:55],s72 offen sc1\ns_mov_b64 exec,-1\n.Lrz14:\nv_max3_u32 v124,v180,v181,v182\n" \
"v_max3_u32 v124,v124,v183,v183\nv_cmp_eq_u32 vcc,-1,v124\ns_add_u32 s72,s95,10485760\ns_cbranch_vccz .Lrz15\ns_mov_b64 exec,vcc\ns_mov_b32 s97,1\nbuffer_load_dwordx4 v[180:183],v240,s[52:55],s72 offen sc1\ns_mov_b64 exec,-1\n" \
".Lrz15:\nv_max3_u32 v124,v184,v185,v186\nv_max3_u32 v124,v124,v187,v187\nv_cmp_eq_u32 vcc,-1,v124\ns_add_u32 s72,s95,12582912\ns_cbranch_vccz .Lrz16\ns_mov_b64 exec,vcc\ns_mov_b32 s97,1\n" \
"buffer_load_dwordx4 v[184:187],v240,s[52:55],s72 offen sc1\ns_mov_b64 exec,-1\n.Lrz16:\nv_max3_u32 v124,v188,v189,v190\nv_max3_u32 v124,v124,v191,v191\nv_cmp_eq_u32 vcc,-1,v124\ns_add_u32 s72,s95,14680064\ns_cbranch_vccz .Lrz17\n" \
"s_mov_b64 exec,vcc\ns_mov_b32 s97,1\nbuffer_load_dwordx4 v[188:191],v240,s[52:55],s72 offen sc1\ns_mov_b64 exec,-1\n.Lrz17:\ns_cmp_lg_u32 s97,0\ns_cbranch_scc1 .Lrm1\ns_cmp_eq_u32 s71,0\n" \
"s_cbranch_scc1 .Lrb1\ns_branch .Lrow1\n.Lrm1:\ns_mov_b32 s71,1\ns_waitcnt vmcnt(0)\ns_sub_u32 s73,s73,1\ns_cbranch_scc1 .Lrb1\ns_sleep 2\n" \
"s_branch .Lrl1\n.Lrs0:\ns_mov_b32 s71,0\n.Lrl0:\ns_mov_b32 s97,0\nv_max3_u32 v60,v128,v129,v130\nv_max3_u32 v60,v60,v131,v131\nv_cmp_eq_u32 vcc,-1,v60\n" \
"s_add_u32 s72,s74,0\ns_cbranch_vccz .Lrz00\ns_mov_b64 exec,vcc\ns_mov_b32 s97,1\nbuffer_load_dwordx4 v[128:131],v240,s[52:55],s72 offen sc1\ns_mov_b64 exec,-1\n.Lrz00:\nv_max3_u32 v60,v132,v133,v134\n" \
"v_max3_u32 v60,v60,v135,v135\nv_cmp_eq_u32 vcc,-1,v60\ns_add_u32 s72,s74,2097152\ns_cbranch_vccz .Lrz01\ns_mov_b64 exec,vcc\ns_mov_b32 s97,1\nbuffer_load_dwordx4 v[132:135],v240,s[52:55],s72 offen sc1\ns_mov_b64 exec,-1\n" \
".Lrz01:\nv_max3_u32 v60,v136,v137,v138\nv_max3_u32 v60,v60,v139,v139\nv_cmp_eq_u32 vcc,-1,v60\ns_add_u32 s72,s74,4194304\ns_cbranch_vccz .Lrz02\ns_mov_b64 exec,vcc\ns_mov_b32 s97,1\n" \
"buffer_load_dwordx4 v[136:139],v240,s[52:55],s72 offen sc1\ns_mov_b64 exec,-1\n.Lrz02:\nv_max3_u32 v60,v140,v141,v142\nv_max3_u32 v60,v60,v143,v143\nv_cmp_eq_u32 vcc,-1,v60\ns_add_u32 s72,s74,6291456\ns_cbranch_vccz .Lrz03\n" \
"s_mov_b64 exec,vcc\ns_mov_b32 s97,1\nbuffer_load_dwordx4 v[140:143],v240,s[52:55],s72 offen sc1\ns_mov_b64 exec,-1\n.Lrz03:\nv_max3_u32 v60,v144,v145,v146\nv_max3_u32 v60,v60,v147,v147\nv_cmp_eq_u32 vcc,-1,v60\n" \
"s_add_u32 s72,s74,8388608\ns_cbranch_vccz .Lrz04\ns_mov_b64 exec,vcc\ns_mov_b32 s97,1\nbuffer_load_dwordx4 v[144:147],v240,s[52:55],s72 offen sc1\ns_mov_b64 exec,-1\n.Lrz04:\nv_max3_u32 v60,v148,v149,v150\n" \
"v_max3_u32 v60,v60,v151,v151\nv_cmp_eq_u32 vcc,-1,v60\ns_add_u32 s72,s74,10485760\ns_cbranch_vccz .Lrz05\ns_mov_b64 exec,vcc\ns_mov_b32 s97,1\nbuffer_load_dwordx4 v[148:151],v240,s[52:55],s72 offen sc1\ns_mov_b64 exec,-1\n" \
".Lrz05:\nv_max3_u32 v60,v152,v153,v154\nv_max3_u32 v60,v60,v155,v155\nv_cmp_eq_u32 vcc,-1,v60\ns_add_u32 s72,s74,12582912\ns_cbranch_vccz .Lrz06\ns_mov_b64 exec,vcc\ns_mov_b32 s97,1\n" \
"buffer_load_dwordx4 v[152:155],v240,s[52:55],s72 offen sc1\ns_mov_b64 exec,-1\n.Lrz06:\nv_max3_u32 v60,v156,v157,v158\nv_max3_u32 v60,v60,v159,v159\nv_cmp_eq_u32 vcc,-1,v60\ns_add_u32 s72,s74,14680064\ns_cbranch_vccz .Lrz07\n" \
"s_mov_b64 exec,vcc\ns_mov_b32 s97,1\nbuffer_load_dwordx4 v[156:159],v240,s[52:55],s72 offen sc1\ns_mov_b64 exec,-1\n.Lrz07:\ns_cmp_lg_u32 s97,0\ns_cbranch_scc1 .Lrm0\ns_cmp_eq_u32 s71,0\n" \
"s_cbranch_scc1 .Lrb0\ns_branch .Lrow0\n.Lrm0:\ns_mov_b32 s71,1\ns_waitcnt vmcnt(0)\ns_sub_u32 s73,s73,1\ns_cbranch_scc1 .Lrb0\ns_sleep 2\n" \
"s_branch .Lrl0\n.Lredo:\ns_waitcnt vmcnt(0) lgkmcnt(0)\ns_barrier\ns_mov_b64 s[90:91],0\ns_mov_b32 s99,0\ns_mov_b32 s17,1\ns_lshl_b32 s70,s63,19\n" \
"s_add_u32 s37,s70,3145728\ns_lshl_b32 s71,s62,15\ns_add_u32 s72,s37,s64\ns_add_u32 s72,s72,491520\ns_sub_u32 s72,s72,s71\ns_add_u32 m0,s64,0\ns_nop 0\nbuffer_load_dwordx4 v240,s[52:55],s72 offen offset:0 sc1 lds\n" \
"buffer_load_dwordx4 v240,s[52:55],s72 offen offset:1024 sc1 lds\nbuffer_load_dwordx4 v240,s[52:55],s72 offen offset:2048 sc1 lds\nbuffer_load_dwordx4 v240,s[52:55],s72 offen offset:3072 sc1 lds\ns_add_u32 m0,s64,65536\ns_add_u32 s72,s72,16777216\nbuffer_load_dwordx4 v240,s[52:55],s72 offen offset:0 sc1 lds\nbuffer_load_dwordx4 v240,s[52:55],s72 offen offset:1024 sc1 lds\nbuffer_load_dwordx4 v240,s[52:55],s72 offen offset:2048 sc1 lds\n" \
"buffer_load_dwordx4 v240,s[52:55],s72 offen offset:3072 sc1 lds\ns_add_u32 s72,s37,s64\ns_add_u32 s72,s72,507904\ns_sub_u32 s72,s72,s71\ns_add_u32 m0,s64,16384\ns_nop 0\nbuffer_load_dwordx4 v240,s[52:55],s72 offen offset:0 sc1 lds\nbuffer_load_dwordx4 v240,s[52:55],s72 offen offset:1024 sc1 lds\n" \
"buffer_load_dwordx4 v240,s[52:55],s72 offen offset:2048 sc1 lds\nbuffer_load_dwordx4 v240,s[52:55],s72 offen offset:3072 sc1 lds\ns_add_u32 m0,s64,81920\ns_add_u32 s72,s72,16777216\nbuffer_load_dwordx4 v240,s[52:55],s72 offen offset:0 sc1 lds\nbuffer_load_dwordx4 v240,s[52:55],s72 offen offset:1024 sc1 lds\nbuffer_load_dwordx4 v240,s[52:55],s72 offen offset:2048 sc1 lds\nbuffer_load_dwordx4 v240,s[52:55],s72 offen offset:3072 sc1 lds\n" \
"s_add_u32 s72,s37,s64\ns_add_u32 s72,s72,16384\ns_add_u32 s72,s72,s71\ns_add_u32 m0,s64,32768\ns_nop 0\nbuffer_load_dwordx4 v240,s[52:55],s72 offen offset:0 sc1 lds\nbuffer_load_dwordx4 v240,s[52:55],s72 offen offset:1024 sc1 lds\nbuffer_load_dwordx4 v240,s[52:55],s72 offen offset:2048 sc1 lds\n" \
"buffer_load_dwordx4 v240,s[52:55],s72 offen offset:3072 sc1 lds\ns_add_u32 m0,s64,98304\ns_add_u32 s72,s72,16777216\nbuffer_load_dwordx4 v240,s[52:55],s72 offen offset:0 sc1 lds\nbuffer_load_dwordx4 v240,s[52:55],s72 offen offset:1024 sc1 lds\nbuffer_load_dwordx4 v240,s[52:55],s72 offen offset:2048 sc1 lds\nbuffer_load_dwordx4 v240,s[52:55],s72 offen offset:3072 sc1 lds\ns_add_u32 s72,s37,s64\n" \
"s_add_u32 s72,s72,0\ns_add_u32 s72,s72,s71\ns_add_u32 m0,s64,49152\ns_nop 0\nbuffer_load_dwordx4 v240,s[52:55],s72 offen offset:0 sc1 lds\nbuffer_load_dwordx4 v240,s[52:55],s72 offen offset:1024 sc1 lds\nbuffer_load_dwordx4 v240,s[52:55],s72 offen offset:2048 sc1 lds\nbuffer_load_dwordx4 v240,s[52:55],s72 offen offset:3072 sc1 lds\n" \
"s_add_u32 m0,s64,114688\ns_add_u32 s72,s72,16777216\nbuffer_load_dwordx4 v240,s[52:55],s72 offen offset:0 sc1 lds\nbuffer_load_dwordx4 v240,s[52:55],s72 offen offset:1024 sc1 lds\nbuffer_load_dwordx4 v240,s[52:55],s72 offen offset:2048 sc1 lds\nbuffer_load_dwordx4 v240,s[52:55],s72 offen offset:3072 sc1 lds\nv_and_b32 v251,15,v249\nv_lshrrev_b32 v250,4,v249\n" \
"v_lshlrev_b32 v250,2,v250\nv_sub_u32 v251,v251,v250\ns_lshl_b32 s70,s62,7\ns_lshl_b32 s72,s7,5\ns_sub_u32 s38,s72,s70\ns_add_u32 s38,s38,1920\ns_sub_u32 s39,s70,s72\ns_add_u32 s39,s39,96\n" \
"v_add_u32 v225,s38,v251\nv_add_u32 v225,0,v225\nv_mov_b32 v224,0xff800000\nv_add_u32 v229,s38,v251\nv_add_u32 v229,16,v229\nv_mov_b32 v228,0xff800000\nv_add_u32 v233,s39,v251\nv_add_u32 v233,0,v233\n" \
"v_mov_b32 v232,0xff800000\nv_add_u32 v237,s39,v251\nv_add_u32 v237,16,v237\nv_mov_b32 v236,0xff800000\ns_sub_u32 s38,524288,s71\ns_add_u32 s38,s38,s37\n.Lrl:\nbuffer_load_dwordx4 a[0:3],v240,s[52:55],s37 offen offset:0 sc1\n" \
"buffer_load_dwordx4 a[4:7],v240,s[52:55],s37 offen offset:1024 sc1\nbuffer_load_dwordx4 a[8:11],v240,s[52:55],s37 offen offset:2048 sc1\nbuffer_load_dwordx4 a[12:15],v240,s[52:55],s37 offen offset:3072 sc1\ns_add_u32 s37,s37,4096\nbuffer_load_dwordx4 a[16:19],v240,s[52:55],s37 offen offset:0 sc1\nbuffer_load_dwordx4 a[20:23],v240,s[52:55],s37 offen offset:1024 sc1\nbuffer_load_dwordx4 a[24:27],v240,s[52:55],s37 offen offset:2048 sc1\nbuffer_load_dwordx4 a[28:31],v240,s[52:55],s37 offen offset:3072 sc1\n" \
"s_add_u32 s37,s37,4096\nbuffer_load_dwordx4 a[32:35],v240,s[52:55],s37 offen offset:0 sc1\nbuffer_load_dwordx4 a[36:39],v240,s[52:55],s37 offen offset:1024 sc1\nbuffer_load_dwordx4 a[40:43],v240,s[52:55],s37 offen offset:2048 sc1\nbuffer_load_dwordx4 a[44:47],v240,s[52:55],s37 offen offset:3072 sc1\ns_add_u32 s37,s37,4096\nbuffer_load_dwordx4 a[48:51],v240,s[52:55],s37 offen offset:0 sc1\nbuffer_load_dwordx4 a[52:55],v240,s[52:55],s37 offen offset:1024 sc1\n" \
"buffer_load_dwordx4 a[56:59],v240,s[52:55],s37 offen offset:2048 sc1\nbuffer_load_dwordx4 a[60:63],v240,s[52:55],s37 offen offset:3072 sc1\ns_add_u32 s37,s37,4096\ns_waitcnt vmcnt(15)\nM v[128:131],a[0:3],a[128:131],0\ns_waitcnt vmcnt(14)\nM v[128:131],a[4:7],a[132:135],v[128:131]\ns_waitcnt vmcnt(13)\n" \
"M v[128:131],a[8:11],a[136:139],v[128:131]\ns_waitcnt vmcnt(12)\nM v[128:131],a[12:15],a[140:143],v[128:131]\nM v[132:135],a[0:3],a[144:147],0\nM v[132:135],a[4:7],a[148:151],v[132:135]\nM v[132:135],a[8:11],a[152:155],v[132:135]\nM v[132:135],a[12:15],a[156:159],v[132:135]\nM v[136:139],a[0:3],a[160:163],0\n" \
"M v[136:139],a[4:7],a[164:167],v[136:139]\nM v[136:139],a[8:11],a[168:171],v[136:139]\nM v[136:139],a[12:15],a[172:175],v[136:139]\nM v[140:143],a[0:3],a[176:179],0\nM v[140:143],a[4:7],a[180:183],v[140:143]\nM v[140:143],a[8:11],a[184:187],v[140:143]\nM v[140:143],a[12:15],a[188:191],v[140:143]\ns_waitcnt vmcnt(11)\n" \
"M v[144:147],a[16:19],a[128:131],0\ns_waitcnt vmcnt(10)\nM v[144:147],a[20:23],a[132:135],v[144:147]\ns_waitcnt vmcnt(9)\nM v[144:147],a[24:27],a[136:139],v[144:147]\ns_waitcnt vmcnt(8)\nM v[144:147],a[28:31],a[140:143],v[144:147]\nM v[148:151],a[16:19],a[144:147],0\n" \
"M v[148:151],a[20:23],a[148:151],v[148:151]\nM v[148:151],a[24:27],a[152:155],v[148:151]\nM v[148:151],a[28:31],a[156:159],v[148:151]\nM v[152:155],a[16:19],a[160:163],0\nM v[152:155],a[20:23],a[164:167],v[152:155]\nM v[152:155],a[24:27],a[168:171],v[152:155]\nM v[152:155],a[28:31],a[172:175],v[152:155]\nM v[156:159],a[16:19],a[176:179],0\n" \
"M v[156:159],a[20:23],a[180:183],v[156:159]\nM v[156:159],a[24:27],a[184:187],v[156:159]\nM v[156:159],a[28:31],a[188:191],v[156:159]\ns_waitcnt vmcnt(7)\nM v[160:163],a[32:35],a[128:131],0\ns_waitcnt vmcnt(6)\nM v[160:163],a[36:39],a[132:135],v[160:163]\ns_waitcnt vmcnt(5)\n" \
"M v[160:163],a[40:43],a[136:139],v[160:163]\ns_waitcnt vmcnt(4)\nM v[160:163],a[44:47],a[140:143],v[160:163]\nM v[164:167],a[32:35],a[144:147],0\nM v[164:167],a[36:39],a[148:151],v[164:167]\nM v[164:167],a[40:43],a[152:155],v[164:167]\nM v[164:167],a[44:47],a[156:159],v[164:167]\nM v[168:171],a[32:35],a[160:163],0\n" \
"M v[168:171],a[36:39],a[164:167],v[168:171]\nM v[168:171],a[40:43],a[168:171],v[168:171]\nM v[168:171],a[44:47],a[172:175],v[168:171]\nM v[172:175],a[32:35],a[176:179],0\nM v[172:175],a[36:39],a[180:183],v[172:175]\nM v[172:175],a[40:43],a[184:187],v[172:175]\nM v[172:175],a[44:47],a[188:191],v[172:175]\ns_waitcnt vmcnt(3)\n" \
"M v[176:179],a[48:51],a[128:131],0\ns_waitcnt vmcnt(2)\nM v[176:179],a[52:55],a[132:135],v[176:179]\ns_waitcnt vmcnt(1)\nM v[176:179],a[56:59],a[136:139],v[176:179]\ns_waitcnt vmcnt(0)\nM v[176:179],a[60:63],a[140:143],v[176:179]\nM v[180:183],a[48:51],a[144:147],0\n" \
"M v[180:183],a[52:55],a[148:151],v[180:183]\nM v[180:183],a[56:59],a[152:155],v[180:183]\nM v[180:183],a[60:63],a[156:159],v[180:183]\nM v[184:187],a[48:51],a[160:163],0\nM v[184:187],a[52:55],a[164:167],v[184:187]\nM v[184:187],a[56:59],a[168:171],v[184:187]\nM v[184:187],a[60:63],a[172:175],v[184:187]\nM v[188:191],a[48:51],a[176:179],0\n" \
"M v[188:191],a[52:55],a[180:183],v[188:191]\nM v[188:191],a[56:59],a[184:187],v[188:191]\nM v[188:191],a[60:63],a[188:191],v[188:191]\nv_cmp_le_i32 vcc,0,v225\ns_nop 1\nv_cndmask_b32 v128,v252,v128,vcc\nv_max_f32 v224,v224,v128\nv_cmp_le_i32 vcc,1,v225\n" \
"s_nop 1\nv_cndmask_b32 v129,v252,v129,vcc\nv_max_f32 v224,v224,v129\nv_cmp_le_i32 vcc,2,v225\ns_nop 1\nv_cndmask_b32 v130,v252,v130,vcc\nv_max_f32 v224,v224,v130\nv_cmp_le_i32 vcc,3,v225\n" \
"s_nop 1\nv_cndmask_b32 v131,v252,v131,vcc\nv_max_f32 v224,v224,v131\nv_cmp_le_i32 vcc,0,v229\ns_nop 1\nv_cndmask_b32 v132,v252,v132,vcc\nv_max_f32 v228,v228,v132\nv_cmp_le_i32 vcc,1,v229\n" \
"s_nop 1\nv_cndmask_b32 v133,v252,v133,vcc\nv_max_f32 v228,v228,v133\nv_cmp_le_i32 vcc,2,v229\ns_nop 1\nv_cndmask_b32 v134,v252,v134,vcc\nv_max_f32 v228,v228,v134\nv_cmp_le_i32 vcc,3,v229\n" \
"s_nop 1\nv_cndmask_b32 v135,v252,v135,vcc\nv_max_f32 v228,v228,v135\nv_cmp_le_i32 vcc,0,v233\ns_nop 1\nv_cndmask_b32 v136,v252,v136,vcc\nv_max_f32 v232,v232,v136\nv_cmp_le_i32 vcc,1,v233\n" \
"s_nop 1\nv_cndmask_b32 v137,v252,v137,vcc\nv_max_f32 v232,v232,v137\nv_cmp_le_i32 vcc,2,v233\ns_nop 1\nv_cndmask_b32 v138,v252,v138,vcc\nv_max_f32 v232,v232,v138\nv_cmp_le_i32 vcc,3,v233\n" \
"s_nop 1\nv_cndmask_b32 v139,v252,v139,vcc\nv_max_f32 v232,v232,v139\nv_cmp_le_i32 vcc,0,v237\ns_nop 1\nv_cndmask_b32 v140,v252,v140,vcc\nv_max_f32 v236,v236,v140\nv_cmp_le_i32 vcc,1,v237\n" \
"s_nop 1\nv_cndmask_b32 v141,v252,v141,vcc\nv_max_f32 v236,v236,v141\nv_cmp_le_i32 vcc,2,v237\ns_nop 1\nv_cndmask_b32 v142,v252,v142,vcc\nv_max_f32 v236,v236,v142\nv_cmp_le_i32 vcc,3,v237\n" \
"s_nop 1\nv_cndmask_b32 v143,v252,v143,vcc\nv_max_f32 v236,v236,v143\nv_cmp_le_i32 vcc,16,v225\ns_nop 1\nv_cndmask_b32 v144,v252,v144,vcc\nv_max_f32 v224,v224,v144\nv_cmp_le_i32 vcc,17,v225\n" \
"s_nop 1\nv_cndmask_b32 v145,v252,v145,vcc\nv_max_f32 v224,v224,v145\nv_cmp_le_i32 vcc,18,v225\ns_nop 1\nv_cndmask_b32 v146,v252,v146,vcc\nv_max_f32 v224,v224,v146\nv_cmp_le_i32 vcc,19,v225\n" \
"s_nop 1\nv_cndmask_b32 v147,v252,v147,vcc\nv_max_f32 v224,v224,v147\nv_cmp_le_i32 vcc,16,v229\ns_nop 1\nv_cndmask_b32 v148,v252,v148,vcc\nv_max_f32 v228,v228,v148\nv_cmp_le_i32 vcc,17,v229\n" \
"s_nop 1\nv_cndmask_b32 v149,v252,v149,vcc\nv_max_f32 v228,v228,v149\nv_cmp_le_i32 vcc,18,v229\ns_nop 1\nv_cndmask_b32 v150,v252,v150,vcc\nv_max_f32 v228,v228,v150\nv_cmp_le_i32 vcc,19,v229\n" \
"s_nop 1\nv_cndmask_b32 v151,v252,v151,vcc\nv_max_f32 v228,v228,v151\nv_cmp_le_i32 vcc,16,v233\ns_nop 1\nv_cndmask_b32 v152,v252,v152,vcc\nv_max_f32 v232,v232,v152\nv_cmp_le_i32 vcc,17,v233\n" \
"s_nop 1\nv_cndmask_b32 v153,v252,v153,vcc\nv_max_f32 v232,v232,v153\nv_cmp_le_i32 vcc,18,v233\ns_nop 1\nv_cndmask_b32 v154,v252,v154,vcc\nv_max_f32 v232,v232,v154\nv_cmp_le_i32 vcc,19,v233\n" \
"s_nop 1\nv_cndmask_b32 v155,v252,v155,vcc\nv_max_f32 v232,v232,v155\nv_cmp_le_i32 vcc,16,v237\ns_nop 1\nv_cndmask_b32 v156,v252,v156,vcc\nv_max_f32 v236,v236,v156\nv_cmp_le_i32 vcc,17,v237\n" \
"s_nop 1\nv_cndmask_b32 v157,v252,v157,vcc\nv_max_f32 v236,v236,v157\nv_cmp_le_i32 vcc,18,v237\ns_nop 1\nv_cndmask_b32 v158,v252,v158,vcc\nv_max_f32 v236,v236,v158\nv_cmp_le_i32 vcc,19,v237\n" \
"s_nop 1\nv_cndmask_b32 v159,v252,v159,vcc\nv_max_f32 v236,v236,v159\nv_cmp_le_i32 vcc,32,v225\ns_nop 1\nv_cndmask_b32 v160,v252,v160,vcc\nv_max_f32 v224,v224,v160\nv_cmp_le_i32 vcc,33,v225\n" \
"s_nop 1\nv_cndmask_b32 v161,v252,v161,vcc\nv_max_f32 v224,v224,v161\nv_cmp_le_i32 vcc,34,v225\ns_nop 1\nv_cndmask_b32 v162,v252,v162,vcc\nv_max_f32 v224,v224,v162\nv_cmp_le_i32 vcc,35,v225\n" \
"s_nop 1\nv_cndmask_b32 v163,v252,v163,vcc\nv_max_f32 v224,v224,v163\nv_cmp_le_i32 vcc,32,v229\ns_nop 1\nv_cndmask_b32 v164,v252,v164,vcc\nv_max_f32 v228,v228,v164\nv_cmp_le_i32 vcc,33,v229\n" \
"s_nop 1\nv_cndmask_b32 v165,v252,v165,vcc\nv_max_f32 v228,v228,v165\nv_cmp_le_i32 vcc,34,v229\ns_nop 1\nv_cndmask_b32 v166,v252,v166,vcc\nv_max_f32 v228,v228,v166\nv_cmp_le_i32 vcc,35,v229\n" \
"s_nop 1\nv_cndmask_b32 v167,v252,v167,vcc\nv_max_f32 v228,v228,v167\nv_cmp_le_i32 vcc,32,v233\ns_nop 1\nv_cndmask_b32 v168,v252,v168,vcc\nv_max_f32 v232,v232,v168\nv_cmp_le_i32 vcc,33,v233\n" \
"s_nop 1\nv_cndmask_b32 v169,v252,v169,vcc\nv_max_f32 v232,v232,v169\nv_cmp_le_i32 vcc,34,v233\ns_nop 1\nv_cndmask_b32 v170,v252,v170,vcc\nv_max_f32 v232,v232,v170\nv_cmp_le_i32 vcc,35,v233\n" \
"s_nop 1\nv_cndmask_b32 v171,v252,v171,vcc\nv_max_f32 v232,v232,v171\nv_cmp_le_i32 vcc,32,v237\ns_nop 1\nv_cndmask_b32 v172,v252,v172,vcc\nv_max_f32 v236,v236,v172\nv_cmp_le_i32 vcc,33,v237\n" \
"s_nop 1\nv_cndmask_b32 v173,v252,v173,vcc\nv_max_f32 v236,v236,v173\nv_cmp_le_i32 vcc,34,v237\ns_nop 1\nv_cndmask_b32 v174,v252,v174,vcc\nv_max_f32 v236,v236,v174\nv_cmp_le_i32 vcc,35,v237\n" \
"s_nop 1\nv_cndmask_b32 v175,v252,v175,vcc\nv_max_f32 v236,v236,v175\nv_cmp_le_i32 vcc,48,v225\ns_nop 1\nv_cndmask_b32 v176,v252,v176,vcc\nv_max_f32 v224,v224,v176\nv_cmp_le_i32 vcc,49,v225\n" \
"s_nop 1\nv_cndmask_b32 v177,v252,v177,vcc\nv_max_f32 v224,v224,v177\nv_cmp_le_i32 vcc,50,v225\ns_nop 1\nv_cndmask_b32 v178,v252,v178,vcc\nv_max_f32 v224,v224,v178\nv_cmp_le_i32 vcc,51,v225\n" \
"s_nop 1\nv_cndmask_b32 v179,v252,v179,vcc\nv_max_f32 v224,v224,v179\nv_cmp_le_i32 vcc,48,v229\ns_nop 1\nv_cndmask_b32 v180,v252,v180,vcc\nv_max_f32 v228,v228,v180\nv_cmp_le_i32 vcc,49,v229\n" \
"s_nop 1\nv_cndmask_b32 v181,v252,v181,vcc\nv_max_f32 v228,v228,v181\nv_cmp_le_i32 vcc,50,v229\ns_nop 1\nv_cndmask_b32 v182,v252,v182,vcc\nv_max_f32 v228,v228,v182\nv_cmp_le_i32 vcc,51,v229\n" \
"s_nop 1\nv_cndmask_b32 v183,v252,v183,vcc\nv_max_f32 v228,v228,v183\nv_cmp_le_i32 vcc,48,v233\ns_nop 1\nv_cndmask_b32 v184,v252,v184,vcc\nv_max_f32 v232,v232,v184\nv_cmp_le_i32 vcc,49,v233\n" \
"s_nop 1\nv_cndmask_b32 v185,v252,v185,vcc\nv_max_f32 v232,v232,v185\nv_cmp_le_i32 vcc,50,v233\ns_nop 1\nv_cndmask_b32 v186,v252,v186,vcc\nv_max_f32 v232,v232,v186\nv_cmp_le_i32 vcc,51,v233\n" \
"s_nop 1\nv_cndmask_b32 v187,v252,v187,vcc\nv_max_f32 v232,v232,v187\nv_cmp_le_i32 vcc,48,v237\ns_nop 1\nv_cndmask_b32 v188,v252,v188,vcc\nv_max_f32 v236,v236,v188\nv_cmp_le_i32 vcc,49,v237\n" \
"s_nop 1\nv_cndmask_b32 v189,v252,v189,vcc\nv_max_f32 v236,v236,v189\nv_cmp_le_i32 vcc,50,v237\ns_nop 1\nv_cndmask_b32 v190,v252,v190,vcc\nv_max_f32 v236,v236,v190\nv_cmp_le_i32 vcc,51,v237\n" \
"s_nop 1\nv_cndmask_b32 v191,v252,v191,vcc\nv_max_f32 v236,v236,v191\nv_subrev_u32 v225,64,v225\nv_subrev_u32 v229,64,v229\nv_subrev_u32 v233,64,v233\nv_subrev_u32 v237,64,v237\ns_cmp_lt_u32 s37,s38\n" \
"s_cbranch_scc1 .Lrl\ns_waitcnt vmcnt(0)\ns_barrier\nds_read_b128 a[0:3],v240 offset:0\nds_read_b128 a[4:7],v240 offset:1024\nds_read_b128 a[8:11],v240 offset:2048\nds_read_b128 a[12:15],v240 offset:3072\nds_read_b128 a[16:19],v240 offset:4096\n" \
"ds_read_b128 a[20:23],v240 offset:5120\nds_read_b128 a[24:27],v240 offset:6144\nds_read_b128 a[28:31],v240 offset:7168\nds_read_b128 a[32:35],v240 offset:8192\nds_read_b128 a[36:39],v240 offset:9216\nds_read_b128 a[40:43],v240 offset:10240\nds_read_b128 a[44:47],v240 offset:11264\nds_read_b128 a[48:51],v240 offset:12288\n" \
"ds_read_b128 a[52:55],v240 offset:13312\nds_read_b128 a[56:59],v240 offset:14336\nds_read_b128 a[60:63],v240 offset:15360\ns_cmp_eq_u32 s62,0\ns_cselect_b32 s36,0x8000,0\ns_add_u32 s36,s36,s64\ns_cmp_eq_u32 s36,s96\ns_cselect_b32 s72,0xc000,s98\n" \
"s_add_u32 s65,s36,s72\ns_cmp_eq_u32 s65,s96\ns_cselect_b32 s72,0xc000,s98\ns_add_u32 s66,s65,s72\ns_cmp_eq_u32 s66,s96\ns_cselect_b32 s72,0xc000,s98\ns_add_u32 s95,s66,s72\ns_add_u32 s97,s96,98304\n" \
"s_add_u32 s15,s64,s46\ns_add_u32 s15,s15,32768\ns_mov_b32 s94,0xc000\nv_and_b32 v251,31,v249\nv_lshlrev_b32 v251,2,v251\ns_lshl_b32 s80,s63,7\ns_add_u32 s80,s80,86016\nv_add_u32 v250,0xc000,v241\n" \
"s_waitcnt lgkmcnt(0)\ns_branch .Lattn\n.Lfin:\n" \
""
typedef __attribute__((address_space(1))) unsigned gu32;
typedef __attribute__((address_space(1))) unsigned long long gu64;
#define RLX_AGENT __ATOMIC_RELAXED, __HIP_MEMORY_SCOPE_AGENT
constexpr size_t WS_HO = 65536, WS_DBG = (size_t)51 << 20, WS_PART = (size_t)52 << 20;
constexpr unsigned HO_FP = 8192, HO_P2 = 32768, HO_KR = HO_P2 + 224 * DM * 4, LDS_BYTES = 163840, WX = 131072, SPIN_CAP = 1u << 20;
#define PSLEEP 4
struct Args {
    const u16 *x, *ckv, *w_q, *w_uk, *w_uv, *w_o, *w_r, *wg, *wu, *wd, *cvec, *w_mod;
    char* ws; u16* out;
};
typedef const __attribute__((address_space(4))) Args* kargs_t;
DI void st16_dev(void* p, const u32x4& v) { asm volatile("global_store_dwordx4 %0, %1, off sc1\n\ts_nop 1" :: "v"(p), "v"(v) : "memory"); }
DI void ld16_dev(u32x4& v, const void* p) { asm volatile("global_load_dwordx4 %0, %1, off sc1" : "=v"(v) : "v"(p) : "memory"); }
DI void sum8(const unsigned* w, float& lo, float& hi) {
    lo = ((lo2f(w[0]) + lo2f(w[1])) + (lo2f(w[2]) + lo2f(w[3]))) + ((lo2f(w[4]) + lo2f(w[5])) + (lo2f(w[6]) + lo2f(w[7])));
    hi = ((hi2f(w[0]) + hi2f(w[1])) + (hi2f(w[2]) + hi2f(w[3]))) + ((hi2f(w[4]) + hi2f(w[5])) + (hi2f(w[6]) + hi2f(w[7])));
}
DI unsigned ord16(float r) { const unsigned u = __float_as_uint(r) >> 16; return (r != r) ? 0u : ((u & 0x8000u) ? (~u & 0xffffu) : (u | 0x8000u)); }
DI float ord16_val(unsigned o) { return __uint_as_float(((o & 0x8000u) ? (o & 0x7fffu) : (~o & 0xffffu)) << 16); }
#define STAMPD(i) do {} while (0)
#define C8(p, a) p #a "0", p #a "1", p #a "2", p #a "3", p #a "4", p #a "5", p #a "6", p #a "7", p #a "8", p #a "9"
#define C100(p, a) C8(p, a##0), C8(p, a##1), C8(p, a##2), C8(p, a##3), C8(p, a##4), C8(p, a##5), C8(p, a##6), C8(p, a##7), C8(p, a##8), C8(p, a##9)
#define C10_99(p) C8(p, 1), C8(p, 2), C8(p, 3), C8(p, 4), C8(p, 5), C8(p, 6), C8(p, 7), C8(p, 8), C8(p, 9)
#define C200_249(p) C8(p, 20), C8(p, 21), C8(p, 22), C8(p, 23), C8(p, 24)
#define BODY_CLOB "memory", "vcc", "scc", "s8", "s9", C8("s", 1), C8("s", 2), "s30", "s31", "s34", "s35", "s36", "s37", "s38", "s39", C8("s", 4), C8("s", 5), C8("s", 6), C8("s", 7), C8("s", 8), C8("s", 9), \
    C8("v", ), C10_99("v"), C8("v", 10), "v110", "v111", C8("v", 12), C8("v", 13), C8("v", 14), C8("v", 15), C8("v", 16), C8("v", 17), C8("v", 18), "v190", "v191", "v228", "v229", C8("v", 23), C8("v", 24), "v250", "v251", "v252", "v253", "v254", \
    C8("a", ), C10_99("a"), C100("a", 1), C200_249("a"), "a250", "a251", "a252", "a253", "a254", "a255"
typedef unsigned u32x8 __attribute__((ext_vector_type(8)));

__global__ __launch_bounds__(256, 1) void k_all(const Args a_) {
    extern __shared__ __attribute__((aligned(16))) char lds[];
    kargs_t ap = (kargs_t)__builtin_amdgcn_kernarg_segment_ptr(); unsigned bid = blockIdx.x, wid = rfl(threadIdx.x >> 6);
    u32x16 FW; u32x8 fd, MPR; u32x4 ooL, ooH, KY; unsigned sv_;
    asm volatile(BODY_ASM : "+{s[4:5]}"(ap), "+{s6}"(bid), "+{s7}"(wid), "={v[192:207]}"(FW), "={v[208:215]}"(fd), "={v[216:219]}"(ooL), "={v[220:223]}"(ooH), "={v[224:227]}"(KY), "={v[112:119]}"(MPR), "={v255}"(sv_) :: BODY_CLOB);
    bid = rfl(bid); wid = rfl(wid);
    char* const ws = ap->ws; char* const ho = ws + WS_HO; const unsigned lane = lane_id();
    const unsigned g = bid & 7u, idx = bid >> 3, j = idx & 3u, xx = idx >> 2, head = 4u * g + j, tid = wid * 64u + lane;
    const u16* __restrict__ x = ap->x; u16* const out = ap->out; const char* const part = ws + WS_PART;
    const unsigned rowL = xx * 128u + 4u * head + wid, rowH = (15u - xx) * 128u + 4u * head + wid;
    const bool ffn = idx < 28u; const unsigned fb = g * 28u + (ffn ? idx : 0u);
    const unsigned c0 = wid * 128u + (lane & 3u) * 32u + (lane >> 2);
    const unsigned kwl = KY[0], kwg = KY[1], kL = KY[2], kH = KY[3];
    u32x2* sk = (u32x2*)lds;
    float* red = (float*)(lds + 1024);
    float* red3 = (float*)(lds + 2048);
    float* fin = (float*)(lds + 4096);
    if (wid == 0) {
        unsigned kh = 0, kl = 0;
        for (unsigned spins = 0; spins < SPIN_CAP; ++spins) {
            u32x4 ka, kb; ld16_dev(ka, ho + HO_KR + g * 2048u + lane * 32u); ld16_dev(kb, ho + HO_KR + g * 2048u + lane * 32u + 16u);
            asm volatile("s_waitcnt vmcnt(0)" : "+v"(ka), "+v"(kb) :: "memory");
            if (lane == (bid >> 2)) { const unsigned o = bid & 3u;
                if (o == 0) { ka[0] = kwl; ka[1] = kwg; } if (o == 1) { ka[2] = kwl; ka[3] = kwg; } if (o == 2) { kb[0] = kwl; kb[1] = kwg; } if (o == 3) { kb[2] = kwl; kb[3] = kwg; } }
            const u32x2 p = ka[1] > ka[3] ? (u32x2){ka[0], ka[1]} : (u32x2){ka[2], ka[3]}, q = kb[1] > kb[3] ? (u32x2){kb[0], kb[1]} : (u32x2){kb[2], kb[3]};
            kh = p[1] > q[1] ? p[1] : q[1]; kl = p[1] > q[1] ? p[0] : q[0];
            if (__all(ka[1] != 0u && ka[3] != 0u && kb[1] != 0u && kb[3] != 0u)) break;
            __builtin_amdgcn_s_sleep(PSLEEP);
        }
        const unsigned mh = wave_umax(kh);
        if (kh == mh) sk[4] = (u32x2){kl, kh};
    }
    LDSBAR();
    const u32x2 key = sk[4];
    const unsigned sel = (2047u - ((key[1] >> 5) & 2047u)) & 2047u;
    STAMPD(14);
    unsigned h2w;
    {   unsigned p[8];
#pragma unroll
        for (int zz = 0; zz < 8; ++zz) p[zz] = __hip_atomic_load((gu32*)(part + ((size_t)zz * SEQ + sel) * (DM * 2) + 4u * tid), RLX_AGENT);
        const unsigned xa = x[(size_t)sel * DM + c0], xb = x[(size_t)sel * DM + c0 + 16u];
        if (key[1] != kwg) {
            if (kL == kwg) st16_dev(out + (size_t)rowL * DM + lane * 8, ooL);
            if (kH == kwg) st16_dev(out + (size_t)rowH * DM + lane * 8, ooH); }
        float a0, a1; sum8(p, a0, a1);
        const float m = hi2f(key[0]), rden = __builtin_amdgcn_rcpf(lo2f(key[0]));
#define MP(i) __uint_as_float(MPR[i])
        const float x10 = rb(lo2f(xa) + rb(MP(0) * rb(a0))), x11 = rb(lo2f(xb) + rb(MP(1) * rb(a1)));
        h2w = cvt2(rb(rb(rb(x10 - m) * rden) * rb(1.f + MP(4))) + MP(2), rb(rb(rb(x11 - m) * rden) * rb(1.f + MP(5))) + MP(3));
        if (bid < 64u) { fin[c0] = x10; fin[c0 + 16u] = x11; fin[512u + c0] = lo2f(h2w); fin[528u + c0] = hi2f(h2w); fin[1024u + c0] = MP(6); fin[1040u + c0] = MP(7); } }
    if (ffn) {
        float v[16];
#pragma unroll
        for (int mtx = 0; mtx < 2; ++mtx)
#pragma unroll
            for (int e = 0; e < 4; ++e) { const unsigned w0 = FW[8 * mtx + e], w1 = FW[8 * mtx + 4 + e];
                v[8 * mtx + 2 * e] = dot2bf(__builtin_amdgcn_perm(w1, w0, 0x05040100u), h2w, 0.f); v[8 * mtx + 2 * e + 1] = dot2bf(__builtin_amdgcn_perm(w1, w0, 0x07060302u), h2w, 0.f); }
        float v8[8], v4[4];
#pragma unroll
        for (int i = 0; i < 8; ++i) v8[i] = swap32_sum(v[i], v[8 + i]);
#pragma unroll
        for (int i = 0; i < 4; ++i) v4[i] = row16_sum(swap16_sum(v8[i], v8[4 + i]));
        if ((lane & 15u) == 0) *reinterpret_cast<f32x4*>(red + wid * 16u + (lane >> 4) * 4u) = (f32x4){v4[0], v4[1], v4[2], v4[3]};
        LDSBAR();
        const unsigned f8 = lane & 7u;
        const float gg = rb((red[f8] + red[16 + f8]) + (red[32 + f8] + red[48 + f8])), uu = rb((red[8 + f8] + red[24 + f8]) + (red[40 + f8] + red[56 + f8]));
        const float gu = rb(rb(gg / (1.f + __expf(-gg))) * uu);
        float a0 = 0.f, a1 = 0.f;
#pragma unroll
        for (int i = 0; i < 8; ++i) { const float gv = __builtin_bit_cast(float, __builtin_amdgcn_readlane(__builtin_bit_cast(int, gu), i)); a0 = fmaf(gv, lo2f(fd[i]), a0); a1 = fmaf(gv, hi2f(fd[i]), a1); }
        const unsigned long long o = ((unsigned long long)min(__float_as_uint(a1), 0xfffffffeu) << 32) | min(__float_as_uint(a0), 0xfffffffeu);
        __hip_atomic_store((gu64*)(ho + HO_P2) + fb * 256u + tid, o, RLX_AGENT);
    }
    STAMPD(15);
    if (bid < 64u) {
        const unsigned cg = g * 8u + idx;
        const char* gp = ho + HO_P2 + (tid >> 1) * 2048u + cg * 32u + (tid & 1u) * 16u;
        u32x4 ga_ = {}, gb_ = {};
        for (unsigned spins = 0; spins < SPIN_CAP; ++spins) {
            ld16_dev(ga_, gp); ld16_dev(gb_, gp + (tid < 192u ? 128u * 2048u : 0u));
            asm volatile("s_waitcnt vmcnt(0)" : "+v"(ga_), "+v"(gb_) :: "memory");
            const unsigned mx = max(max(max(ga_[0], ga_[1]), max(ga_[2], ga_[3])), max(max(gb_[0], gb_[1]), max(gb_[2], gb_[3])));
            if (__all(mx != 0xffffffffu)) break;
            __builtin_amdgcn_s_sleep(PSLEEP);
        }
        float s4[4];
#pragma unroll
        for (int i = 0; i < 4; ++i) { float s = __uint_as_float(ga_[i]) + (tid < 192u ? __uint_as_float(gb_[i]) : 0.f);
            s += DPPF(s, 0x4E); s += DPPF(s, 0x124); s += DPPF(s, 0x128);
            s = swap16_sum(s, s); s4[i] = swap32_sum(s, s); }
        if (lane < 2) *reinterpret_cast<f32x4*>(red3 + wid * 8u + lane * 4u) = (f32x4){s4[0], s4[1], s4[2], s4[3]};
        LDSBAR();
        if (tid < 4u) { const unsigned t = tid, cc = cg * 8u + 2u * t;
            const float s0 = (red3[2 * t] + red3[8 + 2 * t]) + (red3[16 + 2 * t] + red3[24 + 2 * t]), s1 = (red3[2 * t + 1] + red3[9 + 2 * t]) + (red3[17 + 2 * t] + red3[25 + 2 * t]);
            const float rsel = rb(1.f / (1.f + __expf(-ord16_val(key[1] >> 16))));
            const float y0 = rb(rb(s0) * rsel), y1 = rb(rb(s1) * rsel);
            const unsigned ow = cvt2(fin[cc] + rb(fin[1024u + cc] * rb(fin[512u + cc] + y0)), fin[cc + 1u] + rb(fin[1025u + cc] * rb(fin[513u + cc] + y1)));
            __hip_atomic_store((gu32*)(out + (size_t)sel * DM + cc), ow, RLX_AGENT);
        }
    }
    asm volatile(".Lcode_e:");
}
extern "C" void kernel_launch(void* const* d_in, const int* in_sizes, int n_in, void* d_out, int out_size, void* d_ws, size_t ws_size, hipStream_t stream) {
    Args a;
    a.x = (const u16*)d_in[0]; a.ckv = (const u16*)d_in[1]; a.w_q = (const u16*)d_in[2]; a.w_uk = (const u16*)d_in[3]; a.w_uv = (const u16*)d_in[4]; a.w_o = (const u16*)d_in[5];
    a.w_r = (const u16*)d_in[7]; a.wg = (const u16*)d_in[8]; a.wu = (const u16*)d_in[9]; a.wd = (const u16*)d_in[10]; a.cvec = (const u16*)d_in[11]; a.w_mod = (const u16*)d_in[12];
    a.ws = (char*)d_ws; a.out = (u16*)d_out;
    (void)hipFuncSetAttribute((const void*)k_all, hipFuncAttributeMaxDynamicSharedMemorySize, LDS_BYTES);
    k_all<<<256, 256, LDS_BYTES, stream>>>(a);
}
```

```cpp
#include <hip/hip_runtime.h>
#include <hip/hip_bf16.h>
#include <stdint.h>
typedef unsigned short u16;
typedef float f32x4 __attribute__((ext_vector_type(4)));
typedef float f32x2 __attribute__((ext_vector_type(2)));
typedef unsigned u32x4 __attribute__((ext_vector_type(4)));
typedef unsigned u32x2 __attribute__((ext_vector_type(2)));
typedef unsigned u32x16 __attribute__((ext_vector_type(16)));
typedef __bf16 bf16x2_t __attribute__((ext_vector_type(2)));
constexpr int SEQ = 2048, DM = 512, FF = 1792;
#define DI __device__ __forceinline__
#define LDSBAR() do { asm volatile("s_waitcnt lgkmcnt(0)" ::: "memory"); __builtin_amdgcn_s_barrier(); asm volatile("" ::: "memory"); } while (0)
DI unsigned lane_id() { unsigned l; asm volatile("v_mbcnt_lo_u32_b32 %0, -1, 0\n\tv_mbcnt_hi_u32_b32 %0, -1, %0" : "=v"(l)); return l; }
DI unsigned rfl(unsigned x) { return __builtin_amdgcn_readfirstlane(x); }
DI unsigned cvt2(float lo, float hi) { f32x2 v = {lo, hi}; bf16x2_t b = __builtin_convertvector(v, bf16x2_t); return __builtin_bit_cast(unsigned, b); }
DI float rb(float x) { return __uint_as_float(cvt2(0.f, x)); }
DI float dot2bf(unsigned a, unsigned b, float c) { return __builtin_amdgcn_fdot2_f32_bf16(__builtin_bit_cast(bf16x2_t, a), __builtin_bit_cast(bf16x2_t, b), c, false); }
DI float lo2f(unsigned w) { return __uint_as_float(w << 16); }
DI float hi2f(unsigned w) { return __uint_as_float(w & 0xffff0000u); }
#define DPPF(v, ctl) __builtin_bit_cast(float, __builtin_amdgcn_update_dpp(0, __builtin_bit_cast(int, v), ctl, 0xF, 0xF, true))
DI float row16_sum(float v) { v += DPPF(v, 0xB1); v += DPPF(v, 0x4E); v += DPPF(v, 0x141); v += DPPF(v, 0x140); return v; }
DI float swap16_sum(float a, float b) { auto r = __builtin_amdgcn_permlane16_swap(__float_as_uint(a), __float_as_uint(b), false, false); return __uint_as_float(r[0]) + __uint_as_float(r[1]); }
DI float swap32_sum(float a, float b) { auto r = __builtin_amdgcn_permlane32_swap(__float_as_uint(a), __float_as_uint(b), false, false); return __uint_as_float(r[0]) + __uint_as_float(r[1]); }
DI float wave_sum2(float v) { v = row16_sum(v); v = swap16_sum(v, v); return swap32_sum(v, v); }
DI unsigned wave_umax(unsigned v) {
    v = max(v, (unsigned)__builtin_amdgcn_update_dpp(0, (int)v, 0xB1, 0xF, 0xF, true)); v = max(v, (unsigned)__builtin_amdgcn_update_dpp(0, (int)v, 0x4E, 0xF, 0xF, true));
    v = max(v, (unsigned)__builtin_amdgcn_update_dpp(0, (int)v, 0x141, 0xF, 0xF, true)); v = max(v, (unsigned)__builtin_amdgcn_update_dpp(0, (int)v, 0x140, 0xF, 0xF, true));
    { auto r = __builtin_amdgcn_permlane16_swap(v, v, false, false); v = max((unsigned)r[0], (unsigned)r[1]); }
    { auto r = __builtin_amdgcn_permlane32_swap(v, v, false, false); v = max((unsigned)r[0], (unsigned)r[1]); }
    return v;
}
#define BODY_ASM ".macro M a,b,c,d\nv_mfma_f32_16x16x32_bf16 \\a,\\b,\\c,\\d\n.endm\n" \
".Lcode_b:\ns_load_dwordx16 s[8:23],s[4:5],0x0\ns_load_dwordx8 s[24:31],s[4:5],0x40\ns_load_dwordx2 s[34:35],s[4:5],0x60\nv_mbcnt_lo_u32_b32 v249,-1,0\nv_mbcnt_hi_u32_b32 v249,-1,v249\ns_getpc_b64 s[56:57]\n.Lpc0:\n" \
"s_and_b32 s57,s57,0xffff\ns_mov_b32 s58,.Lcode_e-.Lpc0\ns_mov_b32 s59,0x20000\ns_lshr_b32 s70,s6,3\nv_and_b32 v251,1,v249\ns_lshl2_add_u32 s78,s70,s7\nv_lshlrev_b32 v251,14,v251\ns_lshl_b32 s78,s78,7\n" \
"buffer_load_dword v247,v251,s[56:59],s78 offen\ns_bfe_u32 s61,s6,0x20003\ns_lshl_b32 s64,s7,12\ns_and_b32 s60,s6,7\ns_lshr_b32 s62,s6,5\ns_lshl2_add_u32 s63,s60,s61\nv_lshlrev_b32 v240,4,v249\ns_cmp_lt_u32 s6,192\n" \
"s_cselect_b32 s70,s6,0\ns_mul_i32 s71,s70,171\ns_lshr_b32 s71,s71,13\ns_mul_i32 s72,s71,48\ns_sub_u32 s70,s70,s72\ns_lshl2_add_u32 s72,s71,s7\ns_lshl_b32 s72,s72,5\nv_lshrrev_b32 v0,3,v249\n" \
"v_lshl_add_u32 v1,v0,2,s72\nv_and_b32 v2,7,v249\nv_mul_u32_u24 v3,6144,v1\nv_lshl_add_u32 v3,v2,4,v3\ns_lshl_b32 s72,s70,7\ns_add_u32 s72,s72,3072\nv_add_u32 v3,s72,v3\nv_add_u32 v4,12288,v3\n" \
"v_lshlrev_b32 v1,1,v1\ns_mul_i32 s71,s71,12288\ns_lshl_b32 s70,s70,8\ns_add_u32 s79,s70,s71\ns_add_u32 s79,s79,786432\ns_cmp_eq_u32 s7,0\ns_cselect_b32 s75,0,0x8000000\ns_cmp_lt_u32 s6,192\n" \
"s_cselect_b32 s70,s75,0x8000000\ns_or_b32 s79,s79,s70\ns_lshl_b32 s70,s62,7\ns_xor_b32 s71,s63,12\ns_lshl3_add_u32 s71,s71,s70\ns_lshl_b32 s72,s63,3\ns_sub_u32 s72,s72,s70\ns_add_u32 s72,s72,1792\n" \
"s_cmp_lt_u32 s63,16\ns_cselect_b32 s71,s71,s72\ns_lshl_b32 s71,s71,10\ns_lshl_b32 s72,s7,7\ns_add_u32 s71,s71,s72\nv_lshlrev_b32 v5,10,v2\nv_lshl_add_u32 v5,v0,4,v5\nv_add_u32 v5,s71,v5\n" \
"v_bfe_u32 v6,v249,2,3\nv_lshrrev_b32 v7,5,v249\nv_and_b32 v8,48,v240\nv_lshlrev_b32 v6,13,v6\nv_lshl_or_b32 v7,v7,6,v8\nv_or_b32 v6,0x1000,v6\nv_or_b32 v243,v6,v7\nv_add_u32 v244,0xfffffc80,v243\n" \
"v_xor_b32 v6,32,v243\nv_add_u32 v245,63488,v6\nv_add_u32 v246,62592,v6\nv_lshrrev_b32 v6,4,v249\nv_xor_b32 v7,v6,v2\nv_lshlrev_b32 v7,4,v7\nv_lshl_or_b32 v224,v0,10,v7\nv_xor_b32 v7,64,v224\n" \
"v_add_u32 v225,7168,v7\nv_add_u32 v226,14336,v224\nv_add_u32 v227,21504,v7\ns_lshl_b32 s70,s62,1\ns_sub_u32 s71,s7,s70\ns_add_u32 s71,s71,30\ns_sub_u32 s72,s70,s7\ns_add_u32 s72,s72,3\n" \
"s_cmp_lt_u32 s7,2\ns_cselect_b32 s74,s71,s72\ns_lshl_b32 s66,s74,16\ns_mul_i32 s65,s7,24576\ns_lshl_b32 s68,s7,17\ns_lshl_b32 s70,s63,8\ns_add_u32 s68,s68,s70\ns_mov_b32 s38,2097152\n" \
"s_mov_b32 s39,0x20000\ns_mov_b32 s42,4198400\ns_mov_b32 s43,0x20000\ns_mov_b32 s54,71303168\ns_mov_b32 s55,0x20000\ns_mov_b32 s70,94208\ns_waitcnt lgkmcnt(0)\ns_mov_b64 s[52:53],s[34:35]\n" \
"s_mov_b64 s[36:37],s[10:11]\ns_sub_u32 s40,s14,0x1000\ns_subb_u32 s41,s15,0\nbuffer_load_dwordx2 v[230:231],off,s[52:55],s70 sc1\nglobal_load_dwordx2 v[10:11],v1,s[28:29]\nglobal_load_dwordx4 v[12:15],v3,s[30:31] offset:-3072\nglobal_load_dwordx4 v[16:19],v3,s[30:31] offset:3072\nglobal_load_dwordx4 v[20:23],v4,s[30:31] offset:-3072\n" \
"global_load_dwordx4 v[24:27],v4,s[30:31] offset:3072\nglobal_load_dwordx4 v[128:131],v5,s[8:9]\nglobal_load_dwordx4 v[132:135],v5,s[8:9] offset:512\ns_add_u32 m0,s64,98304\ns_add_u32 s69,s68,0\nbuffer_load_dwordx4 v243,s[40:43],s69 offen offset:0 lds\nbuffer_load_dwordx4 v244,s[40:43],s69 offen offset:1024 lds\nbuffer_load_dwordx4 v245,s[40:43],s69 offen offset:2048 lds\n" \
"buffer_load_dwordx4 v246,s[40:43],s69 offen offset:3072 lds\ns_add_u32 m0,s65,0\ns_add_u32 s67,s66,0\nbuffer_load_dwordx4 v224,s[36:39],s67 offen offset:0 lds\nbuffer_load_dwordx4 v225,s[36:39],s67 offen offset:1024 lds\nbuffer_load_dwordx4 v226,s[36:39],s67 offen offset:2048 lds\nbuffer_load_dwordx4 v227,s[36:39],s67 offen offset:3072 lds\ns_add_u32 m0,s65,4096\n" \
"s_add_u32 s67,s66,32768\nbuffer_load_dwordx4 v224,s[36:39],s67 offen offset:0 lds\nbuffer_load_dwordx4 v225,s[36:39],s67 offen offset:1024 lds\nbuffer_load_dwordx4 v226,s[36:39],s67 offen offset:2048 lds\nbuffer_load_dwordx4 v227,s[36:39],s67 offen offset:3072 lds\ns_lshl_b32 s70,s6,11\ns_cmp_lt_u32 s6,112\ns_cselect_b32 s70,s70,0x8000000\n" \
"s_lshl_b32 s71,s7,9\ns_add_u32 s70,s70,s71\ns_add_u32 s70,s70,98304\nv_mov_b32 v28,-1\nv_mov_b32 v29,-1\nv_lshlrev_b32 v30,3,v249\nbuffer_store_dwordx2 v[28:29],v30,s[52:55],s70 offen sc1\ns_lshl_b32 s70,s6,7\n" \
"s_cmp_lt_u32 s6,224\ns_cselect_b32 s71,65536,0x40000000\ns_cmp_lt_u32 s6,128\ns_cselect_b32 s72,556928,0x40000000\nv_mov_b32 v31,s71\nv_mov_b32 v32,s72\nv_cmp_lt_u32 vcc,31,v249\ns_or_b32 s70,s70,s75\n" \
"v_mov_b32 v33,0\nv_cndmask_b32 v31,v31,v32,vcc\nv_lshl_add_u32 v31,v249,2,v31\nbuffer_store_dword v33,v31,s[52:55],s70 offen sc1\nv_and_b32 v34,15,v249\nv_lshrrev_b32 v35,1,v34\nv_xor_b32 v35,v35,v6\nv_lshlrev_b32 v35,4,v35\n" \
"v_lshl_or_b32 v35,v34,7,v35\nv_add_u32 v228,s65,v35\nv_xor_b32 v229,64,v228\nv_bfe_u32 v34,v249,2,2\nv_and_b32 v35,3,v249\nv_lshlrev_b32 v35,3,v35\nv_lshl_or_b32 v35,v34,6,v35\nv_lshl_or_b32 v35,v6,11,v35\n" \
"v_and_b32 v34,16,v249\nv_lshl_or_b32 v35,v34,1,v35\nv_add_u32 v241,98304,v35\nv_xor_b32 v242,32,v241\ns_mov_b32 s73,0x400000\nv_mov_b32 v252,0xff800000\ns_mov_b32 s70,0x05040100\ns_mov_b32 s71,0x07060302\n" \
"s_waitcnt vmcnt(20)\nv_lshlrev_b32 v36,16,v10\nv_and_b32 v37,0xffff0000,v10\nv_lshlrev_b32 v38,16,v11\nv_and_b32 v39,0xffff0000,v11\nv_mul_f32 v40,0xbfb8aa3b,v36\nv_mul_f32 v41,0xbfb8aa3b,v37\nv_mul_f32 v42,0xbfb8aa3b,v38\n" \
"v_mul_f32 v43,0xbfb8aa3b,v39\nv_exp_f32 v40,v40\nv_exp_f32 v41,v41\nv_exp_f32 v42,v42\nv_exp_f32 v43,v43\nv_add_f32 v40,1.0,v40\nv_add_f32 v41,1.0,v41\nv_add_f32 v42,1.0,v42\n" \
"v_add_f32 v43,1.0,v43\nv_rcp_f32 v40,v40\nv_rcp_f32 v41,v41\nv_rcp_f32 v42,v42\nv_rcp_f32 v43,v43\nv_mul_f32 v36,v36,v40\nv_mul_f32 v37,v37,v41\nv_mul_f32 v38,v38,v42\n" \
"v_mul_f32 v39,v39,v43\nv_cvt_pk_bf16_f32 v36,v36,v37\nv_cvt_pk_bf16_f32 v37,v38,v39\ns_waitcnt vmcnt(18)\nv_perm_b32 v48,v16,v12,s70\nv_perm_b32 v49,v16,v12,s71\nv_perm_b32 v50,v17,v13,s70\nv_perm_b32 v51,v17,v13,s71\n" \
"v_perm_b32 v52,v18,v14,s70\nv_perm_b32 v53,v18,v14,s71\nv_perm_b32 v54,v19,v15,s70\nv_perm_b32 v55,v19,v15,s71\nv_dot2_f32_bf16 v40,v48,v36,0\nv_dot2_f32_bf16 v41,v49,v36,0\nv_dot2_f32_bf16 v42,v50,v36,0\nv_dot2_f32_bf16 v43,v51,v36,0\n" \
"v_dot2_f32_bf16 v44,v52,v36,0\nv_dot2_f32_bf16 v45,v53,v36,0\nv_dot2_f32_bf16 v46,v54,v36,0\nv_dot2_f32_bf16 v47,v55,v36,0\ns_waitcnt vmcnt(16)\nv_perm_b32 v48,v24,v20,s70\nv_perm_b32 v49,v24,v20,s71\nv_perm_b32 v50,v25,v21,s70\n" \
"v_perm_b32 v51,v25,v21,s71\nv_perm_b32 v52,v26,v22,s70\nv_perm_b32 v53,v26,v22,s71\nv_perm_b32 v54,v27,v23,s70\nv_perm_b32 v55,v27,v23,s71\nv_dot2_f32_bf16 v40,v48,v37,v40\nv_dot2_f32_bf16 v41,v49,v37,v41\nv_dot2_f32_bf16 v42,v50,v37,v42\n" \
"v_dot2_f32_bf16 v43,v51,v37,v43\nv_dot2_f32_bf16 v44,v52,v37,v44\nv_dot2_f32_bf16 v45,v53,v37,v45\nv_dot2_f32_bf16 v46,v54,v37,v46\nv_dot2_f32_bf16 v47,v55,v37,v47\ns_waitcnt vmcnt(15)\nv_lshlrev_b32 v136,16,v128\nv_and_b32 v137,0xffff0000,v128\n" \
"v_lshlrev_b32 v138,16,v129\nv_and_b32 v139,0xffff0000,v129\nv_lshlrev_b32 v140,16,v130\nv_and_b32 v141,0xffff0000,v130\nv_lshlrev_b32 v142,16,v131\nv_and_b32 v143,0xffff0000,v131\ns_waitcnt vmcnt(14)\nv_lshlrev_b32 v144,16,v132\n" \
"v_and_b32 v145,0xffff0000,v132\nv_lshlrev_b32 v146,16,v133\nv_and_b32 v147,0xffff0000,v133\nv_lshlrev_b32 v148,16,v134\nv_and_b32 v149,0xffff0000,v134\nv_lshlrev_b32 v150,16,v135\nv_and_b32 v151,0xffff0000,v135\nv_permlane32_swap_b32 v40,v44\n" \
"v_permlane32_swap_b32 v41,v45\nv_permlane32_swap_b32 v42,v46\nv_permlane32_swap_b32 v43,v47\nv_add_f32 v40,v40,v44\nv_add_f32 v41,v41,v45\nv_add_f32 v42,v42,v46\nv_add_f32 v43,v43,v47\nv_add_f32 v48,v136,v140\n" \
"v_add_f32 v49,v137,v141\nv_add_f32 v50,v138,v142\nv_add_f32 v51,v139,v143\nv_permlane16_swap_b32 v40,v42\nv_permlane16_swap_b32 v41,v43\nv_add_f32 v52,v144,v148\nv_add_f32 v53,v145,v149\nv_add_f32 v54,v146,v150\n" \
"v_add_f32 v55,v147,v151\nv_add_f32 v40,v40,v42\nv_add_f32 v41,v41,v43\nv_add_f32 v48,v48,v52\nv_add_f32 v49,v49,v53\nv_add_f32 v50,v50,v54\nv_add_f32 v51,v51,v55\nv_add_f32_dpp v40,v40,v40 row_ror:8 row_mask:0xf bank_mask:0xf\n" \
"v_add_f32_dpp v41,v41,v41 row_ror:8 row_mask:0xf bank_mask:0xf\nv_add_f32 v48,v48,v49\nv_add_f32 v50,v50,v51\nv_lshlrev_b32 v42,5,v2\nv_add_f32 v48,v48,v50\nv_lshl_or_b32 v42,v6,3,v42\ns_lshl_b32 s72,s7,8\ns_add_u32 s72,s72,147456\n" \
"v_add_u32 v42,s72,v42\nds_write_b64 v42,v[40:41]\nv_add_f32_dpp v48,v48,v48 row_ror:8 row_mask:0xf bank_mask:0xf\nv_mov_b32 v50,v48\ns_nop 1\nv_permlane16_swap_b32 v48,v50\nv_add_f32 v48,v48,v50\nv_mov_b32 v50,v48\n" \
"s_nop 1\nv_permlane32_swap_b32 v48,v50\nv_add_f32 v48,v48,v50\nv_mul_f32 v48,0x3c000000,v48\nv_sub_f32 v56,v136,v48\nv_sub_f32 v57,v137,v48\nv_sub_f32 v58,v138,v48\nv_sub_f32 v59,v139,v48\n" \
"v_sub_f32 v60,v140,v48\nv_sub_f32 v61,v141,v48\nv_sub_f32 v62,v142,v48\nv_sub_f32 v63,v143,v48\nv_sub_f32 v64,v144,v48\nv_sub_f32 v65,v145,v48\nv_sub_f32 v66,v146,v48\nv_sub_f32 v67,v147,v48\n" \
"v_sub_f32 v68,v148,v48\nv_sub_f32 v69,v149,v48\nv_sub_f32 v70,v150,v48\nv_sub_f32 v71,v151,v48\nv_mul_f32 v52,v56,v56\nv_mul_f32 v53,v57,v57\nv_mul_f32 v54,v58,v58\nv_mul_f32 v55,v59,v59\n" \
"v_fmac_f32 v52,v60,v60\nv_fmac_f32 v53,v61,v61\nv_fmac_f32 v54,v62,v62\nv_fmac_f32 v55,v63,v63\nv_fmac_f32 v52,v64,v64\nv_fmac_f32 v53,v65,v65\nv_fmac_f32 v54,v66,v66\nv_fmac_f32 v55,v67,v67\n" \
"v_fmac_f32 v52,v68,v68\nv_fmac_f32 v53,v69,v69\nv_fmac_f32 v54,v70,v70\nv_fmac_f32 v55,v71,v71\nv_add_f32 v52,v52,v53\nv_add_f32 v54,v54,v55\ns_lshl_b32 s72,s7,3\nv_add_f32 v49,v52,v54\n" \
"s_add_u32 s72,s72,148480\nv_lshl_add_u32 v42,v2,5,s72\nv_add_f32_dpp v49,v49,v49 row_ror:8 row_mask:0xf bank_mask:0xf\nv_mov_b32 v50,v49\ns_nop 1\nv_permlane16_swap_b32 v49,v50\nv_add_f32 v49,v49,v50\nv_mov_b32 v50,v49\n" \
"s_nop 1\nv_permlane32_swap_b32 v49,v50\nv_add_f32 v49,v49,v50\nds_write_b64 v42,v[48:49]\nv_lshlrev_b32 v251,5,v2\nv_add_u32 v251,148480,v251\nv_lshlrev_b32 v250,2,v249\nv_add_u32 v250,147456,v250\n" \
"s_add_u32 m0,s64,114688\ns_add_u32 s69,s68,524288\nbuffer_load_dwordx4 v243,s[40:43],s69 offen offset:0 lds\nbuffer_load_dwordx4 v244,s[40:43],s69 offen offset:1024 lds\nbuffer_load_dwordx4 v245,s[40:43],s69 offen offset:2048 lds\nbuffer_load_dwordx4 v246,s[40:43],s69 offen offset:3072 lds\ns_add_u32 m0,s65,8192\ns_add_u32 s67,s66,128\n" \
"buffer_load_dwordx4 v224,s[36:39],s67 offen offset:0 lds\nbuffer_load_dwordx4 v225,s[36:39],s67 offen offset:1024 lds\nbuffer_load_dwordx4 v226,s[36:39],s67 offen offset:2048 lds\nbuffer_load_dwordx4 v227,s[36:39],s67 offen offset:3072 lds\ns_add_u32 m0,s65,12288\ns_add_u32 s67,s66,32896\nbuffer_load_dwordx4 v224,s[36:39],s67 offen offset:0 lds\nbuffer_load_dwordx4 v225,s[36:39],s67 offen offset:1024 lds\n" \
"buffer_load_dwordx4 v226,s[36:39],s67 offen offset:2048 lds\nbuffer_load_dwordx4 v227,s[36:39],s67 offen offset:3072 lds\ns_waitcnt lgkmcnt(0)\ns_waitcnt vmcnt(14)\ns_barrier\ns_add_u32 m0,s64,131072\ns_add_u32 s69,s68,1048576\nds_read_b128 a[0:3],v228 offset:0\n" \
"ds_read_b128 a[4:7],v228 offset:2048\nds_read_b128 a[8:11],v228 offset:4096\nds_read_b128 a[12:15],v228 offset:6144\nds_read_b128 a[16:19],v229 offset:0\nds_read_b128 a[20:23],v229 offset:2048\nds_read_b128 a[24:27],v229 offset:4096\nds_read_b128 a[28:31],v229 offset:6144\nds_read_b32 v152,v250 offset:0\n" \
"ds_read_b32 v153,v250 offset:256\nds_read_b32 v154,v250 offset:512\nds_read_b32 v155,v250 offset:768\ns_waitcnt lgkmcnt(2)\nv_add_f32 v152,v152,v153\ns_waitcnt lgkmcnt(0)\nv_add_f32 v154,v154,v155\nv_lshlrev_b32 v153,2,v249\n" \
"v_add_f32 v152,v152,v154\nds_read_b64_tr_b16 v[192:193],v241 offset:0\nds_read_b64_tr_b16 v[194:195],v241 offset:256\nds_read_b64_tr_b16 v[196:197],v242 offset:0\nds_read_b64_tr_b16 v[198:199],v242 offset:256\nds_read_b64_tr_b16 v[200:201],v241 offset:512\nds_read_b64_tr_b16 v[202:203],v241 offset:768\nds_read_b64_tr_b16 v[204:205],v242 offset:512\n" \
"ds_read_b64_tr_b16 v[206:207],v242 offset:768\nds_read_b64_tr_b16 v[208:209],v241 offset:1024\nds_read_b64_tr_b16 v[210:211],v241 offset:1280\nds_read_b64_tr_b16 v[212:213],v242 offset:1024\nds_read_b64_tr_b16 v[214:215],v242 offset:1280\nds_read_b64_tr_b16 v[216:217],v241 offset:1536\nds_read_b64_tr_b16 v[218:219],v241 offset:1792\nds_read_b64_tr_b16 v[220:221],v242 offset:1536\n" \
"ds_read_b64_tr_b16 v[222:223],v242 offset:1792\nbuffer_store_dword v152,v153,s[52:55],s79 offen sc1\nds_read_b128 v[156:159],v251\nds_read_b128 v[160:163],v251 offset:16\ns_waitcnt lgkmcnt(15)\nM v[0:3],v[192:195],a[0:3],0\nbuffer_load_dwordx4 v243,s[40:43],s69 offen offset:0 lds\nM v[4:7],v[192:195],a[4:7],0\n" \
"v_and_b32 v254,7,v249\nv_bfe_u32 v253,v249,1,2\nM v[64:67],v[192:195],a[8:11],0\nv_and_b32 v248,1,v249\ns_lshl2_add_u32 s76,s62,s7\nM v[68:71],v[192:195],a[12:15],0\nds_read_b64_tr_b16 v[192:193],v241 offset:8192\nds_read_b64_tr_b16 v[194:195],v241 offset:8448\n" \
"s_waitcnt lgkmcnt(15)\nM v[8:11],v[196:199],a[0:3],0\nv_lshlrev_b32 v253,13,v253\ns_mul_i32 s76,s76,32768\nM v[12:15],v[196:199],a[4:7],0\nbuffer_load_dwordx4 v244,s[40:43],s69 offen offset:1024 lds\nM v[72:75],v[196:199],a[8:11],0\ns_lshl_b32 s77,s63,8\n" \
"v_lshl_or_b32 v253,v248,7,v253\nM v[76:79],v[196:199],a[12:15],0\nds_read_b64_tr_b16 v[196:197],v242 offset:8192\nds_read_b64_tr_b16 v[198:199],v242 offset:8448\ns_add_u32 s76,s76,s77\ns_waitcnt lgkmcnt(7)\nM v[16:19],v[200:203],a[0:3],0\nv_add_u32 v253,s76,v253\n" \
"global_load_dword v247,v253,s[16:17]\nM v[20:23],v[200:203],a[4:7],0\nv_readfirstlane_b32 s92,v230\nv_readfirstlane_b32 s93,v231\nM v[80:83],v[200:203],a[8:11],0\nbuffer_load_dwordx4 v245,s[40:43],s69 offen offset:2048 lds\nM v[84:87],v[200:203],a[12:15],0\nds_read_b64_tr_b16 v[200:201],v241 offset:8704\n" \
"ds_read_b64_tr_b16 v[202:203],v241 offset:8960\nM v[24:27],v[204:207],a[0:3],0\ns_add_u32 s70,s92,1\ns_addc_u32 s71,s93,0\nM v[28:31],v[204:207],a[4:7],0\ns_waitcnt lgkmcnt(4)\nv_add_f32 v164,v156,v158\nM v[88:91],v[204:207],a[8:11],0\n" \
"v_add_f32 v165,v160,v162\nv_add_f32 v164,v164,v165\nM v[92:95],v[204:207],a[12:15],0\nbuffer_load_dwordx4 v246,s[40:43],s69 offen offset:3072 lds\nds_read_b64_tr_b16 v[204:205],v242 offset:8704\nds_read_b64_tr_b16 v[206:207],v242 offset:8960\nM v[32:35],v[208:211],a[0:3],0\ns_add_u32 s92,s92,0x7f4a7c15\n" \
"s_addc_u32 s93,s93,0x9e3779b9\nM v[36:39],v[208:211],a[4:7],0\ns_add_u32 m0,s65,16384\ns_add_u32 s67,s66,256\nM v[96:99],v[208:211],a[8:11],0\nv_and_b32 v165,7,v249\ns_lshl3_add_u32 s72,s6,s75\nM v[100:103],v[208:211],a[12:15],0\n" \
"ds_read_b64_tr_b16 v[208:209],v241 offset:9216\nds_read_b64_tr_b16 v[210:211],v241 offset:9472\nM v[40:43],v[212:215],a[0:3],0\nbuffer_load_dwordx4 v224,s[36:39],s67 offen offset:0 lds\nM v[44:47],v[212:215],a[4:7],0\nv_add_u32 v253,0x100000,v253\nv_mov_b32 v230,s92\nM v[104:107],v[212:215],a[8:11],0\n" \
"v_mov_b32 v231,s93\nv_lshlrev_b32 v165,11,v165\nM v[108:111],v[212:215],a[12:15],0\nds_read_b64_tr_b16 v[212:213],v242 offset:9216\nds_read_b64_tr_b16 v[214:215],v242 offset:9472\nM v[48:51],v[216:219],a[0:3],0\ns_add_u32 s72,s72,573440\ns_lshl2_add_u32 s80,s60,s7\n" \
"M v[52:55],v[216:219],a[4:7],0\nbuffer_load_dwordx4 v225,s[36:39],s67 offen offset:1024 lds\nM v[112:115],v[216:219],a[8:11],0\ns_lshl_b32 s80,s80,9\ns_add_u32 s80,s80,573440\nM v[116:119],v[216:219],a[12:15],0\nds_read_b64_tr_b16 v[216:217],v241 offset:9728\nds_read_b64_tr_b16 v[218:219],v241 offset:9984\n" \
"M v[56:59],v[220:223],a[0:3],0\nv_mul_f32 v164,0x3e800000,v164\nv_sub_f32 v156,v156,v164\nM v[60:63],v[220:223],a[4:7],0\nv_sub_f32 v158,v158,v164\nv_mul_f32 v156,v156,v156\nM v[120:123],v[220:223],a[8:11],0\nbuffer_load_dwordx4 v226,s[36:39],s67 offen offset:2048 lds\n" \
"M v[124:127],v[220:223],a[12:15],0\nds_read_b64_tr_b16 v[220:221],v242 offset:9728\nds_read_b64_tr_b16 v[222:223],v242 offset:9984\nM v[0:3],v[192:195],a[16:19],v[0:3]\nv_sub_f32 v160,v160,v164\nv_fmac_f32 v156,v158,v158\nM v[4:7],v[192:195],a[20:23],v[4:7]\nv_add_f32 v157,v157,v159\n" \
"v_add_f32 v161,v161,v163\nM v[64:67],v[192:195],a[24:27],v[64:67]\nv_sub_f32 v162,v162,v164\nv_fmac_f32 v156,v160,v160\nM v[68:71],v[192:195],a[28:31],v[68:71]\nbuffer_load_dwordx4 v227,s[36:39],s67 offen offset:3072 lds\ns_waitcnt lgkmcnt(4)\nM v[8:11],v[196:199],a[16:19],v[8:11]\n" \
"v_add_f32 v157,v157,v161\nv_fmac_f32 v156,v162,v162\nM v[12:15],v[196:199],a[20:23],v[12:15]\ns_add_u32 m0,s65,20480\ns_add_u32 s67,s66,33024\nM v[72:75],v[196:199],a[24:27],v[72:75]\nv_fmac_f32 v157,0x43000000,v156\nv_mul_f32 v157,0x3b000000,v157\n" \
"M v[76:79],v[196:199],a[28:31],v[76:79]\nv_cvt_pk_bf16_f32 v157,0,v157\nv_add_f32 v157,0x3727c5ac,v157\nM v[16:19],v[200:203],a[16:19],v[16:19]\nbuffer_load_dwordx4 v224,s[36:39],s67 offen offset:0 lds\nM v[20:23],v[200:203],a[20:23],v[20:23]\nv_cvt_pk_bf16_f32 v157,0,v157\nv_mov_b32 v232,s70\n" \
"M v[80:83],v[200:203],a[24:27],v[80:83]\nv_sqrt_f32 v157,v157\nM v[84:87],v[200:203],a[28:31],v[84:87]\nv_mov_b32 v233,s71\ns_lshr_b32 s70,s64,2\nM v[24:27],v[204:207],a[16:19],v[24:27]\nglobal_load_dword v247,v253,s[16:17]\nM v[28:31],v[204:207],a[20:23],v[28:31]\n" \
"buffer_load_dwordx4 v225,s[36:39],s67 offen offset:1024 lds\nM v[88:91],v[204:207],a[24:27],v[88:91]\ns_waitcnt vmcnt(12) lgkmcnt(0)\ns_barrier\nds_read_b128 a[32:35],v228 offset:8192\nds_read_b128 a[36:39],v228 offset:10240\nds_read_b128 a[40:43],v228 offset:12288\nds_read_b128 a[44:47],v228 offset:14336\n" \
"ds_read_b128 a[48:51],v229 offset:8192\nds_read_b128 a[52:55],v229 offset:10240\nds_read_b128 a[56:59],v229 offset:12288\nds_read_b128 a[60:63],v229 offset:14336\nds_read_b64_tr_b16 v[192:193],v241 offset:16384\nds_read_b64_tr_b16 v[194:195],v241 offset:16640\nds_read_b64_tr_b16 v[196:197],v242 offset:16384\nds_read_b64_tr_b16 v[198:199],v242 offset:16640\n" \
"ds_read_b64_tr_b16 v[200:201],v241 offset:16896\nds_read_b64_tr_b16 v[202:203],v241 offset:17152\nM v[92:95],v[204:207],a[28:31],v[92:95]\nds_read_b64_tr_b16 v[204:205],v242 offset:16896\nds_read_b64_tr_b16 v[206:207],v242 offset:17152\nM v[32:35],v[208:211],a[16:19],v[32:35]\nbuffer_store_dwordx2 v[230:231],v165,s[52:55],s72 offen sc1\nM v[36:39],v[208:211],a[20:23],v[36:39]\n" \
"v_add_u32 v253,0x100000,v253\nv_lshlrev_b32 v165,3,v249\nM v[96:99],v[208:211],a[24:27],v[96:99]\nbuffer_load_dwordx4 v226,s[36:39],s67 offen offset:2048 lds\nM v[100:103],v[208:211],a[28:31],v[100:103]\nds_read_b64_tr_b16 v[208:209],v241 offset:17408\nds_read_b64_tr_b16 v[210:211],v241 offset:17664\nM v[40:43],v[212:215],a[16:19],v[40:43]\n" \
"v_cvt_pk_bf16_f32 v164,0,v164\nv_cvt_pk_bf16_f32 v157,0,v157\nM v[44:47],v[212:215],a[20:23],v[44:47]\ns_add_u32 s71,s70,786432\nv_sub_f32 v136,v136,v164\nM v[104:107],v[212:215],a[24:27],v[104:107]\nv_rcp_f32 v157,v157\nM v[108:111],v[212:215],a[28:31],v[108:111]\n" \
"buffer_load_dwordx4 v227,s[36:39],s67 offen offset:3072 lds\nds_read_b64_tr_b16 v[212:213],v242 offset:17408\nds_read_b64_tr_b16 v[214:215],v242 offset:17664\nM v[48:51],v[216:219],a[16:19],v[48:51]\nv_sub_f32 v137,v137,v164\nv_sub_f32 v138,v138,v164\nM v[52:55],v[216:219],a[20:23],v[52:55]\ns_add_u32 m0,s65,0\n" \
"s_add_u32 s67,s66,384\nM v[112:115],v[216:219],a[24:27],v[112:115]\nv_sub_f32 v139,v139,v164\nv_sub_f32 v140,v140,v164\nM v[116:119],v[216:219],a[28:31],v[116:119]\nds_read_b64_tr_b16 v[216:217],v241 offset:17920\nds_read_b64_tr_b16 v[218:219],v241 offset:18176\nM v[56:59],v[220:223],a[16:19],v[56:59]\n" \
"buffer_load_dwordx4 v224,s[36:39],s67 offen offset:0 lds\nM v[60:63],v[220:223],a[20:23],v[60:63]\nv_sub_f32 v141,v141,v164\nv_sub_f32 v142,v142,v164\nM v[120:123],v[220:223],a[24:27],v[120:123]\nv_sub_f32 v143,v143,v164\nv_sub_f32 v144,v144,v164\nM v[124:127],v[220:223],a[28:31],v[124:127]\n" \
"ds_read_b64_tr_b16 v[220:221],v242 offset:17920\nds_read_b64_tr_b16 v[222:223],v242 offset:18176\ns_waitcnt lgkmcnt(6)\nM v[0:3],v[192:195],a[32:35],v[0:3]\nv_sub_f32 v145,v145,v164\nv_sub_f32 v146,v146,v164\nM v[4:7],v[192:195],a[36:39],v[4:7]\nbuffer_load_dwordx4 v225,s[36:39],s67 offen offset:1024 lds\n" \
"M v[64:67],v[192:195],a[40:43],v[64:67]\nv_sub_f32 v147,v147,v164\nv_sub_f32 v148,v148,v164\nM v[68:71],v[192:195],a[44:47],v[68:71]\nds_read_b64_tr_b16 v[192:193],v241 offset:24576\nds_read_b64_tr_b16 v[194:195],v241 offset:24832\nM v[8:11],v[196:199],a[32:35],v[8:11]\nv_sub_f32 v149,v149,v164\n" \
"v_sub_f32 v150,v150,v164\nM v[12:15],v[196:199],a[36:39],v[12:15]\nv_sub_f32 v151,v151,v164\nv_cvt_pk_bf16_f32 v136,0,v136\nM v[72:75],v[196:199],a[40:43],v[72:75]\nbuffer_load_dwordx4 v226,s[36:39],s67 offen offset:2048 lds\nM v[76:79],v[196:199],a[44:47],v[76:79]\nds_read_b64_tr_b16 v[196:197],v242 offset:24576\n" \
"ds_read_b64_tr_b16 v[198:199],v242 offset:24832\nM v[16:19],v[200:203],a[32:35],v[16:19]\nv_cvt_pk_bf16_f32 v137,0,v137\nv_cvt_pk_bf16_f32 v138,0,v138\nM v[20:23],v[200:203],a[36:39],v[20:23]\nv_cvt_pk_bf16_f32 v139,0,v139\nv_cvt_pk_bf16_f32 v140,0,v140\nM v[80:83],v[200:203],a[40:43],v[80:83]\n" \
"v_cvt_pk_bf16_f32 v141,0,v141\nv_cvt_pk_bf16_f32 v142,0,v142\nM v[84:87],v[200:203],a[44:47],v[84:87]\nbuffer_load_dwordx4 v227,s[36:39],s67 offen offset:3072 lds\nds_read_b64_tr_b16 v[200:201],v241 offset:25088\nds_read_b64_tr_b16 v[202:203],v241 offset:25344\nM v[24:27],v[204:207],a[32:35],v[24:27]\nv_cvt_pk_bf16_f32 v143,0,v143\n" \
"v_cvt_pk_bf16_f32 v144,0,v144\nM v[28:31],v[204:207],a[36:39],v[28:31]\ns_add_u32 m0,s65,4096\ns_add_u32 s67,s66,33152\nM v[88:91],v[204:207],a[40:43],v[88:91]\nv_cvt_pk_bf16_f32 v145,0,v145\nv_cvt_pk_bf16_f32 v146,0,v146\nM v[92:95],v[204:207],a[44:47],v[92:95]\n" \
"ds_read_b64_tr_b16 v[204:205],v242 offset:25088\nds_read_b64_tr_b16 v[206:207],v242 offset:25344\nM v[32:35],v[208:211],a[32:35],v[32:35]\nbuffer_load_dwordx4 v224,s[36:39],s67 offen offset:0 lds\nM v[36:39],v[208:211],a[36:39],v[36:39]\nv_cvt_pk_bf16_f32 v147,0,v147\nv_cvt_pk_bf16_f32 v148,0,v148\nM v[96:99],v[208:211],a[40:43],v[96:99]\n" \
"v_cvt_pk_bf16_f32 v149,0,v149\nv_cvt_pk_bf16_f32 v150,0,v150\nM v[100:103],v[208:211],a[44:47],v[100:103]\nds_read_b64_tr_b16 v[208:209],v241 offset:25600\nds_read_b64_tr_b16 v[210:211],v241 offset:25856\ns_waitcnt lgkmcnt(6)\nM v[40:43],v[212:215],a[32:35],v[40:43]\nglobal_load_dword v247,v253,s[16:17]\n" \
"M v[44:47],v[212:215],a[36:39],v[44:47]\nbuffer_load_dwordx4 v225,s[36:39],s67 offen offset:1024 lds\nM v[104:107],v[212:215],a[40:43],v[104:107]\nv_add_u32 v253,0x100000,v253\nv_cvt_pk_bf16_f32 v151,0,v151\nM v[108:111],v[212:215],a[44:47],v[108:111]\nds_read_b64_tr_b16 v[212:213],v242 offset:25600\nds_read_b64_tr_b16 v[214:215],v242 offset:25856\n" \
"M v[48:51],v[216:219],a[32:35],v[48:51]\nv_mul_f32 v136,v136,v157\nv_mul_f32 v137,v137,v157\nM v[52:55],v[216:219],a[36:39],v[52:55]\nv_mul_f32 v138,v138,v157\nv_mul_f32 v139,v139,v157\nM v[112:115],v[216:219],a[40:43],v[112:115]\nbuffer_load_dwordx4 v226,s[36:39],s67 offen offset:2048 lds\n" \
"M v[116:119],v[216:219],a[44:47],v[116:119]\nds_read_b64_tr_b16 v[216:217],v241 offset:26112\nds_read_b64_tr_b16 v[218:219],v241 offset:26368\nM v[56:59],v[220:223],a[32:35],v[56:59]\nv_mul_f32 v140,v140,v157\nv_mul_f32 v141,v141,v157\nM v[60:63],v[220:223],a[36:39],v[60:63]\nv_mul_f32 v142,v142,v157\n" \
"v_mul_f32 v143,v143,v157\nM v[120:123],v[220:223],a[40:43],v[120:123]\nv_mul_f32 v144,v144,v157\nv_mul_f32 v145,v145,v157\nM v[124:127],v[220:223],a[44:47],v[124:127]\nbuffer_load_dwordx4 v227,s[36:39],s67 offen offset:3072 lds\nds_read_b64_tr_b16 v[220:221],v242 offset:26112\nds_read_b64_tr_b16 v[222:223],v242 offset:26368\n" \
"M v[0:3],v[192:195],a[48:51],v[0:3]\nv_mul_f32 v146,v146,v157\nv_mul_f32 v147,v147,v157\nM v[4:7],v[192:195],a[52:55],v[4:7]\ns_add_u32 m0,s64,147456\ns_add_u32 s69,s68,1572864\nM v[64:67],v[192:195],a[56:59],v[64:67]\nv_mul_f32 v148,v148,v157\n" \
"v_mul_f32 v149,v149,v157\nM v[68:71],v[192:195],a[60:63],v[68:71]\nv_mul_f32 v150,v150,v157\nv_mul_f32 v151,v151,v157\nM v[8:11],v[196:199],a[48:51],v[8:11]\nbuffer_load_dwordx4 v243,s[40:43],s69 offen offset:0 lds\nM v[12:15],v[196:199],a[52:55],v[12:15]\nv_lshlrev_b32 v254,14,v254\n" \
"s_add_u32 s78,s78,32768\nM v[72:75],v[196:199],a[56:59],v[72:75]\nv_and_b32 v169,3,v249\nv_mul_u32_u24 v169,12288,v169\nM v[76:79],v[196:199],a[60:63],v[76:79]\nv_and_b32 v170,28,v249\ns_waitcnt lgkmcnt(2)\nM v[16:19],v[200:203],a[48:51],v[16:19]\n" \
"v_add_u32 v169,v169,v170\nv_lshrrev_b32 v251,3,v249\nM v[20:23],v[200:203],a[52:55],v[20:23]\nbuffer_load_dwordx4 v244,s[40:43],s69 offen offset:1024 lds\nM v[80:83],v[200:203],a[56:59],v[80:83]\ns_cmp_eq_u32 s6,0\ns_cselect_b32 s72,s75,0x8000000\nM v[84:87],v[200:203],a[60:63],v[84:87]\n" \
"s_add_u32 s72,s72,94208\nv_lshlrev_b32 v170,3,v170\nM v[24:27],v[204:207],a[48:51],v[24:27]\nv_cvt_pk_bf16_f32 v144,0,v144\nv_cvt_pk_bf16_f32 v145,0,v145\nM v[28:31],v[204:207],a[52:55],v[28:31]\nv_cvt_pk_bf16_f32 v146,0,v146\nv_cvt_pk_bf16_f32 v147,0,v147\n" \
"M v[88:91],v[204:207],a[56:59],v[88:91]\nbuffer_load_dwordx4 v245,s[40:43],s69 offen offset:2048 lds\ns_waitcnt vmcnt(12) lgkmcnt(0)\ns_barrier\nds_read_b128 a[64:67],v228 offset:16384\nds_read_b128 a[68:71],v228 offset:18432\nds_read_b128 a[72:75],v228 offset:20480\nds_read_b128 a[76:79],v228 offset:22528\n" \
"ds_read_b128 a[80:83],v229 offset:16384\nds_read_b128 a[84:87],v229 offset:18432\nds_read_b128 a[88:91],v229 offset:20480\nds_read_b128 a[92:95],v229 offset:22528\nM v[92:95],v[204:207],a[60:63],v[92:95]\nv_cvt_pk_bf16_f32 v136,0,v136\nv_cvt_pk_bf16_f32 v137,0,v137\nM v[32:35],v[208:211],a[48:51],v[32:35]\n" \
"v_cvt_pk_bf16_f32 v138,0,v138\nv_cvt_pk_bf16_f32 v139,0,v139\nM v[36:39],v[208:211],a[52:55],v[36:39]\nv_cvt_pk_bf16_f32 v140,0,v140\nv_cvt_pk_bf16_f32 v141,0,v141\nM v[96:99],v[208:211],a[56:59],v[96:99]\nv_cvt_pk_bf16_f32 v142,0,v142\nv_cvt_pk_bf16_f32 v143,0,v143\n" \
"M v[100:103],v[208:211],a[60:63],v[100:103]\nbuffer_load_dwordx4 v246,s[40:43],s69 offen offset:3072 lds\nM v[40:43],v[212:215],a[48:51],v[40:43]\nv_cvt_pk_bf16_f32 v148,0,v148\nv_cvt_pk_bf16_f32 v149,0,v149\nM v[44:47],v[212:215],a[52:55],v[44:47]\ns_add_u32 m0,s65,8192\ns_add_u32 s67,s66,512\n" \
"ds_read_b64_tr_b16 v[192:193],v241 offset:32768\nds_read_b64_tr_b16 v[194:195],v241 offset:33024\nM v[104:107],v[212:215],a[56:59],v[104:107]\nds_read_b64_tr_b16 v[196:197],v242 offset:32768\nv_bfe_u32 v250,v249,3,2\nM v[108:111],v[212:215],a[60:63],v[108:111]\nds_read_b64_tr_b16 v[198:199],v242 offset:33024\nds_read_b64_tr_b16 v[200:201],v241 offset:33280\n" \
"M v[48:51],v[216:219],a[48:51],v[48:51]\nbuffer_load_dwordx4 v224,s[36:39],s67 offen offset:0 lds\nM v[52:55],v[216:219],a[52:55],v[52:55]\nds_read_b64_tr_b16 v[202:203],v241 offset:33536\nds_read_b64_tr_b16 v[204:205],v242 offset:33280\nM v[112:115],v[216:219],a[56:59],v[112:115]\nds_read_b64_tr_b16 v[206:207],v242 offset:33536\nds_read_b64_tr_b16 v[208:209],v241 offset:33792\n" \
"M v[116:119],v[216:219],a[60:63],v[116:119]\nds_read_b64_tr_b16 v[210:211],v241 offset:34048\nds_read_b64_tr_b16 v[212:213],v242 offset:33792\nM v[56:59],v[220:223],a[48:51],v[56:59]\nglobal_load_dword v247,v253,s[16:17]\nM v[60:63],v[220:223],a[52:55],v[60:63]\nbuffer_load_dwordx4 v225,s[36:39],s67 offen offset:1024 lds\nM v[120:123],v[220:223],a[56:59],v[120:123]\n" \
"ds_read_b64_tr_b16 v[214:215],v242 offset:34048\nds_read_b64_tr_b16 v[216:217],v241 offset:34304\nM v[124:127],v[220:223],a[60:63],v[124:127]\nds_read_b64_tr_b16 v[218:219],v241 offset:34560\ns_waitcnt lgkmcnt(12)\nM v[0:3],v[192:195],a[64:67],v[0:3]\nds_read_b64_tr_b16 v[220:221],v242 offset:34304\nds_read_b64_tr_b16 v[222:223],v242 offset:34560\n" \
"M v[4:7],v[192:195],a[68:71],v[4:7]\nv_add_u32 v253,0xffd00000,v253\nv_cvt_pk_bf16_f32 v150,0,v150\nM v[64:67],v[192:195],a[72:75],v[64:67]\nbuffer_load_dwordx4 v226,s[36:39],s67 offen offset:2048 lds\nM v[68:71],v[192:195],a[76:79],v[68:71]\nds_read_b64_tr_b16 v[192:193],v241 offset:40960\ns_waitcnt lgkmcnt(11)\n" \
"M v[8:11],v[196:199],a[64:67],v[8:11]\nds_read_b64_tr_b16 v[194:195],v241 offset:41216\nv_cvt_pk_bf16_f32 v151,0,v151\nM v[12:15],v[196:199],a[68:71],v[12:15]\ns_mov_b32 s98,0x4000\ns_mov_b32 s0,28672\nM v[72:75],v[196:199],a[72:75],v[72:75]\ns_add_u32 s10,s61,1\n" \
"s_add_u32 s11,s61,2\nM v[76:79],v[196:199],a[76:79],v[76:79]\nbuffer_load_dwordx4 v227,s[36:39],s67 offen offset:3072 lds\nM v[16:19],v[200:203],a[64:67],v[16:19]\nds_read_b64_tr_b16 v[196:197],v242 offset:40960\nds_read_b64_tr_b16 v[198:199],v242 offset:41216\nM v[20:23],v[200:203],a[68:71],v[20:23]\ns_add_u32 m0,s65,12288\n" \
"s_add_u32 s67,s66,33280\nM v[80:83],v[200:203],a[72:75],v[80:83]\ns_add_u32 s14,s61,3\ns_and_b32 s10,s10,3\nM v[84:87],v[200:203],a[76:79],v[84:87]\nds_read_b64_tr_b16 v[200:201],v241 offset:41472\ns_waitcnt lgkmcnt(5)\nM v[24:27],v[204:207],a[64:67],v[24:27]\n" \
"buffer_load_dwordx4 v224,s[36:39],s67 offen offset:0 lds\nM v[28:31],v[204:207],a[68:71],v[28:31]\nds_read_b64_tr_b16 v[202:203],v241 offset:41728\ns_and_b32 s11,s11,3\nM v[88:91],v[204:207],a[72:75],v[88:91]\ns_and_b32 s14,s14,3\ns_mov_b32 s47,0x20000\nM v[92:95],v[204:207],a[76:79],v[92:95]\n" \
"ds_read_b64_tr_b16 v[204:205],v242 offset:41472\nds_read_b64_tr_b16 v[206:207],v242 offset:41728\nM v[32:35],v[208:211],a[64:67],v[32:35]\ns_mov_b32 s51,0x20000\ns_mov_b32 s94,0xc000\nM v[36:39],v[208:211],a[68:71],v[36:39]\nbuffer_load_dwordx4 v225,s[36:39],s67 offen offset:1024 lds\nM v[96:99],v[208:211],a[72:75],v[96:99]\n" \
"s_lshl_b32 s10,s10,13\ns_lshl_b32 s11,s11,13\nM v[100:103],v[208:211],a[76:79],v[100:103]\nds_read_b64_tr_b16 v[208:209],v241 offset:41984\nds_read_b64_tr_b16 v[210:211],v241 offset:42240\nM v[40:43],v[212:215],a[64:67],v[40:43]\ns_lshl_b32 s14,s14,13\ns_mov_b32 s82,0xf0000\n" \
"M v[44:47],v[212:215],a[68:71],v[44:47]\ns_mov_b32 s83,0xfff00ff\ns_mov_b32 s84,0x1f0001\nM v[104:107],v[212:215],a[72:75],v[104:107]\nbuffer_load_dwordx4 v226,s[36:39],s67 offen offset:2048 lds\nM v[108:111],v[212:215],a[76:79],v[108:111]\nds_read_b64_tr_b16 v[212:213],v242 offset:41984\nds_read_b64_tr_b16 v[214:215],v242 offset:42240\n" \
"M v[48:51],v[216:219],a[64:67],v[48:51]\ns_mov_b32 s85,0x1fff01ff\ns_mov_b32 s86,0x3f0003\nM v[52:55],v[216:219],a[68:71],v[52:55]\ns_mov_b32 s87,0x3fff03ff\ns_mov_b32 s88,0x7f0007\nM v[112:115],v[216:219],a[72:75],v[112:115]\ns_mov_b32 s89,0x7fff07ff\n" \
"s_mov_b64 s[90:91],-1\nM v[116:119],v[216:219],a[76:79],v[116:119]\nbuffer_load_dwordx4 v227,s[36:39],s67 offen offset:3072 lds\nM v[56:59],v[220:223],a[64:67],v[56:59]\nds_read_b64_tr_b16 v[216:217],v241 offset:42496\nds_read_b64_tr_b16 v[218:219],v241 offset:42752\nM v[60:63],v[220:223],a[68:71],v[60:63]\ns_add_u32 m0,s64,98304\n" \
"s_add_u32 s69,s68,2097152\nM v[120:123],v[220:223],a[72:75],v[120:123]\ns_mov_b32 s28,0x5f800000\ns_mov_b32 s99,0\nM v[124:127],v[220:223],a[76:79],v[124:127]\nds_read_b64_tr_b16 v[220:221],v242 offset:42496\nds_read_b64_tr_b16 v[222:223],v242 offset:42752\ns_waitcnt lgkmcnt(6)\n" \
"M v[0:3],v[192:195],a[80:83],v[0:3]\nbuffer_load_dwordx4 v243,s[40:43],s69 offen offset:0 lds\nM v[4:7],v[192:195],a[84:87],v[4:7]\nM v[64:67],v[192:195],a[88:91],v[64:67]\nM v[68:71],v[192:195],a[92:95],v[68:71]\nM v[8:11],v[196:199],a[80:83],v[8:11]\nM v[12:15],v[196:199],a[84:87],v[12:15]\nbuffer_load_dwordx4 v244,s[40:43],s69 offen offset:1024 lds\n" \
"M v[72:75],v[196:199],a[88:91],v[72:75]\nM v[76:79],v[196:199],a[92:95],v[76:79]\nM v[16:19],v[200:203],a[80:83],v[16:19]\nM v[20:23],v[200:203],a[84:87],v[20:23]\nM v[80:83],v[200:203],a[88:91],v[80:83]\nbuffer_load_dwordx4 v245,s[40:43],s69 offen offset:2048 lds\nM v[84:87],v[200:203],a[92:95],v[84:87]\nM v[24:27],v[204:207],a[80:83],v[24:27]\n" \
"M v[28:31],v[204:207],a[84:87],v[28:31]\nM v[88:91],v[204:207],a[88:91],v[88:91]\ns_waitcnt vmcnt(12) lgkmcnt(0)\ns_barrier\nds_read_b128 a[96:99],v228 offset:0\nds_read_b128 a[100:103],v228 offset:2048\nds_read_b128 a[104:107],v228 offset:4096\nds_read_b128 a[108:111],v228 offset:6144\n" \
"ds_read_b128 a[112:115],v229 offset:0\nds_read_b128 a[116:119],v229 offset:2048\nds_read_b128 a[120:123],v229 offset:4096\nds_read_b128 a[124:127],v229 offset:6144\nM v[92:95],v[204:207],a[92:95],v[92:95]\nbuffer_load_dwordx4 v246,s[40:43],s69 offen offset:3072 lds\nM v[32:35],v[208:211],a[80:83],v[32:35]\nM v[36:39],v[208:211],a[84:87],v[36:39]\n" \
"s_add_u32 m0,s65,16384\ns_add_u32 s67,s66,640\nM v[96:99],v[208:211],a[88:91],v[96:99]\nM v[100:103],v[208:211],a[92:95],v[100:103]\nM v[40:43],v[212:215],a[80:83],v[40:43]\nbuffer_load_dwordx4 v224,s[36:39],s67 offen offset:0 lds\nM v[44:47],v[212:215],a[84:87],v[44:47]\nds_read_b64_tr_b16 v[192:193],v241 offset:49152\n" \
"ds_read_b64_tr_b16 v[194:195],v241 offset:49408\nM v[104:107],v[212:215],a[88:91],v[104:107]\nds_read_b64_tr_b16 v[196:197],v242 offset:49152\nM v[108:111],v[212:215],a[92:95],v[108:111]\nds_read_b64_tr_b16 v[198:199],v242 offset:49408\nds_read_b64_tr_b16 v[200:201],v241 offset:49664\nM v[48:51],v[216:219],a[80:83],v[48:51]\nds_read_b64_tr_b16 v[202:203],v241 offset:49920\n" \
"ds_read_b64_tr_b16 v[204:205],v242 offset:49664\nM v[52:55],v[216:219],a[84:87],v[52:55]\nbuffer_load_dwordx4 v225,s[36:39],s67 offen offset:1024 lds\nM v[112:115],v[216:219],a[88:91],v[112:115]\nds_read_b64_tr_b16 v[206:207],v242 offset:49920\nds_read_b64_tr_b16 v[208:209],v241 offset:50176\nM v[116:119],v[216:219],a[92:95],v[116:119]\nds_read_b64_tr_b16 v[210:211],v241 offset:50432\n" \
"ds_read_b64_tr_b16 v[212:213],v242 offset:50176\nM v[56:59],v[220:223],a[80:83],v[56:59]\nds_read_b64_tr_b16 v[214:215],v242 offset:50432\nds_read_b64_tr_b16 v[216:217],v241 offset:50688\nM v[60:63],v[220:223],a[84:87],v[60:63]\nds_read_b64_tr_b16 v[218:219],v241 offset:50944\nM v[120:123],v[220:223],a[88:91],v[120:123]\nbuffer_load_dwordx4 v226,s[36:39],s67 offen offset:2048 lds\n" \
"M v[124:127],v[220:223],a[92:95],v[124:127]\nds_read_b64_tr_b16 v[220:221],v242 offset:50688\ns_waitcnt lgkmcnt(13)\nM v[0:3],v[192:195],a[96:99],v[0:3]\nds_read_b64_tr_b16 v[222:223],v242 offset:50944\nM v[4:7],v[192:195],a[100:103],v[4:7]\nM v[64:67],v[192:195],a[104:107],v[64:67]\nM v[68:71],v[192:195],a[108:111],v[68:71]\n" \
"buffer_load_dwordx4 v227,s[36:39],s67 offen offset:3072 lds\ns_waitcnt lgkmcnt(11)\nM v[8:11],v[196:199],a[96:99],v[8:11]\nds_read_b64_tr_b16 v[192:193],v241 offset:57344\nds_read_b64_tr_b16 v[194:195],v241 offset:57600\nM v[12:15],v[196:199],a[100:103],v[12:15]\ns_add_u32 m0,s65,20480\ns_add_u32 s67,s66,33408\n" \
"M v[72:75],v[196:199],a[104:107],v[72:75]\nM v[76:79],v[196:199],a[108:111],v[76:79]\nds_read_b64_tr_b16 v[196:197],v242 offset:57344\ns_waitcnt lgkmcnt(8)\nM v[16:19],v[200:203],a[96:99],v[16:19]\nbuffer_load_dwordx4 v224,s[36:39],s67 offen offset:0 lds\nM v[20:23],v[200:203],a[100:103],v[20:23]\nds_read_b64_tr_b16 v[198:199],v242 offset:57600\n" \
"M v[80:83],v[200:203],a[104:107],v[80:83]\nM v[84:87],v[200:203],a[108:111],v[84:87]\nds_read_b64_tr_b16 v[200:201],v241 offset:57856\nds_read_b64_tr_b16 v[202:203],v241 offset:58112\nM v[24:27],v[204:207],a[96:99],v[24:27]\nM v[28:31],v[204:207],a[100:103],v[28:31]\nbuffer_load_dwordx4 v225,s[36:39],s67 offen offset:1024 lds\nM v[88:91],v[204:207],a[104:107],v[88:91]\n" \
"M v[92:95],v[204:207],a[108:111],v[92:95]\nds_read_b64_tr_b16 v[204:205],v242 offset:57856\nds_read_b64_tr_b16 v[206:207],v242 offset:58112\nM v[32:35],v[208:211],a[96:99],v[32:35]\nM v[36:39],v[208:211],a[100:103],v[36:39]\nM v[96:99],v[208:211],a[104:107],v[96:99]\nbuffer_load_dwordx4 v226,s[36:39],s67 offen offset:2048 lds\nM v[100:103],v[208:211],a[108:111],v[100:103]\n" \
"ds_read_b64_tr_b16 v[208:209],v241 offset:58368\ns_waitcnt lgkmcnt(7)\nM v[40:43],v[212:215],a[96:99],v[40:43]\nds_read_b64_tr_b16 v[210:211],v241 offset:58624\nM v[44:47],v[212:215],a[100:103],v[44:47]\nM v[104:107],v[212:215],a[104:107],v[104:107]\nM v[108:111],v[212:215],a[108:111],v[108:111]\nbuffer_load_dwordx4 v227,s[36:39],s67 offen offset:3072 lds\n" \
"M v[48:51],v[216:219],a[96:99],v[48:51]\nds_read_b64_tr_b16 v[212:213],v242 offset:58368\nds_read_b64_tr_b16 v[214:215],v242 offset:58624\nM v[52:55],v[216:219],a[100:103],v[52:55]\ns_add_u32 m0,s64,114688\ns_add_u32 s69,s68,2621440\nM v[112:115],v[216:219],a[104:107],v[112:115]\nM v[116:119],v[216:219],a[108:111],v[116:119]\n" \
"ds_read_b64_tr_b16 v[216:217],v241 offset:58880\nds_read_b64_tr_b16 v[218:219],v241 offset:59136\nM v[56:59],v[220:223],a[96:99],v[56:59]\nbuffer_load_dwordx4 v243,s[40:43],s69 offen offset:0 lds\nM v[60:63],v[220:223],a[100:103],v[60:63]\nM v[120:123],v[220:223],a[104:107],v[120:123]\nM v[124:127],v[220:223],a[108:111],v[124:127]\nds_read_b64_tr_b16 v[220:221],v242 offset:58880\n" \
"ds_read_b64_tr_b16 v[222:223],v242 offset:59136\nM v[0:3],v[192:195],a[112:115],v[0:3]\nbuffer_load_dwordx2 v[166:167],v165,s[52:55],s80 offen sc1\nM v[4:7],v[192:195],a[116:119],v[4:7]\nbuffer_load_dwordx4 v244,s[40:43],s69 offen offset:1024 lds\nM v[64:67],v[192:195],a[120:123],v[64:67]\nM v[68:71],v[192:195],a[124:127],v[68:71]\ns_waitcnt lgkmcnt(6)\n" \
"M v[8:11],v[196:199],a[112:115],v[8:11]\nM v[12:15],v[196:199],a[116:119],v[12:15]\nM v[72:75],v[196:199],a[120:123],v[72:75]\nbuffer_load_dwordx4 v245,s[40:43],s69 offen offset:2048 lds\nM v[76:79],v[196:199],a[124:127],v[76:79]\nM v[16:19],v[200:203],a[112:115],v[16:19]\nM v[20:23],v[200:203],a[116:119],v[20:23]\nM v[80:83],v[200:203],a[120:123],v[80:83]\n" \
"M v[84:87],v[200:203],a[124:127],v[84:87]\nbuffer_load_dwordx4 v246,s[40:43],s69 offen offset:3072 lds\nM v[24:27],v[204:207],a[112:115],v[24:27]\nM v[28:31],v[204:207],a[116:119],v[28:31]\ns_add_u32 m0,s65,0\ns_add_u32 s67,s66,768\nM v[88:91],v[204:207],a[120:123],v[88:91]\ns_waitcnt vmcnt(13) lgkmcnt(0)\n" \
"s_barrier\nM v[92:95],v[204:207],a[124:127],v[92:95]\nds_read_b128 a[128:131],v228 offset:8192\nds_read_b64_tr_b16 v[192:193],v241 offset:0\nM v[32:35],v[208:211],a[112:115],v[32:35]\nbuffer_load_dwordx4 v224,s[36:39],s67 offen offset:0 lds\nM v[36:39],v[208:211],a[116:119],v[36:39]\nds_read_b64_tr_b16 v[194:195],v241 offset:256\n" \
"ds_read_b128 a[132:135],v228 offset:10240\nM v[96:99],v[208:211],a[120:123],v[96:99]\nds_read_b128 a[136:139],v228 offset:12288\nM v[100:103],v[208:211],a[124:127],v[100:103]\nds_read_b128 a[140:143],v228 offset:14336\nM v[40:43],v[212:215],a[112:115],v[40:43]\nds_read_b64_tr_b16 v[196:197],v242 offset:0\nds_read_b64_tr_b16 v[198:199],v242 offset:256\n" \
"M v[44:47],v[212:215],a[116:119],v[44:47]\nbuffer_load_dwordx4 v225,s[36:39],s67 offen offset:1024 lds\nM v[104:107],v[212:215],a[120:123],v[104:107]\nds_read_b64_tr_b16 v[200:201],v241 offset:512\nds_read_b64_tr_b16 v[202:203],v241 offset:768\nM v[108:111],v[212:215],a[124:127],v[108:111]\nds_read_b64_tr_b16 v[204:205],v242 offset:512\nds_read_b64_tr_b16 v[206:207],v242 offset:768\n" \
"M v[48:51],v[216:219],a[112:115],v[48:51]\nds_read_b64_tr_b16 v[208:209],v241 offset:1024\nds_read_b64_tr_b16 v[210:211],v241 offset:1280\nM v[52:55],v[216:219],a[116:119],v[52:55]\nds_read_b64_tr_b16 v[212:213],v242 offset:1024\nds_read_b64_tr_b16 v[214:215],v242 offset:1280\nM v[112:115],v[216:219],a[120:123],v[112:115]\nbuffer_load_dwordx4 v226,s[36:39],s67 offen offset:2048 lds\n" \
"M v[116:119],v[216:219],a[124:127],v[116:119]\nds_read_b64_tr_b16 v[216:217],v241 offset:1536\nds_read_b64_tr_b16 v[218:219],v241 offset:1792\nM v[56:59],v[220:223],a[112:115],v[56:59]\nds_read_b128 a[144:147],v229 offset:8192\nds_read_b128 a[148:151],v229 offset:10240\nM v[60:63],v[220:223],a[116:119],v[60:63]\nds_read_b128 a[152:155],v229 offset:12288\n" \
"M v[120:123],v[220:223],a[120:123],v[120:123]\nds_read_b128 a[156:159],v229 offset:14336\nM v[124:127],v[220:223],a[124:127],v[124:127]\nbuffer_load_dwordx4 v227,s[36:39],s67 offen offset:3072 lds\ns_waitcnt lgkmcnt(14)\nM v[0:3],v[192:195],a[128:131],v[0:3]\nds_read_b64_tr_b16 v[220:221],v242 offset:1536\nds_read_b64_tr_b16 v[222:223],v242 offset:1792\n" \
"M v[4:7],v[192:195],a[132:135],v[4:7]\ns_add_u32 m0,s65,4096\ns_add_u32 s67,s66,33536\nM v[64:67],v[192:195],a[136:139],v[64:67]\nM v[68:71],v[192:195],a[140:143],v[68:71]\nds_read_b64_tr_b16 v[192:193],v241 offset:8192\nds_read_b64_tr_b16 v[194:195],v241 offset:8448\nM v[8:11],v[196:199],a[128:131],v[8:11]\n" \
"buffer_load_dwordx4 v224,s[36:39],s67 offen offset:0 lds\nM v[12:15],v[196:199],a[132:135],v[12:15]\nM v[72:75],v[196:199],a[136:139],v[72:75]\nM v[76:79],v[196:199],a[140:143],v[76:79]\nds_read_b64_tr_b16 v[196:197],v242 offset:8192\ns_waitcnt lgkmcnt(9)\nM v[16:19],v[200:203],a[128:131],v[16:19]\nds_read_b64_tr_b16 v[198:199],v242 offset:8448\n" \
"M v[20:23],v[200:203],a[132:135],v[20:23]\nbuffer_load_dwordx4 v225,s[36:39],s67 offen offset:1024 lds\nM v[80:83],v[200:203],a[136:139],v[80:83]\nM v[84:87],v[200:203],a[140:143],v[84:87]\nds_read_b64_tr_b16 v[200:201],v241 offset:8704\nds_read_b64_tr_b16 v[202:203],v241 offset:8960\nM v[24:27],v[204:207],a[128:131],v[24:27]\nM v[28:31],v[204:207],a[132:135],v[28:31]\n" \
"M v[88:91],v[204:207],a[136:139],v[88:91]\nbuffer_load_dwordx4 v226,s[36:39],s67 offen offset:2048 lds\nM v[92:95],v[204:207],a[140:143],v[92:95]\nds_read_b64_tr_b16 v[204:205],v242 offset:8704\nds_read_b64_tr_b16 v[206:207],v242 offset:8960\nM v[32:35],v[208:211],a[128:131],v[32:35]\nM v[36:39],v[208:211],a[132:135],v[36:39]\nM v[96:99],v[208:211],a[136:139],v[96:99]\n" \
"M v[100:103],v[208:211],a[140:143],v[100:103]\nbuffer_load_dwordx4 v227,s[36:39],s67 offen offset:3072 lds\nM v[40:43],v[212:215],a[128:131],v[40:43]\nds_read_b64_tr_b16 v[208:209],v241 offset:9216\nds_read_b64_tr_b16 v[210:211],v241 offset:9472\nM v[44:47],v[212:215],a[132:135],v[44:47]\ns_add_u32 m0,s64,131072\ns_add_u32 s69,s68,3145728\n" \
"M v[104:107],v[212:215],a[136:139],v[104:107]\nM v[108:111],v[212:215],a[140:143],v[108:111]\nds_read_b64_tr_b16 v[212:213],v242 offset:9216\nds_read_b64_tr_b16 v[214:215],v242 offset:9472\nM v[48:51],v[216:219],a[128:131],v[48:51]\nbuffer_load_dwordx4 v243,s[40:43],s69 offen offset:0 lds\nM v[52:55],v[216:219],a[132:135],v[52:55]\nM v[112:115],v[216:219],a[136:139],v[112:115]\n" \
"M v[116:119],v[216:219],a[140:143],v[116:119]\nds_read_b64_tr_b16 v[216:217],v241 offset:9728\ns_waitcnt lgkmcnt(7)\nM v[56:59],v[220:223],a[128:131],v[56:59]\nds_read_b64_tr_b16 v[218:219],v241 offset:9984\nM v[60:63],v[220:223],a[132:135],v[60:63]\nbuffer_load_dwordx4 v244,s[40:43],s69 offen offset:1024 lds\nM v[120:123],v[220:223],a[136:139],v[120:123]\n" \
"M v[124:127],v[220:223],a[140:143],v[124:127]\nds_read_b64_tr_b16 v[220:221],v242 offset:9728\nds_read_b64_tr_b16 v[222:223],v242 offset:9984\nM v[0:3],v[192:195],a[144:147],v[0:3]\nM v[4:7],v[192:195],a[148:151],v[4:7]\nM v[64:67],v[192:195],a[152:155],v[64:67]\nbuffer_load_dwordx4 v245,s[40:43],s69 offen offset:2048 lds\nM v[68:71],v[192:195],a[156:159],v[68:71]\n" \
"M v[8:11],v[196:199],a[144:147],v[8:11]\ns_waitcnt vmcnt(14)\nv_xor_b32 v166,v166,v230\nM v[12:15],v[196:199],a[148:151],v[12:15]\nv_xor_b32 v167,v167,v231\nv_or_b32 v166,v166,v167\nM v[72:75],v[196:199],a[152:155],v[72:75]\nv_cmp_ne_u32 vcc,0,v166\n" \
"M v[76:79],v[196:199],a[156:159],v[76:79]\nbuffer_load_dwordx4 v246,s[40:43],s69 offen offset:3072 lds\nM v[16:19],v[200:203],a[144:147],v[16:19]\ns_cbranch_vccnz .Lhs\n.Lhb:\nv_bfe_u32 v231,v249,2,3\nM v[20:23],v[200:203],a[148:151],v[20:23]\ns_add_u32 m0,s65,8192\n" \
"s_add_u32 s67,s66,896\nM v[80:83],v[200:203],a[152:155],v[80:83]\nv_mul_u32_u24 v231,3584,v231\nv_and_b32 v230,31,v249\nM v[84:87],v[200:203],a[156:159],v[84:87]\nv_lshlrev_b32 v230,7,v230\ns_waitcnt lgkmcnt(0)\nM v[24:27],v[204:207],a[144:147],v[24:27]\n" \
"buffer_load_dwordx4 v224,s[36:39],s67 offen offset:0 lds\nM v[28:31],v[204:207],a[148:151],v[28:31]\nM v[88:91],v[204:207],a[152:155],v[88:91]\ns_waitcnt vmcnt(13)\ns_barrier\nM v[92:95],v[204:207],a[156:159],v[92:95]\nds_read_b128 a[160:163],v228 offset:16384\nds_read_b64_tr_b16 v[192:193],v241 offset:16384\n" \
"M v[32:35],v[208:211],a[144:147],v[32:35]\nbuffer_load_dwordx4 v[152:155],v240,s[52:55],s71 offen sc1\nM v[36:39],v[208:211],a[148:151],v[36:39]\ns_add_u32 s71,s70,798720\nds_read_b64_tr_b16 v[194:195],v241 offset:16640\nM v[96:99],v[208:211],a[152:155],v[96:99]\nds_read_b128 a[164:167],v228 offset:18432\nds_read_b128 a[168:171],v228 offset:20480\n" \
"M v[100:103],v[208:211],a[156:159],v[100:103]\nbuffer_load_dwordx4 v225,s[36:39],s67 offen offset:1024 lds\nbuffer_load_dwordx4 v[156:159],v240,s[52:55],s71 offen sc1\nM v[40:43],v[212:215],a[144:147],v[40:43]\ns_add_u32 s71,s70,811008\nds_read_b128 a[172:175],v228 offset:22528\nM v[44:47],v[212:215],a[148:151],v[44:47]\nbuffer_load_dwordx4 v[160:163],v240,s[52:55],s71 offen sc1\n" \
"M v[104:107],v[212:215],a[152:155],v[104:107]\nbuffer_load_dwordx4 v226,s[36:39],s67 offen offset:2048 lds\ns_add_u32 s71,s70,823296\nbuffer_load_dwordx4 v[164:167],v240,s[52:55],s71 offen sc1\nM v[108:111],v[212:215],a[156:159],v[108:111]\nds_read_b64_tr_b16 v[196:197],v242 offset:16384\nds_read_b64_tr_b16 v[198:199],v242 offset:16640\nM v[48:51],v[216:219],a[144:147],v[48:51]\n" \
"ds_read_b64_tr_b16 v[200:201],v241 offset:16896\nds_read_b64_tr_b16 v[202:203],v241 offset:17152\nM v[52:55],v[216:219],a[148:151],v[52:55]\nds_read_b64_tr_b16 v[204:205],v242 offset:16896\nds_read_b64_tr_b16 v[206:207],v242 offset:17152\nM v[112:115],v[216:219],a[152:155],v[112:115]\nds_read_b64_tr_b16 v[208:209],v241 offset:17408\nds_read_b64_tr_b16 v[210:211],v241 offset:17664\n" \
"M v[116:119],v[216:219],a[156:159],v[116:119]\nbuffer_load_dwordx4 v227,s[36:39],s67 offen offset:3072 lds\nM v[56:59],v[220:223],a[144:147],v[56:59]\nds_read_b64_tr_b16 v[212:213],v242 offset:17408\nds_read_b64_tr_b16 v[214:215],v242 offset:17664\nM v[60:63],v[220:223],a[148:151],v[60:63]\ns_add_u32 m0,s65,12288\ns_add_u32 s67,s66,33664\n" \
"M v[120:123],v[220:223],a[152:155],v[120:123]\nds_read_b64_tr_b16 v[216:217],v241 offset:17920\nds_read_b64_tr_b16 v[218:219],v241 offset:18176\nM v[124:127],v[220:223],a[156:159],v[124:127]\nds_read_b64_tr_b16 v[220:221],v242 offset:17920\ns_waitcnt lgkmcnt(13)\nM v[0:3],v[192:195],a[160:163],v[0:3]\nds_read_b64_tr_b16 v[222:223],v242 offset:18176\n" \
"ds_read_b128 a[176:179],v229 offset:16384\nM v[4:7],v[192:195],a[164:167],v[4:7]\nds_read_b128 a[180:183],v229 offset:18432\ns_lshl_b32 s71,s6,5\nM v[64:67],v[192:195],a[168:171],v[64:67]\nds_read_b128 a[184:187],v229 offset:20480\ns_add_u32 s71,s71,790528\nM v[68:71],v[192:195],a[172:175],v[68:71]\n" \
"ds_read_b64_tr_b16 v[192:193],v241 offset:24576\ns_waitcnt lgkmcnt(13)\nM v[8:11],v[196:199],a[160:163],v[8:11]\nds_read_b64_tr_b16 v[194:195],v241 offset:24832\nds_read_b128 a[188:191],v229 offset:22528\nM v[12:15],v[196:199],a[164:167],v[12:15]\ns_cmp_lt_u32 s7,2\ns_cselect_b32 s70,0,1.0\n" \
"M v[72:75],v[196:199],a[168:171],v[72:75]\nglobal_load_dword v247,v253,s[12:13]\nM v[76:79],v[196:199],a[172:175],v[76:79]\nds_read_b64_tr_b16 v[196:197],v242 offset:24576\nds_read_b64_tr_b16 v[198:199],v242 offset:24832\nM v[16:19],v[200:203],a[160:163],v[16:19]\nv_add_u32 v253,0x100000,v253\nM v[20:23],v[200:203],a[164:167],v[20:23]\n" \
"M v[80:83],v[200:203],a[168:171],v[80:83]\nbuffer_load_dword v247,v254,s[56:59],s78 offen\nM v[84:87],v[200:203],a[172:175],v[84:87]\nds_read_b64_tr_b16 v[200:201],v241 offset:25088\ns_waitcnt lgkmcnt(8)\nM v[24:27],v[204:207],a[160:163],v[24:27]\nds_read_b64_tr_b16 v[202:203],v241 offset:25344\nv_and_b32 v254,3,v249\n" \
"M v[28:31],v[204:207],a[164:167],v[28:31]\ns_cmp_eq_u32 s62,5\ns_cselect_b64 s[56:57],s[22:23],s[24:25]\nM v[88:91],v[204:207],a[168:171],v[88:91]\nbuffer_load_dwordx4 v224,s[36:39],s67 offen offset:0 lds\nM v[92:95],v[204:207],a[172:175],v[92:95]\nds_read_b64_tr_b16 v[204:205],v242 offset:25088\nds_read_b64_tr_b16 v[206:207],v242 offset:25344\n" \
"M v[32:35],v[208:211],a[160:163],v[32:35]\nv_lshl_add_u32 v231,v254,7,v231\nM v[36:39],v[208:211],a[164:167],v[36:39]\ns_add_u32 s69,s68,3670016\nM v[96:99],v[208:211],a[168:171],v[96:99]\nbuffer_load_dwordx4 v225,s[36:39],s67 offen offset:1024 lds\nM v[100:103],v[208:211],a[172:175],v[100:103]\nds_read_b64_tr_b16 v[208:209],v241 offset:25600\n" \
"ds_read_b64_tr_b16 v[210:211],v241 offset:25856\nM v[40:43],v[212:215],a[160:163],v[40:43]\nM v[44:47],v[212:215],a[164:167],v[44:47]\nM v[104:107],v[212:215],a[168:171],v[104:107]\nbuffer_load_dwordx4 v226,s[36:39],s67 offen offset:2048 lds\nM v[108:111],v[212:215],a[172:175],v[108:111]\nds_read_b64_tr_b16 v[212:213],v242 offset:25600\nds_read_b64_tr_b16 v[214:215],v242 offset:25856\n" \
"M v[48:51],v[216:219],a[160:163],v[48:51]\nM v[52:55],v[216:219],a[164:167],v[52:55]\nM v[112:115],v[216:219],a[168:171],v[112:115]\nbuffer_load_dwordx4 v227,s[36:39],s67 offen offset:3072 lds\nM v[116:119],v[216:219],a[172:175],v[116:119]\ns_add_u32 m0,s64,147456\nds_read_b64_tr_b16 v[216:217],v241 offset:26112\nM v[56:59],v[220:223],a[160:163],v[56:59]\n" \
"s_cmp_eq_u32 s62,0\ns_cselect_b32 s36,0x8000,0\nM v[60:63],v[220:223],a[164:167],v[60:63]\nds_read_b64_tr_b16 v[218:219],v241 offset:26368\ns_add_u32 s36,s36,s64\nM v[120:123],v[220:223],a[168:171],v[120:123]\nbuffer_load_dwordx4 v243,s[40:43],s69 offen offset:0 lds\nM v[124:127],v[220:223],a[172:175],v[124:127]\n" \
"ds_read_b64_tr_b16 v[220:221],v242 offset:26112\ns_waitcnt lgkmcnt(6)\nM v[0:3],v[192:195],a[176:179],v[0:3]\nds_read_b64_tr_b16 v[222:223],v242 offset:26368\nM v[4:7],v[192:195],a[180:183],v[4:7]\nM v[64:67],v[192:195],a[184:187],v[64:67]\nbuffer_load_dwordx4 v244,s[40:43],s69 offen offset:1024 lds\nM v[68:71],v[192:195],a[188:191],v[68:71]\n" \
"M v[8:11],v[196:199],a[176:179],v[8:11]\nM v[12:15],v[196:199],a[180:183],v[12:15]\nM v[72:75],v[196:199],a[184:187],v[72:75]\nbuffer_load_dwordx4 v245,s[40:43],s69 offen offset:2048 lds\nM v[76:79],v[196:199],a[188:191],v[76:79]\nM v[16:19],v[200:203],a[176:179],v[16:19]\nM v[20:23],v[200:203],a[180:183],v[20:23]\nM v[80:83],v[200:203],a[184:187],v[80:83]\n" \
"global_load_dword v247,v253,s[12:13]\nM v[84:87],v[200:203],a[188:191],v[84:87]\nbuffer_load_dwordx4 v246,s[40:43],s69 offen offset:3072 lds\nM v[24:27],v[204:207],a[176:179],v[24:27]\nv_add_u32 v253,0x100000,v253\ns_add_u32 m0,s64,98304\nM v[28:31],v[204:207],a[180:183],v[28:31]\ns_sub_u32 s40,s16,0x1000\n" \
"s_subb_u32 s41,s17,0\nM v[88:91],v[204:207],a[184:187],v[88:91]\ns_waitcnt vmcnt(19) lgkmcnt(0)\ns_barrier\nM v[92:95],v[204:207],a[188:191],v[92:95]\nds_read_b128 a[192:195],v228 offset:0\nds_read_b64_tr_b16 v[192:193],v241 offset:32768\nM v[32:35],v[208:211],a[176:179],v[32:35]\n" \
"ds_read_b64_tr_b16 v[194:195],v241 offset:33024\nds_read_b128 a[196:199],v228 offset:2048\nM v[36:39],v[208:211],a[180:183],v[36:39]\nds_read_b128 a[200:203],v228 offset:4096\ns_add_u32 s69,s68,0\nM v[96:99],v[208:211],a[184:187],v[96:99]\nds_read_b128 a[204:207],v228 offset:6144\ns_mov_b32 s16,0\n" \
"M v[100:103],v[208:211],a[188:191],v[100:103]\nds_read_b64_tr_b16 v[196:197],v242 offset:32768\ns_mov_b32 s17,3\nM v[40:43],v[212:215],a[176:179],v[40:43]\nds_read_b64_tr_b16 v[198:199],v242 offset:33024\nds_read_b64_tr_b16 v[200:201],v241 offset:33280\nM v[44:47],v[212:215],a[180:183],v[44:47]\nds_read_b64_tr_b16 v[202:203],v241 offset:33536\n" \
"ds_read_b64_tr_b16 v[204:205],v242 offset:33280\nM v[104:107],v[212:215],a[184:187],v[104:107]\nds_read_b64_tr_b16 v[206:207],v242 offset:33536\nds_read_b64_tr_b16 v[208:209],v241 offset:33792\nM v[108:111],v[212:215],a[188:191],v[108:111]\nds_read_b64_tr_b16 v[210:211],v241 offset:34048\nds_read_b64_tr_b16 v[212:213],v242 offset:33792\nM v[48:51],v[216:219],a[176:179],v[48:51]\n" \
"ds_read_b64_tr_b16 v[214:215],v242 offset:34048\nds_read_b128 a[208:211],v229 offset:0\nM v[52:55],v[216:219],a[180:183],v[52:55]\nds_read_b128 a[212:215],v229 offset:2048\ns_waitcnt vmcnt(14)\nv_add_f32 v152,v152,v156\nM v[112:115],v[216:219],a[184:187],v[112:115]\nds_read_b128 a[216:219],v229 offset:4096\n" \
"v_add_f32 v153,v153,v157\nM v[116:119],v[216:219],a[188:191],v[116:119]\nds_read_b64_tr_b16 v[216:217],v241 offset:34304\nv_add_f32 v154,v154,v158\nM v[56:59],v[220:223],a[176:179],v[56:59]\nds_read_b64_tr_b16 v[218:219],v241 offset:34560\nds_read_b128 a[220:223],v229 offset:6144\nM v[60:63],v[220:223],a[180:183],v[60:63]\n" \
"s_waitcnt vmcnt(12)\nv_add_f32 v160,v160,v164\nM v[120:123],v[220:223],a[184:187],v[120:123]\nv_add_f32 v161,v161,v165\nv_add_f32 v162,v162,v166\nM v[124:127],v[220:223],a[188:191],v[124:127]\nds_read_b64_tr_b16 v[220:221],v242 offset:34304\ns_waitcnt lgkmcnt(12)\n" \
"M v[0:3],v[192:195],a[192:195],v[0:3]\nds_read_b64_tr_b16 v[222:223],v242 offset:34560\nv_add_f32 v155,v155,v159\nM v[4:7],v[192:195],a[196:199],v[4:7]\nv_add_f32 v163,v163,v167\nv_add_f32 v152,v152,v160\nM v[64:67],v[192:195],a[200:203],v[64:67]\nbuffer_load_dword v168,v169,s[52:55],s71 offen sc1\n" \
"M v[68:71],v[192:195],a[204:207],v[68:71]\nds_read_b64_tr_b16 v[192:193],v241 offset:40960\nds_read_b64_tr_b16 v[194:195],v241 offset:41216\nM v[8:11],v[196:199],a[192:195],v[8:11]\nv_add_f32 v153,v153,v161\nv_add_f32 v154,v154,v162\nM v[12:15],v[196:199],a[196:199],v[12:15]\nv_add_f32 v155,v155,v163\n" \
"v_cvt_pk_bf16_f32 v152,0,v152\nM v[72:75],v[196:199],a[200:203],v[72:75]\nv_cvt_pk_bf16_f32 v153,0,v153\nv_cvt_pk_bf16_f32 v154,0,v154\nM v[76:79],v[196:199],a[204:207],v[76:79]\nds_read_b64_tr_b16 v[196:197],v242 offset:40960\nds_read_b64_tr_b16 v[198:199],v242 offset:41216\nM v[16:19],v[200:203],a[192:195],v[16:19]\n" \
"v_cvt_pk_bf16_f32 v155,0,v155\nv_add_f32 v152,s70,v152\nM v[20:23],v[200:203],a[196:199],v[20:23]\nv_add_f32 v153,s70,v153\nv_add_f32 v154,s70,v154\nM v[80:83],v[200:203],a[200:203],v[80:83]\nv_add_f32 v155,s70,v155\ns_lshr_b32 s71,s64,2\n" \
"M v[84:87],v[200:203],a[204:207],v[84:87]\nds_read_b64_tr_b16 v[200:201],v241 offset:41472\ns_waitcnt lgkmcnt(5)\nM v[24:27],v[204:207],a[192:195],v[24:27]\nds_read_b64_tr_b16 v[202:203],v241 offset:41728\nv_cvt_pk_bf16_f32 v152,0,v152\nM v[28:31],v[204:207],a[196:199],v[28:31]\nv_cvt_pk_bf16_f32 v153,0,v153\n" \
"v_cvt_pk_bf16_f32 v154,0,v154\nM v[88:91],v[204:207],a[200:203],v[88:91]\nv_cvt_pk_bf16_f32 v155,0,v155\nv_add_u32 v171,s71,v240\nM v[92:95],v[204:207],a[204:207],v[92:95]\nds_read_b64_tr_b16 v[204:205],v242 offset:41472\nds_read_b64_tr_b16 v[206:207],v242 offset:41728\nM v[32:35],v[208:211],a[192:195],v[32:35]\n" \
"ds_write_b128 v171,v[152:155] offset:16384\nM v[36:39],v[208:211],a[196:199],v[36:39]\nbuffer_load_dwordx4 v243,s[40:43],s69 offen offset:0 lds\nM v[96:99],v[208:211],a[200:203],v[96:99]\ns_lshl_b32 s70,s7,8\nv_lshl_add_u32 v251,v251,5,s70\nM v[100:103],v[208:211],a[204:207],v[100:103]\nds_read_b64_tr_b16 v[208:209],v241 offset:41984\n" \
"ds_read_b64_tr_b16 v[210:211],v241 offset:42240\nM v[40:43],v[212:215],a[192:195],v[40:43]\ns_lshl_b32 s71,s6,5\ns_and_b32 s71,s71,0xfffffe00\nM v[44:47],v[212:215],a[196:199],v[44:47]\nglobal_load_dword v247,v253,s[12:13]\nM v[104:107],v[212:215],a[200:203],v[104:107]\nv_add_u32 v253,0x100000,v253\n" \
"s_xor_b32 s70,s63,16\nM v[108:111],v[212:215],a[204:207],v[108:111]\nds_read_b64_tr_b16 v[212:213],v242 offset:41984\nds_read_b64_tr_b16 v[214:215],v242 offset:42240\nM v[48:51],v[216:219],a[192:195],v[48:51]\nbuffer_load_dwordx4 v244,s[40:43],s69 offen offset:1024 lds\nM v[52:55],v[216:219],a[196:199],v[52:55]\ns_lshr_b32 s70,s70,2\n" \
"s_lshl3_add_u32 s70,s62,s70\nM v[112:115],v[216:219],a[200:203],v[112:115]\ns_lshl_b32 s70,s70,15\ns_add_u32 s70,s70,s64\nM v[116:119],v[216:219],a[204:207],v[116:119]\nds_read_b64_tr_b16 v[216:217],v241 offset:42496\nds_read_b64_tr_b16 v[218:219],v241 offset:42752\nM v[56:59],v[220:223],a[192:195],v[56:59]\n" \
"buffer_load_dwordx4 v245,s[40:43],s69 offen offset:2048 lds\nM v[60:63],v[220:223],a[196:199],v[60:63]\nM v[120:123],v[220:223],a[200:203],v[120:123]\nM v[124:127],v[220:223],a[204:207],v[124:127]\nds_read_b64_tr_b16 v[220:221],v242 offset:42496\ns_waitcnt lgkmcnt(5)\nM v[0:3],v[192:195],a[208:211],v[0:3]\nbuffer_load_dwordx4 v246,s[40:43],s69 offen offset:3072 lds\n" \
"M v[4:7],v[192:195],a[212:215],v[4:7]\nds_read_b64_tr_b16 v[222:223],v242 offset:42752\ns_add_u32 m0,s64,114688\nM v[64:67],v[192:195],a[216:219],v[64:67]\ns_add_u32 s69,s68,524288\nM v[68:71],v[192:195],a[220:223],v[68:71]\nM v[8:11],v[196:199],a[208:211],v[8:11]\nbuffer_store_dwordx2 v[232:233],off,s[52:55],s72 sc1\n" \
"M v[12:15],v[196:199],a[212:215],v[12:15]\ns_and_b32 s72,s6,1\ns_lshl_b32 s72,s72,8\nM v[72:75],v[196:199],a[216:219],v[72:75]\ns_add_u32 s71,s71,s72\ns_and_b32 s72,s6,14\nM v[76:79],v[196:199],a[220:223],v[76:79]\ns_lshl1_add_u32 s71,s72,s71\n" \
"s_or_b32 s71,s71,s75\nM v[16:19],v[200:203],a[208:211],v[16:19]\nbuffer_load_dwordx4 v243,s[40:43],s69 offen offset:0 lds\nM v[20:23],v[200:203],a[212:215],v[20:23]\ns_mov_b32 s72,0x3e0293ee\nM v[80:83],v[200:203],a[216:219],v[80:83]\nM v[84:87],v[200:203],a[220:223],v[84:87]\nM v[24:27],v[204:207],a[208:211],v[24:27]\n" \
"buffer_load_dwordx4 v244,s[40:43],s69 offen offset:1024 lds\nM v[28:31],v[204:207],a[212:215],v[28:31]\nM v[88:91],v[204:207],a[216:219],v[88:91]\ns_waitcnt vmcnt(9) lgkmcnt(0)\ns_barrier\nM v[92:95],v[204:207],a[220:223],v[92:95]\nds_read_b128 a[224:227],v228 offset:8192\nds_read_b64_tr_b16 v[192:193],v241 offset:49152\n" \
"M v[32:35],v[208:211],a[208:211],v[32:35]\nds_read_b64_tr_b16 v[194:195],v241 offset:49408\nds_read_b128 a[228:231],v228 offset:10240\nM v[36:39],v[208:211],a[212:215],v[36:39]\nbuffer_load_dwordx4 v245,s[40:43],s69 offen offset:2048 lds\nM v[96:99],v[208:211],a[216:219],v[96:99]\nds_read_b128 a[232:235],v228 offset:12288\nds_read_b128 a[236:239],v228 offset:14336\n" \
"M v[100:103],v[208:211],a[220:223],v[100:103]\nds_read_b64_tr_b16 v[196:197],v242 offset:49152\nv_and_b32 v228,15,v249\nM v[40:43],v[212:215],a[208:211],v[40:43]\nds_read_b64_tr_b16 v[198:199],v242 offset:49408\nds_read_b64_tr_b16 v[200:201],v241 offset:49664\nM v[44:47],v[212:215],a[212:215],v[44:47]\nds_read_b64_tr_b16 v[202:203],v241 offset:49920\n" \
"ds_read_b64_tr_b16 v[204:205],v242 offset:49664\nM v[104:107],v[212:215],a[216:219],v[104:107]\nds_read_b64_tr_b16 v[206:207],v242 offset:49920\nds_read_b64_tr_b16 v[208:209],v241 offset:50176\nM v[108:111],v[212:215],a[220:223],v[108:111]\nds_read_b64_tr_b16 v[210:211],v241 offset:50432\nds_read_b64_tr_b16 v[212:213],v242 offset:50176\nM v[48:51],v[216:219],a[208:211],v[48:51]\n" \
"ds_read_b64_tr_b16 v[214:215],v242 offset:50432\nds_read_b128 a[240:243],v229 offset:8192\nM v[52:55],v[216:219],a[212:215],v[52:55]\nds_read_b128 a[244:247],v229 offset:10240\nv_lshlrev_b32 v228,2,v228\nM v[112:115],v[216:219],a[216:219],v[112:115]\nglobal_load_dword v247,v253,s[12:13]\nM v[116:119],v[216:219],a[220:223],v[116:119]\n" \
"ds_read_b64_tr_b16 v[216:217],v241 offset:50688\nds_read_b64_tr_b16 v[218:219],v241 offset:50944\nM v[56:59],v[220:223],a[208:211],v[56:59]\nds_read_b128 a[248:251],v229 offset:12288\nds_read_b128 a[252:255],v229 offset:14336\nM v[60:63],v[220:223],a[212:215],v[60:63]\nds_read_b128 v[176:179],v251 offset:19456\nv_and_b32 v229,16,v249\n" \
"M v[120:123],v[220:223],a[216:219],v[120:123]\nds_read_b128 v[160:163],v251 offset:17408\nv_lshl_or_b32 v228,v229,4,v228\nM v[124:127],v[220:223],a[220:223],v[124:127]\nds_read_b64_tr_b16 v[220:221],v242 offset:50688\ns_waitcnt lgkmcnt(15)\nM v[0:3],v[192:195],a[224:227],v[0:3]\nds_read_b64_tr_b16 v[222:223],v242 offset:50944\n" \
"ds_read_b128 v[172:175],v251 offset:18448\nM v[4:7],v[192:195],a[228:231],v[4:7]\nds_read_b128 v[180:183],v251 offset:19472\nv_and_b32 v229,15,v249\nM v[64:67],v[192:195],a[232:235],v[64:67]\nds_read_b128 v[152:155],v251 offset:16384\nv_lshlrev_b32 v229,7,v229\nM v[68:71],v[192:195],a[236:239],v[68:71]\n" \
"ds_read_b64_tr_b16 v[192:193],v241 offset:57344\nv_add_u32 v253,0xffd00000,v253\nM v[8:11],v[196:199],a[224:227],v[8:11]\nds_read_b64_tr_b16 v[194:195],v241 offset:57600\nds_read_b128 v[156:159],v251 offset:16400\nM v[12:15],v[196:199],a[228:231],v[12:15]\nds_read_b128 v[164:167],v251 offset:17424\nv_lshrrev_b32 v253,4,v249\n" \
"M v[72:75],v[196:199],a[232:235],v[72:75]\nbuffer_load_dwordx4 v246,s[40:43],s69 offen offset:3072 lds\nM v[76:79],v[196:199],a[236:239],v[76:79]\nds_read_b64_tr_b16 v[196:197],v242 offset:57344\nds_read_b64_tr_b16 v[198:199],v242 offset:57600\nM v[16:19],v[200:203],a[224:227],v[16:19]\ns_waitcnt vmcnt(11)\nv_add_f32_dpp v168,v168,v168 quad_perm:[1,0,3,2] row_mask:0xf bank_mask:0xf\n" \
"M v[20:23],v[200:203],a[228:231],v[20:23]\ns_waitcnt lgkmcnt(11)\nv_mul_f32 v144,v144,v176\nM v[80:83],v[200:203],a[232:235],v[80:83]\nv_add_f32_dpp v168,v168,v168 quad_perm:[2,3,0,1] row_mask:0xf bank_mask:0xf\nv_cvt_pk_bf16_f32 v168,0,v168\nM v[84:87],v[200:203],a[236:239],v[84:87]\nds_read_b64_tr_b16 v[200:201],v241 offset:57856\n" \
"ds_read_b64_tr_b16 v[202:203],v241 offset:58112\nM v[24:27],v[204:207],a[224:227],v[24:27]\nbuffer_store_dword v168,v170,s[52:55],s71 offen sc1\nM v[28:31],v[204:207],a[228:231],v[28:31]\nds_read_b128 v[168:171],v251 offset:18432\nv_mul_f32 v145,v145,v177\nM v[88:91],v[204:207],a[232:235],v[88:91]\nv_cvt_pk_bf16_f32 v144,0,v144\n" \
"v_cvt_pk_bf16_f32 v145,0,v145\nM v[92:95],v[204:207],a[236:239],v[92:95]\nds_read_b64_tr_b16 v[204:205],v242 offset:57856\nds_read_b64_tr_b16 v[206:207],v242 offset:58112\nM v[32:35],v[208:211],a[224:227],v[32:35]\nv_add_f32 v144,v144,v160\nv_add_f32 v145,v145,v161\nM v[36:39],v[208:211],a[228:231],v[36:39]\n" \
"v_cvt_pk_bf16_f32 v144,0,v144\nv_cvt_pk_bf16_f32 v145,0,v145\nM v[96:99],v[208:211],a[232:235],v[96:99]\nv_mul_f32 v144,s72,v144\nv_mul_f32 v145,s72,v145\nM v[100:103],v[208:211],a[236:239],v[100:103]\nds_read_b64_tr_b16 v[208:209],v241 offset:58368\nds_read_b64_tr_b16 v[210:211],v241 offset:58624\n" \
"M v[40:43],v[212:215],a[224:227],v[40:43]\nv_cvt_pk_bf16_f32 v236,v144,v145\nv_mul_f32 v146,v146,v178\nM v[44:47],v[212:215],a[228:231],v[44:47]\nv_mul_f32 v147,v147,v179\nv_cvt_pk_bf16_f32 v146,0,v146\nM v[104:107],v[212:215],a[232:235],v[104:107]\nv_cvt_pk_bf16_f32 v147,0,v147\n" \
"v_add_f32 v146,v146,v162\nM v[108:111],v[212:215],a[236:239],v[108:111]\nds_read_b64_tr_b16 v[212:213],v242 offset:58368\nds_read_b64_tr_b16 v[214:215],v242 offset:58624\nM v[48:51],v[216:219],a[224:227],v[48:51]\nv_add_f32 v147,v147,v163\nv_cvt_pk_bf16_f32 v146,0,v146\nM v[52:55],v[216:219],a[228:231],v[52:55]\n" \
"v_cvt_pk_bf16_f32 v147,0,v147\nv_mul_f32 v146,s72,v146\nM v[112:115],v[216:219],a[232:235],v[112:115]\nv_mul_f32 v147,s72,v147\nv_cvt_pk_bf16_f32 v237,v146,v147\nM v[116:119],v[216:219],a[236:239],v[116:119]\nds_read_b64_tr_b16 v[216:217],v241 offset:58880\ns_waitcnt lgkmcnt(5)\n" \
"M v[56:59],v[220:223],a[224:227],v[56:59]\nds_read_b64_tr_b16 v[218:219],v241 offset:59136\ns_and_b32 s71,s63,2\nM v[60:63],v[220:223],a[228:231],v[60:63]\nv_mul_f32 v136,v136,v168\nv_mul_f32 v137,v137,v169\nM v[120:123],v[220:223],a[232:235],v[120:123]\nv_mul_f32 v138,v138,v170\n" \
"v_mul_f32 v139,v139,v171\nM v[124:127],v[220:223],a[236:239],v[124:127]\nds_read_b64_tr_b16 v[220:221],v242 offset:58880\nds_read_b64_tr_b16 v[222:223],v242 offset:59136\nM v[0:3],v[192:195],a[240:243],v[0:3]\nv_mul_f32 v140,v140,v172\nv_mul_f32 v141,v141,v173\nM v[4:7],v[192:195],a[244:247],v[4:7]\n" \
"v_mul_f32 v142,v142,v174\nv_mul_f32 v143,v143,v175\nM v[64:67],v[192:195],a[248:251],v[64:67]\nv_mul_f32 v148,v148,v180\nv_mul_f32 v149,v149,v181\nM v[68:71],v[192:195],a[252:255],v[68:71]\nv_cvt_pk_bf16_f32 v128,v0,v1\nv_cvt_pk_bf16_f32 v129,v2,v3\n" \
"M v[8:11],v[196:199],a[240:243],v[8:11]\nv_cvt_pk_bf16_f32 v144,v4,v5\nv_cvt_pk_bf16_f32 v145,v6,v7\nM v[12:15],v[196:199],a[244:247],v[12:15]\nv_cvt_pk_bf16_f32 v160,v64,v65\nv_cvt_pk_bf16_f32 v161,v66,v67\nM v[72:75],v[196:199],a[248:251],v[72:75]\nv_cvt_pk_bf16_f32 v176,v68,v69\n" \
"v_cvt_pk_bf16_f32 v177,v70,v71\nM v[76:79],v[196:199],a[252:255],v[76:79]\nv_cvt_pk_bf16_f32 v130,v8,v9\nv_cvt_pk_bf16_f32 v131,v10,v11\nM v[16:19],v[200:203],a[240:243],v[16:19]\nv_cvt_pk_bf16_f32 v146,v12,v13\nv_cvt_pk_bf16_f32 v147,v14,v15\nM v[20:23],v[200:203],a[244:247],v[20:23]\n" \
"s_lshl_b32 s71,s71,9\nv_cvt_pk_bf16_f32 v162,v72,v73\nM v[80:83],v[200:203],a[248:251],v[80:83]\nv_cvt_pk_bf16_f32 v163,v74,v75\nv_cvt_pk_bf16_f32 v136,0,v136\nM v[84:87],v[200:203],a[252:255],v[84:87]\nv_cvt_pk_bf16_f32 v137,0,v137\nv_cvt_pk_bf16_f32 v138,0,v138\n" \
"M v[24:27],v[204:207],a[240:243],v[24:27]\nv_cvt_pk_bf16_f32 v139,0,v139\nv_cvt_pk_bf16_f32 v140,0,v140\nM v[28:31],v[204:207],a[244:247],v[28:31]\nv_cvt_pk_bf16_f32 v141,0,v141\nv_cvt_pk_bf16_f32 v142,0,v142\nM v[88:91],v[204:207],a[248:251],v[88:91]\ns_waitcnt vmcnt(7) lgkmcnt(0)\n" \
"s_barrier\nM v[92:95],v[204:207],a[252:255],v[92:95]\nds_read_b64_tr_b16 v[192:193],v241 offset:0\nds_read_b64_tr_b16 v[194:195],v241 offset:256\nM v[32:35],v[208:211],a[240:243],v[32:35]\nds_read_b64_tr_b16 v[196:197],v242 offset:0\nds_read_b64_tr_b16 v[198:199],v242 offset:256\nM v[36:39],v[208:211],a[244:247],v[36:39]\n" \
"ds_read_b64_tr_b16 v[200:201],v241 offset:512\nds_read_b64_tr_b16 v[202:203],v241 offset:768\nM v[96:99],v[208:211],a[248:251],v[96:99]\nds_read_b64_tr_b16 v[204:205],v242 offset:512\nds_read_b64_tr_b16 v[206:207],v242 offset:768\nM v[100:103],v[208:211],a[252:255],v[100:103]\nds_read_b64_tr_b16 v[208:209],v241 offset:1024\nds_read_b64_tr_b16 v[210:211],v241 offset:1280\n" \
"M v[40:43],v[212:215],a[240:243],v[40:43]\nv_cvt_pk_bf16_f32 v143,0,v143\nv_cvt_pk_bf16_f32 v148,0,v148\nM v[44:47],v[212:215],a[244:247],v[44:47]\nv_cvt_pk_bf16_f32 v149,0,v149\nv_lshrrev_b32 v251,5,v249\nM v[104:107],v[212:215],a[248:251],v[104:107]\ns_add_u32 s70,s70,s71\n" \
"v_add_f32 v136,v136,v152\nM v[108:111],v[212:215],a[252:255],v[108:111]\nv_add_f32 v137,v137,v153\nv_add_f32 v138,v138,v154\nM v[48:51],v[216:219],a[240:243],v[48:51]\nv_add_f32 v139,v139,v155\nv_add_f32 v140,v140,v156\nM v[52:55],v[216:219],a[244:247],v[52:55]\n" \
"v_add_f32 v141,v141,v157\nv_add_f32 v142,v142,v158\nM v[112:115],v[216:219],a[248:251],v[112:115]\nv_add_f32 v143,v143,v159\nv_add_f32 v148,v148,v164\nM v[116:119],v[216:219],a[252:255],v[116:119]\nv_add_f32 v149,v149,v165\nv_lshlrev_b32 v251,11,v251\n" \
"M v[56:59],v[220:223],a[240:243],v[56:59]\ns_and_b32 s71,s63,1\nv_cvt_pk_bf16_f32 v178,v76,v77\nM v[60:63],v[220:223],a[244:247],v[60:63]\nv_cvt_pk_bf16_f32 v179,v78,v79\nv_cvt_pk_bf16_f32 v136,0,v136\nM v[120:123],v[220:223],a[248:251],v[120:123]\nv_cvt_pk_bf16_f32 v137,0,v137\n" \
"v_cvt_pk_bf16_f32 v138,0,v138\nM v[124:127],v[220:223],a[252:255],v[124:127]\nv_cvt_pk_bf16_f32 v139,0,v139\ns_waitcnt lgkmcnt(0)\nM v[0:3],a[0:3],v[192:195],0\nv_cvt_pk_bf16_f32 v140,0,v140\nv_cvt_pk_bf16_f32 v141,0,v141\nM v[4:7],a[4:7],v[192:195],0\n" \
"v_cvt_pk_bf16_f32 v142,0,v142\nv_cvt_pk_bf16_f32 v143,0,v143\nM v[64:67],a[8:11],v[192:195],0\nv_cvt_pk_bf16_f32 v148,0,v148\nv_cvt_pk_bf16_f32 v149,0,v149\nM v[68:71],a[12:15],v[192:195],0\nv_lshl_or_b32 v251,v250,8,v251\ns_lshl_b32 s71,s71,7\n" \
"M v[8:11],a[0:3],v[196:199],0\nv_mul_f32 v136,s72,v136\nv_mul_f32 v137,s72,v137\nM v[12:15],a[4:7],v[196:199],0\nv_mul_f32 v138,s72,v138\nv_mul_f32 v139,s72,v139\nM v[72:75],a[8:11],v[196:199],0\nv_mul_f32 v140,s72,v140\n" \
"v_mul_f32 v141,s72,v141\nM v[76:79],a[12:15],v[196:199],0\nv_mul_f32 v142,s72,v142\nv_mul_f32 v143,s72,v143\nv_mul_f32 v148,s72,v148\nv_mul_f32 v149,s72,v149\nv_and_b32 v250,0x70,v240\ns_add_u32 s70,s70,s71\n" \
"v_cvt_pk_bf16_f32 v132,v16,v17\nv_cvt_pk_bf16_f32 v133,v18,v19\nM v[16:19],a[0:3],v[200:203],0\nv_cvt_pk_bf16_f32 v232,v136,v137\nv_cvt_pk_bf16_f32 v233,v138,v139\nv_cvt_pk_bf16_f32 v234,v140,v141\nv_cvt_pk_bf16_f32 v235,v142,v143\nv_cvt_pk_bf16_f32 v238,v148,v149\n" \
"v_or_b32 v251,v251,v250\ns_add_u32 s70,s70,1048576\nbuffer_store_dwordx4 v[232:235],v251,s[52:55],s70 offen sc1\nv_cvt_pk_bf16_f32 v148,v20,v21\nv_cvt_pk_bf16_f32 v149,v22,v23\nM v[20:23],a[4:7],v[200:203],0\nv_mul_f32 v150,v150,v182\nv_mul_f32 v151,v151,v183\n" \
"v_cvt_pk_bf16_f32 v164,v80,v81\nv_cvt_pk_bf16_f32 v165,v82,v83\nds_read_b64_tr_b16 v[212:213],v242 offset:1024\nds_read_b64_tr_b16 v[214:215],v242 offset:1280\nM v[80:83],a[8:11],v[200:203],0\nv_cvt_pk_bf16_f32 v150,0,v150\nv_cvt_pk_bf16_f32 v151,0,v151\nv_add_f32 v150,v150,v166\n" \
"v_add_f32 v151,v151,v167\nv_cvt_pk_bf16_f32 v180,v84,v85\nv_cvt_pk_bf16_f32 v181,v86,v87\nM v[84:87],a[12:15],v[200:203],0\nv_cvt_pk_bf16_f32 v150,0,v150\nv_cvt_pk_bf16_f32 v151,0,v151\nv_mul_f32 v150,s72,v150\nv_mul_f32 v151,s72,v151\n" \
"v_cvt_pk_bf16_f32 v134,v24,v25\nv_cvt_pk_bf16_f32 v135,v26,v27\nM v[24:27],a[0:3],v[204:207],0\nv_cvt_pk_bf16_f32 v239,v150,v151\ns_add_u32 s71,s70,16384\nbuffer_store_dwordx4 v[236:239],v251,s[52:55],s71 offen sc1\nv_cvt_pk_bf16_f32 v150,v28,v29\nv_cvt_pk_bf16_f32 v151,v30,v31\n" \
"M v[28:31],a[4:7],v[204:207],0\nv_cvt_pk_bf16_f32 v166,v88,v89\nv_cvt_pk_bf16_f32 v167,v90,v91\nds_read_b64_tr_b16 v[216:217],v241 offset:1536\nds_read_b64_tr_b16 v[218:219],v241 offset:1792\nM v[88:91],a[8:11],v[204:207],0\nv_cvt_pk_bf16_f32 v182,v92,v93\nv_cvt_pk_bf16_f32 v183,v94,v95\n" \
"M v[92:95],a[12:15],v[204:207],0\nv_cvt_pk_bf16_f32 v136,v32,v33\nv_cvt_pk_bf16_f32 v137,v34,v35\nM v[32:35],a[0:3],v[208:211],0\ns_lshl_b32 s70,s62,5\ns_add_u32 s70,s70,s63\nv_cvt_pk_bf16_f32 v152,v36,v37\nv_cvt_pk_bf16_f32 v153,v38,v39\n" \
"M v[36:39],a[4:7],v[208:211],0\ns_lshl2_add_u32 s70,s70,s7\ns_lshl2_add_u32 s79,s70,90112\nv_cvt_pk_bf16_f32 v168,v96,v97\nv_cvt_pk_bf16_f32 v169,v98,v99\nds_read_b64_tr_b16 v[220:221],v242 offset:1536\nds_read_b64_tr_b16 v[222:223],v242 offset:1792\nM v[96:99],a[8:11],v[208:211],0\n" \
"s_lshl_b32 s70,s60,4\nv_cvt_pk_bf16_f32 v184,v100,v101\nv_cvt_pk_bf16_f32 v185,v102,v103\ns_sub_u32 s70,s70,s62\nM v[100:103],a[12:15],v[208:211],0\ns_add_u32 s70,s70,15\nv_cvt_pk_bf16_f32 v138,v40,v41\nv_cvt_pk_bf16_f32 v139,v42,v43\n" \
"s_lshl2_add_u32 s70,s70,s7\ns_waitcnt lgkmcnt(4)\nM v[40:43],a[0:3],v[212:215],0\ns_lshl_b32 s70,s70,15\ns_lshl_b32 s71,s61,8\nv_cvt_pk_bf16_f32 v154,v44,v45\nv_cvt_pk_bf16_f32 v155,v46,v47\ns_add_u32 s70,s70,s71\n" \
"M v[44:47],a[4:7],v[212:215],0\ns_add_u32 s78,s70,54525952\nv_cvt_pk_bf16_f32 v170,v104,v105\nv_cvt_pk_bf16_f32 v171,v106,v107\ns_lshl3_add_u32 s70,s62,s7\nds_read_b64_tr_b16 v[192:193],v241 offset:8192\nds_read_b64_tr_b16 v[194:195],v241 offset:8448\nM v[104:107],a[8:11],v[212:215],0\n" \
"s_lshl_b32 s80,s70,6\ns_lshl_b32 s76,s70,15\nv_cvt_pk_bf16_f32 v186,v108,v109\nv_cvt_pk_bf16_f32 v187,v110,v111\ns_lshl_b32 s70,s63,5\nM v[108:111],a[12:15],v[212:215],0\ns_add_u32 s70,s70,s74\nv_cvt_pk_bf16_f32 v140,v48,v49\n" \
"v_cvt_pk_bf16_f32 v141,v50,v51\ns_lshl_b32 s74,s70,14\ns_waitcnt lgkmcnt(2)\nM v[48:51],a[0:3],v[216:219],0\ns_add_u32 s74,s74,3145728\nv_cvt_pk_bf16_f32 v156,v52,v53\nv_cvt_pk_bf16_f32 v157,v54,v55\nbuffer_store_dwordx4 v[128:131],v240,s[52:55],s74 offen offset:0 sc1\n" \
"M v[52:55],a[4:7],v[216:219],0\nv_cvt_pk_bf16_f32 v172,v112,v113\nv_cvt_pk_bf16_f32 v173,v114,v115\nds_read_b64_tr_b16 v[196:197],v242 offset:8192\nds_read_b64_tr_b16 v[198:199],v242 offset:8448\nM v[112:115],a[8:11],v[216:219],0\nv_cvt_pk_bf16_f32 v188,v116,v117\nv_cvt_pk_bf16_f32 v189,v118,v119\n" \
"M v[116:119],a[12:15],v[216:219],0\nv_cvt_pk_bf16_f32 v142,v56,v57\nv_cvt_pk_bf16_f32 v143,v58,v59\nM v[56:59],a[0:3],v[220:223],0\nv_cvt_pk_bf16_f32 v158,v60,v61\nv_cvt_pk_bf16_f32 v159,v62,v63\nM v[60:63],a[4:7],v[220:223],0\nv_cvt_pk_bf16_f32 v174,v120,v121\n" \
"v_cvt_pk_bf16_f32 v175,v122,v123\nds_read_b64_tr_b16 v[200:201],v241 offset:8704\nds_read_b64_tr_b16 v[202:203],v241 offset:8960\nM v[120:123],a[8:11],v[220:223],0\ns_add_u32 m0,s64,131072\ns_add_u32 s69,s68,1048576\nv_cvt_pk_bf16_f32 v190,v124,v125\nv_cvt_pk_bf16_f32 v191,v126,v127\n" \
"M v[124:127],a[12:15],v[220:223],0\nds_read_b64_tr_b16 v[204:205],v242 offset:8704\ns_waitcnt lgkmcnt(5)\nM v[0:3],a[16:19],v[192:195],v[0:3]\nds_read_b64_tr_b16 v[206:207],v242 offset:8960\nds_read_b64_tr_b16 v[208:209],v241 offset:9216\nM v[4:7],a[20:23],v[192:195],v[4:7]\nds_read_b64_tr_b16 v[210:211],v241 offset:9472\n" \
"ds_read_b64_tr_b16 v[212:213],v242 offset:9216\nM v[64:67],a[24:27],v[192:195],v[64:67]\nds_read_b64_tr_b16 v[214:215],v242 offset:9472\nds_read_b64_tr_b16 v[216:217],v241 offset:9728\nM v[68:71],a[28:31],v[192:195],v[68:71]\nds_read_b64_tr_b16 v[218:219],v241 offset:9984\ns_waitcnt lgkmcnt(10)\nM v[8:11],a[16:19],v[196:199],v[8:11]\n" \
"ds_read_b64_tr_b16 v[220:221],v242 offset:9728\nds_read_b64_tr_b16 v[222:223],v242 offset:9984\nM v[12:15],a[20:23],v[196:199],v[12:15]\nbuffer_load_dwordx4 v243,s[40:43],s69 offen offset:0 lds\nM v[72:75],a[24:27],v[196:199],v[72:75]\ns_add_u32 s80,s80,90112\ns_add_u32 s76,s76,1048576\nM v[76:79],a[28:31],v[196:199],v[76:79]\n" \
"s_add_u32 s77,s76,131072\ns_waitcnt lgkmcnt(10)\nM v[16:19],a[16:19],v[200:203],v[16:19]\ns_lshl2_add_u32 s29,s70,81920\ns_lshl_b32 s70,s7,14\nM v[20:23],a[20:23],v[200:203],v[20:23]\nbuffer_store_dwordx4 v[132:135],v240,s[52:55],s74 offen offset:1024 sc1\nM v[80:83],a[24:27],v[200:203],v[80:83]\n" \
"v_add_u32 v250,s70,v240\ns_lshl_b32 s70,s63,19\nM v[84:87],a[28:31],v[200:203],v[84:87]\ns_add_u32 s72,s78,16384\ns_waitcnt lgkmcnt(6)\nM v[24:27],a[16:19],v[204:207],v[24:27]\ns_add_u32 s44,s34,s70\ns_addc_u32 s45,s35,0\n" \
"M v[28:31],a[20:23],v[204:207],v[28:31]\nbuffer_load_dwordx4 v244,s[40:43],s69 offen offset:1024 lds\nM v[88:91],a[24:27],v[204:207],v[88:91]\ns_waitcnt vmcnt(7) lgkmcnt(0)\ns_barrier\nM v[92:95],a[28:31],v[204:207],v[92:95]\nds_read_b64_tr_b16 v[192:193],v241 offset:16384\nds_read_b64_tr_b16 v[194:195],v241 offset:16640\n" \
"M v[32:35],a[16:19],v[208:211],v[32:35]\nds_read_b64_tr_b16 v[196:197],v242 offset:16384\nds_read_b64_tr_b16 v[198:199],v242 offset:16640\nM v[36:39],a[20:23],v[208:211],v[36:39]\nds_read_b64_tr_b16 v[200:201],v241 offset:16896\nds_read_b64_tr_b16 v[202:203],v241 offset:17152\nM v[96:99],a[24:27],v[208:211],v[96:99]\nds_read_b64_tr_b16 v[204:205],v242 offset:16896\n" \
"ds_read_b64_tr_b16 v[206:207],v242 offset:17152\nM v[100:103],a[28:31],v[208:211],v[100:103]\nds_read_b64_tr_b16 v[208:209],v241 offset:17408\nds_read_b64_tr_b16 v[210:211],v241 offset:17664\nM v[40:43],a[16:19],v[212:215],v[40:43]\nbuffer_load_dwordx4 v245,s[40:43],s69 offen offset:2048 lds\nM v[44:47],a[20:23],v[212:215],v[44:47]\ns_lshl_b32 s70,s62,15\n" \
"s_add_u32 s96,s70,s64\nM v[104:107],a[24:27],v[212:215],v[104:107]\ns_sub_u32 s46,491520,s70\ns_sub_u32 s96,s96,16384\nM v[108:111],a[28:31],v[212:215],v[108:111]\nds_read_b64_tr_b16 v[212:213],v242 offset:17408\nds_read_b64_tr_b16 v[214:215],v242 offset:17664\nM v[48:51],a[16:19],v[216:219],v[48:51]\n" \
"buffer_store_dwordx4 v[136:139],v240,s[52:55],s74 offen offset:2048 sc1\nM v[52:55],a[20:23],v[216:219],v[52:55]\nds_write_b64 v250,v[128:129] offset:0\ns_lshl2_add_u32 s70,s62,0\nM v[112:115],a[24:27],v[216:219],v[112:115]\ns_cmp_eq_u32 s36,s96\ns_cselect_b32 s72,0xc000,s98\ns_add_u32 s65,s36,s72\n" \
"M v[116:119],a[28:31],v[216:219],v[116:119]\nds_read_b64_tr_b16 v[216:217],v241 offset:17920\nds_read_b64_tr_b16 v[218:219],v241 offset:18176\nM v[56:59],a[16:19],v[220:223],v[56:59]\nbuffer_load_dwordx4 v246,s[40:43],s69 offen offset:3072 lds\nM v[60:63],a[20:23],v[220:223],v[60:63]\ns_add_u32 m0,s64,147456\ns_add_u32 s69,s68,1572864\n" \
"M v[120:123],a[24:27],v[220:223],v[120:123]\ns_sub_u32 s70,s70,s7\ns_lshl_b32 s70,s70,16\nM v[124:127],a[28:31],v[220:223],v[124:127]\nds_read_b64_tr_b16 v[220:221],v242 offset:17920\ns_waitcnt lgkmcnt(6)\nM v[0:3],a[32:35],v[192:195],v[0:3]\nds_read_b64_tr_b16 v[222:223],v242 offset:18176\n" \
"s_add_u32 s33,s78,s70\nM v[4:7],a[36:39],v[192:195],v[4:7]\nbuffer_load_dwordx4 v243,s[40:43],s69 offen offset:0 lds\nM v[64:67],a[40:43],v[192:195],v[64:67]\ns_cmp_eq_u32 s65,s96\ns_cselect_b32 s72,0xc000,s98\ns_add_u32 s66,s65,s72\nM v[68:71],a[44:47],v[192:195],v[68:71]\n" \
"ds_read_b64_tr_b16 v[192:193],v241 offset:24576\nds_read_b64_tr_b16 v[194:195],v241 offset:24832\nM v[8:11],a[32:35],v[196:199],v[8:11]\nds_write_b64 v250,v[130:131] offset:8\ns_lshl3_add_u32 s70,s62,s7\nM v[12:15],a[36:39],v[196:199],v[12:15]\nbuffer_load_dwordx4 v244,s[40:43],s69 offen offset:1024 lds\nM v[72:75],a[40:43],v[196:199],v[72:75]\n" \
"s_lshl3_add_u32 s70,s70,s60\ns_lshl_b32 s67,s70,15\nM v[76:79],a[44:47],v[196:199],v[76:79]\nds_read_b64_tr_b16 v[196:197],v242 offset:24576\nds_read_b64_tr_b16 v[198:199],v242 offset:24832\nM v[16:19],a[32:35],v[200:203],v[16:19]\ns_cmp_eq_u32 s66,s96\ns_cselect_b32 s72,0xc000,s98\n" \
"s_add_u32 s95,s66,s72\nM v[20:23],a[36:39],v[200:203],v[20:23]\nbuffer_load_dwordx4 v245,s[40:43],s69 offen offset:2048 lds\nM v[80:83],a[40:43],v[200:203],v[80:83]\ns_lshl3_add_u32 s70,s62,s60\ns_lshl2_add_u32 s70,s70,s7\nM v[84:87],a[44:47],v[200:203],v[84:87]\nds_read_b64_tr_b16 v[200:201],v241 offset:25088\n" \
"ds_read_b64_tr_b16 v[202:203],v241 offset:25344\nM v[24:27],a[32:35],v[204:207],v[24:27]\ns_cmp_eq_u32 s62,7\ns_cselect_b32 s72,98304,0\nM v[28:31],a[36:39],v[204:207],v[28:31]\nbuffer_store_dwordx4 v[140:143],v240,s[52:55],s74 offen offset:3072 sc1\nM v[88:91],a[40:43],v[204:207],v[88:91]\ns_add_u32 s74,s74,4096\n" \
"s_add_u32 s97,s96,65536\nM v[92:95],a[44:47],v[204:207],v[92:95]\nds_read_b64_tr_b16 v[204:205],v242 offset:25088\nds_read_b64_tr_b16 v[206:207],v242 offset:25344\nM v[32:35],a[32:35],v[208:211],v[32:35]\nds_write_b64 v250,v[132:133] offset:1024\ns_sub_u32 s97,s97,s72\nM v[36:39],a[36:39],v[208:211],v[36:39]\n" \
"buffer_load_dwordx4 v246,s[40:43],s69 offen offset:3072 lds\nM v[96:99],a[40:43],v[208:211],v[96:99]\ns_add_u32 m0,s64,98304\ns_add_u32 s69,s68,2097152\nM v[100:103],a[44:47],v[208:211],v[100:103]\nds_read_b64_tr_b16 v[208:209],v241 offset:25600\ns_waitcnt lgkmcnt(6)\nM v[40:43],a[32:35],v[212:215],v[40:43]\n" \
"ds_read_b64_tr_b16 v[210:211],v241 offset:25856\ns_mul_i32 s71,s60,14\nM v[44:47],a[36:39],v[212:215],v[44:47]\nbuffer_load_dwordx4 v243,s[40:43],s69 offen offset:0 lds\nM v[104:107],a[40:43],v[212:215],v[104:107]\ns_lshl_b32 s72,s60,1\ns_add_u32 s72,s72,s62\nM v[108:111],a[44:47],v[212:215],v[108:111]\n" \
"ds_read_b64_tr_b16 v[212:213],v242 offset:25600\nds_read_b64_tr_b16 v[214:215],v242 offset:25856\nM v[48:51],a[32:35],v[216:219],v[48:51]\ns_sub_u32 s72,s72,6\ns_lshl_b32 s72,s72,18\nM v[52:55],a[36:39],v[216:219],v[52:55]\nbuffer_load_dwordx4 v244,s[40:43],s69 offen offset:1024 lds\nM v[112:115],a[40:43],v[216:219],v[112:115]\n" \
"s_cmp_eq_u32 s62,7\ns_cselect_b32 s46,229376,s46\nM v[116:119],a[44:47],v[216:219],v[116:119]\nds_read_b64_tr_b16 v[216:217],v241 offset:26112\nds_read_b64_tr_b16 v[218:219],v241 offset:26368\nM v[56:59],a[32:35],v[220:223],v[56:59]\nds_write_b64 v250,v[134:135] offset:1032\nv_and_b32 v251,0xf0,v240\n" \
"M v[60:63],a[36:39],v[220:223],v[60:63]\nbuffer_store_dwordx4 v[144:147],v240,s[52:55],s74 offen offset:0 sc1\nM v[120:123],a[40:43],v[220:223],v[120:123]\ns_add_u32 s48,s44,19922944\ns_addc_u32 s49,s45,0\nM v[124:127],a[44:47],v[220:223],v[124:127]\nds_read_b64_tr_b16 v[220:221],v242 offset:26112\nds_read_b64_tr_b16 v[222:223],v242 offset:26368\n" \
"M v[0:3],a[48:51],v[192:195],v[0:3]\nv_lshl_or_b32 v253,v253,12,v251\ns_add_u32 s15,s64,s46\nM v[4:7],a[52:55],v[192:195],v[4:7]\nbuffer_load_dwordx4 v245,s[40:43],s69 offen offset:2048 lds\nM v[64:67],a[56:59],v[192:195],v[64:67]\ns_add_u32 s44,s44,3145728\ns_addc_u32 s45,s45,0\n" \
"M v[68:71],a[60:63],v[192:195],v[68:71]\ns_mov_b32 s50,s46\ns_add_u32 s15,s15,16384\nM v[8:11],a[48:51],v[196:199],v[8:11]\ns_sub_u32 s33,s33,1867776\ns_add_u32 s67,s67,36700160\nM v[12:15],a[52:55],v[196:199],v[12:15]\nbuffer_load_dwordx4 v246,s[40:43],s69 offen offset:3072 lds\n" \
"M v[72:75],a[56:59],v[196:199],v[72:75]\nM v[76:79],a[60:63],v[196:199],v[76:79]\ns_waitcnt lgkmcnt(2)\nM v[16:19],a[48:51],v[200:203],v[16:19]\nds_write_b64 v250,v[136:137] offset:2048\nM v[20:23],a[52:55],v[200:203],v[20:23]\nM v[80:83],a[56:59],v[200:203],v[80:83]\nM v[84:87],a[60:63],v[200:203],v[84:87]\n" \
"M v[24:27],a[48:51],v[204:207],v[24:27]\ns_add_u32 m0,s64,114688\ns_add_u32 s69,s68,2621440\nM v[28:31],a[52:55],v[204:207],v[28:31]\nM v[88:91],a[56:59],v[204:207],v[88:91]\ns_waitcnt vmcnt(10) lgkmcnt(1)\ns_barrier\nM v[92:95],a[60:63],v[204:207],v[92:95]\n" \
"buffer_store_dwordx4 v[148:151],v240,s[52:55],s74 offen offset:1024 sc1\nM v[32:35],a[48:51],v[208:211],v[32:35]\nds_read_b64_tr_b16 v[192:193],v241 offset:32768\nds_read_b64_tr_b16 v[194:195],v241 offset:33024\nM v[36:39],a[52:55],v[208:211],v[36:39]\nds_read_b64_tr_b16 v[196:197],v242 offset:32768\nds_read_b64_tr_b16 v[198:199],v242 offset:33024\nM v[96:99],a[56:59],v[208:211],v[96:99]\n" \
"ds_read_b64_tr_b16 v[200:201],v241 offset:33280\nds_read_b64_tr_b16 v[202:203],v241 offset:33536\nM v[100:103],a[60:63],v[208:211],v[100:103]\nbuffer_store_dword v252,off,s[52:55],s79 sc1\nM v[40:43],a[48:51],v[212:215],v[40:43]\nds_read_b64_tr_b16 v[204:205],v242 offset:33280\nds_read_b64_tr_b16 v[206:207],v242 offset:33536\nM v[44:47],a[52:55],v[212:215],v[44:47]\n" \
"ds_read_b64_tr_b16 v[208:209],v241 offset:33792\nds_read_b64_tr_b16 v[210:211],v241 offset:34048\nM v[104:107],a[56:59],v[212:215],v[104:107]\nbuffer_load_dwordx4 v243,s[40:43],s69 offen offset:0 lds\nM v[108:111],a[60:63],v[212:215],v[108:111]\nds_read_b64_tr_b16 v[212:213],v242 offset:33792\nds_read_b64_tr_b16 v[214:215],v242 offset:34048\nM v[48:51],a[48:51],v[216:219],v[48:51]\n" \
"ds_write_b64 v250,v[138:139] offset:2056\ns_lshl4_add_u32 s79,s70,65536\nM v[52:55],a[52:55],v[216:219],v[52:55]\ns_lshr_b32 s70,s6,3\ns_sub_u32 s70,s70,16\nM v[112:115],a[56:59],v[216:219],v[112:115]\nbuffer_load_dwordx4 v244,s[40:43],s69 offen offset:1024 lds\nM v[116:119],a[60:63],v[216:219],v[116:119]\n" \
"ds_read_b64_tr_b16 v[216:217],v241 offset:34304\nds_read_b64_tr_b16 v[218:219],v241 offset:34560\nM v[56:59],a[48:51],v[220:223],v[56:59]\ns_add_u32 s2,s70,s71\ns_mul_i32 s71,s60,448\nM v[60:63],a[52:55],v[220:223],v[60:63]\ns_and_b32 s71,s71,0xffffff80\ns_sub_u32 s58,1835008,s71\n" \
"M v[120:123],a[56:59],v[220:223],v[120:123]\nbuffer_load_dwordx4 v245,s[40:43],s69 offen offset:2048 lds\nM v[124:127],a[60:63],v[220:223],v[124:127]\nds_read_b64_tr_b16 v[220:221],v242 offset:34304\ns_waitcnt lgkmcnt(7)\nM v[0:3],a[64:67],v[192:195],v[0:3]\nds_read_b64_tr_b16 v[222:223],v242 offset:34560\ns_lshl_b32 s2,s2,5\n" \
"M v[4:7],a[68:71],v[192:195],v[4:7]\nds_write_b64 v250,v[140:141] offset:3072\nM v[64:67],a[72:75],v[192:195],v[64:67]\nbuffer_store_dwordx4 v[152:155],v240,s[52:55],s74 offen offset:2048 sc1\nM v[68:71],a[76:79],v[192:195],v[68:71]\nds_read_b64_tr_b16 v[192:193],v241 offset:40960\nds_read_b64_tr_b16 v[194:195],v241 offset:41216\nM v[8:11],a[64:67],v[196:199],v[8:11]\n" \
"s_cmp_ge_u32 s62,6\ns_cselect_b64 s[56:57],s[18:19],s[56:57]\ns_cselect_b32 s71,s72,s71\ns_cselect_b32 s58,0x40000,s58\ns_cselect_b32 s0,2048,s0\nM v[12:15],a[68:71],v[196:199],v[12:15]\ns_mul_i32 s72,s60,229376\nM v[72:75],a[72:75],v[196:199],v[72:75]\n" \
"buffer_load_dwordx4 v246,s[40:43],s69 offen offset:3072 lds\nM v[76:79],a[76:79],v[196:199],v[76:79]\nds_read_b64_tr_b16 v[196:197],v242 offset:40960\nds_read_b64_tr_b16 v[198:199],v242 offset:41216\nM v[16:19],a[64:67],v[200:203],v[16:19]\ns_cmp_eq_u32 s62,3\ns_cselect_b64 s[56:57],s[26:27],s[56:57]\ns_cselect_b32 s71,s72,s71\n" \
"s_cselect_b32 s58,229376,s58\ns_cselect_b32 s0,4096,s0\nM v[20:23],a[68:71],v[200:203],v[20:23]\ns_cmp_lt_u32 s70,14\ns_cselect_b32 s2,s2,0\ns_cselect_b32 s3,-1,0\nM v[80:83],a[72:75],v[200:203],v[80:83]\ns_lshl2_add_u32 s70,s61,s7\n" \
"s_mul_i32 s1,s70,s0\nM v[84:87],a[76:79],v[200:203],v[84:87]\nds_read_b64_tr_b16 v[200:201],v241 offset:41472\nds_read_b64_tr_b16 v[202:203],v241 offset:41728\nM v[24:27],a[64:67],v[204:207],v[24:27]\nds_write_b64 v250,v[142:143] offset:3080\ns_lshl_b32 s70,s61,8\nM v[28:31],a[68:71],v[204:207],v[28:31]\n" \
"s_cmp_lt_u32 s62,3\ns_cselect_b32 s58,0,s58\nM v[88:91],a[72:75],v[204:207],v[88:91]\ns_add_u32 s56,s56,s71\ns_addc_u32 s57,s57,0\nM v[92:95],a[76:79],v[204:207],v[92:95]\nds_read_b64_tr_b16 v[204:205],v242 offset:41472\ns_waitcnt lgkmcnt(6)\n" \
"M v[32:35],a[64:67],v[208:211],v[32:35]\nds_read_b64_tr_b16 v[206:207],v242 offset:41728\ns_lshl_b32 s0,s0,4\nM v[36:39],a[68:71],v[208:211],v[36:39]\nM v[96:99],a[72:75],v[208:211],v[96:99]\nbuffer_store_dwordx4 v[156:159],v240,s[52:55],s74 offen offset:3072 sc1\nM v[100:103],a[76:79],v[208:211],v[100:103]\nds_read_b64_tr_b16 v[208:209],v241 offset:41984\n" \
"ds_read_b64_tr_b16 v[210:211],v241 offset:42240\nM v[40:43],a[64:67],v[212:215],v[40:43]\ns_add_u32 s74,s74,4096\nM v[44:47],a[68:71],v[212:215],v[44:47]\nM v[104:107],a[72:75],v[212:215],v[104:107]\nM v[108:111],a[76:79],v[212:215],v[108:111]\nds_read_b64_tr_b16 v[212:213],v242 offset:41984\nds_read_b64_tr_b16 v[214:215],v242 offset:42240\n" \
"M v[48:51],a[64:67],v[216:219],v[48:51]\nds_write_b64 v250,v[144:145] offset:4096\nM v[52:55],a[68:71],v[216:219],v[52:55]\nM v[112:115],a[72:75],v[216:219],v[112:115]\nM v[116:119],a[76:79],v[216:219],v[116:119]\nds_read_b64_tr_b16 v[216:217],v241 offset:42496\nds_read_b64_tr_b16 v[218:219],v241 offset:42752\nM v[56:59],a[64:67],v[220:223],v[56:59]\n" \
"M v[60:63],a[68:71],v[220:223],v[60:63]\nM v[120:123],a[72:75],v[220:223],v[120:123]\nM v[124:127],a[76:79],v[220:223],v[124:127]\nds_read_b64_tr_b16 v[220:221],v242 offset:42496\nds_read_b64_tr_b16 v[222:223],v242 offset:42752\nM v[0:3],a[80:83],v[192:195],v[0:3]\nM v[4:7],a[84:87],v[192:195],v[4:7]\nM v[64:67],a[88:91],v[192:195],v[64:67]\n" \
"M v[68:71],a[92:95],v[192:195],v[68:71]\ns_waitcnt lgkmcnt(7)\nM v[8:11],a[80:83],v[196:199],v[8:11]\nbuffer_store_dwordx4 v[160:163],v240,s[52:55],s74 offen offset:0 sc1\nM v[12:15],a[84:87],v[196:199],v[12:15]\nds_write_b64 v250,v[146:147] offset:4104\nM v[72:75],a[88:91],v[196:199],v[72:75]\nM v[76:79],a[92:95],v[196:199],v[76:79]\n" \
"M v[16:19],a[80:83],v[200:203],v[16:19]\nM v[20:23],a[84:87],v[200:203],v[20:23]\nM v[80:83],a[88:91],v[200:203],v[80:83]\nM v[84:87],a[92:95],v[200:203],v[84:87]\nM v[24:27],a[80:83],v[204:207],v[24:27]\ns_add_u32 m0,s64,131072\ns_add_u32 s69,s68,3145728\nM v[28:31],a[84:87],v[204:207],v[28:31]\n" \
"M v[88:91],a[88:91],v[204:207],v[88:91]\ns_waitcnt vmcnt(14) lgkmcnt(1)\ns_barrier\nM v[92:95],a[92:95],v[204:207],v[92:95]\nds_read_b64_tr_b16 v[192:193],v241 offset:49152\nds_read_b64_tr_b16 v[194:195],v241 offset:49408\nM v[32:35],a[80:83],v[208:211],v[32:35]\nds_read_b64_tr_b16 v[196:197],v242 offset:49152\n" \
"ds_read_b64_tr_b16 v[198:199],v242 offset:49408\nM v[36:39],a[84:87],v[208:211],v[36:39]\nds_read_b64_tr_b16 v[200:201],v241 offset:49664\nds_read_b64_tr_b16 v[202:203],v241 offset:49920\nM v[96:99],a[88:91],v[208:211],v[96:99]\nds_read_b64_tr_b16 v[204:205],v242 offset:49664\nds_read_b64_tr_b16 v[206:207],v242 offset:49920\nM v[100:103],a[92:95],v[208:211],v[100:103]\n" \
"ds_read_b64_tr_b16 v[208:209],v241 offset:50176\nds_read_b64_tr_b16 v[210:211],v241 offset:50432\nM v[40:43],a[80:83],v[212:215],v[40:43]\nbuffer_load_dwordx4 v243,s[40:43],s69 offen offset:0 lds\nM v[44:47],a[84:87],v[212:215],v[44:47]\nds_write_b64 v250,v[148:149] offset:5120\nM v[104:107],a[88:91],v[212:215],v[104:107]\nM v[108:111],a[92:95],v[212:215],v[108:111]\n" \
"ds_read_b64_tr_b16 v[212:213],v242 offset:50176\nds_read_b64_tr_b16 v[214:215],v242 offset:50432\nM v[48:51],a[80:83],v[216:219],v[48:51]\nbuffer_store_dwordx4 v[164:167],v240,s[52:55],s74 offen offset:1024 sc1\nM v[52:55],a[84:87],v[216:219],v[52:55]\nM v[112:115],a[88:91],v[216:219],v[112:115]\nM v[116:119],a[92:95],v[216:219],v[116:119]\nds_read_b64_tr_b16 v[216:217],v241 offset:50688\n" \
"ds_read_b64_tr_b16 v[218:219],v241 offset:50944\nM v[56:59],a[80:83],v[220:223],v[56:59]\nbuffer_load_dwordx4 v244,s[40:43],s69 offen offset:1024 lds\nM v[60:63],a[84:87],v[220:223],v[60:63]\nds_write_b64 v250,v[150:151] offset:5128\nM v[120:123],a[88:91],v[220:223],v[120:123]\nM v[124:127],a[92:95],v[220:223],v[124:127]\nds_read_b64_tr_b16 v[220:221],v242 offset:50688\n" \
"s_waitcnt lgkmcnt(9)\nM v[0:3],a[96:99],v[192:195],v[0:3]\nbuffer_load_dwordx4 v245,s[40:43],s69 offen offset:2048 lds\nM v[4:7],a[100:103],v[192:195],v[4:7]\nds_read_b64_tr_b16 v[222:223],v242 offset:50944\nM v[64:67],a[104:107],v[192:195],v[64:67]\nM v[68:71],a[108:111],v[192:195],v[68:71]\nds_read_b64_tr_b16 v[192:193],v241 offset:57344\n" \
"ds_read_b64_tr_b16 v[194:195],v241 offset:57600\nM v[8:11],a[96:99],v[196:199],v[8:11]\nbuffer_load_dwordx4 v246,s[40:43],s69 offen offset:3072 lds\nM v[12:15],a[100:103],v[196:199],v[12:15]\nM v[72:75],a[104:107],v[196:199],v[72:75]\nM v[76:79],a[108:111],v[196:199],v[76:79]\nds_read_b64_tr_b16 v[196:197],v242 offset:57344\nds_read_b64_tr_b16 v[198:199],v242 offset:57600\n" \
"M v[16:19],a[96:99],v[200:203],v[16:19]\nbuffer_store_dwordx4 v[168:171],v240,s[52:55],s74 offen offset:2048 sc1\nM v[20:23],a[100:103],v[200:203],v[20:23]\nds_write_b64 v250,v[152:153] offset:6144\nM v[80:83],a[104:107],v[200:203],v[80:83]\nM v[84:87],a[108:111],v[200:203],v[84:87]\nds_read_b64_tr_b16 v[200:201],v241 offset:57856\nds_read_b64_tr_b16 v[202:203],v241 offset:58112\n" \
"M v[24:27],a[96:99],v[204:207],v[24:27]\nM v[28:31],a[100:103],v[204:207],v[28:31]\nM v[88:91],a[104:107],v[204:207],v[88:91]\nM v[92:95],a[108:111],v[204:207],v[92:95]\nds_read_b64_tr_b16 v[204:205],v242 offset:57856\ns_waitcnt lgkmcnt(8)\nM v[32:35],a[96:99],v[208:211],v[32:35]\nds_read_b64_tr_b16 v[206:207],v242 offset:58112\n" \
"M v[36:39],a[100:103],v[208:211],v[36:39]\nM v[96:99],a[104:107],v[208:211],v[96:99]\nM v[100:103],a[108:111],v[208:211],v[100:103]\nds_read_b64_tr_b16 v[208:209],v241 offset:58368\nds_read_b64_tr_b16 v[210:211],v241 offset:58624\nM v[40:43],a[96:99],v[212:215],v[40:43]\nds_write_b64 v250,v[154:155] offset:6152\nM v[44:47],a[100:103],v[212:215],v[44:47]\n" \
"M v[104:107],a[104:107],v[212:215],v[104:107]\nM v[108:111],a[108:111],v[212:215],v[108:111]\nds_read_b64_tr_b16 v[212:213],v242 offset:58368\nds_read_b64_tr_b16 v[214:215],v242 offset:58624\nM v[48:51],a[96:99],v[216:219],v[48:51]\nM v[52:55],a[100:103],v[216:219],v[52:55]\nM v[112:115],a[104:107],v[216:219],v[112:115]\nbuffer_store_dwordx4 v[172:175],v240,s[52:55],s74 offen offset:3072 sc1\n" \
"M v[116:119],a[108:111],v[216:219],v[116:119]\nds_read_b64_tr_b16 v[216:217],v241 offset:58880\nds_read_b64_tr_b16 v[218:219],v241 offset:59136\nM v[56:59],a[96:99],v[220:223],v[56:59]\ns_add_u32 s74,s74,4096\nM v[60:63],a[100:103],v[220:223],v[60:63]\nM v[120:123],a[104:107],v[220:223],v[120:123]\nM v[124:127],a[108:111],v[220:223],v[124:127]\n" \
"ds_read_b64_tr_b16 v[220:221],v242 offset:58880\ns_waitcnt lgkmcnt(8)\nM v[0:3],a[112:115],v[192:195],v[0:3]\nds_write_b64 v250,v[156:157] offset:7168\nds_read_b64_tr_b16 v[222:223],v242 offset:59136\nM v[4:7],a[116:119],v[192:195],v[4:7]\nM v[64:67],a[120:123],v[192:195],v[64:67]\nM v[68:71],a[124:127],v[192:195],v[68:71]\n" \
"M v[8:11],a[112:115],v[196:199],v[8:11]\nM v[12:15],a[116:119],v[196:199],v[12:15]\nM v[72:75],a[120:123],v[196:199],v[72:75]\nM v[76:79],a[124:127],v[196:199],v[76:79]\nM v[16:19],a[112:115],v[200:203],v[16:19]\nM v[20:23],a[116:119],v[200:203],v[20:23]\nM v[80:83],a[120:123],v[200:203],v[80:83]\nM v[84:87],a[124:127],v[200:203],v[84:87]\n" \
"M v[24:27],a[112:115],v[204:207],v[24:27]\nbuffer_store_dwordx4 v[176:179],v240,s[52:55],s74 offen offset:0 sc1\nM v[28:31],a[116:119],v[204:207],v[28:31]\ns_add_u32 m0,s64,147456\ns_add_u32 s69,s68,3670016\nM v[88:91],a[120:123],v[204:207],v[88:91]\ns_waitcnt vmcnt(17) lgkmcnt(0)\ns_barrier\n" \
"M v[92:95],a[124:127],v[204:207],v[92:95]\nds_read_b64_tr_b16 v[192:193],v241 offset:0\nds_read_b64_tr_b16 v[194:195],v241 offset:256\nM v[32:35],a[112:115],v[208:211],v[32:35]\nds_read_b64_tr_b16 v[196:197],v242 offset:0\nds_read_b64_tr_b16 v[198:199],v242 offset:256\nM v[36:39],a[116:119],v[208:211],v[36:39]\nds_read_b64_tr_b16 v[200:201],v241 offset:512\n" \
"ds_read_b64_tr_b16 v[202:203],v241 offset:768\nM v[96:99],a[120:123],v[208:211],v[96:99]\nds_read_b64_tr_b16 v[204:205],v242 offset:512\nds_read_b64_tr_b16 v[206:207],v242 offset:768\nM v[100:103],a[124:127],v[208:211],v[100:103]\nds_read_b64_tr_b16 v[208:209],v241 offset:1024\nds_read_b64_tr_b16 v[210:211],v241 offset:1280\nM v[40:43],a[112:115],v[212:215],v[40:43]\n" \
"buffer_load_dwordx4 v243,s[40:43],s69 offen offset:0 lds\nM v[44:47],a[116:119],v[212:215],v[44:47]\nds_write_b64 v250,v[158:159] offset:7176\nM v[104:107],a[120:123],v[212:215],v[104:107]\nM v[108:111],a[124:127],v[212:215],v[108:111]\nds_read_b64_tr_b16 v[212:213],v242 offset:1024\nds_read_b64_tr_b16 v[214:215],v242 offset:1280\nM v[48:51],a[112:115],v[216:219],v[48:51]\n" \
"buffer_load_dwordx4 v244,s[40:43],s69 offen offset:1024 lds\nM v[52:55],a[116:119],v[216:219],v[52:55]\nds_write_b64 v250,v[160:161] offset:8192\nM v[112:115],a[120:123],v[216:219],v[112:115]\nM v[116:119],a[124:127],v[216:219],v[116:119]\nds_read_b64_tr_b16 v[216:217],v241 offset:1536\nds_read_b64_tr_b16 v[218:219],v241 offset:1792\nM v[56:59],a[112:115],v[220:223],v[56:59]\n" \
"buffer_load_dwordx4 v245,s[40:43],s69 offen offset:2048 lds\nM v[60:63],a[116:119],v[220:223],v[60:63]\nM v[120:123],a[120:123],v[220:223],v[120:123]\nM v[124:127],a[124:127],v[220:223],v[124:127]\nds_read_b64_tr_b16 v[220:221],v242 offset:1536\ns_waitcnt lgkmcnt(9)\nM v[0:3],a[128:131],v[192:195],v[0:3]\nbuffer_store_dwordx4 v[180:183],v240,s[52:55],s74 offen offset:1024 sc1\n" \
"M v[4:7],a[132:135],v[192:195],v[4:7]\nds_read_b64_tr_b16 v[222:223],v242 offset:1792\nM v[64:67],a[136:139],v[192:195],v[64:67]\nM v[68:71],a[140:143],v[192:195],v[68:71]\nds_read_b64_tr_b16 v[192:193],v241 offset:8192\nds_read_b64_tr_b16 v[194:195],v241 offset:8448\nM v[8:11],a[128:131],v[196:199],v[8:11]\nbuffer_load_dwordx4 v246,s[40:43],s69 offen offset:3072 lds\n" \
"M v[12:15],a[132:135],v[196:199],v[12:15]\nds_write_b64 v250,v[162:163] offset:8200\nM v[72:75],a[136:139],v[196:199],v[72:75]\nM v[76:79],a[140:143],v[196:199],v[76:79]\nds_read_b64_tr_b16 v[196:197],v242 offset:8192\nds_read_b64_tr_b16 v[198:199],v242 offset:8448\nM v[16:19],a[128:131],v[200:203],v[16:19]\nM v[20:23],a[132:135],v[200:203],v[20:23]\n" \
"M v[80:83],a[136:139],v[200:203],v[80:83]\nM v[84:87],a[140:143],v[200:203],v[84:87]\nds_read_b64_tr_b16 v[200:201],v241 offset:8704\nds_read_b64_tr_b16 v[202:203],v241 offset:8960\nM v[24:27],a[128:131],v[204:207],v[24:27]\nM v[28:31],a[132:135],v[204:207],v[28:31]\nM v[88:91],a[136:139],v[204:207],v[88:91]\nM v[92:95],a[140:143],v[204:207],v[92:95]\n" \
"ds_read_b64_tr_b16 v[204:205],v242 offset:8704\ns_waitcnt lgkmcnt(9)\nM v[32:35],a[128:131],v[208:211],v[32:35]\nbuffer_store_dwordx4 v[184:187],v240,s[52:55],s74 offen offset:2048 sc1\nM v[36:39],a[132:135],v[208:211],v[36:39]\nds_write_b64 v250,v[164:165] offset:9216\nds_read_b64_tr_b16 v[206:207],v242 offset:8960\nM v[96:99],a[136:139],v[208:211],v[96:99]\n" \
"M v[100:103],a[140:143],v[208:211],v[100:103]\nds_read_b64_tr_b16 v[208:209],v241 offset:9216\nds_read_b64_tr_b16 v[210:211],v241 offset:9472\nM v[40:43],a[128:131],v[212:215],v[40:43]\nM v[44:47],a[132:135],v[212:215],v[44:47]\nM v[104:107],a[136:139],v[212:215],v[104:107]\nbuffer_load_dword v248,v228,s[52:55],s80 offen sc1\nM v[108:111],a[140:143],v[212:215],v[108:111]\n" \
"ds_read_b64_tr_b16 v[212:213],v242 offset:9216\nds_read_b64_tr_b16 v[214:215],v242 offset:9472\nM v[48:51],a[128:131],v[216:219],v[48:51]\nM v[52:55],a[132:135],v[216:219],v[52:55]\nM v[112:115],a[136:139],v[216:219],v[112:115]\nM v[116:119],a[140:143],v[216:219],v[116:119]\nds_read_b64_tr_b16 v[216:217],v241 offset:9728\ns_waitcnt lgkmcnt(8)\n" \
"M v[56:59],a[128:131],v[220:223],v[56:59]\nds_write_b64 v250,v[166:167] offset:9224\nds_read_b64_tr_b16 v[218:219],v241 offset:9984\nM v[60:63],a[132:135],v[220:223],v[60:63]\nM v[120:123],a[136:139],v[220:223],v[120:123]\nM v[124:127],a[140:143],v[220:223],v[124:127]\nds_read_b64_tr_b16 v[220:221],v242 offset:9728\nds_read_b64_tr_b16 v[222:223],v242 offset:9984\n" \
"M v[0:3],a[144:147],v[192:195],v[0:3]\nM v[4:7],a[148:151],v[192:195],v[4:7]\nM v[64:67],a[152:155],v[192:195],v[64:67]\nbuffer_store_dwordx4 v[188:191],v240,s[52:55],s74 offen offset:3072 sc1\nM v[68:71],a[156:159],v[192:195],v[68:71]\ns_add_u32 s74,s74,16764928\nM v[8:11],a[144:147],v[196:199],v[8:11]\nM v[12:15],a[148:151],v[196:199],v[12:15]\n" \
"M v[72:75],a[152:155],v[196:199],v[72:75]\nM v[76:79],a[156:159],v[196:199],v[76:79]\nM v[16:19],a[144:147],v[200:203],v[16:19]\nds_write_b64 v250,v[168:169] offset:10240\nM v[20:23],a[148:151],v[200:203],v[20:23]\nM v[80:83],a[152:155],v[200:203],v[80:83]\nM v[84:87],a[156:159],v[200:203],v[84:87]\ns_waitcnt lgkmcnt(5)\n" \
"M v[24:27],a[144:147],v[204:207],v[24:27]\ns_sub_u32 s40,s12,0x1000\ns_subb_u32 s41,s13,0\nM v[28:31],a[148:151],v[204:207],v[28:31]\ns_add_u32 m0,s64,98304\ns_add_u32 s69,s68,0\nM v[88:91],a[152:155],v[204:207],v[88:91]\ns_waitcnt vmcnt(18) lgkmcnt(1)\n" \
"s_barrier\nM v[92:95],a[156:159],v[204:207],v[92:95]\nds_read_b64_tr_b16 v[192:193],v241 offset:16384\nds_read_b64_tr_b16 v[194:195],v241 offset:16640\nM v[32:35],a[144:147],v[208:211],v[32:35]\nds_read_b64_tr_b16 v[196:197],v242 offset:16384\nds_read_b64_tr_b16 v[198:199],v242 offset:16640\nM v[36:39],a[148:151],v[208:211],v[36:39]\n" \
"ds_read_b64_tr_b16 v[200:201],v241 offset:16896\nds_read_b64_tr_b16 v[202:203],v241 offset:17152\nM v[96:99],a[152:155],v[208:211],v[96:99]\nds_read_b64_tr_b16 v[204:205],v242 offset:16896\nds_read_b64_tr_b16 v[206:207],v242 offset:17152\nM v[100:103],a[156:159],v[208:211],v[100:103]\nds_read_b64_tr_b16 v[208:209],v241 offset:17408\nds_read_b64_tr_b16 v[210:211],v241 offset:17664\n" \
"M v[40:43],a[144:147],v[212:215],v[40:43]\nbuffer_load_dwordx4 v243,s[40:43],s69 offen offset:0 lds\nM v[44:47],a[148:151],v[212:215],v[44:47]\nds_write_b64 v250,v[170:171] offset:10248\ns_lshl_b32 s12,s61,13\nM v[104:107],a[152:155],v[212:215],v[104:107]\nM v[108:111],a[156:159],v[212:215],v[108:111]\nds_read_b64_tr_b16 v[212:213],v242 offset:17408\n" \
"ds_read_b64_tr_b16 v[214:215],v242 offset:17664\nM v[48:51],a[144:147],v[216:219],v[48:51]\nbuffer_load_dwordx4 v244,s[40:43],s69 offen offset:1024 lds\nM v[52:55],a[148:151],v[216:219],v[52:55]\nM v[112:115],a[152:155],v[216:219],v[112:115]\nM v[116:119],a[156:159],v[216:219],v[116:119]\nds_read_b64_tr_b16 v[216:217],v241 offset:17920\nds_read_b64_tr_b16 v[218:219],v241 offset:18176\n" \
"M v[56:59],a[144:147],v[220:223],v[56:59]\nbuffer_load_dwordx4 v245,s[40:43],s69 offen offset:2048 lds\nM v[60:63],a[148:151],v[220:223],v[60:63]\nM v[120:123],a[152:155],v[220:223],v[120:123]\nM v[124:127],a[156:159],v[220:223],v[124:127]\nds_read_b64_tr_b16 v[220:221],v242 offset:17920\ns_waitcnt lgkmcnt(8)\nM v[0:3],a[160:163],v[192:195],v[0:3]\n" \
"buffer_load_dwordx4 v246,s[40:43],s69 offen offset:3072 lds\nM v[4:7],a[164:167],v[192:195],v[4:7]\nds_write_b64 v250,v[172:173] offset:11264\nds_read_b64_tr_b16 v[222:223],v242 offset:18176\nM v[64:67],a[168:171],v[192:195],v[64:67]\nM v[68:71],a[172:175],v[192:195],v[68:71]\nds_read_b64_tr_b16 v[192:193],v241 offset:24576\nds_read_b64_tr_b16 v[194:195],v241 offset:24832\n" \
"M v[8:11],a[160:163],v[196:199],v[8:11]\nM v[12:15],a[164:167],v[196:199],v[12:15]\nM v[72:75],a[168:171],v[196:199],v[72:75]\nM v[76:79],a[172:175],v[196:199],v[76:79]\nds_read_b64_tr_b16 v[196:197],v242 offset:24576\nds_read_b64_tr_b16 v[198:199],v242 offset:24832\nM v[16:19],a[160:163],v[200:203],v[16:19]\nM v[20:23],a[164:167],v[200:203],v[20:23]\n" \
"M v[80:83],a[168:171],v[200:203],v[80:83]\nM v[84:87],a[172:175],v[200:203],v[84:87]\nds_read_b64_tr_b16 v[200:201],v241 offset:25088\nds_read_b64_tr_b16 v[202:203],v241 offset:25344\nM v[24:27],a[160:163],v[204:207],v[24:27]\nds_write_b64 v250,v[174:175] offset:11272\nM v[28:31],a[164:167],v[204:207],v[28:31]\ns_waitcnt vmcnt(5)\n" \
"v_cmp_eq_u32 vcc,0,v248\nM v[88:91],a[168:171],v[204:207],v[88:91]\ns_cbranch_vccnz .Lps0\n.Lpb0:\ns_lshl_b32 s80,s63,7\nM v[92:95],a[172:175],v[204:207],v[92:95]\nds_read_b64_tr_b16 v[204:205],v242 offset:25088\ns_waitcnt lgkmcnt(8)\n" \
"M v[32:35],a[160:163],v[208:211],v[32:35]\nds_read_b64_tr_b16 v[206:207],v242 offset:25344\ns_add_u32 s80,s80,81920\nM v[36:39],a[164:167],v[208:211],v[36:39]\nbuffer_load_dwordx4 a[0:3],v240,s[52:55],s76 offen offset:0 sc1\nM v[96:99],a[168:171],v[208:211],v[96:99]\nv_and_b32 v228,31,v249\nv_lshlrev_b32 v228,2,v228\n" \
"M v[100:103],a[172:175],v[208:211],v[100:103]\nds_read_b64_tr_b16 v[208:209],v241 offset:25600\nds_read_b64_tr_b16 v[210:211],v241 offset:25856\nM v[40:43],a[160:163],v[212:215],v[40:43]\nv_mov_b32 v251,v228\nM v[44:47],a[164:167],v[212:215],v[44:47]\nbuffer_load_dwordx4 a[4:7],v240,s[52:55],s76 offen offset:1024 sc1\nM v[104:107],a[168:171],v[212:215],v[104:107]\n" \
"M v[108:111],a[172:175],v[212:215],v[108:111]\nds_read_b64_tr_b16 v[212:213],v242 offset:25600\nds_read_b64_tr_b16 v[214:215],v242 offset:25856\nM v[48:51],a[160:163],v[216:219],v[48:51]\nds_write_b64 v250,v[176:177] offset:12288\nM v[52:55],a[164:167],v[216:219],v[52:55]\nbuffer_load_dwordx4 a[8:11],v240,s[52:55],s77 offen offset:0 sc1\nM v[112:115],a[168:171],v[216:219],v[112:115]\n" \
"M v[116:119],a[172:175],v[216:219],v[116:119]\nds_read_b64_tr_b16 v[216:217],v241 offset:26112\nds_read_b64_tr_b16 v[218:219],v241 offset:26368\nM v[56:59],a[160:163],v[220:223],v[56:59]\nM v[60:63],a[164:167],v[220:223],v[60:63]\nbuffer_load_dwordx4 a[12:15],v240,s[52:55],s77 offen offset:1024 sc1\nM v[120:123],a[168:171],v[220:223],v[120:123]\nM v[124:127],a[172:175],v[220:223],v[124:127]\n" \
"ds_read_b64_tr_b16 v[220:221],v242 offset:26112\ns_waitcnt lgkmcnt(8)\nM v[0:3],a[176:179],v[192:195],v[0:3]\nds_read_b64_tr_b16 v[222:223],v242 offset:26368\nM v[4:7],a[180:183],v[192:195],v[4:7]\nM v[64:67],a[184:187],v[192:195],v[64:67]\nbuffer_load_dwordx4 a[16:19],v240,s[52:55],s76 offen offset:2048 sc1\nM v[68:71],a[188:191],v[192:195],v[68:71]\n" \
"M v[8:11],a[176:179],v[196:199],v[8:11]\nds_write_b64 v250,v[178:179] offset:12296\nM v[12:15],a[180:183],v[196:199],v[12:15]\nM v[72:75],a[184:187],v[196:199],v[72:75]\nM v[76:79],a[188:191],v[196:199],v[76:79]\nM v[16:19],a[176:179],v[200:203],v[16:19]\nM v[20:23],a[180:183],v[200:203],v[20:23]\nM v[80:83],a[184:187],v[200:203],v[80:83]\n" \
"buffer_load_dwordx4 a[20:23],v240,s[52:55],s76 offen offset:3072 sc1\nM v[84:87],a[188:191],v[200:203],v[84:87]\ns_add_u32 s76,s76,4096\nM v[24:27],a[176:179],v[204:207],v[24:27]\ns_add_u32 m0,s64,114688\ns_add_u32 s69,s68,524288\nM v[28:31],a[180:183],v[204:207],v[28:31]\nM v[88:91],a[184:187],v[204:207],v[88:91]\n" \
"s_waitcnt lgkmcnt(1)\ns_barrier\nM v[92:95],a[188:191],v[204:207],v[92:95]\nds_read_b64_tr_b16 v[192:193],v241 offset:32768\nds_read_b64_tr_b16 v[194:195],v241 offset:33024\nM v[32:35],a[176:179],v[208:211],v[32:35]\nds_read_b64_tr_b16 v[196:197],v242 offset:32768\nds_read_b64_tr_b16 v[198:199],v242 offset:33024\n" \
"M v[36:39],a[180:183],v[208:211],v[36:39]\nds_read_b64_tr_b16 v[200:201],v241 offset:33280\nds_read_b64_tr_b16 v[202:203],v241 offset:33536\nM v[96:99],a[184:187],v[208:211],v[96:99]\nds_read_b64_tr_b16 v[204:205],v242 offset:33280\nds_read_b64_tr_b16 v[206:207],v242 offset:33536\nM v[100:103],a[188:191],v[208:211],v[100:103]\nds_read_b64_tr_b16 v[208:209],v241 offset:33792\n" \
"ds_read_b64_tr_b16 v[210:211],v241 offset:34048\nM v[40:43],a[176:179],v[212:215],v[40:43]\nbuffer_load_dwordx4 a[24:27],v240,s[52:55],s77 offen offset:2048 sc1\nM v[44:47],a[180:183],v[212:215],v[44:47]\nds_write_b64 v250,v[180:181] offset:13312\nM v[104:107],a[184:187],v[212:215],v[104:107]\nM v[108:111],a[188:191],v[212:215],v[108:111]\nds_read_b64_tr_b16 v[212:213],v242 offset:33792\n" \
"ds_read_b64_tr_b16 v[214:215],v242 offset:34048\nM v[48:51],a[176:179],v[216:219],v[48:51]\nbuffer_load_dwordx4 v243,s[40:43],s69 offen offset:0 lds\nM v[52:55],a[180:183],v[216:219],v[52:55]\nM v[112:115],a[184:187],v[216:219],v[112:115]\nM v[116:119],a[188:191],v[216:219],v[116:119]\nds_read_b64_tr_b16 v[216:217],v241 offset:34304\nds_read_b64_tr_b16 v[218:219],v241 offset:34560\n" \
"M v[56:59],a[176:179],v[220:223],v[56:59]\nbuffer_load_dwordx4 a[28:31],v240,s[52:55],s77 offen offset:3072 sc1\nM v[60:63],a[180:183],v[220:223],v[60:63]\nds_write_b64 v250,v[182:183] offset:13320\ns_add_u32 s77,s77,4096\nM v[120:123],a[184:187],v[220:223],v[120:123]\nM v[124:127],a[188:191],v[220:223],v[124:127]\nds_read_b64_tr_b16 v[220:221],v242 offset:34304\n" \
"s_waitcnt lgkmcnt(9)\nM v[0:3],a[192:195],v[192:195],v[0:3]\nbuffer_load_dwordx4 v244,s[40:43],s69 offen offset:1024 lds\nM v[4:7],a[196:199],v[192:195],v[4:7]\nds_read_b64_tr_b16 v[222:223],v242 offset:34560\nM v[64:67],a[200:203],v[192:195],v[64:67]\nM v[68:71],a[204:207],v[192:195],v[68:71]\nds_read_b64_tr_b16 v[192:193],v241 offset:40960\n" \
"ds_read_b64_tr_b16 v[194:195],v241 offset:41216\nM v[8:11],a[192:195],v[196:199],v[8:11]\nbuffer_load_dwordx4 a[32:35],v240,s[52:55],s76 offen offset:0 sc1\nM v[12:15],a[196:199],v[196:199],v[12:15]\nM v[72:75],a[200:203],v[196:199],v[72:75]\nM v[76:79],a[204:207],v[196:199],v[76:79]\nds_read_b64_tr_b16 v[196:197],v242 offset:40960\nds_read_b64_tr_b16 v[198:199],v242 offset:41216\n" \
"M v[16:19],a[192:195],v[200:203],v[16:19]\nbuffer_load_dwordx4 v245,s[40:43],s69 offen offset:2048 lds\nM v[20:23],a[196:199],v[200:203],v[20:23]\nds_write_b64 v250,v[184:185] offset:14336\nM v[80:83],a[200:203],v[200:203],v[80:83]\nM v[84:87],a[204:207],v[200:203],v[84:87]\nds_read_b64_tr_b16 v[200:201],v241 offset:41472\nds_read_b64_tr_b16 v[202:203],v241 offset:41728\n" \
"M v[24:27],a[192:195],v[204:207],v[24:27]\nbuffer_load_dwordx4 a[36:39],v240,s[52:55],s76 offen offset:1024 sc1\nM v[28:31],a[196:199],v[204:207],v[28:31]\nM v[88:91],a[200:203],v[204:207],v[88:91]\nM v[92:95],a[204:207],v[204:207],v[92:95]\nds_read_b64_tr_b16 v[204:205],v242 offset:41472\ns_waitcnt lgkmcnt(8)\nM v[32:35],a[192:195],v[208:211],v[32:35]\n" \
"buffer_load_dwordx4 v246,s[40:43],s69 offen offset:3072 lds\nM v[36:39],a[196:199],v[208:211],v[36:39]\nds_read_b64_tr_b16 v[206:207],v242 offset:41728\nM v[96:99],a[200:203],v[208:211],v[96:99]\nM v[100:103],a[204:207],v[208:211],v[100:103]\nds_read_b64_tr_b16 v[208:209],v241 offset:41984\nds_read_b64_tr_b16 v[210:211],v241 offset:42240\nM v[40:43],a[192:195],v[212:215],v[40:43]\n" \
"buffer_load_dwordx4 a[40:43],v240,s[52:55],s77 offen offset:0 sc1\nM v[44:47],a[196:199],v[212:215],v[44:47]\nds_write_b64 v250,v[186:187] offset:14344\nM v[104:107],a[200:203],v[212:215],v[104:107]\nM v[108:111],a[204:207],v[212:215],v[108:111]\nds_read_b64_tr_b16 v[212:213],v242 offset:41984\nds_read_b64_tr_b16 v[214:215],v242 offset:42240\nM v[48:51],a[192:195],v[216:219],v[48:51]\n" \
"s_waitcnt vmcnt(19)\nbuffer_store_dword v252,off,s[52:55],s29 sc1\nM v[52:55],a[196:199],v[216:219],v[52:55]\ns_add_u32 s29,s29,4096\nM v[112:115],a[200:203],v[216:219],v[112:115]\nbuffer_load_dwordx4 a[44:47],v240,s[52:55],s77 offen offset:1024 sc1\nM v[116:119],a[204:207],v[216:219],v[116:119]\nds_read_b64_tr_b16 v[216:217],v241 offset:42496\n" \
"ds_read_b64_tr_b16 v[218:219],v241 offset:42752\nM v[56:59],a[192:195],v[220:223],v[56:59]\nM v[60:63],a[196:199],v[220:223],v[60:63]\nM v[120:123],a[200:203],v[220:223],v[120:123]\nM v[124:127],a[204:207],v[220:223],v[124:127]\nds_read_b64_tr_b16 v[220:221],v242 offset:42496\ns_waitcnt lgkmcnt(8)\nM v[0:3],a[208:211],v[192:195],v[0:3]\n" \
"ds_write_b64 v250,v[188:189] offset:15360\nds_read_b64_tr_b16 v[222:223],v242 offset:42752\nM v[4:7],a[212:215],v[192:195],v[4:7]\nM v[64:67],a[216:219],v[192:195],v[64:67]\nbuffer_load_dwordx4 a[48:51],v240,s[52:55],s76 offen offset:2048 sc1\nM v[68:71],a[220:223],v[192:195],v[68:71]\nM v[8:11],a[208:211],v[196:199],v[8:11]\nM v[12:15],a[212:215],v[196:199],v[12:15]\n" \
"M v[72:75],a[216:219],v[196:199],v[72:75]\nM v[76:79],a[220:223],v[196:199],v[76:79]\nM v[16:19],a[208:211],v[200:203],v[16:19]\nM v[20:23],a[212:215],v[200:203],v[20:23]\nM v[80:83],a[216:219],v[200:203],v[80:83]\nbuffer_load_dwordx4 a[52:55],v240,s[52:55],s76 offen offset:3072 sc1\nM v[84:87],a[220:223],v[200:203],v[84:87]\ns_add_u32 s76,s76,4096\n" \
"M v[24:27],a[208:211],v[204:207],v[24:27]\nds_write_b64 v250,v[190:191] offset:15368\ns_add_u32 m0,s64,131072\nM v[28:31],a[212:215],v[204:207],v[28:31]\ns_add_u32 s69,s68,1048576\nv_add_u32 v250,65536,v250\nM v[88:91],a[216:219],v[204:207],v[88:91]\ns_waitcnt lgkmcnt(1)\n" \
"s_barrier\nM v[92:95],a[220:223],v[204:207],v[92:95]\nds_read_b64_tr_b16 v[192:193],v241 offset:49152\nds_read_b64_tr_b16 v[194:195],v241 offset:49408\nM v[32:35],a[208:211],v[208:211],v[32:35]\nds_read_b64_tr_b16 v[196:197],v242 offset:49152\nds_read_b64_tr_b16 v[198:199],v242 offset:49408\nM v[36:39],a[212:215],v[208:211],v[36:39]\n" \
"ds_read_b64_tr_b16 v[200:201],v241 offset:49664\nds_read_b64_tr_b16 v[202:203],v241 offset:49920\nM v[96:99],a[216:219],v[208:211],v[96:99]\nds_read_b64_tr_b16 v[204:205],v242 offset:49664\nds_read_b64_tr_b16 v[206:207],v242 offset:49920\nM v[100:103],a[220:223],v[208:211],v[100:103]\nds_read_b64_tr_b16 v[208:209],v241 offset:50176\nds_read_b64_tr_b16 v[210:211],v241 offset:50432\n" \
"M v[40:43],a[208:211],v[212:215],v[40:43]\nbuffer_load_dwordx4 a[56:59],v240,s[52:55],s77 offen offset:2048 sc1\nM v[44:47],a[212:215],v[212:215],v[44:47]\nM v[104:107],a[216:219],v[212:215],v[104:107]\nM v[108:111],a[220:223],v[212:215],v[108:111]\nds_read_b64_tr_b16 v[212:213],v242 offset:50176\nds_read_b64_tr_b16 v[214:215],v242 offset:50432\nM v[48:51],a[208:211],v[216:219],v[48:51]\n" \
"buffer_load_dwordx4 v243,s[40:43],s69 offen offset:0 lds\nM v[52:55],a[212:215],v[216:219],v[52:55]\nM v[112:115],a[216:219],v[216:219],v[112:115]\nM v[116:119],a[220:223],v[216:219],v[116:119]\nds_read_b64_tr_b16 v[216:217],v241 offset:50688\nds_read_b64_tr_b16 v[218:219],v241 offset:50944\nM v[56:59],a[208:211],v[220:223],v[56:59]\nbuffer_load_dwordx4 a[60:63],v240,s[52:55],s77 offen offset:3072 sc1\n" \
"M v[60:63],a[212:215],v[220:223],v[60:63]\ns_add_u32 s77,s77,4096\nM v[120:123],a[216:219],v[220:223],v[120:123]\nM v[124:127],a[220:223],v[220:223],v[124:127]\nds_read_b64_tr_b16 v[220:221],v242 offset:50688\ns_waitcnt lgkmcnt(7)\nM v[0:3],a[224:227],v[192:195],v[0:3]\nbuffer_load_dwordx4 v244,s[40:43],s69 offen offset:1024 lds\n" \
"M v[4:7],a[228:231],v[192:195],v[4:7]\nds_read_b64_tr_b16 v[222:223],v242 offset:50944\nM v[64:67],a[232:235],v[192:195],v[64:67]\nM v[68:71],a[236:239],v[192:195],v[68:71]\nds_read_b64_tr_b16 v[192:193],v241 offset:57344\nds_read_b64_tr_b16 v[194:195],v241 offset:57600\nM v[8:11],a[224:227],v[196:199],v[8:11]\nbuffer_load_dwordx4 a[64:67],v240,s[52:55],s76 offen offset:0 sc1\n" \
"M v[12:15],a[228:231],v[196:199],v[12:15]\nM v[72:75],a[232:235],v[196:199],v[72:75]\nM v[76:79],a[236:239],v[196:199],v[76:79]\nds_read_b64_tr_b16 v[196:197],v242 offset:57344\nds_read_b64_tr_b16 v[198:199],v242 offset:57600\nM v[16:19],a[224:227],v[200:203],v[16:19]\nbuffer_load_dwordx4 v245,s[40:43],s69 offen offset:2048 lds\nM v[20:23],a[228:231],v[200:203],v[20:23]\n" \
"M v[80:83],a[232:235],v[200:203],v[80:83]\nM v[84:87],a[236:239],v[200:203],v[84:87]\nds_read_b64_tr_b16 v[200:201],v241 offset:57856\nds_read_b64_tr_b16 v[202:203],v241 offset:58112\nM v[24:27],a[224:227],v[204:207],v[24:27]\nbuffer_load_dwordx4 a[68:71],v240,s[52:55],s76 offen offset:1024 sc1\nM v[28:31],a[228:231],v[204:207],v[28:31]\nM v[88:91],a[232:235],v[204:207],v[88:91]\n" \
"M v[92:95],a[236:239],v[204:207],v[92:95]\nds_read_b64_tr_b16 v[204:205],v242 offset:57856\ns_waitcnt lgkmcnt(7)\nM v[32:35],a[224:227],v[208:211],v[32:35]\nbuffer_load_dwordx4 v246,s[40:43],s69 offen offset:3072 lds\nM v[36:39],a[228:231],v[208:211],v[36:39]\nds_read_b64_tr_b16 v[206:207],v242 offset:58112\nM v[96:99],a[232:235],v[208:211],v[96:99]\n" \
"M v[100:103],a[236:239],v[208:211],v[100:103]\nds_read_b64_tr_b16 v[208:209],v241 offset:58368\nds_read_b64_tr_b16 v[210:211],v241 offset:58624\nM v[40:43],a[224:227],v[212:215],v[40:43]\nbuffer_load_dwordx4 a[72:75],v240,s[52:55],s77 offen offset:0 sc1\nM v[44:47],a[228:231],v[212:215],v[44:47]\nM v[104:107],a[232:235],v[212:215],v[104:107]\nM v[108:111],a[236:239],v[212:215],v[108:111]\n" \
"ds_read_b64_tr_b16 v[212:213],v242 offset:58368\nds_read_b64_tr_b16 v[214:215],v242 offset:58624\nM v[48:51],a[224:227],v[216:219],v[48:51]\nM v[52:55],a[228:231],v[216:219],v[52:55]\nM v[112:115],a[232:235],v[216:219],v[112:115]\nbuffer_load_dwordx4 a[76:79],v240,s[52:55],s77 offen offset:1024 sc1\nM v[116:119],a[236:239],v[216:219],v[116:119]\nds_read_b64_tr_b16 v[216:217],v241 offset:58880\n" \
"ds_read_b64_tr_b16 v[218:219],v241 offset:59136\nM v[56:59],a[224:227],v[220:223],v[56:59]\nM v[60:63],a[228:231],v[220:223],v[60:63]\nM v[120:123],a[232:235],v[220:223],v[120:123]\nM v[124:127],a[236:239],v[220:223],v[124:127]\nds_read_b64_tr_b16 v[220:221],v242 offset:58880\ns_waitcnt lgkmcnt(8)\nM v[0:3],a[240:243],v[192:195],v[0:3]\n" \
"ds_read_b64_tr_b16 v[222:223],v242 offset:59136\nM v[4:7],a[244:247],v[192:195],v[4:7]\nM v[64:67],a[248:251],v[192:195],v[64:67]\nbuffer_load_dwordx4 a[80:83],v240,s[52:55],s76 offen offset:2048 sc1\nM v[68:71],a[252:255],v[192:195],v[68:71]\nM v[8:11],a[240:243],v[196:199],v[8:11]\nM v[12:15],a[244:247],v[196:199],v[12:15]\nM v[72:75],a[248:251],v[196:199],v[72:75]\n" \
"v_cvt_pk_bf16_f32 v128,v0,v1\nv_cvt_pk_bf16_f32 v129,v2,v3\nM v[76:79],a[252:255],v[196:199],v[76:79]\nv_cvt_pk_bf16_f32 v130,v4,v5\nv_cvt_pk_bf16_f32 v131,v6,v7\nM v[16:19],a[240:243],v[200:203],v[16:19]\nv_cvt_pk_bf16_f32 v160,v64,v65\nv_cvt_pk_bf16_f32 v161,v66,v67\n" \
"M v[20:23],a[244:247],v[200:203],v[20:23]\nv_cvt_pk_bf16_f32 v162,v68,v69\nv_cvt_pk_bf16_f32 v163,v70,v71\nM v[80:83],a[248:251],v[200:203],v[80:83]\nv_cvt_pk_bf16_f32 v132,v8,v9\nv_cvt_pk_bf16_f32 v133,v10,v11\nM v[84:87],a[252:255],v[200:203],v[84:87]\nv_cvt_pk_bf16_f32 v134,v12,v13\n" \
"s_waitcnt lgkmcnt(2)\nM v[24:27],a[240:243],v[204:207],v[24:27]\nv_cvt_pk_bf16_f32 v135,v14,v15\nv_cvt_pk_bf16_f32 v164,v72,v73\nM v[28:31],a[244:247],v[204:207],v[28:31]\nv_cvt_pk_bf16_f32 v165,v74,v75\nv_cvt_pk_bf16_f32 v166,v76,v77\nM v[88:91],a[248:251],v[204:207],v[88:91]\n" \
"s_waitcnt vmcnt(23) lgkmcnt(0)\ns_barrier\nM v[92:95],a[252:255],v[204:207],v[92:95]\nds_read_b64_tr_b16 v[192:193],v241 offset:0\nds_read_b64_tr_b16 v[194:195],v241 offset:256\nM v[32:35],a[240:243],v[208:211],v[32:35]\nds_read_b64_tr_b16 v[196:197],v242 offset:0\nds_read_b64_tr_b16 v[198:199],v242 offset:256\n" \
"M v[36:39],a[244:247],v[208:211],v[36:39]\nds_read_b64_tr_b16 v[200:201],v241 offset:512\nds_read_b64_tr_b16 v[202:203],v241 offset:768\nM v[96:99],a[248:251],v[208:211],v[96:99]\nds_read_b64_tr_b16 v[204:205],v242 offset:512\nds_read_b64_tr_b16 v[206:207],v242 offset:768\nM v[100:103],a[252:255],v[208:211],v[100:103]\nds_read_b64_tr_b16 v[208:209],v241 offset:1024\n" \
"ds_read_b64_tr_b16 v[210:211],v241 offset:1280\nM v[40:43],a[240:243],v[212:215],v[40:43]\nv_cvt_pk_bf16_f32 v167,v78,v79\nv_cvt_pk_bf16_f32 v136,v16,v17\nM v[44:47],a[244:247],v[212:215],v[44:47]\nv_cvt_pk_bf16_f32 v137,v18,v19\nv_cvt_pk_bf16_f32 v138,v20,v21\nM v[104:107],a[248:251],v[212:215],v[104:107]\n" \
"v_cvt_pk_bf16_f32 v139,v22,v23\nv_cvt_pk_bf16_f32 v168,v80,v81\nM v[108:111],a[252:255],v[212:215],v[108:111]\nv_cvt_pk_bf16_f32 v169,v82,v83\nds_read_b64_tr_b16 v[212:213],v242 offset:1024\nM v[48:51],a[240:243],v[216:219],v[48:51]\nds_read_b64_tr_b16 v[214:215],v242 offset:1280\nv_cvt_pk_bf16_f32 v170,v84,v85\n" \
"M v[52:55],a[244:247],v[216:219],v[52:55]\nv_cvt_pk_bf16_f32 v171,v86,v87\nv_cvt_pk_bf16_f32 v140,v24,v25\nM v[112:115],a[248:251],v[216:219],v[112:115]\nv_cvt_pk_bf16_f32 v141,v26,v27\nv_cvt_pk_bf16_f32 v142,v28,v29\nM v[116:119],a[252:255],v[216:219],v[116:119]\nv_cvt_pk_bf16_f32 v143,v30,v31\n" \
"v_cvt_pk_bf16_f32 v172,v88,v89\nM v[56:59],a[240:243],v[220:223],v[56:59]\nv_cvt_pk_bf16_f32 v173,v90,v91\nds_read_b64_tr_b16 v[216:217],v241 offset:1536\nM v[60:63],a[244:247],v[220:223],v[60:63]\nds_read_b64_tr_b16 v[218:219],v241 offset:1792\nv_cvt_pk_bf16_f32 v174,v92,v93\nM v[120:123],a[248:251],v[220:223],v[120:123]\n" \
"v_cvt_pk_bf16_f32 v175,v94,v95\nv_cvt_pk_bf16_f32 v144,v32,v33\nM v[124:127],a[252:255],v[220:223],v[124:127]\nv_cvt_pk_bf16_f32 v145,v34,v35\ns_waitcnt lgkmcnt(4)\nM v[0:3],v[192:195],a[0:3],0\nv_cvt_pk_bf16_f32 v146,v36,v37\nv_cvt_pk_bf16_f32 v147,v38,v39\n" \
"M v[4:7],v[192:195],a[4:7],0\nv_cvt_pk_bf16_f32 v176,v96,v97\nv_cvt_pk_bf16_f32 v177,v98,v99\nM v[64:67],v[192:195],a[8:11],0\nds_read_b64_tr_b16 v[220:221],v242 offset:1536\nds_read_b64_tr_b16 v[222:223],v242 offset:1792\nM v[68:71],v[192:195],a[12:15],0\nv_cvt_pk_bf16_f32 v178,v100,v101\n" \
"v_cvt_pk_bf16_f32 v179,v102,v103\nM v[8:11],v[196:199],a[0:3],0\nv_cvt_pk_bf16_f32 v148,v40,v41\nv_cvt_pk_bf16_f32 v149,v42,v43\nM v[12:15],v[196:199],a[4:7],0\nv_cvt_pk_bf16_f32 v150,v44,v45\nv_cvt_pk_bf16_f32 v151,v46,v47\nM v[72:75],v[196:199],a[8:11],0\n" \
"v_cvt_pk_bf16_f32 v180,v104,v105\nv_cvt_pk_bf16_f32 v181,v106,v107\nM v[76:79],v[196:199],a[12:15],0\nds_read_b64_tr_b16 v[192:193],v241 offset:8192\nds_read_b64_tr_b16 v[194:195],v241 offset:8448\nM v[16:19],v[200:203],a[0:3],0\nv_cvt_pk_bf16_f32 v182,v108,v109\nv_cvt_pk_bf16_f32 v183,v110,v111\n" \
"M v[20:23],v[200:203],a[4:7],0\nv_cvt_pk_bf16_f32 v152,v48,v49\nv_cvt_pk_bf16_f32 v153,v50,v51\nM v[80:83],v[200:203],a[8:11],0\nv_cvt_pk_bf16_f32 v154,v52,v53\nv_cvt_pk_bf16_f32 v155,v54,v55\nM v[84:87],v[200:203],a[12:15],0\nv_cvt_pk_bf16_f32 v184,v112,v113\n" \
"v_cvt_pk_bf16_f32 v185,v114,v115\nM v[24:27],v[204:207],a[0:3],0\nds_read_b64_tr_b16 v[196:197],v242 offset:8192\nds_read_b64_tr_b16 v[198:199],v242 offset:8448\nM v[28:31],v[204:207],a[4:7],0\nv_cvt_pk_bf16_f32 v186,v116,v117\nv_cvt_pk_bf16_f32 v187,v118,v119\nM v[88:91],v[204:207],a[8:11],0\n" \
"v_cvt_pk_bf16_f32 v156,v56,v57\nv_cvt_pk_bf16_f32 v157,v58,v59\nM v[92:95],v[204:207],a[12:15],0\nv_cvt_pk_bf16_f32 v158,v60,v61\nv_cvt_pk_bf16_f32 v159,v62,v63\nM v[32:35],v[208:211],a[0:3],0\nv_cvt_pk_bf16_f32 v188,v120,v121\nv_cvt_pk_bf16_f32 v189,v122,v123\n" \
"M v[36:39],v[208:211],a[4:7],0\nds_read_b64_tr_b16 v[200:201],v241 offset:8704\nds_read_b64_tr_b16 v[202:203],v241 offset:8960\nM v[96:99],v[208:211],a[8:11],0\nv_cvt_pk_bf16_f32 v190,v124,v125\nv_cvt_pk_bf16_f32 v191,v126,v127\nM v[100:103],v[208:211],a[12:15],0\nds_read_b64_tr_b16 v[204:205],v242 offset:8704\n" \
"s_waitcnt lgkmcnt(5)\nM v[40:43],v[212:215],a[0:3],0\nbuffer_store_dwordx4 v[128:131],v240,s[52:55],s74 offen offset:0 sc1\nM v[44:47],v[212:215],a[4:7],0\nds_read_b64_tr_b16 v[206:207],v242 offset:8960\nds_read_b64_tr_b16 v[208:209],v241 offset:9216\nM v[104:107],v[212:215],a[8:11],0\nds_read_b64_tr_b16 v[210:211],v241 offset:9472\n" \
"s_add_u32 m0,s64,147456\nM v[108:111],v[212:215],a[12:15],0\nds_read_b64_tr_b16 v[212:213],v242 offset:9216\nds_read_b64_tr_b16 v[214:215],v242 offset:9472\nM v[48:51],v[216:219],a[0:3],0\nbuffer_load_dwordx4 a[84:87],v240,s[52:55],s76 offen offset:3072 sc1\nM v[52:55],v[216:219],a[4:7],0\ns_add_u32 s69,s68,1572864\n" \
"s_add_u32 s76,s76,4096\nM v[112:115],v[216:219],a[8:11],0\nM v[116:119],v[216:219],a[12:15],0\nds_read_b64_tr_b16 v[216:217],v241 offset:9728\nds_read_b64_tr_b16 v[218:219],v241 offset:9984\nM v[56:59],v[220:223],a[0:3],0\nbuffer_load_dwordx4 a[88:91],v240,s[52:55],s77 offen offset:2048 sc1\nM v[60:63],v[220:223],a[4:7],0\n" \
"M v[120:123],v[220:223],a[8:11],0\nM v[124:127],v[220:223],a[12:15],0\nds_read_b64_tr_b16 v[220:221],v242 offset:9728\nds_read_b64_tr_b16 v[222:223],v242 offset:9984\nM v[0:3],v[192:195],a[16:19],v[0:3]\nbuffer_load_dwordx4 a[92:95],v240,s[52:55],s77 offen offset:3072 sc1\nM v[4:7],v[192:195],a[20:23],v[4:7]\ns_add_u32 s77,s77,4096\n" \
"M v[64:67],v[192:195],a[24:27],v[64:67]\ns_waitcnt vmcnt(25)\nM v[68:71],v[192:195],a[28:31],v[68:71]\ns_waitcnt lgkmcnt(6)\nM v[8:11],v[196:199],a[16:19],v[8:11]\nbuffer_load_dwordx4 a[96:99],v240,s[52:55],s76 offen offset:0 sc1\nM v[12:15],v[196:199],a[20:23],v[12:15]\nM v[72:75],v[196:199],a[24:27],v[72:75]\n" \
"M v[76:79],v[196:199],a[28:31],v[76:79]\nM v[16:19],v[200:203],a[16:19],v[16:19]\nbuffer_load_dwordx4 a[100:103],v240,s[52:55],s76 offen offset:1024 sc1\nM v[20:23],v[200:203],a[20:23],v[20:23]\nM v[80:83],v[200:203],a[24:27],v[80:83]\nM v[84:87],v[200:203],a[28:31],v[84:87]\nM v[24:27],v[204:207],a[16:19],v[24:27]\nbuffer_load_dwordx4 a[104:107],v240,s[52:55],s77 offen offset:0 sc1\n" \
"M v[28:31],v[204:207],a[20:23],v[28:31]\nM v[88:91],v[204:207],a[24:27],v[88:91]\ns_waitcnt vmcnt(22) lgkmcnt(0)\ns_barrier\nM v[92:95],v[204:207],a[28:31],v[92:95]\nds_read_b64_tr_b16 v[192:193],v241 offset:16384\nds_read_b64_tr_b16 v[194:195],v241 offset:16640\nM v[32:35],v[208:211],a[16:19],v[32:35]\n" \
"ds_read_b64_tr_b16 v[196:197],v242 offset:16384\nds_read_b64_tr_b16 v[198:199],v242 offset:16640\nM v[36:39],v[208:211],a[20:23],v[36:39]\nds_read_b64_tr_b16 v[200:201],v241 offset:16896\nds_read_b64_tr_b16 v[202:203],v241 offset:17152\nM v[96:99],v[208:211],a[24:27],v[96:99]\nbuffer_store_dwordx4 v[132:135],v240,s[52:55],s74 offen offset:1024 sc1\nM v[100:103],v[208:211],a[28:31],v[100:103]\n" \
"ds_read_b64_tr_b16 v[204:205],v242 offset:16896\nds_read_b64_tr_b16 v[206:207],v242 offset:17152\nM v[40:43],v[212:215],a[16:19],v[40:43]\nds_read_b64_tr_b16 v[208:209],v241 offset:17408\nds_read_b64_tr_b16 v[210:211],v241 offset:17664\nM v[44:47],v[212:215],a[20:23],v[44:47]\nM v[104:107],v[212:215],a[24:27],v[104:107]\nbuffer_load_dwordx4 a[108:111],v240,s[52:55],s77 offen offset:1024 sc1\n" \
"M v[108:111],v[212:215],a[28:31],v[108:111]\nds_read_b64_tr_b16 v[212:213],v242 offset:17408\nds_read_b64_tr_b16 v[214:215],v242 offset:17664\nM v[48:51],v[216:219],a[16:19],v[48:51]\nM v[52:55],v[216:219],a[20:23],v[52:55]\nM v[112:115],v[216:219],a[24:27],v[112:115]\nbuffer_load_dwordx4 a[112:115],v240,s[52:55],s76 offen offset:2048 sc1\nM v[116:119],v[216:219],a[28:31],v[116:119]\n" \
"ds_read_b64_tr_b16 v[216:217],v241 offset:17920\nds_read_b64_tr_b16 v[218:219],v241 offset:18176\nM v[56:59],v[220:223],a[16:19],v[56:59]\nM v[60:63],v[220:223],a[20:23],v[60:63]\nM v[120:123],v[220:223],a[24:27],v[120:123]\nbuffer_load_dwordx4 v243,s[40:43],s69 offen offset:0 lds\nM v[124:127],v[220:223],a[28:31],v[124:127]\nds_read_b64_tr_b16 v[220:221],v242 offset:17920\n" \
"s_waitcnt lgkmcnt(9)\nM v[0:3],v[192:195],a[32:35],v[0:3]\nds_read_b64_tr_b16 v[222:223],v242 offset:18176\nM v[4:7],v[192:195],a[36:39],v[4:7]\nM v[64:67],v[192:195],a[40:43],v[64:67]\ns_waitcnt vmcnt(21)\nM v[68:71],v[192:195],a[44:47],v[68:71]\nds_read_b64_tr_b16 v[192:193],v241 offset:24576\n" \
"ds_read_b64_tr_b16 v[194:195],v241 offset:24832\nM v[8:11],v[196:199],a[32:35],v[8:11]\nbuffer_load_dwordx4 v244,s[40:43],s69 offen offset:1024 lds\nM v[12:15],v[196:199],a[36:39],v[12:15]\nM v[72:75],v[196:199],a[40:43],v[72:75]\nM v[76:79],v[196:199],a[44:47],v[76:79]\nds_read_b64_tr_b16 v[196:197],v242 offset:24576\nds_read_b64_tr_b16 v[198:199],v242 offset:24832\n" \
"M v[16:19],v[200:203],a[32:35],v[16:19]\nbuffer_load_dwordx4 v245,s[40:43],s69 offen offset:2048 lds\nM v[20:23],v[200:203],a[36:39],v[20:23]\nM v[80:83],v[200:203],a[40:43],v[80:83]\nM v[84:87],v[200:203],a[44:47],v[84:87]\nds_read_b64_tr_b16 v[200:201],v241 offset:25088\ns_waitcnt lgkmcnt(7)\nM v[24:27],v[204:207],a[32:35],v[24:27]\n" \
"buffer_load_dwordx4 v246,s[40:43],s69 offen offset:3072 lds\nM v[28:31],v[204:207],a[36:39],v[28:31]\nds_read_b64_tr_b16 v[202:203],v241 offset:25344\ns_add_u32 m0,s64,98304\nM v[88:91],v[204:207],a[40:43],v[88:91]\ns_add_u32 s69,s68,2097152\nM v[92:95],v[204:207],a[44:47],v[92:95]\nds_read_b64_tr_b16 v[204:205],v242 offset:25088\n" \
"ds_read_b64_tr_b16 v[206:207],v242 offset:25344\nM v[32:35],v[208:211],a[32:35],v[32:35]\nbuffer_store_dwordx4 v[136:139],v240,s[52:55],s74 offen offset:2048 sc1\nM v[36:39],v[208:211],a[36:39],v[36:39]\nM v[96:99],v[208:211],a[40:43],v[96:99]\nM v[100:103],v[208:211],a[44:47],v[100:103]\nds_read_b64_tr_b16 v[208:209],v241 offset:25600\nds_read_b64_tr_b16 v[210:211],v241 offset:25856\n" \
"M v[40:43],v[212:215],a[32:35],v[40:43]\nbuffer_load_dwordx4 a[116:119],v240,s[52:55],s76 offen offset:3072 sc1\nM v[44:47],v[212:215],a[36:39],v[44:47]\ns_add_u32 s76,s76,4096\nM v[104:107],v[212:215],a[40:43],v[104:107]\nM v[108:111],v[212:215],a[44:47],v[108:111]\nds_read_b64_tr_b16 v[212:213],v242 offset:25600\nds_read_b64_tr_b16 v[214:215],v242 offset:25856\n" \
"M v[48:51],v[216:219],a[32:35],v[48:51]\nbuffer_load_dwordx4 a[120:123],v240,s[52:55],s77 offen offset:2048 sc1\nM v[52:55],v[216:219],a[36:39],v[52:55]\nM v[112:115],v[216:219],a[40:43],v[112:115]\nM v[116:119],v[216:219],a[44:47],v[116:119]\nds_read_b64_tr_b16 v[216:217],v241 offset:26112\ns_waitcnt lgkmcnt(7)\nM v[56:59],v[220:223],a[32:35],v[56:59]\n" \
"buffer_load_dwordx4 a[124:127],v240,s[52:55],s77 offen offset:3072 sc1\nM v[60:63],v[220:223],a[36:39],v[60:63]\nds_read_b64_tr_b16 v[218:219],v241 offset:26368\ns_add_u32 s77,s77,4096\nM v[120:123],v[220:223],a[40:43],v[120:123]\nM v[124:127],v[220:223],a[44:47],v[124:127]\nds_read_b64_tr_b16 v[220:221],v242 offset:26112\nds_read_b64_tr_b16 v[222:223],v242 offset:26368\n" \
"M v[0:3],v[192:195],a[48:51],v[0:3]\nbuffer_load_dwordx4 a[128:131],v240,s[52:55],s76 offen offset:0 sc1\nM v[4:7],v[192:195],a[52:55],v[4:7]\nM v[64:67],v[192:195],a[56:59],v[64:67]\ns_waitcnt vmcnt(27)\nM v[68:71],v[192:195],a[60:63],v[68:71]\nM v[8:11],v[196:199],a[48:51],v[8:11]\nbuffer_load_dwordx4 a[132:135],v240,s[52:55],s76 offen offset:1024 sc1\n" \
"M v[12:15],v[196:199],a[52:55],v[12:15]\nM v[72:75],v[196:199],a[56:59],v[72:75]\nM v[76:79],v[196:199],a[60:63],v[76:79]\nM v[16:19],v[200:203],a[48:51],v[16:19]\nbuffer_load_dwordx4 a[136:139],v240,s[52:55],s77 offen offset:0 sc1\nM v[20:23],v[200:203],a[52:55],v[20:23]\nM v[80:83],v[200:203],a[56:59],v[80:83]\nM v[84:87],v[200:203],a[60:63],v[84:87]\n" \
"s_waitcnt lgkmcnt(3)\nM v[24:27],v[204:207],a[48:51],v[24:27]\nbuffer_load_dwordx4 a[140:143],v240,s[52:55],s77 offen offset:1024 sc1\nM v[28:31],v[204:207],a[52:55],v[28:31]\nM v[88:91],v[204:207],a[56:59],v[88:91]\ns_waitcnt vmcnt(22) lgkmcnt(0)\ns_barrier\nM v[92:95],v[204:207],a[60:63],v[92:95]\n" \
"ds_read_b64_tr_b16 v[192:193],v241 offset:32768\nds_read_b64_tr_b16 v[194:195],v241 offset:33024\nM v[32:35],v[208:211],a[48:51],v[32:35]\nbuffer_store_dwordx4 v[140:143],v240,s[52:55],s74 offen offset:3072 sc1\nM v[36:39],v[208:211],a[52:55],v[36:39]\nds_read_b64_tr_b16 v[196:197],v242 offset:32768\nds_read_b64_tr_b16 v[198:199],v242 offset:33024\nM v[96:99],v[208:211],a[56:59],v[96:99]\n" \
"ds_read_b64_tr_b16 v[200:201],v241 offset:33280\nds_read_b64_tr_b16 v[202:203],v241 offset:33536\nM v[100:103],v[208:211],a[60:63],v[100:103]\nds_read_b64_tr_b16 v[204:205],v242 offset:33280\nds_read_b64_tr_b16 v[206:207],v242 offset:33536\nM v[40:43],v[212:215],a[48:51],v[40:43]\nds_read_b64_tr_b16 v[208:209],v241 offset:33792\nds_read_b64_tr_b16 v[210:211],v241 offset:34048\n" \
"M v[44:47],v[212:215],a[52:55],v[44:47]\nbuffer_load_dwordx4 a[144:147],v240,s[52:55],s76 offen offset:2048 sc1\nM v[104:107],v[212:215],a[56:59],v[104:107]\ns_add_u32 s74,s74,4096\nM v[108:111],v[212:215],a[60:63],v[108:111]\nds_read_b64_tr_b16 v[212:213],v242 offset:33792\nds_read_b64_tr_b16 v[214:215],v242 offset:34048\nM v[48:51],v[216:219],a[48:51],v[48:51]\n" \
"M v[52:55],v[216:219],a[52:55],v[52:55]\nbuffer_load_dwordx4 v243,s[40:43],s69 offen offset:0 lds\nM v[112:115],v[216:219],a[56:59],v[112:115]\nM v[116:119],v[216:219],a[60:63],v[116:119]\nds_read_b64_tr_b16 v[216:217],v241 offset:34304\nds_read_b64_tr_b16 v[218:219],v241 offset:34560\nM v[56:59],v[220:223],a[48:51],v[56:59]\nM v[60:63],v[220:223],a[52:55],v[60:63]\n" \
"buffer_load_dwordx4 v244,s[40:43],s69 offen offset:1024 lds\nM v[120:123],v[220:223],a[56:59],v[120:123]\nM v[124:127],v[220:223],a[60:63],v[124:127]\nds_read_b64_tr_b16 v[220:221],v242 offset:34304\ns_waitcnt lgkmcnt(9)\nM v[0:3],v[192:195],a[64:67],v[0:3]\nds_read_b64_tr_b16 v[222:223],v242 offset:34560\nM v[4:7],v[192:195],a[68:71],v[4:7]\n" \
"buffer_load_dwordx4 v245,s[40:43],s69 offen offset:2048 lds\nM v[64:67],v[192:195],a[72:75],v[64:67]\nM v[68:71],v[192:195],a[76:79],v[68:71]\nds_read_b64_tr_b16 v[192:193],v241 offset:40960\nds_read_b64_tr_b16 v[194:195],v241 offset:41216\nM v[8:11],v[196:199],a[64:67],v[8:11]\nM v[12:15],v[196:199],a[68:71],v[12:15]\nbuffer_load_dwordx4 v246,s[40:43],s69 offen offset:3072 lds\n" \
"M v[72:75],v[196:199],a[72:75],v[72:75]\ns_add_u32 m0,s64,114688\ns_add_u32 s69,s68,2621440\nM v[76:79],v[196:199],a[76:79],v[76:79]\nds_read_b64_tr_b16 v[196:197],v242 offset:40960\nds_read_b64_tr_b16 v[198:199],v242 offset:41216\nM v[16:19],v[200:203],a[64:67],v[16:19]\nM v[20:23],v[200:203],a[68:71],v[20:23]\n" \
"buffer_load_dwordx4 a[148:151],v240,s[52:55],s76 offen offset:3072 sc1\nM v[80:83],v[200:203],a[72:75],v[80:83]\ns_add_u32 s76,s76,4096\nM v[84:87],v[200:203],a[76:79],v[84:87]\nds_read_b64_tr_b16 v[200:201],v241 offset:41472\ns_waitcnt lgkmcnt(6)\nM v[24:27],v[204:207],a[64:67],v[24:27]\nds_read_b64_tr_b16 v[202:203],v241 offset:41728\n" \
"M v[28:31],v[204:207],a[68:71],v[28:31]\nbuffer_store_dwordx4 v[144:147],v240,s[52:55],s74 offen offset:0 sc1\nM v[88:91],v[204:207],a[72:75],v[88:91]\nM v[92:95],v[204:207],a[76:79],v[92:95]\nds_read_b64_tr_b16 v[204:205],v242 offset:41472\nds_read_b64_tr_b16 v[206:207],v242 offset:41728\nM v[32:35],v[208:211],a[64:67],v[32:35]\nM v[36:39],v[208:211],a[68:71],v[36:39]\n" \
"buffer_load_dwordx4 a[152:155],v240,s[52:55],s77 offen offset:2048 sc1\nM v[96:99],v[208:211],a[72:75],v[96:99]\nM v[100:103],v[208:211],a[76:79],v[100:103]\nds_read_b64_tr_b16 v[208:209],v241 offset:41984\nds_read_b64_tr_b16 v[210:211],v241 offset:42240\nM v[40:43],v[212:215],a[64:67],v[40:43]\nM v[44:47],v[212:215],a[68:71],v[44:47]\nbuffer_load_dwordx4 a[156:159],v240,s[52:55],s77 offen offset:3072 sc1\n" \
"M v[104:107],v[212:215],a[72:75],v[104:107]\ns_add_u32 s77,s77,4096\nM v[108:111],v[212:215],a[76:79],v[108:111]\nds_read_b64_tr_b16 v[212:213],v242 offset:41984\nds_read_b64_tr_b16 v[214:215],v242 offset:42240\nM v[48:51],v[216:219],a[64:67],v[48:51]\nM v[52:55],v[216:219],a[68:71],v[52:55]\nbuffer_load_dwordx4 a[160:163],v240,s[52:55],s76 offen offset:0 sc1\n" \
"M v[112:115],v[216:219],a[72:75],v[112:115]\nM v[116:119],v[216:219],a[76:79],v[116:119]\nds_read_b64_tr_b16 v[216:217],v241 offset:42496\ns_waitcnt lgkmcnt(7)\nM v[56:59],v[220:223],a[64:67],v[56:59]\nds_read_b64_tr_b16 v[218:219],v241 offset:42752\nM v[60:63],v[220:223],a[68:71],v[60:63]\nbuffer_load_dwordx4 a[164:167],v240,s[52:55],s76 offen offset:1024 sc1\n" \
"M v[120:123],v[220:223],a[72:75],v[120:123]\nM v[124:127],v[220:223],a[76:79],v[124:127]\nds_read_b64_tr_b16 v[220:221],v242 offset:42496\nds_read_b64_tr_b16 v[222:223],v242 offset:42752\nM v[0:3],v[192:195],a[80:83],v[0:3]\ns_waitcnt vmcnt(27)\nM v[4:7],v[192:195],a[84:87],v[4:7]\nbuffer_load_dwordx4 a[168:171],v240,s[52:55],s77 offen offset:0 sc1\n" \
"M v[64:67],v[192:195],a[88:91],v[64:67]\nM v[68:71],v[192:195],a[92:95],v[68:71]\nM v[8:11],v[196:199],a[80:83],v[8:11]\nM v[12:15],v[196:199],a[84:87],v[12:15]\nbuffer_load_dwordx4 a[172:175],v240,s[52:55],s77 offen offset:1024 sc1\nM v[72:75],v[196:199],a[88:91],v[72:75]\nM v[76:79],v[196:199],a[92:95],v[76:79]\nM v[16:19],v[200:203],a[80:83],v[16:19]\n" \
"M v[20:23],v[200:203],a[84:87],v[20:23]\nbuffer_load_dwordx4 a[176:179],v240,s[52:55],s76 offen offset:2048 sc1\nM v[80:83],v[200:203],a[88:91],v[80:83]\nM v[84:87],v[200:203],a[92:95],v[84:87]\ns_waitcnt lgkmcnt(2)\nM v[24:27],v[204:207],a[80:83],v[24:27]\nM v[28:31],v[204:207],a[84:87],v[28:31]\nbuffer_store_dwordx4 v[148:151],v240,s[52:55],s74 offen offset:1024 sc1\n" \
"M v[88:91],v[204:207],a[88:91],v[88:91]\ns_waitcnt vmcnt(24) lgkmcnt(0)\ns_barrier\nM v[92:95],v[204:207],a[92:95],v[92:95]\nds_read_b64_tr_b16 v[192:193],v241 offset:49152\nds_read_b64_tr_b16 v[194:195],v241 offset:49408\nM v[32:35],v[208:211],a[80:83],v[32:35]\nds_read_b64_tr_b16 v[196:197],v242 offset:49152\n" \
"ds_read_b64_tr_b16 v[198:199],v242 offset:49408\nM v[36:39],v[208:211],a[84:87],v[36:39]\nds_read_b64_tr_b16 v[200:201],v241 offset:49664\nds_read_b64_tr_b16 v[202:203],v241 offset:49920\nM v[96:99],v[208:211],a[88:91],v[96:99]\nds_read_b64_tr_b16 v[204:205],v242 offset:49664\nds_read_b64_tr_b16 v[206:207],v242 offset:49920\nM v[100:103],v[208:211],a[92:95],v[100:103]\n" \
"ds_read_b64_tr_b16 v[208:209],v241 offset:50176\nds_read_b64_tr_b16 v[210:211],v241 offset:50432\nM v[40:43],v[212:215],a[80:83],v[40:43]\nbuffer_load_dwordx4 v243,s[40:43],s69 offen offset:0 lds\nM v[44:47],v[212:215],a[84:87],v[44:47]\nM v[104:107],v[212:215],a[88:91],v[104:107]\nM v[108:111],v[212:215],a[92:95],v[108:111]\nds_read_b64_tr_b16 v[212:213],v242 offset:50176\n" \
"ds_read_b64_tr_b16 v[214:215],v242 offset:50432\nM v[48:51],v[216:219],a[80:83],v[48:51]\nbuffer_load_dwordx4 v244,s[40:43],s69 offen offset:1024 lds\nM v[52:55],v[216:219],a[84:87],v[52:55]\nM v[112:115],v[216:219],a[88:91],v[112:115]\nM v[116:119],v[216:219],a[92:95],v[116:119]\nds_read_b64_tr_b16 v[216:217],v241 offset:50688\nds_read_b64_tr_b16 v[218:219],v241 offset:50944\n" \
"M v[56:59],v[220:223],a[80:83],v[56:59]\nbuffer_load_dwordx4 v245,s[40:43],s69 offen offset:2048 lds\nM v[60:63],v[220:223],a[84:87],v[60:63]\nM v[120:123],v[220:223],a[88:91],v[120:123]\nM v[124:127],v[220:223],a[92:95],v[124:127]\nds_read_b64_tr_b16 v[220:221],v242 offset:50688\ns_waitcnt lgkmcnt(8)\nM v[0:3],v[192:195],a[96:99],v[0:3]\n" \
"buffer_load_dwordx4 v246,s[40:43],s69 offen offset:3072 lds\nM v[4:7],v[192:195],a[100:103],v[4:7]\nds_read_b64_tr_b16 v[222:223],v242 offset:50944\ns_add_u32 m0,s64,131072\nM v[64:67],v[192:195],a[104:107],v[64:67]\ns_add_u32 s69,s68,3145728\nM v[68:71],v[192:195],a[108:111],v[68:71]\nds_read_b64_tr_b16 v[192:193],v241 offset:57344\n" \
"ds_read_b64_tr_b16 v[194:195],v241 offset:57600\nM v[8:11],v[196:199],a[96:99],v[8:11]\nbuffer_load_dwordx4 a[180:183],v240,s[52:55],s76 offen offset:3072 sc1\nM v[12:15],v[196:199],a[100:103],v[12:15]\ns_add_u32 s76,s76,4096\nM v[72:75],v[196:199],a[104:107],v[72:75]\nM v[76:79],v[196:199],a[108:111],v[76:79]\nds_read_b64_tr_b16 v[196:197],v242 offset:57344\n" \
"ds_read_b64_tr_b16 v[198:199],v242 offset:57600\nM v[16:19],v[200:203],a[96:99],v[16:19]\nbuffer_store_dwordx4 v[152:155],v240,s[52:55],s74 offen offset:2048 sc1\nM v[20:23],v[200:203],a[100:103],v[20:23]\nM v[80:83],v[200:203],a[104:107],v[80:83]\nM v[84:87],v[200:203],a[108:111],v[84:87]\nds_read_b64_tr_b16 v[200:201],v241 offset:57856\ns_waitcnt lgkmcnt(6)\n" \
"M v[24:27],v[204:207],a[96:99],v[24:27]\nbuffer_load_dwordx4 a[184:187],v240,s[52:55],s77 offen offset:2048 sc1\nM v[28:31],v[204:207],a[100:103],v[28:31]\nds_read_b64_tr_b16 v[202:203],v241 offset:58112\nM v[88:91],v[204:207],a[104:107],v[88:91]\nM v[92:95],v[204:207],a[108:111],v[92:95]\nds_read_b64_tr_b16 v[204:205],v242 offset:57856\nds_read_b64_tr_b16 v[206:207],v242 offset:58112\n" \
"M v[32:35],v[208:211],a[96:99],v[32:35]\nbuffer_load_dwordx4 a[188:191],v240,s[52:55],s77 offen offset:3072 sc1\nM v[36:39],v[208:211],a[100:103],v[36:39]\ns_add_u32 s77,s77,4096\nM v[96:99],v[208:211],a[104:107],v[96:99]\nM v[100:103],v[208:211],a[108:111],v[100:103]\nds_read_b64_tr_b16 v[208:209],v241 offset:58368\nds_read_b64_tr_b16 v[210:211],v241 offset:58624\n" \
"M v[40:43],v[212:215],a[96:99],v[40:43]\nbuffer_load_dwordx4 a[192:195],v240,s[52:55],s76 offen offset:0 sc1\nM v[44:47],v[212:215],a[100:103],v[44:47]\nM v[104:107],v[212:215],a[104:107],v[104:107]\nM v[108:111],v[212:215],a[108:111],v[108:111]\nds_read_b64_tr_b16 v[212:213],v242 offset:58368\nds_read_b64_tr_b16 v[214:215],v242 offset:58624\nM v[48:51],v[216:219],a[96:99],v[48:51]\n" \
"buffer_load_dwordx4 a[196:199],v240,s[52:55],s76 offen offset:1024 sc1\nM v[52:55],v[216:219],a[100:103],v[52:55]\nM v[112:115],v[216:219],a[104:107],v[112:115]\nM v[116:119],v[216:219],a[108:111],v[116:119]\nds_read_b64_tr_b16 v[216:217],v241 offset:58880\ns_waitcnt lgkmcnt(7)\nM v[56:59],v[220:223],a[96:99],v[56:59]\nbuffer_load_dwordx4 a[200:203],v240,s[52:55],s77 offen offset:0 sc1\n" \
"M v[60:63],v[220:223],a[100:103],v[60:63]\nds_read_b64_tr_b16 v[218:219],v241 offset:59136\nM v[120:123],v[220:223],a[104:107],v[120:123]\nM v[124:127],v[220:223],a[108:111],v[124:127]\nds_read_b64_tr_b16 v[220:221],v242 offset:58880\nds_read_b64_tr_b16 v[222:223],v242 offset:59136\nM v[0:3],v[192:195],a[112:115],v[0:3]\ns_waitcnt vmcnt(27)\n" \
"M v[4:7],v[192:195],a[116:119],v[4:7]\nbuffer_load_dwordx4 a[204:207],v240,s[52:55],s77 offen offset:1024 sc1\nM v[64:67],v[192:195],a[120:123],v[64:67]\nM v[68:71],v[192:195],a[124:127],v[68:71]\nM v[8:11],v[196:199],a[112:115],v[8:11]\nM v[12:15],v[196:199],a[116:119],v[12:15]\nbuffer_load_dwordx4 a[208:211],v240,s[52:55],s76 offen offset:2048 sc1\nM v[72:75],v[196:199],a[120:123],v[72:75]\n" \
"M v[76:79],v[196:199],a[124:127],v[76:79]\nM v[16:19],v[200:203],a[112:115],v[16:19]\nM v[20:23],v[200:203],a[116:119],v[20:23]\nbuffer_store_dwordx4 v[156:159],v240,s[52:55],s74 offen offset:3072 sc1\nM v[80:83],v[200:203],a[120:123],v[80:83]\ns_add_u32 s74,s74,4096\nM v[84:87],v[200:203],a[124:127],v[84:87]\ns_waitcnt lgkmcnt(3)\n" \
"M v[24:27],v[204:207],a[112:115],v[24:27]\nM v[28:31],v[204:207],a[116:119],v[28:31]\nbuffer_load_dwordx4 v243,s[40:43],s69 offen offset:0 lds\nM v[88:91],v[204:207],a[120:123],v[88:91]\ns_waitcnt vmcnt(24) lgkmcnt(0)\ns_barrier\nM v[92:95],v[204:207],a[124:127],v[92:95]\nds_read_b64_tr_b16 v[192:193],v241 offset:0\n" \
"ds_read_b64_tr_b16 v[194:195],v241 offset:256\nM v[32:35],v[208:211],a[112:115],v[32:35]\nds_read_b64_tr_b16 v[196:197],v242 offset:0\nds_read_b64_tr_b16 v[198:199],v242 offset:256\nM v[36:39],v[208:211],a[116:119],v[36:39]\nds_read_b64_tr_b16 v[200:201],v241 offset:512\nds_read_b64_tr_b16 v[202:203],v241 offset:768\nM v[96:99],v[208:211],a[120:123],v[96:99]\n" \
"ds_read_b64_tr_b16 v[204:205],v242 offset:512\nds_read_b64_tr_b16 v[206:207],v242 offset:768\nM v[100:103],v[208:211],a[124:127],v[100:103]\nds_read_b64_tr_b16 v[208:209],v241 offset:1024\nds_read_b64_tr_b16 v[210:211],v241 offset:1280\nM v[40:43],v[212:215],a[112:115],v[40:43]\nbuffer_load_dwordx4 v244,s[40:43],s69 offen offset:1024 lds\nM v[44:47],v[212:215],a[116:119],v[44:47]\n" \
"M v[104:107],v[212:215],a[120:123],v[104:107]\nM v[108:111],v[212:215],a[124:127],v[108:111]\nds_read_b64_tr_b16 v[212:213],v242 offset:1024\nds_read_b64_tr_b16 v[214:215],v242 offset:1280\nM v[48:51],v[216:219],a[112:115],v[48:51]\nbuffer_load_dwordx4 v245,s[40:43],s69 offen offset:2048 lds\nM v[52:55],v[216:219],a[116:119],v[52:55]\nM v[112:115],v[216:219],a[120:123],v[112:115]\n" \
"M v[116:119],v[216:219],a[124:127],v[116:119]\nds_read_b64_tr_b16 v[216:217],v241 offset:1536\nds_read_b64_tr_b16 v[218:219],v241 offset:1792\nM v[56:59],v[220:223],a[112:115],v[56:59]\nbuffer_load_dwordx4 v246,s[40:43],s69 offen offset:3072 lds\nM v[60:63],v[220:223],a[116:119],v[60:63]\ns_add_u32 m0,s64,147456\ns_add_u32 s69,s68,3670016\n" \
"M v[120:123],v[220:223],a[120:123],v[120:123]\ns_lshl_b32 s68,s60,19\ns_lshl2_add_u32 s68,s64,s68\nM v[124:127],v[220:223],a[124:127],v[124:127]\nds_read_b64_tr_b16 v[220:221],v242 offset:1536\ns_waitcnt lgkmcnt(6)\nM v[0:3],v[192:195],a[128:131],v[0:3]\nds_read_b64_tr_b16 v[222:223],v242 offset:1792\n" \
"s_add_u32 s68,s68,s70\nM v[4:7],v[192:195],a[132:135],v[4:7]\nbuffer_load_dwordx4 a[212:215],v240,s[52:55],s76 offen offset:3072 sc1\nM v[64:67],v[192:195],a[136:139],v[64:67]\ns_add_u32 s76,s76,4096\nM v[68:71],v[192:195],a[140:143],v[68:71]\nds_read_b64_tr_b16 v[192:193],v241 offset:8192\nds_read_b64_tr_b16 v[194:195],v241 offset:8448\n" \
"M v[8:11],v[196:199],a[128:131],v[8:11]\nM v[12:15],v[196:199],a[132:135],v[12:15]\nbuffer_store_dwordx4 v[160:163],v240,s[52:55],s74 offen offset:0 sc1\nM v[72:75],v[196:199],a[136:139],v[72:75]\nM v[76:79],v[196:199],a[140:143],v[76:79]\nds_read_b64_tr_b16 v[196:197],v242 offset:8192\nds_read_b64_tr_b16 v[198:199],v242 offset:8448\nM v[16:19],v[200:203],a[128:131],v[16:19]\n" \
"M v[20:23],v[200:203],a[132:135],v[20:23]\nbuffer_load_dwordx4 a[216:219],v240,s[52:55],s77 offen offset:2048 sc1\nM v[80:83],v[200:203],a[136:139],v[80:83]\nM v[84:87],v[200:203],a[140:143],v[84:87]\nds_read_b64_tr_b16 v[200:201],v241 offset:8704\nds_read_b64_tr_b16 v[202:203],v241 offset:8960\nM v[24:27],v[204:207],a[128:131],v[24:27]\nM v[28:31],v[204:207],a[132:135],v[28:31]\n" \
"buffer_load_dwordx4 a[220:223],v240,s[52:55],s77 offen offset:3072 sc1\nM v[88:91],v[204:207],a[136:139],v[88:91]\ns_add_u32 s77,s77,4096\nM v[92:95],v[204:207],a[140:143],v[92:95]\nds_read_b64_tr_b16 v[204:205],v242 offset:8704\ns_waitcnt lgkmcnt(7)\nM v[32:35],v[208:211],a[128:131],v[32:35]\nds_read_b64_tr_b16 v[206:207],v242 offset:8960\n" \
"M v[36:39],v[208:211],a[132:135],v[36:39]\nbuffer_load_dwordx4 a[224:227],v240,s[52:55],s76 offen offset:0 sc1\nM v[96:99],v[208:211],a[136:139],v[96:99]\nM v[100:103],v[208:211],a[140:143],v[100:103]\nds_read_b64_tr_b16 v[208:209],v241 offset:9216\nds_read_b64_tr_b16 v[210:211],v241 offset:9472\nM v[40:43],v[212:215],a[128:131],v[40:43]\nds_read_b128 a[0:3],v240 offset:0\n" \
"M v[44:47],v[212:215],a[132:135],v[44:47]\nbuffer_load_dword v248,v228,s[52:55],s80 offen sc1\nM v[104:107],v[212:215],a[136:139],v[104:107]\nbuffer_load_dwordx4 a[228:231],v240,s[52:55],s76 offen offset:1024 sc1\nM v[108:111],v[212:215],a[140:143],v[108:111]\nds_read_b64_tr_b16 v[212:213],v242 offset:9216\nds_read_b64_tr_b16 v[214:215],v242 offset:9472\nM v[48:51],v[216:219],a[128:131],v[48:51]\n" \
"M v[52:55],v[216:219],a[132:135],v[52:55]\nM v[112:115],v[216:219],a[136:139],v[112:115]\nbuffer_load_dwordx4 a[232:235],v240,s[52:55],s77 offen offset:0 sc1\nM v[116:119],v[216:219],a[140:143],v[116:119]\nds_read_b64_tr_b16 v[216:217],v241 offset:9728\nds_read_b64_tr_b16 v[218:219],v241 offset:9984\nM v[56:59],v[220:223],a[128:131],v[56:59]\nds_read_b128 a[4:7],v240 offset:1024\n" \
"M v[60:63],v[220:223],a[132:135],v[60:63]\nM v[120:123],v[220:223],a[136:139],v[120:123]\nbuffer_load_dwordx4 a[236:239],v240,s[52:55],s77 offen offset:1024 sc1\nM v[124:127],v[220:223],a[140:143],v[124:127]\nds_read_b64_tr_b16 v[220:221],v242 offset:9728\ns_waitcnt lgkmcnt(8)\nM v[0:3],v[192:195],a[144:147],v[0:3]\nds_read_b64_tr_b16 v[222:223],v242 offset:9984\n" \
"ds_read_b128 a[8:11],v240 offset:2048\nM v[4:7],v[192:195],a[148:151],v[4:7]\ns_waitcnt vmcnt(28)\nM v[64:67],v[192:195],a[152:155],v[64:67]\nbuffer_store_dwordx4 v[164:167],v240,s[52:55],s74 offen offset:1024 sc1\nM v[68:71],v[192:195],a[156:159],v[68:71]\nM v[8:11],v[196:199],a[144:147],v[8:11]\nM v[12:15],v[196:199],a[148:151],v[12:15]\n" \
"M v[72:75],v[196:199],a[152:155],v[72:75]\nbuffer_load_dwordx4 a[240:243],v240,s[52:55],s76 offen offset:2048 sc1\nM v[76:79],v[196:199],a[156:159],v[76:79]\nds_read_b128 a[12:15],v240 offset:3072\nM v[16:19],v[200:203],a[144:147],v[16:19]\nM v[20:23],v[200:203],a[148:151],v[20:23]\nM v[80:83],v[200:203],a[152:155],v[80:83]\nbuffer_load_dwordx4 v243,s[40:43],s69 offen offset:0 lds\n" \
"M v[84:87],v[200:203],a[156:159],v[84:87]\nM v[24:27],v[204:207],a[144:147],v[24:27]\nds_read_b128 a[16:19],v240 offset:4096\nM v[28:31],v[204:207],a[148:151],v[28:31]\nM v[88:91],v[204:207],a[152:155],v[88:91]\ns_waitcnt vmcnt(25) lgkmcnt(3)\ns_barrier\nM v[92:95],v[204:207],a[156:159],v[92:95]\n" \
"ds_read_b64_tr_b16 v[192:193],v241 offset:16384\nds_read_b64_tr_b16 v[194:195],v241 offset:16640\nM v[32:35],v[208:211],a[144:147],v[32:35]\nds_read_b64_tr_b16 v[196:197],v242 offset:16384\nds_read_b64_tr_b16 v[198:199],v242 offset:16640\nM v[36:39],v[208:211],a[148:151],v[36:39]\nds_read_b64_tr_b16 v[200:201],v241 offset:16896\nds_read_b64_tr_b16 v[202:203],v241 offset:17152\n" \
"M v[96:99],v[208:211],a[152:155],v[96:99]\nds_read_b64_tr_b16 v[204:205],v242 offset:16896\nds_read_b64_tr_b16 v[206:207],v242 offset:17152\nM v[100:103],v[208:211],a[156:159],v[100:103]\nds_read_b64_tr_b16 v[208:209],v241 offset:17408\nds_read_b64_tr_b16 v[210:211],v241 offset:17664\nM v[40:43],v[212:215],a[144:147],v[40:43]\nbuffer_load_dwordx4 v244,s[40:43],s69 offen offset:1024 lds\n" \
"M v[44:47],v[212:215],a[148:151],v[44:47]\nds_read_b128 a[20:23],v240 offset:5120\nM v[104:107],v[212:215],a[152:155],v[104:107]\nM v[108:111],v[212:215],a[156:159],v[108:111]\nds_read_b64_tr_b16 v[212:213],v242 offset:17408\nds_read_b64_tr_b16 v[214:215],v242 offset:17664\nM v[48:51],v[216:219],a[144:147],v[48:51]\nbuffer_load_dwordx4 v245,s[40:43],s69 offen offset:2048 lds\n" \
"M v[52:55],v[216:219],a[148:151],v[52:55]\nds_read_b128 a[24:27],v240 offset:6144\nM v[112:115],v[216:219],a[152:155],v[112:115]\nM v[116:119],v[216:219],a[156:159],v[116:119]\nds_read_b64_tr_b16 v[216:217],v241 offset:17920\nds_read_b64_tr_b16 v[218:219],v241 offset:18176\nM v[56:59],v[220:223],a[144:147],v[56:59]\nbuffer_load_dwordx4 v246,s[40:43],s69 offen offset:3072 lds\n" \
"M v[60:63],v[220:223],a[148:151],v[60:63]\ns_sub_u32 s40,s18,0x1000\ns_subb_u32 s41,s19,0\nM v[120:123],v[220:223],a[152:155],v[120:123]\nds_read_b128 a[28:31],v240 offset:7168\nM v[124:127],v[220:223],a[156:159],v[124:127]\nds_read_b64_tr_b16 v[220:221],v242 offset:17920\ns_waitcnt lgkmcnt(8)\n" \
"M v[0:3],v[192:195],a[160:163],v[0:3]\nbuffer_store_dwordx4 v[168:171],v240,s[52:55],s74 offen offset:2048 sc1\nM v[4:7],v[192:195],a[164:167],v[4:7]\nds_read_b64_tr_b16 v[222:223],v242 offset:18176\nM v[64:67],v[192:195],a[168:171],v[64:67]\nM v[68:71],v[192:195],a[172:175],v[68:71]\nds_read_b64_tr_b16 v[192:193],v241 offset:24576\nds_read_b64_tr_b16 v[194:195],v241 offset:24832\n" \
"M v[8:11],v[196:199],a[160:163],v[8:11]\nbuffer_load_dwordx4 a[244:247],v240,s[52:55],s76 offen offset:3072 sc1\nM v[12:15],v[196:199],a[164:167],v[12:15]\nds_read_b128 a[32:35],v240 offset:8192\nM v[72:75],v[196:199],a[168:171],v[72:75]\nM v[76:79],v[196:199],a[172:175],v[76:79]\nds_read_b64_tr_b16 v[196:197],v242 offset:24576\nds_read_b64_tr_b16 v[198:199],v242 offset:24832\n" \
"M v[16:19],v[200:203],a[160:163],v[16:19]\nbuffer_load_dwordx4 a[248:251],v240,s[52:55],s77 offen offset:2048 sc1\nM v[20:23],v[200:203],a[164:167],v[20:23]\nM v[80:83],v[200:203],a[168:171],v[80:83]\nds_read_b128 a[36:39],v240 offset:9216\nM v[84:87],v[200:203],a[172:175],v[84:87]\nds_read_b64_tr_b16 v[200:201],v241 offset:25088\nds_read_b64_tr_b16 v[202:203],v241 offset:25344\n" \
"M v[24:27],v[204:207],a[160:163],v[24:27]\nbuffer_load_dwordx4 a[252:255],v240,s[52:55],s77 offen offset:3072 sc1\nM v[28:31],v[204:207],a[164:167],v[28:31]\nM v[88:91],v[204:207],a[168:171],v[88:91]\nM v[92:95],v[204:207],a[172:175],v[92:95]\nds_read_b64_tr_b16 v[204:205],v242 offset:25088\nds_read_b64_tr_b16 v[206:207],v242 offset:25344\nM v[32:35],v[208:211],a[160:163],v[32:35]\n" \
"ds_read_b128 a[40:43],v240 offset:10240\nM v[36:39],v[208:211],a[164:167],v[36:39]\ns_waitcnt vmcnt(13)\nv_cmp_eq_u32 vcc,0,v248\nM v[96:99],v[208:211],a[168:171],v[96:99]\ns_cbranch_vccnz .Lps1\n.Lpb1:\ns_add_u32 s80,s80,4096\n" \
"M v[100:103],v[208:211],a[172:175],v[100:103]\nds_read_b64_tr_b16 v[208:209],v241 offset:25600\ns_waitcnt lgkmcnt(8)\nM v[40:43],v[212:215],a[160:163],v[40:43]\ns_cmp_ge_u32 s62,6\ns_cselect_b64 vcc,-1,0\nM v[44:47],v[212:215],a[164:167],v[44:47]\nds_read_b64_tr_b16 v[210:211],v241 offset:25856\n" \
"v_cndmask_b32 v254,v231,v229,vcc\nM v[104:107],v[212:215],a[168:171],v[104:107]\ns_cmp_eq_u32 s62,3\ns_cselect_b64 vcc,-1,0\nM v[108:111],v[212:215],a[172:175],v[108:111]\nds_read_b64_tr_b16 v[212:213],v242 offset:25600\nds_read_b64_tr_b16 v[214:215],v242 offset:25856\nM v[48:51],v[216:219],a[160:163],v[48:51]\n" \
"v_cndmask_b32 v254,v254,v230,vcc\nv_bfe_u32 v230,v249,2,3\nM v[52:55],v[216:219],a[164:167],v[52:55]\nv_and_b32 v231,0x70,v243\nv_lshl_or_b32 v230,v230,10,v231\nM v[112:115],v[216:219],a[168:171],v[112:115]\nv_and_b32 v231,16,v249\nv_lshlrev_b32 v231,1,v231\n" \
"M v[116:119],v[216:219],a[172:175],v[116:119]\nds_read_b64_tr_b16 v[216:217],v241 offset:26112\nds_read_b64_tr_b16 v[218:219],v241 offset:26368\nM v[56:59],v[220:223],a[160:163],v[56:59]\nv_xor_b32 v230,v230,v231\nv_add_u32 v243,4096,v230\nM v[60:63],v[220:223],a[164:167],v[60:63]\nv_add_u32 v244,3200,v230\n" \
"v_add_u32 v245,10240,v230\nM v[120:123],v[220:223],a[168:171],v[120:123]\nbuffer_store_dwordx4 v[172:175],v240,s[52:55],s74 offen offset:3072 sc1\nM v[124:127],v[220:223],a[172:175],v[124:127]\nds_read_b64_tr_b16 v[220:221],v242 offset:26112\nds_read_b64_tr_b16 v[222:223],v242 offset:26368\nM v[0:3],v[192:195],a[176:179],v[0:3]\ns_add_u32 s74,s74,4096\n" \
"v_add_u32 v246,9344,v230\nM v[4:7],v[192:195],a[180:183],v[4:7]\nv_and_b32 v229,3,v249\nv_cmp_eq_u32 vcc,0,v229\nM v[64:67],v[192:195],a[184:187],v[64:67]\nv_mov_b32 v230,0x3f803f80\nv_cndmask_b32 v229,0,v230,vcc\nM v[68:71],v[192:195],a[188:191],v[68:71]\n" \
"v_and_b32 v230,0xff,v241\ns_waitcnt lgkmcnt(4)\nM v[8:11],v[196:199],a[176:179],v[8:11]\nv_and_b32 v231,16,v249\nv_lshl_or_b32 v230,v231,4,v230\nM v[12:15],v[196:199],a[180:183],v[12:15]\nv_and_b32 v231,32,v249\nds_read_b128 a[44:47],v240 offset:11264\n" \
"M v[72:75],v[196:199],a[184:187],v[72:75]\nds_read_b128 a[48:51],v240 offset:12288\nds_read_b128 a[52:55],v240 offset:13312\nM v[76:79],v[196:199],a[188:191],v[76:79]\nM v[16:19],v[200:203],a[176:179],v[16:19]\nds_read_b128 a[56:59],v240 offset:14336\nM v[20:23],v[200:203],a[180:183],v[20:23]\nM v[80:83],v[200:203],a[184:187],v[80:83]\n" \
"M v[84:87],v[200:203],a[188:191],v[84:87]\nM v[24:27],v[204:207],a[176:179],v[24:27]\nM v[28:31],v[204:207],a[180:183],v[28:31]\nM v[88:91],v[204:207],a[184:187],v[88:91]\ns_waitcnt lgkmcnt(4)\ns_barrier\nM v[92:95],v[204:207],a[188:191],v[92:95]\nds_read_b64_tr_b16 v[192:193],v241 offset:32768\n" \
"ds_read_b64_tr_b16 v[194:195],v241 offset:33024\nM v[32:35],v[208:211],a[176:179],v[32:35]\nds_read_b64_tr_b16 v[196:197],v242 offset:32768\nds_read_b64_tr_b16 v[198:199],v242 offset:33024\nM v[36:39],v[208:211],a[180:183],v[36:39]\nds_read_b64_tr_b16 v[200:201],v241 offset:33280\nds_read_b64_tr_b16 v[202:203],v241 offset:33536\nM v[96:99],v[208:211],a[184:187],v[96:99]\n" \
"ds_read_b64_tr_b16 v[204:205],v242 offset:33280\nds_read_b64_tr_b16 v[206:207],v242 offset:33536\nM v[100:103],v[208:211],a[188:191],v[100:103]\nds_read_b64_tr_b16 v[208:209],v241 offset:33792\nds_read_b64_tr_b16 v[210:211],v241 offset:34048\nM v[40:43],v[212:215],a[176:179],v[40:43]\nds_write_b64 v250,v[128:129] offset:0\nds_read_b128 a[60:63],v240 offset:15360\n" \
"M v[44:47],v[212:215],a[180:183],v[44:47]\nds_write_b64 v250,v[130:131] offset:8\nM v[104:107],v[212:215],a[184:187],v[104:107]\nds_write_b64 v250,v[132:133] offset:1024\nM v[108:111],v[212:215],a[188:191],v[108:111]\nds_read_b64_tr_b16 v[212:213],v242 offset:33792\nds_read_b64_tr_b16 v[214:215],v242 offset:34048\nM v[48:51],v[216:219],a[176:179],v[48:51]\n" \
"M v[52:55],v[216:219],a[180:183],v[52:55]\nds_write_b64 v250,v[134:135] offset:1032\nM v[112:115],v[216:219],a[184:187],v[112:115]\nM v[116:119],v[216:219],a[188:191],v[116:119]\nds_read_b64_tr_b16 v[216:217],v241 offset:34304\nds_read_b64_tr_b16 v[218:219],v241 offset:34560\nM v[56:59],v[220:223],a[176:179],v[56:59]\nbuffer_store_dwordx4 v[176:179],v240,s[52:55],s74 offen offset:0 sc1\n" \
"M v[60:63],v[220:223],a[180:183],v[60:63]\nds_write_b64 v250,v[136:137] offset:2048\nM v[120:123],v[220:223],a[184:187],v[120:123]\nM v[124:127],v[220:223],a[188:191],v[124:127]\nds_read_b64_tr_b16 v[220:221],v242 offset:34304\ns_waitcnt lgkmcnt(13)\nM v[0:3],v[192:195],a[192:195],v[0:3]\nds_write_b64 v250,v[138:139] offset:2056\n" \
"ds_read_b64_tr_b16 v[222:223],v242 offset:34560\nM v[4:7],v[192:195],a[196:199],v[4:7]\nM v[64:67],v[192:195],a[200:203],v[64:67]\nds_write_b64 v250,v[140:141] offset:3072\nM v[68:71],v[192:195],a[204:207],v[68:71]\nds_read_b64_tr_b16 v[192:193],v241 offset:40960\nds_read_b64_tr_b16 v[194:195],v241 offset:41216\nM v[8:11],v[196:199],a[192:195],v[8:11]\n" \
"M v[12:15],v[196:199],a[196:199],v[12:15]\nds_write_b64 v250,v[142:143] offset:3080\nM v[72:75],v[196:199],a[200:203],v[72:75]\nM v[76:79],v[196:199],a[204:207],v[76:79]\nds_read_b64_tr_b16 v[196:197],v242 offset:40960\nds_read_b64_tr_b16 v[198:199],v242 offset:41216\nM v[16:19],v[200:203],a[192:195],v[16:19]\nds_write_b64 v250,v[144:145] offset:4096\n" \
"M v[20:23],v[200:203],a[196:199],v[20:23]\nM v[80:83],v[200:203],a[200:203],v[80:83]\nM v[84:87],v[200:203],a[204:207],v[84:87]\nds_read_b64_tr_b16 v[200:201],v241 offset:41472\nds_read_b64_tr_b16 v[202:203],v241 offset:41728\nM v[24:27],v[204:207],a[192:195],v[24:27]\nds_write_b64 v250,v[146:147] offset:4104\nM v[28:31],v[204:207],a[196:199],v[28:31]\n" \
"M v[88:91],v[204:207],a[200:203],v[88:91]\nds_write_b64 v250,v[148:149] offset:5120\nM v[92:95],v[204:207],a[204:207],v[92:95]\nds_read_b64_tr_b16 v[204:205],v242 offset:41472\ns_waitcnt lgkmcnt(12)\nM v[32:35],v[208:211],a[192:195],v[32:35]\nds_read_b64_tr_b16 v[206:207],v242 offset:41728\nM v[36:39],v[208:211],a[196:199],v[36:39]\n" \
"ds_write_b64 v250,v[150:151] offset:5128\nM v[96:99],v[208:211],a[200:203],v[96:99]\nM v[100:103],v[208:211],a[204:207],v[100:103]\nds_read_b64_tr_b16 v[208:209],v241 offset:41984\nds_read_b64_tr_b16 v[210:211],v241 offset:42240\nM v[40:43],v[212:215],a[192:195],v[40:43]\nds_write_b64 v250,v[152:153] offset:6144\nM v[44:47],v[212:215],a[196:199],v[44:47]\n" \
"M v[104:107],v[212:215],a[200:203],v[104:107]\nM v[108:111],v[212:215],a[204:207],v[108:111]\nds_read_b64_tr_b16 v[212:213],v242 offset:41984\nds_read_b64_tr_b16 v[214:215],v242 offset:42240\nM v[48:51],v[216:219],a[192:195],v[48:51]\nds_write_b64 v250,v[154:155] offset:6152\nM v[52:55],v[216:219],a[196:199],v[52:55]\nM v[112:115],v[216:219],a[200:203],v[112:115]\n" \
"buffer_store_dwordx4 v[180:183],v240,s[52:55],s74 offen offset:1024 sc1\nM v[116:119],v[216:219],a[204:207],v[116:119]\nds_read_b64_tr_b16 v[216:217],v241 offset:42496\nds_read_b64_tr_b16 v[218:219],v241 offset:42752\nM v[56:59],v[220:223],a[192:195],v[56:59]\nds_write_b64 v250,v[156:157] offset:7168\nM v[60:63],v[220:223],a[196:199],v[60:63]\nds_write_b64 v250,v[158:159] offset:7176\n" \
"M v[120:123],v[220:223],a[200:203],v[120:123]\nM v[124:127],v[220:223],a[204:207],v[124:127]\nds_read_b64_tr_b16 v[220:221],v242 offset:42496\ns_waitcnt lgkmcnt(11)\nM v[0:3],v[192:195],a[208:211],v[0:3]\nds_write_b64 v250,v[160:161] offset:8192\nds_read_b64_tr_b16 v[222:223],v242 offset:42752\nM v[4:7],v[192:195],a[212:215],v[4:7]\n" \
"M v[64:67],v[192:195],a[216:219],v[64:67]\nM v[68:71],v[192:195],a[220:223],v[68:71]\nds_write_b64 v250,v[162:163] offset:8200\nM v[8:11],v[196:199],a[208:211],v[8:11]\nM v[12:15],v[196:199],a[212:215],v[12:15]\nM v[72:75],v[196:199],a[216:219],v[72:75]\nds_write_b64 v250,v[164:165] offset:9216\nM v[76:79],v[196:199],a[220:223],v[76:79]\n" \
"M v[16:19],v[200:203],a[208:211],v[16:19]\nM v[20:23],v[200:203],a[212:215],v[20:23]\nds_write_b64 v250,v[166:167] offset:9224\nM v[80:83],v[200:203],a[216:219],v[80:83]\nM v[84:87],v[200:203],a[220:223],v[84:87]\nM v[24:27],v[204:207],a[208:211],v[24:27]\nds_write_b64 v250,v[168:169] offset:10240\nM v[28:31],v[204:207],a[212:215],v[28:31]\n" \
"M v[88:91],v[204:207],a[216:219],v[88:91]\ns_waitcnt vmcnt(7) lgkmcnt(4)\ns_barrier\nM v[92:95],v[204:207],a[220:223],v[92:95]\nds_read_b64_tr_b16 v[192:193],v241 offset:49152\nds_read_b64_tr_b16 v[194:195],v241 offset:49408\nM v[32:35],v[208:211],a[208:211],v[32:35]\nds_read_b64_tr_b16 v[196:197],v242 offset:49152\n" \
"ds_read_b64_tr_b16 v[198:199],v242 offset:49408\nM v[36:39],v[208:211],a[212:215],v[36:39]\nds_read_b64_tr_b16 v[200:201],v241 offset:49664\nds_read_b64_tr_b16 v[202:203],v241 offset:49920\nM v[96:99],v[208:211],a[216:219],v[96:99]\nds_read_b64_tr_b16 v[204:205],v242 offset:49664\nds_read_b64_tr_b16 v[206:207],v242 offset:49920\nM v[100:103],v[208:211],a[220:223],v[100:103]\n" \
"ds_read_b64_tr_b16 v[208:209],v241 offset:50176\nds_read_b64_tr_b16 v[210:211],v241 offset:50432\nM v[40:43],v[212:215],a[208:211],v[40:43]\nds_write_b64 v250,v[170:171] offset:10248\nM v[44:47],v[212:215],a[212:215],v[44:47]\nds_write_b64 v250,v[172:173] offset:11264\nM v[104:107],v[212:215],a[216:219],v[104:107]\nds_write_b64 v250,v[174:175] offset:11272\n" \
"M v[108:111],v[212:215],a[220:223],v[108:111]\nds_read_b64_tr_b16 v[212:213],v242 offset:50176\nds_read_b64_tr_b16 v[214:215],v242 offset:50432\nM v[48:51],v[216:219],a[208:211],v[48:51]\nbuffer_store_dwordx4 v[184:187],v240,s[52:55],s74 offen offset:2048 sc1\nM v[52:55],v[216:219],a[212:215],v[52:55]\nds_write_b64 v250,v[176:177] offset:12288\nM v[112:115],v[216:219],a[216:219],v[112:115]\n" \
"M v[116:119],v[216:219],a[220:223],v[116:119]\nds_read_b64_tr_b16 v[216:217],v241 offset:50688\nds_read_b64_tr_b16 v[218:219],v241 offset:50944\nM v[56:59],v[220:223],a[208:211],v[56:59]\nds_write_b64 v250,v[178:179] offset:12296\nv_accvgpr_write_b32 a208,v229\nM v[60:63],v[220:223],a[212:215],v[60:63]\nv_accvgpr_write_b32 a209,v229\n" \
"v_accvgpr_write_b32 a210,v229\nM v[120:123],v[220:223],a[216:219],v[120:123]\nds_write_b64 v250,v[180:181] offset:13312\nv_accvgpr_write_b32 a211,v229\nM v[124:127],v[220:223],a[220:223],v[124:127]\nds_read_b64_tr_b16 v[220:221],v242 offset:50688\ns_waitcnt lgkmcnt(11)\nM v[0:3],v[192:195],a[224:227],v[0:3]\n" \
"ds_read_b64_tr_b16 v[222:223],v242 offset:50944\nM v[4:7],v[192:195],a[228:231],v[4:7]\nds_write_b64 v250,v[182:183] offset:13320\nM v[64:67],v[192:195],a[232:235],v[64:67]\nM v[68:71],v[192:195],a[236:239],v[68:71]\nds_read_b64_tr_b16 v[192:193],v241 offset:57344\nds_read_b64_tr_b16 v[194:195],v241 offset:57600\nM v[8:11],v[196:199],a[224:227],v[8:11]\n" \
"ds_write_b64 v250,v[184:185] offset:14336\nM v[12:15],v[196:199],a[228:231],v[12:15]\nM v[72:75],v[196:199],a[232:235],v[72:75]\nM v[76:79],v[196:199],a[236:239],v[76:79]\nds_read_b64_tr_b16 v[196:197],v242 offset:57344\nds_read_b64_tr_b16 v[198:199],v242 offset:57600\nM v[16:19],v[200:203],a[224:227],v[16:19]\nds_write_b64 v250,v[186:187] offset:14344\n" \
"M v[20:23],v[200:203],a[228:231],v[20:23]\nM v[80:83],v[200:203],a[232:235],v[80:83]\nds_write_b64 v250,v[188:189] offset:15360\nM v[84:87],v[200:203],a[236:239],v[84:87]\nds_read_b64_tr_b16 v[200:201],v241 offset:57856\nds_read_b64_tr_b16 v[202:203],v241 offset:58112\nM v[24:27],v[204:207],a[224:227],v[24:27]\nM v[28:31],v[204:207],a[228:231],v[28:31]\n" \
"ds_write_b64 v250,v[190:191] offset:15368\nv_add_u32 v250,114688,v240\nM v[88:91],v[204:207],a[232:235],v[88:91]\nM v[92:95],v[204:207],a[236:239],v[92:95]\nds_read_b64_tr_b16 v[204:205],v242 offset:57856\nds_read_b64_tr_b16 v[206:207],v242 offset:58112\nM v[32:35],v[208:211],a[224:227],v[32:35]\nM v[36:39],v[208:211],a[228:231],v[36:39]\n" \
"M v[96:99],v[208:211],a[232:235],v[96:99]\nM v[100:103],v[208:211],a[236:239],v[100:103]\nds_read_b64_tr_b16 v[208:209],v241 offset:58368\ns_waitcnt lgkmcnt(10)\nM v[40:43],v[212:215],a[224:227],v[40:43]\nds_read_b64_tr_b16 v[210:211],v241 offset:58624\nM v[44:47],v[212:215],a[228:231],v[44:47]\nM v[104:107],v[212:215],a[232:235],v[104:107]\n" \
"buffer_store_dwordx4 v[188:191],v240,s[52:55],s74 offen offset:3072 sc1\nM v[108:111],v[212:215],a[236:239],v[108:111]\nds_read_b64_tr_b16 v[212:213],v242 offset:58368\nds_read_b64_tr_b16 v[214:215],v242 offset:58624\nM v[48:51],v[216:219],a[224:227],v[48:51]\nM v[52:55],v[216:219],a[228:231],v[52:55]\nM v[112:115],v[216:219],a[232:235],v[112:115]\nM v[116:119],v[216:219],a[236:239],v[116:119]\n" \
"ds_read_b64_tr_b16 v[216:217],v241 offset:58880\nds_read_b64_tr_b16 v[218:219],v241 offset:59136\nM v[56:59],v[220:223],a[224:227],v[56:59]\nv_add_u32 v241,65536,v240\nM v[60:63],v[220:223],a[228:231],v[60:63]\nM v[120:123],v[220:223],a[232:235],v[120:123]\nM v[124:127],v[220:223],a[236:239],v[124:127]\nds_read_b64_tr_b16 v[220:221],v242 offset:58880\n" \
"ds_read_b64_tr_b16 v[222:223],v242 offset:59136\nM v[0:3],v[192:195],a[240:243],v[0:3]\nv_lshl_or_b32 v242,v231,6,v230\ns_waitcnt lgkmcnt(8)\nM v[8:11],v[196:199],a[240:243],v[8:11]\nM v[16:19],v[200:203],a[240:243],v[16:19]\nM v[24:27],v[204:207],a[240:243],v[24:27]\ns_waitcnt lgkmcnt(6)\n" \
"M v[32:35],v[208:211],a[240:243],v[32:35]\ns_waitcnt lgkmcnt(4)\nM v[40:43],v[212:215],a[240:243],v[40:43]\nv_cvt_pk_bf16_f32 v0,v0,v1\ns_waitcnt lgkmcnt(2)\nM v[48:51],v[216:219],a[240:243],v[48:51]\nv_cvt_pk_bf16_f32 v16,v16,v17\ns_waitcnt lgkmcnt(0)\n" \
"M v[56:59],v[220:223],a[240:243],v[56:59]\nv_cvt_pk_bf16_f32 v32,v32,v33\ns_waitcnt vmcnt(5)\nM v[4:7],v[192:195],a[244:247],v[4:7]\nv_cvt_pk_bf16_f32 v1,v2,v3\nv_cvt_pk_bf16_f32 v17,v18,v19\nM v[12:15],v[196:199],a[244:247],v[12:15]\nv_cvt_pk_bf16_f32 v48,v48,v49\n" \
"v_cvt_pk_bf16_f32 v33,v34,v35\nM v[20:23],v[200:203],a[244:247],v[20:23]\nv_cvt_pk_bf16_f32 v49,v50,v51\nv_cvt_pk_bf16_f32 v2,v8,v9\nM v[28:31],v[204:207],a[244:247],v[28:31]\nv_cvt_pk_bf16_f32 v4,v4,v5\nv_cvt_pk_bf16_f32 v5,v6,v7\nM v[36:39],v[208:211],a[244:247],v[36:39]\n" \
"v_cvt_pk_bf16_f32 v3,v10,v11\nv_cvt_pk_bf16_f32 v18,v24,v25\nM v[44:47],v[212:215],a[244:247],v[44:47]\nv_cvt_pk_bf16_f32 v20,v20,v21\nv_cvt_pk_bf16_f32 v21,v22,v23\nM v[52:55],v[216:219],a[244:247],v[52:55]\nv_cvt_pk_bf16_f32 v19,v26,v27\nv_cvt_pk_bf16_f32 v34,v40,v41\n" \
"M v[60:63],v[220:223],a[244:247],v[60:63]\nv_cvt_pk_bf16_f32 v36,v36,v37\nv_cvt_pk_bf16_f32 v37,v38,v39\nM v[64:67],v[192:195],a[248:251],v[64:67]\nv_cvt_pk_bf16_f32 v35,v42,v43\nv_cvt_pk_bf16_f32 v50,v56,v57\nM v[72:75],v[196:199],a[248:251],v[72:75]\nv_cvt_pk_bf16_f32 v52,v52,v53\n" \
"v_cvt_pk_bf16_f32 v53,v54,v55\nM v[80:83],v[200:203],a[248:251],v[80:83]\nv_cvt_pk_bf16_f32 v51,v58,v59\nv_cvt_pk_bf16_f32 v6,v12,v13\nM v[88:91],v[204:207],a[248:251],v[88:91]\nv_cvt_pk_bf16_f32 v64,v64,v65\nv_cvt_pk_bf16_f32 v65,v66,v67\nM v[96:99],v[208:211],a[248:251],v[96:99]\n" \
"v_cvt_pk_bf16_f32 v7,v14,v15\nv_cvt_pk_bf16_f32 v22,v28,v29\nM v[104:107],v[212:215],a[248:251],v[104:107]\nv_cvt_pk_bf16_f32 v80,v80,v81\nv_cvt_pk_bf16_f32 v81,v82,v83\nM v[112:115],v[216:219],a[248:251],v[112:115]\nv_cvt_pk_bf16_f32 v23,v30,v31\nv_cvt_pk_bf16_f32 v38,v44,v45\n" \
"M v[120:123],v[220:223],a[248:251],v[120:123]\nv_cvt_pk_bf16_f32 v96,v96,v97\nv_cvt_pk_bf16_f32 v97,v98,v99\nM v[68:71],v[192:195],a[252:255],v[68:71]\nv_cvt_pk_bf16_f32 v39,v46,v47\nv_cvt_pk_bf16_f32 v54,v60,v61\nM v[76:79],v[196:199],a[252:255],v[76:79]\nv_cvt_pk_bf16_f32 v112,v112,v113\n" \
"v_cvt_pk_bf16_f32 v113,v114,v115\nM v[84:87],v[200:203],a[252:255],v[84:87]\nv_cvt_pk_bf16_f32 v55,v62,v63\nv_cvt_pk_bf16_f32 v66,v72,v73\nM v[92:95],v[204:207],a[252:255],v[92:95]\nv_cvt_pk_bf16_f32 v68,v68,v69\nv_cvt_pk_bf16_f32 v69,v70,v71\nM v[100:103],v[208:211],a[252:255],v[100:103]\n" \
"v_cvt_pk_bf16_f32 v67,v74,v75\nv_cvt_pk_bf16_f32 v82,v88,v89\nM v[108:111],v[212:215],a[252:255],v[108:111]\nv_cvt_pk_bf16_f32 v84,v84,v85\nv_cvt_pk_bf16_f32 v85,v86,v87\nM v[116:119],v[216:219],a[252:255],v[116:119]\nv_cvt_pk_bf16_f32 v83,v90,v91\nv_cvt_pk_bf16_f32 v98,v104,v105\n" \
"v_cvt_pk_bf16_f32 v100,v100,v101\nv_cvt_pk_bf16_f32 v101,v102,v103\nM v[124:127],v[220:223],a[252:255],v[124:127]\nv_cvt_pk_bf16_f32 v99,v106,v107\nv_cvt_pk_bf16_f32 v114,v120,v121\nv_cvt_pk_bf16_f32 v115,v122,v123\nv_cvt_pk_bf16_f32 v116,v116,v117\nv_cvt_pk_bf16_f32 v117,v118,v119\n" \
"v_cvt_pk_bf16_f32 v70,v76,v77\nv_cvt_pk_bf16_f32 v71,v78,v79\nv_cvt_pk_bf16_f32 v86,v92,v93\nv_cvt_pk_bf16_f32 v87,v94,v95\nv_cvt_pk_bf16_f32 v102,v108,v109\nv_cvt_pk_bf16_f32 v103,v110,v111\nv_cvt_pk_bf16_f32 v118,v124,v125\nv_cvt_pk_bf16_f32 v119,v126,v127\n" \
"s_waitcnt vmcnt(0)\nbuffer_store_dword v252,off,s[52:55],s29 sc1\nv_accvgpr_write_b32 a244,0\nv_accvgpr_write_b32 a248,-1\nv_accvgpr_write_b32 a245,0\nv_accvgpr_write_b32 a249,-1\nv_accvgpr_write_b32 a246,0\nv_accvgpr_write_b32 a250,-1\n" \
"v_accvgpr_write_b32 a247,0\nv_accvgpr_write_b32 a251,-1\nv_accvgpr_write_b32 a128,v0\nv_accvgpr_write_b32 a129,v1\nv_accvgpr_write_b32 a130,v2\nv_accvgpr_write_b32 a131,v3\nv_accvgpr_write_b32 a132,v16\nv_accvgpr_write_b32 a133,v17\n" \
"v_accvgpr_write_b32 a134,v18\nv_accvgpr_write_b32 a135,v19\nv_accvgpr_write_b32 a136,v32\nv_accvgpr_write_b32 a137,v33\nv_accvgpr_write_b32 a138,v34\nv_accvgpr_write_b32 a139,v35\nv_accvgpr_write_b32 a140,v48\nv_accvgpr_write_b32 a141,v49\n" \
"v_accvgpr_write_b32 a142,v50\nv_accvgpr_write_b32 a143,v51\nv_accvgpr_write_b32 a144,v4\nv_accvgpr_write_b32 a145,v5\nv_accvgpr_write_b32 a146,v6\nv_accvgpr_write_b32 a147,v7\nv_accvgpr_write_b32 a148,v20\nv_accvgpr_write_b32 a149,v21\n" \
"v_accvgpr_write_b32 a150,v22\nv_accvgpr_write_b32 a151,v23\nv_accvgpr_write_b32 a152,v36\nv_accvgpr_write_b32 a153,v37\nv_accvgpr_write_b32 a154,v38\nv_accvgpr_write_b32 a155,v39\nv_accvgpr_write_b32 a156,v52\nv_accvgpr_write_b32 a157,v53\n" \
"v_accvgpr_write_b32 a158,v54\nv_accvgpr_write_b32 a159,v55\nv_accvgpr_write_b32 a160,v64\nv_accvgpr_write_b32 a161,v65\nv_accvgpr_write_b32 a162,v66\nv_accvgpr_write_b32 a163,v67\nv_accvgpr_write_b32 a164,v80\nv_accvgpr_write_b32 a165,v81\n" \
"v_accvgpr_write_b32 a166,v82\nv_accvgpr_write_b32 a167,v83\nv_accvgpr_write_b32 a168,v96\nv_accvgpr_write_b32 a169,v97\nv_accvgpr_write_b32 a170,v98\nv_accvgpr_write_b32 a171,v99\nv_accvgpr_write_b32 a172,v112\nv_accvgpr_write_b32 a173,v113\n" \
"v_accvgpr_write_b32 a174,v114\nv_accvgpr_write_b32 a175,v115\nv_accvgpr_write_b32 a176,v68\nv_accvgpr_write_b32 a177,v69\nv_accvgpr_write_b32 a178,v70\nv_accvgpr_write_b32 a179,v71\nv_accvgpr_write_b32 a180,v84\nv_accvgpr_write_b32 a181,v85\n" \
"v_accvgpr_write_b32 a182,v86\nv_accvgpr_write_b32 a183,v87\nv_accvgpr_write_b32 a184,v100\nv_accvgpr_write_b32 a185,v101\nv_accvgpr_write_b32 a186,v102\nv_accvgpr_write_b32 a187,v103\nv_accvgpr_write_b32 a188,v116\nv_accvgpr_write_b32 a189,v117\n" \
"v_accvgpr_write_b32 a190,v118\nv_accvgpr_write_b32 a191,v119\ns_waitcnt lgkmcnt(0)\n.Lattn:\ns_cmp_lt_u32 s7,2\ns_cbranch_scc0 .Lp23\ns_cmp_eq_u32 s7,0\ns_cbranch_scc0 .Lpro1\n" \
".Lpro0:\ns_barrier\nM v[64:67],a[0:3],a[128:131],0\nds_read_b128 a[32:35],v240 offset:40960\nds_read_b128 a[36:39],v240 offset:41984\nM v[64:67],a[4:7],a[132:135],v[64:67]\nds_read_b128 a[40:43],v240 offset:43008\ns_mov_b32 m0,s64\n" \
"v_mov_b32 v218,0\nM v[64:67],a[8:11],a[136:139],v[64:67]\nds_read_b128 a[44:47],v240 offset:44032\nv_mov_b32 v219,0\nM v[64:67],a[12:15],a[140:143],v[64:67]\nds_read_b128 a[48:51],v240 offset:45056\nbuffer_load_dwordx4 v240,s[44:47],s36 offen offset:0 sc1 lds\nM v[68:71],a[0:3],a[144:147],0\n" \
"ds_read_b128 a[0:3],v240 offset:32768\nM v[68:71],a[4:7],a[148:151],v[68:71]\nds_read_b128 a[4:7],v240 offset:33792\nM v[68:71],a[8:11],a[152:155],v[68:71]\nds_read_b128 a[8:11],v240 offset:34816\nv_max3_f32 v225,v64,v65,v66\nM v[68:71],a[12:15],a[156:159],v[68:71]\nds_read_b128 a[12:15],v240 offset:35840\n" \
"v_max_f32 v225,v225,v67\nv_cndmask_b32 v225,v224,v225,s[90:91]\nM v[76:79],a[16:19],a[144:147],0\nds_read_b128 a[16:19],v240 offset:36864\nv_cndmask_b32 v64,v64,v252,s[82:83]\nM v[76:79],a[20:23],a[148:151],v[76:79]\nds_read_b128 a[20:23],v240 offset:37888\nv_max3_f32 v229,v68,v69,v70\n" \
"v_cndmask_b32 v65,v65,v252,s[84:85]\nM v[76:79],a[24:27],a[152:155],v[76:79]\nds_read_b128 a[24:27],v240 offset:38912\nv_cndmask_b32 v66,v66,v252,s[86:87]\nM v[76:79],a[28:31],a[156:159],v[76:79]\nds_read_b128 a[28:31],v240 offset:39936\nv_cndmask_b32 v67,v67,v252,s[88:89]\ns_waitcnt lgkmcnt(7)\n" \
"M v[136:139],a[0:3],a[160:163],0\nds_read_b128 a[52:55],v240 offset:46080\ns_waitcnt lgkmcnt(7)\nM v[136:139],a[4:7],a[164:167],v[136:139]\nbuffer_load_dwordx4 v240,s[44:47],s36 offen offset:1024 sc1 lds\ns_waitcnt lgkmcnt(6)\nM v[136:139],a[8:11],a[168:171],v[136:139]\nv_max3_f32 v229,v229,v71,v76\n" \
"v_max3_f32 v229,v229,v77,v78\ns_waitcnt lgkmcnt(5)\nM v[136:139],a[12:15],a[172:175],v[136:139]\nv_max_f32 v229,v229,v79\nv_cndmask_b32 v229,v228,v229,s[90:91]\nM v[140:143],a[0:3],a[176:179],0\nds_read_b128 a[56:59],v240 offset:47104\nv_permlane16_swap_b32 v225,v229\n" \
"v_max_f32 v225,v225,v229\nM v[140:143],a[4:7],a[180:183],v[140:143]\nv_mov_b32 v229,v225\nds_read_b128 a[60:63],v240 offset:48128\nM v[140:143],a[8:11],a[184:187],v[140:143]\nv_permlane32_swap_b32 v225,v229\nv_max_f32 v225,v225,v229\nv_mov_b32 v229,v225\n" \
"M v[140:143],a[12:15],a[188:191],v[140:143]\nv_max3_f32 v233,v136,v137,v138\ns_waitcnt lgkmcnt(4)\nM v[152:155],a[16:19],a[160:163],0\nv_permlane16_swap_b32 v225,v229\nv_sub_f32 v224,0,v225\nv_sub_f32 v226,0,v225\nM v[152:155],a[20:23],a[164:167],v[152:155]\n" \
"v_sub_f32 v227,0,v225\nv_sub_f32 v225,0,v225\nM v[152:155],a[24:27],a[168:171],v[152:155]\nv_sub_f32 v228,0,v229\nv_sub_f32 v230,0,v229\ns_waitcnt lgkmcnt(2)\nM v[152:155],a[28:31],a[172:175],v[152:155]\nv_sub_f32 v231,0,v229\n" \
"v_sub_f32 v229,0,v229\nM v[156:159],a[16:19],a[176:179],0\nv_max3_f32 v237,v140,v141,v142\nds_read_b128 a[64:67],v241 offset:0\nds_read_b128 a[68:71],v241 offset:1024\nM v[156:159],a[20:23],a[180:183],v[156:159]\nds_read_b128 a[72:75],v241 offset:2048\nv_max3_f32 v233,v233,v139,v152\n" \
"M v[156:159],a[24:27],a[184:187],v[156:159]\nv_max3_f32 v233,v233,v153,v154\nv_max_f32 v233,v233,v155\nv_cndmask_b32 v233,v232,v233,s[90:91]\nM v[156:159],a[28:31],a[188:191],v[156:159]\nds_read_b128 a[76:79],v241 offset:3072\nv_add_f32 v64,v64,v224\nv_add_f32 v65,v65,v224\n" \
"M v[128:131],a[0:3],a[128:131],v[224:227]\nv_add_f32 v66,v66,v224\nv_add_f32 v67,v67,v224\nM v[128:131],a[4:7],a[132:135],v[128:131]\nv_exp_f32 v64,v64\nv_max3_f32 v237,v237,v143,v156\nM v[128:131],a[8:11],a[136:139],v[128:131]\nv_max3_f32 v237,v237,v157,v158\n" \
"v_max_f32 v237,v237,v159\nM v[128:131],a[12:15],a[140:143],v[128:131]\nv_cndmask_b32 v237,v236,v237,s[90:91]\nv_exp_f32 v65,v65\nM v[132:135],a[0:3],a[144:147],v[228:231]\nv_permlane16_swap_b32 v233,v237\nv_max_f32 v233,v233,v237\nM v[132:135],a[4:7],a[148:151],v[132:135]\n" \
"v_mov_b32 v237,v233\nv_exp_f32 v66,v66\nM v[132:135],a[8:11],a[152:155],v[132:135]\nv_permlane32_swap_b32 v233,v237\nv_max_f32 v233,v233,v237\nM v[132:135],a[12:15],a[156:159],v[132:135]\nv_mov_b32 v237,v233\nv_exp_f32 v67,v67\n" \
"M v[144:147],a[16:19],a[128:131],v[224:227]\nv_permlane16_swap_b32 v233,v237\nv_sub_f32 v232,0,v233\nM v[144:147],a[20:23],a[132:135],v[144:147]\nv_sub_f32 v234,0,v233\nv_sub_f32 v235,0,v233\nv_sub_f32 v233,0,v233\nM v[144:147],a[24:27],a[136:139],v[144:147]\n" \
"v_sub_f32 v236,0,v237\nv_sub_f32 v238,0,v237\nM v[144:147],a[28:31],a[140:143],v[144:147]\nv_sub_f32 v239,0,v237\nv_sub_f32 v237,0,v237\nv_add_f32 v68,v68,v228\nM v[148:151],a[16:19],a[144:147],v[228:231]\nv_add_f32 v69,v69,v228\n" \
"v_cndmask_b32 v76,v76,v252,s[82:83]\nv_cndmask_b32 v77,v77,v252,s[84:85]\nM v[148:151],a[20:23],a[148:151],v[148:151]\nv_cndmask_b32 v78,v78,v252,s[86:87]\nv_cndmask_b32 v79,v79,v252,s[88:89]\nM v[148:151],a[24:27],a[152:155],v[148:151]\nv_add_f32 v70,v70,v228\nv_add_f32 v71,v71,v228\n" \
"v_cvt_pk_bf16_f32 v64,v64,v65\nM v[148:151],a[28:31],a[156:159],v[148:151]\nv_exp_f32 v68,v68\nM v[160:163],a[32:35],a[128:131],v[224:227]\nv_exp_f32 v69,v69\nds_read_b128 a[80:83],v241 offset:4096\nM v[160:163],a[36:39],a[132:135],v[160:163]\nv_exp_f32 v70,v70\n" \
"M v[160:163],a[40:43],a[136:139],v[160:163]\nv_exp_f32 v71,v71\nv_add_f32 v76,v76,v228\nM v[160:163],a[44:47],a[140:143],v[160:163]\nv_add_f32 v77,v77,v228\nv_add_f32 v78,v78,v228\nM v[164:167],a[32:35],a[144:147],v[228:231]\nv_add_f32 v79,v79,v228\n" \
"v_cvt_pk_bf16_f32 v65,v66,v67\nv_mov_b32 v66,0\nM v[164:167],a[36:39],a[148:151],v[164:167]\nv_exp_f32 v76,v76\nM v[164:167],a[40:43],a[152:155],v[164:167]\nv_exp_f32 v77,v77\nv_mov_b32 v67,0\nM v[164:167],a[44:47],a[156:159],v[164:167]\n" \
"v_exp_f32 v78,v78\nM v[176:179],a[48:51],a[128:131],v[224:227]\nv_exp_f32 v79,v79\nv_cvt_pk_bf16_f32 v68,v68,v69\nM v[176:179],a[52:55],a[132:135],v[176:179]\nv_cvt_pk_bf16_f32 v69,v70,v71\nv_cvt_pk_bf16_f32 v70,v76,v77\ns_waitcnt lgkmcnt(0)\n" \
"M v[176:179],a[56:59],a[136:139],v[176:179]\nv_cvt_pk_bf16_f32 v71,v78,v79\nds_read_b128 a[84:87],v241 offset:5120\nM v[176:179],a[60:63],a[140:143],v[176:179]\nbuffer_load_dwordx4 v240,s[44:47],s36 offen offset:2048 sc1 lds\nds_read_b128 a[88:91],v241 offset:6144\nM v[180:183],a[48:51],a[144:147],v[228:231]\nds_read_b128 a[92:95],v241 offset:7168\n" \
"ds_read_b128 a[0:3],v240 offset:49152\nM v[180:183],a[52:55],a[148:151],v[180:183]\nds_read_b128 a[4:7],v240 offset:50176\nv_exp_f32 v128,v128\nM v[180:183],a[56:59],a[152:155],v[180:183]\nds_read_b128 a[8:11],v240 offset:51200\nv_add_f32 v136,v136,v232\nM v[180:183],a[60:63],a[156:159],v[180:183]\n" \
"buffer_load_dwordx4 v240,s[44:47],s36 offen offset:3072 sc1 lds\nds_read_b128 a[12:15],v240 offset:52224\nM v[168:171],a[32:35],a[160:163],v[232:235]\nds_read_b128 a[16:19],v240 offset:53248\nv_add_f32 v137,v137,v232\nM v[168:171],a[36:39],a[164:167],v[168:171]\nv_exp_f32 v129,v129\nds_read_b128 a[20:23],v240 offset:54272\n" \
"M v[168:171],a[40:43],a[168:171],v[168:171]\nv_exp_f32 v130,v130\nM v[168:171],a[44:47],a[172:175],v[168:171]\nv_exp_f32 v131,v131\nv_cvt_pk_bf16_f32 v192,v128,v129\nM v[172:175],a[32:35],a[176:179],v[236:239]\nv_cvt_pk_bf16_f32 v193,v130,v131\nds_read_b128 a[24:27],v240 offset:55296\n" \
"M v[172:175],a[36:39],a[180:183],v[172:175]\nds_read_b128 a[28:31],v240 offset:56320\nv_exp_f32 v132,v132\nM v[172:175],a[40:43],a[184:187],v[172:175]\nv_exp_f32 v133,v133\nv_add_f32 v138,v138,v232\nM v[172:175],a[44:47],a[188:191],v[172:175]\nv_exp_f32 v134,v134\n" \
"M v[188:191],a[48:51],a[176:179],v[236:239]\nv_exp_f32 v135,v135\nv_cvt_pk_bf16_f32 v196,v132,v133\nM v[188:191],a[52:55],a[180:183],v[188:191]\nv_cvt_pk_bf16_f32 v197,v134,v135\nv_add_f32 v139,v139,v232\nM v[188:191],a[56:59],a[184:187],v[188:191]\nv_exp_f32 v144,v144\n" \
"v_add_f32 v140,v140,v236\nM v[188:191],a[60:63],a[188:191],v[188:191]\nv_exp_f32 v145,v145\nM v[0:3],a[64:67],v[64:67],0\nv_exp_f32 v146,v146\nv_cvt_pk_bf16_f32 v194,v144,v145\nM v[4:7],a[64:67],v[68:71],0\nv_exp_f32 v147,v147\n" \
"M v[8:11],a[68:71],v[64:67],0\nbuffer_load_dword v248,v251,s[52:55],s80 offen sc1\nv_cvt_pk_bf16_f32 v195,v146,v147\nM v[12:15],a[68:71],v[68:71],0\nv_exp_f32 v148,v148\nM v[16:19],a[72:75],v[64:67],0\nv_exp_f32 v149,v149\nv_add_f32 v141,v141,v236\n" \
"M v[20:23],a[72:75],v[68:71],0\nv_exp_f32 v150,v150\nM v[24:27],a[76:79],v[64:67],0\nv_exp_f32 v151,v151\nv_cvt_pk_bf16_f32 v198,v148,v149\nM v[28:31],a[76:79],v[68:71],0\nv_cvt_pk_bf16_f32 v199,v150,v151\nv_exp_f32 v136,v136\n" \
"M v[32:35],a[80:83],v[64:67],0\nv_exp_f32 v137,v137\nM v[36:39],a[80:83],v[68:71],0\nv_exp_f32 v138,v138\ns_waitcnt lgkmcnt(0)\nM v[40:43],a[84:87],v[64:67],0\nv_exp_f32 v139,v139\nM v[44:47],a[84:87],v[68:71],0\n" \
"v_cvt_pk_bf16_f32 v200,v136,v137\nv_cvt_pk_bf16_f32 v201,v138,v139\nv_add_f32 v142,v142,v236\nM v[48:51],a[88:91],v[64:67],0\nv_add_f32 v143,v143,v236\nds_read_b128 a[32:35],v240 offset:57344\nM v[52:55],a[88:91],v[68:71],0\nv_exp_f32 v140,v140\n" \
"v_add_f32 v152,v152,v232\nM v[56:59],a[92:95],v[64:67],0\nv_exp_f32 v141,v141\nM v[60:63],a[92:95],v[68:71],0\nv_exp_f32 v142,v142\nv_cvt_pk_bf16_f32 v204,v140,v141\nM a[192:195],a[208:211],v[64:67],0\nv_exp_f32 v143,v143\n" \
"M a[196:199],a[208:211],v[68:71],0\nv_cvt_pk_bf16_f32 v205,v142,v143\nv_add_f32 v153,v153,v232\nv_add_f32 v154,v154,v232\nM v[128:131],a[0:3],a[128:131],v[224:227]\nv_add_f32 v155,v155,v232\nds_read_b128 a[36:39],v240 offset:58368\nM v[128:131],a[4:7],a[132:135],v[128:131]\n" \
"v_exp_f32 v152,v152\nds_read_b128 a[40:43],v240 offset:59392\nM v[128:131],a[8:11],a[136:139],v[128:131]\nv_exp_f32 v153,v153\nM v[128:131],a[12:15],a[140:143],v[128:131]\nv_exp_f32 v154,v154\nv_cvt_pk_bf16_f32 v202,v152,v153\nM v[132:135],a[0:3],a[144:147],v[228:231]\n" \
"v_exp_f32 v155,v155\nds_read_b128 a[44:47],v240 offset:60416\nM v[132:135],a[4:7],a[148:151],v[132:135]\nv_cvt_pk_bf16_f32 v203,v154,v155\nv_add_f32 v156,v156,v236\nM v[132:135],a[8:11],a[152:155],v[132:135]\nv_add_f32 v157,v157,v236\nv_add_f32 v158,v158,v236\n" \
"v_add_f32 v159,v159,v236\nM v[132:135],a[12:15],a[156:159],v[132:135]\nv_exp_f32 v156,v156\nM v[144:147],a[16:19],a[128:131],v[224:227]\nv_exp_f32 v157,v157\nds_read_b128 a[48:51],v240 offset:61440\nM v[144:147],a[20:23],a[132:135],v[144:147]\nv_exp_f32 v158,v158\n" \
"M v[144:147],a[24:27],a[136:139],v[144:147]\nv_exp_f32 v159,v159\nv_cvt_pk_bf16_f32 v206,v156,v157\nM v[144:147],a[28:31],a[140:143],v[144:147]\nv_cvt_pk_bf16_f32 v207,v158,v159\nds_read_b128 a[52:55],v240 offset:62464\nM v[148:151],a[16:19],a[144:147],v[228:231]\nv_exp_f32 v160,v160\n" \
"ds_read_b128 a[56:59],v240 offset:63488\nM v[148:151],a[20:23],a[148:151],v[148:151]\nv_exp_f32 v161,v161\nM v[148:151],a[24:27],a[152:155],v[148:151]\nv_exp_f32 v162,v162\nv_cvt_pk_bf16_f32 v208,v160,v161\nM v[148:151],a[28:31],a[156:159],v[148:151]\nv_exp_f32 v163,v163\n" \
"M v[136:139],a[0:3],a[160:163],v[232:235]\nv_cvt_pk_bf16_f32 v209,v162,v163\nds_read_b128 a[60:63],v240 offset:64512\nv_cndmask_b32 v168,v168,v252,s[82:83]\nM v[136:139],a[4:7],a[164:167],v[136:139]\nv_exp_f32 v164,v164\nv_cndmask_b32 v169,v169,v252,s[84:85]\nM v[136:139],a[8:11],a[168:171],v[136:139]\n" \
"v_exp_f32 v165,v165\nM v[136:139],a[12:15],a[172:175],v[136:139]\nv_exp_f32 v166,v166\nv_cvt_pk_bf16_f32 v212,v164,v165\nM v[140:143],a[0:3],a[176:179],v[236:239]\nv_exp_f32 v167,v167\nM v[140:143],a[4:7],a[180:183],v[140:143]\nv_cvt_pk_bf16_f32 v213,v166,v167\n" \
"v_exp_f32 v176,v176\nM v[140:143],a[8:11],a[184:187],v[140:143]\nv_exp_f32 v177,v177\nM v[140:143],a[12:15],a[188:191],v[140:143]\nv_exp_f32 v178,v178\nv_cvt_pk_bf16_f32 v210,v176,v177\nM v[152:155],a[16:19],a[160:163],v[232:235]\nv_exp_f32 v179,v179\n" \
"M v[152:155],a[20:23],a[164:167],v[152:155]\nv_cvt_pk_bf16_f32 v211,v178,v179\nv_exp_f32 v180,v180\nM v[152:155],a[24:27],a[168:171],v[152:155]\nv_exp_f32 v181,v181\nM v[152:155],a[28:31],a[172:175],v[152:155]\nv_exp_f32 v182,v182\nv_cvt_pk_bf16_f32 v214,v180,v181\n" \
"M v[156:159],a[16:19],a[176:179],v[236:239]\nv_exp_f32 v183,v183\nM v[156:159],a[20:23],a[180:183],v[156:159]\nv_cvt_pk_bf16_f32 v215,v182,v183\nv_cndmask_b32 v170,v170,v252,s[86:87]\nv_cndmask_b32 v171,v171,v252,s[88:89]\nM v[156:159],a[24:27],a[184:187],v[156:159]\nv_exp_f32 v168,v168\n" \
"ds_read_b128 a[64:67],v241 offset:32768\nM v[156:159],a[28:31],a[188:191],v[156:159]\nds_read_b128 a[68:71],v241 offset:33792\ns_waitcnt lgkmcnt(2)\nM v[160:163],a[32:35],a[128:131],v[224:227]\nv_exp_f32 v169,v169\nv_cndmask_b32 v188,v188,v252,s[82:83]\nM v[160:163],a[36:39],a[132:135],v[160:163]\n" \
"v_exp_f32 v170,v170\nM v[160:163],a[40:43],a[136:139],v[160:163]\ns_waitcnt vmcnt(1)\ns_barrier\nv_cvt_pk_bf16_f32 v216,v168,v169\nM v[160:163],a[44:47],a[140:143],v[160:163]\nv_exp_f32 v171,v171\nM v[164:167],a[32:35],a[144:147],v[228:231]\n" \
"v_cvt_pk_bf16_f32 v217,v170,v171\nv_exp_f32 v172,v172\nM v[164:167],a[36:39],a[148:151],v[164:167]\nv_exp_f32 v173,v173\nM v[164:167],a[40:43],a[152:155],v[164:167]\nv_exp_f32 v174,v174\nv_cvt_pk_bf16_f32 v220,v172,v173\nM v[164:167],a[44:47],a[156:159],v[164:167]\n" \
"v_exp_f32 v175,v175\nM v[176:179],a[48:51],a[128:131],v[224:227]\nv_cvt_pk_bf16_f32 v221,v174,v175\nv_cndmask_b32 v189,v189,v252,s[84:85]\nv_cndmask_b32 v190,v190,v252,s[86:87]\nM v[176:179],a[52:55],a[132:135],v[176:179]\nv_cndmask_b32 v191,v191,v252,s[88:89]\nds_read_b128 a[72:75],v241 offset:34816\n" \
"M v[176:179],a[56:59],a[136:139],v[176:179]\nv_exp_f32 v188,v188\nds_read_b128 a[76:79],v241 offset:35840\nM v[176:179],a[60:63],a[140:143],v[176:179]\nv_exp_f32 v189,v189\nds_read_b128 a[80:83],v241 offset:36864\nM v[180:183],a[48:51],a[144:147],v[228:231]\nv_exp_f32 v190,v190\n" \
"M v[180:183],a[52:55],a[148:151],v[180:183]\nv_exp_f32 v191,v191\nv_cvt_pk_bf16_f32 v222,v188,v189\nM v[180:183],a[56:59],a[152:155],v[180:183]\nv_cvt_pk_bf16_f32 v223,v190,v191\nds_read_b128 a[84:87],v241 offset:37888\nM v[180:183],a[60:63],a[156:159],v[180:183]\nds_read_b128 a[88:91],v241 offset:38912\n" \
"ds_read_b128 a[92:95],v241 offset:39936\ns_waitcnt vmcnt(0)\nv_cmp_eq_u32 vcc,0,v248\nM v[168:171],a[32:35],a[160:163],v[232:235]\nds_read_b128 a[96:99],v241 offset:40960\ns_cbranch_vccnz .Lps2\n.Lpb2:\nM v[168:171],a[36:39],a[164:167],v[168:171]\n" \
"ds_read_b128 a[100:103],v241 offset:41984\nv_exp_f32 v128,v128\nM v[168:171],a[40:43],a[168:171],v[168:171]\nds_read_b128 a[104:107],v241 offset:43008\ns_add_u32 m0,s64,65536\nM v[168:171],a[44:47],a[172:175],v[168:171]\nds_read_b128 a[108:111],v241 offset:44032\nbuffer_load_dwordx4 v240,s[48:51],s36 offen offset:0 sc1 lds\n" \
"M v[172:175],a[32:35],a[176:179],v[236:239]\nv_exp_f32 v129,v129\nM v[172:175],a[36:39],a[180:183],v[172:175]\nds_read_b128 a[112:115],v241 offset:45056\nv_exp_f32 v130,v130\nM v[172:175],a[40:43],a[184:187],v[172:175]\nds_read_b128 a[116:119],v241 offset:46080\nds_read_b128 a[120:123],v241 offset:47104\n" \
"M v[172:175],a[44:47],a[188:191],v[172:175]\nds_read_b128 a[124:127],v241 offset:48128\nbuffer_load_dwordx4 v240,s[48:51],s36 offen offset:1024 sc1 lds\nM v[184:187],a[48:51],a[160:163],v[232:235]\nds_read_b128 a[0:3],v240 offset:0\nv_exp_f32 v131,v131\nM v[184:187],a[52:55],a[164:167],v[184:187]\nv_exp_f32 v132,v132\n" \
"M v[184:187],a[56:59],a[168:171],v[184:187]\nds_read_b128 a[4:7],v240 offset:1024\nv_exp_f32 v133,v133\nM v[184:187],a[60:63],a[172:175],v[184:187]\nds_read_b128 a[8:11],v240 offset:2048\nds_read_b128 a[12:15],v240 offset:3072\nM v[188:191],a[48:51],a[176:179],v[236:239]\nds_read_b128 a[16:19],v240 offset:4096\n" \
"buffer_load_dwordx4 v240,s[48:51],s36 offen offset:2048 sc1 lds\nM v[188:191],a[52:55],a[180:183],v[188:191]\nv_exp_f32 v134,v134\nM v[188:191],a[56:59],a[184:187],v[188:191]\nds_read_b128 a[20:23],v240 offset:5120\nv_exp_f32 v135,v135\nM v[188:191],a[60:63],a[188:191],v[188:191]\nds_read_b128 a[24:27],v240 offset:6144\n" \
"s_waitcnt lgkmcnt(8)\nM v[0:3],a[64:67],v[192:195],v[0:3]\nds_read_b128 a[28:31],v240 offset:7168\nbuffer_load_dwordx4 v240,s[48:51],s36 offen offset:3072 sc1 lds\nM v[4:7],a[64:67],v[196:199],v[4:7]\nds_read_b128 a[32:35],v240 offset:8192\nds_read_b128 a[36:39],v240 offset:9216\nM v[64:67],a[64:67],v[200:203],0\n" \
"ds_read_b128 a[40:43],v240 offset:10240\nv_exp_f32 v144,v144\nM v[68:71],a[64:67],v[204:207],0\ns_add_u32 m0,s64,16384\nds_read_b128 a[44:47],v240 offset:11264\nM v[8:11],a[68:71],v[192:195],v[8:11]\nbuffer_load_dwordx4 v240,s[44:47],s65 offen offset:0 sc1 lds\nds_read_b128 a[48:51],v240 offset:12288\n" \
"M v[12:15],a[68:71],v[196:199],v[12:15]\nds_read_b128 a[52:55],v240 offset:13312\nv_exp_f32 v145,v145\nM v[72:75],a[68:71],v[200:203],0\nv_exp_f32 v146,v146\nM v[76:79],a[68:71],v[204:207],0\nds_read_b128 a[56:59],v240 offset:14336\nv_exp_f32 v147,v147\n" \
"M v[16:19],a[72:75],v[192:195],v[16:19]\nbuffer_load_dwordx4 v240,s[44:47],s65 offen offset:1024 sc1 lds\nM v[20:23],a[72:75],v[196:199],v[20:23]\nds_read_b128 a[60:63],v240 offset:15360\nv_exp_f32 v148,v148\nM v[80:83],a[72:75],v[200:203],0\nv_exp_f32 v149,v149\nM v[84:87],a[72:75],v[204:207],0\n" \
"v_exp_f32 v150,v150\nM v[24:27],a[76:79],v[192:195],v[24:27]\nbuffer_load_dwordx4 v240,s[44:47],s65 offen offset:2048 sc1 lds\nM v[28:31],a[76:79],v[196:199],v[28:31]\nv_exp_f32 v151,v151\nM v[88:91],a[76:79],v[200:203],0\nv_exp_f32 v136,v136\nM v[92:95],a[76:79],v[204:207],0\n" \
"v_exp_f32 v137,v137\nM v[32:35],a[80:83],v[192:195],v[32:35]\nbuffer_load_dwordx4 v240,s[44:47],s65 offen offset:3072 sc1 lds\nM v[36:39],a[80:83],v[196:199],v[36:39]\ns_add_u32 m0,s64,81920\nv_exp_f32 v138,v138\nM v[96:99],a[80:83],v[200:203],0\nv_exp_f32 v139,v139\n" \
"M v[100:103],a[80:83],v[204:207],0\nv_exp_f32 v140,v140\nM v[40:43],a[84:87],v[192:195],v[40:43]\nbuffer_load_dwordx4 v240,s[48:51],s65 offen offset:0 sc1 lds\nM v[44:47],a[84:87],v[196:199],v[44:47]\nv_exp_f32 v141,v141\nM v[104:107],a[84:87],v[200:203],0\nv_exp_f32 v142,v142\n" \
"M v[108:111],a[84:87],v[204:207],0\nv_exp_f32 v143,v143\nM v[48:51],a[88:91],v[192:195],v[48:51]\nbuffer_load_dwordx4 v240,s[48:51],s65 offen offset:1024 sc1 lds\nM v[52:55],a[88:91],v[196:199],v[52:55]\nv_exp_f32 v152,v152\nM v[112:115],a[88:91],v[200:203],0\nv_exp_f32 v153,v153\n" \
"M v[116:119],a[88:91],v[204:207],0\nv_exp_f32 v154,v154\nM v[56:59],a[92:95],v[192:195],v[56:59]\nbuffer_load_dwordx4 v240,s[48:51],s65 offen offset:2048 sc1 lds\nM v[60:63],a[92:95],v[196:199],v[60:63]\nv_exp_f32 v155,v155\nM v[120:123],a[92:95],v[200:203],0\nv_exp_f32 v156,v156\n" \
"M v[124:127],a[92:95],v[204:207],0\nv_exp_f32 v157,v157\nM a[192:195],a[208:211],v[192:195],a[192:195]\nbuffer_load_dwordx4 v240,s[48:51],s65 offen offset:3072 sc1 lds\ns_add_u32 m0,s64,32768\nM a[196:199],a[208:211],v[196:199],a[196:199]\nv_exp_f32 v158,v158\nv_cvt_pk_bf16_f32 v192,v128,v129\n" \
"M a[200:203],a[208:211],v[200:203],0\nv_exp_f32 v159,v159\nM a[204:207],a[208:211],v[204:207],0\nv_exp_f32 v160,v160\nv_cvt_pk_bf16_f32 v193,v130,v131\nM v[0:3],a[96:99],v[208:211],v[0:3]\nbuffer_load_dwordx4 v240,s[44:47],s66 offen offset:0 sc1 lds\nM v[4:7],a[96:99],v[212:215],v[4:7]\n" \
"v_exp_f32 v161,v161\nv_cvt_pk_bf16_f32 v194,v144,v145\nM v[64:67],a[96:99],v[216:219],v[64:67]\nv_exp_f32 v162,v162\nM v[68:71],a[96:99],v[220:223],v[68:71]\nv_exp_f32 v163,v163\nv_cvt_pk_bf16_f32 v195,v146,v147\nM v[8:11],a[100:103],v[208:211],v[8:11]\n" \
"buffer_load_dwordx4 v240,s[44:47],s66 offen offset:1024 sc1 lds\nM v[12:15],a[100:103],v[212:215],v[12:15]\nv_exp_f32 v164,v164\nv_cvt_pk_bf16_f32 v196,v132,v133\nM v[72:75],a[100:103],v[216:219],v[72:75]\ns_waitcnt lgkmcnt(0)\ns_barrier\nM v[76:79],a[100:103],v[220:223],v[76:79]\n" \
"v_exp_f32 v165,v165\nv_cvt_pk_bf16_f32 v197,v134,v135\nM v[16:19],a[104:107],v[208:211],v[16:19]\nbuffer_load_dwordx4 v240,s[44:47],s66 offen offset:2048 sc1 lds\nM v[20:23],a[104:107],v[212:215],v[20:23]\nv_exp_f32 v166,v166\nv_cvt_pk_bf16_f32 v198,v148,v149\nM v[80:83],a[104:107],v[216:219],v[80:83]\n" \
"v_exp_f32 v167,v167\nv_cvt_pk_bf16_f32 v199,v150,v151\nM v[84:87],a[104:107],v[220:223],v[84:87]\nv_exp_f32 v176,v176\nM v[24:27],a[108:111],v[208:211],v[24:27]\nbuffer_load_dwordx4 v240,s[44:47],s66 offen offset:3072 sc1 lds\ns_add_u32 m0,s64,98304\nM v[28:31],a[108:111],v[212:215],v[28:31]\n" \
"v_exp_f32 v177,v177\nM v[88:91],a[108:111],v[216:219],v[88:91]\nv_exp_f32 v178,v178\nv_cvt_pk_bf16_f32 v200,v136,v137\nM v[92:95],a[108:111],v[220:223],v[92:95]\nv_exp_f32 v179,v179\nM v[32:35],a[112:115],v[208:211],v[32:35]\nv_exp_f32 v180,v180\n" \
"v_cvt_pk_bf16_f32 v201,v138,v139\nM v[36:39],a[112:115],v[212:215],v[36:39]\nv_exp_f32 v181,v181\nM v[96:99],a[112:115],v[216:219],v[96:99]\nv_exp_f32 v182,v182\nv_cvt_pk_bf16_f32 v202,v152,v153\nM v[100:103],a[112:115],v[220:223],v[100:103]\nv_exp_f32 v183,v183\n" \
"M v[40:43],a[116:119],v[208:211],v[40:43]\nv_exp_f32 v168,v168\nv_cvt_pk_bf16_f32 v203,v154,v155\nM v[44:47],a[116:119],v[212:215],v[44:47]\nv_exp_f32 v169,v169\nM v[104:107],a[116:119],v[216:219],v[104:107]\nv_exp_f32 v170,v170\nv_cvt_pk_bf16_f32 v204,v140,v141\n" \
"M v[108:111],a[116:119],v[220:223],v[108:111]\nv_exp_f32 v171,v171\nv_cvt_pk_bf16_f32 v205,v142,v143\nM v[48:51],a[120:123],v[208:211],v[48:51]\nv_cvt_pk_bf16_f32 v206,v156,v157\nv_cvt_pk_bf16_f32 v207,v158,v159\nM v[52:55],a[120:123],v[212:215],v[52:55]\nbuffer_load_dwordx4 v240,s[48:51],s66 offen offset:0 sc1 lds\n" \
"M v[112:115],a[120:123],v[216:219],v[112:115]\nM v[116:119],a[120:123],v[220:223],v[116:119]\nM v[56:59],a[124:127],v[208:211],v[56:59]\nM v[60:63],a[124:127],v[212:215],v[60:63]\nbuffer_load_dwordx4 v240,s[48:51],s66 offen offset:1024 sc1 lds\nM v[120:123],a[124:127],v[216:219],v[120:123]\nM v[124:127],a[124:127],v[220:223],v[124:127]\nM a[192:195],a[208:211],v[208:211],a[192:195]\n" \
"M a[196:199],a[208:211],v[212:215],a[196:199]\nbuffer_load_dwordx4 v240,s[48:51],s66 offen offset:2048 sc1 lds\nM a[200:203],a[208:211],v[216:219],a[200:203]\nbuffer_load_dwordx4 v240,s[48:51],s66 offen offset:3072 sc1 lds\nM a[204:207],a[208:211],v[220:223],a[204:207]\ns_cmp_lt_u32 s62,2\ns_cbranch_scc1 .Lx01\ns_branch .Ldual\n" \
".Lpro1:\ns_barrier\nM v[64:67],a[0:3],a[128:131],0\nds_read_b128 a[64:67],v241 offset:0\nds_read_b128 a[68:71],v241 offset:1024\nM v[64:67],a[4:7],a[132:135],v[64:67]\nds_read_b128 a[72:75],v241 offset:2048\ns_mov_b32 m0,s64\n" \
"v_mov_b32 v202,0\nM v[64:67],a[8:11],a[136:139],v[64:67]\nds_read_b128 a[76:79],v241 offset:3072\nv_mov_b32 v203,0\nM v[64:67],a[12:15],a[140:143],v[64:67]\nds_read_b128 a[80:83],v241 offset:4096\nbuffer_load_dwordx4 v240,s[44:47],s36 offen offset:0 sc1 lds\nM v[68:71],a[0:3],a[144:147],0\n" \
"ds_read_b128 a[0:3],v240 offset:32768\nM v[68:71],a[4:7],a[148:151],v[68:71]\nds_read_b128 a[4:7],v240 offset:33792\nM v[68:71],a[8:11],a[152:155],v[68:71]\nds_read_b128 a[8:11],v240 offset:34816\nv_max3_f32 v225,v64,v65,v66\nM v[68:71],a[12:15],a[156:159],v[68:71]\nds_read_b128 a[12:15],v240 offset:35840\n" \
"buffer_load_dwordx4 v240,s[44:47],s36 offen offset:1024 sc1 lds\nM v[72:75],a[16:19],a[128:131],0\nds_read_b128 a[84:87],v241 offset:5120\nM v[72:75],a[20:23],a[132:135],v[72:75]\nds_read_b128 a[88:91],v241 offset:6144\nM v[72:75],a[24:27],a[136:139],v[72:75]\nds_read_b128 a[92:95],v241 offset:7168\nv_max3_f32 v229,v68,v69,v70\n" \
"M v[72:75],a[28:31],a[140:143],v[72:75]\nbuffer_load_dwordx4 v240,s[44:47],s36 offen offset:2048 sc1 lds\nds_read_b128 a[96:99],v241 offset:8192\nM v[76:79],a[16:19],a[144:147],0\nds_read_b128 a[16:19],v240 offset:36864\nM v[76:79],a[20:23],a[148:151],v[76:79]\nds_read_b128 a[20:23],v240 offset:37888\nM v[76:79],a[24:27],a[152:155],v[76:79]\n" \
"ds_read_b128 a[24:27],v240 offset:38912\nv_max3_f32 v225,v225,v67,v72\nv_max3_f32 v225,v225,v73,v74\nM v[76:79],a[28:31],a[156:159],v[76:79]\nds_read_b128 a[28:31],v240 offset:39936\nv_max_f32 v225,v225,v75\nv_cndmask_b32 v225,v224,v225,s[90:91]\nM v[80:83],a[32:35],a[128:131],0\n" \
"buffer_load_dwordx4 v240,s[44:47],s36 offen offset:3072 sc1 lds\nM v[80:83],a[36:39],a[132:135],v[80:83]\nds_read_b128 a[100:103],v241 offset:9216\nds_read_b128 a[104:107],v241 offset:10240\nv_max3_f32 v229,v229,v71,v76\nM v[80:83],a[40:43],a[136:139],v[80:83]\nv_max3_f32 v229,v229,v77,v78\nv_max_f32 v229,v229,v79\n" \
"v_cndmask_b32 v229,v228,v229,s[90:91]\nM v[80:83],a[44:47],a[140:143],v[80:83]\nds_read_b128 a[108:111],v241 offset:11264\nv_permlane16_swap_b32 v225,v229\nM v[84:87],a[32:35],a[144:147],0\nv_max_f32 v225,v225,v229\nv_mov_b32 v229,v225\nds_read_b128 a[32:35],v240 offset:40960\n" \
"M v[84:87],a[36:39],a[148:151],v[84:87]\nv_permlane32_swap_b32 v225,v229\nv_max_f32 v225,v225,v229\nM v[84:87],a[40:43],a[152:155],v[84:87]\nv_mov_b32 v229,v225\nds_read_b128 a[36:39],v240 offset:41984\nds_read_b128 a[40:43],v240 offset:43008\nM v[84:87],a[44:47],a[156:159],v[84:87]\n" \
"v_permlane16_swap_b32 v225,v229\nv_sub_f32 v224,0,v225\nv_sub_f32 v226,0,v225\nM v[92:95],a[48:51],a[144:147],0\nv_sub_f32 v227,0,v225\nv_sub_f32 v225,0,v225\nM v[92:95],a[52:55],a[148:151],v[92:95]\nv_sub_f32 v228,0,v229\n" \
"v_sub_f32 v230,0,v229\nv_sub_f32 v231,0,v229\nM v[92:95],a[56:59],a[152:155],v[92:95]\nv_sub_f32 v229,0,v229\nds_read_b128 a[44:47],v240 offset:44032\nds_read_b128 a[48:51],v240 offset:45056\nM v[92:95],a[60:63],a[156:159],v[92:95]\nds_read_b128 a[52:55],v240 offset:46080\n" \
"s_waitcnt lgkmcnt(7)\nM v[136:139],a[0:3],a[160:163],0\nds_read_b128 a[56:59],v240 offset:47104\nv_add_f32 v64,v64,v224\nv_add_f32 v65,v65,v224\nM v[136:139],a[4:7],a[164:167],v[136:139]\nds_read_b128 a[60:63],v240 offset:48128\nv_add_f32 v66,v66,v224\n" \
"M v[136:139],a[8:11],a[168:171],v[136:139]\nv_add_f32 v67,v67,v224\nv_exp_f32 v64,v64\nM v[136:139],a[12:15],a[172:175],v[136:139]\nv_exp_f32 v65,v65\nv_add_f32 v68,v68,v228\nM v[140:143],a[0:3],a[176:179],0\nv_exp_f32 v66,v66\n" \
"M v[140:143],a[4:7],a[180:183],v[140:143]\nv_exp_f32 v67,v67\nv_add_f32 v69,v69,v228\nM v[140:143],a[8:11],a[184:187],v[140:143]\nv_add_f32 v72,v72,v224\nv_add_f32 v73,v73,v224\nv_add_f32 v74,v74,v224\nM v[140:143],a[12:15],a[188:191],v[140:143]\n" \
"v_add_f32 v75,v75,v224\nv_add_f32 v70,v70,v228\nM v[156:159],a[16:19],a[176:179],0\nv_add_f32 v71,v71,v228\nv_exp_f32 v68,v68\nM v[156:159],a[20:23],a[180:183],v[156:159]\nv_exp_f32 v69,v69\nv_cvt_pk_bf16_f32 v64,v64,v65\n" \
"M v[156:159],a[24:27],a[184:187],v[156:159]\nv_exp_f32 v72,v72\nM v[156:159],a[28:31],a[188:191],v[156:159]\nv_exp_f32 v73,v73\nv_add_f32 v76,v76,v228\nM v[128:131],a[0:3],a[128:131],v[224:227]\nv_exp_f32 v74,v74\nM v[128:131],a[4:7],a[132:135],v[128:131]\n" \
"v_exp_f32 v75,v75\nv_add_f32 v77,v77,v228\nM v[128:131],a[8:11],a[136:139],v[128:131]\nv_exp_f32 v70,v70\nv_add_f32 v78,v78,v228\nM v[128:131],a[12:15],a[140:143],v[128:131]\nv_exp_f32 v71,v71\nM v[132:135],a[0:3],a[144:147],v[228:231]\n" \
"v_add_f32 v79,v79,v228\nv_cvt_pk_bf16_f32 v65,v66,v67\nv_cvt_pk_bf16_f32 v66,v72,v73\nM v[132:135],a[4:7],a[148:151],v[132:135]\nv_exp_f32 v76,v76\nv_cvt_pk_bf16_f32 v67,v74,v75\nM v[132:135],a[8:11],a[152:155],v[132:135]\nv_exp_f32 v77,v77\n" \
"M v[132:135],a[12:15],a[156:159],v[132:135]\nv_exp_f32 v78,v78\nv_cvt_pk_bf16_f32 v68,v68,v69\nM v[144:147],a[16:19],a[128:131],v[224:227]\nv_exp_f32 v79,v79\nv_cvt_pk_bf16_f32 v69,v70,v71\nM v[144:147],a[20:23],a[132:135],v[144:147]\nv_cvt_pk_bf16_f32 v70,v76,v77\n" \
"v_cvt_pk_bf16_f32 v71,v78,v79\nM v[144:147],a[24:27],a[136:139],v[144:147]\nv_cndmask_b32 v80,v80,v252,s[82:83]\nv_cndmask_b32 v81,v81,v252,s[84:85]\nv_cndmask_b32 v82,v82,v252,s[86:87]\nM v[144:147],a[28:31],a[140:143],v[144:147]\nv_cndmask_b32 v83,v83,v252,s[88:89]\nv_add_f32 v80,v80,v224\n" \
"M v[148:151],a[16:19],a[144:147],v[228:231]\nv_add_f32 v81,v81,v224\nv_add_f32 v82,v82,v224\nv_add_f32 v83,v83,v224\nM v[148:151],a[20:23],a[148:151],v[148:151]\nv_exp_f32 v80,v80\nv_add_f32 v84,v84,v228\nM v[148:151],a[24:27],a[152:155],v[148:151]\n" \
"v_exp_f32 v81,v81\nM v[148:151],a[28:31],a[156:159],v[148:151]\nv_exp_f32 v82,v82\ns_waitcnt lgkmcnt(0)\nM v[160:163],a[32:35],a[128:131],v[224:227]\nv_exp_f32 v83,v83\nv_add_f32 v85,v85,v228\nM v[160:163],a[36:39],a[132:135],v[160:163]\n" \
"v_cndmask_b32 v92,v92,v252,s[82:83]\nv_cndmask_b32 v93,v93,v252,s[84:85]\nM v[160:163],a[40:43],a[136:139],v[160:163]\nv_cndmask_b32 v94,v94,v252,s[86:87]\nv_cndmask_b32 v95,v95,v252,s[88:89]\nv_add_f32 v86,v86,v228\nM v[160:163],a[44:47],a[140:143],v[160:163]\nv_add_f32 v87,v87,v228\n" \
"v_cvt_pk_bf16_f32 v80,v80,v81\nM v[164:167],a[32:35],a[144:147],v[228:231]\nv_exp_f32 v84,v84\nds_read_b128 a[112:115],v241 offset:12288\nM v[164:167],a[36:39],a[148:151],v[164:167]\nv_exp_f32 v85,v85\nv_add_f32 v92,v92,v228\nM v[164:167],a[40:43],a[152:155],v[164:167]\n" \
"v_exp_f32 v86,v86\nM v[164:167],a[44:47],a[156:159],v[164:167]\nv_exp_f32 v87,v87\nv_add_f32 v93,v93,v228\nM v[176:179],a[48:51],a[128:131],v[224:227]\nv_add_f32 v94,v94,v228\nv_add_f32 v95,v95,v228\nv_cvt_pk_bf16_f32 v81,v82,v83\n" \
"M v[176:179],a[52:55],a[132:135],v[176:179]\nv_exp_f32 v92,v92\nM v[176:179],a[56:59],a[136:139],v[176:179]\nv_exp_f32 v93,v93\nv_mov_b32 v82,0\nM v[176:179],a[60:63],a[140:143],v[176:179]\nv_exp_f32 v94,v94\nv_mov_b32 v83,0\n" \
"M v[180:183],a[48:51],a[144:147],v[228:231]\nv_exp_f32 v95,v95\nM v[180:183],a[52:55],a[148:151],v[180:183]\nv_cvt_pk_bf16_f32 v84,v84,v85\nv_cvt_pk_bf16_f32 v85,v86,v87\nv_cvt_pk_bf16_f32 v86,v92,v93\nM v[180:183],a[56:59],a[152:155],v[180:183]\nv_cvt_pk_bf16_f32 v87,v94,v95\n" \
"ds_read_b128 a[116:119],v241 offset:13312\nM v[180:183],a[60:63],a[156:159],v[180:183]\nds_read_b128 a[120:123],v241 offset:14336\nds_read_b128 a[124:127],v241 offset:15360\nv_max3_f32 v237,v140,v141,v142\nM v[0:3],a[64:67],v[64:67],0\nds_read_b128 a[0:3],v240 offset:49152\nv_exp_f32 v128,v128\n" \
"M v[4:7],a[64:67],v[68:71],0\nds_read_b128 a[4:7],v240 offset:50176\nv_max3_f32 v237,v237,v143,v156\nM v[8:11],a[68:71],v[64:67],0\nbuffer_load_dword v248,v251,s[52:55],s80 offen sc1\nds_read_b128 a[8:11],v240 offset:51200\nM v[12:15],a[68:71],v[68:71],0\nds_read_b128 a[12:15],v240 offset:52224\n" \
"v_exp_f32 v129,v129\nM v[16:19],a[72:75],v[64:67],0\nds_read_b128 a[16:19],v240 offset:53248\nv_cvt_pk_bf16_f32 v192,v128,v129\nM v[20:23],a[72:75],v[68:71],0\nv_exp_f32 v130,v130\nds_read_b128 a[20:23],v240 offset:54272\nM v[24:27],a[76:79],v[64:67],0\n" \
"v_exp_f32 v131,v131\nds_read_b128 a[24:27],v240 offset:55296\nM v[28:31],a[76:79],v[68:71],0\nv_cvt_pk_bf16_f32 v193,v130,v131\nds_read_b128 a[28:31],v240 offset:56320\nM v[32:35],a[80:83],v[64:67],0\nv_exp_f32 v132,v132\nv_max3_f32 v233,v136,v137,v138\n" \
"M v[36:39],a[80:83],v[68:71],0\nv_exp_f32 v133,v133\nM v[40:43],a[84:87],v[64:67],0\nv_exp_f32 v134,v134\nv_cvt_pk_bf16_f32 v196,v132,v133\nM v[44:47],a[84:87],v[68:71],0\nv_exp_f32 v135,v135\nv_max3_f32 v237,v237,v157,v158\n" \
"M v[48:51],a[88:91],v[64:67],0\nv_cvt_pk_bf16_f32 v197,v134,v135\nv_max_f32 v233,v233,v139\nM v[52:55],a[88:91],v[68:71],0\nv_exp_f32 v144,v144\nv_max_f32 v237,v237,v159\nM v[56:59],a[92:95],v[64:67],0\nv_exp_f32 v145,v145\n" \
"v_cndmask_b32 v233,v232,v233,s[90:91]\nM v[60:63],a[92:95],v[68:71],0\nv_exp_f32 v146,v146\nM a[192:195],a[208:211],v[64:67],0\nv_exp_f32 v147,v147\nv_cvt_pk_bf16_f32 v194,v144,v145\nM a[196:199],a[208:211],v[68:71],0\nv_cvt_pk_bf16_f32 v195,v146,v147\n" \
"v_cndmask_b32 v237,v236,v237,s[90:91]\nM v[0:3],a[96:99],v[80:83],v[0:3]\nv_exp_f32 v148,v148\nv_permlane16_swap_b32 v233,v237\nM v[4:7],a[96:99],v[84:87],v[4:7]\nv_max_f32 v233,v233,v237\nv_exp_f32 v149,v149\nM v[8:11],a[100:103],v[80:83],v[8:11]\n" \
"v_exp_f32 v150,v150\nM v[12:15],a[100:103],v[84:87],v[12:15]\nv_exp_f32 v151,v151\nv_mov_b32 v237,v233\nM v[16:19],a[104:107],v[80:83],v[16:19]\nv_cvt_pk_bf16_f32 v198,v148,v149\nv_permlane32_swap_b32 v233,v237\nv_cvt_pk_bf16_f32 v199,v150,v151\n" \
"M v[20:23],a[104:107],v[84:87],v[20:23]\nv_max_f32 v233,v233,v237\nv_mov_b32 v237,v233\nM v[24:27],a[108:111],v[80:83],v[24:27]\nv_cndmask_b32 v136,v136,v252,s[82:83]\nv_permlane16_swap_b32 v233,v237\nv_cndmask_b32 v137,v137,v252,s[84:85]\nM v[28:31],a[108:111],v[84:87],v[28:31]\n" \
"v_cndmask_b32 v138,v138,v252,s[86:87]\nv_cndmask_b32 v139,v139,v252,s[88:89]\ns_waitcnt lgkmcnt(0)\nM v[32:35],a[112:115],v[80:83],v[32:35]\nv_sub_f32 v232,0,v233\nv_sub_f32 v234,0,v233\nM v[36:39],a[112:115],v[84:87],v[36:39]\nv_sub_f32 v235,0,v233\n" \
"v_add_f32 v136,v136,v232\nv_add_f32 v137,v137,v232\nM v[40:43],a[116:119],v[80:83],v[40:43]\nv_add_f32 v138,v138,v232\nv_add_f32 v139,v139,v232\nM v[44:47],a[116:119],v[84:87],v[44:47]\nv_sub_f32 v233,0,v233\nv_exp_f32 v136,v136\n" \
"M v[48:51],a[120:123],v[80:83],v[48:51]\nv_exp_f32 v137,v137\nv_sub_f32 v236,0,v237\nM v[52:55],a[120:123],v[84:87],v[52:55]\nv_exp_f32 v138,v138\nM v[56:59],a[124:127],v[80:83],v[56:59]\nv_exp_f32 v139,v139\nv_cvt_pk_bf16_f32 v200,v136,v137\n" \
"M v[60:63],a[124:127],v[84:87],v[60:63]\nv_cvt_pk_bf16_f32 v201,v138,v139\nv_sub_f32 v238,0,v237\nv_sub_f32 v239,0,v237\nM a[192:195],a[208:211],v[80:83],a[192:195]\nv_add_f32 v140,v140,v236\nv_add_f32 v141,v141,v236\nM a[196:199],a[208:211],v[84:87],a[196:199]\n" \
"v_add_f32 v142,v142,v236\nv_add_f32 v143,v143,v236\nv_sub_f32 v237,0,v237\nM v[128:131],a[0:3],a[128:131],v[224:227]\nv_exp_f32 v140,v140\nds_read_b128 a[32:35],v240 offset:57344\nM v[128:131],a[4:7],a[132:135],v[128:131]\nv_exp_f32 v141,v141\n" \
"M v[128:131],a[8:11],a[136:139],v[128:131]\nv_exp_f32 v142,v142\nv_cvt_pk_bf16_f32 v204,v140,v141\nM v[128:131],a[12:15],a[140:143],v[128:131]\nv_exp_f32 v143,v143\nM v[132:135],a[0:3],a[144:147],v[228:231]\nv_cvt_pk_bf16_f32 v205,v142,v143\nds_read_b128 a[36:39],v240 offset:58368\n" \
"ds_read_b128 a[40:43],v240 offset:59392\nM v[132:135],a[4:7],a[148:151],v[132:135]\nds_read_b128 a[44:47],v240 offset:60416\nv_cndmask_b32 v156,v156,v252,s[82:83]\nv_cndmask_b32 v157,v157,v252,s[84:85]\nM v[132:135],a[8:11],a[152:155],v[132:135]\nv_cndmask_b32 v158,v158,v252,s[86:87]\nv_cndmask_b32 v159,v159,v252,s[88:89]\n" \
"M v[132:135],a[12:15],a[156:159],v[132:135]\nv_add_f32 v156,v156,v236\nv_add_f32 v157,v157,v236\nv_add_f32 v158,v158,v236\nM v[144:147],a[16:19],a[128:131],v[224:227]\nv_add_f32 v159,v159,v236\nv_exp_f32 v156,v156\nM v[144:147],a[20:23],a[132:135],v[144:147]\n" \
"v_exp_f32 v157,v157\nM v[144:147],a[24:27],a[136:139],v[144:147]\nv_exp_f32 v158,v158\nv_cvt_pk_bf16_f32 v206,v156,v157\nM v[144:147],a[28:31],a[140:143],v[144:147]\nv_exp_f32 v159,v159\nM v[148:151],a[16:19],a[144:147],v[228:231]\nv_cvt_pk_bf16_f32 v207,v158,v159\n" \
"ds_read_b128 a[48:51],v240 offset:61440\nds_read_b128 a[52:55],v240 offset:62464\nM v[148:151],a[20:23],a[148:151],v[148:151]\nv_exp_f32 v160,v160\nds_read_b128 a[56:59],v240 offset:63488\nM v[148:151],a[24:27],a[152:155],v[148:151]\nv_exp_f32 v161,v161\nM v[148:151],a[28:31],a[156:159],v[148:151]\n" \
"v_exp_f32 v162,v162\nv_cvt_pk_bf16_f32 v208,v160,v161\nM v[136:139],a[0:3],a[160:163],v[232:235]\ns_waitcnt vmcnt(1)\ns_barrier\nds_read_b128 a[60:63],v240 offset:64512\nM v[136:139],a[4:7],a[164:167],v[136:139]\nv_exp_f32 v163,v163\n" \
"M v[136:139],a[8:11],a[168:171],v[136:139]\nv_cvt_pk_bf16_f32 v209,v162,v163\nv_exp_f32 v164,v164\nM v[136:139],a[12:15],a[172:175],v[136:139]\nv_exp_f32 v165,v165\nds_read_b128 a[64:67],v241 offset:32768\nM v[140:143],a[0:3],a[176:179],v[236:239]\nv_exp_f32 v166,v166\n" \
"M v[140:143],a[4:7],a[180:183],v[140:143]\nv_exp_f32 v167,v167\nv_cvt_pk_bf16_f32 v212,v164,v165\nM v[140:143],a[8:11],a[184:187],v[140:143]\nv_cvt_pk_bf16_f32 v213,v166,v167\nds_read_b128 a[68:71],v241 offset:33792\nM v[140:143],a[12:15],a[188:191],v[140:143]\nv_exp_f32 v176,v176\n" \
"ds_read_b128 a[72:75],v241 offset:34816\nM v[152:155],a[16:19],a[160:163],v[232:235]\nv_exp_f32 v177,v177\nds_read_b128 a[76:79],v241 offset:35840\nM v[152:155],a[20:23],a[164:167],v[152:155]\nv_exp_f32 v178,v178\nM v[152:155],a[24:27],a[168:171],v[152:155]\nv_exp_f32 v179,v179\n" \
"v_cvt_pk_bf16_f32 v210,v176,v177\nM v[152:155],a[28:31],a[172:175],v[152:155]\nv_cvt_pk_bf16_f32 v211,v178,v179\nv_exp_f32 v180,v180\nM v[156:159],a[16:19],a[176:179],v[236:239]\nv_exp_f32 v181,v181\nM v[156:159],a[20:23],a[180:183],v[156:159]\nv_exp_f32 v182,v182\n" \
"v_cvt_pk_bf16_f32 v214,v180,v181\nM v[156:159],a[24:27],a[184:187],v[156:159]\nv_exp_f32 v183,v183\nds_read_b128 a[80:83],v241 offset:36864\nM v[156:159],a[28:31],a[188:191],v[156:159]\nv_cvt_pk_bf16_f32 v215,v182,v183\ns_waitcnt lgkmcnt(3)\nM v[160:163],a[32:35],a[128:131],v[224:227]\n" \
"ds_read_b128 a[84:87],v241 offset:37888\nds_read_b128 a[88:91],v241 offset:38912\ns_waitcnt vmcnt(0)\nv_cmp_eq_u32 vcc,0,v248\nM v[160:163],a[36:39],a[132:135],v[160:163]\nds_read_b128 a[92:95],v241 offset:39936\ns_cbranch_vccnz .Lps3\n.Lpb3:\n" \
"M v[160:163],a[40:43],a[136:139],v[160:163]\nds_read_b128 a[96:99],v241 offset:40960\nv_exp_f32 v128,v128\nM v[160:163],a[44:47],a[140:143],v[160:163]\nds_read_b128 a[100:103],v241 offset:41984\nv_exp_f32 v129,v129\nM v[164:167],a[32:35],a[144:147],v[228:231]\nds_read_b128 a[104:107],v241 offset:43008\n" \
"s_add_u32 m0,s64,65536\nM v[164:167],a[36:39],a[148:151],v[164:167]\nds_read_b128 a[108:111],v241 offset:44032\nbuffer_load_dwordx4 v240,s[48:51],s36 offen offset:0 sc1 lds\nM v[164:167],a[40:43],a[152:155],v[164:167]\nds_read_b128 a[112:115],v241 offset:45056\nv_exp_f32 v130,v130\nM v[164:167],a[44:47],a[156:159],v[164:167]\n" \
"v_exp_f32 v131,v131\nM v[176:179],a[48:51],a[128:131],v[224:227]\nds_read_b128 a[116:119],v241 offset:46080\nv_exp_f32 v132,v132\nM v[176:179],a[52:55],a[132:135],v[176:179]\nds_read_b128 a[120:123],v241 offset:47104\nds_read_b128 a[124:127],v241 offset:48128\nM v[176:179],a[56:59],a[136:139],v[176:179]\n" \
"ds_read_b128 a[0:3],v240 offset:0\nbuffer_load_dwordx4 v240,s[48:51],s36 offen offset:1024 sc1 lds\nM v[176:179],a[60:63],a[140:143],v[176:179]\nds_read_b128 a[4:7],v240 offset:1024\nv_exp_f32 v133,v133\nM v[180:183],a[48:51],a[144:147],v[228:231]\nv_exp_f32 v134,v134\nM v[180:183],a[52:55],a[148:151],v[180:183]\n" \
"ds_read_b128 a[8:11],v240 offset:2048\nv_exp_f32 v135,v135\nM v[180:183],a[56:59],a[152:155],v[180:183]\nds_read_b128 a[12:15],v240 offset:3072\nbuffer_load_dwordx4 v240,s[48:51],s36 offen offset:2048 sc1 lds\nM v[180:183],a[60:63],a[156:159],v[180:183]\nds_read_b128 a[16:19],v240 offset:4096\nds_read_b128 a[20:23],v240 offset:5120\n" \
"M v[168:171],a[32:35],a[160:163],v[232:235]\nds_read_b128 a[24:27],v240 offset:6144\nv_exp_f32 v144,v144\nM v[168:171],a[36:39],a[164:167],v[168:171]\nds_read_b128 a[28:31],v240 offset:7168\nv_exp_f32 v145,v145\nM v[168:171],a[40:43],a[168:171],v[168:171]\nbuffer_load_dwordx4 v240,s[48:51],s36 offen offset:3072 sc1 lds\n" \
"M v[168:171],a[44:47],a[172:175],v[168:171]\ns_add_u32 m0,s64,16384\nv_exp_f32 v146,v146\nM v[172:175],a[32:35],a[176:179],v[236:239]\nv_exp_f32 v147,v147\nM v[172:175],a[36:39],a[180:183],v[172:175]\nds_read_b128 a[32:35],v240 offset:8192\nv_exp_f32 v148,v148\n" \
"M v[172:175],a[40:43],a[184:187],v[172:175]\nds_read_b128 a[36:39],v240 offset:9216\nbuffer_load_dwordx4 v240,s[44:47],s65 offen offset:0 sc1 lds\nM v[172:175],a[44:47],a[188:191],v[172:175]\nds_read_b128 a[40:43],v240 offset:10240\nds_read_b128 a[44:47],v240 offset:11264\nM v[184:187],a[48:51],a[160:163],v[232:235]\nv_exp_f32 v149,v149\n" \
"M v[184:187],a[52:55],a[164:167],v[184:187]\nv_exp_f32 v150,v150\nM v[184:187],a[56:59],a[168:171],v[184:187]\nbuffer_load_dwordx4 v240,s[44:47],s65 offen offset:1024 sc1 lds\nM v[184:187],a[60:63],a[172:175],v[184:187]\nv_exp_f32 v151,v151\nM v[188:191],a[48:51],a[176:179],v[236:239]\nv_exp_f32 v136,v136\n" \
"M v[188:191],a[52:55],a[180:183],v[188:191]\nds_read_b128 a[48:51],v240 offset:12288\nv_exp_f32 v137,v137\nM v[188:191],a[56:59],a[184:187],v[188:191]\nbuffer_load_dwordx4 v240,s[44:47],s65 offen offset:2048 sc1 lds\nds_read_b128 a[52:55],v240 offset:13312\nM v[188:191],a[60:63],a[188:191],v[188:191]\nds_read_b128 a[56:59],v240 offset:14336\n" \
"ds_read_b128 a[60:63],v240 offset:15360\nM v[0:3],a[64:67],v[192:195],v[0:3]\nv_exp_f32 v138,v138\nM v[4:7],a[64:67],v[196:199],v[4:7]\nv_exp_f32 v139,v139\nM v[64:67],a[64:67],v[200:203],0\nbuffer_load_dwordx4 v240,s[44:47],s65 offen offset:3072 sc1 lds\nM v[68:71],a[64:67],v[204:207],0\n" \
"s_add_u32 m0,s64,81920\nv_exp_f32 v140,v140\nM v[8:11],a[68:71],v[192:195],v[8:11]\nv_exp_f32 v141,v141\nM v[12:15],a[68:71],v[196:199],v[12:15]\nv_exp_f32 v142,v142\nM v[72:75],a[68:71],v[200:203],0\nbuffer_load_dwordx4 v240,s[48:51],s65 offen offset:0 sc1 lds\n" \
"M v[76:79],a[68:71],v[204:207],0\ns_waitcnt lgkmcnt(3)\nM v[16:19],a[72:75],v[192:195],v[16:19]\nv_exp_f32 v143,v143\nM v[20:23],a[72:75],v[196:199],v[20:23]\nv_exp_f32 v152,v152\nM v[80:83],a[72:75],v[200:203],0\nbuffer_load_dwordx4 v240,s[48:51],s65 offen offset:1024 sc1 lds\n" \
"M v[84:87],a[72:75],v[204:207],0\nv_exp_f32 v153,v153\nM v[24:27],a[76:79],v[192:195],v[24:27]\nv_exp_f32 v154,v154\nM v[28:31],a[76:79],v[196:199],v[28:31]\nv_exp_f32 v155,v155\nM v[88:91],a[76:79],v[200:203],0\nbuffer_load_dwordx4 v240,s[48:51],s65 offen offset:2048 sc1 lds\n" \
"M v[92:95],a[76:79],v[204:207],0\nv_exp_f32 v156,v156\nM v[32:35],a[80:83],v[192:195],v[32:35]\nv_exp_f32 v157,v157\nM v[36:39],a[80:83],v[196:199],v[36:39]\nv_exp_f32 v158,v158\nM v[96:99],a[80:83],v[200:203],0\nbuffer_load_dwordx4 v240,s[48:51],s65 offen offset:3072 sc1 lds\n" \
"M v[100:103],a[80:83],v[204:207],0\ns_add_u32 m0,s64,32768\nv_exp_f32 v159,v159\nM v[40:43],a[84:87],v[192:195],v[40:43]\nv_exp_f32 v160,v160\nM v[44:47],a[84:87],v[196:199],v[44:47]\nv_exp_f32 v161,v161\nM v[104:107],a[84:87],v[200:203],0\n" \
"buffer_load_dwordx4 v240,s[44:47],s66 offen offset:0 sc1 lds\nM v[108:111],a[84:87],v[204:207],0\nv_exp_f32 v162,v162\nM v[48:51],a[88:91],v[192:195],v[48:51]\ns_barrier\nM v[52:55],a[88:91],v[196:199],v[52:55]\nv_exp_f32 v163,v163\nM v[112:115],a[88:91],v[200:203],0\n" \
"buffer_load_dwordx4 v240,s[44:47],s66 offen offset:1024 sc1 lds\nM v[116:119],a[88:91],v[204:207],0\nv_exp_f32 v164,v164\nM v[56:59],a[92:95],v[192:195],v[56:59]\nv_exp_f32 v165,v165\nM v[60:63],a[92:95],v[196:199],v[60:63]\nv_exp_f32 v166,v166\nM v[120:123],a[92:95],v[200:203],0\n" \
"buffer_load_dwordx4 v240,s[44:47],s66 offen offset:2048 sc1 lds\nM v[124:127],a[92:95],v[204:207],0\nv_exp_f32 v167,v167\nM a[192:195],a[208:211],v[192:195],a[192:195]\nv_exp_f32 v176,v176\nM a[196:199],a[208:211],v[196:199],a[196:199]\nv_exp_f32 v177,v177\nv_cvt_pk_bf16_f32 v192,v128,v129\n" \
"M a[200:203],a[208:211],v[200:203],0\nbuffer_load_dwordx4 v240,s[44:47],s66 offen offset:3072 sc1 lds\ns_add_u32 m0,s64,98304\nM a[204:207],a[208:211],v[204:207],0\nv_exp_f32 v178,v178\nM v[0:3],a[96:99],v[208:211],v[0:3]\nv_exp_f32 v179,v179\nv_cvt_pk_bf16_f32 v193,v130,v131\n" \
"M v[4:7],a[96:99],v[212:215],v[4:7]\nv_exp_f32 v180,v180\nM v[8:11],a[100:103],v[208:211],v[8:11]\nv_exp_f32 v181,v181\nv_cvt_pk_bf16_f32 v194,v144,v145\nM v[12:15],a[100:103],v[212:215],v[12:15]\nv_exp_f32 v182,v182\nv_cvt_pk_bf16_f32 v195,v146,v147\n" \
"M v[16:19],a[104:107],v[208:211],v[16:19]\nv_exp_f32 v183,v183\nM v[20:23],a[104:107],v[212:215],v[20:23]\nv_exp_f32 v168,v168\nv_cvt_pk_bf16_f32 v196,v132,v133\nM v[24:27],a[108:111],v[208:211],v[24:27]\nv_exp_f32 v169,v169\nv_cvt_pk_bf16_f32 v197,v134,v135\n" \
"M v[28:31],a[108:111],v[212:215],v[28:31]\nv_exp_f32 v170,v170\nM v[32:35],a[112:115],v[208:211],v[32:35]\nv_exp_f32 v171,v171\nv_cvt_pk_bf16_f32 v198,v148,v149\nM v[36:39],a[112:115],v[212:215],v[36:39]\nv_cvt_pk_bf16_f32 v199,v150,v151\nv_cvt_pk_bf16_f32 v200,v136,v137\n" \
"v_cvt_pk_bf16_f32 v201,v138,v139\nM v[40:43],a[116:119],v[208:211],v[40:43]\nv_cvt_pk_bf16_f32 v202,v152,v153\nv_cvt_pk_bf16_f32 v203,v154,v155\nM v[44:47],a[116:119],v[212:215],v[44:47]\nv_cvt_pk_bf16_f32 v204,v140,v141\nv_cvt_pk_bf16_f32 v205,v142,v143\nv_cvt_pk_bf16_f32 v206,v156,v157\n" \
"M v[48:51],a[120:123],v[208:211],v[48:51]\nbuffer_load_dwordx4 v240,s[48:51],s66 offen offset:0 sc1 lds\nM v[52:55],a[120:123],v[212:215],v[52:55]\nv_cvt_pk_bf16_f32 v207,v158,v159\nM v[56:59],a[124:127],v[208:211],v[56:59]\nM v[60:63],a[124:127],v[212:215],v[60:63]\nM a[192:195],a[208:211],v[208:211],a[192:195]\nbuffer_load_dwordx4 v240,s[48:51],s66 offen offset:1024 sc1 lds\n" \
"buffer_load_dwordx4 v240,s[48:51],s66 offen offset:2048 sc1 lds\nbuffer_load_dwordx4 v240,s[48:51],s66 offen offset:3072 sc1 lds\nM a[196:199],a[208:211],v[212:215],a[196:199]\ns_cmp_lt_u32 s62,2\ns_cbranch_scc1 .Lx01\ns_branch .Ldual\n.Lp23:\ns_cmp_eq_u32 s7,2\n" \
"s_cbranch_scc0 .Lpro3\n.Lpro2:\ns_barrier\nM v[64:67],a[0:3],a[128:131],0\nds_read_b128 a[64:67],v241 offset:0\nds_read_b128 a[68:71],v241 offset:1024\nM v[64:67],a[4:7],a[132:135],v[64:67]\nds_read_b128 a[72:75],v241 offset:2048\n" \
"s_mov_b32 m0,s64\nv_mov_b32 v184,0xff800000\nM v[64:67],a[8:11],a[136:139],v[64:67]\nds_read_b128 a[76:79],v241 offset:3072\nv_mov_b32 v185,0xff800000\nM v[64:67],a[12:15],a[140:143],v[64:67]\nds_read_b128 a[80:83],v241 offset:4096\nbuffer_load_dwordx4 v240,s[44:47],s36 offen offset:0 sc1 lds\n" \
"M v[68:71],a[0:3],a[144:147],0\nds_read_b128 a[0:3],v240 offset:16384\nv_mov_b32 v186,0xff800000\nv_mov_b32 v187,0xff800000\nM v[68:71],a[4:7],a[148:151],v[68:71]\nds_read_b128 a[4:7],v240 offset:17408\nv_max3_f32 v225,v64,v65,v66\nM v[68:71],a[8:11],a[152:155],v[68:71]\n" \
"ds_read_b128 a[8:11],v240 offset:18432\nM v[68:71],a[12:15],a[156:159],v[68:71]\nbuffer_load_dwordx4 v240,s[44:47],s36 offen offset:1024 sc1 lds\nM v[72:75],a[16:19],a[128:131],0\nds_read_b128 a[12:15],v240 offset:19456\nds_read_b128 a[84:87],v241 offset:5120\nM v[72:75],a[20:23],a[132:135],v[72:75]\nds_read_b128 a[88:91],v241 offset:6144\n" \
"M v[72:75],a[24:27],a[136:139],v[72:75]\nds_read_b128 a[92:95],v241 offset:7168\nv_max3_f32 v229,v68,v69,v70\nM v[72:75],a[28:31],a[140:143],v[72:75]\nds_read_b128 a[96:99],v241 offset:8192\nbuffer_load_dwordx4 v240,s[44:47],s36 offen offset:2048 sc1 lds\nM v[76:79],a[16:19],a[144:147],0\nds_read_b128 a[16:19],v240 offset:20480\n" \
"M v[76:79],a[20:23],a[148:151],v[76:79]\nds_read_b128 a[20:23],v240 offset:21504\nM v[76:79],a[24:27],a[152:155],v[76:79]\nds_read_b128 a[24:27],v240 offset:22528\nv_max3_f32 v225,v225,v67,v72\nv_max3_f32 v225,v225,v73,v74\nM v[76:79],a[28:31],a[156:159],v[76:79]\nv_max_f32 v225,v225,v75\n" \
"v_cndmask_b32 v225,v224,v225,s[90:91]\nM v[80:83],a[32:35],a[128:131],0\nds_read_b128 a[28:31],v240 offset:23552\nbuffer_load_dwordx4 v240,s[44:47],s36 offen offset:3072 sc1 lds\nM v[80:83],a[36:39],a[132:135],v[80:83]\nds_read_b128 a[100:103],v241 offset:9216\nds_read_b128 a[104:107],v241 offset:10240\nM v[80:83],a[40:43],a[136:139],v[80:83]\n" \
"v_max3_f32 v229,v229,v71,v76\nv_max3_f32 v229,v229,v77,v78\nv_max_f32 v229,v229,v79\nM v[80:83],a[44:47],a[140:143],v[80:83]\nv_cndmask_b32 v229,v228,v229,s[90:91]\nds_read_b128 a[108:111],v241 offset:11264\nds_read_b128 a[112:115],v241 offset:12288\nM v[84:87],a[32:35],a[144:147],0\n" \
"v_permlane16_swap_b32 v225,v229\nv_max_f32 v225,v225,v229\nM v[84:87],a[36:39],a[148:151],v[84:87]\nv_mov_b32 v229,v225\nds_read_b128 a[116:119],v241 offset:13312\nds_read_b128 a[120:123],v241 offset:14336\nM v[84:87],a[40:43],a[152:155],v[84:87]\nv_permlane32_swap_b32 v225,v229\n" \
"v_max_f32 v225,v225,v229\nM v[84:87],a[44:47],a[156:159],v[84:87]\nv_mov_b32 v229,v225\nds_read_b128 a[124:127],v241 offset:15360\nds_read_b128 a[32:35],v240 offset:40960\nM v[88:91],a[48:51],a[128:131],0\nv_permlane16_swap_b32 v225,v229\nv_sub_f32 v224,0,v225\n" \
"v_sub_f32 v226,0,v225\nM v[88:91],a[52:55],a[132:135],v[88:91]\nv_sub_f32 v227,0,v225\nv_sub_f32 v225,0,v225\nM v[88:91],a[56:59],a[136:139],v[88:91]\nv_sub_f32 v228,0,v229\nv_sub_f32 v230,0,v229\nv_sub_f32 v231,0,v229\n" \
"M v[88:91],a[60:63],a[140:143],v[88:91]\nv_sub_f32 v229,0,v229\nv_add_f32 v64,v64,v224\nM v[92:95],a[48:51],a[144:147],0\nv_add_f32 v65,v65,v224\nv_add_f32 v66,v66,v224\nv_add_f32 v67,v67,v224\nM v[92:95],a[52:55],a[148:151],v[92:95]\n" \
"v_exp_f32 v64,v64\nv_add_f32 v68,v68,v228\nM v[92:95],a[56:59],a[152:155],v[92:95]\nv_exp_f32 v65,v65\nM v[92:95],a[60:63],a[156:159],v[92:95]\nv_exp_f32 v66,v66\ns_waitcnt lgkmcnt(2)\nM v[96:99],a[0:3],a[128:131],v[224:227]\n" \
"v_exp_f32 v67,v67\nv_add_f32 v69,v69,v228\nM v[96:99],a[4:7],a[132:135],v[96:99]\nv_add_f32 v72,v72,v224\nv_add_f32 v73,v73,v224\nM v[96:99],a[8:11],a[136:139],v[96:99]\nv_add_f32 v74,v74,v224\nv_add_f32 v75,v75,v224\n" \
"v_add_f32 v70,v70,v228\nM v[96:99],a[12:15],a[140:143],v[96:99]\nv_add_f32 v71,v71,v228\nv_cvt_pk_bf16_f32 v64,v64,v65\nM v[100:103],a[0:3],a[144:147],v[228:231]\nv_exp_f32 v68,v68\nv_add_f32 v76,v76,v228\nM v[100:103],a[4:7],a[148:151],v[100:103]\n" \
"v_exp_f32 v69,v69\nv_add_f32 v77,v77,v228\nM v[100:103],a[8:11],a[152:155],v[100:103]\nv_exp_f32 v72,v72\nM v[100:103],a[12:15],a[156:159],v[100:103]\nv_exp_f32 v73,v73\nv_add_f32 v78,v78,v228\nM v[108:111],a[16:19],a[144:147],v[228:231]\n" \
"v_exp_f32 v74,v74\nM v[108:111],a[20:23],a[148:151],v[108:111]\nv_exp_f32 v75,v75\nv_add_f32 v79,v79,v228\nM v[108:111],a[24:27],a[152:155],v[108:111]\nv_exp_f32 v70,v70\nv_exp_f32 v71,v71\nv_cvt_pk_bf16_f32 v65,v66,v67\n" \
"v_exp_f32 v76,v76\nv_exp_f32 v77,v77\nv_exp_f32 v78,v78\nv_exp_f32 v79,v79\nv_cvt_pk_bf16_f32 v66,v72,v73\nv_cvt_pk_bf16_f32 v67,v74,v75\nv_cvt_pk_bf16_f32 v68,v68,v69\nM v[108:111],a[28:31],a[156:159],v[108:111]\n" \
"v_cvt_pk_bf16_f32 v69,v70,v71\nv_cvt_pk_bf16_f32 v70,v76,v77\nv_cvt_pk_bf16_f32 v71,v78,v79\nM v[0:3],a[64:67],v[64:67],0\nv_add_f32 v80,v80,v224\nv_add_f32 v81,v81,v224\nv_add_f32 v82,v82,v224\nM v[4:7],a[64:67],v[68:71],0\n" \
"v_add_f32 v83,v83,v224\nv_add_f32 v84,v84,v228\nM v[8:11],a[68:71],v[64:67],0\nv_exp_f32 v80,v80\nv_add_f32 v85,v85,v228\nM v[12:15],a[68:71],v[68:71],0\nv_exp_f32 v81,v81\nv_add_f32 v88,v88,v224\n" \
"M v[16:19],a[72:75],v[64:67],0\nv_exp_f32 v82,v82\nM v[20:23],a[72:75],v[68:71],0\nv_exp_f32 v83,v83\nv_add_f32 v89,v89,v224\nM v[24:27],a[76:79],v[64:67],0\nv_add_f32 v90,v90,v224\nv_add_f32 v91,v91,v224\n" \
"M v[28:31],a[76:79],v[68:71],0\nv_add_f32 v86,v86,v228\nv_add_f32 v87,v87,v228\nv_cvt_pk_bf16_f32 v80,v80,v81\nM v[32:35],a[80:83],v[64:67],0\nv_exp_f32 v84,v84\nv_add_f32 v92,v92,v228\nM v[36:39],a[80:83],v[68:71],0\n" \
"v_exp_f32 v85,v85\nM v[40:43],a[84:87],v[64:67],0\nv_exp_f32 v88,v88\nv_add_f32 v93,v93,v228\nM v[44:47],a[84:87],v[68:71],0\nv_exp_f32 v89,v89\nM v[48:51],a[88:91],v[64:67],0\nv_exp_f32 v90,v90\n" \
"v_add_f32 v94,v94,v228\nM v[52:55],a[88:91],v[68:71],0\nv_exp_f32 v91,v91\nv_add_f32 v95,v95,v228\nM v[56:59],a[92:95],v[64:67],0\nv_exp_f32 v86,v86\nM v[60:63],a[92:95],v[68:71],0\nv_exp_f32 v87,v87\n" \
"v_cvt_pk_bf16_f32 v81,v82,v83\nM a[192:195],a[208:211],v[64:67],0\nv_exp_f32 v92,v92\nv_exp_f32 v93,v93\nv_exp_f32 v94,v94\nv_exp_f32 v95,v95\nv_cvt_pk_bf16_f32 v82,v88,v89\nv_cvt_pk_bf16_f32 v83,v90,v91\n" \
"v_cvt_pk_bf16_f32 v84,v84,v85\nM a[196:199],a[208:211],v[68:71],0\nv_cvt_pk_bf16_f32 v85,v86,v87\nv_cvt_pk_bf16_f32 v86,v92,v93\nv_cvt_pk_bf16_f32 v87,v94,v95\nM v[0:3],a[96:99],v[80:83],v[0:3]\nds_read_b128 a[0:3],v240 offset:32768\nds_read_b128 a[4:7],v240 offset:33792\n" \
"v_cndmask_b32 v96,v96,v252,s[82:83]\nM v[4:7],a[96:99],v[84:87],v[4:7]\nds_read_b128 a[8:11],v240 offset:34816\nv_cndmask_b32 v97,v97,v252,s[84:85]\nM v[8:11],a[100:103],v[80:83],v[8:11]\nds_read_b128 a[12:15],v240 offset:35840\nv_cndmask_b32 v98,v98,v252,s[86:87]\nv_cndmask_b32 v99,v99,v252,s[88:89]\n" \
"M v[12:15],a[100:103],v[84:87],v[12:15]\nds_read_b128 a[16:19],v240 offset:36864\nv_exp_f32 v96,v96\nM v[16:19],a[104:107],v[80:83],v[16:19]\nds_read_b128 a[20:23],v240 offset:37888\nv_cndmask_b32 v108,v108,v252,s[82:83]\nM v[20:23],a[104:107],v[84:87],v[20:23]\nds_read_b128 a[24:27],v240 offset:38912\n" \
"v_exp_f32 v97,v97\nM v[24:27],a[108:111],v[80:83],v[24:27]\nds_read_b128 a[28:31],v240 offset:39936\nv_cvt_pk_bf16_f32 v96,v96,v97\nM v[28:31],a[108:111],v[84:87],v[28:31]\nds_read_b128 a[36:39],v240 offset:41984\nv_exp_f32 v98,v98\nM v[32:35],a[112:115],v[80:83],v[32:35]\n" \
"ds_read_b128 a[40:43],v240 offset:43008\nv_exp_f32 v99,v99\nM v[36:39],a[112:115],v[84:87],v[36:39]\nds_read_b128 a[44:47],v240 offset:44032\nv_cndmask_b32 v109,v109,v252,s[84:85]\nM v[40:43],a[116:119],v[80:83],v[40:43]\nds_read_b128 a[48:51],v240 offset:45056\nv_exp_f32 v100,v100\n" \
"M v[44:47],a[116:119],v[84:87],v[44:47]\nds_read_b128 a[52:55],v240 offset:46080\nv_cndmask_b32 v110,v110,v252,s[86:87]\nM v[48:51],a[120:123],v[80:83],v[48:51]\nbuffer_load_dword v248,v251,s[52:55],s80 offen sc1\nds_read_b128 a[56:59],v240 offset:47104\nM v[52:55],a[120:123],v[84:87],v[52:55]\nds_read_b128 a[60:63],v240 offset:48128\n" \
"v_cndmask_b32 v111,v111,v252,s[88:89]\ns_waitcnt lgkmcnt(9)\nM v[56:59],a[124:127],v[80:83],v[56:59]\nds_read_b128 a[64:67],v241 offset:16384\nv_cvt_pk_bf16_f32 v97,v98,v99\nM v[60:63],a[124:127],v[84:87],v[60:63]\nds_read_b128 a[68:71],v241 offset:17408\nv_exp_f32 v101,v101\n" \
"M a[192:195],a[208:211],v[80:83],a[192:195]\nds_read_b128 a[72:75],v241 offset:18432\nv_mov_b32 v98,0\nM a[196:199],a[208:211],v[84:87],a[196:199]\nds_read_b128 a[76:79],v241 offset:19456\nv_exp_f32 v102,v102\nM v[128:131],a[0:3],a[128:131],v[224:227]\nds_read_b128 a[80:83],v241 offset:20480\n" \
"v_exp_f32 v103,v103\nM v[128:131],a[4:7],a[132:135],v[128:131]\nv_exp_f32 v108,v108\nM v[128:131],a[8:11],a[136:139],v[128:131]\nv_exp_f32 v109,v109\nv_mov_b32 v99,0\nM v[128:131],a[12:15],a[140:143],v[128:131]\nv_exp_f32 v110,v110\n" \
"M v[132:135],a[0:3],a[144:147],v[228:231]\nv_exp_f32 v111,v111\nv_cvt_pk_bf16_f32 v100,v100,v101\nM v[132:135],a[4:7],a[148:151],v[132:135]\nv_cvt_pk_bf16_f32 v101,v102,v103\nv_cvt_pk_bf16_f32 v102,v108,v109\nv_cvt_pk_bf16_f32 v103,v110,v111\nM v[132:135],a[8:11],a[152:155],v[132:135]\n" \
"ds_read_b128 a[84:87],v241 offset:21504\nds_read_b128 a[88:91],v241 offset:22528\nM v[132:135],a[12:15],a[156:159],v[132:135]\nds_read_b128 a[92:95],v241 offset:23552\nv_exp_f32 v128,v128\nM v[144:147],a[16:19],a[128:131],v[224:227]\nds_read_b128 a[0:3],v240 offset:49152\nv_exp_f32 v129,v129\n" \
"M v[144:147],a[20:23],a[132:135],v[144:147]\nds_read_b128 a[4:7],v240 offset:50176\ns_waitcnt lgkmcnt(5)\nM v[144:147],a[24:27],a[136:139],v[144:147]\nds_read_b128 a[8:11],v240 offset:51200\nv_exp_f32 v130,v130\nM v[144:147],a[28:31],a[140:143],v[144:147]\nds_read_b128 a[12:15],v240 offset:52224\n" \
"v_cvt_pk_bf16_f32 v192,v128,v129\nM v[148:151],a[16:19],a[144:147],v[228:231]\nds_read_b128 a[16:19],v240 offset:53248\nv_exp_f32 v131,v131\nM v[148:151],a[20:23],a[148:151],v[148:151]\nds_read_b128 a[20:23],v240 offset:54272\nv_exp_f32 v132,v132\nM v[148:151],a[24:27],a[152:155],v[148:151]\n" \
"ds_read_b128 a[24:27],v240 offset:55296\nv_cvt_pk_bf16_f32 v193,v130,v131\nM v[148:151],a[28:31],a[156:159],v[148:151]\nds_read_b128 a[28:31],v240 offset:56320\nv_exp_f32 v133,v133\nM v[160:163],a[32:35],a[128:131],v[224:227]\nv_exp_f32 v134,v134\nM v[160:163],a[36:39],a[132:135],v[160:163]\n" \
"v_exp_f32 v135,v135\nv_cvt_pk_bf16_f32 v196,v132,v133\nM v[160:163],a[40:43],a[136:139],v[160:163]\nv_cvt_pk_bf16_f32 v197,v134,v135\nv_exp_f32 v144,v144\nM v[160:163],a[44:47],a[140:143],v[160:163]\nv_exp_f32 v145,v145\nM v[164:167],a[32:35],a[144:147],v[228:231]\n" \
"ds_read_b128 a[32:35],v240 offset:57344\nv_exp_f32 v146,v146\nM v[164:167],a[36:39],a[148:151],v[164:167]\nv_exp_f32 v147,v147\nM v[164:167],a[40:43],a[152:155],v[164:167]\nds_read_b128 a[36:39],v240 offset:58368\nv_cvt_pk_bf16_f32 v194,v144,v145\nv_cvt_pk_bf16_f32 v195,v146,v147\n" \
"M v[164:167],a[44:47],a[156:159],v[164:167]\nds_read_b128 a[40:43],v240 offset:59392\nv_exp_f32 v148,v148\nM v[176:179],a[48:51],a[128:131],v[224:227]\nds_read_b128 a[44:47],v240 offset:60416\nds_read_b128 a[96:99],v241 offset:40960\nM v[176:179],a[52:55],a[132:135],v[176:179]\nv_exp_f32 v149,v149\n" \
"ds_read_b128 a[100:103],v241 offset:41984\nM v[176:179],a[56:59],a[136:139],v[176:179]\nv_exp_f32 v150,v150\nM v[176:179],a[60:63],a[140:143],v[176:179]\nv_exp_f32 v151,v151\nv_cvt_pk_bf16_f32 v198,v148,v149\nM v[180:183],a[48:51],a[144:147],v[228:231]\nv_cvt_pk_bf16_f32 v199,v150,v151\n" \
"ds_read_b128 a[48:51],v240 offset:61440\nds_read_b128 a[104:107],v241 offset:43008\nM v[180:183],a[52:55],a[148:151],v[180:183]\nv_exp_f32 v160,v160\nM v[180:183],a[56:59],a[152:155],v[180:183]\nv_exp_f32 v161,v161\nds_read_b128 a[52:55],v240 offset:62464\nM v[180:183],a[60:63],a[156:159],v[180:183]\n" \
"v_exp_f32 v162,v162\nM v[0:3],a[64:67],v[96:99],v[0:3]\nv_exp_f32 v163,v163\nv_cvt_pk_bf16_f32 v208,v160,v161\nM v[4:7],a[64:67],v[100:103],v[4:7]\nv_cvt_pk_bf16_f32 v209,v162,v163\nds_read_b128 a[56:59],v240 offset:63488\nds_read_b128 a[60:63],v240 offset:64512\n" \
"M v[8:11],a[68:71],v[96:99],v[8:11]\nv_exp_f32 v164,v164\nM v[12:15],a[68:71],v[100:103],v[12:15]\nv_exp_f32 v165,v165\nds_read_b128 a[64:67],v241 offset:32768\nM v[16:19],a[72:75],v[96:99],v[16:19]\nv_exp_f32 v166,v166\nv_cvt_pk_bf16_f32 v212,v164,v165\n" \
"M v[20:23],a[72:75],v[100:103],v[20:23]\nv_exp_f32 v167,v167\nM v[24:27],a[76:79],v[96:99],v[24:27]\nv_cvt_pk_bf16_f32 v213,v166,v167\nv_exp_f32 v176,v176\nM v[28:31],a[76:79],v[100:103],v[28:31]\nv_exp_f32 v177,v177\nM v[32:35],a[80:83],v[96:99],v[32:35]\n" \
"v_exp_f32 v178,v178\nv_cvt_pk_bf16_f32 v210,v176,v177\nM v[36:39],a[80:83],v[100:103],v[36:39]\nv_exp_f32 v179,v179\ns_waitcnt lgkmcnt(3)\nM v[40:43],a[84:87],v[96:99],v[40:43]\nv_cvt_pk_bf16_f32 v211,v178,v179\nds_read_b128 a[68:71],v241 offset:33792\n" \
"M v[44:47],a[84:87],v[100:103],v[44:47]\nv_exp_f32 v180,v180\nds_read_b128 a[72:75],v241 offset:34816\nM v[48:51],a[88:91],v[96:99],v[48:51]\ns_waitcnt vmcnt(1)\ns_barrier\nM v[52:55],a[88:91],v[100:103],v[52:55]\nv_exp_f32 v181,v181\n" \
"ds_read_b128 a[76:79],v241 offset:35840\nM v[56:59],a[92:95],v[96:99],v[56:59]\nv_exp_f32 v182,v182\nv_cvt_pk_bf16_f32 v214,v180,v181\nM v[60:63],a[92:95],v[100:103],v[60:63]\nv_exp_f32 v183,v183\nM a[192:195],a[208:211],v[96:99],a[192:195]\nv_cvt_pk_bf16_f32 v215,v182,v183\n" \
"ds_read_b128 a[80:83],v241 offset:36864\nds_read_b128 a[84:87],v241 offset:37888\nM a[196:199],a[208:211],v[100:103],a[196:199]\nds_read_b128 a[88:91],v241 offset:38912\ns_waitcnt vmcnt(0)\nv_cmp_eq_u32 vcc,0,v248\nM v[64:67],a[244:247],a[244:247],0\nds_read_b128 a[92:95],v241 offset:39936\n" \
"s_cbranch_vccnz .Lps4\n.Lpb4:\ns_add_u32 m0,s64,65536\nM v[68:71],a[244:247],a[244:247],0\nds_read_b128 a[108:111],v241 offset:44032\nbuffer_load_dwordx4 v240,s[48:51],s36 offen offset:0 sc1 lds\nM v[72:75],a[244:247],a[244:247],0\nds_read_b128 a[112:115],v241 offset:45056\n" \
"M v[76:79],a[244:247],a[244:247],0\nds_read_b128 a[116:119],v241 offset:46080\nM v[80:83],a[244:247],a[244:247],0\nds_read_b128 a[120:123],v241 offset:47104\nM v[84:87],a[244:247],a[244:247],0\nds_read_b128 a[124:127],v241 offset:48128\nbuffer_load_dwordx4 v240,s[48:51],s36 offen offset:1024 sc1 lds\nM v[88:91],a[244:247],a[244:247],0\n" \
"M v[92:95],a[244:247],a[244:247],0\nM v[96:99],a[244:247],a[244:247],0\nM v[100:103],a[244:247],a[244:247],0\nbuffer_load_dwordx4 v240,s[48:51],s36 offen offset:2048 sc1 lds\nM v[104:107],a[244:247],a[244:247],0\nM v[108:111],a[244:247],a[244:247],0\nM v[112:115],a[244:247],a[244:247],0\nM v[116:119],a[244:247],a[244:247],0\n" \
"buffer_load_dwordx4 v240,s[48:51],s36 offen offset:3072 sc1 lds\ns_add_u32 m0,s64,16384\nM v[120:123],a[244:247],a[244:247],0\nM v[124:127],a[244:247],a[244:247],0\nM a[200:203],a[244:247],a[244:247],0\nM a[204:207],a[244:247],a[244:247],0\nbuffer_load_dwordx4 v240,s[44:47],s65 offen offset:0 sc1 lds\nM v[136:139],a[0:3],a[160:163],0\n" \
"M v[136:139],a[4:7],a[164:167],v[136:139]\nM v[136:139],a[8:11],a[168:171],v[136:139]\nM v[136:139],a[12:15],a[172:175],v[136:139]\nbuffer_load_dwordx4 v240,s[44:47],s65 offen offset:1024 sc1 lds\nM v[140:143],a[0:3],a[176:179],0\nM v[140:143],a[4:7],a[180:183],v[140:143]\nM v[140:143],a[8:11],a[184:187],v[140:143]\nM v[140:143],a[12:15],a[188:191],v[140:143]\n" \
"buffer_load_dwordx4 v240,s[44:47],s65 offen offset:2048 sc1 lds\nM v[152:155],a[16:19],a[160:163],0\nM v[152:155],a[20:23],a[164:167],v[152:155]\nv_max3_f32 v233,v136,v137,v138\nM v[152:155],a[24:27],a[168:171],v[152:155]\nM v[152:155],a[28:31],a[172:175],v[152:155]\nbuffer_load_dwordx4 v240,s[44:47],s65 offen offset:3072 sc1 lds\ns_add_u32 m0,s64,81920\n" \
"M v[156:159],a[16:19],a[176:179],0\nv_max3_f32 v237,v140,v141,v142\nM v[156:159],a[20:23],a[180:183],v[156:159]\nM v[156:159],a[24:27],a[184:187],v[156:159]\nM v[156:159],a[28:31],a[188:191],v[156:159]\nbuffer_load_dwordx4 v240,s[48:51],s65 offen offset:0 sc1 lds\nv_max3_f32 v233,v233,v139,v152\nM v[128:131],a[0:3],a[128:131],v[224:227]\n" \
"v_max3_f32 v233,v233,v153,v154\nv_max_f32 v233,v233,v155\nv_cndmask_b32 v233,v232,v233,s[90:91]\nM v[128:131],a[4:7],a[132:135],v[128:131]\nM v[128:131],a[8:11],a[136:139],v[128:131]\nv_max3_f32 v237,v237,v143,v156\nv_max3_f32 v237,v237,v157,v158\nv_max_f32 v237,v237,v159\n" \
"M v[128:131],a[12:15],a[140:143],v[128:131]\nbuffer_load_dwordx4 v240,s[48:51],s65 offen offset:1024 sc1 lds\nM v[132:135],a[0:3],a[144:147],v[228:231]\nv_cndmask_b32 v237,v236,v237,s[90:91]\nds_read_b128 a[0:3],v240 offset:0\nM v[132:135],a[4:7],a[148:151],v[132:135]\nv_permlane16_swap_b32 v233,v237\nv_max_f32 v233,v233,v237\n" \
"v_mov_b32 v237,v233\nM v[132:135],a[8:11],a[152:155],v[132:135]\nds_read_b128 a[4:7],v240 offset:1024\nv_permlane32_swap_b32 v233,v237\nM v[132:135],a[12:15],a[156:159],v[132:135]\nv_max_f32 v233,v233,v237\nv_mov_b32 v237,v233\nds_read_b128 a[8:11],v240 offset:2048\n" \
"M v[144:147],a[16:19],a[128:131],v[224:227]\nv_permlane16_swap_b32 v233,v237\nv_sub_f32 v232,0,v233\nv_sub_f32 v234,0,v233\nM v[144:147],a[20:23],a[132:135],v[144:147]\nv_sub_f32 v235,0,v233\nv_sub_f32 v233,0,v233\nM v[144:147],a[24:27],a[136:139],v[144:147]\n" \
"v_sub_f32 v236,0,v237\nv_sub_f32 v238,0,v237\nv_sub_f32 v239,0,v237\nM v[144:147],a[28:31],a[140:143],v[144:147]\nv_sub_f32 v237,0,v237\nds_read_b128 a[12:15],v240 offset:3072\nM v[148:151],a[16:19],a[144:147],v[228:231]\nds_read_b128 a[16:19],v240 offset:4096\n" \
"buffer_load_dwordx4 v240,s[48:51],s65 offen offset:2048 sc1 lds\nM v[148:151],a[20:23],a[148:151],v[148:151]\nds_read_b128 a[20:23],v240 offset:5120\nv_add_f32 v136,v136,v232\nv_add_f32 v137,v137,v232\nM v[148:151],a[24:27],a[152:155],v[148:151]\nds_read_b128 a[24:27],v240 offset:6144\nv_add_f32 v138,v138,v232\n" \
"M v[148:151],a[28:31],a[156:159],v[148:151]\nds_read_b128 a[28:31],v240 offset:7168\nv_add_f32 v139,v139,v232\nv_add_f32 v140,v140,v236\nM v[160:163],a[32:35],a[128:131],v[224:227]\nbuffer_load_dwordx4 v240,s[48:51],s65 offen offset:3072 sc1 lds\nM v[160:163],a[36:39],a[132:135],v[160:163]\ns_add_u32 m0,s64,32768\n" \
"v_add_f32 v141,v141,v236\nv_add_f32 v142,v142,v236\nM v[160:163],a[40:43],a[136:139],v[160:163]\nv_add_f32 v143,v143,v236\nv_add_f32 v152,v152,v232\nv_add_f32 v153,v153,v232\nM v[160:163],a[44:47],a[140:143],v[160:163]\nv_add_f32 v154,v154,v232\n" \
"v_add_f32 v155,v155,v232\nM v[164:167],a[32:35],a[144:147],v[228:231]\nv_add_f32 v156,v156,v236\nv_add_f32 v157,v157,v236\nv_add_f32 v158,v158,v236\nM v[164:167],a[36:39],a[148:151],v[164:167]\nbuffer_load_dwordx4 v240,s[44:47],s66 offen offset:0 sc1 lds\nM v[164:167],a[40:43],a[152:155],v[164:167]\n" \
"v_add_f32 v159,v159,v236\nv_exp_f32 v136,v136\nM v[164:167],a[44:47],a[156:159],v[164:167]\nv_exp_f32 v137,v137\nM v[176:179],a[48:51],a[128:131],v[224:227]\nv_exp_f32 v138,v138\nM v[176:179],a[52:55],a[132:135],v[176:179]\nbuffer_load_dwordx4 v240,s[44:47],s66 offen offset:1024 sc1 lds\n" \
"s_waitcnt lgkmcnt(0)\nM v[176:179],a[56:59],a[136:139],v[176:179]\nv_exp_f32 v139,v139\nM v[176:179],a[60:63],a[140:143],v[176:179]\nv_exp_f32 v140,v140\nv_cvt_pk_bf16_f32 v200,v136,v137\nM v[180:183],a[48:51],a[144:147],v[228:231]\nv_exp_f32 v141,v141\n" \
"v_cvt_pk_bf16_f32 v201,v138,v139\nM v[180:183],a[52:55],a[148:151],v[180:183]\nbuffer_load_dwordx4 v240,s[44:47],s66 offen offset:2048 sc1 lds\nM v[180:183],a[56:59],a[152:155],v[180:183]\nv_exp_f32 v142,v142\nv_cvt_pk_bf16_f32 v204,v140,v141\nM v[180:183],a[60:63],a[156:159],v[180:183]\nv_exp_f32 v143,v143\n" \
"M v[168:171],a[32:35],a[160:163],v[232:235]\ns_barrier\nv_exp_f32 v152,v152\nM v[168:171],a[36:39],a[164:167],v[168:171]\nbuffer_load_dwordx4 v240,s[44:47],s66 offen offset:3072 sc1 lds\ns_add_u32 m0,s64,98304\nM v[168:171],a[40:43],a[168:171],v[168:171]\nv_exp_f32 v153,v153\n" \
"M v[168:171],a[44:47],a[172:175],v[168:171]\nv_exp_f32 v154,v154\nv_cvt_pk_bf16_f32 v202,v152,v153\nM v[172:175],a[32:35],a[176:179],v[236:239]\nds_read_b128 a[32:35],v240 offset:8192\nv_exp_f32 v155,v155\nM v[172:175],a[36:39],a[180:183],v[172:175]\nds_read_b128 a[36:39],v240 offset:9216\n" \
"v_cvt_pk_bf16_f32 v203,v154,v155\nM v[172:175],a[40:43],a[184:187],v[172:175]\nds_read_b128 a[40:43],v240 offset:10240\nv_exp_f32 v156,v156\nM v[172:175],a[44:47],a[188:191],v[172:175]\nds_read_b128 a[44:47],v240 offset:11264\nv_cndmask_b32 v168,v168,v252,s[82:83]\nM v[188:191],a[48:51],a[176:179],v[236:239]\n" \
"ds_read_b128 a[48:51],v240 offset:12288\nv_exp_f32 v157,v157\nM v[188:191],a[52:55],a[180:183],v[188:191]\nds_read_b128 a[52:55],v240 offset:13312\nv_exp_f32 v158,v158\nM v[188:191],a[56:59],a[184:187],v[188:191]\nds_read_b128 a[56:59],v240 offset:14336\nv_cndmask_b32 v169,v169,v252,s[84:85]\n" \
"M v[188:191],a[60:63],a[188:191],v[188:191]\nds_read_b128 a[60:63],v240 offset:15360\nv_exp_f32 v159,v159\nM v[0:3],a[64:67],v[192:195],v[0:3]\nv_exp_f32 v128,v128\nM v[4:7],a[64:67],v[196:199],v[4:7]\nv_exp_f32 v129,v129\nv_cndmask_b32 v170,v170,v252,s[86:87]\n" \
"M v[8:11],a[68:71],v[192:195],v[8:11]\nv_exp_f32 v130,v130\nv_cndmask_b32 v171,v171,v252,s[88:89]\nM v[12:15],a[68:71],v[196:199],v[12:15]\nv_exp_f32 v131,v131\nM v[16:19],a[72:75],v[192:195],v[16:19]\nv_exp_f32 v132,v132\nv_cndmask_b32 v188,v188,v252,s[82:83]\n" \
"M v[20:23],a[72:75],v[196:199],v[20:23]\nv_exp_f32 v133,v133\nM v[24:27],a[76:79],v[192:195],v[24:27]\nv_exp_f32 v134,v134\nv_cndmask_b32 v189,v189,v252,s[84:85]\nM v[28:31],a[76:79],v[196:199],v[28:31]\nv_exp_f32 v135,v135\nv_cndmask_b32 v190,v190,v252,s[86:87]\n" \
"M v[32:35],a[80:83],v[192:195],v[32:35]\nv_exp_f32 v144,v144\nM v[36:39],a[80:83],v[196:199],v[36:39]\nv_exp_f32 v145,v145\nv_cndmask_b32 v191,v191,v252,s[88:89]\nM v[40:43],a[84:87],v[192:195],v[40:43]\nv_exp_f32 v146,v146\nM v[44:47],a[84:87],v[196:199],v[44:47]\n" \
"v_exp_f32 v147,v147\nv_cvt_pk_bf16_f32 v205,v142,v143\nM v[48:51],a[88:91],v[192:195],v[48:51]\nv_exp_f32 v148,v148\nv_cvt_pk_bf16_f32 v206,v156,v157\nM v[52:55],a[88:91],v[196:199],v[52:55]\nv_exp_f32 v149,v149\nM v[56:59],a[92:95],v[192:195],v[56:59]\n" \
"v_exp_f32 v150,v150\nv_cvt_pk_bf16_f32 v207,v158,v159\nM v[60:63],a[92:95],v[196:199],v[60:63]\nv_exp_f32 v151,v151\nM a[192:195],a[208:211],v[192:195],a[192:195]\nv_exp_f32 v160,v160\nv_cvt_pk_bf16_f32 v192,v128,v129\nM a[196:199],a[208:211],v[196:199],a[196:199]\n" \
"v_exp_f32 v161,v161\nv_cvt_pk_bf16_f32 v193,v130,v131\nM v[0:3],a[96:99],v[208:211],v[0:3]\nv_exp_f32 v162,v162\nM v[4:7],a[96:99],v[212:215],v[4:7]\nv_exp_f32 v163,v163\nv_cvt_pk_bf16_f32 v194,v144,v145\nM v[8:11],a[100:103],v[208:211],v[8:11]\n" \
"v_exp_f32 v164,v164\nv_cvt_pk_bf16_f32 v195,v146,v147\nM v[12:15],a[100:103],v[212:215],v[12:15]\nv_exp_f32 v165,v165\nM v[16:19],a[104:107],v[208:211],v[16:19]\nv_exp_f32 v166,v166\nv_cvt_pk_bf16_f32 v196,v132,v133\nM v[20:23],a[104:107],v[212:215],v[20:23]\n" \
"v_exp_f32 v167,v167\nM v[24:27],a[108:111],v[208:211],v[24:27]\nv_exp_f32 v176,v176\nv_cvt_pk_bf16_f32 v197,v134,v135\nM v[28:31],a[108:111],v[212:215],v[28:31]\nv_exp_f32 v177,v177\nv_cvt_pk_bf16_f32 v198,v148,v149\nM v[32:35],a[112:115],v[208:211],v[32:35]\n" \
"v_exp_f32 v178,v178\nM v[36:39],a[112:115],v[212:215],v[36:39]\nv_exp_f32 v179,v179\nv_cvt_pk_bf16_f32 v199,v150,v151\nM v[40:43],a[116:119],v[208:211],v[40:43]\nv_exp_f32 v180,v180\nM v[44:47],a[116:119],v[212:215],v[44:47]\nv_exp_f32 v181,v181\n" \
"M v[48:51],a[120:123],v[208:211],v[48:51]\nv_exp_f32 v182,v182\nM v[52:55],a[120:123],v[212:215],v[52:55]\nv_exp_f32 v183,v183\nM v[56:59],a[124:127],v[208:211],v[56:59]\nv_exp_f32 v168,v168\nM v[60:63],a[124:127],v[212:215],v[60:63]\nv_exp_f32 v169,v169\n" \
"M a[192:195],a[208:211],v[208:211],a[192:195]\nv_exp_f32 v170,v170\nv_exp_f32 v171,v171\nbuffer_load_dwordx4 v240,s[48:51],s66 offen offset:0 sc1 lds\nbuffer_load_dwordx4 v240,s[48:51],s66 offen offset:1024 sc1 lds\nbuffer_load_dwordx4 v240,s[48:51],s66 offen offset:2048 sc1 lds\nbuffer_load_dwordx4 v240,s[48:51],s66 offen offset:3072 sc1 lds\nM a[196:199],a[208:211],v[212:215],a[196:199]\n" \
"s_cmp_lt_u32 s62,2\ns_cbranch_scc1 .Lx01\ns_branch .Ldual\n.Lpro3:\ns_barrier\nM v[64:67],a[0:3],a[128:131],0\nds_read_b128 a[64:67],v241 offset:0\nds_read_b128 a[68:71],v241 offset:1024\n" \
"M v[64:67],a[4:7],a[132:135],v[64:67]\nds_read_b128 a[72:75],v241 offset:2048\ns_mov_b32 m0,s64\nv_mov_b32 v168,0\nM v[64:67],a[8:11],a[136:139],v[64:67]\nds_read_b128 a[76:79],v241 offset:3072\nv_mov_b32 v169,0\nM v[64:67],a[12:15],a[140:143],v[64:67]\n" \
"ds_read_b128 a[80:83],v241 offset:4096\nbuffer_load_dwordx4 v240,s[44:47],s36 offen offset:0 sc1 lds\nM v[68:71],a[0:3],a[144:147],0\nds_read_b128 a[0:3],v240 offset:16384\nv_mov_b32 v170,0\nv_mov_b32 v171,0\nM v[68:71],a[4:7],a[148:151],v[68:71]\nds_read_b128 a[4:7],v240 offset:17408\n" \
"v_max3_f32 v225,v64,v65,v66\nM v[68:71],a[8:11],a[152:155],v[68:71]\nds_read_b128 a[8:11],v240 offset:18432\nv_mov_b32 v172,0xff800000\nv_mov_b32 v173,0xff800000\nM v[68:71],a[12:15],a[156:159],v[68:71]\nds_read_b128 a[12:15],v240 offset:19456\nv_mov_b32 v174,0xff800000\n" \
"M v[72:75],a[16:19],a[128:131],0\nds_read_b128 a[84:87],v241 offset:5120\nbuffer_load_dwordx4 v240,s[44:47],s36 offen offset:1024 sc1 lds\nM v[72:75],a[20:23],a[132:135],v[72:75]\nds_read_b128 a[88:91],v241 offset:6144\nv_mov_b32 v175,0xff800000\nv_max3_f32 v229,v68,v69,v70\nM v[72:75],a[24:27],a[136:139],v[72:75]\n" \
"ds_read_b128 a[92:95],v241 offset:7168\nv_mov_b32 v184,0xff800000\nM v[72:75],a[28:31],a[140:143],v[72:75]\nds_read_b128 a[96:99],v241 offset:8192\nv_mov_b32 v185,0xff800000\nv_mov_b32 v186,0xff800000\nM v[76:79],a[16:19],a[144:147],0\nds_read_b128 a[16:19],v240 offset:20480\n" \
"buffer_load_dwordx4 v240,s[44:47],s36 offen offset:2048 sc1 lds\nM v[76:79],a[20:23],a[148:151],v[76:79]\nds_read_b128 a[20:23],v240 offset:21504\nv_max3_f32 v225,v225,v67,v72\nM v[76:79],a[24:27],a[152:155],v[76:79]\nv_max3_f32 v225,v225,v73,v74\nv_max_f32 v225,v225,v75\nv_cndmask_b32 v225,v224,v225,s[90:91]\n" \
"M v[76:79],a[28:31],a[156:159],v[76:79]\nds_read_b128 a[24:27],v240 offset:22528\nds_read_b128 a[28:31],v240 offset:23552\nM v[80:83],a[32:35],a[128:131],0\nds_read_b128 a[100:103],v241 offset:9216\nbuffer_load_dwordx4 v240,s[44:47],s36 offen offset:3072 sc1 lds\nM v[80:83],a[36:39],a[132:135],v[80:83]\nds_read_b128 a[104:107],v241 offset:10240\n" \
"v_mov_b32 v187,0xff800000\nv_max3_f32 v229,v229,v71,v76\nM v[80:83],a[40:43],a[136:139],v[80:83]\nv_max3_f32 v229,v229,v77,v78\nv_max_f32 v229,v229,v79\nM v[80:83],a[44:47],a[140:143],v[80:83]\nv_cndmask_b32 v229,v228,v229,s[90:91]\nds_read_b128 a[108:111],v241 offset:11264\n" \
"ds_read_b128 a[112:115],v241 offset:12288\nM v[84:87],a[32:35],a[144:147],0\nv_permlane16_swap_b32 v225,v229\nv_max_f32 v225,v225,v229\nM v[84:87],a[36:39],a[148:151],v[84:87]\nv_mov_b32 v229,v225\nds_read_b128 a[32:35],v240 offset:24576\nds_read_b128 a[36:39],v240 offset:25600\n" \
"M v[84:87],a[40:43],a[152:155],v[84:87]\nv_permlane32_swap_b32 v225,v229\nv_max_f32 v225,v225,v229\nv_mov_b32 v229,v225\nM v[84:87],a[44:47],a[156:159],v[84:87]\nds_read_b128 a[40:43],v240 offset:26624\nv_permlane16_swap_b32 v225,v229\nM v[88:91],a[48:51],a[128:131],0\n" \
"v_sub_f32 v224,0,v225\nv_sub_f32 v226,0,v225\nv_sub_f32 v227,0,v225\nM v[88:91],a[52:55],a[132:135],v[88:91]\nv_sub_f32 v225,0,v225\nv_sub_f32 v228,0,v229\nv_sub_f32 v230,0,v229\nM v[88:91],a[56:59],a[136:139],v[88:91]\n" \
"v_sub_f32 v231,0,v229\nv_sub_f32 v229,0,v229\nM v[88:91],a[60:63],a[140:143],v[88:91]\nds_read_b128 a[44:47],v240 offset:27648\nv_add_f32 v64,v64,v224\nv_add_f32 v65,v65,v224\nM v[92:95],a[48:51],a[144:147],0\nds_read_b128 a[48:51],v240 offset:28672\n" \
"v_add_f32 v66,v66,v224\nM v[92:95],a[52:55],a[148:151],v[92:95]\nds_read_b128 a[52:55],v240 offset:29696\nv_add_f32 v67,v67,v224\nv_add_f32 v68,v68,v228\nM v[92:95],a[56:59],a[152:155],v[92:95]\nds_read_b128 a[56:59],v240 offset:30720\nv_exp_f32 v64,v64\n" \
"M v[92:95],a[60:63],a[156:159],v[92:95]\nds_read_b128 a[60:63],v240 offset:31744\ns_waitcnt lgkmcnt(6)\nM v[96:99],a[0:3],a[128:131],v[224:227]\nv_exp_f32 v65,v65\nv_add_f32 v69,v69,v228\nM v[96:99],a[4:7],a[132:135],v[96:99]\nv_exp_f32 v66,v66\n" \
"M v[96:99],a[8:11],a[136:139],v[96:99]\nv_exp_f32 v67,v67\nv_add_f32 v72,v72,v224\nM v[96:99],a[12:15],a[140:143],v[96:99]\nv_add_f32 v73,v73,v224\nv_add_f32 v74,v74,v224\nv_add_f32 v75,v75,v224\nM v[100:103],a[0:3],a[144:147],v[228:231]\n" \
"v_add_f32 v70,v70,v228\nv_add_f32 v71,v71,v228\nM v[100:103],a[4:7],a[148:151],v[100:103]\nv_exp_f32 v68,v68\nv_cvt_pk_bf16_f32 v64,v64,v65\nM v[100:103],a[8:11],a[152:155],v[100:103]\nv_exp_f32 v69,v69\nv_add_f32 v76,v76,v228\n" \
"M v[100:103],a[12:15],a[156:159],v[100:103]\nv_exp_f32 v72,v72\nM v[104:107],a[16:19],a[128:131],v[224:227]\nv_exp_f32 v73,v73\nv_add_f32 v77,v77,v228\nM v[104:107],a[20:23],a[132:135],v[104:107]\nv_exp_f32 v74,v74\nM v[104:107],a[24:27],a[136:139],v[104:107]\n" \
"v_exp_f32 v75,v75\nv_add_f32 v78,v78,v228\nM v[104:107],a[28:31],a[140:143],v[104:107]\nv_exp_f32 v70,v70\nv_add_f32 v79,v79,v228\nM v[108:111],a[16:19],a[144:147],v[228:231]\nv_exp_f32 v71,v71\nM v[108:111],a[20:23],a[148:151],v[108:111]\n" \
"v_cvt_pk_bf16_f32 v65,v66,v67\nv_exp_f32 v76,v76\nM v[108:111],a[24:27],a[152:155],v[108:111]\nv_exp_f32 v77,v77\nv_cvt_pk_bf16_f32 v66,v72,v73\nM v[108:111],a[28:31],a[156:159],v[108:111]\nv_exp_f32 v78,v78\nM v[112:115],a[32:35],a[128:131],v[224:227]\n" \
"v_exp_f32 v79,v79\nv_cvt_pk_bf16_f32 v67,v74,v75\nM v[112:115],a[36:39],a[132:135],v[112:115]\nv_cvt_pk_bf16_f32 v68,v68,v69\ns_waitcnt lgkmcnt(0)\nM v[112:115],a[40:43],a[136:139],v[112:115]\nv_cvt_pk_bf16_f32 v69,v70,v71\nv_cvt_pk_bf16_f32 v70,v76,v77\n" \
"v_cvt_pk_bf16_f32 v71,v78,v79\nM v[112:115],a[44:47],a[140:143],v[112:115]\nv_add_f32 v80,v80,v224\nv_add_f32 v81,v81,v224\nv_add_f32 v82,v82,v224\nM v[116:119],a[32:35],a[144:147],v[228:231]\nv_add_f32 v83,v83,v224\nv_add_f32 v84,v84,v228\n" \
"M v[116:119],a[36:39],a[148:151],v[116:119]\nv_exp_f32 v80,v80\nv_add_f32 v85,v85,v228\nM v[116:119],a[40:43],a[152:155],v[116:119]\nv_exp_f32 v81,v81\nM v[116:119],a[44:47],a[156:159],v[116:119]\nv_exp_f32 v82,v82\nv_add_f32 v88,v88,v224\n" \
"M v[124:127],a[48:51],a[144:147],v[228:231]\nv_exp_f32 v83,v83\nv_add_f32 v89,v89,v224\nM v[124:127],a[52:55],a[148:151],v[124:127]\nv_add_f32 v90,v90,v224\nv_add_f32 v91,v91,v224\nM v[124:127],a[56:59],a[152:155],v[124:127]\nv_add_f32 v86,v86,v228\n" \
"v_add_f32 v87,v87,v228\nv_cvt_pk_bf16_f32 v80,v80,v81\nM v[124:127],a[60:63],a[156:159],v[124:127]\nv_exp_f32 v84,v84\nv_add_f32 v92,v92,v228\nM v[0:3],a[64:67],v[64:67],0\nv_exp_f32 v85,v85\nM v[4:7],a[64:67],v[68:71],0\n" \
"v_exp_f32 v88,v88\nv_add_f32 v93,v93,v228\nM v[8:11],a[68:71],v[64:67],0\nv_exp_f32 v89,v89\nM v[12:15],a[68:71],v[68:71],0\nv_exp_f32 v90,v90\nv_add_f32 v94,v94,v228\nM v[16:19],a[72:75],v[64:67],0\n" \
"v_exp_f32 v91,v91\nv_add_f32 v95,v95,v228\nM v[20:23],a[72:75],v[68:71],0\nv_exp_f32 v86,v86\nM v[24:27],a[76:79],v[64:67],0\nv_exp_f32 v87,v87\nv_cvt_pk_bf16_f32 v81,v82,v83\nM v[28:31],a[76:79],v[68:71],0\n" \
"v_exp_f32 v92,v92\nM v[32:35],a[80:83],v[64:67],0\nv_exp_f32 v93,v93\nv_cvt_pk_bf16_f32 v82,v88,v89\nM v[36:39],a[80:83],v[68:71],0\nv_exp_f32 v94,v94\nv_cvt_pk_bf16_f32 v83,v90,v91\nM v[40:43],a[84:87],v[64:67],0\n" \
"v_exp_f32 v95,v95\nM v[44:47],a[84:87],v[68:71],0\nv_cvt_pk_bf16_f32 v84,v84,v85\nv_cvt_pk_bf16_f32 v85,v86,v87\nv_cvt_pk_bf16_f32 v86,v92,v93\nM v[48:51],a[88:91],v[64:67],0\nv_cvt_pk_bf16_f32 v87,v94,v95\nds_read_b128 a[116:119],v241 offset:13312\n" \
"ds_read_b128 a[120:123],v241 offset:14336\nM v[52:55],a[88:91],v[68:71],0\nds_read_b128 a[124:127],v241 offset:15360\nv_cndmask_b32 v112,v112,v252,s[82:83]\nM v[56:59],a[92:95],v[64:67],0\nbuffer_load_dword v248,v251,s[52:55],s80 offen sc1\nds_read_b128 a[0:3],v240 offset:32768\nM v[60:63],a[92:95],v[68:71],0\n" \
"ds_read_b128 a[4:7],v240 offset:33792\nv_cndmask_b32 v113,v113,v252,s[84:85]\nM a[192:195],a[208:211],v[64:67],0\nds_read_b128 a[8:11],v240 offset:34816\nv_exp_f32 v96,v96\nM a[196:199],a[208:211],v[68:71],0\nds_read_b128 a[12:15],v240 offset:35840\nv_exp_f32 v97,v97\n" \
"M v[0:3],a[96:99],v[80:83],v[0:3]\nds_read_b128 a[16:19],v240 offset:36864\nv_cvt_pk_bf16_f32 v96,v96,v97\nM v[4:7],a[96:99],v[84:87],v[4:7]\nds_read_b128 a[20:23],v240 offset:37888\nv_exp_f32 v98,v98\nM v[8:11],a[100:103],v[80:83],v[8:11]\nds_read_b128 a[24:27],v240 offset:38912\n" \
"v_exp_f32 v99,v99\nM v[12:15],a[100:103],v[84:87],v[12:15]\nds_read_b128 a[28:31],v240 offset:39936\nv_cvt_pk_bf16_f32 v97,v98,v99\nM v[16:19],a[104:107],v[80:83],v[16:19]\nds_read_b128 a[32:35],v240 offset:40960\nv_exp_f32 v100,v100\nM v[20:23],a[104:107],v[84:87],v[20:23]\n" \
"ds_read_b128 a[36:39],v240 offset:41984\nv_cndmask_b32 v114,v114,v252,s[86:87]\nM v[24:27],a[108:111],v[80:83],v[24:27]\nds_read_b128 a[40:43],v240 offset:43008\nv_exp_f32 v101,v101\nM v[28:31],a[108:111],v[84:87],v[28:31]\nds_read_b128 a[44:47],v240 offset:44032\nv_exp_f32 v104,v104\n" \
"M v[32:35],a[112:115],v[80:83],v[32:35]\nds_read_b128 a[48:51],v240 offset:45056\nv_cvt_pk_bf16_f32 v100,v100,v101\nM v[36:39],a[112:115],v[84:87],v[36:39]\nds_read_b128 a[52:55],v240 offset:46080\nv_cndmask_b32 v115,v115,v252,s[88:89]\ns_waitcnt lgkmcnt(9)\nM v[40:43],a[116:119],v[80:83],v[40:43]\n" \
"ds_read_b128 a[56:59],v240 offset:47104\nv_cndmask_b32 v124,v124,v252,s[82:83]\nM v[44:47],a[116:119],v[84:87],v[44:47]\nds_read_b128 a[60:63],v240 offset:48128\nv_exp_f32 v105,v105\nM v[48:51],a[120:123],v[80:83],v[48:51]\nds_read_b128 a[64:67],v241 offset:16384\nv_exp_f32 v106,v106\n" \
"M v[52:55],a[120:123],v[84:87],v[52:55]\nds_read_b128 a[68:71],v241 offset:17408\nv_cvt_pk_bf16_f32 v98,v104,v105\nM v[56:59],a[124:127],v[80:83],v[56:59]\nds_read_b128 a[72:75],v241 offset:18432\nv_exp_f32 v107,v107\nM v[60:63],a[124:127],v[84:87],v[60:63]\nds_read_b128 a[76:79],v241 offset:19456\n" \
"v_exp_f32 v102,v102\nM a[192:195],a[208:211],v[80:83],a[192:195]\nds_read_b128 a[80:83],v241 offset:20480\nv_cvt_pk_bf16_f32 v99,v106,v107\nM a[196:199],a[208:211],v[84:87],a[196:199]\nds_read_b128 a[84:87],v241 offset:21504\nv_exp_f32 v103,v103\nM v[128:131],a[0:3],a[128:131],v[224:227]\n" \
"ds_read_b128 a[88:91],v241 offset:22528\nv_cvt_pk_bf16_f32 v101,v102,v103\nM v[128:131],a[4:7],a[132:135],v[128:131]\nds_read_b128 a[92:95],v241 offset:23552\nv_exp_f32 v108,v108\nM v[128:131],a[8:11],a[136:139],v[128:131]\nds_read_b128 a[96:99],v241 offset:24576\nv_exp_f32 v109,v109\n" \
"M v[128:131],a[12:15],a[140:143],v[128:131]\nds_read_b128 a[100:103],v241 offset:25600\nv_cvt_pk_bf16_f32 v102,v108,v109\nM v[132:135],a[0:3],a[144:147],v[228:231]\nds_read_b128 a[104:107],v241 offset:26624\nv_exp_f32 v110,v110\nM v[132:135],a[4:7],a[148:151],v[132:135]\nds_read_b128 a[108:111],v241 offset:27648\n" \
"v_cndmask_b32 v125,v125,v252,s[84:85]\nM v[132:135],a[8:11],a[152:155],v[132:135]\nds_read_b128 a[112:115],v241 offset:28672\nv_exp_f32 v111,v111\nM v[132:135],a[12:15],a[156:159],v[132:135]\nds_read_b128 a[116:119],v241 offset:29696\nv_cvt_pk_bf16_f32 v103,v110,v111\nv_cndmask_b32 v126,v126,v252,s[86:87]\n" \
"M v[144:147],a[16:19],a[128:131],v[224:227]\nds_read_b128 a[120:123],v241 offset:30720\ns_waitcnt lgkmcnt(9)\nM v[144:147],a[20:23],a[132:135],v[144:147]\nds_read_b128 a[124:127],v241 offset:31744\nv_exp_f32 v112,v112\nM v[144:147],a[24:27],a[136:139],v[144:147]\nv_exp_f32 v113,v113\n" \
"v_cndmask_b32 v127,v127,v252,s[88:89]\nM v[144:147],a[28:31],a[140:143],v[144:147]\nv_exp_f32 v114,v114\nM v[148:151],a[16:19],a[144:147],v[228:231]\nv_exp_f32 v115,v115\nv_cvt_pk_bf16_f32 v112,v112,v113\nM v[148:151],a[20:23],a[148:151],v[148:151]\nv_exp_f32 v116,v116\n" \
"M v[148:151],a[24:27],a[152:155],v[148:151]\nv_exp_f32 v117,v117\nv_cvt_pk_bf16_f32 v113,v114,v115\nM v[148:151],a[28:31],a[156:159],v[148:151]\nv_exp_f32 v118,v118\nv_mov_b32 v114,0\nM v[160:163],a[32:35],a[128:131],v[224:227]\nv_exp_f32 v119,v119\n" \
"M v[160:163],a[36:39],a[132:135],v[160:163]\nv_exp_f32 v124,v124\nv_mov_b32 v115,0\nM v[160:163],a[40:43],a[136:139],v[160:163]\nv_exp_f32 v125,v125\nv_cvt_pk_bf16_f32 v116,v116,v117\nM v[160:163],a[44:47],a[140:143],v[160:163]\nv_exp_f32 v126,v126\n" \
"M v[164:167],a[32:35],a[144:147],v[228:231]\nv_exp_f32 v127,v127\nv_cvt_pk_bf16_f32 v117,v118,v119\nM v[164:167],a[36:39],a[148:151],v[164:167]\nv_cvt_pk_bf16_f32 v118,v124,v125\nv_cvt_pk_bf16_f32 v119,v126,v127\nM v[164:167],a[40:43],a[152:155],v[164:167]\nds_read_b128 a[0:3],v240 offset:49152\n" \
"ds_read_b128 a[4:7],v240 offset:50176\nv_mov_b32 v188,0xff800000\nM v[164:167],a[44:47],a[156:159],v[164:167]\nds_read_b128 a[8:11],v240 offset:51200\nv_exp_f32 v128,v128\nM v[176:179],a[48:51],a[128:131],v[224:227]\nds_read_b128 a[12:15],v240 offset:52224\ns_waitcnt vmcnt(0)\n" \
"v_cmp_eq_u32 vcc,0,v248\nM v[176:179],a[52:55],a[132:135],v[176:179]\nds_read_b128 a[16:19],v240 offset:53248\nv_exp_f32 v129,v129\nM v[176:179],a[56:59],a[136:139],v[176:179]\nds_read_b128 a[20:23],v240 offset:54272\nv_cvt_pk_bf16_f32 v192,v128,v129\nM v[176:179],a[60:63],a[140:143],v[176:179]\n" \
"ds_read_b128 a[24:27],v240 offset:55296\nv_exp_f32 v130,v130\nM v[180:183],a[48:51],a[144:147],v[228:231]\ns_waitcnt lgkmcnt(7)\ns_barrier\nds_read_b128 a[28:31],v240 offset:56320\nM v[180:183],a[52:55],a[148:151],v[180:183]\nv_exp_f32 v131,v131\n" \
"M v[180:183],a[56:59],a[152:155],v[180:183]\nv_cvt_pk_bf16_f32 v193,v130,v131\nds_read_b128 a[32:35],v240 offset:57344\nds_read_b128 a[36:39],v240 offset:58368\nM v[180:183],a[60:63],a[156:159],v[180:183]\nds_read_b128 a[40:43],v240 offset:59392\nv_exp_f32 v132,v132\nM v[0:3],a[64:67],v[96:99],v[0:3]\n" \
"ds_read_b128 a[44:47],v240 offset:60416\ns_cbranch_vccnz .Lps5\n.Lpb5:\nM v[4:7],a[64:67],v[100:103],v[4:7]\nds_read_b128 a[48:51],v240 offset:61440\nv_exp_f32 v133,v133\nM v[8:11],a[68:71],v[96:99],v[8:11]\nds_read_b128 a[52:55],v240 offset:62464\n" \
"v_cvt_pk_bf16_f32 v196,v132,v133\nM v[12:15],a[68:71],v[100:103],v[12:15]\nds_read_b128 a[56:59],v240 offset:63488\nv_exp_f32 v134,v134\nM v[16:19],a[72:75],v[96:99],v[16:19]\nds_read_b128 a[60:63],v240 offset:64512\nv_exp_f32 v135,v135\nM v[20:23],a[72:75],v[100:103],v[20:23]\n" \
"ds_read_b128 a[64:67],v241 offset:32768\nv_cvt_pk_bf16_f32 v197,v134,v135\nM v[24:27],a[76:79],v[96:99],v[24:27]\nds_read_b128 a[68:71],v241 offset:33792\nv_exp_f32 v144,v144\nM v[28:31],a[76:79],v[100:103],v[28:31]\nds_read_b128 a[72:75],v241 offset:34816\ns_add_u32 m0,s64,65536\n" \
"M v[32:35],a[80:83],v[96:99],v[32:35]\nds_read_b128 a[76:79],v241 offset:35840\nv_exp_f32 v145,v145\nM v[36:39],a[80:83],v[100:103],v[36:39]\nds_read_b128 a[80:83],v241 offset:36864\nv_exp_f32 v146,v146\nM v[40:43],a[84:87],v[96:99],v[40:43]\nv_exp_f32 v147,v147\n" \
"M v[44:47],a[84:87],v[100:103],v[44:47]\nds_read_b128 a[84:87],v241 offset:37888\nv_cvt_pk_bf16_f32 v194,v144,v145\nv_cvt_pk_bf16_f32 v195,v146,v147\nM v[48:51],a[88:91],v[96:99],v[48:51]\nv_exp_f32 v148,v148\nv_mov_b32 v189,0xff800000\nM v[52:55],a[88:91],v[100:103],v[52:55]\n" \
"ds_read_b128 a[88:91],v241 offset:38912\nv_mov_b32 v190,0xff800000\nM v[56:59],a[92:95],v[96:99],v[56:59]\nv_exp_f32 v149,v149\nv_mov_b32 v191,0xff800000\nM v[60:63],a[92:95],v[100:103],v[60:63]\nds_read_b128 a[92:95],v241 offset:39936\nv_cvt_pk_bf16_f32 v198,v148,v149\n" \
"M a[192:195],a[208:211],v[96:99],a[192:195]\nv_exp_f32 v150,v150\nv_mov_b32 v202,0\nM a[196:199],a[208:211],v[100:103],a[196:199]\nv_exp_f32 v151,v151\nv_mov_b32 v203,0\nM v[0:3],a[96:99],v[112:115],v[0:3]\nv_exp_f32 v160,v160\n" \
"M v[4:7],a[96:99],v[116:119],v[4:7]\nds_read_b128 a[96:99],v241 offset:40960\nv_exp_f32 v161,v161\nM v[8:11],a[100:103],v[112:115],v[8:11]\nv_cvt_pk_bf16_f32 v199,v150,v151\nv_exp_f32 v162,v162\nM v[12:15],a[100:103],v[116:119],v[12:15]\nds_read_b128 a[100:103],v241 offset:41984\n" \
"v_cvt_pk_bf16_f32 v208,v160,v161\nM v[16:19],a[104:107],v[112:115],v[16:19]\nv_exp_f32 v163,v163\nM v[20:23],a[104:107],v[116:119],v[20:23]\nds_read_b128 a[104:107],v241 offset:43008\nv_cvt_pk_bf16_f32 v209,v162,v163\nM v[24:27],a[108:111],v[112:115],v[24:27]\nv_exp_f32 v164,v164\n" \
"M v[28:31],a[108:111],v[116:119],v[28:31]\nds_read_b128 a[108:111],v241 offset:44032\nv_exp_f32 v165,v165\nM v[32:35],a[112:115],v[112:115],v[32:35]\nv_exp_f32 v166,v166\nM v[36:39],a[112:115],v[116:119],v[36:39]\nds_read_b128 a[112:115],v241 offset:45056\nv_exp_f32 v167,v167\n" \
"M v[40:43],a[116:119],v[112:115],v[40:43]\nv_cvt_pk_bf16_f32 v212,v164,v165\nv_cvt_pk_bf16_f32 v213,v166,v167\nM v[44:47],a[116:119],v[116:119],v[44:47]\nds_read_b128 a[116:119],v241 offset:46080\nv_exp_f32 v176,v176\nM v[48:51],a[120:123],v[112:115],v[48:51]\nv_exp_f32 v177,v177\n" \
"M v[52:55],a[120:123],v[116:119],v[52:55]\nds_read_b128 a[120:123],v241 offset:47104\nv_cvt_pk_bf16_f32 v210,v176,v177\nM v[56:59],a[124:127],v[112:115],v[56:59]\nv_exp_f32 v178,v178\nM v[60:63],a[124:127],v[116:119],v[60:63]\nds_read_b128 a[124:127],v241 offset:48128\nv_exp_f32 v179,v179\n" \
"M a[192:195],a[208:211],v[112:115],a[192:195]\nv_exp_f32 v180,v180\nM a[196:199],a[208:211],v[116:119],a[196:199]\nv_cvt_pk_bf16_f32 v211,v178,v179\nv_exp_f32 v181,v181\nM v[64:67],a[244:247],a[244:247],0\nv_exp_f32 v182,v182\nM v[68:71],a[244:247],a[244:247],0\n" \
"v_exp_f32 v183,v183\nv_cvt_pk_bf16_f32 v214,v180,v181\nM v[72:75],a[244:247],a[244:247],0\nv_cvt_pk_bf16_f32 v215,v182,v183\nbuffer_load_dwordx4 v240,s[48:51],s36 offen offset:0 sc1 lds\nM v[76:79],a[244:247],a[244:247],0\nM v[80:83],a[244:247],a[244:247],0\nM v[84:87],a[244:247],a[244:247],0\n" \
"M v[88:91],a[244:247],a[244:247],0\nbuffer_load_dwordx4 v240,s[48:51],s36 offen offset:1024 sc1 lds\nM v[92:95],a[244:247],a[244:247],0\nM v[96:99],a[244:247],a[244:247],0\nM v[100:103],a[244:247],a[244:247],0\nM v[104:107],a[244:247],a[244:247],0\nbuffer_load_dwordx4 v240,s[48:51],s36 offen offset:2048 sc1 lds\nM v[108:111],a[244:247],a[244:247],0\n" \
"M v[112:115],a[244:247],a[244:247],0\nM v[116:119],a[244:247],a[244:247],0\nM v[120:123],a[244:247],a[244:247],0\nbuffer_load_dwordx4 v240,s[48:51],s36 offen offset:3072 sc1 lds\ns_add_u32 m0,s64,16384\nM v[124:127],a[244:247],a[244:247],0\nM a[200:203],a[244:247],a[244:247],0\nM a[204:207],a[244:247],a[244:247],0\n" \
"s_waitcnt lgkmcnt(0)\nM v[136:139],a[0:3],a[160:163],0\nbuffer_load_dwordx4 v240,s[44:47],s65 offen offset:0 sc1 lds\nM v[136:139],a[4:7],a[164:167],v[136:139]\nM v[136:139],a[8:11],a[168:171],v[136:139]\nM v[136:139],a[12:15],a[172:175],v[136:139]\nM v[140:143],a[0:3],a[176:179],0\nbuffer_load_dwordx4 v240,s[44:47],s65 offen offset:1024 sc1 lds\n" \
"M v[140:143],a[4:7],a[180:183],v[140:143]\nM v[140:143],a[8:11],a[184:187],v[140:143]\nM v[140:143],a[12:15],a[188:191],v[140:143]\nM v[156:159],a[16:19],a[176:179],0\nbuffer_load_dwordx4 v240,s[44:47],s65 offen offset:2048 sc1 lds\nM v[156:159],a[20:23],a[180:183],v[156:159]\nv_max3_f32 v233,v136,v137,v138\nv_max_f32 v233,v233,v139\n" \
"v_cndmask_b32 v233,v232,v233,s[90:91]\nM v[156:159],a[24:27],a[184:187],v[156:159]\nv_cndmask_b32 v136,v136,v252,s[82:83]\nv_max3_f32 v237,v140,v141,v142\nv_cndmask_b32 v137,v137,v252,s[84:85]\nM v[156:159],a[28:31],a[188:191],v[156:159]\nv_cndmask_b32 v138,v138,v252,s[86:87]\nv_cndmask_b32 v139,v139,v252,s[88:89]\n" \
"M v[128:131],a[0:3],a[128:131],v[224:227]\nbuffer_load_dwordx4 v240,s[44:47],s65 offen offset:3072 sc1 lds\ns_add_u32 m0,s64,81920\nM v[128:131],a[4:7],a[132:135],v[128:131]\nM v[128:131],a[8:11],a[136:139],v[128:131]\ns_barrier\nv_max3_f32 v237,v237,v143,v156\nM v[128:131],a[12:15],a[140:143],v[128:131]\n" \
"v_max3_f32 v237,v237,v157,v158\nv_max_f32 v237,v237,v159\nv_cndmask_b32 v237,v236,v237,s[90:91]\nM v[132:135],a[0:3],a[144:147],v[228:231]\nds_read_b128 a[0:3],v240 offset:0\nv_permlane16_swap_b32 v233,v237\nM v[132:135],a[4:7],a[148:151],v[132:135]\nds_read_b128 a[4:7],v240 offset:1024\n" \
"v_max_f32 v233,v233,v237\nv_mov_b32 v237,v233\nM v[132:135],a[8:11],a[152:155],v[132:135]\nds_read_b128 a[8:11],v240 offset:2048\nv_permlane32_swap_b32 v233,v237\nv_max_f32 v233,v233,v237\nM v[132:135],a[12:15],a[156:159],v[132:135]\nds_read_b128 a[12:15],v240 offset:3072\n" \
"v_mov_b32 v237,v233\nM v[144:147],a[16:19],a[128:131],v[224:227]\nbuffer_load_dwordx4 v240,s[48:51],s65 offen offset:0 sc1 lds\nv_permlane16_swap_b32 v233,v237\nM v[144:147],a[20:23],a[132:135],v[144:147]\nv_cndmask_b32 v156,v156,v252,s[82:83]\nv_cndmask_b32 v157,v157,v252,s[84:85]\nM v[144:147],a[24:27],a[136:139],v[144:147]\n" \
"v_cndmask_b32 v158,v158,v252,s[86:87]\nv_cndmask_b32 v159,v159,v252,s[88:89]\nv_sub_f32 v232,0,v233\nM v[144:147],a[28:31],a[140:143],v[144:147]\nv_sub_f32 v236,0,v237\nv_add_f32 v136,v136,v232\nv_add_f32 v137,v137,v232\nM v[148:151],a[16:19],a[144:147],v[228:231]\n" \
"ds_read_b128 a[16:19],v240 offset:4096\nv_add_f32 v138,v138,v232\nM v[148:151],a[20:23],a[148:151],v[148:151]\nds_read_b128 a[20:23],v240 offset:5120\nbuffer_load_dwordx4 v240,s[48:51],s65 offen offset:1024 sc1 lds\nM v[148:151],a[24:27],a[152:155],v[148:151]\nds_read_b128 a[24:27],v240 offset:6144\nv_add_f32 v139,v139,v232\n" \
"v_add_f32 v140,v140,v236\nM v[148:151],a[28:31],a[156:159],v[148:151]\nds_read_b128 a[28:31],v240 offset:7168\nv_add_f32 v141,v141,v236\nM v[160:163],a[32:35],a[128:131],v[224:227]\nv_add_f32 v142,v142,v236\nv_add_f32 v143,v143,v236\nv_add_f32 v156,v156,v236\n" \
"M v[160:163],a[36:39],a[132:135],v[160:163]\nbuffer_load_dwordx4 v240,s[48:51],s65 offen offset:2048 sc1 lds\nM v[160:163],a[40:43],a[136:139],v[160:163]\nv_add_f32 v157,v157,v236\nv_add_f32 v158,v158,v236\nv_add_f32 v159,v159,v236\nM v[160:163],a[44:47],a[140:143],v[160:163]\nv_exp_f32 v136,v136\n" \
"v_sub_f32 v234,0,v233\nM v[164:167],a[32:35],a[144:147],v[228:231]\nds_read_b128 a[32:35],v240 offset:8192\nv_sub_f32 v235,0,v233\nM v[164:167],a[36:39],a[148:151],v[164:167]\nds_read_b128 a[36:39],v240 offset:9216\nbuffer_load_dwordx4 v240,s[48:51],s65 offen offset:3072 sc1 lds\nM v[164:167],a[40:43],a[152:155],v[164:167]\n" \
"ds_read_b128 a[40:43],v240 offset:10240\ns_add_u32 m0,s64,32768\nv_sub_f32 v238,0,v237\nM v[164:167],a[44:47],a[156:159],v[164:167]\nds_read_b128 a[44:47],v240 offset:11264\nv_sub_f32 v239,0,v237\nM v[176:179],a[48:51],a[128:131],v[224:227]\nv_exp_f32 v137,v137\n" \
"v_sub_f32 v233,0,v233\nM v[176:179],a[52:55],a[132:135],v[176:179]\nbuffer_load_dwordx4 v240,s[44:47],s66 offen offset:0 sc1 lds\nM v[176:179],a[56:59],a[136:139],v[176:179]\nv_exp_f32 v138,v138\nv_sub_f32 v237,0,v237\nM v[176:179],a[60:63],a[140:143],v[176:179]\nv_exp_f32 v139,v139\n" \
"v_cvt_pk_bf16_f32 v200,v136,v137\nM v[180:183],a[48:51],a[144:147],v[228:231]\nds_read_b128 a[48:51],v240 offset:12288\nv_cvt_pk_bf16_f32 v201,v138,v139\nM v[180:183],a[52:55],a[148:151],v[180:183]\nds_read_b128 a[52:55],v240 offset:13312\nbuffer_load_dwordx4 v240,s[44:47],s66 offen offset:1024 sc1 lds\nM v[180:183],a[56:59],a[152:155],v[180:183]\n" \
"v_exp_f32 v140,v140\nM v[180:183],a[60:63],a[156:159],v[180:183]\nds_read_b128 a[56:59],v240 offset:14336\nv_exp_f32 v141,v141\nM v[0:3],a[64:67],v[192:195],v[0:3]\nds_read_b128 a[60:63],v240 offset:15360\nv_exp_f32 v142,v142\nM v[4:7],a[64:67],v[196:199],v[4:7]\n" \
"buffer_load_dwordx4 v240,s[44:47],s66 offen offset:2048 sc1 lds\nM v[8:11],a[68:71],v[192:195],v[8:11]\nv_exp_f32 v143,v143\nv_cvt_pk_bf16_f32 v204,v140,v141\nM v[12:15],a[68:71],v[196:199],v[12:15]\nv_exp_f32 v156,v156\nv_cvt_pk_bf16_f32 v205,v142,v143\nM v[16:19],a[72:75],v[192:195],v[16:19]\n" \
"v_exp_f32 v157,v157\nM v[20:23],a[72:75],v[196:199],v[20:23]\nbuffer_load_dwordx4 v240,s[44:47],s66 offen offset:3072 sc1 lds\ns_add_u32 m0,s64,98304\nM v[24:27],a[76:79],v[192:195],v[24:27]\nv_exp_f32 v158,v158\nM v[28:31],a[76:79],v[196:199],v[28:31]\nv_exp_f32 v159,v159\n" \
"v_cvt_pk_bf16_f32 v206,v156,v157\nM v[32:35],a[80:83],v[192:195],v[32:35]\nv_exp_f32 v128,v128\nv_cvt_pk_bf16_f32 v207,v158,v159\nM v[36:39],a[80:83],v[196:199],v[36:39]\nv_exp_f32 v129,v129\nM v[40:43],a[84:87],v[192:195],v[40:43]\nv_exp_f32 v130,v130\n" \
"M v[44:47],a[84:87],v[196:199],v[44:47]\nv_exp_f32 v131,v131\nM v[48:51],a[88:91],v[192:195],v[48:51]\nv_exp_f32 v132,v132\nM v[52:55],a[88:91],v[196:199],v[52:55]\nv_exp_f32 v133,v133\nM v[56:59],a[92:95],v[192:195],v[56:59]\nv_exp_f32 v134,v134\n" \
"M v[60:63],a[92:95],v[196:199],v[60:63]\nv_exp_f32 v135,v135\nM a[192:195],a[208:211],v[192:195],a[192:195]\nv_exp_f32 v144,v144\nv_cvt_pk_bf16_f32 v192,v128,v129\nM a[196:199],a[208:211],v[196:199],a[196:199]\nv_exp_f32 v145,v145\nM v[0:3],a[96:99],v[208:211],v[0:3]\n" \
"v_exp_f32 v146,v146\nv_cvt_pk_bf16_f32 v193,v130,v131\nM v[4:7],a[96:99],v[212:215],v[4:7]\nv_exp_f32 v147,v147\nM v[8:11],a[100:103],v[208:211],v[8:11]\nv_exp_f32 v148,v148\nv_cvt_pk_bf16_f32 v194,v144,v145\nM v[12:15],a[100:103],v[212:215],v[12:15]\n" \
"v_exp_f32 v149,v149\nv_cvt_pk_bf16_f32 v195,v146,v147\nM v[16:19],a[104:107],v[208:211],v[16:19]\nv_exp_f32 v150,v150\nM v[20:23],a[104:107],v[212:215],v[20:23]\nv_exp_f32 v151,v151\nv_cvt_pk_bf16_f32 v196,v132,v133\nM v[24:27],a[108:111],v[208:211],v[24:27]\n" \
"v_exp_f32 v160,v160\nv_cvt_pk_bf16_f32 v197,v134,v135\nM v[28:31],a[108:111],v[212:215],v[28:31]\nv_exp_f32 v161,v161\nM v[32:35],a[112:115],v[208:211],v[32:35]\nv_exp_f32 v162,v162\nv_cvt_pk_bf16_f32 v198,v148,v149\nM v[36:39],a[112:115],v[212:215],v[36:39]\n" \
"v_exp_f32 v163,v163\nM v[40:43],a[116:119],v[208:211],v[40:43]\nv_exp_f32 v164,v164\nv_cvt_pk_bf16_f32 v199,v150,v151\nM v[44:47],a[116:119],v[212:215],v[44:47]\nv_exp_f32 v165,v165\nM v[48:51],a[120:123],v[208:211],v[48:51]\nv_exp_f32 v166,v166\n" \
"M v[52:55],a[120:123],v[212:215],v[52:55]\nv_exp_f32 v167,v167\nM v[56:59],a[124:127],v[208:211],v[56:59]\nv_exp_f32 v176,v176\nM v[60:63],a[124:127],v[212:215],v[60:63]\nv_exp_f32 v177,v177\nM a[192:195],a[208:211],v[208:211],a[192:195]\nv_exp_f32 v178,v178\n" \
"v_exp_f32 v179,v179\nv_exp_f32 v180,v180\nv_exp_f32 v181,v181\nv_exp_f32 v182,v182\nv_exp_f32 v183,v183\nbuffer_load_dwordx4 v240,s[48:51],s66 offen offset:0 sc1 lds\nbuffer_load_dwordx4 v240,s[48:51],s66 offen offset:1024 sc1 lds\nbuffer_load_dwordx4 v240,s[48:51],s66 offen offset:2048 sc1 lds\n" \
"buffer_load_dwordx4 v240,s[48:51],s66 offen offset:3072 sc1 lds\nM a[196:199],a[208:211],v[212:215],a[196:199]\ns_cmp_lt_u32 s62,2\ns_cbranch_scc1 .Lx01\n.Ldual:\ns_waitcnt lgkmcnt(4)\nM v[128:131],a[0:3],a[128:131],v[224:227]\ns_add_u32 m0,s94,s64\n" \
"v_cvt_pk_bf16_f32 v208,v160,v161\nM v[128:131],a[4:7],a[132:135],v[128:131]\nds_read_b128 a[64:67],v250 offset:0\nds_read_b128 a[68:71],v250 offset:1024\nM v[128:131],a[8:11],a[136:139],v[128:131]\nv_cvt_pk_bf16_f32 v209,v162,v163\nv_cvt_pk_bf16_f32 v212,v164,v165\nM v[128:131],a[12:15],a[140:143],v[128:131]\n" \
"v_cvt_pk_bf16_f32 v213,v166,v167\nv_cvt_pk_bf16_f32 v216,v168,v169\nM v[132:135],a[0:3],a[144:147],v[228:231]\nbuffer_load_dwordx4 v240,s[44:47],s95 offen offset:0 sc1 lds\nM v[132:135],a[4:7],a[148:151],v[132:135]\nv_cvt_pk_bf16_f32 v217,v170,v171\nv_cvt_pk_bf16_f32 v210,v176,v177\nM v[132:135],a[8:11],a[152:155],v[132:135]\n" \
"v_exp_f32 v172,v172\nM v[132:135],a[12:15],a[156:159],v[132:135]\nds_read_b128 a[72:75],v250 offset:2048\nds_read_b128 a[76:79],v250 offset:3072\nM v[136:139],a[0:3],a[160:163],v[232:235]\nv_exp_f32 v173,v173\nM v[136:139],a[4:7],a[164:167],v[136:139]\nv_exp_f32 v174,v174\n" \
"M v[136:139],a[8:11],a[168:171],v[136:139]\nv_exp_f32 v175,v175\nM v[136:139],a[12:15],a[172:175],v[136:139]\nv_cvt_pk_bf16_f32 v220,v172,v173\nv_cvt_pk_bf16_f32 v221,v174,v175\nM v[140:143],a[0:3],a[176:179],v[236:239]\nv_cvt_pk_bf16_f32 v211,v178,v179\nv_cvt_pk_bf16_f32 v214,v180,v181\n" \
"M v[140:143],a[4:7],a[180:183],v[140:143]\nds_read_b128 a[80:83],v250 offset:4096\nds_read_b128 a[84:87],v250 offset:5120\nM v[140:143],a[8:11],a[184:187],v[140:143]\nv_cvt_pk_bf16_f32 v215,v182,v183\ns_add_u32 s94,s94,0x4000\nM v[140:143],a[12:15],a[188:191],v[140:143]\nv_exp_f32 v184,v184\n" \
"M v[144:147],a[16:19],a[128:131],v[224:227]\nv_exp_f32 v185,v185\nM v[144:147],a[20:23],a[132:135],v[144:147]\nv_exp_f32 v186,v186\nM v[144:147],a[24:27],a[136:139],v[144:147]\nv_exp_f32 v187,v187\nM v[144:147],a[28:31],a[140:143],v[144:147]\nds_read_b128 a[88:91],v250 offset:6144\n" \
"ds_read_b128 a[92:95],v250 offset:7168\nM v[148:151],a[16:19],a[144:147],v[228:231]\nv_cvt_pk_bf16_f32 v218,v184,v185\nv_cvt_pk_bf16_f32 v219,v186,v187\nM v[148:151],a[20:23],a[148:151],v[148:151]\nv_exp_f32 v188,v188\nM v[148:151],a[24:27],a[152:155],v[148:151]\nbuffer_load_dwordx4 v240,s[44:47],s95 offen offset:1024 sc1 lds\n" \
"M v[148:151],a[28:31],a[156:159],v[148:151]\nv_exp_f32 v189,v189\nM v[152:155],a[16:19],a[160:163],v[232:235]\nv_exp_f32 v190,v190\nM v[152:155],a[20:23],a[164:167],v[152:155]\nds_read_b128 a[96:99],v250 offset:8192\nds_read_b128 a[100:103],v250 offset:9216\nM v[152:155],a[24:27],a[168:171],v[152:155]\n" \
"v_exp_f32 v191,v191\nM v[152:155],a[28:31],a[172:175],v[152:155]\nv_cvt_pk_bf16_f32 v222,v188,v189\nv_cvt_pk_bf16_f32 v223,v190,v191\nM v[156:159],a[16:19],a[176:179],v[236:239]\nv_exp_f32 v128,v128\nM v[156:159],a[20:23],a[180:183],v[156:159]\nv_exp_f32 v129,v129\n" \
"M v[156:159],a[24:27],a[184:187],v[156:159]\nv_exp_f32 v130,v130\nM v[156:159],a[28:31],a[188:191],v[156:159]\nds_read_b128 a[104:107],v250 offset:10240\nds_read_b128 a[108:111],v250 offset:11264\nM v[160:163],a[32:35],a[128:131],v[224:227]\nv_exp_f32 v131,v131\nM v[160:163],a[36:39],a[132:135],v[160:163]\n" \
"v_exp_f32 v144,v144\nM v[160:163],a[40:43],a[136:139],v[160:163]\nv_exp_f32 v145,v145\nM v[160:163],a[44:47],a[140:143],v[160:163]\nv_exp_f32 v146,v146\nM v[164:167],a[32:35],a[144:147],v[228:231]\nv_exp_f32 v147,v147\nM v[164:167],a[36:39],a[148:151],v[164:167]\n" \
"ds_read_b128 a[112:115],v250 offset:12288\nds_read_b128 a[116:119],v250 offset:13312\nM v[164:167],a[40:43],a[152:155],v[164:167]\nv_exp_f32 v132,v132\nM v[164:167],a[44:47],a[156:159],v[164:167]\nv_exp_f32 v133,v133\nM v[168:171],a[32:35],a[160:163],v[232:235]\nbuffer_load_dwordx4 v240,s[44:47],s95 offen offset:2048 sc1 lds\n" \
"M v[168:171],a[36:39],a[164:167],v[168:171]\nv_exp_f32 v134,v134\nM v[168:171],a[40:43],a[168:171],v[168:171]\nv_exp_f32 v135,v135\nM v[168:171],a[44:47],a[172:175],v[168:171]\nds_read_b128 a[120:123],v250 offset:14336\nds_read_b128 a[124:127],v250 offset:15360\nM v[172:175],a[32:35],a[176:179],v[236:239]\n" \
"v_xor_b32 v250,0x18000,v250\ns_and_b32 s94,s94,0xffff\nM v[172:175],a[36:39],a[180:183],v[172:175]\nv_exp_f32 v148,v148\nM v[172:175],a[40:43],a[184:187],v[172:175]\nv_exp_f32 v149,v149\nM v[172:175],a[44:47],a[188:191],v[172:175]\ns_waitcnt lgkmcnt(4)\n" \
"M v[176:179],a[48:51],a[128:131],v[224:227]\nv_exp_f32 v150,v150\nM v[176:179],a[52:55],a[132:135],v[176:179]\nv_exp_f32 v151,v151\nM v[176:179],a[56:59],a[136:139],v[176:179]\nv_exp_f32 v136,v136\nM v[176:179],a[60:63],a[140:143],v[176:179]\nv_exp_f32 v137,v137\n" \
"M v[180:183],a[48:51],a[144:147],v[228:231]\nv_exp_f32 v138,v138\nM v[180:183],a[52:55],a[148:151],v[180:183]\nv_exp_f32 v139,v139\nM v[180:183],a[56:59],a[152:155],v[180:183]\nv_exp_f32 v152,v152\nM v[180:183],a[60:63],a[156:159],v[180:183]\nv_exp_f32 v153,v153\n" \
"M v[184:187],a[48:51],a[160:163],v[232:235]\nv_exp_f32 v154,v154\nM v[184:187],a[52:55],a[164:167],v[184:187]\nv_exp_f32 v155,v155\nM v[184:187],a[56:59],a[168:171],v[184:187]\nbuffer_load_dwordx4 v240,s[44:47],s95 offen offset:3072 sc1 lds\nM v[184:187],a[60:63],a[172:175],v[184:187]\nv_exp_f32 v140,v140\n" \
"M v[188:191],a[48:51],a[176:179],v[236:239]\nv_exp_f32 v141,v141\nM v[188:191],a[52:55],a[180:183],v[188:191]\nv_exp_f32 v142,v142\nM v[188:191],a[56:59],a[184:187],v[188:191]\nv_exp_f32 v143,v143\nM v[188:191],a[60:63],a[188:191],v[188:191]\nv_exp_f32 v156,v156\n" \
"M v[0:3],a[64:67],v[192:195],v[0:3]\ns_waitcnt vmcnt(16) lgkmcnt(0)\ns_barrier\nM v[4:7],a[64:67],v[196:199],v[4:7]\nds_read_b128 a[0:3],v250 offset:0\nds_read_b128 a[4:7],v250 offset:1024\nM v[64:67],a[64:67],v[200:203],v[64:67]\nv_exp_f32 v157,v157\n" \
"M v[68:71],a[64:67],v[204:207],v[68:71]\nv_exp_f32 v158,v158\nM v[8:11],a[68:71],v[192:195],v[8:11]\ns_add_u32 m0,m0,0x10000\nM v[12:15],a[68:71],v[196:199],v[12:15]\nbuffer_load_dwordx4 v240,s[48:51],s95 offen offset:0 sc1 lds\nM v[72:75],a[68:71],v[200:203],v[72:75]\nv_exp_f32 v159,v159\n" \
"M v[76:79],a[68:71],v[204:207],v[76:79]\nv_exp_f32 v160,v160\nM v[16:19],a[72:75],v[192:195],v[16:19]\nv_exp_f32 v161,v161\nM v[20:23],a[72:75],v[196:199],v[20:23]\nds_read_b128 a[8:11],v250 offset:2048\nds_read_b128 a[12:15],v250 offset:3072\nM v[80:83],a[72:75],v[200:203],v[80:83]\n" \
"v_exp_f32 v162,v162\nM v[84:87],a[72:75],v[204:207],v[84:87]\nv_exp_f32 v163,v163\nM v[24:27],a[76:79],v[192:195],v[24:27]\nv_exp_f32 v176,v176\nM v[28:31],a[76:79],v[196:199],v[28:31]\nv_exp_f32 v177,v177\nM v[88:91],a[76:79],v[200:203],v[88:91]\n" \
"v_exp_f32 v178,v178\nM v[92:95],a[76:79],v[204:207],v[92:95]\nv_exp_f32 v179,v179\nM v[32:35],a[80:83],v[192:195],v[32:35]\nv_exp_f32 v164,v164\nM v[36:39],a[80:83],v[196:199],v[36:39]\nds_read_b128 a[16:19],v250 offset:4096\nds_read_b128 a[20:23],v250 offset:5120\n" \
"M v[96:99],a[80:83],v[200:203],v[96:99]\nv_exp_f32 v165,v165\nM v[100:103],a[80:83],v[204:207],v[100:103]\nv_exp_f32 v166,v166\nM v[40:43],a[84:87],v[192:195],v[40:43]\nv_exp_f32 v167,v167\nM v[44:47],a[84:87],v[196:199],v[44:47]\nbuffer_load_dwordx4 v240,s[48:51],s95 offen offset:1024 sc1 lds\n" \
"M v[104:107],a[84:87],v[200:203],v[104:107]\nv_exp_f32 v180,v180\nM v[108:111],a[84:87],v[204:207],v[108:111]\nv_exp_f32 v181,v181\nM v[48:51],a[88:91],v[192:195],v[48:51]\nv_exp_f32 v182,v182\nM v[52:55],a[88:91],v[196:199],v[52:55]\nds_read_b128 a[24:27],v250 offset:6144\n" \
"ds_read_b128 a[28:31],v250 offset:7168\nM v[112:115],a[88:91],v[200:203],v[112:115]\nv_exp_f32 v183,v183\nM v[116:119],a[88:91],v[204:207],v[116:119]\nv_exp_f32 v168,v168\nM v[56:59],a[92:95],v[192:195],v[56:59]\nv_exp_f32 v169,v169\nM v[60:63],a[92:95],v[196:199],v[60:63]\n" \
"v_exp_f32 v170,v170\nM v[120:123],a[92:95],v[200:203],v[120:123]\nv_exp_f32 v171,v171\nM v[124:127],a[92:95],v[204:207],v[124:127]\nM a[192:195],a[208:211],v[192:195],a[192:195]\nv_cvt_pk_bf16_f32 v192,v128,v129\nv_cvt_pk_bf16_f32 v193,v130,v131\nM a[196:199],a[208:211],v[196:199],a[196:199]\n" \
"ds_read_b128 a[32:35],v250 offset:8192\nds_read_b128 a[36:39],v250 offset:9216\nM a[200:203],a[208:211],v[200:203],a[200:203]\nv_cvt_pk_bf16_f32 v194,v144,v145\nv_cvt_pk_bf16_f32 v195,v146,v147\nM a[204:207],a[208:211],v[204:207],a[204:207]\nv_cvt_pk_bf16_f32 v196,v132,v133\nv_cvt_pk_bf16_f32 v197,v134,v135\n" \
"M v[0:3],a[96:99],v[208:211],v[0:3]\nv_cvt_pk_bf16_f32 v198,v148,v149\nv_cvt_pk_bf16_f32 v199,v150,v151\nM v[4:7],a[96:99],v[212:215],v[4:7]\nbuffer_load_dwordx4 v240,s[48:51],s95 offen offset:2048 sc1 lds\nM v[64:67],a[96:99],v[216:219],v[64:67]\nv_cvt_pk_bf16_f32 v200,v136,v137\nv_cvt_pk_bf16_f32 v201,v138,v139\n" \
"M v[68:71],a[96:99],v[220:223],v[68:71]\nv_cvt_pk_bf16_f32 v202,v152,v153\nv_cvt_pk_bf16_f32 v203,v154,v155\nM v[8:11],a[100:103],v[208:211],v[8:11]\nv_cvt_pk_bf16_f32 v204,v140,v141\nv_cvt_pk_bf16_f32 v205,v142,v143\nM v[12:15],a[100:103],v[212:215],v[12:15]\nds_read_b128 a[40:43],v250 offset:10240\n" \
"ds_read_b128 a[44:47],v250 offset:11264\nM v[72:75],a[100:103],v[216:219],v[72:75]\nv_cvt_pk_bf16_f32 v206,v156,v157\nv_cvt_pk_bf16_f32 v207,v158,v159\nM v[76:79],a[100:103],v[220:223],v[76:79]\nM v[16:19],a[104:107],v[208:211],v[16:19]\nM v[20:23],a[104:107],v[212:215],v[20:23]\nM v[80:83],a[104:107],v[216:219],v[80:83]\n" \
"M v[84:87],a[104:107],v[220:223],v[84:87]\nM v[24:27],a[108:111],v[208:211],v[24:27]\nM v[28:31],a[108:111],v[212:215],v[28:31]\nds_read_b128 a[48:51],v250 offset:12288\nds_read_b128 a[52:55],v250 offset:13312\nM v[88:91],a[108:111],v[216:219],v[88:91]\nM v[92:95],a[108:111],v[220:223],v[92:95]\nM v[32:35],a[112:115],v[208:211],v[32:35]\n" \
"M v[36:39],a[112:115],v[212:215],v[36:39]\nbuffer_load_dwordx4 v240,s[48:51],s95 offen offset:3072 sc1 lds\nM v[96:99],a[112:115],v[216:219],v[96:99]\ns_cmp_eq_u32 s95,s96\ns_cselect_b32 s72,0xc000,s98\ns_add_u32 s95,s95,s72\nM v[100:103],a[112:115],v[220:223],v[100:103]\nM v[40:43],a[116:119],v[208:211],v[40:43]\n" \
"M v[44:47],a[116:119],v[212:215],v[44:47]\nds_read_b128 a[56:59],v250 offset:14336\nds_read_b128 a[60:63],v250 offset:15360\nM v[104:107],a[116:119],v[216:219],v[104:107]\nv_add_u32 v250,s94,v241\nM v[108:111],a[116:119],v[220:223],v[108:111]\nM v[48:51],a[120:123],v[208:211],v[48:51]\nM v[52:55],a[120:123],v[212:215],v[52:55]\n" \
"M v[112:115],a[120:123],v[216:219],v[112:115]\nM v[116:119],a[120:123],v[220:223],v[116:119]\nM v[56:59],a[124:127],v[208:211],v[56:59]\nM v[60:63],a[124:127],v[212:215],v[60:63]\nM v[120:123],a[124:127],v[216:219],v[120:123]\nbuffer_load_dword v247,v254,s[56:59],s1 offen\nM v[124:127],a[124:127],v[220:223],v[124:127]\ns_add_u32 s1,s1,s0\n" \
"M a[192:195],a[208:211],v[208:211],a[192:195]\nM a[196:199],a[208:211],v[212:215],a[196:199]\nM a[200:203],a[208:211],v[216:219],a[200:203]\nM a[204:207],a[208:211],v[220:223],a[204:207]\ns_cmp_lg_u32 s95,s97\ns_cbranch_scc1 .Ldual\n.Ldf:\ns_waitcnt lgkmcnt(4)\n" \
"M v[128:131],a[0:3],a[128:131],v[224:227]\ns_add_u32 m0,s94,s64\nv_lshrrev_b32 v248,2,v249\nM v[128:131],a[4:7],a[132:135],v[128:131]\nds_read_b128 a[64:67],v250 offset:0\nds_read_b128 a[68:71],v250 offset:1024\nM v[128:131],a[8:11],a[136:139],v[128:131]\nv_mul_u32_u24 v248,3584,v248\n" \
"v_and_b32 v251,3,v249\nM v[128:131],a[12:15],a[140:143],v[128:131]\ns_mul_i32 s70,s7,458752\nv_lshl_add_u32 v248,v251,3,v248\nM v[132:135],a[0:3],a[144:147],v[228:231]\nbuffer_load_dwordx4 v240,s[44:47],s95 offen offset:0 sc1 lds\nM v[132:135],a[4:7],a[148:151],v[132:135]\ns_add_u32 s70,s70,s2\n" \
"v_add_u32 v248,s70,v248\nM v[132:135],a[8:11],a[152:155],v[132:135]\nv_and_b32 v248,s3,v248\nv_cvt_pk_bf16_f32 v208,v160,v161\nM v[132:135],a[12:15],a[156:159],v[132:135]\nds_read_b128 a[72:75],v250 offset:2048\nds_read_b128 a[76:79],v250 offset:3072\nM v[136:139],a[0:3],a[160:163],v[232:235]\n" \
"v_cvt_pk_bf16_f32 v209,v162,v163\nv_cvt_pk_bf16_f32 v212,v164,v165\nM v[136:139],a[4:7],a[164:167],v[136:139]\nv_cvt_pk_bf16_f32 v213,v166,v167\nv_cvt_pk_bf16_f32 v216,v168,v169\nM v[136:139],a[8:11],a[168:171],v[136:139]\nv_cvt_pk_bf16_f32 v217,v170,v171\nv_cvt_pk_bf16_f32 v210,v176,v177\n" \
"M v[136:139],a[12:15],a[172:175],v[136:139]\nv_exp_f32 v172,v172\nM v[140:143],a[0:3],a[176:179],v[236:239]\nv_exp_f32 v173,v173\nM v[140:143],a[4:7],a[180:183],v[140:143]\nds_read_b128 a[80:83],v250 offset:4096\nds_read_b128 a[84:87],v250 offset:5120\nM v[140:143],a[8:11],a[184:187],v[140:143]\n" \
"global_load_dwordx2 a[212:213],v248,s[22:23]\nM v[140:143],a[12:15],a[188:191],v[140:143]\nv_exp_f32 v174,v174\nM v[144:147],a[16:19],a[128:131],v[224:227]\nv_exp_f32 v175,v175\nM v[144:147],a[20:23],a[132:135],v[144:147]\nv_cvt_pk_bf16_f32 v220,v172,v173\nv_cvt_pk_bf16_f32 v221,v174,v175\n" \
"M v[144:147],a[24:27],a[136:139],v[144:147]\nv_cvt_pk_bf16_f32 v211,v178,v179\ns_and_b32 s71,s3,57344\nM v[144:147],a[28:31],a[140:143],v[144:147]\nds_read_b128 a[88:91],v250 offset:6144\nds_read_b128 a[92:95],v250 offset:7168\nM v[148:151],a[16:19],a[144:147],v[228:231]\nv_cvt_pk_bf16_f32 v214,v180,v181\n" \
"v_cvt_pk_bf16_f32 v215,v182,v183\nM v[148:151],a[20:23],a[148:151],v[148:151]\nv_exp_f32 v184,v184\nM v[148:151],a[24:27],a[152:155],v[148:151]\nbuffer_load_dwordx4 v240,s[44:47],s95 offen offset:1024 sc1 lds\nM v[148:151],a[28:31],a[156:159],v[148:151]\nv_exp_f32 v185,v185\nM v[152:155],a[16:19],a[160:163],v[232:235]\n" \
"v_exp_f32 v186,v186\nM v[152:155],a[20:23],a[164:167],v[152:155]\nds_read_b128 a[96:99],v250 offset:8192\nds_read_b128 a[100:103],v250 offset:9216\nM v[152:155],a[24:27],a[168:171],v[152:155]\nv_exp_f32 v187,v187\nM v[152:155],a[28:31],a[172:175],v[152:155]\nv_cvt_pk_bf16_f32 v218,v184,v185\n" \
"v_cvt_pk_bf16_f32 v219,v186,v187\nM v[156:159],a[16:19],a[176:179],v[236:239]\nv_exp_f32 v188,v188\nM v[156:159],a[20:23],a[180:183],v[156:159]\nv_exp_f32 v189,v189\nM v[156:159],a[24:27],a[184:187],v[156:159]\nv_exp_f32 v190,v190\nM v[156:159],a[28:31],a[188:191],v[156:159]\n" \
"ds_read_b128 a[104:107],v250 offset:10240\nds_read_b128 a[108:111],v250 offset:11264\nM v[160:163],a[32:35],a[128:131],v[224:227]\nglobal_load_dwordx2 a[228:229],v248,s[24:25]\nM v[160:163],a[36:39],a[132:135],v[160:163]\nv_add_u32 v248,s71,v248\nv_cvt_pk_bf16_f32 v222,v188,v189\nM v[160:163],a[40:43],a[136:139],v[160:163]\n" \
"v_exp_f32 v191,v191\nM v[160:163],a[44:47],a[140:143],v[160:163]\nv_cvt_pk_bf16_f32 v223,v190,v191\ns_add_u32 s94,s94,0x4000\nM v[164:167],a[32:35],a[144:147],v[228:231]\nv_exp_f32 v128,v128\nM v[164:167],a[36:39],a[148:151],v[164:167]\nds_read_b128 a[112:115],v250 offset:12288\n" \
"ds_read_b128 a[116:119],v250 offset:13312\nM v[164:167],a[40:43],a[152:155],v[164:167]\nv_exp_f32 v129,v129\nM v[164:167],a[44:47],a[156:159],v[164:167]\nv_exp_f32 v130,v130\nM v[168:171],a[32:35],a[160:163],v[232:235]\nbuffer_load_dwordx4 v240,s[44:47],s95 offen offset:2048 sc1 lds\nM v[168:171],a[36:39],a[164:167],v[168:171]\n" \
"v_exp_f32 v131,v131\nM v[168:171],a[40:43],a[168:171],v[168:171]\nv_exp_f32 v144,v144\nM v[168:171],a[44:47],a[172:175],v[168:171]\nds_read_b128 a[120:123],v250 offset:14336\nds_read_b128 a[124:127],v250 offset:15360\nM v[172:175],a[32:35],a[176:179],v[236:239]\nv_xor_b32 v250,0x18000,v250\n" \
"s_and_b32 s94,s94,0xffff\nM v[172:175],a[36:39],a[180:183],v[172:175]\nv_exp_f32 v145,v145\nM v[172:175],a[40:43],a[184:187],v[172:175]\nv_exp_f32 v146,v146\nM v[172:175],a[44:47],a[188:191],v[172:175]\ns_waitcnt lgkmcnt(4)\nM v[176:179],a[48:51],a[128:131],v[224:227]\n" \
"v_exp_f32 v147,v147\nM v[176:179],a[52:55],a[132:135],v[176:179]\nv_exp_f32 v132,v132\nM v[176:179],a[56:59],a[136:139],v[176:179]\nglobal_load_dwordx2 a[214:215],v248,s[22:23]\nM v[176:179],a[60:63],a[140:143],v[176:179]\nv_exp_f32 v133,v133\nM v[180:183],a[48:51],a[144:147],v[228:231]\n" \
"v_exp_f32 v134,v134\nM v[180:183],a[52:55],a[148:151],v[180:183]\nv_exp_f32 v135,v135\nM v[180:183],a[56:59],a[152:155],v[180:183]\nv_exp_f32 v148,v148\nM v[180:183],a[60:63],a[156:159],v[180:183]\nv_exp_f32 v149,v149\nM v[184:187],a[48:51],a[160:163],v[232:235]\n" \
"v_exp_f32 v150,v150\nM v[184:187],a[52:55],a[164:167],v[184:187]\nv_exp_f32 v151,v151\nM v[184:187],a[56:59],a[168:171],v[184:187]\nbuffer_load_dwordx4 v240,s[44:47],s95 offen offset:3072 sc1 lds\nM v[184:187],a[60:63],a[172:175],v[184:187]\nv_exp_f32 v136,v136\nM v[188:191],a[48:51],a[176:179],v[236:239]\n" \
"v_exp_f32 v137,v137\nM v[188:191],a[52:55],a[180:183],v[188:191]\nv_exp_f32 v138,v138\nM v[188:191],a[56:59],a[184:187],v[188:191]\nglobal_load_dwordx2 a[230:231],v248,s[24:25]\nM v[188:191],a[60:63],a[188:191],v[188:191]\ns_add_u32 m0,m0,0x10000\nv_add_u32 v248,s71,v248\n" \
"M v[0:3],a[64:67],v[192:195],v[0:3]\ns_waitcnt vmcnt(16) lgkmcnt(0)\ns_barrier\nM v[4:7],a[64:67],v[196:199],v[4:7]\nds_read_b128 a[0:3],v250 offset:0\nds_read_b128 a[4:7],v250 offset:1024\nM v[64:67],a[64:67],v[200:203],v[64:67]\nv_exp_f32 v139,v139\n" \
"M v[68:71],a[64:67],v[204:207],v[68:71]\nv_exp_f32 v152,v152\nM v[8:11],a[68:71],v[192:195],v[8:11]\nv_exp_f32 v153,v153\nM v[12:15],a[68:71],v[196:199],v[12:15]\nbuffer_load_dwordx4 v240,s[48:51],s95 offen offset:0 sc1 lds\nM v[72:75],a[68:71],v[200:203],v[72:75]\nv_exp_f32 v154,v154\n" \
"M v[76:79],a[68:71],v[204:207],v[76:79]\nv_exp_f32 v155,v155\nM v[16:19],a[72:75],v[192:195],v[16:19]\nv_exp_f32 v140,v140\nM v[20:23],a[72:75],v[196:199],v[20:23]\nds_read_b128 a[8:11],v250 offset:2048\nds_read_b128 a[12:15],v250 offset:3072\nM v[80:83],a[72:75],v[200:203],v[80:83]\n" \
"v_exp_f32 v141,v141\nM v[84:87],a[72:75],v[204:207],v[84:87]\nv_exp_f32 v142,v142\nM v[24:27],a[76:79],v[192:195],v[24:27]\nv_exp_f32 v143,v143\nM v[28:31],a[76:79],v[196:199],v[28:31]\nglobal_load_dwordx2 a[216:217],v248,s[22:23]\nM v[88:91],a[76:79],v[200:203],v[88:91]\n" \
"v_exp_f32 v156,v156\nM v[92:95],a[76:79],v[204:207],v[92:95]\nv_exp_f32 v157,v157\nM v[32:35],a[80:83],v[192:195],v[32:35]\nv_exp_f32 v158,v158\nM v[36:39],a[80:83],v[196:199],v[36:39]\nds_read_b128 a[16:19],v250 offset:4096\nds_read_b128 a[20:23],v250 offset:5120\n" \
"M v[96:99],a[80:83],v[200:203],v[96:99]\nv_exp_f32 v159,v159\nM v[100:103],a[80:83],v[204:207],v[100:103]\nv_exp_f32 v160,v160\nM v[40:43],a[84:87],v[192:195],v[40:43]\nv_exp_f32 v161,v161\nM v[44:47],a[84:87],v[196:199],v[44:47]\nbuffer_load_dwordx4 v240,s[48:51],s95 offen offset:1024 sc1 lds\n" \
"M v[104:107],a[84:87],v[200:203],v[104:107]\nv_exp_f32 v162,v162\nM v[108:111],a[84:87],v[204:207],v[108:111]\nv_exp_f32 v163,v163\nM v[48:51],a[88:91],v[192:195],v[48:51]\nv_exp_f32 v176,v176\nM v[52:55],a[88:91],v[196:199],v[52:55]\nds_read_b128 a[24:27],v250 offset:6144\n" \
"ds_read_b128 a[28:31],v250 offset:7168\nM v[112:115],a[88:91],v[200:203],v[112:115]\nv_exp_f32 v177,v177\nM v[116:119],a[88:91],v[204:207],v[116:119]\nv_exp_f32 v178,v178\nM v[56:59],a[92:95],v[192:195],v[56:59]\nv_exp_f32 v179,v179\nM v[60:63],a[92:95],v[196:199],v[60:63]\n" \
"global_load_dwordx2 a[232:233],v248,s[24:25]\nM v[120:123],a[92:95],v[200:203],v[120:123]\nv_exp_f32 v164,v164\nM v[124:127],a[92:95],v[204:207],v[124:127]\nv_exp_f32 v165,v165\nM a[192:195],a[208:211],v[192:195],a[192:195]\nv_add_u32 v248,s71,v248\nv_cvt_pk_bf16_f32 v192,v128,v129\n" \
"M a[196:199],a[208:211],v[196:199],a[196:199]\nds_read_b128 a[32:35],v250 offset:8192\nds_read_b128 a[36:39],v250 offset:9216\nM a[200:203],a[208:211],v[200:203],a[200:203]\nv_cvt_pk_bf16_f32 v193,v130,v131\nv_cvt_pk_bf16_f32 v194,v144,v145\nM a[204:207],a[208:211],v[204:207],a[204:207]\nv_cvt_pk_bf16_f32 v195,v146,v147\n" \
"v_cvt_pk_bf16_f32 v196,v132,v133\nM v[0:3],a[96:99],v[208:211],v[0:3]\nv_cvt_pk_bf16_f32 v197,v134,v135\nv_cvt_pk_bf16_f32 v198,v148,v149\nM v[4:7],a[96:99],v[212:215],v[4:7]\nbuffer_load_dwordx4 v240,s[48:51],s95 offen offset:2048 sc1 lds\nM v[64:67],a[96:99],v[216:219],v[64:67]\nv_cvt_pk_bf16_f32 v199,v150,v151\n" \
"v_cvt_pk_bf16_f32 v200,v136,v137\nM v[68:71],a[96:99],v[220:223],v[68:71]\nv_cvt_pk_bf16_f32 v201,v138,v139\nv_cvt_pk_bf16_f32 v202,v152,v153\nM v[8:11],a[100:103],v[208:211],v[8:11]\nv_cvt_pk_bf16_f32 v203,v154,v155\nv_cvt_pk_bf16_f32 v204,v140,v141\nM v[12:15],a[100:103],v[212:215],v[12:15]\n" \
"ds_read_b128 a[40:43],v250 offset:10240\nds_read_b128 a[44:47],v250 offset:11264\nM v[72:75],a[100:103],v[216:219],v[72:75]\nv_cvt_pk_bf16_f32 v205,v142,v143\nv_cvt_pk_bf16_f32 v206,v156,v157\nM v[76:79],a[100:103],v[220:223],v[76:79]\nv_exp_f32 v166,v166\nM v[16:19],a[104:107],v[208:211],v[16:19]\n" \
"v_exp_f32 v167,v167\nM v[20:23],a[104:107],v[212:215],v[20:23]\nglobal_load_dwordx2 a[218:219],v248,s[22:23]\nM v[80:83],a[104:107],v[216:219],v[80:83]\nv_exp_f32 v180,v180\nM v[84:87],a[104:107],v[220:223],v[84:87]\nv_exp_f32 v181,v181\nM v[24:27],a[108:111],v[208:211],v[24:27]\n" \
"v_exp_f32 v182,v182\nM v[28:31],a[108:111],v[212:215],v[28:31]\nds_read_b128 a[48:51],v250 offset:12288\nds_read_b128 a[52:55],v250 offset:13312\nM v[88:91],a[108:111],v[216:219],v[88:91]\nv_exp_f32 v183,v183\nM v[92:95],a[108:111],v[220:223],v[92:95]\nv_exp_f32 v168,v168\n" \
"M v[32:35],a[112:115],v[208:211],v[32:35]\nv_exp_f32 v169,v169\nM v[36:39],a[112:115],v[212:215],v[36:39]\nbuffer_load_dwordx4 v240,s[48:51],s95 offen offset:3072 sc1 lds\nM v[96:99],a[112:115],v[216:219],v[96:99]\nv_cvt_pk_bf16_f32 v207,v158,v159\ns_add_u32 s95,s95,0x4000\nM v[100:103],a[112:115],v[220:223],v[100:103]\n" \
"v_exp_f32 v170,v170\nM v[40:43],a[116:119],v[208:211],v[40:43]\nv_exp_f32 v171,v171\nM v[44:47],a[116:119],v[212:215],v[44:47]\nds_read_b128 a[56:59],v250 offset:14336\nds_read_b128 a[60:63],v250 offset:15360\nM v[104:107],a[116:119],v[216:219],v[104:107]\nv_add_u32 v250,s94,v241\n" \
"M v[108:111],a[116:119],v[220:223],v[108:111]\nM v[48:51],a[120:123],v[208:211],v[48:51]\nM v[52:55],a[120:123],v[212:215],v[52:55]\nglobal_load_dwordx2 a[234:235],v248,s[24:25]\nM v[112:115],a[120:123],v[216:219],v[112:115]\nv_add_u32 v248,s71,v248\nM v[116:119],a[120:123],v[220:223],v[116:119]\nM v[56:59],a[124:127],v[208:211],v[56:59]\n" \
"M v[60:63],a[124:127],v[212:215],v[60:63]\nM v[120:123],a[124:127],v[216:219],v[120:123]\nM v[124:127],a[124:127],v[220:223],v[124:127]\nM a[192:195],a[208:211],v[208:211],a[192:195]\nM a[196:199],a[208:211],v[212:215],a[196:199]\nM a[200:203],a[208:211],v[216:219],a[200:203]\nM a[204:207],a[208:211],v[220:223],a[204:207]\ns_waitcnt lgkmcnt(4)\n" \
"M v[128:131],a[0:3],a[128:131],v[224:227]\ns_add_u32 m0,s94,s64\nv_cvt_pk_bf16_f32 v208,v160,v161\nM v[128:131],a[4:7],a[132:135],v[128:131]\nds_read_b128 a[64:67],v250 offset:0\nds_read_b128 a[68:71],v250 offset:1024\nM v[128:131],a[8:11],a[136:139],v[128:131]\nv_cvt_pk_bf16_f32 v209,v162,v163\n" \
"v_cvt_pk_bf16_f32 v212,v164,v165\nM v[128:131],a[12:15],a[140:143],v[128:131]\nv_cvt_pk_bf16_f32 v213,v166,v167\nv_cvt_pk_bf16_f32 v216,v168,v169\nM v[132:135],a[0:3],a[144:147],v[228:231]\nbuffer_load_dwordx4 v240,s[44:47],s95 offen offset:0 sc1 lds\nM v[132:135],a[4:7],a[148:151],v[132:135]\nv_cvt_pk_bf16_f32 v217,v170,v171\n" \
"v_cvt_pk_bf16_f32 v210,v176,v177\nM v[132:135],a[8:11],a[152:155],v[132:135]\nv_exp_f32 v172,v172\nM v[132:135],a[12:15],a[156:159],v[132:135]\nds_read_b128 a[72:75],v250 offset:2048\nds_read_b128 a[76:79],v250 offset:3072\nM v[136:139],a[0:3],a[160:163],v[232:235]\nv_exp_f32 v173,v173\n" \
"M v[136:139],a[4:7],a[164:167],v[136:139]\nv_exp_f32 v174,v174\nM v[136:139],a[8:11],a[168:171],v[136:139]\nv_exp_f32 v175,v175\nM v[136:139],a[12:15],a[172:175],v[136:139]\nv_cvt_pk_bf16_f32 v220,v172,v173\nv_cvt_pk_bf16_f32 v221,v174,v175\nM v[140:143],a[0:3],a[176:179],v[236:239]\n" \
"v_cvt_pk_bf16_f32 v211,v178,v179\nv_cvt_pk_bf16_f32 v214,v180,v181\nM v[140:143],a[4:7],a[180:183],v[140:143]\nds_read_b128 a[80:83],v250 offset:4096\nds_read_b128 a[84:87],v250 offset:5120\nM v[140:143],a[8:11],a[184:187],v[140:143]\nglobal_load_dwordx2 a[220:221],v248,s[22:23]\nM v[140:143],a[12:15],a[188:191],v[140:143]\n" \
"v_cvt_pk_bf16_f32 v215,v182,v183\ns_add_u32 s94,s94,0x4000\nM v[144:147],a[16:19],a[128:131],v[224:227]\nv_exp_f32 v184,v184\nM v[144:147],a[20:23],a[132:135],v[144:147]\nv_exp_f32 v185,v185\nM v[144:147],a[24:27],a[136:139],v[144:147]\nv_exp_f32 v186,v186\n" \
"M v[144:147],a[28:31],a[140:143],v[144:147]\nds_read_b128 a[88:91],v250 offset:6144\nds_read_b128 a[92:95],v250 offset:7168\nM v[148:151],a[16:19],a[144:147],v[228:231]\nv_exp_f32 v187,v187\nM v[148:151],a[20:23],a[148:151],v[148:151]\nv_cvt_pk_bf16_f32 v218,v184,v185\nv_cvt_pk_bf16_f32 v219,v186,v187\n" \
"M v[148:151],a[24:27],a[152:155],v[148:151]\nbuffer_load_dwordx4 v240,s[44:47],s95 offen offset:1024 sc1 lds\nM v[148:151],a[28:31],a[156:159],v[148:151]\nv_exp_f32 v188,v188\nM v[152:155],a[16:19],a[160:163],v[232:235]\nv_exp_f32 v189,v189\nM v[152:155],a[20:23],a[164:167],v[152:155]\nds_read_b128 a[96:99],v250 offset:8192\n" \
"ds_read_b128 a[100:103],v250 offset:9216\nM v[152:155],a[24:27],a[168:171],v[152:155]\nv_exp_f32 v190,v190\nM v[152:155],a[28:31],a[172:175],v[152:155]\nv_exp_f32 v191,v191\nM v[156:159],a[16:19],a[176:179],v[236:239]\nv_cvt_pk_bf16_f32 v222,v188,v189\nv_cvt_pk_bf16_f32 v223,v190,v191\n" \
"M v[156:159],a[20:23],a[180:183],v[156:159]\nv_exp_f32 v128,v128\nM v[156:159],a[24:27],a[184:187],v[156:159]\nv_exp_f32 v129,v129\nM v[156:159],a[28:31],a[188:191],v[156:159]\nds_read_b128 a[104:107],v250 offset:10240\nds_read_b128 a[108:111],v250 offset:11264\nM v[160:163],a[32:35],a[128:131],v[224:227]\n" \
"global_load_dwordx2 a[236:237],v248,s[24:25]\nM v[160:163],a[36:39],a[132:135],v[160:163]\nv_add_u32 v248,s71,v248\ns_and_b32 s94,s94,0xffff\nM v[160:163],a[40:43],a[136:139],v[160:163]\nv_exp_f32 v130,v130\nM v[160:163],a[44:47],a[140:143],v[160:163]\nv_exp_f32 v131,v131\n" \
"M v[164:167],a[32:35],a[144:147],v[228:231]\nv_exp_f32 v144,v144\nM v[164:167],a[36:39],a[148:151],v[164:167]\nds_read_b128 a[112:115],v250 offset:12288\nds_read_b128 a[116:119],v250 offset:13312\nM v[164:167],a[40:43],a[152:155],v[164:167]\nv_exp_f32 v145,v145\nM v[164:167],a[44:47],a[156:159],v[164:167]\n" \
"v_exp_f32 v146,v146\nM v[168:171],a[32:35],a[160:163],v[232:235]\nbuffer_load_dwordx4 v240,s[44:47],s95 offen offset:2048 sc1 lds\nM v[168:171],a[36:39],a[164:167],v[168:171]\nv_exp_f32 v147,v147\nM v[168:171],a[40:43],a[168:171],v[168:171]\nv_exp_f32 v132,v132\nM v[168:171],a[44:47],a[172:175],v[168:171]\n" \
"ds_read_b128 a[120:123],v250 offset:14336\nds_read_b128 a[124:127],v250 offset:15360\nM v[172:175],a[32:35],a[176:179],v[236:239]\nv_exp_f32 v133,v133\nM v[172:175],a[36:39],a[180:183],v[172:175]\nv_exp_f32 v134,v134\nM v[172:175],a[40:43],a[184:187],v[172:175]\nv_exp_f32 v135,v135\n" \
"M v[172:175],a[44:47],a[188:191],v[172:175]\nv_xor_b32 v250,0x18000,v250\ns_waitcnt lgkmcnt(3)\nM v[176:179],a[48:51],a[128:131],v[224:227]\nv_exp_f32 v148,v148\nM v[176:179],a[52:55],a[132:135],v[176:179]\nv_exp_f32 v149,v149\nM v[176:179],a[56:59],a[136:139],v[176:179]\n" \
"global_load_dwordx2 a[222:223],v248,s[22:23]\nM v[176:179],a[60:63],a[140:143],v[176:179]\nv_exp_f32 v150,v150\nM v[180:183],a[48:51],a[144:147],v[228:231]\nv_exp_f32 v151,v151\nM v[180:183],a[52:55],a[148:151],v[180:183]\nv_exp_f32 v136,v136\nM v[180:183],a[56:59],a[152:155],v[180:183]\n" \
"v_exp_f32 v137,v137\nM v[180:183],a[60:63],a[156:159],v[180:183]\nv_exp_f32 v138,v138\nM v[184:187],a[48:51],a[160:163],v[232:235]\nv_exp_f32 v139,v139\nM v[184:187],a[52:55],a[164:167],v[184:187]\nv_exp_f32 v152,v152\nM v[184:187],a[56:59],a[168:171],v[184:187]\n" \
"buffer_load_dwordx4 v240,s[44:47],s95 offen offset:3072 sc1 lds\nM v[184:187],a[60:63],a[172:175],v[184:187]\nv_exp_f32 v153,v153\nM v[188:191],a[48:51],a[176:179],v[236:239]\nv_exp_f32 v154,v154\nM v[188:191],a[52:55],a[180:183],v[188:191]\nv_exp_f32 v155,v155\nM v[188:191],a[56:59],a[184:187],v[188:191]\n" \
"global_load_dwordx2 a[238:239],v248,s[24:25]\nM v[188:191],a[60:63],a[188:191],v[188:191]\ns_add_u32 m0,m0,0x10000\nv_add_u32 v248,s71,v248\nM v[0:3],a[64:67],v[192:195],v[0:3]\ns_waitcnt vmcnt(16) lgkmcnt(0)\ns_barrier\nM v[4:7],a[64:67],v[196:199],v[4:7]\n" \
"ds_read_b128 a[0:3],v250 offset:0\nds_read_b128 a[4:7],v250 offset:1024\nM v[64:67],a[64:67],v[200:203],v[64:67]\nv_exp_f32 v140,v140\nM v[68:71],a[64:67],v[204:207],v[68:71]\nv_exp_f32 v141,v141\nM v[8:11],a[68:71],v[192:195],v[8:11]\nv_exp_f32 v142,v142\n" \
"M v[12:15],a[68:71],v[196:199],v[12:15]\nbuffer_load_dwordx4 v240,s[48:51],s95 offen offset:0 sc1 lds\nM v[72:75],a[68:71],v[200:203],v[72:75]\nv_exp_f32 v143,v143\nM v[76:79],a[68:71],v[204:207],v[76:79]\nv_exp_f32 v156,v156\nM v[16:19],a[72:75],v[192:195],v[16:19]\nv_exp_f32 v157,v157\n" \
"M v[20:23],a[72:75],v[196:199],v[20:23]\nds_read_b128 a[8:11],v250 offset:2048\nds_read_b128 a[12:15],v250 offset:3072\nM v[80:83],a[72:75],v[200:203],v[80:83]\nv_exp_f32 v158,v158\nM v[84:87],a[72:75],v[204:207],v[84:87]\nv_exp_f32 v159,v159\nM v[24:27],a[76:79],v[192:195],v[24:27]\n" \
"v_exp_f32 v160,v160\nM v[28:31],a[76:79],v[196:199],v[28:31]\nglobal_load_dwordx2 a[224:225],v248,s[22:23]\nM v[88:91],a[76:79],v[200:203],v[88:91]\nv_exp_f32 v161,v161\nM v[92:95],a[76:79],v[204:207],v[92:95]\nv_exp_f32 v162,v162\nM v[32:35],a[80:83],v[192:195],v[32:35]\n" \
"v_exp_f32 v163,v163\nM v[36:39],a[80:83],v[196:199],v[36:39]\nds_read_b128 a[16:19],v250 offset:4096\nds_read_b128 a[20:23],v250 offset:5120\nM v[96:99],a[80:83],v[200:203],v[96:99]\nv_exp_f32 v176,v176\nM v[100:103],a[80:83],v[204:207],v[100:103]\nv_exp_f32 v177,v177\n" \
"M v[40:43],a[84:87],v[192:195],v[40:43]\nv_exp_f32 v178,v178\nM v[44:47],a[84:87],v[196:199],v[44:47]\nbuffer_load_dwordx4 v240,s[48:51],s95 offen offset:1024 sc1 lds\nM v[104:107],a[84:87],v[200:203],v[104:107]\nv_exp_f32 v179,v179\nM v[108:111],a[84:87],v[204:207],v[108:111]\nv_exp_f32 v164,v164\n" \
"M v[48:51],a[88:91],v[192:195],v[48:51]\nv_exp_f32 v165,v165\nM v[52:55],a[88:91],v[196:199],v[52:55]\nds_read_b128 a[24:27],v250 offset:6144\nds_read_b128 a[28:31],v250 offset:7168\nM v[112:115],a[88:91],v[200:203],v[112:115]\nv_exp_f32 v166,v166\nM v[116:119],a[88:91],v[204:207],v[116:119]\n" \
"v_exp_f32 v167,v167\nM v[56:59],a[92:95],v[192:195],v[56:59]\nv_exp_f32 v180,v180\nM v[60:63],a[92:95],v[196:199],v[60:63]\nglobal_load_dwordx2 a[240:241],v248,s[24:25]\nM v[120:123],a[92:95],v[200:203],v[120:123]\nv_exp_f32 v181,v181\nM v[124:127],a[92:95],v[204:207],v[124:127]\n" \
"v_exp_f32 v182,v182\nM a[192:195],a[208:211],v[192:195],a[192:195]\nv_add_u32 v248,s71,v248\nv_cvt_pk_bf16_f32 v192,v128,v129\nM a[196:199],a[208:211],v[196:199],a[196:199]\nds_read_b128 a[32:35],v250 offset:8192\nds_read_b128 a[36:39],v250 offset:9216\nM a[200:203],a[208:211],v[200:203],a[200:203]\n" \
"v_cvt_pk_bf16_f32 v193,v130,v131\nv_cvt_pk_bf16_f32 v194,v144,v145\nM a[204:207],a[208:211],v[204:207],a[204:207]\nv_cvt_pk_bf16_f32 v195,v146,v147\nv_cvt_pk_bf16_f32 v196,v132,v133\nM v[0:3],a[96:99],v[208:211],v[0:3]\nv_cvt_pk_bf16_f32 v197,v134,v135\nv_cvt_pk_bf16_f32 v198,v148,v149\n" \
"M v[4:7],a[96:99],v[212:215],v[4:7]\nbuffer_load_dwordx4 v240,s[48:51],s95 offen offset:2048 sc1 lds\nM v[64:67],a[96:99],v[216:219],v[64:67]\nv_cvt_pk_bf16_f32 v199,v150,v151\nv_cvt_pk_bf16_f32 v200,v136,v137\nM v[68:71],a[96:99],v[220:223],v[68:71]\nv_cvt_pk_bf16_f32 v201,v138,v139\nv_cvt_pk_bf16_f32 v202,v152,v153\n" \
"M v[8:11],a[100:103],v[208:211],v[8:11]\nv_cvt_pk_bf16_f32 v203,v154,v155\nv_cvt_pk_bf16_f32 v204,v140,v141\nM v[12:15],a[100:103],v[212:215],v[12:15]\nds_read_b128 a[40:43],v250 offset:10240\nds_read_b128 a[44:47],v250 offset:11264\nM v[72:75],a[100:103],v[216:219],v[72:75]\nv_cvt_pk_bf16_f32 v205,v142,v143\n" \
"v_cvt_pk_bf16_f32 v206,v156,v157\nM v[76:79],a[100:103],v[220:223],v[76:79]\nv_exp_f32 v183,v183\nM v[16:19],a[104:107],v[208:211],v[16:19]\nv_exp_f32 v168,v168\nM v[20:23],a[104:107],v[212:215],v[20:23]\nglobal_load_dwordx2 a[226:227],v248,s[22:23]\nM v[80:83],a[104:107],v[216:219],v[80:83]\n" \
"v_exp_f32 v169,v169\nM v[84:87],a[104:107],v[220:223],v[84:87]\nv_exp_f32 v170,v170\nM v[24:27],a[108:111],v[208:211],v[24:27]\nv_exp_f32 v171,v171\nM v[28:31],a[108:111],v[212:215],v[28:31]\nds_read_b128 a[48:51],v250 offset:12288\nds_read_b128 a[52:55],v250 offset:13312\n" \
"M v[88:91],a[108:111],v[216:219],v[88:91]\nv_cvt_pk_bf16_f32 v207,v158,v159\nM v[92:95],a[108:111],v[220:223],v[92:95]\nM v[32:35],a[112:115],v[208:211],v[32:35]\nM v[36:39],a[112:115],v[212:215],v[36:39]\nbuffer_load_dwordx4 v240,s[48:51],s95 offen offset:3072 sc1 lds\nM v[96:99],a[112:115],v[216:219],v[96:99]\ns_add_u32 s95,s95,0x4000\n" \
"M v[100:103],a[112:115],v[220:223],v[100:103]\nM v[40:43],a[116:119],v[208:211],v[40:43]\nM v[44:47],a[116:119],v[212:215],v[44:47]\nds_read_b128 a[56:59],v250 offset:14336\nds_read_b128 a[60:63],v250 offset:15360\nM v[104:107],a[116:119],v[216:219],v[104:107]\nv_add_u32 v250,s94,v241\nM v[108:111],a[116:119],v[220:223],v[108:111]\n" \
"M v[48:51],a[120:123],v[208:211],v[48:51]\nM v[52:55],a[120:123],v[212:215],v[52:55]\nglobal_load_dwordx2 a[242:243],v248,s[24:25]\nM v[112:115],a[120:123],v[216:219],v[112:115]\nv_add_u32 v248,s71,v248\nM v[116:119],a[120:123],v[220:223],v[116:119]\nM v[56:59],a[124:127],v[208:211],v[56:59]\nM v[60:63],a[124:127],v[212:215],v[60:63]\n" \
"M v[120:123],a[124:127],v[216:219],v[120:123]\nM v[124:127],a[124:127],v[220:223],v[124:127]\nM a[192:195],a[208:211],v[208:211],a[192:195]\nM a[196:199],a[208:211],v[212:215],a[196:199]\nM a[200:203],a[208:211],v[216:219],a[200:203]\nM a[204:207],a[208:211],v[220:223],a[204:207]\ns_cmp_eq_u32 s62,7\ns_cbranch_scc0 .Ltrans\n" \
"s_waitcnt lgkmcnt(4)\nM v[136:139],a[0:3],a[160:163],v[232:235]\ns_add_u32 m0,s94,s64\nv_cvt_pk_bf16_f32 v216,v168,v169\nM v[136:139],a[4:7],a[164:167],v[136:139]\nds_read_b128 a[64:67],v250 offset:0\nds_read_b128 a[68:71],v250 offset:1024\nM v[136:139],a[8:11],a[168:171],v[136:139]\n" \
"buffer_load_dwordx4 v240,s[44:47],s95 offen offset:0 sc1 lds\nM v[136:139],a[12:15],a[172:175],v[136:139]\nv_cvt_pk_bf16_f32 v217,v170,v171\ns_add_u32 s71,s33,16384\nM v[140:143],a[0:3],a[176:179],v[236:239]\nds_read_b128 a[72:75],v250 offset:2048\nds_read_b128 a[76:79],v250 offset:3072\nM v[140:143],a[4:7],a[180:183],v[140:143]\n" \
"buffer_store_dwordx4 a[248:251],v253,s[52:55],s33 offen offset:0 sc1\nM v[140:143],a[8:11],a[184:187],v[140:143]\nv_exp_f32 v172,v172\nM v[140:143],a[12:15],a[188:191],v[140:143]\nds_read_b128 a[80:83],v250 offset:4096\nds_read_b128 a[84:87],v250 offset:5120\nM v[152:155],a[16:19],a[160:163],v[232:235]\nv_exp_f32 v173,v173\n" \
"M v[152:155],a[20:23],a[164:167],v[152:155]\nv_exp_f32 v174,v174\nM v[152:155],a[24:27],a[168:171],v[152:155]\nds_read_b128 a[88:91],v250 offset:6144\nds_read_b128 a[92:95],v250 offset:7168\nM v[152:155],a[28:31],a[172:175],v[152:155]\nbuffer_load_dwordx4 v240,s[44:47],s95 offen offset:1024 sc1 lds\nM v[156:159],a[16:19],a[176:179],v[236:239]\n" \
"v_exp_f32 v175,v175\nM v[156:159],a[20:23],a[180:183],v[156:159]\nds_read_b128 a[96:99],v250 offset:8192\nds_read_b128 a[100:103],v250 offset:9216\nM v[156:159],a[24:27],a[184:187],v[156:159]\nbuffer_store_dwordx4 a[248:251],v253,s[52:55],s33 offen offset:1024 sc1\nM v[156:159],a[28:31],a[188:191],v[156:159]\nv_cvt_pk_bf16_f32 v220,v172,v173\n" \
"v_cvt_pk_bf16_f32 v221,v174,v175\nM v[168:171],a[32:35],a[160:163],v[232:235]\nds_read_b128 a[104:107],v250 offset:10240\nds_read_b128 a[108:111],v250 offset:11264\nM v[168:171],a[36:39],a[164:167],v[168:171]\nv_exp_f32 v184,v184\nM v[168:171],a[40:43],a[168:171],v[168:171]\nv_exp_f32 v185,v185\n" \
"M v[168:171],a[44:47],a[172:175],v[168:171]\nds_read_b128 a[112:115],v250 offset:12288\nds_read_b128 a[116:119],v250 offset:13312\nM v[172:175],a[32:35],a[176:179],v[236:239]\nbuffer_load_dwordx4 v240,s[44:47],s95 offen offset:2048 sc1 lds\nM v[172:175],a[36:39],a[180:183],v[172:175]\nv_exp_f32 v186,v186\nM v[172:175],a[40:43],a[184:187],v[172:175]\n" \
"v_exp_f32 v187,v187\nds_read_b128 a[120:123],v250 offset:14336\nds_read_b128 a[124:127],v250 offset:15360\nM v[172:175],a[44:47],a[188:191],v[172:175]\nbuffer_store_dwordx4 a[248:251],v253,s[52:55],s33 offen offset:2048 sc1\nv_cvt_pk_bf16_f32 v218,v184,v185\nv_cvt_pk_bf16_f32 v219,v186,v187\ns_waitcnt lgkmcnt(6)\n" \
"M v[184:187],a[48:51],a[160:163],v[232:235]\nv_exp_f32 v188,v188\nM v[184:187],a[52:55],a[164:167],v[184:187]\nv_exp_f32 v189,v189\nM v[184:187],a[56:59],a[168:171],v[184:187]\nv_exp_f32 v190,v190\nv_exp_f32 v191,v191\nM v[184:187],a[60:63],a[172:175],v[184:187]\n" \
"v_cvt_pk_bf16_f32 v222,v188,v189\nv_cvt_pk_bf16_f32 v223,v190,v191\nM v[188:191],a[48:51],a[176:179],v[236:239]\nv_xor_b32 v250,0x18000,v250\nv_cvt_pk_bf16_f32 v208,v160,v161\nM v[188:191],a[52:55],a[180:183],v[188:191]\nbuffer_load_dwordx4 v240,s[44:47],s95 offen offset:3072 sc1 lds\nM v[188:191],a[56:59],a[184:187],v[188:191]\n" \
"s_add_u32 m0,m0,0x10000\nv_cvt_pk_bf16_f32 v209,v162,v163\nM v[188:191],a[60:63],a[188:191],v[188:191]\nv_cvt_pk_bf16_f32 v210,v176,v177\nv_cvt_pk_bf16_f32 v211,v178,v179\nM v[0:3],a[64:67],v[192:195],v[0:3]\nbuffer_store_dwordx4 a[248:251],v253,s[52:55],s33 offen offset:3072 sc1\nM v[4:7],a[64:67],v[196:199],v[4:7]\n" \
"v_cvt_pk_bf16_f32 v212,v164,v165\nv_cvt_pk_bf16_f32 v213,v166,v167\nM v[64:67],a[64:67],v[200:203],v[64:67]\ns_waitcnt vmcnt(16) lgkmcnt(0)\ns_barrier\nM v[68:71],a[64:67],v[204:207],v[68:71]\nds_read_b128 a[0:3],v250 offset:0\nds_read_b128 a[4:7],v250 offset:1024\n" \
"M v[8:11],a[68:71],v[192:195],v[8:11]\nv_cvt_pk_bf16_f32 v214,v180,v181\nv_cvt_pk_bf16_f32 v215,v182,v183\nM v[12:15],a[68:71],v[196:199],v[12:15]\nbuffer_store_dwordx4 a[248:251],v253,s[52:55],s71 offen offset:0 sc1\nM v[72:75],a[68:71],v[200:203],v[72:75]\ns_add_u32 s70,s78,16384\ns_add_u32 s94,s94,0x4000\n" \
"M v[76:79],a[68:71],v[204:207],v[76:79]\nbuffer_load_dwordx4 v240,s[48:51],s95 offen offset:0 sc1 lds\nM v[16:19],a[72:75],v[192:195],v[16:19]\nv_exp_f32 v136,v136\nM v[20:23],a[72:75],v[196:199],v[20:23]\nbuffer_store_dwordx4 a[248:251],v253,s[52:55],s71 offen offset:1024 sc1\nM v[80:83],a[72:75],v[200:203],v[80:83]\nv_exp_f32 v137,v137\n" \
"M v[84:87],a[72:75],v[204:207],v[84:87]\nds_read_b128 a[8:11],v250 offset:2048\nds_read_b128 a[12:15],v250 offset:3072\nM v[24:27],a[76:79],v[192:195],v[24:27]\nv_exp_f32 v138,v138\nM v[28:31],a[76:79],v[196:199],v[28:31]\nbuffer_store_dwordx4 a[248:251],v253,s[52:55],s71 offen offset:2048 sc1\nM v[88:91],a[76:79],v[200:203],v[88:91]\n" \
"v_exp_f32 v139,v139\nM v[92:95],a[76:79],v[204:207],v[92:95]\nv_exp_f32 v152,v152\nM v[32:35],a[80:83],v[192:195],v[32:35]\nv_exp_f32 v153,v153\nM v[36:39],a[80:83],v[196:199],v[36:39]\nbuffer_store_dwordx4 a[248:251],v253,s[52:55],s71 offen offset:3072 sc1\nM v[96:99],a[80:83],v[200:203],v[96:99]\n" \
"v_exp_f32 v154,v154\nM v[100:103],a[80:83],v[204:207],v[100:103]\nds_read_b128 a[16:19],v250 offset:4096\nds_read_b128 a[20:23],v250 offset:5120\nM v[40:43],a[84:87],v[192:195],v[40:43]\nv_exp_f32 v155,v155\nM v[44:47],a[84:87],v[196:199],v[44:47]\nv_exp_f32 v140,v140\n" \
"M v[104:107],a[84:87],v[200:203],v[104:107]\nv_exp_f32 v141,v141\nM v[108:111],a[84:87],v[204:207],v[108:111]\nbuffer_load_dwordx4 v240,s[48:51],s95 offen offset:1024 sc1 lds\nM v[48:51],a[88:91],v[192:195],v[48:51]\nv_exp_f32 v142,v142\nM v[52:55],a[88:91],v[196:199],v[52:55]\nbuffer_store_dwordx4 a[248:251],v253,s[52:55],s78 offen offset:0 sc1\n" \
"M v[112:115],a[88:91],v[200:203],v[112:115]\nv_exp_f32 v143,v143\nM v[116:119],a[88:91],v[204:207],v[116:119]\nds_read_b128 a[24:27],v250 offset:6144\nds_read_b128 a[28:31],v250 offset:7168\nM v[56:59],a[92:95],v[192:195],v[56:59]\nv_exp_f32 v156,v156\nM v[60:63],a[92:95],v[196:199],v[60:63]\n" \
"buffer_store_dwordx4 a[248:251],v253,s[52:55],s78 offen offset:1024 sc1\nM v[120:123],a[92:95],v[200:203],v[120:123]\nv_exp_f32 v157,v157\nM v[124:127],a[92:95],v[204:207],v[124:127]\nv_exp_f32 v158,v158\nM a[192:195],a[208:211],v[192:195],a[192:195]\nv_exp_f32 v159,v159\nM a[196:199],a[208:211],v[196:199],a[196:199]\n" \
"buffer_store_dwordx4 a[248:251],v253,s[52:55],s78 offen offset:2048 sc1\nM a[200:203],a[208:211],v[200:203],a[200:203]\ns_and_b32 s94,s94,0xffff\nv_cvt_pk_bf16_f32 v200,v136,v137\nM a[204:207],a[208:211],v[204:207],a[204:207]\nds_read_b128 a[32:35],v250 offset:8192\nds_read_b128 a[36:39],v250 offset:9216\nM v[0:3],a[96:99],v[208:211],v[0:3]\n" \
"v_cvt_pk_bf16_f32 v201,v138,v139\nv_cvt_pk_bf16_f32 v202,v152,v153\nM v[4:7],a[96:99],v[212:215],v[4:7]\nv_cvt_pk_bf16_f32 v203,v154,v155\nv_cvt_pk_bf16_f32 v204,v140,v141\nM v[64:67],a[96:99],v[216:219],v[64:67]\nv_cvt_pk_bf16_f32 v205,v142,v143\nv_cvt_pk_bf16_f32 v206,v156,v157\n" \
"M v[68:71],a[96:99],v[220:223],v[68:71]\nbuffer_load_dwordx4 v240,s[48:51],s95 offen offset:2048 sc1 lds\nM v[8:11],a[100:103],v[208:211],v[8:11]\nv_exp_f32 v168,v168\nM v[12:15],a[100:103],v[212:215],v[12:15]\nbuffer_store_dwordx4 a[248:251],v253,s[52:55],s78 offen offset:3072 sc1\nM v[72:75],a[100:103],v[216:219],v[72:75]\nv_exp_f32 v169,v169\n" \
"M v[76:79],a[100:103],v[220:223],v[76:79]\nds_read_b128 a[40:43],v250 offset:10240\nds_read_b128 a[44:47],v250 offset:11264\nM v[16:19],a[104:107],v[208:211],v[16:19]\nv_exp_f32 v170,v170\nM v[20:23],a[104:107],v[212:215],v[20:23]\nbuffer_store_dwordx4 a[248:251],v253,s[52:55],s70 offen offset:0 sc1\nM v[80:83],a[104:107],v[216:219],v[80:83]\n" \
"v_exp_f32 v171,v171\nM v[84:87],a[104:107],v[220:223],v[84:87]\nv_exp_f32 v184,v184\nM v[24:27],a[108:111],v[208:211],v[24:27]\nv_exp_f32 v185,v185\nM v[28:31],a[108:111],v[212:215],v[28:31]\nbuffer_store_dwordx4 a[248:251],v253,s[52:55],s70 offen offset:1024 sc1\nM v[88:91],a[108:111],v[216:219],v[88:91]\n" \
"v_exp_f32 v186,v186\nM v[92:95],a[108:111],v[220:223],v[92:95]\nds_read_b128 a[48:51],v250 offset:12288\nds_read_b128 a[52:55],v250 offset:13312\nM v[32:35],a[112:115],v[208:211],v[32:35]\nv_exp_f32 v187,v187\nM v[36:39],a[112:115],v[212:215],v[36:39]\nv_cvt_pk_bf16_f32 v207,v158,v159\n" \
"M v[96:99],a[112:115],v[216:219],v[96:99]\nM v[100:103],a[112:115],v[220:223],v[100:103]\nbuffer_load_dwordx4 v240,s[48:51],s95 offen offset:3072 sc1 lds\nM v[40:43],a[116:119],v[208:211],v[40:43]\ns_add_u32 s95,s95,0xffff4000\nM v[44:47],a[116:119],v[212:215],v[44:47]\nbuffer_store_dwordx4 a[248:251],v253,s[52:55],s70 offen offset:2048 sc1\nM v[104:107],a[116:119],v[216:219],v[104:107]\n" \
"M v[108:111],a[116:119],v[220:223],v[108:111]\nds_read_b128 a[56:59],v250 offset:14336\nds_read_b128 a[60:63],v250 offset:15360\nM v[48:51],a[120:123],v[208:211],v[48:51]\nv_add_u32 v250,s94,v241\nM v[52:55],a[120:123],v[212:215],v[52:55]\nbuffer_store_dwordx4 a[248:251],v253,s[52:55],s70 offen offset:3072 sc1\nM v[112:115],a[120:123],v[216:219],v[112:115]\n" \
"M v[116:119],a[120:123],v[220:223],v[116:119]\nM v[56:59],a[124:127],v[208:211],v[56:59]\nM v[60:63],a[124:127],v[212:215],v[60:63]\nM v[120:123],a[124:127],v[216:219],v[120:123]\nM v[124:127],a[124:127],v[220:223],v[124:127]\nM a[192:195],a[208:211],v[208:211],a[192:195]\nM a[196:199],a[208:211],v[212:215],a[196:199]\nM a[200:203],a[208:211],v[216:219],a[200:203]\n" \
"M a[204:207],a[208:211],v[220:223],a[204:207]\n.Lsl:\ns_waitcnt lgkmcnt(4)\nM v[136:139],a[0:3],a[160:163],v[232:235]\ns_add_u32 m0,s94,s64\nv_cvt_pk_bf16_f32 v216,v168,v169\nM v[136:139],a[4:7],a[164:167],v[136:139]\nds_read_b128 a[64:67],v250 offset:0\n" \
"ds_read_b128 a[68:71],v250 offset:1024\nM v[136:139],a[8:11],a[168:171],v[136:139]\nbuffer_load_dwordx4 v240,s[44:47],s95 offen offset:0 sc1 lds\nM v[136:139],a[12:15],a[172:175],v[136:139]\nv_cvt_pk_bf16_f32 v217,v170,v171\nv_cvt_pk_bf16_f32 v218,v184,v185\nM v[140:143],a[0:3],a[176:179],v[236:239]\nds_read_b128 a[72:75],v250 offset:2048\n" \
"ds_read_b128 a[76:79],v250 offset:3072\nM v[140:143],a[4:7],a[180:183],v[140:143]\nv_exp_f32 v172,v172\nM v[140:143],a[8:11],a[184:187],v[140:143]\nv_exp_f32 v173,v173\nM v[140:143],a[12:15],a[188:191],v[140:143]\nds_read_b128 a[80:83],v250 offset:4096\nds_read_b128 a[84:87],v250 offset:5120\n" \
"M v[152:155],a[16:19],a[160:163],v[232:235]\nv_exp_f32 v174,v174\nM v[152:155],a[20:23],a[164:167],v[152:155]\nv_exp_f32 v175,v175\nM v[152:155],a[24:27],a[168:171],v[152:155]\nds_read_b128 a[88:91],v250 offset:6144\nds_read_b128 a[92:95],v250 offset:7168\nM v[152:155],a[28:31],a[172:175],v[152:155]\n" \
"buffer_load_dwordx4 v240,s[44:47],s95 offen offset:1024 sc1 lds\nM v[156:159],a[16:19],a[176:179],v[236:239]\nv_cvt_pk_bf16_f32 v220,v172,v173\nv_cvt_pk_bf16_f32 v221,v174,v175\nM v[156:159],a[20:23],a[180:183],v[156:159]\nds_read_b128 a[96:99],v250 offset:8192\nds_read_b128 a[100:103],v250 offset:9216\nM v[156:159],a[24:27],a[184:187],v[156:159]\n" \
"v_cvt_pk_bf16_f32 v219,v186,v187\ns_add_u32 s94,s94,0x4000\nM v[156:159],a[28:31],a[188:191],v[156:159]\nv_exp_f32 v188,v188\nM v[168:171],a[32:35],a[160:163],v[232:235]\nds_read_b128 a[104:107],v250 offset:10240\nds_read_b128 a[108:111],v250 offset:11264\nM v[168:171],a[36:39],a[164:167],v[168:171]\n" \
"v_exp_f32 v189,v189\nM v[168:171],a[40:43],a[168:171],v[168:171]\nv_exp_f32 v190,v190\nM v[168:171],a[44:47],a[172:175],v[168:171]\nds_read_b128 a[112:115],v250 offset:12288\nds_read_b128 a[116:119],v250 offset:13312\nM v[172:175],a[32:35],a[176:179],v[236:239]\nbuffer_load_dwordx4 v240,s[44:47],s95 offen offset:2048 sc1 lds\n" \
"M v[172:175],a[36:39],a[180:183],v[172:175]\nv_exp_f32 v191,v191\nM v[172:175],a[40:43],a[184:187],v[172:175]\nds_read_b128 a[120:123],v250 offset:14336\nds_read_b128 a[124:127],v250 offset:15360\nM v[172:175],a[44:47],a[188:191],v[172:175]\nv_cvt_pk_bf16_f32 v222,v188,v189\ns_waitcnt lgkmcnt(7)\n" \
"M v[184:187],a[48:51],a[160:163],v[232:235]\nv_cvt_pk_bf16_f32 v223,v190,v191\nv_xor_b32 v250,0x18000,v250\nM v[184:187],a[52:55],a[164:167],v[184:187]\nv_exp_f32 v136,v136\nM v[184:187],a[56:59],a[168:171],v[184:187]\nv_exp_f32 v137,v137\nM v[184:187],a[60:63],a[172:175],v[184:187]\n" \
"v_exp_f32 v138,v138\nM v[188:191],a[48:51],a[176:179],v[236:239]\nv_exp_f32 v139,v139\nM v[188:191],a[52:55],a[180:183],v[188:191]\nbuffer_load_dwordx4 v240,s[44:47],s95 offen offset:3072 sc1 lds\nM v[188:191],a[56:59],a[184:187],v[188:191]\ns_add_u32 m0,m0,0x10000\ns_and_b32 s94,s94,0xffff\n" \
"M v[188:191],a[60:63],a[188:191],v[188:191]\nv_exp_f32 v152,v152\nM v[64:67],a[64:67],v[200:203],v[64:67]\nv_exp_f32 v153,v153\nM v[68:71],a[64:67],v[204:207],v[68:71]\nv_exp_f32 v154,v154\nM v[72:75],a[68:71],v[200:203],v[72:75]\ns_waitcnt vmcnt(16) lgkmcnt(0)\n" \
"s_barrier\nM v[76:79],a[68:71],v[204:207],v[76:79]\nds_read_b128 a[0:3],v250 offset:0\nds_read_b128 a[4:7],v250 offset:1024\nM v[80:83],a[72:75],v[200:203],v[80:83]\nv_exp_f32 v155,v155\nM v[84:87],a[72:75],v[204:207],v[84:87]\nbuffer_load_dwordx4 v240,s[48:51],s95 offen offset:0 sc1 lds\n" \
"M v[88:91],a[76:79],v[200:203],v[88:91]\nv_exp_f32 v140,v140\nM v[92:95],a[76:79],v[204:207],v[92:95]\nds_read_b128 a[8:11],v250 offset:2048\nds_read_b128 a[12:15],v250 offset:3072\nM v[96:99],a[80:83],v[200:203],v[96:99]\nv_exp_f32 v141,v141\nM v[100:103],a[80:83],v[204:207],v[100:103]\n" \
"v_exp_f32 v142,v142\nM v[104:107],a[84:87],v[200:203],v[104:107]\nv_exp_f32 v143,v143\nM v[108:111],a[84:87],v[204:207],v[108:111]\nds_read_b128 a[16:19],v250 offset:4096\nds_read_b128 a[20:23],v250 offset:5120\nM v[112:115],a[88:91],v[200:203],v[112:115]\nv_exp_f32 v156,v156\n" \
"M v[116:119],a[88:91],v[204:207],v[116:119]\nbuffer_load_dwordx4 v240,s[48:51],s95 offen offset:1024 sc1 lds\nM v[120:123],a[92:95],v[200:203],v[120:123]\nv_exp_f32 v157,v157\nM v[124:127],a[92:95],v[204:207],v[124:127]\nds_read_b128 a[24:27],v250 offset:6144\nds_read_b128 a[28:31],v250 offset:7168\nM a[200:203],a[208:211],v[200:203],a[200:203]\n" \
"v_exp_f32 v158,v158\nM a[204:207],a[208:211],v[204:207],a[204:207]\nv_exp_f32 v159,v159\nM v[64:67],a[96:99],v[216:219],v[64:67]\nv_cvt_pk_bf16_f32 v200,v136,v137\nv_cvt_pk_bf16_f32 v201,v138,v139\nM v[68:71],a[96:99],v[220:223],v[68:71]\nds_read_b128 a[32:35],v250 offset:8192\n" \
"ds_read_b128 a[36:39],v250 offset:9216\nM v[72:75],a[100:103],v[216:219],v[72:75]\nv_cvt_pk_bf16_f32 v202,v152,v153\nv_cvt_pk_bf16_f32 v203,v154,v155\nM v[76:79],a[100:103],v[220:223],v[76:79]\nbuffer_load_dwordx4 v240,s[48:51],s95 offen offset:2048 sc1 lds\nM v[80:83],a[104:107],v[216:219],v[80:83]\nv_cvt_pk_bf16_f32 v204,v140,v141\n" \
"v_cvt_pk_bf16_f32 v205,v142,v143\nM v[84:87],a[104:107],v[220:223],v[84:87]\nds_read_b128 a[40:43],v250 offset:10240\nds_read_b128 a[44:47],v250 offset:11264\nM v[88:91],a[108:111],v[216:219],v[88:91]\nv_cvt_pk_bf16_f32 v206,v156,v157\nv_cvt_pk_bf16_f32 v207,v158,v159\nM v[92:95],a[108:111],v[220:223],v[92:95]\n" \
"v_exp_f32 v168,v168\nM v[96:99],a[112:115],v[216:219],v[96:99]\nv_exp_f32 v169,v169\nM v[100:103],a[112:115],v[220:223],v[100:103]\nds_read_b128 a[48:51],v250 offset:12288\nds_read_b128 a[52:55],v250 offset:13312\nM v[104:107],a[116:119],v[216:219],v[104:107]\nv_exp_f32 v170,v170\n" \
"M v[108:111],a[116:119],v[220:223],v[108:111]\nbuffer_load_dwordx4 v240,s[48:51],s95 offen offset:3072 sc1 lds\nM v[112:115],a[120:123],v[216:219],v[112:115]\nv_exp_f32 v171,v171\nM v[116:119],a[120:123],v[220:223],v[116:119]\nds_read_b128 a[56:59],v250 offset:14336\nds_read_b128 a[60:63],v250 offset:15360\nM v[120:123],a[124:127],v[216:219],v[120:123]\n" \
"v_exp_f32 v184,v184\nM v[124:127],a[124:127],v[220:223],v[124:127]\nv_exp_f32 v185,v185\nM a[200:203],a[208:211],v[216:219],a[200:203]\nv_exp_f32 v186,v186\nv_exp_f32 v187,v187\nv_add_u32 v250,s94,v241\ns_add_u32 s95,s95,0x4000\n" \
"M a[204:207],a[208:211],v[220:223],a[204:207]\ns_cmp_lg_u32 s95,s96\ns_cbranch_scc1 .Lsl\ns_waitcnt lgkmcnt(4)\nM v[128:131],a[0:3],a[128:131],v[224:227]\ns_add_u32 m0,s94,s64\nv_cvt_pk_bf16_f32 v216,v168,v169\nM v[128:131],a[4:7],a[132:135],v[128:131]\n" \
"ds_read_b128 a[64:67],v250 offset:0\nds_read_b128 a[68:71],v250 offset:1024\nM v[128:131],a[8:11],a[136:139],v[128:131]\nbuffer_load_dwordx4 v240,s[44:47],s95 offen offset:0 sc1 lds\nM v[128:131],a[12:15],a[140:143],v[128:131]\nv_exp_f32 v188,v188\nM v[132:135],a[0:3],a[144:147],v[228:231]\nds_read_b128 a[72:75],v250 offset:2048\n" \
"ds_read_b128 a[76:79],v250 offset:3072\nM v[132:135],a[4:7],a[148:151],v[132:135]\nv_exp_f32 v189,v189\nM v[132:135],a[8:11],a[152:155],v[132:135]\nv_exp_f32 v190,v190\nM v[132:135],a[12:15],a[156:159],v[132:135]\nds_read_b128 a[80:83],v250 offset:4096\nds_read_b128 a[84:87],v250 offset:5120\n" \
"M v[144:147],a[16:19],a[128:131],v[224:227]\nv_exp_f32 v191,v191\nM v[144:147],a[20:23],a[132:135],v[144:147]\nv_exp_f32 v172,v172\nM v[144:147],a[24:27],a[136:139],v[144:147]\nds_read_b128 a[88:91],v250 offset:6144\nds_read_b128 a[92:95],v250 offset:7168\nM v[144:147],a[28:31],a[140:143],v[144:147]\n" \
"buffer_load_dwordx4 v240,s[44:47],s95 offen offset:1024 sc1 lds\nM v[148:151],a[16:19],a[144:147],v[228:231]\nv_exp_f32 v173,v173\nM v[148:151],a[20:23],a[148:151],v[148:151]\nds_read_b128 a[96:99],v250 offset:8192\nds_read_b128 a[100:103],v250 offset:9216\nM v[148:151],a[24:27],a[152:155],v[148:151]\nv_exp_f32 v174,v174\n" \
"M v[148:151],a[28:31],a[156:159],v[148:151]\nv_exp_f32 v175,v175\nM v[160:163],a[32:35],a[128:131],v[224:227]\nds_read_b128 a[104:107],v250 offset:10240\nds_read_b128 a[108:111],v250 offset:11264\nM v[160:163],a[36:39],a[132:135],v[160:163]\nv_cvt_pk_bf16_f32 v217,v170,v171\nv_cvt_pk_bf16_f32 v218,v184,v185\n" \
"M v[160:163],a[40:43],a[136:139],v[160:163]\nv_cvt_pk_bf16_f32 v219,v186,v187\nv_cvt_pk_bf16_f32 v220,v172,v173\nM v[160:163],a[44:47],a[140:143],v[160:163]\nds_read_b128 a[112:115],v250 offset:12288\nds_read_b128 a[116:119],v250 offset:13312\nM v[164:167],a[32:35],a[144:147],v[228:231]\nbuffer_load_dwordx4 v240,s[44:47],s95 offen offset:2048 sc1 lds\n" \
"M v[164:167],a[36:39],a[148:151],v[164:167]\nv_cvt_pk_bf16_f32 v221,v174,v175\nv_cvt_pk_bf16_f32 v222,v188,v189\nM v[164:167],a[40:43],a[152:155],v[164:167]\nds_read_b128 a[120:123],v250 offset:14336\nds_read_b128 a[124:127],v250 offset:15360\nM v[164:167],a[44:47],a[156:159],v[164:167]\nv_xor_b32 v250,0x18000,v250\n" \
"s_waitcnt lgkmcnt(7)\nM v[176:179],a[48:51],a[128:131],v[224:227]\nv_cvt_pk_bf16_f32 v223,v190,v191\ns_add_u32 s94,s94,0x4000\nM v[176:179],a[52:55],a[132:135],v[176:179]\nv_exp_f32 v128,v128\nM v[176:179],a[56:59],a[136:139],v[176:179]\nv_exp_f32 v129,v129\n" \
"M v[176:179],a[60:63],a[140:143],v[176:179]\nv_exp_f32 v130,v130\nM v[180:183],a[48:51],a[144:147],v[228:231]\nv_exp_f32 v131,v131\nM v[180:183],a[52:55],a[148:151],v[180:183]\nbuffer_load_dwordx4 v240,s[44:47],s95 offen offset:3072 sc1 lds\nM v[180:183],a[56:59],a[152:155],v[180:183]\ns_add_u32 m0,m0,0x10000\n" \
"s_and_b32 s94,s94,0xffff\nM v[180:183],a[60:63],a[156:159],v[180:183]\nv_exp_f32 v144,v144\nM v[64:67],a[64:67],v[200:203],v[64:67]\nv_exp_f32 v145,v145\nM v[68:71],a[64:67],v[204:207],v[68:71]\nv_exp_f32 v146,v146\nM v[72:75],a[68:71],v[200:203],v[72:75]\n" \
"s_waitcnt vmcnt(16) lgkmcnt(0)\ns_barrier\nM v[76:79],a[68:71],v[204:207],v[76:79]\nds_read_b128 a[0:3],v250 offset:0\nds_read_b128 a[4:7],v250 offset:1024\nM v[80:83],a[72:75],v[200:203],v[80:83]\nv_exp_f32 v147,v147\nM v[84:87],a[72:75],v[204:207],v[84:87]\n" \
"buffer_load_dwordx4 v240,s[48:51],s95 offen offset:0 sc1 lds\nM v[88:91],a[76:79],v[200:203],v[88:91]\nv_exp_f32 v132,v132\nM v[92:95],a[76:79],v[204:207],v[92:95]\nds_read_b128 a[8:11],v250 offset:2048\nds_read_b128 a[12:15],v250 offset:3072\nM v[96:99],a[80:83],v[200:203],v[96:99]\nv_exp_f32 v133,v133\n" \
"M v[100:103],a[80:83],v[204:207],v[100:103]\nv_exp_f32 v134,v134\nM v[104:107],a[84:87],v[200:203],v[104:107]\nv_exp_f32 v135,v135\nM v[108:111],a[84:87],v[204:207],v[108:111]\nds_read_b128 a[16:19],v250 offset:4096\nds_read_b128 a[20:23],v250 offset:5120\nM v[112:115],a[88:91],v[200:203],v[112:115]\n" \
"v_exp_f32 v148,v148\nM v[116:119],a[88:91],v[204:207],v[116:119]\nbuffer_load_dwordx4 v240,s[48:51],s95 offen offset:1024 sc1 lds\nM v[120:123],a[92:95],v[200:203],v[120:123]\nv_exp_f32 v149,v149\nM v[124:127],a[92:95],v[204:207],v[124:127]\nds_read_b128 a[24:27],v250 offset:6144\nds_read_b128 a[28:31],v250 offset:7168\n" \
"M a[200:203],a[208:211],v[200:203],a[200:203]\nv_exp_f32 v150,v150\nM a[204:207],a[208:211],v[204:207],a[204:207]\nv_exp_f32 v151,v151\nM v[64:67],a[96:99],v[216:219],v[64:67]\nv_cvt_pk_bf16_f32 v192,v128,v129\nv_cvt_pk_bf16_f32 v193,v130,v131\nM v[68:71],a[96:99],v[220:223],v[68:71]\n" \
"ds_read_b128 a[32:35],v250 offset:8192\nds_read_b128 a[36:39],v250 offset:9216\nM v[72:75],a[100:103],v[216:219],v[72:75]\nv_cvt_pk_bf16_f32 v194,v144,v145\nv_cvt_pk_bf16_f32 v195,v146,v147\nM v[76:79],a[100:103],v[220:223],v[76:79]\nbuffer_load_dwordx4 v240,s[48:51],s95 offen offset:2048 sc1 lds\nM v[80:83],a[104:107],v[216:219],v[80:83]\n" \
"v_cvt_pk_bf16_f32 v196,v132,v133\nv_cvt_pk_bf16_f32 v197,v134,v135\nM v[84:87],a[104:107],v[220:223],v[84:87]\nds_read_b128 a[40:43],v250 offset:10240\nds_read_b128 a[44:47],v250 offset:11264\nM v[88:91],a[108:111],v[216:219],v[88:91]\nv_cvt_pk_bf16_f32 v198,v148,v149\nv_cvt_pk_bf16_f32 v199,v150,v151\n" \
"M v[92:95],a[108:111],v[220:223],v[92:95]\nv_exp_f32 v160,v160\nM v[96:99],a[112:115],v[216:219],v[96:99]\nv_exp_f32 v161,v161\nM v[100:103],a[112:115],v[220:223],v[100:103]\nds_read_b128 a[48:51],v250 offset:12288\nds_read_b128 a[52:55],v250 offset:13312\nM v[104:107],a[116:119],v[216:219],v[104:107]\n" \
"v_exp_f32 v162,v162\nM v[108:111],a[116:119],v[220:223],v[108:111]\nbuffer_load_dwordx4 v240,s[48:51],s95 offen offset:3072 sc1 lds\nM v[112:115],a[120:123],v[216:219],v[112:115]\nv_exp_f32 v163,v163\nM v[116:119],a[120:123],v[220:223],v[116:119]\nds_read_b128 a[56:59],v250 offset:14336\nds_read_b128 a[60:63],v250 offset:15360\n" \
"M v[120:123],a[124:127],v[216:219],v[120:123]\nv_exp_f32 v176,v176\nM v[124:127],a[124:127],v[220:223],v[124:127]\nv_exp_f32 v177,v177\nM a[200:203],a[208:211],v[216:219],a[200:203]\nv_exp_f32 v178,v178\nv_exp_f32 v179,v179\nv_add_u32 v250,s94,v241\n" \
"s_add_u32 s95,s95,0x4000\nM a[204:207],a[208:211],v[220:223],a[204:207]\ns_branch .Lse\n.Ltrans:\ns_waitcnt lgkmcnt(4)\nM v[128:131],a[0:3],a[128:131],v[224:227]\ns_add_u32 m0,s94,s64\nv_cvt_pk_bf16_f32 v208,v160,v161\n" \
"M v[128:131],a[4:7],a[132:135],v[128:131]\nds_read_b128 a[64:67],v250 offset:0\nds_read_b128 a[68:71],v250 offset:1024\nM v[128:131],a[8:11],a[136:139],v[128:131]\nbuffer_load_dwordx4 v240,s[44:47],s95 offen offset:0 sc1 lds\nM v[128:131],a[12:15],a[140:143],v[128:131]\nv_cvt_pk_bf16_f32 v209,v162,v163\nv_cvt_pk_bf16_f32 v212,v164,v165\n" \
"M v[132:135],a[0:3],a[144:147],v[228:231]\nds_read_b128 a[72:75],v250 offset:2048\nds_read_b128 a[76:79],v250 offset:3072\nM v[132:135],a[4:7],a[148:151],v[132:135]\nbuffer_store_dwordx4 a[248:251],v253,s[52:55],s33 offen offset:0 sc1\nM v[132:135],a[8:11],a[152:155],v[132:135]\nv_cvt_pk_bf16_f32 v213,v166,v167\nv_cvt_pk_bf16_f32 v210,v176,v177\n" \
"M v[132:135],a[12:15],a[156:159],v[132:135]\nds_read_b128 a[80:83],v250 offset:4096\nds_read_b128 a[84:87],v250 offset:5120\nM v[144:147],a[16:19],a[128:131],v[224:227]\nv_cvt_pk_bf16_f32 v211,v178,v179\nv_cvt_pk_bf16_f32 v214,v180,v181\nM v[144:147],a[20:23],a[132:135],v[144:147]\nv_cvt_pk_bf16_f32 v215,v182,v183\n" \
"s_add_u32 s71,s33,16384\nM v[144:147],a[24:27],a[136:139],v[144:147]\nds_read_b128 a[88:91],v250 offset:6144\nds_read_b128 a[92:95],v250 offset:7168\nM v[144:147],a[28:31],a[140:143],v[144:147]\nbuffer_load_dwordx4 v240,s[44:47],s95 offen offset:1024 sc1 lds\nM v[148:151],a[16:19],a[144:147],v[228:231]\nv_exp_f32 v184,v184\n" \
"M v[148:151],a[20:23],a[148:151],v[148:151]\nds_read_b128 a[96:99],v250 offset:8192\nds_read_b128 a[100:103],v250 offset:9216\nM v[148:151],a[24:27],a[152:155],v[148:151]\nbuffer_store_dwordx4 a[248:251],v253,s[52:55],s33 offen offset:1024 sc1\nM v[148:151],a[28:31],a[156:159],v[148:151]\nv_exp_f32 v185,v185\nM v[160:163],a[32:35],a[128:131],v[224:227]\n" \
"ds_read_b128 a[104:107],v250 offset:10240\nds_read_b128 a[108:111],v250 offset:11264\nM v[160:163],a[36:39],a[132:135],v[160:163]\nv_exp_f32 v186,v186\nM v[160:163],a[40:43],a[136:139],v[160:163]\nv_exp_f32 v187,v187\nM v[160:163],a[44:47],a[140:143],v[160:163]\nds_read_b128 a[112:115],v250 offset:12288\n" \
"ds_read_b128 a[116:119],v250 offset:13312\nM v[164:167],a[32:35],a[144:147],v[228:231]\nbuffer_load_dwordx4 v240,s[44:47],s95 offen offset:2048 sc1 lds\nM v[164:167],a[36:39],a[148:151],v[164:167]\nv_exp_f32 v172,v172\nM v[164:167],a[40:43],a[152:155],v[164:167]\nds_read_b128 a[120:123],v250 offset:14336\nds_read_b128 a[124:127],v250 offset:15360\n" \
"M v[164:167],a[44:47],a[156:159],v[164:167]\nbuffer_store_dwordx4 a[248:251],v253,s[52:55],s33 offen offset:2048 sc1\ns_waitcnt lgkmcnt(6)\nM v[176:179],a[48:51],a[128:131],v[224:227]\nv_xor_b32 v250,0x18000,v250\nv_cvt_pk_bf16_f32 v216,v168,v169\nM v[176:179],a[52:55],a[132:135],v[176:179]\nv_exp_f32 v173,v173\n" \
"M v[176:179],a[56:59],a[136:139],v[176:179]\nv_exp_f32 v174,v174\nM v[176:179],a[60:63],a[140:143],v[176:179]\nv_exp_f32 v175,v175\nM v[180:183],a[48:51],a[144:147],v[228:231]\nv_exp_f32 v188,v188\nM v[180:183],a[52:55],a[148:151],v[180:183]\nbuffer_load_dwordx4 v240,s[44:47],s95 offen offset:3072 sc1 lds\n" \
"M v[180:183],a[56:59],a[152:155],v[180:183]\ns_add_u32 m0,m0,0x10000\nv_cvt_pk_bf16_f32 v217,v170,v171\nM v[180:183],a[60:63],a[156:159],v[180:183]\nv_exp_f32 v189,v189\nM v[0:3],a[64:67],v[192:195],v[0:3]\nbuffer_store_dwordx4 a[248:251],v253,s[52:55],s33 offen offset:3072 sc1\nM v[4:7],a[64:67],v[196:199],v[4:7]\n" \
"v_exp_f32 v190,v190\nM v[64:67],a[64:67],v[200:203],v[64:67]\ns_waitcnt vmcnt(16) lgkmcnt(0)\ns_barrier\nM v[68:71],a[64:67],v[204:207],v[68:71]\nds_read_b128 a[0:3],v250 offset:0\nds_read_b128 a[4:7],v250 offset:1024\nM v[8:11],a[68:71],v[192:195],v[8:11]\n" \
"v_exp_f32 v191,v191\nM v[12:15],a[68:71],v[196:199],v[12:15]\nbuffer_store_dwordx4 a[248:251],v253,s[52:55],s71 offen offset:0 sc1\nM v[72:75],a[68:71],v[200:203],v[72:75]\nv_cvt_pk_bf16_f32 v218,v184,v185\nv_cvt_pk_bf16_f32 v219,v186,v187\nM v[76:79],a[68:71],v[204:207],v[76:79]\nbuffer_load_dwordx4 v240,s[48:51],s95 offen offset:0 sc1 lds\n" \
"M v[16:19],a[72:75],v[192:195],v[16:19]\nv_cvt_pk_bf16_f32 v220,v172,v173\nv_cvt_pk_bf16_f32 v221,v174,v175\nM v[20:23],a[72:75],v[196:199],v[20:23]\nbuffer_store_dwordx4 a[248:251],v253,s[52:55],s71 offen offset:1024 sc1\nM v[80:83],a[72:75],v[200:203],v[80:83]\nv_cvt_pk_bf16_f32 v222,v188,v189\nv_cvt_pk_bf16_f32 v223,v190,v191\n" \
"M v[84:87],a[72:75],v[204:207],v[84:87]\nds_read_b128 a[8:11],v250 offset:2048\nds_read_b128 a[12:15],v250 offset:3072\nM v[24:27],a[76:79],v[192:195],v[24:27]\ns_add_u32 s70,s78,16384\ns_add_u32 s94,s94,0x4000\nM v[28:31],a[76:79],v[196:199],v[28:31]\nbuffer_store_dwordx4 a[248:251],v253,s[52:55],s71 offen offset:2048 sc1\n" \
"M v[88:91],a[76:79],v[200:203],v[88:91]\nv_exp_f32 v128,v128\nM v[92:95],a[76:79],v[204:207],v[92:95]\nv_exp_f32 v129,v129\nM v[32:35],a[80:83],v[192:195],v[32:35]\nv_exp_f32 v130,v130\nM v[36:39],a[80:83],v[196:199],v[36:39]\nbuffer_store_dwordx4 a[248:251],v253,s[52:55],s71 offen offset:3072 sc1\n" \
"M v[96:99],a[80:83],v[200:203],v[96:99]\nv_exp_f32 v131,v131\nM v[100:103],a[80:83],v[204:207],v[100:103]\nds_read_b128 a[16:19],v250 offset:4096\nds_read_b128 a[20:23],v250 offset:5120\nM v[40:43],a[84:87],v[192:195],v[40:43]\nv_exp_f32 v144,v144\nM v[44:47],a[84:87],v[196:199],v[44:47]\n" \
"v_exp_f32 v145,v145\nM v[104:107],a[84:87],v[200:203],v[104:107]\nv_exp_f32 v146,v146\nM v[108:111],a[84:87],v[204:207],v[108:111]\nbuffer_load_dwordx4 v240,s[48:51],s95 offen offset:1024 sc1 lds\nM v[48:51],a[88:91],v[192:195],v[48:51]\nv_exp_f32 v147,v147\nM v[52:55],a[88:91],v[196:199],v[52:55]\n" \
"buffer_store_dwordx4 a[248:251],v253,s[52:55],s78 offen offset:0 sc1\nM v[112:115],a[88:91],v[200:203],v[112:115]\nv_exp_f32 v132,v132\nM v[116:119],a[88:91],v[204:207],v[116:119]\nds_read_b128 a[24:27],v250 offset:6144\nds_read_b128 a[28:31],v250 offset:7168\nM v[56:59],a[92:95],v[192:195],v[56:59]\nv_exp_f32 v133,v133\n" \
"M v[60:63],a[92:95],v[196:199],v[60:63]\nbuffer_store_dwordx4 a[248:251],v253,s[52:55],s78 offen offset:1024 sc1\nM v[120:123],a[92:95],v[200:203],v[120:123]\nv_exp_f32 v134,v134\nM v[124:127],a[92:95],v[204:207],v[124:127]\nv_exp_f32 v135,v135\nM a[192:195],a[208:211],v[192:195],a[192:195]\nv_exp_f32 v148,v148\n" \
"M a[196:199],a[208:211],v[196:199],a[196:199]\nbuffer_store_dwordx4 a[248:251],v253,s[52:55],s78 offen offset:2048 sc1\nM a[200:203],a[208:211],v[200:203],a[200:203]\nv_exp_f32 v149,v149\nM a[204:207],a[208:211],v[204:207],a[204:207]\nds_read_b128 a[32:35],v250 offset:8192\nds_read_b128 a[36:39],v250 offset:9216\nM v[0:3],a[96:99],v[208:211],v[0:3]\n" \
"v_exp_f32 v150,v150\nM v[4:7],a[96:99],v[212:215],v[4:7]\nv_exp_f32 v151,v151\nM v[64:67],a[96:99],v[216:219],v[64:67]\ns_and_b32 s94,s94,0xffff\nv_cvt_pk_bf16_f32 v192,v128,v129\nM v[68:71],a[96:99],v[220:223],v[68:71]\nbuffer_load_dwordx4 v240,s[48:51],s95 offen offset:2048 sc1 lds\n" \
"M v[8:11],a[100:103],v[208:211],v[8:11]\nv_cvt_pk_bf16_f32 v193,v130,v131\nv_cvt_pk_bf16_f32 v194,v144,v145\nM v[12:15],a[100:103],v[212:215],v[12:15]\nbuffer_store_dwordx4 a[248:251],v253,s[52:55],s78 offen offset:3072 sc1\nM v[72:75],a[100:103],v[216:219],v[72:75]\nv_cvt_pk_bf16_f32 v195,v146,v147\nv_cvt_pk_bf16_f32 v196,v132,v133\n" \
"M v[76:79],a[100:103],v[220:223],v[76:79]\nds_read_b128 a[40:43],v250 offset:10240\nds_read_b128 a[44:47],v250 offset:11264\nM v[16:19],a[104:107],v[208:211],v[16:19]\nv_cvt_pk_bf16_f32 v197,v134,v135\nv_cvt_pk_bf16_f32 v198,v148,v149\nM v[20:23],a[104:107],v[212:215],v[20:23]\nbuffer_store_dwordx4 a[248:251],v253,s[52:55],s70 offen offset:0 sc1\n" \
"M v[80:83],a[104:107],v[216:219],v[80:83]\nv_exp_f32 v160,v160\nM v[84:87],a[104:107],v[220:223],v[84:87]\nv_exp_f32 v161,v161\nM v[24:27],a[108:111],v[208:211],v[24:27]\nv_exp_f32 v162,v162\nM v[28:31],a[108:111],v[212:215],v[28:31]\nbuffer_store_dwordx4 a[248:251],v253,s[52:55],s70 offen offset:1024 sc1\n" \
"M v[88:91],a[108:111],v[216:219],v[88:91]\nv_exp_f32 v163,v163\nM v[92:95],a[108:111],v[220:223],v[92:95]\nds_read_b128 a[48:51],v250 offset:12288\nds_read_b128 a[52:55],v250 offset:13312\nM v[32:35],a[112:115],v[208:211],v[32:35]\nv_exp_f32 v176,v176\nM v[36:39],a[112:115],v[212:215],v[36:39]\n" \
"v_exp_f32 v177,v177\nM v[96:99],a[112:115],v[216:219],v[96:99]\nv_exp_f32 v178,v178\nM v[100:103],a[112:115],v[220:223],v[100:103]\nbuffer_load_dwordx4 v240,s[48:51],s95 offen offset:3072 sc1 lds\nM v[40:43],a[116:119],v[208:211],v[40:43]\nv_cvt_pk_bf16_f32 v199,v150,v151\ns_add_u32 s95,s95,0x4000\n" \
"M v[44:47],a[116:119],v[212:215],v[44:47]\nbuffer_store_dwordx4 a[248:251],v253,s[52:55],s70 offen offset:2048 sc1\nM v[104:107],a[116:119],v[216:219],v[104:107]\nv_exp_f32 v179,v179\nM v[108:111],a[116:119],v[220:223],v[108:111]\nds_read_b128 a[56:59],v250 offset:14336\nds_read_b128 a[60:63],v250 offset:15360\nM v[48:51],a[120:123],v[208:211],v[48:51]\n" \
"v_add_u32 v250,s94,v241\nM v[52:55],a[120:123],v[212:215],v[52:55]\nbuffer_store_dwordx4 a[248:251],v253,s[52:55],s70 offen offset:3072 sc1\nM v[112:115],a[120:123],v[216:219],v[112:115]\nM v[116:119],a[120:123],v[220:223],v[116:119]\nM v[56:59],a[124:127],v[208:211],v[56:59]\nM v[60:63],a[124:127],v[212:215],v[60:63]\nM v[120:123],a[124:127],v[216:219],v[120:123]\n" \
"M v[124:127],a[124:127],v[220:223],v[124:127]\nM a[192:195],a[208:211],v[208:211],a[192:195]\nM a[196:199],a[208:211],v[212:215],a[196:199]\nM a[200:203],a[208:211],v[216:219],a[200:203]\nM a[204:207],a[208:211],v[220:223],a[204:207]\n.Lse:\ns_waitcnt lgkmcnt(4)\nM v[128:131],a[0:3],a[128:131],v[224:227]\n" \
"s_add_u32 m0,s94,s64\nv_cvt_pk_bf16_f32 v208,v160,v161\nv_cvt_pk_bf16_f32 v209,v162,v163\nv_cvt_pk_bf16_f32 v210,v176,v177\nM v[128:131],a[4:7],a[132:135],v[128:131]\nds_read_b128 a[64:67],v250 offset:0\nds_read_b128 a[68:71],v250 offset:1024\nv_exp_f32 v164,v164\n" \
"M v[128:131],a[8:11],a[136:139],v[128:131]\nbuffer_load_dwordx4 v240,s[44:47],s95 offen offset:0 sc1 lds\nv_exp_f32 v165,v165\nM v[128:131],a[12:15],a[140:143],v[128:131]\nv_exp_f32 v166,v166\nv_exp_f32 v167,v167\nM v[132:135],a[0:3],a[144:147],v[228:231]\nds_read_b128 a[72:75],v250 offset:2048\n" \
"ds_read_b128 a[76:79],v250 offset:3072\nv_cvt_pk_bf16_f32 v212,v164,v165\nv_cvt_pk_bf16_f32 v213,v166,v167\nv_cvt_pk_bf16_f32 v211,v178,v179\nM v[132:135],a[4:7],a[148:151],v[132:135]\nv_exp_f32 v180,v180\nv_exp_f32 v181,v181\nM v[132:135],a[8:11],a[152:155],v[132:135]\n" \
"v_exp_f32 v182,v182\nv_exp_f32 v183,v183\nM v[132:135],a[12:15],a[156:159],v[132:135]\nds_read_b128 a[80:83],v250 offset:4096\nds_read_b128 a[84:87],v250 offset:5120\nv_cvt_pk_bf16_f32 v214,v180,v181\nv_cvt_pk_bf16_f32 v215,v182,v183\nM v[144:147],a[16:19],a[128:131],v[224:227]\n" \
"v_accvgpr_read_b32 v232,a200\nv_accvgpr_read_b32 v236,a204\nv_add_f32 v233,v232,v236\nv_rcp_f32 v232,v232\nM v[144:147],a[20:23],a[132:135],v[144:147]\nv_rcp_f32 v236,v236\nv_add_f32 v233,v233,v232\nv_add_f32 v233,v233,v236\n" \
"M v[144:147],a[24:27],a[136:139],v[144:147]\nds_read_b128 a[88:91],v250 offset:6144\nds_read_b128 a[92:95],v250 offset:7168\nv_cmp_ngt_f32 vcc,s28,v233\ns_add_u32 s37,s12,s67\nM v[144:147],a[28:31],a[140:143],v[144:147]\nbuffer_load_dwordx4 v240,s[44:47],s95 offen offset:1024 sc1 lds\nv_mul_f32 v64,v64,v232\n" \
"v_mul_f32 v65,v65,v232\nM v[148:151],a[16:19],a[144:147],v[228:231]\ns_and_b64 vcc,vcc,s[90:91]\ns_cselect_b32 s39,0x8000000,s16\ns_cselect_b32 s16,s16,0x8000000\ns_cselect_b32 s71,2,1\ns_addc_u32 s99,s99,0\nM v[148:151],a[20:23],a[148:151],v[148:151]\n" \
"ds_read_b128 a[96:99],v250 offset:8192\nds_read_b128 a[100:103],v250 offset:9216\nv_mul_f32 v68,v68,v236\nv_mul_f32 v69,v69,v236\nM v[148:151],a[24:27],a[152:155],v[148:151]\nv_mul_f32 v80,v80,v232\nv_mul_f32 v81,v81,v232\nv_mul_f32 v84,v84,v236\n" \
"v_mul_f32 v85,v85,v236\nM v[148:151],a[28:31],a[156:159],v[148:151]\nv_mul_f32 v96,v96,v232\nv_mul_f32 v97,v97,v232\nv_mul_f32 v100,v100,v236\nv_mul_f32 v101,v101,v236\nM v[160:163],a[32:35],a[128:131],v[224:227]\nds_read_b128 a[104:107],v250 offset:10240\n" \
"ds_read_b128 a[108:111],v250 offset:11264\nv_mul_f32 v112,v112,v232\nv_mul_f32 v113,v113,v232\nM v[160:163],a[36:39],a[132:135],v[160:163]\nv_mul_f32 v116,v116,v236\nv_mul_f32 v117,v117,v236\ns_add_u32 s37,s37,1048576\nv_mul_f32 v66,v66,v232\n" \
"v_mul_f32 v67,v67,v232\nM v[160:163],a[40:43],a[136:139],v[160:163]\nv_cvt_pk_bf16_f32 v64,v64,v65\nv_mul_f32 v70,v70,v236\nv_mul_f32 v71,v71,v236\nv_cvt_pk_bf16_f32 v68,v68,v69\nM v[160:163],a[44:47],a[140:143],v[160:163]\nds_read_b128 a[112:115],v250 offset:12288\n" \
"ds_read_b128 a[116:119],v250 offset:13312\nv_mul_f32 v82,v82,v232\nv_mul_f32 v83,v83,v232\nM v[164:167],a[32:35],a[144:147],v[228:231]\nbuffer_load_dwordx4 v240,s[44:47],s95 offen offset:2048 sc1 lds\nv_cvt_pk_bf16_f32 v80,v80,v81\nv_mul_f32 v86,v86,v236\nM v[164:167],a[36:39],a[148:151],v[164:167]\n" \
"v_mul_f32 v87,v87,v236\nv_cvt_pk_bf16_f32 v84,v84,v85\nv_mul_f32 v98,v98,v232\nv_mul_f32 v99,v99,v232\nv_cvt_pk_bf16_f32 v96,v96,v97\nM v[164:167],a[40:43],a[152:155],v[164:167]\nds_read_b128 a[120:123],v250 offset:14336\nds_read_b128 a[124:127],v250 offset:15360\n" \
"v_xor_b32 v250,0x18000,v250\nv_mul_f32 v102,v102,v236\nM v[164:167],a[44:47],a[156:159],v[164:167]\nv_mul_f32 v103,v103,v236\nv_cvt_pk_bf16_f32 v100,v100,v101\nv_mul_f32 v114,v114,v232\ns_waitcnt lgkmcnt(4)\nM v[176:179],a[48:51],a[128:131],v[224:227]\n" \
"v_mul_f32 v115,v115,v232\nv_cvt_pk_bf16_f32 v112,v112,v113\nv_mul_f32 v118,v118,v236\nv_mul_f32 v119,v119,v236\nM v[176:179],a[52:55],a[132:135],v[176:179]\nv_cvt_pk_bf16_f32 v116,v116,v117\ns_or_b32 s37,s37,s39\nv_mul_f32 v72,v72,v232\n" \
"v_mul_f32 v73,v73,v232\nv_mul_f32 v74,v74,v232\nM v[176:179],a[56:59],a[136:139],v[176:179]\nv_mul_f32 v75,v75,v232\nv_cvt_pk_bf16_f32 v65,v66,v67\nv_mul_f32 v76,v76,v236\nv_mul_f32 v77,v77,v236\nM v[176:179],a[60:63],a[140:143],v[176:179]\n" \
"v_mul_f32 v78,v78,v236\nv_mul_f32 v79,v79,v236\nv_cvt_pk_bf16_f32 v69,v70,v71\nv_mul_f32 v88,v88,v232\nM v[180:183],a[48:51],a[144:147],v[228:231]\nv_mul_f32 v89,v89,v232\nv_mul_f32 v90,v90,v232\nv_mul_f32 v91,v91,v232\n" \
"v_cvt_pk_bf16_f32 v81,v82,v83\nM v[180:183],a[52:55],a[148:151],v[180:183]\nbuffer_load_dwordx4 v240,s[44:47],s95 offen offset:3072 sc1 lds\nv_mul_f32 v92,v92,v236\nv_mul_f32 v93,v93,v236\nM v[180:183],a[56:59],a[152:155],v[180:183]\nv_mul_f32 v94,v94,v236\nv_mul_f32 v95,v95,v236\n" \
"v_cvt_pk_bf16_f32 v85,v86,v87\nv_mul_f32 v104,v104,v232\nv_mul_f32 v105,v105,v232\nM v[180:183],a[60:63],a[156:159],v[180:183]\nv_mul_f32 v106,v106,v232\nv_mul_f32 v107,v107,v232\nv_cvt_pk_bf16_f32 v97,v98,v99\nv_mul_f32 v108,v108,v236\n" \
"M v[0:3],a[64:67],v[192:195],v[0:3]\nv_mul_f32 v109,v109,v236\nv_mul_f32 v110,v110,v236\nv_mul_f32 v111,v111,v236\nv_cvt_pk_bf16_f32 v101,v102,v103\nM v[4:7],a[64:67],v[196:199],v[4:7]\nv_mul_f32 v120,v120,v232\nv_mul_f32 v121,v121,v232\n" \
"v_mul_f32 v122,v122,v232\nv_mul_f32 v123,v123,v232\nM v[8:11],a[68:71],v[192:195],v[8:11]\ns_waitcnt vmcnt(16) lgkmcnt(0)\ns_barrier\nv_cvt_pk_bf16_f32 v113,v114,v115\nv_mul_f32 v124,v124,v236\nv_mul_f32 v125,v125,v236\n" \
"M v[12:15],a[68:71],v[196:199],v[12:15]\nds_read_b128 a[0:3],v250 offset:0\nds_read_b128 a[4:7],v250 offset:1024\nv_mul_f32 v126,v126,v236\nv_mul_f32 v127,v127,v236\nM v[16:19],a[72:75],v[192:195],v[16:19]\nv_cvt_pk_bf16_f32 v117,v118,v119\ns_add_u32 s38,s37,4096\n" \
"v_cvt_pk_bf16_f32 v66,v72,v73\nv_cvt_pk_bf16_f32 v67,v74,v75\nM v[20:23],a[72:75],v[196:199],v[20:23]\nv_cvt_pk_bf16_f32 v70,v76,v77\nv_cvt_pk_bf16_f32 v71,v78,v79\nv_cvt_pk_bf16_f32 v82,v88,v89\nv_cvt_pk_bf16_f32 v83,v90,v91\nv_cvt_pk_bf16_f32 v86,v92,v93\n" \
"v_cvt_pk_bf16_f32 v87,v94,v95\nv_cvt_pk_bf16_f32 v98,v104,v105\nv_cvt_pk_bf16_f32 v99,v106,v107\nv_cvt_pk_bf16_f32 v102,v108,v109\nv_cvt_pk_bf16_f32 v103,v110,v111\nv_cvt_pk_bf16_f32 v114,v120,v121\nv_cvt_pk_bf16_f32 v115,v122,v123\nv_cvt_pk_bf16_f32 v118,v124,v125\n" \
"v_cvt_pk_bf16_f32 v119,v126,v127\ns_add_u32 m0,m0,0x10000\nbuffer_store_dwordx4 v[64:67],v240,s[52:55],s37 offen offset:0 sc1\nbuffer_store_dwordx4 v[68:71],v240,s[52:55],s37 offen offset:1024 sc1\nbuffer_store_dwordx4 v[80:83],v240,s[52:55],s37 offen offset:2048 sc1\nbuffer_store_dwordx4 v[84:87],v240,s[52:55],s37 offen offset:3072 sc1\nbuffer_store_dwordx4 v[96:99],v240,s[52:55],s38 offen offset:0 sc1\nbuffer_store_dwordx4 v[100:103],v240,s[52:55],s38 offen offset:1024 sc1\n" \
"buffer_store_dwordx4 v[112:115],v240,s[52:55],s38 offen offset:2048 sc1\nbuffer_store_dwordx4 v[116:119],v240,s[52:55],s38 offen offset:3072 sc1\nbuffer_load_dwordx4 v240,s[48:51],s95 offen offset:0 sc1 lds\nM v[24:27],a[76:79],v[192:195],v[24:27]\ns_lshl2_add_u32 s77,s61,s79\nv_exp_f32 v128,v128\nv_exp_f32 v129,v129\nM v[28:31],a[76:79],v[196:199],v[28:31]\n" \
"ds_read_b128 a[8:11],v250 offset:2048\nds_read_b128 a[12:15],v250 offset:3072\nv_exp_f32 v130,v130\nM v[32:35],a[80:83],v[192:195],v[32:35]\nv_exp_f32 v131,v131\nv_exp_f32 v144,v144\nM v[36:39],a[80:83],v[196:199],v[36:39]\nv_exp_f32 v145,v145\n" \
"v_exp_f32 v146,v146\nM v[40:43],a[84:87],v[192:195],v[40:43]\nv_exp_f32 v147,v147\nv_exp_f32 v132,v132\nM v[44:47],a[84:87],v[196:199],v[44:47]\nds_read_b128 a[16:19],v250 offset:4096\nds_read_b128 a[20:23],v250 offset:5120\nv_exp_f32 v133,v133\n" \
"s_add_u32 s94,s94,0x4000\nM v[48:51],a[88:91],v[192:195],v[48:51]\nv_exp_f32 v134,v134\nv_exp_f32 v135,v135\nM v[52:55],a[88:91],v[196:199],v[52:55]\nbuffer_load_dwordx4 v240,s[48:51],s95 offen offset:1024 sc1 lds\nv_exp_f32 v148,v148\nM v[56:59],a[92:95],v[192:195],v[56:59]\n" \
"v_exp_f32 v149,v149\nv_exp_f32 v150,v150\nM v[60:63],a[92:95],v[196:199],v[60:63]\nds_read_b128 a[24:27],v250 offset:6144\nds_read_b128 a[28:31],v250 offset:7168\nv_exp_f32 v151,v151\ns_add_u32 s77,s77,4096\nM a[192:195],a[208:211],v[192:195],a[192:195]\n" \
"v_mov_b32 v233,s71\ns_and_b32 s94,s94,0xffff\nv_cvt_pk_bf16_f32 v192,v128,v129\nv_cvt_pk_bf16_f32 v193,v130,v131\nM a[196:199],a[208:211],v[196:199],a[196:199]\nv_cvt_pk_bf16_f32 v194,v144,v145\nv_cvt_pk_bf16_f32 v195,v146,v147\nv_cvt_pk_bf16_f32 v196,v132,v133\n" \
"v_cvt_pk_bf16_f32 v197,v134,v135\nM v[0:3],a[96:99],v[208:211],v[0:3]\nv_cvt_pk_bf16_f32 v198,v148,v149\nv_cvt_pk_bf16_f32 v199,v150,v151\nv_exp_f32 v160,v160\nM v[4:7],a[96:99],v[212:215],v[4:7]\nds_read_b128 a[32:35],v250 offset:8192\nds_read_b128 a[36:39],v250 offset:9216\n" \
"v_exp_f32 v161,v161\nM v[8:11],a[100:103],v[208:211],v[8:11]\nv_exp_f32 v162,v162\nv_exp_f32 v163,v163\nM v[12:15],a[100:103],v[212:215],v[12:15]\nbuffer_load_dwordx4 v240,s[48:51],s95 offen offset:2048 sc1 lds\nv_exp_f32 v176,v176\nM v[16:19],a[104:107],v[208:211],v[16:19]\n" \
"v_exp_f32 v177,v177\nv_exp_f32 v178,v178\nM v[20:23],a[104:107],v[212:215],v[20:23]\nds_read_b128 a[40:43],v250 offset:10240\nds_read_b128 a[44:47],v250 offset:11264\nv_exp_f32 v179,v179\nM v[24:27],a[108:111],v[208:211],v[24:27]\nM v[28:31],a[108:111],v[212:215],v[28:31]\n" \
"M v[32:35],a[112:115],v[208:211],v[32:35]\nM v[36:39],a[112:115],v[212:215],v[36:39]\nds_read_b128 a[48:51],v250 offset:12288\nds_read_b128 a[52:55],v250 offset:13312\nM v[40:43],a[116:119],v[208:211],v[40:43]\nM v[44:47],a[116:119],v[212:215],v[44:47]\nbuffer_load_dwordx4 v240,s[48:51],s95 offen offset:3072 sc1 lds\ns_add_u32 s95,s95,0x4000\n" \
"M v[48:51],a[120:123],v[208:211],v[48:51]\nM v[52:55],a[120:123],v[212:215],v[52:55]\nds_read_b128 a[56:59],v250 offset:14336\nds_read_b128 a[60:63],v250 offset:15360\nv_add_u32 v250,s94,v241\nM v[56:59],a[124:127],v[208:211],v[56:59]\nM v[60:63],a[124:127],v[212:215],v[60:63]\nM a[192:195],a[208:211],v[208:211],a[192:195]\n" \
"M a[196:199],a[208:211],v[212:215],a[196:199]\ns_waitcnt vmcnt(4)\nbuffer_store_dword v233,off,s[52:55],s77 sc1\ns_cmp_ge_u32 s62,6\ns_cbranch_scc1 .Lsw\n.Lsingle:\ns_waitcnt lgkmcnt(4)\nM v[128:131],a[0:3],a[128:131],v[224:227]\n" \
"s_add_u32 m0,s94,s64\nv_cvt_pk_bf16_f32 v208,v160,v161\nM v[128:131],a[4:7],a[132:135],v[128:131]\nds_read_b128 a[64:67],v250 offset:0\nds_read_b128 a[68:71],v250 offset:1024\nM v[128:131],a[8:11],a[136:139],v[128:131]\nbuffer_load_dwordx4 v240,s[44:47],s95 offen offset:0 sc1 lds\nM v[128:131],a[12:15],a[140:143],v[128:131]\n" \
"v_cvt_pk_bf16_f32 v209,v162,v163\nv_cvt_pk_bf16_f32 v210,v176,v177\nM v[132:135],a[0:3],a[144:147],v[228:231]\nds_read_b128 a[72:75],v250 offset:2048\nds_read_b128 a[76:79],v250 offset:3072\nM v[132:135],a[4:7],a[148:151],v[132:135]\nv_exp_f32 v164,v164\nM v[132:135],a[8:11],a[152:155],v[132:135]\n" \
"v_exp_f32 v165,v165\nM v[132:135],a[12:15],a[156:159],v[132:135]\nds_read_b128 a[80:83],v250 offset:4096\nds_read_b128 a[84:87],v250 offset:5120\nM v[144:147],a[16:19],a[128:131],v[224:227]\nv_exp_f32 v166,v166\nM v[144:147],a[20:23],a[132:135],v[144:147]\nv_exp_f32 v167,v167\n" \
"M v[144:147],a[24:27],a[136:139],v[144:147]\nds_read_b128 a[88:91],v250 offset:6144\nds_read_b128 a[92:95],v250 offset:7168\nM v[144:147],a[28:31],a[140:143],v[144:147]\nbuffer_load_dwordx4 v240,s[44:47],s95 offen offset:1024 sc1 lds\nM v[148:151],a[16:19],a[144:147],v[228:231]\nv_cvt_pk_bf16_f32 v212,v164,v165\nv_cvt_pk_bf16_f32 v213,v166,v167\n" \
"M v[148:151],a[20:23],a[148:151],v[148:151]\nds_read_b128 a[96:99],v250 offset:8192\nds_read_b128 a[100:103],v250 offset:9216\nM v[148:151],a[24:27],a[152:155],v[148:151]\nv_cvt_pk_bf16_f32 v211,v178,v179\ns_add_u32 s94,s94,0x4000\nM v[148:151],a[28:31],a[156:159],v[148:151]\nv_exp_f32 v180,v180\n" \
"M v[160:163],a[32:35],a[128:131],v[224:227]\nds_read_b128 a[104:107],v250 offset:10240\nds_read_b128 a[108:111],v250 offset:11264\nM v[160:163],a[36:39],a[132:135],v[160:163]\nv_exp_f32 v181,v181\nM v[160:163],a[40:43],a[136:139],v[160:163]\nv_exp_f32 v182,v182\nM v[160:163],a[44:47],a[140:143],v[160:163]\n" \
"ds_read_b128 a[112:115],v250 offset:12288\nds_read_b128 a[116:119],v250 offset:13312\nM v[164:167],a[32:35],a[144:147],v[228:231]\nbuffer_load_dwordx4 v240,s[44:47],s95 offen offset:2048 sc1 lds\nM v[164:167],a[36:39],a[148:151],v[164:167]\nv_exp_f32 v183,v183\nM v[164:167],a[40:43],a[152:155],v[164:167]\nds_read_b128 a[120:123],v250 offset:14336\n" \
"ds_read_b128 a[124:127],v250 offset:15360\nM v[164:167],a[44:47],a[156:159],v[164:167]\nv_cvt_pk_bf16_f32 v214,v180,v181\ns_waitcnt lgkmcnt(7)\nM v[176:179],a[48:51],a[128:131],v[224:227]\nv_cvt_pk_bf16_f32 v215,v182,v183\nv_xor_b32 v250,0x18000,v250\nM v[176:179],a[52:55],a[132:135],v[176:179]\n" \
"v_exp_f32 v128,v128\nM v[176:179],a[56:59],a[136:139],v[176:179]\nv_exp_f32 v129,v129\nM v[176:179],a[60:63],a[140:143],v[176:179]\nv_exp_f32 v130,v130\nM v[180:183],a[48:51],a[144:147],v[228:231]\nv_exp_f32 v131,v131\nM v[180:183],a[52:55],a[148:151],v[180:183]\n" \
"buffer_load_dwordx4 v240,s[44:47],s95 offen offset:3072 sc1 lds\nM v[180:183],a[56:59],a[152:155],v[180:183]\ns_add_u32 m0,m0,0x10000\ns_and_b32 s94,s94,0xffff\nM v[180:183],a[60:63],a[156:159],v[180:183]\nv_exp_f32 v144,v144\nM v[0:3],a[64:67],v[192:195],v[0:3]\nv_exp_f32 v145,v145\n" \
"M v[4:7],a[64:67],v[196:199],v[4:7]\nv_exp_f32 v146,v146\nM v[8:11],a[68:71],v[192:195],v[8:11]\ns_waitcnt vmcnt(16) lgkmcnt(0)\ns_barrier\nM v[12:15],a[68:71],v[196:199],v[12:15]\nds_read_b128 a[0:3],v250 offset:0\nds_read_b128 a[4:7],v250 offset:1024\n" \
"M v[16:19],a[72:75],v[192:195],v[16:19]\nv_exp_f32 v147,v147\nM v[20:23],a[72:75],v[196:199],v[20:23]\nbuffer_load_dwordx4 v240,s[48:51],s95 offen offset:0 sc1 lds\nM v[24:27],a[76:79],v[192:195],v[24:27]\nv_exp_f32 v132,v132\nM v[28:31],a[76:79],v[196:199],v[28:31]\nds_read_b128 a[8:11],v250 offset:2048\n" \
"ds_read_b128 a[12:15],v250 offset:3072\nM v[32:35],a[80:83],v[192:195],v[32:35]\nv_exp_f32 v133,v133\nM v[36:39],a[80:83],v[196:199],v[36:39]\nv_exp_f32 v134,v134\nM v[40:43],a[84:87],v[192:195],v[40:43]\nv_exp_f32 v135,v135\nM v[44:47],a[84:87],v[196:199],v[44:47]\n" \
"ds_read_b128 a[16:19],v250 offset:4096\nds_read_b128 a[20:23],v250 offset:5120\nM v[48:51],a[88:91],v[192:195],v[48:51]\nv_exp_f32 v148,v148\nM v[52:55],a[88:91],v[196:199],v[52:55]\nbuffer_load_dwordx4 v240,s[48:51],s95 offen offset:1024 sc1 lds\nM v[56:59],a[92:95],v[192:195],v[56:59]\nv_exp_f32 v149,v149\n" \
"M v[60:63],a[92:95],v[196:199],v[60:63]\nds_read_b128 a[24:27],v250 offset:6144\nds_read_b128 a[28:31],v250 offset:7168\nM a[192:195],a[208:211],v[192:195],a[192:195]\nv_exp_f32 v150,v150\nM a[196:199],a[208:211],v[196:199],a[196:199]\nv_exp_f32 v151,v151\nM v[0:3],a[96:99],v[208:211],v[0:3]\n" \
"v_cvt_pk_bf16_f32 v192,v128,v129\nv_cvt_pk_bf16_f32 v193,v130,v131\nM v[4:7],a[96:99],v[212:215],v[4:7]\nds_read_b128 a[32:35],v250 offset:8192\nds_read_b128 a[36:39],v250 offset:9216\nM v[8:11],a[100:103],v[208:211],v[8:11]\nv_cvt_pk_bf16_f32 v194,v144,v145\nv_cvt_pk_bf16_f32 v195,v146,v147\n" \
"M v[12:15],a[100:103],v[212:215],v[12:15]\nbuffer_load_dwordx4 v240,s[48:51],s95 offen offset:2048 sc1 lds\nM v[16:19],a[104:107],v[208:211],v[16:19]\nv_cvt_pk_bf16_f32 v196,v132,v133\nv_cvt_pk_bf16_f32 v197,v134,v135\nM v[20:23],a[104:107],v[212:215],v[20:23]\nds_read_b128 a[40:43],v250 offset:10240\nds_read_b128 a[44:47],v250 offset:11264\n" \
"M v[24:27],a[108:111],v[208:211],v[24:27]\nv_cvt_pk_bf16_f32 v198,v148,v149\nv_cvt_pk_bf16_f32 v199,v150,v151\nM v[28:31],a[108:111],v[212:215],v[28:31]\nv_exp_f32 v160,v160\nM v[32:35],a[112:115],v[208:211],v[32:35]\nv_exp_f32 v161,v161\nM v[36:39],a[112:115],v[212:215],v[36:39]\n" \
"ds_read_b128 a[48:51],v250 offset:12288\nds_read_b128 a[52:55],v250 offset:13312\nM v[40:43],a[116:119],v[208:211],v[40:43]\nv_exp_f32 v162,v162\nM v[44:47],a[116:119],v[212:215],v[44:47]\nbuffer_load_dwordx4 v240,s[48:51],s95 offen offset:3072 sc1 lds\nM v[48:51],a[120:123],v[208:211],v[48:51]\nv_exp_f32 v163,v163\n" \
"M v[52:55],a[120:123],v[212:215],v[52:55]\nds_read_b128 a[56:59],v250 offset:14336\nds_read_b128 a[60:63],v250 offset:15360\nM v[56:59],a[124:127],v[208:211],v[56:59]\nv_exp_f32 v176,v176\nM v[60:63],a[124:127],v[212:215],v[60:63]\nv_exp_f32 v177,v177\nM a[192:195],a[208:211],v[208:211],a[192:195]\n" \
"v_exp_f32 v178,v178\nv_exp_f32 v179,v179\nv_add_u32 v250,s94,v241\ns_add_u32 s95,s95,0x4000\nM a[196:199],a[208:211],v[212:215],a[196:199]\ns_cmp_lg_u32 s95,s15\ns_cbranch_scc1 .Lsingle\n.Lsw:\n" \
"s_waitcnt lgkmcnt(4)\nM v[128:131],a[0:3],a[128:131],v[224:227]\ns_lshl4_add_u32 s69,s12,s68\ns_add_u32 m0,s64,131072\nM v[128:131],a[4:7],a[132:135],v[128:131]\nds_read_b128 a[64:67],v250 offset:0\nds_read_b128 a[68:71],v250 offset:1024\nM v[128:131],a[8:11],a[136:139],v[128:131]\n" \
"buffer_load_dwordx4 v243,s[40:43],s69 offen offset:0 lds\nM v[128:131],a[12:15],a[140:143],v[128:131]\nv_cvt_pk_bf16_f32 v208,v160,v161\nv_cvt_pk_bf16_f32 v209,v162,v163\nM v[132:135],a[0:3],a[144:147],v[228:231]\nds_read_b128 a[72:75],v250 offset:2048\nds_read_b128 a[76:79],v250 offset:3072\nM v[132:135],a[4:7],a[148:151],v[132:135]\n" \
"v_exp_f32 v164,v164\nM v[132:135],a[8:11],a[152:155],v[132:135]\nv_exp_f32 v165,v165\nM v[132:135],a[12:15],a[156:159],v[132:135]\nds_read_b128 a[80:83],v250 offset:4096\nds_read_b128 a[84:87],v250 offset:5120\nM v[144:147],a[16:19],a[128:131],v[224:227]\nv_exp_f32 v166,v166\n" \
"M v[144:147],a[20:23],a[132:135],v[144:147]\nv_exp_f32 v167,v167\nM v[144:147],a[24:27],a[136:139],v[144:147]\nds_read_b128 a[88:91],v250 offset:6144\nds_read_b128 a[92:95],v250 offset:7168\nM v[144:147],a[28:31],a[140:143],v[144:147]\nbuffer_load_dwordx4 v244,s[40:43],s69 offen offset:1024 lds\nM v[148:151],a[16:19],a[144:147],v[228:231]\n" \
"v_cvt_pk_bf16_f32 v212,v164,v165\nv_cvt_pk_bf16_f32 v213,v166,v167\nM v[148:151],a[20:23],a[148:151],v[148:151]\nds_read_b128 a[96:99],v250 offset:8192\nds_read_b128 a[100:103],v250 offset:9216\nM v[148:151],a[24:27],a[152:155],v[148:151]\nv_cvt_pk_bf16_f32 v210,v176,v177\nv_cvt_pk_bf16_f32 v211,v178,v179\n" \
"M v[148:151],a[28:31],a[156:159],v[148:151]\nv_exp_f32 v180,v180\nM v[160:163],a[32:35],a[128:131],v[224:227]\nds_read_b128 a[104:107],v250 offset:10240\nds_read_b128 a[108:111],v250 offset:11264\nM v[160:163],a[36:39],a[132:135],v[160:163]\nv_exp_f32 v181,v181\nM v[160:163],a[40:43],a[136:139],v[160:163]\n" \
"v_exp_f32 v182,v182\nM v[160:163],a[44:47],a[140:143],v[160:163]\nds_read_b128 a[112:115],v250 offset:12288\nds_read_b128 a[116:119],v250 offset:13312\nM v[164:167],a[32:35],a[144:147],v[228:231]\nbuffer_load_dwordx4 v245,s[40:43],s69 offen offset:2048 lds\nM v[164:167],a[36:39],a[148:151],v[164:167]\nv_exp_f32 v183,v183\n" \
"M v[164:167],a[40:43],a[152:155],v[164:167]\nds_read_b128 a[120:123],v250 offset:14336\nds_read_b128 a[124:127],v250 offset:15360\nM v[164:167],a[44:47],a[156:159],v[164:167]\nv_cvt_pk_bf16_f32 v214,v180,v181\ns_waitcnt lgkmcnt(7)\nM v[176:179],a[48:51],a[128:131],v[224:227]\nv_cvt_pk_bf16_f32 v215,v182,v183\n" \
"v_xor_b32 v250,0x18000,v250\nM v[176:179],a[52:55],a[132:135],v[176:179]\nv_exp_f32 v128,v128\nM v[176:179],a[56:59],a[136:139],v[176:179]\nv_exp_f32 v129,v129\nM v[176:179],a[60:63],a[140:143],v[176:179]\nv_exp_f32 v130,v130\nM v[180:183],a[48:51],a[144:147],v[228:231]\n" \
"v_exp_f32 v131,v131\nM v[180:183],a[52:55],a[148:151],v[180:183]\nbuffer_load_dwordx4 v246,s[40:43],s69 offen offset:3072 lds\nM v[180:183],a[56:59],a[152:155],v[180:183]\nv_exp_f32 v144,v144\nM v[180:183],a[60:63],a[156:159],v[180:183]\ns_lshl4_add_u32 s69,s12,s68\ns_add_u32 s69,s69,0x10000\n" \
"M v[0:3],a[64:67],v[192:195],v[0:3]\ns_add_u32 m0,s64,147456\ns_add_u32 s94,s94,0x4000\nM v[4:7],a[64:67],v[196:199],v[4:7]\nv_exp_f32 v145,v145\nM v[8:11],a[68:71],v[192:195],v[8:11]\ns_waitcnt vmcnt(16) lgkmcnt(0)\ns_barrier\n" \
"M v[12:15],a[68:71],v[196:199],v[12:15]\nds_read_b128 a[0:3],v250 offset:0\nds_read_b128 a[4:7],v250 offset:1024\nM v[16:19],a[72:75],v[192:195],v[16:19]\nv_exp_f32 v146,v146\nM v[20:23],a[72:75],v[196:199],v[20:23]\nbuffer_load_dwordx4 v243,s[40:43],s69 offen offset:0 lds\nM v[24:27],a[76:79],v[192:195],v[24:27]\n" \
"v_exp_f32 v147,v147\nM v[28:31],a[76:79],v[196:199],v[28:31]\nds_read_b128 a[8:11],v250 offset:2048\nds_read_b128 a[12:15],v250 offset:3072\nM v[32:35],a[80:83],v[192:195],v[32:35]\nv_exp_f32 v132,v132\nM v[36:39],a[80:83],v[196:199],v[36:39]\nbuffer_load_dwordx4 v244,s[40:43],s69 offen offset:1024 lds\n" \
"M v[40:43],a[84:87],v[192:195],v[40:43]\nv_exp_f32 v133,v133\nM v[44:47],a[84:87],v[196:199],v[44:47]\nds_read_b128 a[16:19],v250 offset:4096\nds_read_b128 a[20:23],v250 offset:5120\nM v[48:51],a[88:91],v[192:195],v[48:51]\nv_exp_f32 v134,v134\nM v[52:55],a[88:91],v[196:199],v[52:55]\n" \
"buffer_load_dwordx4 v245,s[40:43],s69 offen offset:2048 lds\nM v[56:59],a[92:95],v[192:195],v[56:59]\nv_exp_f32 v135,v135\nM v[60:63],a[92:95],v[196:199],v[60:63]\nds_read_b128 a[24:27],v250 offset:6144\nds_read_b128 a[28:31],v250 offset:7168\nM a[192:195],a[208:211],v[192:195],a[192:195]\nv_exp_f32 v148,v148\n" \
"M a[196:199],a[208:211],v[196:199],a[196:199]\nbuffer_load_dwordx4 v246,s[40:43],s69 offen offset:3072 lds\nM v[0:3],a[96:99],v[208:211],v[0:3]\nv_exp_f32 v149,v149\nM v[4:7],a[96:99],v[212:215],v[4:7]\nds_read_b128 a[32:35],v250 offset:8192\nds_read_b128 a[36:39],v250 offset:9216\nM v[8:11],a[100:103],v[208:211],v[8:11]\n" \
"v_exp_f32 v150,v150\nM v[12:15],a[100:103],v[212:215],v[12:15]\nv_exp_f32 v151,v151\nM v[16:19],a[104:107],v[208:211],v[16:19]\ns_and_b32 s94,s94,0xffff\nv_cvt_pk_bf16_f32 v192,v128,v129\nM v[20:23],a[104:107],v[212:215],v[20:23]\nds_read_b128 a[40:43],v250 offset:10240\n" \
"ds_read_b128 a[44:47],v250 offset:11264\nM v[24:27],a[108:111],v[208:211],v[24:27]\nv_cvt_pk_bf16_f32 v193,v130,v131\nv_cvt_pk_bf16_f32 v194,v144,v145\nM v[28:31],a[108:111],v[212:215],v[28:31]\nv_cvt_pk_bf16_f32 v195,v146,v147\nv_cvt_pk_bf16_f32 v196,v132,v133\nM v[32:35],a[112:115],v[208:211],v[32:35]\n" \
"v_cvt_pk_bf16_f32 v197,v134,v135\nv_cvt_pk_bf16_f32 v198,v148,v149\nM v[36:39],a[112:115],v[212:215],v[36:39]\nds_read_b128 a[48:51],v250 offset:12288\nds_read_b128 a[52:55],v250 offset:13312\nM v[40:43],a[116:119],v[208:211],v[40:43]\nv_cvt_pk_bf16_f32 v199,v150,v151\ns_add_u32 s95,s95,0x4000\n" \
"M v[44:47],a[116:119],v[212:215],v[44:47]\nv_exp_f32 v160,v160\nM v[48:51],a[120:123],v[208:211],v[48:51]\nv_exp_f32 v161,v161\nM v[52:55],a[120:123],v[212:215],v[52:55]\nds_read_b128 a[56:59],v250 offset:14336\nds_read_b128 a[60:63],v250 offset:15360\nM v[56:59],a[124:127],v[208:211],v[56:59]\n" \
"v_exp_f32 v162,v162\nM v[60:63],a[124:127],v[212:215],v[60:63]\nv_exp_f32 v163,v163\nM a[192:195],a[208:211],v[208:211],a[192:195]\nv_exp_f32 v176,v176\nM a[196:199],a[208:211],v[212:215],a[196:199]\nv_exp_f32 v177,v177\nv_exp_f32 v178,v178\n" \
"v_exp_f32 v179,v179\nv_add_u32 v250,s94,v241\ns_waitcnt lgkmcnt(2)\nM v[128:131],a[0:3],a[128:131],v[224:227]\ns_lshl2_add_u32 s39,s62,s7\ns_lshl_b32 s39,s39,5\nM v[128:131],a[4:7],a[132:135],v[128:131]\nds_read_b128 a[64:67],v250 offset:0\n" \
"ds_read_b128 a[68:71],v250 offset:1024\nM v[128:131],a[8:11],a[136:139],v[128:131]\ns_add_u32 s39,s39,s63\ns_lshl2_add_u32 s39,s39,73728\nM v[128:131],a[12:15],a[140:143],v[128:131]\nv_cvt_pk_bf16_f32 v208,v160,v161\nv_cvt_pk_bf16_f32 v209,v162,v163\nv_and_b32 v251,3,v249\n" \
"M v[132:135],a[0:3],a[144:147],v[228:231]\nds_read_b128 a[72:75],v250 offset:2048\nds_read_b128 a[76:79],v250 offset:3072\nM v[132:135],a[4:7],a[148:151],v[132:135]\nv_exp_f32 v164,v164\nM v[132:135],a[8:11],a[152:155],v[132:135]\ns_waitcnt vmcnt(16)\nbuffer_store_dword v252,off,s[52:55],s39 sc1\n" \
"M v[132:135],a[12:15],a[156:159],v[132:135]\nds_read_b128 a[80:83],v250 offset:4096\nds_read_b128 a[84:87],v250 offset:5120\nv_lshlrev_b32 v251,2,v251\nM v[144:147],a[16:19],a[128:131],v[224:227]\nv_exp_f32 v165,v165\nM v[144:147],a[20:23],a[132:135],v[144:147]\nv_exp_f32 v166,v166\n" \
"M v[144:147],a[24:27],a[136:139],v[144:147]\nds_read_b128 a[88:91],v250 offset:6144\nds_read_b128 a[92:95],v250 offset:7168\nM v[144:147],a[28:31],a[140:143],v[144:147]\nv_exp_f32 v167,v167\nv_cvt_pk_bf16_f32 v212,v164,v165\nM v[148:151],a[16:19],a[144:147],v[228:231]\nv_cvt_pk_bf16_f32 v213,v166,v167\n" \
"s_add_u32 s80,s79,4096\nM v[148:151],a[20:23],a[148:151],v[148:151]\nds_read_b128 a[96:99],v250 offset:8192\nds_read_b128 a[100:103],v250 offset:9216\nM v[148:151],a[24:27],a[152:155],v[148:151]\nv_cvt_pk_bf16_f32 v210,v176,v177\nv_cvt_pk_bf16_f32 v211,v178,v179\nM v[148:151],a[28:31],a[156:159],v[148:151]\n" \
"v_exp_f32 v180,v180\ns_add_u32 s94,s94,0x4000\nM v[160:163],a[32:35],a[128:131],v[224:227]\nds_read_b128 a[104:107],v250 offset:10240\nds_read_b128 a[108:111],v250 offset:11264\nM v[160:163],a[36:39],a[132:135],v[160:163]\nv_exp_f32 v181,v181\nM v[160:163],a[40:43],a[136:139],v[160:163]\n" \
"v_exp_f32 v182,v182\nM v[160:163],a[44:47],a[140:143],v[160:163]\nds_read_b128 a[112:115],v250 offset:12288\nds_read_b128 a[116:119],v250 offset:13312\nv_cvt_pk_bf16_f32 v214,v180,v181\nM v[164:167],a[32:35],a[144:147],v[228:231]\nbuffer_load_dword v248,v251,s[52:55],s80 offen sc1\nM v[164:167],a[36:39],a[148:151],v[164:167]\n" \
"v_exp_f32 v183,v183\nM v[164:167],a[40:43],a[152:155],v[164:167]\nds_read_b128 a[120:123],v250 offset:14336\nds_read_b128 a[124:127],v250 offset:15360\nM v[164:167],a[44:47],a[156:159],v[164:167]\nv_cvt_pk_bf16_f32 v215,v182,v183\ns_and_b32 s94,s94,0xffff\nv_add_u32 v250,s94,v241\n" \
"M v[176:179],a[48:51],a[128:131],v[224:227]\nv_exp_f32 v128,v128\nM v[176:179],a[52:55],a[132:135],v[176:179]\ns_add_u32 s65,s10,s67\ns_waitcnt lgkmcnt(4)\nM v[176:179],a[56:59],a[136:139],v[176:179]\nv_exp_f32 v129,v129\nM v[176:179],a[60:63],a[140:143],v[176:179]\n" \
"v_exp_f32 v130,v130\ns_add_u32 s65,s65,1048576\nM v[180:183],a[48:51],a[144:147],v[228:231]\nv_exp_f32 v131,v131\nM v[180:183],a[52:55],a[148:151],v[180:183]\nv_exp_f32 v144,v144\nM v[180:183],a[56:59],a[152:155],v[180:183]\ns_lshl4_add_u32 s69,s10,s68\n" \
"s_add_u32 m0,s64,0\nM v[180:183],a[60:63],a[156:159],v[180:183]\nv_exp_f32 v145,v145\nv_add_u32 v226,131072,v242\nM v[0:3],a[64:67],v[192:195],v[0:3]\nv_exp_f32 v146,v146\nM v[4:7],a[64:67],v[196:199],v[4:7]\nv_exp_f32 v147,v147\n" \
"M v[8:11],a[68:71],v[192:195],v[8:11]\ns_waitcnt lgkmcnt(0)\ns_barrier\nM v[12:15],a[68:71],v[196:199],v[12:15]\nds_read_b128 a[64:67],v250 offset:0\nds_read_b128 a[68:71],v250 offset:1024\nv_xor_b32 v227,32,v226\nM v[16:19],a[72:75],v[192:195],v[16:19]\n" \
"buffer_load_dwordx4 v243,s[40:43],s69 offen offset:0 lds\nM v[20:23],a[72:75],v[196:199],v[20:23]\nds_read_b128 a[72:75],v250 offset:2048\ns_add_u32 s37,s12,s67\nM v[24:27],a[76:79],v[192:195],v[24:27]\nv_exp_f32 v132,v132\nM v[28:31],a[76:79],v[196:199],v[28:31]\nds_read_b128 a[76:79],v250 offset:3072\n" \
"v_exp_f32 v133,v133\nM v[32:35],a[80:83],v[192:195],v[32:35]\nbuffer_load_dwordx4 v244,s[40:43],s69 offen offset:1024 lds\nM v[36:39],a[80:83],v[196:199],v[36:39]\nds_read_b128 a[80:83],v250 offset:4096\ns_add_u32 s38,s37,4096\nM v[40:43],a[84:87],v[192:195],v[40:43]\nv_exp_f32 v134,v134\n" \
"M v[44:47],a[84:87],v[196:199],v[44:47]\nv_exp_f32 v135,v135\nds_read_b128 a[84:87],v250 offset:5120\nM v[48:51],a[88:91],v[192:195],v[48:51]\nbuffer_load_dwordx4 v245,s[40:43],s69 offen offset:2048 lds\nM v[52:55],a[88:91],v[196:199],v[52:55]\nv_exp_f32 v148,v148\nM v[56:59],a[92:95],v[192:195],v[56:59]\n" \
"v_exp_f32 v149,v149\nM v[60:63],a[92:95],v[196:199],v[60:63]\nv_exp_f32 v150,v150\nds_read_b128 a[88:91],v250 offset:6144\nM a[192:195],a[208:211],v[192:195],a[192:195]\nbuffer_load_dwordx4 v246,s[40:43],s69 offen offset:3072 lds\nM a[196:199],a[208:211],v[196:199],a[196:199]\ns_lshl4_add_u32 s69,s10,s68\n" \
"s_add_u32 s69,s69,0x10000\nM v[0:3],a[96:99],v[208:211],v[0:3]\ns_add_u32 m0,s64,16384\nv_cvt_pk_bf16_f32 v192,v128,v129\nM v[4:7],a[96:99],v[212:215],v[4:7]\nv_exp_f32 v151,v151\nv_cvt_pk_bf16_f32 v193,v130,v131\nM v[8:11],a[100:103],v[208:211],v[8:11]\n" \
"buffer_load_dwordx4 v243,s[40:43],s69 offen offset:0 lds\nM v[12:15],a[100:103],v[212:215],v[12:15]\nv_cvt_pk_bf16_f32 v194,v144,v145\nv_cvt_pk_bf16_f32 v195,v146,v147\nM v[16:19],a[104:107],v[208:211],v[16:19]\nv_cvt_pk_bf16_f32 v196,v132,v133\nv_cvt_pk_bf16_f32 v197,v134,v135\nM v[20:23],a[104:107],v[212:215],v[20:23]\n" \
"v_cvt_pk_bf16_f32 v198,v148,v149\nv_cvt_pk_bf16_f32 v199,v150,v151\nds_read_b128 a[92:95],v250 offset:7168\nM v[24:27],a[108:111],v[208:211],v[24:27]\nbuffer_load_dwordx4 v244,s[40:43],s69 offen offset:1024 lds\nM v[28:31],a[108:111],v[212:215],v[28:31]\nds_read_b128 a[96:99],v250 offset:8192\nds_read_b128 a[100:103],v250 offset:9216\n" \
"M v[32:35],a[112:115],v[208:211],v[32:35]\nds_read_b128 a[104:107],v250 offset:10240\ns_lshl2_add_u32 s76,s61,s79\nM v[36:39],a[112:115],v[212:215],v[36:39]\nds_read_b128 a[108:111],v250 offset:11264\nv_exp_f32 v160,v160\nM v[40:43],a[116:119],v[208:211],v[40:43]\nbuffer_load_dwordx4 v245,s[40:43],s69 offen offset:2048 lds\n" \
"M v[44:47],a[116:119],v[212:215],v[44:47]\nv_exp_f32 v161,v161\nM v[48:51],a[120:123],v[208:211],v[48:51]\nv_exp_f32 v162,v162\nM v[52:55],a[120:123],v[212:215],v[52:55]\nv_exp_f32 v163,v163\nds_read_b128 a[112:115],v250 offset:12288\nM v[56:59],a[124:127],v[208:211],v[56:59]\n" \
"buffer_load_dwordx4 v246,s[40:43],s69 offen offset:3072 lds\nM v[60:63],a[124:127],v[212:215],v[60:63]\ns_waitcnt vmcnt(8)\nv_and_b32 v248,s17,v248\nM a[192:195],a[208:211],v[208:211],a[192:195]\nv_cmp_eq_u32 vcc,0,v248\nv_cvt_pk_bf16_f32 v208,v160,v161\nM a[196:199],a[208:211],v[212:215],a[196:199]\n" \
"s_cbranch_vccnz .Lps6\n.Lpb6:\nv_cvt_pk_bf16_f32 v209,v162,v163\ns_waitcnt lgkmcnt(1)\nM v[0:3],a[64:67],v[192:195],v[0:3]\nbuffer_load_dwordx4 v[72:75],v240,s[52:55],s65 offen offset:0 sc1\nM v[4:7],a[64:67],v[196:199],v[4:7]\nv_exp_f32 v176,v176\n" \
"M v[8:11],a[68:71],v[192:195],v[8:11]\nv_exp_f32 v177,v177\nM v[12:15],a[68:71],v[196:199],v[12:15]\nv_exp_f32 v178,v178\nv_cvt_pk_bf16_f32 v210,v176,v177\nM v[16:19],a[72:75],v[192:195],v[16:19]\nv_exp_f32 v179,v179\nM v[20:23],a[72:75],v[196:199],v[20:23]\n" \
"buffer_load_dwordx4 v[76:79],v240,s[52:55],s65 offen offset:1024 sc1\nM v[24:27],a[76:79],v[192:195],v[24:27]\nv_exp_f32 v164,v164\nM v[28:31],a[76:79],v[196:199],v[28:31]\nv_exp_f32 v165,v165\nv_cvt_pk_bf16_f32 v211,v178,v179\nM v[32:35],a[80:83],v[192:195],v[32:35]\nv_exp_f32 v166,v166\n" \
"M v[36:39],a[80:83],v[196:199],v[36:39]\nv_exp_f32 v167,v167\nM v[40:43],a[84:87],v[192:195],v[40:43]\nbuffer_load_dwordx4 v[88:91],v240,s[52:55],s65 offen offset:2048 sc1\nM v[44:47],a[84:87],v[196:199],v[44:47]\nv_exp_f32 v180,v180\nv_cvt_pk_bf16_f32 v212,v164,v165\nM v[48:51],a[88:91],v[192:195],v[48:51]\n" \
"v_exp_f32 v181,v181\nM v[52:55],a[88:91],v[196:199],v[52:55]\nv_exp_f32 v182,v182\nM v[56:59],a[92:95],v[192:195],v[56:59]\nv_exp_f32 v183,v183\nM v[60:63],a[92:95],v[196:199],v[60:63]\nbuffer_load_dwordx4 v[92:95],v240,s[52:55],s65 offen offset:3072 sc1\nv_cvt_pk_bf16_f32 v213,v166,v167\n" \
"M a[192:195],a[208:211],v[192:195],a[192:195]\nv_cvt_pk_bf16_f32 v214,v180,v181\nv_cvt_pk_bf16_f32 v215,v182,v183\nM a[196:199],a[208:211],v[196:199],a[196:199]\nds_read_b128 a[116:119],v250 offset:13312\nds_read_b128 a[120:123],v250 offset:14336\nM v[0:3],a[96:99],v[208:211],v[0:3]\ns_barrier\n" \
"s_add_u32 s65,s65,4096\nM v[4:7],a[96:99],v[212:215],v[4:7]\nds_read_b128 a[124:127],v250 offset:15360\nds_read_b64_tr_b16 v[200:201],v226 offset:0\nv_cmp_eq_u32 vcc,2,v248\nM v[8:11],a[100:103],v[208:211],v[8:11]\nbuffer_load_dwordx4 v[104:107],v240,s[52:55],s65 offen offset:0 sc1\nM v[12:15],a[100:103],v[212:215],v[12:15]\n" \
"ds_read_b64_tr_b16 v[202:203],v226 offset:4096\nds_read_b64_tr_b16 v[204:205],v227 offset:0\nM v[16:19],a[104:107],v[208:211],v[16:19]\nds_read_b64_tr_b16 v[206:207],v227 offset:4096\nds_read_b64_tr_b16 v[216:217],v226 offset:512\nM v[20:23],a[104:107],v[212:215],v[20:23]\nds_read_b64_tr_b16 v[218:219],v226 offset:4608\nds_read_b64_tr_b16 v[220:221],v227 offset:512\n" \
"ds_read_b64_tr_b16 v[222:223],v227 offset:4608\nM v[24:27],a[108:111],v[208:211],v[24:27]\nds_read_b64_tr_b16 v[192:193],v226 offset:1024\nds_read_b64_tr_b16 v[194:195],v226 offset:5120\nM v[28:31],a[108:111],v[212:215],v[28:31]\nbuffer_load_dwordx4 v[108:111],v240,s[52:55],s65 offen offset:1024 sc1\ns_waitcnt lgkmcnt(11)\nM v[32:35],a[112:115],v[208:211],v[32:35]\n" \
"ds_read_b64_tr_b16 v[196:197],v227 offset:1024\nds_read_b64_tr_b16 v[198:199],v227 offset:5120\nM v[36:39],a[112:115],v[212:215],v[36:39]\ns_cmp_lg_u64 vcc,0\ns_addc_u32 s99,s99,0\nv_xor_b32 v250,0x8000,v250\nM v[40:43],a[116:119],v[208:211],v[40:43]\nv_add_u32 v229,s64,v250\n" \
"s_lshr_b32 s70,s60,1\nM v[44:47],a[116:119],v[212:215],v[44:47]\ns_lshl_b32 s70,s70,7\ns_lshl_b32 s80,s62,9\nM v[48:51],a[120:123],v[208:211],v[48:51]\nbuffer_load_dwordx4 v[120:123],v240,s[52:55],s65 offen offset:2048 sc1\nM v[52:55],a[120:123],v[212:215],v[52:55]\ns_add_u32 s80,s80,73728\n" \
"v_and_b32 v236,15,v249\ns_waitcnt lgkmcnt(2)\nM v[56:59],a[124:127],v[208:211],v[56:59]\ns_lshl2_add_u32 s74,s63,s7\ns_lshl_b32 s72,s62,7\nM v[60:63],a[124:127],v[212:215],v[60:63]\nv_lshrrev_b32 v235,4,v249\nv_lshlrev_b32 v236,1,v236\n" \
"M a[192:195],a[208:211],v[208:211],a[192:195]\nds_read_b64_tr_b16 v[208:209],v226 offset:1536\nds_read_b64_tr_b16 v[210:211],v226 offset:5632\nM a[196:199],a[208:211],v[212:215],a[196:199]\nbuffer_load_dwordx4 v[124:127],v240,s[52:55],s65 offen offset:3072 sc1\nds_read_b64_tr_b16 v[212:213],v227 offset:1536\nM v[128:131],v[64:67],v[200:203],0\nds_read_b64_tr_b16 v[214:215],v227 offset:5632\n" \
"s_lshl4_add_u32 s69,s11,s68\ns_add_u32 m0,s64,32768\nM v[132:135],v[68:71],v[200:203],0\nds_read_b64_tr_b16 v[200:201],v226 offset:8192\nds_read_b64_tr_b16 v[202:203],v226 offset:12288\nv_accvgpr_read_b32 v224,a192\nM v[136:139],v[64:67],v[204:207],0\nv_accvgpr_read_b32 v228,a196\n" \
"v_add_f32 v225,v224,v228\ns_add_u32 s65,s11,s67\nM v[140:143],v[68:71],v[204:207],0\nds_read_b64_tr_b16 v[204:205],v227 offset:8192\nds_read_b64_tr_b16 v[206:207],v227 offset:12288\ns_add_u32 s65,s65,1048576\nM v[144:147],v[64:67],v[216:219],0\nbuffer_load_dwordx4 v243,s[40:43],s69 offen offset:0 lds\n" \
"s_add_u32 s95,s74,s72\nM v[148:151],v[68:71],v[216:219],0\nds_read_b64_tr_b16 v[216:217],v226 offset:8704\nds_read_b64_tr_b16 v[218:219],v226 offset:12800\nv_lshl_or_b32 v235,v235,8,v236\nM v[152:155],v[64:67],v[220:223],0\nv_rcp_f32 v224,v224\ns_lshl_b32 s95,s95,10\n" \
"M v[156:159],v[68:71],v[220:223],0\nds_read_b64_tr_b16 v[220:221],v227 offset:8704\nds_read_b64_tr_b16 v[222:223],v227 offset:12800\nv_mul_f32 v0,v0,v224\nM v[160:163],v[64:67],v[192:195],0\nbuffer_load_dwordx4 v244,s[40:43],s69 offen offset:1024 lds\nv_mul_f32 v1,v1,v224\nM v[164:167],v[68:71],v[192:195],0\n" \
"ds_read_b64_tr_b16 v[192:193],v226 offset:9216\nds_read_b64_tr_b16 v[194:195],v226 offset:13312\ns_waitcnt lgkmcnt(6)\nM v[168:171],v[64:67],v[196:199],0\nv_mul_f32 v2,v2,v224\nv_mul_f32 v3,v3,v224\nv_cvt_pk_bf16_f32 v0,v0,v1\nM v[172:175],v[68:71],v[196:199],0\n" \
"ds_read_b64_tr_b16 v[196:197],v227 offset:9216\nds_read_b64_tr_b16 v[198:199],v227 offset:13312\nv_mul_f32 v8,v8,v224\nM v[176:179],v[64:67],v[208:211],0\nbuffer_load_dwordx4 v245,s[40:43],s69 offen offset:2048 lds\nv_mul_f32 v9,v9,v224\nM v[180:183],v[68:71],v[208:211],0\nds_read_b64_tr_b16 v[208:209],v226 offset:9728\n" \
"ds_read_b64_tr_b16 v[210:211],v226 offset:13824\nv_mul_f32 v10,v10,v224\nM v[184:187],v[64:67],v[212:215],0\nv_mul_f32 v11,v11,v224\nv_cvt_pk_bf16_f32 v1,v2,v3\nv_cvt_pk_bf16_f32 v2,v8,v9\nM v[188:191],v[68:71],v[212:215],0\nds_read_b64_tr_b16 v[212:213],v227 offset:9728\n" \
"ds_read_b64_tr_b16 v[214:215],v227 offset:13824\nv_cvt_pk_bf16_f32 v3,v10,v11\nM v[128:131],v[80:83],v[200:203],v[128:131]\nbuffer_load_dwordx4 v246,s[40:43],s69 offen offset:3072 lds\ns_lshl4_add_u32 s69,s11,s68\nM v[132:135],v[84:87],v[200:203],v[132:135]\ns_add_u32 s69,s69,0x10000\ns_add_u32 m0,s64,49152\n" \
"ds_read_b64_tr_b16 v[200:201],v226 offset:16384\nM v[136:139],v[80:83],v[204:207],v[136:139]\nds_read_b64_tr_b16 v[202:203],v226 offset:20480\nv_rcp_f32 v228,v228\nM v[140:143],v[84:87],v[204:207],v[140:143]\nds_read_b64_tr_b16 v[204:205],v227 offset:16384\nds_read_b64_tr_b16 v[206:207],v227 offset:20480\ns_waitcnt lgkmcnt(6)\n" \
"M v[144:147],v[80:83],v[216:219],v[144:147]\nbuffer_load_dwordx4 v243,s[40:43],s69 offen offset:0 lds\nv_mul_f32 v4,v4,v228\nM v[148:151],v[84:87],v[216:219],v[148:151]\nds_read_b64_tr_b16 v[216:217],v226 offset:16896\nds_read_b64_tr_b16 v[218:219],v226 offset:20992\nv_mul_f32 v5,v5,v228\nM v[152:155],v[80:83],v[220:223],v[152:155]\n" \
"buffer_store_dwordx4 v[0:3],v240,s[52:55],s37 offen offset:0 sc1\nv_mul_f32 v6,v6,v228\nM v[156:159],v[84:87],v[220:223],v[156:159]\nds_read_b64_tr_b16 v[220:221],v227 offset:16896\nds_read_b64_tr_b16 v[222:223],v227 offset:20992\nv_mul_f32 v7,v7,v228\nM v[160:163],v[80:83],v[192:195],v[160:163]\nbuffer_load_dwordx4 v244,s[40:43],s69 offen offset:1024 lds\n" \
"v_cvt_pk_bf16_f32 v4,v4,v5\nM v[164:167],v[84:87],v[192:195],v[164:167]\nds_read_b64_tr_b16 v[192:193],v226 offset:17408\nds_read_b64_tr_b16 v[194:195],v226 offset:21504\nv_mul_f32 v12,v12,v228\nM v[168:171],v[80:83],v[196:199],v[168:171]\nv_mul_f32 v13,v13,v228\nv_mul_f32 v14,v14,v228\n" \
"v_mul_f32 v15,v15,v228\nM v[172:175],v[84:87],v[196:199],v[172:175]\nv_cvt_pk_bf16_f32 v5,v6,v7\nds_read_b64_tr_b16 v[196:197],v227 offset:17408\nds_read_b64_tr_b16 v[198:199],v227 offset:21504\nM v[176:179],v[80:83],v[208:211],v[176:179]\nbuffer_load_dwordx4 v245,s[40:43],s69 offen offset:2048 lds\nv_cvt_pk_bf16_f32 v6,v12,v13\n" \
"M v[180:183],v[84:87],v[208:211],v[180:183]\nv_cvt_pk_bf16_f32 v7,v14,v15\nv_add_f32 v225,v225,v224\ns_waitcnt lgkmcnt(4)\nM v[184:187],v[80:83],v[212:215],v[184:187]\nds_read_b64_tr_b16 v[208:209],v226 offset:17920\nds_read_b64_tr_b16 v[210:211],v226 offset:22016\nv_add_f32 v225,v225,v228\n" \
"M v[188:191],v[84:87],v[212:215],v[188:191]\nv_cmp_ngt_f32 vcc,s28,v225\nds_read_b64_tr_b16 v[212:213],v227 offset:17920\nds_read_b64_tr_b16 v[214:215],v227 offset:22016\nM v[128:131],v[96:99],v[200:203],v[128:131]\nbuffer_store_dwordx4 v[4:7],v240,s[52:55],s37 offen offset:1024 sc1\nbuffer_load_dwordx4 v246,s[40:43],s69 offen offset:3072 lds\nM v[132:135],v[100:103],v[200:203],v[132:135]\n" \
"s_and_b64 vcc,vcc,s[90:91]\ns_addc_u32 s99,s99,0\nv_mov_b32 v230,s99\nM v[136:139],v[96:99],v[204:207],v[136:139]\nds_write_b32 v229,v230\nds_read_b64_tr_b16 v[200:201],v226 offset:24576\nds_read_b64_tr_b16 v[202:203],v226 offset:28672\nM v[140:143],v[100:103],v[204:207],v[140:143]\n" \
"ds_read_b64_tr_b16 v[204:205],v227 offset:24576\nds_read_b64_tr_b16 v[206:207],v227 offset:28672\nv_mul_f32 v16,v16,v224\nM v[144:147],v[96:99],v[216:219],v[144:147]\nv_mul_f32 v17,v17,v224\nv_mul_f32 v18,v18,v224\nv_mul_f32 v19,v19,v224\nM v[148:151],v[100:103],v[216:219],v[148:151]\n" \
"v_cvt_pk_bf16_f32 v16,v16,v17\nv_mul_f32 v24,v24,v224\nv_mul_f32 v25,v25,v224\nM v[152:155],v[96:99],v[220:223],v[152:155]\nbuffer_load_dwordx4 a[0:3],v240,s[52:55],s65 offen offset:0 sc1\nv_mul_f32 v26,v26,v224\nM v[156:159],v[100:103],v[220:223],v[156:159]\nv_mul_f32 v27,v27,v224\n" \
"v_cvt_pk_bf16_f32 v17,v18,v19\ns_waitcnt lgkmcnt(5)\nM v[160:163],v[96:99],v[192:195],v[160:163]\nds_read_b64_tr_b16 v[216:217],v226 offset:25088\nds_read_b64_tr_b16 v[218:219],v226 offset:29184\nv_cvt_pk_bf16_f32 v18,v24,v25\nM v[164:167],v[100:103],v[192:195],v[164:167]\nv_cvt_pk_bf16_f32 v19,v26,v27\n" \
"ds_read_b64_tr_b16 v[220:221],v227 offset:25088\nds_read_b64_tr_b16 v[222:223],v227 offset:29184\nM v[168:171],v[96:99],v[196:199],v[168:171]\nbuffer_store_dwordx4 v[16:19],v240,s[52:55],s37 offen offset:2048 sc1\nbuffer_load_dwordx4 a[4:7],v240,s[52:55],s65 offen offset:1024 sc1\nM v[172:175],v[100:103],v[196:199],v[172:175]\nds_read_b64_tr_b16 v[192:193],v226 offset:25600\nds_read_b64_tr_b16 v[194:195],v226 offset:29696\n" \
"ds_read_b64_tr_b16 v[196:197],v227 offset:25600\nM v[176:179],v[96:99],v[208:211],v[176:179]\nds_read_b64_tr_b16 v[198:199],v227 offset:29696\nv_lshl_add_u32 v229,v251,10,v250\nv_mul_f32 v20,v20,v228\nM v[180:183],v[100:103],v[208:211],v[180:183]\nds_read_b64_tr_b16 v[208:209],v226 offset:26112\nds_read_b64_tr_b16 v[210:211],v226 offset:30208\n" \
"v_mul_f32 v21,v21,v228\nM v[184:187],v[96:99],v[212:215],v[184:187]\ns_waitcnt vmcnt(14) lgkmcnt(8)\ns_barrier\nbuffer_load_dwordx4 a[8:11],v240,s[52:55],s65 offen offset:2048 sc1\nM v[188:191],v[100:103],v[212:215],v[188:191]\nds_read_b32 v230,v229\nv_mul_f32 v22,v22,v228\n" \
"v_mul_f32 v23,v23,v228\nM v[128:131],v[112:115],v[200:203],v[128:131]\nv_cvt_pk_bf16_f32 v20,v20,v21\nv_mul_f32 v28,v28,v228\nv_mul_f32 v29,v29,v228\nM v[132:135],v[116:119],v[200:203],v[132:135]\nv_mul_f32 v30,v30,v228\nv_mul_f32 v31,v31,v228\n" \
"v_cvt_pk_bf16_f32 v21,v22,v23\nM v[136:139],v[112:115],v[204:207],v[136:139]\nbuffer_load_dwordx4 a[12:15],v240,s[52:55],s65 offen offset:3072 sc1\nds_read_b64_tr_b16 v[212:213],v227 offset:26112\nM v[140:143],v[116:119],v[204:207],v[140:143]\nds_read_b64_tr_b16 v[214:215],v227 offset:30208\nv_cvt_pk_bf16_f32 v22,v28,v29\nv_cvt_pk_bf16_f32 v23,v30,v31\n" \
"M v[144:147],v[112:115],v[216:219],v[144:147]\nbuffer_store_dwordx4 v[20:23],v240,s[52:55],s37 offen offset:3072 sc1\nv_add_u32 v226,0,v242\nM v[148:151],v[116:119],v[216:219],v[148:151]\nds_read_b64_tr_b16 v[200:201],v226 offset:0\nds_read_b64_tr_b16 v[202:203],v226 offset:4096\ns_waitcnt lgkmcnt(4)\nM v[152:155],v[112:115],v[220:223],v[152:155]\n" \
"s_add_u32 s65,s65,4096\nbuffer_load_dwordx4 a[16:19],v240,s[52:55],s65 offen offset:0 sc1\nM v[156:159],v[116:119],v[220:223],v[156:159]\nv_xor_b32 v227,32,v226\nds_read_b64_tr_b16 v[204:205],v227 offset:0\nds_read_b64_tr_b16 v[206:207],v227 offset:4096\nM v[160:163],v[112:115],v[192:195],v[160:163]\nds_read_b64_tr_b16 v[216:217],v226 offset:512\n" \
"ds_read_b64_tr_b16 v[218:219],v226 offset:4608\nds_read_b64_tr_b16 v[220:221],v227 offset:512\nM v[164:167],v[116:119],v[192:195],v[164:167]\nds_read_b64_tr_b16 v[222:223],v227 offset:4608\nv_cmp_ne_u32 vcc,0,v230\nv_mul_f32 v32,v32,v224\nM v[168:171],v[112:115],v[196:199],v[168:171]\nbuffer_load_dwordx4 a[20:23],v240,s[52:55],s65 offen offset:1024 sc1\n" \
"v_mul_f32 v33,v33,v224\nM v[172:175],v[116:119],v[196:199],v[172:175]\ns_cbranch_vccnz .Lredo\nv_mul_f32 v34,v34,v224\nv_mul_f32 v35,v35,v224\nM v[176:179],v[112:115],v[208:211],v[176:179]\nv_cvt_pk_bf16_f32 v32,v32,v33\nv_mul_f32 v40,v40,v224\n" \
"v_mul_f32 v41,v41,v224\nM v[180:183],v[116:119],v[208:211],v[180:183]\nv_mul_f32 v42,v42,v224\nv_mul_f32 v43,v43,v224\nv_cvt_pk_bf16_f32 v33,v34,v35\nds_read_b64_tr_b16 v[192:193],v226 offset:1024\nds_read_b64_tr_b16 v[194:195],v226 offset:5120\ns_waitcnt lgkmcnt(4)\n" \
"M v[184:187],v[112:115],v[212:215],v[184:187]\nv_cvt_pk_bf16_f32 v34,v40,v41\nv_cvt_pk_bf16_f32 v35,v42,v43\nbuffer_store_dwordx4 v[32:35],v240,s[52:55],s38 offen offset:0 sc1\nbuffer_load_dwordx4 a[24:27],v240,s[52:55],s65 offen offset:2048 sc1\nM v[188:191],v[116:119],v[212:215],v[188:191]\nds_read_b64_tr_b16 v[196:197],v227 offset:1024\nds_read_b64_tr_b16 v[198:199],v227 offset:5120\n" \
"ds_read_b64_tr_b16 v[208:209],v226 offset:1536\nM v[128:131],v[72:75],v[200:203],v[128:131]\nds_read_b64_tr_b16 v[210:211],v226 offset:5632\nds_read_b64_tr_b16 v[212:213],v227 offset:1536\nv_mul_f32 v36,v36,v228\nM v[132:135],v[76:79],v[200:203],v[132:135]\nds_read_b64_tr_b16 v[214:215],v227 offset:5632\nds_read_b64_tr_b16 v[200:201],v226 offset:8192\n" \
"v_mul_f32 v37,v37,v228\nM v[136:139],v[72:75],v[204:207],v[136:139]\nbuffer_load_dwordx4 a[28:31],v240,s[52:55],s65 offen offset:3072 sc1\nds_read_b64_tr_b16 v[202:203],v226 offset:12288\nM v[140:143],v[76:79],v[204:207],v[140:143]\nv_mul_f32 v38,v38,v228\nv_mul_f32 v39,v39,v228\nv_cvt_pk_bf16_f32 v36,v36,v37\n" \
"M v[144:147],v[72:75],v[216:219],v[144:147]\nv_mul_f32 v44,v44,v228\nv_mul_f32 v45,v45,v228\nv_mul_f32 v46,v46,v228\nM v[148:151],v[76:79],v[216:219],v[148:151]\nv_mul_f32 v47,v47,v228\nv_cvt_pk_bf16_f32 v37,v38,v39\ns_waitcnt lgkmcnt(5)\n" \
"M v[152:155],v[72:75],v[220:223],v[152:155]\nds_read_b64_tr_b16 v[204:205],v227 offset:8192\nds_read_b64_tr_b16 v[206:207],v227 offset:12288\nv_cvt_pk_bf16_f32 v38,v44,v45\nM v[156:159],v[76:79],v[220:223],v[156:159]\nv_cvt_pk_bf16_f32 v39,v46,v47\ns_lshl4_add_u32 s69,s14,s68\ns_add_u32 m0,s64,65536\n" \
"M v[160:163],v[72:75],v[192:195],v[160:163]\nbuffer_store_dwordx4 v[36:39],v240,s[52:55],s38 offen offset:1024 sc1\nbuffer_load_dwordx4 v243,s[40:43],s69 offen offset:0 lds\nM v[164:167],v[76:79],v[192:195],v[164:167]\nds_read_b64_tr_b16 v[216:217],v226 offset:8704\nds_read_b64_tr_b16 v[218:219],v226 offset:12800\nds_read_b64_tr_b16 v[220:221],v227 offset:8704\nM v[168:171],v[72:75],v[196:199],v[168:171]\n" \
"ds_read_b64_tr_b16 v[222:223],v227 offset:12800\nds_read_b64_tr_b16 v[192:193],v226 offset:9216\nv_mul_f32 v48,v48,v224\nM v[172:175],v[76:79],v[196:199],v[172:175]\nds_read_b64_tr_b16 v[194:195],v226 offset:13312\nds_read_b64_tr_b16 v[196:197],v227 offset:9216\ns_waitcnt lgkmcnt(9)\nM v[176:179],v[72:75],v[208:211],v[176:179]\n" \
"buffer_load_dwordx4 v244,s[40:43],s69 offen offset:1024 lds\nds_read_b64_tr_b16 v[198:199],v227 offset:13312\nM v[180:183],v[76:79],v[208:211],v[180:183]\nv_mul_f32 v49,v49,v224\nv_mul_f32 v50,v50,v224\nv_mul_f32 v51,v51,v224\nM v[184:187],v[72:75],v[212:215],v[184:187]\nv_cvt_pk_bf16_f32 v48,v48,v49\n" \
"v_mul_f32 v56,v56,v224\nv_mul_f32 v57,v57,v224\nM v[188:191],v[76:79],v[212:215],v[188:191]\nv_mul_f32 v58,v58,v224\nv_mul_f32 v59,v59,v224\nv_cvt_pk_bf16_f32 v49,v50,v51\nM v[128:131],v[88:91],v[200:203],v[128:131]\nbuffer_load_dwordx4 v245,s[40:43],s69 offen offset:2048 lds\n" \
"ds_read_b64_tr_b16 v[208:209],v226 offset:9728\nM v[132:135],v[92:95],v[200:203],v[132:135]\nds_read_b64_tr_b16 v[210:211],v226 offset:13824\nv_cvt_pk_bf16_f32 v50,v56,v57\ns_waitcnt lgkmcnt(3)\nM v[136:139],v[88:91],v[204:207],v[136:139]\nv_cvt_pk_bf16_f32 v51,v58,v59\nbuffer_store_dwordx4 v[48:51],v240,s[52:55],s38 offen offset:2048 sc1\n" \
"M v[140:143],v[92:95],v[204:207],v[140:143]\nds_read_b64_tr_b16 v[212:213],v227 offset:9728\nds_read_b64_tr_b16 v[214:215],v227 offset:13824\nds_read_b64_tr_b16 v[200:201],v226 offset:16384\nM v[144:147],v[88:91],v[216:219],v[144:147]\nbuffer_load_dwordx4 v246,s[40:43],s69 offen offset:3072 lds\nds_read_b64_tr_b16 v[202:203],v226 offset:20480\nM v[148:151],v[92:95],v[216:219],v[148:151]\n" \
"s_lshl4_add_u32 s69,s14,s68\nds_read_b64_tr_b16 v[204:205],v227 offset:16384\nds_read_b64_tr_b16 v[206:207],v227 offset:20480\nM v[152:155],v[88:91],v[220:223],v[152:155]\ns_add_u32 s69,s69,0x10000\ns_add_u32 m0,s64,98304\nds_read_b64_tr_b16 v[216:217],v226 offset:16896\nM v[156:159],v[92:95],v[220:223],v[156:159]\n" \
"ds_read_b64_tr_b16 v[218:219],v226 offset:20992\nv_mul_f32 v52,v52,v228\nv_mul_f32 v53,v53,v228\nM v[160:163],v[88:91],v[192:195],v[160:163]\nbuffer_load_dwordx4 v243,s[40:43],s69 offen offset:0 lds\nv_mul_f32 v54,v54,v228\nM v[164:167],v[92:95],v[192:195],v[164:167]\nv_mul_f32 v55,v55,v228\n" \
"v_cvt_pk_bf16_f32 v52,v52,v53\ns_waitcnt lgkmcnt(5)\nM v[168:171],v[88:91],v[196:199],v[168:171]\nv_mul_f32 v60,v60,v228\nv_mul_f32 v61,v61,v228\nv_mul_f32 v62,v62,v228\nM v[172:175],v[92:95],v[196:199],v[172:175]\nv_mul_f32 v63,v63,v228\n" \
"v_cvt_pk_bf16_f32 v53,v54,v55\nds_read_b64_tr_b16 v[220:221],v227 offset:16896\nds_read_b64_tr_b16 v[222:223],v227 offset:20992\nM v[176:179],v[88:91],v[208:211],v[176:179]\nv_cvt_pk_bf16_f32 v54,v60,v61\nv_cvt_pk_bf16_f32 v55,v62,v63\nbuffer_store_dwordx4 v[52:55],v240,s[52:55],s38 offen offset:3072 sc1\nbuffer_load_dwordx4 v244,s[40:43],s69 offen offset:1024 lds\n" \
"M v[180:183],v[92:95],v[208:211],v[180:183]\nds_read_b64_tr_b16 v[192:193],v226 offset:17408\nds_read_b64_tr_b16 v[194:195],v226 offset:21504\nds_read_b64_tr_b16 v[196:197],v227 offset:17408\nM v[184:187],v[88:91],v[212:215],v[184:187]\nds_read_b64_tr_b16 v[198:199],v227 offset:21504\nds_read_b64_tr_b16 v[208:209],v226 offset:17920\ns_add_u32 s65,s14,s67\n" \
"M v[188:191],v[92:95],v[212:215],v[188:191]\nds_read_b64_tr_b16 v[210:211],v226 offset:22016\nds_read_b64_tr_b16 v[212:213],v227 offset:17920\ns_waitcnt lgkmcnt(9)\nM v[128:131],v[104:107],v[200:203],v[128:131]\nbuffer_load_dwordx4 v245,s[40:43],s69 offen offset:2048 lds\nds_read_b64_tr_b16 v[214:215],v227 offset:22016\nM v[132:135],v[108:111],v[200:203],v[132:135]\n" \
"ds_read_b64_tr_b16 v[200:201],v226 offset:24576\nds_read_b64_tr_b16 v[202:203],v226 offset:28672\ns_add_u32 s65,s65,1048576\nM v[136:139],v[104:107],v[204:207],v[136:139]\nv_mov_b32 v250,81920\nv_and_b32 v228,31,v249\nv_and_b32 v229,32,v249\nM v[140:143],v[108:111],v[204:207],v[140:143]\n" \
"ds_read_b64_tr_b16 v[204:205],v227 offset:24576\nds_read_b64_tr_b16 v[206:207],v227 offset:28672\nv_lshl_add_u32 v228,v228,2,s70\nM v[144:147],v[104:107],v[216:219],v[144:147]\nbuffer_load_dwordx4 v246,s[40:43],s69 offen offset:3072 lds\nM v[148:151],v[108:111],v[216:219],v[148:151]\nds_read_b64_tr_b16 v[216:217],v226 offset:25088\ns_waitcnt lgkmcnt(6)\n" \
"M v[152:155],v[104:107],v[220:223],v[152:155]\nds_read_b64_tr_b16 v[218:219],v226 offset:29184\nv_mul_u32_u24 v229,12,v229\nM v[156:159],v[108:111],v[220:223],v[156:159]\nds_read_b64_tr_b16 v[220:221],v227 offset:25088\nds_read_b64_tr_b16 v[222:223],v227 offset:29184\nM v[160:163],v[104:107],v[192:195],v[160:163]\nv_xor_b32 v228,v228,v229\n" \
"v_add_u32 v236,s95,v235\nM v[164:167],v[108:111],v[192:195],v[164:167]\nds_read_b64_tr_b16 v[192:193],v226 offset:25600\nds_read_b64_tr_b16 v[194:195],v226 offset:29696\nM v[168:171],v[104:107],v[196:199],v[168:171]\nbuffer_load_dwordx4 a[32:35],v240,s[52:55],s65 offen offset:0 sc1\nM v[172:175],v[108:111],v[196:199],v[172:175]\nds_read_b64_tr_b16 v[196:197],v227 offset:25600\n" \
"ds_read_b64_tr_b16 v[198:199],v227 offset:29696\nM v[176:179],v[104:107],v[208:211],v[176:179]\ns_sub_u32 s74,s74,s72\ns_add_u32 s74,s74,1920\nM v[180:183],v[108:111],v[208:211],v[180:183]\nds_read_b64_tr_b16 v[208:209],v226 offset:26112\ns_waitcnt lgkmcnt(7)\nM v[184:187],v[104:107],v[212:215],v[184:187]\n" \
"s_waitcnt vmcnt(22)\ns_barrier\nM v[188:191],v[108:111],v[212:215],v[188:191]\nds_read_b64_tr_b16 v[210:211],v226 offset:30208\nds_read_b64_tr_b16 v[212:213],v227 offset:26112\nM v[128:131],v[120:123],v[200:203],v[128:131]\nbuffer_load_dwordx4 a[36:39],v240,s[52:55],s65 offen offset:1024 sc1\nM v[132:135],v[124:127],v[200:203],v[132:135]\n" \
"ds_read_b64_tr_b16 v[214:215],v227 offset:30208\nds_read_b64_tr_b16 v[200:201],v226 offset:32768\nM v[136:139],v[120:123],v[204:207],v[136:139]\nds_read_b64_tr_b16 v[202:203],v226 offset:36864\nds_write_b64 v250,a[244:245]\nM v[140:143],v[124:127],v[204:207],v[140:143]\nds_read_b64_tr_b16 v[204:205],v227 offset:32768\nds_read_b64_tr_b16 v[206:207],v227 offset:36864\n" \
"M v[144:147],v[120:123],v[216:219],v[144:147]\ns_lshl_b32 s74,s74,10\nv_add_u32 v237,s74,v235\nM v[148:151],v[124:127],v[216:219],v[148:151]\nds_read_b64_tr_b16 v[216:217],v226 offset:33280\ns_waitcnt lgkmcnt(9)\nM v[152:155],v[120:123],v[220:223],v[152:155]\nbuffer_load_dwordx4 a[40:43],v240,s[52:55],s65 offen offset:2048 sc1\n" \
"M v[156:159],v[124:127],v[220:223],v[156:159]\nds_read_b64_tr_b16 v[218:219],v226 offset:37376\nds_read_b64_tr_b16 v[220:221],v227 offset:33280\nM v[160:163],v[120:123],v[192:195],v[160:163]\nds_read_b64_tr_b16 v[222:223],v227 offset:37376\ns_lshl_b32 s72,s7,9\nM v[164:167],v[124:127],v[192:195],v[164:167]\nds_read_b64_tr_b16 v[192:193],v226 offset:33792\n" \
"ds_read_b64_tr_b16 v[194:195],v226 offset:37888\nM v[168:171],v[120:123],v[196:199],v[168:171]\nv_lshl_add_u32 v239,v249,3,s72\ns_mov_b32 s97,4096\nM v[172:175],v[124:127],v[196:199],v[172:175]\nds_read_b64_tr_b16 v[196:197],v227 offset:33792\ns_waitcnt lgkmcnt(7)\nM v[176:179],v[120:123],v[208:211],v[176:179]\n" \
"buffer_load_dwordx4 a[44:47],v240,s[52:55],s65 offen offset:3072 sc1\nM v[180:183],v[124:127],v[208:211],v[180:183]\nds_read_b64_tr_b16 v[198:199],v227 offset:37888\nds_read_b64_tr_b16 v[208:209],v226 offset:34304\nM v[184:187],v[120:123],v[212:215],v[184:187]\nds_read_b64_tr_b16 v[210:211],v226 offset:38400\ns_add_u32 s65,s65,4096\nM v[188:191],v[124:127],v[212:215],v[188:191]\n" \
"ds_read_b64_tr_b16 v[212:213],v227 offset:34304\nds_read_b64_tr_b16 v[214:215],v227 offset:38400\nM v[128:131],a[0:3],v[200:203],v[128:131]\ns_add_u32 s38,s33,16384\ns_waitcnt vmcnt(21)\nM v[132:135],a[4:7],v[200:203],v[132:135]\nds_read_b64_tr_b16 v[200:201],v226 offset:40960\nds_read_b64_tr_b16 v[202:203],v226 offset:45056\n" \
"M v[136:139],a[0:3],v[204:207],v[136:139]\nbuffer_load_dwordx4 a[48:51],v240,s[52:55],s65 offen offset:0 sc1\nM v[140:143],a[4:7],v[204:207],v[140:143]\nds_read_b64_tr_b16 v[204:205],v227 offset:40960\ns_waitcnt lgkmcnt(8)\nM v[144:147],a[0:3],v[216:219],v[144:147]\nds_read_b64_tr_b16 v[206:207],v227 offset:45056\nv_lshlrev_b32 v238,1,v240\n" \
"M v[148:151],a[4:7],v[216:219],v[148:151]\nds_read_b64_tr_b16 v[216:217],v226 offset:41472\nds_read_b64_tr_b16 v[218:219],v226 offset:45568\nM v[152:155],a[0:3],v[220:223],v[152:155]\nv_bfe_u32 v234,v249,2,2\nv_accvgpr_write_b32 a194,0\nM v[156:159],a[4:7],v[220:223],v[156:159]\nds_read_b64_tr_b16 v[220:221],v227 offset:41472\n" \
"ds_read_b64_tr_b16 v[222:223],v227 offset:45568\nM v[160:163],a[0:3],v[192:195],v[160:163]\nbuffer_load_dwordx4 a[52:55],v240,s[52:55],s65 offen offset:1024 sc1\nM v[164:167],a[4:7],v[192:195],v[164:167]\nds_read_b64_tr_b16 v[192:193],v226 offset:41984\ns_waitcnt lgkmcnt(7)\nM v[168:171],a[0:3],v[196:199],v[168:171]\nds_read_b64_tr_b16 v[194:195],v226 offset:46080\n" \
"v_accvgpr_write_b32 a195,0\nM v[172:175],a[4:7],v[196:199],v[172:175]\nds_read_b64_tr_b16 v[196:197],v227 offset:41984\nds_read_b64_tr_b16 v[198:199],v227 offset:46080\nM v[176:179],a[0:3],v[208:211],v[176:179]\nv_accvgpr_write_b32 a196,0\nv_accvgpr_write_b32 a197,0\nM v[180:183],a[4:7],v[208:211],v[180:183]\n" \
"ds_read_b64_tr_b16 v[208:209],v226 offset:42496\nds_read_b64_tr_b16 v[210:211],v226 offset:46592\nM v[184:187],a[0:3],v[212:215],v[184:187]\nbuffer_load_dwordx4 a[56:59],v240,s[52:55],s65 offen offset:2048 sc1\nM v[188:191],a[4:7],v[212:215],v[188:191]\nds_read_b64_tr_b16 v[212:213],v227 offset:42496\nds_read_b64_tr_b16 v[214:215],v227 offset:46592\nM v[128:131],a[8:11],v[200:203],v[128:131]\n" \
"s_load_dwordx2 s[44:45],s[4:5],0x68\ns_mov_b32 s46,2097152\nM v[132:135],a[12:15],v[200:203],v[132:135]\nds_read_b64_tr_b16 v[200:201],v226 offset:49152\ns_waitcnt lgkmcnt(7)\nM v[136:139],a[8:11],v[204:207],v[136:139]\nds_read_b64_tr_b16 v[202:203],v226 offset:53248\ns_add_u32 s95,s95,54525952\n" \
"M v[140:143],a[12:15],v[204:207],v[140:143]\nds_read_b64_tr_b16 v[204:205],v227 offset:49152\nds_read_b64_tr_b16 v[206:207],v227 offset:53248\nM v[144:147],a[8:11],v[216:219],v[144:147]\nbuffer_load_dwordx4 a[60:63],v240,s[52:55],s65 offen offset:3072 sc1\nM v[148:151],a[12:15],v[216:219],v[148:151]\nds_read_b64_tr_b16 v[216:217],v226 offset:49664\nds_read_b64_tr_b16 v[218:219],v226 offset:53760\n" \
"M v[152:155],a[8:11],v[220:223],v[152:155]\ns_add_u32 s65,s10,s67\ns_add_u32 s74,s74,54525952\nM v[156:159],a[12:15],v[220:223],v[156:159]\nds_read_b64_tr_b16 v[220:221],v227 offset:49664\nds_read_b64_tr_b16 v[222:223],v227 offset:53760\nM v[160:163],a[8:11],v[192:195],v[160:163]\nM v[164:167],a[12:15],v[192:195],v[164:167]\n" \
"ds_read_b64_tr_b16 v[192:193],v226 offset:50176\ns_waitcnt lgkmcnt(8)\nM v[168:171],a[8:11],v[196:199],v[168:171]\nds_read_b64_tr_b16 v[194:195],v226 offset:54272\nM v[172:175],a[12:15],v[196:199],v[172:175]\nds_read_b64_tr_b16 v[196:197],v227 offset:50176\nds_read_b64_tr_b16 v[198:199],v227 offset:54272\nM v[176:179],a[8:11],v[208:211],v[176:179]\n" \
"s_waitcnt vmcnt(11)\nbuffer_store_dword v252,off,s[52:55],s76 sc1\nM v[180:183],a[12:15],v[208:211],v[180:183]\nds_read_b64_tr_b16 v[208:209],v226 offset:50688\nds_read_b64_tr_b16 v[210:211],v226 offset:54784\nM v[184:187],a[8:11],v[212:215],v[184:187]\nM v[188:191],a[12:15],v[212:215],v[188:191]\nds_read_b64_tr_b16 v[212:213],v227 offset:50688\n" \
"s_waitcnt lgkmcnt(8)\nM v[128:131],a[16:19],v[200:203],v[128:131]\nds_read_b64_tr_b16 v[214:215],v227 offset:54784\nM v[132:135],a[20:23],v[200:203],v[132:135]\nds_read_b64_tr_b16 v[200:201],v226 offset:57344\nds_read_b64_tr_b16 v[202:203],v226 offset:61440\nM v[136:139],a[16:19],v[204:207],v[136:139]\nM v[140:143],a[20:23],v[204:207],v[140:143]\n" \
"ds_read_b64_tr_b16 v[204:205],v227 offset:57344\nds_read_b64_tr_b16 v[206:207],v227 offset:61440\nM v[144:147],a[16:19],v[216:219],v[144:147]\nM v[148:151],a[20:23],v[216:219],v[148:151]\nds_read_b64_tr_b16 v[216:217],v226 offset:57856\ns_waitcnt lgkmcnt(9)\nM v[152:155],a[16:19],v[220:223],v[152:155]\nbuffer_load_dword v248,v228,s[52:55],s80 offen sc1\n" \
"M v[156:159],a[20:23],v[220:223],v[156:159]\nds_read_b64_tr_b16 v[218:219],v226 offset:61952\nds_read_b64_tr_b16 v[220:221],v227 offset:57856\nM v[160:163],a[16:19],v[192:195],v[160:163]\nds_read_b64_tr_b16 v[222:223],v227 offset:61952\nM v[164:167],a[20:23],v[192:195],v[164:167]\nds_read_b64_tr_b16 v[192:193],v226 offset:58368\nds_read_b64_tr_b16 v[194:195],v226 offset:62464\n" \
"M v[168:171],a[16:19],v[196:199],v[168:171]\nglobal_load_ushort v64,v236,s[8:9] offset:0\nM v[172:175],a[20:23],v[196:199],v[172:175]\nds_read_b64_tr_b16 v[196:197],v227 offset:58368\ns_waitcnt lgkmcnt(7)\nM v[176:179],a[16:19],v[208:211],v[176:179]\nglobal_load_ushort v80,v237,s[8:9] offset:0\nM v[180:183],a[20:23],v[208:211],v[180:183]\n" \
"ds_read_b64_tr_b16 v[198:199],v227 offset:62464\nds_read_b64_tr_b16 v[208:209],v226 offset:58880\nM v[184:187],a[16:19],v[212:215],v[184:187]\ns_waitcnt vmcnt(11)\ns_barrier\nM v[188:191],a[20:23],v[212:215],v[188:191]\nglobal_load_ushort v96,v235,s[20:21] offset:0\nM v[128:131],a[24:27],v[200:203],v[128:131]\n" \
"ds_read_b64_tr_b16 v[210:211],v226 offset:62976\nds_read_b64_tr_b16 v[212:213],v227 offset:58880\nM v[132:135],a[28:31],v[200:203],v[132:135]\nglobal_load_ushort v65,v236,s[8:9] offset:32\nM v[136:139],a[24:27],v[204:207],v[136:139]\nds_read_b64_tr_b16 v[214:215],v227 offset:62976\nv_add_u32 v226,65536,v242\nM v[140:143],a[28:31],v[204:207],v[140:143]\n" \
"global_load_ushort v81,v237,s[8:9] offset:32\ns_waitcnt lgkmcnt(5)\nM v[144:147],a[24:27],v[216:219],v[144:147]\nds_read_b64_tr_b16 v[200:201],v226 offset:0\nds_read_b64_tr_b16 v[202:203],v226 offset:4096\nM v[148:151],a[28:31],v[216:219],v[148:151]\nglobal_load_ushort v97,v235,s[20:21] offset:32\nM v[152:155],a[24:27],v[220:223],v[152:155]\n" \
"v_xor_b32 v227,32,v226\nds_read_b64_tr_b16 v[204:205],v227 offset:0\nM v[156:159],a[28:31],v[220:223],v[156:159]\nds_read_b64_tr_b16 v[206:207],v227 offset:4096\nglobal_load_ushort v66,v236,s[8:9] offset:64\nM v[160:163],a[24:27],v[192:195],v[160:163]\nds_read_b64_tr_b16 v[216:217],v226 offset:512\nds_read_b64_tr_b16 v[218:219],v226 offset:4608\n" \
"M v[164:167],a[28:31],v[192:195],v[164:167]\nglobal_load_ushort v82,v237,s[8:9] offset:64\ns_waitcnt lgkmcnt(6)\nM v[168:171],a[24:27],v[196:199],v[168:171]\nds_read_b64_tr_b16 v[220:221],v227 offset:512\nds_read_b64_tr_b16 v[222:223],v227 offset:4608\nM v[172:175],a[28:31],v[196:199],v[172:175]\nglobal_load_ushort v98,v235,s[20:21] offset:64\n" \
"M v[176:179],a[24:27],v[208:211],v[176:179]\nds_read_b64_tr_b16 v[192:193],v226 offset:1024\nds_read_b64_tr_b16 v[194:195],v226 offset:5120\nM v[180:183],a[28:31],v[208:211],v[180:183]\nglobal_load_ushort v67,v236,s[8:9] offset:96\nM v[184:187],a[24:27],v[212:215],v[184:187]\nds_read_b64_tr_b16 v[196:197],v227 offset:1024\nds_read_b64_tr_b16 v[198:199],v227 offset:5120\n" \
"M v[188:191],a[28:31],v[212:215],v[188:191]\nglobal_load_ushort v83,v237,s[8:9] offset:96\ns_waitcnt lgkmcnt(6)\nM v[128:131],a[32:35],v[200:203],v[128:131]\nds_read_b64_tr_b16 v[208:209],v226 offset:1536\ns_waitcnt vmcnt(16)\nM v[132:135],a[36:39],v[200:203],v[132:135]\nds_read_b64_tr_b16 v[210:211],v226 offset:5632\n" \
"global_load_ushort v99,v235,s[20:21] offset:96\nM v[136:139],a[32:35],v[204:207],v[136:139]\nds_read_b64_tr_b16 v[212:213],v227 offset:1536\nds_read_b64_tr_b16 v[214:215],v227 offset:5632\nM v[140:143],a[36:39],v[204:207],v[140:143]\nglobal_load_ushort v68,v236,s[8:9] offset:128\nM v[144:147],a[32:35],v[216:219],v[144:147]\nds_read_b64_tr_b16 v[200:201],v226 offset:8192\n" \
"ds_read_b64_tr_b16 v[202:203],v226 offset:12288\nM v[148:151],a[36:39],v[216:219],v[148:151]\nglobal_load_ushort v84,v237,s[8:9] offset:128\ns_waitcnt lgkmcnt(6)\nM v[152:155],a[32:35],v[220:223],v[152:155]\nds_read_b64_tr_b16 v[204:205],v227 offset:8192\nds_read_b64_tr_b16 v[206:207],v227 offset:12288\nM v[156:159],a[36:39],v[220:223],v[156:159]\n" \
"global_load_ushort v100,v235,s[20:21] offset:128\nM v[160:163],a[32:35],v[192:195],v[160:163]\nds_read_b64_tr_b16 v[216:217],v226 offset:8704\nds_read_b64_tr_b16 v[218:219],v226 offset:12800\nM v[164:167],a[36:39],v[192:195],v[164:167]\nglobal_load_ushort v69,v236,s[8:9] offset:160\nM v[168:171],a[32:35],v[196:199],v[168:171]\nds_read_b64_tr_b16 v[220:221],v227 offset:8704\n" \
"ds_read_b64_tr_b16 v[222:223],v227 offset:12800\nM v[172:175],a[36:39],v[196:199],v[172:175]\nglobal_load_ushort v85,v237,s[8:9] offset:160\ns_waitcnt lgkmcnt(6)\nM v[176:179],a[32:35],v[208:211],v[176:179]\nds_read_b64_tr_b16 v[192:193],v226 offset:9216\nds_read_b64_tr_b16 v[194:195],v226 offset:13312\nM v[180:183],a[36:39],v[208:211],v[180:183]\n" \
"global_load_ushort v101,v235,s[20:21] offset:160\nM v[184:187],a[32:35],v[212:215],v[184:187]\nds_read_b64_tr_b16 v[196:197],v227 offset:9216\nds_read_b64_tr_b16 v[198:199],v227 offset:13312\nM v[188:191],a[36:39],v[212:215],v[188:191]\nglobal_load_ushort v70,v236,s[8:9] offset:192\nM v[128:131],a[40:43],v[200:203],v[128:131]\nds_read_b64_tr_b16 v[208:209],v226 offset:9728\n" \
"ds_read_b64_tr_b16 v[210:211],v226 offset:13824\nM v[132:135],a[44:47],v[200:203],v[132:135]\nglobal_load_ushort v86,v237,s[8:9] offset:192\ns_waitcnt lgkmcnt(6)\nM v[136:139],a[40:43],v[204:207],v[136:139]\ns_waitcnt vmcnt(20)\nv_cmp_eq_u32 vcc,0,v248\nM v[140:143],a[44:47],v[204:207],v[140:143]\n" \
"ds_read_b64_tr_b16 v[212:213],v227 offset:9728\nds_read_b64_tr_b16 v[214:215],v227 offset:13824\nglobal_load_ushort v102,v235,s[20:21] offset:192\nM v[144:147],a[40:43],v[216:219],v[144:147]\ns_cbranch_vccnz .Lps7\n.Lpb7:\nbuffer_load_dword v248,v251,s[52:55],s79 offen sc1\nM v[148:151],a[44:47],v[216:219],v[148:151]\n" \
"ds_read_b64_tr_b16 v[200:201],v226 offset:32768\nds_read_b64_tr_b16 v[202:203],v226 offset:36864\nglobal_load_ushort v71,v236,s[8:9] offset:224\nM v[152:155],a[40:43],v[220:223],v[152:155]\nds_read_b64_tr_b16 v[204:205],v227 offset:32768\nds_read_b64_tr_b16 v[206:207],v227 offset:36864\nM v[156:159],a[44:47],v[220:223],v[156:159]\nglobal_load_ushort v87,v237,s[8:9] offset:224\n" \
"s_waitcnt lgkmcnt(6)\nM v[160:163],a[40:43],v[192:195],v[160:163]\nds_read_b64_tr_b16 v[216:217],v226 offset:33280\nds_read_b64_tr_b16 v[218:219],v226 offset:37376\nM v[164:167],a[44:47],v[192:195],v[164:167]\nglobal_load_ushort v103,v235,s[20:21] offset:224\nM v[168:171],a[40:43],v[196:199],v[168:171]\nds_read_b64_tr_b16 v[220:221],v227 offset:33280\n" \
"ds_read_b64_tr_b16 v[222:223],v227 offset:37376\nM v[172:175],a[44:47],v[196:199],v[172:175]\nbuffer_load_dwordx2 v[112:113],v239,s[52:55],0 offen offset:0 sc1\nM v[176:179],a[40:43],v[208:211],v[176:179]\nds_read_b64_tr_b16 v[192:193],v226 offset:33792\nds_read_b64_tr_b16 v[194:195],v226 offset:37888\nM v[180:183],a[44:47],v[208:211],v[180:183]\nbuffer_load_dwordx2 v[114:115],v239,s[52:55],0 offen offset:2048 sc1\n" \
"s_waitcnt lgkmcnt(6)\nM v[184:187],a[40:43],v[212:215],v[184:187]\nds_read_b64_tr_b16 v[196:197],v227 offset:33792\nds_read_b64_tr_b16 v[198:199],v227 offset:37888\nM v[188:191],a[44:47],v[212:215],v[188:191]\nbuffer_load_dwordx2 v[116:117],v239,s[52:55],s97 offen offset:0 sc1\nM v[128:131],a[48:51],v[200:203],v[128:131]\nds_read_b64_tr_b16 v[208:209],v226 offset:34304\n" \
"ds_read_b64_tr_b16 v[210:211],v226 offset:38400\nM v[132:135],a[52:55],v[200:203],v[132:135]\nbuffer_load_dwordx2 v[118:119],v239,s[52:55],s97 offen offset:2048 sc1\nM v[136:139],a[48:51],v[204:207],v[136:139]\nds_read_b64_tr_b16 v[212:213],v227 offset:34304\nds_read_b64_tr_b16 v[214:215],v227 offset:38400\nM v[140:143],a[52:55],v[204:207],v[140:143]\nds_read_b64_tr_b16 v[200:201],v226 offset:40960\n" \
"s_waitcnt lgkmcnt(7)\nM v[144:147],a[48:51],v[216:219],v[144:147]\nds_read_b64_tr_b16 v[202:203],v226 offset:45056\nds_read_b64_tr_b16 v[204:205],v227 offset:40960\nM v[148:151],a[52:55],v[216:219],v[148:151]\nds_read_b64_tr_b16 v[206:207],v227 offset:45056\nds_read_b64_tr_b16 v[216:217],v226 offset:41472\nM v[152:155],a[48:51],v[220:223],v[152:155]\n" \
"ds_read_b64_tr_b16 v[218:219],v226 offset:45568\nv_lshrrev_b32 v235,4,v249\nM v[156:159],a[52:55],v[220:223],v[156:159]\nds_read_b64_tr_b16 v[220:221],v227 offset:41472\nds_read_b64_tr_b16 v[222:223],v227 offset:45568\nM v[160:163],a[48:51],v[192:195],v[160:163]\nM v[164:167],a[52:55],v[192:195],v[164:167]\nds_read_b64_tr_b16 v[192:193],v226 offset:41984\n" \
"s_waitcnt lgkmcnt(11)\nM v[168:171],a[48:51],v[196:199],v[168:171]\nds_read_b64_tr_b16 v[194:195],v226 offset:46080\nM v[172:175],a[52:55],v[196:199],v[172:175]\nds_read_b64_tr_b16 v[196:197],v227 offset:41984\nds_read_b64_tr_b16 v[198:199],v227 offset:46080\nM v[176:179],a[48:51],v[208:211],v[176:179]\nM v[180:183],a[52:55],v[208:211],v[180:183]\n" \
"ds_read_b64_tr_b16 v[208:209],v226 offset:42496\ns_waitcnt lgkmcnt(10)\nM v[184:187],a[48:51],v[212:215],v[184:187]\nds_read_b64_tr_b16 v[210:211],v226 offset:46592\nv_add_u32 v226,131072,v242\nM v[188:191],a[52:55],v[212:215],v[188:191]\nds_read_b64_tr_b16 v[212:213],v227 offset:42496\nds_read_b64_tr_b16 v[214:215],v227 offset:46592\n" \
"M v[128:131],a[56:59],v[200:203],v[128:131]\nv_xor_b32 v227,32,v226\nM v[132:135],a[60:63],v[200:203],v[132:135]\nds_read_b64_tr_b16 v[200:201],v226 offset:0\ns_waitcnt lgkmcnt(8)\nM v[136:139],a[56:59],v[204:207],v[136:139]\nds_read_b64_tr_b16 v[202:203],v226 offset:4096\nM v[140:143],a[60:63],v[204:207],v[140:143]\n" \
"ds_read_b64_tr_b16 v[204:205],v227 offset:0\nds_read_b64_tr_b16 v[206:207],v227 offset:4096\nM v[144:147],a[56:59],v[216:219],v[144:147]\nM v[148:151],a[60:63],v[216:219],v[148:151]\nds_read_b64_tr_b16 v[216:217],v226 offset:512\nds_read_b64_tr_b16 v[218:219],v226 offset:4608\nM v[152:155],a[56:59],v[220:223],v[152:155]\ns_waitcnt vmcnt(7)\n" \
"v_cmp_eq_u32 vcc,0,v248\nM v[156:159],a[60:63],v[220:223],v[156:159]\ns_cbranch_vccnz .Lps8\n.Lpb8:\ns_waitcnt lgkmcnt(7)\nM v[160:163],a[56:59],v[192:195],v[160:163]\nbuffer_load_dwordx4 a[64:67],v240,s[52:55],s65 offen offset:0 sc1\nM v[164:167],a[60:63],v[192:195],v[164:167]\n" \
"ds_read_b64_tr_b16 v[220:221],v227 offset:512\nds_read_b64_tr_b16 v[222:223],v227 offset:4608\nM v[168:171],a[56:59],v[196:199],v[168:171]\nds_read_b64_tr_b16 v[192:193],v226 offset:1024\nds_read_b64_tr_b16 v[194:195],v226 offset:5120\nM v[172:175],a[60:63],v[196:199],v[172:175]\nds_read_b64_tr_b16 v[196:197],v227 offset:1024\nds_read_b64_tr_b16 v[198:199],v227 offset:5120\n" \
"M v[176:179],a[56:59],v[208:211],v[176:179]\nbuffer_load_dwordx4 a[68:71],v240,s[52:55],s65 offen offset:1024 sc1\nM v[180:183],a[60:63],v[208:211],v[180:183]\nds_read_b64_tr_b16 v[208:209],v226 offset:1536\ns_waitcnt lgkmcnt(7)\nM v[184:187],a[56:59],v[212:215],v[184:187]\nds_read_b64_tr_b16 v[210:211],v226 offset:5632\nv_cvt_pk_bf16_f32 v128,v128,v136\n" \
"M v[188:191],a[60:63],v[212:215],v[188:191]\nds_read_b64_tr_b16 v[212:213],v227 offset:1536\nds_read_b64_tr_b16 v[214:215],v227 offset:5632\nM v[8:11],v[0:3],v[200:203],0\nbuffer_load_dwordx4 a[72:75],v240,s[52:55],s65 offen offset:2048 sc1\nM v[12:15],v[4:7],v[200:203],0\nds_read_b64_tr_b16 v[200:201],v226 offset:8192\nds_read_b64_tr_b16 v[202:203],v226 offset:12288\n" \
"M v[24:27],v[0:3],v[204:207],0\nv_cvt_pk_bf16_f32 v129,v129,v137\nv_cvt_pk_bf16_f32 v130,v130,v138\nM v[28:31],v[4:7],v[204:207],0\nds_read_b64_tr_b16 v[204:205],v227 offset:8192\nds_read_b64_tr_b16 v[206:207],v227 offset:12288\nM v[40:43],v[0:3],v[216:219],0\nbuffer_load_dwordx4 a[76:79],v240,s[52:55],s65 offen offset:3072 sc1\n" \
"M v[44:47],v[4:7],v[216:219],0\ns_add_u32 s65,s65,4096\ns_waitcnt lgkmcnt(6)\nM v[56:59],v[0:3],v[220:223],0\nds_read_b64_tr_b16 v[216:217],v226 offset:8704\nds_read_b64_tr_b16 v[218:219],v226 offset:12800\nM v[60:63],v[4:7],v[220:223],0\nds_read_b64_tr_b16 v[220:221],v227 offset:8704\n" \
"ds_read_b64_tr_b16 v[222:223],v227 offset:12800\nM v[72:75],v[0:3],v[192:195],0\nbuffer_load_dwordx4 a[80:83],v240,s[52:55],s65 offen offset:0 sc1\nM v[76:79],v[4:7],v[192:195],0\nds_read_b64_tr_b16 v[192:193],v226 offset:9216\nds_read_b64_tr_b16 v[194:195],v226 offset:13312\nM v[88:91],v[0:3],v[196:199],0\nv_cvt_pk_bf16_f32 v131,v131,v139\n" \
"v_cvt_pk_bf16_f32 v144,v144,v152\nM v[92:95],v[4:7],v[196:199],0\nv_cvt_pk_bf16_f32 v160,v160,v168\nv_cvt_pk_bf16_f32 v176,v176,v184\nM v[104:107],v[0:3],v[208:211],0\nbuffer_load_dwordx4 a[84:87],v240,s[52:55],s65 offen offset:1024 sc1\nM v[108:111],v[4:7],v[208:211],0\nv_min_u32 v136,-2,v128\n" \
"s_waitcnt lgkmcnt(5)\nM v[120:123],v[0:3],v[212:215],0\nv_min_u32 v137,-2,v144\nv_min_u32 v138,-2,v160\nv_min_u32 v139,-2,v176\nbuffer_store_dwordx4 v[136:139],v253,s[52:55],s33 offen offset:0 sc1\nM v[124:127],v[4:7],v[212:215],0\nds_read_b64_tr_b16 v[196:197],v227 offset:9216\n" \
"ds_read_b64_tr_b16 v[198:199],v227 offset:13312\nM v[8:11],v[16:19],v[200:203],v[8:11]\nbuffer_load_dwordx4 a[88:91],v240,s[52:55],s65 offen offset:2048 sc1\nM v[12:15],v[20:23],v[200:203],v[12:15]\nds_read_b64_tr_b16 v[208:209],v226 offset:9728\nds_read_b64_tr_b16 v[210:211],v226 offset:13824\nM v[24:27],v[16:19],v[204:207],v[24:27]\nds_read_b64_tr_b16 v[212:213],v227 offset:9728\n" \
"ds_read_b64_tr_b16 v[214:215],v227 offset:13824\nM v[28:31],v[20:23],v[204:207],v[28:31]\nds_read_b64_tr_b16 v[200:201],v226 offset:16384\ns_waitcnt lgkmcnt(7)\nM v[40:43],v[16:19],v[216:219],v[40:43]\nbuffer_load_dwordx4 a[92:95],v240,s[52:55],s65 offen offset:3072 sc1\nM v[44:47],v[20:23],v[216:219],v[44:47]\nds_read_b64_tr_b16 v[202:203],v226 offset:20480\n" \
"ds_read_b64_tr_b16 v[204:205],v227 offset:16384\nM v[56:59],v[16:19],v[220:223],v[56:59]\nds_read_b64_tr_b16 v[206:207],v227 offset:20480\ns_add_u32 s65,s11,s67\nM v[60:63],v[20:23],v[220:223],v[60:63]\nds_read_b64_tr_b16 v[216:217],v226 offset:16896\nds_read_b64_tr_b16 v[218:219],v226 offset:20992\nM v[72:75],v[16:19],v[192:195],v[72:75]\n" \
"buffer_load_dwordx4 a[96:99],v240,s[52:55],s65 offen offset:0 sc1\nM v[76:79],v[20:23],v[192:195],v[76:79]\nds_read_b64_tr_b16 v[220:221],v227 offset:16896\ns_waitcnt lgkmcnt(8)\nM v[88:91],v[16:19],v[196:199],v[88:91]\nds_read_b64_tr_b16 v[222:223],v227 offset:20992\nds_read_b64_tr_b16 v[192:193],v226 offset:17408\nM v[92:95],v[20:23],v[196:199],v[92:95]\n" \
"ds_read_b64_tr_b16 v[194:195],v226 offset:21504\nv_cvt_pk_bf16_f32 v145,v145,v153\nM v[104:107],v[16:19],v[208:211],v[104:107]\nbuffer_load_dwordx4 a[100:103],v240,s[52:55],s65 offen offset:1024 sc1\nM v[108:111],v[20:23],v[208:211],v[108:111]\nv_cvt_pk_bf16_f32 v146,v146,v154\nv_cvt_pk_bf16_f32 v147,v147,v155\nv_cvt_pk_bf16_f32 v161,v161,v169\n" \
"v_cvt_pk_bf16_f32 v177,v177,v185\ns_waitcnt lgkmcnt(6)\nM v[120:123],v[16:19],v[212:215],v[120:123]\nv_min_u32 v152,-2,v129\nv_min_u32 v153,-2,v145\nv_min_u32 v154,-2,v161\nv_min_u32 v155,-2,v177\nbuffer_store_dwordx4 v[152:155],v253,s[52:55],s33 offen offset:1024 sc1\n" \
"M v[124:127],v[20:23],v[212:215],v[124:127]\nds_read_b64_tr_b16 v[196:197],v227 offset:17408\nds_read_b64_tr_b16 v[198:199],v227 offset:21504\nM v[8:11],v[32:35],v[200:203],v[8:11]\nbuffer_load_dwordx4 a[104:107],v240,s[52:55],s65 offen offset:2048 sc1\nM v[12:15],v[36:39],v[200:203],v[12:15]\nds_read_b64_tr_b16 v[208:209],v226 offset:17920\nds_read_b64_tr_b16 v[210:211],v226 offset:22016\n" \
"M v[24:27],v[32:35],v[204:207],v[24:27]\nds_read_b64_tr_b16 v[212:213],v227 offset:17920\nds_read_b64_tr_b16 v[214:215],v227 offset:22016\nM v[28:31],v[36:39],v[204:207],v[28:31]\nds_read_b64_tr_b16 v[200:201],v226 offset:24576\ns_waitcnt lgkmcnt(7)\nM v[40:43],v[32:35],v[216:219],v[40:43]\nbuffer_load_dwordx4 a[108:111],v240,s[52:55],s65 offen offset:3072 sc1\n" \
"M v[44:47],v[36:39],v[216:219],v[44:47]\nds_read_b64_tr_b16 v[202:203],v226 offset:28672\nds_read_b64_tr_b16 v[204:205],v227 offset:24576\nM v[56:59],v[32:35],v[220:223],v[56:59]\nds_read_b64_tr_b16 v[206:207],v227 offset:28672\ns_add_u32 s65,s65,4096\nM v[60:63],v[36:39],v[220:223],v[60:63]\nds_read_b64_tr_b16 v[216:217],v226 offset:25088\n" \
"ds_read_b64_tr_b16 v[218:219],v226 offset:29184\nM v[72:75],v[32:35],v[192:195],v[72:75]\nbuffer_load_dwordx4 a[112:115],v240,s[52:55],s65 offen offset:0 sc1\nM v[76:79],v[36:39],v[192:195],v[76:79]\nds_read_b64_tr_b16 v[220:221],v227 offset:25088\ns_waitcnt lgkmcnt(8)\nM v[88:91],v[32:35],v[196:199],v[88:91]\nds_read_b64_tr_b16 v[222:223],v227 offset:29184\n" \
"ds_read_b64_tr_b16 v[192:193],v226 offset:25600\nM v[92:95],v[36:39],v[196:199],v[92:95]\nds_read_b64_tr_b16 v[194:195],v226 offset:29696\nv_cvt_pk_bf16_f32 v162,v162,v170\nM v[104:107],v[32:35],v[208:211],v[104:107]\nbuffer_load_dwordx4 a[116:119],v240,s[52:55],s65 offen offset:1024 sc1\nM v[108:111],v[36:39],v[208:211],v[108:111]\nv_cvt_pk_bf16_f32 v163,v163,v171\n" \
"v_cvt_pk_bf16_f32 v178,v178,v186\ns_waitcnt lgkmcnt(6)\nM v[120:123],v[32:35],v[212:215],v[120:123]\nv_min_u32 v168,-2,v130\nv_min_u32 v169,-2,v146\nv_min_u32 v170,-2,v162\nv_min_u32 v171,-2,v178\nbuffer_store_dwordx4 v[168:171],v253,s[52:55],s33 offen offset:2048 sc1\n" \
"M v[124:127],v[36:39],v[212:215],v[124:127]\nds_read_b64_tr_b16 v[196:197],v227 offset:25600\nds_read_b64_tr_b16 v[198:199],v227 offset:29696\nM v[8:11],v[48:51],v[200:203],v[8:11]\nbuffer_load_dwordx4 a[120:123],v240,s[52:55],s65 offen offset:2048 sc1\nM v[12:15],v[52:55],v[200:203],v[12:15]\nds_read_b64_tr_b16 v[208:209],v226 offset:26112\nds_read_b64_tr_b16 v[210:211],v226 offset:30208\n" \
"M v[24:27],v[48:51],v[204:207],v[24:27]\nds_read_b64_tr_b16 v[212:213],v227 offset:26112\nds_read_b64_tr_b16 v[214:215],v227 offset:30208\nM v[28:31],v[52:55],v[204:207],v[28:31]\nv_add_u32 v226,0,v242\ns_waitcnt lgkmcnt(6)\nM v[40:43],v[48:51],v[216:219],v[40:43]\nbuffer_load_dwordx4 a[124:127],v240,s[52:55],s65 offen offset:3072 sc1\n" \
"M v[44:47],v[52:55],v[216:219],v[44:47]\nds_read_b64_tr_b16 v[200:201],v226 offset:0\nds_read_b64_tr_b16 v[202:203],v226 offset:4096\nM v[56:59],v[48:51],v[220:223],v[56:59]\nv_xor_b32 v227,32,v226\nds_read_b64_tr_b16 v[204:205],v227 offset:0\nM v[60:63],v[52:55],v[220:223],v[60:63]\nds_read_b64_tr_b16 v[206:207],v227 offset:4096\n" \
"s_add_u32 s65,s14,s67\nM v[72:75],v[48:51],v[192:195],v[72:75]\nbuffer_load_dwordx4 a[128:131],v240,s[52:55],s65 offen offset:0 sc1\nM v[76:79],v[52:55],v[192:195],v[76:79]\nds_read_b64_tr_b16 v[216:217],v226 offset:512\nds_read_b64_tr_b16 v[218:219],v226 offset:4608\ns_waitcnt lgkmcnt(6)\nM v[88:91],v[48:51],v[196:199],v[88:91]\n" \
"ds_read_b64_tr_b16 v[220:221],v227 offset:512\nds_read_b64_tr_b16 v[222:223],v227 offset:4608\nM v[92:95],v[52:55],v[196:199],v[92:95]\nv_cvt_pk_bf16_f32 v179,v179,v187\nds_read_b64_tr_b16 v[192:193],v226 offset:1024\nM v[104:107],v[48:51],v[208:211],v[104:107]\nbuffer_load_dwordx4 a[132:135],v240,s[52:55],s65 offen offset:1024 sc1\nM v[108:111],v[52:55],v[208:211],v[108:111]\n" \
"ds_read_b64_tr_b16 v[194:195],v226 offset:5120\nv_min_u32 v184,-2,v131\nM v[120:123],v[48:51],v[212:215],v[120:123]\nv_min_u32 v185,-2,v147\nv_min_u32 v186,-2,v163\nv_min_u32 v187,-2,v179\nbuffer_store_dwordx4 v[184:187],v253,s[52:55],s33 offen offset:3072 sc1\nM v[124:127],v[52:55],v[212:215],v[124:127]\n" \
"ds_read_b64_tr_b16 v[196:197],v227 offset:1024\nds_read_b64_tr_b16 v[198:199],v227 offset:5120\ns_waitcnt vmcnt(18) lgkmcnt(6)\nM v[8:11],a[64:67],v[200:203],v[8:11]\nbuffer_load_dwordx4 a[136:139],v240,s[52:55],s65 offen offset:2048 sc1\nM v[12:15],a[68:71],v[200:203],v[12:15]\nds_read_b64_tr_b16 v[208:209],v226 offset:1536\nds_read_b64_tr_b16 v[210:211],v226 offset:5632\n" \
"M v[24:27],a[64:67],v[204:207],v[24:27]\nds_read_b64_tr_b16 v[212:213],v227 offset:1536\nds_read_b64_tr_b16 v[214:215],v227 offset:5632\nM v[28:31],a[68:71],v[204:207],v[28:31]\nds_read_b64_tr_b16 v[200:201],v226 offset:8192\nds_read_b64_tr_b16 v[202:203],v226 offset:12288\nM v[40:43],a[64:67],v[216:219],v[40:43]\nbuffer_load_dwordx4 a[140:143],v240,s[52:55],s65 offen offset:3072 sc1\n" \
"M v[44:47],a[68:71],v[216:219],v[44:47]\nds_read_b64_tr_b16 v[204:205],v227 offset:8192\ns_waitcnt lgkmcnt(9)\nM v[56:59],a[64:67],v[220:223],v[56:59]\nds_read_b64_tr_b16 v[206:207],v227 offset:12288\ns_add_u32 s65,s65,4096\nM v[60:63],a[68:71],v[220:223],v[60:63]\nds_read_b64_tr_b16 v[216:217],v226 offset:8704\n" \
"ds_read_b64_tr_b16 v[218:219],v226 offset:12800\nM v[72:75],a[64:67],v[192:195],v[72:75]\nbuffer_load_dwordx4 a[144:147],v240,s[52:55],s65 offen offset:0 sc1\nM v[76:79],a[68:71],v[192:195],v[76:79]\nds_read_b64_tr_b16 v[220:221],v227 offset:8704\ns_waitcnt lgkmcnt(8)\nM v[88:91],a[64:67],v[196:199],v[88:91]\nds_read_b64_tr_b16 v[222:223],v227 offset:12800\n" \
"v_cvt_pk_bf16_f32 v132,v132,v140\nM v[92:95],a[68:71],v[196:199],v[92:95]\nv_cvt_pk_bf16_f32 v133,v133,v141\nv_cvt_pk_bf16_f32 v134,v134,v142\nM v[104:107],a[64:67],v[208:211],v[104:107]\nbuffer_load_dwordx4 a[148:151],v240,s[52:55],s65 offen offset:1024 sc1\nM v[108:111],a[68:71],v[208:211],v[108:111]\nv_cvt_pk_bf16_f32 v135,v135,v143\n" \
"v_cvt_pk_bf16_f32 v148,v148,v156\nv_cvt_pk_bf16_f32 v164,v164,v172\nv_cvt_pk_bf16_f32 v180,v180,v188\nds_read_b64_tr_b16 v[192:193],v226 offset:9216\nds_read_b64_tr_b16 v[194:195],v226 offset:13312\ns_waitcnt lgkmcnt(4)\nM v[120:123],a[64:67],v[212:215],v[120:123]\nv_min_u32 v140,-2,v132\n" \
"v_min_u32 v141,-2,v148\nv_min_u32 v142,-2,v164\nv_min_u32 v143,-2,v180\nbuffer_store_dwordx4 v[140:143],v253,s[52:55],s38 offen offset:0 sc1\nM v[124:127],a[68:71],v[212:215],v[124:127]\nds_read_b64_tr_b16 v[196:197],v227 offset:9216\nds_read_b64_tr_b16 v[198:199],v227 offset:13312\nM v[8:11],a[72:75],v[200:203],v[8:11]\n" \
"buffer_load_dwordx4 a[152:155],v240,s[52:55],s65 offen offset:2048 sc1\nM v[12:15],a[76:79],v[200:203],v[12:15]\nds_read_b64_tr_b16 v[208:209],v226 offset:9728\nds_read_b64_tr_b16 v[210:211],v226 offset:13824\nM v[24:27],a[72:75],v[204:207],v[24:27]\nds_read_b64_tr_b16 v[212:213],v227 offset:9728\nds_read_b64_tr_b16 v[214:215],v227 offset:13824\nM v[28:31],a[76:79],v[204:207],v[28:31]\n" \
"ds_read_b64_tr_b16 v[200:201],v226 offset:16384\nds_read_b64_tr_b16 v[202:203],v226 offset:20480\nM v[40:43],a[72:75],v[216:219],v[40:43]\nbuffer_load_dwordx4 a[156:159],v240,s[52:55],s65 offen offset:3072 sc1\nM v[44:47],a[76:79],v[216:219],v[44:47]\nds_read_b64_tr_b16 v[204:205],v227 offset:16384\ns_waitcnt lgkmcnt(9)\nM v[56:59],a[72:75],v[220:223],v[56:59]\n" \
"ds_read_b64_tr_b16 v[206:207],v227 offset:20480\nds_read_b64_tr_b16 v[216:217],v226 offset:16896\nM v[60:63],a[76:79],v[220:223],v[60:63]\nds_read_b64_tr_b16 v[218:219],v226 offset:20992\nds_read_b64_tr_b16 v[220:221],v227 offset:16896\nM v[72:75],a[72:75],v[192:195],v[72:75]\nds_read_b64_tr_b16 v[222:223],v227 offset:20992\nv_cvt_pk_bf16_f32 v149,v149,v157\n" \
"M v[76:79],a[76:79],v[192:195],v[76:79]\nv_cvt_pk_bf16_f32 v150,v150,v158\ns_waitcnt lgkmcnt(9)\nM v[88:91],a[72:75],v[196:199],v[88:91]\nv_cvt_pk_bf16_f32 v151,v151,v159\nv_cvt_pk_bf16_f32 v165,v165,v173\nM v[92:95],a[76:79],v[196:199],v[92:95]\nv_cvt_pk_bf16_f32 v181,v181,v189\n" \
"ds_read_b64_tr_b16 v[192:193],v226 offset:17408\nM v[104:107],a[72:75],v[208:211],v[104:107]\nds_read_b64_tr_b16 v[194:195],v226 offset:21504\nv_min_u32 v156,-2,v133\nM v[108:111],a[76:79],v[208:211],v[108:111]\nv_min_u32 v157,-2,v149\ns_waitcnt lgkmcnt(6)\nM v[120:123],a[72:75],v[212:215],v[120:123]\n" \
"v_min_u32 v158,-2,v165\nv_min_u32 v159,-2,v181\nbuffer_store_dwordx4 v[156:159],v253,s[52:55],s38 offen offset:1024 sc1\nM v[124:127],a[76:79],v[212:215],v[124:127]\nds_read_b64_tr_b16 v[196:197],v227 offset:17408\nds_read_b64_tr_b16 v[198:199],v227 offset:21504\ns_waitcnt vmcnt(24)\nM v[8:11],a[80:83],v[200:203],v[8:11]\n" \
"ds_read_b64_tr_b16 v[208:209],v226 offset:17920\nds_read_b64_tr_b16 v[210:211],v226 offset:22016\nM v[12:15],a[84:87],v[200:203],v[12:15]\nds_read_b64_tr_b16 v[212:213],v227 offset:17920\nds_read_b64_tr_b16 v[214:215],v227 offset:22016\nM v[24:27],a[80:83],v[204:207],v[24:27]\nds_read_b64_tr_b16 v[200:201],v226 offset:24576\nds_read_b64_tr_b16 v[202:203],v226 offset:28672\n" \
"M v[28:31],a[84:87],v[204:207],v[28:31]\nds_read_b64_tr_b16 v[204:205],v227 offset:24576\ns_waitcnt lgkmcnt(9)\nM v[40:43],a[80:83],v[216:219],v[40:43]\nds_read_b64_tr_b16 v[206:207],v227 offset:28672\nv_cvt_pk_bf16_f32 v166,v166,v174\nM v[44:47],a[84:87],v[216:219],v[44:47]\nds_read_b64_tr_b16 v[216:217],v226 offset:25088\n" \
"ds_read_b64_tr_b16 v[218:219],v226 offset:29184\nM v[56:59],a[80:83],v[220:223],v[56:59]\nv_cvt_pk_bf16_f32 v167,v167,v175\nv_cvt_pk_bf16_f32 v182,v182,v190\nM v[60:63],a[84:87],v[220:223],v[60:63]\nds_read_b64_tr_b16 v[220:221],v227 offset:25088\nds_read_b64_tr_b16 v[222:223],v227 offset:29184\nM v[72:75],a[80:83],v[192:195],v[72:75]\n" \
"v_min_u32 v172,-2,v134\nv_min_u32 v173,-2,v150\nM v[76:79],a[84:87],v[192:195],v[76:79]\nds_read_b64_tr_b16 v[192:193],v226 offset:25600\ns_waitcnt lgkmcnt(8)\nM v[88:91],a[80:83],v[196:199],v[88:91]\nds_read_b64_tr_b16 v[194:195],v226 offset:29696\nv_min_u32 v174,-2,v166\n" \
"M v[92:95],a[84:87],v[196:199],v[92:95]\nv_min_u32 v175,-2,v182\nds_read_b64_tr_b16 v[196:197],v227 offset:25600\nM v[104:107],a[80:83],v[208:211],v[104:107]\nds_read_b64_tr_b16 v[198:199],v227 offset:29696\nv_cvt_pk_bf16_f32 v183,v183,v191\nM v[108:111],a[84:87],v[208:211],v[108:111]\nds_read_b64_tr_b16 v[208:209],v226 offset:26112\n" \
"ds_read_b64_tr_b16 v[210:211],v226 offset:30208\nM v[120:123],a[80:83],v[212:215],v[120:123]\nbuffer_store_dwordx4 v[172:175],v253,s[52:55],s38 offen offset:2048 sc1\nM v[124:127],a[84:87],v[212:215],v[124:127]\nds_read_b64_tr_b16 v[212:213],v227 offset:26112\ns_waitcnt vmcnt(20) lgkmcnt(7)\nM v[8:11],a[88:91],v[200:203],v[8:11]\nds_read_b64_tr_b16 v[214:215],v227 offset:30208\n" \
"v_min_u32 v188,-2,v135\nM v[12:15],a[92:95],v[200:203],v[12:15]\nds_read_b64_tr_b16 v[200:201],v226 offset:32768\nds_read_b64_tr_b16 v[202:203],v226 offset:36864\nM v[24:27],a[88:91],v[204:207],v[24:27]\nv_min_u32 v189,-2,v151\nv_min_u32 v190,-2,v167\nM v[28:31],a[92:95],v[204:207],v[28:31]\n" \
"ds_read_b64_tr_b16 v[204:205],v227 offset:32768\nds_read_b64_tr_b16 v[206:207],v227 offset:36864\nM v[40:43],a[88:91],v[216:219],v[40:43]\nv_min_u32 v191,-2,v183\nv_cmp_eq_u32 vcc,v234,v235\nM v[44:47],a[92:95],v[216:219],v[44:47]\nds_read_b64_tr_b16 v[216:217],v226 offset:33280\nds_read_b64_tr_b16 v[218:219],v226 offset:37376\n" \
"M v[56:59],a[88:91],v[220:223],v[56:59]\nv_and_b32 v234,1,v249\nv_lshlrev_b32 v234,4,v234\nM v[60:63],a[92:95],v[220:223],v[60:63]\nds_read_b64_tr_b16 v[220:221],v227 offset:33280\ns_waitcnt lgkmcnt(7)\nM v[72:75],a[88:91],v[192:195],v[72:75]\nds_read_b64_tr_b16 v[222:223],v227 offset:37376\n" \
"v_mov_b32 v235,0x3f80\nM v[76:79],a[92:95],v[192:195],v[76:79]\nds_read_b64_tr_b16 v[192:193],v226 offset:33792\nds_read_b64_tr_b16 v[194:195],v226 offset:37888\nM v[88:91],a[88:91],v[196:199],v[88:91]\nv_lshlrev_b32 v234,v234,v235\nv_cndmask_b32 v234,0,v234,vcc\nM v[92:95],a[92:95],v[196:199],v[92:95]\n" \
"ds_read_b64_tr_b16 v[196:197],v227 offset:33792\nds_read_b64_tr_b16 v[198:199],v227 offset:37888\nM v[104:107],a[88:91],v[208:211],v[104:107]\nv_and_b32 v235,2,v249\nv_cmp_eq_u32 vcc,0,v235\nM v[108:111],a[92:95],v[208:211],v[108:111]\nds_read_b64_tr_b16 v[208:209],v226 offset:34304\nds_read_b64_tr_b16 v[210:211],v226 offset:38400\n" \
"M v[120:123],a[88:91],v[212:215],v[120:123]\nbuffer_store_dwordx4 v[188:191],v253,s[52:55],s38 offen offset:3072 sc1\nM v[124:127],a[92:95],v[212:215],v[124:127]\nds_read_b64_tr_b16 v[212:213],v227 offset:34304\ns_waitcnt lgkmcnt(7)\nM v[8:11],a[96:99],v[200:203],v[8:11]\nds_read_b64_tr_b16 v[214:215],v227 offset:38400\nv_cndmask_b32 v235,0,v234,vcc\n" \
"M v[12:15],a[100:103],v[200:203],v[12:15]\nds_read_b64_tr_b16 v[200:201],v226 offset:40960\nds_read_b64_tr_b16 v[202:203],v226 offset:45056\nM v[24:27],a[96:99],v[204:207],v[24:27]\nv_xor_b32 v234,v234,v235\ns_add_u32 s38,s78,16384\nM v[28:31],a[100:103],v[204:207],v[28:31]\nds_read_b64_tr_b16 v[204:205],v227 offset:40960\n" \
"ds_read_b64_tr_b16 v[206:207],v227 offset:45056\nM v[40:43],a[96:99],v[216:219],v[40:43]\nv_lshlrev_b32 v64,16,v64\nv_lshlrev_b32 v65,16,v65\nM v[44:47],a[100:103],v[216:219],v[44:47]\nds_read_b64_tr_b16 v[216:217],v226 offset:41472\nds_read_b64_tr_b16 v[218:219],v226 offset:45568\nM v[56:59],a[96:99],v[220:223],v[56:59]\n" \
"buffer_load_dwordx4 v[0:3],v238,s[52:55],0 offen offset:0 sc1\nM v[60:63],a[100:103],v[220:223],v[60:63]\nds_read_b64_tr_b16 v[220:221],v227 offset:41472\ns_waitcnt lgkmcnt(7)\nM v[72:75],a[96:99],v[192:195],v[72:75]\nds_read_b64_tr_b16 v[222:223],v227 offset:45568\nv_lshlrev_b32 v66,16,v66\nM v[76:79],a[100:103],v[192:195],v[76:79]\n" \
"ds_read_b64_tr_b16 v[192:193],v226 offset:41984\nds_read_b64_tr_b16 v[194:195],v226 offset:46080\nM v[88:91],a[96:99],v[196:199],v[88:91]\nv_lshlrev_b32 v67,16,v67\nv_lshlrev_b32 v68,16,v68\nM v[92:95],a[100:103],v[196:199],v[92:95]\nds_read_b64_tr_b16 v[196:197],v227 offset:41984\nds_read_b64_tr_b16 v[198:199],v227 offset:46080\n" \
"M v[104:107],a[96:99],v[208:211],v[104:107]\nbuffer_load_dwordx4 v[4:7],v238,s[52:55],0 offen offset:16 sc1\nM v[108:111],a[100:103],v[208:211],v[108:111]\nds_read_b64_tr_b16 v[208:209],v226 offset:42496\nds_read_b64_tr_b16 v[210:211],v226 offset:46592\nM v[120:123],a[96:99],v[212:215],v[120:123]\nv_lshlrev_b32 v69,16,v69\nv_lshlrev_b32 v70,16,v70\n" \
"M v[124:127],a[100:103],v[212:215],v[124:127]\nds_read_b64_tr_b16 v[212:213],v227 offset:42496\ns_waitcnt vmcnt(18) lgkmcnt(7)\nM v[8:11],a[104:107],v[200:203],v[8:11]\nds_read_b64_tr_b16 v[214:215],v227 offset:46592\nv_lshlrev_b32 v71,16,v71\nM v[12:15],a[108:111],v[200:203],v[12:15]\nds_read_b64_tr_b16 v[200:201],v226 offset:49152\n" \
"ds_read_b64_tr_b16 v[202:203],v226 offset:53248\nM v[24:27],a[104:107],v[204:207],v[24:27]\nbuffer_load_dwordx4 v[16:19],v238,s[52:55],0 offen offset:2048 sc1\nM v[28:31],a[108:111],v[204:207],v[28:31]\nds_read_b64_tr_b16 v[204:205],v227 offset:49152\nds_read_b64_tr_b16 v[206:207],v227 offset:53248\nM v[40:43],a[104:107],v[216:219],v[40:43]\nv_lshlrev_b32 v80,16,v80\n" \
"v_lshlrev_b32 v81,16,v81\nM v[44:47],a[108:111],v[216:219],v[44:47]\nds_read_b64_tr_b16 v[216:217],v226 offset:49664\nds_read_b64_tr_b16 v[218:219],v226 offset:53760\nM v[56:59],a[104:107],v[220:223],v[56:59]\nv_lshlrev_b32 v82,16,v82\nv_lshlrev_b32 v83,16,v83\nM v[60:63],a[108:111],v[220:223],v[60:63]\n" \
"ds_read_b64_tr_b16 v[220:221],v227 offset:49664\ns_waitcnt lgkmcnt(7)\nM v[72:75],a[104:107],v[192:195],v[72:75]\nbuffer_load_dwordx4 v[20:23],v238,s[52:55],0 offen offset:2064 sc1\nM v[76:79],a[108:111],v[192:195],v[76:79]\nds_read_b64_tr_b16 v[222:223],v227 offset:53760\nds_read_b64_tr_b16 v[192:193],v226 offset:50176\nM v[88:91],a[104:107],v[196:199],v[88:91]\n" \
"ds_read_b64_tr_b16 v[194:195],v226 offset:54272\nv_lshlrev_b32 v84,16,v84\nM v[92:95],a[108:111],v[196:199],v[92:95]\nds_read_b64_tr_b16 v[196:197],v227 offset:50176\nds_read_b64_tr_b16 v[198:199],v227 offset:54272\nM v[104:107],a[104:107],v[208:211],v[104:107]\nv_lshlrev_b32 v85,16,v85\nv_lshlrev_b32 v86,16,v86\n" \
"M v[108:111],a[108:111],v[208:211],v[108:111]\nds_read_b64_tr_b16 v[208:209],v226 offset:50688\nds_read_b64_tr_b16 v[210:211],v226 offset:54784\nM v[120:123],a[104:107],v[212:215],v[120:123]\nbuffer_load_dwordx4 v[32:35],v238,s[52:55],s97 offen offset:0 sc1\nM v[124:127],a[108:111],v[212:215],v[124:127]\nds_read_b64_tr_b16 v[212:213],v227 offset:50688\ns_waitcnt lgkmcnt(8)\n" \
"M v[8:11],a[112:115],v[200:203],v[8:11]\nds_read_b64_tr_b16 v[214:215],v227 offset:54784\nv_lshlrev_b32 v87,16,v87\nM v[12:15],a[116:119],v[200:203],v[12:15]\nds_read_b64_tr_b16 v[200:201],v226 offset:57344\nds_read_b64_tr_b16 v[202:203],v226 offset:61440\nM v[24:27],a[112:115],v[204:207],v[24:27]\nv_lshlrev_b32 v96,16,v96\n" \
"v_lshlrev_b32 v97,16,v97\nM v[28:31],a[116:119],v[204:207],v[28:31]\nds_read_b64_tr_b16 v[204:205],v227 offset:57344\nds_read_b64_tr_b16 v[206:207],v227 offset:61440\nM v[40:43],a[112:115],v[216:219],v[40:43]\nbuffer_load_dwordx4 v[36:39],v238,s[52:55],s97 offen offset:16 sc1\nM v[44:47],a[116:119],v[216:219],v[44:47]\nds_read_b64_tr_b16 v[216:217],v226 offset:57856\n" \
"s_waitcnt lgkmcnt(7)\nM v[56:59],a[112:115],v[220:223],v[56:59]\nds_read_b64_tr_b16 v[218:219],v226 offset:61952\nv_lshlrev_b32 v98,16,v98\nM v[60:63],a[116:119],v[220:223],v[60:63]\nds_read_b64_tr_b16 v[220:221],v227 offset:57856\nds_read_b64_tr_b16 v[222:223],v227 offset:61952\nM v[72:75],a[112:115],v[192:195],v[72:75]\n" \
"v_lshlrev_b32 v99,16,v99\nv_lshlrev_b32 v100,16,v100\nM v[76:79],a[116:119],v[192:195],v[76:79]\nds_read_b64_tr_b16 v[192:193],v226 offset:58368\nds_read_b64_tr_b16 v[194:195],v226 offset:62464\nM v[88:91],a[112:115],v[196:199],v[88:91]\nbuffer_load_dwordx4 v[48:51],v238,s[52:55],s97 offen offset:2048 sc1\nM v[92:95],a[116:119],v[196:199],v[92:95]\n" \
"ds_read_b64_tr_b16 v[196:197],v227 offset:58368\nds_read_b64_tr_b16 v[198:199],v227 offset:62464\nM v[104:107],a[112:115],v[208:211],v[104:107]\nv_lshlrev_b32 v101,16,v101\nv_lshlrev_b32 v102,16,v102\nM v[108:111],a[116:119],v[208:211],v[108:111]\nds_read_b64_tr_b16 v[208:209],v226 offset:58880\ns_waitcnt lgkmcnt(7)\n" \
"M v[120:123],a[112:115],v[212:215],v[120:123]\nds_read_b64_tr_b16 v[210:211],v226 offset:62976\nv_add_u32 v226,65536,v242\nM v[124:127],a[116:119],v[212:215],v[124:127]\nds_read_b64_tr_b16 v[212:213],v227 offset:58880\ns_waitcnt vmcnt(18)\nM v[8:11],a[120:123],v[200:203],v[8:11]\nbuffer_load_dwordx4 v[52:55],v238,s[52:55],s97 offen offset:2064 sc1\n" \
"M v[12:15],a[124:127],v[200:203],v[12:15]\nds_read_b64_tr_b16 v[214:215],v227 offset:62976\nds_read_b64_tr_b16 v[200:201],v226 offset:0\nM v[24:27],a[120:123],v[204:207],v[24:27]\nds_read_b64_tr_b16 v[202:203],v226 offset:4096\nv_xor_b32 v227,32,v226\nM v[28:31],a[124:127],v[204:207],v[28:31]\nds_read_b64_tr_b16 v[204:205],v227 offset:0\n" \
"ds_read_b64_tr_b16 v[206:207],v227 offset:4096\nM v[40:43],a[120:123],v[216:219],v[40:43]\nv_lshlrev_b32 v103,16,v103\nv_accvgpr_write_b32 a192,v235\nM v[44:47],a[124:127],v[216:219],v[44:47]\nds_read_b64_tr_b16 v[216:217],v226 offset:512\ns_waitcnt lgkmcnt(7)\nM v[56:59],a[120:123],v[220:223],v[56:59]\n" \
"ds_read_b64_tr_b16 v[218:219],v226 offset:4608\nv_accvgpr_write_b32 a193,v234\nM v[60:63],a[124:127],v[220:223],v[60:63]\nds_read_b64_tr_b16 v[220:221],v227 offset:512\nds_read_b64_tr_b16 v[222:223],v227 offset:4608\nM v[72:75],a[120:123],v[192:195],v[72:75]\nv_accvgpr_write_b32 a198,v235\nv_accvgpr_write_b32 a199,v234\n" \
"M v[76:79],a[124:127],v[192:195],v[76:79]\nds_read_b64_tr_b16 v[192:193],v226 offset:1024\nds_read_b64_tr_b16 v[194:195],v226 offset:5120\nM v[88:91],a[120:123],v[196:199],v[88:91]\nM v[92:95],a[124:127],v[196:199],v[92:95]\nds_read_b64_tr_b16 v[196:197],v227 offset:1024\nds_read_b64_tr_b16 v[198:199],v227 offset:5120\nM v[104:107],a[120:123],v[208:211],v[104:107]\n" \
"M v[108:111],a[124:127],v[208:211],v[108:111]\nds_read_b64_tr_b16 v[208:209],v226 offset:1536\ns_waitcnt lgkmcnt(8)\nM v[120:123],a[120:123],v[212:215],v[120:123]\nds_read_b64_tr_b16 v[210:211],v226 offset:5632\nM v[124:127],a[124:127],v[212:215],v[124:127]\nds_read_b64_tr_b16 v[212:213],v227 offset:1536\nds_read_b64_tr_b16 v[214:215],v227 offset:5632\n" \
"M v[8:11],a[128:131],v[200:203],v[8:11]\nM v[12:15],a[132:135],v[200:203],v[12:15]\nds_read_b64_tr_b16 v[200:201],v226 offset:8192\nds_read_b64_tr_b16 v[202:203],v226 offset:12288\nM v[24:27],a[128:131],v[204:207],v[24:27]\nM v[28:31],a[132:135],v[204:207],v[28:31]\nds_read_b64_tr_b16 v[204:205],v227 offset:8192\ns_waitcnt lgkmcnt(9)\n" \
"M v[40:43],a[128:131],v[216:219],v[40:43]\nds_read_b64_tr_b16 v[206:207],v227 offset:12288\nM v[44:47],a[132:135],v[216:219],v[44:47]\nds_read_b64_tr_b16 v[216:217],v226 offset:8704\nds_read_b64_tr_b16 v[218:219],v226 offset:12800\nM v[56:59],a[128:131],v[220:223],v[56:59]\nM v[60:63],a[132:135],v[220:223],v[60:63]\nds_read_b64_tr_b16 v[220:221],v227 offset:8704\n" \
"ds_read_b64_tr_b16 v[222:223],v227 offset:12800\nM v[72:75],a[128:131],v[192:195],v[72:75]\nM v[76:79],a[132:135],v[192:195],v[76:79]\nds_read_b64_tr_b16 v[192:193],v226 offset:9216\ns_waitcnt lgkmcnt(9)\nM v[88:91],a[128:131],v[196:199],v[88:91]\nds_read_b64_tr_b16 v[194:195],v226 offset:13312\nM v[92:95],a[132:135],v[196:199],v[92:95]\n" \
"ds_read_b64_tr_b16 v[196:197],v227 offset:9216\nds_read_b64_tr_b16 v[198:199],v227 offset:13312\nM v[104:107],a[128:131],v[208:211],v[104:107]\nM v[108:111],a[132:135],v[208:211],v[108:111]\nds_read_b64_tr_b16 v[208:209],v226 offset:9728\nds_read_b64_tr_b16 v[210:211],v226 offset:13824\nM v[120:123],a[128:131],v[212:215],v[120:123]\nM v[124:127],a[132:135],v[212:215],v[124:127]\n" \
"ds_read_b64_tr_b16 v[212:213],v227 offset:9728\ns_waitcnt vmcnt(14) lgkmcnt(9)\nM v[8:11],a[136:139],v[200:203],v[8:11]\nds_read_b64_tr_b16 v[214:215],v227 offset:13824\nM v[12:15],a[140:143],v[200:203],v[12:15]\nds_read_b64_tr_b16 v[200:201],v226 offset:32768\nds_read_b64_tr_b16 v[202:203],v226 offset:36864\nM v[24:27],a[136:139],v[204:207],v[24:27]\n" \
"M v[28:31],a[140:143],v[204:207],v[28:31]\nds_read_b64_tr_b16 v[204:205],v227 offset:32768\nds_read_b64_tr_b16 v[206:207],v227 offset:36864\nM v[40:43],a[136:139],v[216:219],v[40:43]\nM v[44:47],a[140:143],v[216:219],v[44:47]\nds_read_b64_tr_b16 v[216:217],v226 offset:33280\ns_waitcnt lgkmcnt(9)\nM v[56:59],a[136:139],v[220:223],v[56:59]\n" \
"ds_read_b64_tr_b16 v[218:219],v226 offset:37376\nM v[60:63],a[140:143],v[220:223],v[60:63]\nds_read_b64_tr_b16 v[220:221],v227 offset:33280\nds_read_b64_tr_b16 v[222:223],v227 offset:37376\nM v[72:75],a[136:139],v[192:195],v[72:75]\nM v[76:79],a[140:143],v[192:195],v[76:79]\nds_read_b64_tr_b16 v[192:193],v226 offset:33792\nds_read_b64_tr_b16 v[194:195],v226 offset:37888\n" \
"M v[88:91],a[136:139],v[196:199],v[88:91]\nM v[92:95],a[140:143],v[196:199],v[92:95]\nds_read_b64_tr_b16 v[196:197],v227 offset:33792\ns_waitcnt lgkmcnt(9)\nM v[104:107],a[136:139],v[208:211],v[104:107]\nds_read_b64_tr_b16 v[198:199],v227 offset:37888\nM v[108:111],a[140:143],v[208:211],v[108:111]\nds_read_b64_tr_b16 v[208:209],v226 offset:34304\n" \
"ds_read_b64_tr_b16 v[210:211],v226 offset:38400\nM v[120:123],a[136:139],v[212:215],v[120:123]\nM v[124:127],a[140:143],v[212:215],v[124:127]\nds_read_b64_tr_b16 v[212:213],v227 offset:34304\nds_read_b64_tr_b16 v[214:215],v227 offset:38400\nM v[8:11],a[144:147],v[200:203],v[8:11]\nM v[12:15],a[148:151],v[200:203],v[12:15]\nds_read_b64_tr_b16 v[200:201],v226 offset:40960\n" \
"s_waitcnt lgkmcnt(9)\nM v[24:27],a[144:147],v[204:207],v[24:27]\nds_read_b64_tr_b16 v[202:203],v226 offset:45056\nM v[28:31],a[148:151],v[204:207],v[28:31]\nds_read_b64_tr_b16 v[204:205],v227 offset:40960\nds_read_b64_tr_b16 v[206:207],v227 offset:45056\nM v[40:43],a[144:147],v[216:219],v[40:43]\nM v[44:47],a[148:151],v[216:219],v[44:47]\n" \
"ds_read_b64_tr_b16 v[216:217],v226 offset:41472\nds_read_b64_tr_b16 v[218:219],v226 offset:45568\nM v[56:59],a[144:147],v[220:223],v[56:59]\nM v[60:63],a[148:151],v[220:223],v[60:63]\nds_read_b64_tr_b16 v[220:221],v227 offset:41472\ns_waitcnt lgkmcnt(9)\nM v[72:75],a[144:147],v[192:195],v[72:75]\nds_read_b64_tr_b16 v[222:223],v227 offset:45568\n" \
"M v[76:79],a[148:151],v[192:195],v[76:79]\nds_read_b64_tr_b16 v[192:193],v226 offset:41984\nds_read_b64_tr_b16 v[194:195],v226 offset:46080\nM v[88:91],a[144:147],v[196:199],v[88:91]\nM v[92:95],a[148:151],v[196:199],v[92:95]\nds_read_b64_tr_b16 v[196:197],v227 offset:41984\nds_read_b64_tr_b16 v[198:199],v227 offset:46080\nM v[104:107],a[144:147],v[208:211],v[104:107]\n" \
"M v[108:111],a[148:151],v[208:211],v[108:111]\nds_read_b64_tr_b16 v[208:209],v226 offset:42496\ns_waitcnt lgkmcnt(9)\nM v[120:123],a[144:147],v[212:215],v[120:123]\nds_read_b64_tr_b16 v[210:211],v226 offset:46592\nM v[124:127],a[148:151],v[212:215],v[124:127]\nds_read_b64_tr_b16 v[212:213],v227 offset:42496\ns_waitcnt vmcnt(11)\n" \
"M v[8:11],a[152:155],v[200:203],v[8:11]\nds_read_b64_tr_b16 v[214:215],v227 offset:46592\nM v[12:15],a[156:159],v[200:203],v[12:15]\nM v[24:27],a[152:155],v[204:207],v[24:27]\nM v[28:31],a[156:159],v[204:207],v[28:31]\ns_waitcnt lgkmcnt(6)\nM v[40:43],a[152:155],v[216:219],v[40:43]\nM v[44:47],a[156:159],v[216:219],v[44:47]\n" \
"M v[56:59],a[152:155],v[220:223],v[56:59]\nM v[60:63],a[156:159],v[220:223],v[60:63]\nM v[72:75],a[152:155],v[192:195],v[72:75]\nM v[76:79],a[156:159],v[192:195],v[76:79]\nv_cvt_pk_bf16_f32 v8,v8,v24\ns_waitcnt lgkmcnt(3)\nM v[88:91],a[152:155],v[196:199],v[88:91]\nv_cvt_pk_bf16_f32 v9,v9,v25\n" \
"v_cvt_pk_bf16_f32 v10,v10,v26\nM v[92:95],a[156:159],v[196:199],v[92:95]\nv_cvt_pk_bf16_f32 v11,v11,v27\ns_waitcnt lgkmcnt(0)\nM v[104:107],a[152:155],v[208:211],v[104:107]\nv_cvt_pk_bf16_f32 v40,v40,v56\nv_cvt_pk_bf16_f32 v41,v41,v57\nM v[108:111],a[156:159],v[208:211],v[108:111]\n" \
"v_cvt_pk_bf16_f32 v42,v42,v58\nv_cvt_pk_bf16_f32 v43,v43,v59\nM v[120:123],a[152:155],v[212:215],v[120:123]\nv_cvt_pk_bf16_f32 v72,v72,v88\nv_cvt_pk_bf16_f32 v73,v73,v89\nv_cvt_pk_bf16_f32 v74,v74,v90\nv_cvt_pk_bf16_f32 v75,v75,v91\nv_cvt_pk_bf16_f32 v12,v12,v28\n" \
"v_cvt_pk_bf16_f32 v13,v13,v29\nv_cvt_pk_bf16_f32 v14,v14,v30\nv_cvt_pk_bf16_f32 v15,v15,v31\nv_cvt_pk_bf16_f32 v104,v104,v120\nv_cvt_pk_bf16_f32 v105,v105,v121\nv_cvt_pk_bf16_f32 v106,v106,v122\nv_cvt_pk_bf16_f32 v107,v107,v123\nv_cvt_pk_bf16_f32 v44,v44,v60\n" \
"v_cvt_pk_bf16_f32 v45,v45,v61\nv_cvt_pk_bf16_f32 v46,v46,v62\nv_cvt_pk_bf16_f32 v47,v47,v63\nv_cvt_pk_bf16_f32 v76,v76,v92\nv_cvt_pk_bf16_f32 v77,v77,v93\nv_cvt_pk_bf16_f32 v78,v78,v94\nv_cvt_pk_bf16_f32 v79,v79,v95\nM v[124:127],a[156:159],v[212:215],v[124:127]\n" \
"v_min_u32 v24,-2,v8\nv_min_u32 v25,-2,v40\nv_min_u32 v26,-2,v72\nv_min_u32 v27,-2,v104\nv_min_u32 v56,-2,v9\nv_min_u32 v57,-2,v41\nv_min_u32 v58,-2,v73\nv_min_u32 v59,-2,v105\n" \
"v_cvt_pk_bf16_f32 v108,v108,v124\nv_cvt_pk_bf16_f32 v109,v109,v125\nv_cvt_pk_bf16_f32 v110,v110,v126\nv_cvt_pk_bf16_f32 v111,v111,v127\nv_min_u32 v88,-2,v10\nv_min_u32 v89,-2,v42\nv_min_u32 v90,-2,v74\nv_min_u32 v91,-2,v106\n" \
"v_min_u32 v120,-2,v11\nv_min_u32 v121,-2,v43\nv_min_u32 v122,-2,v75\nv_min_u32 v123,-2,v107\nv_min_u32 v28,-2,v12\nv_min_u32 v29,-2,v44\nv_min_u32 v30,-2,v76\nv_min_u32 v31,-2,v108\n" \
"v_min_u32 v60,-2,v13\nv_min_u32 v61,-2,v45\nv_min_u32 v62,-2,v77\nv_min_u32 v63,-2,v109\nv_min_u32 v92,-2,v14\nv_min_u32 v93,-2,v46\nv_min_u32 v94,-2,v78\nv_min_u32 v95,-2,v110\n" \
"v_min_u32 v124,-2,v15\nv_min_u32 v125,-2,v47\nv_min_u32 v126,-2,v79\nv_min_u32 v127,-2,v111\nbuffer_store_dwordx4 v[24:27],v253,s[52:55],s78 offen offset:0 sc1\nbuffer_store_dwordx4 v[56:59],v253,s[52:55],s78 offen offset:1024 sc1\nbuffer_store_dwordx4 v[88:91],v253,s[52:55],s78 offen offset:2048 sc1\nbuffer_store_dwordx4 v[120:123],v253,s[52:55],s78 offen offset:3072 sc1\n" \
"buffer_store_dwordx4 v[28:31],v253,s[52:55],s38 offen offset:0 sc1\nbuffer_store_dwordx4 v[60:63],v253,s[52:55],s38 offen offset:1024 sc1\nbuffer_store_dwordx4 v[92:95],v253,s[52:55],s38 offen offset:2048 sc1\nbuffer_store_dwordx4 v[124:127],v253,s[52:55],s38 offen offset:3072 sc1\n.Ltail:\ns_add_u32 s72,s95,0\nbuffer_load_dwordx4 v[160:163],v240,s[52:55],s72 offen sc1\ns_add_u32 s72,s95,2097152\n" \
"buffer_load_dwordx4 v[164:167],v240,s[52:55],s72 offen sc1\ns_add_u32 s72,s95,4194304\nbuffer_load_dwordx4 v[168:171],v240,s[52:55],s72 offen sc1\ns_add_u32 s72,s95,6291456\nbuffer_load_dwordx4 v[172:175],v240,s[52:55],s72 offen sc1\ns_add_u32 s72,s95,8388608\nbuffer_load_dwordx4 v[176:179],v240,s[52:55],s72 offen sc1\ns_add_u32 s72,s95,10485760\n" \
"buffer_load_dwordx4 v[180:183],v240,s[52:55],s72 offen sc1\ns_add_u32 s72,s95,12582912\nbuffer_load_dwordx4 v[184:187],v240,s[52:55],s72 offen sc1\ns_add_u32 s72,s95,14680064\nbuffer_load_dwordx4 v[188:191],v240,s[52:55],s72 offen sc1\ns_add_u32 s72,s74,0\nbuffer_load_dwordx4 v[128:131],v240,s[52:55],s72 offen sc1\ns_add_u32 s72,s74,2097152\n" \
"buffer_load_dwordx4 v[132:135],v240,s[52:55],s72 offen sc1\ns_add_u32 s72,s74,4194304\nbuffer_load_dwordx4 v[136:139],v240,s[52:55],s72 offen sc1\ns_add_u32 s72,s74,6291456\nbuffer_load_dwordx4 v[140:143],v240,s[52:55],s72 offen sc1\ns_add_u32 s72,s74,8388608\nbuffer_load_dwordx4 v[144:147],v240,s[52:55],s72 offen sc1\ns_add_u32 s72,s74,10485760\n" \
"buffer_load_dwordx4 v[148:151],v240,s[52:55],s72 offen sc1\ns_add_u32 s72,s74,12582912\nbuffer_load_dwordx4 v[152:155],v240,s[52:55],s72 offen sc1\ns_add_u32 s72,s74,14680064\nbuffer_load_dwordx4 v[156:159],v240,s[52:55],s72 offen sc1\ns_lshl_b32 s70,s2,9\ns_lshl_b32 s71,s7,8\ns_add_u32 s70,s70,s71\n" \
"s_add_u32 s70,s70,4096\nv_lshl_add_u32 v228,v249,2,s70\nv_add_u32 v229,8192,v228\nglobal_load_dword a0,v228,s[26:27] offset:-4096\nglobal_load_dword a1,v228,s[26:27] offset:-3072\nglobal_load_dword a2,v228,s[26:27] offset:-2048\nglobal_load_dword a3,v228,s[26:27] offset:-1024\nglobal_load_dword a4,v228,s[26:27] offset:0\n" \
"global_load_dword a5,v228,s[26:27] offset:1024\nglobal_load_dword a6,v228,s[26:27] offset:2048\nglobal_load_dword a7,v228,s[26:27] offset:3072\nglobal_load_dword a8,v229,s[26:27] offset:-4096\nglobal_load_dword a9,v229,s[26:27] offset:-3072\nglobal_load_dword a10,v229,s[26:27] offset:-2048\nglobal_load_dword a11,v229,s[26:27] offset:-1024\nglobal_load_dword a12,v229,s[26:27] offset:0\n" \
"global_load_dword a13,v229,s[26:27] offset:1024\nglobal_load_dword a14,v229,s[26:27] offset:2048\nglobal_load_dword a15,v229,s[26:27] offset:3072\nv_and_b32 v228,15,v249\nv_and_b32 v229,0x300,v240\nv_lshl_or_b32 v228,v228,1,v229\ns_add_u32 s70,s64,82944\nv_add_u32 v228,s70,v228\n" \
"v_add_u32 v229,s70,v240\nv_lshlrev_b32 v230,11,v249\nv_cmp_lt_u32 vcc,7,v249\nv_mov_b32 v231,0x40000000\ns_lshl3_add_u32 s37,s6,s75\nv_cndmask_b32 v230,v230,v231,vcc\ns_add_u32 s37,s37,557056\n.Lrow1:\n" \
"s_waitcnt vmcnt(31)\nM v[72:75],a[192:195],v[160:163],0\nM v[76:79],a[196:199],v[160:163],0\nv_add_f32 v104,1.0,v32\nv_add_f32 v105,1.0,v33\nv_add_f32 v106,1.0,v34\nv_add_f32 v107,1.0,v35\nv_add_f32 v108,1.0,v36\n" \
"v_add_f32 v109,1.0,v37\nv_add_f32 v110,1.0,v38\nv_add_f32 v111,1.0,v39\nv_cvt_pk_bf16_f32 v232,0,v104\nv_cvt_pk_bf16_f32 v233,0,v105\nv_cvt_pk_bf16_f32 v234,0,v106\nv_cvt_pk_bf16_f32 v235,0,v107\nv_cvt_pk_bf16_f32 v236,0,v108\n" \
"v_cvt_pk_bf16_f32 v237,0,v109\nv_cvt_pk_bf16_f32 v238,0,v110\nv_cvt_pk_bf16_f32 v239,0,v111\ns_waitcnt vmcnt(30)\nM v[72:75],a[192:195],v[164:167],v[72:75]\nM v[76:79],a[196:199],v[164:167],v[76:79]\ns_waitcnt vmcnt(29)\nM v[72:75],a[192:195],v[168:171],v[72:75]\n" \
"M v[76:79],a[196:199],v[168:171],v[76:79]\ns_waitcnt vmcnt(28)\nM v[72:75],a[192:195],v[172:175],v[72:75]\nM v[76:79],a[196:199],v[172:175],v[76:79]\ns_waitcnt vmcnt(27)\nM v[72:75],a[192:195],v[176:179],v[72:75]\nM v[76:79],a[196:199],v[176:179],v[76:79]\ns_waitcnt vmcnt(26)\n" \
"M v[72:75],a[192:195],v[180:183],v[72:75]\nM v[76:79],a[196:199],v[180:183],v[76:79]\ns_waitcnt vmcnt(25)\nM v[72:75],a[192:195],v[184:187],v[72:75]\nM v[76:79],a[196:199],v[184:187],v[76:79]\ns_waitcnt vmcnt(24)\nM v[72:75],a[192:195],v[188:191],v[72:75]\nM v[76:79],a[196:199],v[188:191],v[76:79]\n" \
"s_sub_u32 s38,56622112,s95\ns_lshr_b32 s38,s38,5\ns_nop 4\nv_cvt_pk_bf16_f32 v72,0,v72\nv_cvt_pk_bf16_f32 v73,0,v73\nv_cvt_pk_bf16_f32 v74,0,v74\nv_cvt_pk_bf16_f32 v75,0,v75\nv_cvt_pk_bf16_f32 v76,0,v76\n" \
"v_cvt_pk_bf16_f32 v77,0,v77\nv_cvt_pk_bf16_f32 v78,0,v78\nv_cvt_pk_bf16_f32 v79,0,v79\nv_pk_mul_f32 v[72:73],v[72:73],v[0:1]\nv_pk_mul_f32 v[74:75],v[74:75],v[2:3]\nv_pk_mul_f32 v[76:77],v[76:77],v[4:5]\nv_pk_mul_f32 v[78:79],v[78:79],v[6:7]\nv_cvt_pk_bf16_f32 v72,0,v72\n" \
"v_cvt_pk_bf16_f32 v73,0,v73\nv_cvt_pk_bf16_f32 v74,0,v74\nv_cvt_pk_bf16_f32 v75,0,v75\nv_cvt_pk_bf16_f32 v76,0,v76\nv_cvt_pk_bf16_f32 v77,0,v77\nv_cvt_pk_bf16_f32 v78,0,v78\nv_cvt_pk_bf16_f32 v79,0,v79\nv_pk_add_f32 v[72:73],v[72:73],v[64:65]\n" \
"v_pk_add_f32 v[74:75],v[74:75],v[66:67]\nv_pk_add_f32 v[76:77],v[76:77],v[68:69]\nv_pk_add_f32 v[78:79],v[78:79],v[70:71]\nv_cvt_pk_bf16_f32 v72,0,v72\nv_cvt_pk_bf16_f32 v73,0,v73\nv_cvt_pk_bf16_f32 v74,0,v74\nv_cvt_pk_bf16_f32 v75,0,v75\nv_cvt_pk_bf16_f32 v76,0,v76\n" \
"v_cvt_pk_bf16_f32 v77,0,v77\nv_cvt_pk_bf16_f32 v78,0,v78\nv_cvt_pk_bf16_f32 v79,0,v79\nv_readfirstlane_b32 s70,v72\ns_nop 1\nv_subrev_f32 v104,s70,v72\nv_subrev_f32 v105,s70,v73\nv_subrev_f32 v106,s70,v74\n" \
"v_subrev_f32 v107,s70,v75\nv_subrev_f32 v108,s70,v76\nv_subrev_f32 v109,s70,v77\nv_subrev_f32 v110,s70,v78\nv_subrev_f32 v111,s70,v79\nv_pk_mul_f32 v[124:125],v[104:105],v[104:105]\nv_pk_add_f32 v[120:121],v[104:105],v[106:107]\nv_pk_fma_f32 v[124:125],v[106:107],v[106:107],v[124:125]\n" \
"v_pk_fma_f32 v[124:125],v[108:109],v[108:109],v[124:125]\nv_pk_fma_f32 v[124:125],v[110:111],v[110:111],v[124:125]\nv_pk_add_f32 v[108:109],v[108:109],v[110:111]\nv_pk_add_f32 v[120:121],v[120:121],v[108:109]\nv_add_f32 v120,v120,v121\nv_add_f32 v121,v124,v125\ns_nop 1\nv_permlane16_swap_b32 v120,v121\n" \
"v_add_f32 v120,v120,v121\nv_mov_b32 v121,v120\ns_nop 1\nv_permlane32_swap_b32 v120,v121\nv_add_f32 v120,v120,v121\ns_nop 1\nv_add_f32_dpp v120,v120,v120 quad_perm:[1,0,3,2] row_mask:0xf bank_mask:0xf\ns_nop 1\n" \
"v_add_f32_dpp v120,v120,v120 quad_perm:[2,3,0,1] row_mask:0xf bank_mask:0xf\ns_nop 1\nv_add_f32_dpp v120,v120,v120 row_half_mirror row_mask:0xf bank_mask:0xf\ns_nop 1\nv_add_f32_dpp v120,v120,v120 row_mirror row_mask:0xf bank_mask:0xf\nv_mul_f32 v120,0x3b000000,v120\nv_mov_b32 v121,v120\nv_cmp_u_f32 vcc,v120,v120\n" \
"s_nop 0\nv_permlane16_swap_b32 v120,v121\ns_cbranch_vccnz .Lrs1\n.Lrb1:\nv_fma_f32 v121,-v120,v120,v121\nv_add_f32 v122,s70,v120\nv_max_f32 v121,0,v121\nv_cvt_pk_bf16_f32 v122,0,v122\n" \
"v_cvt_pk_bf16_f32 v121,0,v121\nv_add_f32 v121,0x3727c5ac,v121\nv_cvt_pk_bf16_f32 v121,0,v121\nv_sqrt_f32 v121,v121\nv_pk_add_f32 v[88:89],v[72:73],v[122:123] op_sel_hi:[1,0] neg_lo:[0,1] neg_hi:[0,1]\nv_cvt_pk_bf16_f32 v121,0,v121\nv_rcp_f32 v123,v121\nv_cvt_pk_bf16_f32 v88,0,v88\n" \
"v_pk_add_f32 v[90:91],v[74:75],v[122:123] op_sel_hi:[1,0] neg_lo:[0,1] neg_hi:[0,1]\nv_pk_add_f32 v[92:93],v[76:77],v[122:123] op_sel_hi:[1,0] neg_lo:[0,1] neg_hi:[0,1]\nv_pk_add_f32 v[94:95],v[78:79],v[122:123] op_sel_hi:[1,0] neg_lo:[0,1] neg_hi:[0,1]\nv_cvt_pk_bf16_f32 v89,0,v89\nv_pk_mul_f32 v[88:89],v[88:89],v[122:123] op_sel:[0,1]\nv_cvt_pk_bf16_f32 v88,0,v88\nv_cvt_pk_bf16_f32 v89,0,v89\nv_pk_mul_f32 v[88:89],v[88:89],v[232:233]\n" \
"v_cvt_pk_bf16_f32 v88,0,v88\nv_cvt_pk_bf16_f32 v89,0,v89\nv_pk_add_f32 v[88:89],v[88:89],v[16:17]\nv_cvt_pk_bf16_f32 v88,0,v88\nv_cvt_pk_bf16_f32 v89,0,v89\nv_pk_mul_f32 v[124:125],v[88:89],v[96:97]\nv_cvt_pk_bf16_f32 v90,0,v90\nv_cvt_pk_bf16_f32 v91,0,v91\n" \
"v_pk_mul_f32 v[90:91],v[90:91],v[122:123] op_sel:[0,1]\nv_cvt_pk_bf16_f32 v90,0,v90\nv_cvt_pk_bf16_f32 v91,0,v91\nv_pk_mul_f32 v[90:91],v[90:91],v[234:235]\nv_cvt_pk_bf16_f32 v90,0,v90\nv_cvt_pk_bf16_f32 v91,0,v91\nv_pk_add_f32 v[90:91],v[90:91],v[18:19]\nv_cvt_pk_bf16_f32 v90,0,v90\n" \
"v_cvt_pk_bf16_f32 v91,0,v91\nv_pk_fma_f32 v[124:125],v[90:91],v[98:99],v[124:125]\nv_cvt_pk_bf16_f32 v92,0,v92\nv_cvt_pk_bf16_f32 v93,0,v93\nv_pk_mul_f32 v[92:93],v[92:93],v[122:123] op_sel:[0,1]\nv_cvt_pk_bf16_f32 v92,0,v92\nv_cvt_pk_bf16_f32 v93,0,v93\nv_pk_mul_f32 v[92:93],v[92:93],v[236:237]\n" \
"v_cvt_pk_bf16_f32 v92,0,v92\nv_cvt_pk_bf16_f32 v93,0,v93\nv_pk_add_f32 v[92:93],v[92:93],v[20:21]\nv_cvt_pk_bf16_f32 v92,0,v92\nv_cvt_pk_bf16_f32 v93,0,v93\nv_pk_fma_f32 v[124:125],v[92:93],v[100:101],v[124:125]\nv_cvt_pk_bf16_f32 v94,0,v94\nv_cvt_pk_bf16_f32 v95,0,v95\n" \
"v_pk_mul_f32 v[94:95],v[94:95],v[122:123] op_sel:[0,1]\nv_cvt_pk_bf16_f32 v94,0,v94\nv_cvt_pk_bf16_f32 v95,0,v95\nv_pk_mul_f32 v[94:95],v[94:95],v[238:239]\nv_cvt_pk_bf16_f32 v94,0,v94\nv_cvt_pk_bf16_f32 v95,0,v95\nv_pk_add_f32 v[94:95],v[94:95],v[22:23]\nv_cvt_pk_bf16_f32 v94,0,v94\n" \
"v_cvt_pk_bf16_f32 v95,0,v95\nv_pk_fma_f32 v[124:125],v[94:95],v[102:103],v[124:125]\nv_add_f32 v124,v124,v125\nv_mov_b32 v125,v124\nv_lshrrev_b32 v104,16,v121\nv_or_b32 v104,v104,v122\nv_permlane16_swap_b32 v124,v125\nv_add_f32 v124,v124,v125\n" \
"v_mov_b32 v125,v124\nv_pk_mul_f32 v[88:89],v[88:89],v[48:49]\nv_pk_mul_f32 v[90:91],v[90:91],v[50:51]\nv_permlane32_swap_b32 v124,v125\nv_add_f32 v124,v124,v125\nv_pk_mul_f32 v[92:93],v[92:93],v[52:53]\nv_pk_mul_f32 v[94:95],v[94:95],v[54:55]\nv_add_f32_dpp v124,v124,v124 quad_perm:[1,0,3,2] row_mask:0xf bank_mask:0xf\n" \
"v_cvt_pk_bf16_f32 v88,0,v88\nv_cvt_pk_bf16_f32 v89,0,v89\nv_add_f32_dpp v124,v124,v124 quad_perm:[2,3,0,1] row_mask:0xf bank_mask:0xf\nv_cvt_pk_bf16_f32 v90,0,v90\nv_cvt_pk_bf16_f32 v91,0,v91\nv_add_f32_dpp v124,v124,v124 row_half_mirror row_mask:0xf bank_mask:0xf\nv_cvt_pk_bf16_f32 v92,0,v92\nv_cvt_pk_bf16_f32 v93,0,v93\n" \
"v_add_f32_dpp v124,v124,v124 row_mirror row_mask:0xf bank_mask:0xf\nv_cvt_pk_bf16_f32 v124,0,v124\nv_cvt_pk_bf16_f32 v94,0,v94\nv_readfirstlane_b32 s77,v124\nv_cvt_pk_bf16_f32 v95,0,v95\ns_ashr_i32 s71,s77,31\ns_and_b32 s72,s77,0x7fffffff\ns_or_b32 s71,s71,0x80000000\n" \
"s_xor_b32 s77,s77,s71\ns_and_b32 s77,s77,0xffff0000\ns_cmp_gt_u32 s72,0x7f800000\ns_cselect_b32 s77,0,s77\ns_or_b32 s77,s77,s38\nv_mov_b32 v105,s77\nds_max_u64 v250,v[104:105]\nv_pk_add_f32 v[88:89],v[88:89],v[72:73]\n" \
"v_pk_add_f32 v[90:91],v[90:91],v[74:75]\nv_pk_add_f32 v[92:93],v[92:93],v[76:77]\nv_pk_add_f32 v[94:95],v[94:95],v[78:79]\nv_cvt_pk_bf16_f32 v88,0,v88\nds_write_b16_d16_hi v228,v88 offset:0\nv_cvt_pk_bf16_f32 v89,0,v89\nds_write_b16_d16_hi v228,v89 offset:32\nv_cvt_pk_bf16_f32 v90,0,v90\n" \
"ds_write_b16_d16_hi v228,v90 offset:64\nv_cvt_pk_bf16_f32 v91,0,v91\nds_write_b16_d16_hi v228,v91 offset:96\nv_cvt_pk_bf16_f32 v92,0,v92\nds_write_b16_d16_hi v228,v92 offset:128\nv_cvt_pk_bf16_f32 v93,0,v93\nds_write_b16_d16_hi v228,v93 offset:160\nv_cvt_pk_bf16_f32 v94,0,v94\n" \
"ds_write_b16_d16_hi v228,v94 offset:192\nv_cvt_pk_bf16_f32 v95,0,v95\nds_write_b16_d16_hi v228,v95 offset:224\nds_read_b128 v[216:219],v229 offset:0\n.Lrow0:\ns_waitcnt vmcnt(23)\nM v[8:11],a[192:195],v[128:131],0\nM v[12:15],a[196:199],v[128:131],0\n" \
"s_waitcnt vmcnt(22)\nM v[8:11],a[192:195],v[132:135],v[8:11]\nM v[12:15],a[196:199],v[132:135],v[12:15]\ns_waitcnt vmcnt(21)\nM v[8:11],a[192:195],v[136:139],v[8:11]\nM v[12:15],a[196:199],v[136:139],v[12:15]\ns_waitcnt vmcnt(20)\nM v[8:11],a[192:195],v[140:143],v[8:11]\n" \
"M v[12:15],a[196:199],v[140:143],v[12:15]\ns_waitcnt vmcnt(19)\nM v[8:11],a[192:195],v[144:147],v[8:11]\nM v[12:15],a[196:199],v[144:147],v[12:15]\ns_waitcnt vmcnt(18)\nM v[8:11],a[192:195],v[148:151],v[8:11]\nM v[12:15],a[196:199],v[148:151],v[12:15]\ns_waitcnt vmcnt(17)\n" \
"M v[8:11],a[192:195],v[152:155],v[8:11]\nM v[12:15],a[196:199],v[152:155],v[12:15]\ns_waitcnt vmcnt(16)\nM v[8:11],a[192:195],v[156:159],v[8:11]\nM v[12:15],a[196:199],v[156:159],v[12:15]\ns_sub_u32 s39,56622112,s74\ns_lshr_b32 s39,s39,5\ns_nop 4\n" \
"v_cvt_pk_bf16_f32 v8,0,v8\nv_cvt_pk_bf16_f32 v9,0,v9\nv_cvt_pk_bf16_f32 v10,0,v10\nv_cvt_pk_bf16_f32 v11,0,v11\nv_cvt_pk_bf16_f32 v12,0,v12\nv_cvt_pk_bf16_f32 v13,0,v13\nv_cvt_pk_bf16_f32 v14,0,v14\nv_cvt_pk_bf16_f32 v15,0,v15\n" \
"v_pk_mul_f32 v[8:9],v[8:9],v[0:1]\nv_pk_mul_f32 v[10:11],v[10:11],v[2:3]\nv_pk_mul_f32 v[12:13],v[12:13],v[4:5]\nv_pk_mul_f32 v[14:15],v[14:15],v[6:7]\nv_cvt_pk_bf16_f32 v8,0,v8\nv_cvt_pk_bf16_f32 v9,0,v9\nv_cvt_pk_bf16_f32 v10,0,v10\nv_cvt_pk_bf16_f32 v11,0,v11\n" \
"v_cvt_pk_bf16_f32 v12,0,v12\nv_cvt_pk_bf16_f32 v13,0,v13\nv_cvt_pk_bf16_f32 v14,0,v14\nv_cvt_pk_bf16_f32 v15,0,v15\nv_pk_add_f32 v[8:9],v[8:9],v[80:81]\nv_pk_add_f32 v[10:11],v[10:11],v[82:83]\nv_pk_add_f32 v[12:13],v[12:13],v[84:85]\nv_pk_add_f32 v[14:15],v[14:15],v[86:87]\n" \
"v_cvt_pk_bf16_f32 v8,0,v8\nv_cvt_pk_bf16_f32 v9,0,v9\nv_cvt_pk_bf16_f32 v10,0,v10\nv_cvt_pk_bf16_f32 v11,0,v11\nv_cvt_pk_bf16_f32 v12,0,v12\nv_cvt_pk_bf16_f32 v13,0,v13\nv_cvt_pk_bf16_f32 v14,0,v14\nv_cvt_pk_bf16_f32 v15,0,v15\n" \
"v_readfirstlane_b32 s70,v8\ns_nop 1\nv_subrev_f32 v40,s70,v8\nv_subrev_f32 v41,s70,v9\nv_subrev_f32 v42,s70,v10\nv_subrev_f32 v43,s70,v11\nv_subrev_f32 v44,s70,v12\nv_subrev_f32 v45,s70,v13\n" \
"v_subrev_f32 v46,s70,v14\nv_subrev_f32 v47,s70,v15\nv_pk_mul_f32 v[60:61],v[40:41],v[40:41]\nv_pk_add_f32 v[56:57],v[40:41],v[42:43]\nv_pk_fma_f32 v[60:61],v[42:43],v[42:43],v[60:61]\nv_pk_fma_f32 v[60:61],v[44:45],v[44:45],v[60:61]\nv_pk_fma_f32 v[60:61],v[46:47],v[46:47],v[60:61]\nv_pk_add_f32 v[44:45],v[44:45],v[46:47]\n" \
"v_pk_add_f32 v[56:57],v[56:57],v[44:45]\nv_add_f32 v56,v56,v57\nv_add_f32 v57,v60,v61\ns_nop 1\nv_permlane16_swap_b32 v56,v57\nv_add_f32 v56,v56,v57\nv_mov_b32 v57,v56\ns_nop 1\n" \
"v_permlane32_swap_b32 v56,v57\nv_add_f32 v56,v56,v57\ns_nop 1\nv_add_f32_dpp v56,v56,v56 quad_perm:[1,0,3,2] row_mask:0xf bank_mask:0xf\ns_nop 1\nv_add_f32_dpp v56,v56,v56 quad_perm:[2,3,0,1] row_mask:0xf bank_mask:0xf\ns_nop 1\nv_add_f32_dpp v56,v56,v56 row_half_mirror row_mask:0xf bank_mask:0xf\n" \
"s_nop 1\nv_add_f32_dpp v56,v56,v56 row_mirror row_mask:0xf bank_mask:0xf\nv_mul_f32 v56,0x3b000000,v56\nv_mov_b32 v57,v56\nv_cmp_u_f32 vcc,v56,v56\ns_nop 0\nv_permlane16_swap_b32 v56,v57\ns_cbranch_vccnz .Lrs0\n" \
".Lrb0:\nv_fma_f32 v57,-v56,v56,v57\nv_add_f32 v58,s70,v56\nv_max_f32 v57,0,v57\nv_cvt_pk_bf16_f32 v58,0,v58\nv_cvt_pk_bf16_f32 v57,0,v57\nv_add_f32 v57,0x3727c5ac,v57\nv_cvt_pk_bf16_f32 v57,0,v57\n" \
"v_sqrt_f32 v57,v57\nv_pk_add_f32 v[24:25],v[8:9],v[58:59] op_sel_hi:[1,0] neg_lo:[0,1] neg_hi:[0,1]\nv_cvt_pk_bf16_f32 v57,0,v57\nv_rcp_f32 v59,v57\nv_cvt_pk_bf16_f32 v24,0,v24\nv_pk_add_f32 v[26:27],v[10:11],v[58:59] op_sel_hi:[1,0] neg_lo:[0,1] neg_hi:[0,1]\nv_pk_add_f32 v[28:29],v[12:13],v[58:59] op_sel_hi:[1,0] neg_lo:[0,1] neg_hi:[0,1]\nv_pk_add_f32 v[30:31],v[14:15],v[58:59] op_sel_hi:[1,0] neg_lo:[0,1] neg_hi:[0,1]\n" \
"v_cvt_pk_bf16_f32 v25,0,v25\nv_pk_mul_f32 v[24:25],v[24:25],v[58:59] op_sel:[0,1]\nv_cvt_pk_bf16_f32 v24,0,v24\nv_cvt_pk_bf16_f32 v25,0,v25\nv_pk_mul_f32 v[24:25],v[24:25],v[232:233]\nv_cvt_pk_bf16_f32 v24,0,v24\nv_cvt_pk_bf16_f32 v25,0,v25\nv_pk_add_f32 v[24:25],v[24:25],v[16:17]\n" \
"v_cvt_pk_bf16_f32 v24,0,v24\nv_cvt_pk_bf16_f32 v25,0,v25\nv_pk_mul_f32 v[60:61],v[24:25],v[96:97]\nv_cvt_pk_bf16_f32 v26,0,v26\nv_cvt_pk_bf16_f32 v27,0,v27\nv_pk_mul_f32 v[26:27],v[26:27],v[58:59] op_sel:[0,1]\nv_cvt_pk_bf16_f32 v26,0,v26\nv_cvt_pk_bf16_f32 v27,0,v27\n" \
"v_pk_mul_f32 v[26:27],v[26:27],v[234:235]\nv_cvt_pk_bf16_f32 v26,0,v26\nv_cvt_pk_bf16_f32 v27,0,v27\nv_pk_add_f32 v[26:27],v[26:27],v[18:19]\nv_cvt_pk_bf16_f32 v26,0,v26\nv_cvt_pk_bf16_f32 v27,0,v27\nv_pk_fma_f32 v[60:61],v[26:27],v[98:99],v[60:61]\nv_cvt_pk_bf16_f32 v28,0,v28\n" \
"v_cvt_pk_bf16_f32 v29,0,v29\nv_pk_mul_f32 v[28:29],v[28:29],v[58:59] op_sel:[0,1]\nv_cvt_pk_bf16_f32 v28,0,v28\nv_cvt_pk_bf16_f32 v29,0,v29\nv_pk_mul_f32 v[28:29],v[28:29],v[236:237]\nv_cvt_pk_bf16_f32 v28,0,v28\nv_cvt_pk_bf16_f32 v29,0,v29\nv_pk_add_f32 v[28:29],v[28:29],v[20:21]\n" \
"v_cvt_pk_bf16_f32 v28,0,v28\nv_cvt_pk_bf16_f32 v29,0,v29\nv_pk_fma_f32 v[60:61],v[28:29],v[100:101],v[60:61]\nv_cvt_pk_bf16_f32 v30,0,v30\nv_cvt_pk_bf16_f32 v31,0,v31\nv_pk_mul_f32 v[30:31],v[30:31],v[58:59] op_sel:[0,1]\nv_cvt_pk_bf16_f32 v30,0,v30\nv_cvt_pk_bf16_f32 v31,0,v31\n" \
"v_pk_mul_f32 v[30:31],v[30:31],v[238:239]\nv_cvt_pk_bf16_f32 v30,0,v30\nv_cvt_pk_bf16_f32 v31,0,v31\nv_pk_add_f32 v[30:31],v[30:31],v[22:23]\nv_cvt_pk_bf16_f32 v30,0,v30\nv_cvt_pk_bf16_f32 v31,0,v31\nv_pk_fma_f32 v[60:61],v[30:31],v[102:103],v[60:61]\nv_add_f32 v60,v60,v61\n" \
"v_mov_b32 v61,v60\nv_lshrrev_b32 v40,16,v57\nv_or_b32 v40,v40,v58\nv_permlane16_swap_b32 v60,v61\nv_add_f32 v60,v60,v61\nv_mov_b32 v61,v60\nv_pk_mul_f32 v[24:25],v[24:25],v[48:49]\nv_pk_mul_f32 v[26:27],v[26:27],v[50:51]\n" \
"v_permlane32_swap_b32 v60,v61\nv_add_f32 v60,v60,v61\nv_pk_mul_f32 v[28:29],v[28:29],v[52:53]\nv_pk_mul_f32 v[30:31],v[30:31],v[54:55]\nv_add_f32_dpp v60,v60,v60 quad_perm:[1,0,3,2] row_mask:0xf bank_mask:0xf\nv_cvt_pk_bf16_f32 v24,0,v24\nv_cvt_pk_bf16_f32 v25,0,v25\nv_add_f32_dpp v60,v60,v60 quad_perm:[2,3,0,1] row_mask:0xf bank_mask:0xf\n" \
"v_cvt_pk_bf16_f32 v26,0,v26\nv_cvt_pk_bf16_f32 v27,0,v27\nv_add_f32_dpp v60,v60,v60 row_half_mirror row_mask:0xf bank_mask:0xf\nv_cvt_pk_bf16_f32 v28,0,v28\nv_cvt_pk_bf16_f32 v29,0,v29\nv_add_f32_dpp v60,v60,v60 row_mirror row_mask:0xf bank_mask:0xf\nv_cvt_pk_bf16_f32 v60,0,v60\nv_cvt_pk_bf16_f32 v30,0,v30\n" \
"v_readfirstlane_b32 s76,v60\nv_cvt_pk_bf16_f32 v31,0,v31\ns_ashr_i32 s71,s76,31\ns_and_b32 s72,s76,0x7fffffff\ns_or_b32 s71,s71,0x80000000\ns_xor_b32 s76,s76,s71\ns_and_b32 s76,s76,0xffff0000\ns_cmp_gt_u32 s72,0x7f800000\n" \
"s_cselect_b32 s76,0,s76\ns_or_b32 s76,s76,s39\nv_mov_b32 v41,s76\nds_max_u64 v250,v[40:41]\ns_waitcnt lgkmcnt(0)\ns_barrier\nds_read_b64 v[224:225],v250\ns_waitcnt lgkmcnt(0)\n" \
"buffer_store_dwordx2 v[224:225],v230,s[52:55],s37 offen sc1\nv_pk_add_f32 v[24:25],v[24:25],v[8:9]\nv_pk_add_f32 v[26:27],v[26:27],v[10:11]\nv_pk_add_f32 v[28:29],v[28:29],v[12:13]\nv_pk_add_f32 v[30:31],v[30:31],v[14:15]\nv_cvt_pk_bf16_f32 v24,0,v24\nds_write_b16_d16_hi v228,v24 offset:1024\nv_cvt_pk_bf16_f32 v25,0,v25\n" \
"ds_write_b16_d16_hi v228,v25 offset:1056\nv_cvt_pk_bf16_f32 v26,0,v26\nds_write_b16_d16_hi v228,v26 offset:1088\nv_cvt_pk_bf16_f32 v27,0,v27\nds_write_b16_d16_hi v228,v27 offset:1120\nv_cvt_pk_bf16_f32 v28,0,v28\nds_write_b16_d16_hi v228,v28 offset:1152\nv_cvt_pk_bf16_f32 v29,0,v29\n" \
"ds_write_b16_d16_hi v228,v29 offset:1184\nv_cvt_pk_bf16_f32 v30,0,v30\nds_write_b16_d16_hi v228,v30 offset:1216\nv_cvt_pk_bf16_f32 v31,0,v31\nds_write_b16_d16_hi v228,v31 offset:1248\nds_read_b128 v[220:223],v229 offset:1024\nv_readfirstlane_b32 s70,v225\nv_mov_b32 v226,s77\n" \
"v_mov_b32 v227,s76\ns_sub_u32 s95,s95,54525952\ns_sub_u32 s74,s74,54525952\ns_cmp_eq_u32 s70,s77\ns_cselect_b32 s95,0x40000000,s95\ns_cmp_eq_u32 s70,s76\ns_cselect_b32 s74,0x40000000,s74\ns_waitcnt lgkmcnt(0)\n" \
"buffer_store_dwordx4 v[216:219],v240,s[44:47],s95 offen sc1\nbuffer_store_dwordx4 v[220:223],v240,s[44:47],s74 offen sc1\ns_waitcnt vmcnt(3)\ns_branch .Lfin\n.Lhs:\ns_sub_u32 s73,s73,1\ns_cbranch_scc1 .Lhb\ns_sleep 2\n" \
"buffer_load_dwordx2 v[166:167],v165,s[52:55],s80 offen sc1\ns_waitcnt vmcnt(0)\nv_xor_b32 v166,v166,v230\nv_xor_b32 v167,v167,v231\nv_or_b32 v166,v166,v167\nv_cmp_ne_u32 vcc,0,v166\ns_nop 0\ns_cbranch_vccnz .Lhs\n" \
"s_branch .Lhb\n.Lps0:\ns_sub_u32 s73,s73,1\ns_cbranch_scc1 .Lpb0\ns_sleep 1\nbuffer_load_dword v248,v228,s[52:55],s80 offen sc1\ns_waitcnt vmcnt(0)\nv_cmp_eq_u32 vcc,0,v248\n" \
"s_nop 0\ns_cbranch_vccnz .Lps0\ns_branch .Lpb0\n.Lps1:\ns_sub_u32 s73,s73,1\ns_cbranch_scc1 .Lpb1\ns_sleep 1\nbuffer_load_dword v248,v228,s[52:55],s80 offen sc1\n" \
"s_waitcnt vmcnt(0)\nv_cmp_eq_u32 vcc,0,v248\ns_nop 0\ns_cbranch_vccnz .Lps1\ns_branch .Lpb1\n.Lps2:\ns_sub_u32 s73,s73,1\ns_cbranch_scc1 .Lpb2\n" \
"s_sleep 1\nbuffer_load_dword v248,v251,s[52:55],s80 offen sc1\ns_waitcnt vmcnt(0)\nv_cmp_eq_u32 vcc,0,v248\ns_nop 0\ns_cbranch_vccnz .Lps2\ns_branch .Lpb2\n.Lps3:\n" \
"s_sub_u32 s73,s73,1\ns_cbranch_scc1 .Lpb3\ns_sleep 1\nbuffer_load_dword v248,v251,s[52:55],s80 offen sc1\ns_waitcnt vmcnt(0)\nv_cmp_eq_u32 vcc,0,v248\ns_nop 0\ns_cbranch_vccnz .Lps3\n" \
"s_branch .Lpb3\n.Lps4:\ns_sub_u32 s73,s73,1\ns_cbranch_scc1 .Lpb4\ns_sleep 1\nbuffer_load_dword v248,v251,s[52:55],s80 offen sc1\ns_waitcnt vmcnt(0)\nv_cmp_eq_u32 vcc,0,v248\n" \
"s_nop 0\ns_cbranch_vccnz .Lps4\ns_branch .Lpb4\n.Lps5:\ns_sub_u32 s73,s73,1\ns_cbranch_scc1 .Lpb5\ns_sleep 1\nbuffer_load_dword v248,v251,s[52:55],s80 offen sc1\n" \
"s_waitcnt vmcnt(0)\nv_cmp_eq_u32 vcc,0,v248\ns_nop 0\ns_cbranch_vccnz .Lps5\ns_branch .Lpb5\n.Lx01:\ns_cmp_eq_u32 s62,0\ns_cbranch_scc1 .Ltrans\n" \
"s_branch .Ldf\n.Lps6:\ns_sub_u32 s73,s73,1\ns_cbranch_scc1 .Lpb6\ns_sleep 1\nbuffer_load_dword v248,v251,s[52:55],s80 offen sc1\ns_waitcnt vmcnt(0)\nv_and_b32 v248,s17,v248\n" \
"v_cmp_eq_u32 vcc,0,v248\ns_nop 0\ns_cbranch_vccnz .Lps6\ns_branch .Lpb6\n.Lps7:\ns_sub_u32 s73,s73,1\ns_cbranch_scc1 .Lpb7\ns_sleep 1\n" \
"buffer_load_dword v248,v228,s[52:55],s80 offen sc1\ns_waitcnt vmcnt(0)\nv_cmp_eq_u32 vcc,0,v248\ns_nop 0\ns_cbranch_vccnz .Lps7\ns_branch .Lpb7\n.Lps8:\ns_sub_u32 s73,s73,1\n" \
"s_cbranch_scc1 .Lpb8\ns_sleep 1\nbuffer_load_dword v248,v251,s[52:55],s79 offen sc1\ns_waitcnt vmcnt(0)\nv_cmp_eq_u32 vcc,0,v248\ns_nop 0\ns_cbranch_vccnz .Lps8\ns_branch .Lpb8\n" \
".Lrs1:\ns_mov_b32 s71,0\n.Lrl1:\ns_mov_b32 s97,0\nv_max3_u32 v124,v160,v161,v162\nv_max3_u32 v124,v124,v163,v163\nv_cmp_eq_u32 vcc,-1,v124\ns_add_u32 s72,s95,0\n" \
"s_cbranch_vccz .Lrz10\ns_mov_b64 exec,vcc\ns_mov_b32 s97,1\nbuffer_load_dwordx4 v[160:163],v240,s[52:55],s72 offen sc1\ns_mov_b64 exec,-1\n.Lrz10:\nv_max3_u32 v124,v164,v165,v166\nv_max3_u32 v124,v124,v167,v167\n" \
"v_cmp_eq_u32 vcc,-1,v124\ns_add_u32 s72,s95,2097152\ns_cbranch_vccz .Lrz11\ns_mov_b64 exec,vcc\ns_mov_b32 s97,1\nbuffer_load_dwordx4 v[164:167],v240,s[52:55],s72 offen sc1\ns_mov_b64 exec,-1\n.Lrz11:\n" \
"v_max3_u32 v124,v168,v169,v170\nv_max3_u32 v124,v124,v171,v171\nv_cmp_eq_u32 vcc,-1,v124\ns_add_u32 s72,s95,4194304\ns_cbranch_vccz .Lrz12\ns_mov_b64 exec,vcc\ns_mov_b32 s97,1\nbuffer_load_dwordx4 v[168:171],v240,s[52:55],s72 offen sc1\n" \
"s_mov_b64 exec,-1\n.Lrz12:\nv_max3_u32 v124,v172,v173,v174\nv_max3_u32 v124,v124,v175,v175\nv_cmp_eq_u32 vcc,-1,v124\ns_add_u32 s72,s95,6291456\ns_cbranch_vccz .Lrz13\ns_mov_b64 exec,vcc\n" \
"s_mov_b32 s97,1\nbuffer_load_dwordx4 v[172:175],v240,s[52:55],s72 offen sc1\ns_mov_b64 exec,-1\n.Lrz13:\nv_max3_u32 v124,v176,v177,v178\nv_max3_u32 v124,v124,v179,v179\nv_cmp_eq_u32 vcc,-1,v124\ns_add_u32 s72,s95,8388608\n" \
"s_cbranch_vccz .Lrz14\ns_mov_b64 exec,vcc\ns_mov_b32 s97,1\nbuffer_load_dwordx4 v[176:179],v240,s[52:55],s72 offen sc1\ns_mov_b64 exec,-1\n.Lrz14:\nv_max3_u32 v124,v180,v181,v182\nv_max3_u32 v124,v124,v183,v183\n" \
"v_cmp_eq_u32 vcc,-1,v124\ns_add_u32 s72,s95,10485760\ns_cbranch_vccz .Lrz15\ns_mov_b64 exec,vcc\ns_mov_b32 s97,1\nbuffer_load_dwordx4 v[180:183],v240,s[52:55],s72 offen sc1\ns_mov_b64 exec,-1\n.Lrz15:\n" \
"v_max3_u32 v124,v184,v185,v186\nv_max3_u32 v124,v124,v187,v187\nv_cmp_eq_u32 vcc,-1,v124\ns_add_u32 s72,s95,12582912\ns_cbranch_vccz .Lrz16\ns_mov_b64 exec,vcc\ns_mov_b32 s97,1\nbuffer_load_dwordx4 v[184:187],v240,s[52:55],s72 offen sc1\n" \
"s_mov_b64 exec,-1\n.Lrz16:\nv_max3_u32 v124,v188,v189,v190\nv_max3_u32 v124,v124,v191,v191\nv_cmp_eq_u32 vcc,-1,v124\ns_add_u32 s72,s95,14680064\ns_cbranch_vccz .Lrz17\ns_mov_b64 exec,vcc\n" \
"s_mov_b32 s97,1\nbuffer_load_dwordx4 v[188:191],v240,s[52:55],s72 offen sc1\ns_mov_b64 exec,-1\n.Lrz17:\ns_cmp_lg_u32 s97,0\ns_cbranch_scc1 .Lrm1\ns_cmp_eq_u32 s71,0\ns_cbranch_scc1 .Lrb1\n" \
"s_branch .Lrow1\n.Lrm1:\ns_mov_b32 s71,1\ns_waitcnt vmcnt(0)\ns_sub_u32 s73,s73,1\ns_cbranch_scc1 .Lrb1\ns_sleep 2\ns_branch .Lrl1\n" \
".Lrs0:\ns_mov_b32 s71,0\n.Lrl0:\ns_mov_b32 s97,0\nv_max3_u32 v60,v128,v129,v130\nv_max3_u32 v60,v60,v131,v131\nv_cmp_eq_u32 vcc,-1,v60\ns_add_u32 s72,s74,0\n" \
"s_cbranch_vccz .Lrz00\ns_mov_b64 exec,vcc\ns_mov_b32 s97,1\nbuffer_load_dwordx4 v[128:131],v240,s[52:55],s72 offen sc1\ns_mov_b64 exec,-1\n.Lrz00:\nv_max3_u32 v60,v132,v133,v134\nv_max3_u32 v60,v60,v135,v135\n" \
"v_cmp_eq_u32 vcc,-1,v60\ns_add_u32 s72,s74,2097152\ns_cbranch_vccz .Lrz01\ns_mov_b64 exec,vcc\ns_mov_b32 s97,1\nbuffer_load_dwordx4 v[132:135],v240,s[52:55],s72 offen sc1\ns_mov_b64 exec,-1\n.Lrz01:\n" \
"v_max3_u32 v60,v136,v137,v138\nv_max3_u32 v60,v60,v139,v139\nv_cmp_eq_u32 vcc,-1,v60\ns_add_u32 s72,s74,4194304\ns_cbranch_vccz .Lrz02\ns_mov_b64 exec,vcc\ns_mov_b32 s97,1\nbuffer_load_dwordx4 v[136:139],v240,s[52:55],s72 offen sc1\n" \
"s_mov_b64 exec,-1\n.Lrz02:\nv_max3_u32 v60,v140,v141,v142\nv_max3_u32 v60,v60,v143,v143\nv_cmp_eq_u32 vcc,-1,v60\ns_add_u32 s72,s74,6291456\ns_cbranch_vccz .Lrz03\ns_mov_b64 exec,vcc\n" \
"s_mov_b32 s97,1\nbuffer_load_dwordx4 v[140:143],v240,s[52:55],s72 offen sc1\ns_mov_b64 exec,-1\n.Lrz03:\nv_max3_u32 v60,v144,v145,v146\nv_max3_u32 v60,v60,v147,v147\nv_cmp_eq_u32 vcc,-1,v60\ns_add_u32 s72,s74,8388608\n" \
"s_cbranch_vccz .Lrz04\ns_mov_b64 exec,vcc\ns_mov_b32 s97,1\nbuffer_load_dwordx4 v[144:147],v240,s[52:55],s72 offen sc1\ns_mov_b64 exec,-1\n.Lrz04:\nv_max3_u32 v60,v148,v149,v150\nv_max3_u32 v60,v60,v151,v151\n" \
"v_cmp_eq_u32 vcc,-1,v60\ns_add_u32 s72,s74,10485760\ns_cbranch_vccz .Lrz05\ns_mov_b64 exec,vcc\ns_mov_b32 s97,1\nbuffer_load_dwordx4 v[148:151],v240,s[52:55],s72 offen sc1\ns_mov_b64 exec,-1\n.Lrz05:\n" \
"v_max3_u32 v60,v152,v153,v154\nv_max3_u32 v60,v60,v155,v155\nv_cmp_eq_u32 vcc,-1,v60\ns_add_u32 s72,s74,12582912\ns_cbranch_vccz .Lrz06\ns_mov_b64 exec,vcc\ns_mov_b32 s97,1\nbuffer_load_dwordx4 v[152:155],v240,s[52:55],s72 offen sc1\n" \
"s_mov_b64 exec,-1\n.Lrz06:\nv_max3_u32 v60,v156,v157,v158\nv_max3_u32 v60,v60,v159,v159\nv_cmp_eq_u32 vcc,-1,v60\ns_add_u32 s72,s74,14680064\ns_cbranch_vccz .Lrz07\ns_mov_b64 exec,vcc\n" \
"s_mov_b32 s97,1\nbuffer_load_dwordx4 v[156:159],v240,s[52:55],s72 offen sc1\ns_mov_b64 exec,-1\n.Lrz07:\ns_cmp_lg_u32 s97,0\ns_cbranch_scc1 .Lrm0\ns_cmp_eq_u32 s71,0\ns_cbranch_scc1 .Lrb0\n" \
"s_branch .Lrow0\n.Lrm0:\ns_mov_b32 s71,1\ns_waitcnt vmcnt(0)\ns_sub_u32 s73,s73,1\ns_cbranch_scc1 .Lrb0\ns_sleep 2\ns_branch .Lrl0\n" \
".Lredo:\ns_waitcnt vmcnt(0) lgkmcnt(0)\ns_barrier\ns_mov_b64 s[90:91],0\ns_mov_b32 s99,0\ns_mov_b32 s17,1\ns_lshl_b32 s70,s63,19\ns_add_u32 s37,s70,3145728\n" \
"s_lshl_b32 s71,s62,15\ns_add_u32 s72,s37,s64\ns_add_u32 s72,s72,491520\ns_sub_u32 s72,s72,s71\ns_add_u32 m0,s64,0\ns_nop 0\nbuffer_load_dwordx4 v240,s[52:55],s72 offen offset:0 sc1 lds\nbuffer_load_dwordx4 v240,s[52:55],s72 offen offset:1024 sc1 lds\n" \
"buffer_load_dwordx4 v240,s[52:55],s72 offen offset:2048 sc1 lds\nbuffer_load_dwordx4 v240,s[52:55],s72 offen offset:3072 sc1 lds\ns_add_u32 m0,s64,65536\ns_add_u32 s72,s72,16777216\nbuffer_load_dwordx4 v240,s[52:55],s72 offen offset:0 sc1 lds\nbuffer_load_dwordx4 v240,s[52:55],s72 offen offset:1024 sc1 lds\nbuffer_load_dwordx4 v240,s[52:55],s72 offen offset:2048 sc1 lds\nbuffer_load_dwordx4 v240,s[52:55],s72 offen offset:3072 sc1 lds\n" \
"s_add_u32 s72,s37,s64\ns_add_u32 s72,s72,507904\ns_sub_u32 s72,s72,s71\ns_add_u32 m0,s64,16384\ns_nop 0\nbuffer_load_dwordx4 v240,s[52:55],s72 offen offset:0 sc1 lds\nbuffer_load_dwordx4 v240,s[52:55],s72 offen offset:1024 sc1 lds\nbuffer_load_dwordx4 v240,s[52:55],s72 offen offset:2048 sc1 lds\n" \
"buffer_load_dwordx4 v240,s[52:55],s72 offen offset:3072 sc1 lds\ns_add_u32 m0,s64,81920\ns_add_u32 s72,s72,16777216\nbuffer_load_dwordx4 v240,s[52:55],s72 offen offset:0 sc1 lds\nbuffer_load_dwordx4 v240,s[52:55],s72 offen offset:1024 sc1 lds\nbuffer_load_dwordx4 v240,s[52:55],s72 offen offset:2048 sc1 lds\nbuffer_load_dwordx4 v240,s[52:55],s72 offen offset:3072 sc1 lds\ns_add_u32 s72,s37,s64\n" \
"s_add_u32 s72,s72,16384\ns_add_u32 s72,s72,s71\ns_add_u32 m0,s64,32768\ns_nop 0\nbuffer_load_dwordx4 v240,s[52:55],s72 offen offset:0 sc1 lds\nbuffer_load_dwordx4 v240,s[52:55],s72 offen offset:1024 sc1 lds\nbuffer_load_dwordx4 v240,s[52:55],s72 offen offset:2048 sc1 lds\nbuffer_load_dwordx4 v240,s[52:55],s72 offen offset:3072 sc1 lds\n" \
"s_add_u32 m0,s64,98304\ns_add_u32 s72,s72,16777216\nbuffer_load_dwordx4 v240,s[52:55],s72 offen offset:0 sc1 lds\nbuffer_load_dwordx4 v240,s[52:55],s72 offen offset:1024 sc1 lds\nbuffer_load_dwordx4 v240,s[52:55],s72 offen offset:2048 sc1 lds\nbuffer_load_dwordx4 v240,s[52:55],s72 offen offset:3072 sc1 lds\ns_add_u32 s72,s37,s64\ns_add_u32 s72,s72,0\n" \
"s_add_u32 s72,s72,s71\ns_add_u32 m0,s64,49152\ns_nop 0\nbuffer_load_dwordx4 v240,s[52:55],s72 offen offset:0 sc1 lds\nbuffer_load_dwordx4 v240,s[52:55],s72 offen offset:1024 sc1 lds\nbuffer_load_dwordx4 v240,s[52:55],s72 offen offset:2048 sc1 lds\nbuffer_load_dwordx4 v240,s[52:55],s72 offen offset:3072 sc1 lds\ns_add_u32 m0,s64,114688\n" \
"s_add_u32 s72,s72,16777216\nbuffer_load_dwordx4 v240,s[52:55],s72 offen offset:0 sc1 lds\nbuffer_load_dwordx4 v240,s[52:55],s72 offen offset:1024 sc1 lds\nbuffer_load_dwordx4 v240,s[52:55],s72 offen offset:2048 sc1 lds\nbuffer_load_dwordx4 v240,s[52:55],s72 offen offset:3072 sc1 lds\nv_and_b32 v251,15,v249\nv_lshrrev_b32 v250,4,v249\nv_lshlrev_b32 v250,2,v250\n" \
"v_sub_u32 v251,v251,v250\ns_lshl_b32 s70,s62,7\ns_lshl_b32 s72,s7,5\ns_sub_u32 s38,s72,s70\ns_add_u32 s38,s38,1920\ns_sub_u32 s39,s70,s72\ns_add_u32 s39,s39,96\nv_add_u32 v225,s38,v251\n" \
"v_add_u32 v225,0,v225\nv_mov_b32 v224,0xff800000\nv_add_u32 v229,s38,v251\nv_add_u32 v229,16,v229\nv_mov_b32 v228,0xff800000\nv_add_u32 v233,s39,v251\nv_add_u32 v233,0,v233\nv_mov_b32 v232,0xff800000\n" \
"v_add_u32 v237,s39,v251\nv_add_u32 v237,16,v237\nv_mov_b32 v236,0xff800000\ns_sub_u32 s38,524288,s71\ns_add_u32 s38,s38,s37\n.Lrl:\nbuffer_load_dwordx4 a[0:3],v240,s[52:55],s37 offen offset:0 sc1\nbuffer_load_dwordx4 a[4:7],v240,s[52:55],s37 offen offset:1024 sc1\n" \
"buffer_load_dwordx4 a[8:11],v240,s[52:55],s37 offen offset:2048 sc1\nbuffer_load_dwordx4 a[12:15],v240,s[52:55],s37 offen offset:3072 sc1\ns_add_u32 s37,s37,4096\nbuffer_load_dwordx4 a[16:19],v240,s[52:55],s37 offen offset:0 sc1\nbuffer_load_dwordx4 a[20:23],v240,s[52:55],s37 offen offset:1024 sc1\nbuffer_load_dwordx4 a[24:27],v240,s[52:55],s37 offen offset:2048 sc1\nbuffer_load_dwordx4 a[28:31],v240,s[52:55],s37 offen offset:3072 sc1\ns_add_u32 s37,s37,4096\n" \
"buffer_load_dwordx4 a[32:35],v240,s[52:55],s37 offen offset:0 sc1\nbuffer_load_dwordx4 a[36:39],v240,s[52:55],s37 offen offset:1024 sc1\nbuffer_load_dwordx4 a[40:43],v240,s[52:55],s37 offen offset:2048 sc1\nbuffer_load_dwordx4 a[44:47],v240,s[52:55],s37 offen offset:3072 sc1\ns_add_u32 s37,s37,4096\nbuffer_load_dwordx4 a[48:51],v240,s[52:55],s37 offen offset:0 sc1\nbuffer_load_dwordx4 a[52:55],v240,s[52:55],s37 offen offset:1024 sc1\nbuffer_load_dwordx4 a[56:59],v240,s[52:55],s37 offen offset:2048 sc1\n" \
"buffer_load_dwordx4 a[60:63],v240,s[52:55],s37 offen offset:3072 sc1\ns_add_u32 s37,s37,4096\ns_waitcnt vmcnt(15)\nM v[128:131],a[0:3],a[128:131],0\ns_waitcnt vmcnt(14)\nM v[128:131],a[4:7],a[132:135],v[128:131]\ns_waitcnt vmcnt(13)\nM v[128:131],a[8:11],a[136:139],v[128:131]\n" \
"s_waitcnt vmcnt(12)\nM v[128:131],a[12:15],a[140:143],v[128:131]\nM v[132:135],a[0:3],a[144:147],0\nM v[132:135],a[4:7],a[148:151],v[132:135]\nM v[132:135],a[8:11],a[152:155],v[132:135]\nM v[132:135],a[12:15],a[156:159],v[132:135]\nM v[136:139],a[0:3],a[160:163],0\nM v[136:139],a[4:7],a[164:167],v[136:139]\n" \
"M v[136:139],a[8:11],a[168:171],v[136:139]\nM v[136:139],a[12:15],a[172:175],v[136:139]\nM v[140:143],a[0:3],a[176:179],0\nM v[140:143],a[4:7],a[180:183],v[140:143]\nM v[140:143],a[8:11],a[184:187],v[140:143]\nM v[140:143],a[12:15],a[188:191],v[140:143]\ns_waitcnt vmcnt(11)\nM v[144:147],a[16:19],a[128:131],0\n" \
"s_waitcnt vmcnt(10)\nM v[144:147],a[20:23],a[132:135],v[144:147]\ns_waitcnt vmcnt(9)\nM v[144:147],a[24:27],a[136:139],v[144:147]\ns_waitcnt vmcnt(8)\nM v[144:147],a[28:31],a[140:143],v[144:147]\nM v[148:151],a[16:19],a[144:147],0\nM v[148:151],a[20:23],a[148:151],v[148:151]\n" \
"M v[148:151],a[24:27],a[152:155],v[148:151]\nM v[148:151],a[28:31],a[156:159],v[148:151]\nM v[152:155],a[16:19],a[160:163],0\nM v[152:155],a[20:23],a[164:167],v[152:155]\nM v[152:155],a[24:27],a[168:171],v[152:155]\nM v[152:155],a[28:31],a[172:175],v[152:155]\nM v[156:159],a[16:19],a[176:179],0\nM v[156:159],a[20:23],a[180:183],v[156:159]\n" \
"M v[156:159],a[24:27],a[184:187],v[156:159]\nM v[156:159],a[28:31],a[188:191],v[156:159]\ns_waitcnt vmcnt(7)\nM v[160:163],a[32:35],a[128:131],0\ns_waitcnt vmcnt(6)\nM v[160:163],a[36:39],a[132:135],v[160:163]\ns_waitcnt vmcnt(5)\nM v[160:163],a[40:43],a[136:139],v[160:163]\n" \
"s_waitcnt vmcnt(4)\nM v[160:163],a[44:47],a[140:143],v[160:163]\nM v[164:167],a[32:35],a[144:147],0\nM v[164:167],a[36:39],a[148:151],v[164:167]\nM v[164:167],a[40:43],a[152:155],v[164:167]\nM v[164:167],a[44:47],a[156:159],v[164:167]\nM v[168:171],a[32:35],a[160:163],0\nM v[168:171],a[36:39],a[164:167],v[168:171]\n" \
"M v[168:171],a[40:43],a[168:171],v[168:171]\nM v[168:171],a[44:47],a[172:175],v[168:171]\nM v[172:175],a[32:35],a[176:179],0\nM v[172:175],a[36:39],a[180:183],v[172:175]\nM v[172:175],a[40:43],a[184:187],v[172:175]\nM v[172:175],a[44:47],a[188:191],v[172:175]\ns_waitcnt vmcnt(3)\nM v[176:179],a[48:51],a[128:131],0\n" \
"s_waitcnt vmcnt(2)\nM v[176:179],a[52:55],a[132:135],v[176:179]\ns_waitcnt vmcnt(1)\nM v[176:179],a[56:59],a[136:139],v[176:179]\ns_waitcnt vmcnt(0)\nM v[176:179],a[60:63],a[140:143],v[176:179]\nM v[180:183],a[48:51],a[144:147],0\nM v[180:183],a[52:55],a[148:151],v[180:183]\n" \
"M v[180:183],a[56:59],a[152:155],v[180:183]\nM v[180:183],a[60:63],a[156:159],v[180:183]\nM v[184:187],a[48:51],a[160:163],0\nM v[184:187],a[52:55],a[164:167],v[184:187]\nM v[184:187],a[56:59],a[168:171],v[184:187]\nM v[184:187],a[60:63],a[172:175],v[184:187]\nM v[188:191],a[48:51],a[176:179],0\nM v[188:191],a[52:55],a[180:183],v[188:191]\n" \
"M v[188:191],a[56:59],a[184:187],v[188:191]\nM v[188:191],a[60:63],a[188:191],v[188:191]\nv_cmp_le_i32 vcc,0,v225\ns_nop 1\nv_cndmask_b32 v128,v252,v128,vcc\nv_max_f32 v224,v224,v128\nv_cmp_le_i32 vcc,1,v225\ns_nop 1\n" \
"v_cndmask_b32 v129,v252,v129,vcc\nv_max_f32 v224,v224,v129\nv_cmp_le_i32 vcc,2,v225\ns_nop 1\nv_cndmask_b32 v130,v252,v130,vcc\nv_max_f32 v224,v224,v130\nv_cmp_le_i32 vcc,3,v225\ns_nop 1\n" \
"v_cndmask_b32 v131,v252,v131,vcc\nv_max_f32 v224,v224,v131\nv_cmp_le_i32 vcc,0,v229\ns_nop 1\nv_cndmask_b32 v132,v252,v132,vcc\nv_max_f32 v228,v228,v132\nv_cmp_le_i32 vcc,1,v229\ns_nop 1\n" \
"v_cndmask_b32 v133,v252,v133,vcc\nv_max_f32 v228,v228,v133\nv_cmp_le_i32 vcc,2,v229\ns_nop 1\nv_cndmask_b32 v134,v252,v134,vcc\nv_max_f32 v228,v228,v134\nv_cmp_le_i32 vcc,3,v229\ns_nop 1\n" \
"v_cndmask_b32 v135,v252,v135,vcc\nv_max_f32 v228,v228,v135\nv_cmp_le_i32 vcc,0,v233\ns_nop 1\nv_cndmask_b32 v136,v252,v136,vcc\nv_max_f32 v232,v232,v136\nv_cmp_le_i32 vcc,1,v233\ns_nop 1\n" \
"v_cndmask_b32 v137,v252,v137,vcc\nv_max_f32 v232,v232,v137\nv_cmp_le_i32 vcc,2,v233\ns_nop 1\nv_cndmask_b32 v138,v252,v138,vcc\nv_max_f32 v232,v232,v138\nv_cmp_le_i32 vcc,3,v233\ns_nop 1\n" \
"v_cndmask_b32 v139,v252,v139,vcc\nv_max_f32 v232,v232,v139\nv_cmp_le_i32 vcc,0,v237\ns_nop 1\nv_cndmask_b32 v140,v252,v140,vcc\nv_max_f32 v236,v236,v140\nv_cmp_le_i32 vcc,1,v237\ns_nop 1\n" \
"v_cndmask_b32 v141,v252,v141,vcc\nv_max_f32 v236,v236,v141\nv_cmp_le_i32 vcc,2,v237\ns_nop 1\nv_cndmask_b32 v142,v252,v142,vcc\nv_max_f32 v236,v236,v142\nv_cmp_le_i32 vcc,3,v237\ns_nop 1\n" \
"v_cndmask_b32 v143,v252,v143,vcc\nv_max_f32 v236,v236,v143\nv_cmp_le_i32 vcc,16,v225\ns_nop 1\nv_cndmask_b32 v144,v252,v144,vcc\nv_max_f32 v224,v224,v144\nv_cmp_le_i32 vcc,17,v225\ns_nop 1\n" \
"v_cndmask_b32 v145,v252,v145,vcc\nv_max_f32 v224,v224,v145\nv_cmp_le_i32 vcc,18,v225\ns_nop 1\nv_cndmask_b32 v146,v252,v146,vcc\nv_max_f32 v224,v224,v146\nv_cmp_le_i32 vcc,19,v225\ns_nop 1\n" \
"v_cndmask_b32 v147,v252,v147,vcc\nv_max_f32 v224,v224,v147\nv_cmp_le_i32 vcc,16,v229\ns_nop 1\nv_cndmask_b32 v148,v252,v148,vcc\nv_max_f32 v228,v228,v148\nv_cmp_le_i32 vcc,17,v229\ns_nop 1\n" \
"v_cndmask_b32 v149,v252,v149,vcc\nv_max_f32 v228,v228,v149\nv_cmp_le_i32 vcc,18,v229\ns_nop 1\nv_cndmask_b32 v150,v252,v150,vcc\nv_max_f32 v228,v228,v150\nv_cmp_le_i32 vcc,19,v229\ns_nop 1\n" \
"v_cndmask_b32 v151,v252,v151,vcc\nv_max_f32 v228,v228,v151\nv_cmp_le_i32 vcc,16,v233\ns_nop 1\nv_cndmask_b32 v152,v252,v152,vcc\nv_max_f32 v232,v232,v152\nv_cmp_le_i32 vcc,17,v233\ns_nop 1\n" \
"v_cndmask_b32 v153,v252,v153,vcc\nv_max_f32 v232,v232,v153\nv_cmp_le_i32 vcc,18,v233\ns_nop 1\nv_cndmask_b32 v154,v252,v154,vcc\nv_max_f32 v232,v232,v154\nv_cmp_le_i32 vcc,19,v233\ns_nop 1\n" \
"v_cndmask_b32 v155,v252,v155,vcc\nv_max_f32 v232,v232,v155\nv_cmp_le_i32 vcc,16,v237\ns_nop 1\nv_cndmask_b32 v156,v252,v156,vcc\nv_max_f32 v236,v236,v156\nv_cmp_le_i32 vcc,17,v237\ns_nop 1\n" \
"v_cndmask_b32 v157,v252,v157,vcc\nv_max_f32 v236,v236,v157\nv_cmp_le_i32 vcc,18,v237\ns_nop 1\nv_cndmask_b32 v158,v252,v158,vcc\nv_max_f32 v236,v236,v158\nv_cmp_le_i32 vcc,19,v237\ns_nop 1\n" \
"v_cndmask_b32 v159,v252,v159,vcc\nv_max_f32 v236,v236,v159\nv_cmp_le_i32 vcc,32,v225\ns_nop 1\nv_cndmask_b32 v160,v252,v160,vcc\nv_max_f32 v224,v224,v160\nv_cmp_le_i32 vcc,33,v225\ns_nop 1\n" \
"v_cndmask_b32 v161,v252,v161,vcc\nv_max_f32 v224,v224,v161\nv_cmp_le_i32 vcc,34,v225\ns_nop 1\nv_cndmask_b32 v162,v252,v162,vcc\nv_max_f32 v224,v224,v162\nv_cmp_le_i32 vcc,35,v225\ns_nop 1\n" \
"v_cndmask_b32 v163,v252,v163,vcc\nv_max_f32 v224,v224,v163\nv_cmp_le_i32 vcc,32,v229\ns_nop 1\nv_cndmask_b32 v164,v252,v164,vcc\nv_max_f32 v228,v228,v164\nv_cmp_le_i32 vcc,33,v229\ns_nop 1\n" \
"v_cndmask_b32 v165,v252,v165,vcc\nv_max_f32 v228,v228,v165\nv_cmp_le_i32 vcc,34,v229\ns_nop 1\nv_cndmask_b32 v166,v252,v166,vcc\nv_max_f32 v228,v228,v166\nv_cmp_le_i32 vcc,35,v229\ns_nop 1\n" \
"v_cndmask_b32 v167,v252,v167,vcc\nv_max_f32 v228,v228,v167\nv_cmp_le_i32 vcc,32,v233\ns_nop 1\nv_cndmask_b32 v168,v252,v168,vcc\nv_max_f32 v232,v232,v168\nv_cmp_le_i32 vcc,33,v233\ns_nop 1\n" \
"v_cndmask_b32 v169,v252,v169,vcc\nv_max_f32 v232,v232,v169\nv_cmp_le_i32 vcc,34,v233\ns_nop 1\nv_cndmask_b32 v170,v252,v170,vcc\nv_max_f32 v232,v232,v170\nv_cmp_le_i32 vcc,35,v233\ns_nop 1\n" \
"v_cndmask_b32 v171,v252,v171,vcc\nv_max_f32 v232,v232,v171\nv_cmp_le_i32 vcc,32,v237\ns_nop 1\nv_cndmask_b32 v172,v252,v172,vcc\nv_max_f32 v236,v236,v172\nv_cmp_le_i32 vcc,33,v237\ns_nop 1\n" \
"v_cndmask_b32 v173,v252,v173,vcc\nv_max_f32 v236,v236,v173\nv_cmp_le_i32 vcc,34,v237\ns_nop 1\nv_cndmask_b32 v174,v252,v174,vcc\nv_max_f32 v236,v236,v174\nv_cmp_le_i32 vcc,35,v237\ns_nop 1\n" \
"v_cndmask_b32 v175,v252,v175,vcc\nv_max_f32 v236,v236,v175\nv_cmp_le_i32 vcc,48,v225\ns_nop 1\nv_cndmask_b32 v176,v252,v176,vcc\nv_max_f32 v224,v224,v176\nv_cmp_le_i32 vcc,49,v225\ns_nop 1\n" \
"v_cndmask_b32 v177,v252,v177,vcc\nv_max_f32 v224,v224,v177\nv_cmp_le_i32 vcc,50,v225\ns_nop 1\nv_cndmask_b32 v178,v252,v178,vcc\nv_max_f32 v224,v224,v178\nv_cmp_le_i32 vcc,51,v225\ns_nop 1\n" \
"v_cndmask_b32 v179,v252,v179,vcc\nv_max_f32 v224,v224,v179\nv_cmp_le_i32 vcc,48,v229\ns_nop 1\nv_cndmask_b32 v180,v252,v180,vcc\nv_max_f32 v228,v228,v180\nv_cmp_le_i32 vcc,49,v229\ns_nop 1\n" \
"v_cndmask_b32 v181,v252,v181,vcc\nv_max_f32 v228,v228,v181\nv_cmp_le_i32 vcc,50,v229\ns_nop 1\nv_cndmask_b32 v182,v252,v182,vcc\nv_max_f32 v228,v228,v182\nv_cmp_le_i32 vcc,51,v229\ns_nop 1\n" \
"v_cndmask_b32 v183,v252,v183,vcc\nv_max_f32 v228,v228,v183\nv_cmp_le_i32 vcc,48,v233\ns_nop 1\nv_cndmask_b32 v184,v252,v184,vcc\nv_max_f32 v232,v232,v184\nv_cmp_le_i32 vcc,49,v233\ns_nop 1\n" \
"v_cndmask_b32 v185,v252,v185,vcc\nv_max_f32 v232,v232,v185\nv_cmp_le_i32 vcc,50,v233\ns_nop 1\nv_cndmask_b32 v186,v252,v186,vcc\nv_max_f32 v232,v232,v186\nv_cmp_le_i32 vcc,51,v233\ns_nop 1\n" \
"v_cndmask_b32 v187,v252,v187,vcc\nv_max_f32 v232,v232,v187\nv_cmp_le_i32 vcc,48,v237\ns_nop 1\nv_cndmask_b32 v188,v252,v188,vcc\nv_max_f32 v236,v236,v188\nv_cmp_le_i32 vcc,49,v237\ns_nop 1\n" \
"v_cndmask_b32 v189,v252,v189,vcc\nv_max_f32 v236,v236,v189\nv_cmp_le_i32 vcc,50,v237\ns_nop 1\nv_cndmask_b32 v190,v252,v190,vcc\nv_max_f32 v236,v236,v190\nv_cmp_le_i32 vcc,51,v237\ns_nop 1\n" \
"v_cndmask_b32 v191,v252,v191,vcc\nv_max_f32 v236,v236,v191\nv_subrev_u32 v225,64,v225\nv_subrev_u32 v229,64,v229\nv_subrev_u32 v233,64,v233\nv_subrev_u32 v237,64,v237\ns_cmp_lt_u32 s37,s38\ns_cbranch_scc1 .Lrl\n" \
"s_waitcnt vmcnt(0)\ns_barrier\nds_read_b128 a[0:3],v240 offset:0\nds_read_b128 a[4:7],v240 offset:1024\nds_read_b128 a[8:11],v240 offset:2048\nds_read_b128 a[12:15],v240 offset:3072\nds_read_b128 a[16:19],v240 offset:4096\nds_read_b128 a[20:23],v240 offset:5120\n" \
"ds_read_b128 a[24:27],v240 offset:6144\nds_read_b128 a[28:31],v240 offset:7168\nds_read_b128 a[32:35],v240 offset:8192\nds_read_b128 a[36:39],v240 offset:9216\nds_read_b128 a[40:43],v240 offset:10240\nds_read_b128 a[44:47],v240 offset:11264\nds_read_b128 a[48:51],v240 offset:12288\nds_read_b128 a[52:55],v240 offset:13312\n" \
"ds_read_b128 a[56:59],v240 offset:14336\nds_read_b128 a[60:63],v240 offset:15360\ns_cmp_eq_u32 s62,0\ns_cselect_b32 s36,0x8000,0\ns_add_u32 s36,s36,s64\ns_cmp_eq_u32 s36,s96\ns_cselect_b32 s72,0xc000,s98\ns_add_u32 s65,s36,s72\n" \
"s_cmp_eq_u32 s65,s96\ns_cselect_b32 s72,0xc000,s98\ns_add_u32 s66,s65,s72\ns_cmp_eq_u32 s66,s96\ns_cselect_b32 s72,0xc000,s98\ns_add_u32 s95,s66,s72\ns_add_u32 s97,s96,65536\ns_add_u32 s15,s64,s46\n" \
"s_add_u32 s15,s15,16384\ns_mov_b32 s94,0xc000\ns_cmp_eq_u32 s62,7\ns_cselect_b32 s72,98304,0\ns_sub_u32 s97,s97,s72\nv_and_b32 v251,31,v249\nv_lshlrev_b32 v251,2,v251\ns_lshl_b32 s80,s63,7\n" \
"s_add_u32 s80,s80,86016\nv_add_u32 v250,0xc000,v241\ns_waitcnt lgkmcnt(0)\ns_branch .Lattn\n.Lfin:\n" \
""
typedef __attribute__((address_space(1))) unsigned gu32;
typedef __attribute__((address_space(1))) unsigned long long gu64;
#define RLX_AGENT __ATOMIC_RELAXED, __HIP_MEMORY_SCOPE_AGENT
constexpr size_t WS_HO = 65536, WS_DBG = (size_t)51 << 20, WS_PART = (size_t)52 << 20;
constexpr unsigned HO_FP = 8192, HO_P2 = 32768, HO_KR = HO_P2 + 224 * DM * 4, LDS_BYTES = 163840, WX = 131072, SPIN_CAP = 1u << 20;
#define PSLEEP 4
struct Args {
    const u16 *x, *ckv, *w_q, *w_uk, *w_uv, *w_o, *w_r, *wg, *wu, *wd, *cvec, *w_mod;
    char* ws; u16* out;
};
typedef const __attribute__((address_space(4))) Args* kargs_t;
DI void st16_dev(void* p, const u32x4& v) { asm volatile("global_store_dwordx4 %0, %1, off sc1\n\ts_nop 1" :: "v"(p), "v"(v) : "memory"); }
DI void ld16_dev(u32x4& v, const void* p) { asm volatile("global_load_dwordx4 %0, %1, off sc1" : "=v"(v) : "v"(p) : "memory"); }
DI void sum8(const unsigned* w, float& lo, float& hi) {
    lo = ((lo2f(w[0]) + lo2f(w[1])) + (lo2f(w[2]) + lo2f(w[3]))) + ((lo2f(w[4]) + lo2f(w[5])) + (lo2f(w[6]) + lo2f(w[7])));
    hi = ((hi2f(w[0]) + hi2f(w[1])) + (hi2f(w[2]) + hi2f(w[3]))) + ((hi2f(w[4]) + hi2f(w[5])) + (hi2f(w[6]) + hi2f(w[7])));
}
DI unsigned ord16(float r) { const unsigned u = __float_as_uint(r) >> 16; return (r != r) ? 0u : ((u & 0x8000u) ? (~u & 0xffffu) : (u | 0x8000u)); }
DI float ord16_val(unsigned o) { return __uint_as_float(((o & 0x8000u) ? (o & 0x7fffu) : (~o & 0xffffu)) << 16); }
#define STAMPD(i) do {} while (0)
#define C8(p, a) p #a "0", p #a "1", p #a "2", p #a "3", p #a "4", p #a "5", p #a "6", p #a "7", p #a "8", p #a "9"
#define C100(p, a) C8(p, a##0), C8(p, a##1), C8(p, a##2), C8(p, a##3), C8(p, a##4), C8(p, a##5), C8(p, a##6), C8(p, a##7), C8(p, a##8), C8(p, a##9)
#define C10_99(p) C8(p, 1), C8(p, 2), C8(p, 3), C8(p, 4), C8(p, 5), C8(p, 6), C8(p, 7), C8(p, 8), C8(p, 9)
#define C200_249(p) C8(p, 20), C8(p, 21), C8(p, 22), C8(p, 23), C8(p, 24)
#define BODY_CLOB "memory", "vcc", "scc", "s2", "s3", "s8", "s9", C8("s", 1), C8("s", 2), "s30", "s31", "s33", "s0", "s1", "s34", "s35", "s36", "s37", "s38", "s39", C8("s", 4), C8("s", 5), C8("s", 6), C8("s", 7), C8("s", 8), C8("s", 9), \
    C8("v", ), C10_99("v"), C8("v", 10), "v110", "v111", C8("v", 12), C8("v", 13), C8("v", 14), C8("v", 15), C8("v", 16), C8("v", 17), C8("v", 18), C8("v", 19), C8("v", 20), "v210", "v211", "v212", "v213", "v214", "v215", "v228", "v229", C8("v", 23), C8("v", 24), "v250", "v251", "v252", "v253", "v254", \
    "a16", "a17", "a18", "a19", C8("a", 2), C8("a", 3), C8("a", 4), C8("a", 5), C8("a", 6), C8("a", 7), C8("a", 8), C8("a", 9), C100("a", 1), C8("a", 20), "a210", "a211", "a244", "a245", "a246", "a247", "a248", "a249", "a250", "a251", "a252", "a253", "a254", "a255"
typedef unsigned u32x8 __attribute__((ext_vector_type(8)));

__global__ __launch_bounds__(256, 1) void k_all(const Args a_) {
    extern __shared__ __attribute__((aligned(16))) char lds[];
    kargs_t ap = (kargs_t)__builtin_amdgcn_kernarg_segment_ptr(); unsigned bid = blockIdx.x, wid = rfl(threadIdx.x >> 6);
    u32x16 FG, FU, fd; u32x8 MPR; u32x4 ooL, ooH, KY; unsigned sv_;
    asm volatile(BODY_ASM : "+{s[4:5]}"(ap), "+{s6}"(bid), "+{s7}"(wid), "={a[212:227]}"(FG), "={a[228:243]}"(FU), "={a[0:15]}"(fd), "={v[216:219]}"(ooL), "={v[220:223]}"(ooH), "={v[224:227]}"(KY), "={v[112:119]}"(MPR), "={v255}"(sv_) :: BODY_CLOB);
    bid = rfl(bid); wid = rfl(wid);
    asm volatile("" : "+v"(FG), "+v"(FU), "+v"(fd));
    char* const ws = ap->ws; char* const ho = ws + WS_HO; const unsigned lane = lane_id();
    const unsigned g = bid & 7u, idx = bid >> 3, j = idx & 3u, xx = idx >> 2, head = 4u * g + j, tid = wid * 64u + lane;
    const u16* __restrict__ x = ap->x; u16* const out = ap->out; const char* const part = ws + WS_PART;
    const unsigned rowL = xx * 128u + 4u * head + wid, rowH = (15u - xx) * 128u + 4u * head + wid;
    const bool ffn = idx - 16u < 14u; const unsigned fb = g * 14u + idx - 16u;
    const unsigned c0 = wid * 128u + (lane & 3u) * 32u + (lane >> 2);
    const unsigned kwl = KY[0], kwg = KY[1], kL = KY[2], kH = KY[3];
    u32x2* sk = (u32x2*)lds;
    float* red = (float*)(lds + 1024);
    float* red3 = (float*)(lds + 2048);
    float* fin = (float*)(lds + 4096);
    if (wid == 0) {
        unsigned kh = 0, kl = 0;
        for (unsigned spins = 0; spins < SPIN_CAP; ++spins) {
            u32x4 ka, kb; ld16_dev(ka, ho + HO_KR + g * 2048u + lane * 32u); ld16_dev(kb, ho + HO_KR + g * 2048u + lane * 32u + 16u);
            asm volatile("s_waitcnt vmcnt(0)" : "+v"(ka), "+v"(kb) :: "memory");
            if (lane == (bid >> 2)) { const unsigned o = bid & 3u;
                if (o == 0) { ka[0] = kwl; ka[1] = kwg; } if (o == 1) { ka[2] = kwl; ka[3] = kwg; } if (o == 2) { kb[0] = kwl; kb[1] = kwg; } if (o == 3) { kb[2] = kwl; kb[3] = kwg; } }
            const u32x2 p = ka[1] > ka[3] ? (u32x2){ka[0], ka[1]} : (u32x2){ka[2], ka[3]}, q = kb[1] > kb[3] ? (u32x2){kb[0], kb[1]} : (u32x2){kb[2], kb[3]};
            kh = p[1] > q[1] ? p[1] : q[1]; kl = p[1] > q[1] ? p[0] : q[0];
            if (__all(ka[1] != 0u && ka[3] != 0u && kb[1] != 0u && kb[3] != 0u)) break;
            __builtin_amdgcn_s_sleep(PSLEEP);
        }
        const unsigned mh = wave_umax(kh);
        if (kh == mh) sk[4] = (u32x2){kl, kh};
    }
    LDSBAR();
    const u32x2 key = sk[4];
    const unsigned sel = (2047u - ((key[1] >> 5) & 2047u)) & 2047u;
    STAMPD(14);
    unsigned h2w;
    {   unsigned p[8];
#pragma unroll
        for (int zz = 0; zz < 8; ++zz) p[zz] = __hip_atomic_load((gu32*)(part + ((size_t)zz * SEQ + sel) * (DM * 2) + 4u * tid), RLX_AGENT);
        const unsigned xa = x[(size_t)sel * DM + c0], xb = x[(size_t)sel * DM + c0 + 16u];
        if (key[1] != kwg) {
            if (kL == kwg) st16_dev(out + (size_t)rowL * DM + lane * 8, ooL);
            if (kH == kwg) st16_dev(out + (size_t)rowH * DM + lane * 8, ooH); }
        float a0, a1; sum8(p, a0, a1);
        const float m = hi2f(key[0]), rden = __builtin_amdgcn_rcpf(lo2f(key[0]));
#define MP(i) __uint_as_float(MPR[i])
        const float x10 = rb(lo2f(xa) + rb(MP(0) * rb(a0))), x11 = rb(lo2f(xb) + rb(MP(1) * rb(a1)));
        h2w = cvt2(rb(rb(rb(x10 - m) * rden) * rb(1.f + MP(4))) + MP(2), rb(rb(rb(x11 - m) * rden) * rb(1.f + MP(5))) + MP(3));
        if (bid < 64u) { fin[c0] = x10; fin[c0 + 16u] = x11; fin[512u + c0] = lo2f(h2w); fin[528u + c0] = hi2f(h2w); fin[1024u + c0] = MP(6); fin[1040u + c0] = MP(7); } }
    if (ffn) {
        float v[8] = {};
#define QB(c) (unsigned)__builtin_amdgcn_update_dpp(0, (int)h2w, c, 0xF, 0xF, true)
        const unsigned hq4[4] = {QB(0x00), QB(0x55), QB(0xAA), QB(0xFF)};
#pragma unroll
        for (int q = 0; q < 4; ++q) { const unsigned hq = hq4[q];
#pragma unroll
            for (int e = 0; e < 2; ++e) {
                v[2 * e] = dot2bf(__builtin_amdgcn_perm(FG[4 * q + 2 + e], FG[4 * q + e], 0x05040100u), hq, v[2 * e]); v[2 * e + 1] = dot2bf(__builtin_amdgcn_perm(FG[4 * q + 2 + e], FG[4 * q + e], 0x07060302u), hq, v[2 * e + 1]);
                v[4 + 2 * e] = dot2bf(__builtin_amdgcn_perm(FU[4 * q + 2 + e], FU[4 * q + e], 0x05040100u), hq, v[4 + 2 * e]); v[5 + 2 * e] = dot2bf(__builtin_amdgcn_perm(FU[4 * q + 2 + e], FU[4 * q + e], 0x07060302u), hq, v[5 + 2 * e]); } }
        float v4[4], v2[2];
#pragma unroll
        for (int i = 0; i < 4; ++i) v4[i] = swap32_sum(v[i], v[4 + i]);
#pragma unroll
        for (int i = 0; i < 2; ++i) { float s = swap16_sum(v4[i], v4[2 + i]); s += DPPF(s, 0x124); s += DPPF(s, 0x128); v2[i] = s; }
        if ((lane & 12u) == 0) *reinterpret_cast<f32x2*>(red + wid * 32u + (lane >> 5) * 16u + (lane & 3u) * 4u + ((lane >> 4) & 1u) * 2u) = (f32x2){v2[0], v2[1]};
        LDSBAR();
        const unsigned f = lane & 15u;
        const float gg = rb((red[f] + red[32 + f]) + (red[64 + f] + red[96 + f])), uu = rb((red[16 + f] + red[48 + f]) + (red[80 + f] + red[112 + f]));
        const float gu = rb(rb(gg * __builtin_amdgcn_rcpf(1.f + __expf(-gg))) * uu);
        float a0 = 0.f, a1 = 0.f;
#pragma unroll
        for (int i = 0; i < 16; ++i) { const float gv = __builtin_bit_cast(float, __builtin_amdgcn_readlane(__builtin_bit_cast(int, gu), i)); a0 = fmaf(gv, lo2f(fd[i]), a0); a1 = fmaf(gv, hi2f(fd[i]), a1); }
        const unsigned long long o = ((unsigned long long)min(__float_as_uint(a1), 0xfffffffeu) << 32) | min(__float_as_uint(a0), 0xfffffffeu);
        __hip_atomic_store((gu64*)(ho + HO_P2) + fb * 256u + tid, o, RLX_AGENT);
    }
    STAMPD(15);
    if (bid < 64u) {
        const unsigned cg = g * 8u + idx;
        const char* gp = ho + HO_P2 + (tid < 224u ? tid >> 1 : 0u) * 2048u + cg * 32u + (tid & 1u) * 16u;
        u32x4 ga_ = {};
        for (unsigned spins = 0; spins < SPIN_CAP; ++spins) {
            ld16_dev(ga_, gp);
            asm volatile("s_waitcnt vmcnt(0)" : "+v"(ga_) :: "memory");
            if (__all(max(max(ga_[0], ga_[1]), max(ga_[2], ga_[3])) != 0xffffffffu)) break;
            __builtin_amdgcn_s_sleep(PSLEEP);
        }
        float s4[4];
#pragma unroll
        for (int i = 0; i < 4; ++i) { float s = tid < 224u ? __uint_as_float(ga_[i]) : 0.f;
            s += DPPF(s, 0x4E); s += DPPF(s, 0x124); s += DPPF(s, 0x128);
            s = swap16_sum(s, s); s4[i] = swap32_sum(s, s); }
        if (lane < 2) *reinterpret_cast<f32x4*>(red3 + wid * 8u + lane * 4u) = (f32x4){s4[0], s4[1], s4[2], s4[3]};
        LDSBAR();
        if (tid < 4u) { const unsigned t = tid, cc = cg * 8u + 2u * t;
            const float s0 = (red3[2 * t] + red3[8 + 2 * t]) + (red3[16 + 2 * t] + red3[24 + 2 * t]), s1 = (red3[2 * t + 1] + red3[9 + 2 * t]) + (red3[17 + 2 * t] + red3[25 + 2 * t]);
            const float rsel = rb(1.f / (1.f + __expf(-ord16_val(key[1] >> 16))));
            const float y0 = rb(rb(s0) * rsel), y1 = rb(rb(s1) * rsel);
            const unsigned ow = cvt2(fin[cc] + rb(fin[1024u + cc] * rb(fin[512u + cc] + y0)), fin[cc + 1u] + rb(fin[1025u + cc] * rb(fin[513u + cc] + y1)));
            __hip_atomic_store((gu32*)(out + (size_t)sel * DM + cc), ow, RLX_AGENT);
        }
    }
    asm volatile(".Lcode_e:");
}
extern "C" void kernel_launch(void* const* d_in, const int* in_sizes, int n_in, void* d_out, int out_size, void* d_ws, size_t ws_size, hipStream_t stream) {
    Args a;
    a.x = (const u16*)d_in[0]; a.ckv = (const u16*)d_in[1]; a.w_q = (const u16*)d_in[2]; a.w_uk = (const u16*)d_in[3]; a.w_uv = (const u16*)d_in[4]; a.w_o = (const u16*)d_in[5];
    a.w_r = (const u16*)d_in[7]; a.wg = (const u16*)d_in[8]; a.wu = (const u16*)d_in[9]; a.wd = (const u16*)d_in[10]; a.cvec = (const u16*)d_in[11]; a.w_mod = (const u16*)d_in[12];
    a.ws = (char*)d_ws;
    a.out = (u16*)d_out;
    (void)hipFuncSetAttribute((const void*)k_all, hipFuncAttributeMaxDynamicSharedMemorySize, LDS_BYTES);
    k_all<<<256, 256, LDS_BYTES, stream>>>(a);
}
```

```cpp
#include <hip/hip_runtime.h>
#include <hip/hip_bf16.h>
#include <stdint.h>
typedef unsigned short u16;
#define DI __device__ __forceinline__
DI unsigned lane_id() { unsigned l; asm volatile("v_mbcnt_lo_u32_b32 %0, -1, 0\n\tv_mbcnt_hi_u32_b32 %0, -1, %0" : "=v"(l)); return l; }
DI unsigned rfl(unsigned x) { return __builtin_amdgcn_readfirstlane(x); }
#define BODY_ASM ".macro M a,b,c,d\nv_mfma_f32_16x16x32_bf16 \\a,\\b,\\c,\\d\n.endm\n" \
".Lcode_b:\ns_load_dwordx16 s[8:23],s[4:5],0x0\ns_load_dwordx8 s[24:31],s[4:5],0x40\ns_load_dwordx2 s[34:35],s[4:5],0x60\nv_mbcnt_lo_u32_b32 v249,-1,0\nv_mbcnt_hi_u32_b32 v249,-1,v249\ns_getpc_b64 s[56:57]\n.Lpc0:\n" \
"s_and_b32 s57,s57,0xffff\ns_mov_b32 s58,.Lcode_e-.Lpc0\ns_mov_b32 s59,0x20000\ns_lshr_b32 s70,s6,3\nv_and_b32 v251,1,v249\ns_lshl2_add_u32 s78,s70,s7\nv_lshlrev_b32 v251,14,v251\ns_lshl_b32 s78,s78,7\n" \
"buffer_load_dword v247,v251,s[56:59],s78 offen\ns_bfe_u32 s61,s6,0x20003\ns_lshl_b32 s64,s7,12\ns_and_b32 s60,s6,7\ns_lshr_b32 s62,s6,5\ns_lshl2_add_u32 s63,s60,s61\nv_lshlrev_b32 v240,4,v249\ns_cmp_lt_u32 s6,192\n" \
"s_cselect_b32 s70,s6,0\ns_mul_i32 s71,s70,171\ns_lshr_b32 s71,s71,13\ns_mul_i32 s72,s71,48\ns_sub_u32 s70,s70,s72\ns_lshl2_add_u32 s72,s71,s7\ns_lshl_b32 s72,s72,5\nv_lshrrev_b32 v0,3,v249\n" \
"v_lshl_add_u32 v1,v0,2,s72\nv_and_b32 v2,7,v249\nv_mul_u32_u24 v3,6144,v1\nv_lshl_add_u32 v3,v2,4,v3\ns_lshl_b32 s72,s70,7\ns_add_u32 s72,s72,3072\nv_add_u32 v3,s72,v3\nv_add_u32 v4,12288,v3\n" \
"v_lshlrev_b32 v1,1,v1\ns_mul_i32 s71,s71,12288\ns_lshl_b32 s70,s70,8\ns_add_u32 s79,s70,s71\ns_add_u32 s79,s79,786432\ns_cmp_eq_u32 s7,0\ns_cselect_b32 s75,0,0x8000000\ns_cmp_lt_u32 s6,192\n" \
"s_cselect_b32 s70,s75,0x8000000\ns_or_b32 s79,s79,s70\ns_lshl_b32 s70,s62,7\ns_xor_b32 s71,s63,12\ns_lshl3_add_u32 s71,s71,s70\ns_lshl_b32 s72,s63,3\ns_sub_u32 s72,s72,s70\ns_add_u32 s72,s72,1792\n" \
"s_cmp_lt_u32 s63,16\ns_cselect_b32 s71,s71,s72\ns_lshl_b32 s71,s71,10\ns_lshl_b32 s72,s7,7\ns_add_u32 s71,s71,s72\nv_lshlrev_b32 v5,10,v2\nv_lshl_add_u32 v5,v0,4,v5\nv_add_u32 v5,s71,v5\n" \
"v_bfe_u32 v6,v249,2,3\nv_lshrrev_b32 v7,5,v249\nv_and_b32 v8,48,v240\nv_lshlrev_b32 v6,13,v6\nv_lshl_or_b32 v7,v7,6,v8\nv_or_b32 v6,0x1000,v6\nv_or_b32 v243,v6,v7\nv_add_u32 v244,0xfffffc80,v243\n" \
"v_xor_b32 v6,32,v243\nv_add_u32 v245,63488,v6\nv_add_u32 v246,62592,v6\nv_lshrrev_b32 v6,4,v249\nv_xor_b32 v7,v6,v2\nv_lshlrev_b32 v7,4,v7\nv_lshl_or_b32 v224,v0,10,v7\nv_xor_b32 v7,64,v224\n" \
"v_add_u32 v225,7168,v7\nv_add_u32 v226,14336,v224\nv_add_u32 v227,21504,v7\ns_lshl_b32 s70,s62,1\ns_sub_u32 s71,s7,s70\ns_add_u32 s71,s71,30\ns_sub_u32 s72,s70,s7\ns_add_u32 s72,s72,3\n" \
"s_cmp_lt_u32 s7,2\ns_cselect_b32 s74,s71,s72\ns_lshl_b32 s66,s74,16\ns_mul_i32 s65,s7,24576\ns_lshl_b32 s68,s7,17\ns_lshl_b32 s70,s63,8\ns_add_u32 s68,s68,s70\ns_mov_b32 s38,2097152\n" \
"s_mov_b32 s39,0x20000\ns_mov_b32 s42,4198400\ns_mov_b32 s43,0x20000\ns_mov_b32 s54,71303168\ns_mov_b32 s55,0x20000\ns_mov_b32 s70,94208\ns_waitcnt lgkmcnt(0)\ns_mov_b64 s[52:53],s[34:35]\n" \
"s_mov_b64 s[36:37],s[10:11]\ns_sub_u32 s40,s14,0x1000\ns_subb_u32 s41,s15,0\nbuffer_load_dwordx2 v[230:231],off,s[52:55],s70 sc1\nglobal_load_dwordx2 v[10:11],v1,s[28:29]\nglobal_load_dwordx4 v[12:15],v3,s[30:31] offset:-3072\nglobal_load_dwordx4 v[16:19],v3,s[30:31] offset:3072\nglobal_load_dwordx4 v[20:23],v4,s[30:31] offset:-3072\n" \
"global_load_dwordx4 v[24:27],v4,s[30:31] offset:3072\nglobal_load_dwordx4 v[128:131],v5,s[8:9]\nglobal_load_dwordx4 v[132:135],v5,s[8:9] offset:512\ns_add_u32 m0,s64,98304\ns_add_u32 s69,s68,0\nbuffer_load_dwordx4 v243,s[40:43],s69 offen offset:0 lds\nbuffer_load_dwordx4 v244,s[40:43],s69 offen offset:1024 lds\nbuffer_load_dwordx4 v245,s[40:43],s69 offen offset:2048 lds\n" \
"buffer_load_dwordx4 v246,s[40:43],s69 offen offset:3072 lds\ns_add_u32 m0,s65,0\ns_add_u32 s67,s66,0\nbuffer_load_dwordx4 v224,s[36:39],s67 offen offset:0 lds\nbuffer_load_dwordx4 v225,s[36:39],s67 offen offset:1024 lds\nbuffer_load_dwordx4 v226,s[36:39],s67 offen offset:2048 lds\nbuffer_load_dwordx4 v227,s[36:39],s67 offen offset:3072 lds\ns_add_u32 m0,s65,4096\n" \
"s_add_u32 s67,s66,32768\nbuffer_load_dwordx4 v224,s[36:39],s67 offen offset:0 lds\nbuffer_load_dwordx4 v225,s[36:39],s67 offen offset:1024 lds\nbuffer_load_dwordx4 v226,s[36:39],s67 offen offset:2048 lds\nbuffer_load_dwordx4 v227,s[36:39],s67 offen offset:3072 lds\ns_lshl_b32 s70,s6,11\ns_cmp_lt_u32 s6,112\ns_cselect_b32 s70,s70,0x8000000\n" \
"s_lshl_b32 s71,s7,9\ns_add_u32 s70,s70,s71\ns_add_u32 s70,s70,98304\nv_mov_b32 v28,-1\nv_mov_b32 v29,-1\nv_lshlrev_b32 v30,3,v249\nbuffer_store_dwordx2 v[28:29],v30,s[52:55],s70 offen sc1\ns_lshl_b32 s70,s6,7\n" \
"s_cmp_lt_u32 s6,224\ns_cselect_b32 s71,65536,0x40000000\ns_cmp_lt_u32 s6,128\ns_cselect_b32 s72,556928,0x40000000\nv_mov_b32 v31,s71\nv_mov_b32 v32,s72\nv_cmp_lt_u32 vcc,31,v249\ns_or_b32 s70,s70,s75\n" \
"v_mov_b32 v33,0\nv_cndmask_b32 v31,v31,v32,vcc\nv_lshl_add_u32 v31,v249,2,v31\nbuffer_store_dword v33,v31,s[52:55],s70 offen sc1\nv_and_b32 v34,15,v249\nv_lshrrev_b32 v35,1,v34\nv_xor_b32 v35,v35,v6\nv_lshlrev_b32 v35,4,v35\n" \
"v_lshl_or_b32 v35,v34,7,v35\nv_add_u32 v228,s65,v35\nv_xor_b32 v229,64,v228\nv_bfe_u32 v34,v249,2,2\nv_and_b32 v35,3,v249\nv_lshlrev_b32 v35,3,v35\nv_lshl_or_b32 v35,v34,6,v35\nv_lshl_or_b32 v35,v6,11,v35\n" \
"v_and_b32 v34,16,v249\nv_lshl_or_b32 v35,v34,1,v35\nv_add_u32 v241,98304,v35\nv_xor_b32 v242,32,v241\ns_mov_b32 s73,0x400000\nv_mov_b32 v252,0xff800000\ns_mov_b32 s70,0x05040100\ns_mov_b32 s71,0x07060302\n" \
"s_waitcnt vmcnt(20)\nv_lshlrev_b32 v36,16,v10\nv_and_b32 v37,0xffff0000,v10\nv_lshlrev_b32 v38,16,v11\nv_and_b32 v39,0xffff0000,v11\nv_mul_f32 v40,0xbfb8aa3b,v36\nv_mul_f32 v41,0xbfb8aa3b,v37\nv_mul_f32 v42,0xbfb8aa3b,v38\n" \
"v_mul_f32 v43,0xbfb8aa3b,v39\nv_exp_f32 v40,v40\nv_exp_f32 v41,v41\nv_exp_f32 v42,v42\nv_exp_f32 v43,v43\nv_add_f32 v40,1.0,v40\nv_add_f32 v41,1.0,v41\nv_add_f32 v42,1.0,v42\n" \
"v_add_f32 v43,1.0,v43\nv_rcp_f32 v40,v40\nv_rcp_f32 v41,v41\nv_rcp_f32 v42,v42\nv_rcp_f32 v43,v43\nv_mul_f32 v36,v36,v40\nv_mul_f32 v37,v37,v41\nv_mul_f32 v38,v38,v42\n" \
"v_mul_f32 v39,v39,v43\nv_cvt_pk_bf16_f32 v36,v36,v37\nv_cvt_pk_bf16_f32 v37,v38,v39\ns_waitcnt vmcnt(18)\nv_perm_b32 v48,v16,v12,s70\nv_perm_b32 v49,v16,v12,s71\nv_perm_b32 v50,v17,v13,s70\nv_perm_b32 v51,v17,v13,s71\n" \
"v_perm_b32 v52,v18,v14,s70\nv_perm_b32 v53,v18,v14,s71\nv_perm_b32 v54,v19,v15,s70\nv_perm_b32 v55,v19,v15,s71\nv_dot2_f32_bf16 v40,v48,v36,0\nv_dot2_f32_bf16 v41,v49,v36,0\nv_dot2_f32_bf16 v42,v50,v36,0\nv_dot2_f32_bf16 v43,v51,v36,0\n" \
"v_dot2_f32_bf16 v44,v52,v36,0\nv_dot2_f32_bf16 v45,v53,v36,0\nv_dot2_f32_bf16 v46,v54,v36,0\nv_dot2_f32_bf16 v47,v55,v36,0\ns_waitcnt vmcnt(16)\nv_perm_b32 v48,v24,v20,s70\nv_perm_b32 v49,v24,v20,s71\nv_perm_b32 v50,v25,v21,s70\n" \
"v_perm_b32 v51,v25,v21,s71\nv_perm_b32 v52,v26,v22,s70\nv_perm_b32 v53,v26,v22,s71\nv_perm_b32 v54,v27,v23,s70\nv_perm_b32 v55,v27,v23,s71\nv_dot2_f32_bf16 v40,v48,v37,v40\nv_dot2_f32_bf16 v41,v49,v37,v41\nv_dot2_f32_bf16 v42,v50,v37,v42\n" \
"v_dot2_f32_bf16 v43,v51,v37,v43\nv_dot2_f32_bf16 v44,v52,v37,v44\nv_dot2_f32_bf16 v45,v53,v37,v45\nv_dot2_f32_bf16 v46,v54,v37,v46\nv_dot2_f32_bf16 v47,v55,v37,v47\ns_waitcnt vmcnt(15)\nv_lshlrev_b32 v136,16,v128\nv_and_b32 v137,0xffff0000,v128\n" \
"v_lshlrev_b32 v138,16,v129\nv_and_b32 v139,0xffff0000,v129\nv_lshlrev_b32 v140,16,v130\nv_and_b32 v141,0xffff0000,v130\nv_lshlrev_b32 v142,16,v131\nv_and_b32 v143,0xffff0000,v131\ns_waitcnt vmcnt(14)\nv_lshlrev_b32 v144,16,v132\n" \
"v_and_b32 v145,0xffff0000,v132\nv_lshlrev_b32 v146,16,v133\nv_and_b32 v147,0xffff0000,v133\nv_lshlrev_b32 v148,16,v134\nv_and_b32 v149,0xffff0000,v134\nv_lshlrev_b32 v150,16,v135\nv_and_b32 v151,0xffff0000,v135\nv_permlane32_swap_b32 v40,v44\n" \
"v_permlane32_swap_b32 v41,v45\nv_permlane32_swap_b32 v42,v46\nv_permlane32_swap_b32 v43,v47\nv_add_f32 v40,v40,v44\nv_add_f32 v41,v41,v45\nv_add_f32 v42,v42,v46\nv_add_f32 v43,v43,v47\nv_add_f32 v48,v136,v140\n" \
"v_add_f32 v49,v137,v141\nv_add_f32 v50,v138,v142\nv_add_f32 v51,v139,v143\nv_permlane16_swap_b32 v40,v42\nv_permlane16_swap_b32 v41,v43\nv_add_f32 v52,v144,v148\nv_add_f32 v53,v145,v149\nv_add_f32 v54,v146,v150\n" \
"v_add_f32 v55,v147,v151\nv_add_f32 v40,v40,v42\nv_add_f32 v41,v41,v43\nv_add_f32 v48,v48,v52\nv_add_f32 v49,v49,v53\nv_add_f32 v50,v50,v54\nv_add_f32 v51,v51,v55\nv_add_f32_dpp v40,v40,v40 row_ror:8 row_mask:0xf bank_mask:0xf\n" \
"v_add_f32_dpp v41,v41,v41 row_ror:8 row_mask:0xf bank_mask:0xf\nv_add_f32 v48,v48,v49\nv_add_f32 v50,v50,v51\nv_lshlrev_b32 v42,5,v2\nv_add_f32 v48,v48,v50\nv_lshl_or_b32 v42,v6,3,v42\ns_lshl_b32 s72,s7,8\ns_add_u32 s72,s72,147456\n" \
"v_add_u32 v42,s72,v42\nds_write_b64 v42,v[40:41]\nv_add_f32_dpp v48,v48,v48 row_ror:8 row_mask:0xf bank_mask:0xf\nv_mov_b32 v50,v48\ns_nop 1\nv_permlane16_swap_b32 v48,v50\nv_add_f32 v48,v48,v50\nv_mov_b32 v50,v48\n" \
"s_nop 1\nv_permlane32_swap_b32 v48,v50\nv_add_f32 v48,v48,v50\nv_mul_f32 v48,0x3c000000,v48\nv_sub_f32 v56,v136,v48\nv_sub_f32 v57,v137,v48\nv_sub_f32 v58,v138,v48\nv_sub_f32 v59,v139,v48\n" \
"v_sub_f32 v60,v140,v48\nv_sub_f32 v61,v141,v48\nv_sub_f32 v62,v142,v48\nv_sub_f32 v63,v143,v48\nv_sub_f32 v64,v144,v48\nv_sub_f32 v65,v145,v48\nv_sub_f32 v66,v146,v48\nv_sub_f32 v67,v147,v48\n" \
"v_sub_f32 v68,v148,v48\nv_sub_f32 v69,v149,v48\nv_sub_f32 v70,v150,v48\nv_sub_f32 v71,v151,v48\nv_mul_f32 v52,v56,v56\nv_mul_f32 v53,v57,v57\nv_mul_f32 v54,v58,v58\nv_mul_f32 v55,v59,v59\n" \
"v_fmac_f32 v52,v60,v60\nv_fmac_f32 v53,v61,v61\nv_fmac_f32 v54,v62,v62\nv_fmac_f32 v55,v63,v63\nv_fmac_f32 v52,v64,v64\nv_fmac_f32 v53,v65,v65\nv_fmac_f32 v54,v66,v66\nv_fmac_f32 v55,v67,v67\n" \
"v_fmac_f32 v52,v68,v68\nv_fmac_f32 v53,v69,v69\nv_fmac_f32 v54,v70,v70\nv_fmac_f32 v55,v71,v71\nv_add_f32 v52,v52,v53\nv_add_f32 v54,v54,v55\ns_lshl_b32 s72,s7,3\nv_add_f32 v49,v52,v54\n" \
"s_add_u32 s72,s72,148480\nv_lshl_add_u32 v42,v2,5,s72\nv_add_f32_dpp v49,v49,v49 row_ror:8 row_mask:0xf bank_mask:0xf\nv_mov_b32 v50,v49\ns_nop 1\nv_permlane16_swap_b32 v49,v50\nv_add_f32 v49,v49,v50\nv_mov_b32 v50,v49\n" \
"s_nop 1\nv_permlane32_swap_b32 v49,v50\nv_add_f32 v49,v49,v50\nds_write_b64 v42,v[48:49]\nv_lshlrev_b32 v251,5,v2\nv_add_u32 v251,148480,v251\nv_lshlrev_b32 v250,2,v249\nv_add_u32 v250,147456,v250\n" \
"s_add_u32 m0,s64,114688\ns_add_u32 s69,s68,524288\nbuffer_load_dwordx4 v243,s[40:43],s69 offen offset:0 lds\nbuffer_load_dwordx4 v244,s[40:43],s69 offen offset:1024 lds\nbuffer_load_dwordx4 v245,s[40:43],s69 offen offset:2048 lds\nbuffer_load_dwordx4 v246,s[40:43],s69 offen offset:3072 lds\ns_add_u32 m0,s65,8192\ns_add_u32 s67,s66,128\n" \
"buffer_load_dwordx4 v224,s[36:39],s67 offen offset:0 lds\nbuffer_load_dwordx4 v225,s[36:39],s67 offen offset:1024 lds\nbuffer_load_dwordx4 v226,s[36:39],s67 offen offset:2048 lds\nbuffer_load_dwordx4 v227,s[36:39],s67 offen offset:3072 lds\ns_add_u32 m0,s65,12288\ns_add_u32 s67,s66,32896\nbuffer_load_dwordx4 v224,s[36:39],s67 offen offset:0 lds\nbuffer_load_dwordx4 v225,s[36:39],s67 offen offset:1024 lds\n" \
"buffer_load_dwordx4 v226,s[36:39],s67 offen offset:2048 lds\nbuffer_load_dwordx4 v227,s[36:39],s67 offen offset:3072 lds\ns_waitcnt lgkmcnt(0)\ns_waitcnt vmcnt(14)\ns_barrier\ns_add_u32 m0,s64,131072\ns_add_u32 s69,s68,1048576\nds_read_b128 a[0:3],v228 offset:0\n" \
"ds_read_b128 a[4:7],v228 offset:2048\nds_read_b128 a[8:11],v228 offset:4096\nds_read_b128 a[12:15],v228 offset:6144\nds_read_b128 a[16:19],v229 offset:0\nds_read_b128 a[20:23],v229 offset:2048\nds_read_b128 a[24:27],v229 offset:4096\nds_read_b128 a[28:31],v229 offset:6144\nds_read_b32 v152,v250 offset:0\n" \
"ds_read_b32 v153,v250 offset:256\nds_read_b32 v154,v250 offset:512\nds_read_b32 v155,v250 offset:768\ns_waitcnt lgkmcnt(2)\nv_add_f32 v152,v152,v153\ns_waitcnt lgkmcnt(0)\nv_add_f32 v154,v154,v155\nv_lshlrev_b32 v153,2,v249\n" \
"v_add_f32 v152,v152,v154\nds_read_b64_tr_b16 v[192:193],v241 offset:0\nds_read_b64_tr_b16 v[194:195],v241 offset:256\nds_read_b64_tr_b16 v[196:197],v242 offset:0\nds_read_b64_tr_b16 v[198:199],v242 offset:256\nds_read_b64_tr_b16 v[200:201],v241 offset:512\nds_read_b64_tr_b16 v[202:203],v241 offset:768\nds_read_b64_tr_b16 v[204:205],v242 offset:512\n" \
"ds_read_b64_tr_b16 v[206:207],v242 offset:768\nds_read_b64_tr_b16 v[208:209],v241 offset:1024\nds_read_b64_tr_b16 v[210:211],v241 offset:1280\nds_read_b64_tr_b16 v[212:213],v242 offset:1024\nds_read_b64_tr_b16 v[214:215],v242 offset:1280\nds_read_b64_tr_b16 v[216:217],v241 offset:1536\nds_read_b64_tr_b16 v[218:219],v241 offset:1792\nds_read_b64_tr_b16 v[220:221],v242 offset:1536\n" \
"ds_read_b64_tr_b16 v[222:223],v242 offset:1792\nbuffer_store_dword v152,v153,s[52:55],s79 offen sc1\nds_read_b128 v[156:159],v251\nds_read_b128 v[160:163],v251 offset:16\ns_waitcnt lgkmcnt(15)\nM v[0:3],v[192:195],a[0:3],0\nbuffer_load_dwordx4 v243,s[40:43],s69 offen offset:0 lds\nM v[4:7],v[192:195],a[4:7],0\n" \
"v_and_b32 v254,7,v249\nv_bfe_u32 v253,v249,1,2\nM v[64:67],v[192:195],a[8:11],0\nv_and_b32 v248,1,v249\ns_lshl2_add_u32 s76,s62,s7\nM v[68:71],v[192:195],a[12:15],0\nds_read_b64_tr_b16 v[192:193],v241 offset:8192\nds_read_b64_tr_b16 v[194:195],v241 offset:8448\n" \
"s_waitcnt lgkmcnt(15)\nM v[8:11],v[196:199],a[0:3],0\nv_lshlrev_b32 v253,13,v253\ns_mul_i32 s76,s76,32768\nM v[12:15],v[196:199],a[4:7],0\nbuffer_load_dwordx4 v244,s[40:43],s69 offen offset:1024 lds\nM v[72:75],v[196:199],a[8:11],0\ns_lshl_b32 s77,s63,8\n" \
"v_lshl_or_b32 v253,v248,7,v253\nM v[76:79],v[196:199],a[12:15],0\nds_read_b64_tr_b16 v[196:197],v242 offset:8192\nds_read_b64_tr_b16 v[198:199],v242 offset:8448\ns_add_u32 s76,s76,s77\ns_waitcnt lgkmcnt(7)\nM v[16:19],v[200:203],a[0:3],0\nv_add_u32 v253,s76,v253\n" \
"global_load_dword v247,v253,s[16:17]\nM v[20:23],v[200:203],a[4:7],0\nv_readfirstlane_b32 s92,v230\nv_readfirstlane_b32 s93,v231\nM v[80:83],v[200:203],a[8:11],0\nbuffer_load_dwordx4 v245,s[40:43],s69 offen offset:2048 lds\nM v[84:87],v[200:203],a[12:15],0\nds_read_b64_tr_b16 v[200:201],v241 offset:8704\n" \
"ds_read_b64_tr_b16 v[202:203],v241 offset:8960\nM v[24:27],v[204:207],a[0:3],0\ns_add_u32 s70,s92,1\ns_addc_u32 s71,s93,0\nM v[28:31],v[204:207],a[4:7],0\ns_waitcnt lgkmcnt(4)\nv_add_f32 v164,v156,v158\nM v[88:91],v[204:207],a[8:11],0\n" \
"v_add_f32 v165,v160,v162\nv_add_f32 v164,v164,v165\nM v[92:95],v[204:207],a[12:15],0\nbuffer_load_dwordx4 v246,s[40:43],s69 offen offset:3072 lds\nds_read_b64_tr_b16 v[204:205],v242 offset:8704\nds_read_b64_tr_b16 v[206:207],v242 offset:8960\nM v[32:35],v[208:211],a[0:3],0\ns_add_u32 s92,s92,0x7f4a7c15\n" \
"s_addc_u32 s93,s93,0x9e3779b9\nM v[36:39],v[208:211],a[4:7],0\ns_add_u32 m0,s65,16384\ns_add_u32 s67,s66,256\nM v[96:99],v[208:211],a[8:11],0\nv_and_b32 v165,7,v249\ns_lshl3_add_u32 s72,s6,s75\nM v[100:103],v[208:211],a[12:15],0\n" \
"ds_read_b64_tr_b16 v[208:209],v241 offset:9216\nds_read_b64_tr_b16 v[210:211],v241 offset:9472\nM v[40:43],v[212:215],a[0:3],0\nbuffer_load_dwordx4 v224,s[36:39],s67 offen offset:0 lds\nM v[44:47],v[212:215],a[4:7],0\nv_add_u32 v253,0x100000,v253\nv_mov_b32 v230,s92\nM v[104:107],v[212:215],a[8:11],0\n" \
"v_mov_b32 v231,s93\nv_lshlrev_b32 v165,11,v165\nM v[108:111],v[212:215],a[12:15],0\nds_read_b64_tr_b16 v[212:213],v242 offset:9216\nds_read_b64_tr_b16 v[214:215],v242 offset:9472\nM v[48:51],v[216:219],a[0:3],0\ns_add_u32 s72,s72,573440\ns_lshl2_add_u32 s80,s60,s7\n" \
"M v[52:55],v[216:219],a[4:7],0\nbuffer_load_dwordx4 v225,s[36:39],s67 offen offset:1024 lds\nM v[112:115],v[216:219],a[8:11],0\ns_lshl_b32 s80,s80,9\ns_add_u32 s80,s80,573440\nM v[116:119],v[216:219],a[12:15],0\nds_read_b64_tr_b16 v[216:217],v241 offset:9728\nds_read_b64_tr_b16 v[218:219],v241 offset:9984\n" \
"M v[56:59],v[220:223],a[0:3],0\nv_mul_f32 v164,0x3e800000,v164\nv_sub_f32 v156,v156,v164\nM v[60:63],v[220:223],a[4:7],0\nv_sub_f32 v158,v158,v164\nv_mul_f32 v156,v156,v156\nM v[120:123],v[220:223],a[8:11],0\nbuffer_load_dwordx4 v226,s[36:39],s67 offen offset:2048 lds\n" \
"M v[124:127],v[220:223],a[12:15],0\nds_read_b64_tr_b16 v[220:221],v242 offset:9728\nds_read_b64_tr_b16 v[222:223],v242 offset:9984\nM v[0:3],v[192:195],a[16:19],v[0:3]\nv_sub_f32 v160,v160,v164\nv_fmac_f32 v156,v158,v158\nM v[4:7],v[192:195],a[20:23],v[4:7]\nv_add_f32 v157,v157,v159\n" \
"v_add_f32 v161,v161,v163\nM v[64:67],v[192:195],a[24:27],v[64:67]\nv_sub_f32 v162,v162,v164\nv_fmac_f32 v156,v160,v160\nM v[68:71],v[192:195],a[28:31],v[68:71]\nbuffer_load_dwordx4 v227,s[36:39],s67 offen offset:3072 lds\ns_waitcnt lgkmcnt(4)\nM v[8:11],v[196:199],a[16:19],v[8:11]\n" \
"v_add_f32 v157,v157,v161\nv_fmac_f32 v156,v162,v162\nM v[12:15],v[196:199],a[20:23],v[12:15]\ns_add_u32 m0,s65,20480\ns_add_u32 s67,s66,33024\nM v[72:75],v[196:199],a[24:27],v[72:75]\nv_fmac_f32 v157,0x43000000,v156\nv_mul_f32 v157,0x3b000000,v157\n" \
"M v[76:79],v[196:199],a[28:31],v[76:79]\nv_cvt_pk_bf16_f32 v157,0,v157\nv_add_f32 v157,0x3727c5ac,v157\nM v[16:19],v[200:203],a[16:19],v[16:19]\nbuffer_load_dwordx4 v224,s[36:39],s67 offen offset:0 lds\nM v[20:23],v[200:203],a[20:23],v[20:23]\nv_cvt_pk_bf16_f32 v157,0,v157\nv_mov_b32 v232,s70\n" \
"M v[80:83],v[200:203],a[24:27],v[80:83]\nv_sqrt_f32 v157,v157\nM v[84:87],v[200:203],a[28:31],v[84:87]\nv_mov_b32 v233,s71\ns_lshr_b32 s70,s64,2\nM v[24:27],v[204:207],a[16:19],v[24:27]\nglobal_load_dword v247,v253,s[16:17]\nM v[28:31],v[204:207],a[20:23],v[28:31]\n" \
"buffer_load_dwordx4 v225,s[36:39],s67 offen offset:1024 lds\nM v[88:91],v[204:207],a[24:27],v[88:91]\ns_waitcnt vmcnt(12) lgkmcnt(0)\ns_barrier\nds_read_b128 a[32:35],v228 offset:8192\nds_read_b128 a[36:39],v228 offset:10240\nds_read_b128 a[40:43],v228 offset:12288\nds_read_b128 a[44:47],v228 offset:14336\n" \
"ds_read_b128 a[48:51],v229 offset:8192\nds_read_b128 a[52:55],v229 offset:10240\nds_read_b128 a[56:59],v229 offset:12288\nds_read_b128 a[60:63],v229 offset:14336\nds_read_b64_tr_b16 v[192:193],v241 offset:16384\nds_read_b64_tr_b16 v[194:195],v241 offset:16640\nds_read_b64_tr_b16 v[196:197],v242 offset:16384\nds_read_b64_tr_b16 v[198:199],v242 offset:16640\n" \
"ds_read_b64_tr_b16 v[200:201],v241 offset:16896\nds_read_b64_tr_b16 v[202:203],v241 offset:17152\nM v[92:95],v[204:207],a[28:31],v[92:95]\nds_read_b64_tr_b16 v[204:205],v242 offset:16896\nds_read_b64_tr_b16 v[206:207],v242 offset:17152\nM v[32:35],v[208:211],a[16:19],v[32:35]\nbuffer_store_dwordx2 v[230:231],v165,s[52:55],s72 offen sc1\nM v[36:39],v[208:211],a[20:23],v[36:39]\n" \
"v_add_u32 v253,0x100000,v253\nv_lshlrev_b32 v165,3,v249\nM v[96:99],v[208:211],a[24:27],v[96:99]\nbuffer_load_dwordx4 v226,s[36:39],s67 offen offset:2048 lds\nM v[100:103],v[208:211],a[28:31],v[100:103]\nds_read_b64_tr_b16 v[208:209],v241 offset:17408\nds_read_b64_tr_b16 v[210:211],v241 offset:17664\nM v[40:43],v[212:215],a[16:19],v[40:43]\n" \
"v_cvt_pk_bf16_f32 v164,0,v164\nv_cvt_pk_bf16_f32 v157,0,v157\nM v[44:47],v[212:215],a[20:23],v[44:47]\ns_add_u32 s71,s70,786432\nv_sub_f32 v136,v136,v164\nM v[104:107],v[212:215],a[24:27],v[104:107]\nv_rcp_f32 v157,v157\nM v[108:111],v[212:215],a[28:31],v[108:111]\n" \
"buffer_load_dwordx4 v227,s[36:39],s67 offen offset:3072 lds\nds_read_b64_tr_b16 v[212:213],v242 offset:17408\nds_read_b64_tr_b16 v[214:215],v242 offset:17664\nM v[48:51],v[216:219],a[16:19],v[48:51]\nv_sub_f32 v137,v137,v164\nv_sub_f32 v138,v138,v164\nM v[52:55],v[216:219],a[20:23],v[52:55]\ns_add_u32 m0,s65,0\n" \
"s_add_u32 s67,s66,384\nM v[112:115],v[216:219],a[24:27],v[112:115]\nv_sub_f32 v139,v139,v164\nv_sub_f32 v140,v140,v164\nM v[116:119],v[216:219],a[28:31],v[116:119]\nds_read_b64_tr_b16 v[216:217],v241 offset:17920\nds_read_b64_tr_b16 v[218:219],v241 offset:18176\nM v[56:59],v[220:223],a[16:19],v[56:59]\n" \
"buffer_load_dwordx4 v224,s[36:39],s67 offen offset:0 lds\nM v[60:63],v[220:223],a[20:23],v[60:63]\nv_sub_f32 v141,v141,v164\nv_sub_f32 v142,v142,v164\nM v[120:123],v[220:223],a[24:27],v[120:123]\nv_sub_f32 v143,v143,v164\nv_sub_f32 v144,v144,v164\nM v[124:127],v[220:223],a[28:31],v[124:127]\n" \
"ds_read_b64_tr_b16 v[220:221],v242 offset:17920\nds_read_b64_tr_b16 v[222:223],v242 offset:18176\ns_waitcnt lgkmcnt(6)\nM v[0:3],v[192:195],a[32:35],v[0:3]\nv_sub_f32 v145,v145,v164\nv_sub_f32 v146,v146,v164\nM v[4:7],v[192:195],a[36:39],v[4:7]\nbuffer_load_dwordx4 v225,s[36:39],s67 offen offset:1024 lds\n" \
"M v[64:67],v[192:195],a[40:43],v[64:67]\nv_sub_f32 v147,v147,v164\nv_sub_f32 v148,v148,v164\nM v[68:71],v[192:195],a[44:47],v[68:71]\nds_read_b64_tr_b16 v[192:193],v241 offset:24576\nds_read_b64_tr_b16 v[194:195],v241 offset:24832\nM v[8:11],v[196:199],a[32:35],v[8:11]\nv_sub_f32 v149,v149,v164\n" \
"v_sub_f32 v150,v150,v164\nM v[12:15],v[196:199],a[36:39],v[12:15]\nv_sub_f32 v151,v151,v164\nv_cvt_pk_bf16_f32 v136,0,v136\nM v[72:75],v[196:199],a[40:43],v[72:75]\nbuffer_load_dwordx4 v226,s[36:39],s67 offen offset:2048 lds\nM v[76:79],v[196:199],a[44:47],v[76:79]\nds_read_b64_tr_b16 v[196:197],v242 offset:24576\n" \
"ds_read_b64_tr_b16 v[198:199],v242 offset:24832\nM v[16:19],v[200:203],a[32:35],v[16:19]\nv_cvt_pk_bf16_f32 v137,0,v137\nv_cvt_pk_bf16_f32 v138,0,v138\nM v[20:23],v[200:203],a[36:39],v[20:23]\nv_cvt_pk_bf16_f32 v139,0,v139\nv_cvt_pk_bf16_f32 v140,0,v140\nM v[80:83],v[200:203],a[40:43],v[80:83]\n" \
"v_cvt_pk_bf16_f32 v141,0,v141\nv_cvt_pk_bf16_f32 v142,0,v142\nM v[84:87],v[200:203],a[44:47],v[84:87]\nbuffer_load_dwordx4 v227,s[36:39],s67 offen offset:3072 lds\nds_read_b64_tr_b16 v[200:201],v241 offset:25088\nds_read_b64_tr_b16 v[202:203],v241 offset:25344\nM v[24:27],v[204:207],a[32:35],v[24:27]\nv_cvt_pk_bf16_f32 v143,0,v143\n" \
"v_cvt_pk_bf16_f32 v144,0,v144\nM v[28:31],v[204:207],a[36:39],v[28:31]\ns_add_u32 m0,s65,4096\ns_add_u32 s67,s66,33152\nM v[88:91],v[204:207],a[40:43],v[88:91]\nv_cvt_pk_bf16_f32 v145,0,v145\nv_cvt_pk_bf16_f32 v146,0,v146\nM v[92:95],v[204:207],a[44:47],v[92:95]\n" \
"ds_read_b64_tr_b16 v[204:205],v242 offset:25088\nds_read_b64_tr_b16 v[206:207],v242 offset:25344\nM v[32:35],v[208:211],a[32:35],v[32:35]\nbuffer_load_dwordx4 v224,s[36:39],s67 offen offset:0 lds\nM v[36:39],v[208:211],a[36:39],v[36:39]\nv_cvt_pk_bf16_f32 v147,0,v147\nv_cvt_pk_bf16_f32 v148,0,v148\nM v[96:99],v[208:211],a[40:43],v[96:99]\n" \
"v_cvt_pk_bf16_f32 v149,0,v149\nv_cvt_pk_bf16_f32 v150,0,v150\nM v[100:103],v[208:211],a[44:47],v[100:103]\nds_read_b64_tr_b16 v[208:209],v241 offset:25600\nds_read_b64_tr_b16 v[210:211],v241 offset:25856\ns_waitcnt lgkmcnt(6)\nM v[40:43],v[212:215],a[32:35],v[40:43]\nglobal_load_dword v247,v253,s[16:17]\n" \
"M v[44:47],v[212:215],a[36:39],v[44:47]\nbuffer_load_dwordx4 v225,s[36:39],s67 offen offset:1024 lds\nM v[104:107],v[212:215],a[40:43],v[104:107]\nv_add_u32 v253,0x100000,v253\nv_cvt_pk_bf16_f32 v151,0,v151\nM v[108:111],v[212:215],a[44:47],v[108:111]\nds_read_b64_tr_b16 v[212:213],v242 offset:25600\nds_read_b64_tr_b16 v[214:215],v242 offset:25856\n" \
"M v[48:51],v[216:219],a[32:35],v[48:51]\nv_mul_f32 v136,v136,v157\nv_mul_f32 v137,v137,v157\nM v[52:55],v[216:219],a[36:39],v[52:55]\nv_mul_f32 v138,v138,v157\nv_mul_f32 v139,v139,v157\nM v[112:115],v[216:219],a[40:43],v[112:115]\nbuffer_load_dwordx4 v226,s[36:39],s67 offen offset:2048 lds\n" \
"M v[116:119],v[216:219],a[44:47],v[116:119]\nds_read_b64_tr_b16 v[216:217],v241 offset:26112\nds_read_b64_tr_b16 v[218:219],v241 offset:26368\nM v[56:59],v[220:223],a[32:35],v[56:59]\nv_mul_f32 v140,v140,v157\nv_mul_f32 v141,v141,v157\nM v[60:63],v[220:223],a[36:39],v[60:63]\nv_mul_f32 v142,v142,v157\n" \
"v_mul_f32 v143,v143,v157\nM v[120:123],v[220:223],a[40:43],v[120:123]\nv_mul_f32 v144,v144,v157\nv_mul_f32 v145,v145,v157\nM v[124:127],v[220:223],a[44:47],v[124:127]\nbuffer_load_dwordx4 v227,s[36:39],s67 offen offset:3072 lds\nds_read_b64_tr_b16 v[220:221],v242 offset:26112\nds_read_b64_tr_b16 v[222:223],v242 offset:26368\n" \
"M v[0:3],v[192:195],a[48:51],v[0:3]\nv_mul_f32 v146,v146,v157\nv_mul_f32 v147,v147,v157\nM v[4:7],v[192:195],a[52:55],v[4:7]\ns_add_u32 m0,s64,147456\ns_add_u32 s69,s68,1572864\nM v[64:67],v[192:195],a[56:59],v[64:67]\nv_mul_f32 v148,v148,v157\n" \
"v_mul_f32 v149,v149,v157\nM v[68:71],v[192:195],a[60:63],v[68:71]\nv_mul_f32 v150,v150,v157\nv_mul_f32 v151,v151,v157\nM v[8:11],v[196:199],a[48:51],v[8:11]\nbuffer_load_dwordx4 v243,s[40:43],s69 offen offset:0 lds\nM v[12:15],v[196:199],a[52:55],v[12:15]\nv_lshlrev_b32 v254,14,v254\n" \
"s_add_u32 s78,s78,32768\nM v[72:75],v[196:199],a[56:59],v[72:75]\nv_and_b32 v169,3,v249\nv_mul_u32_u24 v169,12288,v169\nM v[76:79],v[196:199],a[60:63],v[76:79]\nv_and_b32 v170,28,v249\ns_waitcnt lgkmcnt(2)\nM v[16:19],v[200:203],a[48:51],v[16:19]\n" \
"v_add_u32 v169,v169,v170\nv_lshrrev_b32 v251,3,v249\nM v[20:23],v[200:203],a[52:55],v[20:23]\nbuffer_load_dwordx4 v244,s[40:43],s69 offen offset:1024 lds\nM v[80:83],v[200:203],a[56:59],v[80:83]\ns_cmp_eq_u32 s6,0\ns_cselect_b32 s72,s75,0x8000000\nM v[84:87],v[200:203],a[60:63],v[84:87]\n" \
"s_add_u32 s72,s72,94208\nv_lshlrev_b32 v170,3,v170\nM v[24:27],v[204:207],a[48:51],v[24:27]\nv_cvt_pk_bf16_f32 v144,0,v144\nv_cvt_pk_bf16_f32 v145,0,v145\nM v[28:31],v[204:207],a[52:55],v[28:31]\nv_cvt_pk_bf16_f32 v146,0,v146\nv_cvt_pk_bf16_f32 v147,0,v147\n" \
"M v[88:91],v[204:207],a[56:59],v[88:91]\nbuffer_load_dwordx4 v245,s[40:43],s69 offen offset:2048 lds\ns_waitcnt vmcnt(12) lgkmcnt(0)\ns_barrier\nds_read_b128 a[64:67],v228 offset:16384\nds_read_b128 a[68:71],v228 offset:18432\nds_read_b128 a[72:75],v228 offset:20480\nds_read_b128 a[76:79],v228 offset:22528\n" \
"ds_read_b128 a[80:83],v229 offset:16384\nds_read_b128 a[84:87],v229 offset:18432\nds_read_b128 a[88:91],v229 offset:20480\nds_read_b128 a[92:95],v229 offset:22528\nM v[92:95],v[204:207],a[60:63],v[92:95]\nv_cvt_pk_bf16_f32 v136,0,v136\nv_cvt_pk_bf16_f32 v137,0,v137\nM v[32:35],v[208:211],a[48:51],v[32:35]\n" \
"v_cvt_pk_bf16_f32 v138,0,v138\nv_cvt_pk_bf16_f32 v139,0,v139\nM v[36:39],v[208:211],a[52:55],v[36:39]\nv_cvt_pk_bf16_f32 v140,0,v140\nv_cvt_pk_bf16_f32 v141,0,v141\nM v[96:99],v[208:211],a[56:59],v[96:99]\nv_cvt_pk_bf16_f32 v142,0,v142\nv_cvt_pk_bf16_f32 v143,0,v143\n" \
"M v[100:103],v[208:211],a[60:63],v[100:103]\nbuffer_load_dwordx4 v246,s[40:43],s69 offen offset:3072 lds\nM v[40:43],v[212:215],a[48:51],v[40:43]\nv_cvt_pk_bf16_f32 v148,0,v148\nv_cvt_pk_bf16_f32 v149,0,v149\nM v[44:47],v[212:215],a[52:55],v[44:47]\ns_add_u32 m0,s65,8192\ns_add_u32 s67,s66,512\n" \
"ds_read_b64_tr_b16 v[192:193],v241 offset:32768\nds_read_b64_tr_b16 v[194:195],v241 offset:33024\nM v[104:107],v[212:215],a[56:59],v[104:107]\nds_read_b64_tr_b16 v[196:197],v242 offset:32768\nv_bfe_u32 v250,v249,3,2\nM v[108:111],v[212:215],a[60:63],v[108:111]\nds_read_b64_tr_b16 v[198:199],v242 offset:33024\nds_read_b64_tr_b16 v[200:201],v241 offset:33280\n" \
"M v[48:51],v[216:219],a[48:51],v[48:51]\nbuffer_load_dwordx4 v224,s[36:39],s67 offen offset:0 lds\nM v[52:55],v[216:219],a[52:55],v[52:55]\nds_read_b64_tr_b16 v[202:203],v241 offset:33536\nds_read_b64_tr_b16 v[204:205],v242 offset:33280\nM v[112:115],v[216:219],a[56:59],v[112:115]\nds_read_b64_tr_b16 v[206:207],v242 offset:33536\nds_read_b64_tr_b16 v[208:209],v241 offset:33792\n" \
"M v[116:119],v[216:219],a[60:63],v[116:119]\nds_read_b64_tr_b16 v[210:211],v241 offset:34048\nds_read_b64_tr_b16 v[212:213],v242 offset:33792\nM v[56:59],v[220:223],a[48:51],v[56:59]\nglobal_load_dword v247,v253,s[16:17]\nM v[60:63],v[220:223],a[52:55],v[60:63]\nbuffer_load_dwordx4 v225,s[36:39],s67 offen offset:1024 lds\nM v[120:123],v[220:223],a[56:59],v[120:123]\n" \
"ds_read_b64_tr_b16 v[214:215],v242 offset:34048\nds_read_b64_tr_b16 v[216:217],v241 offset:34304\nM v[124:127],v[220:223],a[60:63],v[124:127]\nds_read_b64_tr_b16 v[218:219],v241 offset:34560\ns_waitcnt lgkmcnt(12)\nM v[0:3],v[192:195],a[64:67],v[0:3]\nds_read_b64_tr_b16 v[220:221],v242 offset:34304\nds_read_b64_tr_b16 v[222:223],v242 offset:34560\n" \
"M v[4:7],v[192:195],a[68:71],v[4:7]\nv_add_u32 v253,0xffd00000,v253\nv_cvt_pk_bf16_f32 v150,0,v150\nM v[64:67],v[192:195],a[72:75],v[64:67]\nbuffer_load_dwordx4 v226,s[36:39],s67 offen offset:2048 lds\nM v[68:71],v[192:195],a[76:79],v[68:71]\nds_read_b64_tr_b16 v[192:193],v241 offset:40960\ns_waitcnt lgkmcnt(11)\n" \
"M v[8:11],v[196:199],a[64:67],v[8:11]\nds_read_b64_tr_b16 v[194:195],v241 offset:41216\nv_cvt_pk_bf16_f32 v151,0,v151\nM v[12:15],v[196:199],a[68:71],v[12:15]\ns_mov_b32 s98,0x4000\ns_mov_b32 s0,28672\nM v[72:75],v[196:199],a[72:75],v[72:75]\ns_add_u32 s10,s61,1\n" \
"s_add_u32 s11,s61,2\nM v[76:79],v[196:199],a[76:79],v[76:79]\nbuffer_load_dwordx4 v227,s[36:39],s67 offen offset:3072 lds\nM v[16:19],v[200:203],a[64:67],v[16:19]\nds_read_b64_tr_b16 v[196:197],v242 offset:40960\nds_read_b64_tr_b16 v[198:199],v242 offset:41216\nM v[20:23],v[200:203],a[68:71],v[20:23]\ns_add_u32 m0,s65,12288\n" \
"s_add_u32 s67,s66,33280\nM v[80:83],v[200:203],a[72:75],v[80:83]\ns_add_u32 s14,s61,3\ns_and_b32 s10,s10,3\nM v[84:87],v[200:203],a[76:79],v[84:87]\nds_read_b64_tr_b16 v[200:201],v241 offset:41472\ns_waitcnt lgkmcnt(5)\nM v[24:27],v[204:207],a[64:67],v[24:27]\n" \
"buffer_load_dwordx4 v224,s[36:39],s67 offen offset:0 lds\nM v[28:31],v[204:207],a[68:71],v[28:31]\nds_read_b64_tr_b16 v[202:203],v241 offset:41728\ns_and_b32 s11,s11,3\nM v[88:91],v[204:207],a[72:75],v[88:91]\ns_and_b32 s14,s14,3\ns_mov_b32 s47,0x20000\nM v[92:95],v[204:207],a[76:79],v[92:95]\n" \
"ds_read_b64_tr_b16 v[204:205],v242 offset:41472\nds_read_b64_tr_b16 v[206:207],v242 offset:41728\nM v[32:35],v[208:211],a[64:67],v[32:35]\ns_mov_b32 s51,0x20000\ns_mov_b32 s94,0xc000\nM v[36:39],v[208:211],a[68:71],v[36:39]\nbuffer_load_dwordx4 v225,s[36:39],s67 offen offset:1024 lds\nM v[96:99],v[208:211],a[72:75],v[96:99]\n" \
"s_lshl_b32 s10,s10,13\ns_lshl_b32 s11,s11,13\nM v[100:103],v[208:211],a[76:79],v[100:103]\nds_read_b64_tr_b16 v[208:209],v241 offset:41984\nds_read_b64_tr_b16 v[210:211],v241 offset:42240\nM v[40:43],v[212:215],a[64:67],v[40:43]\ns_lshl_b32 s14,s14,13\ns_mov_b32 s82,0xf0000\n" \
"M v[44:47],v[212:215],a[68:71],v[44:47]\ns_mov_b32 s83,0xfff00ff\ns_mov_b32 s84,0x1f0001\nM v[104:107],v[212:215],a[72:75],v[104:107]\nbuffer_load_dwordx4 v226,s[36:39],s67 offen offset:2048 lds\nM v[108:111],v[212:215],a[76:79],v[108:111]\nds_read_b64_tr_b16 v[212:213],v242 offset:41984\nds_read_b64_tr_b16 v[214:215],v242 offset:42240\n" \
"M v[48:51],v[216:219],a[64:67],v[48:51]\ns_mov_b32 s85,0x1fff01ff\ns_mov_b32 s86,0x3f0003\nM v[52:55],v[216:219],a[68:71],v[52:55]\ns_mov_b32 s87,0x3fff03ff\ns_mov_b32 s88,0x7f0007\nM v[112:115],v[216:219],a[72:75],v[112:115]\ns_mov_b32 s89,0x7fff07ff\n" \
"s_mov_b64 s[90:91],-1\nM v[116:119],v[216:219],a[76:79],v[116:119]\nbuffer_load_dwordx4 v227,s[36:39],s67 offen offset:3072 lds\nM v[56:59],v[220:223],a[64:67],v[56:59]\nds_read_b64_tr_b16 v[216:217],v241 offset:42496\nds_read_b64_tr_b16 v[218:219],v241 offset:42752\nM v[60:63],v[220:223],a[68:71],v[60:63]\ns_add_u32 m0,s64,98304\n" \
"s_add_u32 s69,s68,2097152\nM v[120:123],v[220:223],a[72:75],v[120:123]\ns_mov_b32 s28,0x5f800000\ns_mov_b32 s99,0\nM v[124:127],v[220:223],a[76:79],v[124:127]\nds_read_b64_tr_b16 v[220:221],v242 offset:42496\nds_read_b64_tr_b16 v[222:223],v242 offset:42752\ns_waitcnt lgkmcnt(6)\n" \
"M v[0:3],v[192:195],a[80:83],v[0:3]\nbuffer_load_dwordx4 v243,s[40:43],s69 offen offset:0 lds\nM v[4:7],v[192:195],a[84:87],v[4:7]\nM v[64:67],v[192:195],a[88:91],v[64:67]\nM v[68:71],v[192:195],a[92:95],v[68:71]\nM v[8:11],v[196:199],a[80:83],v[8:11]\nM v[12:15],v[196:199],a[84:87],v[12:15]\nbuffer_load_dwordx4 v244,s[40:43],s69 offen offset:1024 lds\n" \
"M v[72:75],v[196:199],a[88:91],v[72:75]\nM v[76:79],v[196:199],a[92:95],v[76:79]\nM v[16:19],v[200:203],a[80:83],v[16:19]\nM v[20:23],v[200:203],a[84:87],v[20:23]\nM v[80:83],v[200:203],a[88:91],v[80:83]\nbuffer_load_dwordx4 v245,s[40:43],s69 offen offset:2048 lds\nM v[84:87],v[200:203],a[92:95],v[84:87]\nM v[24:27],v[204:207],a[80:83],v[24:27]\n" \
"M v[28:31],v[204:207],a[84:87],v[28:31]\nM v[88:91],v[204:207],a[88:91],v[88:91]\ns_waitcnt vmcnt(12) lgkmcnt(0)\ns_barrier\nds_read_b128 a[96:99],v228 offset:0\nds_read_b128 a[100:103],v228 offset:2048\nds_read_b128 a[104:107],v228 offset:4096\nds_read_b128 a[108:111],v228 offset:6144\n" \
"ds_read_b128 a[112:115],v229 offset:0\nds_read_b128 a[116:119],v229 offset:2048\nds_read_b128 a[120:123],v229 offset:4096\nds_read_b128 a[124:127],v229 offset:6144\nM v[92:95],v[204:207],a[92:95],v[92:95]\nbuffer_load_dwordx4 v246,s[40:43],s69 offen offset:3072 lds\nM v[32:35],v[208:211],a[80:83],v[32:35]\nM v[36:39],v[208:211],a[84:87],v[36:39]\n" \
"s_add_u32 m0,s65,16384\ns_add_u32 s67,s66,640\nM v[96:99],v[208:211],a[88:91],v[96:99]\nM v[100:103],v[208:211],a[92:95],v[100:103]\nM v[40:43],v[212:215],a[80:83],v[40:43]\nbuffer_load_dwordx4 v224,s[36:39],s67 offen offset:0 lds\nM v[44:47],v[212:215],a[84:87],v[44:47]\nds_read_b64_tr_b16 v[192:193],v241 offset:49152\n" \
"ds_read_b64_tr_b16 v[194:195],v241 offset:49408\nM v[104:107],v[212:215],a[88:91],v[104:107]\nds_read_b64_tr_b16 v[196:197],v242 offset:49152\nM v[108:111],v[212:215],a[92:95],v[108:111]\nds_read_b64_tr_b16 v[198:199],v242 offset:49408\nds_read_b64_tr_b16 v[200:201],v241 offset:49664\nM v[48:51],v[216:219],a[80:83],v[48:51]\nds_read_b64_tr_b16 v[202:203],v241 offset:49920\n" \
"ds_read_b64_tr_b16 v[204:205],v242 offset:49664\nM v[52:55],v[216:219],a[84:87],v[52:55]\nbuffer_load_dwordx4 v225,s[36:39],s67 offen offset:1024 lds\nM v[112:115],v[216:219],a[88:91],v[112:115]\nds_read_b64_tr_b16 v[206:207],v242 offset:49920\nds_read_b64_tr_b16 v[208:209],v241 offset:50176\nM v[116:119],v[216:219],a[92:95],v[116:119]\nds_read_b64_tr_b16 v[210:211],v241 offset:50432\n" \
"ds_read_b64_tr_b16 v[212:213],v242 offset:50176\nM v[56:59],v[220:223],a[80:83],v[56:59]\nds_read_b64_tr_b16 v[214:215],v242 offset:50432\nds_read_b64_tr_b16 v[216:217],v241 offset:50688\nM v[60:63],v[220:223],a[84:87],v[60:63]\nds_read_b64_tr_b16 v[218:219],v241 offset:50944\nM v[120:123],v[220:223],a[88:91],v[120:123]\nbuffer_load_dwordx4 v226,s[36:39],s67 offen offset:2048 lds\n" \
"M v[124:127],v[220:223],a[92:95],v[124:127]\nds_read_b64_tr_b16 v[220:221],v242 offset:50688\ns_waitcnt lgkmcnt(13)\nM v[0:3],v[192:195],a[96:99],v[0:3]\nds_read_b64_tr_b16 v[222:223],v242 offset:50944\nM v[4:7],v[192:195],a[100:103],v[4:7]\nM v[64:67],v[192:195],a[104:107],v[64:67]\nM v[68:71],v[192:195],a[108:111],v[68:71]\n" \
"buffer_load_dwordx4 v227,s[36:39],s67 offen offset:3072 lds\ns_waitcnt lgkmcnt(11)\nM v[8:11],v[196:199],a[96:99],v[8:11]\nds_read_b64_tr_b16 v[192:193],v241 offset:57344\nds_read_b64_tr_b16 v[194:195],v241 offset:57600\nM v[12:15],v[196:199],a[100:103],v[12:15]\ns_add_u32 m0,s65,20480\ns_add_u32 s67,s66,33408\n" \
"M v[72:75],v[196:199],a[104:107],v[72:75]\nM v[76:79],v[196:199],a[108:111],v[76:79]\nds_read_b64_tr_b16 v[196:197],v242 offset:57344\ns_waitcnt lgkmcnt(8)\nM v[16:19],v[200:203],a[96:99],v[16:19]\nbuffer_load_dwordx4 v224,s[36:39],s67 offen offset:0 lds\nM v[20:23],v[200:203],a[100:103],v[20:23]\nds_read_b64_tr_b16 v[198:199],v242 offset:57600\n" \
"M v[80:83],v[200:203],a[104:107],v[80:83]\nM v[84:87],v[200:203],a[108:111],v[84:87]\nds_read_b64_tr_b16 v[200:201],v241 offset:57856\nds_read_b64_tr_b16 v[202:203],v241 offset:58112\nM v[24:27],v[204:207],a[96:99],v[24:27]\nM v[28:31],v[204:207],a[100:103],v[28:31]\nbuffer_load_dwordx4 v225,s[36:39],s67 offen offset:1024 lds\nM v[88:91],v[204:207],a[104:107],v[88:91]\n" \
"M v[92:95],v[204:207],a[108:111],v[92:95]\nds_read_b64_tr_b16 v[204:205],v242 offset:57856\nds_read_b64_tr_b16 v[206:207],v242 offset:58112\nM v[32:35],v[208:211],a[96:99],v[32:35]\nM v[36:39],v[208:211],a[100:103],v[36:39]\nM v[96:99],v[208:211],a[104:107],v[96:99]\nbuffer_load_dwordx4 v226,s[36:39],s67 offen offset:2048 lds\nM v[100:103],v[208:211],a[108:111],v[100:103]\n" \
"ds_read_b64_tr_b16 v[208:209],v241 offset:58368\ns_waitcnt lgkmcnt(7)\nM v[40:43],v[212:215],a[96:99],v[40:43]\nds_read_b64_tr_b16 v[210:211],v241 offset:58624\nM v[44:47],v[212:215],a[100:103],v[44:47]\nM v[104:107],v[212:215],a[104:107],v[104:107]\nM v[108:111],v[212:215],a[108:111],v[108:111]\nbuffer_load_dwordx4 v227,s[36:39],s67 offen offset:3072 lds\n" \
"M v[48:51],v[216:219],a[96:99],v[48:51]\nds_read_b64_tr_b16 v[212:213],v242 offset:58368\nds_read_b64_tr_b16 v[214:215],v242 offset:58624\nM v[52:55],v[216:219],a[100:103],v[52:55]\ns_add_u32 m0,s64,114688\ns_add_u32 s69,s68,2621440\nM v[112:115],v[216:219],a[104:107],v[112:115]\nM v[116:119],v[216:219],a[108:111],v[116:119]\n" \
"ds_read_b64_tr_b16 v[216:217],v241 offset:58880\nds_read_b64_tr_b16 v[218:219],v241 offset:59136\nM v[56:59],v[220:223],a[96:99],v[56:59]\nbuffer_load_dwordx4 v243,s[40:43],s69 offen offset:0 lds\nM v[60:63],v[220:223],a[100:103],v[60:63]\nM v[120:123],v[220:223],a[104:107],v[120:123]\nM v[124:127],v[220:223],a[108:111],v[124:127]\nds_read_b64_tr_b16 v[220:221],v242 offset:58880\n" \
"ds_read_b64_tr_b16 v[222:223],v242 offset:59136\nM v[0:3],v[192:195],a[112:115],v[0:3]\nbuffer_load_dwordx2 v[166:167],v165,s[52:55],s80 offen sc1\nM v[4:7],v[192:195],a[116:119],v[4:7]\nbuffer_load_dwordx4 v244,s[40:43],s69 offen offset:1024 lds\nM v[64:67],v[192:195],a[120:123],v[64:67]\nM v[68:71],v[192:195],a[124:127],v[68:71]\ns_waitcnt lgkmcnt(6)\n" \
"M v[8:11],v[196:199],a[112:115],v[8:11]\nM v[12:15],v[196:199],a[116:119],v[12:15]\nM v[72:75],v[196:199],a[120:123],v[72:75]\nbuffer_load_dwordx4 v245,s[40:43],s69 offen offset:2048 lds\nM v[76:79],v[196:199],a[124:127],v[76:79]\nM v[16:19],v[200:203],a[112:115],v[16:19]\nM v[20:23],v[200:203],a[116:119],v[20:23]\nM v[80:83],v[200:203],a[120:123],v[80:83]\n" \
"M v[84:87],v[200:203],a[124:127],v[84:87]\nbuffer_load_dwordx4 v246,s[40:43],s69 offen offset:3072 lds\nM v[24:27],v[204:207],a[112:115],v[24:27]\nM v[28:31],v[204:207],a[116:119],v[28:31]\ns_add_u32 m0,s65,0\ns_add_u32 s67,s66,768\nM v[88:91],v[204:207],a[120:123],v[88:91]\ns_waitcnt vmcnt(13) lgkmcnt(0)\n" \
"s_barrier\nM v[92:95],v[204:207],a[124:127],v[92:95]\nds_read_b128 a[128:131],v228 offset:8192\nds_read_b64_tr_b16 v[192:193],v241 offset:0\nM v[32:35],v[208:211],a[112:115],v[32:35]\nbuffer_load_dwordx4 v224,s[36:39],s67 offen offset:0 lds\nM v[36:39],v[208:211],a[116:119],v[36:39]\nds_read_b64_tr_b16 v[194:195],v241 offset:256\n" \
"ds_read_b128 a[132:135],v228 offset:10240\nM v[96:99],v[208:211],a[120:123],v[96:99]\nds_read_b128 a[136:139],v228 offset:12288\nM v[100:103],v[208:211],a[124:127],v[100:103]\nds_read_b128 a[140:143],v228 offset:14336\nM v[40:43],v[212:215],a[112:115],v[40:43]\nds_read_b64_tr_b16 v[196:197],v242 offset:0\nds_read_b64_tr_b16 v[198:199],v242 offset:256\n" \
"M v[44:47],v[212:215],a[116:119],v[44:47]\nbuffer_load_dwordx4 v225,s[36:39],s67 offen offset:1024 lds\nM v[104:107],v[212:215],a[120:123],v[104:107]\nds_read_b64_tr_b16 v[200:201],v241 offset:512\nds_read_b64_tr_b16 v[202:203],v241 offset:768\nM v[108:111],v[212:215],a[124:127],v[108:111]\nds_read_b64_tr_b16 v[204:205],v242 offset:512\nds_read_b64_tr_b16 v[206:207],v242 offset:768\n" \
"M v[48:51],v[216:219],a[112:115],v[48:51]\nds_read_b64_tr_b16 v[208:209],v241 offset:1024\nds_read_b64_tr_b16 v[210:211],v241 offset:1280\nM v[52:55],v[216:219],a[116:119],v[52:55]\nds_read_b64_tr_b16 v[212:213],v242 offset:1024\nds_read_b64_tr_b16 v[214:215],v242 offset:1280\nM v[112:115],v[216:219],a[120:123],v[112:115]\nbuffer_load_dwordx4 v226,s[36:39],s67 offen offset:2048 lds\n" \
"M v[116:119],v[216:219],a[124:127],v[116:119]\nds_read_b64_tr_b16 v[216:217],v241 offset:1536\nds_read_b64_tr_b16 v[218:219],v241 offset:1792\nM v[56:59],v[220:223],a[112:115],v[56:59]\nds_read_b128 a[144:147],v229 offset:8192\nds_read_b128 a[148:151],v229 offset:10240\nM v[60:63],v[220:223],a[116:119],v[60:63]\nds_read_b128 a[152:155],v229 offset:12288\n" \
"M v[120:123],v[220:223],a[120:123],v[120:123]\nds_read_b128 a[156:159],v229 offset:14336\nM v[124:127],v[220:223],a[124:127],v[124:127]\nbuffer_load_dwordx4 v227,s[36:39],s67 offen offset:3072 lds\ns_waitcnt lgkmcnt(14)\nM v[0:3],v[192:195],a[128:131],v[0:3]\nds_read_b64_tr_b16 v[220:221],v242 offset:1536\nds_read_b64_tr_b16 v[222:223],v242 offset:1792\n" \
"M v[4:7],v[192:195],a[132:135],v[4:7]\ns_add_u32 m0,s65,4096\ns_add_u32 s67,s66,33536\nM v[64:67],v[192:195],a[136:139],v[64:67]\nM v[68:71],v[192:195],a[140:143],v[68:71]\nds_read_b64_tr_b16 v[192:193],v241 offset:8192\nds_read_b64_tr_b16 v[194:195],v241 offset:8448\nM v[8:11],v[196:199],a[128:131],v[8:11]\n" \
"buffer_load_dwordx4 v224,s[36:39],s67 offen offset:0 lds\nM v[12:15],v[196:199],a[132:135],v[12:15]\nM v[72:75],v[196:199],a[136:139],v[72:75]\nM v[76:79],v[196:199],a[140:143],v[76:79]\nds_read_b64_tr_b16 v[196:197],v242 offset:8192\ns_waitcnt lgkmcnt(9)\nM v[16:19],v[200:203],a[128:131],v[16:19]\nds_read_b64_tr_b16 v[198:199],v242 offset:8448\n" \
"M v[20:23],v[200:203],a[132:135],v[20:23]\nbuffer_load_dwordx4 v225,s[36:39],s67 offen offset:1024 lds\nM v[80:83],v[200:203],a[136:139],v[80:83]\nM v[84:87],v[200:203],a[140:143],v[84:87]\nds_read_b64_tr_b16 v[200:201],v241 offset:8704\nds_read_b64_tr_b16 v[202:203],v241 offset:8960\nM v[24:27],v[204:207],a[128:131],v[24:27]\nM v[28:31],v[204:207],a[132:135],v[28:31]\n" \
"M v[88:91],v[204:207],a[136:139],v[88:91]\nbuffer_load_dwordx4 v226,s[36:39],s67 offen offset:2048 lds\nM v[92:95],v[204:207],a[140:143],v[92:95]\nds_read_b64_tr_b16 v[204:205],v242 offset:8704\nds_read_b64_tr_b16 v[206:207],v242 offset:8960\nM v[32:35],v[208:211],a[128:131],v[32:35]\nM v[36:39],v[208:211],a[132:135],v[36:39]\nM v[96:99],v[208:211],a[136:139],v[96:99]\n" \
"M v[100:103],v[208:211],a[140:143],v[100:103]\nbuffer_load_dwordx4 v227,s[36:39],s67 offen offset:3072 lds\nM v[40:43],v[212:215],a[128:131],v[40:43]\nds_read_b64_tr_b16 v[208:209],v241 offset:9216\nds_read_b64_tr_b16 v[210:211],v241 offset:9472\nM v[44:47],v[212:215],a[132:135],v[44:47]\ns_add_u32 m0,s64,131072\ns_add_u32 s69,s68,3145728\n" \
"M v[104:107],v[212:215],a[136:139],v[104:107]\nM v[108:111],v[212:215],a[140:143],v[108:111]\nds_read_b64_tr_b16 v[212:213],v242 offset:9216\nds_read_b64_tr_b16 v[214:215],v242 offset:9472\nM v[48:51],v[216:219],a[128:131],v[48:51]\nbuffer_load_dwordx4 v243,s[40:43],s69 offen offset:0 lds\nM v[52:55],v[216:219],a[132:135],v[52:55]\nM v[112:115],v[216:219],a[136:139],v[112:115]\n" \
"M v[116:119],v[216:219],a[140:143],v[116:119]\nds_read_b64_tr_b16 v[216:217],v241 offset:9728\ns_waitcnt lgkmcnt(7)\nM v[56:59],v[220:223],a[128:131],v[56:59]\nds_read_b64_tr_b16 v[218:219],v241 offset:9984\nM v[60:63],v[220:223],a[132:135],v[60:63]\nbuffer_load_dwordx4 v244,s[40:43],s69 offen offset:1024 lds\nM v[120:123],v[220:223],a[136:139],v[120:123]\n" \
"M v[124:127],v[220:223],a[140:143],v[124:127]\nds_read_b64_tr_b16 v[220:221],v242 offset:9728\nds_read_b64_tr_b16 v[222:223],v242 offset:9984\nM v[0:3],v[192:195],a[144:147],v[0:3]\nM v[4:7],v[192:195],a[148:151],v[4:7]\nM v[64:67],v[192:195],a[152:155],v[64:67]\nbuffer_load_dwordx4 v245,s[40:43],s69 offen offset:2048 lds\nM v[68:71],v[192:195],a[156:159],v[68:71]\n" \
"M v[8:11],v[196:199],a[144:147],v[8:11]\ns_waitcnt vmcnt(14)\nv_xor_b32 v166,v166,v230\nM v[12:15],v[196:199],a[148:151],v[12:15]\nv_xor_b32 v167,v167,v231\nv_or_b32 v166,v166,v167\nM v[72:75],v[196:199],a[152:155],v[72:75]\nv_cmp_ne_u32 vcc,0,v166\n" \
"M v[76:79],v[196:199],a[156:159],v[76:79]\nbuffer_load_dwordx4 v246,s[40:43],s69 offen offset:3072 lds\nM v[16:19],v[200:203],a[144:147],v[16:19]\ns_cbranch_vccnz .Lhs\n.Lhb:\nv_bfe_u32 v231,v249,2,3\nM v[20:23],v[200:203],a[148:151],v[20:23]\ns_add_u32 m0,s65,8192\n" \
"s_add_u32 s67,s66,896\nM v[80:83],v[200:203],a[152:155],v[80:83]\nv_mul_u32_u24 v231,3584,v231\nv_and_b32 v230,31,v249\nM v[84:87],v[200:203],a[156:159],v[84:87]\nv_lshlrev_b32 v230,7,v230\ns_waitcnt lgkmcnt(0)\nM v[24:27],v[204:207],a[144:147],v[24:27]\n" \
"buffer_load_dwordx4 v224,s[36:39],s67 offen offset:0 lds\nM v[28:31],v[204:207],a[148:151],v[28:31]\nM v[88:91],v[204:207],a[152:155],v[88:91]\ns_waitcnt vmcnt(13)\ns_barrier\nM v[92:95],v[204:207],a[156:159],v[92:95]\nds_read_b128 a[160:163],v228 offset:16384\nds_read_b64_tr_b16 v[192:193],v241 offset:16384\n" \
"M v[32:35],v[208:211],a[144:147],v[32:35]\nbuffer_load_dwordx4 v[152:155],v240,s[52:55],s71 offen sc1\nM v[36:39],v[208:211],a[148:151],v[36:39]\ns_add_u32 s71,s70,798720\nds_read_b64_tr_b16 v[194:195],v241 offset:16640\nM v[96:99],v[208:211],a[152:155],v[96:99]\nds_read_b128 a[164:167],v228 offset:18432\nds_read_b128 a[168:171],v228 offset:20480\n" \
"M v[100:103],v[208:211],a[156:159],v[100:103]\nbuffer_load_dwordx4 v225,s[36:39],s67 offen offset:1024 lds\nbuffer_load_dwordx4 v[156:159],v240,s[52:55],s71 offen sc1\nM v[40:43],v[212:215],a[144:147],v[40:43]\ns_add_u32 s71,s70,811008\nds_read_b128 a[172:175],v228 offset:22528\nM v[44:47],v[212:215],a[148:151],v[44:47]\nbuffer_load_dwordx4 v[160:163],v240,s[52:55],s71 offen sc1\n" \
"M v[104:107],v[212:215],a[152:155],v[104:107]\nbuffer_load_dwordx4 v226,s[36:39],s67 offen offset:2048 lds\ns_add_u32 s71,s70,823296\nbuffer_load_dwordx4 v[164:167],v240,s[52:55],s71 offen sc1\nM v[108:111],v[212:215],a[156:159],v[108:111]\nds_read_b64_tr_b16 v[196:197],v242 offset:16384\nds_read_b64_tr_b16 v[198:199],v242 offset:16640\nM v[48:51],v[216:219],a[144:147],v[48:51]\n" \
"ds_read_b64_tr_b16 v[200:201],v241 offset:16896\nds_read_b64_tr_b16 v[202:203],v241 offset:17152\nM v[52:55],v[216:219],a[148:151],v[52:55]\nds_read_b64_tr_b16 v[204:205],v242 offset:16896\nds_read_b64_tr_b16 v[206:207],v242 offset:17152\nM v[112:115],v[216:219],a[152:155],v[112:115]\nds_read_b64_tr_b16 v[208:209],v241 offset:17408\nds_read_b64_tr_b16 v[210:211],v241 offset:17664\n" \
"M v[116:119],v[216:219],a[156:159],v[116:119]\nbuffer_load_dwordx4 v227,s[36:39],s67 offen offset:3072 lds\nM v[56:59],v[220:223],a[144:147],v[56:59]\nds_read_b64_tr_b16 v[212:213],v242 offset:17408\nds_read_b64_tr_b16 v[214:215],v242 offset:17664\nM v[60:63],v[220:223],a[148:151],v[60:63]\ns_add_u32 m0,s65,12288\ns_add_u32 s67,s66,33664\n" \
"M v[120:123],v[220:223],a[152:155],v[120:123]\nds_read_b64_tr_b16 v[216:217],v241 offset:17920\nds_read_b64_tr_b16 v[218:219],v241 offset:18176\nM v[124:127],v[220:223],a[156:159],v[124:127]\nds_read_b64_tr_b16 v[220:221],v242 offset:17920\ns_waitcnt lgkmcnt(13)\nM v[0:3],v[192:195],a[160:163],v[0:3]\nds_read_b64_tr_b16 v[222:223],v242 offset:18176\n" \
"ds_read_b128 a[176:179],v229 offset:16384\nM v[4:7],v[192:195],a[164:167],v[4:7]\nds_read_b128 a[180:183],v229 offset:18432\ns_lshl_b32 s71,s6,5\nM v[64:67],v[192:195],a[168:171],v[64:67]\nds_read_b128 a[184:187],v229 offset:20480\ns_add_u32 s71,s71,790528\nM v[68:71],v[192:195],a[172:175],v[68:71]\n" \
"ds_read_b64_tr_b16 v[192:193],v241 offset:24576\ns_waitcnt lgkmcnt(13)\nM v[8:11],v[196:199],a[160:163],v[8:11]\nds_read_b64_tr_b16 v[194:195],v241 offset:24832\nds_read_b128 a[188:191],v229 offset:22528\nM v[12:15],v[196:199],a[164:167],v[12:15]\ns_cmp_lt_u32 s7,2\ns_cselect_b32 s70,0,1.0\n" \
"M v[72:75],v[196:199],a[168:171],v[72:75]\nglobal_load_dword v247,v253,s[12:13]\nM v[76:79],v[196:199],a[172:175],v[76:79]\nds_read_b64_tr_b16 v[196:197],v242 offset:24576\nds_read_b64_tr_b16 v[198:199],v242 offset:24832\nM v[16:19],v[200:203],a[160:163],v[16:19]\nv_add_u32 v253,0x100000,v253\nM v[20:23],v[200:203],a[164:167],v[20:23]\n" \
"M v[80:83],v[200:203],a[168:171],v[80:83]\nbuffer_load_dword v247,v254,s[56:59],s78 offen\nM v[84:87],v[200:203],a[172:175],v[84:87]\nds_read_b64_tr_b16 v[200:201],v241 offset:25088\ns_waitcnt lgkmcnt(8)\nM v[24:27],v[204:207],a[160:163],v[24:27]\nds_read_b64_tr_b16 v[202:203],v241 offset:25344\nv_and_b32 v254,3,v249\n" \
"M v[28:31],v[204:207],a[164:167],v[28:31]\ns_cmp_eq_u32 s62,5\ns_cselect_b64 s[56:57],s[22:23],s[24:25]\nM v[88:91],v[204:207],a[168:171],v[88:91]\nbuffer_load_dwordx4 v224,s[36:39],s67 offen offset:0 lds\nM v[92:95],v[204:207],a[172:175],v[92:95]\nds_read_b64_tr_b16 v[204:205],v242 offset:25088\nds_read_b64_tr_b16 v[206:207],v242 offset:25344\n" \
"M v[32:35],v[208:211],a[160:163],v[32:35]\nv_lshl_add_u32 v231,v254,7,v231\nM v[36:39],v[208:211],a[164:167],v[36:39]\ns_add_u32 s69,s68,3670016\nM v[96:99],v[208:211],a[168:171],v[96:99]\nbuffer_load_dwordx4 v225,s[36:39],s67 offen offset:1024 lds\nM v[100:103],v[208:211],a[172:175],v[100:103]\nds_read_b64_tr_b16 v[208:209],v241 offset:25600\n" \
"ds_read_b64_tr_b16 v[210:211],v241 offset:25856\nM v[40:43],v[212:215],a[160:163],v[40:43]\nM v[44:47],v[212:215],a[164:167],v[44:47]\nM v[104:107],v[212:215],a[168:171],v[104:107]\nbuffer_load_dwordx4 v226,s[36:39],s67 offen offset:2048 lds\nM v[108:111],v[212:215],a[172:175],v[108:111]\nds_read_b64_tr_b16 v[212:213],v242 offset:25600\nds_read_b64_tr_b16 v[214:215],v242 offset:25856\n" \
"M v[48:51],v[216:219],a[160:163],v[48:51]\nM v[52:55],v[216:219],a[164:167],v[52:55]\nM v[112:115],v[216:219],a[168:171],v[112:115]\nbuffer_load_dwordx4 v227,s[36:39],s67 offen offset:3072 lds\nM v[116:119],v[216:219],a[172:175],v[116:119]\ns_add_u32 m0,s64,147456\nds_read_b64_tr_b16 v[216:217],v241 offset:26112\nM v[56:59],v[220:223],a[160:163],v[56:59]\n" \
"s_cmp_eq_u32 s62,0\ns_cselect_b32 s36,0x8000,0\nM v[60:63],v[220:223],a[164:167],v[60:63]\nds_read_b64_tr_b16 v[218:219],v241 offset:26368\ns_add_u32 s36,s36,s64\nM v[120:123],v[220:223],a[168:171],v[120:123]\nbuffer_load_dwordx4 v243,s[40:43],s69 offen offset:0 lds\nM v[124:127],v[220:223],a[172:175],v[124:127]\n" \
"ds_read_b64_tr_b16 v[220:221],v242 offset:26112\ns_waitcnt lgkmcnt(6)\nM v[0:3],v[192:195],a[176:179],v[0:3]\nds_read_b64_tr_b16 v[222:223],v242 offset:26368\nM v[4:7],v[192:195],a[180:183],v[4:7]\nM v[64:67],v[192:195],a[184:187],v[64:67]\nbuffer_load_dwordx4 v244,s[40:43],s69 offen offset:1024 lds\nM v[68:71],v[192:195],a[188:191],v[68:71]\n" \
"M v[8:11],v[196:199],a[176:179],v[8:11]\nM v[12:15],v[196:199],a[180:183],v[12:15]\nM v[72:75],v[196:199],a[184:187],v[72:75]\nbuffer_load_dwordx4 v245,s[40:43],s69 offen offset:2048 lds\nM v[76:79],v[196:199],a[188:191],v[76:79]\nM v[16:19],v[200:203],a[176:179],v[16:19]\nM v[20:23],v[200:203],a[180:183],v[20:23]\nM v[80:83],v[200:203],a[184:187],v[80:83]\n" \
"global_load_dword v247,v253,s[12:13]\nM v[84:87],v[200:203],a[188:191],v[84:87]\nbuffer_load_dwordx4 v246,s[40:43],s69 offen offset:3072 lds\nM v[24:27],v[204:207],a[176:179],v[24:27]\nv_add_u32 v253,0x100000,v253\ns_add_u32 m0,s64,98304\nM v[28:31],v[204:207],a[180:183],v[28:31]\ns_sub_u32 s40,s16,0x1000\n" \
"s_subb_u32 s41,s17,0\nM v[88:91],v[204:207],a[184:187],v[88:91]\ns_waitcnt vmcnt(19) lgkmcnt(0)\ns_barrier\nM v[92:95],v[204:207],a[188:191],v[92:95]\nds_read_b128 a[192:195],v228 offset:0\nds_read_b64_tr_b16 v[192:193],v241 offset:32768\nM v[32:35],v[208:211],a[176:179],v[32:35]\n" \
"ds_read_b64_tr_b16 v[194:195],v241 offset:33024\nds_read_b128 a[196:199],v228 offset:2048\nM v[36:39],v[208:211],a[180:183],v[36:39]\nds_read_b128 a[200:203],v228 offset:4096\ns_add_u32 s69,s68,0\nM v[96:99],v[208:211],a[184:187],v[96:99]\nds_read_b128 a[204:207],v228 offset:6144\ns_mov_b32 s16,0\n" \
"M v[100:103],v[208:211],a[188:191],v[100:103]\nds_read_b64_tr_b16 v[196:197],v242 offset:32768\ns_mov_b32 s17,3\nM v[40:43],v[212:215],a[176:179],v[40:43]\nds_read_b64_tr_b16 v[198:199],v242 offset:33024\nds_read_b64_tr_b16 v[200:201],v241 offset:33280\nM v[44:47],v[212:215],a[180:183],v[44:47]\nds_read_b64_tr_b16 v[202:203],v241 offset:33536\n" \
"ds_read_b64_tr_b16 v[204:205],v242 offset:33280\nM v[104:107],v[212:215],a[184:187],v[104:107]\nds_read_b64_tr_b16 v[206:207],v242 offset:33536\nds_read_b64_tr_b16 v[208:209],v241 offset:33792\nM v[108:111],v[212:215],a[188:191],v[108:111]\nds_read_b64_tr_b16 v[210:211],v241 offset:34048\nds_read_b64_tr_b16 v[212:213],v242 offset:33792\nM v[48:51],v[216:219],a[176:179],v[48:51]\n" \
"ds_read_b64_tr_b16 v[214:215],v242 offset:34048\nds_read_b128 a[208:211],v229 offset:0\nM v[52:55],v[216:219],a[180:183],v[52:55]\nds_read_b128 a[212:215],v229 offset:2048\ns_waitcnt vmcnt(14)\nv_add_f32 v152,v152,v156\nM v[112:115],v[216:219],a[184:187],v[112:115]\nds_read_b128 a[216:219],v229 offset:4096\n" \
"v_add_f32 v153,v153,v157\nM v[116:119],v[216:219],a[188:191],v[116:119]\nds_read_b64_tr_b16 v[216:217],v241 offset:34304\nv_add_f32 v154,v154,v158\nM v[56:59],v[220:223],a[176:179],v[56:59]\nds_read_b64_tr_b16 v[218:219],v241 offset:34560\nds_read_b128 a[220:223],v229 offset:6144\nM v[60:63],v[220:223],a[180:183],v[60:63]\n" \
"s_waitcnt vmcnt(12)\nv_add_f32 v160,v160,v164\nM v[120:123],v[220:223],a[184:187],v[120:123]\nv_add_f32 v161,v161,v165\nv_add_f32 v162,v162,v166\nM v[124:127],v[220:223],a[188:191],v[124:127]\nds_read_b64_tr_b16 v[220:221],v242 offset:34304\ns_waitcnt lgkmcnt(12)\n" \
"M v[0:3],v[192:195],a[192:195],v[0:3]\nds_read_b64_tr_b16 v[222:223],v242 offset:34560\nv_add_f32 v155,v155,v159\nM v[4:7],v[192:195],a[196:199],v[4:7]\nv_add_f32 v163,v163,v167\nv_add_f32 v152,v152,v160\nM v[64:67],v[192:195],a[200:203],v[64:67]\nbuffer_load_dword v168,v169,s[52:55],s71 offen sc1\n" \
"M v[68:71],v[192:195],a[204:207],v[68:71]\nds_read_b64_tr_b16 v[192:193],v241 offset:40960\nds_read_b64_tr_b16 v[194:195],v241 offset:41216\nM v[8:11],v[196:199],a[192:195],v[8:11]\nv_add_f32 v153,v153,v161\nv_add_f32 v154,v154,v162\nM v[12:15],v[196:199],a[196:199],v[12:15]\nv_add_f32 v155,v155,v163\n" \
"v_cvt_pk_bf16_f32 v152,0,v152\nM v[72:75],v[196:199],a[200:203],v[72:75]\nv_cvt_pk_bf16_f32 v153,0,v153\nv_cvt_pk_bf16_f32 v154,0,v154\nM v[76:79],v[196:199],a[204:207],v[76:79]\nds_read_b64_tr_b16 v[196:197],v242 offset:40960\nds_read_b64_tr_b16 v[198:199],v242 offset:41216\nM v[16:19],v[200:203],a[192:195],v[16:19]\n" \
"v_cvt_pk_bf16_f32 v155,0,v155\nv_add_f32 v152,s70,v152\nM v[20:23],v[200:203],a[196:199],v[20:23]\nv_add_f32 v153,s70,v153\nv_add_f32 v154,s70,v154\nM v[80:83],v[200:203],a[200:203],v[80:83]\nv_add_f32 v155,s70,v155\ns_lshr_b32 s71,s64,2\n" \
"M v[84:87],v[200:203],a[204:207],v[84:87]\nds_read_b64_tr_b16 v[200:201],v241 offset:41472\ns_waitcnt lgkmcnt(5)\nM v[24:27],v[204:207],a[192:195],v[24:27]\nds_read_b64_tr_b16 v[202:203],v241 offset:41728\nv_cvt_pk_bf16_f32 v152,0,v152\nM v[28:31],v[204:207],a[196:199],v[28:31]\nv_cvt_pk_bf16_f32 v153,0,v153\n" \
"v_cvt_pk_bf16_f32 v154,0,v154\nM v[88:91],v[204:207],a[200:203],v[88:91]\nv_cvt_pk_bf16_f32 v155,0,v155\nv_add_u32 v171,s71,v240\nM v[92:95],v[204:207],a[204:207],v[92:95]\nds_read_b64_tr_b16 v[204:205],v242 offset:41472\nds_read_b64_tr_b16 v[206:207],v242 offset:41728\nM v[32:35],v[208:211],a[192:195],v[32:35]\n" \
"ds_write_b128 v171,v[152:155] offset:16384\nM v[36:39],v[208:211],a[196:199],v[36:39]\nbuffer_load_dwordx4 v243,s[40:43],s69 offen offset:0 lds\nM v[96:99],v[208:211],a[200:203],v[96:99]\ns_lshl_b32 s70,s7,8\nv_lshl_add_u32 v251,v251,5,s70\nM v[100:103],v[208:211],a[204:207],v[100:103]\nds_read_b64_tr_b16 v[208:209],v241 offset:41984\n" \
"ds_read_b64_tr_b16 v[210:211],v241 offset:42240\nM v[40:43],v[212:215],a[192:195],v[40:43]\ns_lshl_b32 s71,s6,5\ns_and_b32 s71,s71,0xfffffe00\nM v[44:47],v[212:215],a[196:199],v[44:47]\nglobal_load_dword v247,v253,s[12:13]\nM v[104:107],v[212:215],a[200:203],v[104:107]\nv_add_u32 v253,0x100000,v253\n" \
"s_xor_b32 s70,s63,16\nM v[108:111],v[212:215],a[204:207],v[108:111]\nds_read_b64_tr_b16 v[212:213],v242 offset:41984\nds_read_b64_tr_b16 v[214:215],v242 offset:42240\nM v[48:51],v[216:219],a[192:195],v[48:51]\nbuffer_load_dwordx4 v244,s[40:43],s69 offen offset:1024 lds\nM v[52:55],v[216:219],a[196:199],v[52:55]\ns_lshr_b32 s70,s70,2\n" \
"s_lshl3_add_u32 s70,s62,s70\nM v[112:115],v[216:219],a[200:203],v[112:115]\ns_lshl_b32 s70,s70,15\ns_add_u32 s70,s70,s64\nM v[116:119],v[216:219],a[204:207],v[116:119]\nds_read_b64_tr_b16 v[216:217],v241 offset:42496\nds_read_b64_tr_b16 v[218:219],v241 offset:42752\nM v[56:59],v[220:223],a[192:195],v[56:59]\n" \
"buffer_load_dwordx4 v245,s[40:43],s69 offen offset:2048 lds\nM v[60:63],v[220:223],a[196:199],v[60:63]\nM v[120:123],v[220:223],a[200:203],v[120:123]\nM v[124:127],v[220:223],a[204:207],v[124:127]\nds_read_b64_tr_b16 v[220:221],v242 offset:42496\ns_waitcnt lgkmcnt(5)\nM v[0:3],v[192:195],a[208:211],v[0:3]\nbuffer_load_dwordx4 v246,s[40:43],s69 offen offset:3072 lds\n" \
"M v[4:7],v[192:195],a[212:215],v[4:7]\nds_read_b64_tr_b16 v[222:223],v242 offset:42752\ns_add_u32 m0,s64,114688\nM v[64:67],v[192:195],a[216:219],v[64:67]\ns_add_u32 s69,s68,524288\nM v[68:71],v[192:195],a[220:223],v[68:71]\nM v[8:11],v[196:199],a[208:211],v[8:11]\nbuffer_store_dwordx2 v[232:233],off,s[52:55],s72 sc1\n" \
"M v[12:15],v[196:199],a[212:215],v[12:15]\ns_and_b32 s72,s6,1\ns_lshl_b32 s72,s72,8\nM v[72:75],v[196:199],a[216:219],v[72:75]\ns_add_u32 s71,s71,s72\ns_and_b32 s72,s6,14\nM v[76:79],v[196:199],a[220:223],v[76:79]\ns_lshl1_add_u32 s71,s72,s71\n" \
"s_or_b32 s71,s71,s75\nM v[16:19],v[200:203],a[208:211],v[16:19]\nbuffer_load_dwordx4 v243,s[40:43],s69 offen offset:0 lds\nM v[20:23],v[200:203],a[212:215],v[20:23]\ns_mov_b32 s72,0x3e0293ee\nM v[80:83],v[200:203],a[216:219],v[80:83]\nM v[84:87],v[200:203],a[220:223],v[84:87]\nM v[24:27],v[204:207],a[208:211],v[24:27]\n" \
"buffer_load_dwordx4 v244,s[40:43],s69 offen offset:1024 lds\nM v[28:31],v[204:207],a[212:215],v[28:31]\nM v[88:91],v[204:207],a[216:219],v[88:91]\ns_waitcnt vmcnt(9) lgkmcnt(0)\ns_barrier\nM v[92:95],v[204:207],a[220:223],v[92:95]\nds_read_b128 a[224:227],v228 offset:8192\nds_read_b64_tr_b16 v[192:193],v241 offset:49152\n" \
"M v[32:35],v[208:211],a[208:211],v[32:35]\nds_read_b64_tr_b16 v[194:195],v241 offset:49408\nds_read_b128 a[228:231],v228 offset:10240\nM v[36:39],v[208:211],a[212:215],v[36:39]\nbuffer_load_dwordx4 v245,s[40:43],s69 offen offset:2048 lds\nM v[96:99],v[208:211],a[216:219],v[96:99]\nds_read_b128 a[232:235],v228 offset:12288\nds_read_b128 a[236:239],v228 offset:14336\n" \
"M v[100:103],v[208:211],a[220:223],v[100:103]\nds_read_b64_tr_b16 v[196:197],v242 offset:49152\nv_and_b32 v228,15,v249\nM v[40:43],v[212:215],a[208:211],v[40:43]\nds_read_b64_tr_b16 v[198:199],v242 offset:49408\nds_read_b64_tr_b16 v[200:201],v241 offset:49664\nM v[44:47],v[212:215],a[212:215],v[44:47]\nds_read_b64_tr_b16 v[202:203],v241 offset:49920\n" \
"ds_read_b64_tr_b16 v[204:205],v242 offset:49664\nM v[104:107],v[212:215],a[216:219],v[104:107]\nds_read_b64_tr_b16 v[206:207],v242 offset:49920\nds_read_b64_tr_b16 v[208:209],v241 offset:50176\nM v[108:111],v[212:215],a[220:223],v[108:111]\nds_read_b64_tr_b16 v[210:211],v241 offset:50432\nds_read_b64_tr_b16 v[212:213],v242 offset:50176\nM v[48:51],v[216:219],a[208:211],v[48:51]\n" \
"ds_read_b64_tr_b16 v[214:215],v242 offset:50432\nds_read_b128 a[240:243],v229 offset:8192\nM v[52:55],v[216:219],a[212:215],v[52:55]\nds_read_b128 a[244:247],v229 offset:10240\nv_lshlrev_b32 v228,2,v228\nM v[112:115],v[216:219],a[216:219],v[112:115]\nglobal_load_dword v247,v253,s[12:13]\nM v[116:119],v[216:219],a[220:223],v[116:119]\n" \
"ds_read_b64_tr_b16 v[216:217],v241 offset:50688\nds_read_b64_tr_b16 v[218:219],v241 offset:50944\nM v[56:59],v[220:223],a[208:211],v[56:59]\nds_read_b128 a[248:251],v229 offset:12288\nds_read_b128 a[252:255],v229 offset:14336\nM v[60:63],v[220:223],a[212:215],v[60:63]\nds_read_b128 v[176:179],v251 offset:19456\nv_and_b32 v229,16,v249\n" \
"M v[120:123],v[220:223],a[216:219],v[120:123]\nds_read_b128 v[160:163],v251 offset:17408\nv_lshl_or_b32 v228,v229,4,v228\nM v[124:127],v[220:223],a[220:223],v[124:127]\nds_read_b64_tr_b16 v[220:221],v242 offset:50688\ns_waitcnt lgkmcnt(15)\nM v[0:3],v[192:195],a[224:227],v[0:3]\nds_read_b64_tr_b16 v[222:223],v242 offset:50944\n" \
"ds_read_b128 v[172:175],v251 offset:18448\nM v[4:7],v[192:195],a[228:231],v[4:7]\nds_read_b128 v[180:183],v251 offset:19472\nv_and_b32 v229,15,v249\nM v[64:67],v[192:195],a[232:235],v[64:67]\nds_read_b128 v[152:155],v251 offset:16384\nv_lshlrev_b32 v229,7,v229\nM v[68:71],v[192:195],a[236:239],v[68:71]\n" \
"ds_read_b64_tr_b16 v[192:193],v241 offset:57344\nv_add_u32 v253,0xffd00000,v253\nM v[8:11],v[196:199],a[224:227],v[8:11]\nds_read_b64_tr_b16 v[194:195],v241 offset:57600\nds_read_b128 v[156:159],v251 offset:16400\nM v[12:15],v[196:199],a[228:231],v[12:15]\nds_read_b128 v[164:167],v251 offset:17424\nv_lshrrev_b32 v253,4,v249\n" \
"M v[72:75],v[196:199],a[232:235],v[72:75]\nbuffer_load_dwordx4 v246,s[40:43],s69 offen offset:3072 lds\nM v[76:79],v[196:199],a[236:239],v[76:79]\nds_read_b64_tr_b16 v[196:197],v242 offset:57344\nds_read_b64_tr_b16 v[198:199],v242 offset:57600\nM v[16:19],v[200:203],a[224:227],v[16:19]\ns_waitcnt vmcnt(11)\nv_add_f32_dpp v168,v168,v168 quad_perm:[1,0,3,2] row_mask:0xf bank_mask:0xf\n" \
"M v[20:23],v[200:203],a[228:231],v[20:23]\ns_waitcnt lgkmcnt(11)\nv_mul_f32 v144,v144,v176\nM v[80:83],v[200:203],a[232:235],v[80:83]\nv_add_f32_dpp v168,v168,v168 quad_perm:[2,3,0,1] row_mask:0xf bank_mask:0xf\nv_cvt_pk_bf16_f32 v168,0,v168\nM v[84:87],v[200:203],a[236:239],v[84:87]\nds_read_b64_tr_b16 v[200:201],v241 offset:57856\n" \
"ds_read_b64_tr_b16 v[202:203],v241 offset:58112\nM v[24:27],v[204:207],a[224:227],v[24:27]\nbuffer_store_dword v168,v170,s[52:55],s71 offen sc1\nM v[28:31],v[204:207],a[228:231],v[28:31]\nds_read_b128 v[168:171],v251 offset:18432\nv_mul_f32 v145,v145,v177\nM v[88:91],v[204:207],a[232:235],v[88:91]\nv_cvt_pk_bf16_f32 v144,0,v144\n" \
"v_cvt_pk_bf16_f32 v145,0,v145\nM v[92:95],v[204:207],a[236:239],v[92:95]\nds_read_b64_tr_b16 v[204:205],v242 offset:57856\nds_read_b64_tr_b16 v[206:207],v242 offset:58112\nM v[32:35],v[208:211],a[224:227],v[32:35]\nv_add_f32 v144,v144,v160\nv_add_f32 v145,v145,v161\nM v[36:39],v[208:211],a[228:231],v[36:39]\n" \
"v_cvt_pk_bf16_f32 v144,0,v144\nv_cvt_pk_bf16_f32 v145,0,v145\nM v[96:99],v[208:211],a[232:235],v[96:99]\nv_mul_f32 v144,s72,v144\nv_mul_f32 v145,s72,v145\nM v[100:103],v[208:211],a[236:239],v[100:103]\nds_read_b64_tr_b16 v[208:209],v241 offset:58368\nds_read_b64_tr_b16 v[210:211],v241 offset:58624\n" \
"M v[40:43],v[212:215],a[224:227],v[40:43]\nv_cvt_pk_bf16_f32 v236,v144,v145\nv_mul_f32 v146,v146,v178\nM v[44:47],v[212:215],a[228:231],v[44:47]\nv_mul_f32 v147,v147,v179\nv_cvt_pk_bf16_f32 v146,0,v146\nM v[104:107],v[212:215],a[232:235],v[104:107]\nv_cvt_pk_bf16_f32 v147,0,v147\n" \
"v_add_f32 v146,v146,v162\nM v[108:111],v[212:215],a[236:239],v[108:111]\nds_read_b64_tr_b16 v[212:213],v242 offset:58368\nds_read_b64_tr_b16 v[214:215],v242 offset:58624\nM v[48:51],v[216:219],a[224:227],v[48:51]\nv_add_f32 v147,v147,v163\nv_cvt_pk_bf16_f32 v146,0,v146\nM v[52:55],v[216:219],a[228:231],v[52:55]\n" \
"v_cvt_pk_bf16_f32 v147,0,v147\nv_mul_f32 v146,s72,v146\nM v[112:115],v[216:219],a[232:235],v[112:115]\nv_mul_f32 v147,s72,v147\nv_cvt_pk_bf16_f32 v237,v146,v147\nM v[116:119],v[216:219],a[236:239],v[116:119]\nds_read_b64_tr_b16 v[216:217],v241 offset:58880\ns_waitcnt lgkmcnt(5)\n" \
"M v[56:59],v[220:223],a[224:227],v[56:59]\nds_read_b64_tr_b16 v[218:219],v241 offset:59136\ns_and_b32 s71,s63,2\nM v[60:63],v[220:223],a[228:231],v[60:63]\nv_mul_f32 v136,v136,v168\nv_mul_f32 v137,v137,v169\nM v[120:123],v[220:223],a[232:235],v[120:123]\nv_mul_f32 v138,v138,v170\n" \
"v_mul_f32 v139,v139,v171\nM v[124:127],v[220:223],a[236:239],v[124:127]\nds_read_b64_tr_b16 v[220:221],v242 offset:58880\nds_read_b64_tr_b16 v[222:223],v242 offset:59136\nM v[0:3],v[192:195],a[240:243],v[0:3]\nv_mul_f32 v140,v140,v172\nv_mul_f32 v141,v141,v173\nM v[4:7],v[192:195],a[244:247],v[4:7]\n" \
"v_mul_f32 v142,v142,v174\nv_mul_f32 v143,v143,v175\nM v[64:67],v[192:195],a[248:251],v[64:67]\nv_mul_f32 v148,v148,v180\nv_mul_f32 v149,v149,v181\nM v[68:71],v[192:195],a[252:255],v[68:71]\nv_cvt_pk_bf16_f32 v128,v0,v1\nv_cvt_pk_bf16_f32 v129,v2,v3\n" \
"M v[8:11],v[196:199],a[240:243],v[8:11]\nv_cvt_pk_bf16_f32 v144,v4,v5\nv_cvt_pk_bf16_f32 v145,v6,v7\nM v[12:15],v[196:199],a[244:247],v[12:15]\nv_cvt_pk_bf16_f32 v160,v64,v65\nv_cvt_pk_bf16_f32 v161,v66,v67\nM v[72:75],v[196:199],a[248:251],v[72:75]\nv_cvt_pk_bf16_f32 v176,v68,v69\n" \
"v_cvt_pk_bf16_f32 v177,v70,v71\nM v[76:79],v[196:199],a[252:255],v[76:79]\nv_cvt_pk_bf16_f32 v130,v8,v9\nv_cvt_pk_bf16_f32 v131,v10,v11\nM v[16:19],v[200:203],a[240:243],v[16:19]\nv_cvt_pk_bf16_f32 v146,v12,v13\nv_cvt_pk_bf16_f32 v147,v14,v15\nM v[20:23],v[200:203],a[244:247],v[20:23]\n" \
"s_lshl_b32 s71,s71,9\nv_cvt_pk_bf16_f32 v162,v72,v73\nM v[80:83],v[200:203],a[248:251],v[80:83]\nv_cvt_pk_bf16_f32 v163,v74,v75\nv_cvt_pk_bf16_f32 v136,0,v136\nM v[84:87],v[200:203],a[252:255],v[84:87]\nv_cvt_pk_bf16_f32 v137,0,v137\nv_cvt_pk_bf16_f32 v138,0,v138\n" \
"M v[24:27],v[204:207],a[240:243],v[24:27]\nv_cvt_pk_bf16_f32 v139,0,v139\nv_cvt_pk_bf16_f32 v140,0,v140\nM v[28:31],v[204:207],a[244:247],v[28:31]\nv_cvt_pk_bf16_f32 v141,0,v141\nv_cvt_pk_bf16_f32 v142,0,v142\nM v[88:91],v[204:207],a[248:251],v[88:91]\ns_waitcnt vmcnt(7) lgkmcnt(0)\n" \
"s_barrier\nM v[92:95],v[204:207],a[252:255],v[92:95]\nds_read_b64_tr_b16 v[192:193],v241 offset:0\nds_read_b64_tr_b16 v[194:195],v241 offset:256\nM v[32:35],v[208:211],a[240:243],v[32:35]\nds_read_b64_tr_b16 v[196:197],v242 offset:0\nds_read_b64_tr_b16 v[198:199],v242 offset:256\nM v[36:39],v[208:211],a[244:247],v[36:39]\n" \
"ds_read_b64_tr_b16 v[200:201],v241 offset:512\nds_read_b64_tr_b16 v[202:203],v241 offset:768\nM v[96:99],v[208:211],a[248:251],v[96:99]\nds_read_b64_tr_b16 v[204:205],v242 offset:512\nds_read_b64_tr_b16 v[206:207],v242 offset:768\nM v[100:103],v[208:211],a[252:255],v[100:103]\nds_read_b64_tr_b16 v[208:209],v241 offset:1024\nds_read_b64_tr_b16 v[210:211],v241 offset:1280\n" \
"M v[40:43],v[212:215],a[240:243],v[40:43]\nv_cvt_pk_bf16_f32 v143,0,v143\nv_cvt_pk_bf16_f32 v148,0,v148\nM v[44:47],v[212:215],a[244:247],v[44:47]\nv_cvt_pk_bf16_f32 v149,0,v149\nv_lshrrev_b32 v251,5,v249\nM v[104:107],v[212:215],a[248:251],v[104:107]\ns_add_u32 s70,s70,s71\n" \
"v_add_f32 v136,v136,v152\nM v[108:111],v[212:215],a[252:255],v[108:111]\nv_add_f32 v137,v137,v153\nv_add_f32 v138,v138,v154\nM v[48:51],v[216:219],a[240:243],v[48:51]\nv_add_f32 v139,v139,v155\nv_add_f32 v140,v140,v156\nM v[52:55],v[216:219],a[244:247],v[52:55]\n" \
"v_add_f32 v141,v141,v157\nv_add_f32 v142,v142,v158\nM v[112:115],v[216:219],a[248:251],v[112:115]\nv_add_f32 v143,v143,v159\nv_add_f32 v148,v148,v164\nM v[116:119],v[216:219],a[252:255],v[116:119]\nv_add_f32 v149,v149,v165\nv_lshlrev_b32 v251,11,v251\n" \
"M v[56:59],v[220:223],a[240:243],v[56:59]\ns_and_b32 s71,s63,1\nv_cvt_pk_bf16_f32 v178,v76,v77\nM v[60:63],v[220:223],a[244:247],v[60:63]\nv_cvt_pk_bf16_f32 v179,v78,v79\nv_cvt_pk_bf16_f32 v136,0,v136\nM v[120:123],v[220:223],a[248:251],v[120:123]\nv_cvt_pk_bf16_f32 v137,0,v137\n" \
"v_cvt_pk_bf16_f32 v138,0,v138\nM v[124:127],v[220:223],a[252:255],v[124:127]\nv_cvt_pk_bf16_f32 v139,0,v139\ns_waitcnt lgkmcnt(0)\nM v[0:3],a[0:3],v[192:195],0\nv_cvt_pk_bf16_f32 v140,0,v140\nv_cvt_pk_bf16_f32 v141,0,v141\nM v[4:7],a[4:7],v[192:195],0\n" \
"v_cvt_pk_bf16_f32 v142,0,v142\nv_cvt_pk_bf16_f32 v143,0,v143\nM v[64:67],a[8:11],v[192:195],0\nv_cvt_pk_bf16_f32 v148,0,v148\nv_cvt_pk_bf16_f32 v149,0,v149\nM v[68:71],a[12:15],v[192:195],0\nv_lshl_or_b32 v251,v250,8,v251\ns_lshl_b32 s71,s71,7\n" \
"M v[8:11],a[0:3],v[196:199],0\nv_mul_f32 v136,s72,v136\nv_mul_f32 v137,s72,v137\nM v[12:15],a[4:7],v[196:199],0\nv_mul_f32 v138,s72,v138\nv_mul_f32 v139,s72,v139\nM v[72:75],a[8:11],v[196:199],0\nv_mul_f32 v140,s72,v140\n" \
"v_mul_f32 v141,s72,v141\nM v[76:79],a[12:15],v[196:199],0\nv_mul_f32 v142,s72,v142\nv_mul_f32 v143,s72,v143\nv_mul_f32 v148,s72,v148\nv_mul_f32 v149,s72,v149\nv_and_b32 v250,0x70,v240\ns_add_u32 s70,s70,s71\n" \
"v_cvt_pk_bf16_f32 v132,v16,v17\nv_cvt_pk_bf16_f32 v133,v18,v19\nM v[16:19],a[0:3],v[200:203],0\nv_cvt_pk_bf16_f32 v232,v136,v137\nv_cvt_pk_bf16_f32 v233,v138,v139\nv_cvt_pk_bf16_f32 v234,v140,v141\nv_cvt_pk_bf16_f32 v235,v142,v143\nv_cvt_pk_bf16_f32 v238,v148,v149\n" \
"v_or_b32 v251,v251,v250\ns_add_u32 s70,s70,1048576\nbuffer_store_dwordx4 v[232:235],v251,s[52:55],s70 offen sc1\nv_cvt_pk_bf16_f32 v148,v20,v21\nv_cvt_pk_bf16_f32 v149,v22,v23\nM v[20:23],a[4:7],v[200:203],0\nv_mul_f32 v150,v150,v182\nv_mul_f32 v151,v151,v183\n" \
"v_cvt_pk_bf16_f32 v164,v80,v81\nv_cvt_pk_bf16_f32 v165,v82,v83\nds_read_b64_tr_b16 v[212:213],v242 offset:1024\nds_read_b64_tr_b16 v[214:215],v242 offset:1280\nM v[80:83],a[8:11],v[200:203],0\nv_cvt_pk_bf16_f32 v150,0,v150\nv_cvt_pk_bf16_f32 v151,0,v151\nv_add_f32 v150,v150,v166\n" \
"v_add_f32 v151,v151,v167\nv_cvt_pk_bf16_f32 v180,v84,v85\nv_cvt_pk_bf16_f32 v181,v86,v87\nM v[84:87],a[12:15],v[200:203],0\nv_cvt_pk_bf16_f32 v150,0,v150\nv_cvt_pk_bf16_f32 v151,0,v151\nv_mul_f32 v150,s72,v150\nv_mul_f32 v151,s72,v151\n" \
"v_cvt_pk_bf16_f32 v134,v24,v25\nv_cvt_pk_bf16_f32 v135,v26,v27\nM v[24:27],a[0:3],v[204:207],0\nv_cvt_pk_bf16_f32 v239,v150,v151\ns_add_u32 s71,s70,16384\nbuffer_store_dwordx4 v[236:239],v251,s[52:55],s71 offen sc1\nv_cvt_pk_bf16_f32 v150,v28,v29\nv_cvt_pk_bf16_f32 v151,v30,v31\n" \
"M v[28:31],a[4:7],v[204:207],0\nv_cvt_pk_bf16_f32 v166,v88,v89\nv_cvt_pk_bf16_f32 v167,v90,v91\nds_read_b64_tr_b16 v[216:217],v241 offset:1536\nds_read_b64_tr_b16 v[218:219],v241 offset:1792\nM v[88:91],a[8:11],v[204:207],0\nv_cvt_pk_bf16_f32 v182,v92,v93\nv_cvt_pk_bf16_f32 v183,v94,v95\n" \
"M v[92:95],a[12:15],v[204:207],0\nv_cvt_pk_bf16_f32 v136,v32,v33\nv_cvt_pk_bf16_f32 v137,v34,v35\nM v[32:35],a[0:3],v[208:211],0\ns_lshl_b32 s70,s62,5\ns_add_u32 s70,s70,s63\nv_cvt_pk_bf16_f32 v152,v36,v37\nv_cvt_pk_bf16_f32 v153,v38,v39\n" \
"M v[36:39],a[4:7],v[208:211],0\ns_lshl2_add_u32 s70,s70,s7\ns_lshl2_add_u32 s79,s70,90112\nv_cvt_pk_bf16_f32 v168,v96,v97\nv_cvt_pk_bf16_f32 v169,v98,v99\nds_read_b64_tr_b16 v[220:221],v242 offset:1536\nds_read_b64_tr_b16 v[222:223],v242 offset:1792\nM v[96:99],a[8:11],v[208:211],0\n" \
"s_lshl_b32 s70,s60,4\nv_cvt_pk_bf16_f32 v184,v100,v101\nv_cvt_pk_bf16_f32 v185,v102,v103\ns_sub_u32 s70,s70,s62\nM v[100:103],a[12:15],v[208:211],0\ns_add_u32 s70,s70,15\nv_cvt_pk_bf16_f32 v138,v40,v41\nv_cvt_pk_bf16_f32 v139,v42,v43\n" \
"s_lshl2_add_u32 s70,s70,s7\ns_waitcnt lgkmcnt(4)\nM v[40:43],a[0:3],v[212:215],0\ns_lshl_b32 s70,s70,15\ns_lshl_b32 s71,s61,8\nv_cvt_pk_bf16_f32 v154,v44,v45\nv_cvt_pk_bf16_f32 v155,v46,v47\ns_add_u32 s70,s70,s71\n" \
"M v[44:47],a[4:7],v[212:215],0\ns_add_u32 s78,s70,54525952\nv_cvt_pk_bf16_f32 v170,v104,v105\nv_cvt_pk_bf16_f32 v171,v106,v107\ns_lshl3_add_u32 s70,s62,s7\nds_read_b64_tr_b16 v[192:193],v241 offset:8192\nds_read_b64_tr_b16 v[194:195],v241 offset:8448\nM v[104:107],a[8:11],v[212:215],0\n" \
"s_lshl_b32 s80,s70,6\ns_lshl_b32 s76,s70,15\nv_cvt_pk_bf16_f32 v186,v108,v109\nv_cvt_pk_bf16_f32 v187,v110,v111\ns_lshl_b32 s70,s63,5\nM v[108:111],a[12:15],v[212:215],0\ns_add_u32 s70,s70,s74\nv_cvt_pk_bf16_f32 v140,v48,v49\n" \
"v_cvt_pk_bf16_f32 v141,v50,v51\ns_lshl_b32 s74,s70,14\ns_waitcnt lgkmcnt(2)\nM v[48:51],a[0:3],v[216:219],0\ns_add_u32 s74,s74,3145728\nv_cvt_pk_bf16_f32 v156,v52,v53\nv_cvt_pk_bf16_f32 v157,v54,v55\nbuffer_store_dwordx4 v[128:131],v240,s[52:55],s74 offen offset:0 sc1\n" \
"M v[52:55],a[4:7],v[216:219],0\nv_cvt_pk_bf16_f32 v172,v112,v113\nv_cvt_pk_bf16_f32 v173,v114,v115\nds_read_b64_tr_b16 v[196:197],v242 offset:8192\nds_read_b64_tr_b16 v[198:199],v242 offset:8448\nM v[112:115],a[8:11],v[216:219],0\nv_cvt_pk_bf16_f32 v188,v116,v117\nv_cvt_pk_bf16_f32 v189,v118,v119\n" \
"M v[116:119],a[12:15],v[216:219],0\nv_cvt_pk_bf16_f32 v142,v56,v57\nv_cvt_pk_bf16_f32 v143,v58,v59\nM v[56:59],a[0:3],v[220:223],0\nv_cvt_pk_bf16_f32 v158,v60,v61\nv_cvt_pk_bf16_f32 v159,v62,v63\nM v[60:63],a[4:7],v[220:223],0\nv_cvt_pk_bf16_f32 v174,v120,v121\n" \
"v_cvt_pk_bf16_f32 v175,v122,v123\nds_read_b64_tr_b16 v[200:201],v241 offset:8704\nds_read_b64_tr_b16 v[202:203],v241 offset:8960\nM v[120:123],a[8:11],v[220:223],0\ns_add_u32 m0,s64,131072\ns_add_u32 s69,s68,1048576\nv_cvt_pk_bf16_f32 v190,v124,v125\nv_cvt_pk_bf16_f32 v191,v126,v127\n" \
"M v[124:127],a[12:15],v[220:223],0\nds_read_b64_tr_b16 v[204:205],v242 offset:8704\ns_waitcnt lgkmcnt(5)\nM v[0:3],a[16:19],v[192:195],v[0:3]\nds_read_b64_tr_b16 v[206:207],v242 offset:8960\nds_read_b64_tr_b16 v[208:209],v241 offset:9216\nM v[4:7],a[20:23],v[192:195],v[4:7]\nds_read_b64_tr_b16 v[210:211],v241 offset:9472\n" \
"ds_read_b64_tr_b16 v[212:213],v242 offset:9216\nM v[64:67],a[24:27],v[192:195],v[64:67]\nds_read_b64_tr_b16 v[214:215],v242 offset:9472\nds_read_b64_tr_b16 v[216:217],v241 offset:9728\nM v[68:71],a[28:31],v[192:195],v[68:71]\nds_read_b64_tr_b16 v[218:219],v241 offset:9984\ns_waitcnt lgkmcnt(10)\nM v[8:11],a[16:19],v[196:199],v[8:11]\n" \
"ds_read_b64_tr_b16 v[220:221],v242 offset:9728\nds_read_b64_tr_b16 v[222:223],v242 offset:9984\nM v[12:15],a[20:23],v[196:199],v[12:15]\nbuffer_load_dwordx4 v243,s[40:43],s69 offen offset:0 lds\nM v[72:75],a[24:27],v[196:199],v[72:75]\ns_add_u32 s80,s80,90112\ns_add_u32 s76,s76,1048576\nM v[76:79],a[28:31],v[196:199],v[76:79]\n" \
"s_add_u32 s77,s76,131072\ns_waitcnt lgkmcnt(10)\nM v[16:19],a[16:19],v[200:203],v[16:19]\ns_lshl2_add_u32 s29,s70,81920\ns_lshl_b32 s70,s7,14\nM v[20:23],a[20:23],v[200:203],v[20:23]\nbuffer_store_dwordx4 v[132:135],v240,s[52:55],s74 offen offset:1024 sc1\nM v[80:83],a[24:27],v[200:203],v[80:83]\n" \
"v_add_u32 v250,s70,v240\ns_lshl_b32 s70,s63,19\nM v[84:87],a[28:31],v[200:203],v[84:87]\ns_add_u32 s72,s78,16384\ns_waitcnt lgkmcnt(6)\nM v[24:27],a[16:19],v[204:207],v[24:27]\ns_add_u32 s44,s34,s70\ns_addc_u32 s45,s35,0\n" \
"M v[28:31],a[20:23],v[204:207],v[28:31]\nbuffer_load_dwordx4 v244,s[40:43],s69 offen offset:1024 lds\nM v[88:91],a[24:27],v[204:207],v[88:91]\ns_waitcnt vmcnt(7) lgkmcnt(0)\ns_barrier\nM v[92:95],a[28:31],v[204:207],v[92:95]\nds_read_b64_tr_b16 v[192:193],v241 offset:16384\nds_read_b64_tr_b16 v[194:195],v241 offset:16640\n" \
"M v[32:35],a[16:19],v[208:211],v[32:35]\nds_read_b64_tr_b16 v[196:197],v242 offset:16384\nds_read_b64_tr_b16 v[198:199],v242 offset:16640\nM v[36:39],a[20:23],v[208:211],v[36:39]\nds_read_b64_tr_b16 v[200:201],v241 offset:16896\nds_read_b64_tr_b16 v[202:203],v241 offset:17152\nM v[96:99],a[24:27],v[208:211],v[96:99]\nds_read_b64_tr_b16 v[204:205],v242 offset:16896\n" \
"ds_read_b64_tr_b16 v[206:207],v242 offset:17152\nM v[100:103],a[28:31],v[208:211],v[100:103]\nds_read_b64_tr_b16 v[208:209],v241 offset:17408\nds_read_b64_tr_b16 v[210:211],v241 offset:17664\nM v[40:43],a[16:19],v[212:215],v[40:43]\nbuffer_load_dwordx4 v245,s[40:43],s69 offen offset:2048 lds\nM v[44:47],a[20:23],v[212:215],v[44:47]\ns_lshl_b32 s70,s62,15\n" \
"s_add_u32 s96,s70,s64\nM v[104:107],a[24:27],v[212:215],v[104:107]\ns_sub_u32 s46,491520,s70\ns_sub_u32 s96,s96,16384\nM v[108:111],a[28:31],v[212:215],v[108:111]\nds_read_b64_tr_b16 v[212:213],v242 offset:17408\nds_read_b64_tr_b16 v[214:215],v242 offset:17664\nM v[48:51],a[16:19],v[216:219],v[48:51]\n" \
"buffer_store_dwordx4 v[136:139],v240,s[52:55],s74 offen offset:2048 sc1\nM v[52:55],a[20:23],v[216:219],v[52:55]\nds_write_b64 v250,v[128:129] offset:0\ns_lshl2_add_u32 s70,s62,0\nM v[112:115],a[24:27],v[216:219],v[112:115]\ns_cmp_eq_u32 s36,s96\ns_cselect_b32 s72,0xc000,s98\ns_add_u32 s65,s36,s72\n" \
"M v[116:119],a[28:31],v[216:219],v[116:119]\nds_read_b64_tr_b16 v[216:217],v241 offset:17920\nds_read_b64_tr_b16 v[218:219],v241 offset:18176\nM v[56:59],a[16:19],v[220:223],v[56:59]\nbuffer_load_dwordx4 v246,s[40:43],s69 offen offset:3072 lds\nM v[60:63],a[20:23],v[220:223],v[60:63]\ns_add_u32 m0,s64,147456\ns_add_u32 s69,s68,1572864\n" \
"M v[120:123],a[24:27],v[220:223],v[120:123]\ns_sub_u32 s70,s70,s7\ns_lshl_b32 s70,s70,16\nM v[124:127],a[28:31],v[220:223],v[124:127]\nds_read_b64_tr_b16 v[220:221],v242 offset:17920\ns_waitcnt lgkmcnt(6)\nM v[0:3],a[32:35],v[192:195],v[0:3]\nds_read_b64_tr_b16 v[222:223],v242 offset:18176\n" \
"s_add_u32 s33,s78,s70\nM v[4:7],a[36:39],v[192:195],v[4:7]\nbuffer_load_dwordx4 v243,s[40:43],s69 offen offset:0 lds\nM v[64:67],a[40:43],v[192:195],v[64:67]\ns_cmp_eq_u32 s65,s96\ns_cselect_b32 s72,0xc000,s98\ns_add_u32 s66,s65,s72\nM v[68:71],a[44:47],v[192:195],v[68:71]\n" \
"ds_read_b64_tr_b16 v[192:193],v241 offset:24576\nds_read_b64_tr_b16 v[194:195],v241 offset:24832\nM v[8:11],a[32:35],v[196:199],v[8:11]\nds_write_b64 v250,v[130:131] offset:8\ns_lshl3_add_u32 s70,s62,s7\nM v[12:15],a[36:39],v[196:199],v[12:15]\nbuffer_load_dwordx4 v244,s[40:43],s69 offen offset:1024 lds\nM v[72:75],a[40:43],v[196:199],v[72:75]\n" \
"s_lshl3_add_u32 s70,s70,s60\ns_lshl_b32 s67,s70,15\nM v[76:79],a[44:47],v[196:199],v[76:79]\nds_read_b64_tr_b16 v[196:197],v242 offset:24576\nds_read_b64_tr_b16 v[198:199],v242 offset:24832\nM v[16:19],a[32:35],v[200:203],v[16:19]\ns_cmp_eq_u32 s66,s96\ns_cselect_b32 s72,0xc000,s98\n" \
"s_add_u32 s95,s66,s72\nM v[20:23],a[36:39],v[200:203],v[20:23]\nbuffer_load_dwordx4 v245,s[40:43],s69 offen offset:2048 lds\nM v[80:83],a[40:43],v[200:203],v[80:83]\ns_lshl3_add_u32 s70,s62,s60\ns_lshl2_add_u32 s70,s70,s7\nM v[84:87],a[44:47],v[200:203],v[84:87]\nds_read_b64_tr_b16 v[200:201],v241 offset:25088\n" \
"ds_read_b64_tr_b16 v[202:203],v241 offset:25344\nM v[24:27],a[32:35],v[204:207],v[24:27]\ns_cmp_eq_u32 s62,7\ns_cselect_b32 s72,98304,0\nM v[28:31],a[36:39],v[204:207],v[28:31]\nbuffer_store_dwordx4 v[140:143],v240,s[52:55],s74 offen offset:3072 sc1\nM v[88:91],a[40:43],v[204:207],v[88:91]\ns_add_u32 s74,s74,4096\n" \
"s_add_u32 s97,s96,65536\nM v[92:95],a[44:47],v[204:207],v[92:95]\nds_read_b64_tr_b16 v[204:205],v242 offset:25088\nds_read_b64_tr_b16 v[206:207],v242 offset:25344\nM v[32:35],a[32:35],v[208:211],v[32:35]\nds_write_b64 v250,v[132:133] offset:1024\ns_sub_u32 s97,s97,s72\nM v[36:39],a[36:39],v[208:211],v[36:39]\n" \
"buffer_load_dwordx4 v246,s[40:43],s69 offen offset:3072 lds\nM v[96:99],a[40:43],v[208:211],v[96:99]\ns_add_u32 m0,s64,98304\ns_add_u32 s69,s68,2097152\nM v[100:103],a[44:47],v[208:211],v[100:103]\nds_read_b64_tr_b16 v[208:209],v241 offset:25600\ns_waitcnt lgkmcnt(6)\nM v[40:43],a[32:35],v[212:215],v[40:43]\n" \
"ds_read_b64_tr_b16 v[210:211],v241 offset:25856\ns_mul_i32 s71,s60,14\nM v[44:47],a[36:39],v[212:215],v[44:47]\nbuffer_load_dwordx4 v243,s[40:43],s69 offen offset:0 lds\nM v[104:107],a[40:43],v[212:215],v[104:107]\ns_lshl_b32 s72,s60,1\ns_add_u32 s72,s72,s62\nM v[108:111],a[44:47],v[212:215],v[108:111]\n" \
"ds_read_b64_tr_b16 v[212:213],v242 offset:25600\nds_read_b64_tr_b16 v[214:215],v242 offset:25856\nM v[48:51],a[32:35],v[216:219],v[48:51]\ns_sub_u32 s72,s72,6\ns_lshl_b32 s72,s72,18\nM v[52:55],a[36:39],v[216:219],v[52:55]\nbuffer_load_dwordx4 v244,s[40:43],s69 offen offset:1024 lds\nM v[112:115],a[40:43],v[216:219],v[112:115]\n" \
"s_cmp_eq_u32 s62,7\ns_cselect_b32 s46,229376,s46\nM v[116:119],a[44:47],v[216:219],v[116:119]\nds_read_b64_tr_b16 v[216:217],v241 offset:26112\nds_read_b64_tr_b16 v[218:219],v241 offset:26368\nM v[56:59],a[32:35],v[220:223],v[56:59]\nds_write_b64 v250,v[134:135] offset:1032\nv_and_b32 v251,0xf0,v240\n" \
"M v[60:63],a[36:39],v[220:223],v[60:63]\nbuffer_store_dwordx4 v[144:147],v240,s[52:55],s74 offen offset:0 sc1\nM v[120:123],a[40:43],v[220:223],v[120:123]\ns_add_u32 s48,s44,19922944\ns_addc_u32 s49,s45,0\nM v[124:127],a[44:47],v[220:223],v[124:127]\nds_read_b64_tr_b16 v[220:221],v242 offset:26112\nds_read_b64_tr_b16 v[222:223],v242 offset:26368\n" \
"M v[0:3],a[48:51],v[192:195],v[0:3]\nv_lshl_or_b32 v253,v253,12,v251\ns_add_u32 s15,s64,s46\nM v[4:7],a[52:55],v[192:195],v[4:7]\nbuffer_load_dwordx4 v245,s[40:43],s69 offen offset:2048 lds\nM v[64:67],a[56:59],v[192:195],v[64:67]\ns_add_u32 s44,s44,3145728\ns_addc_u32 s45,s45,0\n" \
"M v[68:71],a[60:63],v[192:195],v[68:71]\ns_mov_b32 s50,s46\ns_add_u32 s15,s15,16384\nM v[8:11],a[48:51],v[196:199],v[8:11]\ns_sub_u32 s33,s33,1867776\ns_add_u32 s67,s67,36700160\nM v[12:15],a[52:55],v[196:199],v[12:15]\nbuffer_load_dwordx4 v246,s[40:43],s69 offen offset:3072 lds\n" \
"M v[72:75],a[56:59],v[196:199],v[72:75]\nM v[76:79],a[60:63],v[196:199],v[76:79]\ns_waitcnt lgkmcnt(2)\nM v[16:19],a[48:51],v[200:203],v[16:19]\nds_write_b64 v250,v[136:137] offset:2048\nM v[20:23],a[52:55],v[200:203],v[20:23]\nM v[80:83],a[56:59],v[200:203],v[80:83]\nM v[84:87],a[60:63],v[200:203],v[84:87]\n" \
"M v[24:27],a[48:51],v[204:207],v[24:27]\ns_add_u32 m0,s64,114688\ns_add_u32 s69,s68,2621440\nM v[28:31],a[52:55],v[204:207],v[28:31]\nM v[88:91],a[56:59],v[204:207],v[88:91]\ns_waitcnt vmcnt(10) lgkmcnt(1)\ns_barrier\nM v[92:95],a[60:63],v[204:207],v[92:95]\n" \
"buffer_store_dwordx4 v[148:151],v240,s[52:55],s74 offen offset:1024 sc1\nM v[32:35],a[48:51],v[208:211],v[32:35]\nds_read_b64_tr_b16 v[192:193],v241 offset:32768\nds_read_b64_tr_b16 v[194:195],v241 offset:33024\nM v[36:39],a[52:55],v[208:211],v[36:39]\nds_read_b64_tr_b16 v[196:197],v242 offset:32768\nds_read_b64_tr_b16 v[198:199],v242 offset:33024\nM v[96:99],a[56:59],v[208:211],v[96:99]\n" \
"ds_read_b64_tr_b16 v[200:201],v241 offset:33280\nds_read_b64_tr_b16 v[202:203],v241 offset:33536\nM v[100:103],a[60:63],v[208:211],v[100:103]\nbuffer_store_dword v252,off,s[52:55],s79 sc1\nM v[40:43],a[48:51],v[212:215],v[40:43]\nds_read_b64_tr_b16 v[204:205],v242 offset:33280\nds_read_b64_tr_b16 v[206:207],v242 offset:33536\nM v[44:47],a[52:55],v[212:215],v[44:47]\n" \
"ds_read_b64_tr_b16 v[208:209],v241 offset:33792\nds_read_b64_tr_b16 v[210:211],v241 offset:34048\nM v[104:107],a[56:59],v[212:215],v[104:107]\nbuffer_load_dwordx4 v243,s[40:43],s69 offen offset:0 lds\nM v[108:111],a[60:63],v[212:215],v[108:111]\nds_read_b64_tr_b16 v[212:213],v242 offset:33792\nds_read_b64_tr_b16 v[214:215],v242 offset:34048\nM v[48:51],a[48:51],v[216:219],v[48:51]\n" \
"ds_write_b64 v250,v[138:139] offset:2056\ns_lshl4_add_u32 s79,s70,65536\nM v[52:55],a[52:55],v[216:219],v[52:55]\ns_lshr_b32 s70,s6,3\ns_sub_u32 s70,s70,16\nM v[112:115],a[56:59],v[216:219],v[112:115]\nbuffer_load_dwordx4 v244,s[40:43],s69 offen offset:1024 lds\nM v[116:119],a[60:63],v[216:219],v[116:119]\n" \
"ds_read_b64_tr_b16 v[216:217],v241 offset:34304\nds_read_b64_tr_b16 v[218:219],v241 offset:34560\nM v[56:59],a[48:51],v[220:223],v[56:59]\ns_add_u32 s2,s70,s71\ns_mul_i32 s71,s60,448\nM v[60:63],a[52:55],v[220:223],v[60:63]\ns_and_b32 s71,s71,0xffffff80\ns_sub_u32 s58,1835008,s71\n" \
"M v[120:123],a[56:59],v[220:223],v[120:123]\nbuffer_load_dwordx4 v245,s[40:43],s69 offen offset:2048 lds\nM v[124:127],a[60:63],v[220:223],v[124:127]\nds_read_b64_tr_b16 v[220:221],v242 offset:34304\ns_waitcnt lgkmcnt(7)\nM v[0:3],a[64:67],v[192:195],v[0:3]\nds_read_b64_tr_b16 v[222:223],v242 offset:34560\ns_lshl_b32 s2,s2,5\n" \
"M v[4:7],a[68:71],v[192:195],v[4:7]\nds_write_b64 v250,v[140:141] offset:3072\nM v[64:67],a[72:75],v[192:195],v[64:67]\nbuffer_store_dwordx4 v[152:155],v240,s[52:55],s74 offen offset:2048 sc1\nM v[68:71],a[76:79],v[192:195],v[68:71]\nds_read_b64_tr_b16 v[192:193],v241 offset:40960\nds_read_b64_tr_b16 v[194:195],v241 offset:41216\nM v[8:11],a[64:67],v[196:199],v[8:11]\n" \
"s_cmp_ge_u32 s62,6\ns_cselect_b64 s[56:57],s[18:19],s[56:57]\ns_cselect_b32 s71,s72,s71\ns_cselect_b32 s58,0x40000,s58\ns_cselect_b32 s0,2048,s0\nM v[12:15],a[68:71],v[196:199],v[12:15]\ns_mul_i32 s72,s60,229376\nM v[72:75],a[72:75],v[196:199],v[72:75]\n" \
"buffer_load_dwordx4 v246,s[40:43],s69 offen offset:3072 lds\nM v[76:79],a[76:79],v[196:199],v[76:79]\nds_read_b64_tr_b16 v[196:197],v242 offset:40960\nds_read_b64_tr_b16 v[198:199],v242 offset:41216\nM v[16:19],a[64:67],v[200:203],v[16:19]\ns_cmp_eq_u32 s62,3\ns_cselect_b64 s[56:57],s[26:27],s[56:57]\ns_cselect_b32 s71,s72,s71\n" \
"s_cselect_b32 s58,229376,s58\ns_cselect_b32 s0,4096,s0\nM v[20:23],a[68:71],v[200:203],v[20:23]\ns_cmp_lt_u32 s70,14\ns_cselect_b32 s2,s2,0\ns_cselect_b32 s3,-1,0\nM v[80:83],a[72:75],v[200:203],v[80:83]\ns_lshl2_add_u32 s70,s61,s7\n" \
"s_mul_i32 s1,s70,s0\nM v[84:87],a[76:79],v[200:203],v[84:87]\nds_read_b64_tr_b16 v[200:201],v241 offset:41472\nds_read_b64_tr_b16 v[202:203],v241 offset:41728\nM v[24:27],a[64:67],v[204:207],v[24:27]\nds_write_b64 v250,v[142:143] offset:3080\ns_lshl_b32 s70,s61,8\nM v[28:31],a[68:71],v[204:207],v[28:31]\n" \
"s_cmp_lt_u32 s62,3\ns_cselect_b32 s58,0,s58\nM v[88:91],a[72:75],v[204:207],v[88:91]\ns_add_u32 s56,s56,s71\ns_addc_u32 s57,s57,0\nM v[92:95],a[76:79],v[204:207],v[92:95]\nds_read_b64_tr_b16 v[204:205],v242 offset:41472\ns_waitcnt lgkmcnt(6)\n" \
"M v[32:35],a[64:67],v[208:211],v[32:35]\nds_read_b64_tr_b16 v[206:207],v242 offset:41728\ns_lshl_b32 s0,s0,4\nM v[36:39],a[68:71],v[208:211],v[36:39]\nM v[96:99],a[72:75],v[208:211],v[96:99]\nbuffer_store_dwordx4 v[156:159],v240,s[52:55],s74 offen offset:3072 sc1\nM v[100:103],a[76:79],v[208:211],v[100:103]\nds_read_b64_tr_b16 v[208:209],v241 offset:41984\n" \
"ds_read_b64_tr_b16 v[210:211],v241 offset:42240\nM v[40:43],a[64:67],v[212:215],v[40:43]\ns_add_u32 s74,s74,4096\nM v[44:47],a[68:71],v[212:215],v[44:47]\nM v[104:107],a[72:75],v[212:215],v[104:107]\nM v[108:111],a[76:79],v[212:215],v[108:111]\nds_read_b64_tr_b16 v[212:213],v242 offset:41984\nds_read_b64_tr_b16 v[214:215],v242 offset:42240\n" \
"M v[48:51],a[64:67],v[216:219],v[48:51]\nds_write_b64 v250,v[144:145] offset:4096\nM v[52:55],a[68:71],v[216:219],v[52:55]\nM v[112:115],a[72:75],v[216:219],v[112:115]\nM v[116:119],a[76:79],v[216:219],v[116:119]\nds_read_b64_tr_b16 v[216:217],v241 offset:42496\nds_read_b64_tr_b16 v[218:219],v241 offset:42752\nM v[56:59],a[64:67],v[220:223],v[56:59]\n" \
"M v[60:63],a[68:71],v[220:223],v[60:63]\nM v[120:123],a[72:75],v[220:223],v[120:123]\nM v[124:127],a[76:79],v[220:223],v[124:127]\nds_read_b64_tr_b16 v[220:221],v242 offset:42496\nds_read_b64_tr_b16 v[222:223],v242 offset:42752\nM v[0:3],a[80:83],v[192:195],v[0:3]\nM v[4:7],a[84:87],v[192:195],v[4:7]\nM v[64:67],a[88:91],v[192:195],v[64:67]\n" \
"M v[68:71],a[92:95],v[192:195],v[68:71]\ns_waitcnt lgkmcnt(7)\nM v[8:11],a[80:83],v[196:199],v[8:11]\nbuffer_store_dwordx4 v[160:163],v240,s[52:55],s74 offen offset:0 sc1\nM v[12:15],a[84:87],v[196:199],v[12:15]\nds_write_b64 v250,v[146:147] offset:4104\nM v[72:75],a[88:91],v[196:199],v[72:75]\nM v[76:79],a[92:95],v[196:199],v[76:79]\n" \
"M v[16:19],a[80:83],v[200:203],v[16:19]\nM v[20:23],a[84:87],v[200:203],v[20:23]\nM v[80:83],a[88:91],v[200:203],v[80:83]\nM v[84:87],a[92:95],v[200:203],v[84:87]\nM v[24:27],a[80:83],v[204:207],v[24:27]\ns_add_u32 m0,s64,131072\ns_add_u32 s69,s68,3145728\nM v[28:31],a[84:87],v[204:207],v[28:31]\n" \
"M v[88:91],a[88:91],v[204:207],v[88:91]\ns_waitcnt vmcnt(14) lgkmcnt(1)\ns_barrier\nM v[92:95],a[92:95],v[204:207],v[92:95]\nds_read_b64_tr_b16 v[192:193],v241 offset:49152\nds_read_b64_tr_b16 v[194:195],v241 offset:49408\nM v[32:35],a[80:83],v[208:211],v[32:35]\nds_read_b64_tr_b16 v[196:197],v242 offset:49152\n" \
"ds_read_b64_tr_b16 v[198:199],v242 offset:49408\nM v[36:39],a[84:87],v[208:211],v[36:39]\nds_read_b64_tr_b16 v[200:201],v241 offset:49664\nds_read_b64_tr_b16 v[202:203],v241 offset:49920\nM v[96:99],a[88:91],v[208:211],v[96:99]\nds_read_b64_tr_b16 v[204:205],v242 offset:49664\nds_read_b64_tr_b16 v[206:207],v242 offset:49920\nM v[100:103],a[92:95],v[208:211],v[100:103]\n" \
"ds_read_b64_tr_b16 v[208:209],v241 offset:50176\nds_read_b64_tr_b16 v[210:211],v241 offset:50432\nM v[40:43],a[80:83],v[212:215],v[40:43]\nbuffer_load_dwordx4 v243,s[40:43],s69 offen offset:0 lds\nM v[44:47],a[84:87],v[212:215],v[44:47]\nds_write_b64 v250,v[148:149] offset:5120\nM v[104:107],a[88:91],v[212:215],v[104:107]\nM v[108:111],a[92:95],v[212:215],v[108:111]\n" \
"ds_read_b64_tr_b16 v[212:213],v242 offset:50176\nds_read_b64_tr_b16 v[214:215],v242 offset:50432\nM v[48:51],a[80:83],v[216:219],v[48:51]\nbuffer_store_dwordx4 v[164:167],v240,s[52:55],s74 offen offset:1024 sc1\nM v[52:55],a[84:87],v[216:219],v[52:55]\nM v[112:115],a[88:91],v[216:219],v[112:115]\nM v[116:119],a[92:95],v[216:219],v[116:119]\nds_read_b64_tr_b16 v[216:217],v241 offset:50688\n" \
"ds_read_b64_tr_b16 v[218:219],v241 offset:50944\nM v[56:59],a[80:83],v[220:223],v[56:59]\nbuffer_load_dwordx4 v244,s[40:43],s69 offen offset:1024 lds\nM v[60:63],a[84:87],v[220:223],v[60:63]\nds_write_b64 v250,v[150:151] offset:5128\nM v[120:123],a[88:91],v[220:223],v[120:123]\nM v[124:127],a[92:95],v[220:223],v[124:127]\nds_read_b64_tr_b16 v[220:221],v242 offset:50688\n" \
"s_waitcnt lgkmcnt(9)\nM v[0:3],a[96:99],v[192:195],v[0:3]\nbuffer_load_dwordx4 v245,s[40:43],s69 offen offset:2048 lds\nM v[4:7],a[100:103],v[192:195],v[4:7]\nds_read_b64_tr_b16 v[222:223],v242 offset:50944\nM v[64:67],a[104:107],v[192:195],v[64:67]\nM v[68:71],a[108:111],v[192:195],v[68:71]\nds_read_b64_tr_b16 v[192:193],v241 offset:57344\n" \
"ds_read_b64_tr_b16 v[194:195],v241 offset:57600\nM v[8:11],a[96:99],v[196:199],v[8:11]\nbuffer_load_dwordx4 v246,s[40:43],s69 offen offset:3072 lds\nM v[12:15],a[100:103],v[196:199],v[12:15]\nM v[72:75],a[104:107],v[196:199],v[72:75]\nM v[76:79],a[108:111],v[196:199],v[76:79]\nds_read_b64_tr_b16 v[196:197],v242 offset:57344\nds_read_b64_tr_b16 v[198:199],v242 offset:57600\n" \
"M v[16:19],a[96:99],v[200:203],v[16:19]\nbuffer_store_dwordx4 v[168:171],v240,s[52:55],s74 offen offset:2048 sc1\nM v[20:23],a[100:103],v[200:203],v[20:23]\nds_write_b64 v250,v[152:153] offset:6144\nM v[80:83],a[104:107],v[200:203],v[80:83]\nM v[84:87],a[108:111],v[200:203],v[84:87]\nds_read_b64_tr_b16 v[200:201],v241 offset:57856\nds_read_b64_tr_b16 v[202:203],v241 offset:58112\n" \
"M v[24:27],a[96:99],v[204:207],v[24:27]\nM v[28:31],a[100:103],v[204:207],v[28:31]\nM v[88:91],a[104:107],v[204:207],v[88:91]\nM v[92:95],a[108:111],v[204:207],v[92:95]\nds_read_b64_tr_b16 v[204:205],v242 offset:57856\ns_waitcnt lgkmcnt(8)\nM v[32:35],a[96:99],v[208:211],v[32:35]\nds_read_b64_tr_b16 v[206:207],v242 offset:58112\n" \
"M v[36:39],a[100:103],v[208:211],v[36:39]\nM v[96:99],a[104:107],v[208:211],v[96:99]\nM v[100:103],a[108:111],v[208:211],v[100:103]\nds_read_b64_tr_b16 v[208:209],v241 offset:58368\nds_read_b64_tr_b16 v[210:211],v241 offset:58624\nM v[40:43],a[96:99],v[212:215],v[40:43]\nds_write_b64 v250,v[154:155] offset:6152\nM v[44:47],a[100:103],v[212:215],v[44:47]\n" \
"M v[104:107],a[104:107],v[212:215],v[104:107]\nM v[108:111],a[108:111],v[212:215],v[108:111]\nds_read_b64_tr_b16 v[212:213],v242 offset:58368\nds_read_b64_tr_b16 v[214:215],v242 offset:58624\nM v[48:51],a[96:99],v[216:219],v[48:51]\nM v[52:55],a[100:103],v[216:219],v[52:55]\nM v[112:115],a[104:107],v[216:219],v[112:115]\nbuffer_store_dwordx4 v[172:175],v240,s[52:55],s74 offen offset:3072 sc1\n" \
"M v[116:119],a[108:111],v[216:219],v[116:119]\nds_read_b64_tr_b16 v[216:217],v241 offset:58880\nds_read_b64_tr_b16 v[218:219],v241 offset:59136\nM v[56:59],a[96:99],v[220:223],v[56:59]\ns_add_u32 s74,s74,4096\nM v[60:63],a[100:103],v[220:223],v[60:63]\nM v[120:123],a[104:107],v[220:223],v[120:123]\nM v[124:127],a[108:111],v[220:223],v[124:127]\n" \
"ds_read_b64_tr_b16 v[220:221],v242 offset:58880\ns_waitcnt lgkmcnt(8)\nM v[0:3],a[112:115],v[192:195],v[0:3]\nds_write_b64 v250,v[156:157] offset:7168\nds_read_b64_tr_b16 v[222:223],v242 offset:59136\nM v[4:7],a[116:119],v[192:195],v[4:7]\nM v[64:67],a[120:123],v[192:195],v[64:67]\nM v[68:71],a[124:127],v[192:195],v[68:71]\n" \
"M v[8:11],a[112:115],v[196:199],v[8:11]\nM v[12:15],a[116:119],v[196:199],v[12:15]\nM v[72:75],a[120:123],v[196:199],v[72:75]\nM v[76:79],a[124:127],v[196:199],v[76:79]\nM v[16:19],a[112:115],v[200:203],v[16:19]\nM v[20:23],a[116:119],v[200:203],v[20:23]\nM v[80:83],a[120:123],v[200:203],v[80:83]\nM v[84:87],a[124:127],v[200:203],v[84:87]\n" \
"M v[24:27],a[112:115],v[204:207],v[24:27]\nbuffer_store_dwordx4 v[176:179],v240,s[52:55],s74 offen offset:0 sc1\nM v[28:31],a[116:119],v[204:207],v[28:31]\ns_add_u32 m0,s64,147456\ns_add_u32 s69,s68,3670016\nM v[88:91],a[120:123],v[204:207],v[88:91]\ns_waitcnt vmcnt(17) lgkmcnt(0)\ns_barrier\n" \
"M v[92:95],a[124:127],v[204:207],v[92:95]\nds_read_b64_tr_b16 v[192:193],v241 offset:0\nds_read_b64_tr_b16 v[194:195],v241 offset:256\nM v[32:35],a[112:115],v[208:211],v[32:35]\nds_read_b64_tr_b16 v[196:197],v242 offset:0\nds_read_b64_tr_b16 v[198:199],v242 offset:256\nM v[36:39],a[116:119],v[208:211],v[36:39]\nds_read_b64_tr_b16 v[200:201],v241 offset:512\n" \
"ds_read_b64_tr_b16 v[202:203],v241 offset:768\nM v[96:99],a[120:123],v[208:211],v[96:99]\nds_read_b64_tr_b16 v[204:205],v242 offset:512\nds_read_b64_tr_b16 v[206:207],v242 offset:768\nM v[100:103],a[124:127],v[208:211],v[100:103]\nds_read_b64_tr_b16 v[208:209],v241 offset:1024\nds_read_b64_tr_b16 v[210:211],v241 offset:1280\nM v[40:43],a[112:115],v[212:215],v[40:43]\n" \
"buffer_load_dwordx4 v243,s[40:43],s69 offen offset:0 lds\nM v[44:47],a[116:119],v[212:215],v[44:47]\nds_write_b64 v250,v[158:159] offset:7176\nM v[104:107],a[120:123],v[212:215],v[104:107]\nM v[108:111],a[124:127],v[212:215],v[108:111]\nds_read_b64_tr_b16 v[212:213],v242 offset:1024\nds_read_b64_tr_b16 v[214:215],v242 offset:1280\nM v[48:51],a[112:115],v[216:219],v[48:51]\n" \
"buffer_load_dwordx4 v244,s[40:43],s69 offen offset:1024 lds\nM v[52:55],a[116:119],v[216:219],v[52:55]\nds_write_b64 v250,v[160:161] offset:8192\nM v[112:115],a[120:123],v[216:219],v[112:115]\nM v[116:119],a[124:127],v[216:219],v[116:119]\nds_read_b64_tr_b16 v[216:217],v241 offset:1536\nds_read_b64_tr_b16 v[218:219],v241 offset:1792\nM v[56:59],a[112:115],v[220:223],v[56:59]\n" \
"buffer_load_dwordx4 v245,s[40:43],s69 offen offset:2048 lds\nM v[60:63],a[116:119],v[220:223],v[60:63]\nM v[120:123],a[120:123],v[220:223],v[120:123]\nM v[124:127],a[124:127],v[220:223],v[124:127]\nds_read_b64_tr_b16 v[220:221],v242 offset:1536\ns_waitcnt lgkmcnt(9)\nM v[0:3],a[128:131],v[192:195],v[0:3]\nbuffer_store_dwordx4 v[180:183],v240,s[52:55],s74 offen offset:1024 sc1\n" \
"M v[4:7],a[132:135],v[192:195],v[4:7]\nds_read_b64_tr_b16 v[222:223],v242 offset:1792\nM v[64:67],a[136:139],v[192:195],v[64:67]\nM v[68:71],a[140:143],v[192:195],v[68:71]\nds_read_b64_tr_b16 v[192:193],v241 offset:8192\nds_read_b64_tr_b16 v[194:195],v241 offset:8448\nM v[8:11],a[128:131],v[196:199],v[8:11]\nbuffer_load_dwordx4 v246,s[40:43],s69 offen offset:3072 lds\n" \
"M v[12:15],a[132:135],v[196:199],v[12:15]\nds_write_b64 v250,v[162:163] offset:8200\nM v[72:75],a[136:139],v[196:199],v[72:75]\nM v[76:79],a[140:143],v[196:199],v[76:79]\nds_read_b64_tr_b16 v[196:197],v242 offset:8192\nds_read_b64_tr_b16 v[198:199],v242 offset:8448\nM v[16:19],a[128:131],v[200:203],v[16:19]\nM v[20:23],a[132:135],v[200:203],v[20:23]\n" \
"M v[80:83],a[136:139],v[200:203],v[80:83]\nM v[84:87],a[140:143],v[200:203],v[84:87]\nds_read_b64_tr_b16 v[200:201],v241 offset:8704\nds_read_b64_tr_b16 v[202:203],v241 offset:8960\nM v[24:27],a[128:131],v[204:207],v[24:27]\nM v[28:31],a[132:135],v[204:207],v[28:31]\nM v[88:91],a[136:139],v[204:207],v[88:91]\nM v[92:95],a[140:143],v[204:207],v[92:95]\n" \
"ds_read_b64_tr_b16 v[204:205],v242 offset:8704\ns_waitcnt lgkmcnt(9)\nM v[32:35],a[128:131],v[208:211],v[32:35]\nbuffer_store_dwordx4 v[184:187],v240,s[52:55],s74 offen offset:2048 sc1\nM v[36:39],a[132:135],v[208:211],v[36:39]\nds_write_b64 v250,v[164:165] offset:9216\nds_read_b64_tr_b16 v[206:207],v242 offset:8960\nM v[96:99],a[136:139],v[208:211],v[96:99]\n" \
"M v[100:103],a[140:143],v[208:211],v[100:103]\nds_read_b64_tr_b16 v[208:209],v241 offset:9216\nds_read_b64_tr_b16 v[210:211],v241 offset:9472\nM v[40:43],a[128:131],v[212:215],v[40:43]\nM v[44:47],a[132:135],v[212:215],v[44:47]\nM v[104:107],a[136:139],v[212:215],v[104:107]\nbuffer_load_dword v248,v228,s[52:55],s80 offen sc1\nM v[108:111],a[140:143],v[212:215],v[108:111]\n" \
"ds_read_b64_tr_b16 v[212:213],v242 offset:9216\nds_read_b64_tr_b16 v[214:215],v242 offset:9472\nM v[48:51],a[128:131],v[216:219],v[48:51]\nM v[52:55],a[132:135],v[216:219],v[52:55]\nM v[112:115],a[136:139],v[216:219],v[112:115]\nM v[116:119],a[140:143],v[216:219],v[116:119]\nds_read_b64_tr_b16 v[216:217],v241 offset:9728\ns_waitcnt lgkmcnt(8)\n" \
"M v[56:59],a[128:131],v[220:223],v[56:59]\nds_write_b64 v250,v[166:167] offset:9224\nds_read_b64_tr_b16 v[218:219],v241 offset:9984\nM v[60:63],a[132:135],v[220:223],v[60:63]\nM v[120:123],a[136:139],v[220:223],v[120:123]\nM v[124:127],a[140:143],v[220:223],v[124:127]\nds_read_b64_tr_b16 v[220:221],v242 offset:9728\nds_read_b64_tr_b16 v[222:223],v242 offset:9984\n" \
"M v[0:3],a[144:147],v[192:195],v[0:3]\nM v[4:7],a[148:151],v[192:195],v[4:7]\nM v[64:67],a[152:155],v[192:195],v[64:67]\nbuffer_store_dwordx4 v[188:191],v240,s[52:55],s74 offen offset:3072 sc1\nM v[68:71],a[156:159],v[192:195],v[68:71]\ns_add_u32 s74,s74,16764928\nM v[8:11],a[144:147],v[196:199],v[8:11]\nM v[12:15],a[148:151],v[196:199],v[12:15]\n" \
"M v[72:75],a[152:155],v[196:199],v[72:75]\nM v[76:79],a[156:159],v[196:199],v[76:79]\nM v[16:19],a[144:147],v[200:203],v[16:19]\nds_write_b64 v250,v[168:169] offset:10240\nM v[20:23],a[148:151],v[200:203],v[20:23]\nM v[80:83],a[152:155],v[200:203],v[80:83]\nM v[84:87],a[156:159],v[200:203],v[84:87]\ns_waitcnt lgkmcnt(5)\n" \
"M v[24:27],a[144:147],v[204:207],v[24:27]\ns_sub_u32 s40,s12,0x1000\ns_subb_u32 s41,s13,0\nM v[28:31],a[148:151],v[204:207],v[28:31]\ns_add_u32 m0,s64,98304\ns_add_u32 s69,s68,0\nM v[88:91],a[152:155],v[204:207],v[88:91]\ns_waitcnt vmcnt(18) lgkmcnt(1)\n" \
"s_barrier\nM v[92:95],a[156:159],v[204:207],v[92:95]\nds_read_b64_tr_b16 v[192:193],v241 offset:16384\nds_read_b64_tr_b16 v[194:195],v241 offset:16640\nM v[32:35],a[144:147],v[208:211],v[32:35]\nds_read_b64_tr_b16 v[196:197],v242 offset:16384\nds_read_b64_tr_b16 v[198:199],v242 offset:16640\nM v[36:39],a[148:151],v[208:211],v[36:39]\n" \
"ds_read_b64_tr_b16 v[200:201],v241 offset:16896\nds_read_b64_tr_b16 v[202:203],v241 offset:17152\nM v[96:99],a[152:155],v[208:211],v[96:99]\nds_read_b64_tr_b16 v[204:205],v242 offset:16896\nds_read_b64_tr_b16 v[206:207],v242 offset:17152\nM v[100:103],a[156:159],v[208:211],v[100:103]\nds_read_b64_tr_b16 v[208:209],v241 offset:17408\nds_read_b64_tr_b16 v[210:211],v241 offset:17664\n" \
"M v[40:43],a[144:147],v[212:215],v[40:43]\nbuffer_load_dwordx4 v243,s[40:43],s69 offen offset:0 lds\nM v[44:47],a[148:151],v[212:215],v[44:47]\nds_write_b64 v250,v[170:171] offset:10248\ns_lshl_b32 s12,s61,13\nM v[104:107],a[152:155],v[212:215],v[104:107]\nM v[108:111],a[156:159],v[212:215],v[108:111]\nds_read_b64_tr_b16 v[212:213],v242 offset:17408\n" \
"ds_read_b64_tr_b16 v[214:215],v242 offset:17664\nM v[48:51],a[144:147],v[216:219],v[48:51]\nbuffer_load_dwordx4 v244,s[40:43],s69 offen offset:1024 lds\nM v[52:55],a[148:151],v[216:219],v[52:55]\nM v[112:115],a[152:155],v[216:219],v[112:115]\nM v[116:119],a[156:159],v[216:219],v[116:119]\nds_read_b64_tr_b16 v[216:217],v241 offset:17920\nds_read_b64_tr_b16 v[218:219],v241 offset:18176\n" \
"M v[56:59],a[144:147],v[220:223],v[56:59]\nbuffer_load_dwordx4 v245,s[40:43],s69 offen offset:2048 lds\nM v[60:63],a[148:151],v[220:223],v[60:63]\nM v[120:123],a[152:155],v[220:223],v[120:123]\nM v[124:127],a[156:159],v[220:223],v[124:127]\nds_read_b64_tr_b16 v[220:221],v242 offset:17920\ns_waitcnt lgkmcnt(8)\nM v[0:3],a[160:163],v[192:195],v[0:3]\n" \
"buffer_load_dwordx4 v246,s[40:43],s69 offen offset:3072 lds\nM v[4:7],a[164:167],v[192:195],v[4:7]\nds_write_b64 v250,v[172:173] offset:11264\nds_read_b64_tr_b16 v[222:223],v242 offset:18176\nM v[64:67],a[168:171],v[192:195],v[64:67]\nM v[68:71],a[172:175],v[192:195],v[68:71]\nds_read_b64_tr_b16 v[192:193],v241 offset:24576\nds_read_b64_tr_b16 v[194:195],v241 offset:24832\n" \
"M v[8:11],a[160:163],v[196:199],v[8:11]\nM v[12:15],a[164:167],v[196:199],v[12:15]\nM v[72:75],a[168:171],v[196:199],v[72:75]\nM v[76:79],a[172:175],v[196:199],v[76:79]\nds_read_b64_tr_b16 v[196:197],v242 offset:24576\nds_read_b64_tr_b16 v[198:199],v242 offset:24832\nM v[16:19],a[160:163],v[200:203],v[16:19]\nM v[20:23],a[164:167],v[200:203],v[20:23]\n" \
"M v[80:83],a[168:171],v[200:203],v[80:83]\nM v[84:87],a[172:175],v[200:203],v[84:87]\nds_read_b64_tr_b16 v[200:201],v241 offset:25088\nds_read_b64_tr_b16 v[202:203],v241 offset:25344\nM v[24:27],a[160:163],v[204:207],v[24:27]\nds_write_b64 v250,v[174:175] offset:11272\nM v[28:31],a[164:167],v[204:207],v[28:31]\ns_waitcnt vmcnt(5)\n" \
"v_cmp_eq_u32 vcc,0,v248\nM v[88:91],a[168:171],v[204:207],v[88:91]\ns_cbranch_vccnz .Lps0\n.Lpb0:\ns_lshl_b32 s80,s63,7\nM v[92:95],a[172:175],v[204:207],v[92:95]\nds_read_b64_tr_b16 v[204:205],v242 offset:25088\ns_waitcnt lgkmcnt(8)\n" \
"M v[32:35],a[160:163],v[208:211],v[32:35]\nds_read_b64_tr_b16 v[206:207],v242 offset:25344\ns_add_u32 s80,s80,81920\nM v[36:39],a[164:167],v[208:211],v[36:39]\nbuffer_load_dwordx4 a[0:3],v240,s[52:55],s76 offen offset:0 sc1\nM v[96:99],a[168:171],v[208:211],v[96:99]\nv_and_b32 v228,31,v249\nv_lshlrev_b32 v228,2,v228\n" \
"M v[100:103],a[172:175],v[208:211],v[100:103]\nds_read_b64_tr_b16 v[208:209],v241 offset:25600\nds_read_b64_tr_b16 v[210:211],v241 offset:25856\nM v[40:43],a[160:163],v[212:215],v[40:43]\nv_mov_b32 v251,v228\nM v[44:47],a[164:167],v[212:215],v[44:47]\nbuffer_load_dwordx4 a[4:7],v240,s[52:55],s76 offen offset:1024 sc1\nM v[104:107],a[168:171],v[212:215],v[104:107]\n" \
"M v[108:111],a[172:175],v[212:215],v[108:111]\nds_read_b64_tr_b16 v[212:213],v242 offset:25600\nds_read_b64_tr_b16 v[214:215],v242 offset:25856\nM v[48:51],a[160:163],v[216:219],v[48:51]\nds_write_b64 v250,v[176:177] offset:12288\nM v[52:55],a[164:167],v[216:219],v[52:55]\nbuffer_load_dwordx4 a[8:11],v240,s[52:55],s77 offen offset:0 sc1\nM v[112:115],a[168:171],v[216:219],v[112:115]\n" \
"M v[116:119],a[172:175],v[216:219],v[116:119]\nds_read_b64_tr_b16 v[216:217],v241 offset:26112\nds_read_b64_tr_b16 v[218:219],v241 offset:26368\nM v[56:59],a[160:163],v[220:223],v[56:59]\nM v[60:63],a[164:167],v[220:223],v[60:63]\nbuffer_load_dwordx4 a[12:15],v240,s[52:55],s77 offen offset:1024 sc1\nM v[120:123],a[168:171],v[220:223],v[120:123]\nM v[124:127],a[172:175],v[220:223],v[124:127]\n" \
"ds_read_b64_tr_b16 v[220:221],v242 offset:26112\ns_waitcnt lgkmcnt(8)\nM v[0:3],a[176:179],v[192:195],v[0:3]\nds_read_b64_tr_b16 v[222:223],v242 offset:26368\nM v[4:7],a[180:183],v[192:195],v[4:7]\nM v[64:67],a[184:187],v[192:195],v[64:67]\nbuffer_load_dwordx4 a[16:19],v240,s[52:55],s76 offen offset:2048 sc1\nM v[68:71],a[188:191],v[192:195],v[68:71]\n" \
"M v[8:11],a[176:179],v[196:199],v[8:11]\nds_write_b64 v250,v[178:179] offset:12296\nM v[12:15],a[180:183],v[196:199],v[12:15]\nM v[72:75],a[184:187],v[196:199],v[72:75]\nM v[76:79],a[188:191],v[196:199],v[76:79]\nM v[16:19],a[176:179],v[200:203],v[16:19]\nM v[20:23],a[180:183],v[200:203],v[20:23]\nM v[80:83],a[184:187],v[200:203],v[80:83]\n" \
"buffer_load_dwordx4 a[20:23],v240,s[52:55],s76 offen offset:3072 sc1\nM v[84:87],a[188:191],v[200:203],v[84:87]\ns_add_u32 s76,s76,4096\nM v[24:27],a[176:179],v[204:207],v[24:27]\ns_add_u32 m0,s64,114688\ns_add_u32 s69,s68,524288\nM v[28:31],a[180:183],v[204:207],v[28:31]\nM v[88:91],a[184:187],v[204:207],v[88:91]\n" \
"s_waitcnt lgkmcnt(1)\ns_barrier\nM v[92:95],a[188:191],v[204:207],v[92:95]\nds_read_b64_tr_b16 v[192:193],v241 offset:32768\nds_read_b64_tr_b16 v[194:195],v241 offset:33024\nM v[32:35],a[176:179],v[208:211],v[32:35]\nds_read_b64_tr_b16 v[196:197],v242 offset:32768\nds_read_b64_tr_b16 v[198:199],v242 offset:33024\n" \
"M v[36:39],a[180:183],v[208:211],v[36:39]\nds_read_b64_tr_b16 v[200:201],v241 offset:33280\nds_read_b64_tr_b16 v[202:203],v241 offset:33536\nM v[96:99],a[184:187],v[208:211],v[96:99]\nds_read_b64_tr_b16 v[204:205],v242 offset:33280\nds_read_b64_tr_b16 v[206:207],v242 offset:33536\nM v[100:103],a[188:191],v[208:211],v[100:103]\nds_read_b64_tr_b16 v[208:209],v241 offset:33792\n" \
"ds_read_b64_tr_b16 v[210:211],v241 offset:34048\nM v[40:43],a[176:179],v[212:215],v[40:43]\nbuffer_load_dwordx4 a[24:27],v240,s[52:55],s77 offen offset:2048 sc1\nM v[44:47],a[180:183],v[212:215],v[44:47]\nds_write_b64 v250,v[180:181] offset:13312\nM v[104:107],a[184:187],v[212:215],v[104:107]\nM v[108:111],a[188:191],v[212:215],v[108:111]\nds_read_b64_tr_b16 v[212:213],v242 offset:33792\n" \
"ds_read_b64_tr_b16 v[214:215],v242 offset:34048\nM v[48:51],a[176:179],v[216:219],v[48:51]\nbuffer_load_dwordx4 v243,s[40:43],s69 offen offset:0 lds\nM v[52:55],a[180:183],v[216:219],v[52:55]\nM v[112:115],a[184:187],v[216:219],v[112:115]\nM v[116:119],a[188:191],v[216:219],v[116:119]\nds_read_b64_tr_b16 v[216:217],v241 offset:34304\nds_read_b64_tr_b16 v[218:219],v241 offset:34560\n" \
"M v[56:59],a[176:179],v[220:223],v[56:59]\nbuffer_load_dwordx4 a[28:31],v240,s[52:55],s77 offen offset:3072 sc1\nM v[60:63],a[180:183],v[220:223],v[60:63]\nds_write_b64 v250,v[182:183] offset:13320\ns_add_u32 s77,s77,4096\nM v[120:123],a[184:187],v[220:223],v[120:123]\nM v[124:127],a[188:191],v[220:223],v[124:127]\nds_read_b64_tr_b16 v[220:221],v242 offset:34304\n" \
"s_waitcnt lgkmcnt(9)\nM v[0:3],a[192:195],v[192:195],v[0:3]\nbuffer_load_dwordx4 v244,s[40:43],s69 offen offset:1024 lds\nM v[4:7],a[196:199],v[192:195],v[4:7]\nds_read_b64_tr_b16 v[222:223],v242 offset:34560\nM v[64:67],a[200:203],v[192:195],v[64:67]\nM v[68:71],a[204:207],v[192:195],v[68:71]\nds_read_b64_tr_b16 v[192:193],v241 offset:40960\n" \
"ds_read_b64_tr_b16 v[194:195],v241 offset:41216\nM v[8:11],a[192:195],v[196:199],v[8:11]\nbuffer_load_dwordx4 a[32:35],v240,s[52:55],s76 offen offset:0 sc1\nM v[12:15],a[196:199],v[196:199],v[12:15]\nM v[72:75],a[200:203],v[196:199],v[72:75]\nM v[76:79],a[204:207],v[196:199],v[76:79]\nds_read_b64_tr_b16 v[196:197],v242 offset:40960\nds_read_b64_tr_b16 v[198:199],v242 offset:41216\n" \
"M v[16:19],a[192:195],v[200:203],v[16:19]\nbuffer_load_dwordx4 v245,s[40:43],s69 offen offset:2048 lds\nM v[20:23],a[196:199],v[200:203],v[20:23]\nds_write_b64 v250,v[184:185] offset:14336\nM v[80:83],a[200:203],v[200:203],v[80:83]\nM v[84:87],a[204:207],v[200:203],v[84:87]\nds_read_b64_tr_b16 v[200:201],v241 offset:41472\nds_read_b64_tr_b16 v[202:203],v241 offset:41728\n" \
"M v[24:27],a[192:195],v[204:207],v[24:27]\nbuffer_load_dwordx4 a[36:39],v240,s[52:55],s76 offen offset:1024 sc1\nM v[28:31],a[196:199],v[204:207],v[28:31]\nM v[88:91],a[200:203],v[204:207],v[88:91]\nM v[92:95],a[204:207],v[204:207],v[92:95]\nds_read_b64_tr_b16 v[204:205],v242 offset:41472\ns_waitcnt lgkmcnt(8)\nM v[32:35],a[192:195],v[208:211],v[32:35]\n" \
"buffer_load_dwordx4 v246,s[40:43],s69 offen offset:3072 lds\nM v[36:39],a[196:199],v[208:211],v[36:39]\nds_read_b64_tr_b16 v[206:207],v242 offset:41728\nM v[96:99],a[200:203],v[208:211],v[96:99]\nM v[100:103],a[204:207],v[208:211],v[100:103]\nds_read_b64_tr_b16 v[208:209],v241 offset:41984\nds_read_b64_tr_b16 v[210:211],v241 offset:42240\nM v[40:43],a[192:195],v[212:215],v[40:43]\n" \
"buffer_load_dwordx4 a[40:43],v240,s[52:55],s77 offen offset:0 sc1\nM v[44:47],a[196:199],v[212:215],v[44:47]\nds_write_b64 v250,v[186:187] offset:14344\nM v[104:107],a[200:203],v[212:215],v[104:107]\nM v[108:111],a[204:207],v[212:215],v[108:111]\nds_read_b64_tr_b16 v[212:213],v242 offset:41984\nds_read_b64_tr_b16 v[214:215],v242 offset:42240\nM v[48:51],a[192:195],v[216:219],v[48:51]\n" \
"s_waitcnt vmcnt(19)\nbuffer_store_dword v252,off,s[52:55],s29 sc1\nM v[52:55],a[196:199],v[216:219],v[52:55]\ns_add_u32 s29,s29,4096\nM v[112:115],a[200:203],v[216:219],v[112:115]\nbuffer_load_dwordx4 a[44:47],v240,s[52:55],s77 offen offset:1024 sc1\nM v[116:119],a[204:207],v[216:219],v[116:119]\nds_read_b64_tr_b16 v[216:217],v241 offset:42496\n" \
"ds_read_b64_tr_b16 v[218:219],v241 offset:42752\nM v[56:59],a[192:195],v[220:223],v[56:59]\nM v[60:63],a[196:199],v[220:223],v[60:63]\nM v[120:123],a[200:203],v[220:223],v[120:123]\nM v[124:127],a[204:207],v[220:223],v[124:127]\nds_read_b64_tr_b16 v[220:221],v242 offset:42496\ns_waitcnt lgkmcnt(8)\nM v[0:3],a[208:211],v[192:195],v[0:3]\n" \
"ds_write_b64 v250,v[188:189] offset:15360\nds_read_b64_tr_b16 v[222:223],v242 offset:42752\nM v[4:7],a[212:215],v[192:195],v[4:7]\nM v[64:67],a[216:219],v[192:195],v[64:67]\nbuffer_load_dwordx4 a[48:51],v240,s[52:55],s76 offen offset:2048 sc1\nM v[68:71],a[220:223],v[192:195],v[68:71]\nM v[8:11],a[208:211],v[196:199],v[8:11]\nM v[12:15],a[212:215],v[196:199],v[12:15]\n" \
"M v[72:75],a[216:219],v[196:199],v[72:75]\nM v[76:79],a[220:223],v[196:199],v[76:79]\nM v[16:19],a[208:211],v[200:203],v[16:19]\nM v[20:23],a[212:215],v[200:203],v[20:23]\nM v[80:83],a[216:219],v[200:203],v[80:83]\nbuffer_load_dwordx4 a[52:55],v240,s[52:55],s76 offen offset:3072 sc1\nM v[84:87],a[220:223],v[200:203],v[84:87]\ns_add_u32 s76,s76,4096\n" \
"M v[24:27],a[208:211],v[204:207],v[24:27]\nds_write_b64 v250,v[190:191] offset:15368\ns_add_u32 m0,s64,131072\nM v[28:31],a[212:215],v[204:207],v[28:31]\ns_add_u32 s69,s68,1048576\nv_add_u32 v250,65536,v250\nM v[88:91],a[216:219],v[204:207],v[88:91]\ns_waitcnt lgkmcnt(1)\n" \
"s_barrier\nM v[92:95],a[220:223],v[204:207],v[92:95]\nds_read_b64_tr_b16 v[192:193],v241 offset:49152\nds_read_b64_tr_b16 v[194:195],v241 offset:49408\nM v[32:35],a[208:211],v[208:211],v[32:35]\nds_read_b64_tr_b16 v[196:197],v242 offset:49152\nds_read_b64_tr_b16 v[198:199],v242 offset:49408\nM v[36:39],a[212:215],v[208:211],v[36:39]\n" \
"ds_read_b64_tr_b16 v[200:201],v241 offset:49664\nds_read_b64_tr_b16 v[202:203],v241 offset:49920\nM v[96:99],a[216:219],v[208:211],v[96:99]\nds_read_b64_tr_b16 v[204:205],v242 offset:49664\nds_read_b64_tr_b16 v[206:207],v242 offset:49920\nM v[100:103],a[220:223],v[208:211],v[100:103]\nds_read_b64_tr_b16 v[208:209],v241 offset:50176\nds_read_b64_tr_b16 v[210:211],v241 offset:50432\n" \
"M v[40:43],a[208:211],v[212:215],v[40:43]\nbuffer_load_dwordx4 a[56:59],v240,s[52:55],s77 offen offset:2048 sc1\nM v[44:47],a[212:215],v[212:215],v[44:47]\nM v[104:107],a[216:219],v[212:215],v[104:107]\nM v[108:111],a[220:223],v[212:215],v[108:111]\nds_read_b64_tr_b16 v[212:213],v242 offset:50176\nds_read_b64_tr_b16 v[214:215],v242 offset:50432\nM v[48:51],a[208:211],v[216:219],v[48:51]\n" \
"buffer_load_dwordx4 v243,s[40:43],s69 offen offset:0 lds\nM v[52:55],a[212:215],v[216:219],v[52:55]\nM v[112:115],a[216:219],v[216:219],v[112:115]\nM v[116:119],a[220:223],v[216:219],v[116:119]\nds_read_b64_tr_b16 v[216:217],v241 offset:50688\nds_read_b64_tr_b16 v[218:219],v241 offset:50944\nM v[56:59],a[208:211],v[220:223],v[56:59]\nbuffer_load_dwordx4 a[60:63],v240,s[52:55],s77 offen offset:3072 sc1\n" \
"M v[60:63],a[212:215],v[220:223],v[60:63]\ns_add_u32 s77,s77,4096\nM v[120:123],a[216:219],v[220:223],v[120:123]\nM v[124:127],a[220:223],v[220:223],v[124:127]\nds_read_b64_tr_b16 v[220:221],v242 offset:50688\ns_waitcnt lgkmcnt(7)\nM v[0:3],a[224:227],v[192:195],v[0:3]\nbuffer_load_dwordx4 v244,s[40:43],s69 offen offset:1024 lds\n" \
"M v[4:7],a[228:231],v[192:195],v[4:7]\nds_read_b64_tr_b16 v[222:223],v242 offset:50944\nM v[64:67],a[232:235],v[192:195],v[64:67]\nM v[68:71],a[236:239],v[192:195],v[68:71]\nds_read_b64_tr_b16 v[192:193],v241 offset:57344\nds_read_b64_tr_b16 v[194:195],v241 offset:57600\nM v[8:11],a[224:227],v[196:199],v[8:11]\nbuffer_load_dwordx4 a[64:67],v240,s[52:55],s76 offen offset:0 sc1\n" \
"M v[12:15],a[228:231],v[196:199],v[12:15]\nM v[72:75],a[232:235],v[196:199],v[72:75]\nM v[76:79],a[236:239],v[196:199],v[76:79]\nds_read_b64_tr_b16 v[196:197],v242 offset:57344\nds_read_b64_tr_b16 v[198:199],v242 offset:57600\nM v[16:19],a[224:227],v[200:203],v[16:19]\nbuffer_load_dwordx4 v245,s[40:43],s69 offen offset:2048 lds\nM v[20:23],a[228:231],v[200:203],v[20:23]\n" \
"M v[80:83],a[232:235],v[200:203],v[80:83]\nM v[84:87],a[236:239],v[200:203],v[84:87]\nds_read_b64_tr_b16 v[200:201],v241 offset:57856\nds_read_b64_tr_b16 v[202:203],v241 offset:58112\nM v[24:27],a[224:227],v[204:207],v[24:27]\nbuffer_load_dwordx4 a[68:71],v240,s[52:55],s76 offen offset:1024 sc1\nM v[28:31],a[228:231],v[204:207],v[28:31]\nM v[88:91],a[232:235],v[204:207],v[88:91]\n" \
"M v[92:95],a[236:239],v[204:207],v[92:95]\nds_read_b64_tr_b16 v[204:205],v242 offset:57856\ns_waitcnt lgkmcnt(7)\nM v[32:35],a[224:227],v[208:211],v[32:35]\nbuffer_load_dwordx4 v246,s[40:43],s69 offen offset:3072 lds\nM v[36:39],a[228:231],v[208:211],v[36:39]\nds_read_b64_tr_b16 v[206:207],v242 offset:58112\nM v[96:99],a[232:235],v[208:211],v[96:99]\n" \
"M v[100:103],a[236:239],v[208:211],v[100:103]\nds_read_b64_tr_b16 v[208:209],v241 offset:58368\nds_read_b64_tr_b16 v[210:211],v241 offset:58624\nM v[40:43],a[224:227],v[212:215],v[40:43]\nbuffer_load_dwordx4 a[72:75],v240,s[52:55],s77 offen offset:0 sc1\nM v[44:47],a[228:231],v[212:215],v[44:47]\nM v[104:107],a[232:235],v[212:215],v[104:107]\nM v[108:111],a[236:239],v[212:215],v[108:111]\n" \
"ds_read_b64_tr_b16 v[212:213],v242 offset:58368\nds_read_b64_tr_b16 v[214:215],v242 offset:58624\nM v[48:51],a[224:227],v[216:219],v[48:51]\nM v[52:55],a[228:231],v[216:219],v[52:55]\nM v[112:115],a[232:235],v[216:219],v[112:115]\nbuffer_load_dwordx4 a[76:79],v240,s[52:55],s77 offen offset:1024 sc1\nM v[116:119],a[236:239],v[216:219],v[116:119]\nds_read_b64_tr_b16 v[216:217],v241 offset:58880\n" \
"ds_read_b64_tr_b16 v[218:219],v241 offset:59136\nM v[56:59],a[224:227],v[220:223],v[56:59]\nM v[60:63],a[228:231],v[220:223],v[60:63]\nM v[120:123],a[232:235],v[220:223],v[120:123]\nM v[124:127],a[236:239],v[220:223],v[124:127]\nds_read_b64_tr_b16 v[220:221],v242 offset:58880\ns_waitcnt lgkmcnt(8)\nM v[0:3],a[240:243],v[192:195],v[0:3]\n" \
"ds_read_b64_tr_b16 v[222:223],v242 offset:59136\nM v[4:7],a[244:247],v[192:195],v[4:7]\nM v[64:67],a[248:251],v[192:195],v[64:67]\nbuffer_load_dwordx4 a[80:83],v240,s[52:55],s76 offen offset:2048 sc1\nM v[68:71],a[252:255],v[192:195],v[68:71]\nM v[8:11],a[240:243],v[196:199],v[8:11]\nM v[12:15],a[244:247],v[196:199],v[12:15]\nM v[72:75],a[248:251],v[196:199],v[72:75]\n" \
"v_cvt_pk_bf16_f32 v128,v0,v1\nv_cvt_pk_bf16_f32 v129,v2,v3\nM v[76:79],a[252:255],v[196:199],v[76:79]\nv_cvt_pk_bf16_f32 v130,v4,v5\nv_cvt_pk_bf16_f32 v131,v6,v7\nM v[16:19],a[240:243],v[200:203],v[16:19]\nv_cvt_pk_bf16_f32 v160,v64,v65\nv_cvt_pk_bf16_f32 v161,v66,v67\n" \
"M v[20:23],a[244:247],v[200:203],v[20:23]\nv_cvt_pk_bf16_f32 v162,v68,v69\nv_cvt_pk_bf16_f32 v163,v70,v71\nM v[80:83],a[248:251],v[200:203],v[80:83]\nv_cvt_pk_bf16_f32 v132,v8,v9\nv_cvt_pk_bf16_f32 v133,v10,v11\nM v[84:87],a[252:255],v[200:203],v[84:87]\nv_cvt_pk_bf16_f32 v134,v12,v13\n" \
"s_waitcnt lgkmcnt(2)\nM v[24:27],a[240:243],v[204:207],v[24:27]\nv_cvt_pk_bf16_f32 v135,v14,v15\nv_cvt_pk_bf16_f32 v164,v72,v73\nM v[28:31],a[244:247],v[204:207],v[28:31]\nv_cvt_pk_bf16_f32 v165,v74,v75\nv_cvt_pk_bf16_f32 v166,v76,v77\nM v[88:91],a[248:251],v[204:207],v[88:91]\n" \
"s_waitcnt vmcnt(23) lgkmcnt(0)\ns_barrier\nM v[92:95],a[252:255],v[204:207],v[92:95]\nds_read_b64_tr_b16 v[192:193],v241 offset:0\nds_read_b64_tr_b16 v[194:195],v241 offset:256\nM v[32:35],a[240:243],v[208:211],v[32:35]\nds_read_b64_tr_b16 v[196:197],v242 offset:0\nds_read_b64_tr_b16 v[198:199],v242 offset:256\n" \
"M v[36:39],a[244:247],v[208:211],v[36:39]\nds_read_b64_tr_b16 v[200:201],v241 offset:512\nds_read_b64_tr_b16 v[202:203],v241 offset:768\nM v[96:99],a[248:251],v[208:211],v[96:99]\nds_read_b64_tr_b16 v[204:205],v242 offset:512\nds_read_b64_tr_b16 v[206:207],v242 offset:768\nM v[100:103],a[252:255],v[208:211],v[100:103]\nds_read_b64_tr_b16 v[208:209],v241 offset:1024\n" \
"ds_read_b64_tr_b16 v[210:211],v241 offset:1280\nM v[40:43],a[240:243],v[212:215],v[40:43]\nv_cvt_pk_bf16_f32 v167,v78,v79\nv_cvt_pk_bf16_f32 v136,v16,v17\nM v[44:47],a[244:247],v[212:215],v[44:47]\nv_cvt_pk_bf16_f32 v137,v18,v19\nv_cvt_pk_bf16_f32 v138,v20,v21\nM v[104:107],a[248:251],v[212:215],v[104:107]\n" \
"v_cvt_pk_bf16_f32 v139,v22,v23\nv_cvt_pk_bf16_f32 v168,v80,v81\nM v[108:111],a[252:255],v[212:215],v[108:111]\nv_cvt_pk_bf16_f32 v169,v82,v83\nds_read_b64_tr_b16 v[212:213],v242 offset:1024\nM v[48:51],a[240:243],v[216:219],v[48:51]\nds_read_b64_tr_b16 v[214:215],v242 offset:1280\nv_cvt_pk_bf16_f32 v170,v84,v85\n" \
"M v[52:55],a[244:247],v[216:219],v[52:55]\nv_cvt_pk_bf16_f32 v171,v86,v87\nv_cvt_pk_bf16_f32 v140,v24,v25\nM v[112:115],a[248:251],v[216:219],v[112:115]\nv_cvt_pk_bf16_f32 v141,v26,v27\nv_cvt_pk_bf16_f32 v142,v28,v29\nM v[116:119],a[252:255],v[216:219],v[116:119]\nv_cvt_pk_bf16_f32 v143,v30,v31\n" \
"v_cvt_pk_bf16_f32 v172,v88,v89\nM v[56:59],a[240:243],v[220:223],v[56:59]\nv_cvt_pk_bf16_f32 v173,v90,v91\nds_read_b64_tr_b16 v[216:217],v241 offset:1536\nM v[60:63],a[244:247],v[220:223],v[60:63]\nds_read_b64_tr_b16 v[218:219],v241 offset:1792\nv_cvt_pk_bf16_f32 v174,v92,v93\nM v[120:123],a[248:251],v[220:223],v[120:123]\n" \
"v_cvt_pk_bf16_f32 v175,v94,v95\nv_cvt_pk_bf16_f32 v144,v32,v33\nM v[124:127],a[252:255],v[220:223],v[124:127]\nv_cvt_pk_bf16_f32 v145,v34,v35\ns_waitcnt lgkmcnt(4)\nM v[0:3],v[192:195],a[0:3],0\nv_cvt_pk_bf16_f32 v146,v36,v37\nv_cvt_pk_bf16_f32 v147,v38,v39\n" \
"M v[4:7],v[192:195],a[4:7],0\nv_cvt_pk_bf16_f32 v176,v96,v97\nv_cvt_pk_bf16_f32 v177,v98,v99\nM v[64:67],v[192:195],a[8:11],0\nds_read_b64_tr_b16 v[220:221],v242 offset:1536\nds_read_b64_tr_b16 v[222:223],v242 offset:1792\nM v[68:71],v[192:195],a[12:15],0\nv_cvt_pk_bf16_f32 v178,v100,v101\n" \
"v_cvt_pk_bf16_f32 v179,v102,v103\nM v[8:11],v[196:199],a[0:3],0\nv_cvt_pk_bf16_f32 v148,v40,v41\nv_cvt_pk_bf16_f32 v149,v42,v43\nM v[12:15],v[196:199],a[4:7],0\nv_cvt_pk_bf16_f32 v150,v44,v45\nv_cvt_pk_bf16_f32 v151,v46,v47\nM v[72:75],v[196:199],a[8:11],0\n" \
"v_cvt_pk_bf16_f32 v180,v104,v105\nv_cvt_pk_bf16_f32 v181,v106,v107\nM v[76:79],v[196:199],a[12:15],0\nds_read_b64_tr_b16 v[192:193],v241 offset:8192\nds_read_b64_tr_b16 v[194:195],v241 offset:8448\nM v[16:19],v[200:203],a[0:3],0\nv_cvt_pk_bf16_f32 v182,v108,v109\nv_cvt_pk_bf16_f32 v183,v110,v111\n" \
"M v[20:23],v[200:203],a[4:7],0\nv_cvt_pk_bf16_f32 v152,v48,v49\nv_cvt_pk_bf16_f32 v153,v50,v51\nM v[80:83],v[200:203],a[8:11],0\nv_cvt_pk_bf16_f32 v154,v52,v53\nv_cvt_pk_bf16_f32 v155,v54,v55\nM v[84:87],v[200:203],a[12:15],0\nv_cvt_pk_bf16_f32 v184,v112,v113\n" \
"v_cvt_pk_bf16_f32 v185,v114,v115\nM v[24:27],v[204:207],a[0:3],0\nds_read_b64_tr_b16 v[196:197],v242 offset:8192\nds_read_b64_tr_b16 v[198:199],v242 offset:8448\nM v[28:31],v[204:207],a[4:7],0\nv_cvt_pk_bf16_f32 v186,v116,v117\nv_cvt_pk_bf16_f32 v187,v118,v119\nM v[88:91],v[204:207],a[8:11],0\n" \
"v_cvt_pk_bf16_f32 v156,v56,v57\nv_cvt_pk_bf16_f32 v157,v58,v59\nM v[92:95],v[204:207],a[12:15],0\nv_cvt_pk_bf16_f32 v158,v60,v61\nv_cvt_pk_bf16_f32 v159,v62,v63\nM v[32:35],v[208:211],a[0:3],0\nv_cvt_pk_bf16_f32 v188,v120,v121\nv_cvt_pk_bf16_f32 v189,v122,v123\n" \
"M v[36:39],v[208:211],a[4:7],0\nds_read_b64_tr_b16 v[200:201],v241 offset:8704\nds_read_b64_tr_b16 v[202:203],v241 offset:8960\nM v[96:99],v[208:211],a[8:11],0\nv_cvt_pk_bf16_f32 v190,v124,v125\nv_cvt_pk_bf16_f32 v191,v126,v127\nM v[100:103],v[208:211],a[12:15],0\nds_read_b64_tr_b16 v[204:205],v242 offset:8704\n" \
"s_waitcnt lgkmcnt(5)\nM v[40:43],v[212:215],a[0:3],0\nbuffer_store_dwordx4 v[128:131],v240,s[52:55],s74 offen offset:0 sc1\nM v[44:47],v[212:215],a[4:7],0\nds_read_b64_tr_b16 v[206:207],v242 offset:8960\nds_read_b64_tr_b16 v[208:209],v241 offset:9216\nM v[104:107],v[212:215],a[8:11],0\nds_read_b64_tr_b16 v[210:211],v241 offset:9472\n" \
"s_add_u32 m0,s64,147456\nM v[108:111],v[212:215],a[12:15],0\nds_read_b64_tr_b16 v[212:213],v242 offset:9216\nds_read_b64_tr_b16 v[214:215],v242 offset:9472\nM v[48:51],v[216:219],a[0:3],0\nbuffer_load_dwordx4 a[84:87],v240,s[52:55],s76 offen offset:3072 sc1\nM v[52:55],v[216:219],a[4:7],0\ns_add_u32 s69,s68,1572864\n" \
"s_add_u32 s76,s76,4096\nM v[112:115],v[216:219],a[8:11],0\nM v[116:119],v[216:219],a[12:15],0\nds_read_b64_tr_b16 v[216:217],v241 offset:9728\nds_read_b64_tr_b16 v[218:219],v241 offset:9984\nM v[56:59],v[220:223],a[0:3],0\nbuffer_load_dwordx4 a[88:91],v240,s[52:55],s77 offen offset:2048 sc1\nM v[60:63],v[220:223],a[4:7],0\n" \
"M v[120:123],v[220:223],a[8:11],0\nM v[124:127],v[220:223],a[12:15],0\nds_read_b64_tr_b16 v[220:221],v242 offset:9728\nds_read_b64_tr_b16 v[222:223],v242 offset:9984\nM v[0:3],v[192:195],a[16:19],v[0:3]\nbuffer_load_dwordx4 a[92:95],v240,s[52:55],s77 offen offset:3072 sc1\nM v[4:7],v[192:195],a[20:23],v[4:7]\ns_add_u32 s77,s77,4096\n" \
"M v[64:67],v[192:195],a[24:27],v[64:67]\ns_waitcnt vmcnt(25)\nM v[68:71],v[192:195],a[28:31],v[68:71]\ns_waitcnt lgkmcnt(6)\nM v[8:11],v[196:199],a[16:19],v[8:11]\nbuffer_load_dwordx4 a[96:99],v240,s[52:55],s76 offen offset:0 sc1\nM v[12:15],v[196:199],a[20:23],v[12:15]\nM v[72:75],v[196:199],a[24:27],v[72:75]\n" \
"M v[76:79],v[196:199],a[28:31],v[76:79]\nM v[16:19],v[200:203],a[16:19],v[16:19]\nbuffer_load_dwordx4 a[100:103],v240,s[52:55],s76 offen offset:1024 sc1\nM v[20:23],v[200:203],a[20:23],v[20:23]\nM v[80:83],v[200:203],a[24:27],v[80:83]\nM v[84:87],v[200:203],a[28:31],v[84:87]\nM v[24:27],v[204:207],a[16:19],v[24:27]\nbuffer_load_dwordx4 a[104:107],v240,s[52:55],s77 offen offset:0 sc1\n" \
"M v[28:31],v[204:207],a[20:23],v[28:31]\nM v[88:91],v[204:207],a[24:27],v[88:91]\ns_waitcnt vmcnt(22) lgkmcnt(0)\ns_barrier\nM v[92:95],v[204:207],a[28:31],v[92:95]\nds_read_b64_tr_b16 v[192:193],v241 offset:16384\nds_read_b64_tr_b16 v[194:195],v241 offset:16640\nM v[32:35],v[208:211],a[16:19],v[32:35]\n" \
"ds_read_b64_tr_b16 v[196:197],v242 offset:16384\nds_read_b64_tr_b16 v[198:199],v242 offset:16640\nM v[36:39],v[208:211],a[20:23],v[36:39]\nds_read_b64_tr_b16 v[200:201],v241 offset:16896\nds_read_b64_tr_b16 v[202:203],v241 offset:17152\nM v[96:99],v[208:211],a[24:27],v[96:99]\nbuffer_store_dwordx4 v[132:135],v240,s[52:55],s74 offen offset:1024 sc1\nM v[100:103],v[208:211],a[28:31],v[100:103]\n" \
"ds_read_b64_tr_b16 v[204:205],v242 offset:16896\nds_read_b64_tr_b16 v[206:207],v242 offset:17152\nM v[40:43],v[212:215],a[16:19],v[40:43]\nds_read_b64_tr_b16 v[208:209],v241 offset:17408\nds_read_b64_tr_b16 v[210:211],v241 offset:17664\nM v[44:47],v[212:215],a[20:23],v[44:47]\nM v[104:107],v[212:215],a[24:27],v[104:107]\nbuffer_load_dwordx4 a[108:111],v240,s[52:55],s77 offen offset:1024 sc1\n" \
"M v[108:111],v[212:215],a[28:31],v[108:111]\nds_read_b64_tr_b16 v[212:213],v242 offset:17408\nds_read_b64_tr_b16 v[214:215],v242 offset:17664\nM v[48:51],v[216:219],a[16:19],v[48:51]\nM v[52:55],v[216:219],a[20:23],v[52:55]\nM v[112:115],v[216:219],a[24:27],v[112:115]\nbuffer_load_dwordx4 a[112:115],v240,s[52:55],s76 offen offset:2048 sc1\nM v[116:119],v[216:219],a[28:31],v[116:119]\n" \
"ds_read_b64_tr_b16 v[216:217],v241 offset:17920\nds_read_b64_tr_b16 v[218:219],v241 offset:18176\nM v[56:59],v[220:223],a[16:19],v[56:59]\nM v[60:63],v[220:223],a[20:23],v[60:63]\nM v[120:123],v[220:223],a[24:27],v[120:123]\nbuffer_load_dwordx4 v243,s[40:43],s69 offen offset:0 lds\nM v[124:127],v[220:223],a[28:31],v[124:127]\nds_read_b64_tr_b16 v[220:221],v242 offset:17920\n" \
"s_waitcnt lgkmcnt(9)\nM v[0:3],v[192:195],a[32:35],v[0:3]\nds_read_b64_tr_b16 v[222:223],v242 offset:18176\nM v[4:7],v[192:195],a[36:39],v[4:7]\nM v[64:67],v[192:195],a[40:43],v[64:67]\ns_waitcnt vmcnt(21)\nM v[68:71],v[192:195],a[44:47],v[68:71]\nds_read_b64_tr_b16 v[192:193],v241 offset:24576\n" \
"ds_read_b64_tr_b16 v[194:195],v241 offset:24832\nM v[8:11],v[196:199],a[32:35],v[8:11]\nbuffer_load_dwordx4 v244,s[40:43],s69 offen offset:1024 lds\nM v[12:15],v[196:199],a[36:39],v[12:15]\nM v[72:75],v[196:199],a[40:43],v[72:75]\nM v[76:79],v[196:199],a[44:47],v[76:79]\nds_read_b64_tr_b16 v[196:197],v242 offset:24576\nds_read_b64_tr_b16 v[198:199],v242 offset:24832\n" \
"M v[16:19],v[200:203],a[32:35],v[16:19]\nbuffer_load_dwordx4 v245,s[40:43],s69 offen offset:2048 lds\nM v[20:23],v[200:203],a[36:39],v[20:23]\nM v[80:83],v[200:203],a[40:43],v[80:83]\nM v[84:87],v[200:203],a[44:47],v[84:87]\nds_read_b64_tr_b16 v[200:201],v241 offset:25088\ns_waitcnt lgkmcnt(7)\nM v[24:27],v[204:207],a[32:35],v[24:27]\n" \
"buffer_load_dwordx4 v246,s[40:43],s69 offen offset:3072 lds\nM v[28:31],v[204:207],a[36:39],v[28:31]\nds_read_b64_tr_b16 v[202:203],v241 offset:25344\ns_add_u32 m0,s64,98304\nM v[88:91],v[204:207],a[40:43],v[88:91]\ns_add_u32 s69,s68,2097152\nM v[92:95],v[204:207],a[44:47],v[92:95]\nds_read_b64_tr_b16 v[204:205],v242 offset:25088\n" \
"ds_read_b64_tr_b16 v[206:207],v242 offset:25344\nM v[32:35],v[208:211],a[32:35],v[32:35]\nbuffer_store_dwordx4 v[136:139],v240,s[52:55],s74 offen offset:2048 sc1\nM v[36:39],v[208:211],a[36:39],v[36:39]\nM v[96:99],v[208:211],a[40:43],v[96:99]\nM v[100:103],v[208:211],a[44:47],v[100:103]\nds_read_b64_tr_b16 v[208:209],v241 offset:25600\nds_read_b64_tr_b16 v[210:211],v241 offset:25856\n" \
"M v[40:43],v[212:215],a[32:35],v[40:43]\nbuffer_load_dwordx4 a[116:119],v240,s[52:55],s76 offen offset:3072 sc1\nM v[44:47],v[212:215],a[36:39],v[44:47]\ns_add_u32 s76,s76,4096\nM v[104:107],v[212:215],a[40:43],v[104:107]\nM v[108:111],v[212:215],a[44:47],v[108:111]\nds_read_b64_tr_b16 v[212:213],v242 offset:25600\nds_read_b64_tr_b16 v[214:215],v242 offset:25856\n" \
"M v[48:51],v[216:219],a[32:35],v[48:51]\nbuffer_load_dwordx4 a[120:123],v240,s[52:55],s77 offen offset:2048 sc1\nM v[52:55],v[216:219],a[36:39],v[52:55]\nM v[112:115],v[216:219],a[40:43],v[112:115]\nM v[116:119],v[216:219],a[44:47],v[116:119]\nds_read_b64_tr_b16 v[216:217],v241 offset:26112\ns_waitcnt lgkmcnt(7)\nM v[56:59],v[220:223],a[32:35],v[56:59]\n" \
"buffer_load_dwordx4 a[124:127],v240,s[52:55],s77 offen offset:3072 sc1\nM v[60:63],v[220:223],a[36:39],v[60:63]\nds_read_b64_tr_b16 v[218:219],v241 offset:26368\ns_add_u32 s77,s77,4096\nM v[120:123],v[220:223],a[40:43],v[120:123]\nM v[124:127],v[220:223],a[44:47],v[124:127]\nds_read_b64_tr_b16 v[220:221],v242 offset:26112\nds_read_b64_tr_b16 v[222:223],v242 offset:26368\n" \
"M v[0:3],v[192:195],a[48:51],v[0:3]\nbuffer_load_dwordx4 a[128:131],v240,s[52:55],s76 offen offset:0 sc1\nM v[4:7],v[192:195],a[52:55],v[4:7]\nM v[64:67],v[192:195],a[56:59],v[64:67]\ns_waitcnt vmcnt(27)\nM v[68:71],v[192:195],a[60:63],v[68:71]\nM v[8:11],v[196:199],a[48:51],v[8:11]\nbuffer_load_dwordx4 a[132:135],v240,s[52:55],s76 offen offset:1024 sc1\n" \
"M v[12:15],v[196:199],a[52:55],v[12:15]\nM v[72:75],v[196:199],a[56:59],v[72:75]\nM v[76:79],v[196:199],a[60:63],v[76:79]\nM v[16:19],v[200:203],a[48:51],v[16:19]\nbuffer_load_dwordx4 a[136:139],v240,s[52:55],s77 offen offset:0 sc1\nM v[20:23],v[200:203],a[52:55],v[20:23]\nM v[80:83],v[200:203],a[56:59],v[80:83]\nM v[84:87],v[200:203],a[60:63],v[84:87]\n" \
"s_waitcnt lgkmcnt(3)\nM v[24:27],v[204:207],a[48:51],v[24:27]\nbuffer_load_dwordx4 a[140:143],v240,s[52:55],s77 offen offset:1024 sc1\nM v[28:31],v[204:207],a[52:55],v[28:31]\nM v[88:91],v[204:207],a[56:59],v[88:91]\ns_waitcnt vmcnt(22) lgkmcnt(0)\ns_barrier\nM v[92:95],v[204:207],a[60:63],v[92:95]\n" \
"ds_read_b64_tr_b16 v[192:193],v241 offset:32768\nds_read_b64_tr_b16 v[194:195],v241 offset:33024\nM v[32:35],v[208:211],a[48:51],v[32:35]\nbuffer_store_dwordx4 v[140:143],v240,s[52:55],s74 offen offset:3072 sc1\nM v[36:39],v[208:211],a[52:55],v[36:39]\nds_read_b64_tr_b16 v[196:197],v242 offset:32768\nds_read_b64_tr_b16 v[198:199],v242 offset:33024\nM v[96:99],v[208:211],a[56:59],v[96:99]\n" \
"ds_read_b64_tr_b16 v[200:201],v241 offset:33280\nds_read_b64_tr_b16 v[202:203],v241 offset:33536\nM v[100:103],v[208:211],a[60:63],v[100:103]\nds_read_b64_tr_b16 v[204:205],v242 offset:33280\nds_read_b64_tr_b16 v[206:207],v242 offset:33536\nM v[40:43],v[212:215],a[48:51],v[40:43]\nds_read_b64_tr_b16 v[208:209],v241 offset:33792\nds_read_b64_tr_b16 v[210:211],v241 offset:34048\n" \
"M v[44:47],v[212:215],a[52:55],v[44:47]\nbuffer_load_dwordx4 a[144:147],v240,s[52:55],s76 offen offset:2048 sc1\nM v[104:107],v[212:215],a[56:59],v[104:107]\ns_add_u32 s74,s74,4096\nM v[108:111],v[212:215],a[60:63],v[108:111]\nds_read_b64_tr_b16 v[212:213],v242 offset:33792\nds_read_b64_tr_b16 v[214:215],v242 offset:34048\nM v[48:51],v[216:219],a[48:51],v[48:51]\n" \
"M v[52:55],v[216:219],a[52:55],v[52:55]\nbuffer_load_dwordx4 v243,s[40:43],s69 offen offset:0 lds\nM v[112:115],v[216:219],a[56:59],v[112:115]\nM v[116:119],v[216:219],a[60:63],v[116:119]\nds_read_b64_tr_b16 v[216:217],v241 offset:34304\nds_read_b64_tr_b16 v[218:219],v241 offset:34560\nM v[56:59],v[220:223],a[48:51],v[56:59]\nM v[60:63],v[220:223],a[52:55],v[60:63]\n" \
"buffer_load_dwordx4 v244,s[40:43],s69 offen offset:1024 lds\nM v[120:123],v[220:223],a[56:59],v[120:123]\nM v[124:127],v[220:223],a[60:63],v[124:127]\nds_read_b64_tr_b16 v[220:221],v242 offset:34304\ns_waitcnt lgkmcnt(9)\nM v[0:3],v[192:195],a[64:67],v[0:3]\nds_read_b64_tr_b16 v[222:223],v242 offset:34560\nM v[4:7],v[192:195],a[68:71],v[4:7]\n" \
"buffer_load_dwordx4 v245,s[40:43],s69 offen offset:2048 lds\nM v[64:67],v[192:195],a[72:75],v[64:67]\nM v[68:71],v[192:195],a[76:79],v[68:71]\nds_read_b64_tr_b16 v[192:193],v241 offset:40960\nds_read_b64_tr_b16 v[194:195],v241 offset:41216\nM v[8:11],v[196:199],a[64:67],v[8:11]\nM v[12:15],v[196:199],a[68:71],v[12:15]\nbuffer_load_dwordx4 v246,s[40:43],s69 offen offset:3072 lds\n" \
"M v[72:75],v[196:199],a[72:75],v[72:75]\ns_add_u32 m0,s64,114688\ns_add_u32 s69,s68,2621440\nM v[76:79],v[196:199],a[76:79],v[76:79]\nds_read_b64_tr_b16 v[196:197],v242 offset:40960\nds_read_b64_tr_b16 v[198:199],v242 offset:41216\nM v[16:19],v[200:203],a[64:67],v[16:19]\nM v[20:23],v[200:203],a[68:71],v[20:23]\n" \
"buffer_load_dwordx4 a[148:151],v240,s[52:55],s76 offen offset:3072 sc1\nM v[80:83],v[200:203],a[72:75],v[80:83]\ns_add_u32 s76,s76,4096\nM v[84:87],v[200:203],a[76:79],v[84:87]\nds_read_b64_tr_b16 v[200:201],v241 offset:41472\ns_waitcnt lgkmcnt(6)\nM v[24:27],v[204:207],a[64:67],v[24:27]\nds_read_b64_tr_b16 v[202:203],v241 offset:41728\n" \
"M v[28:31],v[204:207],a[68:71],v[28:31]\nbuffer_store_dwordx4 v[144:147],v240,s[52:55],s74 offen offset:0 sc1\nM v[88:91],v[204:207],a[72:75],v[88:91]\nM v[92:95],v[204:207],a[76:79],v[92:95]\nds_read_b64_tr_b16 v[204:205],v242 offset:41472\nds_read_b64_tr_b16 v[206:207],v242 offset:41728\nM v[32:35],v[208:211],a[64:67],v[32:35]\nM v[36:39],v[208:211],a[68:71],v[36:39]\n" \
"buffer_load_dwordx4 a[152:155],v240,s[52:55],s77 offen offset:2048 sc1\nM v[96:99],v[208:211],a[72:75],v[96:99]\nM v[100:103],v[208:211],a[76:79],v[100:103]\nds_read_b64_tr_b16 v[208:209],v241 offset:41984\nds_read_b64_tr_b16 v[210:211],v241 offset:42240\nM v[40:43],v[212:215],a[64:67],v[40:43]\nM v[44:47],v[212:215],a[68:71],v[44:47]\nbuffer_load_dwordx4 a[156:159],v240,s[52:55],s77 offen offset:3072 sc1\n" \
"M v[104:107],v[212:215],a[72:75],v[104:107]\ns_add_u32 s77,s77,4096\nM v[108:111],v[212:215],a[76:79],v[108:111]\nds_read_b64_tr_b16 v[212:213],v242 offset:41984\nds_read_b64_tr_b16 v[214:215],v242 offset:42240\nM v[48:51],v[216:219],a[64:67],v[48:51]\nM v[52:55],v[216:219],a[68:71],v[52:55]\nbuffer_load_dwordx4 a[160:163],v240,s[52:55],s76 offen offset:0 sc1\n" \
"M v[112:115],v[216:219],a[72:75],v[112:115]\nM v[116:119],v[216:219],a[76:79],v[116:119]\nds_read_b64_tr_b16 v[216:217],v241 offset:42496\ns_waitcnt lgkmcnt(7)\nM v[56:59],v[220:223],a[64:67],v[56:59]\nds_read_b64_tr_b16 v[218:219],v241 offset:42752\nM v[60:63],v[220:223],a[68:71],v[60:63]\nbuffer_load_dwordx4 a[164:167],v240,s[52:55],s76 offen offset:1024 sc1\n" \
"M v[120:123],v[220:223],a[72:75],v[120:123]\nM v[124:127],v[220:223],a[76:79],v[124:127]\nds_read_b64_tr_b16 v[220:221],v242 offset:42496\nds_read_b64_tr_b16 v[222:223],v242 offset:42752\nM v[0:3],v[192:195],a[80:83],v[0:3]\ns_waitcnt vmcnt(27)\nM v[4:7],v[192:195],a[84:87],v[4:7]\nbuffer_load_dwordx4 a[168:171],v240,s[52:55],s77 offen offset:0 sc1\n" \
"M v[64:67],v[192:195],a[88:91],v[64:67]\nM v[68:71],v[192:195],a[92:95],v[68:71]\nM v[8:11],v[196:199],a[80:83],v[8:11]\nM v[12:15],v[196:199],a[84:87],v[12:15]\nbuffer_load_dwordx4 a[172:175],v240,s[52:55],s77 offen offset:1024 sc1\nM v[72:75],v[196:199],a[88:91],v[72:75]\nM v[76:79],v[196:199],a[92:95],v[76:79]\nM v[16:19],v[200:203],a[80:83],v[16:19]\n" \
"M v[20:23],v[200:203],a[84:87],v[20:23]\nbuffer_load_dwordx4 a[176:179],v240,s[52:55],s76 offen offset:2048 sc1\nM v[80:83],v[200:203],a[88:91],v[80:83]\nM v[84:87],v[200:203],a[92:95],v[84:87]\ns_waitcnt lgkmcnt(2)\nM v[24:27],v[204:207],a[80:83],v[24:27]\nM v[28:31],v[204:207],a[84:87],v[28:31]\nbuffer_store_dwordx4 v[148:151],v240,s[52:55],s74 offen offset:1024 sc1\n" \
"M v[88:91],v[204:207],a[88:91],v[88:91]\ns_waitcnt vmcnt(24) lgkmcnt(0)\ns_barrier\nM v[92:95],v[204:207],a[92:95],v[92:95]\nds_read_b64_tr_b16 v[192:193],v241 offset:49152\nds_read_b64_tr_b16 v[194:195],v241 offset:49408\nM v[32:35],v[208:211],a[80:83],v[32:35]\nds_read_b64_tr_b16 v[196:197],v242 offset:49152\n" \
"ds_read_b64_tr_b16 v[198:199],v242 offset:49408\nM v[36:39],v[208:211],a[84:87],v[36:39]\nds_read_b64_tr_b16 v[200:201],v241 offset:49664\nds_read_b64_tr_b16 v[202:203],v241 offset:49920\nM v[96:99],v[208:211],a[88:91],v[96:99]\nds_read_b64_tr_b16 v[204:205],v242 offset:49664\nds_read_b64_tr_b16 v[206:207],v242 offset:49920\nM v[100:103],v[208:211],a[92:95],v[100:103]\n" \
"ds_read_b64_tr_b16 v[208:209],v241 offset:50176\nds_read_b64_tr_b16 v[210:211],v241 offset:50432\nM v[40:43],v[212:215],a[80:83],v[40:43]\nbuffer_load_dwordx4 v243,s[40:43],s69 offen offset:0 lds\nM v[44:47],v[212:215],a[84:87],v[44:47]\nM v[104:107],v[212:215],a[88:91],v[104:107]\nM v[108:111],v[212:215],a[92:95],v[108:111]\nds_read_b64_tr_b16 v[212:213],v242 offset:50176\n" \
"ds_read_b64_tr_b16 v[214:215],v242 offset:50432\nM v[48:51],v[216:219],a[80:83],v[48:51]\nbuffer_load_dwordx4 v244,s[40:43],s69 offen offset:1024 lds\nM v[52:55],v[216:219],a[84:87],v[52:55]\nM v[112:115],v[216:219],a[88:91],v[112:115]\nM v[116:119],v[216:219],a[92:95],v[116:119]\nds_read_b64_tr_b16 v[216:217],v241 offset:50688\nds_read_b64_tr_b16 v[218:219],v241 offset:50944\n" \
"M v[56:59],v[220:223],a[80:83],v[56:59]\nbuffer_load_dwordx4 v245,s[40:43],s69 offen offset:2048 lds\nM v[60:63],v[220:223],a[84:87],v[60:63]\nM v[120:123],v[220:223],a[88:91],v[120:123]\nM v[124:127],v[220:223],a[92:95],v[124:127]\nds_read_b64_tr_b16 v[220:221],v242 offset:50688\ns_waitcnt lgkmcnt(8)\nM v[0:3],v[192:195],a[96:99],v[0:3]\n" \
"buffer_load_dwordx4 v246,s[40:43],s69 offen offset:3072 lds\nM v[4:7],v[192:195],a[100:103],v[4:7]\nds_read_b64_tr_b16 v[222:223],v242 offset:50944\ns_add_u32 m0,s64,131072\nM v[64:67],v[192:195],a[104:107],v[64:67]\ns_add_u32 s69,s68,3145728\nM v[68:71],v[192:195],a[108:111],v[68:71]\nds_read_b64_tr_b16 v[192:193],v241 offset:57344\n" \
"ds_read_b64_tr_b16 v[194:195],v241 offset:57600\nM v[8:11],v[196:199],a[96:99],v[8:11]\nbuffer_load_dwordx4 a[180:183],v240,s[52:55],s76 offen offset:3072 sc1\nM v[12:15],v[196:199],a[100:103],v[12:15]\ns_add_u32 s76,s76,4096\nM v[72:75],v[196:199],a[104:107],v[72:75]\nM v[76:79],v[196:199],a[108:111],v[76:79]\nds_read_b64_tr_b16 v[196:197],v242 offset:57344\n" \
"ds_read_b64_tr_b16 v[198:199],v242 offset:57600\nM v[16:19],v[200:203],a[96:99],v[16:19]\nbuffer_store_dwordx4 v[152:155],v240,s[52:55],s74 offen offset:2048 sc1\nM v[20:23],v[200:203],a[100:103],v[20:23]\nM v[80:83],v[200:203],a[104:107],v[80:83]\nM v[84:87],v[200:203],a[108:111],v[84:87]\nds_read_b64_tr_b16 v[200:201],v241 offset:57856\ns_waitcnt lgkmcnt(6)\n" \
"M v[24:27],v[204:207],a[96:99],v[24:27]\nbuffer_load_dwordx4 a[184:187],v240,s[52:55],s77 offen offset:2048 sc1\nM v[28:31],v[204:207],a[100:103],v[28:31]\nds_read_b64_tr_b16 v[202:203],v241 offset:58112\nM v[88:91],v[204:207],a[104:107],v[88:91]\nM v[92:95],v[204:207],a[108:111],v[92:95]\nds_read_b64_tr_b16 v[204:205],v242 offset:57856\nds_read_b64_tr_b16 v[206:207],v242 offset:58112\n" \
"M v[32:35],v[208:211],a[96:99],v[32:35]\nbuffer_load_dwordx4 a[188:191],v240,s[52:55],s77 offen offset:3072 sc1\nM v[36:39],v[208:211],a[100:103],v[36:39]\ns_add_u32 s77,s77,4096\nM v[96:99],v[208:211],a[104:107],v[96:99]\nM v[100:103],v[208:211],a[108:111],v[100:103]\nds_read_b64_tr_b16 v[208:209],v241 offset:58368\nds_read_b64_tr_b16 v[210:211],v241 offset:58624\n" \
"M v[40:43],v[212:215],a[96:99],v[40:43]\nbuffer_load_dwordx4 a[192:195],v240,s[52:55],s76 offen offset:0 sc1\nM v[44:47],v[212:215],a[100:103],v[44:47]\nM v[104:107],v[212:215],a[104:107],v[104:107]\nM v[108:111],v[212:215],a[108:111],v[108:111]\nds_read_b64_tr_b16 v[212:213],v242 offset:58368\nds_read_b64_tr_b16 v[214:215],v242 offset:58624\nM v[48:51],v[216:219],a[96:99],v[48:51]\n" \
"buffer_load_dwordx4 a[196:199],v240,s[52:55],s76 offen offset:1024 sc1\nM v[52:55],v[216:219],a[100:103],v[52:55]\nM v[112:115],v[216:219],a[104:107],v[112:115]\nM v[116:119],v[216:219],a[108:111],v[116:119]\nds_read_b64_tr_b16 v[216:217],v241 offset:58880\ns_waitcnt lgkmcnt(7)\nM v[56:59],v[220:223],a[96:99],v[56:59]\nbuffer_load_dwordx4 a[200:203],v240,s[52:55],s77 offen offset:0 sc1\n" \
"M v[60:63],v[220:223],a[100:103],v[60:63]\nds_read_b64_tr_b16 v[218:219],v241 offset:59136\nM v[120:123],v[220:223],a[104:107],v[120:123]\nM v[124:127],v[220:223],a[108:111],v[124:127]\nds_read_b64_tr_b16 v[220:221],v242 offset:58880\nds_read_b64_tr_b16 v[222:223],v242 offset:59136\nM v[0:3],v[192:195],a[112:115],v[0:3]\ns_waitcnt vmcnt(27)\n" \
"M v[4:7],v[192:195],a[116:119],v[4:7]\nbuffer_load_dwordx4 a[204:207],v240,s[52:55],s77 offen offset:1024 sc1\nM v[64:67],v[192:195],a[120:123],v[64:67]\nM v[68:71],v[192:195],a[124:127],v[68:71]\nM v[8:11],v[196:199],a[112:115],v[8:11]\nM v[12:15],v[196:199],a[116:119],v[12:15]\nbuffer_load_dwordx4 a[208:211],v240,s[52:55],s76 offen offset:2048 sc1\nM v[72:75],v[196:199],a[120:123],v[72:75]\n" \
"M v[76:79],v[196:199],a[124:127],v[76:79]\nM v[16:19],v[200:203],a[112:115],v[16:19]\nM v[20:23],v[200:203],a[116:119],v[20:23]\nbuffer_store_dwordx4 v[156:159],v240,s[52:55],s74 offen offset:3072 sc1\nM v[80:83],v[200:203],a[120:123],v[80:83]\ns_add_u32 s74,s74,4096\nM v[84:87],v[200:203],a[124:127],v[84:87]\ns_waitcnt lgkmcnt(3)\n" \
"M v[24:27],v[204:207],a[112:115],v[24:27]\nM v[28:31],v[204:207],a[116:119],v[28:31]\nbuffer_load_dwordx4 v243,s[40:43],s69 offen offset:0 lds\nM v[88:91],v[204:207],a[120:123],v[88:91]\ns_waitcnt vmcnt(24) lgkmcnt(0)\ns_barrier\nM v[92:95],v[204:207],a[124:127],v[92:95]\nds_read_b64_tr_b16 v[192:193],v241 offset:0\n" \
"ds_read_b64_tr_b16 v[194:195],v241 offset:256\nM v[32:35],v[208:211],a[112:115],v[32:35]\nds_read_b64_tr_b16 v[196:197],v242 offset:0\nds_read_b64_tr_b16 v[198:199],v242 offset:256\nM v[36:39],v[208:211],a[116:119],v[36:39]\nds_read_b64_tr_b16 v[200:201],v241 offset:512\nds_read_b64_tr_b16 v[202:203],v241 offset:768\nM v[96:99],v[208:211],a[120:123],v[96:99]\n" \
"ds_read_b64_tr_b16 v[204:205],v242 offset:512\nds_read_b64_tr_b16 v[206:207],v242 offset:768\nM v[100:103],v[208:211],a[124:127],v[100:103]\nds_read_b64_tr_b16 v[208:209],v241 offset:1024\nds_read_b64_tr_b16 v[210:211],v241 offset:1280\nM v[40:43],v[212:215],a[112:115],v[40:43]\nbuffer_load_dwordx4 v244,s[40:43],s69 offen offset:1024 lds\nM v[44:47],v[212:215],a[116:119],v[44:47]\n" \
"M v[104:107],v[212:215],a[120:123],v[104:107]\nM v[108:111],v[212:215],a[124:127],v[108:111]\nds_read_b64_tr_b16 v[212:213],v242 offset:1024\nds_read_b64_tr_b16 v[214:215],v242 offset:1280\nM v[48:51],v[216:219],a[112:115],v[48:51]\nbuffer_load_dwordx4 v245,s[40:43],s69 offen offset:2048 lds\nM v[52:55],v[216:219],a[116:119],v[52:55]\nM v[112:115],v[216:219],a[120:123],v[112:115]\n" \
"M v[116:119],v[216:219],a[124:127],v[116:119]\nds_read_b64_tr_b16 v[216:217],v241 offset:1536\nds_read_b64_tr_b16 v[218:219],v241 offset:1792\nM v[56:59],v[220:223],a[112:115],v[56:59]\nbuffer_load_dwordx4 v246,s[40:43],s69 offen offset:3072 lds\nM v[60:63],v[220:223],a[116:119],v[60:63]\ns_add_u32 m0,s64,147456\ns_add_u32 s69,s68,3670016\n" \
"M v[120:123],v[220:223],a[120:123],v[120:123]\ns_lshl_b32 s68,s60,19\ns_lshl2_add_u32 s68,s64,s68\nM v[124:127],v[220:223],a[124:127],v[124:127]\nds_read_b64_tr_b16 v[220:221],v242 offset:1536\ns_waitcnt lgkmcnt(6)\nM v[0:3],v[192:195],a[128:131],v[0:3]\nds_read_b64_tr_b16 v[222:223],v242 offset:1792\n" \
"s_add_u32 s68,s68,s70\nM v[4:7],v[192:195],a[132:135],v[4:7]\nbuffer_load_dwordx4 a[212:215],v240,s[52:55],s76 offen offset:3072 sc1\nM v[64:67],v[192:195],a[136:139],v[64:67]\ns_add_u32 s76,s76,4096\nM v[68:71],v[192:195],a[140:143],v[68:71]\nds_read_b64_tr_b16 v[192:193],v241 offset:8192\nds_read_b64_tr_b16 v[194:195],v241 offset:8448\n" \
"M v[8:11],v[196:199],a[128:131],v[8:11]\nM v[12:15],v[196:199],a[132:135],v[12:15]\nbuffer_store_dwordx4 v[160:163],v240,s[52:55],s74 offen offset:0 sc1\nM v[72:75],v[196:199],a[136:139],v[72:75]\nM v[76:79],v[196:199],a[140:143],v[76:79]\nds_read_b64_tr_b16 v[196:197],v242 offset:8192\nds_read_b64_tr_b16 v[198:199],v242 offset:8448\nM v[16:19],v[200:203],a[128:131],v[16:19]\n" \
"M v[20:23],v[200:203],a[132:135],v[20:23]\nbuffer_load_dwordx4 a[216:219],v240,s[52:55],s77 offen offset:2048 sc1\nM v[80:83],v[200:203],a[136:139],v[80:83]\nM v[84:87],v[200:203],a[140:143],v[84:87]\nds_read_b64_tr_b16 v[200:201],v241 offset:8704\nds_read_b64_tr_b16 v[202:203],v241 offset:8960\nM v[24:27],v[204:207],a[128:131],v[24:27]\nM v[28:31],v[204:207],a[132:135],v[28:31]\n" \
"buffer_load_dwordx4 a[220:223],v240,s[52:55],s77 offen offset:3072 sc1\nM v[88:91],v[204:207],a[136:139],v[88:91]\ns_add_u32 s77,s77,4096\nM v[92:95],v[204:207],a[140:143],v[92:95]\nds_read_b64_tr_b16 v[204:205],v242 offset:8704\ns_waitcnt lgkmcnt(7)\nM v[32:35],v[208:211],a[128:131],v[32:35]\nds_read_b64_tr_b16 v[206:207],v242 offset:8960\n" \
"M v[36:39],v[208:211],a[132:135],v[36:39]\nbuffer_load_dwordx4 a[224:227],v240,s[52:55],s76 offen offset:0 sc1\nM v[96:99],v[208:211],a[136:139],v[96:99]\nM v[100:103],v[208:211],a[140:143],v[100:103]\nds_read_b64_tr_b16 v[208:209],v241 offset:9216\nds_read_b64_tr_b16 v[210:211],v241 offset:9472\nM v[40:43],v[212:215],a[128:131],v[40:43]\nds_read_b128 a[0:3],v240 offset:0\n" \
"M v[44:47],v[212:215],a[132:135],v[44:47]\nbuffer_load_dword v248,v228,s[52:55],s80 offen sc1\nM v[104:107],v[212:215],a[136:139],v[104:107]\nbuffer_load_dwordx4 a[228:231],v240,s[52:55],s76 offen offset:1024 sc1\nM v[108:111],v[212:215],a[140:143],v[108:111]\nds_read_b64_tr_b16 v[212:213],v242 offset:9216\nds_read_b64_tr_b16 v[214:215],v242 offset:9472\nM v[48:51],v[216:219],a[128:131],v[48:51]\n" \
"M v[52:55],v[216:219],a[132:135],v[52:55]\nM v[112:115],v[216:219],a[136:139],v[112:115]\nbuffer_load_dwordx4 a[232:235],v240,s[52:55],s77 offen offset:0 sc1\nM v[116:119],v[216:219],a[140:143],v[116:119]\nds_read_b64_tr_b16 v[216:217],v241 offset:9728\nds_read_b64_tr_b16 v[218:219],v241 offset:9984\nM v[56:59],v[220:223],a[128:131],v[56:59]\nds_read_b128 a[4:7],v240 offset:1024\n" \
"M v[60:63],v[220:223],a[132:135],v[60:63]\nM v[120:123],v[220:223],a[136:139],v[120:123]\nbuffer_load_dwordx4 a[236:239],v240,s[52:55],s77 offen offset:1024 sc1\nM v[124:127],v[220:223],a[140:143],v[124:127]\nds_read_b64_tr_b16 v[220:221],v242 offset:9728\ns_waitcnt lgkmcnt(8)\nM v[0:3],v[192:195],a[144:147],v[0:3]\nds_read_b64_tr_b16 v[222:223],v242 offset:9984\n" \
"ds_read_b128 a[8:11],v240 offset:2048\nM v[4:7],v[192:195],a[148:151],v[4:7]\ns_waitcnt vmcnt(28)\nM v[64:67],v[192:195],a[152:155],v[64:67]\nbuffer_store_dwordx4 v[164:167],v240,s[52:55],s74 offen offset:1024 sc1\nM v[68:71],v[192:195],a[156:159],v[68:71]\nM v[8:11],v[196:199],a[144:147],v[8:11]\nM v[12:15],v[196:199],a[148:151],v[12:15]\n" \
"M v[72:75],v[196:199],a[152:155],v[72:75]\nbuffer_load_dwordx4 a[240:243],v240,s[52:55],s76 offen offset:2048 sc1\nM v[76:79],v[196:199],a[156:159],v[76:79]\nds_read_b128 a[12:15],v240 offset:3072\nM v[16:19],v[200:203],a[144:147],v[16:19]\nM v[20:23],v[200:203],a[148:151],v[20:23]\nM v[80:83],v[200:203],a[152:155],v[80:83]\nbuffer_load_dwordx4 v243,s[40:43],s69 offen offset:0 lds\n" \
"M v[84:87],v[200:203],a[156:159],v[84:87]\nM v[24:27],v[204:207],a[144:147],v[24:27]\nds_read_b128 a[16:19],v240 offset:4096\nM v[28:31],v[204:207],a[148:151],v[28:31]\nM v[88:91],v[204:207],a[152:155],v[88:91]\ns_waitcnt vmcnt(25) lgkmcnt(3)\ns_barrier\nM v[92:95],v[204:207],a[156:159],v[92:95]\n" \
"ds_read_b64_tr_b16 v[192:193],v241 offset:16384\nds_read_b64_tr_b16 v[194:195],v241 offset:16640\nM v[32:35],v[208:211],a[144:147],v[32:35]\nds_read_b64_tr_b16 v[196:197],v242 offset:16384\nds_read_b64_tr_b16 v[198:199],v242 offset:16640\nM v[36:39],v[208:211],a[148:151],v[36:39]\nds_read_b64_tr_b16 v[200:201],v241 offset:16896\nds_read_b64_tr_b16 v[202:203],v241 offset:17152\n" \
"M v[96:99],v[208:211],a[152:155],v[96:99]\nds_read_b64_tr_b16 v[204:205],v242 offset:16896\nds_read_b64_tr_b16 v[206:207],v242 offset:17152\nM v[100:103],v[208:211],a[156:159],v[100:103]\nds_read_b64_tr_b16 v[208:209],v241 offset:17408\nds_read_b64_tr_b16 v[210:211],v241 offset:17664\nM v[40:43],v[212:215],a[144:147],v[40:43]\nbuffer_load_dwordx4 v244,s[40:43],s69 offen offset:1024 lds\n" \
"M v[44:47],v[212:215],a[148:151],v[44:47]\nds_read_b128 a[20:23],v240 offset:5120\nM v[104:107],v[212:215],a[152:155],v[104:107]\nM v[108:111],v[212:215],a[156:159],v[108:111]\nds_read_b64_tr_b16 v[212:213],v242 offset:17408\nds_read_b64_tr_b16 v[214:215],v242 offset:17664\nM v[48:51],v[216:219],a[144:147],v[48:51]\nbuffer_load_dwordx4 v245,s[40:43],s69 offen offset:2048 lds\n" \
"M v[52:55],v[216:219],a[148:151],v[52:55]\nds_read_b128 a[24:27],v240 offset:6144\nM v[112:115],v[216:219],a[152:155],v[112:115]\nM v[116:119],v[216:219],a[156:159],v[116:119]\nds_read_b64_tr_b16 v[216:217],v241 offset:17920\nds_read_b64_tr_b16 v[218:219],v241 offset:18176\nM v[56:59],v[220:223],a[144:147],v[56:59]\nbuffer_load_dwordx4 v246,s[40:43],s69 offen offset:3072 lds\n" \
"M v[60:63],v[220:223],a[148:151],v[60:63]\ns_sub_u32 s40,s18,0x1000\ns_subb_u32 s41,s19,0\nM v[120:123],v[220:223],a[152:155],v[120:123]\nds_read_b128 a[28:31],v240 offset:7168\nM v[124:127],v[220:223],a[156:159],v[124:127]\nds_read_b64_tr_b16 v[220:221],v242 offset:17920\ns_waitcnt lgkmcnt(8)\n" \
"M v[0:3],v[192:195],a[160:163],v[0:3]\nbuffer_store_dwordx4 v[168:171],v240,s[52:55],s74 offen offset:2048 sc1\nM v[4:7],v[192:195],a[164:167],v[4:7]\nds_read_b64_tr_b16 v[222:223],v242 offset:18176\nM v[64:67],v[192:195],a[168:171],v[64:67]\nM v[68:71],v[192:195],a[172:175],v[68:71]\nds_read_b64_tr_b16 v[192:193],v241 offset:24576\nds_read_b64_tr_b16 v[194:195],v241 offset:24832\n" \
"M v[8:11],v[196:199],a[160:163],v[8:11]\nbuffer_load_dwordx4 a[244:247],v240,s[52:55],s76 offen offset:3072 sc1\nM v[12:15],v[196:199],a[164:167],v[12:15]\nds_read_b128 a[32:35],v240 offset:8192\nM v[72:75],v[196:199],a[168:171],v[72:75]\nM v[76:79],v[196:199],a[172:175],v[76:79]\nds_read_b64_tr_b16 v[196:197],v242 offset:24576\nds_read_b64_tr_b16 v[198:199],v242 offset:24832\n" \
"M v[16:19],v[200:203],a[160:163],v[16:19]\nbuffer_load_dwordx4 a[248:251],v240,s[52:55],s77 offen offset:2048 sc1\nM v[20:23],v[200:203],a[164:167],v[20:23]\nM v[80:83],v[200:203],a[168:171],v[80:83]\nds_read_b128 a[36:39],v240 offset:9216\nM v[84:87],v[200:203],a[172:175],v[84:87]\nds_read_b64_tr_b16 v[200:201],v241 offset:25088\nds_read_b64_tr_b16 v[202:203],v241 offset:25344\n" \
"M v[24:27],v[204:207],a[160:163],v[24:27]\nbuffer_load_dwordx4 a[252:255],v240,s[52:55],s77 offen offset:3072 sc1\nM v[28:31],v[204:207],a[164:167],v[28:31]\nM v[88:91],v[204:207],a[168:171],v[88:91]\nM v[92:95],v[204:207],a[172:175],v[92:95]\nds_read_b64_tr_b16 v[204:205],v242 offset:25088\nds_read_b64_tr_b16 v[206:207],v242 offset:25344\nM v[32:35],v[208:211],a[160:163],v[32:35]\n" \
"ds_read_b128 a[40:43],v240 offset:10240\nM v[36:39],v[208:211],a[164:167],v[36:39]\ns_waitcnt vmcnt(13)\nv_cmp_eq_u32 vcc,0,v248\nM v[96:99],v[208:211],a[168:171],v[96:99]\ns_cbranch_vccnz .Lps1\n.Lpb1:\ns_add_u32 s80,s80,4096\n" \
"M v[100:103],v[208:211],a[172:175],v[100:103]\nds_read_b64_tr_b16 v[208:209],v241 offset:25600\ns_waitcnt lgkmcnt(8)\nM v[40:43],v[212:215],a[160:163],v[40:43]\ns_cmp_ge_u32 s62,6\ns_cselect_b64 vcc,-1,0\nM v[44:47],v[212:215],a[164:167],v[44:47]\nds_read_b64_tr_b16 v[210:211],v241 offset:25856\n" \
"v_cndmask_b32 v254,v231,v229,vcc\nM v[104:107],v[212:215],a[168:171],v[104:107]\ns_cmp_eq_u32 s62,3\ns_cselect_b64 vcc,-1,0\nM v[108:111],v[212:215],a[172:175],v[108:111]\nds_read_b64_tr_b16 v[212:213],v242 offset:25600\nds_read_b64_tr_b16 v[214:215],v242 offset:25856\nM v[48:51],v[216:219],a[160:163],v[48:51]\n" \
"v_cndmask_b32 v254,v254,v230,vcc\nv_bfe_u32 v230,v249,2,3\nM v[52:55],v[216:219],a[164:167],v[52:55]\nv_and_b32 v231,0x70,v243\nv_lshl_or_b32 v230,v230,10,v231\nM v[112:115],v[216:219],a[168:171],v[112:115]\nv_and_b32 v231,16,v249\nv_lshlrev_b32 v231,1,v231\n" \
"M v[116:119],v[216:219],a[172:175],v[116:119]\nds_read_b64_tr_b16 v[216:217],v241 offset:26112\nds_read_b64_tr_b16 v[218:219],v241 offset:26368\nM v[56:59],v[220:223],a[160:163],v[56:59]\nv_xor_b32 v230,v230,v231\nv_add_u32 v243,4096,v230\nM v[60:63],v[220:223],a[164:167],v[60:63]\nv_add_u32 v244,3200,v230\n" \
"v_add_u32 v245,10240,v230\nM v[120:123],v[220:223],a[168:171],v[120:123]\nbuffer_store_dwordx4 v[172:175],v240,s[52:55],s74 offen offset:3072 sc1\nM v[124:127],v[220:223],a[172:175],v[124:127]\nds_read_b64_tr_b16 v[220:221],v242 offset:26112\nds_read_b64_tr_b16 v[222:223],v242 offset:26368\nM v[0:3],v[192:195],a[176:179],v[0:3]\ns_add_u32 s74,s74,4096\n" \
"v_add_u32 v246,9344,v230\nM v[4:7],v[192:195],a[180:183],v[4:7]\nv_and_b32 v229,3,v249\nv_cmp_eq_u32 vcc,0,v229\nM v[64:67],v[192:195],a[184:187],v[64:67]\nv_mov_b32 v230,0x3f803f80\nv_cndmask_b32 v229,0,v230,vcc\nM v[68:71],v[192:195],a[188:191],v[68:71]\n" \
"v_and_b32 v230,0xff,v241\ns_waitcnt lgkmcnt(4)\nM v[8:11],v[196:199],a[176:179],v[8:11]\nv_and_b32 v231,16,v249\nv_lshl_or_b32 v230,v231,4,v230\nM v[12:15],v[196:199],a[180:183],v[12:15]\nv_and_b32 v231,32,v249\nds_read_b128 a[44:47],v240 offset:11264\n" \
"M v[72:75],v[196:199],a[184:187],v[72:75]\nds_read_b128 a[48:51],v240 offset:12288\nds_read_b128 a[52:55],v240 offset:13312\nM v[76:79],v[196:199],a[188:191],v[76:79]\nM v[16:19],v[200:203],a[176:179],v[16:19]\nds_read_b128 a[56:59],v240 offset:14336\nM v[20:23],v[200:203],a[180:183],v[20:23]\nM v[80:83],v[200:203],a[184:187],v[80:83]\n" \
"M v[84:87],v[200:203],a[188:191],v[84:87]\nM v[24:27],v[204:207],a[176:179],v[24:27]\nM v[28:31],v[204:207],a[180:183],v[28:31]\nM v[88:91],v[204:207],a[184:187],v[88:91]\ns_waitcnt lgkmcnt(4)\ns_barrier\nM v[92:95],v[204:207],a[188:191],v[92:95]\nds_read_b64_tr_b16 v[192:193],v241 offset:32768\n" \
"ds_read_b64_tr_b16 v[194:195],v241 offset:33024\nM v[32:35],v[208:211],a[176:179],v[32:35]\nds_read_b64_tr_b16 v[196:197],v242 offset:32768\nds_read_b64_tr_b16 v[198:199],v242 offset:33024\nM v[36:39],v[208:211],a[180:183],v[36:39]\nds_read_b64_tr_b16 v[200:201],v241 offset:33280\nds_read_b64_tr_b16 v[202:203],v241 offset:33536\nM v[96:99],v[208:211],a[184:187],v[96:99]\n" \
"ds_read_b64_tr_b16 v[204:205],v242 offset:33280\nds_read_b64_tr_b16 v[206:207],v242 offset:33536\nM v[100:103],v[208:211],a[188:191],v[100:103]\nds_read_b64_tr_b16 v[208:209],v241 offset:33792\nds_read_b64_tr_b16 v[210:211],v241 offset:34048\nM v[40:43],v[212:215],a[176:179],v[40:43]\nds_write_b64 v250,v[128:129] offset:0\nds_read_b128 a[60:63],v240 offset:15360\n" \
"M v[44:47],v[212:215],a[180:183],v[44:47]\nds_write_b64 v250,v[130:131] offset:8\nM v[104:107],v[212:215],a[184:187],v[104:107]\nds_write_b64 v250,v[132:133] offset:1024\nM v[108:111],v[212:215],a[188:191],v[108:111]\nds_read_b64_tr_b16 v[212:213],v242 offset:33792\nds_read_b64_tr_b16 v[214:215],v242 offset:34048\nM v[48:51],v[216:219],a[176:179],v[48:51]\n" \
"M v[52:55],v[216:219],a[180:183],v[52:55]\nds_write_b64 v250,v[134:135] offset:1032\nM v[112:115],v[216:219],a[184:187],v[112:115]\nM v[116:119],v[216:219],a[188:191],v[116:119]\nds_read_b64_tr_b16 v[216:217],v241 offset:34304\nds_read_b64_tr_b16 v[218:219],v241 offset:34560\nM v[56:59],v[220:223],a[176:179],v[56:59]\nbuffer_store_dwordx4 v[176:179],v240,s[52:55],s74 offen offset:0 sc1\n" \
"M v[60:63],v[220:223],a[180:183],v[60:63]\nds_write_b64 v250,v[136:137] offset:2048\nM v[120:123],v[220:223],a[184:187],v[120:123]\nM v[124:127],v[220:223],a[188:191],v[124:127]\nds_read_b64_tr_b16 v[220:221],v242 offset:34304\ns_waitcnt lgkmcnt(13)\nM v[0:3],v[192:195],a[192:195],v[0:3]\nds_write_b64 v250,v[138:139] offset:2056\n" \
"ds_read_b64_tr_b16 v[222:223],v242 offset:34560\nM v[4:7],v[192:195],a[196:199],v[4:7]\nM v[64:67],v[192:195],a[200:203],v[64:67]\nds_write_b64 v250,v[140:141] offset:3072\nM v[68:71],v[192:195],a[204:207],v[68:71]\nds_read_b64_tr_b16 v[192:193],v241 offset:40960\nds_read_b64_tr_b16 v[194:195],v241 offset:41216\nM v[8:11],v[196:199],a[192:195],v[8:11]\n" \
"M v[12:15],v[196:199],a[196:199],v[12:15]\nds_write_b64 v250,v[142:143] offset:3080\nM v[72:75],v[196:199],a[200:203],v[72:75]\nM v[76:79],v[196:199],a[204:207],v[76:79]\nds_read_b64_tr_b16 v[196:197],v242 offset:40960\nds_read_b64_tr_b16 v[198:199],v242 offset:41216\nM v[16:19],v[200:203],a[192:195],v[16:19]\nds_write_b64 v250,v[144:145] offset:4096\n" \
"M v[20:23],v[200:203],a[196:199],v[20:23]\nM v[80:83],v[200:203],a[200:203],v[80:83]\nM v[84:87],v[200:203],a[204:207],v[84:87]\nds_read_b64_tr_b16 v[200:201],v241 offset:41472\nds_read_b64_tr_b16 v[202:203],v241 offset:41728\nM v[24:27],v[204:207],a[192:195],v[24:27]\nds_write_b64 v250,v[146:147] offset:4104\nM v[28:31],v[204:207],a[196:199],v[28:31]\n" \
"M v[88:91],v[204:207],a[200:203],v[88:91]\nds_write_b64 v250,v[148:149] offset:5120\nM v[92:95],v[204:207],a[204:207],v[92:95]\nds_read_b64_tr_b16 v[204:205],v242 offset:41472\ns_waitcnt lgkmcnt(12)\nM v[32:35],v[208:211],a[192:195],v[32:35]\nds_read_b64_tr_b16 v[206:207],v242 offset:41728\nM v[36:39],v[208:211],a[196:199],v[36:39]\n" \
"ds_write_b64 v250,v[150:151] offset:5128\nM v[96:99],v[208:211],a[200:203],v[96:99]\nM v[100:103],v[208:211],a[204:207],v[100:103]\nds_read_b64_tr_b16 v[208:209],v241 offset:41984\nds_read_b64_tr_b16 v[210:211],v241 offset:42240\nM v[40:43],v[212:215],a[192:195],v[40:43]\nds_write_b64 v250,v[152:153] offset:6144\nM v[44:47],v[212:215],a[196:199],v[44:47]\n" \
"M v[104:107],v[212:215],a[200:203],v[104:107]\nM v[108:111],v[212:215],a[204:207],v[108:111]\nds_read_b64_tr_b16 v[212:213],v242 offset:41984\nds_read_b64_tr_b16 v[214:215],v242 offset:42240\nM v[48:51],v[216:219],a[192:195],v[48:51]\nds_write_b64 v250,v[154:155] offset:6152\nM v[52:55],v[216:219],a[196:199],v[52:55]\nM v[112:115],v[216:219],a[200:203],v[112:115]\n" \
"buffer_store_dwordx4 v[180:183],v240,s[52:55],s74 offen offset:1024 sc1\nM v[116:119],v[216:219],a[204:207],v[116:119]\nds_read_b64_tr_b16 v[216:217],v241 offset:42496\nds_read_b64_tr_b16 v[218:219],v241 offset:42752\nM v[56:59],v[220:223],a[192:195],v[56:59]\nds_write_b64 v250,v[156:157] offset:7168\nM v[60:63],v[220:223],a[196:199],v[60:63]\nds_write_b64 v250,v[158:159] offset:7176\n" \
"M v[120:123],v[220:223],a[200:203],v[120:123]\nM v[124:127],v[220:223],a[204:207],v[124:127]\nds_read_b64_tr_b16 v[220:221],v242 offset:42496\ns_waitcnt lgkmcnt(11)\nM v[0:3],v[192:195],a[208:211],v[0:3]\nds_write_b64 v250,v[160:161] offset:8192\nds_read_b64_tr_b16 v[222:223],v242 offset:42752\nM v[4:7],v[192:195],a[212:215],v[4:7]\n" \
"M v[64:67],v[192:195],a[216:219],v[64:67]\nM v[68:71],v[192:195],a[220:223],v[68:71]\nds_write_b64 v250,v[162:163] offset:8200\nM v[8:11],v[196:199],a[208:211],v[8:11]\nM v[12:15],v[196:199],a[212:215],v[12:15]\nM v[72:75],v[196:199],a[216:219],v[72:75]\nds_write_b64 v250,v[164:165] offset:9216\nM v[76:79],v[196:199],a[220:223],v[76:79]\n" \
"M v[16:19],v[200:203],a[208:211],v[16:19]\nM v[20:23],v[200:203],a[212:215],v[20:23]\nds_write_b64 v250,v[166:167] offset:9224\nM v[80:83],v[200:203],a[216:219],v[80:83]\nM v[84:87],v[200:203],a[220:223],v[84:87]\nM v[24:27],v[204:207],a[208:211],v[24:27]\nds_write_b64 v250,v[168:169] offset:10240\nM v[28:31],v[204:207],a[212:215],v[28:31]\n" \
"M v[88:91],v[204:207],a[216:219],v[88:91]\ns_waitcnt vmcnt(7) lgkmcnt(4)\ns_barrier\nM v[92:95],v[204:207],a[220:223],v[92:95]\nds_read_b64_tr_b16 v[192:193],v241 offset:49152\nds_read_b64_tr_b16 v[194:195],v241 offset:49408\nM v[32:35],v[208:211],a[208:211],v[32:35]\nds_read_b64_tr_b16 v[196:197],v242 offset:49152\n" \
"ds_read_b64_tr_b16 v[198:199],v242 offset:49408\nM v[36:39],v[208:211],a[212:215],v[36:39]\nds_read_b64_tr_b16 v[200:201],v241 offset:49664\nds_read_b64_tr_b16 v[202:203],v241 offset:49920\nM v[96:99],v[208:211],a[216:219],v[96:99]\nds_read_b64_tr_b16 v[204:205],v242 offset:49664\nds_read_b64_tr_b16 v[206:207],v242 offset:49920\nM v[100:103],v[208:211],a[220:223],v[100:103]\n" \
"ds_read_b64_tr_b16 v[208:209],v241 offset:50176\nds_read_b64_tr_b16 v[210:211],v241 offset:50432\nM v[40:43],v[212:215],a[208:211],v[40:43]\nds_write_b64 v250,v[170:171] offset:10248\nM v[44:47],v[212:215],a[212:215],v[44:47]\nds_write_b64 v250,v[172:173] offset:11264\nM v[104:107],v[212:215],a[216:219],v[104:107]\nds_write_b64 v250,v[174:175] offset:11272\n" \
"M v[108:111],v[212:215],a[220:223],v[108:111]\nds_read_b64_tr_b16 v[212:213],v242 offset:50176\nds_read_b64_tr_b16 v[214:215],v242 offset:50432\nM v[48:51],v[216:219],a[208:211],v[48:51]\nbuffer_store_dwordx4 v[184:187],v240,s[52:55],s74 offen offset:2048 sc1\nM v[52:55],v[216:219],a[212:215],v[52:55]\nds_write_b64 v250,v[176:177] offset:12288\nM v[112:115],v[216:219],a[216:219],v[112:115]\n" \
"M v[116:119],v[216:219],a[220:223],v[116:119]\nds_read_b64_tr_b16 v[216:217],v241 offset:50688\nds_read_b64_tr_b16 v[218:219],v241 offset:50944\nM v[56:59],v[220:223],a[208:211],v[56:59]\nds_write_b64 v250,v[178:179] offset:12296\nv_accvgpr_write_b32 a208,v229\nM v[60:63],v[220:223],a[212:215],v[60:63]\nv_accvgpr_write_b32 a209,v229\n" \
"v_accvgpr_write_b32 a210,v229\nM v[120:123],v[220:223],a[216:219],v[120:123]\nds_write_b64 v250,v[180:181] offset:13312\nv_accvgpr_write_b32 a211,v229\nM v[124:127],v[220:223],a[220:223],v[124:127]\nds_read_b64_tr_b16 v[220:221],v242 offset:50688\ns_waitcnt lgkmcnt(11)\nM v[0:3],v[192:195],a[224:227],v[0:3]\n" \
"ds_read_b64_tr_b16 v[222:223],v242 offset:50944\nM v[4:7],v[192:195],a[228:231],v[4:7]\nds_write_b64 v250,v[182:183] offset:13320\nM v[64:67],v[192:195],a[232:235],v[64:67]\nM v[68:71],v[192:195],a[236:239],v[68:71]\nds_read_b64_tr_b16 v[192:193],v241 offset:57344\nds_read_b64_tr_b16 v[194:195],v241 offset:57600\nM v[8:11],v[196:199],a[224:227],v[8:11]\n" \
"ds_write_b64 v250,v[184:185] offset:14336\nM v[12:15],v[196:199],a[228:231],v[12:15]\nM v[72:75],v[196:199],a[232:235],v[72:75]\nM v[76:79],v[196:199],a[236:239],v[76:79]\nds_read_b64_tr_b16 v[196:197],v242 offset:57344\nds_read_b64_tr_b16 v[198:199],v242 offset:57600\nM v[16:19],v[200:203],a[224:227],v[16:19]\nds_write_b64 v250,v[186:187] offset:14344\n" \
"M v[20:23],v[200:203],a[228:231],v[20:23]\nM v[80:83],v[200:203],a[232:235],v[80:83]\nds_write_b64 v250,v[188:189] offset:15360\nM v[84:87],v[200:203],a[236:239],v[84:87]\nds_read_b64_tr_b16 v[200:201],v241 offset:57856\nds_read_b64_tr_b16 v[202:203],v241 offset:58112\nM v[24:27],v[204:207],a[224:227],v[24:27]\nM v[28:31],v[204:207],a[228:231],v[28:31]\n" \
"ds_write_b64 v250,v[190:191] offset:15368\nv_add_u32 v250,114688,v240\nM v[88:91],v[204:207],a[232:235],v[88:91]\nM v[92:95],v[204:207],a[236:239],v[92:95]\nds_read_b64_tr_b16 v[204:205],v242 offset:57856\nds_read_b64_tr_b16 v[206:207],v242 offset:58112\nM v[32:35],v[208:211],a[224:227],v[32:35]\nM v[36:39],v[208:211],a[228:231],v[36:39]\n" \
"M v[96:99],v[208:211],a[232:235],v[96:99]\nM v[100:103],v[208:211],a[236:239],v[100:103]\nds_read_b64_tr_b16 v[208:209],v241 offset:58368\ns_waitcnt lgkmcnt(10)\nM v[40:43],v[212:215],a[224:227],v[40:43]\nds_read_b64_tr_b16 v[210:211],v241 offset:58624\nM v[44:47],v[212:215],a[228:231],v[44:47]\nM v[104:107],v[212:215],a[232:235],v[104:107]\n" \
"buffer_store_dwordx4 v[188:191],v240,s[52:55],s74 offen offset:3072 sc1\nM v[108:111],v[212:215],a[236:239],v[108:111]\nds_read_b64_tr_b16 v[212:213],v242 offset:58368\nds_read_b64_tr_b16 v[214:215],v242 offset:58624\nM v[48:51],v[216:219],a[224:227],v[48:51]\nM v[52:55],v[216:219],a[228:231],v[52:55]\nM v[112:115],v[216:219],a[232:235],v[112:115]\nM v[116:119],v[216:219],a[236:239],v[116:119]\n" \
"ds_read_b64_tr_b16 v[216:217],v241 offset:58880\nds_read_b64_tr_b16 v[218:219],v241 offset:59136\nM v[56:59],v[220:223],a[224:227],v[56:59]\nv_add_u32 v241,65536,v240\nM v[60:63],v[220:223],a[228:231],v[60:63]\nM v[120:123],v[220:223],a[232:235],v[120:123]\nM v[124:127],v[220:223],a[236:239],v[124:127]\nds_read_b64_tr_b16 v[220:221],v242 offset:58880\n" \
"ds_read_b64_tr_b16 v[222:223],v242 offset:59136\nM v[0:3],v[192:195],a[240:243],v[0:3]\nv_lshl_or_b32 v242,v231,6,v230\ns_waitcnt lgkmcnt(8)\nM v[8:11],v[196:199],a[240:243],v[8:11]\nM v[16:19],v[200:203],a[240:243],v[16:19]\nM v[24:27],v[204:207],a[240:243],v[24:27]\ns_waitcnt lgkmcnt(6)\n" \
"M v[32:35],v[208:211],a[240:243],v[32:35]\ns_waitcnt lgkmcnt(4)\nM v[40:43],v[212:215],a[240:243],v[40:43]\nv_cvt_pk_bf16_f32 v0,v0,v1\ns_waitcnt lgkmcnt(2)\nM v[48:51],v[216:219],a[240:243],v[48:51]\nv_cvt_pk_bf16_f32 v16,v16,v17\ns_waitcnt lgkmcnt(0)\n" \
"M v[56:59],v[220:223],a[240:243],v[56:59]\nv_cvt_pk_bf16_f32 v32,v32,v33\ns_waitcnt vmcnt(5)\nM v[4:7],v[192:195],a[244:247],v[4:7]\nv_cvt_pk_bf16_f32 v1,v2,v3\nv_cvt_pk_bf16_f32 v17,v18,v19\nM v[12:15],v[196:199],a[244:247],v[12:15]\nv_cvt_pk_bf16_f32 v48,v48,v49\n" \
"v_cvt_pk_bf16_f32 v33,v34,v35\nM v[20:23],v[200:203],a[244:247],v[20:23]\nv_cvt_pk_bf16_f32 v49,v50,v51\nv_cvt_pk_bf16_f32 v2,v8,v9\nM v[28:31],v[204:207],a[244:247],v[28:31]\nv_cvt_pk_bf16_f32 v4,v4,v5\nv_cvt_pk_bf16_f32 v5,v6,v7\nM v[36:39],v[208:211],a[244:247],v[36:39]\n" \
"v_cvt_pk_bf16_f32 v3,v10,v11\nv_cvt_pk_bf16_f32 v18,v24,v25\nM v[44:47],v[212:215],a[244:247],v[44:47]\nv_cvt_pk_bf16_f32 v20,v20,v21\nv_cvt_pk_bf16_f32 v21,v22,v23\nM v[52:55],v[216:219],a[244:247],v[52:55]\nv_cvt_pk_bf16_f32 v19,v26,v27\nv_cvt_pk_bf16_f32 v34,v40,v41\n" \
"M v[60:63],v[220:223],a[244:247],v[60:63]\nv_cvt_pk_bf16_f32 v36,v36,v37\nv_cvt_pk_bf16_f32 v37,v38,v39\nM v[64:67],v[192:195],a[248:251],v[64:67]\nv_cvt_pk_bf16_f32 v35,v42,v43\nv_cvt_pk_bf16_f32 v50,v56,v57\nM v[72:75],v[196:199],a[248:251],v[72:75]\nv_cvt_pk_bf16_f32 v52,v52,v53\n" \
"v_cvt_pk_bf16_f32 v53,v54,v55\nM v[80:83],v[200:203],a[248:251],v[80:83]\nv_cvt_pk_bf16_f32 v51,v58,v59\nv_cvt_pk_bf16_f32 v6,v12,v13\nM v[88:91],v[204:207],a[248:251],v[88:91]\nv_cvt_pk_bf16_f32 v64,v64,v65\nv_cvt_pk_bf16_f32 v65,v66,v67\nM v[96:99],v[208:211],a[248:251],v[96:99]\n" \
"v_cvt_pk_bf16_f32 v7,v14,v15\nv_cvt_pk_bf16_f32 v22,v28,v29\nM v[104:107],v[212:215],a[248:251],v[104:107]\nv_cvt_pk_bf16_f32 v80,v80,v81\nv_cvt_pk_bf16_f32 v81,v82,v83\nM v[112:115],v[216:219],a[248:251],v[112:115]\nv_cvt_pk_bf16_f32 v23,v30,v31\nv_cvt_pk_bf16_f32 v38,v44,v45\n" \
"M v[120:123],v[220:223],a[248:251],v[120:123]\nv_cvt_pk_bf16_f32 v96,v96,v97\nv_cvt_pk_bf16_f32 v97,v98,v99\nM v[68:71],v[192:195],a[252:255],v[68:71]\nv_cvt_pk_bf16_f32 v39,v46,v47\nv_cvt_pk_bf16_f32 v54,v60,v61\nM v[76:79],v[196:199],a[252:255],v[76:79]\nv_cvt_pk_bf16_f32 v112,v112,v113\n" \
"v_cvt_pk_bf16_f32 v113,v114,v115\nM v[84:87],v[200:203],a[252:255],v[84:87]\nv_cvt_pk_bf16_f32 v55,v62,v63\nv_cvt_pk_bf16_f32 v66,v72,v73\nM v[92:95],v[204:207],a[252:255],v[92:95]\nv_cvt_pk_bf16_f32 v68,v68,v69\nv_cvt_pk_bf16_f32 v69,v70,v71\nM v[100:103],v[208:211],a[252:255],v[100:103]\n" \
"v_cvt_pk_bf16_f32 v67,v74,v75\nv_cvt_pk_bf16_f32 v82,v88,v89\nM v[108:111],v[212:215],a[252:255],v[108:111]\nv_cvt_pk_bf16_f32 v84,v84,v85\nv_cvt_pk_bf16_f32 v85,v86,v87\nM v[116:119],v[216:219],a[252:255],v[116:119]\nv_cvt_pk_bf16_f32 v83,v90,v91\nv_cvt_pk_bf16_f32 v98,v104,v105\n" \
"v_cvt_pk_bf16_f32 v100,v100,v101\nv_cvt_pk_bf16_f32 v101,v102,v103\nM v[124:127],v[220:223],a[252:255],v[124:127]\nv_cvt_pk_bf16_f32 v99,v106,v107\nv_cvt_pk_bf16_f32 v114,v120,v121\nv_cvt_pk_bf16_f32 v115,v122,v123\nv_cvt_pk_bf16_f32 v116,v116,v117\nv_cvt_pk_bf16_f32 v117,v118,v119\n" \
"v_cvt_pk_bf16_f32 v70,v76,v77\nv_cvt_pk_bf16_f32 v71,v78,v79\nv_cvt_pk_bf16_f32 v86,v92,v93\nv_cvt_pk_bf16_f32 v87,v94,v95\nv_cvt_pk_bf16_f32 v102,v108,v109\nv_cvt_pk_bf16_f32 v103,v110,v111\nv_cvt_pk_bf16_f32 v118,v124,v125\nv_cvt_pk_bf16_f32 v119,v126,v127\n" \
"s_waitcnt vmcnt(0)\nbuffer_store_dword v252,off,s[52:55],s29 sc1\nv_accvgpr_write_b32 a244,0\nv_accvgpr_write_b32 a248,-1\nv_accvgpr_write_b32 a245,0\nv_accvgpr_write_b32 a249,-1\nv_accvgpr_write_b32 a246,0\nv_accvgpr_write_b32 a250,-1\n" \
"v_accvgpr_write_b32 a247,0\nv_accvgpr_write_b32 a251,-1\nv_accvgpr_write_b32 a128,v0\nv_accvgpr_write_b32 a129,v1\nv_accvgpr_write_b32 a130,v2\nv_accvgpr_write_b32 a131,v3\nv_accvgpr_write_b32 a132,v16\nv_accvgpr_write_b32 a133,v17\n" \
"v_accvgpr_write_b32 a134,v18\nv_accvgpr_write_b32 a135,v19\nv_accvgpr_write_b32 a136,v32\nv_accvgpr_write_b32 a137,v33\nv_accvgpr_write_b32 a138,v34\nv_accvgpr_write_b32 a139,v35\nv_accvgpr_write_b32 a140,v48\nv_accvgpr_write_b32 a141,v49\n" \
"v_accvgpr_write_b32 a142,v50\nv_accvgpr_write_b32 a143,v51\nv_accvgpr_write_b32 a144,v4\nv_accvgpr_write_b32 a145,v5\nv_accvgpr_write_b32 a146,v6\nv_accvgpr_write_b32 a147,v7\nv_accvgpr_write_b32 a148,v20\nv_accvgpr_write_b32 a149,v21\n" \
"v_accvgpr_write_b32 a150,v22\nv_accvgpr_write_b32 a151,v23\nv_accvgpr_write_b32 a152,v36\nv_accvgpr_write_b32 a153,v37\nv_accvgpr_write_b32 a154,v38\nv_accvgpr_write_b32 a155,v39\nv_accvgpr_write_b32 a156,v52\nv_accvgpr_write_b32 a157,v53\n" \
"v_accvgpr_write_b32 a158,v54\nv_accvgpr_write_b32 a159,v55\nv_accvgpr_write_b32 a160,v64\nv_accvgpr_write_b32 a161,v65\nv_accvgpr_write_b32 a162,v66\nv_accvgpr_write_b32 a163,v67\nv_accvgpr_write_b32 a164,v80\nv_accvgpr_write_b32 a165,v81\n" \
"v_accvgpr_write_b32 a166,v82\nv_accvgpr_write_b32 a167,v83\nv_accvgpr_write_b32 a168,v96\nv_accvgpr_write_b32 a169,v97\nv_accvgpr_write_b32 a170,v98\nv_accvgpr_write_b32 a171,v99\nv_accvgpr_write_b32 a172,v112\nv_accvgpr_write_b32 a173,v113\n" \
"v_accvgpr_write_b32 a174,v114\nv_accvgpr_write_b32 a175,v115\nv_accvgpr_write_b32 a176,v68\nv_accvgpr_write_b32 a177,v69\nv_accvgpr_write_b32 a178,v70\nv_accvgpr_write_b32 a179,v71\nv_accvgpr_write_b32 a180,v84\nv_accvgpr_write_b32 a181,v85\n" \
"v_accvgpr_write_b32 a182,v86\nv_accvgpr_write_b32 a183,v87\nv_accvgpr_write_b32 a184,v100\nv_accvgpr_write_b32 a185,v101\nv_accvgpr_write_b32 a186,v102\nv_accvgpr_write_b32 a187,v103\nv_accvgpr_write_b32 a188,v116\nv_accvgpr_write_b32 a189,v117\n" \
"v_accvgpr_write_b32 a190,v118\nv_accvgpr_write_b32 a191,v119\ns_waitcnt lgkmcnt(0)\n.Lattn:\ns_cmp_lt_u32 s7,2\ns_cbranch_scc0 .Lp23\ns_cmp_eq_u32 s7,0\ns_cbranch_scc0 .Lpro1\n" \
".Lpro0:\ns_barrier\nM v[64:67],a[0:3],a[128:131],0\nds_read_b128 a[32:35],v240 offset:40960\nds_read_b128 a[36:39],v240 offset:41984\nM v[64:67],a[4:7],a[132:135],v[64:67]\nds_read_b128 a[40:43],v240 offset:43008\ns_mov_b32 m0,s64\n" \
"v_mov_b32 v218,0\nM v[64:67],a[8:11],a[136:139],v[64:67]\nds_read_b128 a[44:47],v240 offset:44032\nv_mov_b32 v219,0\nM v[64:67],a[12:15],a[140:143],v[64:67]\nds_read_b128 a[48:51],v240 offset:45056\nbuffer_load_dwordx4 v240,s[44:47],s36 offen offset:0 sc1 lds\nM v[68:71],a[0:3],a[144:147],0\n" \
"ds_read_b128 a[0:3],v240 offset:32768\nM v[68:71],a[4:7],a[148:151],v[68:71]\nds_read_b128 a[4:7],v240 offset:33792\nM v[68:71],a[8:11],a[152:155],v[68:71]\nds_read_b128 a[8:11],v240 offset:34816\nv_max3_f32 v225,v64,v65,v66\nM v[68:71],a[12:15],a[156:159],v[68:71]\nds_read_b128 a[12:15],v240 offset:35840\n" \
"v_max_f32 v225,v225,v67\nv_cndmask_b32 v225,v224,v225,s[90:91]\nM v[76:79],a[16:19],a[144:147],0\nds_read_b128 a[16:19],v240 offset:36864\nv_cndmask_b32 v64,v64,v252,s[82:83]\nM v[76:79],a[20:23],a[148:151],v[76:79]\nds_read_b128 a[20:23],v240 offset:37888\nv_max3_f32 v229,v68,v69,v70\n" \
"v_cndmask_b32 v65,v65,v252,s[84:85]\nM v[76:79],a[24:27],a[152:155],v[76:79]\nds_read_b128 a[24:27],v240 offset:38912\nv_cndmask_b32 v66,v66,v252,s[86:87]\nM v[76:79],a[28:31],a[156:159],v[76:79]\nds_read_b128 a[28:31],v240 offset:39936\nv_cndmask_b32 v67,v67,v252,s[88:89]\ns_waitcnt lgkmcnt(7)\n" \
"M v[136:139],a[0:3],a[160:163],0\nds_read_b128 a[52:55],v240 offset:46080\ns_waitcnt lgkmcnt(7)\nM v[136:139],a[4:7],a[164:167],v[136:139]\nbuffer_load_dwordx4 v240,s[44:47],s36 offen offset:1024 sc1 lds\ns_waitcnt lgkmcnt(6)\nM v[136:139],a[8:11],a[168:171],v[136:139]\nv_max3_f32 v229,v229,v71,v76\n" \
"v_max3_f32 v229,v229,v77,v78\ns_waitcnt lgkmcnt(5)\nM v[136:139],a[12:15],a[172:175],v[136:139]\nv_max_f32 v229,v229,v79\nv_cndmask_b32 v229,v228,v229,s[90:91]\nM v[140:143],a[0:3],a[176:179],0\nds_read_b128 a[56:59],v240 offset:47104\nv_permlane16_swap_b32 v225,v229\n" \
"v_max_f32 v225,v225,v229\nM v[140:143],a[4:7],a[180:183],v[140:143]\nv_mov_b32 v229,v225\nds_read_b128 a[60:63],v240 offset:48128\nM v[140:143],a[8:11],a[184:187],v[140:143]\nv_permlane32_swap_b32 v225,v229\nv_max_f32 v225,v225,v229\nv_mov_b32 v229,v225\n" \
"M v[140:143],a[12:15],a[188:191],v[140:143]\nv_max3_f32 v233,v136,v137,v138\ns_waitcnt lgkmcnt(4)\nM v[152:155],a[16:19],a[160:163],0\nv_permlane16_swap_b32 v225,v229\nv_sub_f32 v224,0,v225\nv_sub_f32 v226,0,v225\nM v[152:155],a[20:23],a[164:167],v[152:155]\n" \
"v_sub_f32 v227,0,v225\nv_sub_f32 v225,0,v225\nM v[152:155],a[24:27],a[168:171],v[152:155]\nv_sub_f32 v228,0,v229\nv_sub_f32 v230,0,v229\ns_waitcnt lgkmcnt(2)\nM v[152:155],a[28:31],a[172:175],v[152:155]\nv_sub_f32 v231,0,v229\n" \
"v_sub_f32 v229,0,v229\nM v[156:159],a[16:19],a[176:179],0\nv_max3_f32 v237,v140,v141,v142\nds_read_b128 a[64:67],v241 offset:0\nds_read_b128 a[68:71],v241 offset:1024\nM v[156:159],a[20:23],a[180:183],v[156:159]\nds_read_b128 a[72:75],v241 offset:2048\nv_max3_f32 v233,v233,v139,v152\n" \
"M v[156:159],a[24:27],a[184:187],v[156:159]\nv_max3_f32 v233,v233,v153,v154\nv_max_f32 v233,v233,v155\nv_cndmask_b32 v233,v232,v233,s[90:91]\nM v[156:159],a[28:31],a[188:191],v[156:159]\nds_read_b128 a[76:79],v241 offset:3072\nv_add_f32 v64,v64,v224\nv_add_f32 v65,v65,v224\n" \
"M v[128:131],a[0:3],a[128:131],v[224:227]\nv_add_f32 v66,v66,v224\nv_add_f32 v67,v67,v224\nM v[128:131],a[4:7],a[132:135],v[128:131]\nv_exp_f32 v64,v64\nv_max3_f32 v237,v237,v143,v156\nM v[128:131],a[8:11],a[136:139],v[128:131]\nv_max3_f32 v237,v237,v157,v158\n" \
"v_max_f32 v237,v237,v159\nM v[128:131],a[12:15],a[140:143],v[128:131]\nv_cndmask_b32 v237,v236,v237,s[90:91]\nv_exp_f32 v65,v65\nM v[132:135],a[0:3],a[144:147],v[228:231]\nv_permlane16_swap_b32 v233,v237\nv_max_f32 v233,v233,v237\nM v[132:135],a[4:7],a[148:151],v[132:135]\n" \
"v_mov_b32 v237,v233\nv_exp_f32 v66,v66\nM v[132:135],a[8:11],a[152:155],v[132:135]\nv_permlane32_swap_b32 v233,v237\nv_max_f32 v233,v233,v237\nM v[132:135],a[12:15],a[156:159],v[132:135]\nv_mov_b32 v237,v233\nv_exp_f32 v67,v67\n" \
"M v[144:147],a[16:19],a[128:131],v[224:227]\nv_permlane16_swap_b32 v233,v237\nv_sub_f32 v232,0,v233\nM v[144:147],a[20:23],a[132:135],v[144:147]\nv_sub_f32 v234,0,v233\nv_sub_f32 v235,0,v233\nv_sub_f32 v233,0,v233\nM v[144:147],a[24:27],a[136:139],v[144:147]\n" \
"v_sub_f32 v236,0,v237\nv_sub_f32 v238,0,v237\nM v[144:147],a[28:31],a[140:143],v[144:147]\nv_sub_f32 v239,0,v237\nv_sub_f32 v237,0,v237\nv_add_f32 v68,v68,v228\nM v[148:151],a[16:19],a[144:147],v[228:231]\nv_add_f32 v69,v69,v228\n" \
"v_cndmask_b32 v76,v76,v252,s[82:83]\nv_cndmask_b32 v77,v77,v252,s[84:85]\nM v[148:151],a[20:23],a[148:151],v[148:151]\nv_cndmask_b32 v78,v78,v252,s[86:87]\nv_cndmask_b32 v79,v79,v252,s[88:89]\nM v[148:151],a[24:27],a[152:155],v[148:151]\nv_add_f32 v70,v70,v228\nv_add_f32 v71,v71,v228\n" \
"v_cvt_pk_bf16_f32 v64,v64,v65\nM v[148:151],a[28:31],a[156:159],v[148:151]\nv_exp_f32 v68,v68\nM v[160:163],a[32:35],a[128:131],v[224:227]\nv_exp_f32 v69,v69\nds_read_b128 a[80:83],v241 offset:4096\nM v[160:163],a[36:39],a[132:135],v[160:163]\nv_exp_f32 v70,v70\n" \
"M v[160:163],a[40:43],a[136:139],v[160:163]\nv_exp_f32 v71,v71\nv_add_f32 v76,v76,v228\nM v[160:163],a[44:47],a[140:143],v[160:163]\nv_add_f32 v77,v77,v228\nv_add_f32 v78,v78,v228\nM v[164:167],a[32:35],a[144:147],v[228:231]\nv_add_f32 v79,v79,v228\n" \
"v_cvt_pk_bf16_f32 v65,v66,v67\nv_mov_b32 v66,0\nM v[164:167],a[36:39],a[148:151],v[164:167]\nv_exp_f32 v76,v76\nM v[164:167],a[40:43],a[152:155],v[164:167]\nv_exp_f32 v77,v77\nv_mov_b32 v67,0\nM v[164:167],a[44:47],a[156:159],v[164:167]\n" \
"v_exp_f32 v78,v78\nM v[176:179],a[48:51],a[128:131],v[224:227]\nv_exp_f32 v79,v79\nv_cvt_pk_bf16_f32 v68,v68,v69\nM v[176:179],a[52:55],a[132:135],v[176:179]\nv_cvt_pk_bf16_f32 v69,v70,v71\nv_cvt_pk_bf16_f32 v70,v76,v77\ns_waitcnt lgkmcnt(0)\n" \
"M v[176:179],a[56:59],a[136:139],v[176:179]\nv_cvt_pk_bf16_f32 v71,v78,v79\nds_read_b128 a[84:87],v241 offset:5120\nM v[176:179],a[60:63],a[140:143],v[176:179]\nbuffer_load_dwordx4 v240,s[44:47],s36 offen offset:2048 sc1 lds\nds_read_b128 a[88:91],v241 offset:6144\nM v[180:183],a[48:51],a[144:147],v[228:231]\nds_read_b128 a[92:95],v241 offset:7168\n" \
"ds_read_b128 a[0:3],v240 offset:49152\nM v[180:183],a[52:55],a[148:151],v[180:183]\nds_read_b128 a[4:7],v240 offset:50176\nv_exp_f32 v128,v128\nM v[180:183],a[56:59],a[152:155],v[180:183]\nds_read_b128 a[8:11],v240 offset:51200\nv_add_f32 v136,v136,v232\nM v[180:183],a[60:63],a[156:159],v[180:183]\n" \
"buffer_load_dwordx4 v240,s[44:47],s36 offen offset:3072 sc1 lds\nds_read_b128 a[12:15],v240 offset:52224\nM v[168:171],a[32:35],a[160:163],v[232:235]\nds_read_b128 a[16:19],v240 offset:53248\nv_add_f32 v137,v137,v232\nM v[168:171],a[36:39],a[164:167],v[168:171]\nv_exp_f32 v129,v129\nds_read_b128 a[20:23],v240 offset:54272\n" \
"M v[168:171],a[40:43],a[168:171],v[168:171]\nv_exp_f32 v130,v130\nM v[168:171],a[44:47],a[172:175],v[168:171]\nv_exp_f32 v131,v131\nv_cvt_pk_bf16_f32 v192,v128,v129\nM v[172:175],a[32:35],a[176:179],v[236:239]\nv_cvt_pk_bf16_f32 v193,v130,v131\nds_read_b128 a[24:27],v240 offset:55296\n" \
"M v[172:175],a[36:39],a[180:183],v[172:175]\nds_read_b128 a[28:31],v240 offset:56320\nv_exp_f32 v132,v132\nM v[172:175],a[40:43],a[184:187],v[172:175]\nv_exp_f32 v133,v133\nv_add_f32 v138,v138,v232\nM v[172:175],a[44:47],a[188:191],v[172:175]\nv_exp_f32 v134,v134\n" \
"M v[188:191],a[48:51],a[176:179],v[236:239]\nv_exp_f32 v135,v135\nv_cvt_pk_bf16_f32 v196,v132,v133\nM v[188:191],a[52:55],a[180:183],v[188:191]\nv_cvt_pk_bf16_f32 v197,v134,v135\nv_add_f32 v139,v139,v232\nM v[188:191],a[56:59],a[184:187],v[188:191]\nv_exp_f32 v144,v144\n" \
"v_add_f32 v140,v140,v236\nM v[188:191],a[60:63],a[188:191],v[188:191]\nv_exp_f32 v145,v145\nM v[0:3],a[64:67],v[64:67],0\nv_exp_f32 v146,v146\nv_cvt_pk_bf16_f32 v194,v144,v145\nM v[4:7],a[64:67],v[68:71],0\nv_exp_f32 v147,v147\n" \
"M v[8:11],a[68:71],v[64:67],0\nbuffer_load_dword v248,v251,s[52:55],s80 offen sc1\nv_cvt_pk_bf16_f32 v195,v146,v147\nM v[12:15],a[68:71],v[68:71],0\nv_exp_f32 v148,v148\nM v[16:19],a[72:75],v[64:67],0\nv_exp_f32 v149,v149\nv_add_f32 v141,v141,v236\n" \
"M v[20:23],a[72:75],v[68:71],0\nv_exp_f32 v150,v150\nM v[24:27],a[76:79],v[64:67],0\nv_exp_f32 v151,v151\nv_cvt_pk_bf16_f32 v198,v148,v149\nM v[28:31],a[76:79],v[68:71],0\nv_cvt_pk_bf16_f32 v199,v150,v151\nv_exp_f32 v136,v136\n" \
"M v[32:35],a[80:83],v[64:67],0\nv_exp_f32 v137,v137\nM v[36:39],a[80:83],v[68:71],0\nv_exp_f32 v138,v138\ns_waitcnt lgkmcnt(0)\nM v[40:43],a[84:87],v[64:67],0\nv_exp_f32 v139,v139\nM v[44:47],a[84:87],v[68:71],0\n" \
"v_cvt_pk_bf16_f32 v200,v136,v137\nv_cvt_pk_bf16_f32 v201,v138,v139\nv_add_f32 v142,v142,v236\nM v[48:51],a[88:91],v[64:67],0\nv_add_f32 v143,v143,v236\nds_read_b128 a[32:35],v240 offset:57344\nM v[52:55],a[88:91],v[68:71],0\nv_exp_f32 v140,v140\n" \
"v_add_f32 v152,v152,v232\nM v[56:59],a[92:95],v[64:67],0\nv_exp_f32 v141,v141\nM v[60:63],a[92:95],v[68:71],0\nv_exp_f32 v142,v142\nv_cvt_pk_bf16_f32 v204,v140,v141\nM a[192:195],a[208:211],v[64:67],0\nv_exp_f32 v143,v143\n" \
"M a[196:199],a[208:211],v[68:71],0\nv_cvt_pk_bf16_f32 v205,v142,v143\nv_add_f32 v153,v153,v232\nv_add_f32 v154,v154,v232\nM v[128:131],a[0:3],a[128:131],v[224:227]\nv_add_f32 v155,v155,v232\nds_read_b128 a[36:39],v240 offset:58368\nM v[128:131],a[4:7],a[132:135],v[128:131]\n" \
"v_exp_f32 v152,v152\nds_read_b128 a[40:43],v240 offset:59392\nM v[128:131],a[8:11],a[136:139],v[128:131]\nv_exp_f32 v153,v153\nM v[128:131],a[12:15],a[140:143],v[128:131]\nv_exp_f32 v154,v154\nv_cvt_pk_bf16_f32 v202,v152,v153\nM v[132:135],a[0:3],a[144:147],v[228:231]\n" \
"v_exp_f32 v155,v155\nds_read_b128 a[44:47],v240 offset:60416\nM v[132:135],a[4:7],a[148:151],v[132:135]\nv_cvt_pk_bf16_f32 v203,v154,v155\nv_add_f32 v156,v156,v236\nM v[132:135],a[8:11],a[152:155],v[132:135]\nv_add_f32 v157,v157,v236\nv_add_f32 v158,v158,v236\n" \
"v_add_f32 v159,v159,v236\nM v[132:135],a[12:15],a[156:159],v[132:135]\nv_exp_f32 v156,v156\nM v[144:147],a[16:19],a[128:131],v[224:227]\nv_exp_f32 v157,v157\nds_read_b128 a[48:51],v240 offset:61440\nM v[144:147],a[20:23],a[132:135],v[144:147]\nv_exp_f32 v158,v158\n" \
"M v[144:147],a[24:27],a[136:139],v[144:147]\nv_exp_f32 v159,v159\nv_cvt_pk_bf16_f32 v206,v156,v157\nM v[144:147],a[28:31],a[140:143],v[144:147]\nv_cvt_pk_bf16_f32 v207,v158,v159\nds_read_b128 a[52:55],v240 offset:62464\nM v[148:151],a[16:19],a[144:147],v[228:231]\nv_exp_f32 v160,v160\n" \
"ds_read_b128 a[56:59],v240 offset:63488\nM v[148:151],a[20:23],a[148:151],v[148:151]\nv_exp_f32 v161,v161\nM v[148:151],a[24:27],a[152:155],v[148:151]\nv_exp_f32 v162,v162\nv_cvt_pk_bf16_f32 v208,v160,v161\nM v[148:151],a[28:31],a[156:159],v[148:151]\nv_exp_f32 v163,v163\n" \
"M v[136:139],a[0:3],a[160:163],v[232:235]\nv_cvt_pk_bf16_f32 v209,v162,v163\nds_read_b128 a[60:63],v240 offset:64512\nv_cndmask_b32 v168,v168,v252,s[82:83]\nM v[136:139],a[4:7],a[164:167],v[136:139]\nv_exp_f32 v164,v164\nv_cndmask_b32 v169,v169,v252,s[84:85]\nM v[136:139],a[8:11],a[168:171],v[136:139]\n" \
"v_exp_f32 v165,v165\nM v[136:139],a[12:15],a[172:175],v[136:139]\nv_exp_f32 v166,v166\nv_cvt_pk_bf16_f32 v212,v164,v165\nM v[140:143],a[0:3],a[176:179],v[236:239]\nv_exp_f32 v167,v167\nM v[140:143],a[4:7],a[180:183],v[140:143]\nv_cvt_pk_bf16_f32 v213,v166,v167\n" \
"v_exp_f32 v176,v176\nM v[140:143],a[8:11],a[184:187],v[140:143]\nv_exp_f32 v177,v177\nM v[140:143],a[12:15],a[188:191],v[140:143]\nv_exp_f32 v178,v178\nv_cvt_pk_bf16_f32 v210,v176,v177\nM v[152:155],a[16:19],a[160:163],v[232:235]\nv_exp_f32 v179,v179\n" \
"M v[152:155],a[20:23],a[164:167],v[152:155]\nv_cvt_pk_bf16_f32 v211,v178,v179\nv_exp_f32 v180,v180\nM v[152:155],a[24:27],a[168:171],v[152:155]\nv_exp_f32 v181,v181\nM v[152:155],a[28:31],a[172:175],v[152:155]\nv_exp_f32 v182,v182\nv_cvt_pk_bf16_f32 v214,v180,v181\n" \
"M v[156:159],a[16:19],a[176:179],v[236:239]\nv_exp_f32 v183,v183\nM v[156:159],a[20:23],a[180:183],v[156:159]\nv_cvt_pk_bf16_f32 v215,v182,v183\nv_cndmask_b32 v170,v170,v252,s[86:87]\nv_cndmask_b32 v171,v171,v252,s[88:89]\nM v[156:159],a[24:27],a[184:187],v[156:159]\nv_exp_f32 v168,v168\n" \
"ds_read_b128 a[64:67],v241 offset:32768\nM v[156:159],a[28:31],a[188:191],v[156:159]\nds_read_b128 a[68:71],v241 offset:33792\ns_waitcnt lgkmcnt(2)\nM v[160:163],a[32:35],a[128:131],v[224:227]\nv_exp_f32 v169,v169\nv_cndmask_b32 v188,v188,v252,s[82:83]\nM v[160:163],a[36:39],a[132:135],v[160:163]\n" \
"v_exp_f32 v170,v170\nM v[160:163],a[40:43],a[136:139],v[160:163]\ns_waitcnt vmcnt(1)\ns_barrier\nv_cvt_pk_bf16_f32 v216,v168,v169\nM v[160:163],a[44:47],a[140:143],v[160:163]\nv_exp_f32 v171,v171\nM v[164:167],a[32:35],a[144:147],v[228:231]\n" \
"v_cvt_pk_bf16_f32 v217,v170,v171\nv_exp_f32 v172,v172\nM v[164:167],a[36:39],a[148:151],v[164:167]\nv_exp_f32 v173,v173\nM v[164:167],a[40:43],a[152:155],v[164:167]\nv_exp_f32 v174,v174\nv_cvt_pk_bf16_f32 v220,v172,v173\nM v[164:167],a[44:47],a[156:159],v[164:167]\n" \
"v_exp_f32 v175,v175\nM v[176:179],a[48:51],a[128:131],v[224:227]\nv_cvt_pk_bf16_f32 v221,v174,v175\nv_cndmask_b32 v189,v189,v252,s[84:85]\nv_cndmask_b32 v190,v190,v252,s[86:87]\nM v[176:179],a[52:55],a[132:135],v[176:179]\nv_cndmask_b32 v191,v191,v252,s[88:89]\nds_read_b128 a[72:75],v241 offset:34816\n" \
"M v[176:179],a[56:59],a[136:139],v[176:179]\nv_exp_f32 v188,v188\nds_read_b128 a[76:79],v241 offset:35840\nM v[176:179],a[60:63],a[140:143],v[176:179]\nv_exp_f32 v189,v189\nds_read_b128 a[80:83],v241 offset:36864\nM v[180:183],a[48:51],a[144:147],v[228:231]\nv_exp_f32 v190,v190\n" \
"M v[180:183],a[52:55],a[148:151],v[180:183]\nv_exp_f32 v191,v191\nv_cvt_pk_bf16_f32 v222,v188,v189\nM v[180:183],a[56:59],a[152:155],v[180:183]\nv_cvt_pk_bf16_f32 v223,v190,v191\nds_read_b128 a[84:87],v241 offset:37888\nM v[180:183],a[60:63],a[156:159],v[180:183]\nds_read_b128 a[88:91],v241 offset:38912\n" \
"ds_read_b128 a[92:95],v241 offset:39936\ns_waitcnt vmcnt(0)\nv_cmp_eq_u32 vcc,0,v248\nM v[168:171],a[32:35],a[160:163],v[232:235]\nds_read_b128 a[96:99],v241 offset:40960\ns_cbranch_vccnz .Lps2\n.Lpb2:\nM v[168:171],a[36:39],a[164:167],v[168:171]\n" \
"ds_read_b128 a[100:103],v241 offset:41984\nv_exp_f32 v128,v128\nM v[168:171],a[40:43],a[168:171],v[168:171]\nds_read_b128 a[104:107],v241 offset:43008\ns_add_u32 m0,s64,65536\nM v[168:171],a[44:47],a[172:175],v[168:171]\nds_read_b128 a[108:111],v241 offset:44032\nbuffer_load_dwordx4 v240,s[48:51],s36 offen offset:0 sc1 lds\n" \
"M v[172:175],a[32:35],a[176:179],v[236:239]\nv_exp_f32 v129,v129\nM v[172:175],a[36:39],a[180:183],v[172:175]\nds_read_b128 a[112:115],v241 offset:45056\nv_exp_f32 v130,v130\nM v[172:175],a[40:43],a[184:187],v[172:175]\nds_read_b128 a[116:119],v241 offset:46080\nds_read_b128 a[120:123],v241 offset:47104\n" \
"M v[172:175],a[44:47],a[188:191],v[172:175]\nds_read_b128 a[124:127],v241 offset:48128\nbuffer_load_dwordx4 v240,s[48:51],s36 offen offset:1024 sc1 lds\nM v[184:187],a[48:51],a[160:163],v[232:235]\nds_read_b128 a[0:3],v240 offset:0\nv_exp_f32 v131,v131\nM v[184:187],a[52:55],a[164:167],v[184:187]\nv_exp_f32 v132,v132\n" \
"M v[184:187],a[56:59],a[168:171],v[184:187]\nds_read_b128 a[4:7],v240 offset:1024\nv_exp_f32 v133,v133\nM v[184:187],a[60:63],a[172:175],v[184:187]\nds_read_b128 a[8:11],v240 offset:2048\nds_read_b128 a[12:15],v240 offset:3072\nM v[188:191],a[48:51],a[176:179],v[236:239]\nds_read_b128 a[16:19],v240 offset:4096\n" \
"buffer_load_dwordx4 v240,s[48:51],s36 offen offset:2048 sc1 lds\nM v[188:191],a[52:55],a[180:183],v[188:191]\nv_exp_f32 v134,v134\nM v[188:191],a[56:59],a[184:187],v[188:191]\nds_read_b128 a[20:23],v240 offset:5120\nv_exp_f32 v135,v135\nM v[188:191],a[60:63],a[188:191],v[188:191]\nds_read_b128 a[24:27],v240 offset:6144\n" \
"s_waitcnt lgkmcnt(8)\nM v[0:3],a[64:67],v[192:195],v[0:3]\nds_read_b128 a[28:31],v240 offset:7168\nbuffer_load_dwordx4 v240,s[48:51],s36 offen offset:3072 sc1 lds\nM v[4:7],a[64:67],v[196:199],v[4:7]\nds_read_b128 a[32:35],v240 offset:8192\nds_read_b128 a[36:39],v240 offset:9216\nM v[64:67],a[64:67],v[200:203],0\n" \
"ds_read_b128 a[40:43],v240 offset:10240\nv_exp_f32 v144,v144\nM v[68:71],a[64:67],v[204:207],0\ns_add_u32 m0,s64,16384\nds_read_b128 a[44:47],v240 offset:11264\nM v[8:11],a[68:71],v[192:195],v[8:11]\nbuffer_load_dwordx4 v240,s[44:47],s65 offen offset:0 sc1 lds\nds_read_b128 a[48:51],v240 offset:12288\n" \
"M v[12:15],a[68:71],v[196:199],v[12:15]\nds_read_b128 a[52:55],v240 offset:13312\nv_exp_f32 v145,v145\nM v[72:75],a[68:71],v[200:203],0\nv_exp_f32 v146,v146\nM v[76:79],a[68:71],v[204:207],0\nds_read_b128 a[56:59],v240 offset:14336\nv_exp_f32 v147,v147\n" \
"M v[16:19],a[72:75],v[192:195],v[16:19]\nbuffer_load_dwordx4 v240,s[44:47],s65 offen offset:1024 sc1 lds\nM v[20:23],a[72:75],v[196:199],v[20:23]\nds_read_b128 a[60:63],v240 offset:15360\nv_exp_f32 v148,v148\nM v[80:83],a[72:75],v[200:203],0\nv_exp_f32 v149,v149\nM v[84:87],a[72:75],v[204:207],0\n" \
"v_exp_f32 v150,v150\nM v[24:27],a[76:79],v[192:195],v[24:27]\nbuffer_load_dwordx4 v240,s[44:47],s65 offen offset:2048 sc1 lds\nM v[28:31],a[76:79],v[196:199],v[28:31]\nv_exp_f32 v151,v151\nM v[88:91],a[76:79],v[200:203],0\nv_exp_f32 v136,v136\nM v[92:95],a[76:79],v[204:207],0\n" \
"v_exp_f32 v137,v137\nM v[32:35],a[80:83],v[192:195],v[32:35]\nbuffer_load_dwordx4 v240,s[44:47],s65 offen offset:3072 sc1 lds\nM v[36:39],a[80:83],v[196:199],v[36:39]\ns_add_u32 m0,s64,81920\nv_exp_f32 v138,v138\nM v[96:99],a[80:83],v[200:203],0\nv_exp_f32 v139,v139\n" \
"M v[100:103],a[80:83],v[204:207],0\nv_exp_f32 v140,v140\nM v[40:43],a[84:87],v[192:195],v[40:43]\nbuffer_load_dwordx4 v240,s[48:51],s65 offen offset:0 sc1 lds\nM v[44:47],a[84:87],v[196:199],v[44:47]\nv_exp_f32 v141,v141\nM v[104:107],a[84:87],v[200:203],0\nv_exp_f32 v142,v142\n" \
"M v[108:111],a[84:87],v[204:207],0\nv_exp_f32 v143,v143\nM v[48:51],a[88:91],v[192:195],v[48:51]\nbuffer_load_dwordx4 v240,s[48:51],s65 offen offset:1024 sc1 lds\nM v[52:55],a[88:91],v[196:199],v[52:55]\nv_exp_f32 v152,v152\nM v[112:115],a[88:91],v[200:203],0\nv_exp_f32 v153,v153\n" \
"M v[116:119],a[88:91],v[204:207],0\nv_exp_f32 v154,v154\nM v[56:59],a[92:95],v[192:195],v[56:59]\nbuffer_load_dwordx4 v240,s[48:51],s65 offen offset:2048 sc1 lds\nM v[60:63],a[92:95],v[196:199],v[60:63]\nv_exp_f32 v155,v155\nM v[120:123],a[92:95],v[200:203],0\nv_exp_f32 v156,v156\n" \
"M v[124:127],a[92:95],v[204:207],0\nv_exp_f32 v157,v157\nM a[192:195],a[208:211],v[192:195],a[192:195]\nbuffer_load_dwordx4 v240,s[48:51],s65 offen offset:3072 sc1 lds\ns_add_u32 m0,s64,32768\nM a[196:199],a[208:211],v[196:199],a[196:199]\nv_exp_f32 v158,v158\nv_cvt_pk_bf16_f32 v192,v128,v129\n" \
"M a[200:203],a[208:211],v[200:203],0\nv_exp_f32 v159,v159\nM a[204:207],a[208:211],v[204:207],0\nv_exp_f32 v160,v160\nv_cvt_pk_bf16_f32 v193,v130,v131\nM v[0:3],a[96:99],v[208:211],v[0:3]\nbuffer_load_dwordx4 v240,s[44:47],s66 offen offset:0 sc1 lds\nM v[4:7],a[96:99],v[212:215],v[4:7]\n" \
"v_exp_f32 v161,v161\nv_cvt_pk_bf16_f32 v194,v144,v145\nM v[64:67],a[96:99],v[216:219],v[64:67]\nv_exp_f32 v162,v162\nM v[68:71],a[96:99],v[220:223],v[68:71]\nv_exp_f32 v163,v163\nv_cvt_pk_bf16_f32 v195,v146,v147\nM v[8:11],a[100:103],v[208:211],v[8:11]\n" \
"buffer_load_dwordx4 v240,s[44:47],s66 offen offset:1024 sc1 lds\nM v[12:15],a[100:103],v[212:215],v[12:15]\nv_exp_f32 v164,v164\nv_cvt_pk_bf16_f32 v196,v132,v133\nM v[72:75],a[100:103],v[216:219],v[72:75]\ns_waitcnt lgkmcnt(0)\ns_barrier\nM v[76:79],a[100:103],v[220:223],v[76:79]\n" \
"v_exp_f32 v165,v165\nv_cvt_pk_bf16_f32 v197,v134,v135\nM v[16:19],a[104:107],v[208:211],v[16:19]\nbuffer_load_dwordx4 v240,s[44:47],s66 offen offset:2048 sc1 lds\nM v[20:23],a[104:107],v[212:215],v[20:23]\nv_exp_f32 v166,v166\nv_cvt_pk_bf16_f32 v198,v148,v149\nM v[80:83],a[104:107],v[216:219],v[80:83]\n" \
"v_exp_f32 v167,v167\nv_cvt_pk_bf16_f32 v199,v150,v151\nM v[84:87],a[104:107],v[220:223],v[84:87]\nv_exp_f32 v176,v176\nM v[24:27],a[108:111],v[208:211],v[24:27]\nbuffer_load_dwordx4 v240,s[44:47],s66 offen offset:3072 sc1 lds\ns_add_u32 m0,s64,98304\nM v[28:31],a[108:111],v[212:215],v[28:31]\n" \
"v_exp_f32 v177,v177\nM v[88:91],a[108:111],v[216:219],v[88:91]\nv_exp_f32 v178,v178\nv_cvt_pk_bf16_f32 v200,v136,v137\nM v[92:95],a[108:111],v[220:223],v[92:95]\nv_exp_f32 v179,v179\nM v[32:35],a[112:115],v[208:211],v[32:35]\nv_exp_f32 v180,v180\n" \
"v_cvt_pk_bf16_f32 v201,v138,v139\nM v[36:39],a[112:115],v[212:215],v[36:39]\nv_exp_f32 v181,v181\nM v[96:99],a[112:115],v[216:219],v[96:99]\nv_exp_f32 v182,v182\nv_cvt_pk_bf16_f32 v202,v152,v153\nM v[100:103],a[112:115],v[220:223],v[100:103]\nv_exp_f32 v183,v183\n" \
"M v[40:43],a[116:119],v[208:211],v[40:43]\nv_exp_f32 v168,v168\nv_cvt_pk_bf16_f32 v203,v154,v155\nM v[44:47],a[116:119],v[212:215],v[44:47]\nv_exp_f32 v169,v169\nM v[104:107],a[116:119],v[216:219],v[104:107]\nv_exp_f32 v170,v170\nv_cvt_pk_bf16_f32 v204,v140,v141\n" \
"M v[108:111],a[116:119],v[220:223],v[108:111]\nv_exp_f32 v171,v171\nv_cvt_pk_bf16_f32 v205,v142,v143\nM v[48:51],a[120:123],v[208:211],v[48:51]\nv_cvt_pk_bf16_f32 v206,v156,v157\nv_cvt_pk_bf16_f32 v207,v158,v159\nM v[52:55],a[120:123],v[212:215],v[52:55]\nbuffer_load_dwordx4 v240,s[48:51],s66 offen offset:0 sc1 lds\n" \
"M v[112:115],a[120:123],v[216:219],v[112:115]\nM v[116:119],a[120:123],v[220:223],v[116:119]\nM v[56:59],a[124:127],v[208:211],v[56:59]\nM v[60:63],a[124:127],v[212:215],v[60:63]\nbuffer_load_dwordx4 v240,s[48:51],s66 offen offset:1024 sc1 lds\nM v[120:123],a[124:127],v[216:219],v[120:123]\nM v[124:127],a[124:127],v[220:223],v[124:127]\nM a[192:195],a[208:211],v[208:211],a[192:195]\n" \
"M a[196:199],a[208:211],v[212:215],a[196:199]\nbuffer_load_dwordx4 v240,s[48:51],s66 offen offset:2048 sc1 lds\nM a[200:203],a[208:211],v[216:219],a[200:203]\nbuffer_load_dwordx4 v240,s[48:51],s66 offen offset:3072 sc1 lds\nM a[204:207],a[208:211],v[220:223],a[204:207]\ns_cmp_lt_u32 s62,2\ns_cbranch_scc1 .Lx01\ns_branch .Ldual\n" \
".Lpro1:\ns_barrier\nM v[64:67],a[0:3],a[128:131],0\nds_read_b128 a[64:67],v241 offset:0\nds_read_b128 a[68:71],v241 offset:1024\nM v[64:67],a[4:7],a[132:135],v[64:67]\nds_read_b128 a[72:75],v241 offset:2048\ns_mov_b32 m0,s64\n" \
"v_mov_b32 v202,0\nM v[64:67],a[8:11],a[136:139],v[64:67]\nds_read_b128 a[76:79],v241 offset:3072\nv_mov_b32 v203,0\nM v[64:67],a[12:15],a[140:143],v[64:67]\nds_read_b128 a[80:83],v241 offset:4096\nbuffer_load_dwordx4 v240,s[44:47],s36 offen offset:0 sc1 lds\nM v[68:71],a[0:3],a[144:147],0\n" \
"ds_read_b128 a[0:3],v240 offset:32768\nM v[68:71],a[4:7],a[148:151],v[68:71]\nds_read_b128 a[4:7],v240 offset:33792\nM v[68:71],a[8:11],a[152:155],v[68:71]\nds_read_b128 a[8:11],v240 offset:34816\nv_max3_f32 v225,v64,v65,v66\nM v[68:71],a[12:15],a[156:159],v[68:71]\nds_read_b128 a[12:15],v240 offset:35840\n" \
"buffer_load_dwordx4 v240,s[44:47],s36 offen offset:1024 sc1 lds\nM v[72:75],a[16:19],a[128:131],0\nds_read_b128 a[84:87],v241 offset:5120\nM v[72:75],a[20:23],a[132:135],v[72:75]\nds_read_b128 a[88:91],v241 offset:6144\nM v[72:75],a[24:27],a[136:139],v[72:75]\nds_read_b128 a[92:95],v241 offset:7168\nv_max3_f32 v229,v68,v69,v70\n" \
"M v[72:75],a[28:31],a[140:143],v[72:75]\nbuffer_load_dwordx4 v240,s[44:47],s36 offen offset:2048 sc1 lds\nds_read_b128 a[96:99],v241 offset:8192\nM v[76:79],a[16:19],a[144:147],0\nds_read_b128 a[16:19],v240 offset:36864\nM v[76:79],a[20:23],a[148:151],v[76:79]\nds_read_b128 a[20:23],v240 offset:37888\nM v[76:79],a[24:27],a[152:155],v[76:79]\n" \
"ds_read_b128 a[24:27],v240 offset:38912\nv_max3_f32 v225,v225,v67,v72\nv_max3_f32 v225,v225,v73,v74\nM v[76:79],a[28:31],a[156:159],v[76:79]\nds_read_b128 a[28:31],v240 offset:39936\nv_max_f32 v225,v225,v75\nv_cndmask_b32 v225,v224,v225,s[90:91]\nM v[80:83],a[32:35],a[128:131],0\n" \
"buffer_load_dwordx4 v240,s[44:47],s36 offen offset:3072 sc1 lds\nM v[80:83],a[36:39],a[132:135],v[80:83]\nds_read_b128 a[100:103],v241 offset:9216\nds_read_b128 a[104:107],v241 offset:10240\nv_max3_f32 v229,v229,v71,v76\nM v[80:83],a[40:43],a[136:139],v[80:83]\nv_max3_f32 v229,v229,v77,v78\nv_max_f32 v229,v229,v79\n" \
"v_cndmask_b32 v229,v228,v229,s[90:91]\nM v[80:83],a[44:47],a[140:143],v[80:83]\nds_read_b128 a[108:111],v241 offset:11264\nv_permlane16_swap_b32 v225,v229\nM v[84:87],a[32:35],a[144:147],0\nv_max_f32 v225,v225,v229\nv_mov_b32 v229,v225\nds_read_b128 a[32:35],v240 offset:40960\n" \
"M v[84:87],a[36:39],a[148:151],v[84:87]\nv_permlane32_swap_b32 v225,v229\nv_max_f32 v225,v225,v229\nM v[84:87],a[40:43],a[152:155],v[84:87]\nv_mov_b32 v229,v225\nds_read_b128 a[36:39],v240 offset:41984\nds_read_b128 a[40:43],v240 offset:43008\nM v[84:87],a[44:47],a[156:159],v[84:87]\n" \
"v_permlane16_swap_b32 v225,v229\nv_sub_f32 v224,0,v225\nv_sub_f32 v226,0,v225\nM v[92:95],a[48:51],a[144:147],0\nv_sub_f32 v227,0,v225\nv_sub_f32 v225,0,v225\nM v[92:95],a[52:55],a[148:151],v[92:95]\nv_sub_f32 v228,0,v229\n" \
"v_sub_f32 v230,0,v229\nv_sub_f32 v231,0,v229\nM v[92:95],a[56:59],a[152:155],v[92:95]\nv_sub_f32 v229,0,v229\nds_read_b128 a[44:47],v240 offset:44032\nds_read_b128 a[48:51],v240 offset:45056\nM v[92:95],a[60:63],a[156:159],v[92:95]\nds_read_b128 a[52:55],v240 offset:46080\n" \
"s_waitcnt lgkmcnt(7)\nM v[136:139],a[0:3],a[160:163],0\nds_read_b128 a[56:59],v240 offset:47104\nv_add_f32 v64,v64,v224\nv_add_f32 v65,v65,v224\nM v[136:139],a[4:7],a[164:167],v[136:139]\nds_read_b128 a[60:63],v240 offset:48128\nv_add_f32 v66,v66,v224\n" \
"M v[136:139],a[8:11],a[168:171],v[136:139]\nv_add_f32 v67,v67,v224\nv_exp_f32 v64,v64\nM v[136:139],a[12:15],a[172:175],v[136:139]\nv_exp_f32 v65,v65\nv_add_f32 v68,v68,v228\nM v[140:143],a[0:3],a[176:179],0\nv_exp_f32 v66,v66\n" \
"M v[140:143],a[4:7],a[180:183],v[140:143]\nv_exp_f32 v67,v67\nv_add_f32 v69,v69,v228\nM v[140:143],a[8:11],a[184:187],v[140:143]\nv_add_f32 v72,v72,v224\nv_add_f32 v73,v73,v224\nv_add_f32 v74,v74,v224\nM v[140:143],a[12:15],a[188:191],v[140:143]\n" \
"v_add_f32 v75,v75,v224\nv_add_f32 v70,v70,v228\nM v[156:159],a[16:19],a[176:179],0\nv_add_f32 v71,v71,v228\nv_exp_f32 v68,v68\nM v[156:159],a[20:23],a[180:183],v[156:159]\nv_exp_f32 v69,v69\nv_cvt_pk_bf16_f32 v64,v64,v65\n" \
"M v[156:159],a[24:27],a[184:187],v[156:159]\nv_exp_f32 v72,v72\nM v[156:159],a[28:31],a[188:191],v[156:159]\nv_exp_f32 v73,v73\nv_add_f32 v76,v76,v228\nM v[128:131],a[0:3],a[128:131],v[224:227]\nv_exp_f32 v74,v74\nM v[128:131],a[4:7],a[132:135],v[128:131]\n" \
"v_exp_f32 v75,v75\nv_add_f32 v77,v77,v228\nM v[128:131],a[8:11],a[136:139],v[128:131]\nv_exp_f32 v70,v70\nv_add_f32 v78,v78,v228\nM v[128:131],a[12:15],a[140:143],v[128:131]\nv_exp_f32 v71,v71\nM v[132:135],a[0:3],a[144:147],v[228:231]\n" \
"v_add_f32 v79,v79,v228\nv_cvt_pk_bf16_f32 v65,v66,v67\nv_cvt_pk_bf16_f32 v66,v72,v73\nM v[132:135],a[4:7],a[148:151],v[132:135]\nv_exp_f32 v76,v76\nv_cvt_pk_bf16_f32 v67,v74,v75\nM v[132:135],a[8:11],a[152:155],v[132:135]\nv_exp_f32 v77,v77\n" \
"M v[132:135],a[12:15],a[156:159],v[132:135]\nv_exp_f32 v78,v78\nv_cvt_pk_bf16_f32 v68,v68,v69\nM v[144:147],a[16:19],a[128:131],v[224:227]\nv_exp_f32 v79,v79\nv_cvt_pk_bf16_f32 v69,v70,v71\nM v[144:147],a[20:23],a[132:135],v[144:147]\nv_cvt_pk_bf16_f32 v70,v76,v77\n" \
"v_cvt_pk_bf16_f32 v71,v78,v79\nM v[144:147],a[24:27],a[136:139],v[144:147]\nv_cndmask_b32 v80,v80,v252,s[82:83]\nv_cndmask_b32 v81,v81,v252,s[84:85]\nv_cndmask_b32 v82,v82,v252,s[86:87]\nM v[144:147],a[28:31],a[140:143],v[144:147]\nv_cndmask_b32 v83,v83,v252,s[88:89]\nv_add_f32 v80,v80,v224\n" \
"M v[148:151],a[16:19],a[144:147],v[228:231]\nv_add_f32 v81,v81,v224\nv_add_f32 v82,v82,v224\nv_add_f32 v83,v83,v224\nM v[148:151],a[20:23],a[148:151],v[148:151]\nv_exp_f32 v80,v80\nv_add_f32 v84,v84,v228\nM v[148:151],a[24:27],a[152:155],v[148:151]\n" \
"v_exp_f32 v81,v81\nM v[148:151],a[28:31],a[156:159],v[148:151]\nv_exp_f32 v82,v82\ns_waitcnt lgkmcnt(0)\nM v[160:163],a[32:35],a[128:131],v[224:227]\nv_exp_f32 v83,v83\nv_add_f32 v85,v85,v228\nM v[160:163],a[36:39],a[132:135],v[160:163]\n" \
"v_cndmask_b32 v92,v92,v252,s[82:83]\nv_cndmask_b32 v93,v93,v252,s[84:85]\nM v[160:163],a[40:43],a[136:139],v[160:163]\nv_cndmask_b32 v94,v94,v252,s[86:87]\nv_cndmask_b32 v95,v95,v252,s[88:89]\nv_add_f32 v86,v86,v228\nM v[160:163],a[44:47],a[140:143],v[160:163]\nv_add_f32 v87,v87,v228\n" \
"v_cvt_pk_bf16_f32 v80,v80,v81\nM v[164:167],a[32:35],a[144:147],v[228:231]\nv_exp_f32 v84,v84\nds_read_b128 a[112:115],v241 offset:12288\nM v[164:167],a[36:39],a[148:151],v[164:167]\nv_exp_f32 v85,v85\nv_add_f32 v92,v92,v228\nM v[164:167],a[40:43],a[152:155],v[164:167]\n" \
"v_exp_f32 v86,v86\nM v[164:167],a[44:47],a[156:159],v[164:167]\nv_exp_f32 v87,v87\nv_add_f32 v93,v93,v228\nM v[176:179],a[48:51],a[128:131],v[224:227]\nv_add_f32 v94,v94,v228\nv_add_f32 v95,v95,v228\nv_cvt_pk_bf16_f32 v81,v82,v83\n" \
"M v[176:179],a[52:55],a[132:135],v[176:179]\nv_exp_f32 v92,v92\nM v[176:179],a[56:59],a[136:139],v[176:179]\nv_exp_f32 v93,v93\nv_mov_b32 v82,0\nM v[176:179],a[60:63],a[140:143],v[176:179]\nv_exp_f32 v94,v94\nv_mov_b32 v83,0\n" \
"M v[180:183],a[48:51],a[144:147],v[228:231]\nv_exp_f32 v95,v95\nM v[180:183],a[52:55],a[148:151],v[180:183]\nv_cvt_pk_bf16_f32 v84,v84,v85\nv_cvt_pk_bf16_f32 v85,v86,v87\nv_cvt_pk_bf16_f32 v86,v92,v93\nM v[180:183],a[56:59],a[152:155],v[180:183]\nv_cvt_pk_bf16_f32 v87,v94,v95\n" \
"ds_read_b128 a[116:119],v241 offset:13312\nM v[180:183],a[60:63],a[156:159],v[180:183]\nds_read_b128 a[120:123],v241 offset:14336\nds_read_b128 a[124:127],v241 offset:15360\nv_max3_f32 v237,v140,v141,v142\nM v[0:3],a[64:67],v[64:67],0\nds_read_b128 a[0:3],v240 offset:49152\nv_exp_f32 v128,v128\n" \
"M v[4:7],a[64:67],v[68:71],0\nds_read_b128 a[4:7],v240 offset:50176\nv_max3_f32 v237,v237,v143,v156\nM v[8:11],a[68:71],v[64:67],0\nbuffer_load_dword v248,v251,s[52:55],s80 offen sc1\nds_read_b128 a[8:11],v240 offset:51200\nM v[12:15],a[68:71],v[68:71],0\nds_read_b128 a[12:15],v240 offset:52224\n" \
"v_exp_f32 v129,v129\nM v[16:19],a[72:75],v[64:67],0\nds_read_b128 a[16:19],v240 offset:53248\nv_cvt_pk_bf16_f32 v192,v128,v129\nM v[20:23],a[72:75],v[68:71],0\nv_exp_f32 v130,v130\nds_read_b128 a[20:23],v240 offset:54272\nM v[24:27],a[76:79],v[64:67],0\n" \
"v_exp_f32 v131,v131\nds_read_b128 a[24:27],v240 offset:55296\nM v[28:31],a[76:79],v[68:71],0\nv_cvt_pk_bf16_f32 v193,v130,v131\nds_read_b128 a[28:31],v240 offset:56320\nM v[32:35],a[80:83],v[64:67],0\nv_exp_f32 v132,v132\nv_max3_f32 v233,v136,v137,v138\n" \
"M v[36:39],a[80:83],v[68:71],0\nv_exp_f32 v133,v133\nM v[40:43],a[84:87],v[64:67],0\nv_exp_f32 v134,v134\nv_cvt_pk_bf16_f32 v196,v132,v133\nM v[44:47],a[84:87],v[68:71],0\nv_exp_f32 v135,v135\nv_max3_f32 v237,v237,v157,v158\n" \
"M v[48:51],a[88:91],v[64:67],0\nv_cvt_pk_bf16_f32 v197,v134,v135\nv_max_f32 v233,v233,v139\nM v[52:55],a[88:91],v[68:71],0\nv_exp_f32 v144,v144\nv_max_f32 v237,v237,v159\nM v[56:59],a[92:95],v[64:67],0\nv_exp_f32 v145,v145\n" \
"v_cndmask_b32 v233,v232,v233,s[90:91]\nM v[60:63],a[92:95],v[68:71],0\nv_exp_f32 v146,v146\nM a[192:195],a[208:211],v[64:67],0\nv_exp_f32 v147,v147\nv_cvt_pk_bf16_f32 v194,v144,v145\nM a[196:199],a[208:211],v[68:71],0\nv_cvt_pk_bf16_f32 v195,v146,v147\n" \
"v_cndmask_b32 v237,v236,v237,s[90:91]\nM v[0:3],a[96:99],v[80:83],v[0:3]\nv_exp_f32 v148,v148\nv_permlane16_swap_b32 v233,v237\nM v[4:7],a[96:99],v[84:87],v[4:7]\nv_max_f32 v233,v233,v237\nv_exp_f32 v149,v149\nM v[8:11],a[100:103],v[80:83],v[8:11]\n" \
"v_exp_f32 v150,v150\nM v[12:15],a[100:103],v[84:87],v[12:15]\nv_exp_f32 v151,v151\nv_mov_b32 v237,v233\nM v[16:19],a[104:107],v[80:83],v[16:19]\nv_cvt_pk_bf16_f32 v198,v148,v149\nv_permlane32_swap_b32 v233,v237\nv_cvt_pk_bf16_f32 v199,v150,v151\n" \
"M v[20:23],a[104:107],v[84:87],v[20:23]\nv_max_f32 v233,v233,v237\nv_mov_b32 v237,v233\nM v[24:27],a[108:111],v[80:83],v[24:27]\nv_cndmask_b32 v136,v136,v252,s[82:83]\nv_permlane16_swap_b32 v233,v237\nv_cndmask_b32 v137,v137,v252,s[84:85]\nM v[28:31],a[108:111],v[84:87],v[28:31]\n" \
"v_cndmask_b32 v138,v138,v252,s[86:87]\nv_cndmask_b32 v139,v139,v252,s[88:89]\ns_waitcnt lgkmcnt(0)\nM v[32:35],a[112:115],v[80:83],v[32:35]\nv_sub_f32 v232,0,v233\nv_sub_f32 v234,0,v233\nM v[36:39],a[112:115],v[84:87],v[36:39]\nv_sub_f32 v235,0,v233\n" \
"v_add_f32 v136,v136,v232\nv_add_f32 v137,v137,v232\nM v[40:43],a[116:119],v[80:83],v[40:43]\nv_add_f32 v138,v138,v232\nv_add_f32 v139,v139,v232\nM v[44:47],a[116:119],v[84:87],v[44:47]\nv_sub_f32 v233,0,v233\nv_exp_f32 v136,v136\n" \
"M v[48:51],a[120:123],v[80:83],v[48:51]\nv_exp_f32 v137,v137\nv_sub_f32 v236,0,v237\nM v[52:55],a[120:123],v[84:87],v[52:55]\nv_exp_f32 v138,v138\nM v[56:59],a[124:127],v[80:83],v[56:59]\nv_exp_f32 v139,v139\nv_cvt_pk_bf16_f32 v200,v136,v137\n" \
"M v[60:63],a[124:127],v[84:87],v[60:63]\nv_cvt_pk_bf16_f32 v201,v138,v139\nv_sub_f32 v238,0,v237\nv_sub_f32 v239,0,v237\nM a[192:195],a[208:211],v[80:83],a[192:195]\nv_add_f32 v140,v140,v236\nv_add_f32 v141,v141,v236\nM a[196:199],a[208:211],v[84:87],a[196:199]\n" \
"v_add_f32 v142,v142,v236\nv_add_f32 v143,v143,v236\nv_sub_f32 v237,0,v237\nM v[128:131],a[0:3],a[128:131],v[224:227]\nv_exp_f32 v140,v140\nds_read_b128 a[32:35],v240 offset:57344\nM v[128:131],a[4:7],a[132:135],v[128:131]\nv_exp_f32 v141,v141\n" \
"M v[128:131],a[8:11],a[136:139],v[128:131]\nv_exp_f32 v142,v142\nv_cvt_pk_bf16_f32 v204,v140,v141\nM v[128:131],a[12:15],a[140:143],v[128:131]\nv_exp_f32 v143,v143\nM v[132:135],a[0:3],a[144:147],v[228:231]\nv_cvt_pk_bf16_f32 v205,v142,v143\nds_read_b128 a[36:39],v240 offset:58368\n" \
"ds_read_b128 a[40:43],v240 offset:59392\nM v[132:135],a[4:7],a[148:151],v[132:135]\nds_read_b128 a[44:47],v240 offset:60416\nv_cndmask_b32 v156,v156,v252,s[82:83]\nv_cndmask_b32 v157,v157,v252,s[84:85]\nM v[132:135],a[8:11],a[152:155],v[132:135]\nv_cndmask_b32 v158,v158,v252,s[86:87]\nv_cndmask_b32 v159,v159,v252,s[88:89]\n" \
"M v[132:135],a[12:15],a[156:159],v[132:135]\nv_add_f32 v156,v156,v236\nv_add_f32 v157,v157,v236\nv_add_f32 v158,v158,v236\nM v[144:147],a[16:19],a[128:131],v[224:227]\nv_add_f32 v159,v159,v236\nv_exp_f32 v156,v156\nM v[144:147],a[20:23],a[132:135],v[144:147]\n" \
"v_exp_f32 v157,v157\nM v[144:147],a[24:27],a[136:139],v[144:147]\nv_exp_f32 v158,v158\nv_cvt_pk_bf16_f32 v206,v156,v157\nM v[144:147],a[28:31],a[140:143],v[144:147]\nv_exp_f32 v159,v159\nM v[148:151],a[16:19],a[144:147],v[228:231]\nv_cvt_pk_bf16_f32 v207,v158,v159\n" \
"ds_read_b128 a[48:51],v240 offset:61440\nds_read_b128 a[52:55],v240 offset:62464\nM v[148:151],a[20:23],a[148:151],v[148:151]\nv_exp_f32 v160,v160\nds_read_b128 a[56:59],v240 offset:63488\nM v[148:151],a[24:27],a[152:155],v[148:151]\nv_exp_f32 v161,v161\nM v[148:151],a[28:31],a[156:159],v[148:151]\n" \
"v_exp_f32 v162,v162\nv_cvt_pk_bf16_f32 v208,v160,v161\nM v[136:139],a[0:3],a[160:163],v[232:235]\ns_waitcnt vmcnt(1)\ns_barrier\nds_read_b128 a[60:63],v240 offset:64512\nM v[136:139],a[4:7],a[164:167],v[136:139]\nv_exp_f32 v163,v163\n" \
"M v[136:139],a[8:11],a[168:171],v[136:139]\nv_cvt_pk_bf16_f32 v209,v162,v163\nv_exp_f32 v164,v164\nM v[136:139],a[12:15],a[172:175],v[136:139]\nv_exp_f32 v165,v165\nds_read_b128 a[64:67],v241 offset:32768\nM v[140:143],a[0:3],a[176:179],v[236:239]\nv_exp_f32 v166,v166\n" \
"M v[140:143],a[4:7],a[180:183],v[140:143]\nv_exp_f32 v167,v167\nv_cvt_pk_bf16_f32 v212,v164,v165\nM v[140:143],a[8:11],a[184:187],v[140:143]\nv_cvt_pk_bf16_f32 v213,v166,v167\nds_read_b128 a[68:71],v241 offset:33792\nM v[140:143],a[12:15],a[188:191],v[140:143]\nv_exp_f32 v176,v176\n" \
"ds_read_b128 a[72:75],v241 offset:34816\nM v[152:155],a[16:19],a[160:163],v[232:235]\nv_exp_f32 v177,v177\nds_read_b128 a[76:79],v241 offset:35840\nM v[152:155],a[20:23],a[164:167],v[152:155]\nv_exp_f32 v178,v178\nM v[152:155],a[24:27],a[168:171],v[152:155]\nv_exp_f32 v179,v179\n" \
"v_cvt_pk_bf16_f32 v210,v176,v177\nM v[152:155],a[28:31],a[172:175],v[152:155]\nv_cvt_pk_bf16_f32 v211,v178,v179\nv_exp_f32 v180,v180\nM v[156:159],a[16:19],a[176:179],v[236:239]\nv_exp_f32 v181,v181\nM v[156:159],a[20:23],a[180:183],v[156:159]\nv_exp_f32 v182,v182\n" \
"v_cvt_pk_bf16_f32 v214,v180,v181\nM v[156:159],a[24:27],a[184:187],v[156:159]\nv_exp_f32 v183,v183\nds_read_b128 a[80:83],v241 offset:36864\nM v[156:159],a[28:31],a[188:191],v[156:159]\nv_cvt_pk_bf16_f32 v215,v182,v183\ns_waitcnt lgkmcnt(3)\nM v[160:163],a[32:35],a[128:131],v[224:227]\n" \
"ds_read_b128 a[84:87],v241 offset:37888\nds_read_b128 a[88:91],v241 offset:38912\ns_waitcnt vmcnt(0)\nv_cmp_eq_u32 vcc,0,v248\nM v[160:163],a[36:39],a[132:135],v[160:163]\nds_read_b128 a[92:95],v241 offset:39936\ns_cbranch_vccnz .Lps3\n.Lpb3:\n" \
"M v[160:163],a[40:43],a[136:139],v[160:163]\nds_read_b128 a[96:99],v241 offset:40960\nv_exp_f32 v128,v128\nM v[160:163],a[44:47],a[140:143],v[160:163]\nds_read_b128 a[100:103],v241 offset:41984\nv_exp_f32 v129,v129\nM v[164:167],a[32:35],a[144:147],v[228:231]\nds_read_b128 a[104:107],v241 offset:43008\n" \
"s_add_u32 m0,s64,65536\nM v[164:167],a[36:39],a[148:151],v[164:167]\nds_read_b128 a[108:111],v241 offset:44032\nbuffer_load_dwordx4 v240,s[48:51],s36 offen offset:0 sc1 lds\nM v[164:167],a[40:43],a[152:155],v[164:167]\nds_read_b128 a[112:115],v241 offset:45056\nv_exp_f32 v130,v130\nM v[164:167],a[44:47],a[156:159],v[164:167]\n" \
"v_exp_f32 v131,v131\nM v[176:179],a[48:51],a[128:131],v[224:227]\nds_read_b128 a[116:119],v241 offset:46080\nv_exp_f32 v132,v132\nM v[176:179],a[52:55],a[132:135],v[176:179]\nds_read_b128 a[120:123],v241 offset:47104\nds_read_b128 a[124:127],v241 offset:48128\nM v[176:179],a[56:59],a[136:139],v[176:179]\n" \
"ds_read_b128 a[0:3],v240 offset:0\nbuffer_load_dwordx4 v240,s[48:51],s36 offen offset:1024 sc1 lds\nM v[176:179],a[60:63],a[140:143],v[176:179]\nds_read_b128 a[4:7],v240 offset:1024\nv_exp_f32 v133,v133\nM v[180:183],a[48:51],a[144:147],v[228:231]\nv_exp_f32 v134,v134\nM v[180:183],a[52:55],a[148:151],v[180:183]\n" \
"ds_read_b128 a[8:11],v240 offset:2048\nv_exp_f32 v135,v135\nM v[180:183],a[56:59],a[152:155],v[180:183]\nds_read_b128 a[12:15],v240 offset:3072\nbuffer_load_dwordx4 v240,s[48:51],s36 offen offset:2048 sc1 lds\nM v[180:183],a[60:63],a[156:159],v[180:183]\nds_read_b128 a[16:19],v240 offset:4096\nds_read_b128 a[20:23],v240 offset:5120\n" \
"M v[168:171],a[32:35],a[160:163],v[232:235]\nds_read_b128 a[24:27],v240 offset:6144\nv_exp_f32 v144,v144\nM v[168:171],a[36:39],a[164:167],v[168:171]\nds_read_b128 a[28:31],v240 offset:7168\nv_exp_f32 v145,v145\nM v[168:171],a[40:43],a[168:171],v[168:171]\nbuffer_load_dwordx4 v240,s[48:51],s36 offen offset:3072 sc1 lds\n" \
"M v[168:171],a[44:47],a[172:175],v[168:171]\ns_add_u32 m0,s64,16384\nv_exp_f32 v146,v146\nM v[172:175],a[32:35],a[176:179],v[236:239]\nv_exp_f32 v147,v147\nM v[172:175],a[36:39],a[180:183],v[172:175]\nds_read_b128 a[32:35],v240 offset:8192\nv_exp_f32 v148,v148\n" \
"M v[172:175],a[40:43],a[184:187],v[172:175]\nds_read_b128 a[36:39],v240 offset:9216\nbuffer_load_dwordx4 v240,s[44:47],s65 offen offset:0 sc1 lds\nM v[172:175],a[44:47],a[188:191],v[172:175]\nds_read_b128 a[40:43],v240 offset:10240\nds_read_b128 a[44:47],v240 offset:11264\nM v[184:187],a[48:51],a[160:163],v[232:235]\nv_exp_f32 v149,v149\n" \
"M v[184:187],a[52:55],a[164:167],v[184:187]\nv_exp_f32 v150,v150\nM v[184:187],a[56:59],a[168:171],v[184:187]\nbuffer_load_dwordx4 v240,s[44:47],s65 offen offset:1024 sc1 lds\nM v[184:187],a[60:63],a[172:175],v[184:187]\nv_exp_f32 v151,v151\nM v[188:191],a[48:51],a[176:179],v[236:239]\nv_exp_f32 v136,v136\n" \
"M v[188:191],a[52:55],a[180:183],v[188:191]\nds_read_b128 a[48:51],v240 offset:12288\nv_exp_f32 v137,v137\nM v[188:191],a[56:59],a[184:187],v[188:191]\nbuffer_load_dwordx4 v240,s[44:47],s65 offen offset:2048 sc1 lds\nds_read_b128 a[52:55],v240 offset:13312\nM v[188:191],a[60:63],a[188:191],v[188:191]\nds_read_b128 a[56:59],v240 offset:14336\n" \
"ds_read_b128 a[60:63],v240 offset:15360\nM v[0:3],a[64:67],v[192:195],v[0:3]\nv_exp_f32 v138,v138\nM v[4:7],a[64:67],v[196:199],v[4:7]\nv_exp_f32 v139,v139\nM v[64:67],a[64:67],v[200:203],0\nbuffer_load_dwordx4 v240,s[44:47],s65 offen offset:3072 sc1 lds\nM v[68:71],a[64:67],v[204:207],0\n" \
"s_add_u32 m0,s64,81920\nv_exp_f32 v140,v140\nM v[8:11],a[68:71],v[192:195],v[8:11]\nv_exp_f32 v141,v141\nM v[12:15],a[68:71],v[196:199],v[12:15]\nv_exp_f32 v142,v142\nM v[72:75],a[68:71],v[200:203],0\nbuffer_load_dwordx4 v240,s[48:51],s65 offen offset:0 sc1 lds\n" \
"M v[76:79],a[68:71],v[204:207],0\ns_waitcnt lgkmcnt(3)\nM v[16:19],a[72:75],v[192:195],v[16:19]\nv_exp_f32 v143,v143\nM v[20:23],a[72:75],v[196:199],v[20:23]\nv_exp_f32 v152,v152\nM v[80:83],a[72:75],v[200:203],0\nbuffer_load_dwordx4 v240,s[48:51],s65 offen offset:1024 sc1 lds\n" \
"M v[84:87],a[72:75],v[204:207],0\nv_exp_f32 v153,v153\nM v[24:27],a[76:79],v[192:195],v[24:27]\nv_exp_f32 v154,v154\nM v[28:31],a[76:79],v[196:199],v[28:31]\nv_exp_f32 v155,v155\nM v[88:91],a[76:79],v[200:203],0\nbuffer_load_dwordx4 v240,s[48:51],s65 offen offset:2048 sc1 lds\n" \
"M v[92:95],a[76:79],v[204:207],0\nv_exp_f32 v156,v156\nM v[32:35],a[80:83],v[192:195],v[32:35]\nv_exp_f32 v157,v157\nM v[36:39],a[80:83],v[196:199],v[36:39]\nv_exp_f32 v158,v158\nM v[96:99],a[80:83],v[200:203],0\nbuffer_load_dwordx4 v240,s[48:51],s65 offen offset:3072 sc1 lds\n" \
"M v[100:103],a[80:83],v[204:207],0\ns_add_u32 m0,s64,32768\nv_exp_f32 v159,v159\nM v[40:43],a[84:87],v[192:195],v[40:43]\nv_exp_f32 v160,v160\nM v[44:47],a[84:87],v[196:199],v[44:47]\nv_exp_f32 v161,v161\nM v[104:107],a[84:87],v[200:203],0\n" \
"buffer_load_dwordx4 v240,s[44:47],s66 offen offset:0 sc1 lds\nM v[108:111],a[84:87],v[204:207],0\nv_exp_f32 v162,v162\nM v[48:51],a[88:91],v[192:195],v[48:51]\ns_barrier\nM v[52:55],a[88:91],v[196:199],v[52:55]\nv_exp_f32 v163,v163\nM v[112:115],a[88:91],v[200:203],0\n" \
"buffer_load_dwordx4 v240,s[44:47],s66 offen offset:1024 sc1 lds\nM v[116:119],a[88:91],v[204:207],0\nv_exp_f32 v164,v164\nM v[56:59],a[92:95],v[192:195],v[56:59]\nv_exp_f32 v165,v165\nM v[60:63],a[92:95],v[196:199],v[60:63]\nv_exp_f32 v166,v166\nM v[120:123],a[92:95],v[200:203],0\n" \
"buffer_load_dwordx4 v240,s[44:47],s66 offen offset:2048 sc1 lds\nM v[124:127],a[92:95],v[204:207],0\nv_exp_f32 v167,v167\nM a[192:195],a[208:211],v[192:195],a[192:195]\nv_exp_f32 v176,v176\nM a[196:199],a[208:211],v[196:199],a[196:199]\nv_exp_f32 v177,v177\nv_cvt_pk_bf16_f32 v192,v128,v129\n" \
"M a[200:203],a[208:211],v[200:203],0\nbuffer_load_dwordx4 v240,s[44:47],s66 offen offset:3072 sc1 lds\ns_add_u32 m0,s64,98304\nM a[204:207],a[208:211],v[204:207],0\nv_exp_f32 v178,v178\nM v[0:3],a[96:99],v[208:211],v[0:3]\nv_exp_f32 v179,v179\nv_cvt_pk_bf16_f32 v193,v130,v131\n" \
"M v[4:7],a[96:99],v[212:215],v[4:7]\nv_exp_f32 v180,v180\nM v[8:11],a[100:103],v[208:211],v[8:11]\nv_exp_f32 v181,v181\nv_cvt_pk_bf16_f32 v194,v144,v145\nM v[12:15],a[100:103],v[212:215],v[12:15]\nv_exp_f32 v182,v182\nv_cvt_pk_bf16_f32 v195,v146,v147\n" \
"M v[16:19],a[104:107],v[208:211],v[16:19]\nv_exp_f32 v183,v183\nM v[20:23],a[104:107],v[212:215],v[20:23]\nv_exp_f32 v168,v168\nv_cvt_pk_bf16_f32 v196,v132,v133\nM v[24:27],a[108:111],v[208:211],v[24:27]\nv_exp_f32 v169,v169\nv_cvt_pk_bf16_f32 v197,v134,v135\n" \
"M v[28:31],a[108:111],v[212:215],v[28:31]\nv_exp_f32 v170,v170\nM v[32:35],a[112:115],v[208:211],v[32:35]\nv_exp_f32 v171,v171\nv_cvt_pk_bf16_f32 v198,v148,v149\nM v[36:39],a[112:115],v[212:215],v[36:39]\nv_cvt_pk_bf16_f32 v199,v150,v151\nv_cvt_pk_bf16_f32 v200,v136,v137\n" \
"v_cvt_pk_bf16_f32 v201,v138,v139\nM v[40:43],a[116:119],v[208:211],v[40:43]\nv_cvt_pk_bf16_f32 v202,v152,v153\nv_cvt_pk_bf16_f32 v203,v154,v155\nM v[44:47],a[116:119],v[212:215],v[44:47]\nv_cvt_pk_bf16_f32 v204,v140,v141\nv_cvt_pk_bf16_f32 v205,v142,v143\nv_cvt_pk_bf16_f32 v206,v156,v157\n" \
"M v[48:51],a[120:123],v[208:211],v[48:51]\nbuffer_load_dwordx4 v240,s[48:51],s66 offen offset:0 sc1 lds\nM v[52:55],a[120:123],v[212:215],v[52:55]\nv_cvt_pk_bf16_f32 v207,v158,v159\nM v[56:59],a[124:127],v[208:211],v[56:59]\nM v[60:63],a[124:127],v[212:215],v[60:63]\nM a[192:195],a[208:211],v[208:211],a[192:195]\nbuffer_load_dwordx4 v240,s[48:51],s66 offen offset:1024 sc1 lds\n" \
"buffer_load_dwordx4 v240,s[48:51],s66 offen offset:2048 sc1 lds\nbuffer_load_dwordx4 v240,s[48:51],s66 offen offset:3072 sc1 lds\nM a[196:199],a[208:211],v[212:215],a[196:199]\ns_cmp_lt_u32 s62,2\ns_cbranch_scc1 .Lx01\ns_branch .Ldual\n.Lp23:\ns_cmp_eq_u32 s7,2\n" \
"s_cbranch_scc0 .Lpro3\n.Lpro2:\ns_barrier\nM v[64:67],a[0:3],a[128:131],0\nds_read_b128 a[64:67],v241 offset:0\nds_read_b128 a[68:71],v241 offset:1024\nM v[64:67],a[4:7],a[132:135],v[64:67]\nds_read_b128 a[72:75],v241 offset:2048\n" \
"s_mov_b32 m0,s64\nv_mov_b32 v184,0xff800000\nM v[64:67],a[8:11],a[136:139],v[64:67]\nds_read_b128 a[76:79],v241 offset:3072\nv_mov_b32 v185,0xff800000\nM v[64:67],a[12:15],a[140:143],v[64:67]\nds_read_b128 a[80:83],v241 offset:4096\nbuffer_load_dwordx4 v240,s[44:47],s36 offen offset:0 sc1 lds\n" \
"M v[68:71],a[0:3],a[144:147],0\nds_read_b128 a[0:3],v240 offset:16384\nv_mov_b32 v186,0xff800000\nv_mov_b32 v187,0xff800000\nM v[68:71],a[4:7],a[148:151],v[68:71]\nds_read_b128 a[4:7],v240 offset:17408\nv_max3_f32 v225,v64,v65,v66\nM v[68:71],a[8:11],a[152:155],v[68:71]\n" \
"ds_read_b128 a[8:11],v240 offset:18432\nM v[68:71],a[12:15],a[156:159],v[68:71]\nbuffer_load_dwordx4 v240,s[44:47],s36 offen offset:1024 sc1 lds\nM v[72:75],a[16:19],a[128:131],0\nds_read_b128 a[12:15],v240 offset:19456\nds_read_b128 a[84:87],v241 offset:5120\nM v[72:75],a[20:23],a[132:135],v[72:75]\nds_read_b128 a[88:91],v241 offset:6144\n" \
"M v[72:75],a[24:27],a[136:139],v[72:75]\nds_read_b128 a[92:95],v241 offset:7168\nv_max3_f32 v229,v68,v69,v70\nM v[72:75],a[28:31],a[140:143],v[72:75]\nds_read_b128 a[96:99],v241 offset:8192\nbuffer_load_dwordx4 v240,s[44:47],s36 offen offset:2048 sc1 lds\nM v[76:79],a[16:19],a[144:147],0\nds_read_b128 a[16:19],v240 offset:20480\n" \
"M v[76:79],a[20:23],a[148:151],v[76:79]\nds_read_b128 a[20:23],v240 offset:21504\nM v[76:79],a[24:27],a[152:155],v[76:79]\nds_read_b128 a[24:27],v240 offset:22528\nv_max3_f32 v225,v225,v67,v72\nv_max3_f32 v225,v225,v73,v74\nM v[76:79],a[28:31],a[156:159],v[76:79]\nv_max_f32 v225,v225,v75\n" \
"v_cndmask_b32 v225,v224,v225,s[90:91]\nM v[80:83],a[32:35],a[128:131],0\nds_read_b128 a[28:31],v240 offset:23552\nbuffer_load_dwordx4 v240,s[44:47],s36 offen offset:3072 sc1 lds\nM v[80:83],a[36:39],a[132:135],v[80:83]\nds_read_b128 a[100:103],v241 offset:9216\nds_read_b128 a[104:107],v241 offset:10240\nM v[80:83],a[40:43],a[136:139],v[80:83]\n" \
"v_max3_f32 v229,v229,v71,v76\nv_max3_f32 v229,v229,v77,v78\nv_max_f32 v229,v229,v79\nM v[80:83],a[44:47],a[140:143],v[80:83]\nv_cndmask_b32 v229,v228,v229,s[90:91]\nds_read_b128 a[108:111],v241 offset:11264\nds_read_b128 a[112:115],v241 offset:12288\nM v[84:87],a[32:35],a[144:147],0\n" \
"v_permlane16_swap_b32 v225,v229\nv_max_f32 v225,v225,v229\nM v[84:87],a[36:39],a[148:151],v[84:87]\nv_mov_b32 v229,v225\nds_read_b128 a[116:119],v241 offset:13312\nds_read_b128 a[120:123],v241 offset:14336\nM v[84:87],a[40:43],a[152:155],v[84:87]\nv_permlane32_swap_b32 v225,v229\n" \
"v_max_f32 v225,v225,v229\nM v[84:87],a[44:47],a[156:159],v[84:87]\nv_mov_b32 v229,v225\nds_read_b128 a[124:127],v241 offset:15360\nds_read_b128 a[32:35],v240 offset:40960\nM v[88:91],a[48:51],a[128:131],0\nv_permlane16_swap_b32 v225,v229\nv_sub_f32 v224,0,v225\n" \
"v_sub_f32 v226,0,v225\nM v[88:91],a[52:55],a[132:135],v[88:91]\nv_sub_f32 v227,0,v225\nv_sub_f32 v225,0,v225\nM v[88:91],a[56:59],a[136:139],v[88:91]\nv_sub_f32 v228,0,v229\nv_sub_f32 v230,0,v229\nv_sub_f32 v231,0,v229\n" \
"M v[88:91],a[60:63],a[140:143],v[88:91]\nv_sub_f32 v229,0,v229\nv_add_f32 v64,v64,v224\nM v[92:95],a[48:51],a[144:147],0\nv_add_f32 v65,v65,v224\nv_add_f32 v66,v66,v224\nv_add_f32 v67,v67,v224\nM v[92:95],a[52:55],a[148:151],v[92:95]\n" \
"v_exp_f32 v64,v64\nv_add_f32 v68,v68,v228\nM v[92:95],a[56:59],a[152:155],v[92:95]\nv_exp_f32 v65,v65\nM v[92:95],a[60:63],a[156:159],v[92:95]\nv_exp_f32 v66,v66\ns_waitcnt lgkmcnt(2)\nM v[96:99],a[0:3],a[128:131],v[224:227]\n" \
"v_exp_f32 v67,v67\nv_add_f32 v69,v69,v228\nM v[96:99],a[4:7],a[132:135],v[96:99]\nv_add_f32 v72,v72,v224\nv_add_f32 v73,v73,v224\nM v[96:99],a[8:11],a[136:139],v[96:99]\nv_add_f32 v74,v74,v224\nv_add_f32 v75,v75,v224\n" \
"v_add_f32 v70,v70,v228\nM v[96:99],a[12:15],a[140:143],v[96:99]\nv_add_f32 v71,v71,v228\nv_cvt_pk_bf16_f32 v64,v64,v65\nM v[100:103],a[0:3],a[144:147],v[228:231]\nv_exp_f32 v68,v68\nv_add_f32 v76,v76,v228\nM v[100:103],a[4:7],a[148:151],v[100:103]\n" \
"v_exp_f32 v69,v69\nv_add_f32 v77,v77,v228\nM v[100:103],a[8:11],a[152:155],v[100:103]\nv_exp_f32 v72,v72\nM v[100:103],a[12:15],a[156:159],v[100:103]\nv_exp_f32 v73,v73\nv_add_f32 v78,v78,v228\nM v[108:111],a[16:19],a[144:147],v[228:231]\n" \
"v_exp_f32 v74,v74\nM v[108:111],a[20:23],a[148:151],v[108:111]\nv_exp_f32 v75,v75\nv_add_f32 v79,v79,v228\nM v[108:111],a[24:27],a[152:155],v[108:111]\nv_exp_f32 v70,v70\nv_exp_f32 v71,v71\nv_cvt_pk_bf16_f32 v65,v66,v67\n" \
"v_exp_f32 v76,v76\nv_exp_f32 v77,v77\nv_exp_f32 v78,v78\nv_exp_f32 v79,v79\nv_cvt_pk_bf16_f32 v66,v72,v73\nv_cvt_pk_bf16_f32 v67,v74,v75\nv_cvt_pk_bf16_f32 v68,v68,v69\nM v[108:111],a[28:31],a[156:159],v[108:111]\n" \
"v_cvt_pk_bf16_f32 v69,v70,v71\nv_cvt_pk_bf16_f32 v70,v76,v77\nv_cvt_pk_bf16_f32 v71,v78,v79\nM v[0:3],a[64:67],v[64:67],0\nv_add_f32 v80,v80,v224\nv_add_f32 v81,v81,v224\nv_add_f32 v82,v82,v224\nM v[4:7],a[64:67],v[68:71],0\n" \
"v_add_f32 v83,v83,v224\nv_add_f32 v84,v84,v228\nM v[8:11],a[68:71],v[64:67],0\nv_exp_f32 v80,v80\nv_add_f32 v85,v85,v228\nM v[12:15],a[68:71],v[68:71],0\nv_exp_f32 v81,v81\nv_add_f32 v88,v88,v224\n" \
"M v[16:19],a[72:75],v[64:67],0\nv_exp_f32 v82,v82\nM v[20:23],a[72:75],v[68:71],0\nv_exp_f32 v83,v83\nv_add_f32 v89,v89,v224\nM v[24:27],a[76:79],v[64:67],0\nv_add_f32 v90,v90,v224\nv_add_f32 v91,v91,v224\n" \
"M v[28:31],a[76:79],v[68:71],0\nv_add_f32 v86,v86,v228\nv_add_f32 v87,v87,v228\nv_cvt_pk_bf16_f32 v80,v80,v81\nM v[32:35],a[80:83],v[64:67],0\nv_exp_f32 v84,v84\nv_add_f32 v92,v92,v228\nM v[36:39],a[80:83],v[68:71],0\n" \
"v_exp_f32 v85,v85\nM v[40:43],a[84:87],v[64:67],0\nv_exp_f32 v88,v88\nv_add_f32 v93,v93,v228\nM v[44:47],a[84:87],v[68:71],0\nv_exp_f32 v89,v89\nM v[48:51],a[88:91],v[64:67],0\nv_exp_f32 v90,v90\n" \
"v_add_f32 v94,v94,v228\nM v[52:55],a[88:91],v[68:71],0\nv_exp_f32 v91,v91\nv_add_f32 v95,v95,v228\nM v[56:59],a[92:95],v[64:67],0\nv_exp_f32 v86,v86\nM v[60:63],a[92:95],v[68:71],0\nv_exp_f32 v87,v87\n" \
"v_cvt_pk_bf16_f32 v81,v82,v83\nM a[192:195],a[208:211],v[64:67],0\nv_exp_f32 v92,v92\nv_exp_f32 v93,v93\nv_exp_f32 v94,v94\nv_exp_f32 v95,v95\nv_cvt_pk_bf16_f32 v82,v88,v89\nv_cvt_pk_bf16_f32 v83,v90,v91\n" \
"v_cvt_pk_bf16_f32 v84,v84,v85\nM a[196:199],a[208:211],v[68:71],0\nv_cvt_pk_bf16_f32 v85,v86,v87\nv_cvt_pk_bf16_f32 v86,v92,v93\nv_cvt_pk_bf16_f32 v87,v94,v95\nM v[0:3],a[96:99],v[80:83],v[0:3]\nds_read_b128 a[0:3],v240 offset:32768\nds_read_b128 a[4:7],v240 offset:33792\n" \
"v_cndmask_b32 v96,v96,v252,s[82:83]\nM v[4:7],a[96:99],v[84:87],v[4:7]\nds_read_b128 a[8:11],v240 offset:34816\nv_cndmask_b32 v97,v97,v252,s[84:85]\nM v[8:11],a[100:103],v[80:83],v[8:11]\nds_read_b128 a[12:15],v240 offset:35840\nv_cndmask_b32 v98,v98,v252,s[86:87]\nv_cndmask_b32 v99,v99,v252,s[88:89]\n" \
"M v[12:15],a[100:103],v[84:87],v[12:15]\nds_read_b128 a[16:19],v240 offset:36864\nv_exp_f32 v96,v96\nM v[16:19],a[104:107],v[80:83],v[16:19]\nds_read_b128 a[20:23],v240 offset:37888\nv_cndmask_b32 v108,v108,v252,s[82:83]\nM v[20:23],a[104:107],v[84:87],v[20:23]\nds_read_b128 a[24:27],v240 offset:38912\n" \
"v_exp_f32 v97,v97\nM v[24:27],a[108:111],v[80:83],v[24:27]\nds_read_b128 a[28:31],v240 offset:39936\nv_cvt_pk_bf16_f32 v96,v96,v97\nM v[28:31],a[108:111],v[84:87],v[28:31]\nds_read_b128 a[36:39],v240 offset:41984\nv_exp_f32 v98,v98\nM v[32:35],a[112:115],v[80:83],v[32:35]\n" \
"ds_read_b128 a[40:43],v240 offset:43008\nv_exp_f32 v99,v99\nM v[36:39],a[112:115],v[84:87],v[36:39]\nds_read_b128 a[44:47],v240 offset:44032\nv_cndmask_b32 v109,v109,v252,s[84:85]\nM v[40:43],a[116:119],v[80:83],v[40:43]\nds_read_b128 a[48:51],v240 offset:45056\nv_exp_f32 v100,v100\n" \
"M v[44:47],a[116:119],v[84:87],v[44:47]\nds_read_b128 a[52:55],v240 offset:46080\nv_cndmask_b32 v110,v110,v252,s[86:87]\nM v[48:51],a[120:123],v[80:83],v[48:51]\nbuffer_load_dword v248,v251,s[52:55],s80 offen sc1\nds_read_b128 a[56:59],v240 offset:47104\nM v[52:55],a[120:123],v[84:87],v[52:55]\nds_read_b128 a[60:63],v240 offset:48128\n" \
"v_cndmask_b32 v111,v111,v252,s[88:89]\ns_waitcnt lgkmcnt(9)\nM v[56:59],a[124:127],v[80:83],v[56:59]\nds_read_b128 a[64:67],v241 offset:16384\nv_cvt_pk_bf16_f32 v97,v98,v99\nM v[60:63],a[124:127],v[84:87],v[60:63]\nds_read_b128 a[68:71],v241 offset:17408\nv_exp_f32 v101,v101\n" \
"M a[192:195],a[208:211],v[80:83],a[192:195]\nds_read_b128 a[72:75],v241 offset:18432\nv_mov_b32 v98,0\nM a[196:199],a[208:211],v[84:87],a[196:199]\nds_read_b128 a[76:79],v241 offset:19456\nv_exp_f32 v102,v102\nM v[128:131],a[0:3],a[128:131],v[224:227]\nds_read_b128 a[80:83],v241 offset:20480\n" \
"v_exp_f32 v103,v103\nM v[128:131],a[4:7],a[132:135],v[128:131]\nv_exp_f32 v108,v108\nM v[128:131],a[8:11],a[136:139],v[128:131]\nv_exp_f32 v109,v109\nv_mov_b32 v99,0\nM v[128:131],a[12:15],a[140:143],v[128:131]\nv_exp_f32 v110,v110\n" \
"M v[132:135],a[0:3],a[144:147],v[228:231]\nv_exp_f32 v111,v111\nv_cvt_pk_bf16_f32 v100,v100,v101\nM v[132:135],a[4:7],a[148:151],v[132:135]\nv_cvt_pk_bf16_f32 v101,v102,v103\nv_cvt_pk_bf16_f32 v102,v108,v109\nv_cvt_pk_bf16_f32 v103,v110,v111\nM v[132:135],a[8:11],a[152:155],v[132:135]\n" \
"ds_read_b128 a[84:87],v241 offset:21504\nds_read_b128 a[88:91],v241 offset:22528\nM v[132:135],a[12:15],a[156:159],v[132:135]\nds_read_b128 a[92:95],v241 offset:23552\nv_exp_f32 v128,v128\nM v[144:147],a[16:19],a[128:131],v[224:227]\nds_read_b128 a[0:3],v240 offset:49152\nv_exp_f32 v129,v129\n" \
"M v[144:147],a[20:23],a[132:135],v[144:147]\nds_read_b128 a[4:7],v240 offset:50176\ns_waitcnt lgkmcnt(5)\nM v[144:147],a[24:27],a[136:139],v[144:147]\nds_read_b128 a[8:11],v240 offset:51200\nv_exp_f32 v130,v130\nM v[144:147],a[28:31],a[140:143],v[144:147]\nds_read_b128 a[12:15],v240 offset:52224\n" \
"v_cvt_pk_bf16_f32 v192,v128,v129\nM v[148:151],a[16:19],a[144:147],v[228:231]\nds_read_b128 a[16:19],v240 offset:53248\nv_exp_f32 v131,v131\nM v[148:151],a[20:23],a[148:151],v[148:151]\nds_read_b128 a[20:23],v240 offset:54272\nv_exp_f32 v132,v132\nM v[148:151],a[24:27],a[152:155],v[148:151]\n" \
"ds_read_b128 a[24:27],v240 offset:55296\nv_cvt_pk_bf16_f32 v193,v130,v131\nM v[148:151],a[28:31],a[156:159],v[148:151]\nds_read_b128 a[28:31],v240 offset:56320\nv_exp_f32 v133,v133\nM v[160:163],a[32:35],a[128:131],v[224:227]\nv_exp_f32 v134,v134\nM v[160:163],a[36:39],a[132:135],v[160:163]\n" \
"v_exp_f32 v135,v135\nv_cvt_pk_bf16_f32 v196,v132,v133\nM v[160:163],a[40:43],a[136:139],v[160:163]\nv_cvt_pk_bf16_f32 v197,v134,v135\nv_exp_f32 v144,v144\nM v[160:163],a[44:47],a[140:143],v[160:163]\nv_exp_f32 v145,v145\nM v[164:167],a[32:35],a[144:147],v[228:231]\n" \
"ds_read_b128 a[32:35],v240 offset:57344\nv_exp_f32 v146,v146\nM v[164:167],a[36:39],a[148:151],v[164:167]\nv_exp_f32 v147,v147\nM v[164:167],a[40:43],a[152:155],v[164:167]\nds_read_b128 a[36:39],v240 offset:58368\nv_cvt_pk_bf16_f32 v194,v144,v145\nv_cvt_pk_bf16_f32 v195,v146,v147\n" \
"M v[164:167],a[44:47],a[156:159],v[164:167]\nds_read_b128 a[40:43],v240 offset:59392\nv_exp_f32 v148,v148\nM v[176:179],a[48:51],a[128:131],v[224:227]\nds_read_b128 a[44:47],v240 offset:60416\nds_read_b128 a[96:99],v241 offset:40960\nM v[176:179],a[52:55],a[132:135],v[176:179]\nv_exp_f32 v149,v149\n" \
"ds_read_b128 a[100:103],v241 offset:41984\nM v[176:179],a[56:59],a[136:139],v[176:179]\nv_exp_f32 v150,v150\nM v[176:179],a[60:63],a[140:143],v[176:179]\nv_exp_f32 v151,v151\nv_cvt_pk_bf16_f32 v198,v148,v149\nM v[180:183],a[48:51],a[144:147],v[228:231]\nv_cvt_pk_bf16_f32 v199,v150,v151\n" \
"ds_read_b128 a[48:51],v240 offset:61440\nds_read_b128 a[104:107],v241 offset:43008\nM v[180:183],a[52:55],a[148:151],v[180:183]\nv_exp_f32 v160,v160\nM v[180:183],a[56:59],a[152:155],v[180:183]\nv_exp_f32 v161,v161\nds_read_b128 a[52:55],v240 offset:62464\nM v[180:183],a[60:63],a[156:159],v[180:183]\n" \
"v_exp_f32 v162,v162\nM v[0:3],a[64:67],v[96:99],v[0:3]\nv_exp_f32 v163,v163\nv_cvt_pk_bf16_f32 v208,v160,v161\nM v[4:7],a[64:67],v[100:103],v[4:7]\nv_cvt_pk_bf16_f32 v209,v162,v163\nds_read_b128 a[56:59],v240 offset:63488\nds_read_b128 a[60:63],v240 offset:64512\n" \
"M v[8:11],a[68:71],v[96:99],v[8:11]\nv_exp_f32 v164,v164\nM v[12:15],a[68:71],v[100:103],v[12:15]\nv_exp_f32 v165,v165\nds_read_b128 a[64:67],v241 offset:32768\nM v[16:19],a[72:75],v[96:99],v[16:19]\nv_exp_f32 v166,v166\nv_cvt_pk_bf16_f32 v212,v164,v165\n" \
"M v[20:23],a[72:75],v[100:103],v[20:23]\nv_exp_f32 v167,v167\nM v[24:27],a[76:79],v[96:99],v[24:27]\nv_cvt_pk_bf16_f32 v213,v166,v167\nv_exp_f32 v176,v176\nM v[28:31],a[76:79],v[100:103],v[28:31]\nv_exp_f32 v177,v177\nM v[32:35],a[80:83],v[96:99],v[32:35]\n" \
"v_exp_f32 v178,v178\nv_cvt_pk_bf16_f32 v210,v176,v177\nM v[36:39],a[80:83],v[100:103],v[36:39]\nv_exp_f32 v179,v179\ns_waitcnt lgkmcnt(3)\nM v[40:43],a[84:87],v[96:99],v[40:43]\nv_cvt_pk_bf16_f32 v211,v178,v179\nds_read_b128 a[68:71],v241 offset:33792\n" \
"M v[44:47],a[84:87],v[100:103],v[44:47]\nv_exp_f32 v180,v180\nds_read_b128 a[72:75],v241 offset:34816\nM v[48:51],a[88:91],v[96:99],v[48:51]\ns_waitcnt vmcnt(1)\ns_barrier\nM v[52:55],a[88:91],v[100:103],v[52:55]\nv_exp_f32 v181,v181\n" \
"ds_read_b128 a[76:79],v241 offset:35840\nM v[56:59],a[92:95],v[96:99],v[56:59]\nv_exp_f32 v182,v182\nv_cvt_pk_bf16_f32 v214,v180,v181\nM v[60:63],a[92:95],v[100:103],v[60:63]\nv_exp_f32 v183,v183\nM a[192:195],a[208:211],v[96:99],a[192:195]\nv_cvt_pk_bf16_f32 v215,v182,v183\n" \
"ds_read_b128 a[80:83],v241 offset:36864\nds_read_b128 a[84:87],v241 offset:37888\nM a[196:199],a[208:211],v[100:103],a[196:199]\nds_read_b128 a[88:91],v241 offset:38912\ns_waitcnt vmcnt(0)\nv_cmp_eq_u32 vcc,0,v248\nM v[64:67],a[244:247],a[244:247],0\nds_read_b128 a[92:95],v241 offset:39936\n" \
"s_cbranch_vccnz .Lps4\n.Lpb4:\ns_add_u32 m0,s64,65536\nM v[68:71],a[244:247],a[244:247],0\nds_read_b128 a[108:111],v241 offset:44032\nbuffer_load_dwordx4 v240,s[48:51],s36 offen offset:0 sc1 lds\nM v[72:75],a[244:247],a[244:247],0\nds_read_b128 a[112:115],v241 offset:45056\n" \
"M v[76:79],a[244:247],a[244:247],0\nds_read_b128 a[116:119],v241 offset:46080\nM v[80:83],a[244:247],a[244:247],0\nds_read_b128 a[120:123],v241 offset:47104\nM v[84:87],a[244:247],a[244:247],0\nds_read_b128 a[124:127],v241 offset:48128\nbuffer_load_dwordx4 v240,s[48:51],s36 offen offset:1024 sc1 lds\nM v[88:91],a[244:247],a[244:247],0\n" \
"M v[92:95],a[244:247],a[244:247],0\nM v[96:99],a[244:247],a[244:247],0\nM v[100:103],a[244:247],a[244:247],0\nbuffer_load_dwordx4 v240,s[48:51],s36 offen offset:2048 sc1 lds\nM v[104:107],a[244:247],a[244:247],0\nM v[108:111],a[244:247],a[244:247],0\nM v[112:115],a[244:247],a[244:247],0\nM v[116:119],a[244:247],a[244:247],0\n" \
"buffer_load_dwordx4 v240,s[48:51],s36 offen offset:3072 sc1 lds\ns_add_u32 m0,s64,16384\nM v[120:123],a[244:247],a[244:247],0\nM v[124:127],a[244:247],a[244:247],0\nM a[200:203],a[244:247],a[244:247],0\nM a[204:207],a[244:247],a[244:247],0\nbuffer_load_dwordx4 v240,s[44:47],s65 offen offset:0 sc1 lds\nM v[136:139],a[0:3],a[160:163],0\n" \
"M v[136:139],a[4:7],a[164:167],v[136:139]\nM v[136:139],a[8:11],a[168:171],v[136:139]\nM v[136:139],a[12:15],a[172:175],v[136:139]\nbuffer_load_dwordx4 v240,s[44:47],s65 offen offset:1024 sc1 lds\nM v[140:143],a[0:3],a[176:179],0\nM v[140:143],a[4:7],a[180:183],v[140:143]\nM v[140:143],a[8:11],a[184:187],v[140:143]\nM v[140:143],a[12:15],a[188:191],v[140:143]\n" \
"buffer_load_dwordx4 v240,s[44:47],s65 offen offset:2048 sc1 lds\nM v[152:155],a[16:19],a[160:163],0\nM v[152:155],a[20:23],a[164:167],v[152:155]\nv_max3_f32 v233,v136,v137,v138\nM v[152:155],a[24:27],a[168:171],v[152:155]\nM v[152:155],a[28:31],a[172:175],v[152:155]\nbuffer_load_dwordx4 v240,s[44:47],s65 offen offset:3072 sc1 lds\ns_add_u32 m0,s64,81920\n" \
"M v[156:159],a[16:19],a[176:179],0\nv_max3_f32 v237,v140,v141,v142\nM v[156:159],a[20:23],a[180:183],v[156:159]\nM v[156:159],a[24:27],a[184:187],v[156:159]\nM v[156:159],a[28:31],a[188:191],v[156:159]\nbuffer_load_dwordx4 v240,s[48:51],s65 offen offset:0 sc1 lds\nv_max3_f32 v233,v233,v139,v152\nM v[128:131],a[0:3],a[128:131],v[224:227]\n" \
"v_max3_f32 v233,v233,v153,v154\nv_max_f32 v233,v233,v155\nv_cndmask_b32 v233,v232,v233,s[90:91]\nM v[128:131],a[4:7],a[132:135],v[128:131]\nM v[128:131],a[8:11],a[136:139],v[128:131]\nv_max3_f32 v237,v237,v143,v156\nv_max3_f32 v237,v237,v157,v158\nv_max_f32 v237,v237,v159\n" \
"M v[128:131],a[12:15],a[140:143],v[128:131]\nbuffer_load_dwordx4 v240,s[48:51],s65 offen offset:1024 sc1 lds\nM v[132:135],a[0:3],a[144:147],v[228:231]\nv_cndmask_b32 v237,v236,v237,s[90:91]\nds_read_b128 a[0:3],v240 offset:0\nM v[132:135],a[4:7],a[148:151],v[132:135]\nv_permlane16_swap_b32 v233,v237\nv_max_f32 v233,v233,v237\n" \
"v_mov_b32 v237,v233\nM v[132:135],a[8:11],a[152:155],v[132:135]\nds_read_b128 a[4:7],v240 offset:1024\nv_permlane32_swap_b32 v233,v237\nM v[132:135],a[12:15],a[156:159],v[132:135]\nv_max_f32 v233,v233,v237\nv_mov_b32 v237,v233\nds_read_b128 a[8:11],v240 offset:2048\n" \
"M v[144:147],a[16:19],a[128:131],v[224:227]\nv_permlane16_swap_b32 v233,v237\nv_sub_f32 v232,0,v233\nv_sub_f32 v234,0,v233\nM v[144:147],a[20:23],a[132:135],v[144:147]\nv_sub_f32 v235,0,v233\nv_sub_f32 v233,0,v233\nM v[144:147],a[24:27],a[136:139],v[144:147]\n" \
"v_sub_f32 v236,0,v237\nv_sub_f32 v238,0,v237\nv_sub_f32 v239,0,v237\nM v[144:147],a[28:31],a[140:143],v[144:147]\nv_sub_f32 v237,0,v237\nds_read_b128 a[12:15],v240 offset:3072\nM v[148:151],a[16:19],a[144:147],v[228:231]\nds_read_b128 a[16:19],v240 offset:4096\n" \
"buffer_load_dwordx4 v240,s[48:51],s65 offen offset:2048 sc1 lds\nM v[148:151],a[20:23],a[148:151],v[148:151]\nds_read_b128 a[20:23],v240 offset:5120\nv_add_f32 v136,v136,v232\nv_add_f32 v137,v137,v232\nM v[148:151],a[24:27],a[152:155],v[148:151]\nds_read_b128 a[24:27],v240 offset:6144\nv_add_f32 v138,v138,v232\n" \
"M v[148:151],a[28:31],a[156:159],v[148:151]\nds_read_b128 a[28:31],v240 offset:7168\nv_add_f32 v139,v139,v232\nv_add_f32 v140,v140,v236\nM v[160:163],a[32:35],a[128:131],v[224:227]\nbuffer_load_dwordx4 v240,s[48:51],s65 offen offset:3072 sc1 lds\nM v[160:163],a[36:39],a[132:135],v[160:163]\ns_add_u32 m0,s64,32768\n" \
"v_add_f32 v141,v141,v236\nv_add_f32 v142,v142,v236\nM v[160:163],a[40:43],a[136:139],v[160:163]\nv_add_f32 v143,v143,v236\nv_add_f32 v152,v152,v232\nv_add_f32 v153,v153,v232\nM v[160:163],a[44:47],a[140:143],v[160:163]\nv_add_f32 v154,v154,v232\n" \
"v_add_f32 v155,v155,v232\nM v[164:167],a[32:35],a[144:147],v[228:231]\nv_add_f32 v156,v156,v236\nv_add_f32 v157,v157,v236\nv_add_f32 v158,v158,v236\nM v[164:167],a[36:39],a[148:151],v[164:167]\nbuffer_load_dwordx4 v240,s[44:47],s66 offen offset:0 sc1 lds\nM v[164:167],a[40:43],a[152:155],v[164:167]\n" \
"v_add_f32 v159,v159,v236\nv_exp_f32 v136,v136\nM v[164:167],a[44:47],a[156:159],v[164:167]\nv_exp_f32 v137,v137\nM v[176:179],a[48:51],a[128:131],v[224:227]\nv_exp_f32 v138,v138\nM v[176:179],a[52:55],a[132:135],v[176:179]\nbuffer_load_dwordx4 v240,s[44:47],s66 offen offset:1024 sc1 lds\n" \
"s_waitcnt lgkmcnt(0)\nM v[176:179],a[56:59],a[136:139],v[176:179]\nv_exp_f32 v139,v139\nM v[176:179],a[60:63],a[140:143],v[176:179]\nv_exp_f32 v140,v140\nv_cvt_pk_bf16_f32 v200,v136,v137\nM v[180:183],a[48:51],a[144:147],v[228:231]\nv_exp_f32 v141,v141\n" \
"v_cvt_pk_bf16_f32 v201,v138,v139\nM v[180:183],a[52:55],a[148:151],v[180:183]\nbuffer_load_dwordx4 v240,s[44:47],s66 offen offset:2048 sc1 lds\nM v[180:183],a[56:59],a[152:155],v[180:183]\nv_exp_f32 v142,v142\nv_cvt_pk_bf16_f32 v204,v140,v141\nM v[180:183],a[60:63],a[156:159],v[180:183]\nv_exp_f32 v143,v143\n" \
"M v[168:171],a[32:35],a[160:163],v[232:235]\ns_barrier\nv_exp_f32 v152,v152\nM v[168:171],a[36:39],a[164:167],v[168:171]\nbuffer_load_dwordx4 v240,s[44:47],s66 offen offset:3072 sc1 lds\ns_add_u32 m0,s64,98304\nM v[168:171],a[40:43],a[168:171],v[168:171]\nv_exp_f32 v153,v153\n" \
"M v[168:171],a[44:47],a[172:175],v[168:171]\nv_exp_f32 v154,v154\nv_cvt_pk_bf16_f32 v202,v152,v153\nM v[172:175],a[32:35],a[176:179],v[236:239]\nds_read_b128 a[32:35],v240 offset:8192\nv_exp_f32 v155,v155\nM v[172:175],a[36:39],a[180:183],v[172:175]\nds_read_b128 a[36:39],v240 offset:9216\n" \
"v_cvt_pk_bf16_f32 v203,v154,v155\nM v[172:175],a[40:43],a[184:187],v[172:175]\nds_read_b128 a[40:43],v240 offset:10240\nv_exp_f32 v156,v156\nM v[172:175],a[44:47],a[188:191],v[172:175]\nds_read_b128 a[44:47],v240 offset:11264\nv_cndmask_b32 v168,v168,v252,s[82:83]\nM v[188:191],a[48:51],a[176:179],v[236:239]\n" \
"ds_read_b128 a[48:51],v240 offset:12288\nv_exp_f32 v157,v157\nM v[188:191],a[52:55],a[180:183],v[188:191]\nds_read_b128 a[52:55],v240 offset:13312\nv_exp_f32 v158,v158\nM v[188:191],a[56:59],a[184:187],v[188:191]\nds_read_b128 a[56:59],v240 offset:14336\nv_cndmask_b32 v169,v169,v252,s[84:85]\n" \
"M v[188:191],a[60:63],a[188:191],v[188:191]\nds_read_b128 a[60:63],v240 offset:15360\nv_exp_f32 v159,v159\nM v[0:3],a[64:67],v[192:195],v[0:3]\nv_exp_f32 v128,v128\nM v[4:7],a[64:67],v[196:199],v[4:7]\nv_exp_f32 v129,v129\nv_cndmask_b32 v170,v170,v252,s[86:87]\n" \
"M v[8:11],a[68:71],v[192:195],v[8:11]\nv_exp_f32 v130,v130\nv_cndmask_b32 v171,v171,v252,s[88:89]\nM v[12:15],a[68:71],v[196:199],v[12:15]\nv_exp_f32 v131,v131\nM v[16:19],a[72:75],v[192:195],v[16:19]\nv_exp_f32 v132,v132\nv_cndmask_b32 v188,v188,v252,s[82:83]\n" \
"M v[20:23],a[72:75],v[196:199],v[20:23]\nv_exp_f32 v133,v133\nM v[24:27],a[76:79],v[192:195],v[24:27]\nv_exp_f32 v134,v134\nv_cndmask_b32 v189,v189,v252,s[84:85]\nM v[28:31],a[76:79],v[196:199],v[28:31]\nv_exp_f32 v135,v135\nv_cndmask_b32 v190,v190,v252,s[86:87]\n" \
"M v[32:35],a[80:83],v[192:195],v[32:35]\nv_exp_f32 v144,v144\nM v[36:39],a[80:83],v[196:199],v[36:39]\nv_exp_f32 v145,v145\nv_cndmask_b32 v191,v191,v252,s[88:89]\nM v[40:43],a[84:87],v[192:195],v[40:43]\nv_exp_f32 v146,v146\nM v[44:47],a[84:87],v[196:199],v[44:47]\n" \
"v_exp_f32 v147,v147\nv_cvt_pk_bf16_f32 v205,v142,v143\nM v[48:51],a[88:91],v[192:195],v[48:51]\nv_exp_f32 v148,v148\nv_cvt_pk_bf16_f32 v206,v156,v157\nM v[52:55],a[88:91],v[196:199],v[52:55]\nv_exp_f32 v149,v149\nM v[56:59],a[92:95],v[192:195],v[56:59]\n" \
"v_exp_f32 v150,v150\nv_cvt_pk_bf16_f32 v207,v158,v159\nM v[60:63],a[92:95],v[196:199],v[60:63]\nv_exp_f32 v151,v151\nM a[192:195],a[208:211],v[192:195],a[192:195]\nv_exp_f32 v160,v160\nv_cvt_pk_bf16_f32 v192,v128,v129\nM a[196:199],a[208:211],v[196:199],a[196:199]\n" \
"v_exp_f32 v161,v161\nv_cvt_pk_bf16_f32 v193,v130,v131\nM v[0:3],a[96:99],v[208:211],v[0:3]\nv_exp_f32 v162,v162\nM v[4:7],a[96:99],v[212:215],v[4:7]\nv_exp_f32 v163,v163\nv_cvt_pk_bf16_f32 v194,v144,v145\nM v[8:11],a[100:103],v[208:211],v[8:11]\n" \
"v_exp_f32 v164,v164\nv_cvt_pk_bf16_f32 v195,v146,v147\nM v[12:15],a[100:103],v[212:215],v[12:15]\nv_exp_f32 v165,v165\nM v[16:19],a[104:107],v[208:211],v[16:19]\nv_exp_f32 v166,v166\nv_cvt_pk_bf16_f32 v196,v132,v133\nM v[20:23],a[104:107],v[212:215],v[20:23]\n" \
"v_exp_f32 v167,v167\nM v[24:27],a[108:111],v[208:211],v[24:27]\nv_exp_f32 v176,v176\nv_cvt_pk_bf16_f32 v197,v134,v135\nM v[28:31],a[108:111],v[212:215],v[28:31]\nv_exp_f32 v177,v177\nv_cvt_pk_bf16_f32 v198,v148,v149\nM v[32:35],a[112:115],v[208:211],v[32:35]\n" \
"v_exp_f32 v178,v178\nM v[36:39],a[112:115],v[212:215],v[36:39]\nv_exp_f32 v179,v179\nv_cvt_pk_bf16_f32 v199,v150,v151\nM v[40:43],a[116:119],v[208:211],v[40:43]\nv_exp_f32 v180,v180\nM v[44:47],a[116:119],v[212:215],v[44:47]\nv_exp_f32 v181,v181\n" \
"M v[48:51],a[120:123],v[208:211],v[48:51]\nv_exp_f32 v182,v182\nM v[52:55],a[120:123],v[212:215],v[52:55]\nv_exp_f32 v183,v183\nM v[56:59],a[124:127],v[208:211],v[56:59]\nv_exp_f32 v168,v168\nM v[60:63],a[124:127],v[212:215],v[60:63]\nv_exp_f32 v169,v169\n" \
"M a[192:195],a[208:211],v[208:211],a[192:195]\nv_exp_f32 v170,v170\nv_exp_f32 v171,v171\nbuffer_load_dwordx4 v240,s[48:51],s66 offen offset:0 sc1 lds\nbuffer_load_dwordx4 v240,s[48:51],s66 offen offset:1024 sc1 lds\nbuffer_load_dwordx4 v240,s[48:51],s66 offen offset:2048 sc1 lds\nbuffer_load_dwordx4 v240,s[48:51],s66 offen offset:3072 sc1 lds\nM a[196:199],a[208:211],v[212:215],a[196:199]\n" \
"s_cmp_lt_u32 s62,2\ns_cbranch_scc1 .Lx01\ns_branch .Ldual\n.Lpro3:\ns_barrier\nM v[64:67],a[0:3],a[128:131],0\nds_read_b128 a[64:67],v241 offset:0\nds_read_b128 a[68:71],v241 offset:1024\n" \
"M v[64:67],a[4:7],a[132:135],v[64:67]\nds_read_b128 a[72:75],v241 offset:2048\ns_mov_b32 m0,s64\nv_mov_b32 v168,0\nM v[64:67],a[8:11],a[136:139],v[64:67]\nds_read_b128 a[76:79],v241 offset:3072\nv_mov_b32 v169,0\nM v[64:67],a[12:15],a[140:143],v[64:67]\n" \
"ds_read_b128 a[80:83],v241 offset:4096\nbuffer_load_dwordx4 v240,s[44:47],s36 offen offset:0 sc1 lds\nM v[68:71],a[0:3],a[144:147],0\nds_read_b128 a[0:3],v240 offset:16384\nv_mov_b32 v170,0\nv_mov_b32 v171,0\nM v[68:71],a[4:7],a[148:151],v[68:71]\nds_read_b128 a[4:7],v240 offset:17408\n" \
"v_max3_f32 v225,v64,v65,v66\nM v[68:71],a[8:11],a[152:155],v[68:71]\nds_read_b128 a[8:11],v240 offset:18432\nv_mov_b32 v172,0xff800000\nv_mov_b32 v173,0xff800000\nM v[68:71],a[12:15],a[156:159],v[68:71]\nds_read_b128 a[12:15],v240 offset:19456\nv_mov_b32 v174,0xff800000\n" \
"M v[72:75],a[16:19],a[128:131],0\nds_read_b128 a[84:87],v241 offset:5120\nbuffer_load_dwordx4 v240,s[44:47],s36 offen offset:1024 sc1 lds\nM v[72:75],a[20:23],a[132:135],v[72:75]\nds_read_b128 a[88:91],v241 offset:6144\nv_mov_b32 v175,0xff800000\nv_max3_f32 v229,v68,v69,v70\nM v[72:75],a[24:27],a[136:139],v[72:75]\n" \
"ds_read_b128 a[92:95],v241 offset:7168\nv_mov_b32 v184,0xff800000\nM v[72:75],a[28:31],a[140:143],v[72:75]\nds_read_b128 a[96:99],v241 offset:8192\nv_mov_b32 v185,0xff800000\nv_mov_b32 v186,0xff800000\nM v[76:79],a[16:19],a[144:147],0\nds_read_b128 a[16:19],v240 offset:20480\n" \
"buffer_load_dwordx4 v240,s[44:47],s36 offen offset:2048 sc1 lds\nM v[76:79],a[20:23],a[148:151],v[76:79]\nds_read_b128 a[20:23],v240 offset:21504\nv_max3_f32 v225,v225,v67,v72\nM v[76:79],a[24:27],a[152:155],v[76:79]\nv_max3_f32 v225,v225,v73,v74\nv_max_f32 v225,v225,v75\nv_cndmask_b32 v225,v224,v225,s[90:91]\n" \
"M v[76:79],a[28:31],a[156:159],v[76:79]\nds_read_b128 a[24:27],v240 offset:22528\nds_read_b128 a[28:31],v240 offset:23552\nM v[80:83],a[32:35],a[128:131],0\nds_read_b128 a[100:103],v241 offset:9216\nbuffer_load_dwordx4 v240,s[44:47],s36 offen offset:3072 sc1 lds\nM v[80:83],a[36:39],a[132:135],v[80:83]\nds_read_b128 a[104:107],v241 offset:10240\n" \
"v_mov_b32 v187,0xff800000\nv_max3_f32 v229,v229,v71,v76\nM v[80:83],a[40:43],a[136:139],v[80:83]\nv_max3_f32 v229,v229,v77,v78\nv_max_f32 v229,v229,v79\nM v[80:83],a[44:47],a[140:143],v[80:83]\nv_cndmask_b32 v229,v228,v229,s[90:91]\nds_read_b128 a[108:111],v241 offset:11264\n" \
"ds_read_b128 a[112:115],v241 offset:12288\nM v[84:87],a[32:35],a[144:147],0\nv_permlane16_swap_b32 v225,v229\nv_max_f32 v225,v225,v229\nM v[84:87],a[36:39],a[148:151],v[84:87]\nv_mov_b32 v229,v225\nds_read_b128 a[32:35],v240 offset:24576\nds_read_b128 a[36:39],v240 offset:25600\n" \
"M v[84:87],a[40:43],a[152:155],v[84:87]\nv_permlane32_swap_b32 v225,v229\nv_max_f32 v225,v225,v229\nv_mov_b32 v229,v225\nM v[84:87],a[44:47],a[156:159],v[84:87]\nds_read_b128 a[40:43],v240 offset:26624\nv_permlane16_swap_b32 v225,v229\nM v[88:91],a[48:51],a[128:131],0\n" \
"v_sub_f32 v224,0,v225\nv_sub_f32 v226,0,v225\nv_sub_f32 v227,0,v225\nM v[88:91],a[52:55],a[132:135],v[88:91]\nv_sub_f32 v225,0,v225\nv_sub_f32 v228,0,v229\nv_sub_f32 v230,0,v229\nM v[88:91],a[56:59],a[136:139],v[88:91]\n" \
"v_sub_f32 v231,0,v229\nv_sub_f32 v229,0,v229\nM v[88:91],a[60:63],a[140:143],v[88:91]\nds_read_b128 a[44:47],v240 offset:27648\nv_add_f32 v64,v64,v224\nv_add_f32 v65,v65,v224\nM v[92:95],a[48:51],a[144:147],0\nds_read_b128 a[48:51],v240 offset:28672\n" \
"v_add_f32 v66,v66,v224\nM v[92:95],a[52:55],a[148:151],v[92:95]\nds_read_b128 a[52:55],v240 offset:29696\nv_add_f32 v67,v67,v224\nv_add_f32 v68,v68,v228\nM v[92:95],a[56:59],a[152:155],v[92:95]\nds_read_b128 a[56:59],v240 offset:30720\nv_exp_f32 v64,v64\n" \
"M v[92:95],a[60:63],a[156:159],v[92:95]\nds_read_b128 a[60:63],v240 offset:31744\ns_waitcnt lgkmcnt(6)\nM v[96:99],a[0:3],a[128:131],v[224:227]\nv_exp_f32 v65,v65\nv_add_f32 v69,v69,v228\nM v[96:99],a[4:7],a[132:135],v[96:99]\nv_exp_f32 v66,v66\n" \
"M v[96:99],a[8:11],a[136:139],v[96:99]\nv_exp_f32 v67,v67\nv_add_f32 v72,v72,v224\nM v[96:99],a[12:15],a[140:143],v[96:99]\nv_add_f32 v73,v73,v224\nv_add_f32 v74,v74,v224\nv_add_f32 v75,v75,v224\nM v[100:103],a[0:3],a[144:147],v[228:231]\n" \
"v_add_f32 v70,v70,v228\nv_add_f32 v71,v71,v228\nM v[100:103],a[4:7],a[148:151],v[100:103]\nv_exp_f32 v68,v68\nv_cvt_pk_bf16_f32 v64,v64,v65\nM v[100:103],a[8:11],a[152:155],v[100:103]\nv_exp_f32 v69,v69\nv_add_f32 v76,v76,v228\n" \
"M v[100:103],a[12:15],a[156:159],v[100:103]\nv_exp_f32 v72,v72\nM v[104:107],a[16:19],a[128:131],v[224:227]\nv_exp_f32 v73,v73\nv_add_f32 v77,v77,v228\nM v[104:107],a[20:23],a[132:135],v[104:107]\nv_exp_f32 v74,v74\nM v[104:107],a[24:27],a[136:139],v[104:107]\n" \
"v_exp_f32 v75,v75\nv_add_f32 v78,v78,v228\nM v[104:107],a[28:31],a[140:143],v[104:107]\nv_exp_f32 v70,v70\nv_add_f32 v79,v79,v228\nM v[108:111],a[16:19],a[144:147],v[228:231]\nv_exp_f32 v71,v71\nM v[108:111],a[20:23],a[148:151],v[108:111]\n" \
"v_cvt_pk_bf16_f32 v65,v66,v67\nv_exp_f32 v76,v76\nM v[108:111],a[24:27],a[152:155],v[108:111]\nv_exp_f32 v77,v77\nv_cvt_pk_bf16_f32 v66,v72,v73\nM v[108:111],a[28:31],a[156:159],v[108:111]\nv_exp_f32 v78,v78\nM v[112:115],a[32:35],a[128:131],v[224:227]\n" \
"v_exp_f32 v79,v79\nv_cvt_pk_bf16_f32 v67,v74,v75\nM v[112:115],a[36:39],a[132:135],v[112:115]\nv_cvt_pk_bf16_f32 v68,v68,v69\ns_waitcnt lgkmcnt(0)\nM v[112:115],a[40:43],a[136:139],v[112:115]\nv_cvt_pk_bf16_f32 v69,v70,v71\nv_cvt_pk_bf16_f32 v70,v76,v77\n" \
"v_cvt_pk_bf16_f32 v71,v78,v79\nM v[112:115],a[44:47],a[140:143],v[112:115]\nv_add_f32 v80,v80,v224\nv_add_f32 v81,v81,v224\nv_add_f32 v82,v82,v224\nM v[116:119],a[32:35],a[144:147],v[228:231]\nv_add_f32 v83,v83,v224\nv_add_f32 v84,v84,v228\n" \
"M v[116:119],a[36:39],a[148:151],v[116:119]\nv_exp_f32 v80,v80\nv_add_f32 v85,v85,v228\nM v[116:119],a[40:43],a[152:155],v[116:119]\nv_exp_f32 v81,v81\nM v[116:119],a[44:47],a[156:159],v[116:119]\nv_exp_f32 v82,v82\nv_add_f32 v88,v88,v224\n" \
"M v[124:127],a[48:51],a[144:147],v[228:231]\nv_exp_f32 v83,v83\nv_add_f32 v89,v89,v224\nM v[124:127],a[52:55],a[148:151],v[124:127]\nv_add_f32 v90,v90,v224\nv_add_f32 v91,v91,v224\nM v[124:127],a[56:59],a[152:155],v[124:127]\nv_add_f32 v86,v86,v228\n" \
"v_add_f32 v87,v87,v228\nv_cvt_pk_bf16_f32 v80,v80,v81\nM v[124:127],a[60:63],a[156:159],v[124:127]\nv_exp_f32 v84,v84\nv_add_f32 v92,v92,v228\nM v[0:3],a[64:67],v[64:67],0\nv_exp_f32 v85,v85\nM v[4:7],a[64:67],v[68:71],0\n" \
"v_exp_f32 v88,v88\nv_add_f32 v93,v93,v228\nM v[8:11],a[68:71],v[64:67],0\nv_exp_f32 v89,v89\nM v[12:15],a[68:71],v[68:71],0\nv_exp_f32 v90,v90\nv_add_f32 v94,v94,v228\nM v[16:19],a[72:75],v[64:67],0\n" \
"v_exp_f32 v91,v91\nv_add_f32 v95,v95,v228\nM v[20:23],a[72:75],v[68:71],0\nv_exp_f32 v86,v86\nM v[24:27],a[76:79],v[64:67],0\nv_exp_f32 v87,v87\nv_cvt_pk_bf16_f32 v81,v82,v83\nM v[28:31],a[76:79],v[68:71],0\n" \
"v_exp_f32 v92,v92\nM v[32:35],a[80:83],v[64:67],0\nv_exp_f32 v93,v93\nv_cvt_pk_bf16_f32 v82,v88,v89\nM v[36:39],a[80:83],v[68:71],0\nv_exp_f32 v94,v94\nv_cvt_pk_bf16_f32 v83,v90,v91\nM v[40:43],a[84:87],v[64:67],0\n" \
"v_exp_f32 v95,v95\nM v[44:47],a[84:87],v[68:71],0\nv_cvt_pk_bf16_f32 v84,v84,v85\nv_cvt_pk_bf16_f32 v85,v86,v87\nv_cvt_pk_bf16_f32 v86,v92,v93\nM v[48:51],a[88:91],v[64:67],0\nv_cvt_pk_bf16_f32 v87,v94,v95\nds_read_b128 a[116:119],v241 offset:13312\n" \
"ds_read_b128 a[120:123],v241 offset:14336\nM v[52:55],a[88:91],v[68:71],0\nds_read_b128 a[124:127],v241 offset:15360\nv_cndmask_b32 v112,v112,v252,s[82:83]\nM v[56:59],a[92:95],v[64:67],0\nbuffer_load_dword v248,v251,s[52:55],s80 offen sc1\nds_read_b128 a[0:3],v240 offset:32768\nM v[60:63],a[92:95],v[68:71],0\n" \
"ds_read_b128 a[4:7],v240 offset:33792\nv_cndmask_b32 v113,v113,v252,s[84:85]\nM a[192:195],a[208:211],v[64:67],0\nds_read_b128 a[8:11],v240 offset:34816\nv_exp_f32 v96,v96\nM a[196:199],a[208:211],v[68:71],0\nds_read_b128 a[12:15],v240 offset:35840\nv_exp_f32 v97,v97\n" \
"M v[0:3],a[96:99],v[80:83],v[0:3]\nds_read_b128 a[16:19],v240 offset:36864\nv_cvt_pk_bf16_f32 v96,v96,v97\nM v[4:7],a[96:99],v[84:87],v[4:7]\nds_read_b128 a[20:23],v240 offset:37888\nv_exp_f32 v98,v98\nM v[8:11],a[100:103],v[80:83],v[8:11]\nds_read_b128 a[24:27],v240 offset:38912\n" \
"v_exp_f32 v99,v99\nM v[12:15],a[100:103],v[84:87],v[12:15]\nds_read_b128 a[28:31],v240 offset:39936\nv_cvt_pk_bf16_f32 v97,v98,v99\nM v[16:19],a[104:107],v[80:83],v[16:19]\nds_read_b128 a[32:35],v240 offset:40960\nv_exp_f32 v100,v100\nM v[20:23],a[104:107],v[84:87],v[20:23]\n" \
"ds_read_b128 a[36:39],v240 offset:41984\nv_cndmask_b32 v114,v114,v252,s[86:87]\nM v[24:27],a[108:111],v[80:83],v[24:27]\nds_read_b128 a[40:43],v240 offset:43008\nv_exp_f32 v101,v101\nM v[28:31],a[108:111],v[84:87],v[28:31]\nds_read_b128 a[44:47],v240 offset:44032\nv_exp_f32 v104,v104\n" \
"M v[32:35],a[112:115],v[80:83],v[32:35]\nds_read_b128 a[48:51],v240 offset:45056\nv_cvt_pk_bf16_f32 v100,v100,v101\nM v[36:39],a[112:115],v[84:87],v[36:39]\nds_read_b128 a[52:55],v240 offset:46080\nv_cndmask_b32 v115,v115,v252,s[88:89]\ns_waitcnt lgkmcnt(9)\nM v[40:43],a[116:119],v[80:83],v[40:43]\n" \
"ds_read_b128 a[56:59],v240 offset:47104\nv_cndmask_b32 v124,v124,v252,s[82:83]\nM v[44:47],a[116:119],v[84:87],v[44:47]\nds_read_b128 a[60:63],v240 offset:48128\nv_exp_f32 v105,v105\nM v[48:51],a[120:123],v[80:83],v[48:51]\nds_read_b128 a[64:67],v241 offset:16384\nv_exp_f32 v106,v106\n" \
"M v[52:55],a[120:123],v[84:87],v[52:55]\nds_read_b128 a[68:71],v241 offset:17408\nv_cvt_pk_bf16_f32 v98,v104,v105\nM v[56:59],a[124:127],v[80:83],v[56:59]\nds_read_b128 a[72:75],v241 offset:18432\nv_exp_f32 v107,v107\nM v[60:63],a[124:127],v[84:87],v[60:63]\nds_read_b128 a[76:79],v241 offset:19456\n" \
"v_exp_f32 v102,v102\nM a[192:195],a[208:211],v[80:83],a[192:195]\nds_read_b128 a[80:83],v241 offset:20480\nv_cvt_pk_bf16_f32 v99,v106,v107\nM a[196:199],a[208:211],v[84:87],a[196:199]\nds_read_b128 a[84:87],v241 offset:21504\nv_exp_f32 v103,v103\nM v[128:131],a[0:3],a[128:131],v[224:227]\n" \
"ds_read_b128 a[88:91],v241 offset:22528\nv_cvt_pk_bf16_f32 v101,v102,v103\nM v[128:131],a[4:7],a[132:135],v[128:131]\nds_read_b128 a[92:95],v241 offset:23552\nv_exp_f32 v108,v108\nM v[128:131],a[8:11],a[136:139],v[128:131]\nds_read_b128 a[96:99],v241 offset:24576\nv_exp_f32 v109,v109\n" \
"M v[128:131],a[12:15],a[140:143],v[128:131]\nds_read_b128 a[100:103],v241 offset:25600\nv_cvt_pk_bf16_f32 v102,v108,v109\nM v[132:135],a[0:3],a[144:147],v[228:231]\nds_read_b128 a[104:107],v241 offset:26624\nv_exp_f32 v110,v110\nM v[132:135],a[4:7],a[148:151],v[132:135]\nds_read_b128 a[108:111],v241 offset:27648\n" \
"v_cndmask_b32 v125,v125,v252,s[84:85]\nM v[132:135],a[8:11],a[152:155],v[132:135]\nds_read_b128 a[112:115],v241 offset:28672\nv_exp_f32 v111,v111\nM v[132:135],a[12:15],a[156:159],v[132:135]\nds_read_b128 a[116:119],v241 offset:29696\nv_cvt_pk_bf16_f32 v103,v110,v111\nv_cndmask_b32 v126,v126,v252,s[86:87]\n" \
"M v[144:147],a[16:19],a[128:131],v[224:227]\nds_read_b128 a[120:123],v241 offset:30720\ns_waitcnt lgkmcnt(9)\nM v[144:147],a[20:23],a[132:135],v[144:147]\nds_read_b128 a[124:127],v241 offset:31744\nv_exp_f32 v112,v112\nM v[144:147],a[24:27],a[136:139],v[144:147]\nv_exp_f32 v113,v113\n" \
"v_cndmask_b32 v127,v127,v252,s[88:89]\nM v[144:147],a[28:31],a[140:143],v[144:147]\nv_exp_f32 v114,v114\nM v[148:151],a[16:19],a[144:147],v[228:231]\nv_exp_f32 v115,v115\nv_cvt_pk_bf16_f32 v112,v112,v113\nM v[148:151],a[20:23],a[148:151],v[148:151]\nv_exp_f32 v116,v116\n" \
"M v[148:151],a[24:27],a[152:155],v[148:151]\nv_exp_f32 v117,v117\nv_cvt_pk_bf16_f32 v113,v114,v115\nM v[148:151],a[28:31],a[156:159],v[148:151]\nv_exp_f32 v118,v118\nv_mov_b32 v114,0\nM v[160:163],a[32:35],a[128:131],v[224:227]\nv_exp_f32 v119,v119\n" \
"M v[160:163],a[36:39],a[132:135],v[160:163]\nv_exp_f32 v124,v124\nv_mov_b32 v115,0\nM v[160:163],a[40:43],a[136:139],v[160:163]\nv_exp_f32 v125,v125\nv_cvt_pk_bf16_f32 v116,v116,v117\nM v[160:163],a[44:47],a[140:143],v[160:163]\nv_exp_f32 v126,v126\n" \
"M v[164:167],a[32:35],a[144:147],v[228:231]\nv_exp_f32 v127,v127\nv_cvt_pk_bf16_f32 v117,v118,v119\nM v[164:167],a[36:39],a[148:151],v[164:167]\nv_cvt_pk_bf16_f32 v118,v124,v125\nv_cvt_pk_bf16_f32 v119,v126,v127\nM v[164:167],a[40:43],a[152:155],v[164:167]\nds_read_b128 a[0:3],v240 offset:49152\n" \
"ds_read_b128 a[4:7],v240 offset:50176\nv_mov_b32 v188,0xff800000\nM v[164:167],a[44:47],a[156:159],v[164:167]\nds_read_b128 a[8:11],v240 offset:51200\nv_exp_f32 v128,v128\nM v[176:179],a[48:51],a[128:131],v[224:227]\nds_read_b128 a[12:15],v240 offset:52224\ns_waitcnt vmcnt(0)\n" \
"v_cmp_eq_u32 vcc,0,v248\nM v[176:179],a[52:55],a[132:135],v[176:179]\nds_read_b128 a[16:19],v240 offset:53248\nv_exp_f32 v129,v129\nM v[176:179],a[56:59],a[136:139],v[176:179]\nds_read_b128 a[20:23],v240 offset:54272\nv_cvt_pk_bf16_f32 v192,v128,v129\nM v[176:179],a[60:63],a[140:143],v[176:179]\n" \
"ds_read_b128 a[24:27],v240 offset:55296\nv_exp_f32 v130,v130\nM v[180:183],a[48:51],a[144:147],v[228:231]\ns_waitcnt lgkmcnt(7)\ns_barrier\nds_read_b128 a[28:31],v240 offset:56320\nM v[180:183],a[52:55],a[148:151],v[180:183]\nv_exp_f32 v131,v131\n" \
"M v[180:183],a[56:59],a[152:155],v[180:183]\nv_cvt_pk_bf16_f32 v193,v130,v131\nds_read_b128 a[32:35],v240 offset:57344\nds_read_b128 a[36:39],v240 offset:58368\nM v[180:183],a[60:63],a[156:159],v[180:183]\nds_read_b128 a[40:43],v240 offset:59392\nv_exp_f32 v132,v132\nM v[0:3],a[64:67],v[96:99],v[0:3]\n" \
"ds_read_b128 a[44:47],v240 offset:60416\ns_cbranch_vccnz .Lps5\n.Lpb5:\nM v[4:7],a[64:67],v[100:103],v[4:7]\nds_read_b128 a[48:51],v240 offset:61440\nv_exp_f32 v133,v133\nM v[8:11],a[68:71],v[96:99],v[8:11]\nds_read_b128 a[52:55],v240 offset:62464\n" \
"v_cvt_pk_bf16_f32 v196,v132,v133\nM v[12:15],a[68:71],v[100:103],v[12:15]\nds_read_b128 a[56:59],v240 offset:63488\nv_exp_f32 v134,v134\nM v[16:19],a[72:75],v[96:99],v[16:19]\nds_read_b128 a[60:63],v240 offset:64512\nv_exp_f32 v135,v135\nM v[20:23],a[72:75],v[100:103],v[20:23]\n" \
"ds_read_b128 a[64:67],v241 offset:32768\nv_cvt_pk_bf16_f32 v197,v134,v135\nM v[24:27],a[76:79],v[96:99],v[24:27]\nds_read_b128 a[68:71],v241 offset:33792\nv_exp_f32 v144,v144\nM v[28:31],a[76:79],v[100:103],v[28:31]\nds_read_b128 a[72:75],v241 offset:34816\ns_add_u32 m0,s64,65536\n" \
"M v[32:35],a[80:83],v[96:99],v[32:35]\nds_read_b128 a[76:79],v241 offset:35840\nv_exp_f32 v145,v145\nM v[36:39],a[80:83],v[100:103],v[36:39]\nds_read_b128 a[80:83],v241 offset:36864\nv_exp_f32 v146,v146\nM v[40:43],a[84:87],v[96:99],v[40:43]\nv_exp_f32 v147,v147\n" \
"M v[44:47],a[84:87],v[100:103],v[44:47]\nds_read_b128 a[84:87],v241 offset:37888\nv_cvt_pk_bf16_f32 v194,v144,v145\nv_cvt_pk_bf16_f32 v195,v146,v147\nM v[48:51],a[88:91],v[96:99],v[48:51]\nv_exp_f32 v148,v148\nv_mov_b32 v189,0xff800000\nM v[52:55],a[88:91],v[100:103],v[52:55]\n" \
"ds_read_b128 a[88:91],v241 offset:38912\nv_mov_b32 v190,0xff800000\nM v[56:59],a[92:95],v[96:99],v[56:59]\nv_exp_f32 v149,v149\nv_mov_b32 v191,0xff800000\nM v[60:63],a[92:95],v[100:103],v[60:63]\nds_read_b128 a[92:95],v241 offset:39936\nv_cvt_pk_bf16_f32 v198,v148,v149\n" \
"M a[192:195],a[208:211],v[96:99],a[192:195]\nv_exp_f32 v150,v150\nv_mov_b32 v202,0\nM a[196:199],a[208:211],v[100:103],a[196:199]\nv_exp_f32 v151,v151\nv_mov_b32 v203,0\nM v[0:3],a[96:99],v[112:115],v[0:3]\nv_exp_f32 v160,v160\n" \
"M v[4:7],a[96:99],v[116:119],v[4:7]\nds_read_b128 a[96:99],v241 offset:40960\nv_exp_f32 v161,v161\nM v[8:11],a[100:103],v[112:115],v[8:11]\nv_cvt_pk_bf16_f32 v199,v150,v151\nv_exp_f32 v162,v162\nM v[12:15],a[100:103],v[116:119],v[12:15]\nds_read_b128 a[100:103],v241 offset:41984\n" \
"v_cvt_pk_bf16_f32 v208,v160,v161\nM v[16:19],a[104:107],v[112:115],v[16:19]\nv_exp_f32 v163,v163\nM v[20:23],a[104:107],v[116:119],v[20:23]\nds_read_b128 a[104:107],v241 offset:43008\nv_cvt_pk_bf16_f32 v209,v162,v163\nM v[24:27],a[108:111],v[112:115],v[24:27]\nv_exp_f32 v164,v164\n" \
"M v[28:31],a[108:111],v[116:119],v[28:31]\nds_read_b128 a[108:111],v241 offset:44032\nv_exp_f32 v165,v165\nM v[32:35],a[112:115],v[112:115],v[32:35]\nv_exp_f32 v166,v166\nM v[36:39],a[112:115],v[116:119],v[36:39]\nds_read_b128 a[112:115],v241 offset:45056\nv_exp_f32 v167,v167\n" \
"M v[40:43],a[116:119],v[112:115],v[40:43]\nv_cvt_pk_bf16_f32 v212,v164,v165\nv_cvt_pk_bf16_f32 v213,v166,v167\nM v[44:47],a[116:119],v[116:119],v[44:47]\nds_read_b128 a[116:119],v241 offset:46080\nv_exp_f32 v176,v176\nM v[48:51],a[120:123],v[112:115],v[48:51]\nv_exp_f32 v177,v177\n" \
"M v[52:55],a[120:123],v[116:119],v[52:55]\nds_read_b128 a[120:123],v241 offset:47104\nv_cvt_pk_bf16_f32 v210,v176,v177\nM v[56:59],a[124:127],v[112:115],v[56:59]\nv_exp_f32 v178,v178\nM v[60:63],a[124:127],v[116:119],v[60:63]\nds_read_b128 a[124:127],v241 offset:48128\nv_exp_f32 v179,v179\n" \
"M a[192:195],a[208:211],v[112:115],a[192:195]\nv_exp_f32 v180,v180\nM a[196:199],a[208:211],v[116:119],a[196:199]\nv_cvt_pk_bf16_f32 v211,v178,v179\nv_exp_f32 v181,v181\nM v[64:67],a[244:247],a[244:247],0\nv_exp_f32 v182,v182\nM v[68:71],a[244:247],a[244:247],0\n" \
"v_exp_f32 v183,v183\nv_cvt_pk_bf16_f32 v214,v180,v181\nM v[72:75],a[244:247],a[244:247],0\nv_cvt_pk_bf16_f32 v215,v182,v183\nbuffer_load_dwordx4 v240,s[48:51],s36 offen offset:0 sc1 lds\nM v[76:79],a[244:247],a[244:247],0\nM v[80:83],a[244:247],a[244:247],0\nM v[84:87],a[244:247],a[244:247],0\n" \
"M v[88:91],a[244:247],a[244:247],0\nbuffer_load_dwordx4 v240,s[48:51],s36 offen offset:1024 sc1 lds\nM v[92:95],a[244:247],a[244:247],0\nM v[96:99],a[244:247],a[244:247],0\nM v[100:103],a[244:247],a[244:247],0\nM v[104:107],a[244:247],a[244:247],0\nbuffer_load_dwordx4 v240,s[48:51],s36 offen offset:2048 sc1 lds\nM v[108:111],a[244:247],a[244:247],0\n" \
"M v[112:115],a[244:247],a[244:247],0\nM v[116:119],a[244:247],a[244:247],0\nM v[120:123],a[244:247],a[244:247],0\nbuffer_load_dwordx4 v240,s[48:51],s36 offen offset:3072 sc1 lds\ns_add_u32 m0,s64,16384\nM v[124:127],a[244:247],a[244:247],0\nM a[200:203],a[244:247],a[244:247],0\nM a[204:207],a[244:247],a[244:247],0\n" \
"s_waitcnt lgkmcnt(0)\nM v[136:139],a[0:3],a[160:163],0\nbuffer_load_dwordx4 v240,s[44:47],s65 offen offset:0 sc1 lds\nM v[136:139],a[4:7],a[164:167],v[136:139]\nM v[136:139],a[8:11],a[168:171],v[136:139]\nM v[136:139],a[12:15],a[172:175],v[136:139]\nM v[140:143],a[0:3],a[176:179],0\nbuffer_load_dwordx4 v240,s[44:47],s65 offen offset:1024 sc1 lds\n" \
"M v[140:143],a[4:7],a[180:183],v[140:143]\nM v[140:143],a[8:11],a[184:187],v[140:143]\nM v[140:143],a[12:15],a[188:191],v[140:143]\nM v[156:159],a[16:19],a[176:179],0\nbuffer_load_dwordx4 v240,s[44:47],s65 offen offset:2048 sc1 lds\nM v[156:159],a[20:23],a[180:183],v[156:159]\nv_max3_f32 v233,v136,v137,v138\nv_max_f32 v233,v233,v139\n" \
"v_cndmask_b32 v233,v232,v233,s[90:91]\nM v[156:159],a[24:27],a[184:187],v[156:159]\nv_cndmask_b32 v136,v136,v252,s[82:83]\nv_max3_f32 v237,v140,v141,v142\nv_cndmask_b32 v137,v137,v252,s[84:85]\nM v[156:159],a[28:31],a[188:191],v[156:159]\nv_cndmask_b32 v138,v138,v252,s[86:87]\nv_cndmask_b32 v139,v139,v252,s[88:89]\n" \
"M v[128:131],a[0:3],a[128:131],v[224:227]\nbuffer_load_dwordx4 v240,s[44:47],s65 offen offset:3072 sc1 lds\ns_add_u32 m0,s64,81920\nM v[128:131],a[4:7],a[132:135],v[128:131]\nM v[128:131],a[8:11],a[136:139],v[128:131]\ns_barrier\nv_max3_f32 v237,v237,v143,v156\nM v[128:131],a[12:15],a[140:143],v[128:131]\n" \
"v_max3_f32 v237,v237,v157,v158\nv_max_f32 v237,v237,v159\nv_cndmask_b32 v237,v236,v237,s[90:91]\nM v[132:135],a[0:3],a[144:147],v[228:231]\nds_read_b128 a[0:3],v240 offset:0\nv_permlane16_swap_b32 v233,v237\nM v[132:135],a[4:7],a[148:151],v[132:135]\nds_read_b128 a[4:7],v240 offset:1024\n" \
"v_max_f32 v233,v233,v237\nv_mov_b32 v237,v233\nM v[132:135],a[8:11],a[152:155],v[132:135]\nds_read_b128 a[8:11],v240 offset:2048\nv_permlane32_swap_b32 v233,v237\nv_max_f32 v233,v233,v237\nM v[132:135],a[12:15],a[156:159],v[132:135]\nds_read_b128 a[12:15],v240 offset:3072\n" \
"v_mov_b32 v237,v233\nM v[144:147],a[16:19],a[128:131],v[224:227]\nbuffer_load_dwordx4 v240,s[48:51],s65 offen offset:0 sc1 lds\nv_permlane16_swap_b32 v233,v237\nM v[144:147],a[20:23],a[132:135],v[144:147]\nv_cndmask_b32 v156,v156,v252,s[82:83]\nv_cndmask_b32 v157,v157,v252,s[84:85]\nM v[144:147],a[24:27],a[136:139],v[144:147]\n" \
"v_cndmask_b32 v158,v158,v252,s[86:87]\nv_cndmask_b32 v159,v159,v252,s[88:89]\nv_sub_f32 v232,0,v233\nM v[144:147],a[28:31],a[140:143],v[144:147]\nv_sub_f32 v236,0,v237\nv_add_f32 v136,v136,v232\nv_add_f32 v137,v137,v232\nM v[148:151],a[16:19],a[144:147],v[228:231]\n" \
"ds_read_b128 a[16:19],v240 offset:4096\nv_add_f32 v138,v138,v232\nM v[148:151],a[20:23],a[148:151],v[148:151]\nds_read_b128 a[20:23],v240 offset:5120\nbuffer_load_dwordx4 v240,s[48:51],s65 offen offset:1024 sc1 lds\nM v[148:151],a[24:27],a[152:155],v[148:151]\nds_read_b128 a[24:27],v240 offset:6144\nv_add_f32 v139,v139,v232\n" \
"v_add_f32 v140,v140,v236\nM v[148:151],a[28:31],a[156:159],v[148:151]\nds_read_b128 a[28:31],v240 offset:7168\nv_add_f32 v141,v141,v236\nM v[160:163],a[32:35],a[128:131],v[224:227]\nv_add_f32 v142,v142,v236\nv_add_f32 v143,v143,v236\nv_add_f32 v156,v156,v236\n" \
"M v[160:163],a[36:39],a[132:135],v[160:163]\nbuffer_load_dwordx4 v240,s[48:51],s65 offen offset:2048 sc1 lds\nM v[160:163],a[40:43],a[136:139],v[160:163]\nv_add_f32 v157,v157,v236\nv_add_f32 v158,v158,v236\nv_add_f32 v159,v159,v236\nM v[160:163],a[44:47],a[140:143],v[160:163]\nv_exp_f32 v136,v136\n" \
"v_sub_f32 v234,0,v233\nM v[164:167],a[32:35],a[144:147],v[228:231]\nds_read_b128 a[32:35],v240 offset:8192\nv_sub_f32 v235,0,v233\nM v[164:167],a[36:39],a[148:151],v[164:167]\nds_read_b128 a[36:39],v240 offset:9216\nbuffer_load_dwordx4 v240,s[48:51],s65 offen offset:3072 sc1 lds\nM v[164:167],a[40:43],a[152:155],v[164:167]\n" \
"ds_read_b128 a[40:43],v240 offset:10240\ns_add_u32 m0,s64,32768\nv_sub_f32 v238,0,v237\nM v[164:167],a[44:47],a[156:159],v[164:167]\nds_read_b128 a[44:47],v240 offset:11264\nv_sub_f32 v239,0,v237\nM v[176:179],a[48:51],a[128:131],v[224:227]\nv_exp_f32 v137,v137\n" \
"v_sub_f32 v233,0,v233\nM v[176:179],a[52:55],a[132:135],v[176:179]\nbuffer_load_dwordx4 v240,s[44:47],s66 offen offset:0 sc1 lds\nM v[176:179],a[56:59],a[136:139],v[176:179]\nv_exp_f32 v138,v138\nv_sub_f32 v237,0,v237\nM v[176:179],a[60:63],a[140:143],v[176:179]\nv_exp_f32 v139,v139\n" \
"v_cvt_pk_bf16_f32 v200,v136,v137\nM v[180:183],a[48:51],a[144:147],v[228:231]\nds_read_b128 a[48:51],v240 offset:12288\nv_cvt_pk_bf16_f32 v201,v138,v139\nM v[180:183],a[52:55],a[148:151],v[180:183]\nds_read_b128 a[52:55],v240 offset:13312\nbuffer_load_dwordx4 v240,s[44:47],s66 offen offset:1024 sc1 lds\nM v[180:183],a[56:59],a[152:155],v[180:183]\n" \
"v_exp_f32 v140,v140\nM v[180:183],a[60:63],a[156:159],v[180:183]\nds_read_b128 a[56:59],v240 offset:14336\nv_exp_f32 v141,v141\nM v[0:3],a[64:67],v[192:195],v[0:3]\nds_read_b128 a[60:63],v240 offset:15360\nv_exp_f32 v142,v142\nM v[4:7],a[64:67],v[196:199],v[4:7]\n" \
"buffer_load_dwordx4 v240,s[44:47],s66 offen offset:2048 sc1 lds\nM v[8:11],a[68:71],v[192:195],v[8:11]\nv_exp_f32 v143,v143\nv_cvt_pk_bf16_f32 v204,v140,v141\nM v[12:15],a[68:71],v[196:199],v[12:15]\nv_exp_f32 v156,v156\nv_cvt_pk_bf16_f32 v205,v142,v143\nM v[16:19],a[72:75],v[192:195],v[16:19]\n" \
"v_exp_f32 v157,v157\nM v[20:23],a[72:75],v[196:199],v[20:23]\nbuffer_load_dwordx4 v240,s[44:47],s66 offen offset:3072 sc1 lds\ns_add_u32 m0,s64,98304\nM v[24:27],a[76:79],v[192:195],v[24:27]\nv_exp_f32 v158,v158\nM v[28:31],a[76:79],v[196:199],v[28:31]\nv_exp_f32 v159,v159\n" \
"v_cvt_pk_bf16_f32 v206,v156,v157\nM v[32:35],a[80:83],v[192:195],v[32:35]\nv_exp_f32 v128,v128\nv_cvt_pk_bf16_f32 v207,v158,v159\nM v[36:39],a[80:83],v[196:199],v[36:39]\nv_exp_f32 v129,v129\nM v[40:43],a[84:87],v[192:195],v[40:43]\nv_exp_f32 v130,v130\n" \
"M v[44:47],a[84:87],v[196:199],v[44:47]\nv_exp_f32 v131,v131\nM v[48:51],a[88:91],v[192:195],v[48:51]\nv_exp_f32 v132,v132\nM v[52:55],a[88:91],v[196:199],v[52:55]\nv_exp_f32 v133,v133\nM v[56:59],a[92:95],v[192:195],v[56:59]\nv_exp_f32 v134,v134\n" \
"M v[60:63],a[92:95],v[196:199],v[60:63]\nv_exp_f32 v135,v135\nM a[192:195],a[208:211],v[192:195],a[192:195]\nv_exp_f32 v144,v144\nv_cvt_pk_bf16_f32 v192,v128,v129\nM a[196:199],a[208:211],v[196:199],a[196:199]\nv_exp_f32 v145,v145\nM v[0:3],a[96:99],v[208:211],v[0:3]\n" \
"v_exp_f32 v146,v146\nv_cvt_pk_bf16_f32 v193,v130,v131\nM v[4:7],a[96:99],v[212:215],v[4:7]\nv_exp_f32 v147,v147\nM v[8:11],a[100:103],v[208:211],v[8:11]\nv_exp_f32 v148,v148\nv_cvt_pk_bf16_f32 v194,v144,v145\nM v[12:15],a[100:103],v[212:215],v[12:15]\n" \
"v_exp_f32 v149,v149\nv_cvt_pk_bf16_f32 v195,v146,v147\nM v[16:19],a[104:107],v[208:211],v[16:19]\nv_exp_f32 v150,v150\nM v[20:23],a[104:107],v[212:215],v[20:23]\nv_exp_f32 v151,v151\nv_cvt_pk_bf16_f32 v196,v132,v133\nM v[24:27],a[108:111],v[208:211],v[24:27]\n" \
"v_exp_f32 v160,v160\nv_cvt_pk_bf16_f32 v197,v134,v135\nM v[28:31],a[108:111],v[212:215],v[28:31]\nv_exp_f32 v161,v161\nM v[32:35],a[112:115],v[208:211],v[32:35]\nv_exp_f32 v162,v162\nv_cvt_pk_bf16_f32 v198,v148,v149\nM v[36:39],a[112:115],v[212:215],v[36:39]\n" \
"v_exp_f32 v163,v163\nM v[40:43],a[116:119],v[208:211],v[40:43]\nv_exp_f32 v164,v164\nv_cvt_pk_bf16_f32 v199,v150,v151\nM v[44:47],a[116:119],v[212:215],v[44:47]\nv_exp_f32 v165,v165\nM v[48:51],a[120:123],v[208:211],v[48:51]\nv_exp_f32 v166,v166\n" \
"M v[52:55],a[120:123],v[212:215],v[52:55]\nv_exp_f32 v167,v167\nM v[56:59],a[124:127],v[208:211],v[56:59]\nv_exp_f32 v176,v176\nM v[60:63],a[124:127],v[212:215],v[60:63]\nv_exp_f32 v177,v177\nM a[192:195],a[208:211],v[208:211],a[192:195]\nv_exp_f32 v178,v178\n" \
"v_exp_f32 v179,v179\nv_exp_f32 v180,v180\nv_exp_f32 v181,v181\nv_exp_f32 v182,v182\nv_exp_f32 v183,v183\nbuffer_load_dwordx4 v240,s[48:51],s66 offen offset:0 sc1 lds\nbuffer_load_dwordx4 v240,s[48:51],s66 offen offset:1024 sc1 lds\nbuffer_load_dwordx4 v240,s[48:51],s66 offen offset:2048 sc1 lds\n" \
"buffer_load_dwordx4 v240,s[48:51],s66 offen offset:3072 sc1 lds\nM a[196:199],a[208:211],v[212:215],a[196:199]\ns_cmp_lt_u32 s62,2\ns_cbranch_scc1 .Lx01\n.Ldual:\ns_waitcnt lgkmcnt(4)\nM v[128:131],a[0:3],a[128:131],v[224:227]\ns_add_u32 m0,s94,s64\n" \
"v_cvt_pk_bf16_f32 v208,v160,v161\nM v[128:131],a[4:7],a[132:135],v[128:131]\nds_read_b128 a[64:67],v250 offset:0\nds_read_b128 a[68:71],v250 offset:1024\nM v[128:131],a[8:11],a[136:139],v[128:131]\nv_cvt_pk_bf16_f32 v209,v162,v163\nv_cvt_pk_bf16_f32 v212,v164,v165\nM v[128:131],a[12:15],a[140:143],v[128:131]\n" \
"v_cvt_pk_bf16_f32 v213,v166,v167\nv_cvt_pk_bf16_f32 v216,v168,v169\nM v[132:135],a[0:3],a[144:147],v[228:231]\nbuffer_load_dwordx4 v240,s[44:47],s95 offen offset:0 sc1 lds\nM v[132:135],a[4:7],a[148:151],v[132:135]\nv_cvt_pk_bf16_f32 v217,v170,v171\nv_cvt_pk_bf16_f32 v210,v176,v177\nM v[132:135],a[8:11],a[152:155],v[132:135]\n" \
"v_exp_f32 v172,v172\nM v[132:135],a[12:15],a[156:159],v[132:135]\nds_read_b128 a[72:75],v250 offset:2048\nds_read_b128 a[76:79],v250 offset:3072\nM v[136:139],a[0:3],a[160:163],v[232:235]\nv_exp_f32 v173,v173\nM v[136:139],a[4:7],a[164:167],v[136:139]\nv_exp_f32 v174,v174\n" \
"M v[136:139],a[8:11],a[168:171],v[136:139]\nv_exp_f32 v175,v175\nM v[136:139],a[12:15],a[172:175],v[136:139]\nv_cvt_pk_bf16_f32 v220,v172,v173\nv_cvt_pk_bf16_f32 v221,v174,v175\nM v[140:143],a[0:3],a[176:179],v[236:239]\nv_cvt_pk_bf16_f32 v211,v178,v179\nv_cvt_pk_bf16_f32 v214,v180,v181\n" \
"M v[140:143],a[4:7],a[180:183],v[140:143]\nds_read_b128 a[80:83],v250 offset:4096\nds_read_b128 a[84:87],v250 offset:5120\nM v[140:143],a[8:11],a[184:187],v[140:143]\nv_cvt_pk_bf16_f32 v215,v182,v183\ns_add_u32 s94,s94,0x4000\nM v[140:143],a[12:15],a[188:191],v[140:143]\nv_exp_f32 v184,v184\n" \
"M v[144:147],a[16:19],a[128:131],v[224:227]\nv_exp_f32 v185,v185\nM v[144:147],a[20:23],a[132:135],v[144:147]\nv_exp_f32 v186,v186\nM v[144:147],a[24:27],a[136:139],v[144:147]\nv_exp_f32 v187,v187\nM v[144:147],a[28:31],a[140:143],v[144:147]\nds_read_b128 a[88:91],v250 offset:6144\n" \
"ds_read_b128 a[92:95],v250 offset:7168\nM v[148:151],a[16:19],a[144:147],v[228:231]\nv_cvt_pk_bf16_f32 v218,v184,v185\nv_cvt_pk_bf16_f32 v219,v186,v187\nM v[148:151],a[20:23],a[148:151],v[148:151]\nv_exp_f32 v188,v188\nM v[148:151],a[24:27],a[152:155],v[148:151]\nbuffer_load_dwordx4 v240,s[44:47],s95 offen offset:1024 sc1 lds\n" \
"M v[148:151],a[28:31],a[156:159],v[148:151]\nv_exp_f32 v189,v189\nM v[152:155],a[16:19],a[160:163],v[232:235]\nv_exp_f32 v190,v190\nM v[152:155],a[20:23],a[164:167],v[152:155]\nds_read_b128 a[96:99],v250 offset:8192\nds_read_b128 a[100:103],v250 offset:9216\nM v[152:155],a[24:27],a[168:171],v[152:155]\n" \
"v_exp_f32 v191,v191\nM v[152:155],a[28:31],a[172:175],v[152:155]\nv_cvt_pk_bf16_f32 v222,v188,v189\nv_cvt_pk_bf16_f32 v223,v190,v191\nM v[156:159],a[16:19],a[176:179],v[236:239]\nv_exp_f32 v128,v128\nM v[156:159],a[20:23],a[180:183],v[156:159]\nv_exp_f32 v129,v129\n" \
"M v[156:159],a[24:27],a[184:187],v[156:159]\nv_exp_f32 v130,v130\nM v[156:159],a[28:31],a[188:191],v[156:159]\nds_read_b128 a[104:107],v250 offset:10240\nds_read_b128 a[108:111],v250 offset:11264\nM v[160:163],a[32:35],a[128:131],v[224:227]\nv_exp_f32 v131,v131\nM v[160:163],a[36:39],a[132:135],v[160:163]\n" \
"v_exp_f32 v144,v144\nM v[160:163],a[40:43],a[136:139],v[160:163]\nv_exp_f32 v145,v145\nM v[160:163],a[44:47],a[140:143],v[160:163]\nv_exp_f32 v146,v146\nM v[164:167],a[32:35],a[144:147],v[228:231]\nv_exp_f32 v147,v147\nM v[164:167],a[36:39],a[148:151],v[164:167]\n" \
"ds_read_b128 a[112:115],v250 offset:12288\nds_read_b128 a[116:119],v250 offset:13312\nM v[164:167],a[40:43],a[152:155],v[164:167]\nv_exp_f32 v132,v132\nM v[164:167],a[44:47],a[156:159],v[164:167]\nv_exp_f32 v133,v133\nM v[168:171],a[32:35],a[160:163],v[232:235]\nbuffer_load_dwordx4 v240,s[44:47],s95 offen offset:2048 sc1 lds\n" \
"M v[168:171],a[36:39],a[164:167],v[168:171]\nv_exp_f32 v134,v134\nM v[168:171],a[40:43],a[168:171],v[168:171]\nv_exp_f32 v135,v135\nM v[168:171],a[44:47],a[172:175],v[168:171]\nds_read_b128 a[120:123],v250 offset:14336\nds_read_b128 a[124:127],v250 offset:15360\nM v[172:175],a[32:35],a[176:179],v[236:239]\n" \
"v_xor_b32 v250,0x18000,v250\ns_and_b32 s94,s94,0xffff\nM v[172:175],a[36:39],a[180:183],v[172:175]\nv_exp_f32 v148,v148\nM v[172:175],a[40:43],a[184:187],v[172:175]\nv_exp_f32 v149,v149\nM v[172:175],a[44:47],a[188:191],v[172:175]\ns_waitcnt lgkmcnt(4)\n" \
"M v[176:179],a[48:51],a[128:131],v[224:227]\nv_exp_f32 v150,v150\nM v[176:179],a[52:55],a[132:135],v[176:179]\nv_exp_f32 v151,v151\nM v[176:179],a[56:59],a[136:139],v[176:179]\nv_exp_f32 v136,v136\nM v[176:179],a[60:63],a[140:143],v[176:179]\nv_exp_f32 v137,v137\n" \
"M v[180:183],a[48:51],a[144:147],v[228:231]\nv_exp_f32 v138,v138\nM v[180:183],a[52:55],a[148:151],v[180:183]\nv_exp_f32 v139,v139\nM v[180:183],a[56:59],a[152:155],v[180:183]\nv_exp_f32 v152,v152\nM v[180:183],a[60:63],a[156:159],v[180:183]\nv_exp_f32 v153,v153\n" \
"M v[184:187],a[48:51],a[160:163],v[232:235]\nv_exp_f32 v154,v154\nM v[184:187],a[52:55],a[164:167],v[184:187]\nv_exp_f32 v155,v155\nM v[184:187],a[56:59],a[168:171],v[184:187]\nbuffer_load_dwordx4 v240,s[44:47],s95 offen offset:3072 sc1 lds\nM v[184:187],a[60:63],a[172:175],v[184:187]\nv_exp_f32 v140,v140\n" \
"M v[188:191],a[48:51],a[176:179],v[236:239]\nv_exp_f32 v141,v141\nM v[188:191],a[52:55],a[180:183],v[188:191]\nv_exp_f32 v142,v142\nM v[188:191],a[56:59],a[184:187],v[188:191]\nv_exp_f32 v143,v143\nM v[188:191],a[60:63],a[188:191],v[188:191]\nv_exp_f32 v156,v156\n" \
"M v[0:3],a[64:67],v[192:195],v[0:3]\ns_waitcnt vmcnt(16) lgkmcnt(0)\ns_barrier\nM v[4:7],a[64:67],v[196:199],v[4:7]\nds_read_b128 a[0:3],v250 offset:0\nds_read_b128 a[4:7],v250 offset:1024\nM v[64:67],a[64:67],v[200:203],v[64:67]\nv_exp_f32 v157,v157\n" \
"M v[68:71],a[64:67],v[204:207],v[68:71]\nv_exp_f32 v158,v158\nM v[8:11],a[68:71],v[192:195],v[8:11]\ns_add_u32 m0,m0,0x10000\nM v[12:15],a[68:71],v[196:199],v[12:15]\nbuffer_load_dwordx4 v240,s[48:51],s95 offen offset:0 sc1 lds\nM v[72:75],a[68:71],v[200:203],v[72:75]\nv_exp_f32 v159,v159\n" \
"M v[76:79],a[68:71],v[204:207],v[76:79]\nv_exp_f32 v160,v160\nM v[16:19],a[72:75],v[192:195],v[16:19]\nv_exp_f32 v161,v161\nM v[20:23],a[72:75],v[196:199],v[20:23]\nds_read_b128 a[8:11],v250 offset:2048\nds_read_b128 a[12:15],v250 offset:3072\nM v[80:83],a[72:75],v[200:203],v[80:83]\n" \
"v_exp_f32 v162,v162\nM v[84:87],a[72:75],v[204:207],v[84:87]\nv_exp_f32 v163,v163\nM v[24:27],a[76:79],v[192:195],v[24:27]\nv_exp_f32 v176,v176\nM v[28:31],a[76:79],v[196:199],v[28:31]\nv_exp_f32 v177,v177\nM v[88:91],a[76:79],v[200:203],v[88:91]\n" \
"v_exp_f32 v178,v178\nM v[92:95],a[76:79],v[204:207],v[92:95]\nv_exp_f32 v179,v179\nM v[32:35],a[80:83],v[192:195],v[32:35]\nv_exp_f32 v164,v164\nM v[36:39],a[80:83],v[196:199],v[36:39]\nds_read_b128 a[16:19],v250 offset:4096\nds_read_b128 a[20:23],v250 offset:5120\n" \
"M v[96:99],a[80:83],v[200:203],v[96:99]\nv_exp_f32 v165,v165\nM v[100:103],a[80:83],v[204:207],v[100:103]\nv_exp_f32 v166,v166\nM v[40:43],a[84:87],v[192:195],v[40:43]\nv_exp_f32 v167,v167\nM v[44:47],a[84:87],v[196:199],v[44:47]\nbuffer_load_dwordx4 v240,s[48:51],s95 offen offset:1024 sc1 lds\n" \
"M v[104:107],a[84:87],v[200:203],v[104:107]\nv_exp_f32 v180,v180\nM v[108:111],a[84:87],v[204:207],v[108:111]\nv_exp_f32 v181,v181\nM v[48:51],a[88:91],v[192:195],v[48:51]\nv_exp_f32 v182,v182\nM v[52:55],a[88:91],v[196:199],v[52:55]\nds_read_b128 a[24:27],v250 offset:6144\n" \
"ds_read_b128 a[28:31],v250 offset:7168\nM v[112:115],a[88:91],v[200:203],v[112:115]\nv_exp_f32 v183,v183\nM v[116:119],a[88:91],v[204:207],v[116:119]\nv_exp_f32 v168,v168\nM v[56:59],a[92:95],v[192:195],v[56:59]\nv_exp_f32 v169,v169\nM v[60:63],a[92:95],v[196:199],v[60:63]\n" \
"v_exp_f32 v170,v170\nM v[120:123],a[92:95],v[200:203],v[120:123]\nv_exp_f32 v171,v171\nM v[124:127],a[92:95],v[204:207],v[124:127]\nM a[192:195],a[208:211],v[192:195],a[192:195]\nv_cvt_pk_bf16_f32 v192,v128,v129\nv_cvt_pk_bf16_f32 v193,v130,v131\nM a[196:199],a[208:211],v[196:199],a[196:199]\n" \
"ds_read_b128 a[32:35],v250 offset:8192\nds_read_b128 a[36:39],v250 offset:9216\nM a[200:203],a[208:211],v[200:203],a[200:203]\nv_cvt_pk_bf16_f32 v194,v144,v145\nv_cvt_pk_bf16_f32 v195,v146,v147\nM a[204:207],a[208:211],v[204:207],a[204:207]\nv_cvt_pk_bf16_f32 v196,v132,v133\nv_cvt_pk_bf16_f32 v197,v134,v135\n" \
"M v[0:3],a[96:99],v[208:211],v[0:3]\nv_cvt_pk_bf16_f32 v198,v148,v149\nv_cvt_pk_bf16_f32 v199,v150,v151\nM v[4:7],a[96:99],v[212:215],v[4:7]\nbuffer_load_dwordx4 v240,s[48:51],s95 offen offset:2048 sc1 lds\nM v[64:67],a[96:99],v[216:219],v[64:67]\nv_cvt_pk_bf16_f32 v200,v136,v137\nv_cvt_pk_bf16_f32 v201,v138,v139\n" \
"M v[68:71],a[96:99],v[220:223],v[68:71]\nv_cvt_pk_bf16_f32 v202,v152,v153\nv_cvt_pk_bf16_f32 v203,v154,v155\nM v[8:11],a[100:103],v[208:211],v[8:11]\nv_cvt_pk_bf16_f32 v204,v140,v141\nv_cvt_pk_bf16_f32 v205,v142,v143\nM v[12:15],a[100:103],v[212:215],v[12:15]\nds_read_b128 a[40:43],v250 offset:10240\n" \
"ds_read_b128 a[44:47],v250 offset:11264\nM v[72:75],a[100:103],v[216:219],v[72:75]\nv_cvt_pk_bf16_f32 v206,v156,v157\nv_cvt_pk_bf16_f32 v207,v158,v159\nM v[76:79],a[100:103],v[220:223],v[76:79]\nM v[16:19],a[104:107],v[208:211],v[16:19]\nM v[20:23],a[104:107],v[212:215],v[20:23]\nM v[80:83],a[104:107],v[216:219],v[80:83]\n" \
"M v[84:87],a[104:107],v[220:223],v[84:87]\nM v[24:27],a[108:111],v[208:211],v[24:27]\nM v[28:31],a[108:111],v[212:215],v[28:31]\nds_read_b128 a[48:51],v250 offset:12288\nds_read_b128 a[52:55],v250 offset:13312\nM v[88:91],a[108:111],v[216:219],v[88:91]\nM v[92:95],a[108:111],v[220:223],v[92:95]\nM v[32:35],a[112:115],v[208:211],v[32:35]\n" \
"M v[36:39],a[112:115],v[212:215],v[36:39]\nbuffer_load_dwordx4 v240,s[48:51],s95 offen offset:3072 sc1 lds\nM v[96:99],a[112:115],v[216:219],v[96:99]\ns_cmp_eq_u32 s95,s96\ns_cselect_b32 s72,0xc000,s98\ns_add_u32 s95,s95,s72\nM v[100:103],a[112:115],v[220:223],v[100:103]\nM v[40:43],a[116:119],v[208:211],v[40:43]\n" \
"M v[44:47],a[116:119],v[212:215],v[44:47]\nds_read_b128 a[56:59],v250 offset:14336\nds_read_b128 a[60:63],v250 offset:15360\nM v[104:107],a[116:119],v[216:219],v[104:107]\nv_add_u32 v250,s94,v241\nM v[108:111],a[116:119],v[220:223],v[108:111]\nM v[48:51],a[120:123],v[208:211],v[48:51]\nM v[52:55],a[120:123],v[212:215],v[52:55]\n" \
"M v[112:115],a[120:123],v[216:219],v[112:115]\nM v[116:119],a[120:123],v[220:223],v[116:119]\nM v[56:59],a[124:127],v[208:211],v[56:59]\nM v[60:63],a[124:127],v[212:215],v[60:63]\nM v[120:123],a[124:127],v[216:219],v[120:123]\nbuffer_load_dword v247,v254,s[56:59],s1 offen\nM v[124:127],a[124:127],v[220:223],v[124:127]\ns_add_u32 s1,s1,s0\n" \
"M a[192:195],a[208:211],v[208:211],a[192:195]\nM a[196:199],a[208:211],v[212:215],a[196:199]\nM a[200:203],a[208:211],v[216:219],a[200:203]\nM a[204:207],a[208:211],v[220:223],a[204:207]\ns_cmp_lg_u32 s95,s97\ns_cbranch_scc1 .Ldual\n.Ldf:\ns_waitcnt lgkmcnt(4)\n" \
"M v[128:131],a[0:3],a[128:131],v[224:227]\ns_add_u32 m0,s94,s64\nv_lshrrev_b32 v248,2,v249\nM v[128:131],a[4:7],a[132:135],v[128:131]\nds_read_b128 a[64:67],v250 offset:0\nds_read_b128 a[68:71],v250 offset:1024\nM v[128:131],a[8:11],a[136:139],v[128:131]\nv_mul_u32_u24 v248,3584,v248\n" \
"v_and_b32 v251,3,v249\nM v[128:131],a[12:15],a[140:143],v[128:131]\ns_mul_i32 s70,s7,458752\nv_lshl_add_u32 v248,v251,3,v248\nM v[132:135],a[0:3],a[144:147],v[228:231]\nbuffer_load_dwordx4 v240,s[44:47],s95 offen offset:0 sc1 lds\nM v[132:135],a[4:7],a[148:151],v[132:135]\ns_add_u32 s70,s70,s2\n" \
"v_add_u32 v248,s70,v248\nM v[132:135],a[8:11],a[152:155],v[132:135]\nv_and_b32 v248,s3,v248\nv_cvt_pk_bf16_f32 v208,v160,v161\nM v[132:135],a[12:15],a[156:159],v[132:135]\nds_read_b128 a[72:75],v250 offset:2048\nds_read_b128 a[76:79],v250 offset:3072\nM v[136:139],a[0:3],a[160:163],v[232:235]\n" \
"v_cvt_pk_bf16_f32 v209,v162,v163\nv_cvt_pk_bf16_f32 v212,v164,v165\nM v[136:139],a[4:7],a[164:167],v[136:139]\nv_cvt_pk_bf16_f32 v213,v166,v167\nv_cvt_pk_bf16_f32 v216,v168,v169\nM v[136:139],a[8:11],a[168:171],v[136:139]\nv_cvt_pk_bf16_f32 v217,v170,v171\nv_cvt_pk_bf16_f32 v210,v176,v177\n" \
"M v[136:139],a[12:15],a[172:175],v[136:139]\nv_exp_f32 v172,v172\nM v[140:143],a[0:3],a[176:179],v[236:239]\nv_exp_f32 v173,v173\nM v[140:143],a[4:7],a[180:183],v[140:143]\nds_read_b128 a[80:83],v250 offset:4096\nds_read_b128 a[84:87],v250 offset:5120\nM v[140:143],a[8:11],a[184:187],v[140:143]\n" \
"global_load_dwordx2 a[212:213],v248,s[22:23]\nM v[140:143],a[12:15],a[188:191],v[140:143]\nv_exp_f32 v174,v174\nM v[144:147],a[16:19],a[128:131],v[224:227]\nv_exp_f32 v175,v175\nM v[144:147],a[20:23],a[132:135],v[144:147]\nv_cvt_pk_bf16_f32 v220,v172,v173\nv_cvt_pk_bf16_f32 v221,v174,v175\n" \
"M v[144:147],a[24:27],a[136:139],v[144:147]\nv_cvt_pk_bf16_f32 v211,v178,v179\ns_and_b32 s71,s3,57344\nM v[144:147],a[28:31],a[140:143],v[144:147]\nds_read_b128 a[88:91],v250 offset:6144\nds_read_b128 a[92:95],v250 offset:7168\nM v[148:151],a[16:19],a[144:147],v[228:231]\nv_cvt_pk_bf16_f32 v214,v180,v181\n" \
"v_cvt_pk_bf16_f32 v215,v182,v183\nM v[148:151],a[20:23],a[148:151],v[148:151]\nv_exp_f32 v184,v184\nM v[148:151],a[24:27],a[152:155],v[148:151]\nbuffer_load_dwordx4 v240,s[44:47],s95 offen offset:1024 sc1 lds\nM v[148:151],a[28:31],a[156:159],v[148:151]\nv_exp_f32 v185,v185\nM v[152:155],a[16:19],a[160:163],v[232:235]\n" \
"v_exp_f32 v186,v186\nM v[152:155],a[20:23],a[164:167],v[152:155]\nds_read_b128 a[96:99],v250 offset:8192\nds_read_b128 a[100:103],v250 offset:9216\nM v[152:155],a[24:27],a[168:171],v[152:155]\nv_exp_f32 v187,v187\nM v[152:155],a[28:31],a[172:175],v[152:155]\nv_cvt_pk_bf16_f32 v218,v184,v185\n" \
"v_cvt_pk_bf16_f32 v219,v186,v187\nM v[156:159],a[16:19],a[176:179],v[236:239]\nv_exp_f32 v188,v188\nM v[156:159],a[20:23],a[180:183],v[156:159]\nv_exp_f32 v189,v189\nM v[156:159],a[24:27],a[184:187],v[156:159]\nv_exp_f32 v190,v190\nM v[156:159],a[28:31],a[188:191],v[156:159]\n" \
"ds_read_b128 a[104:107],v250 offset:10240\nds_read_b128 a[108:111],v250 offset:11264\nM v[160:163],a[32:35],a[128:131],v[224:227]\nglobal_load_dwordx2 a[228:229],v248,s[24:25]\nM v[160:163],a[36:39],a[132:135],v[160:163]\nv_add_u32 v248,s71,v248\nv_cvt_pk_bf16_f32 v222,v188,v189\nM v[160:163],a[40:43],a[136:139],v[160:163]\n" \
"v_exp_f32 v191,v191\nM v[160:163],a[44:47],a[140:143],v[160:163]\nv_cvt_pk_bf16_f32 v223,v190,v191\ns_add_u32 s94,s94,0x4000\nM v[164:167],a[32:35],a[144:147],v[228:231]\nv_exp_f32 v128,v128\nM v[164:167],a[36:39],a[148:151],v[164:167]\nds_read_b128 a[112:115],v250 offset:12288\n" \
"ds_read_b128 a[116:119],v250 offset:13312\nM v[164:167],a[40:43],a[152:155],v[164:167]\nv_exp_f32 v129,v129\nM v[164:167],a[44:47],a[156:159],v[164:167]\nv_exp_f32 v130,v130\nM v[168:171],a[32:35],a[160:163],v[232:235]\nbuffer_load_dwordx4 v240,s[44:47],s95 offen offset:2048 sc1 lds\nM v[168:171],a[36:39],a[164:167],v[168:171]\n" \
"v_exp_f32 v131,v131\nM v[168:171],a[40:43],a[168:171],v[168:171]\nv_exp_f32 v144,v144\nM v[168:171],a[44:47],a[172:175],v[168:171]\nds_read_b128 a[120:123],v250 offset:14336\nds_read_b128 a[124:127],v250 offset:15360\nM v[172:175],a[32:35],a[176:179],v[236:239]\nv_xor_b32 v250,0x18000,v250\n" \
"s_and_b32 s94,s94,0xffff\nM v[172:175],a[36:39],a[180:183],v[172:175]\nv_exp_f32 v145,v145\nM v[172:175],a[40:43],a[184:187],v[172:175]\nv_exp_f32 v146,v146\nM v[172:175],a[44:47],a[188:191],v[172:175]\ns_waitcnt lgkmcnt(4)\nM v[176:179],a[48:51],a[128:131],v[224:227]\n" \
"v_exp_f32 v147,v147\nM v[176:179],a[52:55],a[132:135],v[176:179]\nv_exp_f32 v132,v132\nM v[176:179],a[56:59],a[136:139],v[176:179]\nglobal_load_dwordx2 a[214:215],v248,s[22:23]\nM v[176:179],a[60:63],a[140:143],v[176:179]\nv_exp_f32 v133,v133\nM v[180:183],a[48:51],a[144:147],v[228:231]\n" \
"v_exp_f32 v134,v134\nM v[180:183],a[52:55],a[148:151],v[180:183]\nv_exp_f32 v135,v135\nM v[180:183],a[56:59],a[152:155],v[180:183]\nv_exp_f32 v148,v148\nM v[180:183],a[60:63],a[156:159],v[180:183]\nv_exp_f32 v149,v149\nM v[184:187],a[48:51],a[160:163],v[232:235]\n" \
"v_exp_f32 v150,v150\nM v[184:187],a[52:55],a[164:167],v[184:187]\nv_exp_f32 v151,v151\nM v[184:187],a[56:59],a[168:171],v[184:187]\nbuffer_load_dwordx4 v240,s[44:47],s95 offen offset:3072 sc1 lds\nM v[184:187],a[60:63],a[172:175],v[184:187]\nv_exp_f32 v136,v136\nM v[188:191],a[48:51],a[176:179],v[236:239]\n" \
"v_exp_f32 v137,v137\nM v[188:191],a[52:55],a[180:183],v[188:191]\nv_exp_f32 v138,v138\nM v[188:191],a[56:59],a[184:187],v[188:191]\nglobal_load_dwordx2 a[230:231],v248,s[24:25]\nM v[188:191],a[60:63],a[188:191],v[188:191]\ns_add_u32 m0,m0,0x10000\nv_add_u32 v248,s71,v248\n" \
"M v[0:3],a[64:67],v[192:195],v[0:3]\ns_waitcnt vmcnt(16) lgkmcnt(0)\ns_barrier\nM v[4:7],a[64:67],v[196:199],v[4:7]\nds_read_b128 a[0:3],v250 offset:0\nds_read_b128 a[4:7],v250 offset:1024\nM v[64:67],a[64:67],v[200:203],v[64:67]\nv_exp_f32 v139,v139\n" \
"M v[68:71],a[64:67],v[204:207],v[68:71]\nv_exp_f32 v152,v152\nM v[8:11],a[68:71],v[192:195],v[8:11]\nv_exp_f32 v153,v153\nM v[12:15],a[68:71],v[196:199],v[12:15]\nbuffer_load_dwordx4 v240,s[48:51],s95 offen offset:0 sc1 lds\nM v[72:75],a[68:71],v[200:203],v[72:75]\nv_exp_f32 v154,v154\n" \
"M v[76:79],a[68:71],v[204:207],v[76:79]\nv_exp_f32 v155,v155\nM v[16:19],a[72:75],v[192:195],v[16:19]\nv_exp_f32 v140,v140\nM v[20:23],a[72:75],v[196:199],v[20:23]\nds_read_b128 a[8:11],v250 offset:2048\nds_read_b128 a[12:15],v250 offset:3072\nM v[80:83],a[72:75],v[200:203],v[80:83]\n" \
"v_exp_f32 v141,v141\nM v[84:87],a[72:75],v[204:207],v[84:87]\nv_exp_f32 v142,v142\nM v[24:27],a[76:79],v[192:195],v[24:27]\nv_exp_f32 v143,v143\nM v[28:31],a[76:79],v[196:199],v[28:31]\nglobal_load_dwordx2 a[216:217],v248,s[22:23]\nM v[88:91],a[76:79],v[200:203],v[88:91]\n" \
"v_exp_f32 v156,v156\nM v[92:95],a[76:79],v[204:207],v[92:95]\nv_exp_f32 v157,v157\nM v[32:35],a[80:83],v[192:195],v[32:35]\nv_exp_f32 v158,v158\nM v[36:39],a[80:83],v[196:199],v[36:39]\nds_read_b128 a[16:19],v250 offset:4096\nds_read_b128 a[20:23],v250 offset:5120\n" \
"M v[96:99],a[80:83],v[200:203],v[96:99]\nv_exp_f32 v159,v159\nM v[100:103],a[80:83],v[204:207],v[100:103]\nv_exp_f32 v160,v160\nM v[40:43],a[84:87],v[192:195],v[40:43]\nv_exp_f32 v161,v161\nM v[44:47],a[84:87],v[196:199],v[44:47]\nbuffer_load_dwordx4 v240,s[48:51],s95 offen offset:1024 sc1 lds\n" \
"M v[104:107],a[84:87],v[200:203],v[104:107]\nv_exp_f32 v162,v162\nM v[108:111],a[84:87],v[204:207],v[108:111]\nv_exp_f32 v163,v163\nM v[48:51],a[88:91],v[192:195],v[48:51]\nv_exp_f32 v176,v176\nM v[52:55],a[88:91],v[196:199],v[52:55]\nds_read_b128 a[24:27],v250 offset:6144\n" \
"ds_read_b128 a[28:31],v250 offset:7168\nM v[112:115],a[88:91],v[200:203],v[112:115]\nv_exp_f32 v177,v177\nM v[116:119],a[88:91],v[204:207],v[116:119]\nv_exp_f32 v178,v178\nM v[56:59],a[92:95],v[192:195],v[56:59]\nv_exp_f32 v179,v179\nM v[60:63],a[92:95],v[196:199],v[60:63]\n" \
"global_load_dwordx2 a[232:233],v248,s[24:25]\nM v[120:123],a[92:95],v[200:203],v[120:123]\nv_exp_f32 v164,v164\nM v[124:127],a[92:95],v[204:207],v[124:127]\nv_exp_f32 v165,v165\nM a[192:195],a[208:211],v[192:195],a[192:195]\nv_add_u32 v248,s71,v248\nv_cvt_pk_bf16_f32 v192,v128,v129\n" \
"M a[196:199],a[208:211],v[196:199],a[196:199]\nds_read_b128 a[32:35],v250 offset:8192\nds_read_b128 a[36:39],v250 offset:9216\nM a[200:203],a[208:211],v[200:203],a[200:203]\nv_cvt_pk_bf16_f32 v193,v130,v131\nv_cvt_pk_bf16_f32 v194,v144,v145\nM a[204:207],a[208:211],v[204:207],a[204:207]\nv_cvt_pk_bf16_f32 v195,v146,v147\n" \
"v_cvt_pk_bf16_f32 v196,v132,v133\nM v[0:3],a[96:99],v[208:211],v[0:3]\nv_cvt_pk_bf16_f32 v197,v134,v135\nv_cvt_pk_bf16_f32 v198,v148,v149\nM v[4:7],a[96:99],v[212:215],v[4:7]\nbuffer_load_dwordx4 v240,s[48:51],s95 offen offset:2048 sc1 lds\nM v[64:67],a[96:99],v[216:219],v[64:67]\nv_cvt_pk_bf16_f32 v199,v150,v151\n" \
"v_cvt_pk_bf16_f32 v200,v136,v137\nM v[68:71],a[96:99],v[220:223],v[68:71]\nv_cvt_pk_bf16_f32 v201,v138,v139\nv_cvt_pk_bf16_f32 v202,v152,v153\nM v[8:11],a[100:103],v[208:211],v[8:11]\nv_cvt_pk_bf16_f32 v203,v154,v155\nv_cvt_pk_bf16_f32 v204,v140,v141\nM v[12:15],a[100:103],v[212:215],v[12:15]\n" \
"ds_read_b128 a[40:43],v250 offset:10240\nds_read_b128 a[44:47],v250 offset:11264\nM v[72:75],a[100:103],v[216:219],v[72:75]\nv_cvt_pk_bf16_f32 v205,v142,v143\nv_cvt_pk_bf16_f32 v206,v156,v157\nM v[76:79],a[100:103],v[220:223],v[76:79]\nv_exp_f32 v166,v166\nM v[16:19],a[104:107],v[208:211],v[16:19]\n" \
"v_exp_f32 v167,v167\nM v[20:23],a[104:107],v[212:215],v[20:23]\nglobal_load_dwordx2 a[218:219],v248,s[22:23]\nM v[80:83],a[104:107],v[216:219],v[80:83]\nv_exp_f32 v180,v180\nM v[84:87],a[104:107],v[220:223],v[84:87]\nv_exp_f32 v181,v181\nM v[24:27],a[108:111],v[208:211],v[24:27]\n" \
"v_exp_f32 v182,v182\nM v[28:31],a[108:111],v[212:215],v[28:31]\nds_read_b128 a[48:51],v250 offset:12288\nds_read_b128 a[52:55],v250 offset:13312\nM v[88:91],a[108:111],v[216:219],v[88:91]\nv_exp_f32 v183,v183\nM v[92:95],a[108:111],v[220:223],v[92:95]\nv_exp_f32 v168,v168\n" \
"M v[32:35],a[112:115],v[208:211],v[32:35]\nv_exp_f32 v169,v169\nM v[36:39],a[112:115],v[212:215],v[36:39]\nbuffer_load_dwordx4 v240,s[48:51],s95 offen offset:3072 sc1 lds\nM v[96:99],a[112:115],v[216:219],v[96:99]\nv_cvt_pk_bf16_f32 v207,v158,v159\ns_add_u32 s95,s95,0x4000\nM v[100:103],a[112:115],v[220:223],v[100:103]\n" \
"v_exp_f32 v170,v170\nM v[40:43],a[116:119],v[208:211],v[40:43]\nv_exp_f32 v171,v171\nM v[44:47],a[116:119],v[212:215],v[44:47]\nds_read_b128 a[56:59],v250 offset:14336\nds_read_b128 a[60:63],v250 offset:15360\nM v[104:107],a[116:119],v[216:219],v[104:107]\nv_add_u32 v250,s94,v241\n" \
"M v[108:111],a[116:119],v[220:223],v[108:111]\nM v[48:51],a[120:123],v[208:211],v[48:51]\nM v[52:55],a[120:123],v[212:215],v[52:55]\nglobal_load_dwordx2 a[234:235],v248,s[24:25]\nM v[112:115],a[120:123],v[216:219],v[112:115]\nv_add_u32 v248,s71,v248\nM v[116:119],a[120:123],v[220:223],v[116:119]\nM v[56:59],a[124:127],v[208:211],v[56:59]\n" \
"M v[60:63],a[124:127],v[212:215],v[60:63]\nM v[120:123],a[124:127],v[216:219],v[120:123]\nM v[124:127],a[124:127],v[220:223],v[124:127]\nM a[192:195],a[208:211],v[208:211],a[192:195]\nM a[196:199],a[208:211],v[212:215],a[196:199]\nM a[200:203],a[208:211],v[216:219],a[200:203]\nM a[204:207],a[208:211],v[220:223],a[204:207]\ns_waitcnt lgkmcnt(4)\n" \
"M v[128:131],a[0:3],a[128:131],v[224:227]\ns_add_u32 m0,s94,s64\nv_cvt_pk_bf16_f32 v208,v160,v161\nM v[128:131],a[4:7],a[132:135],v[128:131]\nds_read_b128 a[64:67],v250 offset:0\nds_read_b128 a[68:71],v250 offset:1024\nM v[128:131],a[8:11],a[136:139],v[128:131]\nv_cvt_pk_bf16_f32 v209,v162,v163\n" \
"v_cvt_pk_bf16_f32 v212,v164,v165\nM v[128:131],a[12:15],a[140:143],v[128:131]\nv_cvt_pk_bf16_f32 v213,v166,v167\nv_cvt_pk_bf16_f32 v216,v168,v169\nM v[132:135],a[0:3],a[144:147],v[228:231]\nbuffer_load_dwordx4 v240,s[44:47],s95 offen offset:0 sc1 lds\nM v[132:135],a[4:7],a[148:151],v[132:135]\nv_cvt_pk_bf16_f32 v217,v170,v171\n" \
"v_cvt_pk_bf16_f32 v210,v176,v177\nM v[132:135],a[8:11],a[152:155],v[132:135]\nv_exp_f32 v172,v172\nM v[132:135],a[12:15],a[156:159],v[132:135]\nds_read_b128 a[72:75],v250 offset:2048\nds_read_b128 a[76:79],v250 offset:3072\nM v[136:139],a[0:3],a[160:163],v[232:235]\nv_exp_f32 v173,v173\n" \
"M v[136:139],a[4:7],a[164:167],v[136:139]\nv_exp_f32 v174,v174\nM v[136:139],a[8:11],a[168:171],v[136:139]\nv_exp_f32 v175,v175\nM v[136:139],a[12:15],a[172:175],v[136:139]\nv_cvt_pk_bf16_f32 v220,v172,v173\nv_cvt_pk_bf16_f32 v221,v174,v175\nM v[140:143],a[0:3],a[176:179],v[236:239]\n" \
"v_cvt_pk_bf16_f32 v211,v178,v179\nv_cvt_pk_bf16_f32 v214,v180,v181\nM v[140:143],a[4:7],a[180:183],v[140:143]\nds_read_b128 a[80:83],v250 offset:4096\nds_read_b128 a[84:87],v250 offset:5120\nM v[140:143],a[8:11],a[184:187],v[140:143]\nglobal_load_dwordx2 a[220:221],v248,s[22:23]\nM v[140:143],a[12:15],a[188:191],v[140:143]\n" \
"v_cvt_pk_bf16_f32 v215,v182,v183\ns_add_u32 s94,s94,0x4000\nM v[144:147],a[16:19],a[128:131],v[224:227]\nv_exp_f32 v184,v184\nM v[144:147],a[20:23],a[132:135],v[144:147]\nv_exp_f32 v185,v185\nM v[144:147],a[24:27],a[136:139],v[144:147]\nv_exp_f32 v186,v186\n" \
"M v[144:147],a[28:31],a[140:143],v[144:147]\nds_read_b128 a[88:91],v250 offset:6144\nds_read_b128 a[92:95],v250 offset:7168\nM v[148:151],a[16:19],a[144:147],v[228:231]\nv_exp_f32 v187,v187\nM v[148:151],a[20:23],a[148:151],v[148:151]\nv_cvt_pk_bf16_f32 v218,v184,v185\nv_cvt_pk_bf16_f32 v219,v186,v187\n" \
"M v[148:151],a[24:27],a[152:155],v[148:151]\nbuffer_load_dwordx4 v240,s[44:47],s95 offen offset:1024 sc1 lds\nM v[148:151],a[28:31],a[156:159],v[148:151]\nv_exp_f32 v188,v188\nM v[152:155],a[16:19],a[160:163],v[232:235]\nv_exp_f32 v189,v189\nM v[152:155],a[20:23],a[164:167],v[152:155]\nds_read_b128 a[96:99],v250 offset:8192\n" \
"ds_read_b128 a[100:103],v250 offset:9216\nM v[152:155],a[24:27],a[168:171],v[152:155]\nv_exp_f32 v190,v190\nM v[152:155],a[28:31],a[172:175],v[152:155]\nv_exp_f32 v191,v191\nM v[156:159],a[16:19],a[176:179],v[236:239]\nv_cvt_pk_bf16_f32 v222,v188,v189\nv_cvt_pk_bf16_f32 v223,v190,v191\n" \
"M v[156:159],a[20:23],a[180:183],v[156:159]\nv_exp_f32 v128,v128\nM v[156:159],a[24:27],a[184:187],v[156:159]\nv_exp_f32 v129,v129\nM v[156:159],a[28:31],a[188:191],v[156:159]\nds_read_b128 a[104:107],v250 offset:10240\nds_read_b128 a[108:111],v250 offset:11264\nM v[160:163],a[32:35],a[128:131],v[224:227]\n" \
"global_load_dwordx2 a[236:237],v248,s[24:25]\nM v[160:163],a[36:39],a[132:135],v[160:163]\nv_add_u32 v248,s71,v248\ns_and_b32 s94,s94,0xffff\nM v[160:163],a[40:43],a[136:139],v[160:163]\nv_exp_f32 v130,v130\nM v[160:163],a[44:47],a[140:143],v[160:163]\nv_exp_f32 v131,v131\n" \
"M v[164:167],a[32:35],a[144:147],v[228:231]\nv_exp_f32 v144,v144\nM v[164:167],a[36:39],a[148:151],v[164:167]\nds_read_b128 a[112:115],v250 offset:12288\nds_read_b128 a[116:119],v250 offset:13312\nM v[164:167],a[40:43],a[152:155],v[164:167]\nv_exp_f32 v145,v145\nM v[164:167],a[44:47],a[156:159],v[164:167]\n" \
"v_exp_f32 v146,v146\nM v[168:171],a[32:35],a[160:163],v[232:235]\nbuffer_load_dwordx4 v240,s[44:47],s95 offen offset:2048 sc1 lds\nM v[168:171],a[36:39],a[164:167],v[168:171]\nv_exp_f32 v147,v147\nM v[168:171],a[40:43],a[168:171],v[168:171]\nv_exp_f32 v132,v132\nM v[168:171],a[44:47],a[172:175],v[168:171]\n" \
"ds_read_b128 a[120:123],v250 offset:14336\nds_read_b128 a[124:127],v250 offset:15360\nM v[172:175],a[32:35],a[176:179],v[236:239]\nv_exp_f32 v133,v133\nM v[172:175],a[36:39],a[180:183],v[172:175]\nv_exp_f32 v134,v134\nM v[172:175],a[40:43],a[184:187],v[172:175]\nv_exp_f32 v135,v135\n" \
"M v[172:175],a[44:47],a[188:191],v[172:175]\nv_xor_b32 v250,0x18000,v250\ns_waitcnt lgkmcnt(3)\nM v[176:179],a[48:51],a[128:131],v[224:227]\nv_exp_f32 v148,v148\nM v[176:179],a[52:55],a[132:135],v[176:179]\nv_exp_f32 v149,v149\nM v[176:179],a[56:59],a[136:139],v[176:179]\n" \
"global_load_dwordx2 a[222:223],v248,s[22:23]\nM v[176:179],a[60:63],a[140:143],v[176:179]\nv_exp_f32 v150,v150\nM v[180:183],a[48:51],a[144:147],v[228:231]\nv_exp_f32 v151,v151\nM v[180:183],a[52:55],a[148:151],v[180:183]\nv_exp_f32 v136,v136\nM v[180:183],a[56:59],a[152:155],v[180:183]\n" \
"v_exp_f32 v137,v137\nM v[180:183],a[60:63],a[156:159],v[180:183]\nv_exp_f32 v138,v138\nM v[184:187],a[48:51],a[160:163],v[232:235]\nv_exp_f32 v139,v139\nM v[184:187],a[52:55],a[164:167],v[184:187]\nv_exp_f32 v152,v152\nM v[184:187],a[56:59],a[168:171],v[184:187]\n" \
"buffer_load_dwordx4 v240,s[44:47],s95 offen offset:3072 sc1 lds\nM v[184:187],a[60:63],a[172:175],v[184:187]\nv_exp_f32 v153,v153\nM v[188:191],a[48:51],a[176:179],v[236:239]\nv_exp_f32 v154,v154\nM v[188:191],a[52:55],a[180:183],v[188:191]\nv_exp_f32 v155,v155\nM v[188:191],a[56:59],a[184:187],v[188:191]\n" \
"global_load_dwordx2 a[238:239],v248,s[24:25]\nM v[188:191],a[60:63],a[188:191],v[188:191]\ns_add_u32 m0,m0,0x10000\nv_add_u32 v248,s71,v248\nM v[0:3],a[64:67],v[192:195],v[0:3]\ns_waitcnt vmcnt(16) lgkmcnt(0)\ns_barrier\nM v[4:7],a[64:67],v[196:199],v[4:7]\n" \
"ds_read_b128 a[0:3],v250 offset:0\nds_read_b128 a[4:7],v250 offset:1024\nM v[64:67],a[64:67],v[200:203],v[64:67]\nv_exp_f32 v140,v140\nM v[68:71],a[64:67],v[204:207],v[68:71]\nv_exp_f32 v141,v141\nM v[8:11],a[68:71],v[192:195],v[8:11]\nv_exp_f32 v142,v142\n" \
"M v[12:15],a[68:71],v[196:199],v[12:15]\nbuffer_load_dwordx4 v240,s[48:51],s95 offen offset:0 sc1 lds\nM v[72:75],a[68:71],v[200:203],v[72:75]\nv_exp_f32 v143,v143\nM v[76:79],a[68:71],v[204:207],v[76:79]\nv_exp_f32 v156,v156\nM v[16:19],a[72:75],v[192:195],v[16:19]\nv_exp_f32 v157,v157\n" \
"M v[20:23],a[72:75],v[196:199],v[20:23]\nds_read_b128 a[8:11],v250 offset:2048\nds_read_b128 a[12:15],v250 offset:3072\nM v[80:83],a[72:75],v[200:203],v[80:83]\nv_exp_f32 v158,v158\nM v[84:87],a[72:75],v[204:207],v[84:87]\nv_exp_f32 v159,v159\nM v[24:27],a[76:79],v[192:195],v[24:27]\n" \
"v_exp_f32 v160,v160\nM v[28:31],a[76:79],v[196:199],v[28:31]\nglobal_load_dwordx2 a[224:225],v248,s[22:23]\nM v[88:91],a[76:79],v[200:203],v[88:91]\nv_exp_f32 v161,v161\nM v[92:95],a[76:79],v[204:207],v[92:95]\nv_exp_f32 v162,v162\nM v[32:35],a[80:83],v[192:195],v[32:35]\n" \
"v_exp_f32 v163,v163\nM v[36:39],a[80:83],v[196:199],v[36:39]\nds_read_b128 a[16:19],v250 offset:4096\nds_read_b128 a[20:23],v250 offset:5120\nM v[96:99],a[80:83],v[200:203],v[96:99]\nv_exp_f32 v176,v176\nM v[100:103],a[80:83],v[204:207],v[100:103]\nv_exp_f32 v177,v177\n" \
"M v[40:43],a[84:87],v[192:195],v[40:43]\nv_exp_f32 v178,v178\nM v[44:47],a[84:87],v[196:199],v[44:47]\nbuffer_load_dwordx4 v240,s[48:51],s95 offen offset:1024 sc1 lds\nM v[104:107],a[84:87],v[200:203],v[104:107]\nv_exp_f32 v179,v179\nM v[108:111],a[84:87],v[204:207],v[108:111]\nv_exp_f32 v164,v164\n" \
"M v[48:51],a[88:91],v[192:195],v[48:51]\nv_exp_f32 v165,v165\nM v[52:55],a[88:91],v[196:199],v[52:55]\nds_read_b128 a[24:27],v250 offset:6144\nds_read_b128 a[28:31],v250 offset:7168\nM v[112:115],a[88:91],v[200:203],v[112:115]\nv_exp_f32 v166,v166\nM v[116:119],a[88:91],v[204:207],v[116:119]\n" \
"v_exp_f32 v167,v167\nM v[56:59],a[92:95],v[192:195],v[56:59]\nv_exp_f32 v180,v180\nM v[60:63],a[92:95],v[196:199],v[60:63]\nglobal_load_dwordx2 a[240:241],v248,s[24:25]\nM v[120:123],a[92:95],v[200:203],v[120:123]\nv_exp_f32 v181,v181\nM v[124:127],a[92:95],v[204:207],v[124:127]\n" \
"v_exp_f32 v182,v182\nM a[192:195],a[208:211],v[192:195],a[192:195]\nv_add_u32 v248,s71,v248\nv_cvt_pk_bf16_f32 v192,v128,v129\nM a[196:199],a[208:211],v[196:199],a[196:199]\nds_read_b128 a[32:35],v250 offset:8192\nds_read_b128 a[36:39],v250 offset:9216\nM a[200:203],a[208:211],v[200:203],a[200:203]\n" \
"v_cvt_pk_bf16_f32 v193,v130,v131\nv_cvt_pk_bf16_f32 v194,v144,v145\nM a[204:207],a[208:211],v[204:207],a[204:207]\nv_cvt_pk_bf16_f32 v195,v146,v147\nv_cvt_pk_bf16_f32 v196,v132,v133\nM v[0:3],a[96:99],v[208:211],v[0:3]\nv_cvt_pk_bf16_f32 v197,v134,v135\nv_cvt_pk_bf16_f32 v198,v148,v149\n" \
"M v[4:7],a[96:99],v[212:215],v[4:7]\nbuffer_load_dwordx4 v240,s[48:51],s95 offen offset:2048 sc1 lds\nM v[64:67],a[96:99],v[216:219],v[64:67]\nv_cvt_pk_bf16_f32 v199,v150,v151\nv_cvt_pk_bf16_f32 v200,v136,v137\nM v[68:71],a[96:99],v[220:223],v[68:71]\nv_cvt_pk_bf16_f32 v201,v138,v139\nv_cvt_pk_bf16_f32 v202,v152,v153\n" \
"M v[8:11],a[100:103],v[208:211],v[8:11]\nv_cvt_pk_bf16_f32 v203,v154,v155\nv_cvt_pk_bf16_f32 v204,v140,v141\nM v[12:15],a[100:103],v[212:215],v[12:15]\nds_read_b128 a[40:43],v250 offset:10240\nds_read_b128 a[44:47],v250 offset:11264\nM v[72:75],a[100:103],v[216:219],v[72:75]\nv_cvt_pk_bf16_f32 v205,v142,v143\n" \
"v_cvt_pk_bf16_f32 v206,v156,v157\nM v[76:79],a[100:103],v[220:223],v[76:79]\nv_exp_f32 v183,v183\nM v[16:19],a[104:107],v[208:211],v[16:19]\nv_exp_f32 v168,v168\nM v[20:23],a[104:107],v[212:215],v[20:23]\nglobal_load_dwordx2 a[226:227],v248,s[22:23]\nM v[80:83],a[104:107],v[216:219],v[80:83]\n" \
"v_exp_f32 v169,v169\nM v[84:87],a[104:107],v[220:223],v[84:87]\nv_exp_f32 v170,v170\nM v[24:27],a[108:111],v[208:211],v[24:27]\nv_exp_f32 v171,v171\nM v[28:31],a[108:111],v[212:215],v[28:31]\nds_read_b128 a[48:51],v250 offset:12288\nds_read_b128 a[52:55],v250 offset:13312\n" \
"M v[88:91],a[108:111],v[216:219],v[88:91]\nv_cvt_pk_bf16_f32 v207,v158,v159\nM v[92:95],a[108:111],v[220:223],v[92:95]\nM v[32:35],a[112:115],v[208:211],v[32:35]\nM v[36:39],a[112:115],v[212:215],v[36:39]\nbuffer_load_dwordx4 v240,s[48:51],s95 offen offset:3072 sc1 lds\nM v[96:99],a[112:115],v[216:219],v[96:99]\ns_add_u32 s95,s95,0x4000\n" \
"M v[100:103],a[112:115],v[220:223],v[100:103]\nM v[40:43],a[116:119],v[208:211],v[40:43]\nM v[44:47],a[116:119],v[212:215],v[44:47]\nds_read_b128 a[56:59],v250 offset:14336\nds_read_b128 a[60:63],v250 offset:15360\nM v[104:107],a[116:119],v[216:219],v[104:107]\nv_add_u32 v250,s94,v241\nM v[108:111],a[116:119],v[220:223],v[108:111]\n" \
"M v[48:51],a[120:123],v[208:211],v[48:51]\nM v[52:55],a[120:123],v[212:215],v[52:55]\nglobal_load_dwordx2 a[242:243],v248,s[24:25]\nM v[112:115],a[120:123],v[216:219],v[112:115]\nv_add_u32 v248,s71,v248\nM v[116:119],a[120:123],v[220:223],v[116:119]\nM v[56:59],a[124:127],v[208:211],v[56:59]\nM v[60:63],a[124:127],v[212:215],v[60:63]\n" \
"M v[120:123],a[124:127],v[216:219],v[120:123]\nM v[124:127],a[124:127],v[220:223],v[124:127]\nM a[192:195],a[208:211],v[208:211],a[192:195]\nM a[196:199],a[208:211],v[212:215],a[196:199]\nM a[200:203],a[208:211],v[216:219],a[200:203]\nM a[204:207],a[208:211],v[220:223],a[204:207]\ns_cmp_eq_u32 s62,7\ns_cbranch_scc0 .Ltrans\n" \
"s_waitcnt lgkmcnt(4)\nM v[136:139],a[0:3],a[160:163],v[232:235]\ns_add_u32 m0,s94,s64\nv_cvt_pk_bf16_f32 v216,v168,v169\nM v[136:139],a[4:7],a[164:167],v[136:139]\nds_read_b128 a[64:67],v250 offset:0\nds_read_b128 a[68:71],v250 offset:1024\nM v[136:139],a[8:11],a[168:171],v[136:139]\n" \
"buffer_load_dwordx4 v240,s[44:47],s95 offen offset:0 sc1 lds\nM v[136:139],a[12:15],a[172:175],v[136:139]\nv_cvt_pk_bf16_f32 v217,v170,v171\ns_add_u32 s71,s33,16384\nM v[140:143],a[0:3],a[176:179],v[236:239]\nds_read_b128 a[72:75],v250 offset:2048\nds_read_b128 a[76:79],v250 offset:3072\nM v[140:143],a[4:7],a[180:183],v[140:143]\n" \
"buffer_store_dwordx4 a[248:251],v253,s[52:55],s33 offen offset:0 sc1\nM v[140:143],a[8:11],a[184:187],v[140:143]\nv_exp_f32 v172,v172\nM v[140:143],a[12:15],a[188:191],v[140:143]\nds_read_b128 a[80:83],v250 offset:4096\nds_read_b128 a[84:87],v250 offset:5120\nM v[152:155],a[16:19],a[160:163],v[232:235]\nv_exp_f32 v173,v173\n" \
"M v[152:155],a[20:23],a[164:167],v[152:155]\nv_exp_f32 v174,v174\nM v[152:155],a[24:27],a[168:171],v[152:155]\nds_read_b128 a[88:91],v250 offset:6144\nds_read_b128 a[92:95],v250 offset:7168\nM v[152:155],a[28:31],a[172:175],v[152:155]\nbuffer_load_dwordx4 v240,s[44:47],s95 offen offset:1024 sc1 lds\nM v[156:159],a[16:19],a[176:179],v[236:239]\n" \
"v_exp_f32 v175,v175\nM v[156:159],a[20:23],a[180:183],v[156:159]\nds_read_b128 a[96:99],v250 offset:8192\nds_read_b128 a[100:103],v250 offset:9216\nM v[156:159],a[24:27],a[184:187],v[156:159]\nbuffer_store_dwordx4 a[248:251],v253,s[52:55],s33 offen offset:1024 sc1\nM v[156:159],a[28:31],a[188:191],v[156:159]\nv_cvt_pk_bf16_f32 v220,v172,v173\n" \
"v_cvt_pk_bf16_f32 v221,v174,v175\nM v[168:171],a[32:35],a[160:163],v[232:235]\nds_read_b128 a[104:107],v250 offset:10240\nds_read_b128 a[108:111],v250 offset:11264\nM v[168:171],a[36:39],a[164:167],v[168:171]\nv_exp_f32 v184,v184\nM v[168:171],a[40:43],a[168:171],v[168:171]\nv_exp_f32 v185,v185\n" \
"M v[168:171],a[44:47],a[172:175],v[168:171]\nds_read_b128 a[112:115],v250 offset:12288\nds_read_b128 a[116:119],v250 offset:13312\nM v[172:175],a[32:35],a[176:179],v[236:239]\nbuffer_load_dwordx4 v240,s[44:47],s95 offen offset:2048 sc1 lds\nM v[172:175],a[36:39],a[180:183],v[172:175]\nv_exp_f32 v186,v186\nM v[172:175],a[40:43],a[184:187],v[172:175]\n" \
"v_exp_f32 v187,v187\nds_read_b128 a[120:123],v250 offset:14336\nds_read_b128 a[124:127],v250 offset:15360\nM v[172:175],a[44:47],a[188:191],v[172:175]\nbuffer_store_dwordx4 a[248:251],v253,s[52:55],s33 offen offset:2048 sc1\nv_cvt_pk_bf16_f32 v218,v184,v185\nv_cvt_pk_bf16_f32 v219,v186,v187\ns_waitcnt lgkmcnt(6)\n" \
"M v[184:187],a[48:51],a[160:163],v[232:235]\nv_exp_f32 v188,v188\nM v[184:187],a[52:55],a[164:167],v[184:187]\nv_exp_f32 v189,v189\nM v[184:187],a[56:59],a[168:171],v[184:187]\nv_exp_f32 v190,v190\nv_exp_f32 v191,v191\nM v[184:187],a[60:63],a[172:175],v[184:187]\n" \
"v_cvt_pk_bf16_f32 v222,v188,v189\nv_cvt_pk_bf16_f32 v223,v190,v191\nM v[188:191],a[48:51],a[176:179],v[236:239]\nv_xor_b32 v250,0x18000,v250\nv_cvt_pk_bf16_f32 v208,v160,v161\nM v[188:191],a[52:55],a[180:183],v[188:191]\nbuffer_load_dwordx4 v240,s[44:47],s95 offen offset:3072 sc1 lds\nM v[188:191],a[56:59],a[184:187],v[188:191]\n" \
"s_add_u32 m0,m0,0x10000\nv_cvt_pk_bf16_f32 v209,v162,v163\nM v[188:191],a[60:63],a[188:191],v[188:191]\nv_cvt_pk_bf16_f32 v210,v176,v177\nv_cvt_pk_bf16_f32 v211,v178,v179\nM v[0:3],a[64:67],v[192:195],v[0:3]\nbuffer_store_dwordx4 a[248:251],v253,s[52:55],s33 offen offset:3072 sc1\nM v[4:7],a[64:67],v[196:199],v[4:7]\n" \
"v_cvt_pk_bf16_f32 v212,v164,v165\nv_cvt_pk_bf16_f32 v213,v166,v167\nM v[64:67],a[64:67],v[200:203],v[64:67]\ns_waitcnt vmcnt(16) lgkmcnt(0)\ns_barrier\nM v[68:71],a[64:67],v[204:207],v[68:71]\nds_read_b128 a[0:3],v250 offset:0\nds_read_b128 a[4:7],v250 offset:1024\n" \
"M v[8:11],a[68:71],v[192:195],v[8:11]\nv_cvt_pk_bf16_f32 v214,v180,v181\nv_cvt_pk_bf16_f32 v215,v182,v183\nM v[12:15],a[68:71],v[196:199],v[12:15]\nbuffer_store_dwordx4 a[248:251],v253,s[52:55],s71 offen offset:0 sc1\nM v[72:75],a[68:71],v[200:203],v[72:75]\ns_add_u32 s70,s78,16384\ns_add_u32 s94,s94,0x4000\n" \
"M v[76:79],a[68:71],v[204:207],v[76:79]\nbuffer_load_dwordx4 v240,s[48:51],s95 offen offset:0 sc1 lds\nM v[16:19],a[72:75],v[192:195],v[16:19]\nv_exp_f32 v136,v136\nM v[20:23],a[72:75],v[196:199],v[20:23]\nbuffer_store_dwordx4 a[248:251],v253,s[52:55],s71 offen offset:1024 sc1\nM v[80:83],a[72:75],v[200:203],v[80:83]\nv_exp_f32 v137,v137\n" \
"M v[84:87],a[72:75],v[204:207],v[84:87]\nds_read_b128 a[8:11],v250 offset:2048\nds_read_b128 a[12:15],v250 offset:3072\nM v[24:27],a[76:79],v[192:195],v[24:27]\nv_exp_f32 v138,v138\nM v[28:31],a[76:79],v[196:199],v[28:31]\nbuffer_store_dwordx4 a[248:251],v253,s[52:55],s71 offen offset:2048 sc1\nM v[88:91],a[76:79],v[200:203],v[88:91]\n" \
"v_exp_f32 v139,v139\nM v[92:95],a[76:79],v[204:207],v[92:95]\nv_exp_f32 v152,v152\nM v[32:35],a[80:83],v[192:195],v[32:35]\nv_exp_f32 v153,v153\nM v[36:39],a[80:83],v[196:199],v[36:39]\nbuffer_store_dwordx4 a[248:251],v253,s[52:55],s71 offen offset:3072 sc1\nM v[96:99],a[80:83],v[200:203],v[96:99]\n" \
"v_exp_f32 v154,v154\nM v[100:103],a[80:83],v[204:207],v[100:103]\nds_read_b128 a[16:19],v250 offset:4096\nds_read_b128 a[20:23],v250 offset:5120\nM v[40:43],a[84:87],v[192:195],v[40:43]\nv_exp_f32 v155,v155\nM v[44:47],a[84:87],v[196:199],v[44:47]\nv_exp_f32 v140,v140\n" \
"M v[104:107],a[84:87],v[200:203],v[104:107]\nv_exp_f32 v141,v141\nM v[108:111],a[84:87],v[204:207],v[108:111]\nbuffer_load_dwordx4 v240,s[48:51],s95 offen offset:1024 sc1 lds\nM v[48:51],a[88:91],v[192:195],v[48:51]\nv_exp_f32 v142,v142\nM v[52:55],a[88:91],v[196:199],v[52:55]\nbuffer_store_dwordx4 a[248:251],v253,s[52:55],s78 offen offset:0 sc1\n" \
"M v[112:115],a[88:91],v[200:203],v[112:115]\nv_exp_f32 v143,v143\nM v[116:119],a[88:91],v[204:207],v[116:119]\nds_read_b128 a[24:27],v250 offset:6144\nds_read_b128 a[28:31],v250 offset:7168\nM v[56:59],a[92:95],v[192:195],v[56:59]\nv_exp_f32 v156,v156\nM v[60:63],a[92:95],v[196:199],v[60:63]\n" \
"buffer_store_dwordx4 a[248:251],v253,s[52:55],s78 offen offset:1024 sc1\nM v[120:123],a[92:95],v[200:203],v[120:123]\nv_exp_f32 v157,v157\nM v[124:127],a[92:95],v[204:207],v[124:127]\nv_exp_f32 v158,v158\nM a[192:195],a[208:211],v[192:195],a[192:195]\nv_exp_f32 v159,v159\nM a[196:199],a[208:211],v[196:199],a[196:199]\n" \
"buffer_store_dwordx4 a[248:251],v253,s[52:55],s78 offen offset:2048 sc1\nM a[200:203],a[208:211],v[200:203],a[200:203]\ns_and_b32 s94,s94,0xffff\nv_cvt_pk_bf16_f32 v200,v136,v137\nM a[204:207],a[208:211],v[204:207],a[204:207]\nds_read_b128 a[32:35],v250 offset:8192\nds_read_b128 a[36:39],v250 offset:9216\nM v[0:3],a[96:99],v[208:211],v[0:3]\n" \
"v_cvt_pk_bf16_f32 v201,v138,v139\nv_cvt_pk_bf16_f32 v202,v152,v153\nM v[4:7],a[96:99],v[212:215],v[4:7]\nv_cvt_pk_bf16_f32 v203,v154,v155\nv_cvt_pk_bf16_f32 v204,v140,v141\nM v[64:67],a[96:99],v[216:219],v[64:67]\nv_cvt_pk_bf16_f32 v205,v142,v143\nv_cvt_pk_bf16_f32 v206,v156,v157\n" \
"M v[68:71],a[96:99],v[220:223],v[68:71]\nbuffer_load_dwordx4 v240,s[48:51],s95 offen offset:2048 sc1 lds\nM v[8:11],a[100:103],v[208:211],v[8:11]\nv_exp_f32 v168,v168\nM v[12:15],a[100:103],v[212:215],v[12:15]\nbuffer_store_dwordx4 a[248:251],v253,s[52:55],s78 offen offset:3072 sc1\nM v[72:75],a[100:103],v[216:219],v[72:75]\nv_exp_f32 v169,v169\n" \
"M v[76:79],a[100:103],v[220:223],v[76:79]\nds_read_b128 a[40:43],v250 offset:10240\nds_read_b128 a[44:47],v250 offset:11264\nM v[16:19],a[104:107],v[208:211],v[16:19]\nv_exp_f32 v170,v170\nM v[20:23],a[104:107],v[212:215],v[20:23]\nbuffer_store_dwordx4 a[248:251],v253,s[52:55],s70 offen offset:0 sc1\nM v[80:83],a[104:107],v[216:219],v[80:83]\n" \
"v_exp_f32 v171,v171\nM v[84:87],a[104:107],v[220:223],v[84:87]\nv_exp_f32 v184,v184\nM v[24:27],a[108:111],v[208:211],v[24:27]\nv_exp_f32 v185,v185\nM v[28:31],a[108:111],v[212:215],v[28:31]\nbuffer_store_dwordx4 a[248:251],v253,s[52:55],s70 offen offset:1024 sc1\nM v[88:91],a[108:111],v[216:219],v[88:91]\n" \
"v_exp_f32 v186,v186\nM v[92:95],a[108:111],v[220:223],v[92:95]\nds_read_b128 a[48:51],v250 offset:12288\nds_read_b128 a[52:55],v250 offset:13312\nM v[32:35],a[112:115],v[208:211],v[32:35]\nv_exp_f32 v187,v187\nM v[36:39],a[112:115],v[212:215],v[36:39]\nv_cvt_pk_bf16_f32 v207,v158,v159\n" \
"M v[96:99],a[112:115],v[216:219],v[96:99]\nM v[100:103],a[112:115],v[220:223],v[100:103]\nbuffer_load_dwordx4 v240,s[48:51],s95 offen offset:3072 sc1 lds\nM v[40:43],a[116:119],v[208:211],v[40:43]\ns_add_u32 s95,s95,0xffff4000\nM v[44:47],a[116:119],v[212:215],v[44:47]\nbuffer_store_dwordx4 a[248:251],v253,s[52:55],s70 offen offset:2048 sc1\nM v[104:107],a[116:119],v[216:219],v[104:107]\n" \
"M v[108:111],a[116:119],v[220:223],v[108:111]\nds_read_b128 a[56:59],v250 offset:14336\nds_read_b128 a[60:63],v250 offset:15360\nM v[48:51],a[120:123],v[208:211],v[48:51]\nv_add_u32 v250,s94,v241\nM v[52:55],a[120:123],v[212:215],v[52:55]\nbuffer_store_dwordx4 a[248:251],v253,s[52:55],s70 offen offset:3072 sc1\nM v[112:115],a[120:123],v[216:219],v[112:115]\n" \
"M v[116:119],a[120:123],v[220:223],v[116:119]\nM v[56:59],a[124:127],v[208:211],v[56:59]\nM v[60:63],a[124:127],v[212:215],v[60:63]\nM v[120:123],a[124:127],v[216:219],v[120:123]\nM v[124:127],a[124:127],v[220:223],v[124:127]\nM a[192:195],a[208:211],v[208:211],a[192:195]\nM a[196:199],a[208:211],v[212:215],a[196:199]\nM a[200:203],a[208:211],v[216:219],a[200:203]\n" \
"M a[204:207],a[208:211],v[220:223],a[204:207]\n.Lsl:\ns_waitcnt lgkmcnt(4)\nM v[136:139],a[0:3],a[160:163],v[232:235]\ns_add_u32 m0,s94,s64\nv_cvt_pk_bf16_f32 v216,v168,v169\nM v[136:139],a[4:7],a[164:167],v[136:139]\nds_read_b128 a[64:67],v250 offset:0\n" \
"ds_read_b128 a[68:71],v250 offset:1024\nM v[136:139],a[8:11],a[168:171],v[136:139]\nbuffer_load_dwordx4 v240,s[44:47],s95 offen offset:0 sc1 lds\nM v[136:139],a[12:15],a[172:175],v[136:139]\nv_cvt_pk_bf16_f32 v217,v170,v171\nv_cvt_pk_bf16_f32 v218,v184,v185\nM v[140:143],a[0:3],a[176:179],v[236:239]\nds_read_b128 a[72:75],v250 offset:2048\n" \
"ds_read_b128 a[76:79],v250 offset:3072\nM v[140:143],a[4:7],a[180:183],v[140:143]\nv_exp_f32 v172,v172\nM v[140:143],a[8:11],a[184:187],v[140:143]\nv_exp_f32 v173,v173\nM v[140:143],a[12:15],a[188:191],v[140:143]\nds_read_b128 a[80:83],v250 offset:4096\nds_read_b128 a[84:87],v250 offset:5120\n" \
"M v[152:155],a[16:19],a[160:163],v[232:235]\nv_exp_f32 v174,v174\nM v[152:155],a[20:23],a[164:167],v[152:155]\nv_exp_f32 v175,v175\nM v[152:155],a[24:27],a[168:171],v[152:155]\nds_read_b128 a[88:91],v250 offset:6144\nds_read_b128 a[92:95],v250 offset:7168\nM v[152:155],a[28:31],a[172:175],v[152:155]\n" \
"buffer_load_dwordx4 v240,s[44:47],s95 offen offset:1024 sc1 lds\nM v[156:159],a[16:19],a[176:179],v[236:239]\nv_cvt_pk_bf16_f32 v220,v172,v173\nv_cvt_pk_bf16_f32 v221,v174,v175\nM v[156:159],a[20:23],a[180:183],v[156:159]\nds_read_b128 a[96:99],v250 offset:8192\nds_read_b128 a[100:103],v250 offset:9216\nM v[156:159],a[24:27],a[184:187],v[156:159]\n" \
"v_cvt_pk_bf16_f32 v219,v186,v187\ns_add_u32 s94,s94,0x4000\nM v[156:159],a[28:31],a[188:191],v[156:159]\nv_exp_f32 v188,v188\nM v[168:171],a[32:35],a[160:163],v[232:235]\nds_read_b128 a[104:107],v250 offset:10240\nds_read_b128 a[108:111],v250 offset:11264\nM v[168:171],a[36:39],a[164:167],v[168:171]\n" \
"v_exp_f32 v189,v189\nM v[168:171],a[40:43],a[168:171],v[168:171]\nv_exp_f32 v190,v190\nM v[168:171],a[44:47],a[172:175],v[168:171]\nds_read_b128 a[112:115],v250 offset:12288\nds_read_b128 a[116:119],v250 offset:13312\nM v[172:175],a[32:35],a[176:179],v[236:239]\nbuffer_load_dwordx4 v240,s[44:47],s95 offen offset:2048 sc1 lds\n" \
"M v[172:175],a[36:39],a[180:183],v[172:175]\nv_exp_f32 v191,v191\nM v[172:175],a[40:43],a[184:187],v[172:175]\nds_read_b128 a[120:123],v250 offset:14336\nds_read_b128 a[124:127],v250 offset:15360\nM v[172:175],a[44:47],a[188:191],v[172:175]\nv_cvt_pk_bf16_f32 v222,v188,v189\ns_waitcnt lgkmcnt(7)\n" \
"M v[184:187],a[48:51],a[160:163],v[232:235]\nv_cvt_pk_bf16_f32 v223,v190,v191\nv_xor_b32 v250,0x18000,v250\nM v[184:187],a[52:55],a[164:167],v[184:187]\nv_exp_f32 v136,v136\nM v[184:187],a[56:59],a[168:171],v[184:187]\nv_exp_f32 v137,v137\nM v[184:187],a[60:63],a[172:175],v[184:187]\n" \
"v_exp_f32 v138,v138\nM v[188:191],a[48:51],a[176:179],v[236:239]\nv_exp_f32 v139,v139\nM v[188:191],a[52:55],a[180:183],v[188:191]\nbuffer_load_dwordx4 v240,s[44:47],s95 offen offset:3072 sc1 lds\nM v[188:191],a[56:59],a[184:187],v[188:191]\ns_add_u32 m0,m0,0x10000\ns_and_b32 s94,s94,0xffff\n" \
"M v[188:191],a[60:63],a[188:191],v[188:191]\nv_exp_f32 v152,v152\nM v[64:67],a[64:67],v[200:203],v[64:67]\nv_exp_f32 v153,v153\nM v[68:71],a[64:67],v[204:207],v[68:71]\nv_exp_f32 v154,v154\nM v[72:75],a[68:71],v[200:203],v[72:75]\ns_waitcnt vmcnt(16) lgkmcnt(0)\n" \
"s_barrier\nM v[76:79],a[68:71],v[204:207],v[76:79]\nds_read_b128 a[0:3],v250 offset:0\nds_read_b128 a[4:7],v250 offset:1024\nM v[80:83],a[72:75],v[200:203],v[80:83]\nv_exp_f32 v155,v155\nM v[84:87],a[72:75],v[204:207],v[84:87]\nbuffer_load_dwordx4 v240,s[48:51],s95 offen offset:0 sc1 lds\n" \
"M v[88:91],a[76:79],v[200:203],v[88:91]\nv_exp_f32 v140,v140\nM v[92:95],a[76:79],v[204:207],v[92:95]\nds_read_b128 a[8:11],v250 offset:2048\nds_read_b128 a[12:15],v250 offset:3072\nM v[96:99],a[80:83],v[200:203],v[96:99]\nv_exp_f32 v141,v141\nM v[100:103],a[80:83],v[204:207],v[100:103]\n" \
"v_exp_f32 v142,v142\nM v[104:107],a[84:87],v[200:203],v[104:107]\nv_exp_f32 v143,v143\nM v[108:111],a[84:87],v[204:207],v[108:111]\nds_read_b128 a[16:19],v250 offset:4096\nds_read_b128 a[20:23],v250 offset:5120\nM v[112:115],a[88:91],v[200:203],v[112:115]\nv_exp_f32 v156,v156\n" \
"M v[116:119],a[88:91],v[204:207],v[116:119]\nbuffer_load_dwordx4 v240,s[48:51],s95 offen offset:1024 sc1 lds\nM v[120:123],a[92:95],v[200:203],v[120:123]\nv_exp_f32 v157,v157\nM v[124:127],a[92:95],v[204:207],v[124:127]\nds_read_b128 a[24:27],v250 offset:6144\nds_read_b128 a[28:31],v250 offset:7168\nM a[200:203],a[208:211],v[200:203],a[200:203]\n" \
"v_exp_f32 v158,v158\nM a[204:207],a[208:211],v[204:207],a[204:207]\nv_exp_f32 v159,v159\nM v[64:67],a[96:99],v[216:219],v[64:67]\nv_cvt_pk_bf16_f32 v200,v136,v137\nv_cvt_pk_bf16_f32 v201,v138,v139\nM v[68:71],a[96:99],v[220:223],v[68:71]\nds_read_b128 a[32:35],v250 offset:8192\n" \
"ds_read_b128 a[36:39],v250 offset:9216\nM v[72:75],a[100:103],v[216:219],v[72:75]\nv_cvt_pk_bf16_f32 v202,v152,v153\nv_cvt_pk_bf16_f32 v203,v154,v155\nM v[76:79],a[100:103],v[220:223],v[76:79]\nbuffer_load_dwordx4 v240,s[48:51],s95 offen offset:2048 sc1 lds\nM v[80:83],a[104:107],v[216:219],v[80:83]\nv_cvt_pk_bf16_f32 v204,v140,v141\n" \
"v_cvt_pk_bf16_f32 v205,v142,v143\nM v[84:87],a[104:107],v[220:223],v[84:87]\nds_read_b128 a[40:43],v250 offset:10240\nds_read_b128 a[44:47],v250 offset:11264\nM v[88:91],a[108:111],v[216:219],v[88:91]\nv_cvt_pk_bf16_f32 v206,v156,v157\nv_cvt_pk_bf16_f32 v207,v158,v159\nM v[92:95],a[108:111],v[220:223],v[92:95]\n" \
"v_exp_f32 v168,v168\nM v[96:99],a[112:115],v[216:219],v[96:99]\nv_exp_f32 v169,v169\nM v[100:103],a[112:115],v[220:223],v[100:103]\nds_read_b128 a[48:51],v250 offset:12288\nds_read_b128 a[52:55],v250 offset:13312\nM v[104:107],a[116:119],v[216:219],v[104:107]\nv_exp_f32 v170,v170\n" \
"M v[108:111],a[116:119],v[220:223],v[108:111]\nbuffer_load_dwordx4 v240,s[48:51],s95 offen offset:3072 sc1 lds\nM v[112:115],a[120:123],v[216:219],v[112:115]\nv_exp_f32 v171,v171\nM v[116:119],a[120:123],v[220:223],v[116:119]\nds_read_b128 a[56:59],v250 offset:14336\nds_read_b128 a[60:63],v250 offset:15360\nM v[120:123],a[124:127],v[216:219],v[120:123]\n" \
"v_exp_f32 v184,v184\nM v[124:127],a[124:127],v[220:223],v[124:127]\nv_exp_f32 v185,v185\nM a[200:203],a[208:211],v[216:219],a[200:203]\nv_exp_f32 v186,v186\nv_exp_f32 v187,v187\nv_add_u32 v250,s94,v241\ns_add_u32 s95,s95,0x4000\n" \
"M a[204:207],a[208:211],v[220:223],a[204:207]\ns_cmp_lg_u32 s95,s96\ns_cbranch_scc1 .Lsl\ns_waitcnt lgkmcnt(4)\nM v[128:131],a[0:3],a[128:131],v[224:227]\ns_add_u32 m0,s94,s64\nv_cvt_pk_bf16_f32 v216,v168,v169\nM v[128:131],a[4:7],a[132:135],v[128:131]\n" \
"ds_read_b128 a[64:67],v250 offset:0\nds_read_b128 a[68:71],v250 offset:1024\nM v[128:131],a[8:11],a[136:139],v[128:131]\nbuffer_load_dwordx4 v240,s[44:47],s95 offen offset:0 sc1 lds\nM v[128:131],a[12:15],a[140:143],v[128:131]\nv_exp_f32 v188,v188\nM v[132:135],a[0:3],a[144:147],v[228:231]\nds_read_b128 a[72:75],v250 offset:2048\n" \
"ds_read_b128 a[76:79],v250 offset:3072\nM v[132:135],a[4:7],a[148:151],v[132:135]\nv_exp_f32 v189,v189\nM v[132:135],a[8:11],a[152:155],v[132:135]\nv_exp_f32 v190,v190\nM v[132:135],a[12:15],a[156:159],v[132:135]\nds_read_b128 a[80:83],v250 offset:4096\nds_read_b128 a[84:87],v250 offset:5120\n" \
"M v[144:147],a[16:19],a[128:131],v[224:227]\nv_exp_f32 v191,v191\nM v[144:147],a[20:23],a[132:135],v[144:147]\nv_exp_f32 v172,v172\nM v[144:147],a[24:27],a[136:139],v[144:147]\nds_read_b128 a[88:91],v250 offset:6144\nds_read_b128 a[92:95],v250 offset:7168\nM v[144:147],a[28:31],a[140:143],v[144:147]\n" \
"buffer_load_dwordx4 v240,s[44:47],s95 offen offset:1024 sc1 lds\nM v[148:151],a[16:19],a[144:147],v[228:231]\nv_exp_f32 v173,v173\nM v[148:151],a[20:23],a[148:151],v[148:151]\nds_read_b128 a[96:99],v250 offset:8192\nds_read_b128 a[100:103],v250 offset:9216\nM v[148:151],a[24:27],a[152:155],v[148:151]\nv_exp_f32 v174,v174\n" \
"M v[148:151],a[28:31],a[156:159],v[148:151]\nv_exp_f32 v175,v175\nM v[160:163],a[32:35],a[128:131],v[224:227]\nds_read_b128 a[104:107],v250 offset:10240\nds_read_b128 a[108:111],v250 offset:11264\nM v[160:163],a[36:39],a[132:135],v[160:163]\nv_cvt_pk_bf16_f32 v217,v170,v171\nv_cvt_pk_bf16_f32 v218,v184,v185\n" \
"M v[160:163],a[40:43],a[136:139],v[160:163]\nv_cvt_pk_bf16_f32 v219,v186,v187\nv_cvt_pk_bf16_f32 v220,v172,v173\nM v[160:163],a[44:47],a[140:143],v[160:163]\nds_read_b128 a[112:115],v250 offset:12288\nds_read_b128 a[116:119],v250 offset:13312\nM v[164:167],a[32:35],a[144:147],v[228:231]\nbuffer_load_dwordx4 v240,s[44:47],s95 offen offset:2048 sc1 lds\n" \
"M v[164:167],a[36:39],a[148:151],v[164:167]\nv_cvt_pk_bf16_f32 v221,v174,v175\nv_cvt_pk_bf16_f32 v222,v188,v189\nM v[164:167],a[40:43],a[152:155],v[164:167]\nds_read_b128 a[120:123],v250 offset:14336\nds_read_b128 a[124:127],v250 offset:15360\nM v[164:167],a[44:47],a[156:159],v[164:167]\nv_xor_b32 v250,0x18000,v250\n" \
"s_waitcnt lgkmcnt(7)\nM v[176:179],a[48:51],a[128:131],v[224:227]\nv_cvt_pk_bf16_f32 v223,v190,v191\ns_add_u32 s94,s94,0x4000\nM v[176:179],a[52:55],a[132:135],v[176:179]\nv_exp_f32 v128,v128\nM v[176:179],a[56:59],a[136:139],v[176:179]\nv_exp_f32 v129,v129\n" \
"M v[176:179],a[60:63],a[140:143],v[176:179]\nv_exp_f32 v130,v130\nM v[180:183],a[48:51],a[144:147],v[228:231]\nv_exp_f32 v131,v131\nM v[180:183],a[52:55],a[148:151],v[180:183]\nbuffer_load_dwordx4 v240,s[44:47],s95 offen offset:3072 sc1 lds\nM v[180:183],a[56:59],a[152:155],v[180:183]\ns_add_u32 m0,m0,0x10000\n" \
"s_and_b32 s94,s94,0xffff\nM v[180:183],a[60:63],a[156:159],v[180:183]\nv_exp_f32 v144,v144\nM v[64:67],a[64:67],v[200:203],v[64:67]\nv_exp_f32 v145,v145\nM v[68:71],a[64:67],v[204:207],v[68:71]\nv_exp_f32 v146,v146\nM v[72:75],a[68:71],v[200:203],v[72:75]\n" \
"s_waitcnt vmcnt(16) lgkmcnt(0)\ns_barrier\nM v[76:79],a[68:71],v[204:207],v[76:79]\nds_read_b128 a[0:3],v250 offset:0\nds_read_b128 a[4:7],v250 offset:1024\nM v[80:83],a[72:75],v[200:203],v[80:83]\nv_exp_f32 v147,v147\nM v[84:87],a[72:75],v[204:207],v[84:87]\n" \
"buffer_load_dwordx4 v240,s[48:51],s95 offen offset:0 sc1 lds\nM v[88:91],a[76:79],v[200:203],v[88:91]\nv_exp_f32 v132,v132\nM v[92:95],a[76:79],v[204:207],v[92:95]\nds_read_b128 a[8:11],v250 offset:2048\nds_read_b128 a[12:15],v250 offset:3072\nM v[96:99],a[80:83],v[200:203],v[96:99]\nv_exp_f32 v133,v133\n" \
"M v[100:103],a[80:83],v[204:207],v[100:103]\nv_exp_f32 v134,v134\nM v[104:107],a[84:87],v[200:203],v[104:107]\nv_exp_f32 v135,v135\nM v[108:111],a[84:87],v[204:207],v[108:111]\nds_read_b128 a[16:19],v250 offset:4096\nds_read_b128 a[20:23],v250 offset:5120\nM v[112:115],a[88:91],v[200:203],v[112:115]\n" \
"v_exp_f32 v148,v148\nM v[116:119],a[88:91],v[204:207],v[116:119]\nbuffer_load_dwordx4 v240,s[48:51],s95 offen offset:1024 sc1 lds\nM v[120:123],a[92:95],v[200:203],v[120:123]\nv_exp_f32 v149,v149\nM v[124:127],a[92:95],v[204:207],v[124:127]\nds_read_b128 a[24:27],v250 offset:6144\nds_read_b128 a[28:31],v250 offset:7168\n" \
"M a[200:203],a[208:211],v[200:203],a[200:203]\nv_exp_f32 v150,v150\nM a[204:207],a[208:211],v[204:207],a[204:207]\nv_exp_f32 v151,v151\nM v[64:67],a[96:99],v[216:219],v[64:67]\nv_cvt_pk_bf16_f32 v192,v128,v129\nv_cvt_pk_bf16_f32 v193,v130,v131\nM v[68:71],a[96:99],v[220:223],v[68:71]\n" \
"ds_read_b128 a[32:35],v250 offset:8192\nds_read_b128 a[36:39],v250 offset:9216\nM v[72:75],a[100:103],v[216:219],v[72:75]\nv_cvt_pk_bf16_f32 v194,v144,v145\nv_cvt_pk_bf16_f32 v195,v146,v147\nM v[76:79],a[100:103],v[220:223],v[76:79]\nbuffer_load_dwordx4 v240,s[48:51],s95 offen offset:2048 sc1 lds\nM v[80:83],a[104:107],v[216:219],v[80:83]\n" \
"v_cvt_pk_bf16_f32 v196,v132,v133\nv_cvt_pk_bf16_f32 v197,v134,v135\nM v[84:87],a[104:107],v[220:223],v[84:87]\nds_read_b128 a[40:43],v250 offset:10240\nds_read_b128 a[44:47],v250 offset:11264\nM v[88:91],a[108:111],v[216:219],v[88:91]\nv_cvt_pk_bf16_f32 v198,v148,v149\nv_cvt_pk_bf16_f32 v199,v150,v151\n" \
"M v[92:95],a[108:111],v[220:223],v[92:95]\nv_exp_f32 v160,v160\nM v[96:99],a[112:115],v[216:219],v[96:99]\nv_exp_f32 v161,v161\nM v[100:103],a[112:115],v[220:223],v[100:103]\nds_read_b128 a[48:51],v250 offset:12288\nds_read_b128 a[52:55],v250 offset:13312\nM v[104:107],a[116:119],v[216:219],v[104:107]\n" \
"v_exp_f32 v162,v162\nM v[108:111],a[116:119],v[220:223],v[108:111]\nbuffer_load_dwordx4 v240,s[48:51],s95 offen offset:3072 sc1 lds\nM v[112:115],a[120:123],v[216:219],v[112:115]\nv_exp_f32 v163,v163\nM v[116:119],a[120:123],v[220:223],v[116:119]\nds_read_b128 a[56:59],v250 offset:14336\nds_read_b128 a[60:63],v250 offset:15360\n" \
"M v[120:123],a[124:127],v[216:219],v[120:123]\nv_exp_f32 v176,v176\nM v[124:127],a[124:127],v[220:223],v[124:127]\nv_exp_f32 v177,v177\nM a[200:203],a[208:211],v[216:219],a[200:203]\nv_exp_f32 v178,v178\nv_exp_f32 v179,v179\nv_add_u32 v250,s94,v241\n" \
"s_add_u32 s95,s95,0x4000\nM a[204:207],a[208:211],v[220:223],a[204:207]\ns_branch .Lse\n.Ltrans:\ns_waitcnt lgkmcnt(4)\nM v[128:131],a[0:3],a[128:131],v[224:227]\ns_add_u32 m0,s94,s64\nv_cvt_pk_bf16_f32 v208,v160,v161\n" \
"M v[128:131],a[4:7],a[132:135],v[128:131]\nds_read_b128 a[64:67],v250 offset:0\nds_read_b128 a[68:71],v250 offset:1024\nM v[128:131],a[8:11],a[136:139],v[128:131]\nbuffer_load_dwordx4 v240,s[44:47],s95 offen offset:0 sc1 lds\nM v[128:131],a[12:15],a[140:143],v[128:131]\nv_cvt_pk_bf16_f32 v209,v162,v163\nv_cvt_pk_bf16_f32 v212,v164,v165\n" \
"M v[132:135],a[0:3],a[144:147],v[228:231]\nds_read_b128 a[72:75],v250 offset:2048\nds_read_b128 a[76:79],v250 offset:3072\nM v[132:135],a[4:7],a[148:151],v[132:135]\nbuffer_store_dwordx4 a[248:251],v253,s[52:55],s33 offen offset:0 sc1\nM v[132:135],a[8:11],a[152:155],v[132:135]\nv_cvt_pk_bf16_f32 v213,v166,v167\nv_cvt_pk_bf16_f32 v210,v176,v177\n" \
"M v[132:135],a[12:15],a[156:159],v[132:135]\nds_read_b128 a[80:83],v250 offset:4096\nds_read_b128 a[84:87],v250 offset:5120\nM v[144:147],a[16:19],a[128:131],v[224:227]\nv_cvt_pk_bf16_f32 v211,v178,v179\nv_cvt_pk_bf16_f32 v214,v180,v181\nM v[144:147],a[20:23],a[132:135],v[144:147]\nv_cvt_pk_bf16_f32 v215,v182,v183\n" \
"s_add_u32 s71,s33,16384\nM v[144:147],a[24:27],a[136:139],v[144:147]\nds_read_b128 a[88:91],v250 offset:6144\nds_read_b128 a[92:95],v250 offset:7168\nM v[144:147],a[28:31],a[140:143],v[144:147]\nbuffer_load_dwordx4 v240,s[44:47],s95 offen offset:1024 sc1 lds\nM v[148:151],a[16:19],a[144:147],v[228:231]\nv_exp_f32 v184,v184\n" \
"M v[148:151],a[20:23],a[148:151],v[148:151]\nds_read_b128 a[96:99],v250 offset:8192\nds_read_b128 a[100:103],v250 offset:9216\nM v[148:151],a[24:27],a[152:155],v[148:151]\nbuffer_store_dwordx4 a[248:251],v253,s[52:55],s33 offen offset:1024 sc1\nM v[148:151],a[28:31],a[156:159],v[148:151]\nv_exp_f32 v185,v185\nM v[160:163],a[32:35],a[128:131],v[224:227]\n" \
"ds_read_b128 a[104:107],v250 offset:10240\nds_read_b128 a[108:111],v250 offset:11264\nM v[160:163],a[36:39],a[132:135],v[160:163]\nv_exp_f32 v186,v186\nM v[160:163],a[40:43],a[136:139],v[160:163]\nv_exp_f32 v187,v187\nM v[160:163],a[44:47],a[140:143],v[160:163]\nds_read_b128 a[112:115],v250 offset:12288\n" \
"ds_read_b128 a[116:119],v250 offset:13312\nM v[164:167],a[32:35],a[144:147],v[228:231]\nbuffer_load_dwordx4 v240,s[44:47],s95 offen offset:2048 sc1 lds\nM v[164:167],a[36:39],a[148:151],v[164:167]\nv_exp_f32 v172,v172\nM v[164:167],a[40:43],a[152:155],v[164:167]\nds_read_b128 a[120:123],v250 offset:14336\nds_read_b128 a[124:127],v250 offset:15360\n" \
"M v[164:167],a[44:47],a[156:159],v[164:167]\nbuffer_store_dwordx4 a[248:251],v253,s[52:55],s33 offen offset:2048 sc1\ns_waitcnt lgkmcnt(6)\nM v[176:179],a[48:51],a[128:131],v[224:227]\nv_xor_b32 v250,0x18000,v250\nv_cvt_pk_bf16_f32 v216,v168,v169\nM v[176:179],a[52:55],a[132:135],v[176:179]\nv_exp_f32 v173,v173\n" \
"M v[176:179],a[56:59],a[136:139],v[176:179]\nv_exp_f32 v174,v174\nM v[176:179],a[60:63],a[140:143],v[176:179]\nv_exp_f32 v175,v175\nM v[180:183],a[48:51],a[144:147],v[228:231]\nv_exp_f32 v188,v188\nM v[180:183],a[52:55],a[148:151],v[180:183]\nbuffer_load_dwordx4 v240,s[44:47],s95 offen offset:3072 sc1 lds\n" \
"M v[180:183],a[56:59],a[152:155],v[180:183]\ns_add_u32 m0,m0,0x10000\nv_cvt_pk_bf16_f32 v217,v170,v171\nM v[180:183],a[60:63],a[156:159],v[180:183]\nv_exp_f32 v189,v189\nM v[0:3],a[64:67],v[192:195],v[0:3]\nbuffer_store_dwordx4 a[248:251],v253,s[52:55],s33 offen offset:3072 sc1\nM v[4:7],a[64:67],v[196:199],v[4:7]\n" \
"v_exp_f32 v190,v190\nM v[64:67],a[64:67],v[200:203],v[64:67]\ns_waitcnt vmcnt(16) lgkmcnt(0)\ns_barrier\nM v[68:71],a[64:67],v[204:207],v[68:71]\nds_read_b128 a[0:3],v250 offset:0\nds_read_b128 a[4:7],v250 offset:1024\nM v[8:11],a[68:71],v[192:195],v[8:11]\n" \
"v_exp_f32 v191,v191\nM v[12:15],a[68:71],v[196:199],v[12:15]\nbuffer_store_dwordx4 a[248:251],v253,s[52:55],s71 offen offset:0 sc1\nM v[72:75],a[68:71],v[200:203],v[72:75]\nv_cvt_pk_bf16_f32 v218,v184,v185\nv_cvt_pk_bf16_f32 v219,v186,v187\nM v[76:79],a[68:71],v[204:207],v[76:79]\nbuffer_load_dwordx4 v240,s[48:51],s95 offen offset:0 sc1 lds\n" \
"M v[16:19],a[72:75],v[192:195],v[16:19]\nv_cvt_pk_bf16_f32 v220,v172,v173\nv_cvt_pk_bf16_f32 v221,v174,v175\nM v[20:23],a[72:75],v[196:199],v[20:23]\nbuffer_store_dwordx4 a[248:251],v253,s[52:55],s71 offen offset:1024 sc1\nM v[80:83],a[72:75],v[200:203],v[80:83]\nv_cvt_pk_bf16_f32 v222,v188,v189\nv_cvt_pk_bf16_f32 v223,v190,v191\n" \
"M v[84:87],a[72:75],v[204:207],v[84:87]\nds_read_b128 a[8:11],v250 offset:2048\nds_read_b128 a[12:15],v250 offset:3072\nM v[24:27],a[76:79],v[192:195],v[24:27]\ns_add_u32 s70,s78,16384\ns_add_u32 s94,s94,0x4000\nM v[28:31],a[76:79],v[196:199],v[28:31]\nbuffer_store_dwordx4 a[248:251],v253,s[52:55],s71 offen offset:2048 sc1\n" \
"M v[88:91],a[76:79],v[200:203],v[88:91]\nv_exp_f32 v128,v128\nM v[92:95],a[76:79],v[204:207],v[92:95]\nv_exp_f32 v129,v129\nM v[32:35],a[80:83],v[192:195],v[32:35]\nv_exp_f32 v130,v130\nM v[36:39],a[80:83],v[196:199],v[36:39]\nbuffer_store_dwordx4 a[248:251],v253,s[52:55],s71 offen offset:3072 sc1\n" \
"M v[96:99],a[80:83],v[200:203],v[96:99]\nv_exp_f32 v131,v131\nM v[100:103],a[80:83],v[204:207],v[100:103]\nds_read_b128 a[16:19],v250 offset:4096\nds_read_b128 a[20:23],v250 offset:5120\nM v[40:43],a[84:87],v[192:195],v[40:43]\nv_exp_f32 v144,v144\nM v[44:47],a[84:87],v[196:199],v[44:47]\n" \
"v_exp_f32 v145,v145\nM v[104:107],a[84:87],v[200:203],v[104:107]\nv_exp_f32 v146,v146\nM v[108:111],a[84:87],v[204:207],v[108:111]\nbuffer_load_dwordx4 v240,s[48:51],s95 offen offset:1024 sc1 lds\nM v[48:51],a[88:91],v[192:195],v[48:51]\nv_exp_f32 v147,v147\nM v[52:55],a[88:91],v[196:199],v[52:55]\n" \
"buffer_store_dwordx4 a[248:251],v253,s[52:55],s78 offen offset:0 sc1\nM v[112:115],a[88:91],v[200:203],v[112:115]\nv_exp_f32 v132,v132\nM v[116:119],a[88:91],v[204:207],v[116:119]\nds_read_b128 a[24:27],v250 offset:6144\nds_read_b128 a[28:31],v250 offset:7168\nM v[56:59],a[92:95],v[192:195],v[56:59]\nv_exp_f32 v133,v133\n" \
"M v[60:63],a[92:95],v[196:199],v[60:63]\nbuffer_store_dwordx4 a[248:251],v253,s[52:55],s78 offen offset:1024 sc1\nM v[120:123],a[92:95],v[200:203],v[120:123]\nv_exp_f32 v134,v134\nM v[124:127],a[92:95],v[204:207],v[124:127]\nv_exp_f32 v135,v135\nM a[192:195],a[208:211],v[192:195],a[192:195]\nv_exp_f32 v148,v148\n" \
"M a[196:199],a[208:211],v[196:199],a[196:199]\nbuffer_store_dwordx4 a[248:251],v253,s[52:55],s78 offen offset:2048 sc1\nM a[200:203],a[208:211],v[200:203],a[200:203]\nv_exp_f32 v149,v149\nM a[204:207],a[208:211],v[204:207],a[204:207]\nds_read_b128 a[32:35],v250 offset:8192\nds_read_b128 a[36:39],v250 offset:9216\nM v[0:3],a[96:99],v[208:211],v[0:3]\n" \
"v_exp_f32 v150,v150\nM v[4:7],a[96:99],v[212:215],v[4:7]\nv_exp_f32 v151,v151\nM v[64:67],a[96:99],v[216:219],v[64:67]\ns_and_b32 s94,s94,0xffff\nv_cvt_pk_bf16_f32 v192,v128,v129\nM v[68:71],a[96:99],v[220:223],v[68:71]\nbuffer_load_dwordx4 v240,s[48:51],s95 offen offset:2048 sc1 lds\n" \
"M v[8:11],a[100:103],v[208:211],v[8:11]\nv_cvt_pk_bf16_f32 v193,v130,v131\nv_cvt_pk_bf16_f32 v194,v144,v145\nM v[12:15],a[100:103],v[212:215],v[12:15]\nbuffer_store_dwordx4 a[248:251],v253,s[52:55],s78 offen offset:3072 sc1\nM v[72:75],a[100:103],v[216:219],v[72:75]\nv_cvt_pk_bf16_f32 v195,v146,v147\nv_cvt_pk_bf16_f32 v196,v132,v133\n" \
"M v[76:79],a[100:103],v[220:223],v[76:79]\nds_read_b128 a[40:43],v250 offset:10240\nds_read_b128 a[44:47],v250 offset:11264\nM v[16:19],a[104:107],v[208:211],v[16:19]\nv_cvt_pk_bf16_f32 v197,v134,v135\nv_cvt_pk_bf16_f32 v198,v148,v149\nM v[20:23],a[104:107],v[212:215],v[20:23]\nbuffer_store_dwordx4 a[248:251],v253,s[52:55],s70 offen offset:0 sc1\n" \
"M v[80:83],a[104:107],v[216:219],v[80:83]\nv_exp_f32 v160,v160\nM v[84:87],a[104:107],v[220:223],v[84:87]\nv_exp_f32 v161,v161\nM v[24:27],a[108:111],v[208:211],v[24:27]\nv_exp_f32 v162,v162\nM v[28:31],a[108:111],v[212:215],v[28:31]\nbuffer_store_dwordx4 a[248:251],v253,s[52:55],s70 offen offset:1024 sc1\n" \
"M v[88:91],a[108:111],v[216:219],v[88:91]\nv_exp_f32 v163,v163\nM v[92:95],a[108:111],v[220:223],v[92:95]\nds_read_b128 a[48:51],v250 offset:12288\nds_read_b128 a[52:55],v250 offset:13312\nM v[32:35],a[112:115],v[208:211],v[32:35]\nv_exp_f32 v176,v176\nM v[36:39],a[112:115],v[212:215],v[36:39]\n" \
"v_exp_f32 v177,v177\nM v[96:99],a[112:115],v[216:219],v[96:99]\nv_exp_f32 v178,v178\nM v[100:103],a[112:115],v[220:223],v[100:103]\nbuffer_load_dwordx4 v240,s[48:51],s95 offen offset:3072 sc1 lds\nM v[40:43],a[116:119],v[208:211],v[40:43]\nv_cvt_pk_bf16_f32 v199,v150,v151\ns_add_u32 s95,s95,0x4000\n" \
"M v[44:47],a[116:119],v[212:215],v[44:47]\nbuffer_store_dwordx4 a[248:251],v253,s[52:55],s70 offen offset:2048 sc1\nM v[104:107],a[116:119],v[216:219],v[104:107]\nv_exp_f32 v179,v179\nM v[108:111],a[116:119],v[220:223],v[108:111]\nds_read_b128 a[56:59],v250 offset:14336\nds_read_b128 a[60:63],v250 offset:15360\nM v[48:51],a[120:123],v[208:211],v[48:51]\n" \
"v_add_u32 v250,s94,v241\nM v[52:55],a[120:123],v[212:215],v[52:55]\nbuffer_store_dwordx4 a[248:251],v253,s[52:55],s70 offen offset:3072 sc1\nM v[112:115],a[120:123],v[216:219],v[112:115]\nM v[116:119],a[120:123],v[220:223],v[116:119]\nM v[56:59],a[124:127],v[208:211],v[56:59]\nM v[60:63],a[124:127],v[212:215],v[60:63]\nM v[120:123],a[124:127],v[216:219],v[120:123]\n" \
"M v[124:127],a[124:127],v[220:223],v[124:127]\nM a[192:195],a[208:211],v[208:211],a[192:195]\nM a[196:199],a[208:211],v[212:215],a[196:199]\nM a[200:203],a[208:211],v[216:219],a[200:203]\nM a[204:207],a[208:211],v[220:223],a[204:207]\n.Lse:\ns_waitcnt lgkmcnt(4)\nM v[128:131],a[0:3],a[128:131],v[224:227]\n" \
"s_add_u32 m0,s94,s64\ns_add_u32 s37,s12,s67\ns_add_u32 s37,s37,1048576\nv_cvt_pk_bf16_f32 v208,v160,v161\nM v[128:131],a[4:7],a[132:135],v[128:131]\nds_read_b128 a[64:67],v250 offset:0\nds_read_b128 a[68:71],v250 offset:1024\nv_accvgpr_read_b32 v232,a200\n" \
"v_accvgpr_read_b32 v236,a204\nM v[128:131],a[8:11],a[136:139],v[128:131]\nbuffer_load_dwordx4 v240,s[44:47],s95 offen offset:0 sc1 lds\nv_add_f32 v233,v232,v236\nv_cvt_pk_bf16_f32 v209,v162,v163\nM v[128:131],a[12:15],a[140:143],v[128:131]\nv_rcp_f32 v232,v232\nv_rcp_f32 v236,v236\n" \
"M v[132:135],a[0:3],a[144:147],v[228:231]\nds_read_b128 a[72:75],v250 offset:2048\nds_read_b128 a[76:79],v250 offset:3072\nv_add_f32 v233,v233,v232\nv_add_f32 v233,v233,v236\nM v[132:135],a[4:7],a[148:151],v[132:135]\nv_cmp_ngt_f32 vcc,s28,v233\nv_mul_f32 v64,v64,v232\n" \
"v_mul_f32 v65,v65,v232\nv_mul_f32 v66,v66,v232\nv_mul_f32 v67,v67,v232\nM v[132:135],a[8:11],a[152:155],v[132:135]\ns_and_b64 vcc,vcc,s[90:91]\ns_cselect_b32 s39,0x8000000,s16\ns_cselect_b32 s16,s16,0x8000000\ns_cselect_b32 s71,2,1\n" \
"s_addc_u32 s99,s99,0\nM v[132:135],a[12:15],a[156:159],v[132:135]\nds_read_b128 a[80:83],v250 offset:4096\nds_read_b128 a[84:87],v250 offset:5120\nv_cvt_pk_bf16_f32 v64,v64,v65\nv_mul_f32 v72,v72,v232\nM v[144:147],a[16:19],a[128:131],v[224:227]\nv_mul_f32 v73,v73,v232\n" \
"v_mul_f32 v74,v74,v232\nv_mul_f32 v75,v75,v232\nv_cvt_pk_bf16_f32 v65,v66,v67\nM v[144:147],a[20:23],a[132:135],v[144:147]\ns_or_b32 s37,s37,s39\nv_cvt_pk_bf16_f32 v66,v72,v73\nv_cvt_pk_bf16_f32 v67,v74,v75\nbuffer_store_dwordx4 v[64:67],v240,s[52:55],s37 offen offset:0 sc1\n" \
"M v[144:147],a[24:27],a[136:139],v[144:147]\nds_read_b128 a[88:91],v250 offset:6144\nds_read_b128 a[92:95],v250 offset:7168\nv_mul_f32 v68,v68,v236\nv_mul_f32 v69,v69,v236\nv_mul_f32 v70,v70,v236\nM v[144:147],a[28:31],a[140:143],v[144:147]\nbuffer_load_dwordx4 v240,s[44:47],s95 offen offset:1024 sc1 lds\n" \
"v_mul_f32 v71,v71,v236\nv_cvt_pk_bf16_f32 v68,v68,v69\nM v[148:151],a[16:19],a[144:147],v[228:231]\nv_mul_f32 v76,v76,v236\nv_mul_f32 v77,v77,v236\nv_mul_f32 v78,v78,v236\nv_mul_f32 v79,v79,v236\nM v[148:151],a[20:23],a[148:151],v[148:151]\n" \
"ds_read_b128 a[96:99],v250 offset:8192\nds_read_b128 a[100:103],v250 offset:9216\nv_cvt_pk_bf16_f32 v69,v70,v71\nv_cvt_pk_bf16_f32 v70,v76,v77\nM v[148:151],a[24:27],a[152:155],v[148:151]\nv_cvt_pk_bf16_f32 v71,v78,v79\nv_exp_f32 v164,v164\nv_mul_f32 v80,v80,v232\n" \
"M v[148:151],a[28:31],a[156:159],v[148:151]\nv_exp_f32 v165,v165\nv_exp_f32 v166,v166\nM v[160:163],a[32:35],a[128:131],v[224:227]\nds_read_b128 a[104:107],v250 offset:10240\nds_read_b128 a[108:111],v250 offset:11264\nv_exp_f32 v167,v167\nv_cvt_pk_bf16_f32 v212,v164,v165\n" \
"M v[160:163],a[36:39],a[132:135],v[160:163]\nbuffer_store_dwordx4 v[68:71],v240,s[52:55],s37 offen offset:1024 sc1\nv_cvt_pk_bf16_f32 v213,v166,v167\nv_mul_f32 v81,v81,v232\nM v[160:163],a[40:43],a[136:139],v[160:163]\nv_cvt_pk_bf16_f32 v210,v176,v177\nv_cvt_pk_bf16_f32 v211,v178,v179\nv_mul_f32 v82,v82,v232\n" \
"v_mul_f32 v83,v83,v232\nM v[160:163],a[44:47],a[140:143],v[160:163]\nds_read_b128 a[112:115],v250 offset:12288\nds_read_b128 a[116:119],v250 offset:13312\nv_cvt_pk_bf16_f32 v80,v80,v81\nv_mul_f32 v88,v88,v232\nM v[164:167],a[32:35],a[144:147],v[228:231]\nbuffer_load_dwordx4 v240,s[44:47],s95 offen offset:2048 sc1 lds\n" \
"v_mul_f32 v89,v89,v232\nv_mul_f32 v90,v90,v232\nM v[164:167],a[36:39],a[148:151],v[164:167]\nv_mul_f32 v91,v91,v232\nv_cvt_pk_bf16_f32 v81,v82,v83\nv_cvt_pk_bf16_f32 v82,v88,v89\nv_cvt_pk_bf16_f32 v83,v90,v91\nv_mul_f32 v84,v84,v236\n" \
"M v[164:167],a[40:43],a[152:155],v[164:167]\nds_read_b128 a[120:123],v250 offset:14336\nds_read_b128 a[124:127],v250 offset:15360\nv_exp_f32 v180,v180\nM v[164:167],a[44:47],a[156:159],v[164:167]\nv_exp_f32 v181,v181\nv_mul_f32 v85,v85,v236\ns_waitcnt lgkmcnt(4)\n" \
"M v[176:179],a[48:51],a[128:131],v[224:227]\nv_exp_f32 v182,v182\nv_exp_f32 v183,v183\nM v[176:179],a[52:55],a[132:135],v[176:179]\nbuffer_store_dwordx4 v[80:83],v240,s[52:55],s37 offen offset:2048 sc1\nv_cvt_pk_bf16_f32 v214,v180,v181\nv_cvt_pk_bf16_f32 v215,v182,v183\nM v[176:179],a[56:59],a[136:139],v[176:179]\n" \
"v_mul_f32 v86,v86,v236\nv_mul_f32 v87,v87,v236\nv_cvt_pk_bf16_f32 v84,v84,v85\nv_mul_f32 v92,v92,v236\nM v[176:179],a[60:63],a[140:143],v[176:179]\nv_mul_f32 v93,v93,v236\nv_mul_f32 v94,v94,v236\nv_mul_f32 v95,v95,v236\n" \
"v_cvt_pk_bf16_f32 v85,v86,v87\nv_cvt_pk_bf16_f32 v86,v92,v93\nM v[180:183],a[48:51],a[144:147],v[228:231]\nv_cvt_pk_bf16_f32 v87,v94,v95\nv_xor_b32 v250,0x18000,v250\nv_mul_f32 v96,v96,v232\nv_mul_f32 v97,v97,v232\nM v[180:183],a[52:55],a[148:151],v[180:183]\n" \
"buffer_load_dwordx4 v240,s[44:47],s95 offen offset:3072 sc1 lds\ns_add_u32 m0,m0,0x10000\nv_mul_f32 v98,v98,v232\nM v[180:183],a[56:59],a[152:155],v[180:183]\nv_mul_f32 v99,v99,v232\nv_cvt_pk_bf16_f32 v96,v96,v97\nv_mul_f32 v104,v104,v232\nv_mul_f32 v105,v105,v232\n" \
"M v[180:183],a[60:63],a[156:159],v[180:183]\nv_mul_f32 v106,v106,v232\nv_mul_f32 v107,v107,v232\nv_cvt_pk_bf16_f32 v97,v98,v99\ns_add_u32 s38,s37,4096\nM v[0:3],a[64:67],v[192:195],v[0:3]\nv_cvt_pk_bf16_f32 v98,v104,v105\nv_cvt_pk_bf16_f32 v99,v106,v107\n" \
"v_mul_f32 v100,v100,v236\nv_mul_f32 v101,v101,v236\nv_mul_f32 v102,v102,v236\nM v[4:7],a[64:67],v[196:199],v[4:7]\nbuffer_store_dwordx4 v[84:87],v240,s[52:55],s37 offen offset:3072 sc1\nv_mul_f32 v103,v103,v236\nv_cvt_pk_bf16_f32 v100,v100,v101\nM v[8:11],a[68:71],v[192:195],v[8:11]\n" \
"s_waitcnt vmcnt(16) lgkmcnt(0)\ns_barrier\nv_mul_f32 v108,v108,v236\nv_mul_f32 v109,v109,v236\nM v[12:15],a[68:71],v[196:199],v[12:15]\nds_read_b128 a[0:3],v250 offset:0\nds_read_b128 a[4:7],v250 offset:1024\nv_mul_f32 v110,v110,v236\n" \
"v_mul_f32 v111,v111,v236\nM v[16:19],a[72:75],v[192:195],v[16:19]\nv_cvt_pk_bf16_f32 v101,v102,v103\nv_cvt_pk_bf16_f32 v102,v108,v109\nv_cvt_pk_bf16_f32 v103,v110,v111\nv_mul_f32 v112,v112,v232\nM v[20:23],a[72:75],v[196:199],v[20:23]\nbuffer_load_dwordx4 v240,s[48:51],s95 offen offset:0 sc1 lds\n" \
"v_mul_f32 v113,v113,v232\nv_mul_f32 v114,v114,v232\nv_mul_f32 v115,v115,v232\nM v[24:27],a[76:79],v[192:195],v[24:27]\nv_cvt_pk_bf16_f32 v112,v112,v113\nv_mul_f32 v120,v120,v232\nv_mul_f32 v121,v121,v232\nv_mul_f32 v122,v122,v232\n" \
"M v[28:31],a[76:79],v[196:199],v[28:31]\nds_read_b128 a[8:11],v250 offset:2048\nds_read_b128 a[12:15],v250 offset:3072\nv_mul_f32 v123,v123,v232\nv_cvt_pk_bf16_f32 v113,v114,v115\nM v[32:35],a[80:83],v[192:195],v[32:35]\nv_cvt_pk_bf16_f32 v114,v120,v121\nv_cvt_pk_bf16_f32 v115,v122,v123\n" \
"v_mul_f32 v116,v116,v236\nv_mul_f32 v117,v117,v236\nM v[36:39],a[80:83],v[196:199],v[36:39]\nbuffer_store_dwordx4 v[96:99],v240,s[52:55],s38 offen offset:0 sc1\nv_mul_f32 v118,v118,v236\nv_mul_f32 v119,v119,v236\nM v[40:43],a[84:87],v[192:195],v[40:43]\nv_cvt_pk_bf16_f32 v116,v116,v117\n" \
"v_mul_f32 v124,v124,v236\nv_mul_f32 v125,v125,v236\nv_mul_f32 v126,v126,v236\nM v[44:47],a[84:87],v[196:199],v[44:47]\nds_read_b128 a[16:19],v250 offset:4096\nds_read_b128 a[20:23],v250 offset:5120\nv_mul_f32 v127,v127,v236\nv_cvt_pk_bf16_f32 v117,v118,v119\n" \
"v_cvt_pk_bf16_f32 v118,v124,v125\nM v[48:51],a[88:91],v[192:195],v[48:51]\nv_cvt_pk_bf16_f32 v119,v126,v127\nv_exp_f32 v128,v128\ns_add_u32 s94,s94,0x4000\nM v[52:55],a[88:91],v[196:199],v[52:55]\nbuffer_load_dwordx4 v240,s[48:51],s95 offen offset:1024 sc1 lds\nv_exp_f32 v129,v129\n" \
"M v[56:59],a[92:95],v[192:195],v[56:59]\nv_exp_f32 v130,v130\nv_exp_f32 v131,v131\nM v[60:63],a[92:95],v[196:199],v[60:63]\nds_read_b128 a[24:27],v250 offset:6144\nds_read_b128 a[28:31],v250 offset:7168\nv_exp_f32 v144,v144\nM a[192:195],a[208:211],v[192:195],a[192:195]\n" \
"v_exp_f32 v145,v145\nv_exp_f32 v146,v146\ns_lshl2_add_u32 s77,s61,s79\nM a[196:199],a[208:211],v[196:199],a[196:199]\nbuffer_store_dwordx4 v[100:103],v240,s[52:55],s38 offen offset:1024 sc1\nv_exp_f32 v147,v147\nM v[0:3],a[96:99],v[208:211],v[0:3]\nv_exp_f32 v132,v132\n" \
"v_exp_f32 v133,v133\nM v[4:7],a[96:99],v[212:215],v[4:7]\nds_read_b128 a[32:35],v250 offset:8192\nds_read_b128 a[36:39],v250 offset:9216\nv_exp_f32 v134,v134\nM v[8:11],a[100:103],v[208:211],v[8:11]\nv_exp_f32 v135,v135\nv_exp_f32 v148,v148\n" \
"M v[12:15],a[100:103],v[212:215],v[12:15]\nbuffer_load_dwordx4 v240,s[48:51],s95 offen offset:2048 sc1 lds\nv_exp_f32 v149,v149\nM v[16:19],a[104:107],v[208:211],v[16:19]\nv_exp_f32 v150,v150\nv_exp_f32 v151,v151\ns_and_b32 s94,s94,0xffff\nM v[20:23],a[104:107],v[212:215],v[20:23]\n" \
"ds_read_b128 a[40:43],v250 offset:10240\nds_read_b128 a[44:47],v250 offset:11264\ns_add_u32 s77,s77,4096\nv_mov_b32 v233,s71\nM v[24:27],a[108:111],v[208:211],v[24:27]\nv_cvt_pk_bf16_f32 v192,v128,v129\nv_cvt_pk_bf16_f32 v193,v130,v131\nv_cvt_pk_bf16_f32 v194,v144,v145\n" \
"v_cvt_pk_bf16_f32 v195,v146,v147\nM v[28:31],a[108:111],v[212:215],v[28:31]\nbuffer_store_dwordx4 v[112:115],v240,s[52:55],s38 offen offset:2048 sc1\nv_cvt_pk_bf16_f32 v196,v132,v133\nv_cvt_pk_bf16_f32 v197,v134,v135\nM v[32:35],a[112:115],v[208:211],v[32:35]\nv_cvt_pk_bf16_f32 v198,v148,v149\nv_cvt_pk_bf16_f32 v199,v150,v151\n" \
"v_exp_f32 v160,v160\nM v[36:39],a[112:115],v[212:215],v[36:39]\nds_read_b128 a[48:51],v250 offset:12288\nds_read_b128 a[52:55],v250 offset:13312\nv_exp_f32 v161,v161\nM v[40:43],a[116:119],v[208:211],v[40:43]\nv_exp_f32 v162,v162\nv_exp_f32 v163,v163\n" \
"M v[44:47],a[116:119],v[212:215],v[44:47]\nbuffer_load_dwordx4 v240,s[48:51],s95 offen offset:3072 sc1 lds\nv_exp_f32 v176,v176\nM v[48:51],a[120:123],v[208:211],v[48:51]\nv_exp_f32 v177,v177\nv_exp_f32 v178,v178\nM v[52:55],a[120:123],v[212:215],v[52:55]\nds_read_b128 a[56:59],v250 offset:14336\n" \
"ds_read_b128 a[60:63],v250 offset:15360\nv_exp_f32 v179,v179\nM v[56:59],a[124:127],v[208:211],v[56:59]\nv_add_u32 v250,s94,v241\ns_add_u32 s95,s95,0x4000\nM v[60:63],a[124:127],v[212:215],v[60:63]\nbuffer_store_dwordx4 v[116:119],v240,s[52:55],s38 offen offset:3072 sc1\nM a[192:195],a[208:211],v[208:211],a[192:195]\n" \
"M a[196:199],a[208:211],v[212:215],a[196:199]\ns_cmp_ge_u32 s62,6\ns_cbranch_scc1 .Lsw\n.Lsingle:\ns_waitcnt lgkmcnt(4)\nM v[128:131],a[0:3],a[128:131],v[224:227]\ns_add_u32 m0,s94,s64\nv_cvt_pk_bf16_f32 v208,v160,v161\n" \
"M v[128:131],a[4:7],a[132:135],v[128:131]\nds_read_b128 a[64:67],v250 offset:0\nds_read_b128 a[68:71],v250 offset:1024\nM v[128:131],a[8:11],a[136:139],v[128:131]\nbuffer_load_dwordx4 v240,s[44:47],s95 offen offset:0 sc1 lds\nM v[128:131],a[12:15],a[140:143],v[128:131]\nv_cvt_pk_bf16_f32 v209,v162,v163\nv_cvt_pk_bf16_f32 v210,v176,v177\n" \
"M v[132:135],a[0:3],a[144:147],v[228:231]\nds_read_b128 a[72:75],v250 offset:2048\nds_read_b128 a[76:79],v250 offset:3072\nM v[132:135],a[4:7],a[148:151],v[132:135]\nv_exp_f32 v164,v164\nM v[132:135],a[8:11],a[152:155],v[132:135]\nv_exp_f32 v165,v165\nM v[132:135],a[12:15],a[156:159],v[132:135]\n" \
"ds_read_b128 a[80:83],v250 offset:4096\nds_read_b128 a[84:87],v250 offset:5120\nM v[144:147],a[16:19],a[128:131],v[224:227]\nv_exp_f32 v166,v166\nM v[144:147],a[20:23],a[132:135],v[144:147]\nv_exp_f32 v167,v167\nM v[144:147],a[24:27],a[136:139],v[144:147]\nds_read_b128 a[88:91],v250 offset:6144\n" \
"ds_read_b128 a[92:95],v250 offset:7168\nM v[144:147],a[28:31],a[140:143],v[144:147]\nbuffer_load_dwordx4 v240,s[44:47],s95 offen offset:1024 sc1 lds\nM v[148:151],a[16:19],a[144:147],v[228:231]\nv_cvt_pk_bf16_f32 v212,v164,v165\nv_cvt_pk_bf16_f32 v213,v166,v167\nM v[148:151],a[20:23],a[148:151],v[148:151]\nds_read_b128 a[96:99],v250 offset:8192\n" \
"ds_read_b128 a[100:103],v250 offset:9216\nM v[148:151],a[24:27],a[152:155],v[148:151]\nv_cvt_pk_bf16_f32 v211,v178,v179\ns_add_u32 s94,s94,0x4000\nM v[148:151],a[28:31],a[156:159],v[148:151]\nv_exp_f32 v180,v180\nM v[160:163],a[32:35],a[128:131],v[224:227]\nds_read_b128 a[104:107],v250 offset:10240\n" \
"ds_read_b128 a[108:111],v250 offset:11264\nM v[160:163],a[36:39],a[132:135],v[160:163]\nv_exp_f32 v181,v181\nM v[160:163],a[40:43],a[136:139],v[160:163]\nv_exp_f32 v182,v182\nM v[160:163],a[44:47],a[140:143],v[160:163]\nds_read_b128 a[112:115],v250 offset:12288\nds_read_b128 a[116:119],v250 offset:13312\n" \
"M v[164:167],a[32:35],a[144:147],v[228:231]\nbuffer_load_dwordx4 v240,s[44:47],s95 offen offset:2048 sc1 lds\nM v[164:167],a[36:39],a[148:151],v[164:167]\nv_exp_f32 v183,v183\nM v[164:167],a[40:43],a[152:155],v[164:167]\nds_read_b128 a[120:123],v250 offset:14336\nds_read_b128 a[124:127],v250 offset:15360\nM v[164:167],a[44:47],a[156:159],v[164:167]\n" \
"v_cvt_pk_bf16_f32 v214,v180,v181\ns_waitcnt lgkmcnt(7)\nM v[176:179],a[48:51],a[128:131],v[224:227]\nv_cvt_pk_bf16_f32 v215,v182,v183\nv_xor_b32 v250,0x18000,v250\nM v[176:179],a[52:55],a[132:135],v[176:179]\nv_exp_f32 v128,v128\nM v[176:179],a[56:59],a[136:139],v[176:179]\n" \
"v_exp_f32 v129,v129\nM v[176:179],a[60:63],a[140:143],v[176:179]\nv_exp_f32 v130,v130\nM v[180:183],a[48:51],a[144:147],v[228:231]\nv_exp_f32 v131,v131\nM v[180:183],a[52:55],a[148:151],v[180:183]\nbuffer_load_dwordx4 v240,s[44:47],s95 offen offset:3072 sc1 lds\nM v[180:183],a[56:59],a[152:155],v[180:183]\n" \
"s_add_u32 m0,m0,0x10000\ns_and_b32 s94,s94,0xffff\nM v[180:183],a[60:63],a[156:159],v[180:183]\nv_exp_f32 v144,v144\nM v[0:3],a[64:67],v[192:195],v[0:3]\nv_exp_f32 v145,v145\nM v[4:7],a[64:67],v[196:199],v[4:7]\nv_exp_f32 v146,v146\n" \
"M v[8:11],a[68:71],v[192:195],v[8:11]\ns_waitcnt vmcnt(16) lgkmcnt(0)\ns_barrier\nM v[12:15],a[68:71],v[196:199],v[12:15]\nds_read_b128 a[0:3],v250 offset:0\nds_read_b128 a[4:7],v250 offset:1024\nM v[16:19],a[72:75],v[192:195],v[16:19]\nv_exp_f32 v147,v147\n" \
"M v[20:23],a[72:75],v[196:199],v[20:23]\nbuffer_load_dwordx4 v240,s[48:51],s95 offen offset:0 sc1 lds\nM v[24:27],a[76:79],v[192:195],v[24:27]\nv_exp_f32 v132,v132\nM v[28:31],a[76:79],v[196:199],v[28:31]\nds_read_b128 a[8:11],v250 offset:2048\nds_read_b128 a[12:15],v250 offset:3072\nM v[32:35],a[80:83],v[192:195],v[32:35]\n" \
"v_exp_f32 v133,v133\nM v[36:39],a[80:83],v[196:199],v[36:39]\nv_exp_f32 v134,v134\nM v[40:43],a[84:87],v[192:195],v[40:43]\nv_exp_f32 v135,v135\nM v[44:47],a[84:87],v[196:199],v[44:47]\nds_read_b128 a[16:19],v250 offset:4096\nds_read_b128 a[20:23],v250 offset:5120\n" \
"M v[48:51],a[88:91],v[192:195],v[48:51]\nv_exp_f32 v148,v148\nM v[52:55],a[88:91],v[196:199],v[52:55]\nbuffer_load_dwordx4 v240,s[48:51],s95 offen offset:1024 sc1 lds\nM v[56:59],a[92:95],v[192:195],v[56:59]\nv_exp_f32 v149,v149\nM v[60:63],a[92:95],v[196:199],v[60:63]\nds_read_b128 a[24:27],v250 offset:6144\n" \
"ds_read_b128 a[28:31],v250 offset:7168\nM a[192:195],a[208:211],v[192:195],a[192:195]\nv_exp_f32 v150,v150\nM a[196:199],a[208:211],v[196:199],a[196:199]\nv_exp_f32 v151,v151\nM v[0:3],a[96:99],v[208:211],v[0:3]\nv_cvt_pk_bf16_f32 v192,v128,v129\nv_cvt_pk_bf16_f32 v193,v130,v131\n" \
"M v[4:7],a[96:99],v[212:215],v[4:7]\nds_read_b128 a[32:35],v250 offset:8192\nds_read_b128 a[36:39],v250 offset:9216\nM v[8:11],a[100:103],v[208:211],v[8:11]\nv_cvt_pk_bf16_f32 v194,v144,v145\nv_cvt_pk_bf16_f32 v195,v146,v147\nM v[12:15],a[100:103],v[212:215],v[12:15]\nbuffer_load_dwordx4 v240,s[48:51],s95 offen offset:2048 sc1 lds\n" \
"M v[16:19],a[104:107],v[208:211],v[16:19]\nv_cvt_pk_bf16_f32 v196,v132,v133\nv_cvt_pk_bf16_f32 v197,v134,v135\nM v[20:23],a[104:107],v[212:215],v[20:23]\nds_read_b128 a[40:43],v250 offset:10240\nds_read_b128 a[44:47],v250 offset:11264\nM v[24:27],a[108:111],v[208:211],v[24:27]\nv_cvt_pk_bf16_f32 v198,v148,v149\n" \
"v_cvt_pk_bf16_f32 v199,v150,v151\nM v[28:31],a[108:111],v[212:215],v[28:31]\nv_exp_f32 v160,v160\nM v[32:35],a[112:115],v[208:211],v[32:35]\nv_exp_f32 v161,v161\nM v[36:39],a[112:115],v[212:215],v[36:39]\nds_read_b128 a[48:51],v250 offset:12288\nds_read_b128 a[52:55],v250 offset:13312\n" \
"M v[40:43],a[116:119],v[208:211],v[40:43]\nv_exp_f32 v162,v162\nM v[44:47],a[116:119],v[212:215],v[44:47]\nbuffer_load_dwordx4 v240,s[48:51],s95 offen offset:3072 sc1 lds\nM v[48:51],a[120:123],v[208:211],v[48:51]\nv_exp_f32 v163,v163\nM v[52:55],a[120:123],v[212:215],v[52:55]\nds_read_b128 a[56:59],v250 offset:14336\n" \
"ds_read_b128 a[60:63],v250 offset:15360\nM v[56:59],a[124:127],v[208:211],v[56:59]\nv_exp_f32 v176,v176\nM v[60:63],a[124:127],v[212:215],v[60:63]\nv_exp_f32 v177,v177\nM a[192:195],a[208:211],v[208:211],a[192:195]\nv_exp_f32 v178,v178\nv_exp_f32 v179,v179\n" \
"v_add_u32 v250,s94,v241\ns_add_u32 s95,s95,0x4000\nM a[196:199],a[208:211],v[212:215],a[196:199]\ns_cmp_lg_u32 s95,s15\ns_cbranch_scc1 .Lsingle\n.Lsw:\ns_waitcnt lgkmcnt(4)\nM v[128:131],a[0:3],a[128:131],v[224:227]\n" \
"s_lshl4_add_u32 s69,s12,s68\ns_add_u32 m0,s64,131072\nM v[128:131],a[4:7],a[132:135],v[128:131]\nds_read_b128 a[64:67],v250 offset:0\nds_read_b128 a[68:71],v250 offset:1024\nv_cvt_pk_bf16_f32 v208,v160,v161\nM v[128:131],a[8:11],a[136:139],v[128:131]\nbuffer_load_dwordx4 v243,s[40:43],s69 offen offset:0 lds\n" \
"M v[128:131],a[12:15],a[140:143],v[128:131]\nv_cvt_pk_bf16_f32 v209,v162,v163\nv_exp_f32 v164,v164\nM v[132:135],a[0:3],a[144:147],v[228:231]\nds_read_b128 a[72:75],v250 offset:2048\nds_read_b128 a[76:79],v250 offset:3072\nM v[132:135],a[4:7],a[148:151],v[132:135]\nbuffer_load_dwordx4 v244,s[40:43],s69 offen offset:1024 lds\n" \
"v_cvt_pk_bf16_f32 v210,v176,v177\nM v[132:135],a[8:11],a[152:155],v[132:135]\nv_exp_f32 v165,v165\nM v[132:135],a[12:15],a[156:159],v[132:135]\nds_read_b128 a[80:83],v250 offset:4096\nds_read_b128 a[84:87],v250 offset:5120\nv_cvt_pk_bf16_f32 v212,v164,v165\nM v[144:147],a[16:19],a[128:131],v[224:227]\n" \
"buffer_load_dwordx4 v245,s[40:43],s69 offen offset:2048 lds\nM v[144:147],a[20:23],a[132:135],v[144:147]\nv_exp_f32 v166,v166\nv_cvt_pk_bf16_f32 v211,v178,v179\nM v[144:147],a[24:27],a[136:139],v[144:147]\nds_read_b128 a[88:91],v250 offset:6144\nds_read_b128 a[92:95],v250 offset:7168\nM v[144:147],a[28:31],a[140:143],v[144:147]\n" \
"buffer_load_dwordx4 v246,s[40:43],s69 offen offset:3072 lds\ns_lshl4_add_u32 s69,s12,s68\nM v[148:151],a[16:19],a[144:147],v[228:231]\ns_add_u32 s69,s69,0x10000\ns_add_u32 m0,s64,147456\nM v[148:151],a[20:23],a[148:151],v[148:151]\nds_read_b128 a[96:99],v250 offset:8192\nds_read_b128 a[100:103],v250 offset:9216\n" \
"s_add_u32 s94,s94,0x4000\nM v[148:151],a[24:27],a[152:155],v[148:151]\nv_exp_f32 v167,v167\nM v[148:151],a[28:31],a[156:159],v[148:151]\nv_cvt_pk_bf16_f32 v213,v166,v167\nv_exp_f32 v180,v180\nM v[160:163],a[32:35],a[128:131],v[224:227]\nds_read_b128 a[104:107],v250 offset:10240\n" \
"ds_read_b128 a[108:111],v250 offset:11264\ns_and_b32 s94,s94,0xffff\nM v[160:163],a[36:39],a[132:135],v[160:163]\nbuffer_load_dwordx4 v243,s[40:43],s69 offen offset:0 lds\nM v[160:163],a[40:43],a[136:139],v[160:163]\nv_exp_f32 v181,v181\ns_add_u32 s95,s95,0x4000\nM v[160:163],a[44:47],a[140:143],v[160:163]\n" \
"ds_read_b128 a[112:115],v250 offset:12288\nds_read_b128 a[116:119],v250 offset:13312\nM v[164:167],a[32:35],a[144:147],v[228:231]\nv_exp_f32 v182,v182\nv_cvt_pk_bf16_f32 v214,v180,v181\nM v[164:167],a[36:39],a[148:151],v[164:167]\nbuffer_load_dwordx4 v244,s[40:43],s69 offen offset:1024 lds\nM v[164:167],a[40:43],a[152:155],v[164:167]\n" \
"ds_read_b128 a[120:123],v250 offset:14336\nds_read_b128 a[124:127],v250 offset:15360\nv_xor_b32 v250,0x18000,v250\nM v[164:167],a[44:47],a[156:159],v[164:167]\ns_waitcnt lgkmcnt(6)\nM v[176:179],a[48:51],a[128:131],v[224:227]\ns_waitcnt vmcnt(6)\nbuffer_store_dword v233,off,s[52:55],s77 sc1\n" \
"M v[176:179],a[52:55],a[132:135],v[176:179]\nbuffer_load_dwordx4 v245,s[40:43],s69 offen offset:2048 lds\nM v[176:179],a[56:59],a[136:139],v[176:179]\nv_exp_f32 v183,v183\nM v[176:179],a[60:63],a[140:143],v[176:179]\nv_cvt_pk_bf16_f32 v215,v182,v183\nM v[180:183],a[48:51],a[144:147],v[228:231]\nv_exp_f32 v128,v128\n" \
"M v[180:183],a[52:55],a[148:151],v[180:183]\nbuffer_load_dwordx4 v246,s[40:43],s69 offen offset:3072 lds\nM v[180:183],a[56:59],a[152:155],v[180:183]\nv_exp_f32 v129,v129\nM v[180:183],a[60:63],a[156:159],v[180:183]\ns_lshl4_add_u32 s69,s10,s68\nv_exp_f32 v130,v130\nM v[0:3],a[64:67],v[192:195],v[0:3]\n" \
"v_exp_f32 v131,v131\nM v[4:7],a[64:67],v[196:199],v[4:7]\ns_add_u32 m0,s64,0\nv_exp_f32 v144,v144\nM v[8:11],a[68:71],v[192:195],v[8:11]\ns_waitcnt vmcnt(16) lgkmcnt(0)\ns_barrier\nM v[12:15],a[68:71],v[196:199],v[12:15]\n" \
"ds_read_b128 a[0:3],v250 offset:0\nds_read_b128 a[4:7],v250 offset:1024\nM v[16:19],a[72:75],v[192:195],v[16:19]\nv_exp_f32 v145,v145\nM v[20:23],a[72:75],v[196:199],v[20:23]\nbuffer_load_dwordx4 v243,s[40:43],s69 offen offset:0 lds\nM v[24:27],a[76:79],v[192:195],v[24:27]\nv_exp_f32 v146,v146\n" \
"M v[28:31],a[76:79],v[196:199],v[28:31]\nds_read_b128 a[8:11],v250 offset:2048\nds_read_b128 a[12:15],v250 offset:3072\nM v[32:35],a[80:83],v[192:195],v[32:35]\nv_exp_f32 v147,v147\nM v[36:39],a[80:83],v[196:199],v[36:39]\nbuffer_load_dwordx4 v244,s[40:43],s69 offen offset:1024 lds\nM v[40:43],a[84:87],v[192:195],v[40:43]\n" \
"v_exp_f32 v132,v132\nM v[44:47],a[84:87],v[196:199],v[44:47]\nds_read_b128 a[16:19],v250 offset:4096\nds_read_b128 a[20:23],v250 offset:5120\nM v[48:51],a[88:91],v[192:195],v[48:51]\nv_exp_f32 v133,v133\nM v[52:55],a[88:91],v[196:199],v[52:55]\nbuffer_load_dwordx4 v245,s[40:43],s69 offen offset:2048 lds\n" \
"M v[56:59],a[92:95],v[192:195],v[56:59]\nv_exp_f32 v134,v134\nM v[60:63],a[92:95],v[196:199],v[60:63]\nds_read_b128 a[24:27],v250 offset:6144\nds_read_b128 a[28:31],v250 offset:7168\nM a[192:195],a[208:211],v[192:195],a[192:195]\nv_exp_f32 v135,v135\nv_cvt_pk_bf16_f32 v192,v128,v129\n" \
"M a[196:199],a[208:211],v[196:199],a[196:199]\nbuffer_load_dwordx4 v246,s[40:43],s69 offen offset:3072 lds\nM v[0:3],a[96:99],v[208:211],v[0:3]\ns_lshl4_add_u32 s69,s10,s68\ns_add_u32 s69,s69,0x10000\ns_add_u32 m0,s64,32768\nM v[4:7],a[96:99],v[212:215],v[4:7]\nds_read_b128 a[32:35],v250 offset:8192\n" \
"ds_read_b128 a[36:39],v250 offset:9216\nM v[8:11],a[100:103],v[208:211],v[8:11]\nv_exp_f32 v148,v148\nv_cvt_pk_bf16_f32 v193,v130,v131\nM v[12:15],a[100:103],v[212:215],v[12:15]\nbuffer_load_dwordx4 v243,s[40:43],s69 offen offset:0 lds\nM v[16:19],a[104:107],v[208:211],v[16:19]\nv_exp_f32 v149,v149\n" \
"v_cvt_pk_bf16_f32 v194,v144,v145\nM v[20:23],a[104:107],v[212:215],v[20:23]\nds_read_b128 a[40:43],v250 offset:10240\nds_read_b128 a[44:47],v250 offset:11264\nM v[24:27],a[108:111],v[208:211],v[24:27]\nv_exp_f32 v150,v150\nv_cvt_pk_bf16_f32 v195,v146,v147\nM v[28:31],a[108:111],v[212:215],v[28:31]\n" \
"buffer_load_dwordx4 v244,s[40:43],s69 offen offset:1024 lds\nM v[32:35],a[112:115],v[208:211],v[32:35]\nv_exp_f32 v151,v151\nv_cvt_pk_bf16_f32 v196,v132,v133\nM v[36:39],a[112:115],v[212:215],v[36:39]\nds_read_b128 a[48:51],v250 offset:12288\nds_read_b128 a[52:55],v250 offset:13312\nM v[40:43],a[116:119],v[208:211],v[40:43]\n" \
"v_cvt_pk_bf16_f32 v197,v134,v135\nv_cvt_pk_bf16_f32 v198,v148,v149\nv_cvt_pk_bf16_f32 v199,v150,v151\nM v[44:47],a[116:119],v[212:215],v[44:47]\nbuffer_load_dwordx4 v245,s[40:43],s69 offen offset:2048 lds\nM v[48:51],a[120:123],v[208:211],v[48:51]\nv_exp_f32 v160,v160\nM v[52:55],a[120:123],v[212:215],v[52:55]\n" \
"ds_read_b128 a[56:59],v250 offset:14336\nds_read_b128 a[60:63],v250 offset:15360\nv_add_u32 v250,s94,v241\nM v[56:59],a[124:127],v[208:211],v[56:59]\nv_exp_f32 v161,v161\nM v[60:63],a[124:127],v[212:215],v[60:63]\nbuffer_load_dwordx4 v246,s[40:43],s69 offen offset:3072 lds\nM a[192:195],a[208:211],v[208:211],a[192:195]\n" \
"v_exp_f32 v162,v162\nM a[196:199],a[208:211],v[212:215],a[196:199]\nv_exp_f32 v163,v163\nv_exp_f32 v176,v176\nv_exp_f32 v177,v177\nv_exp_f32 v178,v178\nv_exp_f32 v179,v179\ns_waitcnt lgkmcnt(2)\n" \
"M v[128:131],a[0:3],a[128:131],v[224:227]\ns_lshl2_add_u32 s39,s62,s7\ns_lshl_b32 s39,s39,5\nM v[128:131],a[4:7],a[132:135],v[128:131]\nds_read_b128 a[64:67],v250 offset:0\nds_read_b128 a[68:71],v250 offset:1024\nM v[128:131],a[8:11],a[136:139],v[128:131]\ns_add_u32 s39,s39,s63\n" \
"s_lshl2_add_u32 s39,s39,73728\nM v[128:131],a[12:15],a[140:143],v[128:131]\nv_cvt_pk_bf16_f32 v208,v160,v161\nv_cvt_pk_bf16_f32 v209,v162,v163\nv_and_b32 v251,3,v249\nM v[132:135],a[0:3],a[144:147],v[228:231]\nds_read_b128 a[72:75],v250 offset:2048\nds_read_b128 a[76:79],v250 offset:3072\n" \
"M v[132:135],a[4:7],a[148:151],v[132:135]\nv_exp_f32 v164,v164\nM v[132:135],a[8:11],a[152:155],v[132:135]\ns_waitcnt vmcnt(16)\nbuffer_store_dword v252,off,s[52:55],s39 sc1\nM v[132:135],a[12:15],a[156:159],v[132:135]\nds_read_b128 a[80:83],v250 offset:4096\nds_read_b128 a[84:87],v250 offset:5120\n" \
"v_lshlrev_b32 v251,2,v251\nM v[144:147],a[16:19],a[128:131],v[224:227]\nv_exp_f32 v165,v165\nM v[144:147],a[20:23],a[132:135],v[144:147]\nv_exp_f32 v166,v166\nM v[144:147],a[24:27],a[136:139],v[144:147]\nds_read_b128 a[88:91],v250 offset:6144\nds_read_b128 a[92:95],v250 offset:7168\n" \
"M v[144:147],a[28:31],a[140:143],v[144:147]\nv_exp_f32 v167,v167\nv_cvt_pk_bf16_f32 v212,v164,v165\nM v[148:151],a[16:19],a[144:147],v[228:231]\nv_cvt_pk_bf16_f32 v213,v166,v167\ns_add_u32 s80,s79,4096\nM v[148:151],a[20:23],a[148:151],v[148:151]\nds_read_b128 a[96:99],v250 offset:8192\n" \
"ds_read_b128 a[100:103],v250 offset:9216\nM v[148:151],a[24:27],a[152:155],v[148:151]\nv_cvt_pk_bf16_f32 v210,v176,v177\nv_cvt_pk_bf16_f32 v211,v178,v179\nM v[148:151],a[28:31],a[156:159],v[148:151]\nv_exp_f32 v180,v180\ns_add_u32 s94,s94,0x4000\nM v[160:163],a[32:35],a[128:131],v[224:227]\n" \
"ds_read_b128 a[104:107],v250 offset:10240\nds_read_b128 a[108:111],v250 offset:11264\nM v[160:163],a[36:39],a[132:135],v[160:163]\nv_exp_f32 v181,v181\nM v[160:163],a[40:43],a[136:139],v[160:163]\nv_exp_f32 v182,v182\nM v[160:163],a[44:47],a[140:143],v[160:163]\nds_read_b128 a[112:115],v250 offset:12288\n" \
"ds_read_b128 a[116:119],v250 offset:13312\nv_cvt_pk_bf16_f32 v214,v180,v181\nM v[164:167],a[32:35],a[144:147],v[228:231]\nbuffer_load_dword v248,v251,s[52:55],s80 offen sc1\nM v[164:167],a[36:39],a[148:151],v[164:167]\nv_exp_f32 v183,v183\nM v[164:167],a[40:43],a[152:155],v[164:167]\nds_read_b128 a[120:123],v250 offset:14336\n" \
"ds_read_b128 a[124:127],v250 offset:15360\nM v[164:167],a[44:47],a[156:159],v[164:167]\nv_cvt_pk_bf16_f32 v215,v182,v183\ns_and_b32 s94,s94,0xffff\nv_add_u32 v250,s94,v241\nM v[176:179],a[48:51],a[128:131],v[224:227]\nv_exp_f32 v128,v128\nM v[176:179],a[52:55],a[132:135],v[176:179]\n" \
"s_add_u32 s65,s10,s67\ns_waitcnt lgkmcnt(4)\nM v[176:179],a[56:59],a[136:139],v[176:179]\nv_exp_f32 v129,v129\nM v[176:179],a[60:63],a[140:143],v[176:179]\nv_exp_f32 v130,v130\ns_add_u32 s65,s65,1048576\nM v[180:183],a[48:51],a[144:147],v[228:231]\n" \
"v_exp_f32 v131,v131\nM v[180:183],a[52:55],a[148:151],v[180:183]\nv_exp_f32 v144,v144\nM v[180:183],a[56:59],a[152:155],v[180:183]\nv_exp_f32 v145,v145\nM v[180:183],a[60:63],a[156:159],v[180:183]\nv_exp_f32 v146,v146\nv_add_u32 v226,131072,v242\n" \
"M v[0:3],a[64:67],v[192:195],v[0:3]\nv_exp_f32 v147,v147\nM v[4:7],a[64:67],v[196:199],v[4:7]\nv_exp_f32 v132,v132\nM v[8:11],a[68:71],v[192:195],v[8:11]\ns_waitcnt lgkmcnt(0)\ns_barrier\nM v[12:15],a[68:71],v[196:199],v[12:15]\n" \
"s_lshl4_add_u32 s69,s11,s68\ns_add_u32 m0,s64,16384\nds_read_b128 a[64:67],v250 offset:0\nM v[16:19],a[72:75],v[192:195],v[16:19]\nds_read_b128 a[68:71],v250 offset:1024\nv_xor_b32 v227,32,v226\nM v[20:23],a[72:75],v[196:199],v[20:23]\nds_read_b128 a[72:75],v250 offset:2048\n" \
"s_add_u32 s37,s12,s67\nM v[24:27],a[76:79],v[192:195],v[24:27]\nv_exp_f32 v133,v133\nM v[28:31],a[76:79],v[196:199],v[28:31]\nds_read_b128 a[76:79],v250 offset:3072\nv_exp_f32 v134,v134\nM v[32:35],a[80:83],v[192:195],v[32:35]\nbuffer_load_dwordx4 v243,s[40:43],s69 offen offset:0 lds\n" \
"M v[36:39],a[80:83],v[196:199],v[36:39]\nds_read_b128 a[80:83],v250 offset:4096\ns_add_u32 s38,s37,4096\nM v[40:43],a[84:87],v[192:195],v[40:43]\nv_exp_f32 v135,v135\nM v[44:47],a[84:87],v[196:199],v[44:47]\nv_exp_f32 v148,v148\nds_read_b128 a[84:87],v250 offset:5120\n" \
"M v[48:51],a[88:91],v[192:195],v[48:51]\nv_exp_f32 v149,v149\nM v[52:55],a[88:91],v[196:199],v[52:55]\nv_exp_f32 v150,v150\nM v[56:59],a[92:95],v[192:195],v[56:59]\nv_exp_f32 v151,v151\nM v[60:63],a[92:95],v[196:199],v[60:63]\nds_read_b128 a[88:91],v250 offset:6144\n" \
"ds_read_b128 a[92:95],v250 offset:7168\ns_lshl2_add_u32 s76,s61,s79\nM a[192:195],a[208:211],v[192:195],a[192:195]\nbuffer_load_dwordx4 v244,s[40:43],s69 offen offset:1024 lds\nM a[196:199],a[208:211],v[196:199],a[196:199]\nv_cvt_pk_bf16_f32 v192,v128,v129\nv_cvt_pk_bf16_f32 v193,v130,v131\nM v[0:3],a[96:99],v[208:211],v[0:3]\n" \
"v_cvt_pk_bf16_f32 v194,v144,v145\nv_cvt_pk_bf16_f32 v195,v146,v147\nM v[4:7],a[96:99],v[212:215],v[4:7]\nv_cvt_pk_bf16_f32 v196,v132,v133\nv_cvt_pk_bf16_f32 v197,v134,v135\nv_cvt_pk_bf16_f32 v198,v148,v149\nM v[8:11],a[100:103],v[208:211],v[8:11]\nv_cvt_pk_bf16_f32 v199,v150,v151\n" \
"ds_read_b128 a[96:99],v250 offset:8192\nM v[12:15],a[100:103],v[212:215],v[12:15]\nds_read_b128 a[100:103],v250 offset:9216\ns_lshr_b32 s70,s60,1\nM v[16:19],a[104:107],v[208:211],v[16:19]\nv_exp_f32 v160,v160\nM v[20:23],a[104:107],v[212:215],v[20:23]\nds_read_b128 a[104:107],v250 offset:10240\n" \
"v_exp_f32 v161,v161\nM v[24:27],a[108:111],v[208:211],v[24:27]\nbuffer_load_dwordx4 v245,s[40:43],s69 offen offset:2048 lds\nM v[28:31],a[108:111],v[212:215],v[28:31]\nds_read_b128 a[108:111],v250 offset:11264\ns_lshl_b32 s70,s70,7\nM v[32:35],a[112:115],v[208:211],v[32:35]\nv_exp_f32 v162,v162\n" \
"M v[36:39],a[112:115],v[212:215],v[36:39]\nv_exp_f32 v163,v163\nds_read_b128 a[112:115],v250 offset:12288\nM v[40:43],a[116:119],v[208:211],v[40:43]\nv_exp_f32 v176,v176\nM v[44:47],a[116:119],v[212:215],v[44:47]\nv_exp_f32 v177,v177\nM v[48:51],a[120:123],v[208:211],v[48:51]\n" \
"v_exp_f32 v178,v178\nM v[52:55],a[120:123],v[212:215],v[52:55]\nv_exp_f32 v179,v179\nds_read_b128 a[116:119],v250 offset:13312\nM v[56:59],a[124:127],v[208:211],v[56:59]\nbuffer_load_dwordx4 v246,s[40:43],s69 offen offset:3072 lds\nM v[60:63],a[124:127],v[212:215],v[60:63]\ns_waitcnt vmcnt(4)\n" \
"v_and_b32 v248,s17,v248\nM a[192:195],a[208:211],v[208:211],a[192:195]\nv_cmp_eq_u32 vcc,0,v248\ns_lshl4_add_u32 s69,s11,s68\nM a[196:199],a[208:211],v[212:215],a[196:199]\ns_cbranch_vccnz .Lps6\n.Lpb6:\ns_add_u32 s69,s69,0x10000\n" \
"s_waitcnt lgkmcnt(1)\nM v[0:3],a[64:67],v[192:195],v[0:3]\nbuffer_load_dwordx4 v[72:75],v240,s[52:55],s65 offen offset:0 sc1\nM v[4:7],a[64:67],v[196:199],v[4:7]\ns_add_u32 m0,s64,49152\nv_cvt_pk_bf16_f32 v208,v160,v161\nM v[8:11],a[68:71],v[192:195],v[8:11]\nv_exp_f32 v164,v164\n" \
"M v[12:15],a[68:71],v[196:199],v[12:15]\nv_exp_f32 v165,v165\nv_cvt_pk_bf16_f32 v209,v162,v163\nM v[16:19],a[72:75],v[192:195],v[16:19]\nbuffer_load_dwordx4 v243,s[40:43],s69 offen offset:0 lds\nM v[20:23],a[72:75],v[196:199],v[20:23]\nv_exp_f32 v166,v166\nM v[24:27],a[76:79],v[192:195],v[24:27]\n" \
"v_exp_f32 v167,v167\nM v[28:31],a[76:79],v[196:199],v[28:31]\nv_exp_f32 v180,v180\nv_cvt_pk_bf16_f32 v210,v176,v177\nM v[32:35],a[80:83],v[192:195],v[32:35]\nbuffer_load_dwordx4 v[76:79],v240,s[52:55],s65 offen offset:1024 sc1\nM v[36:39],a[80:83],v[196:199],v[36:39]\nv_exp_f32 v181,v181\n" \
"M v[40:43],a[84:87],v[192:195],v[40:43]\nv_exp_f32 v182,v182\nM v[44:47],a[84:87],v[196:199],v[44:47]\nv_exp_f32 v183,v183\nv_cvt_pk_bf16_f32 v211,v178,v179\nM v[48:51],a[88:91],v[192:195],v[48:51]\nbuffer_load_dwordx4 v244,s[40:43],s69 offen offset:1024 lds\nM v[52:55],a[88:91],v[196:199],v[52:55]\n" \
"v_cvt_pk_bf16_f32 v212,v164,v165\nv_cvt_pk_bf16_f32 v213,v166,v167\nM v[56:59],a[92:95],v[192:195],v[56:59]\nv_cvt_pk_bf16_f32 v214,v180,v181\nv_cvt_pk_bf16_f32 v215,v182,v183\nM v[60:63],a[92:95],v[196:199],v[60:63]\nds_read_b128 a[120:123],v250 offset:14336\nds_read_b128 a[124:127],v250 offset:15360\n" \
"v_cmp_eq_u32 vcc,2,v248\nM a[192:195],a[208:211],v[192:195],a[192:195]\nbuffer_load_dwordx4 v[88:91],v240,s[52:55],s65 offen offset:2048 sc1\nM a[196:199],a[208:211],v[196:199],a[196:199]\ns_cmp_lg_u64 vcc,0\ns_addc_u32 s99,s99,0\nM v[0:3],a[96:99],v[208:211],v[0:3]\ns_barrier\n" \
"ds_read_b64_tr_b16 v[200:201],v226 offset:0\nM v[4:7],a[96:99],v[212:215],v[4:7]\nds_read_b64_tr_b16 v[202:203],v226 offset:4096\nds_read_b64_tr_b16 v[204:205],v227 offset:0\nds_read_b64_tr_b16 v[206:207],v227 offset:4096\nM v[8:11],a[100:103],v[208:211],v[8:11]\nbuffer_load_dwordx4 v245,s[40:43],s69 offen offset:2048 lds\nM v[12:15],a[100:103],v[212:215],v[12:15]\n" \
"ds_read_b64_tr_b16 v[216:217],v226 offset:512\nds_read_b64_tr_b16 v[218:219],v226 offset:4608\nM v[16:19],a[104:107],v[208:211],v[16:19]\nds_read_b64_tr_b16 v[220:221],v227 offset:512\nds_read_b64_tr_b16 v[222:223],v227 offset:4608\nM v[20:23],a[104:107],v[212:215],v[20:23]\nds_read_b64_tr_b16 v[192:193],v226 offset:1024\nds_read_b64_tr_b16 v[194:195],v226 offset:5120\n" \
"ds_read_b64_tr_b16 v[196:197],v227 offset:1024\nM v[24:27],a[108:111],v[208:211],v[24:27]\nbuffer_load_dwordx4 v[92:95],v240,s[52:55],s65 offen offset:3072 sc1\nM v[28:31],a[108:111],v[212:215],v[28:31]\ns_add_u32 s65,s65,4096\nds_read_b64_tr_b16 v[198:199],v227 offset:5120\nM v[32:35],a[112:115],v[208:211],v[32:35]\nv_xor_b32 v250,0x8000,v250\n" \
"v_add_u32 v229,s64,v250\nM v[36:39],a[112:115],v[212:215],v[36:39]\ns_lshl_b32 s80,s62,9\ns_add_u32 s80,s80,73728\ns_waitcnt lgkmcnt(8)\nM v[40:43],a[116:119],v[208:211],v[40:43]\nbuffer_load_dwordx4 v246,s[40:43],s69 offen offset:3072 lds\nM v[44:47],a[116:119],v[212:215],v[44:47]\n" \
"v_and_b32 v236,15,v249\ns_lshl2_add_u32 s74,s63,s7\nM v[48:51],a[120:123],v[208:211],v[48:51]\ns_lshl_b32 s72,s62,7\nv_lshrrev_b32 v235,4,v249\nM v[52:55],a[120:123],v[212:215],v[52:55]\nv_lshlrev_b32 v236,1,v236\ns_add_u32 s95,s74,s72\n" \
"v_lshl_or_b32 v235,v235,8,v236\nM v[56:59],a[124:127],v[208:211],v[56:59]\nbuffer_load_dwordx4 v[104:107],v240,s[52:55],s65 offen offset:0 sc1\nM v[60:63],a[124:127],v[212:215],v[60:63]\ns_lshl_b32 s95,s95,10\nv_add_u32 v236,s95,v235\nM a[192:195],a[208:211],v[208:211],a[192:195]\nds_read_b64_tr_b16 v[208:209],v226 offset:1536\n" \
"ds_read_b64_tr_b16 v[210:211],v226 offset:5632\nM a[196:199],a[208:211],v[212:215],a[196:199]\nds_read_b64_tr_b16 v[212:213],v227 offset:1536\nds_read_b64_tr_b16 v[214:215],v227 offset:5632\ns_sub_u32 s74,s74,s72\nM v[128:131],v[64:67],v[200:203],0\ns_add_u32 s74,s74,1920\nv_accvgpr_read_b32 v224,a192\n" \
"s_lshl_b32 s74,s74,10\nM v[132:135],v[68:71],v[200:203],0\nds_read_b64_tr_b16 v[200:201],v226 offset:8192\nds_read_b64_tr_b16 v[202:203],v226 offset:12288\nv_accvgpr_read_b32 v228,a196\nM v[136:139],v[64:67],v[204:207],0\nv_add_f32 v225,v224,v228\nv_rcp_f32 v224,v224\n" \
"M v[140:143],v[68:71],v[204:207],0\nds_read_b64_tr_b16 v[204:205],v227 offset:8192\nds_read_b64_tr_b16 v[206:207],v227 offset:12288\ns_waitcnt lgkmcnt(8)\nM v[144:147],v[64:67],v[216:219],0\nbuffer_load_dwordx4 v[108:111],v240,s[52:55],s65 offen offset:1024 sc1\nv_mul_f32 v0,v0,v224\nM v[148:151],v[68:71],v[216:219],0\n" \
"ds_read_b64_tr_b16 v[216:217],v226 offset:8704\nds_read_b64_tr_b16 v[218:219],v226 offset:12800\nv_mul_f32 v1,v1,v224\nM v[152:155],v[64:67],v[220:223],0\nv_mul_f32 v2,v2,v224\nv_mul_f32 v3,v3,v224\nv_cvt_pk_bf16_f32 v0,v0,v1\nM v[156:159],v[68:71],v[220:223],0\n" \
"ds_read_b64_tr_b16 v[220:221],v227 offset:8704\nds_read_b64_tr_b16 v[222:223],v227 offset:12800\nv_mul_f32 v8,v8,v224\nM v[160:163],v[64:67],v[192:195],0\nv_mul_f32 v9,v9,v224\nv_mul_f32 v10,v10,v224\nv_mul_f32 v11,v11,v224\nM v[164:167],v[68:71],v[192:195],0\n" \
"ds_read_b64_tr_b16 v[192:193],v226 offset:9216\nds_read_b64_tr_b16 v[194:195],v226 offset:13312\nv_cvt_pk_bf16_f32 v1,v2,v3\nM v[168:171],v[64:67],v[196:199],0\nv_cvt_pk_bf16_f32 v2,v8,v9\nv_cvt_pk_bf16_f32 v3,v10,v11\nv_add_f32 v225,v225,v224\nM v[172:175],v[68:71],v[196:199],0\n" \
"ds_read_b64_tr_b16 v[196:197],v227 offset:9216\nds_read_b64_tr_b16 v[198:199],v227 offset:13312\ns_waitcnt lgkmcnt(6)\nM v[176:179],v[64:67],v[208:211],0\nbuffer_load_dwordx4 v[120:123],v240,s[52:55],s65 offen offset:2048 sc1\nv_mul_f32 v16,v16,v224\nM v[180:183],v[68:71],v[208:211],0\nds_read_b64_tr_b16 v[208:209],v226 offset:9728\n" \
"ds_read_b64_tr_b16 v[210:211],v226 offset:13824\nv_mul_f32 v17,v17,v224\nM v[184:187],v[64:67],v[212:215],0\nv_rcp_f32 v228,v228\nv_mul_f32 v18,v18,v224\nM v[188:191],v[68:71],v[212:215],0\nds_read_b64_tr_b16 v[212:213],v227 offset:9728\nds_read_b64_tr_b16 v[214:215],v227 offset:13824\n" \
"v_mul_f32 v4,v4,v228\nM v[128:131],v[80:83],v[200:203],v[128:131]\nbuffer_store_dwordx4 v[0:3],v240,s[52:55],s37 offen offset:0 sc1\nv_mul_f32 v5,v5,v228\nM v[132:135],v[84:87],v[200:203],v[132:135]\nds_read_b64_tr_b16 v[200:201],v226 offset:16384\nds_read_b64_tr_b16 v[202:203],v226 offset:20480\nv_mul_f32 v6,v6,v228\n" \
"M v[136:139],v[80:83],v[204:207],v[136:139]\nv_mul_f32 v7,v7,v228\nv_cvt_pk_bf16_f32 v4,v4,v5\nv_mul_f32 v12,v12,v228\nM v[140:143],v[84:87],v[204:207],v[140:143]\nds_read_b64_tr_b16 v[204:205],v227 offset:16384\nds_read_b64_tr_b16 v[206:207],v227 offset:20480\nv_mul_f32 v13,v13,v228\n" \
"M v[144:147],v[80:83],v[216:219],v[144:147]\nbuffer_load_dwordx4 v[124:127],v240,s[52:55],s65 offen offset:3072 sc1\ns_add_u32 s65,s11,s67\nM v[148:151],v[84:87],v[216:219],v[148:151]\nds_read_b64_tr_b16 v[216:217],v226 offset:16896\nds_read_b64_tr_b16 v[218:219],v226 offset:20992\ns_waitcnt lgkmcnt(6)\nM v[152:155],v[80:83],v[220:223],v[152:155]\n" \
"s_add_u32 s65,s65,1048576\nv_mul_f32 v14,v14,v228\nv_mul_f32 v15,v15,v228\nM v[156:159],v[84:87],v[220:223],v[156:159]\nds_read_b64_tr_b16 v[220:221],v227 offset:16896\nds_read_b64_tr_b16 v[222:223],v227 offset:20992\nv_cvt_pk_bf16_f32 v5,v6,v7\nM v[160:163],v[80:83],v[192:195],v[160:163]\n" \
"v_cvt_pk_bf16_f32 v6,v12,v13\nv_cvt_pk_bf16_f32 v7,v14,v15\nv_add_f32 v225,v225,v228\nM v[164:167],v[84:87],v[192:195],v[164:167]\nds_read_b64_tr_b16 v[192:193],v226 offset:17408\nds_read_b64_tr_b16 v[194:195],v226 offset:21504\nv_cmp_ngt_f32 vcc,s28,v225\nM v[168:171],v[80:83],v[196:199],v[168:171]\n" \
"buffer_store_dwordx4 v[4:7],v240,s[52:55],s37 offen offset:1024 sc1\nv_mul_f32 v19,v19,v224\nM v[172:175],v[84:87],v[196:199],v[172:175]\nds_read_b64_tr_b16 v[196:197],v227 offset:17408\nds_read_b64_tr_b16 v[198:199],v227 offset:21504\nv_cvt_pk_bf16_f32 v16,v16,v17\nM v[176:179],v[80:83],v[208:211],v[176:179]\nbuffer_load_dwordx4 a[0:3],v240,s[52:55],s65 offen offset:0 sc1\n" \
"v_mul_f32 v24,v24,v224\nM v[180:183],v[84:87],v[208:211],v[180:183]\nds_read_b64_tr_b16 v[208:209],v226 offset:17920\nds_read_b64_tr_b16 v[210:211],v226 offset:22016\nv_mul_f32 v25,v25,v224\nM v[184:187],v[80:83],v[212:215],v[184:187]\ns_and_b64 vcc,vcc,s[90:91]\ns_addc_u32 s99,s99,0\n" \
"v_mov_b32 v230,s99\nM v[188:191],v[84:87],v[212:215],v[188:191]\nds_read_b64_tr_b16 v[212:213],v227 offset:17920\nds_read_b64_tr_b16 v[214:215],v227 offset:22016\ns_waitcnt lgkmcnt(6)\nM v[128:131],v[96:99],v[200:203],v[128:131]\nds_write_b32 v229,v230\nv_mul_f32 v26,v26,v224\n" \
"v_mul_f32 v27,v27,v224\nM v[132:135],v[100:103],v[200:203],v[132:135]\nds_read_b64_tr_b16 v[200:201],v226 offset:24576\nds_read_b64_tr_b16 v[202:203],v226 offset:28672\nv_cvt_pk_bf16_f32 v17,v18,v19\nM v[136:139],v[96:99],v[204:207],v[136:139]\nv_cvt_pk_bf16_f32 v18,v24,v25\nv_cvt_pk_bf16_f32 v19,v26,v27\n" \
"v_lshl_add_u32 v229,v251,10,v250\nM v[140:143],v[100:103],v[204:207],v[140:143]\nds_read_b64_tr_b16 v[204:205],v227 offset:24576\nds_read_b64_tr_b16 v[206:207],v227 offset:28672\nv_mul_f32 v20,v20,v228\nM v[144:147],v[96:99],v[216:219],v[144:147]\nbuffer_load_dwordx4 a[4:7],v240,s[52:55],s65 offen offset:1024 sc1\nv_mul_f32 v21,v21,v228\n" \
"M v[148:151],v[100:103],v[216:219],v[148:151]\nds_read_b64_tr_b16 v[216:217],v226 offset:25088\nds_read_b64_tr_b16 v[218:219],v226 offset:29184\nv_mul_f32 v22,v22,v228\nM v[152:155],v[96:99],v[220:223],v[152:155]\nv_mul_f32 v23,v23,v228\nv_cvt_pk_bf16_f32 v20,v20,v21\nv_mul_f32 v28,v28,v228\n" \
"M v[156:159],v[100:103],v[220:223],v[156:159]\nds_read_b64_tr_b16 v[220:221],v227 offset:25088\nds_read_b64_tr_b16 v[222:223],v227 offset:29184\nv_mul_f32 v29,v29,v228\nM v[160:163],v[96:99],v[192:195],v[160:163]\nbuffer_store_dwordx4 v[16:19],v240,s[52:55],s37 offen offset:2048 sc1\ns_lshl4_add_u32 s69,s14,s68\nM v[164:167],v[100:103],v[192:195],v[164:167]\n" \
"ds_read_b64_tr_b16 v[192:193],v226 offset:25600\nds_read_b64_tr_b16 v[194:195],v226 offset:29696\ns_waitcnt lgkmcnt(6)\nM v[168:171],v[96:99],v[196:199],v[168:171]\ns_add_u32 m0,s64,65536\nv_mul_f32 v30,v30,v228\nv_mul_f32 v31,v31,v228\nM v[172:175],v[100:103],v[196:199],v[172:175]\n" \
"ds_read_b64_tr_b16 v[196:197],v227 offset:25600\nds_read_b64_tr_b16 v[198:199],v227 offset:29696\nv_cvt_pk_bf16_f32 v21,v22,v23\nM v[176:179],v[96:99],v[208:211],v[176:179]\nbuffer_load_dwordx4 a[8:11],v240,s[52:55],s65 offen offset:2048 sc1\nv_cvt_pk_bf16_f32 v22,v28,v29\nM v[180:183],v[100:103],v[208:211],v[180:183]\nds_read_b64_tr_b16 v[208:209],v226 offset:26112\n" \
"ds_read_b64_tr_b16 v[210:211],v226 offset:30208\nv_cvt_pk_bf16_f32 v23,v30,v31\nM v[184:187],v[96:99],v[212:215],v[184:187]\ns_barrier\nbuffer_load_dwordx4 v243,s[40:43],s69 offen offset:0 lds\nM v[188:191],v[100:103],v[212:215],v[188:191]\nds_read_b32 v230,v229\nds_read_b64_tr_b16 v[212:213],v227 offset:26112\n" \
"ds_read_b64_tr_b16 v[214:215],v227 offset:30208\nM v[128:131],v[112:115],v[200:203],v[128:131]\nv_add_u32 v226,0,v242\nv_xor_b32 v227,32,v226\nv_mul_f32 v32,v32,v224\nM v[132:135],v[116:119],v[200:203],v[132:135]\nds_read_b64_tr_b16 v[200:201],v226 offset:0\nds_read_b64_tr_b16 v[202:203],v226 offset:4096\n" \
"v_mul_f32 v33,v33,v224\nM v[136:139],v[112:115],v[204:207],v[136:139]\nv_mul_f32 v34,v34,v224\nv_mul_f32 v35,v35,v224\nv_cvt_pk_bf16_f32 v32,v32,v33\nM v[140:143],v[116:119],v[204:207],v[140:143]\nds_read_b64_tr_b16 v[204:205],v227 offset:0\nds_read_b64_tr_b16 v[206:207],v227 offset:4096\n" \
"s_waitcnt lgkmcnt(7)\nM v[144:147],v[112:115],v[216:219],v[144:147]\nbuffer_store_dwordx4 v[20:23],v240,s[52:55],s37 offen offset:3072 sc1\nbuffer_load_dwordx4 a[12:15],v240,s[52:55],s65 offen offset:3072 sc1\nM v[148:151],v[116:119],v[216:219],v[148:151]\nds_read_b64_tr_b16 v[216:217],v226 offset:512\nds_read_b64_tr_b16 v[218:219],v226 offset:4608\ns_add_u32 s65,s65,4096\n" \
"M v[152:155],v[112:115],v[220:223],v[152:155]\nbuffer_load_dwordx4 v244,s[40:43],s69 offen offset:1024 lds\nv_mul_f32 v40,v40,v224\nM v[156:159],v[116:119],v[220:223],v[156:159]\nds_read_b64_tr_b16 v[220:221],v227 offset:512\nds_read_b64_tr_b16 v[222:223],v227 offset:4608\nv_mul_f32 v41,v41,v224\nM v[160:163],v[112:115],v[192:195],v[160:163]\n" \
"v_mul_f32 v42,v42,v224\nv_mul_f32 v43,v43,v224\nv_cvt_pk_bf16_f32 v33,v34,v35\nM v[164:167],v[116:119],v[192:195],v[164:167]\ns_waitcnt lgkmcnt(4)\nv_cmp_ne_u32 vcc,0,v230\nds_read_b64_tr_b16 v[192:193],v226 offset:1024\nM v[168:171],v[112:115],v[196:199],v[168:171]\n" \
"s_cbranch_vccnz .Lredo\nds_read_b64_tr_b16 v[194:195],v226 offset:5120\nv_cvt_pk_bf16_f32 v34,v40,v41\nM v[172:175],v[116:119],v[196:199],v[172:175]\nv_cvt_pk_bf16_f32 v35,v42,v43\nds_read_b64_tr_b16 v[196:197],v227 offset:1024\nds_read_b64_tr_b16 v[198:199],v227 offset:5120\nM v[176:179],v[112:115],v[208:211],v[176:179]\n" \
"buffer_load_dwordx4 a[16:19],v240,s[52:55],s65 offen offset:0 sc1\nv_mul_f32 v36,v36,v228\nM v[180:183],v[116:119],v[208:211],v[180:183]\nds_read_b64_tr_b16 v[208:209],v226 offset:1536\nds_read_b64_tr_b16 v[210:211],v226 offset:5632\nv_mul_f32 v37,v37,v228\nM v[184:187],v[112:115],v[212:215],v[184:187]\nbuffer_store_dwordx4 v[32:35],v240,s[52:55],s38 offen offset:0 sc1\n" \
"buffer_load_dwordx4 v245,s[40:43],s69 offen offset:2048 lds\nM v[188:191],v[116:119],v[212:215],v[188:191]\nds_read_b64_tr_b16 v[212:213],v227 offset:1536\nds_read_b64_tr_b16 v[214:215],v227 offset:5632\ns_waitcnt vmcnt(24)\nM v[128:131],v[72:75],v[200:203],v[128:131]\nv_mul_f32 v38,v38,v228\nv_mul_f32 v39,v39,v228\n" \
"s_waitcnt vmcnt(22)\nM v[132:135],v[76:79],v[200:203],v[132:135]\nds_read_b64_tr_b16 v[200:201],v226 offset:8192\nds_read_b64_tr_b16 v[202:203],v226 offset:12288\nv_cvt_pk_bf16_f32 v36,v36,v37\nM v[136:139],v[72:75],v[204:207],v[136:139]\nv_mul_f32 v44,v44,v228\nv_mul_f32 v45,v45,v228\n" \
"v_mul_f32 v46,v46,v228\nM v[140:143],v[76:79],v[204:207],v[140:143]\nv_mul_f32 v47,v47,v228\nv_cvt_pk_bf16_f32 v37,v38,v39\ns_waitcnt lgkmcnt(4)\nM v[144:147],v[72:75],v[216:219],v[144:147]\nbuffer_load_dwordx4 a[20:23],v240,s[52:55],s65 offen offset:1024 sc1\nds_read_b64_tr_b16 v[204:205],v227 offset:8192\n" \
"M v[148:151],v[76:79],v[216:219],v[148:151]\nds_read_b64_tr_b16 v[206:207],v227 offset:12288\nv_cvt_pk_bf16_f32 v38,v44,v45\nv_cvt_pk_bf16_f32 v39,v46,v47\nM v[152:155],v[72:75],v[220:223],v[152:155]\nbuffer_load_dwordx4 v246,s[40:43],s69 offen offset:3072 lds\nds_read_b64_tr_b16 v[216:217],v226 offset:8704\nM v[156:159],v[76:79],v[220:223],v[156:159]\n" \
"ds_read_b64_tr_b16 v[218:219],v226 offset:12800\nds_read_b64_tr_b16 v[220:221],v227 offset:8704\nds_read_b64_tr_b16 v[222:223],v227 offset:12800\nM v[160:163],v[72:75],v[192:195],v[160:163]\nbuffer_store_dwordx4 v[36:39],v240,s[52:55],s38 offen offset:1024 sc1\ns_lshl4_add_u32 s69,s14,s68\nM v[164:167],v[76:79],v[192:195],v[164:167]\nds_read_b64_tr_b16 v[192:193],v226 offset:9216\n" \
"ds_read_b64_tr_b16 v[194:195],v226 offset:13312\ns_add_u32 s69,s69,0x10000\nM v[168:171],v[72:75],v[196:199],v[168:171]\ns_add_u32 m0,s64,98304\nv_mul_f32 v48,v48,v224\nv_mul_f32 v49,v49,v224\nM v[172:175],v[76:79],v[196:199],v[172:175]\nds_read_b64_tr_b16 v[196:197],v227 offset:9216\n" \
"ds_read_b64_tr_b16 v[198:199],v227 offset:13312\nv_mul_f32 v50,v50,v224\nM v[176:179],v[72:75],v[208:211],v[176:179]\nbuffer_load_dwordx4 a[24:27],v240,s[52:55],s65 offen offset:2048 sc1\nv_mul_f32 v51,v51,v224\nM v[180:183],v[76:79],v[208:211],v[180:183]\nv_cvt_pk_bf16_f32 v48,v48,v49\nv_mul_f32 v56,v56,v224\n" \
"s_waitcnt lgkmcnt(4)\nM v[184:187],v[72:75],v[212:215],v[184:187]\nbuffer_load_dwordx4 v243,s[40:43],s69 offen offset:0 lds\nv_mul_f32 v57,v57,v224\nM v[188:191],v[76:79],v[212:215],v[188:191]\nv_mul_f32 v58,v58,v224\nv_mul_f32 v59,v59,v224\ns_waitcnt vmcnt(25)\n" \
"M v[128:131],v[88:91],v[200:203],v[128:131]\nv_cvt_pk_bf16_f32 v49,v50,v51\nds_read_b64_tr_b16 v[208:209],v226 offset:9728\ns_waitcnt vmcnt(23)\nM v[132:135],v[92:95],v[200:203],v[132:135]\nds_read_b64_tr_b16 v[210:211],v226 offset:13824\nv_cvt_pk_bf16_f32 v50,v56,v57\nv_cvt_pk_bf16_f32 v51,v58,v59\n" \
"M v[136:139],v[88:91],v[204:207],v[136:139]\nbuffer_store_dwordx4 v[48:51],v240,s[52:55],s38 offen offset:2048 sc1\nds_read_b64_tr_b16 v[212:213],v227 offset:9728\nM v[140:143],v[92:95],v[204:207],v[140:143]\nds_read_b64_tr_b16 v[214:215],v227 offset:13824\nds_read_b64_tr_b16 v[200:201],v226 offset:32768\nds_read_b64_tr_b16 v[202:203],v226 offset:36864\nM v[144:147],v[88:91],v[216:219],v[144:147]\n" \
"buffer_load_dwordx4 a[28:31],v240,s[52:55],s65 offen offset:3072 sc1\nds_read_b64_tr_b16 v[204:205],v227 offset:32768\nM v[148:151],v[92:95],v[216:219],v[148:151]\nds_read_b64_tr_b16 v[206:207],v227 offset:36864\nds_read_b64_tr_b16 v[216:217],v226 offset:33280\nds_read_b64_tr_b16 v[218:219],v226 offset:37376\nM v[152:155],v[88:91],v[220:223],v[152:155]\nbuffer_load_dwordx4 v244,s[40:43],s69 offen offset:1024 lds\n" \
"v_mul_f32 v52,v52,v228\nM v[156:159],v[92:95],v[220:223],v[156:159]\nv_mul_f32 v53,v53,v228\nv_mul_f32 v54,v54,v228\ns_waitcnt lgkmcnt(8)\nM v[160:163],v[88:91],v[192:195],v[160:163]\nv_mul_f32 v55,v55,v228\nv_cvt_pk_bf16_f32 v52,v52,v53\n" \
"v_mul_f32 v60,v60,v228\nM v[164:167],v[92:95],v[192:195],v[164:167]\nv_mul_f32 v61,v61,v228\nv_mul_f32 v62,v62,v228\nv_mul_f32 v63,v63,v228\nM v[168:171],v[88:91],v[196:199],v[168:171]\nv_cvt_pk_bf16_f32 v53,v54,v55\ns_add_u32 s65,s14,s67\n" \
"ds_read_b64_tr_b16 v[220:221],v227 offset:33280\nM v[172:175],v[92:95],v[196:199],v[172:175]\nds_read_b64_tr_b16 v[222:223],v227 offset:37376\nv_cvt_pk_bf16_f32 v54,v60,v61\nv_cvt_pk_bf16_f32 v55,v62,v63\nM v[176:179],v[88:91],v[208:211],v[176:179]\ns_add_u32 s65,s65,1048576\nbuffer_store_dwordx4 v[52:55],v240,s[52:55],s38 offen offset:3072 sc1\n" \
"buffer_load_dwordx4 a[32:35],v240,s[52:55],s65 offen offset:0 sc1\nM v[180:183],v[92:95],v[208:211],v[180:183]\nds_read_b64_tr_b16 v[192:193],v226 offset:33792\nds_read_b64_tr_b16 v[194:195],v226 offset:37888\ns_waitcnt lgkmcnt(4)\nM v[184:187],v[88:91],v[212:215],v[184:187]\nbuffer_load_dwordx4 v245,s[40:43],s69 offen offset:2048 lds\nds_read_b64_tr_b16 v[196:197],v227 offset:33792\n" \
"M v[188:191],v[92:95],v[212:215],v[188:191]\nds_read_b64_tr_b16 v[198:199],v227 offset:37888\nds_read_b64_tr_b16 v[208:209],v226 offset:34304\ns_waitcnt vmcnt(25)\nM v[128:131],v[104:107],v[200:203],v[128:131]\nds_read_b64_tr_b16 v[210:211],v226 offset:38400\nds_read_b64_tr_b16 v[212:213],v227 offset:34304\nds_read_b64_tr_b16 v[214:215],v227 offset:38400\n" \
"M v[132:135],v[108:111],v[200:203],v[132:135]\nds_read_b64_tr_b16 v[200:201],v226 offset:40960\nds_read_b64_tr_b16 v[202:203],v226 offset:45056\nv_mov_b32 v250,81920\nM v[136:139],v[104:107],v[204:207],v[136:139]\nv_and_b32 v228,31,v249\nv_and_b32 v229,32,v249\nv_lshl_add_u32 v228,v228,2,s70\n" \
"M v[140:143],v[108:111],v[204:207],v[140:143]\nds_read_b64_tr_b16 v[204:205],v227 offset:40960\nds_read_b64_tr_b16 v[206:207],v227 offset:45056\nv_mul_u32_u24 v229,12,v229\nM v[144:147],v[104:107],v[216:219],v[144:147]\nbuffer_load_dwordx4 a[36:39],v240,s[52:55],s65 offen offset:1024 sc1\nM v[148:151],v[108:111],v[216:219],v[148:151]\nds_read_b64_tr_b16 v[216:217],v226 offset:41472\n" \
"s_waitcnt lgkmcnt(8)\nM v[152:155],v[104:107],v[220:223],v[152:155]\nbuffer_load_dwordx4 v246,s[40:43],s69 offen offset:3072 lds\nM v[156:159],v[108:111],v[220:223],v[156:159]\nds_read_b64_tr_b16 v[218:219],v226 offset:45568\nds_read_b64_tr_b16 v[220:221],v227 offset:41472\nM v[160:163],v[104:107],v[192:195],v[160:163]\nds_read_b64_tr_b16 v[222:223],v227 offset:45568\n" \
"v_xor_b32 v228,v228,v229\nM v[164:167],v[108:111],v[192:195],v[164:167]\nds_read_b64_tr_b16 v[192:193],v226 offset:41984\nds_read_b64_tr_b16 v[194:195],v226 offset:46080\nM v[168:171],v[104:107],v[196:199],v[168:171]\nv_add_u32 v237,s74,v235\ns_lshl_b32 s72,s7,9\nM v[172:175],v[108:111],v[196:199],v[172:175]\n" \
"ds_read_b64_tr_b16 v[196:197],v227 offset:41984\ns_waitcnt lgkmcnt(7)\nM v[176:179],v[104:107],v[208:211],v[176:179]\nbuffer_load_dwordx4 a[40:43],v240,s[52:55],s65 offen offset:2048 sc1\nM v[180:183],v[108:111],v[208:211],v[180:183]\nds_read_b64_tr_b16 v[198:199],v227 offset:46080\nds_read_b64_tr_b16 v[208:209],v226 offset:42496\nM v[184:187],v[104:107],v[212:215],v[184:187]\n" \
"s_barrier\nds_read_b64_tr_b16 v[210:211],v226 offset:46592\nM v[188:191],v[108:111],v[212:215],v[188:191]\nds_read_b64_tr_b16 v[212:213],v227 offset:42496\nds_read_b64_tr_b16 v[214:215],v227 offset:46592\nM v[128:131],v[120:123],v[200:203],v[128:131]\nv_lshl_add_u32 v239,v249,3,s72\ns_waitcnt vmcnt(26)\n" \
"M v[132:135],v[124:127],v[200:203],v[132:135]\nds_read_b64_tr_b16 v[200:201],v226 offset:16384\nds_read_b64_tr_b16 v[202:203],v226 offset:20480\nM v[136:139],v[120:123],v[204:207],v[136:139]\nds_write_b128 v250,a[244:247]\nM v[140:143],v[124:127],v[204:207],v[140:143]\nds_read_b64_tr_b16 v[204:205],v227 offset:16384\ns_waitcnt lgkmcnt(9)\n" \
"M v[144:147],v[120:123],v[216:219],v[144:147]\nbuffer_load_dwordx4 a[44:47],v240,s[52:55],s65 offen offset:3072 sc1\nM v[148:151],v[124:127],v[216:219],v[148:151]\nds_read_b64_tr_b16 v[206:207],v227 offset:20480\nds_read_b64_tr_b16 v[216:217],v226 offset:16896\nM v[152:155],v[120:123],v[220:223],v[152:155]\nds_read_b64_tr_b16 v[218:219],v226 offset:20992\ns_add_u32 s65,s65,4096\n" \
"M v[156:159],v[124:127],v[220:223],v[156:159]\nds_read_b64_tr_b16 v[220:221],v227 offset:16896\nds_read_b64_tr_b16 v[222:223],v227 offset:20992\nM v[160:163],v[120:123],v[192:195],v[160:163]\ns_mov_b32 s97,4096\ns_add_u32 s38,s33,16384\nM v[164:167],v[124:127],v[192:195],v[164:167]\nds_read_b64_tr_b16 v[192:193],v226 offset:17408\n" \
"s_waitcnt lgkmcnt(8)\nM v[168:171],v[120:123],v[196:199],v[168:171]\nds_read_b64_tr_b16 v[194:195],v226 offset:21504\nv_lshlrev_b32 v238,1,v240\nM v[172:175],v[124:127],v[196:199],v[172:175]\nds_read_b64_tr_b16 v[196:197],v227 offset:17408\nds_read_b64_tr_b16 v[198:199],v227 offset:21504\nM v[176:179],v[120:123],v[208:211],v[176:179]\n" \
"buffer_load_dwordx4 a[48:51],v240,s[52:55],s65 offen offset:0 sc1\nM v[180:183],v[124:127],v[208:211],v[180:183]\nds_read_b64_tr_b16 v[208:209],v226 offset:17920\nds_read_b64_tr_b16 v[210:211],v226 offset:22016\nM v[184:187],v[120:123],v[212:215],v[184:187]\nv_bfe_u32 v234,v249,2,2\nv_accvgpr_write_b32 a194,0\nM v[188:191],v[124:127],v[212:215],v[188:191]\n" \
"ds_read_b64_tr_b16 v[212:213],v227 offset:17920\ns_waitcnt vmcnt(25)\nM v[128:131],a[0:3],v[200:203],v[128:131]\nds_read_b64_tr_b16 v[214:215],v227 offset:22016\nv_accvgpr_write_b32 a195,0\nM v[132:135],a[4:7],v[200:203],v[132:135]\nds_read_b64_tr_b16 v[200:201],v226 offset:24576\ns_waitcnt lgkmcnt(7)\n" \
"M v[136:139],a[0:3],v[204:207],v[136:139]\nds_read_b64_tr_b16 v[202:203],v226 offset:28672\nv_accvgpr_write_b32 a196,0\nM v[140:143],a[4:7],v[204:207],v[140:143]\nds_read_b64_tr_b16 v[204:205],v227 offset:24576\nds_read_b64_tr_b16 v[206:207],v227 offset:28672\nM v[144:147],a[0:3],v[216:219],v[144:147]\nbuffer_load_dwordx4 a[52:55],v240,s[52:55],s65 offen offset:1024 sc1\n" \
"M v[148:151],a[4:7],v[216:219],v[148:151]\nds_read_b64_tr_b16 v[216:217],v226 offset:25088\nds_read_b64_tr_b16 v[218:219],v226 offset:29184\nM v[152:155],a[0:3],v[220:223],v[152:155]\nv_accvgpr_write_b32 a197,0\ns_load_dwordx2 s[44:45],s[4:5],0x68\nM v[156:159],a[4:7],v[220:223],v[156:159]\nds_read_b64_tr_b16 v[220:221],v227 offset:25088\n" \
"ds_read_b64_tr_b16 v[222:223],v227 offset:29184\nM v[160:163],a[0:3],v[192:195],v[160:163]\ns_mov_b32 s46,2097152\ns_add_u32 s95,s95,54525952\nM v[164:167],a[4:7],v[192:195],v[164:167]\nds_read_b64_tr_b16 v[192:193],v226 offset:25600\ns_waitcnt lgkmcnt(7)\nM v[168:171],a[0:3],v[196:199],v[168:171]\n" \
"ds_read_b64_tr_b16 v[194:195],v226 offset:29696\ns_add_u32 s74,s74,54525952\nM v[172:175],a[4:7],v[196:199],v[172:175]\nds_read_b64_tr_b16 v[196:197],v227 offset:25600\nds_read_b64_tr_b16 v[198:199],v227 offset:29696\nM v[176:179],a[0:3],v[208:211],v[176:179]\nbuffer_load_dwordx4 a[56:59],v240,s[52:55],s65 offen offset:2048 sc1\nM v[180:183],a[4:7],v[208:211],v[180:183]\n" \
"ds_read_b64_tr_b16 v[208:209],v226 offset:26112\nds_read_b64_tr_b16 v[210:211],v226 offset:30208\nM v[184:187],a[0:3],v[212:215],v[184:187]\nM v[188:191],a[4:7],v[212:215],v[188:191]\nds_read_b64_tr_b16 v[212:213],v227 offset:26112\ns_waitcnt vmcnt(25)\nM v[128:131],a[8:11],v[200:203],v[128:131]\nds_read_b64_tr_b16 v[214:215],v227 offset:30208\n" \
"s_waitcnt vmcnt(22)\nM v[132:135],a[12:15],v[200:203],v[132:135]\nds_read_b64_tr_b16 v[200:201],v226 offset:49152\ns_waitcnt lgkmcnt(8)\nM v[136:139],a[8:11],v[204:207],v[136:139]\nds_read_b64_tr_b16 v[202:203],v226 offset:53248\nM v[140:143],a[12:15],v[204:207],v[140:143]\nds_read_b64_tr_b16 v[204:205],v227 offset:49152\n" \
"ds_read_b64_tr_b16 v[206:207],v227 offset:53248\nM v[144:147],a[8:11],v[216:219],v[144:147]\nbuffer_load_dwordx4 a[60:63],v240,s[52:55],s65 offen offset:3072 sc1\nM v[148:151],a[12:15],v[216:219],v[148:151]\nds_read_b64_tr_b16 v[216:217],v226 offset:49664\nds_read_b64_tr_b16 v[218:219],v226 offset:53760\nM v[152:155],a[8:11],v[220:223],v[152:155]\ns_add_u32 s65,s10,s67\n" \
"M v[156:159],a[12:15],v[220:223],v[156:159]\nds_read_b64_tr_b16 v[220:221],v227 offset:49664\ns_waitcnt lgkmcnt(8)\nM v[160:163],a[8:11],v[192:195],v[160:163]\nds_read_b64_tr_b16 v[222:223],v227 offset:53760\nM v[164:167],a[12:15],v[192:195],v[164:167]\ns_waitcnt vmcnt(9)\nbuffer_store_dword v252,off,s[52:55],s76 sc1\n" \
"M v[168:171],a[8:11],v[196:199],v[168:171]\nds_read_b64_tr_b16 v[192:193],v226 offset:50176\nds_read_b64_tr_b16 v[194:195],v226 offset:54272\nM v[172:175],a[12:15],v[196:199],v[172:175]\nds_read_b64_tr_b16 v[196:197],v227 offset:50176\nds_read_b64_tr_b16 v[198:199],v227 offset:54272\nM v[176:179],a[8:11],v[208:211],v[176:179]\nM v[180:183],a[12:15],v[208:211],v[180:183]\n" \
"ds_read_b64_tr_b16 v[208:209],v226 offset:50688\ns_waitcnt lgkmcnt(7)\nM v[184:187],a[8:11],v[212:215],v[184:187]\nds_read_b64_tr_b16 v[210:211],v226 offset:54784\nM v[188:191],a[12:15],v[212:215],v[188:191]\nds_read_b64_tr_b16 v[212:213],v227 offset:50688\nds_read_b64_tr_b16 v[214:215],v227 offset:54784\nM v[128:131],a[16:19],v[200:203],v[128:131]\n" \
"M v[132:135],a[20:23],v[200:203],v[132:135]\nds_read_b64_tr_b16 v[200:201],v226 offset:57344\nds_read_b64_tr_b16 v[202:203],v226 offset:61440\nM v[136:139],a[16:19],v[204:207],v[136:139]\nM v[140:143],a[20:23],v[204:207],v[140:143]\nds_read_b64_tr_b16 v[204:205],v227 offset:57344\nds_read_b64_tr_b16 v[206:207],v227 offset:61440\nM v[144:147],a[16:19],v[216:219],v[144:147]\n" \
"M v[148:151],a[20:23],v[216:219],v[148:151]\nds_read_b64_tr_b16 v[216:217],v226 offset:57856\ns_waitcnt lgkmcnt(9)\nM v[152:155],a[16:19],v[220:223],v[152:155]\nbuffer_load_dword v248,v228,s[52:55],s80 offen sc1\nM v[156:159],a[20:23],v[220:223],v[156:159]\nds_read_b64_tr_b16 v[218:219],v226 offset:61952\nds_read_b64_tr_b16 v[220:221],v227 offset:57856\n" \
"M v[160:163],a[16:19],v[192:195],v[160:163]\nds_read_b64_tr_b16 v[222:223],v227 offset:61952\nM v[164:167],a[20:23],v[192:195],v[164:167]\nds_read_b64_tr_b16 v[192:193],v226 offset:58368\nds_read_b64_tr_b16 v[194:195],v226 offset:62464\nM v[168:171],a[16:19],v[196:199],v[168:171]\nglobal_load_ushort v64,v236,s[8:9] offset:0\nM v[172:175],a[20:23],v[196:199],v[172:175]\n" \
"ds_read_b64_tr_b16 v[196:197],v227 offset:58368\ns_waitcnt lgkmcnt(7)\nM v[176:179],a[16:19],v[208:211],v[176:179]\nglobal_load_ushort v80,v237,s[8:9] offset:0\nM v[180:183],a[20:23],v[208:211],v[180:183]\nds_read_b64_tr_b16 v[198:199],v227 offset:62464\nds_read_b64_tr_b16 v[208:209],v226 offset:58880\nM v[184:187],a[16:19],v[212:215],v[184:187]\n" \
"s_waitcnt vmcnt(9)\ns_barrier\nM v[188:191],a[20:23],v[212:215],v[188:191]\nglobal_load_ushort v96,v235,s[20:21] offset:0\nM v[128:131],a[24:27],v[200:203],v[128:131]\nds_read_b64_tr_b16 v[210:211],v226 offset:62976\nds_read_b64_tr_b16 v[212:213],v227 offset:58880\nM v[132:135],a[28:31],v[200:203],v[132:135]\n" \
"global_load_ushort v65,v236,s[8:9] offset:32\nM v[136:139],a[24:27],v[204:207],v[136:139]\nds_read_b64_tr_b16 v[214:215],v227 offset:62976\nv_add_u32 v226,65536,v242\nM v[140:143],a[28:31],v[204:207],v[140:143]\nglobal_load_ushort v81,v237,s[8:9] offset:32\ns_waitcnt lgkmcnt(5)\nM v[144:147],a[24:27],v[216:219],v[144:147]\n" \
"ds_read_b64_tr_b16 v[200:201],v226 offset:0\nds_read_b64_tr_b16 v[202:203],v226 offset:4096\nM v[148:151],a[28:31],v[216:219],v[148:151]\nglobal_load_ushort v97,v235,s[20:21] offset:32\nM v[152:155],a[24:27],v[220:223],v[152:155]\nv_xor_b32 v227,32,v226\nds_read_b64_tr_b16 v[204:205],v227 offset:0\nM v[156:159],a[28:31],v[220:223],v[156:159]\n" \
"ds_read_b64_tr_b16 v[206:207],v227 offset:4096\nglobal_load_ushort v66,v236,s[8:9] offset:64\nM v[160:163],a[24:27],v[192:195],v[160:163]\nds_read_b64_tr_b16 v[216:217],v226 offset:512\nds_read_b64_tr_b16 v[218:219],v226 offset:4608\nM v[164:167],a[28:31],v[192:195],v[164:167]\nglobal_load_ushort v82,v237,s[8:9] offset:64\ns_waitcnt lgkmcnt(6)\n" \
"M v[168:171],a[24:27],v[196:199],v[168:171]\nds_read_b64_tr_b16 v[220:221],v227 offset:512\nds_read_b64_tr_b16 v[222:223],v227 offset:4608\nM v[172:175],a[28:31],v[196:199],v[172:175]\nglobal_load_ushort v98,v235,s[20:21] offset:64\nM v[176:179],a[24:27],v[208:211],v[176:179]\nds_read_b64_tr_b16 v[192:193],v226 offset:1024\nds_read_b64_tr_b16 v[194:195],v226 offset:5120\n" \
"M v[180:183],a[28:31],v[208:211],v[180:183]\nglobal_load_ushort v67,v236,s[8:9] offset:96\nM v[184:187],a[24:27],v[212:215],v[184:187]\nds_read_b64_tr_b16 v[196:197],v227 offset:1024\nds_read_b64_tr_b16 v[198:199],v227 offset:5120\nM v[188:191],a[28:31],v[212:215],v[188:191]\nglobal_load_ushort v83,v237,s[8:9] offset:96\ns_waitcnt lgkmcnt(6)\n" \
"M v[128:131],a[32:35],v[200:203],v[128:131]\nds_read_b64_tr_b16 v[208:209],v226 offset:1536\nds_read_b64_tr_b16 v[210:211],v226 offset:5632\nM v[132:135],a[36:39],v[200:203],v[132:135]\nglobal_load_ushort v99,v235,s[20:21] offset:96\nM v[136:139],a[32:35],v[204:207],v[136:139]\nds_read_b64_tr_b16 v[212:213],v227 offset:1536\nds_read_b64_tr_b16 v[214:215],v227 offset:5632\n" \
"M v[140:143],a[36:39],v[204:207],v[140:143]\nglobal_load_ushort v68,v236,s[8:9] offset:128\nM v[144:147],a[32:35],v[216:219],v[144:147]\nds_read_b64_tr_b16 v[200:201],v226 offset:8192\nds_read_b64_tr_b16 v[202:203],v226 offset:12288\nM v[148:151],a[36:39],v[216:219],v[148:151]\nglobal_load_ushort v84,v237,s[8:9] offset:128\ns_waitcnt lgkmcnt(6)\n" \
"M v[152:155],a[32:35],v[220:223],v[152:155]\nds_read_b64_tr_b16 v[204:205],v227 offset:8192\nds_read_b64_tr_b16 v[206:207],v227 offset:12288\nM v[156:159],a[36:39],v[220:223],v[156:159]\nglobal_load_ushort v100,v235,s[20:21] offset:128\nM v[160:163],a[32:35],v[192:195],v[160:163]\nds_read_b64_tr_b16 v[216:217],v226 offset:8704\nds_read_b64_tr_b16 v[218:219],v226 offset:12800\n" \
"M v[164:167],a[36:39],v[192:195],v[164:167]\nglobal_load_ushort v69,v236,s[8:9] offset:160\nM v[168:171],a[32:35],v[196:199],v[168:171]\nds_read_b64_tr_b16 v[220:221],v227 offset:8704\nds_read_b64_tr_b16 v[222:223],v227 offset:12800\nM v[172:175],a[36:39],v[196:199],v[172:175]\nglobal_load_ushort v85,v237,s[8:9] offset:160\ns_waitcnt lgkmcnt(6)\n" \
"M v[176:179],a[32:35],v[208:211],v[176:179]\nds_read_b64_tr_b16 v[192:193],v226 offset:9216\nds_read_b64_tr_b16 v[194:195],v226 offset:13312\nM v[180:183],a[36:39],v[208:211],v[180:183]\nglobal_load_ushort v101,v235,s[20:21] offset:160\nM v[184:187],a[32:35],v[212:215],v[184:187]\nds_read_b64_tr_b16 v[196:197],v227 offset:9216\nds_read_b64_tr_b16 v[198:199],v227 offset:13312\n" \
"M v[188:191],a[36:39],v[212:215],v[188:191]\nglobal_load_ushort v70,v236,s[8:9] offset:192\nM v[128:131],a[40:43],v[200:203],v[128:131]\nds_read_b64_tr_b16 v[208:209],v226 offset:9728\ns_waitcnt vmcnt(22)\nM v[132:135],a[44:47],v[200:203],v[132:135]\nds_read_b64_tr_b16 v[210:211],v226 offset:13824\nglobal_load_ushort v86,v237,s[8:9] offset:192\n" \
"s_waitcnt lgkmcnt(6)\nM v[136:139],a[40:43],v[204:207],v[136:139]\ns_waitcnt vmcnt(20)\nv_cmp_eq_u32 vcc,0,v248\nM v[140:143],a[44:47],v[204:207],v[140:143]\nds_read_b64_tr_b16 v[212:213],v227 offset:9728\nds_read_b64_tr_b16 v[214:215],v227 offset:13824\nglobal_load_ushort v102,v235,s[20:21] offset:192\n" \
"M v[144:147],a[40:43],v[216:219],v[144:147]\ns_cbranch_vccnz .Lps7\n.Lpb7:\nbuffer_load_dword v248,v251,s[52:55],s79 offen sc1\nM v[148:151],a[44:47],v[216:219],v[148:151]\nds_read_b64_tr_b16 v[200:201],v226 offset:32768\nds_read_b64_tr_b16 v[202:203],v226 offset:36864\nglobal_load_ushort v71,v236,s[8:9] offset:224\n" \
"M v[152:155],a[40:43],v[220:223],v[152:155]\nds_read_b64_tr_b16 v[204:205],v227 offset:32768\nds_read_b64_tr_b16 v[206:207],v227 offset:36864\nM v[156:159],a[44:47],v[220:223],v[156:159]\nglobal_load_ushort v87,v237,s[8:9] offset:224\ns_waitcnt lgkmcnt(6)\nM v[160:163],a[40:43],v[192:195],v[160:163]\nds_read_b64_tr_b16 v[216:217],v226 offset:33280\n" \
"ds_read_b64_tr_b16 v[218:219],v226 offset:37376\nM v[164:167],a[44:47],v[192:195],v[164:167]\nglobal_load_ushort v103,v235,s[20:21] offset:224\nM v[168:171],a[40:43],v[196:199],v[168:171]\nds_read_b64_tr_b16 v[220:221],v227 offset:33280\nds_read_b64_tr_b16 v[222:223],v227 offset:37376\nM v[172:175],a[44:47],v[196:199],v[172:175]\nbuffer_load_dwordx2 v[112:113],v239,s[52:55],0 offen offset:0 sc1\n" \
"M v[176:179],a[40:43],v[208:211],v[176:179]\nds_read_b64_tr_b16 v[192:193],v226 offset:33792\nds_read_b64_tr_b16 v[194:195],v226 offset:37888\nM v[180:183],a[44:47],v[208:211],v[180:183]\nbuffer_load_dwordx2 v[114:115],v239,s[52:55],0 offen offset:2048 sc1\ns_waitcnt lgkmcnt(6)\nM v[184:187],a[40:43],v[212:215],v[184:187]\nds_read_b64_tr_b16 v[196:197],v227 offset:33792\n" \
"ds_read_b64_tr_b16 v[198:199],v227 offset:37888\nM v[188:191],a[44:47],v[212:215],v[188:191]\nbuffer_load_dwordx2 v[116:117],v239,s[52:55],s97 offen offset:0 sc1\nM v[128:131],a[48:51],v[200:203],v[128:131]\nds_read_b64_tr_b16 v[208:209],v226 offset:34304\nds_read_b64_tr_b16 v[210:211],v226 offset:38400\nM v[132:135],a[52:55],v[200:203],v[132:135]\nbuffer_load_dwordx2 v[118:119],v239,s[52:55],s97 offen offset:2048 sc1\n" \
"M v[136:139],a[48:51],v[204:207],v[136:139]\nds_read_b64_tr_b16 v[212:213],v227 offset:34304\nds_read_b64_tr_b16 v[214:215],v227 offset:38400\nM v[140:143],a[52:55],v[204:207],v[140:143]\nds_read_b64_tr_b16 v[200:201],v226 offset:40960\ns_waitcnt lgkmcnt(7)\nM v[144:147],a[48:51],v[216:219],v[144:147]\nds_read_b64_tr_b16 v[202:203],v226 offset:45056\n" \
"ds_read_b64_tr_b16 v[204:205],v227 offset:40960\nM v[148:151],a[52:55],v[216:219],v[148:151]\nds_read_b64_tr_b16 v[206:207],v227 offset:45056\nds_read_b64_tr_b16 v[216:217],v226 offset:41472\nM v[152:155],a[48:51],v[220:223],v[152:155]\nds_read_b64_tr_b16 v[218:219],v226 offset:45568\nv_lshrrev_b32 v235,4,v249\nM v[156:159],a[52:55],v[220:223],v[156:159]\n" \
"ds_read_b64_tr_b16 v[220:221],v227 offset:41472\nds_read_b64_tr_b16 v[222:223],v227 offset:45568\nM v[160:163],a[48:51],v[192:195],v[160:163]\nM v[164:167],a[52:55],v[192:195],v[164:167]\nds_read_b64_tr_b16 v[192:193],v226 offset:41984\ns_waitcnt lgkmcnt(11)\nM v[168:171],a[48:51],v[196:199],v[168:171]\nds_read_b64_tr_b16 v[194:195],v226 offset:46080\n" \
"M v[172:175],a[52:55],v[196:199],v[172:175]\nds_read_b64_tr_b16 v[196:197],v227 offset:41984\nds_read_b64_tr_b16 v[198:199],v227 offset:46080\nM v[176:179],a[48:51],v[208:211],v[176:179]\nM v[180:183],a[52:55],v[208:211],v[180:183]\nds_read_b64_tr_b16 v[208:209],v226 offset:42496\ns_waitcnt lgkmcnt(10)\nM v[184:187],a[48:51],v[212:215],v[184:187]\n" \
"ds_read_b64_tr_b16 v[210:211],v226 offset:46592\nv_add_u32 v226,131072,v242\nM v[188:191],a[52:55],v[212:215],v[188:191]\nds_read_b64_tr_b16 v[212:213],v227 offset:42496\nds_read_b64_tr_b16 v[214:215],v227 offset:46592\nM v[128:131],a[56:59],v[200:203],v[128:131]\nv_xor_b32 v227,32,v226\nM v[132:135],a[60:63],v[200:203],v[132:135]\n" \
"ds_read_b64_tr_b16 v[200:201],v226 offset:0\ns_waitcnt lgkmcnt(8)\nM v[136:139],a[56:59],v[204:207],v[136:139]\nds_read_b64_tr_b16 v[202:203],v226 offset:4096\nM v[140:143],a[60:63],v[204:207],v[140:143]\nds_read_b64_tr_b16 v[204:205],v227 offset:0\nds_read_b64_tr_b16 v[206:207],v227 offset:4096\nM v[144:147],a[56:59],v[216:219],v[144:147]\n" \
"M v[148:151],a[60:63],v[216:219],v[148:151]\nds_read_b64_tr_b16 v[216:217],v226 offset:512\nds_read_b64_tr_b16 v[218:219],v226 offset:4608\nM v[152:155],a[56:59],v[220:223],v[152:155]\ns_waitcnt vmcnt(7)\nv_cmp_eq_u32 vcc,0,v248\nM v[156:159],a[60:63],v[220:223],v[156:159]\ns_cbranch_vccnz .Lps8\n" \
".Lpb8:\ns_waitcnt lgkmcnt(7)\nM v[160:163],a[56:59],v[192:195],v[160:163]\nbuffer_load_dwordx4 a[64:67],v240,s[52:55],s65 offen offset:0 sc1\nM v[164:167],a[60:63],v[192:195],v[164:167]\nds_read_b64_tr_b16 v[220:221],v227 offset:512\nds_read_b64_tr_b16 v[222:223],v227 offset:4608\nM v[168:171],a[56:59],v[196:199],v[168:171]\n" \
"ds_read_b64_tr_b16 v[192:193],v226 offset:1024\nds_read_b64_tr_b16 v[194:195],v226 offset:5120\nM v[172:175],a[60:63],v[196:199],v[172:175]\nds_read_b64_tr_b16 v[196:197],v227 offset:1024\nds_read_b64_tr_b16 v[198:199],v227 offset:5120\nM v[176:179],a[56:59],v[208:211],v[176:179]\nbuffer_load_dwordx4 a[68:71],v240,s[52:55],s65 offen offset:1024 sc1\nM v[180:183],a[60:63],v[208:211],v[180:183]\n" \
"ds_read_b64_tr_b16 v[208:209],v226 offset:1536\ns_waitcnt lgkmcnt(7)\nM v[184:187],a[56:59],v[212:215],v[184:187]\nds_read_b64_tr_b16 v[210:211],v226 offset:5632\nv_cvt_pk_bf16_f32 v128,v128,v136\nM v[188:191],a[60:63],v[212:215],v[188:191]\nds_read_b64_tr_b16 v[212:213],v227 offset:1536\nds_read_b64_tr_b16 v[214:215],v227 offset:5632\n" \
"M v[8:11],v[0:3],v[200:203],0\nbuffer_load_dwordx4 a[72:75],v240,s[52:55],s65 offen offset:2048 sc1\nM v[12:15],v[4:7],v[200:203],0\nds_read_b64_tr_b16 v[200:201],v226 offset:8192\nds_read_b64_tr_b16 v[202:203],v226 offset:12288\nM v[24:27],v[0:3],v[204:207],0\nv_cvt_pk_bf16_f32 v129,v129,v137\nv_cvt_pk_bf16_f32 v130,v130,v138\n" \
"M v[28:31],v[4:7],v[204:207],0\nds_read_b64_tr_b16 v[204:205],v227 offset:8192\nds_read_b64_tr_b16 v[206:207],v227 offset:12288\nM v[40:43],v[0:3],v[216:219],0\nbuffer_load_dwordx4 a[76:79],v240,s[52:55],s65 offen offset:3072 sc1\nM v[44:47],v[4:7],v[216:219],0\ns_add_u32 s65,s65,4096\ns_waitcnt lgkmcnt(6)\n" \
"M v[56:59],v[0:3],v[220:223],0\nds_read_b64_tr_b16 v[216:217],v226 offset:8704\nds_read_b64_tr_b16 v[218:219],v226 offset:12800\nM v[60:63],v[4:7],v[220:223],0\nds_read_b64_tr_b16 v[220:221],v227 offset:8704\nds_read_b64_tr_b16 v[222:223],v227 offset:12800\nM v[72:75],v[0:3],v[192:195],0\nbuffer_load_dwordx4 a[80:83],v240,s[52:55],s65 offen offset:0 sc1\n" \
"M v[76:79],v[4:7],v[192:195],0\nds_read_b64_tr_b16 v[192:193],v226 offset:9216\nds_read_b64_tr_b16 v[194:195],v226 offset:13312\nM v[88:91],v[0:3],v[196:199],0\nv_cvt_pk_bf16_f32 v131,v131,v139\nv_cvt_pk_bf16_f32 v144,v144,v152\nM v[92:95],v[4:7],v[196:199],0\nv_cvt_pk_bf16_f32 v160,v160,v168\n" \
"v_cvt_pk_bf16_f32 v176,v176,v184\nM v[104:107],v[0:3],v[208:211],0\nbuffer_load_dwordx4 a[84:87],v240,s[52:55],s65 offen offset:1024 sc1\nM v[108:111],v[4:7],v[208:211],0\nv_min_u32 v136,-2,v128\ns_waitcnt lgkmcnt(5)\nM v[120:123],v[0:3],v[212:215],0\nv_min_u32 v137,-2,v144\n" \
"v_min_u32 v138,-2,v160\nv_min_u32 v139,-2,v176\nbuffer_store_dwordx4 v[136:139],v253,s[52:55],s33 offen offset:0 sc1\nM v[124:127],v[4:7],v[212:215],0\nds_read_b64_tr_b16 v[196:197],v227 offset:9216\nds_read_b64_tr_b16 v[198:199],v227 offset:13312\nM v[8:11],v[16:19],v[200:203],v[8:11]\nbuffer_load_dwordx4 a[88:91],v240,s[52:55],s65 offen offset:2048 sc1\n" \
"M v[12:15],v[20:23],v[200:203],v[12:15]\nds_read_b64_tr_b16 v[208:209],v226 offset:9728\nds_read_b64_tr_b16 v[210:211],v226 offset:13824\nM v[24:27],v[16:19],v[204:207],v[24:27]\nds_read_b64_tr_b16 v[212:213],v227 offset:9728\nds_read_b64_tr_b16 v[214:215],v227 offset:13824\nM v[28:31],v[20:23],v[204:207],v[28:31]\nds_read_b64_tr_b16 v[200:201],v226 offset:16384\n" \
"s_waitcnt lgkmcnt(7)\nM v[40:43],v[16:19],v[216:219],v[40:43]\nbuffer_load_dwordx4 a[92:95],v240,s[52:55],s65 offen offset:3072 sc1\nM v[44:47],v[20:23],v[216:219],v[44:47]\nds_read_b64_tr_b16 v[202:203],v226 offset:20480\nds_read_b64_tr_b16 v[204:205],v227 offset:16384\nM v[56:59],v[16:19],v[220:223],v[56:59]\nds_read_b64_tr_b16 v[206:207],v227 offset:20480\n" \
"s_add_u32 s65,s11,s67\nM v[60:63],v[20:23],v[220:223],v[60:63]\nds_read_b64_tr_b16 v[216:217],v226 offset:16896\nds_read_b64_tr_b16 v[218:219],v226 offset:20992\nM v[72:75],v[16:19],v[192:195],v[72:75]\nbuffer_load_dwordx4 a[96:99],v240,s[52:55],s65 offen offset:0 sc1\nM v[76:79],v[20:23],v[192:195],v[76:79]\nds_read_b64_tr_b16 v[220:221],v227 offset:16896\n" \
"s_waitcnt lgkmcnt(8)\nM v[88:91],v[16:19],v[196:199],v[88:91]\nds_read_b64_tr_b16 v[222:223],v227 offset:20992\nds_read_b64_tr_b16 v[192:193],v226 offset:17408\nM v[92:95],v[20:23],v[196:199],v[92:95]\nds_read_b64_tr_b16 v[194:195],v226 offset:21504\nv_cvt_pk_bf16_f32 v145,v145,v153\nM v[104:107],v[16:19],v[208:211],v[104:107]\n" \
"buffer_load_dwordx4 a[100:103],v240,s[52:55],s65 offen offset:1024 sc1\nM v[108:111],v[20:23],v[208:211],v[108:111]\nv_cvt_pk_bf16_f32 v146,v146,v154\nv_cvt_pk_bf16_f32 v147,v147,v155\nv_cvt_pk_bf16_f32 v161,v161,v169\nv_cvt_pk_bf16_f32 v177,v177,v185\ns_waitcnt lgkmcnt(6)\nM v[120:123],v[16:19],v[212:215],v[120:123]\n" \
"v_min_u32 v152,-2,v129\nv_min_u32 v153,-2,v145\nv_min_u32 v154,-2,v161\nv_min_u32 v155,-2,v177\nbuffer_store_dwordx4 v[152:155],v253,s[52:55],s33 offen offset:1024 sc1\nM v[124:127],v[20:23],v[212:215],v[124:127]\nds_read_b64_tr_b16 v[196:197],v227 offset:17408\nds_read_b64_tr_b16 v[198:199],v227 offset:21504\n" \
"M v[8:11],v[32:35],v[200:203],v[8:11]\nbuffer_load_dwordx4 a[104:107],v240,s[52:55],s65 offen offset:2048 sc1\nM v[12:15],v[36:39],v[200:203],v[12:15]\nds_read_b64_tr_b16 v[208:209],v226 offset:17920\nds_read_b64_tr_b16 v[210:211],v226 offset:22016\nM v[24:27],v[32:35],v[204:207],v[24:27]\nds_read_b64_tr_b16 v[212:213],v227 offset:17920\nds_read_b64_tr_b16 v[214:215],v227 offset:22016\n" \
"M v[28:31],v[36:39],v[204:207],v[28:31]\nds_read_b64_tr_b16 v[200:201],v226 offset:24576\ns_waitcnt lgkmcnt(7)\nM v[40:43],v[32:35],v[216:219],v[40:43]\nbuffer_load_dwordx4 a[108:111],v240,s[52:55],s65 offen offset:3072 sc1\nM v[44:47],v[36:39],v[216:219],v[44:47]\nds_read_b64_tr_b16 v[202:203],v226 offset:28672\nds_read_b64_tr_b16 v[204:205],v227 offset:24576\n" \
"M v[56:59],v[32:35],v[220:223],v[56:59]\nds_read_b64_tr_b16 v[206:207],v227 offset:28672\ns_add_u32 s65,s65,4096\nM v[60:63],v[36:39],v[220:223],v[60:63]\nds_read_b64_tr_b16 v[216:217],v226 offset:25088\nds_read_b64_tr_b16 v[218:219],v226 offset:29184\nM v[72:75],v[32:35],v[192:195],v[72:75]\nbuffer_load_dwordx4 a[112:115],v240,s[52:55],s65 offen offset:0 sc1\n" \
"M v[76:79],v[36:39],v[192:195],v[76:79]\nds_read_b64_tr_b16 v[220:221],v227 offset:25088\ns_waitcnt lgkmcnt(8)\nM v[88:91],v[32:35],v[196:199],v[88:91]\nds_read_b64_tr_b16 v[222:223],v227 offset:29184\nds_read_b64_tr_b16 v[192:193],v226 offset:25600\nM v[92:95],v[36:39],v[196:199],v[92:95]\nds_read_b64_tr_b16 v[194:195],v226 offset:29696\n" \
"v_cvt_pk_bf16_f32 v162,v162,v170\nM v[104:107],v[32:35],v[208:211],v[104:107]\nbuffer_load_dwordx4 a[116:119],v240,s[52:55],s65 offen offset:1024 sc1\nM v[108:111],v[36:39],v[208:211],v[108:111]\nv_cvt_pk_bf16_f32 v163,v163,v171\nv_cvt_pk_bf16_f32 v178,v178,v186\ns_waitcnt lgkmcnt(6)\nM v[120:123],v[32:35],v[212:215],v[120:123]\n" \
"v_min_u32 v168,-2,v130\nv_min_u32 v169,-2,v146\nv_min_u32 v170,-2,v162\nv_min_u32 v171,-2,v178\nbuffer_store_dwordx4 v[168:171],v253,s[52:55],s33 offen offset:2048 sc1\nM v[124:127],v[36:39],v[212:215],v[124:127]\nds_read_b64_tr_b16 v[196:197],v227 offset:25600\nds_read_b64_tr_b16 v[198:199],v227 offset:29696\n" \
"M v[8:11],v[48:51],v[200:203],v[8:11]\nbuffer_load_dwordx4 a[120:123],v240,s[52:55],s65 offen offset:2048 sc1\nM v[12:15],v[52:55],v[200:203],v[12:15]\nds_read_b64_tr_b16 v[208:209],v226 offset:26112\nds_read_b64_tr_b16 v[210:211],v226 offset:30208\nM v[24:27],v[48:51],v[204:207],v[24:27]\nds_read_b64_tr_b16 v[212:213],v227 offset:26112\nds_read_b64_tr_b16 v[214:215],v227 offset:30208\n" \
"M v[28:31],v[52:55],v[204:207],v[28:31]\nv_add_u32 v226,0,v242\ns_waitcnt lgkmcnt(6)\nM v[40:43],v[48:51],v[216:219],v[40:43]\nbuffer_load_dwordx4 a[124:127],v240,s[52:55],s65 offen offset:3072 sc1\nM v[44:47],v[52:55],v[216:219],v[44:47]\nds_read_b64_tr_b16 v[200:201],v226 offset:0\nds_read_b64_tr_b16 v[202:203],v226 offset:4096\n" \
"M v[56:59],v[48:51],v[220:223],v[56:59]\nv_xor_b32 v227,32,v226\nds_read_b64_tr_b16 v[204:205],v227 offset:0\nM v[60:63],v[52:55],v[220:223],v[60:63]\nds_read_b64_tr_b16 v[206:207],v227 offset:4096\ns_add_u32 s65,s14,s67\nM v[72:75],v[48:51],v[192:195],v[72:75]\nbuffer_load_dwordx4 a[128:131],v240,s[52:55],s65 offen offset:0 sc1\n" \
"M v[76:79],v[52:55],v[192:195],v[76:79]\nds_read_b64_tr_b16 v[216:217],v226 offset:512\nds_read_b64_tr_b16 v[218:219],v226 offset:4608\ns_waitcnt lgkmcnt(6)\nM v[88:91],v[48:51],v[196:199],v[88:91]\nds_read_b64_tr_b16 v[220:221],v227 offset:512\nds_read_b64_tr_b16 v[222:223],v227 offset:4608\nM v[92:95],v[52:55],v[196:199],v[92:95]\n" \
"v_cvt_pk_bf16_f32 v179,v179,v187\nds_read_b64_tr_b16 v[192:193],v226 offset:1024\nM v[104:107],v[48:51],v[208:211],v[104:107]\nbuffer_load_dwordx4 a[132:135],v240,s[52:55],s65 offen offset:1024 sc1\nM v[108:111],v[52:55],v[208:211],v[108:111]\nds_read_b64_tr_b16 v[194:195],v226 offset:5120\nv_min_u32 v184,-2,v131\nM v[120:123],v[48:51],v[212:215],v[120:123]\n" \
"v_min_u32 v185,-2,v147\nv_min_u32 v186,-2,v163\nv_min_u32 v187,-2,v179\nbuffer_store_dwordx4 v[184:187],v253,s[52:55],s33 offen offset:3072 sc1\nM v[124:127],v[52:55],v[212:215],v[124:127]\nds_read_b64_tr_b16 v[196:197],v227 offset:1024\nds_read_b64_tr_b16 v[198:199],v227 offset:5120\ns_waitcnt vmcnt(18) lgkmcnt(6)\n" \
"M v[8:11],a[64:67],v[200:203],v[8:11]\nbuffer_load_dwordx4 a[136:139],v240,s[52:55],s65 offen offset:2048 sc1\nM v[12:15],a[68:71],v[200:203],v[12:15]\nds_read_b64_tr_b16 v[208:209],v226 offset:1536\nds_read_b64_tr_b16 v[210:211],v226 offset:5632\nM v[24:27],a[64:67],v[204:207],v[24:27]\nds_read_b64_tr_b16 v[212:213],v227 offset:1536\nds_read_b64_tr_b16 v[214:215],v227 offset:5632\n" \
"M v[28:31],a[68:71],v[204:207],v[28:31]\nds_read_b64_tr_b16 v[200:201],v226 offset:8192\nds_read_b64_tr_b16 v[202:203],v226 offset:12288\nM v[40:43],a[64:67],v[216:219],v[40:43]\nbuffer_load_dwordx4 a[140:143],v240,s[52:55],s65 offen offset:3072 sc1\nM v[44:47],a[68:71],v[216:219],v[44:47]\nds_read_b64_tr_b16 v[204:205],v227 offset:8192\ns_waitcnt lgkmcnt(9)\n" \
"M v[56:59],a[64:67],v[220:223],v[56:59]\nds_read_b64_tr_b16 v[206:207],v227 offset:12288\ns_add_u32 s65,s65,4096\nM v[60:63],a[68:71],v[220:223],v[60:63]\nds_read_b64_tr_b16 v[216:217],v226 offset:8704\nds_read_b64_tr_b16 v[218:219],v226 offset:12800\nM v[72:75],a[64:67],v[192:195],v[72:75]\nbuffer_load_dwordx4 a[144:147],v240,s[52:55],s65 offen offset:0 sc1\n" \
"M v[76:79],a[68:71],v[192:195],v[76:79]\nds_read_b64_tr_b16 v[220:221],v227 offset:8704\ns_waitcnt lgkmcnt(8)\nM v[88:91],a[64:67],v[196:199],v[88:91]\nds_read_b64_tr_b16 v[222:223],v227 offset:12800\nv_cvt_pk_bf16_f32 v132,v132,v140\nM v[92:95],a[68:71],v[196:199],v[92:95]\nv_cvt_pk_bf16_f32 v133,v133,v141\n" \
"v_cvt_pk_bf16_f32 v134,v134,v142\nM v[104:107],a[64:67],v[208:211],v[104:107]\nbuffer_load_dwordx4 a[148:151],v240,s[52:55],s65 offen offset:1024 sc1\nM v[108:111],a[68:71],v[208:211],v[108:111]\nv_cvt_pk_bf16_f32 v135,v135,v143\nv_cvt_pk_bf16_f32 v148,v148,v156\nv_cvt_pk_bf16_f32 v164,v164,v172\nv_cvt_pk_bf16_f32 v180,v180,v188\n" \
"ds_read_b64_tr_b16 v[192:193],v226 offset:9216\nds_read_b64_tr_b16 v[194:195],v226 offset:13312\ns_waitcnt lgkmcnt(4)\nM v[120:123],a[64:67],v[212:215],v[120:123]\nv_min_u32 v140,-2,v132\nv_min_u32 v141,-2,v148\nv_min_u32 v142,-2,v164\nv_min_u32 v143,-2,v180\n" \
"buffer_store_dwordx4 v[140:143],v253,s[52:55],s38 offen offset:0 sc1\nM v[124:127],a[68:71],v[212:215],v[124:127]\nds_read_b64_tr_b16 v[196:197],v227 offset:9216\nds_read_b64_tr_b16 v[198:199],v227 offset:13312\nM v[8:11],a[72:75],v[200:203],v[8:11]\nbuffer_load_dwordx4 a[152:155],v240,s[52:55],s65 offen offset:2048 sc1\nM v[12:15],a[76:79],v[200:203],v[12:15]\nds_read_b64_tr_b16 v[208:209],v226 offset:9728\n" \
"ds_read_b64_tr_b16 v[210:211],v226 offset:13824\nM v[24:27],a[72:75],v[204:207],v[24:27]\nds_read_b64_tr_b16 v[212:213],v227 offset:9728\nds_read_b64_tr_b16 v[214:215],v227 offset:13824\nM v[28:31],a[76:79],v[204:207],v[28:31]\nds_read_b64_tr_b16 v[200:201],v226 offset:32768\nds_read_b64_tr_b16 v[202:203],v226 offset:36864\nM v[40:43],a[72:75],v[216:219],v[40:43]\n" \
"buffer_load_dwordx4 a[156:159],v240,s[52:55],s65 offen offset:3072 sc1\nM v[44:47],a[76:79],v[216:219],v[44:47]\nds_read_b64_tr_b16 v[204:205],v227 offset:32768\ns_waitcnt lgkmcnt(9)\nM v[56:59],a[72:75],v[220:223],v[56:59]\nds_read_b64_tr_b16 v[206:207],v227 offset:36864\nds_read_b64_tr_b16 v[216:217],v226 offset:33280\nM v[60:63],a[76:79],v[220:223],v[60:63]\n" \
"ds_read_b64_tr_b16 v[218:219],v226 offset:37376\nds_read_b64_tr_b16 v[220:221],v227 offset:33280\nM v[72:75],a[72:75],v[192:195],v[72:75]\nds_read_b64_tr_b16 v[222:223],v227 offset:37376\nv_cvt_pk_bf16_f32 v149,v149,v157\nM v[76:79],a[76:79],v[192:195],v[76:79]\nv_cvt_pk_bf16_f32 v150,v150,v158\ns_waitcnt lgkmcnt(9)\n" \
"M v[88:91],a[72:75],v[196:199],v[88:91]\nv_cvt_pk_bf16_f32 v151,v151,v159\nv_cvt_pk_bf16_f32 v165,v165,v173\nM v[92:95],a[76:79],v[196:199],v[92:95]\nv_cvt_pk_bf16_f32 v181,v181,v189\nds_read_b64_tr_b16 v[192:193],v226 offset:33792\nM v[104:107],a[72:75],v[208:211],v[104:107]\nds_read_b64_tr_b16 v[194:195],v226 offset:37888\n" \
"v_min_u32 v156,-2,v133\nM v[108:111],a[76:79],v[208:211],v[108:111]\nv_min_u32 v157,-2,v149\ns_waitcnt lgkmcnt(6)\nM v[120:123],a[72:75],v[212:215],v[120:123]\nv_min_u32 v158,-2,v165\nv_min_u32 v159,-2,v181\nbuffer_store_dwordx4 v[156:159],v253,s[52:55],s38 offen offset:1024 sc1\n" \
"M v[124:127],a[76:79],v[212:215],v[124:127]\nds_read_b64_tr_b16 v[196:197],v227 offset:33792\nds_read_b64_tr_b16 v[198:199],v227 offset:37888\ns_waitcnt vmcnt(24)\nM v[8:11],a[80:83],v[200:203],v[8:11]\nds_read_b64_tr_b16 v[208:209],v226 offset:34304\nds_read_b64_tr_b16 v[210:211],v226 offset:38400\nM v[12:15],a[84:87],v[200:203],v[12:15]\n" \
"ds_read_b64_tr_b16 v[212:213],v227 offset:34304\nds_read_b64_tr_b16 v[214:215],v227 offset:38400\nM v[24:27],a[80:83],v[204:207],v[24:27]\nds_read_b64_tr_b16 v[200:201],v226 offset:40960\nds_read_b64_tr_b16 v[202:203],v226 offset:45056\nM v[28:31],a[84:87],v[204:207],v[28:31]\nds_read_b64_tr_b16 v[204:205],v227 offset:40960\ns_waitcnt lgkmcnt(9)\n" \
"M v[40:43],a[80:83],v[216:219],v[40:43]\nds_read_b64_tr_b16 v[206:207],v227 offset:45056\nv_cvt_pk_bf16_f32 v166,v166,v174\nM v[44:47],a[84:87],v[216:219],v[44:47]\nds_read_b64_tr_b16 v[216:217],v226 offset:41472\nds_read_b64_tr_b16 v[218:219],v226 offset:45568\nM v[56:59],a[80:83],v[220:223],v[56:59]\nv_cvt_pk_bf16_f32 v167,v167,v175\n" \
"v_cvt_pk_bf16_f32 v182,v182,v190\nM v[60:63],a[84:87],v[220:223],v[60:63]\nds_read_b64_tr_b16 v[220:221],v227 offset:41472\nds_read_b64_tr_b16 v[222:223],v227 offset:45568\nM v[72:75],a[80:83],v[192:195],v[72:75]\nv_min_u32 v172,-2,v134\nv_min_u32 v173,-2,v150\nM v[76:79],a[84:87],v[192:195],v[76:79]\n" \
"ds_read_b64_tr_b16 v[192:193],v226 offset:41984\ns_waitcnt lgkmcnt(8)\nM v[88:91],a[80:83],v[196:199],v[88:91]\nds_read_b64_tr_b16 v[194:195],v226 offset:46080\nv_min_u32 v174,-2,v166\nM v[92:95],a[84:87],v[196:199],v[92:95]\nv_min_u32 v175,-2,v182\nds_read_b64_tr_b16 v[196:197],v227 offset:41984\n" \
"M v[104:107],a[80:83],v[208:211],v[104:107]\nds_read_b64_tr_b16 v[198:199],v227 offset:46080\nv_cvt_pk_bf16_f32 v183,v183,v191\nM v[108:111],a[84:87],v[208:211],v[108:111]\nds_read_b64_tr_b16 v[208:209],v226 offset:42496\nds_read_b64_tr_b16 v[210:211],v226 offset:46592\nM v[120:123],a[80:83],v[212:215],v[120:123]\nbuffer_store_dwordx4 v[172:175],v253,s[52:55],s38 offen offset:2048 sc1\n" \
"M v[124:127],a[84:87],v[212:215],v[124:127]\nds_read_b64_tr_b16 v[212:213],v227 offset:42496\ns_waitcnt vmcnt(20) lgkmcnt(7)\nM v[8:11],a[88:91],v[200:203],v[8:11]\nds_read_b64_tr_b16 v[214:215],v227 offset:46592\nv_min_u32 v188,-2,v135\nM v[12:15],a[92:95],v[200:203],v[12:15]\nds_read_b64_tr_b16 v[200:201],v226 offset:16384\n" \
"ds_read_b64_tr_b16 v[202:203],v226 offset:20480\nM v[24:27],a[88:91],v[204:207],v[24:27]\nv_min_u32 v189,-2,v151\nv_min_u32 v190,-2,v167\nM v[28:31],a[92:95],v[204:207],v[28:31]\nds_read_b64_tr_b16 v[204:205],v227 offset:16384\nds_read_b64_tr_b16 v[206:207],v227 offset:20480\nM v[40:43],a[88:91],v[216:219],v[40:43]\n" \
"v_min_u32 v191,-2,v183\nv_cmp_eq_u32 vcc,v234,v235\nM v[44:47],a[92:95],v[216:219],v[44:47]\nds_read_b64_tr_b16 v[216:217],v226 offset:16896\nds_read_b64_tr_b16 v[218:219],v226 offset:20992\nM v[56:59],a[88:91],v[220:223],v[56:59]\nv_and_b32 v234,1,v249\nv_lshlrev_b32 v234,4,v234\n" \
"M v[60:63],a[92:95],v[220:223],v[60:63]\nds_read_b64_tr_b16 v[220:221],v227 offset:16896\ns_waitcnt lgkmcnt(7)\nM v[72:75],a[88:91],v[192:195],v[72:75]\nds_read_b64_tr_b16 v[222:223],v227 offset:20992\nv_mov_b32 v235,0x3f80\nM v[76:79],a[92:95],v[192:195],v[76:79]\nds_read_b64_tr_b16 v[192:193],v226 offset:17408\n" \
"ds_read_b64_tr_b16 v[194:195],v226 offset:21504\nM v[88:91],a[88:91],v[196:199],v[88:91]\nv_lshlrev_b32 v234,v234,v235\nv_cndmask_b32 v234,0,v234,vcc\nM v[92:95],a[92:95],v[196:199],v[92:95]\nds_read_b64_tr_b16 v[196:197],v227 offset:17408\nds_read_b64_tr_b16 v[198:199],v227 offset:21504\nM v[104:107],a[88:91],v[208:211],v[104:107]\n" \
"v_and_b32 v235,2,v249\nv_cmp_eq_u32 vcc,0,v235\nM v[108:111],a[92:95],v[208:211],v[108:111]\nds_read_b64_tr_b16 v[208:209],v226 offset:17920\nds_read_b64_tr_b16 v[210:211],v226 offset:22016\nM v[120:123],a[88:91],v[212:215],v[120:123]\nbuffer_store_dwordx4 v[188:191],v253,s[52:55],s38 offen offset:3072 sc1\nM v[124:127],a[92:95],v[212:215],v[124:127]\n" \
"ds_read_b64_tr_b16 v[212:213],v227 offset:17920\ns_waitcnt lgkmcnt(7)\nM v[8:11],a[96:99],v[200:203],v[8:11]\nds_read_b64_tr_b16 v[214:215],v227 offset:22016\nv_cndmask_b32 v235,0,v234,vcc\nM v[12:15],a[100:103],v[200:203],v[12:15]\nds_read_b64_tr_b16 v[200:201],v226 offset:24576\nds_read_b64_tr_b16 v[202:203],v226 offset:28672\n" \
"M v[24:27],a[96:99],v[204:207],v[24:27]\nv_xor_b32 v234,v234,v235\ns_add_u32 s38,s78,16384\nM v[28:31],a[100:103],v[204:207],v[28:31]\nds_read_b64_tr_b16 v[204:205],v227 offset:24576\nds_read_b64_tr_b16 v[206:207],v227 offset:28672\nM v[40:43],a[96:99],v[216:219],v[40:43]\nv_lshlrev_b32 v64,16,v64\n" \
"v_lshlrev_b32 v65,16,v65\nM v[44:47],a[100:103],v[216:219],v[44:47]\nds_read_b64_tr_b16 v[216:217],v226 offset:25088\nds_read_b64_tr_b16 v[218:219],v226 offset:29184\nM v[56:59],a[96:99],v[220:223],v[56:59]\nbuffer_load_dwordx4 v[0:3],v238,s[52:55],0 offen offset:0 sc1\nM v[60:63],a[100:103],v[220:223],v[60:63]\nds_read_b64_tr_b16 v[220:221],v227 offset:25088\n" \
"s_waitcnt lgkmcnt(7)\nM v[72:75],a[96:99],v[192:195],v[72:75]\nds_read_b64_tr_b16 v[222:223],v227 offset:29184\nv_lshlrev_b32 v66,16,v66\nM v[76:79],a[100:103],v[192:195],v[76:79]\nds_read_b64_tr_b16 v[192:193],v226 offset:25600\nds_read_b64_tr_b16 v[194:195],v226 offset:29696\nM v[88:91],a[96:99],v[196:199],v[88:91]\n" \
"v_lshlrev_b32 v67,16,v67\nv_lshlrev_b32 v68,16,v68\nM v[92:95],a[100:103],v[196:199],v[92:95]\nds_read_b64_tr_b16 v[196:197],v227 offset:25600\nds_read_b64_tr_b16 v[198:199],v227 offset:29696\nM v[104:107],a[96:99],v[208:211],v[104:107]\nbuffer_load_dwordx4 v[4:7],v238,s[52:55],0 offen offset:16 sc1\nM v[108:111],a[100:103],v[208:211],v[108:111]\n" \
"ds_read_b64_tr_b16 v[208:209],v226 offset:26112\nds_read_b64_tr_b16 v[210:211],v226 offset:30208\nM v[120:123],a[96:99],v[212:215],v[120:123]\nv_lshlrev_b32 v69,16,v69\nv_lshlrev_b32 v70,16,v70\nM v[124:127],a[100:103],v[212:215],v[124:127]\nds_read_b64_tr_b16 v[212:213],v227 offset:26112\ns_waitcnt vmcnt(18) lgkmcnt(7)\n" \
"M v[8:11],a[104:107],v[200:203],v[8:11]\nds_read_b64_tr_b16 v[214:215],v227 offset:30208\nv_lshlrev_b32 v71,16,v71\nM v[12:15],a[108:111],v[200:203],v[12:15]\nds_read_b64_tr_b16 v[200:201],v226 offset:49152\nds_read_b64_tr_b16 v[202:203],v226 offset:53248\nM v[24:27],a[104:107],v[204:207],v[24:27]\nbuffer_load_dwordx4 v[16:19],v238,s[52:55],0 offen offset:2048 sc1\n" \
"M v[28:31],a[108:111],v[204:207],v[28:31]\nds_read_b64_tr_b16 v[204:205],v227 offset:49152\nds_read_b64_tr_b16 v[206:207],v227 offset:53248\nM v[40:43],a[104:107],v[216:219],v[40:43]\nv_lshlrev_b32 v80,16,v80\nv_lshlrev_b32 v81,16,v81\nM v[44:47],a[108:111],v[216:219],v[44:47]\nds_read_b64_tr_b16 v[216:217],v226 offset:49664\n" \
"ds_read_b64_tr_b16 v[218:219],v226 offset:53760\nM v[56:59],a[104:107],v[220:223],v[56:59]\nv_lshlrev_b32 v82,16,v82\nv_lshlrev_b32 v83,16,v83\nM v[60:63],a[108:111],v[220:223],v[60:63]\nds_read_b64_tr_b16 v[220:221],v227 offset:49664\ns_waitcnt lgkmcnt(7)\nM v[72:75],a[104:107],v[192:195],v[72:75]\n" \
"buffer_load_dwordx4 v[20:23],v238,s[52:55],0 offen offset:2064 sc1\nM v[76:79],a[108:111],v[192:195],v[76:79]\nds_read_b64_tr_b16 v[222:223],v227 offset:53760\nds_read_b64_tr_b16 v[192:193],v226 offset:50176\nM v[88:91],a[104:107],v[196:199],v[88:91]\nds_read_b64_tr_b16 v[194:195],v226 offset:54272\nv_lshlrev_b32 v84,16,v84\nM v[92:95],a[108:111],v[196:199],v[92:95]\n" \
"ds_read_b64_tr_b16 v[196:197],v227 offset:50176\nds_read_b64_tr_b16 v[198:199],v227 offset:54272\nM v[104:107],a[104:107],v[208:211],v[104:107]\nv_lshlrev_b32 v85,16,v85\nv_lshlrev_b32 v86,16,v86\nM v[108:111],a[108:111],v[208:211],v[108:111]\nds_read_b64_tr_b16 v[208:209],v226 offset:50688\nds_read_b64_tr_b16 v[210:211],v226 offset:54784\n" \
"M v[120:123],a[104:107],v[212:215],v[120:123]\nbuffer_load_dwordx4 v[32:35],v238,s[52:55],s97 offen offset:0 sc1\nM v[124:127],a[108:111],v[212:215],v[124:127]\nds_read_b64_tr_b16 v[212:213],v227 offset:50688\ns_waitcnt lgkmcnt(8)\nM v[8:11],a[112:115],v[200:203],v[8:11]\nds_read_b64_tr_b16 v[214:215],v227 offset:54784\nv_lshlrev_b32 v87,16,v87\n" \
"M v[12:15],a[116:119],v[200:203],v[12:15]\nds_read_b64_tr_b16 v[200:201],v226 offset:57344\nds_read_b64_tr_b16 v[202:203],v226 offset:61440\nM v[24:27],a[112:115],v[204:207],v[24:27]\nv_lshlrev_b32 v96,16,v96\nv_lshlrev_b32 v97,16,v97\nM v[28:31],a[116:119],v[204:207],v[28:31]\nds_read_b64_tr_b16 v[204:205],v227 offset:57344\n" \
"ds_read_b64_tr_b16 v[206:207],v227 offset:61440\nM v[40:43],a[112:115],v[216:219],v[40:43]\nbuffer_load_dwordx4 v[36:39],v238,s[52:55],s97 offen offset:16 sc1\nM v[44:47],a[116:119],v[216:219],v[44:47]\nds_read_b64_tr_b16 v[216:217],v226 offset:57856\ns_waitcnt lgkmcnt(7)\nM v[56:59],a[112:115],v[220:223],v[56:59]\nds_read_b64_tr_b16 v[218:219],v226 offset:61952\n" \
"v_lshlrev_b32 v98,16,v98\nM v[60:63],a[116:119],v[220:223],v[60:63]\nds_read_b64_tr_b16 v[220:221],v227 offset:57856\nds_read_b64_tr_b16 v[222:223],v227 offset:61952\nM v[72:75],a[112:115],v[192:195],v[72:75]\nv_lshlrev_b32 v99,16,v99\nv_lshlrev_b32 v100,16,v100\nM v[76:79],a[116:119],v[192:195],v[76:79]\n" \
"ds_read_b64_tr_b16 v[192:193],v226 offset:58368\nds_read_b64_tr_b16 v[194:195],v226 offset:62464\nM v[88:91],a[112:115],v[196:199],v[88:91]\nbuffer_load_dwordx4 v[48:51],v238,s[52:55],s97 offen offset:2048 sc1\nM v[92:95],a[116:119],v[196:199],v[92:95]\nds_read_b64_tr_b16 v[196:197],v227 offset:58368\nds_read_b64_tr_b16 v[198:199],v227 offset:62464\nM v[104:107],a[112:115],v[208:211],v[104:107]\n" \
"v_lshlrev_b32 v101,16,v101\nv_lshlrev_b32 v102,16,v102\nM v[108:111],a[116:119],v[208:211],v[108:111]\nds_read_b64_tr_b16 v[208:209],v226 offset:58880\ns_waitcnt lgkmcnt(7)\nM v[120:123],a[112:115],v[212:215],v[120:123]\nds_read_b64_tr_b16 v[210:211],v226 offset:62976\nv_add_u32 v226,65536,v242\n" \
"M v[124:127],a[116:119],v[212:215],v[124:127]\nds_read_b64_tr_b16 v[212:213],v227 offset:58880\ns_waitcnt vmcnt(18)\nM v[8:11],a[120:123],v[200:203],v[8:11]\nbuffer_load_dwordx4 v[52:55],v238,s[52:55],s97 offen offset:2064 sc1\nM v[12:15],a[124:127],v[200:203],v[12:15]\nds_read_b64_tr_b16 v[214:215],v227 offset:62976\nds_read_b64_tr_b16 v[200:201],v226 offset:0\n" \
"M v[24:27],a[120:123],v[204:207],v[24:27]\nds_read_b64_tr_b16 v[202:203],v226 offset:4096\nv_xor_b32 v227,32,v226\nM v[28:31],a[124:127],v[204:207],v[28:31]\nds_read_b64_tr_b16 v[204:205],v227 offset:0\nds_read_b64_tr_b16 v[206:207],v227 offset:4096\nM v[40:43],a[120:123],v[216:219],v[40:43]\nv_lshlrev_b32 v103,16,v103\n" \
"v_accvgpr_write_b32 a192,v235\nM v[44:47],a[124:127],v[216:219],v[44:47]\nds_read_b64_tr_b16 v[216:217],v226 offset:512\ns_waitcnt lgkmcnt(7)\nM v[56:59],a[120:123],v[220:223],v[56:59]\nds_read_b64_tr_b16 v[218:219],v226 offset:4608\nv_accvgpr_write_b32 a193,v234\nM v[60:63],a[124:127],v[220:223],v[60:63]\n" \
"ds_read_b64_tr_b16 v[220:221],v227 offset:512\nds_read_b64_tr_b16 v[222:223],v227 offset:4608\nM v[72:75],a[120:123],v[192:195],v[72:75]\nv_accvgpr_write_b32 a198,v235\nv_accvgpr_write_b32 a199,v234\nM v[76:79],a[124:127],v[192:195],v[76:79]\nds_read_b64_tr_b16 v[192:193],v226 offset:1024\nds_read_b64_tr_b16 v[194:195],v226 offset:5120\n" \
"M v[88:91],a[120:123],v[196:199],v[88:91]\nM v[92:95],a[124:127],v[196:199],v[92:95]\nds_read_b64_tr_b16 v[196:197],v227 offset:1024\nds_read_b64_tr_b16 v[198:199],v227 offset:5120\nM v[104:107],a[120:123],v[208:211],v[104:107]\nM v[108:111],a[124:127],v[208:211],v[108:111]\nds_read_b64_tr_b16 v[208:209],v226 offset:1536\ns_waitcnt lgkmcnt(8)\n" \
"M v[120:123],a[120:123],v[212:215],v[120:123]\nds_read_b64_tr_b16 v[210:211],v226 offset:5632\nM v[124:127],a[124:127],v[212:215],v[124:127]\nds_read_b64_tr_b16 v[212:213],v227 offset:1536\nds_read_b64_tr_b16 v[214:215],v227 offset:5632\nM v[8:11],a[128:131],v[200:203],v[8:11]\nM v[12:15],a[132:135],v[200:203],v[12:15]\nds_read_b64_tr_b16 v[200:201],v226 offset:8192\n" \
"ds_read_b64_tr_b16 v[202:203],v226 offset:12288\nM v[24:27],a[128:131],v[204:207],v[24:27]\nM v[28:31],a[132:135],v[204:207],v[28:31]\nds_read_b64_tr_b16 v[204:205],v227 offset:8192\ns_waitcnt lgkmcnt(9)\nM v[40:43],a[128:131],v[216:219],v[40:43]\nds_read_b64_tr_b16 v[206:207],v227 offset:12288\nM v[44:47],a[132:135],v[216:219],v[44:47]\n" \
"ds_read_b64_tr_b16 v[216:217],v226 offset:8704\nds_read_b64_tr_b16 v[218:219],v226 offset:12800\nM v[56:59],a[128:131],v[220:223],v[56:59]\nM v[60:63],a[132:135],v[220:223],v[60:63]\nds_read_b64_tr_b16 v[220:221],v227 offset:8704\nds_read_b64_tr_b16 v[222:223],v227 offset:12800\nM v[72:75],a[128:131],v[192:195],v[72:75]\nM v[76:79],a[132:135],v[192:195],v[76:79]\n" \
"ds_read_b64_tr_b16 v[192:193],v226 offset:9216\ns_waitcnt lgkmcnt(9)\nM v[88:91],a[128:131],v[196:199],v[88:91]\nds_read_b64_tr_b16 v[194:195],v226 offset:13312\nM v[92:95],a[132:135],v[196:199],v[92:95]\nds_read_b64_tr_b16 v[196:197],v227 offset:9216\nds_read_b64_tr_b16 v[198:199],v227 offset:13312\nM v[104:107],a[128:131],v[208:211],v[104:107]\n" \
"M v[108:111],a[132:135],v[208:211],v[108:111]\nds_read_b64_tr_b16 v[208:209],v226 offset:9728\nds_read_b64_tr_b16 v[210:211],v226 offset:13824\nM v[120:123],a[128:131],v[212:215],v[120:123]\nM v[124:127],a[132:135],v[212:215],v[124:127]\nds_read_b64_tr_b16 v[212:213],v227 offset:9728\ns_waitcnt vmcnt(14) lgkmcnt(9)\nM v[8:11],a[136:139],v[200:203],v[8:11]\n" \
"ds_read_b64_tr_b16 v[214:215],v227 offset:13824\nM v[12:15],a[140:143],v[200:203],v[12:15]\nds_read_b64_tr_b16 v[200:201],v226 offset:32768\nds_read_b64_tr_b16 v[202:203],v226 offset:36864\nM v[24:27],a[136:139],v[204:207],v[24:27]\nM v[28:31],a[140:143],v[204:207],v[28:31]\nds_read_b64_tr_b16 v[204:205],v227 offset:32768\nds_read_b64_tr_b16 v[206:207],v227 offset:36864\n" \
"M v[40:43],a[136:139],v[216:219],v[40:43]\nM v[44:47],a[140:143],v[216:219],v[44:47]\nds_read_b64_tr_b16 v[216:217],v226 offset:33280\ns_waitcnt lgkmcnt(9)\nM v[56:59],a[136:139],v[220:223],v[56:59]\nds_read_b64_tr_b16 v[218:219],v226 offset:37376\nM v[60:63],a[140:143],v[220:223],v[60:63]\nds_read_b64_tr_b16 v[220:221],v227 offset:33280\n" \
"ds_read_b64_tr_b16 v[222:223],v227 offset:37376\nM v[72:75],a[136:139],v[192:195],v[72:75]\nM v[76:79],a[140:143],v[192:195],v[76:79]\nds_read_b64_tr_b16 v[192:193],v226 offset:33792\nds_read_b64_tr_b16 v[194:195],v226 offset:37888\nM v[88:91],a[136:139],v[196:199],v[88:91]\nM v[92:95],a[140:143],v[196:199],v[92:95]\nds_read_b64_tr_b16 v[196:197],v227 offset:33792\n" \
"s_waitcnt lgkmcnt(9)\nM v[104:107],a[136:139],v[208:211],v[104:107]\nds_read_b64_tr_b16 v[198:199],v227 offset:37888\nM v[108:111],a[140:143],v[208:211],v[108:111]\nds_read_b64_tr_b16 v[208:209],v226 offset:34304\nds_read_b64_tr_b16 v[210:211],v226 offset:38400\nM v[120:123],a[136:139],v[212:215],v[120:123]\nM v[124:127],a[140:143],v[212:215],v[124:127]\n" \
"ds_read_b64_tr_b16 v[212:213],v227 offset:34304\nds_read_b64_tr_b16 v[214:215],v227 offset:38400\nM v[8:11],a[144:147],v[200:203],v[8:11]\nM v[12:15],a[148:151],v[200:203],v[12:15]\nds_read_b64_tr_b16 v[200:201],v226 offset:40960\ns_waitcnt lgkmcnt(9)\nM v[24:27],a[144:147],v[204:207],v[24:27]\nds_read_b64_tr_b16 v[202:203],v226 offset:45056\n" \
"M v[28:31],a[148:151],v[204:207],v[28:31]\nds_read_b64_tr_b16 v[204:205],v227 offset:40960\nds_read_b64_tr_b16 v[206:207],v227 offset:45056\nM v[40:43],a[144:147],v[216:219],v[40:43]\nM v[44:47],a[148:151],v[216:219],v[44:47]\nds_read_b64_tr_b16 v[216:217],v226 offset:41472\nds_read_b64_tr_b16 v[218:219],v226 offset:45568\nM v[56:59],a[144:147],v[220:223],v[56:59]\n" \
"M v[60:63],a[148:151],v[220:223],v[60:63]\nds_read_b64_tr_b16 v[220:221],v227 offset:41472\ns_waitcnt lgkmcnt(9)\nM v[72:75],a[144:147],v[192:195],v[72:75]\nds_read_b64_tr_b16 v[222:223],v227 offset:45568\nM v[76:79],a[148:151],v[192:195],v[76:79]\nds_read_b64_tr_b16 v[192:193],v226 offset:41984\nds_read_b64_tr_b16 v[194:195],v226 offset:46080\n" \
"M v[88:91],a[144:147],v[196:199],v[88:91]\nM v[92:95],a[148:151],v[196:199],v[92:95]\nds_read_b64_tr_b16 v[196:197],v227 offset:41984\nds_read_b64_tr_b16 v[198:199],v227 offset:46080\nM v[104:107],a[144:147],v[208:211],v[104:107]\nM v[108:111],a[148:151],v[208:211],v[108:111]\nds_read_b64_tr_b16 v[208:209],v226 offset:42496\ns_waitcnt lgkmcnt(9)\n" \
"M v[120:123],a[144:147],v[212:215],v[120:123]\nds_read_b64_tr_b16 v[210:211],v226 offset:46592\nM v[124:127],a[148:151],v[212:215],v[124:127]\nds_read_b64_tr_b16 v[212:213],v227 offset:42496\ns_waitcnt vmcnt(11)\nM v[8:11],a[152:155],v[200:203],v[8:11]\nds_read_b64_tr_b16 v[214:215],v227 offset:46592\nM v[12:15],a[156:159],v[200:203],v[12:15]\n" \
"M v[24:27],a[152:155],v[204:207],v[24:27]\nM v[28:31],a[156:159],v[204:207],v[28:31]\ns_waitcnt lgkmcnt(6)\nM v[40:43],a[152:155],v[216:219],v[40:43]\nM v[44:47],a[156:159],v[216:219],v[44:47]\nM v[56:59],a[152:155],v[220:223],v[56:59]\nM v[60:63],a[156:159],v[220:223],v[60:63]\nM v[72:75],a[152:155],v[192:195],v[72:75]\n" \
"M v[76:79],a[156:159],v[192:195],v[76:79]\nv_cvt_pk_bf16_f32 v8,v8,v24\ns_waitcnt lgkmcnt(3)\nM v[88:91],a[152:155],v[196:199],v[88:91]\nv_cvt_pk_bf16_f32 v9,v9,v25\nv_cvt_pk_bf16_f32 v10,v10,v26\nM v[92:95],a[156:159],v[196:199],v[92:95]\nv_cvt_pk_bf16_f32 v11,v11,v27\n" \
"s_waitcnt lgkmcnt(0)\nM v[104:107],a[152:155],v[208:211],v[104:107]\nv_cvt_pk_bf16_f32 v40,v40,v56\nv_cvt_pk_bf16_f32 v41,v41,v57\nM v[108:111],a[156:159],v[208:211],v[108:111]\nv_cvt_pk_bf16_f32 v42,v42,v58\nv_cvt_pk_bf16_f32 v43,v43,v59\nM v[120:123],a[152:155],v[212:215],v[120:123]\n" \
"v_cvt_pk_bf16_f32 v72,v72,v88\nv_cvt_pk_bf16_f32 v73,v73,v89\nv_cvt_pk_bf16_f32 v74,v74,v90\nv_cvt_pk_bf16_f32 v75,v75,v91\nv_cvt_pk_bf16_f32 v12,v12,v28\nv_cvt_pk_bf16_f32 v13,v13,v29\nv_cvt_pk_bf16_f32 v14,v14,v30\nv_cvt_pk_bf16_f32 v15,v15,v31\n" \
"v_cvt_pk_bf16_f32 v104,v104,v120\nv_cvt_pk_bf16_f32 v105,v105,v121\nv_cvt_pk_bf16_f32 v106,v106,v122\nv_cvt_pk_bf16_f32 v107,v107,v123\nv_cvt_pk_bf16_f32 v44,v44,v60\nv_cvt_pk_bf16_f32 v45,v45,v61\nv_cvt_pk_bf16_f32 v46,v46,v62\nv_cvt_pk_bf16_f32 v47,v47,v63\n" \
"v_cvt_pk_bf16_f32 v76,v76,v92\nv_cvt_pk_bf16_f32 v77,v77,v93\nv_cvt_pk_bf16_f32 v78,v78,v94\nv_cvt_pk_bf16_f32 v79,v79,v95\nM v[124:127],a[156:159],v[212:215],v[124:127]\nv_min_u32 v24,-2,v8\nv_min_u32 v25,-2,v40\nv_min_u32 v26,-2,v72\n" \
"v_min_u32 v27,-2,v104\nv_min_u32 v56,-2,v9\nv_min_u32 v57,-2,v41\nv_min_u32 v58,-2,v73\nv_min_u32 v59,-2,v105\nv_cvt_pk_bf16_f32 v108,v108,v124\nv_cvt_pk_bf16_f32 v109,v109,v125\nv_cvt_pk_bf16_f32 v110,v110,v126\n" \
"v_cvt_pk_bf16_f32 v111,v111,v127\nv_min_u32 v88,-2,v10\nv_min_u32 v89,-2,v42\nv_min_u32 v90,-2,v74\nv_min_u32 v91,-2,v106\nv_min_u32 v120,-2,v11\nv_min_u32 v121,-2,v43\nv_min_u32 v122,-2,v75\n" \
"v_min_u32 v123,-2,v107\nv_min_u32 v28,-2,v12\nv_min_u32 v29,-2,v44\nv_min_u32 v30,-2,v76\nv_min_u32 v31,-2,v108\nv_min_u32 v60,-2,v13\nv_min_u32 v61,-2,v45\nv_min_u32 v62,-2,v77\n" \
"v_min_u32 v63,-2,v109\nv_min_u32 v92,-2,v14\nv_min_u32 v93,-2,v46\nv_min_u32 v94,-2,v78\nv_min_u32 v95,-2,v110\nv_min_u32 v124,-2,v15\nv_min_u32 v125,-2,v47\nv_min_u32 v126,-2,v79\n" \
"v_min_u32 v127,-2,v111\nbuffer_store_dwordx4 v[24:27],v253,s[52:55],s78 offen offset:0 sc1\nbuffer_store_dwordx4 v[56:59],v253,s[52:55],s78 offen offset:1024 sc1\nbuffer_store_dwordx4 v[88:91],v253,s[52:55],s78 offen offset:2048 sc1\nbuffer_store_dwordx4 v[120:123],v253,s[52:55],s78 offen offset:3072 sc1\nbuffer_store_dwordx4 v[28:31],v253,s[52:55],s38 offen offset:0 sc1\nbuffer_store_dwordx4 v[60:63],v253,s[52:55],s38 offen offset:1024 sc1\nbuffer_store_dwordx4 v[92:95],v253,s[52:55],s38 offen offset:2048 sc1\n" \
"buffer_store_dwordx4 v[124:127],v253,s[52:55],s38 offen offset:3072 sc1\n.Ltail:\ns_add_u32 s72,s95,0\nbuffer_load_dwordx4 v[160:163],v240,s[52:55],s72 offen sc1\ns_add_u32 s72,s95,2097152\nbuffer_load_dwordx4 v[164:167],v240,s[52:55],s72 offen sc1\ns_add_u32 s72,s95,4194304\nbuffer_load_dwordx4 v[168:171],v240,s[52:55],s72 offen sc1\n" \
"s_add_u32 s72,s95,6291456\nbuffer_load_dwordx4 v[172:175],v240,s[52:55],s72 offen sc1\ns_add_u32 s72,s95,8388608\nbuffer_load_dwordx4 v[176:179],v240,s[52:55],s72 offen sc1\ns_add_u32 s72,s95,10485760\nbuffer_load_dwordx4 v[180:183],v240,s[52:55],s72 offen sc1\ns_add_u32 s72,s95,12582912\nbuffer_load_dwordx4 v[184:187],v240,s[52:55],s72 offen sc1\n" \
"s_add_u32 s72,s95,14680064\nbuffer_load_dwordx4 v[188:191],v240,s[52:55],s72 offen sc1\ns_add_u32 s72,s74,0\nbuffer_load_dwordx4 v[128:131],v240,s[52:55],s72 offen sc1\ns_add_u32 s72,s74,2097152\nbuffer_load_dwordx4 v[132:135],v240,s[52:55],s72 offen sc1\ns_add_u32 s72,s74,4194304\nbuffer_load_dwordx4 v[136:139],v240,s[52:55],s72 offen sc1\n" \
"s_add_u32 s72,s74,6291456\nbuffer_load_dwordx4 v[140:143],v240,s[52:55],s72 offen sc1\ns_add_u32 s72,s74,8388608\nbuffer_load_dwordx4 v[144:147],v240,s[52:55],s72 offen sc1\ns_add_u32 s72,s74,10485760\nbuffer_load_dwordx4 v[148:151],v240,s[52:55],s72 offen sc1\ns_add_u32 s72,s74,12582912\nbuffer_load_dwordx4 v[152:155],v240,s[52:55],s72 offen sc1\n" \
"s_add_u32 s72,s74,14680064\nbuffer_load_dwordx4 v[156:159],v240,s[52:55],s72 offen sc1\ns_lshl_b32 s70,s2,9\ns_lshl_b32 s71,s7,8\nv_lshl_add_u32 v241,v249,2,s71\ns_add_u32 s70,s70,4096\nv_add_u32 v244,s70,v241\nv_add_u32 v245,8192,v244\n" \
"global_load_dword a0,v244,s[26:27] offset:-4096\nglobal_load_dword a1,v244,s[26:27] offset:-3072\nglobal_load_dword a2,v244,s[26:27] offset:-2048\nglobal_load_dword a3,v244,s[26:27] offset:-1024\nglobal_load_dword a4,v244,s[26:27] offset:0\nglobal_load_dword a5,v244,s[26:27] offset:1024\nglobal_load_dword a6,v244,s[26:27] offset:2048\nglobal_load_dword a7,v244,s[26:27] offset:3072\n" \
"global_load_dword a8,v245,s[26:27] offset:-4096\nglobal_load_dword a9,v245,s[26:27] offset:-3072\nglobal_load_dword a10,v245,s[26:27] offset:-2048\nglobal_load_dword a11,v245,s[26:27] offset:-1024\nglobal_load_dword a12,v245,s[26:27] offset:0\nglobal_load_dword a13,v245,s[26:27] offset:1024\nglobal_load_dword a14,v245,s[26:27] offset:2048\nglobal_load_dword a15,v245,s[26:27] offset:3072\n" \
"s_lshr_b32 s70,s6,3\ns_and_b32 s70,s70,15\ns_lshl4_add_u32 s98,s60,s70\nv_lshrrev_b32 v242,4,v249\ns_lshl_b32 s71,s98,2\nv_add_u32 v242,s71,v242\nv_and_b32 v244,15,v242\nv_bfe_u32 v245,v242,4,3\n" \
"v_lshl_add_u32 v244,v244,3,v245\nv_and_b32 v245,0x180,v242\nv_add_lshl_u32 v243,v244,v245,1\nv_lshlrev_b32 v244,1,v243\ns_mov_b32 s70,4096\nbuffer_load_dword v62,v244,s[52:55],0 offen offset:0 sc1\nbuffer_load_dword v63,v244,s[52:55],0 offen offset:2048 sc1\nbuffer_load_dword v126,v244,s[52:55],s70 offen offset:0 sc1\n" \
"buffer_load_dword v127,v244,s[52:55],s70 offen offset:2048 sc1\nv_and_b32 v244,15,v249\nv_and_b32 v245,0x300,v240\nv_lshl_or_b32 v244,v244,1,v245\ns_add_u32 s70,s64,82944\nv_add_u32 v244,s70,v244\nv_add_u32 v245,s70,v240\nv_lshlrev_b32 v246,11,v249\n" \
"v_cmp_lt_u32 vcc,7,v249\nv_mov_b32 v247,0x40000000\ns_lshl3_add_u32 s37,s6,s75\nv_cndmask_b32 v246,v246,v247,vcc\ns_add_u32 s37,s37,557056\ns_waitcnt vmcnt(44)\ns_mov_b32 s70,0x05040100\ns_mov_b32 s71,0x07060302\n" \
"v_add_f32 v32,1.0,v32\nv_add_f32 v33,1.0,v33\nv_add_f32 v34,1.0,v34\nv_add_f32 v35,1.0,v35\nv_add_f32 v36,1.0,v36\nv_add_f32 v37,1.0,v37\nv_add_f32 v38,1.0,v38\nv_add_f32 v39,1.0,v39\n" \
"v_add_f32 v116,1.0,v116\nv_add_f32 v117,1.0,v117\nv_cvt_pk_bf16_f32 v32,0,v32\nv_cvt_pk_bf16_f32 v33,0,v33\nv_cvt_pk_bf16_f32 v34,0,v34\nv_cvt_pk_bf16_f32 v35,0,v35\nv_cvt_pk_bf16_f32 v36,0,v36\nv_cvt_pk_bf16_f32 v37,0,v37\n" \
"v_cvt_pk_bf16_f32 v38,0,v38\nv_cvt_pk_bf16_f32 v39,0,v39\nv_cvt_pk_bf16_f32 v116,0,v116\nv_cvt_pk_bf16_f32 v117,0,v117\nv_accvgpr_read_b32 v228,a212\nv_accvgpr_read_b32 v229,a213\nv_accvgpr_read_b32 v230,a214\nv_accvgpr_read_b32 v231,a215\n" \
"v_perm_b32 v192,v230,v228,s70\nv_perm_b32 v193,v230,v228,s71\nv_perm_b32 v194,v231,v229,s70\nv_perm_b32 v195,v231,v229,s71\nv_accvgpr_read_b32 v228,a216\nv_accvgpr_read_b32 v229,a217\nv_accvgpr_read_b32 v230,a218\nv_accvgpr_read_b32 v231,a219\n" \
"v_perm_b32 v196,v230,v228,s70\nv_perm_b32 v197,v230,v228,s71\nv_perm_b32 v198,v231,v229,s70\nv_perm_b32 v199,v231,v229,s71\nv_accvgpr_read_b32 v228,a220\nv_accvgpr_read_b32 v229,a221\nv_accvgpr_read_b32 v230,a222\nv_accvgpr_read_b32 v231,a223\n" \
"v_perm_b32 v200,v230,v228,s70\nv_perm_b32 v201,v230,v228,s71\nv_perm_b32 v202,v231,v229,s70\nv_perm_b32 v203,v231,v229,s71\nv_accvgpr_read_b32 v228,a224\nv_accvgpr_read_b32 v229,a225\nv_accvgpr_read_b32 v230,a226\nv_accvgpr_read_b32 v231,a227\n" \
"v_perm_b32 v204,v230,v228,s70\nv_perm_b32 v205,v230,v228,s71\nv_perm_b32 v206,v231,v229,s70\nv_perm_b32 v207,v231,v229,s71\nv_accvgpr_read_b32 v228,a228\nv_accvgpr_read_b32 v229,a229\nv_accvgpr_read_b32 v230,a230\nv_accvgpr_read_b32 v231,a231\n" \
"v_perm_b32 v208,v230,v228,s70\nv_perm_b32 v209,v230,v228,s71\nv_perm_b32 v210,v231,v229,s70\nv_perm_b32 v211,v231,v229,s71\nv_accvgpr_read_b32 v228,a232\nv_accvgpr_read_b32 v229,a233\nv_accvgpr_read_b32 v230,a234\nv_accvgpr_read_b32 v231,a235\n" \
"v_perm_b32 v212,v230,v228,s70\nv_perm_b32 v213,v230,v228,s71\nv_perm_b32 v214,v231,v229,s70\nv_perm_b32 v215,v231,v229,s71\nv_accvgpr_read_b32 v228,a236\nv_accvgpr_read_b32 v229,a237\nv_accvgpr_read_b32 v230,a238\nv_accvgpr_read_b32 v231,a239\n" \
"v_perm_b32 v216,v230,v228,s70\nv_perm_b32 v217,v230,v228,s71\nv_perm_b32 v218,v231,v229,s70\nv_perm_b32 v219,v231,v229,s71\nv_accvgpr_read_b32 v228,a240\nv_accvgpr_read_b32 v229,a241\nv_accvgpr_read_b32 v230,a242\nv_accvgpr_read_b32 v231,a243\n" \
"v_perm_b32 v220,v230,v228,s70\nv_perm_b32 v221,v230,v228,s71\nv_perm_b32 v222,v231,v229,s70\nv_perm_b32 v223,v231,v229,s71\nv_and_b32 v228,3,v249\nv_lshrrev_b32 v229,2,v249\nv_lshl_add_u32 v229,v228,5,v229\ns_lshl_b32 s70,s7,8\n" \
"v_lshl_add_u32 v248,v229,1,s70\nv_lshlrev_b32 v242,1,v242\nv_lshlrev_b32 v251,5,v249\ns_lshl_b32 s80,s60,11\ns_add_u32 s80,s80,557056\ns_or_b32 s80,s80,s75\nv_lshrrev_b32 v229,4,v249\nv_and_b32 v230,1,v229\n" \
"v_lshrrev_b32 v229,1,v229\nv_lshlrev_b32 v230,5,v230\nv_lshl_or_b32 v230,v228,6,v230\nv_lshl_or_b32 v230,v229,8,v230\ns_lshl2_add_u32 s70,s7,81984\nv_add_u32 v252,s70,v230\nv_and_b32 v230,0xf0,v240\nv_add_u32 v253,81984,v230\n" \
".Lrow1:\ns_waitcnt vmcnt(35)\nM v[72:75],a[192:195],v[160:163],0\nM v[76:79],a[196:199],v[160:163],0\ns_waitcnt vmcnt(34)\nM v[72:75],a[192:195],v[164:167],v[72:75]\nM v[76:79],a[196:199],v[164:167],v[76:79]\ns_waitcnt vmcnt(33)\n" \
"M v[72:75],a[192:195],v[168:171],v[72:75]\nM v[76:79],a[196:199],v[168:171],v[76:79]\ns_waitcnt vmcnt(32)\nM v[72:75],a[192:195],v[172:175],v[72:75]\nM v[76:79],a[196:199],v[172:175],v[76:79]\ns_waitcnt vmcnt(31)\nM v[72:75],a[192:195],v[176:179],v[72:75]\nM v[76:79],a[196:199],v[176:179],v[76:79]\n" \
"s_waitcnt vmcnt(30)\nM v[72:75],a[192:195],v[180:183],v[72:75]\nM v[76:79],a[196:199],v[180:183],v[76:79]\ns_waitcnt vmcnt(29)\nM v[72:75],a[192:195],v[184:187],v[72:75]\nM v[76:79],a[196:199],v[184:187],v[76:79]\ns_waitcnt vmcnt(28)\nM v[72:75],a[192:195],v[188:191],v[72:75]\n" \
"M v[76:79],a[196:199],v[188:191],v[76:79]\ns_sub_u32 s38,56622112,s95\ns_lshr_b32 s38,s38,5\ns_nop 4\nv_cvt_pk_bf16_f32 v72,0,v72\nv_cvt_pk_bf16_f32 v73,0,v73\nv_cvt_pk_bf16_f32 v74,0,v74\nv_cvt_pk_bf16_f32 v75,0,v75\n" \
"v_cvt_pk_bf16_f32 v76,0,v76\nv_cvt_pk_bf16_f32 v77,0,v77\nv_cvt_pk_bf16_f32 v78,0,v78\nv_cvt_pk_bf16_f32 v79,0,v79\nv_pk_mul_f32 v[72:73],v[72:73],v[0:1]\nv_pk_mul_f32 v[74:75],v[74:75],v[2:3]\nv_pk_mul_f32 v[76:77],v[76:77],v[4:5]\nv_pk_mul_f32 v[78:79],v[78:79],v[6:7]\n" \
"v_cvt_pk_bf16_f32 v72,0,v72\nv_cvt_pk_bf16_f32 v73,0,v73\nv_cvt_pk_bf16_f32 v74,0,v74\nv_cvt_pk_bf16_f32 v75,0,v75\nv_cvt_pk_bf16_f32 v76,0,v76\nv_cvt_pk_bf16_f32 v77,0,v77\nv_cvt_pk_bf16_f32 v78,0,v78\nv_cvt_pk_bf16_f32 v79,0,v79\n" \
"v_pk_add_f32 v[72:73],v[72:73],v[64:65]\nv_pk_add_f32 v[74:75],v[74:75],v[66:67]\nv_pk_add_f32 v[76:77],v[76:77],v[68:69]\nv_pk_add_f32 v[78:79],v[78:79],v[70:71]\nv_cvt_pk_bf16_f32 v72,0,v72\nv_cvt_pk_bf16_f32 v73,0,v73\nv_cvt_pk_bf16_f32 v74,0,v74\nv_cvt_pk_bf16_f32 v75,0,v75\n" \
"v_cvt_pk_bf16_f32 v76,0,v76\nv_cvt_pk_bf16_f32 v77,0,v77\nv_cvt_pk_bf16_f32 v78,0,v78\nv_cvt_pk_bf16_f32 v79,0,v79\nv_readfirstlane_b32 s70,v72\ns_nop 1\nv_subrev_f32 v104,s70,v72\nv_subrev_f32 v105,s70,v73\n" \
"v_subrev_f32 v106,s70,v74\nv_subrev_f32 v107,s70,v75\nv_subrev_f32 v108,s70,v76\nv_subrev_f32 v109,s70,v77\nv_subrev_f32 v110,s70,v78\nv_subrev_f32 v111,s70,v79\nv_pk_mul_f32 v[124:125],v[104:105],v[104:105]\nv_pk_add_f32 v[120:121],v[104:105],v[106:107]\n" \
"v_pk_fma_f32 v[124:125],v[106:107],v[106:107],v[124:125]\nv_pk_fma_f32 v[124:125],v[108:109],v[108:109],v[124:125]\nv_pk_fma_f32 v[124:125],v[110:111],v[110:111],v[124:125]\nv_pk_add_f32 v[108:109],v[108:109],v[110:111]\nv_pk_add_f32 v[120:121],v[120:121],v[108:109]\nv_add_f32 v120,v120,v121\nv_add_f32 v121,v124,v125\ns_nop 1\n" \
"v_permlane16_swap_b32 v120,v121\nv_add_f32 v120,v120,v121\nv_mov_b32 v121,v120\ns_nop 1\nv_permlane32_swap_b32 v120,v121\nv_add_f32 v120,v120,v121\ns_nop 1\nv_add_f32_dpp v120,v120,v120 quad_perm:[1,0,3,2] row_mask:0xf bank_mask:0xf\n" \
"s_nop 1\nv_add_f32_dpp v120,v120,v120 quad_perm:[2,3,0,1] row_mask:0xf bank_mask:0xf\ns_nop 1\nv_add_f32_dpp v120,v120,v120 row_half_mirror row_mask:0xf bank_mask:0xf\ns_nop 1\nv_add_f32_dpp v120,v120,v120 row_mirror row_mask:0xf bank_mask:0xf\nv_mul_f32 v120,0x3b000000,v120\nv_mov_b32 v121,v120\n" \
"v_cmp_u_f32 vcc,v120,v120\ns_nop 0\nv_permlane16_swap_b32 v120,v121\ns_cbranch_vccnz .Lrs1\n.Lrb1:\nv_fma_f32 v121,-v120,v120,v121\nv_add_f32 v122,s70,v120\nv_max_f32 v121,0,v121\n" \
"v_cvt_pk_bf16_f32 v122,0,v122\nv_cvt_pk_bf16_f32 v121,0,v121\nv_add_f32 v121,0x3727c5ac,v121\nv_cvt_pk_bf16_f32 v121,0,v121\nv_sqrt_f32 v121,v121\nv_pk_add_f32 v[88:89],v[72:73],v[122:123] op_sel_hi:[1,0] neg_lo:[0,1] neg_hi:[0,1]\nv_cvt_pk_bf16_f32 v121,0,v121\nv_rcp_f32 v123,v121\n" \
"v_cvt_pk_bf16_f32 v88,0,v88\nv_pk_add_f32 v[90:91],v[74:75],v[122:123] op_sel_hi:[1,0] neg_lo:[0,1] neg_hi:[0,1]\nv_pk_add_f32 v[92:93],v[76:77],v[122:123] op_sel_hi:[1,0] neg_lo:[0,1] neg_hi:[0,1]\nv_pk_add_f32 v[94:95],v[78:79],v[122:123] op_sel_hi:[1,0] neg_lo:[0,1] neg_hi:[0,1]\nv_cvt_pk_bf16_f32 v89,0,v89\nv_pk_mul_f32 v[88:89],v[88:89],v[122:123] op_sel:[0,1]\nv_cvt_pk_bf16_f32 v88,0,v88\nv_cvt_pk_bf16_f32 v89,0,v89\n" \
"v_pk_mul_f32 v[88:89],v[88:89],v[32:33]\nv_cvt_pk_bf16_f32 v88,0,v88\nv_cvt_pk_bf16_f32 v89,0,v89\nv_pk_add_f32 v[88:89],v[88:89],v[16:17]\nv_cvt_pk_bf16_f32 v88,0,v88\nv_cvt_pk_bf16_f32 v89,0,v89\nv_pk_mul_f32 v[124:125],v[88:89],v[96:97]\nv_cvt_pk_bf16_f32 v90,0,v90\n" \
"v_cvt_pk_bf16_f32 v91,0,v91\nv_pk_mul_f32 v[90:91],v[90:91],v[122:123] op_sel:[0,1]\nv_cvt_pk_bf16_f32 v90,0,v90\nv_cvt_pk_bf16_f32 v91,0,v91\nv_pk_mul_f32 v[90:91],v[90:91],v[34:35]\nv_cvt_pk_bf16_f32 v90,0,v90\nv_cvt_pk_bf16_f32 v91,0,v91\nv_pk_add_f32 v[90:91],v[90:91],v[18:19]\n" \
"v_cvt_pk_bf16_f32 v90,0,v90\nv_cvt_pk_bf16_f32 v91,0,v91\nv_pk_fma_f32 v[124:125],v[90:91],v[98:99],v[124:125]\nv_cvt_pk_bf16_f32 v92,0,v92\nv_cvt_pk_bf16_f32 v93,0,v93\nv_pk_mul_f32 v[92:93],v[92:93],v[122:123] op_sel:[0,1]\nv_cvt_pk_bf16_f32 v92,0,v92\nv_cvt_pk_bf16_f32 v93,0,v93\n" \
"v_pk_mul_f32 v[92:93],v[92:93],v[36:37]\nv_cvt_pk_bf16_f32 v92,0,v92\nv_cvt_pk_bf16_f32 v93,0,v93\nv_pk_add_f32 v[92:93],v[92:93],v[20:21]\nv_cvt_pk_bf16_f32 v92,0,v92\nv_cvt_pk_bf16_f32 v93,0,v93\nv_pk_fma_f32 v[124:125],v[92:93],v[100:101],v[124:125]\nv_cvt_pk_bf16_f32 v94,0,v94\n" \
"v_cvt_pk_bf16_f32 v95,0,v95\nv_pk_mul_f32 v[94:95],v[94:95],v[122:123] op_sel:[0,1]\nv_cvt_pk_bf16_f32 v94,0,v94\nv_cvt_pk_bf16_f32 v95,0,v95\nv_pk_mul_f32 v[94:95],v[94:95],v[38:39]\nv_cvt_pk_bf16_f32 v94,0,v94\nv_cvt_pk_bf16_f32 v95,0,v95\nv_pk_add_f32 v[94:95],v[94:95],v[22:23]\n" \
"v_cvt_pk_bf16_f32 v94,0,v94\nv_cvt_pk_bf16_f32 v95,0,v95\nv_pk_fma_f32 v[124:125],v[94:95],v[102:103],v[124:125]\nv_add_f32 v124,v124,v125\nv_mov_b32 v125,v124\nv_lshrrev_b32 v104,16,v121\nv_or_b32 v104,v104,v122\nv_permlane16_swap_b32 v124,v125\n" \
"v_add_f32 v124,v124,v125\nv_mov_b32 v125,v124\ns_nop 1\nv_permlane32_swap_b32 v124,v125\nv_add_f32 v124,v124,v125\ns_nop 1\nv_add_f32_dpp v124,v124,v124 quad_perm:[1,0,3,2] row_mask:0xf bank_mask:0xf\ns_nop 1\n" \
"v_add_f32_dpp v124,v124,v124 quad_perm:[2,3,0,1] row_mask:0xf bank_mask:0xf\ns_nop 1\nv_add_f32_dpp v124,v124,v124 row_half_mirror row_mask:0xf bank_mask:0xf\ns_nop 1\nv_add_f32_dpp v124,v124,v124 row_mirror row_mask:0xf bank_mask:0xf\nv_cvt_pk_bf16_f32 v124,0,v124\ns_nop 0\nv_readfirstlane_b32 s77,v124\n" \
"s_nop 0\ns_ashr_i32 s71,s77,31\ns_and_b32 s72,s77,0x7fffffff\ns_or_b32 s71,s71,0x80000000\ns_xor_b32 s77,s77,s71\ns_and_b32 s77,s77,0xffff0000\ns_cmp_gt_u32 s72,0x7f800000\ns_cselect_b32 s77,0,s77\n" \
"s_or_b32 s77,s77,s38\nv_mov_b32 v105,s77\nds_max_u64 v250,v[104:105]\n.Lrow0:\ns_waitcnt vmcnt(27)\nM v[8:11],a[192:195],v[128:131],0\nM v[12:15],a[196:199],v[128:131],0\ns_waitcnt vmcnt(26)\n" \
"M v[8:11],a[192:195],v[132:135],v[8:11]\nM v[12:15],a[196:199],v[132:135],v[12:15]\ns_waitcnt vmcnt(25)\nM v[8:11],a[192:195],v[136:139],v[8:11]\nM v[12:15],a[196:199],v[136:139],v[12:15]\ns_waitcnt vmcnt(24)\nM v[8:11],a[192:195],v[140:143],v[8:11]\nM v[12:15],a[196:199],v[140:143],v[12:15]\n" \
"s_waitcnt vmcnt(23)\nM v[8:11],a[192:195],v[144:147],v[8:11]\nM v[12:15],a[196:199],v[144:147],v[12:15]\ns_waitcnt vmcnt(22)\nM v[8:11],a[192:195],v[148:151],v[8:11]\nM v[12:15],a[196:199],v[148:151],v[12:15]\ns_waitcnt vmcnt(21)\nM v[8:11],a[192:195],v[152:155],v[8:11]\n" \
"M v[12:15],a[196:199],v[152:155],v[12:15]\ns_waitcnt vmcnt(20)\nM v[8:11],a[192:195],v[156:159],v[8:11]\nM v[12:15],a[196:199],v[156:159],v[12:15]\ns_sub_u32 s39,56622112,s74\ns_lshr_b32 s39,s39,5\ns_nop 4\nv_cvt_pk_bf16_f32 v8,0,v8\n" \
"v_cvt_pk_bf16_f32 v9,0,v9\nv_cvt_pk_bf16_f32 v10,0,v10\nv_cvt_pk_bf16_f32 v11,0,v11\nv_cvt_pk_bf16_f32 v12,0,v12\nv_cvt_pk_bf16_f32 v13,0,v13\nv_cvt_pk_bf16_f32 v14,0,v14\nv_cvt_pk_bf16_f32 v15,0,v15\nv_pk_mul_f32 v[8:9],v[8:9],v[0:1]\n" \
"v_pk_mul_f32 v[10:11],v[10:11],v[2:3]\nv_pk_mul_f32 v[12:13],v[12:13],v[4:5]\nv_pk_mul_f32 v[14:15],v[14:15],v[6:7]\nv_cvt_pk_bf16_f32 v8,0,v8\nv_cvt_pk_bf16_f32 v9,0,v9\nv_cvt_pk_bf16_f32 v10,0,v10\nv_cvt_pk_bf16_f32 v11,0,v11\nv_cvt_pk_bf16_f32 v12,0,v12\n" \
"v_cvt_pk_bf16_f32 v13,0,v13\nv_cvt_pk_bf16_f32 v14,0,v14\nv_cvt_pk_bf16_f32 v15,0,v15\nv_pk_add_f32 v[8:9],v[8:9],v[80:81]\nv_pk_add_f32 v[10:11],v[10:11],v[82:83]\nv_pk_add_f32 v[12:13],v[12:13],v[84:85]\nv_pk_add_f32 v[14:15],v[14:15],v[86:87]\nv_cvt_pk_bf16_f32 v8,0,v8\n" \
"v_cvt_pk_bf16_f32 v9,0,v9\nv_cvt_pk_bf16_f32 v10,0,v10\nv_cvt_pk_bf16_f32 v11,0,v11\nv_cvt_pk_bf16_f32 v12,0,v12\nv_cvt_pk_bf16_f32 v13,0,v13\nv_cvt_pk_bf16_f32 v14,0,v14\nv_cvt_pk_bf16_f32 v15,0,v15\nv_readfirstlane_b32 s70,v8\n" \
"s_nop 1\nv_subrev_f32 v40,s70,v8\nv_subrev_f32 v41,s70,v9\nv_subrev_f32 v42,s70,v10\nv_subrev_f32 v43,s70,v11\nv_subrev_f32 v44,s70,v12\nv_subrev_f32 v45,s70,v13\nv_subrev_f32 v46,s70,v14\n" \
"v_subrev_f32 v47,s70,v15\nv_pk_mul_f32 v[60:61],v[40:41],v[40:41]\nv_pk_add_f32 v[56:57],v[40:41],v[42:43]\nv_pk_fma_f32 v[60:61],v[42:43],v[42:43],v[60:61]\nv_pk_fma_f32 v[60:61],v[44:45],v[44:45],v[60:61]\nv_pk_fma_f32 v[60:61],v[46:47],v[46:47],v[60:61]\nv_pk_add_f32 v[44:45],v[44:45],v[46:47]\nv_pk_add_f32 v[56:57],v[56:57],v[44:45]\n" \
"v_add_f32 v56,v56,v57\nv_add_f32 v57,v60,v61\ns_nop 1\nv_permlane16_swap_b32 v56,v57\nv_add_f32 v56,v56,v57\nv_mov_b32 v57,v56\ns_nop 1\nv_permlane32_swap_b32 v56,v57\n" \
"v_add_f32 v56,v56,v57\ns_nop 1\nv_add_f32_dpp v56,v56,v56 quad_perm:[1,0,3,2] row_mask:0xf bank_mask:0xf\ns_nop 1\nv_add_f32_dpp v56,v56,v56 quad_perm:[2,3,0,1] row_mask:0xf bank_mask:0xf\ns_nop 1\nv_add_f32_dpp v56,v56,v56 row_half_mirror row_mask:0xf bank_mask:0xf\ns_nop 1\n" \
"v_add_f32_dpp v56,v56,v56 row_mirror row_mask:0xf bank_mask:0xf\nv_mul_f32 v56,0x3b000000,v56\nv_mov_b32 v57,v56\nv_cmp_u_f32 vcc,v56,v56\ns_nop 0\nv_permlane16_swap_b32 v56,v57\ns_cbranch_vccnz .Lrs0\n.Lrb0:\n" \
"buffer_load_dwordx4 v[232:235],v251,s[52:55],s80 offen sc1\nbuffer_load_dwordx4 v[236:239],v251,s[52:55],s80 offen offset:16 sc1\nv_fma_f32 v57,-v56,v56,v57\nv_add_f32 v58,s70,v56\nv_max_f32 v57,0,v57\nv_cvt_pk_bf16_f32 v58,0,v58\nv_cvt_pk_bf16_f32 v57,0,v57\nv_add_f32 v57,0x3727c5ac,v57\n" \
"v_cvt_pk_bf16_f32 v57,0,v57\nv_sqrt_f32 v57,v57\nv_pk_add_f32 v[24:25],v[8:9],v[58:59] op_sel_hi:[1,0] neg_lo:[0,1] neg_hi:[0,1]\nv_cvt_pk_bf16_f32 v57,0,v57\nv_rcp_f32 v59,v57\nv_cvt_pk_bf16_f32 v24,0,v24\nv_pk_add_f32 v[26:27],v[10:11],v[58:59] op_sel_hi:[1,0] neg_lo:[0,1] neg_hi:[0,1]\nv_pk_add_f32 v[28:29],v[12:13],v[58:59] op_sel_hi:[1,0] neg_lo:[0,1] neg_hi:[0,1]\n" \
"v_pk_add_f32 v[30:31],v[14:15],v[58:59] op_sel_hi:[1,0] neg_lo:[0,1] neg_hi:[0,1]\nv_cvt_pk_bf16_f32 v25,0,v25\nv_pk_mul_f32 v[24:25],v[24:25],v[58:59] op_sel:[0,1]\nv_cvt_pk_bf16_f32 v24,0,v24\nv_cvt_pk_bf16_f32 v25,0,v25\nv_pk_mul_f32 v[24:25],v[24:25],v[32:33]\nv_cvt_pk_bf16_f32 v24,0,v24\nv_cvt_pk_bf16_f32 v25,0,v25\n" \
"v_pk_add_f32 v[24:25],v[24:25],v[16:17]\nv_cvt_pk_bf16_f32 v24,0,v24\nv_cvt_pk_bf16_f32 v25,0,v25\nv_pk_mul_f32 v[60:61],v[24:25],v[96:97]\nv_cvt_pk_bf16_f32 v26,0,v26\nv_cvt_pk_bf16_f32 v27,0,v27\nv_pk_mul_f32 v[26:27],v[26:27],v[58:59] op_sel:[0,1]\nv_cvt_pk_bf16_f32 v26,0,v26\n" \
"v_cvt_pk_bf16_f32 v27,0,v27\nv_pk_mul_f32 v[26:27],v[26:27],v[34:35]\nv_cvt_pk_bf16_f32 v26,0,v26\nv_cvt_pk_bf16_f32 v27,0,v27\nv_pk_add_f32 v[26:27],v[26:27],v[18:19]\nv_cvt_pk_bf16_f32 v26,0,v26\nv_cvt_pk_bf16_f32 v27,0,v27\nv_pk_fma_f32 v[60:61],v[26:27],v[98:99],v[60:61]\n" \
"v_cvt_pk_bf16_f32 v28,0,v28\nv_cvt_pk_bf16_f32 v29,0,v29\nv_pk_mul_f32 v[28:29],v[28:29],v[58:59] op_sel:[0,1]\nv_cvt_pk_bf16_f32 v28,0,v28\nv_cvt_pk_bf16_f32 v29,0,v29\nv_pk_mul_f32 v[28:29],v[28:29],v[36:37]\nv_cvt_pk_bf16_f32 v28,0,v28\nv_cvt_pk_bf16_f32 v29,0,v29\n" \
"v_pk_add_f32 v[28:29],v[28:29],v[20:21]\nv_cvt_pk_bf16_f32 v28,0,v28\nv_cvt_pk_bf16_f32 v29,0,v29\nv_pk_fma_f32 v[60:61],v[28:29],v[100:101],v[60:61]\nv_cvt_pk_bf16_f32 v30,0,v30\nv_cvt_pk_bf16_f32 v31,0,v31\nv_pk_mul_f32 v[30:31],v[30:31],v[58:59] op_sel:[0,1]\nv_cvt_pk_bf16_f32 v30,0,v30\n" \
"v_cvt_pk_bf16_f32 v31,0,v31\nv_pk_mul_f32 v[30:31],v[30:31],v[38:39]\nv_cvt_pk_bf16_f32 v30,0,v30\nv_cvt_pk_bf16_f32 v31,0,v31\nv_pk_add_f32 v[30:31],v[30:31],v[22:23]\nv_cvt_pk_bf16_f32 v30,0,v30\nv_cvt_pk_bf16_f32 v31,0,v31\nv_pk_fma_f32 v[60:61],v[30:31],v[102:103],v[60:61]\n" \
"v_add_f32 v60,v60,v61\nv_mov_b32 v61,v60\nv_lshrrev_b32 v40,16,v57\nv_or_b32 v40,v40,v58\nv_permlane16_swap_b32 v60,v61\nv_add_f32 v60,v60,v61\nv_mov_b32 v61,v60\ns_nop 1\n" \
"v_permlane32_swap_b32 v60,v61\nv_add_f32 v60,v60,v61\ns_nop 1\nv_add_f32_dpp v60,v60,v60 quad_perm:[1,0,3,2] row_mask:0xf bank_mask:0xf\ns_nop 1\nv_add_f32_dpp v60,v60,v60 quad_perm:[2,3,0,1] row_mask:0xf bank_mask:0xf\ns_nop 1\nv_add_f32_dpp v60,v60,v60 row_half_mirror row_mask:0xf bank_mask:0xf\n" \
"s_nop 1\nv_add_f32_dpp v60,v60,v60 row_mirror row_mask:0xf bank_mask:0xf\nv_cvt_pk_bf16_f32 v60,0,v60\ns_nop 0\nv_readfirstlane_b32 s76,v60\ns_nop 0\ns_ashr_i32 s71,s76,31\ns_and_b32 s72,s76,0x7fffffff\n" \
"s_or_b32 s71,s71,0x80000000\ns_xor_b32 s76,s76,s71\ns_and_b32 s76,s76,0xffff0000\ns_cmp_gt_u32 s72,0x7f800000\ns_cselect_b32 s76,0,s76\ns_or_b32 s76,s76,s39\nv_mov_b32 v41,s76\nds_max_u64 v250,v[40:41]\n" \
"s_waitcnt lgkmcnt(0)\ns_barrier\nds_read_b64 v[224:225],v250\ns_waitcnt lgkmcnt(0)\nbuffer_store_dwordx2 v[224:225],v246,s[52:55],s37 offen sc1\nv_readfirstlane_b32 s97,v225\ns_sub_u32 s95,s95,54525952\ns_sub_u32 s74,s74,54525952\n" \
"s_mov_b32 s94,0\ns_cmp_eq_u32 s97,s77\ns_cselect_b32 s65,s95,0x40000000\ns_cselect_b32 s95,0x40000000,s95\ns_cmp_eq_u32 s97,s76\ns_cselect_b32 s66,s74,0x40000000\ns_cselect_b32 s74,0x40000000,s74\ns_lshr_b32 s70,s6,7\n" \
"s_or_b32 s38,s7,s70\ns_cmp_lg_u32 s3,0\ns_waitcnt vmcnt(1)\ns_cbranch_scc1 .Lkp\nv_pk_mul_f32 v[88:89],v[88:89],v[48:49]\nv_pk_mul_f32 v[90:91],v[90:91],v[50:51]\nv_pk_mul_f32 v[92:93],v[92:93],v[52:53]\nv_pk_mul_f32 v[94:95],v[94:95],v[54:55]\n" \
"v_cvt_pk_bf16_f32 v88,0,v88\nv_cvt_pk_bf16_f32 v89,0,v89\nv_cvt_pk_bf16_f32 v90,0,v90\nv_cvt_pk_bf16_f32 v91,0,v91\nv_cvt_pk_bf16_f32 v92,0,v92\nv_cvt_pk_bf16_f32 v93,0,v93\nv_cvt_pk_bf16_f32 v94,0,v94\nv_cvt_pk_bf16_f32 v95,0,v95\n" \
"v_pk_add_f32 v[88:89],v[88:89],v[72:73]\nv_pk_add_f32 v[90:91],v[90:91],v[74:75]\nv_pk_add_f32 v[92:93],v[92:93],v[76:77]\nv_pk_add_f32 v[94:95],v[94:95],v[78:79]\nv_cvt_pk_bf16_f32 v88,0,v88\nds_write_b16_d16_hi v244,v88 offset:0\nv_cvt_pk_bf16_f32 v89,0,v89\nds_write_b16_d16_hi v244,v89 offset:32\n" \
"v_cvt_pk_bf16_f32 v90,0,v90\nds_write_b16_d16_hi v244,v90 offset:64\nv_cvt_pk_bf16_f32 v91,0,v91\nds_write_b16_d16_hi v244,v91 offset:96\nv_cvt_pk_bf16_f32 v92,0,v92\nds_write_b16_d16_hi v244,v92 offset:128\nv_cvt_pk_bf16_f32 v93,0,v93\nds_write_b16_d16_hi v244,v93 offset:160\n" \
"v_cvt_pk_bf16_f32 v94,0,v94\nds_write_b16_d16_hi v244,v94 offset:192\nv_cvt_pk_bf16_f32 v95,0,v95\nds_write_b16_d16_hi v244,v95 offset:224\nds_read_b128 v[64:67],v245 offset:0\nv_pk_mul_f32 v[24:25],v[24:25],v[48:49]\nv_pk_mul_f32 v[26:27],v[26:27],v[50:51]\nv_pk_mul_f32 v[28:29],v[28:29],v[52:53]\n" \
"v_pk_mul_f32 v[30:31],v[30:31],v[54:55]\nv_cvt_pk_bf16_f32 v24,0,v24\nv_cvt_pk_bf16_f32 v25,0,v25\nv_cvt_pk_bf16_f32 v26,0,v26\nv_cvt_pk_bf16_f32 v27,0,v27\nv_cvt_pk_bf16_f32 v28,0,v28\nv_cvt_pk_bf16_f32 v29,0,v29\nv_cvt_pk_bf16_f32 v30,0,v30\n" \
"v_cvt_pk_bf16_f32 v31,0,v31\nv_pk_add_f32 v[24:25],v[24:25],v[8:9]\nv_pk_add_f32 v[26:27],v[26:27],v[10:11]\nv_pk_add_f32 v[28:29],v[28:29],v[12:13]\nv_pk_add_f32 v[30:31],v[30:31],v[14:15]\nv_cvt_pk_bf16_f32 v24,0,v24\nds_write_b16_d16_hi v244,v24 offset:1024\nv_cvt_pk_bf16_f32 v25,0,v25\n" \
"ds_write_b16_d16_hi v244,v25 offset:1056\nv_cvt_pk_bf16_f32 v26,0,v26\nds_write_b16_d16_hi v244,v26 offset:1088\nv_cvt_pk_bf16_f32 v27,0,v27\nds_write_b16_d16_hi v244,v27 offset:1120\nv_cvt_pk_bf16_f32 v28,0,v28\nds_write_b16_d16_hi v244,v28 offset:1152\nv_cvt_pk_bf16_f32 v29,0,v29\n" \
"ds_write_b16_d16_hi v244,v29 offset:1184\nv_cvt_pk_bf16_f32 v30,0,v30\nds_write_b16_d16_hi v244,v30 offset:1216\nv_cvt_pk_bf16_f32 v31,0,v31\nds_write_b16_d16_hi v244,v31 offset:1248\nds_read_b128 v[68:71],v245 offset:1024\ns_waitcnt lgkmcnt(0)\nbuffer_store_dwordx4 v[64:67],v240,s[44:47],s95 offen sc1\n" \
"buffer_store_dwordx4 v[68:71],v240,s[44:47],s74 offen sc1\ns_cmp_eq_u32 s7,0\ns_cselect_b32 s70,1,0\ns_xor_b32 s71,s65,s66\ns_or_b32 s70,s70,s71\ns_cbranch_scc0 .Lfin\n.Lkp:\ns_cmp_lg_u32 s7,0\n" \
"s_cbranch_scc1 .Lkl\nv_max3_u32 v228,v233,v235,v237\nv_min3_u32 v229,v233,v235,v237\nv_max_u32 v228,v228,v239\nv_min_u32 v229,v229,v239\nv_cmp_lt_u32 vcc,s94,v228\nv_cmp_eq_u32 s[90:91],0,v229\ns_cbranch_vccnz .Lnew\n" \
"s_cmp_eq_u64 s[90:91],0\ns_cbranch_scc1 .Lv0\ns_sub_u32 s73,s73,1\ns_cbranch_scc1 .Lv0\ns_sleep 3\nbuffer_load_dwordx4 v[232:235],v251,s[52:55],s80 offen sc1\nbuffer_load_dwordx4 v[236:239],v251,s[52:55],s80 offen offset:16 sc1\ns_waitcnt vmcnt(0)\n" \
"s_branch .Lkp\n.Lv0:\nv_mov_b32 v230,s96\ns_or_b32 s72,s94,2\nv_mov_b32 v231,s72\nds_write_b64 v250,v[230:231] offset:8\ns_branch .Lver\n.Lkl:\n" \
"ds_read_b64 v[230:231],v250 offset:8\ns_mov_b32 s91,0\ns_waitcnt lgkmcnt(0)\nv_readfirstlane_b32 s72,v231\nv_readfirstlane_b32 s96,v230\ns_and_b32 s90,s72,2\ns_xor_b32 s72,s72,s90\ns_xor_b32 s90,s90,2\n" \
"s_cmp_lg_u32 s72,s94\ns_mov_b32 s94,s72\ns_cbranch_scc1 .Lsp\ns_cmp_eq_u32 s90,0\ns_cbranch_scc1 .Lver\ns_sub_u32 s73,s73,1\ns_cbranch_scc1 .Lver\ns_sleep 1\n" \
".Lyield:\ns_branch .Lkl\n.Lnew:\nv_mov_b32 v230,v228\nv_cmp_eq_u32 vcc,v228,v233\ns_cmp_eq_u64 s[90:91],0\ns_cselect_b32 s71,2,0\nv_max_u32_dpp v230,v230,v230 quad_perm:[1,0,3,2] row_mask:0xf bank_mask:0xf\n" \
"v_cndmask_b32 v229,v238,v232,vcc\nv_cmp_eq_u32 vcc,v228,v235\nv_max_u32_dpp v230,v230,v230 quad_perm:[2,3,0,1] row_mask:0xf bank_mask:0xf\ns_nop 1\nv_max_u32_dpp v230,v230,v230 row_half_mirror row_mask:0xf bank_mask:0xf\nv_cndmask_b32 v229,v229,v234,vcc\nv_cmp_eq_u32 vcc,v228,v237\nv_max_u32_dpp v230,v230,v230 row_mirror row_mask:0xf bank_mask:0xf\n" \
"v_mov_b32 v231,v230\nv_cndmask_b32 v229,v229,v236,vcc\ns_nop 0\nv_permlane16_swap_b32 v230,v231\nv_max_u32 v230,v230,v231\nv_mov_b32 v231,v230\ns_nop 1\nv_permlane32_swap_b32 v230,v231\n" \
"v_max_u32 v230,v230,v231\ns_nop 0\nv_readfirstlane_b32 s94,v230\ns_nop 1\nv_cmp_eq_u32 vcc,s94,v228\ns_nop 0\ns_ff1_i32_b64 s72,vcc\nv_readlane_b32 s96,v229,s72\n" \
"s_nop 1\nv_mov_b32 v230,s96\ns_or_b32 s72,s94,s71\nv_mov_b32 v231,s72\nds_write_b64 v250,v[230:231] offset:8\n.Lsp:\ns_not_b32 s15,s94\ns_bfe_u32 s15,s15,0xb0005\n" \
"s_lshl_b32 s15,s15,10\ns_cmp_eq_u32 s3,0\ns_cbranch_scc1 .Lkp\ns_add_u32 s72,s15,54525952\nbuffer_load_dword v129,v241,s[52:55],s72 offen sc1\ns_add_u32 s72,s15,56623104\nbuffer_load_dword v131,v241,s[52:55],s72 offen sc1\ns_add_u32 s72,s15,58720256\n" \
"buffer_load_dword v133,v241,s[52:55],s72 offen sc1\ns_add_u32 s72,s15,60817408\nbuffer_load_dword v135,v241,s[52:55],s72 offen sc1\ns_add_u32 s72,s15,62914560\nbuffer_load_dword v137,v241,s[52:55],s72 offen sc1\ns_add_u32 s72,s15,65011712\nbuffer_load_dword v139,v241,s[52:55],s72 offen sc1\ns_add_u32 s72,s15,67108864\n" \
"buffer_load_dword v141,v241,s[52:55],s72 offen sc1\ns_add_u32 s72,s15,69206016\nbuffer_load_dword v143,v241,s[52:55],s72 offen sc1\nv_add_u32 v146,s15,v248\nglobal_load_ushort v144,v146,s[8:9]\nglobal_load_ushort v145,v146,s[8:9] offset:32\nbuffer_load_dwordx4 v[232:235],v251,s[52:55],s80 offen sc1\nbuffer_load_dwordx4 v[236:239],v251,s[52:55],s80 offen offset:16 sc1\n" \
"v_accvgpr_read_b32 v161,a0\nv_lshlrev_b32 v160,16,v161\nv_and_b32 v161,0xffff0000,v161\nv_accvgpr_read_b32 v163,a1\nv_lshlrev_b32 v162,16,v163\nv_and_b32 v163,0xffff0000,v163\nv_accvgpr_read_b32 v165,a2\nv_lshlrev_b32 v164,16,v165\n" \
"v_and_b32 v165,0xffff0000,v165\nv_accvgpr_read_b32 v167,a3\nv_lshlrev_b32 v166,16,v167\nv_and_b32 v167,0xffff0000,v167\nv_accvgpr_read_b32 v169,a4\nv_lshlrev_b32 v168,16,v169\nv_and_b32 v169,0xffff0000,v169\nv_accvgpr_read_b32 v171,a5\n" \
"v_lshlrev_b32 v170,16,v171\nv_and_b32 v171,0xffff0000,v171\nv_accvgpr_read_b32 v173,a6\nv_lshlrev_b32 v172,16,v173\nv_and_b32 v173,0xffff0000,v173\nv_accvgpr_read_b32 v175,a7\nv_lshlrev_b32 v174,16,v175\nv_and_b32 v175,0xffff0000,v175\n" \
"v_accvgpr_read_b32 v177,a8\nv_lshlrev_b32 v176,16,v177\nv_and_b32 v177,0xffff0000,v177\nv_accvgpr_read_b32 v179,a9\nv_lshlrev_b32 v178,16,v179\nv_and_b32 v179,0xffff0000,v179\nv_accvgpr_read_b32 v181,a10\nv_lshlrev_b32 v180,16,v181\n" \
"v_and_b32 v181,0xffff0000,v181\nv_accvgpr_read_b32 v183,a11\nv_lshlrev_b32 v182,16,v183\nv_and_b32 v183,0xffff0000,v183\nv_accvgpr_read_b32 v185,a12\nv_lshlrev_b32 v184,16,v185\nv_and_b32 v185,0xffff0000,v185\nv_accvgpr_read_b32 v187,a13\n" \
"v_lshlrev_b32 v186,16,v187\nv_and_b32 v187,0xffff0000,v187\nv_accvgpr_read_b32 v189,a14\nv_lshlrev_b32 v188,16,v189\nv_and_b32 v189,0xffff0000,v189\nv_accvgpr_read_b32 v191,a15\nv_lshlrev_b32 v190,16,v191\nv_and_b32 v191,0xffff0000,v191\n" \
"s_and_b32 s82,s96,0xffff0000\ns_lshl_b32 s83,s96,16\nv_rcp_f32 v148,s83\ns_waitcnt vmcnt(11)\nv_lshlrev_b32 v128,16,v129\nv_and_b32 v129,0xffff0000,v129\ns_waitcnt vmcnt(10)\nv_lshlrev_b32 v130,16,v131\n" \
"v_and_b32 v131,0xffff0000,v131\nv_pk_add_f32 v[128:129],v[128:129],v[130:131]\ns_waitcnt vmcnt(9)\nv_lshlrev_b32 v132,16,v133\nv_and_b32 v133,0xffff0000,v133\ns_waitcnt vmcnt(8)\nv_lshlrev_b32 v134,16,v135\nv_and_b32 v135,0xffff0000,v135\n" \
"v_pk_add_f32 v[132:133],v[132:133],v[134:135]\nv_pk_add_f32 v[128:129],v[128:129],v[132:133]\ns_waitcnt vmcnt(7)\nv_lshlrev_b32 v136,16,v137\nv_and_b32 v137,0xffff0000,v137\ns_waitcnt vmcnt(6)\nv_lshlrev_b32 v138,16,v139\nv_and_b32 v139,0xffff0000,v139\n" \
"v_pk_add_f32 v[136:137],v[136:137],v[138:139]\ns_waitcnt vmcnt(5)\nv_lshlrev_b32 v140,16,v141\nv_and_b32 v141,0xffff0000,v141\ns_waitcnt vmcnt(4)\nv_lshlrev_b32 v142,16,v143\nv_and_b32 v143,0xffff0000,v143\nv_pk_add_f32 v[140:141],v[140:141],v[142:143]\n" \
"v_pk_add_f32 v[136:137],v[136:137],v[140:141]\nv_pk_add_f32 v[128:129],v[128:129],v[136:137]\nv_cvt_pk_bf16_f32 v128,0,v128\nv_cvt_pk_bf16_f32 v129,0,v129\nv_pk_mul_f32 v[128:129],v[128:129],v[112:113]\nv_cvt_pk_bf16_f32 v128,0,v128\nv_cvt_pk_bf16_f32 v129,0,v129\ns_waitcnt vmcnt(3)\n" \
"v_lshlrev_b32 v144,16,v144\ns_waitcnt vmcnt(2)\nv_lshlrev_b32 v145,16,v145\nv_pk_add_f32 v[128:129],v[128:129],v[144:145]\nv_cvt_pk_bf16_f32 v128,0,v128\nv_cvt_pk_bf16_f32 v129,0,v129\nv_pk_add_f32 v[128:129],v[128:129],s[82:83] op_sel_hi:[1,0] neg_lo:[0,1] neg_hi:[0,1]\nv_cvt_pk_bf16_f32 v128,0,v128\n" \
"v_cvt_pk_bf16_f32 v129,0,v129\nv_pk_mul_f32 v[128:129],v[128:129],v[148:149] op_sel_hi:[1,0]\nv_cvt_pk_bf16_f32 v128,0,v128\nv_cvt_pk_bf16_f32 v129,0,v129\nv_pk_mul_f32 v[128:129],v[128:129],v[116:117]\nv_cvt_pk_bf16_f32 v128,0,v128\nv_cvt_pk_bf16_f32 v129,0,v129\nv_pk_add_f32 v[128:129],v[128:129],v[114:115]\n" \
"v_cvt_pk_bf16_f32 v128,v128,v129\ns_nop 1\nv_mov_b32_dpp v132,v128 quad_perm:[0,0,0,0] row_mask:0xf bank_mask:0xf\nv_mov_b32_dpp v133,v128 quad_perm:[1,1,1,1] row_mask:0xf bank_mask:0xf\nv_mov_b32_dpp v134,v128 quad_perm:[2,2,2,2] row_mask:0xf bank_mask:0xf\nv_mov_b32_dpp v135,v128 quad_perm:[3,3,3,3] row_mask:0xf bank_mask:0xf\nv_dot2_f32_bf16 v136,v192,v132,0\nv_dot2_f32_bf16 v137,v193,v132,0\n" \
"v_dot2_f32_bf16 v138,v194,v132,0\nv_dot2_f32_bf16 v139,v195,v132,0\nv_dot2_f32_bf16 v140,v208,v132,0\nv_dot2_f32_bf16 v141,v209,v132,0\nv_dot2_f32_bf16 v142,v210,v132,0\nv_dot2_f32_bf16 v143,v211,v132,0\nv_dot2_f32_bf16 v136,v196,v133,v136\nv_dot2_f32_bf16 v137,v197,v133,v137\n" \
"v_dot2_f32_bf16 v138,v198,v133,v138\nv_dot2_f32_bf16 v139,v199,v133,v139\nv_dot2_f32_bf16 v140,v212,v133,v140\nv_dot2_f32_bf16 v141,v213,v133,v141\nv_dot2_f32_bf16 v142,v214,v133,v142\nv_dot2_f32_bf16 v143,v215,v133,v143\nv_dot2_f32_bf16 v136,v200,v134,v136\nv_dot2_f32_bf16 v137,v201,v134,v137\n" \
"v_dot2_f32_bf16 v138,v202,v134,v138\nv_dot2_f32_bf16 v139,v203,v134,v139\nv_dot2_f32_bf16 v140,v216,v134,v140\nv_dot2_f32_bf16 v141,v217,v134,v141\nv_dot2_f32_bf16 v142,v218,v134,v142\nv_dot2_f32_bf16 v143,v219,v134,v143\nv_dot2_f32_bf16 v136,v204,v135,v136\nv_dot2_f32_bf16 v137,v205,v135,v137\n" \
"v_dot2_f32_bf16 v138,v206,v135,v138\nv_dot2_f32_bf16 v139,v207,v135,v139\nv_dot2_f32_bf16 v140,v220,v135,v140\nv_dot2_f32_bf16 v141,v221,v135,v141\nv_dot2_f32_bf16 v142,v222,v135,v142\nv_dot2_f32_bf16 v143,v223,v135,v143\nv_permlane32_swap_b32 v136,v140\nv_add_f32 v136,v136,v140\n" \
"v_permlane32_swap_b32 v137,v141\nv_add_f32 v137,v137,v141\nv_permlane32_swap_b32 v138,v142\nv_add_f32 v138,v138,v142\nv_permlane32_swap_b32 v139,v143\nv_add_f32 v139,v139,v143\nv_permlane16_swap_b32 v136,v138\nv_add_f32 v136,v136,v138\n" \
"v_permlane16_swap_b32 v137,v139\nv_add_f32 v137,v137,v139\nv_add_f32_dpp v136,v136,v136 row_ror:4 row_mask:0xf bank_mask:0xf\ns_nop 1\nv_add_f32_dpp v136,v136,v136 row_ror:8 row_mask:0xf bank_mask:0xf\nv_add_f32_dpp v137,v137,v137 row_ror:4 row_mask:0xf bank_mask:0xf\ns_nop 1\nv_add_f32_dpp v137,v137,v137 row_ror:8 row_mask:0xf bank_mask:0xf\n" \
"s_cmp_eq_u64 s[90:91],0\ns_cbranch_scc1 .Lver\ns_waitcnt vmcnt(0)\ns_branch .Lkp\n.Lver:\ns_cmp_eq_u32 s3,0\ns_cbranch_scc1 .Lnf\nds_write_b32 v252,v136\n" \
"ds_write_b32 v252,v137 offset:16\ns_lshl_b32 s70,s2,6\ns_add_u32 s70,s70,98304\nv_lshlrev_b32 v241,1,v241\ns_waitcnt lgkmcnt(0)\ns_barrier\nds_read_b128 v[128:131],v253\nds_read_b128 v[132:135],v253 offset:256\n" \
"s_waitcnt lgkmcnt(1)\nv_pk_add_f32 v[128:129],v[128:129],v[130:131]\nv_add_f32 v128,v128,v129\nv_cvt_pk_bf16_f32 v128,0,v128\nv_mul_f32 v129,0xbfb8aa3b,v128\nv_exp_f32 v129,v129\ns_waitcnt lgkmcnt(0)\nv_pk_add_f32 v[132:133],v[132:133],v[134:135]\n" \
"v_add_f32 v129,1.0,v129\nv_rcp_f32 v129,v129\nv_add_f32 v132,v132,v133\nv_cvt_pk_bf16_f32 v132,0,v132\nv_mul_f32 v128,v128,v129\nv_cvt_pk_bf16_f32 v128,0,v128\nv_mul_f32 v128,v128,v132\nv_cvt_pk_bf16_f32 v128,0,v128\n" \
"s_nop 0\nv_readlane_b32 s8,v128,0\nv_readlane_b32 s9,v128,1\nv_readlane_b32 s10,v128,2\nv_readlane_b32 s11,v128,3\nv_readlane_b32 s12,v128,4\nv_readlane_b32 s13,v128,5\nv_readlane_b32 s14,v128,6\n" \
"v_readlane_b32 s15,v128,7\nv_readlane_b32 s16,v128,8\nv_readlane_b32 s17,v128,9\nv_readlane_b32 s18,v128,10\nv_readlane_b32 s19,v128,11\nv_readlane_b32 s20,v128,12\nv_readlane_b32 s21,v128,13\nv_readlane_b32 s22,v128,14\n" \
"v_readlane_b32 s23,v128,15\nv_pk_mul_f32 v[136:137],v[160:161],s[8:9] op_sel_hi:[1,0]\nv_pk_mul_f32 v[138:139],v[162:163],s[8:9] op_sel:[0,1]\nv_pk_fma_f32 v[136:137],v[164:165],s[10:11],v[136:137] op_sel_hi:[1,0,1]\nv_pk_fma_f32 v[138:139],v[166:167],s[10:11],v[138:139] op_sel:[0,1,0]\nv_pk_fma_f32 v[136:137],v[168:169],s[12:13],v[136:137] op_sel_hi:[1,0,1]\nv_pk_fma_f32 v[138:139],v[170:171],s[12:13],v[138:139] op_sel:[0,1,0]\nv_pk_fma_f32 v[136:137],v[172:173],s[14:15],v[136:137] op_sel_hi:[1,0,1]\n" \
"v_pk_fma_f32 v[138:139],v[174:175],s[14:15],v[138:139] op_sel:[0,1,0]\nv_pk_fma_f32 v[136:137],v[176:177],s[16:17],v[136:137] op_sel_hi:[1,0,1]\nv_pk_fma_f32 v[138:139],v[178:179],s[16:17],v[138:139] op_sel:[0,1,0]\nv_pk_fma_f32 v[136:137],v[180:181],s[18:19],v[136:137] op_sel_hi:[1,0,1]\nv_pk_fma_f32 v[138:139],v[182:183],s[18:19],v[138:139] op_sel:[0,1,0]\nv_pk_fma_f32 v[136:137],v[184:185],s[20:21],v[136:137] op_sel_hi:[1,0,1]\nv_pk_fma_f32 v[138:139],v[186:187],s[20:21],v[138:139] op_sel:[0,1,0]\nv_pk_fma_f32 v[136:137],v[188:189],s[22:23],v[136:137] op_sel_hi:[1,0,1]\n" \
"v_pk_fma_f32 v[138:139],v[190:191],s[22:23],v[138:139] op_sel:[0,1,0]\nv_pk_add_f32 v[136:137],v[136:137],v[138:139]\nv_min_u32 v136,-2,v136\nv_min_u32 v137,-2,v137\nbuffer_store_dwordx2 v[136:137],v241,s[52:55],s70 offen sc1\nv_pk_mul_f32 v[88:89],v[88:89],v[48:49]\nv_pk_mul_f32 v[90:91],v[90:91],v[50:51]\nv_pk_mul_f32 v[92:93],v[92:93],v[52:53]\n" \
"v_pk_mul_f32 v[94:95],v[94:95],v[54:55]\nv_cvt_pk_bf16_f32 v88,0,v88\nv_cvt_pk_bf16_f32 v89,0,v89\nv_cvt_pk_bf16_f32 v90,0,v90\nv_cvt_pk_bf16_f32 v91,0,v91\nv_cvt_pk_bf16_f32 v92,0,v92\nv_cvt_pk_bf16_f32 v93,0,v93\nv_cvt_pk_bf16_f32 v94,0,v94\n" \
"v_cvt_pk_bf16_f32 v95,0,v95\nv_pk_add_f32 v[88:89],v[88:89],v[72:73]\nv_pk_add_f32 v[90:91],v[90:91],v[74:75]\nv_pk_add_f32 v[92:93],v[92:93],v[76:77]\nv_pk_add_f32 v[94:95],v[94:95],v[78:79]\nv_cvt_pk_bf16_f32 v88,0,v88\nds_write_b16_d16_hi v244,v88 offset:0\nv_cvt_pk_bf16_f32 v89,0,v89\n" \
"ds_write_b16_d16_hi v244,v89 offset:32\nv_cvt_pk_bf16_f32 v90,0,v90\nds_write_b16_d16_hi v244,v90 offset:64\nv_cvt_pk_bf16_f32 v91,0,v91\nds_write_b16_d16_hi v244,v91 offset:96\nv_cvt_pk_bf16_f32 v92,0,v92\nds_write_b16_d16_hi v244,v92 offset:128\nv_cvt_pk_bf16_f32 v93,0,v93\n" \
"ds_write_b16_d16_hi v244,v93 offset:160\nv_cvt_pk_bf16_f32 v94,0,v94\nds_write_b16_d16_hi v244,v94 offset:192\nv_cvt_pk_bf16_f32 v95,0,v95\nds_write_b16_d16_hi v244,v95 offset:224\nds_read_b128 v[64:67],v245 offset:0\nv_pk_mul_f32 v[24:25],v[24:25],v[48:49]\nv_pk_mul_f32 v[26:27],v[26:27],v[50:51]\n" \
"v_pk_mul_f32 v[28:29],v[28:29],v[52:53]\nv_pk_mul_f32 v[30:31],v[30:31],v[54:55]\nv_cvt_pk_bf16_f32 v24,0,v24\nv_cvt_pk_bf16_f32 v25,0,v25\nv_cvt_pk_bf16_f32 v26,0,v26\nv_cvt_pk_bf16_f32 v27,0,v27\nv_cvt_pk_bf16_f32 v28,0,v28\nv_cvt_pk_bf16_f32 v29,0,v29\n" \
"v_cvt_pk_bf16_f32 v30,0,v30\nv_cvt_pk_bf16_f32 v31,0,v31\nv_pk_add_f32 v[24:25],v[24:25],v[8:9]\nv_pk_add_f32 v[26:27],v[26:27],v[10:11]\nv_pk_add_f32 v[28:29],v[28:29],v[12:13]\nv_pk_add_f32 v[30:31],v[30:31],v[14:15]\nv_cvt_pk_bf16_f32 v24,0,v24\nds_write_b16_d16_hi v244,v24 offset:1024\n" \
"v_cvt_pk_bf16_f32 v25,0,v25\nds_write_b16_d16_hi v244,v25 offset:1056\nv_cvt_pk_bf16_f32 v26,0,v26\nds_write_b16_d16_hi v244,v26 offset:1088\nv_cvt_pk_bf16_f32 v27,0,v27\nds_write_b16_d16_hi v244,v27 offset:1120\nv_cvt_pk_bf16_f32 v28,0,v28\nds_write_b16_d16_hi v244,v28 offset:1152\n" \
"v_cvt_pk_bf16_f32 v29,0,v29\nds_write_b16_d16_hi v244,v29 offset:1184\nv_cvt_pk_bf16_f32 v30,0,v30\nds_write_b16_d16_hi v244,v30 offset:1216\nv_cvt_pk_bf16_f32 v31,0,v31\nds_write_b16_d16_hi v244,v31 offset:1248\nds_read_b128 v[68:71],v245 offset:1024\ns_waitcnt lgkmcnt(0)\n" \
"buffer_store_dwordx4 v[64:67],v240,s[44:47],s95 offen sc1\nbuffer_store_dwordx4 v[68:71],v240,s[44:47],s74 offen sc1\n.Lnf:\ns_cmp_eq_u32 s94,s97\ns_cbranch_scc1 .Lwin\nbuffer_store_dwordx4 v[64:67],v240,s[44:47],s65 offen sc1\nbuffer_store_dwordx4 v[68:71],v240,s[44:47],s66 offen sc1\n.Lwin:\n" \
"s_cmp_lg_u32 s38,0\ns_cbranch_scc1 .Lfin\ns_add_u32 s72,s15,54525952\nbuffer_load_ushort v128,v243,s[52:55],s72 offen sc1\ns_add_u32 s72,s15,56623104\nbuffer_load_ushort v129,v243,s[52:55],s72 offen sc1\ns_add_u32 s72,s15,58720256\nbuffer_load_ushort v130,v243,s[52:55],s72 offen sc1\n" \
"s_add_u32 s72,s15,60817408\nbuffer_load_ushort v131,v243,s[52:55],s72 offen sc1\ns_add_u32 s72,s15,62914560\nbuffer_load_ushort v132,v243,s[52:55],s72 offen sc1\ns_add_u32 s72,s15,65011712\nbuffer_load_ushort v133,v243,s[52:55],s72 offen sc1\ns_add_u32 s72,s15,67108864\nbuffer_load_ushort v134,v243,s[52:55],s72 offen sc1\n" \
"s_add_u32 s72,s15,69206016\nbuffer_load_ushort v135,v243,s[52:55],s72 offen sc1\nv_add_u32 v242,s15,v242\nglobal_load_ushort v144,v242,s[8:9]\nv_min_u32 v168,55,v249\nv_lshlrev_b32 v168,11,v168\ns_lshl4_add_u32 s70,s98,98304\ns_add_u32 s71,s70,114688\n" \
"s_and_b32 s82,s96,0xffff0000\ns_lshl_b32 s83,s96,16\nv_rcp_f32 v148,s83\ns_ashr_i32 s37,s94,31\ns_not_b32 s37,s37\ns_or_b32 s37,s37,0x80000000\ns_xor_b32 s37,s37,s94\ns_and_b32 s37,s37,0xffff0000\n" \
"v_mov_b32 v149,0xbfb8aa3b\nv_mul_f32 v149,s37,v149\nv_exp_f32 v149,v149\nv_add_f32 v126,1.0,v126\nv_add_f32 v149,1.0,v149\nv_rcp_f32 v149,v149\nv_cvt_pk_bf16_f32 v126,0,v126\nv_cvt_pk_bf16_f32 v149,0,v149\n" \
"v_and_b32 v150,15,v249\nv_cmp_eq_u32 vcc,0,v150\nv_mov_b32 v150,0x40000000\nv_cmp_gt_u32 s[90:91],56,v249\nv_cndmask_b32 v150,v150,v242,vcc\ns_waitcnt vmcnt(8)\nv_lshlrev_b32 v128,16,v128\ns_waitcnt vmcnt(7)\n" \
"v_lshlrev_b32 v129,16,v129\nv_add_f32 v128,v128,v129\ns_waitcnt vmcnt(6)\nv_lshlrev_b32 v130,16,v130\ns_waitcnt vmcnt(5)\nv_lshlrev_b32 v131,16,v131\nv_add_f32 v130,v130,v131\nv_add_f32 v128,v128,v130\n" \
"s_waitcnt vmcnt(4)\nv_lshlrev_b32 v132,16,v132\ns_waitcnt vmcnt(3)\nv_lshlrev_b32 v133,16,v133\nv_add_f32 v132,v132,v133\ns_waitcnt vmcnt(2)\nv_lshlrev_b32 v134,16,v134\ns_waitcnt vmcnt(1)\n" \
"v_lshlrev_b32 v135,16,v135\nv_add_f32 v134,v134,v135\nv_add_f32 v132,v132,v134\nv_add_f32 v128,v128,v132\nv_cvt_pk_bf16_f32 v128,0,v128\nv_mul_f32 v128,v128,v62\nv_cvt_pk_bf16_f32 v128,0,v128\ns_waitcnt vmcnt(0)\n" \
"v_lshlrev_b32 v144,16,v144\nv_add_f32 v128,v128,v144\nv_cvt_pk_bf16_f32 v128,0,v128\nv_subrev_f32 v129,s82,v128\nv_cvt_pk_bf16_f32 v129,0,v129\nv_mul_f32 v129,v129,v148\nv_cvt_pk_bf16_f32 v129,0,v129\nv_mul_f32 v129,v129,v126\n" \
"v_cvt_pk_bf16_f32 v129,0,v129\nv_add_f32 v129,v129,v63\nv_cvt_pk_bf16_f32 v129,0,v129\n.Lp2:\nbuffer_load_dwordx4 v[160:163],v168,s[52:55],s70 offen sc1\nbuffer_load_dwordx4 v[164:167],v168,s[52:55],s71 offen sc1\ns_waitcnt vmcnt(0)\nv_max3_u32 v228,v160,v161,v162\n" \
"v_max3_u32 v229,v163,v164,v165\nv_max3_u32 v228,v228,v166,v167\nv_max_u32 v228,v228,v229\nv_pk_add_f32 v[160:161],v[160:161],v[164:165]\nv_cmp_eq_u32 vcc,-1,v228\nv_pk_add_f32 v[162:163],v[162:163],v[166:167]\ns_sub_u32 s73,s73,1\ns_cselect_b64 vcc,0,vcc\n" \
"s_cmp_lg_u64 vcc,0\ns_cselect_b32 s72,0x8000000,0\nv_cndmask_b32 v160,0,v160,s[90:91]\nv_cndmask_b32 v161,0,v161,s[90:91]\nv_cndmask_b32 v162,0,v162,s[90:91]\nv_cndmask_b32 v163,0,v163,s[90:91]\ns_nop 0\nv_permlane32_swap_b32 v160,v162\n" \
"v_permlane32_swap_b32 v161,v163\nv_pk_add_f32 v[160:161],v[160:161],v[162:163]\ns_nop 1\nv_permlane16_swap_b32 v160,v161\nv_add_f32 v160,v160,v161\ns_nop 1\nv_add_f32_dpp v160,v160,v160 quad_perm:[1,0,3,2] row_mask:0xf bank_mask:0xf\ns_nop 1\n" \
"v_add_f32_dpp v160,v160,v160 quad_perm:[2,3,0,1] row_mask:0xf bank_mask:0xf\ns_nop 1\nv_add_f32_dpp v160,v160,v160 row_half_mirror row_mask:0xf bank_mask:0xf\ns_nop 1\nv_add_f32_dpp v160,v160,v160 row_mirror row_mask:0xf bank_mask:0xf\nv_cvt_pk_bf16_f32 v160,0,v160\nv_mul_f32 v160,v160,v149\nv_cvt_pk_bf16_f32 v160,0,v160\n" \
"v_add_f32 v160,v160,v129\nv_cvt_pk_bf16_f32 v160,0,v160\nv_mul_f32 v160,v160,v127\nv_cvt_pk_bf16_f32 v160,0,v160\nv_add_f32 v160,v160,v128\nv_cvt_pk_bf16_f32 v160,0,v160\nbuffer_store_short_d16_hi v160,v150,s[44:47],s72 offen sc1\n.Lp2s:\n" \
"s_cbranch_scc1 .Lp2\ns_branch .Lfin\n.Lhs:\ns_sub_u32 s73,s73,1\ns_cbranch_scc1 .Lhb\ns_sleep 2\nbuffer_load_dwordx2 v[166:167],v165,s[52:55],s80 offen sc1\ns_waitcnt vmcnt(0)\n" \
"v_xor_b32 v166,v166,v230\nv_xor_b32 v167,v167,v231\nv_or_b32 v166,v166,v167\nv_cmp_ne_u32 vcc,0,v166\ns_nop 0\ns_cbranch_vccnz .Lhs\ns_branch .Lhb\n.Lps0:\n" \
"s_sub_u32 s73,s73,1\ns_cbranch_scc1 .Lpb0\ns_sleep 1\nbuffer_load_dword v248,v228,s[52:55],s80 offen sc1\ns_waitcnt vmcnt(0)\nv_cmp_eq_u32 vcc,0,v248\ns_nop 0\ns_cbranch_vccnz .Lps0\n" \
"s_branch .Lpb0\n.Lps1:\ns_sub_u32 s73,s73,1\ns_cbranch_scc1 .Lpb1\ns_sleep 1\nbuffer_load_dword v248,v228,s[52:55],s80 offen sc1\ns_waitcnt vmcnt(0)\nv_cmp_eq_u32 vcc,0,v248\n" \
"s_nop 0\ns_cbranch_vccnz .Lps1\ns_branch .Lpb1\n.Lps2:\ns_sub_u32 s73,s73,1\ns_cbranch_scc1 .Lpb2\ns_sleep 1\nbuffer_load_dword v248,v251,s[52:55],s80 offen sc1\n" \
"s_waitcnt vmcnt(0)\nv_cmp_eq_u32 vcc,0,v248\ns_nop 0\ns_cbranch_vccnz .Lps2\ns_branch .Lpb2\n.Lps3:\ns_sub_u32 s73,s73,1\ns_cbranch_scc1 .Lpb3\n" \
"s_sleep 1\nbuffer_load_dword v248,v251,s[52:55],s80 offen sc1\ns_waitcnt vmcnt(0)\nv_cmp_eq_u32 vcc,0,v248\ns_nop 0\ns_cbranch_vccnz .Lps3\ns_branch .Lpb3\n.Lps4:\n" \
"s_sub_u32 s73,s73,1\ns_cbranch_scc1 .Lpb4\ns_sleep 1\nbuffer_load_dword v248,v251,s[52:55],s80 offen sc1\ns_waitcnt vmcnt(0)\nv_cmp_eq_u32 vcc,0,v248\ns_nop 0\ns_cbranch_vccnz .Lps4\n" \
"s_branch .Lpb4\n.Lps5:\ns_sub_u32 s73,s73,1\ns_cbranch_scc1 .Lpb5\ns_sleep 1\nbuffer_load_dword v248,v251,s[52:55],s80 offen sc1\ns_waitcnt vmcnt(0)\nv_cmp_eq_u32 vcc,0,v248\n" \
"s_nop 0\ns_cbranch_vccnz .Lps5\ns_branch .Lpb5\n.Lx01:\ns_cmp_eq_u32 s62,0\ns_cbranch_scc1 .Ltrans\ns_branch .Ldf\n.Lps6:\n" \
"s_sub_u32 s73,s73,1\ns_cbranch_scc1 .Lpb6\ns_sleep 1\nbuffer_load_dword v248,v251,s[52:55],s80 offen sc1\ns_waitcnt vmcnt(0)\nv_and_b32 v248,s17,v248\nv_cmp_eq_u32 vcc,0,v248\ns_nop 0\n" \
"s_cbranch_vccnz .Lps6\ns_branch .Lpb6\n.Lps7:\ns_sub_u32 s73,s73,1\ns_cbranch_scc1 .Lpb7\ns_sleep 1\nbuffer_load_dword v248,v228,s[52:55],s80 offen sc1\ns_waitcnt vmcnt(0)\n" \
"v_cmp_eq_u32 vcc,0,v248\ns_nop 0\ns_cbranch_vccnz .Lps7\ns_branch .Lpb7\n.Lps8:\ns_sub_u32 s73,s73,1\ns_cbranch_scc1 .Lpb8\ns_sleep 1\n" \
"buffer_load_dword v248,v251,s[52:55],s79 offen sc1\ns_waitcnt vmcnt(0)\nv_cmp_eq_u32 vcc,0,v248\ns_nop 0\ns_cbranch_vccnz .Lps8\ns_branch .Lpb8\n.Lrs1:\ns_mov_b32 s71,0\n" \
".Lrl1:\ns_mov_b32 s97,0\nv_max3_u32 v124,v160,v161,v162\nv_max3_u32 v124,v124,v163,v163\nv_cmp_eq_u32 vcc,-1,v124\ns_add_u32 s72,s95,0\ns_cbranch_vccz .Lrz10\ns_mov_b64 exec,vcc\n" \
"s_mov_b32 s97,1\nbuffer_load_dwordx4 v[160:163],v240,s[52:55],s72 offen sc1\ns_mov_b64 exec,-1\n.Lrz10:\nv_max3_u32 v124,v164,v165,v166\nv_max3_u32 v124,v124,v167,v167\nv_cmp_eq_u32 vcc,-1,v124\ns_add_u32 s72,s95,2097152\n" \
"s_cbranch_vccz .Lrz11\ns_mov_b64 exec,vcc\ns_mov_b32 s97,1\nbuffer_load_dwordx4 v[164:167],v240,s[52:55],s72 offen sc1\ns_mov_b64 exec,-1\n.Lrz11:\nv_max3_u32 v124,v168,v169,v170\nv_max3_u32 v124,v124,v171,v171\n" \
"v_cmp_eq_u32 vcc,-1,v124\ns_add_u32 s72,s95,4194304\ns_cbranch_vccz .Lrz12\ns_mov_b64 exec,vcc\ns_mov_b32 s97,1\nbuffer_load_dwordx4 v[168:171],v240,s[52:55],s72 offen sc1\ns_mov_b64 exec,-1\n.Lrz12:\n" \
"v_max3_u32 v124,v172,v173,v174\nv_max3_u32 v124,v124,v175,v175\nv_cmp_eq_u32 vcc,-1,v124\ns_add_u32 s72,s95,6291456\ns_cbranch_vccz .Lrz13\ns_mov_b64 exec,vcc\ns_mov_b32 s97,1\nbuffer_load_dwordx4 v[172:175],v240,s[52:55],s72 offen sc1\n" \
"s_mov_b64 exec,-1\n.Lrz13:\nv_max3_u32 v124,v176,v177,v178\nv_max3_u32 v124,v124,v179,v179\nv_cmp_eq_u32 vcc,-1,v124\ns_add_u32 s72,s95,8388608\ns_cbranch_vccz .Lrz14\ns_mov_b64 exec,vcc\n" \
"s_mov_b32 s97,1\nbuffer_load_dwordx4 v[176:179],v240,s[52:55],s72 offen sc1\ns_mov_b64 exec,-1\n.Lrz14:\nv_max3_u32 v124,v180,v181,v182\nv_max3_u32 v124,v124,v183,v183\nv_cmp_eq_u32 vcc,-1,v124\ns_add_u32 s72,s95,10485760\n" \
"s_cbranch_vccz .Lrz15\ns_mov_b64 exec,vcc\ns_mov_b32 s97,1\nbuffer_load_dwordx4 v[180:183],v240,s[52:55],s72 offen sc1\ns_mov_b64 exec,-1\n.Lrz15:\nv_max3_u32 v124,v184,v185,v186\nv_max3_u32 v124,v124,v187,v187\n" \
"v_cmp_eq_u32 vcc,-1,v124\ns_add_u32 s72,s95,12582912\ns_cbranch_vccz .Lrz16\ns_mov_b64 exec,vcc\ns_mov_b32 s97,1\nbuffer_load_dwordx4 v[184:187],v240,s[52:55],s72 offen sc1\ns_mov_b64 exec,-1\n.Lrz16:\n" \
"v_max3_u32 v124,v188,v189,v190\nv_max3_u32 v124,v124,v191,v191\nv_cmp_eq_u32 vcc,-1,v124\ns_add_u32 s72,s95,14680064\ns_cbranch_vccz .Lrz17\ns_mov_b64 exec,vcc\ns_mov_b32 s97,1\nbuffer_load_dwordx4 v[188:191],v240,s[52:55],s72 offen sc1\n" \
"s_mov_b64 exec,-1\n.Lrz17:\ns_cmp_lg_u32 s97,0\ns_cbranch_scc1 .Lrm1\ns_cmp_eq_u32 s71,0\ns_cbranch_scc1 .Lrb1\ns_branch .Lrow1\n.Lrm1:\n" \
"s_mov_b32 s71,1\ns_waitcnt vmcnt(0)\ns_sub_u32 s73,s73,1\ns_cbranch_scc1 .Lrb1\ns_sleep 2\ns_branch .Lrl1\n.Lrs0:\ns_mov_b32 s71,0\n" \
".Lrl0:\ns_mov_b32 s97,0\nv_max3_u32 v60,v128,v129,v130\nv_max3_u32 v60,v60,v131,v131\nv_cmp_eq_u32 vcc,-1,v60\ns_add_u32 s72,s74,0\ns_cbranch_vccz .Lrz00\ns_mov_b64 exec,vcc\n" \
"s_mov_b32 s97,1\nbuffer_load_dwordx4 v[128:131],v240,s[52:55],s72 offen sc1\ns_mov_b64 exec,-1\n.Lrz00:\nv_max3_u32 v60,v132,v133,v134\nv_max3_u32 v60,v60,v135,v135\nv_cmp_eq_u32 vcc,-1,v60\ns_add_u32 s72,s74,2097152\n" \
"s_cbranch_vccz .Lrz01\ns_mov_b64 exec,vcc\ns_mov_b32 s97,1\nbuffer_load_dwordx4 v[132:135],v240,s[52:55],s72 offen sc1\ns_mov_b64 exec,-1\n.Lrz01:\nv_max3_u32 v60,v136,v137,v138\nv_max3_u32 v60,v60,v139,v139\n" \
"v_cmp_eq_u32 vcc,-1,v60\ns_add_u32 s72,s74,4194304\ns_cbranch_vccz .Lrz02\ns_mov_b64 exec,vcc\ns_mov_b32 s97,1\nbuffer_load_dwordx4 v[136:139],v240,s[52:55],s72 offen sc1\ns_mov_b64 exec,-1\n.Lrz02:\n" \
"v_max3_u32 v60,v140,v141,v142\nv_max3_u32 v60,v60,v143,v143\nv_cmp_eq_u32 vcc,-1,v60\ns_add_u32 s72,s74,6291456\ns_cbranch_vccz .Lrz03\ns_mov_b64 exec,vcc\ns_mov_b32 s97,1\nbuffer_load_dwordx4 v[140:143],v240,s[52:55],s72 offen sc1\n" \
"s_mov_b64 exec,-1\n.Lrz03:\nv_max3_u32 v60,v144,v145,v146\nv_max3_u32 v60,v60,v147,v147\nv_cmp_eq_u32 vcc,-1,v60\ns_add_u32 s72,s74,8388608\ns_cbranch_vccz .Lrz04\ns_mov_b64 exec,vcc\n" \
"s_mov_b32 s97,1\nbuffer_load_dwordx4 v[144:147],v240,s[52:55],s72 offen sc1\ns_mov_b64 exec,-1\n.Lrz04:\nv_max3_u32 v60,v148,v149,v150\nv_max3_u32 v60,v60,v151,v151\nv_cmp_eq_u32 vcc,-1,v60\ns_add_u32 s72,s74,10485760\n" \
"s_cbranch_vccz .Lrz05\ns_mov_b64 exec,vcc\ns_mov_b32 s97,1\nbuffer_load_dwordx4 v[148:151],v240,s[52:55],s72 offen sc1\ns_mov_b64 exec,-1\n.Lrz05:\nv_max3_u32 v60,v152,v153,v154\nv_max3_u32 v60,v60,v155,v155\n" \
"v_cmp_eq_u32 vcc,-1,v60\ns_add_u32 s72,s74,12582912\ns_cbranch_vccz .Lrz06\ns_mov_b64 exec,vcc\ns_mov_b32 s97,1\nbuffer_load_dwordx4 v[152:155],v240,s[52:55],s72 offen sc1\ns_mov_b64 exec,-1\n.Lrz06:\n" \
"v_max3_u32 v60,v156,v157,v158\nv_max3_u32 v60,v60,v159,v159\nv_cmp_eq_u32 vcc,-1,v60\ns_add_u32 s72,s74,14680064\ns_cbranch_vccz .Lrz07\ns_mov_b64 exec,vcc\ns_mov_b32 s97,1\nbuffer_load_dwordx4 v[156:159],v240,s[52:55],s72 offen sc1\n" \
"s_mov_b64 exec,-1\n.Lrz07:\ns_cmp_lg_u32 s97,0\ns_cbranch_scc1 .Lrm0\ns_cmp_eq_u32 s71,0\ns_cbranch_scc1 .Lrb0\ns_branch .Lrow0\n.Lrm0:\n" \
"s_mov_b32 s71,1\ns_waitcnt vmcnt(0)\ns_sub_u32 s73,s73,1\ns_cbranch_scc1 .Lrb0\ns_sleep 2\ns_branch .Lrl0\n.Lredo:\ns_waitcnt vmcnt(0) lgkmcnt(0)\n" \
"s_barrier\ns_mov_b64 s[90:91],0\ns_mov_b32 s99,0\ns_mov_b32 s17,1\ns_lshl_b32 s70,s63,19\ns_add_u32 s37,s70,3145728\ns_lshl_b32 s71,s62,15\ns_add_u32 s72,s37,s64\n" \
"s_add_u32 s72,s72,491520\ns_sub_u32 s72,s72,s71\ns_add_u32 m0,s64,0\ns_nop 0\nbuffer_load_dwordx4 v240,s[52:55],s72 offen offset:0 sc1 lds\nbuffer_load_dwordx4 v240,s[52:55],s72 offen offset:1024 sc1 lds\nbuffer_load_dwordx4 v240,s[52:55],s72 offen offset:2048 sc1 lds\nbuffer_load_dwordx4 v240,s[52:55],s72 offen offset:3072 sc1 lds\n" \
"s_add_u32 m0,s64,65536\ns_add_u32 s72,s72,16777216\nbuffer_load_dwordx4 v240,s[52:55],s72 offen offset:0 sc1 lds\nbuffer_load_dwordx4 v240,s[52:55],s72 offen offset:1024 sc1 lds\nbuffer_load_dwordx4 v240,s[52:55],s72 offen offset:2048 sc1 lds\nbuffer_load_dwordx4 v240,s[52:55],s72 offen offset:3072 sc1 lds\ns_add_u32 s72,s37,s64\ns_add_u32 s72,s72,507904\n" \
"s_sub_u32 s72,s72,s71\ns_add_u32 m0,s64,16384\ns_nop 0\nbuffer_load_dwordx4 v240,s[52:55],s72 offen offset:0 sc1 lds\nbuffer_load_dwordx4 v240,s[52:55],s72 offen offset:1024 sc1 lds\nbuffer_load_dwordx4 v240,s[52:55],s72 offen offset:2048 sc1 lds\nbuffer_load_dwordx4 v240,s[52:55],s72 offen offset:3072 sc1 lds\ns_add_u32 m0,s64,81920\n" \
"s_add_u32 s72,s72,16777216\nbuffer_load_dwordx4 v240,s[52:55],s72 offen offset:0 sc1 lds\nbuffer_load_dwordx4 v240,s[52:55],s72 offen offset:1024 sc1 lds\nbuffer_load_dwordx4 v240,s[52:55],s72 offen offset:2048 sc1 lds\nbuffer_load_dwordx4 v240,s[52:55],s72 offen offset:3072 sc1 lds\ns_add_u32 s72,s37,s64\ns_add_u32 s72,s72,16384\ns_add_u32 s72,s72,s71\n" \
"s_add_u32 m0,s64,32768\ns_nop 0\nbuffer_load_dwordx4 v240,s[52:55],s72 offen offset:0 sc1 lds\nbuffer_load_dwordx4 v240,s[52:55],s72 offen offset:1024 sc1 lds\nbuffer_load_dwordx4 v240,s[52:55],s72 offen offset:2048 sc1 lds\nbuffer_load_dwordx4 v240,s[52:55],s72 offen offset:3072 sc1 lds\ns_add_u32 m0,s64,98304\ns_add_u32 s72,s72,16777216\n" \
"buffer_load_dwordx4 v240,s[52:55],s72 offen offset:0 sc1 lds\nbuffer_load_dwordx4 v240,s[52:55],s72 offen offset:1024 sc1 lds\nbuffer_load_dwordx4 v240,s[52:55],s72 offen offset:2048 sc1 lds\nbuffer_load_dwordx4 v240,s[52:55],s72 offen offset:3072 sc1 lds\ns_add_u32 s72,s37,s64\ns_add_u32 s72,s72,0\ns_add_u32 s72,s72,s71\ns_add_u32 m0,s64,49152\n" \
"s_nop 0\nbuffer_load_dwordx4 v240,s[52:55],s72 offen offset:0 sc1 lds\nbuffer_load_dwordx4 v240,s[52:55],s72 offen offset:1024 sc1 lds\nbuffer_load_dwordx4 v240,s[52:55],s72 offen offset:2048 sc1 lds\nbuffer_load_dwordx4 v240,s[52:55],s72 offen offset:3072 sc1 lds\ns_add_u32 m0,s64,114688\ns_add_u32 s72,s72,16777216\nbuffer_load_dwordx4 v240,s[52:55],s72 offen offset:0 sc1 lds\n" \
"buffer_load_dwordx4 v240,s[52:55],s72 offen offset:1024 sc1 lds\nbuffer_load_dwordx4 v240,s[52:55],s72 offen offset:2048 sc1 lds\nbuffer_load_dwordx4 v240,s[52:55],s72 offen offset:3072 sc1 lds\nv_and_b32 v251,15,v249\nv_lshrrev_b32 v250,4,v249\nv_lshlrev_b32 v250,2,v250\nv_sub_u32 v251,v251,v250\ns_lshl_b32 s70,s62,7\n" \
"s_lshl_b32 s72,s7,5\ns_sub_u32 s38,s72,s70\ns_add_u32 s38,s38,1920\ns_sub_u32 s39,s70,s72\ns_add_u32 s39,s39,96\nv_add_u32 v225,s38,v251\nv_add_u32 v225,0,v225\nv_mov_b32 v224,0xff800000\n" \
"v_add_u32 v229,s38,v251\nv_add_u32 v229,16,v229\nv_mov_b32 v228,0xff800000\nv_add_u32 v233,s39,v251\nv_add_u32 v233,0,v233\nv_mov_b32 v232,0xff800000\nv_add_u32 v237,s39,v251\nv_add_u32 v237,16,v237\n" \
"v_mov_b32 v236,0xff800000\ns_sub_u32 s38,524288,s71\ns_add_u32 s38,s38,s37\n.Lrl:\nbuffer_load_dwordx4 a[0:3],v240,s[52:55],s37 offen offset:0 sc1\nbuffer_load_dwordx4 a[4:7],v240,s[52:55],s37 offen offset:1024 sc1\nbuffer_load_dwordx4 a[8:11],v240,s[52:55],s37 offen offset:2048 sc1\nbuffer_load_dwordx4 a[12:15],v240,s[52:55],s37 offen offset:3072 sc1\n" \
"s_add_u32 s37,s37,4096\nbuffer_load_dwordx4 a[16:19],v240,s[52:55],s37 offen offset:0 sc1\nbuffer_load_dwordx4 a[20:23],v240,s[52:55],s37 offen offset:1024 sc1\nbuffer_load_dwordx4 a[24:27],v240,s[52:55],s37 offen offset:2048 sc1\nbuffer_load_dwordx4 a[28:31],v240,s[52:55],s37 offen offset:3072 sc1\ns_add_u32 s37,s37,4096\nbuffer_load_dwordx4 a[32:35],v240,s[52:55],s37 offen offset:0 sc1\nbuffer_load_dwordx4 a[36:39],v240,s[52:55],s37 offen offset:1024 sc1\n" \
"buffer_load_dwordx4 a[40:43],v240,s[52:55],s37 offen offset:2048 sc1\nbuffer_load_dwordx4 a[44:47],v240,s[52:55],s37 offen offset:3072 sc1\ns_add_u32 s37,s37,4096\nbuffer_load_dwordx4 a[48:51],v240,s[52:55],s37 offen offset:0 sc1\nbuffer_load_dwordx4 a[52:55],v240,s[52:55],s37 offen offset:1024 sc1\nbuffer_load_dwordx4 a[56:59],v240,s[52:55],s37 offen offset:2048 sc1\nbuffer_load_dwordx4 a[60:63],v240,s[52:55],s37 offen offset:3072 sc1\ns_add_u32 s37,s37,4096\n" \
"s_waitcnt vmcnt(15)\nM v[128:131],a[0:3],a[128:131],0\ns_waitcnt vmcnt(14)\nM v[128:131],a[4:7],a[132:135],v[128:131]\ns_waitcnt vmcnt(13)\nM v[128:131],a[8:11],a[136:139],v[128:131]\ns_waitcnt vmcnt(12)\nM v[128:131],a[12:15],a[140:143],v[128:131]\n" \
"M v[132:135],a[0:3],a[144:147],0\nM v[132:135],a[4:7],a[148:151],v[132:135]\nM v[132:135],a[8:11],a[152:155],v[132:135]\nM v[132:135],a[12:15],a[156:159],v[132:135]\nM v[136:139],a[0:3],a[160:163],0\nM v[136:139],a[4:7],a[164:167],v[136:139]\nM v[136:139],a[8:11],a[168:171],v[136:139]\nM v[136:139],a[12:15],a[172:175],v[136:139]\n" \
"M v[140:143],a[0:3],a[176:179],0\nM v[140:143],a[4:7],a[180:183],v[140:143]\nM v[140:143],a[8:11],a[184:187],v[140:143]\nM v[140:143],a[12:15],a[188:191],v[140:143]\ns_waitcnt vmcnt(11)\nM v[144:147],a[16:19],a[128:131],0\ns_waitcnt vmcnt(10)\nM v[144:147],a[20:23],a[132:135],v[144:147]\n" \
"s_waitcnt vmcnt(9)\nM v[144:147],a[24:27],a[136:139],v[144:147]\ns_waitcnt vmcnt(8)\nM v[144:147],a[28:31],a[140:143],v[144:147]\nM v[148:151],a[16:19],a[144:147],0\nM v[148:151],a[20:23],a[148:151],v[148:151]\nM v[148:151],a[24:27],a[152:155],v[148:151]\nM v[148:151],a[28:31],a[156:159],v[148:151]\n" \
"M v[152:155],a[16:19],a[160:163],0\nM v[152:155],a[20:23],a[164:167],v[152:155]\nM v[152:155],a[24:27],a[168:171],v[152:155]\nM v[152:155],a[28:31],a[172:175],v[152:155]\nM v[156:159],a[16:19],a[176:179],0\nM v[156:159],a[20:23],a[180:183],v[156:159]\nM v[156:159],a[24:27],a[184:187],v[156:159]\nM v[156:159],a[28:31],a[188:191],v[156:159]\n" \
"s_waitcnt vmcnt(7)\nM v[160:163],a[32:35],a[128:131],0\ns_waitcnt vmcnt(6)\nM v[160:163],a[36:39],a[132:135],v[160:163]\ns_waitcnt vmcnt(5)\nM v[160:163],a[40:43],a[136:139],v[160:163]\ns_waitcnt vmcnt(4)\nM v[160:163],a[44:47],a[140:143],v[160:163]\n" \
"M v[164:167],a[32:35],a[144:147],0\nM v[164:167],a[36:39],a[148:151],v[164:167]\nM v[164:167],a[40:43],a[152:155],v[164:167]\nM v[164:167],a[44:47],a[156:159],v[164:167]\nM v[168:171],a[32:35],a[160:163],0\nM v[168:171],a[36:39],a[164:167],v[168:171]\nM v[168:171],a[40:43],a[168:171],v[168:171]\nM v[168:171],a[44:47],a[172:175],v[168:171]\n" \
"M v[172:175],a[32:35],a[176:179],0\nM v[172:175],a[36:39],a[180:183],v[172:175]\nM v[172:175],a[40:43],a[184:187],v[172:175]\nM v[172:175],a[44:47],a[188:191],v[172:175]\ns_waitcnt vmcnt(3)\nM v[176:179],a[48:51],a[128:131],0\ns_waitcnt vmcnt(2)\nM v[176:179],a[52:55],a[132:135],v[176:179]\n" \
"s_waitcnt vmcnt(1)\nM v[176:179],a[56:59],a[136:139],v[176:179]\ns_waitcnt vmcnt(0)\nM v[176:179],a[60:63],a[140:143],v[176:179]\nM v[180:183],a[48:51],a[144:147],0\nM v[180:183],a[52:55],a[148:151],v[180:183]\nM v[180:183],a[56:59],a[152:155],v[180:183]\nM v[180:183],a[60:63],a[156:159],v[180:183]\n" \
"M v[184:187],a[48:51],a[160:163],0\nM v[184:187],a[52:55],a[164:167],v[184:187]\nM v[184:187],a[56:59],a[168:171],v[184:187]\nM v[184:187],a[60:63],a[172:175],v[184:187]\nM v[188:191],a[48:51],a[176:179],0\nM v[188:191],a[52:55],a[180:183],v[188:191]\nM v[188:191],a[56:59],a[184:187],v[188:191]\nM v[188:191],a[60:63],a[188:191],v[188:191]\n" \
"v_cmp_le_i32 vcc,0,v225\ns_nop 1\nv_cndmask_b32 v128,v252,v128,vcc\nv_max_f32 v224,v224,v128\nv_cmp_le_i32 vcc,1,v225\ns_nop 1\nv_cndmask_b32 v129,v252,v129,vcc\nv_max_f32 v224,v224,v129\n" \
"v_cmp_le_i32 vcc,2,v225\ns_nop 1\nv_cndmask_b32 v130,v252,v130,vcc\nv_max_f32 v224,v224,v130\nv_cmp_le_i32 vcc,3,v225\ns_nop 1\nv_cndmask_b32 v131,v252,v131,vcc\nv_max_f32 v224,v224,v131\n" \
"v_cmp_le_i32 vcc,0,v229\ns_nop 1\nv_cndmask_b32 v132,v252,v132,vcc\nv_max_f32 v228,v228,v132\nv_cmp_le_i32 vcc,1,v229\ns_nop 1\nv_cndmask_b32 v133,v252,v133,vcc\nv_max_f32 v228,v228,v133\n" \
"v_cmp_le_i32 vcc,2,v229\ns_nop 1\nv_cndmask_b32 v134,v252,v134,vcc\nv_max_f32 v228,v228,v134\nv_cmp_le_i32 vcc,3,v229\ns_nop 1\nv_cndmask_b32 v135,v252,v135,vcc\nv_max_f32 v228,v228,v135\n" \
"v_cmp_le_i32 vcc,0,v233\ns_nop 1\nv_cndmask_b32 v136,v252,v136,vcc\nv_max_f32 v232,v232,v136\nv_cmp_le_i32 vcc,1,v233\ns_nop 1\nv_cndmask_b32 v137,v252,v137,vcc\nv_max_f32 v232,v232,v137\n" \
"v_cmp_le_i32 vcc,2,v233\ns_nop 1\nv_cndmask_b32 v138,v252,v138,vcc\nv_max_f32 v232,v232,v138\nv_cmp_le_i32 vcc,3,v233\ns_nop 1\nv_cndmask_b32 v139,v252,v139,vcc\nv_max_f32 v232,v232,v139\n" \
"v_cmp_le_i32 vcc,0,v237\ns_nop 1\nv_cndmask_b32 v140,v252,v140,vcc\nv_max_f32 v236,v236,v140\nv_cmp_le_i32 vcc,1,v237\ns_nop 1\nv_cndmask_b32 v141,v252,v141,vcc\nv_max_f32 v236,v236,v141\n" \
"v_cmp_le_i32 vcc,2,v237\ns_nop 1\nv_cndmask_b32 v142,v252,v142,vcc\nv_max_f32 v236,v236,v142\nv_cmp_le_i32 vcc,3,v237\ns_nop 1\nv_cndmask_b32 v143,v252,v143,vcc\nv_max_f32 v236,v236,v143\n" \
"v_cmp_le_i32 vcc,16,v225\ns_nop 1\nv_cndmask_b32 v144,v252,v144,vcc\nv_max_f32 v224,v224,v144\nv_cmp_le_i32 vcc,17,v225\ns_nop 1\nv_cndmask_b32 v145,v252,v145,vcc\nv_max_f32 v224,v224,v145\n" \
"v_cmp_le_i32 vcc,18,v225\ns_nop 1\nv_cndmask_b32 v146,v252,v146,vcc\nv_max_f32 v224,v224,v146\nv_cmp_le_i32 vcc,19,v225\ns_nop 1\nv_cndmask_b32 v147,v252,v147,vcc\nv_max_f32 v224,v224,v147\n" \
"v_cmp_le_i32 vcc,16,v229\ns_nop 1\nv_cndmask_b32 v148,v252,v148,vcc\nv_max_f32 v228,v228,v148\nv_cmp_le_i32 vcc,17,v229\ns_nop 1\nv_cndmask_b32 v149,v252,v149,vcc\nv_max_f32 v228,v228,v149\n" \
"v_cmp_le_i32 vcc,18,v229\ns_nop 1\nv_cndmask_b32 v150,v252,v150,vcc\nv_max_f32 v228,v228,v150\nv_cmp_le_i32 vcc,19,v229\ns_nop 1\nv_cndmask_b32 v151,v252,v151,vcc\nv_max_f32 v228,v228,v151\n" \
"v_cmp_le_i32 vcc,16,v233\ns_nop 1\nv_cndmask_b32 v152,v252,v152,vcc\nv_max_f32 v232,v232,v152\nv_cmp_le_i32 vcc,17,v233\ns_nop 1\nv_cndmask_b32 v153,v252,v153,vcc\nv_max_f32 v232,v232,v153\n" \
"v_cmp_le_i32 vcc,18,v233\ns_nop 1\nv_cndmask_b32 v154,v252,v154,vcc\nv_max_f32 v232,v232,v154\nv_cmp_le_i32 vcc,19,v233\ns_nop 1\nv_cndmask_b32 v155,v252,v155,vcc\nv_max_f32 v232,v232,v155\n" \
"v_cmp_le_i32 vcc,16,v237\ns_nop 1\nv_cndmask_b32 v156,v252,v156,vcc\nv_max_f32 v236,v236,v156\nv_cmp_le_i32 vcc,17,v237\ns_nop 1\nv_cndmask_b32 v157,v252,v157,vcc\nv_max_f32 v236,v236,v157\n" \
"v_cmp_le_i32 vcc,18,v237\ns_nop 1\nv_cndmask_b32 v158,v252,v158,vcc\nv_max_f32 v236,v236,v158\nv_cmp_le_i32 vcc,19,v237\ns_nop 1\nv_cndmask_b32 v159,v252,v159,vcc\nv_max_f32 v236,v236,v159\n" \
"v_cmp_le_i32 vcc,32,v225\ns_nop 1\nv_cndmask_b32 v160,v252,v160,vcc\nv_max_f32 v224,v224,v160\nv_cmp_le_i32 vcc,33,v225\ns_nop 1\nv_cndmask_b32 v161,v252,v161,vcc\nv_max_f32 v224,v224,v161\n" \
"v_cmp_le_i32 vcc,34,v225\ns_nop 1\nv_cndmask_b32 v162,v252,v162,vcc\nv_max_f32 v224,v224,v162\nv_cmp_le_i32 vcc,35,v225\ns_nop 1\nv_cndmask_b32 v163,v252,v163,vcc\nv_max_f32 v224,v224,v163\n" \
"v_cmp_le_i32 vcc,32,v229\ns_nop 1\nv_cndmask_b32 v164,v252,v164,vcc\nv_max_f32 v228,v228,v164\nv_cmp_le_i32 vcc,33,v229\ns_nop 1\nv_cndmask_b32 v165,v252,v165,vcc\nv_max_f32 v228,v228,v165\n" \
"v_cmp_le_i32 vcc,34,v229\ns_nop 1\nv_cndmask_b32 v166,v252,v166,vcc\nv_max_f32 v228,v228,v166\nv_cmp_le_i32 vcc,35,v229\ns_nop 1\nv_cndmask_b32 v167,v252,v167,vcc\nv_max_f32 v228,v228,v167\n" \
"v_cmp_le_i32 vcc,32,v233\ns_nop 1\nv_cndmask_b32 v168,v252,v168,vcc\nv_max_f32 v232,v232,v168\nv_cmp_le_i32 vcc,33,v233\ns_nop 1\nv_cndmask_b32 v169,v252,v169,vcc\nv_max_f32 v232,v232,v169\n" \
"v_cmp_le_i32 vcc,34,v233\ns_nop 1\nv_cndmask_b32 v170,v252,v170,vcc\nv_max_f32 v232,v232,v170\nv_cmp_le_i32 vcc,35,v233\ns_nop 1\nv_cndmask_b32 v171,v252,v171,vcc\nv_max_f32 v232,v232,v171\n" \
"v_cmp_le_i32 vcc,32,v237\ns_nop 1\nv_cndmask_b32 v172,v252,v172,vcc\nv_max_f32 v236,v236,v172\nv_cmp_le_i32 vcc,33,v237\ns_nop 1\nv_cndmask_b32 v173,v252,v173,vcc\nv_max_f32 v236,v236,v173\n" \
"v_cmp_le_i32 vcc,34,v237\ns_nop 1\nv_cndmask_b32 v174,v252,v174,vcc\nv_max_f32 v236,v236,v174\nv_cmp_le_i32 vcc,35,v237\ns_nop 1\nv_cndmask_b32 v175,v252,v175,vcc\nv_max_f32 v236,v236,v175\n" \
"v_cmp_le_i32 vcc,48,v225\ns_nop 1\nv_cndmask_b32 v176,v252,v176,vcc\nv_max_f32 v224,v224,v176\nv_cmp_le_i32 vcc,49,v225\ns_nop 1\nv_cndmask_b32 v177,v252,v177,vcc\nv_max_f32 v224,v224,v177\n" \
"v_cmp_le_i32 vcc,50,v225\ns_nop 1\nv_cndmask_b32 v178,v252,v178,vcc\nv_max_f32 v224,v224,v178\nv_cmp_le_i32 vcc,51,v225\ns_nop 1\nv_cndmask_b32 v179,v252,v179,vcc\nv_max_f32 v224,v224,v179\n" \
"v_cmp_le_i32 vcc,48,v229\ns_nop 1\nv_cndmask_b32 v180,v252,v180,vcc\nv_max_f32 v228,v228,v180\nv_cmp_le_i32 vcc,49,v229\ns_nop 1\nv_cndmask_b32 v181,v252,v181,vcc\nv_max_f32 v228,v228,v181\n" \
"v_cmp_le_i32 vcc,50,v229\ns_nop 1\nv_cndmask_b32 v182,v252,v182,vcc\nv_max_f32 v228,v228,v182\nv_cmp_le_i32 vcc,51,v229\ns_nop 1\nv_cndmask_b32 v183,v252,v183,vcc\nv_max_f32 v228,v228,v183\n" \
"v_cmp_le_i32 vcc,48,v233\ns_nop 1\nv_cndmask_b32 v184,v252,v184,vcc\nv_max_f32 v232,v232,v184\nv_cmp_le_i32 vcc,49,v233\ns_nop 1\nv_cndmask_b32 v185,v252,v185,vcc\nv_max_f32 v232,v232,v185\n" \
"v_cmp_le_i32 vcc,50,v233\ns_nop 1\nv_cndmask_b32 v186,v252,v186,vcc\nv_max_f32 v232,v232,v186\nv_cmp_le_i32 vcc,51,v233\ns_nop 1\nv_cndmask_b32 v187,v252,v187,vcc\nv_max_f32 v232,v232,v187\n" \
"v_cmp_le_i32 vcc,48,v237\ns_nop 1\nv_cndmask_b32 v188,v252,v188,vcc\nv_max_f32 v236,v236,v188\nv_cmp_le_i32 vcc,49,v237\ns_nop 1\nv_cndmask_b32 v189,v252,v189,vcc\nv_max_f32 v236,v236,v189\n" \
"v_cmp_le_i32 vcc,50,v237\ns_nop 1\nv_cndmask_b32 v190,v252,v190,vcc\nv_max_f32 v236,v236,v190\nv_cmp_le_i32 vcc,51,v237\ns_nop 1\nv_cndmask_b32 v191,v252,v191,vcc\nv_max_f32 v236,v236,v191\n" \
"v_subrev_u32 v225,64,v225\nv_subrev_u32 v229,64,v229\nv_subrev_u32 v233,64,v233\nv_subrev_u32 v237,64,v237\ns_cmp_lt_u32 s37,s38\ns_cbranch_scc1 .Lrl\ns_waitcnt vmcnt(0)\ns_barrier\n" \
"ds_read_b128 a[0:3],v240 offset:0\nds_read_b128 a[4:7],v240 offset:1024\nds_read_b128 a[8:11],v240 offset:2048\nds_read_b128 a[12:15],v240 offset:3072\nds_read_b128 a[16:19],v240 offset:4096\nds_read_b128 a[20:23],v240 offset:5120\nds_read_b128 a[24:27],v240 offset:6144\nds_read_b128 a[28:31],v240 offset:7168\n" \
"ds_read_b128 a[32:35],v240 offset:8192\nds_read_b128 a[36:39],v240 offset:9216\nds_read_b128 a[40:43],v240 offset:10240\nds_read_b128 a[44:47],v240 offset:11264\nds_read_b128 a[48:51],v240 offset:12288\nds_read_b128 a[52:55],v240 offset:13312\nds_read_b128 a[56:59],v240 offset:14336\nds_read_b128 a[60:63],v240 offset:15360\n" \
"s_cmp_eq_u32 s62,0\ns_cselect_b32 s36,0x8000,0\ns_add_u32 s36,s36,s64\ns_cmp_eq_u32 s36,s96\ns_cselect_b32 s72,0xc000,s98\ns_add_u32 s65,s36,s72\ns_cmp_eq_u32 s65,s96\ns_cselect_b32 s72,0xc000,s98\n" \
"s_add_u32 s66,s65,s72\ns_cmp_eq_u32 s66,s96\ns_cselect_b32 s72,0xc000,s98\ns_add_u32 s95,s66,s72\ns_add_u32 s97,s96,65536\ns_add_u32 s15,s64,s46\ns_add_u32 s15,s15,16384\ns_mov_b32 s94,0xc000\n" \
"s_cmp_eq_u32 s62,7\ns_cselect_b32 s72,98304,0\ns_sub_u32 s97,s97,s72\nv_and_b32 v251,31,v249\nv_lshlrev_b32 v251,2,v251\ns_lshl_b32 s80,s63,7\ns_add_u32 s80,s80,86016\nv_add_u32 v250,0xc000,v241\n" \
"s_waitcnt lgkmcnt(0)\ns_branch .Lattn\n.Lfin:\n" \
""
constexpr size_t WS_DBG = (size_t)51 << 20;
constexpr unsigned LDS_BYTES = 163840;
struct Args {
    const u16 *x, *ckv, *w_q, *w_uk, *w_uv, *w_o, *w_r, *wg, *wu, *wd, *cvec, *w_mod;
    char* ws; u16* out;
};
typedef const __attribute__((address_space(4))) Args* kargs_t;
#define STAMPD(i) do {} while (0)
#define C8(p, a) p #a "0", p #a "1", p #a "2", p #a "3", p #a "4", p #a "5", p #a "6", p #a "7", p #a "8", p #a "9"
#define C100(p, a) C8(p, a##0), C8(p, a##1), C8(p, a##2), C8(p, a##3), C8(p, a##4), C8(p, a##5), C8(p, a##6), C8(p, a##7), C8(p, a##8), C8(p, a##9)
#define C10_99(p) C8(p, 1), C8(p, 2), C8(p, 3), C8(p, 4), C8(p, 5), C8(p, 6), C8(p, 7), C8(p, 8), C8(p, 9)
#define C200_249(p) C8(p, 20), C8(p, 21), C8(p, 22), C8(p, 23), C8(p, 24)
#define BODY_CLOB "memory", "vcc", "scc", "s0", "s1", "s2", "s3", "s8", "s9", C8("s", 1), C8("s", 2), "s30", "s31", "s33", "s34", "s35", "s36", "s37", "s38", "s39", C8("s", 4), C8("s", 5), C8("s", 6), C8("s", 7), C8("s", 8), C8("s", 9), \
    C8("v", ), C10_99("v"), C100("v", 1), C200_249("v"), "v250", "v251", "v252", "v253", "v254", C8("a", ), C10_99("a"), C100("a", 1), C200_249("a"), "a250", "a251", "a252", "a253", "a254", "a255"

__global__ __launch_bounds__(256, 1) void k_all(const Args a_) {
    kargs_t ap = (kargs_t)__builtin_amdgcn_kernarg_segment_ptr(); unsigned bid = blockIdx.x, wid = rfl(threadIdx.x >> 6), sv_;
    asm volatile(BODY_ASM : "+{s[4:5]}"(ap), "+{s6}"(bid), "+{s7}"(wid), "={v255}"(sv_) :: BODY_CLOB);
    asm volatile(".Lcode_e:");
}
extern "C" void kernel_launch(void* const* d_in, const int* in_sizes, int n_in, void* d_out, int out_size, void* d_ws, size_t ws_size, hipStream_t stream) {
    Args a;
    a.x = (const u16*)d_in[0]; a.ckv = (const u16*)d_in[1]; a.w_q = (const u16*)d_in[2]; a.w_uk = (const u16*)d_in[3]; a.w_uv = (const u16*)d_in[4]; a.w_o = (const u16*)d_in[5];
    a.w_r = (const u16*)d_in[7]; a.wg = (const u16*)d_in[8]; a.wu = (const u16*)d_in[9]; a.wd = (const u16*)d_in[10]; a.cvec = (const u16*)d_in[11]; a.w_mod = (const u16*)d_in[12];
    a.ws = (char*)d_ws;
    a.out = (u16*)d_out;
    (void)hipFuncSetAttribute((const void*)k_all, hipFuncAttributeMaxDynamicSharedMemorySize, LDS_BYTES);
    k_all<<<256, 256, LDS_BYTES, stream>>>(a);
}
```

```cpp
#include <hip/hip_runtime.h>
#include <hip/hip_bf16.h>
#include <stdint.h>
typedef unsigned short u16;
#define DI __device__ __forceinline__
DI unsigned lane_id() { unsigned l; asm volatile("v_mbcnt_lo_u32_b32 %0, -1, 0\n\tv_mbcnt_hi_u32_b32 %0, -1, %0" : "=v"(l)); return l; }
DI unsigned rfl(unsigned x) { return __builtin_amdgcn_readfirstlane(x); }
#define BODY_ASM ".macro M a,b,c,d\nv_mfma_f32_16x16x32_bf16 \\a,\\b,\\c,\\d\n.endm\n" \
".Lcode_b:\ns_load_dwordx16 s[8:23],s[4:5],0x0\ns_load_dwordx8 s[24:31],s[4:5],0x40\ns_load_dwordx2 s[34:35],s[4:5],0x60\nv_mbcnt_lo_u32_b32 v249,-1,0\nv_mbcnt_hi_u32_b32 v249,-1,v249\ns_getpc_b64 s[56:57]\n.Lpc0:\n" \
"s_and_b32 s57,s57,0xffff\ns_mov_b32 s58,.Lcode_e-.Lpc0\ns_mov_b32 s59,0x20000\ns_lshr_b32 s70,s6,3\nv_and_b32 v251,1,v249\ns_lshl2_add_u32 s78,s70,s7\nv_lshlrev_b32 v251,14,v251\ns_lshl_b32 s78,s78,7\n" \
"buffer_load_dword v247,v251,s[56:59],s78 offen\ns_bfe_u32 s61,s6,0x20003\ns_lshl_b32 s64,s7,12\ns_and_b32 s60,s6,7\ns_lshr_b32 s62,s6,5\ns_lshl2_add_u32 s63,s60,s61\nv_lshlrev_b32 v240,4,v249\ns_cmp_lt_u32 s6,192\n" \
"s_cselect_b32 s70,s6,0\ns_mul_i32 s71,s70,171\ns_lshr_b32 s71,s71,13\ns_mul_i32 s72,s71,48\ns_sub_u32 s70,s70,s72\ns_lshl2_add_u32 s72,s71,s7\ns_lshl_b32 s72,s72,5\nv_lshrrev_b32 v0,3,v249\n" \
"v_lshl_add_u32 v1,v0,2,s72\nv_and_b32 v2,7,v249\nv_mul_u32_u24 v3,6144,v1\nv_lshl_add_u32 v3,v2,4,v3\ns_lshl_b32 s72,s70,7\ns_add_u32 s72,s72,3072\nv_add_u32 v3,s72,v3\nv_add_u32 v4,12288,v3\n" \
"v_lshlrev_b32 v1,1,v1\ns_mul_i32 s71,s71,12288\ns_lshl_b32 s70,s70,8\ns_add_u32 s79,s70,s71\ns_add_u32 s79,s79,786432\ns_cmp_eq_u32 s7,0\ns_cselect_b32 s75,0,0x8000000\ns_cmp_lt_u32 s6,192\n" \
"s_cselect_b32 s70,s75,0x8000000\ns_or_b32 s79,s79,s70\ns_lshl_b32 s70,s62,7\ns_xor_b32 s71,s63,12\ns_lshl3_add_u32 s71,s71,s70\ns_lshl_b32 s72,s63,3\ns_sub_u32 s72,s72,s70\ns_add_u32 s72,s72,1792\n" \
"s_cmp_lt_u32 s63,16\ns_cselect_b32 s71,s71,s72\ns_lshl_b32 s71,s71,10\ns_lshl_b32 s72,s7,7\ns_add_u32 s71,s71,s72\nv_lshlrev_b32 v5,10,v2\nv_lshl_add_u32 v5,v0,4,v5\nv_add_u32 v5,s71,v5\n" \
"v_bfe_u32 v6,v249,2,3\nv_lshrrev_b32 v7,5,v249\nv_and_b32 v8,48,v240\nv_lshlrev_b32 v6,13,v6\nv_lshl_or_b32 v7,v7,6,v8\nv_or_b32 v6,0x1000,v6\nv_or_b32 v243,v6,v7\nv_add_u32 v244,0xfffffc80,v243\n" \
"v_xor_b32 v6,32,v243\nv_add_u32 v245,63488,v6\nv_add_u32 v246,62592,v6\nv_lshrrev_b32 v6,4,v249\nv_xor_b32 v7,v6,v2\nv_lshlrev_b32 v7,4,v7\nv_lshl_or_b32 v224,v0,10,v7\nv_xor_b32 v7,64,v224\n" \
"v_add_u32 v225,7168,v7\nv_add_u32 v226,14336,v224\nv_add_u32 v227,21504,v7\ns_lshl_b32 s70,s62,1\ns_sub_u32 s71,s7,s70\ns_add_u32 s71,s71,30\ns_sub_u32 s72,s70,s7\ns_add_u32 s72,s72,3\n" \
"s_cmp_lt_u32 s7,2\ns_cselect_b32 s74,s71,s72\ns_lshl_b32 s66,s74,16\ns_mul_i32 s65,s7,24576\ns_lshl_b32 s68,s7,17\ns_lshl_b32 s70,s63,8\ns_add_u32 s68,s68,s70\ns_mov_b32 s38,2097152\n" \
"s_mov_b32 s39,0x20000\ns_mov_b32 s42,4198400\ns_mov_b32 s43,0x20000\ns_mov_b32 s54,71303168\ns_mov_b32 s55,0x20000\ns_mov_b32 s70,94208\ns_waitcnt lgkmcnt(0)\ns_mov_b64 s[52:53],s[34:35]\n" \
"s_mov_b64 s[36:37],s[10:11]\ns_sub_u32 s40,s14,0x1000\ns_subb_u32 s41,s15,0\nbuffer_load_dwordx2 v[230:231],off,s[52:55],s70 sc1\nglobal_load_dwordx2 v[10:11],v1,s[28:29]\nglobal_load_dwordx4 v[12:15],v3,s[30:31] offset:-3072\nglobal_load_dwordx4 v[16:19],v3,s[30:31] offset:3072\nglobal_load_dwordx4 v[20:23],v4,s[30:31] offset:-3072\n" \
"global_load_dwordx4 v[24:27],v4,s[30:31] offset:3072\nglobal_load_dwordx4 v[128:131],v5,s[8:9]\nglobal_load_dwordx4 v[132:135],v5,s[8:9] offset:512\ns_add_u32 m0,s64,98304\ns_add_u32 s69,s68,0\nbuffer_load_dwordx4 v243,s[40:43],s69 offen offset:0 lds\nbuffer_load_dwordx4 v244,s[40:43],s69 offen offset:1024 lds\nbuffer_load_dwordx4 v245,s[40:43],s69 offen offset:2048 lds\n" \
"buffer_load_dwordx4 v246,s[40:43],s69 offen offset:3072 lds\ns_add_u32 m0,s65,0\ns_add_u32 s67,s66,0\nbuffer_load_dwordx4 v224,s[36:39],s67 offen offset:0 lds\nbuffer_load_dwordx4 v225,s[36:39],s67 offen offset:1024 lds\nbuffer_load_dwordx4 v226,s[36:39],s67 offen offset:2048 lds\nbuffer_load_dwordx4 v227,s[36:39],s67 offen offset:3072 lds\ns_add_u32 m0,s65,4096\n" \
"s_add_u32 s67,s66,32768\nbuffer_load_dwordx4 v224,s[36:39],s67 offen offset:0 lds\nbuffer_load_dwordx4 v225,s[36:39],s67 offen offset:1024 lds\nbuffer_load_dwordx4 v226,s[36:39],s67 offen offset:2048 lds\nbuffer_load_dwordx4 v227,s[36:39],s67 offen offset:3072 lds\ns_lshl_b32 s70,s6,11\ns_cmp_lt_u32 s6,112\ns_cselect_b32 s70,s70,0x8000000\n" \
"s_lshl_b32 s71,s7,9\ns_add_u32 s70,s70,s71\ns_add_u32 s70,s70,98304\nv_mov_b32 v28,-1\nv_mov_b32 v29,-1\nv_lshlrev_b32 v30,3,v249\nbuffer_store_dwordx2 v[28:29],v30,s[52:55],s70 offen sc1\ns_lshl_b32 s70,s6,7\n" \
"s_cmp_lt_u32 s6,224\ns_cselect_b32 s71,65536,0x40000000\ns_cmp_lt_u32 s6,128\ns_cselect_b32 s72,556928,0x40000000\nv_mov_b32 v31,s71\nv_mov_b32 v32,s72\nv_cmp_lt_u32 vcc,31,v249\ns_or_b32 s70,s70,s75\n" \
"v_mov_b32 v33,0\nv_cndmask_b32 v31,v31,v32,vcc\nv_lshl_add_u32 v31,v249,2,v31\nbuffer_store_dword v33,v31,s[52:55],s70 offen sc1\nv_and_b32 v34,15,v249\nv_lshrrev_b32 v35,1,v34\nv_xor_b32 v35,v35,v6\nv_lshlrev_b32 v35,4,v35\n" \
"v_lshl_or_b32 v35,v34,7,v35\nv_add_u32 v228,s65,v35\nv_xor_b32 v229,64,v228\nv_bfe_u32 v34,v249,2,2\nv_and_b32 v35,3,v249\nv_lshlrev_b32 v35,3,v35\nv_lshl_or_b32 v35,v34,6,v35\nv_lshl_or_b32 v35,v6,11,v35\n" \
"v_and_b32 v34,16,v249\nv_lshl_or_b32 v35,v34,1,v35\nv_add_u32 v241,98304,v35\nv_xor_b32 v242,32,v241\ns_mov_b32 s73,0x400000\nv_mov_b32 v252,0xff800000\ns_mov_b32 s70,0x05040100\ns_mov_b32 s71,0x07060302\n" \
"s_waitcnt vmcnt(20)\nv_lshlrev_b32 v36,16,v10\nv_and_b32 v37,0xffff0000,v10\nv_lshlrev_b32 v38,16,v11\nv_and_b32 v39,0xffff0000,v11\nv_mul_f32 v40,0xbfb8aa3b,v36\nv_mul_f32 v41,0xbfb8aa3b,v37\nv_mul_f32 v42,0xbfb8aa3b,v38\n" \
"v_mul_f32 v43,0xbfb8aa3b,v39\nv_exp_f32 v40,v40\nv_exp_f32 v41,v41\nv_exp_f32 v42,v42\nv_exp_f32 v43,v43\nv_add_f32 v40,1.0,v40\nv_add_f32 v41,1.0,v41\nv_add_f32 v42,1.0,v42\n" \
"v_add_f32 v43,1.0,v43\nv_rcp_f32 v40,v40\nv_rcp_f32 v41,v41\nv_rcp_f32 v42,v42\nv_rcp_f32 v43,v43\nv_mul_f32 v36,v36,v40\nv_mul_f32 v37,v37,v41\nv_mul_f32 v38,v38,v42\n" \
"v_mul_f32 v39,v39,v43\nv_cvt_pk_bf16_f32 v36,v36,v37\nv_cvt_pk_bf16_f32 v37,v38,v39\ns_waitcnt vmcnt(18)\nv_perm_b32 v48,v16,v12,s70\nv_perm_b32 v49,v16,v12,s71\nv_perm_b32 v50,v17,v13,s70\nv_perm_b32 v51,v17,v13,s71\n" \
"v_perm_b32 v52,v18,v14,s70\nv_perm_b32 v53,v18,v14,s71\nv_perm_b32 v54,v19,v15,s70\nv_perm_b32 v55,v19,v15,s71\nv_dot2_f32_bf16 v40,v48,v36,0\nv_dot2_f32_bf16 v41,v49,v36,0\nv_dot2_f32_bf16 v42,v50,v36,0\nv_dot2_f32_bf16 v43,v51,v36,0\n" \
"v_dot2_f32_bf16 v44,v52,v36,0\nv_dot2_f32_bf16 v45,v53,v36,0\nv_dot2_f32_bf16 v46,v54,v36,0\nv_dot2_f32_bf16 v47,v55,v36,0\ns_waitcnt vmcnt(16)\nv_perm_b32 v48,v24,v20,s70\nv_perm_b32 v49,v24,v20,s71\nv_perm_b32 v50,v25,v21,s70\n" \
"v_perm_b32 v51,v25,v21,s71\nv_perm_b32 v52,v26,v22,s70\nv_perm_b32 v53,v26,v22,s71\nv_perm_b32 v54,v27,v23,s70\nv_perm_b32 v55,v27,v23,s71\nv_dot2_f32_bf16 v40,v48,v37,v40\nv_dot2_f32_bf16 v41,v49,v37,v41\nv_dot2_f32_bf16 v42,v50,v37,v42\n" \
"v_dot2_f32_bf16 v43,v51,v37,v43\nv_dot2_f32_bf16 v44,v52,v37,v44\nv_dot2_f32_bf16 v45,v53,v37,v45\nv_dot2_f32_bf16 v46,v54,v37,v46\nv_dot2_f32_bf16 v47,v55,v37,v47\ns_waitcnt vmcnt(15)\nv_lshlrev_b32 v136,16,v128\nv_and_b32 v137,0xffff0000,v128\n" \
"v_lshlrev_b32 v138,16,v129\nv_and_b32 v139,0xffff0000,v129\nv_lshlrev_b32 v140,16,v130\nv_and_b32 v141,0xffff0000,v130\nv_lshlrev_b32 v142,16,v131\nv_and_b32 v143,0xffff0000,v131\ns_waitcnt vmcnt(14)\nv_lshlrev_b32 v144,16,v132\n" \
"v_and_b32 v145,0xffff0000,v132\nv_lshlrev_b32 v146,16,v133\nv_and_b32 v147,0xffff0000,v133\nv_lshlrev_b32 v148,16,v134\nv_and_b32 v149,0xffff0000,v134\nv_lshlrev_b32 v150,16,v135\nv_and_b32 v151,0xffff0000,v135\nv_permlane32_swap_b32 v40,v44\n" \
"v_permlane32_swap_b32 v41,v45\nv_permlane32_swap_b32 v42,v46\nv_permlane32_swap_b32 v43,v47\nv_add_f32 v40,v40,v44\nv_add_f32 v41,v41,v45\nv_add_f32 v42,v42,v46\nv_add_f32 v43,v43,v47\nv_add_f32 v48,v136,v140\n" \
"v_add_f32 v49,v137,v141\nv_add_f32 v50,v138,v142\nv_add_f32 v51,v139,v143\nv_permlane16_swap_b32 v40,v42\nv_permlane16_swap_b32 v41,v43\nv_add_f32 v52,v144,v148\nv_add_f32 v53,v145,v149\nv_add_f32 v54,v146,v150\n" \
"v_add_f32 v55,v147,v151\nv_add_f32 v40,v40,v42\nv_add_f32 v41,v41,v43\nv_add_f32 v48,v48,v52\nv_add_f32 v49,v49,v53\nv_add_f32 v50,v50,v54\nv_add_f32 v51,v51,v55\nv_add_f32_dpp v40,v40,v40 row_ror:8 row_mask:0xf bank_mask:0xf\n" \
"v_add_f32_dpp v41,v41,v41 row_ror:8 row_mask:0xf bank_mask:0xf\nv_add_f32 v48,v48,v49\nv_add_f32 v50,v50,v51\nv_lshlrev_b32 v42,5,v2\nv_add_f32 v48,v48,v50\nv_lshl_or_b32 v42,v6,3,v42\ns_lshl_b32 s72,s7,8\ns_add_u32 s72,s72,147456\n" \
"v_add_u32 v42,s72,v42\nds_write_b64 v42,v[40:41]\nv_add_f32_dpp v48,v48,v48 row_ror:8 row_mask:0xf bank_mask:0xf\nv_mov_b32 v50,v48\ns_nop 1\nv_permlane16_swap_b32 v48,v50\nv_add_f32 v48,v48,v50\nv_mov_b32 v50,v48\n" \
"s_nop 1\nv_permlane32_swap_b32 v48,v50\nv_add_f32 v48,v48,v50\nv_mul_f32 v48,0x3c000000,v48\nv_sub_f32 v56,v136,v48\nv_sub_f32 v57,v137,v48\nv_sub_f32 v58,v138,v48\nv_sub_f32 v59,v139,v48\n" \
"v_sub_f32 v60,v140,v48\nv_sub_f32 v61,v141,v48\nv_sub_f32 v62,v142,v48\nv_sub_f32 v63,v143,v48\nv_sub_f32 v64,v144,v48\nv_sub_f32 v65,v145,v48\nv_sub_f32 v66,v146,v48\nv_sub_f32 v67,v147,v48\n" \
"v_sub_f32 v68,v148,v48\nv_sub_f32 v69,v149,v48\nv_sub_f32 v70,v150,v48\nv_sub_f32 v71,v151,v48\nv_mul_f32 v52,v56,v56\nv_mul_f32 v53,v57,v57\nv_mul_f32 v54,v58,v58\nv_mul_f32 v55,v59,v59\n" \
"v_fmac_f32 v52,v60,v60\nv_fmac_f32 v53,v61,v61\nv_fmac_f32 v54,v62,v62\nv_fmac_f32 v55,v63,v63\nv_fmac_f32 v52,v64,v64\nv_fmac_f32 v53,v65,v65\nv_fmac_f32 v54,v66,v66\nv_fmac_f32 v55,v67,v67\n" \
"v_fmac_f32 v52,v68,v68\nv_fmac_f32 v53,v69,v69\nv_fmac_f32 v54,v70,v70\nv_fmac_f32 v55,v71,v71\nv_add_f32 v52,v52,v53\nv_add_f32 v54,v54,v55\ns_lshl_b32 s72,s7,3\nv_add_f32 v49,v52,v54\n" \
"s_add_u32 s72,s72,148480\nv_lshl_add_u32 v42,v2,5,s72\nv_add_f32_dpp v49,v49,v49 row_ror:8 row_mask:0xf bank_mask:0xf\nv_mov_b32 v50,v49\ns_nop 1\nv_permlane16_swap_b32 v49,v50\nv_add_f32 v49,v49,v50\nv_mov_b32 v50,v49\n" \
"s_nop 1\nv_permlane32_swap_b32 v49,v50\nv_add_f32 v49,v49,v50\nds_write_b64 v42,v[48:49]\nv_lshlrev_b32 v251,5,v2\nv_add_u32 v251,148480,v251\nv_lshlrev_b32 v250,2,v249\nv_add_u32 v250,147456,v250\n" \
"s_add_u32 m0,s64,114688\ns_add_u32 s69,s68,524288\nbuffer_load_dwordx4 v243,s[40:43],s69 offen offset:0 lds\nbuffer_load_dwordx4 v244,s[40:43],s69 offen offset:1024 lds\nbuffer_load_dwordx4 v245,s[40:43],s69 offen offset:2048 lds\nbuffer_load_dwordx4 v246,s[40:43],s69 offen offset:3072 lds\ns_add_u32 m0,s65,8192\ns_add_u32 s67,s66,128\n" \
"buffer_load_dwordx4 v224,s[36:39],s67 offen offset:0 lds\nbuffer_load_dwordx4 v225,s[36:39],s67 offen offset:1024 lds\nbuffer_load_dwordx4 v226,s[36:39],s67 offen offset:2048 lds\nbuffer_load_dwordx4 v227,s[36:39],s67 offen offset:3072 lds\ns_add_u32 m0,s65,12288\ns_add_u32 s67,s66,32896\nbuffer_load_dwordx4 v224,s[36:39],s67 offen offset:0 lds\nbuffer_load_dwordx4 v225,s[36:39],s67 offen offset:1024 lds\n" \
"buffer_load_dwordx4 v226,s[36:39],s67 offen offset:2048 lds\nbuffer_load_dwordx4 v227,s[36:39],s67 offen offset:3072 lds\ns_waitcnt lgkmcnt(0)\ns_waitcnt vmcnt(14)\ns_barrier\ns_add_u32 m0,s64,131072\ns_add_u32 s69,s68,1048576\nds_read_b128 a[0:3],v228 offset:0\n" \
"ds_read_b128 a[4:7],v228 offset:2048\nds_read_b128 a[8:11],v228 offset:4096\nds_read_b128 a[12:15],v228 offset:6144\nds_read_b128 a[16:19],v229 offset:0\nds_read_b128 a[20:23],v229 offset:2048\nds_read_b128 a[24:27],v229 offset:4096\nds_read_b128 a[28:31],v229 offset:6144\nds_read_b32 v152,v250 offset:0\n" \
"ds_read_b32 v153,v250 offset:256\nds_read_b32 v154,v250 offset:512\nds_read_b32 v155,v250 offset:768\ns_waitcnt lgkmcnt(2)\nv_add_f32 v152,v152,v153\ns_waitcnt lgkmcnt(0)\nv_add_f32 v154,v154,v155\nv_lshlrev_b32 v153,2,v249\n" \
"v_add_f32 v152,v152,v154\nds_read_b64_tr_b16 v[192:193],v241 offset:0\nds_read_b64_tr_b16 v[194:195],v241 offset:256\nds_read_b64_tr_b16 v[196:197],v242 offset:0\nds_read_b64_tr_b16 v[198:199],v242 offset:256\nds_read_b64_tr_b16 v[200:201],v241 offset:512\nds_read_b64_tr_b16 v[202:203],v241 offset:768\nds_read_b64_tr_b16 v[204:205],v242 offset:512\n" \
"ds_read_b64_tr_b16 v[206:207],v242 offset:768\nds_read_b64_tr_b16 v[208:209],v241 offset:1024\nds_read_b64_tr_b16 v[210:211],v241 offset:1280\nds_read_b64_tr_b16 v[212:213],v242 offset:1024\nds_read_b64_tr_b16 v[214:215],v242 offset:1280\nds_read_b64_tr_b16 v[216:217],v241 offset:1536\nds_read_b64_tr_b16 v[218:219],v241 offset:1792\nds_read_b64_tr_b16 v[220:221],v242 offset:1536\n" \
"ds_read_b64_tr_b16 v[222:223],v242 offset:1792\nbuffer_store_dword v152,v153,s[52:55],s79 offen sc1\nds_read_b128 v[156:159],v251\nds_read_b128 v[160:163],v251 offset:16\ns_waitcnt lgkmcnt(15)\nM v[0:3],v[192:195],a[0:3],0\nbuffer_load_dwordx4 v243,s[40:43],s69 offen offset:0 lds\nM v[4:7],v[192:195],a[4:7],0\n" \
"v_bfe_u32 v253,v249,1,2\ns_lshl2_add_u32 s76,s62,s7\nv_and_b32 v248,1,v249\nv_lshlrev_b32 v253,13,v253\ns_mul_i32 s76,s76,32768\ns_lshl_b32 s77,s63,8\nM v[64:67],v[192:195],a[8:11],0\nv_lshl_or_b32 v253,v248,7,v253\n" \
"s_add_u32 s76,s76,s77\nv_and_b32 v254,7,v249\nv_add_u32 v253,s76,v253\nM v[68:71],v[192:195],a[12:15],0\nds_read_b64_tr_b16 v[192:193],v241 offset:8192\nds_read_b64_tr_b16 v[194:195],v241 offset:8448\ns_waitcnt lgkmcnt(15)\nM v[8:11],v[196:199],a[0:3],0\n" \
"global_load_dword v247,v253,s[16:17]\nM v[12:15],v[196:199],a[4:7],0\nbuffer_load_dwordx4 v244,s[40:43],s69 offen offset:1024 lds\nM v[72:75],v[196:199],a[8:11],0\ns_lshl3_add_u32 s72,s6,s75\nv_add_u32 v253,0x100000,v253\nM v[76:79],v[196:199],a[12:15],0\nds_read_b64_tr_b16 v[196:197],v242 offset:8192\n" \
"ds_read_b64_tr_b16 v[198:199],v242 offset:8448\ns_waitcnt lgkmcnt(4)\nM v[16:19],v[200:203],a[0:3],0\nv_readfirstlane_b32 s92,v230\nv_readfirstlane_b32 s93,v231\nM v[20:23],v[200:203],a[4:7],0\ns_add_u32 s70,s92,1\ns_addc_u32 s71,s93,0\n" \
"M v[80:83],v[200:203],a[8:11],0\nbuffer_load_dwordx4 v245,s[40:43],s69 offen offset:2048 lds\nM v[84:87],v[200:203],a[12:15],0\nds_read_b64_tr_b16 v[200:201],v241 offset:8704\nds_read_b64_tr_b16 v[202:203],v241 offset:8960\nM v[24:27],v[204:207],a[0:3],0\nv_add_f32 v164,v156,v158\nv_add_f32 v165,v160,v162\n" \
"M v[28:31],v[204:207],a[4:7],0\nv_add_f32 v164,v164,v165\nv_and_b32 v165,7,v249\nM v[88:91],v[204:207],a[8:11],0\ns_add_u32 s92,s92,0x7f4a7c15\ns_addc_u32 s93,s93,0x9e3779b9\nM v[92:95],v[204:207],a[12:15],0\nbuffer_load_dwordx4 v246,s[40:43],s69 offen offset:3072 lds\n" \
"ds_read_b64_tr_b16 v[204:205],v242 offset:8704\nds_read_b64_tr_b16 v[206:207],v242 offset:8960\nM v[32:35],v[208:211],a[0:3],0\nv_mov_b32 v230,s92\nv_mov_b32 v231,s93\nM v[36:39],v[208:211],a[4:7],0\ns_add_u32 m0,s65,16384\ns_add_u32 s67,s66,256\n" \
"M v[96:99],v[208:211],a[8:11],0\nv_lshlrev_b32 v165,11,v165\ns_add_u32 s72,s72,573440\nM v[100:103],v[208:211],a[12:15],0\nds_read_b64_tr_b16 v[208:209],v241 offset:9216\nds_read_b64_tr_b16 v[210:211],v241 offset:9472\nM v[40:43],v[212:215],a[0:3],0\nbuffer_load_dwordx4 v224,s[36:39],s67 offen offset:0 lds\n" \
"M v[44:47],v[212:215],a[4:7],0\ns_lshl2_add_u32 s80,s60,s7\ns_lshl_b32 s80,s80,9\nM v[104:107],v[212:215],a[8:11],0\ns_add_u32 s80,s80,573440\nv_mul_f32 v164,0x3e800000,v164\nM v[108:111],v[212:215],a[12:15],0\nds_read_b64_tr_b16 v[212:213],v242 offset:9216\n" \
"ds_read_b64_tr_b16 v[214:215],v242 offset:9472\nM v[48:51],v[216:219],a[0:3],0\nv_sub_f32 v156,v156,v164\nv_sub_f32 v158,v158,v164\nM v[52:55],v[216:219],a[4:7],0\nbuffer_load_dwordx4 v225,s[36:39],s67 offen offset:1024 lds\nM v[112:115],v[216:219],a[8:11],0\nv_mul_f32 v156,v156,v156\n" \
"v_sub_f32 v160,v160,v164\nM v[116:119],v[216:219],a[12:15],0\nds_read_b64_tr_b16 v[216:217],v241 offset:9728\nds_read_b64_tr_b16 v[218:219],v241 offset:9984\nM v[56:59],v[220:223],a[0:3],0\nv_fmac_f32 v156,v158,v158\nv_add_f32 v157,v157,v159\nM v[60:63],v[220:223],a[4:7],0\n" \
"v_add_f32 v161,v161,v163\nv_sub_f32 v162,v162,v164\nM v[120:123],v[220:223],a[8:11],0\nbuffer_load_dwordx4 v226,s[36:39],s67 offen offset:2048 lds\nM v[124:127],v[220:223],a[12:15],0\nds_read_b64_tr_b16 v[220:221],v242 offset:9728\nds_read_b64_tr_b16 v[222:223],v242 offset:9984\ns_waitcnt lgkmcnt(6)\n" \
"M v[0:3],v[192:195],a[16:19],v[0:3]\nv_fmac_f32 v156,v160,v160\nv_add_f32 v157,v157,v161\nM v[4:7],v[192:195],a[20:23],v[4:7]\nv_fmac_f32 v156,v162,v162\nv_fmac_f32 v157,0x43000000,v156\nM v[64:67],v[192:195],a[24:27],v[64:67]\nv_mul_f32 v157,0x3b000000,v157\n" \
"v_cvt_pk_bf16_f32 v157,0,v157\nM v[68:71],v[192:195],a[28:31],v[68:71]\nbuffer_load_dwordx4 v227,s[36:39],s67 offen offset:3072 lds\nM v[8:11],v[196:199],a[16:19],v[8:11]\nv_add_f32 v157,0x3727c5ac,v157\nv_cvt_pk_bf16_f32 v157,0,v157\nM v[12:15],v[196:199],a[20:23],v[12:15]\ns_add_u32 m0,s65,20480\n" \
"s_add_u32 s67,s66,33024\nM v[72:75],v[196:199],a[24:27],v[72:75]\nv_sqrt_f32 v157,v157\nM v[76:79],v[196:199],a[28:31],v[76:79]\nv_mov_b32 v232,s70\nv_mov_b32 v233,s71\nM v[16:19],v[200:203],a[16:19],v[16:19]\nbuffer_load_dwordx4 v224,s[36:39],s67 offen offset:0 lds\n" \
"M v[20:23],v[200:203],a[20:23],v[20:23]\ns_lshr_b32 s70,s64,2\nv_cvt_pk_bf16_f32 v164,0,v164\nM v[80:83],v[200:203],a[24:27],v[80:83]\nv_cvt_pk_bf16_f32 v157,0,v157\ns_add_u32 s71,s70,786432\nM v[84:87],v[200:203],a[28:31],v[84:87]\nv_rcp_f32 v157,v157\n" \
"M v[24:27],v[204:207],a[16:19],v[24:27]\nglobal_load_dword v247,v253,s[16:17]\nM v[28:31],v[204:207],a[20:23],v[28:31]\nbuffer_load_dwordx4 v225,s[36:39],s67 offen offset:1024 lds\nM v[88:91],v[204:207],a[24:27],v[88:91]\ns_waitcnt vmcnt(12) lgkmcnt(0)\ns_barrier\nds_read_b128 a[32:35],v228 offset:8192\n" \
"ds_read_b128 a[36:39],v228 offset:10240\nds_read_b128 a[40:43],v228 offset:12288\nds_read_b128 a[44:47],v228 offset:14336\nds_read_b128 a[48:51],v229 offset:8192\nds_read_b128 a[52:55],v229 offset:10240\nds_read_b128 a[56:59],v229 offset:12288\nds_read_b128 a[60:63],v229 offset:14336\nds_read_b64_tr_b16 v[192:193],v241 offset:16384\n" \
"ds_read_b64_tr_b16 v[194:195],v241 offset:16640\nds_read_b64_tr_b16 v[196:197],v242 offset:16384\nds_read_b64_tr_b16 v[198:199],v242 offset:16640\nds_read_b64_tr_b16 v[200:201],v241 offset:16896\nds_read_b64_tr_b16 v[202:203],v241 offset:17152\nM v[92:95],v[204:207],a[28:31],v[92:95]\nds_read_b64_tr_b16 v[204:205],v242 offset:16896\nds_read_b64_tr_b16 v[206:207],v242 offset:17152\n" \
"M v[32:35],v[208:211],a[16:19],v[32:35]\nbuffer_store_dwordx2 v[230:231],v165,s[52:55],s72 offen sc1\nM v[36:39],v[208:211],a[20:23],v[36:39]\nv_add_u32 v253,0x100000,v253\nv_lshlrev_b32 v165,3,v249\nM v[96:99],v[208:211],a[24:27],v[96:99]\nbuffer_load_dwordx4 v226,s[36:39],s67 offen offset:2048 lds\nM v[100:103],v[208:211],a[28:31],v[100:103]\n" \
"ds_read_b64_tr_b16 v[208:209],v241 offset:17408\nds_read_b64_tr_b16 v[210:211],v241 offset:17664\nM v[40:43],v[212:215],a[16:19],v[40:43]\nv_sub_f32 v136,v136,v164\nv_sub_f32 v137,v137,v164\nM v[44:47],v[212:215],a[20:23],v[44:47]\nv_sub_f32 v138,v138,v164\nv_sub_f32 v139,v139,v164\n" \
"M v[104:107],v[212:215],a[24:27],v[104:107]\nv_sub_f32 v140,v140,v164\nv_sub_f32 v141,v141,v164\nM v[108:111],v[212:215],a[28:31],v[108:111]\nbuffer_load_dwordx4 v227,s[36:39],s67 offen offset:3072 lds\nds_read_b64_tr_b16 v[212:213],v242 offset:17408\nds_read_b64_tr_b16 v[214:215],v242 offset:17664\nM v[48:51],v[216:219],a[16:19],v[48:51]\n" \
"v_sub_f32 v142,v142,v164\nv_sub_f32 v143,v143,v164\nM v[52:55],v[216:219],a[20:23],v[52:55]\ns_add_u32 m0,s65,0\ns_add_u32 s67,s66,384\nM v[112:115],v[216:219],a[24:27],v[112:115]\nv_sub_f32 v144,v144,v164\nv_sub_f32 v145,v145,v164\n" \
"M v[116:119],v[216:219],a[28:31],v[116:119]\nds_read_b64_tr_b16 v[216:217],v241 offset:17920\nds_read_b64_tr_b16 v[218:219],v241 offset:18176\nM v[56:59],v[220:223],a[16:19],v[56:59]\nbuffer_load_dwordx4 v224,s[36:39],s67 offen offset:0 lds\nM v[60:63],v[220:223],a[20:23],v[60:63]\nv_sub_f32 v146,v146,v164\nv_sub_f32 v147,v147,v164\n" \
"M v[120:123],v[220:223],a[24:27],v[120:123]\nv_sub_f32 v148,v148,v164\nv_sub_f32 v149,v149,v164\nM v[124:127],v[220:223],a[28:31],v[124:127]\nds_read_b64_tr_b16 v[220:221],v242 offset:17920\nds_read_b64_tr_b16 v[222:223],v242 offset:18176\ns_waitcnt lgkmcnt(6)\nM v[0:3],v[192:195],a[32:35],v[0:3]\n" \
"v_sub_f32 v150,v150,v164\nv_sub_f32 v151,v151,v164\nM v[4:7],v[192:195],a[36:39],v[4:7]\nbuffer_load_dwordx4 v225,s[36:39],s67 offen offset:1024 lds\nM v[64:67],v[192:195],a[40:43],v[64:67]\nv_cvt_pk_bf16_f32 v136,0,v136\nv_cvt_pk_bf16_f32 v137,0,v137\nM v[68:71],v[192:195],a[44:47],v[68:71]\n" \
"ds_read_b64_tr_b16 v[192:193],v241 offset:24576\nds_read_b64_tr_b16 v[194:195],v241 offset:24832\nM v[8:11],v[196:199],a[32:35],v[8:11]\nv_cvt_pk_bf16_f32 v138,0,v138\nv_cvt_pk_bf16_f32 v139,0,v139\nM v[12:15],v[196:199],a[36:39],v[12:15]\nv_cvt_pk_bf16_f32 v140,0,v140\nv_cvt_pk_bf16_f32 v141,0,v141\n" \
"M v[72:75],v[196:199],a[40:43],v[72:75]\nbuffer_load_dwordx4 v226,s[36:39],s67 offen offset:2048 lds\nM v[76:79],v[196:199],a[44:47],v[76:79]\nds_read_b64_tr_b16 v[196:197],v242 offset:24576\nds_read_b64_tr_b16 v[198:199],v242 offset:24832\nM v[16:19],v[200:203],a[32:35],v[16:19]\nv_cvt_pk_bf16_f32 v142,0,v142\nv_cvt_pk_bf16_f32 v143,0,v143\n" \
"M v[20:23],v[200:203],a[36:39],v[20:23]\nv_cvt_pk_bf16_f32 v144,0,v144\nv_cvt_pk_bf16_f32 v145,0,v145\nM v[80:83],v[200:203],a[40:43],v[80:83]\nv_cvt_pk_bf16_f32 v146,0,v146\nv_cvt_pk_bf16_f32 v147,0,v147\nM v[84:87],v[200:203],a[44:47],v[84:87]\nbuffer_load_dwordx4 v227,s[36:39],s67 offen offset:3072 lds\n" \
"ds_read_b64_tr_b16 v[200:201],v241 offset:25088\nds_read_b64_tr_b16 v[202:203],v241 offset:25344\nM v[24:27],v[204:207],a[32:35],v[24:27]\nv_cvt_pk_bf16_f32 v148,0,v148\nv_cvt_pk_bf16_f32 v149,0,v149\nM v[28:31],v[204:207],a[36:39],v[28:31]\ns_add_u32 m0,s65,4096\ns_add_u32 s67,s66,33152\n" \
"M v[88:91],v[204:207],a[40:43],v[88:91]\nv_cvt_pk_bf16_f32 v150,0,v150\nv_cvt_pk_bf16_f32 v151,0,v151\nM v[92:95],v[204:207],a[44:47],v[92:95]\nds_read_b64_tr_b16 v[204:205],v242 offset:25088\nds_read_b64_tr_b16 v[206:207],v242 offset:25344\nM v[32:35],v[208:211],a[32:35],v[32:35]\nbuffer_load_dwordx4 v224,s[36:39],s67 offen offset:0 lds\n" \
"M v[36:39],v[208:211],a[36:39],v[36:39]\nv_mul_f32 v136,v136,v157\nv_mul_f32 v137,v137,v157\nM v[96:99],v[208:211],a[40:43],v[96:99]\nv_mul_f32 v138,v138,v157\nv_mul_f32 v139,v139,v157\nM v[100:103],v[208:211],a[44:47],v[100:103]\nds_read_b64_tr_b16 v[208:209],v241 offset:25600\n" \
"ds_read_b64_tr_b16 v[210:211],v241 offset:25856\ns_waitcnt lgkmcnt(6)\nM v[40:43],v[212:215],a[32:35],v[40:43]\nglobal_load_dword v247,v253,s[16:17]\nM v[44:47],v[212:215],a[36:39],v[44:47]\nbuffer_load_dwordx4 v225,s[36:39],s67 offen offset:1024 lds\nM v[104:107],v[212:215],a[40:43],v[104:107]\nv_add_u32 v253,0x100000,v253\n" \
"v_mul_f32 v140,v140,v157\nM v[108:111],v[212:215],a[44:47],v[108:111]\nds_read_b64_tr_b16 v[212:213],v242 offset:25600\nds_read_b64_tr_b16 v[214:215],v242 offset:25856\nM v[48:51],v[216:219],a[32:35],v[48:51]\nv_mul_f32 v141,v141,v157\nv_mul_f32 v142,v142,v157\nM v[52:55],v[216:219],a[36:39],v[52:55]\n" \
"v_mul_f32 v143,v143,v157\nv_mul_f32 v144,v144,v157\nM v[112:115],v[216:219],a[40:43],v[112:115]\nbuffer_load_dwordx4 v226,s[36:39],s67 offen offset:2048 lds\nM v[116:119],v[216:219],a[44:47],v[116:119]\nds_read_b64_tr_b16 v[216:217],v241 offset:26112\nds_read_b64_tr_b16 v[218:219],v241 offset:26368\nM v[56:59],v[220:223],a[32:35],v[56:59]\n" \
"v_mul_f32 v145,v145,v157\nv_mul_f32 v146,v146,v157\nM v[60:63],v[220:223],a[36:39],v[60:63]\nv_mul_f32 v147,v147,v157\nv_mul_f32 v148,v148,v157\nM v[120:123],v[220:223],a[40:43],v[120:123]\nv_mul_f32 v149,v149,v157\nv_mul_f32 v150,v150,v157\n" \
"M v[124:127],v[220:223],a[44:47],v[124:127]\nbuffer_load_dwordx4 v227,s[36:39],s67 offen offset:3072 lds\nds_read_b64_tr_b16 v[220:221],v242 offset:26112\nds_read_b64_tr_b16 v[222:223],v242 offset:26368\nM v[0:3],v[192:195],a[48:51],v[0:3]\nv_mul_f32 v151,v151,v157\nv_lshlrev_b32 v254,14,v254\nM v[4:7],v[192:195],a[52:55],v[4:7]\n" \
"s_add_u32 m0,s64,147456\ns_add_u32 s69,s68,1572864\nM v[64:67],v[192:195],a[56:59],v[64:67]\ns_add_u32 s78,s78,32768\nv_and_b32 v169,3,v249\nM v[68:71],v[192:195],a[60:63],v[68:71]\nv_mul_u32_u24 v169,12288,v169\nv_and_b32 v170,28,v249\n" \
"M v[8:11],v[196:199],a[48:51],v[8:11]\nbuffer_load_dwordx4 v243,s[40:43],s69 offen offset:0 lds\nM v[12:15],v[196:199],a[52:55],v[12:15]\nv_add_u32 v169,v169,v170\nv_lshrrev_b32 v251,3,v249\nM v[72:75],v[196:199],a[56:59],v[72:75]\ns_cmp_eq_u32 s6,0\ns_cselect_b32 s72,s75,0x8000000\n" \
"M v[76:79],v[196:199],a[60:63],v[76:79]\ns_add_u32 s72,s72,94208\ns_waitcnt lgkmcnt(2)\nM v[16:19],v[200:203],a[48:51],v[16:19]\nv_lshlrev_b32 v170,3,v170\nv_cvt_pk_bf16_f32 v144,0,v144\nM v[20:23],v[200:203],a[52:55],v[20:23]\nbuffer_load_dwordx4 v244,s[40:43],s69 offen offset:1024 lds\n" \
"M v[80:83],v[200:203],a[56:59],v[80:83]\nv_cvt_pk_bf16_f32 v145,0,v145\nv_cvt_pk_bf16_f32 v146,0,v146\nM v[84:87],v[200:203],a[60:63],v[84:87]\nv_cvt_pk_bf16_f32 v147,0,v147\nv_cvt_pk_bf16_f32 v136,0,v136\nM v[24:27],v[204:207],a[48:51],v[24:27]\nv_cvt_pk_bf16_f32 v137,0,v137\n" \
"v_cvt_pk_bf16_f32 v138,0,v138\nM v[28:31],v[204:207],a[52:55],v[28:31]\nv_cvt_pk_bf16_f32 v139,0,v139\nv_cvt_pk_bf16_f32 v140,0,v140\nM v[88:91],v[204:207],a[56:59],v[88:91]\nbuffer_load_dwordx4 v245,s[40:43],s69 offen offset:2048 lds\ns_waitcnt vmcnt(12) lgkmcnt(0)\ns_barrier\n" \
"ds_read_b128 a[64:67],v228 offset:16384\nds_read_b128 a[68:71],v228 offset:18432\nds_read_b128 a[72:75],v228 offset:20480\nds_read_b128 a[76:79],v228 offset:22528\nds_read_b128 a[80:83],v229 offset:16384\nds_read_b128 a[84:87],v229 offset:18432\nds_read_b128 a[88:91],v229 offset:20480\nds_read_b128 a[92:95],v229 offset:22528\n" \
"M v[92:95],v[204:207],a[60:63],v[92:95]\nv_cvt_pk_bf16_f32 v141,0,v141\nv_cvt_pk_bf16_f32 v142,0,v142\nM v[32:35],v[208:211],a[48:51],v[32:35]\nv_cvt_pk_bf16_f32 v143,0,v143\nv_cvt_pk_bf16_f32 v148,0,v148\nM v[36:39],v[208:211],a[52:55],v[36:39]\nv_cvt_pk_bf16_f32 v149,0,v149\n" \
"v_bfe_u32 v250,v249,3,2\nM v[96:99],v[208:211],a[56:59],v[96:99]\nv_cvt_pk_bf16_f32 v150,0,v150\nv_cvt_pk_bf16_f32 v151,0,v151\nM v[100:103],v[208:211],a[60:63],v[100:103]\nbuffer_load_dwordx4 v246,s[40:43],s69 offen offset:3072 lds\nM v[40:43],v[212:215],a[48:51],v[40:43]\ns_mov_b32 s98,0x4000\n" \
"s_mov_b32 s0,28672\nM v[44:47],v[212:215],a[52:55],v[44:47]\ns_add_u32 m0,s65,8192\ns_add_u32 s67,s66,512\nds_read_b64_tr_b16 v[192:193],v241 offset:32768\nds_read_b64_tr_b16 v[194:195],v241 offset:33024\nM v[104:107],v[212:215],a[56:59],v[104:107]\nds_read_b64_tr_b16 v[196:197],v242 offset:32768\n" \
"s_add_u32 s10,s61,1\nM v[108:111],v[212:215],a[60:63],v[108:111]\nds_read_b64_tr_b16 v[198:199],v242 offset:33024\nds_read_b64_tr_b16 v[200:201],v241 offset:33280\nM v[48:51],v[216:219],a[48:51],v[48:51]\nbuffer_load_dwordx4 v224,s[36:39],s67 offen offset:0 lds\nM v[52:55],v[216:219],a[52:55],v[52:55]\nds_read_b64_tr_b16 v[202:203],v241 offset:33536\n" \
"ds_read_b64_tr_b16 v[204:205],v242 offset:33280\nM v[112:115],v[216:219],a[56:59],v[112:115]\nds_read_b64_tr_b16 v[206:207],v242 offset:33536\nds_read_b64_tr_b16 v[208:209],v241 offset:33792\nM v[116:119],v[216:219],a[60:63],v[116:119]\nds_read_b64_tr_b16 v[210:211],v241 offset:34048\nds_read_b64_tr_b16 v[212:213],v242 offset:33792\nM v[56:59],v[220:223],a[48:51],v[56:59]\n" \
"global_load_dword v247,v253,s[16:17]\nM v[60:63],v[220:223],a[52:55],v[60:63]\nbuffer_load_dwordx4 v225,s[36:39],s67 offen offset:1024 lds\nM v[120:123],v[220:223],a[56:59],v[120:123]\nds_read_b64_tr_b16 v[214:215],v242 offset:34048\nds_read_b64_tr_b16 v[216:217],v241 offset:34304\nM v[124:127],v[220:223],a[60:63],v[124:127]\nds_read_b64_tr_b16 v[218:219],v241 offset:34560\n" \
"s_waitcnt lgkmcnt(12)\nM v[0:3],v[192:195],a[64:67],v[0:3]\nds_read_b64_tr_b16 v[220:221],v242 offset:34304\nds_read_b64_tr_b16 v[222:223],v242 offset:34560\nM v[4:7],v[192:195],a[68:71],v[4:7]\nv_add_u32 v253,0xffd00000,v253\ns_add_u32 s11,s61,2\nM v[64:67],v[192:195],a[72:75],v[64:67]\n" \
"buffer_load_dwordx4 v226,s[36:39],s67 offen offset:2048 lds\nM v[68:71],v[192:195],a[76:79],v[68:71]\nds_read_b64_tr_b16 v[192:193],v241 offset:40960\ns_waitcnt lgkmcnt(11)\nM v[8:11],v[196:199],a[64:67],v[8:11]\nds_read_b64_tr_b16 v[194:195],v241 offset:41216\ns_add_u32 s14,s61,3\nM v[12:15],v[196:199],a[68:71],v[12:15]\n" \
"s_and_b32 s10,s10,3\ns_and_b32 s11,s11,3\nM v[72:75],v[196:199],a[72:75],v[72:75]\ns_and_b32 s14,s14,3\ns_mov_b32 s47,0x20000\nM v[76:79],v[196:199],a[76:79],v[76:79]\nbuffer_load_dwordx4 v227,s[36:39],s67 offen offset:3072 lds\nM v[16:19],v[200:203],a[64:67],v[16:19]\n" \
"ds_read_b64_tr_b16 v[196:197],v242 offset:40960\nds_read_b64_tr_b16 v[198:199],v242 offset:41216\nM v[20:23],v[200:203],a[68:71],v[20:23]\ns_add_u32 m0,s65,12288\ns_add_u32 s67,s66,33280\nM v[80:83],v[200:203],a[72:75],v[80:83]\ns_mov_b32 s51,0x20000\ns_mov_b32 s94,0xc000\n" \
"M v[84:87],v[200:203],a[76:79],v[84:87]\nds_read_b64_tr_b16 v[200:201],v241 offset:41472\ns_waitcnt lgkmcnt(5)\nM v[24:27],v[204:207],a[64:67],v[24:27]\nbuffer_load_dwordx4 v224,s[36:39],s67 offen offset:0 lds\nM v[28:31],v[204:207],a[68:71],v[28:31]\nds_read_b64_tr_b16 v[202:203],v241 offset:41728\ns_lshl_b32 s10,s10,13\n" \
"M v[88:91],v[204:207],a[72:75],v[88:91]\ns_lshl_b32 s11,s11,13\ns_lshl_b32 s14,s14,13\nM v[92:95],v[204:207],a[76:79],v[92:95]\nds_read_b64_tr_b16 v[204:205],v242 offset:41472\nds_read_b64_tr_b16 v[206:207],v242 offset:41728\nM v[32:35],v[208:211],a[64:67],v[32:35]\ns_mov_b32 s82,0xf0000\n" \
"s_mov_b32 s83,0xfff00ff\nM v[36:39],v[208:211],a[68:71],v[36:39]\nbuffer_load_dwordx4 v225,s[36:39],s67 offen offset:1024 lds\nM v[96:99],v[208:211],a[72:75],v[96:99]\ns_mov_b32 s84,0x1f0001\ns_mov_b32 s85,0x1fff01ff\nM v[100:103],v[208:211],a[76:79],v[100:103]\nds_read_b64_tr_b16 v[208:209],v241 offset:41984\n" \
"ds_read_b64_tr_b16 v[210:211],v241 offset:42240\nM v[40:43],v[212:215],a[64:67],v[40:43]\ns_mov_b32 s86,0x3f0003\ns_mov_b32 s87,0x3fff03ff\nM v[44:47],v[212:215],a[68:71],v[44:47]\ns_mov_b32 s88,0x7f0007\ns_mov_b32 s89,0x7fff07ff\nM v[104:107],v[212:215],a[72:75],v[104:107]\n" \
"buffer_load_dwordx4 v226,s[36:39],s67 offen offset:2048 lds\nM v[108:111],v[212:215],a[76:79],v[108:111]\nds_read_b64_tr_b16 v[212:213],v242 offset:41984\nds_read_b64_tr_b16 v[214:215],v242 offset:42240\nM v[48:51],v[216:219],a[64:67],v[48:51]\ns_mov_b64 s[90:91],-1\ns_mov_b32 s28,0x5f800000\nM v[52:55],v[216:219],a[68:71],v[52:55]\n" \
"s_mov_b32 s99,0\nM v[112:115],v[216:219],a[72:75],v[112:115]\nM v[116:119],v[216:219],a[76:79],v[116:119]\nbuffer_load_dwordx4 v227,s[36:39],s67 offen offset:3072 lds\nM v[56:59],v[220:223],a[64:67],v[56:59]\nds_read_b64_tr_b16 v[216:217],v241 offset:42496\nds_read_b64_tr_b16 v[218:219],v241 offset:42752\nM v[60:63],v[220:223],a[68:71],v[60:63]\n" \
"s_add_u32 m0,s64,98304\ns_add_u32 s69,s68,2097152\nM v[120:123],v[220:223],a[72:75],v[120:123]\nM v[124:127],v[220:223],a[76:79],v[124:127]\nds_read_b64_tr_b16 v[220:221],v242 offset:42496\nds_read_b64_tr_b16 v[222:223],v242 offset:42752\ns_waitcnt lgkmcnt(6)\nM v[0:3],v[192:195],a[80:83],v[0:3]\n" \
"buffer_load_dwordx4 v243,s[40:43],s69 offen offset:0 lds\nM v[4:7],v[192:195],a[84:87],v[4:7]\nM v[64:67],v[192:195],a[88:91],v[64:67]\nM v[68:71],v[192:195],a[92:95],v[68:71]\nM v[8:11],v[196:199],a[80:83],v[8:11]\nM v[12:15],v[196:199],a[84:87],v[12:15]\nbuffer_load_dwordx4 v244,s[40:43],s69 offen offset:1024 lds\nM v[72:75],v[196:199],a[88:91],v[72:75]\n" \
"M v[76:79],v[196:199],a[92:95],v[76:79]\nM v[16:19],v[200:203],a[80:83],v[16:19]\nM v[20:23],v[200:203],a[84:87],v[20:23]\nM v[80:83],v[200:203],a[88:91],v[80:83]\nbuffer_load_dwordx4 v245,s[40:43],s69 offen offset:2048 lds\nM v[84:87],v[200:203],a[92:95],v[84:87]\nM v[24:27],v[204:207],a[80:83],v[24:27]\nM v[28:31],v[204:207],a[84:87],v[28:31]\n" \
"M v[88:91],v[204:207],a[88:91],v[88:91]\ns_waitcnt vmcnt(12) lgkmcnt(0)\ns_barrier\nds_read_b128 a[96:99],v228 offset:0\nds_read_b128 a[100:103],v228 offset:2048\nds_read_b128 a[104:107],v228 offset:4096\nds_read_b128 a[108:111],v228 offset:6144\nds_read_b128 a[112:115],v229 offset:0\n" \
"ds_read_b128 a[116:119],v229 offset:2048\nds_read_b128 a[120:123],v229 offset:4096\nds_read_b128 a[124:127],v229 offset:6144\nM v[92:95],v[204:207],a[92:95],v[92:95]\nbuffer_load_dwordx4 v246,s[40:43],s69 offen offset:3072 lds\nM v[32:35],v[208:211],a[80:83],v[32:35]\nM v[36:39],v[208:211],a[84:87],v[36:39]\ns_add_u32 m0,s65,16384\n" \
"s_add_u32 s67,s66,640\nM v[96:99],v[208:211],a[88:91],v[96:99]\nM v[100:103],v[208:211],a[92:95],v[100:103]\nM v[40:43],v[212:215],a[80:83],v[40:43]\nbuffer_load_dwordx4 v224,s[36:39],s67 offen offset:0 lds\nM v[44:47],v[212:215],a[84:87],v[44:47]\nds_read_b64_tr_b16 v[192:193],v241 offset:49152\nds_read_b64_tr_b16 v[194:195],v241 offset:49408\n" \
"M v[104:107],v[212:215],a[88:91],v[104:107]\nds_read_b64_tr_b16 v[196:197],v242 offset:49152\nM v[108:111],v[212:215],a[92:95],v[108:111]\nds_read_b64_tr_b16 v[198:199],v242 offset:49408\nds_read_b64_tr_b16 v[200:201],v241 offset:49664\nM v[48:51],v[216:219],a[80:83],v[48:51]\nds_read_b64_tr_b16 v[202:203],v241 offset:49920\nds_read_b64_tr_b16 v[204:205],v242 offset:49664\n" \
"M v[52:55],v[216:219],a[84:87],v[52:55]\nbuffer_load_dwordx4 v225,s[36:39],s67 offen offset:1024 lds\nM v[112:115],v[216:219],a[88:91],v[112:115]\nds_read_b64_tr_b16 v[206:207],v242 offset:49920\nds_read_b64_tr_b16 v[208:209],v241 offset:50176\nM v[116:119],v[216:219],a[92:95],v[116:119]\nds_read_b64_tr_b16 v[210:211],v241 offset:50432\nds_read_b64_tr_b16 v[212:213],v242 offset:50176\n" \
"M v[56:59],v[220:223],a[80:83],v[56:59]\nds_read_b64_tr_b16 v[214:215],v242 offset:50432\nds_read_b64_tr_b16 v[216:217],v241 offset:50688\nM v[60:63],v[220:223],a[84:87],v[60:63]\nds_read_b64_tr_b16 v[218:219],v241 offset:50944\nM v[120:123],v[220:223],a[88:91],v[120:123]\nbuffer_load_dwordx4 v226,s[36:39],s67 offen offset:2048 lds\nM v[124:127],v[220:223],a[92:95],v[124:127]\n" \
"ds_read_b64_tr_b16 v[220:221],v242 offset:50688\ns_waitcnt lgkmcnt(13)\nM v[0:3],v[192:195],a[96:99],v[0:3]\nds_read_b64_tr_b16 v[222:223],v242 offset:50944\nM v[4:7],v[192:195],a[100:103],v[4:7]\nM v[64:67],v[192:195],a[104:107],v[64:67]\nM v[68:71],v[192:195],a[108:111],v[68:71]\nbuffer_load_dwordx4 v227,s[36:39],s67 offen offset:3072 lds\n" \
"s_waitcnt lgkmcnt(11)\nM v[8:11],v[196:199],a[96:99],v[8:11]\nds_read_b64_tr_b16 v[192:193],v241 offset:57344\nds_read_b64_tr_b16 v[194:195],v241 offset:57600\nM v[12:15],v[196:199],a[100:103],v[12:15]\ns_add_u32 m0,s65,20480\ns_add_u32 s67,s66,33408\nM v[72:75],v[196:199],a[104:107],v[72:75]\n" \
"M v[76:79],v[196:199],a[108:111],v[76:79]\nds_read_b64_tr_b16 v[196:197],v242 offset:57344\ns_waitcnt lgkmcnt(8)\nM v[16:19],v[200:203],a[96:99],v[16:19]\nbuffer_load_dwordx4 v224,s[36:39],s67 offen offset:0 lds\nM v[20:23],v[200:203],a[100:103],v[20:23]\nds_read_b64_tr_b16 v[198:199],v242 offset:57600\nM v[80:83],v[200:203],a[104:107],v[80:83]\n" \
"M v[84:87],v[200:203],a[108:111],v[84:87]\nds_read_b64_tr_b16 v[200:201],v241 offset:57856\nds_read_b64_tr_b16 v[202:203],v241 offset:58112\nM v[24:27],v[204:207],a[96:99],v[24:27]\nM v[28:31],v[204:207],a[100:103],v[28:31]\nbuffer_load_dwordx4 v225,s[36:39],s67 offen offset:1024 lds\nM v[88:91],v[204:207],a[104:107],v[88:91]\nM v[92:95],v[204:207],a[108:111],v[92:95]\n" \
"ds_read_b64_tr_b16 v[204:205],v242 offset:57856\nds_read_b64_tr_b16 v[206:207],v242 offset:58112\nM v[32:35],v[208:211],a[96:99],v[32:35]\nM v[36:39],v[208:211],a[100:103],v[36:39]\nM v[96:99],v[208:211],a[104:107],v[96:99]\nbuffer_load_dwordx4 v226,s[36:39],s67 offen offset:2048 lds\nM v[100:103],v[208:211],a[108:111],v[100:103]\nds_read_b64_tr_b16 v[208:209],v241 offset:58368\n" \
"s_waitcnt lgkmcnt(7)\nM v[40:43],v[212:215],a[96:99],v[40:43]\nds_read_b64_tr_b16 v[210:211],v241 offset:58624\nM v[44:47],v[212:215],a[100:103],v[44:47]\nM v[104:107],v[212:215],a[104:107],v[104:107]\nM v[108:111],v[212:215],a[108:111],v[108:111]\nbuffer_load_dwordx4 v227,s[36:39],s67 offen offset:3072 lds\nM v[48:51],v[216:219],a[96:99],v[48:51]\n" \
"ds_read_b64_tr_b16 v[212:213],v242 offset:58368\nds_read_b64_tr_b16 v[214:215],v242 offset:58624\nM v[52:55],v[216:219],a[100:103],v[52:55]\ns_add_u32 m0,s64,114688\ns_add_u32 s69,s68,2621440\nM v[112:115],v[216:219],a[104:107],v[112:115]\nM v[116:119],v[216:219],a[108:111],v[116:119]\nds_read_b64_tr_b16 v[216:217],v241 offset:58880\n" \
"ds_read_b64_tr_b16 v[218:219],v241 offset:59136\nM v[56:59],v[220:223],a[96:99],v[56:59]\nbuffer_load_dwordx4 v243,s[40:43],s69 offen offset:0 lds\nM v[60:63],v[220:223],a[100:103],v[60:63]\nM v[120:123],v[220:223],a[104:107],v[120:123]\nM v[124:127],v[220:223],a[108:111],v[124:127]\nds_read_b64_tr_b16 v[220:221],v242 offset:58880\nds_read_b64_tr_b16 v[222:223],v242 offset:59136\n" \
"M v[0:3],v[192:195],a[112:115],v[0:3]\nbuffer_load_dwordx2 v[166:167],v165,s[52:55],s80 offen sc1\nM v[4:7],v[192:195],a[116:119],v[4:7]\nbuffer_load_dwordx4 v244,s[40:43],s69 offen offset:1024 lds\nM v[64:67],v[192:195],a[120:123],v[64:67]\nM v[68:71],v[192:195],a[124:127],v[68:71]\ns_waitcnt lgkmcnt(6)\nM v[8:11],v[196:199],a[112:115],v[8:11]\n" \
"M v[12:15],v[196:199],a[116:119],v[12:15]\nM v[72:75],v[196:199],a[120:123],v[72:75]\nbuffer_load_dwordx4 v245,s[40:43],s69 offen offset:2048 lds\nM v[76:79],v[196:199],a[124:127],v[76:79]\nM v[16:19],v[200:203],a[112:115],v[16:19]\nM v[20:23],v[200:203],a[116:119],v[20:23]\nM v[80:83],v[200:203],a[120:123],v[80:83]\nM v[84:87],v[200:203],a[124:127],v[84:87]\n" \
"buffer_load_dwordx4 v246,s[40:43],s69 offen offset:3072 lds\nM v[24:27],v[204:207],a[112:115],v[24:27]\nM v[28:31],v[204:207],a[116:119],v[28:31]\ns_add_u32 m0,s65,0\ns_add_u32 s67,s66,768\nM v[88:91],v[204:207],a[120:123],v[88:91]\ns_waitcnt vmcnt(13) lgkmcnt(0)\ns_barrier\n" \
"M v[92:95],v[204:207],a[124:127],v[92:95]\nds_read_b128 a[128:131],v228 offset:8192\nds_read_b64_tr_b16 v[192:193],v241 offset:0\nM v[32:35],v[208:211],a[112:115],v[32:35]\nbuffer_load_dwordx4 v224,s[36:39],s67 offen offset:0 lds\nM v[36:39],v[208:211],a[116:119],v[36:39]\nds_read_b64_tr_b16 v[194:195],v241 offset:256\nds_read_b128 a[132:135],v228 offset:10240\n" \
"M v[96:99],v[208:211],a[120:123],v[96:99]\nds_read_b128 a[136:139],v228 offset:12288\nM v[100:103],v[208:211],a[124:127],v[100:103]\nds_read_b128 a[140:143],v228 offset:14336\nM v[40:43],v[212:215],a[112:115],v[40:43]\nds_read_b64_tr_b16 v[196:197],v242 offset:0\nds_read_b64_tr_b16 v[198:199],v242 offset:256\nM v[44:47],v[212:215],a[116:119],v[44:47]\n" \
"buffer_load_dwordx4 v225,s[36:39],s67 offen offset:1024 lds\nM v[104:107],v[212:215],a[120:123],v[104:107]\nds_read_b64_tr_b16 v[200:201],v241 offset:512\nds_read_b64_tr_b16 v[202:203],v241 offset:768\nM v[108:111],v[212:215],a[124:127],v[108:111]\nds_read_b64_tr_b16 v[204:205],v242 offset:512\nds_read_b64_tr_b16 v[206:207],v242 offset:768\nM v[48:51],v[216:219],a[112:115],v[48:51]\n" \
"ds_read_b64_tr_b16 v[208:209],v241 offset:1024\nds_read_b64_tr_b16 v[210:211],v241 offset:1280\nM v[52:55],v[216:219],a[116:119],v[52:55]\nds_read_b64_tr_b16 v[212:213],v242 offset:1024\nds_read_b64_tr_b16 v[214:215],v242 offset:1280\nM v[112:115],v[216:219],a[120:123],v[112:115]\nbuffer_load_dwordx4 v226,s[36:39],s67 offen offset:2048 lds\nM v[116:119],v[216:219],a[124:127],v[116:119]\n" \
"ds_read_b64_tr_b16 v[216:217],v241 offset:1536\nds_read_b64_tr_b16 v[218:219],v241 offset:1792\nM v[56:59],v[220:223],a[112:115],v[56:59]\nds_read_b128 a[144:147],v229 offset:8192\nds_read_b128 a[148:151],v229 offset:10240\nM v[60:63],v[220:223],a[116:119],v[60:63]\nds_read_b128 a[152:155],v229 offset:12288\nM v[120:123],v[220:223],a[120:123],v[120:123]\n" \
"ds_read_b128 a[156:159],v229 offset:14336\nM v[124:127],v[220:223],a[124:127],v[124:127]\nbuffer_load_dwordx4 v227,s[36:39],s67 offen offset:3072 lds\ns_waitcnt lgkmcnt(14)\nM v[0:3],v[192:195],a[128:131],v[0:3]\nds_read_b64_tr_b16 v[220:221],v242 offset:1536\nds_read_b64_tr_b16 v[222:223],v242 offset:1792\nM v[4:7],v[192:195],a[132:135],v[4:7]\n" \
"s_add_u32 m0,s65,4096\ns_add_u32 s67,s66,33536\nM v[64:67],v[192:195],a[136:139],v[64:67]\nM v[68:71],v[192:195],a[140:143],v[68:71]\nds_read_b64_tr_b16 v[192:193],v241 offset:8192\nds_read_b64_tr_b16 v[194:195],v241 offset:8448\nM v[8:11],v[196:199],a[128:131],v[8:11]\nbuffer_load_dwordx4 v224,s[36:39],s67 offen offset:0 lds\n" \
"M v[12:15],v[196:199],a[132:135],v[12:15]\nM v[72:75],v[196:199],a[136:139],v[72:75]\nM v[76:79],v[196:199],a[140:143],v[76:79]\nds_read_b64_tr_b16 v[196:197],v242 offset:8192\ns_waitcnt lgkmcnt(9)\nM v[16:19],v[200:203],a[128:131],v[16:19]\nds_read_b64_tr_b16 v[198:199],v242 offset:8448\nM v[20:23],v[200:203],a[132:135],v[20:23]\n" \
"buffer_load_dwordx4 v225,s[36:39],s67 offen offset:1024 lds\nM v[80:83],v[200:203],a[136:139],v[80:83]\nM v[84:87],v[200:203],a[140:143],v[84:87]\nds_read_b64_tr_b16 v[200:201],v241 offset:8704\nds_read_b64_tr_b16 v[202:203],v241 offset:8960\nM v[24:27],v[204:207],a[128:131],v[24:27]\nM v[28:31],v[204:207],a[132:135],v[28:31]\nM v[88:91],v[204:207],a[136:139],v[88:91]\n" \
"buffer_load_dwordx4 v226,s[36:39],s67 offen offset:2048 lds\nM v[92:95],v[204:207],a[140:143],v[92:95]\nds_read_b64_tr_b16 v[204:205],v242 offset:8704\nds_read_b64_tr_b16 v[206:207],v242 offset:8960\nM v[32:35],v[208:211],a[128:131],v[32:35]\nM v[36:39],v[208:211],a[132:135],v[36:39]\nM v[96:99],v[208:211],a[136:139],v[96:99]\nM v[100:103],v[208:211],a[140:143],v[100:103]\n" \
"buffer_load_dwordx4 v227,s[36:39],s67 offen offset:3072 lds\nM v[40:43],v[212:215],a[128:131],v[40:43]\nds_read_b64_tr_b16 v[208:209],v241 offset:9216\nds_read_b64_tr_b16 v[210:211],v241 offset:9472\nM v[44:47],v[212:215],a[132:135],v[44:47]\ns_add_u32 m0,s64,131072\ns_add_u32 s69,s68,3145728\nM v[104:107],v[212:215],a[136:139],v[104:107]\n" \
"M v[108:111],v[212:215],a[140:143],v[108:111]\nds_read_b64_tr_b16 v[212:213],v242 offset:9216\nds_read_b64_tr_b16 v[214:215],v242 offset:9472\nM v[48:51],v[216:219],a[128:131],v[48:51]\nbuffer_load_dwordx4 v243,s[40:43],s69 offen offset:0 lds\nM v[52:55],v[216:219],a[132:135],v[52:55]\nM v[112:115],v[216:219],a[136:139],v[112:115]\nM v[116:119],v[216:219],a[140:143],v[116:119]\n" \
"ds_read_b64_tr_b16 v[216:217],v241 offset:9728\ns_waitcnt lgkmcnt(7)\nM v[56:59],v[220:223],a[128:131],v[56:59]\nds_read_b64_tr_b16 v[218:219],v241 offset:9984\nM v[60:63],v[220:223],a[132:135],v[60:63]\nbuffer_load_dwordx4 v244,s[40:43],s69 offen offset:1024 lds\nM v[120:123],v[220:223],a[136:139],v[120:123]\nM v[124:127],v[220:223],a[140:143],v[124:127]\n" \
"ds_read_b64_tr_b16 v[220:221],v242 offset:9728\nds_read_b64_tr_b16 v[222:223],v242 offset:9984\nM v[0:3],v[192:195],a[144:147],v[0:3]\nM v[4:7],v[192:195],a[148:151],v[4:7]\nM v[64:67],v[192:195],a[152:155],v[64:67]\nbuffer_load_dwordx4 v245,s[40:43],s69 offen offset:2048 lds\nM v[68:71],v[192:195],a[156:159],v[68:71]\nM v[8:11],v[196:199],a[144:147],v[8:11]\n" \
"s_waitcnt vmcnt(14)\nv_xor_b32 v166,v166,v230\nM v[12:15],v[196:199],a[148:151],v[12:15]\nv_xor_b32 v167,v167,v231\nv_or_b32 v166,v166,v167\nM v[72:75],v[196:199],a[152:155],v[72:75]\nv_cmp_ne_u32 vcc,0,v166\nM v[76:79],v[196:199],a[156:159],v[76:79]\n" \
"buffer_load_dwordx4 v246,s[40:43],s69 offen offset:3072 lds\nM v[16:19],v[200:203],a[144:147],v[16:19]\ns_cbranch_vccnz .Lhs\n.Lhb:\nv_bfe_u32 v231,v249,2,3\nM v[20:23],v[200:203],a[148:151],v[20:23]\ns_add_u32 m0,s65,8192\ns_add_u32 s67,s66,896\n" \
"M v[80:83],v[200:203],a[152:155],v[80:83]\nv_mul_u32_u24 v231,3584,v231\nv_and_b32 v230,31,v249\nM v[84:87],v[200:203],a[156:159],v[84:87]\nv_lshlrev_b32 v230,7,v230\ns_waitcnt lgkmcnt(0)\nM v[24:27],v[204:207],a[144:147],v[24:27]\nbuffer_load_dwordx4 v224,s[36:39],s67 offen offset:0 lds\n" \
"M v[28:31],v[204:207],a[148:151],v[28:31]\nM v[88:91],v[204:207],a[152:155],v[88:91]\ns_waitcnt vmcnt(13)\ns_barrier\nM v[92:95],v[204:207],a[156:159],v[92:95]\nds_read_b128 a[160:163],v228 offset:16384\nds_read_b64_tr_b16 v[192:193],v241 offset:16384\nM v[32:35],v[208:211],a[144:147],v[32:35]\n" \
"buffer_load_dwordx4 v[152:155],v240,s[52:55],s71 offen sc1\nM v[36:39],v[208:211],a[148:151],v[36:39]\ns_add_u32 s71,s70,798720\nds_read_b64_tr_b16 v[194:195],v241 offset:16640\nM v[96:99],v[208:211],a[152:155],v[96:99]\nds_read_b128 a[164:167],v228 offset:18432\nds_read_b128 a[168:171],v228 offset:20480\nM v[100:103],v[208:211],a[156:159],v[100:103]\n" \
"buffer_load_dwordx4 v225,s[36:39],s67 offen offset:1024 lds\nbuffer_load_dwordx4 v[156:159],v240,s[52:55],s71 offen sc1\nM v[40:43],v[212:215],a[144:147],v[40:43]\ns_add_u32 s71,s70,811008\nds_read_b128 a[172:175],v228 offset:22528\nM v[44:47],v[212:215],a[148:151],v[44:47]\nbuffer_load_dwordx4 v[160:163],v240,s[52:55],s71 offen sc1\nM v[104:107],v[212:215],a[152:155],v[104:107]\n" \
"buffer_load_dwordx4 v226,s[36:39],s67 offen offset:2048 lds\ns_add_u32 s71,s70,823296\nbuffer_load_dwordx4 v[164:167],v240,s[52:55],s71 offen sc1\nM v[108:111],v[212:215],a[156:159],v[108:111]\nds_read_b64_tr_b16 v[196:197],v242 offset:16384\nds_read_b64_tr_b16 v[198:199],v242 offset:16640\nM v[48:51],v[216:219],a[144:147],v[48:51]\nds_read_b64_tr_b16 v[200:201],v241 offset:16896\n" \
"ds_read_b64_tr_b16 v[202:203],v241 offset:17152\nM v[52:55],v[216:219],a[148:151],v[52:55]\nds_read_b64_tr_b16 v[204:205],v242 offset:16896\nds_read_b64_tr_b16 v[206:207],v242 offset:17152\nM v[112:115],v[216:219],a[152:155],v[112:115]\nds_read_b64_tr_b16 v[208:209],v241 offset:17408\nds_read_b64_tr_b16 v[210:211],v241 offset:17664\nM v[116:119],v[216:219],a[156:159],v[116:119]\n" \
"buffer_load_dwordx4 v227,s[36:39],s67 offen offset:3072 lds\nM v[56:59],v[220:223],a[144:147],v[56:59]\nds_read_b64_tr_b16 v[212:213],v242 offset:17408\nds_read_b64_tr_b16 v[214:215],v242 offset:17664\nM v[60:63],v[220:223],a[148:151],v[60:63]\ns_add_u32 m0,s65,12288\ns_add_u32 s67,s66,33664\nM v[120:123],v[220:223],a[152:155],v[120:123]\n" \
"ds_read_b64_tr_b16 v[216:217],v241 offset:17920\nds_read_b64_tr_b16 v[218:219],v241 offset:18176\nM v[124:127],v[220:223],a[156:159],v[124:127]\nds_read_b64_tr_b16 v[220:221],v242 offset:17920\ns_waitcnt lgkmcnt(13)\nM v[0:3],v[192:195],a[160:163],v[0:3]\nds_read_b64_tr_b16 v[222:223],v242 offset:18176\nds_read_b128 a[176:179],v229 offset:16384\n" \
"M v[4:7],v[192:195],a[164:167],v[4:7]\nds_read_b128 a[180:183],v229 offset:18432\ns_lshl_b32 s71,s6,5\nM v[64:67],v[192:195],a[168:171],v[64:67]\nds_read_b128 a[184:187],v229 offset:20480\ns_add_u32 s71,s71,790528\nM v[68:71],v[192:195],a[172:175],v[68:71]\nds_read_b64_tr_b16 v[192:193],v241 offset:24576\n" \
"s_waitcnt lgkmcnt(13)\nM v[8:11],v[196:199],a[160:163],v[8:11]\nds_read_b64_tr_b16 v[194:195],v241 offset:24832\nds_read_b128 a[188:191],v229 offset:22528\nM v[12:15],v[196:199],a[164:167],v[12:15]\ns_cmp_lt_u32 s7,2\ns_cselect_b32 s70,0,1.0\nM v[72:75],v[196:199],a[168:171],v[72:75]\n" \
"global_load_dword v247,v253,s[12:13]\nM v[76:79],v[196:199],a[172:175],v[76:79]\nds_read_b64_tr_b16 v[196:197],v242 offset:24576\nds_read_b64_tr_b16 v[198:199],v242 offset:24832\nM v[16:19],v[200:203],a[160:163],v[16:19]\nv_add_u32 v253,0x100000,v253\nM v[20:23],v[200:203],a[164:167],v[20:23]\nM v[80:83],v[200:203],a[168:171],v[80:83]\n" \
"buffer_load_dword v247,v254,s[56:59],s78 offen\nM v[84:87],v[200:203],a[172:175],v[84:87]\nds_read_b64_tr_b16 v[200:201],v241 offset:25088\ns_waitcnt lgkmcnt(8)\nM v[24:27],v[204:207],a[160:163],v[24:27]\nds_read_b64_tr_b16 v[202:203],v241 offset:25344\nv_and_b32 v254,3,v249\nM v[28:31],v[204:207],a[164:167],v[28:31]\n" \
"s_cmp_eq_u32 s62,5\ns_cselect_b64 s[56:57],s[22:23],s[24:25]\nM v[88:91],v[204:207],a[168:171],v[88:91]\nbuffer_load_dwordx4 v224,s[36:39],s67 offen offset:0 lds\nM v[92:95],v[204:207],a[172:175],v[92:95]\nds_read_b64_tr_b16 v[204:205],v242 offset:25088\nds_read_b64_tr_b16 v[206:207],v242 offset:25344\nM v[32:35],v[208:211],a[160:163],v[32:35]\n" \
"v_lshl_add_u32 v231,v254,7,v231\nM v[36:39],v[208:211],a[164:167],v[36:39]\ns_add_u32 s69,s68,3670016\nM v[96:99],v[208:211],a[168:171],v[96:99]\nbuffer_load_dwordx4 v225,s[36:39],s67 offen offset:1024 lds\nM v[100:103],v[208:211],a[172:175],v[100:103]\nds_read_b64_tr_b16 v[208:209],v241 offset:25600\nds_read_b64_tr_b16 v[210:211],v241 offset:25856\n" \
"M v[40:43],v[212:215],a[160:163],v[40:43]\nM v[44:47],v[212:215],a[164:167],v[44:47]\nM v[104:107],v[212:215],a[168:171],v[104:107]\nbuffer_load_dwordx4 v226,s[36:39],s67 offen offset:2048 lds\nM v[108:111],v[212:215],a[172:175],v[108:111]\nds_read_b64_tr_b16 v[212:213],v242 offset:25600\nds_read_b64_tr_b16 v[214:215],v242 offset:25856\nM v[48:51],v[216:219],a[160:163],v[48:51]\n" \
"M v[52:55],v[216:219],a[164:167],v[52:55]\nM v[112:115],v[216:219],a[168:171],v[112:115]\nbuffer_load_dwordx4 v227,s[36:39],s67 offen offset:3072 lds\nM v[116:119],v[216:219],a[172:175],v[116:119]\ns_add_u32 m0,s64,147456\nds_read_b64_tr_b16 v[216:217],v241 offset:26112\nM v[56:59],v[220:223],a[160:163],v[56:59]\ns_cmp_eq_u32 s62,0\n" \
"s_cselect_b32 s36,0x8000,0\nM v[60:63],v[220:223],a[164:167],v[60:63]\nds_read_b64_tr_b16 v[218:219],v241 offset:26368\ns_add_u32 s36,s36,s64\nM v[120:123],v[220:223],a[168:171],v[120:123]\nbuffer_load_dwordx4 v243,s[40:43],s69 offen offset:0 lds\nM v[124:127],v[220:223],a[172:175],v[124:127]\nds_read_b64_tr_b16 v[220:221],v242 offset:26112\n" \
"s_waitcnt lgkmcnt(6)\nM v[0:3],v[192:195],a[176:179],v[0:3]\nds_read_b64_tr_b16 v[222:223],v242 offset:26368\nM v[4:7],v[192:195],a[180:183],v[4:7]\nM v[64:67],v[192:195],a[184:187],v[64:67]\nbuffer_load_dwordx4 v244,s[40:43],s69 offen offset:1024 lds\nM v[68:71],v[192:195],a[188:191],v[68:71]\nM v[8:11],v[196:199],a[176:179],v[8:11]\n" \
"M v[12:15],v[196:199],a[180:183],v[12:15]\nM v[72:75],v[196:199],a[184:187],v[72:75]\nbuffer_load_dwordx4 v245,s[40:43],s69 offen offset:2048 lds\nM v[76:79],v[196:199],a[188:191],v[76:79]\nM v[16:19],v[200:203],a[176:179],v[16:19]\nM v[20:23],v[200:203],a[180:183],v[20:23]\nM v[80:83],v[200:203],a[184:187],v[80:83]\nglobal_load_dword v247,v253,s[12:13]\n" \
"M v[84:87],v[200:203],a[188:191],v[84:87]\nbuffer_load_dwordx4 v246,s[40:43],s69 offen offset:3072 lds\nM v[24:27],v[204:207],a[176:179],v[24:27]\nv_add_u32 v253,0x100000,v253\ns_add_u32 m0,s64,98304\nM v[28:31],v[204:207],a[180:183],v[28:31]\ns_sub_u32 s40,s16,0x1000\ns_subb_u32 s41,s17,0\n" \
"M v[88:91],v[204:207],a[184:187],v[88:91]\ns_waitcnt vmcnt(19) lgkmcnt(0)\ns_barrier\nM v[92:95],v[204:207],a[188:191],v[92:95]\nds_read_b128 a[192:195],v228 offset:0\nds_read_b64_tr_b16 v[192:193],v241 offset:32768\nM v[32:35],v[208:211],a[176:179],v[32:35]\nds_read_b64_tr_b16 v[194:195],v241 offset:33024\n" \
"ds_read_b128 a[196:199],v228 offset:2048\nM v[36:39],v[208:211],a[180:183],v[36:39]\nds_read_b128 a[200:203],v228 offset:4096\ns_add_u32 s69,s68,0\nM v[96:99],v[208:211],a[184:187],v[96:99]\nds_read_b128 a[204:207],v228 offset:6144\ns_mov_b32 s16,0\nM v[100:103],v[208:211],a[188:191],v[100:103]\n" \
"ds_read_b64_tr_b16 v[196:197],v242 offset:32768\ns_mov_b32 s17,3\nM v[40:43],v[212:215],a[176:179],v[40:43]\nds_read_b64_tr_b16 v[198:199],v242 offset:33024\nds_read_b64_tr_b16 v[200:201],v241 offset:33280\nM v[44:47],v[212:215],a[180:183],v[44:47]\nds_read_b64_tr_b16 v[202:203],v241 offset:33536\nds_read_b64_tr_b16 v[204:205],v242 offset:33280\n" \
"M v[104:107],v[212:215],a[184:187],v[104:107]\nds_read_b64_tr_b16 v[206:207],v242 offset:33536\nds_read_b64_tr_b16 v[208:209],v241 offset:33792\nM v[108:111],v[212:215],a[188:191],v[108:111]\nds_read_b64_tr_b16 v[210:211],v241 offset:34048\nds_read_b64_tr_b16 v[212:213],v242 offset:33792\nM v[48:51],v[216:219],a[176:179],v[48:51]\nds_read_b64_tr_b16 v[214:215],v242 offset:34048\n" \
"ds_read_b128 a[208:211],v229 offset:0\nM v[52:55],v[216:219],a[180:183],v[52:55]\nds_read_b128 a[212:215],v229 offset:2048\ns_waitcnt vmcnt(14)\nv_add_f32 v152,v152,v156\nM v[112:115],v[216:219],a[184:187],v[112:115]\nds_read_b128 a[216:219],v229 offset:4096\nv_add_f32 v153,v153,v157\n" \
"M v[116:119],v[216:219],a[188:191],v[116:119]\nds_read_b64_tr_b16 v[216:217],v241 offset:34304\nv_add_f32 v154,v154,v158\nM v[56:59],v[220:223],a[176:179],v[56:59]\nds_read_b64_tr_b16 v[218:219],v241 offset:34560\nds_read_b128 a[220:223],v229 offset:6144\nM v[60:63],v[220:223],a[180:183],v[60:63]\ns_waitcnt vmcnt(12)\n" \
"v_add_f32 v160,v160,v164\nM v[120:123],v[220:223],a[184:187],v[120:123]\nv_add_f32 v161,v161,v165\nv_add_f32 v162,v162,v166\nM v[124:127],v[220:223],a[188:191],v[124:127]\nds_read_b64_tr_b16 v[220:221],v242 offset:34304\ns_waitcnt lgkmcnt(12)\nM v[0:3],v[192:195],a[192:195],v[0:3]\n" \
"ds_read_b64_tr_b16 v[222:223],v242 offset:34560\nv_add_f32 v155,v155,v159\nM v[4:7],v[192:195],a[196:199],v[4:7]\nv_add_f32 v163,v163,v167\nv_add_f32 v152,v152,v160\nM v[64:67],v[192:195],a[200:203],v[64:67]\nbuffer_load_dword v168,v169,s[52:55],s71 offen sc1\nM v[68:71],v[192:195],a[204:207],v[68:71]\n" \
"ds_read_b64_tr_b16 v[192:193],v241 offset:40960\nds_read_b64_tr_b16 v[194:195],v241 offset:41216\nM v[8:11],v[196:199],a[192:195],v[8:11]\nv_add_f32 v153,v153,v161\nv_add_f32 v154,v154,v162\nM v[12:15],v[196:199],a[196:199],v[12:15]\nv_add_f32 v155,v155,v163\nv_cvt_pk_bf16_f32 v152,0,v152\n" \
"M v[72:75],v[196:199],a[200:203],v[72:75]\nv_cvt_pk_bf16_f32 v153,0,v153\nv_cvt_pk_bf16_f32 v154,0,v154\nM v[76:79],v[196:199],a[204:207],v[76:79]\nds_read_b64_tr_b16 v[196:197],v242 offset:40960\nds_read_b64_tr_b16 v[198:199],v242 offset:41216\nM v[16:19],v[200:203],a[192:195],v[16:19]\nv_cvt_pk_bf16_f32 v155,0,v155\n" \
"v_add_f32 v152,s70,v152\nM v[20:23],v[200:203],a[196:199],v[20:23]\nv_add_f32 v153,s70,v153\nv_add_f32 v154,s70,v154\nM v[80:83],v[200:203],a[200:203],v[80:83]\nv_add_f32 v155,s70,v155\ns_lshr_b32 s71,s64,2\nM v[84:87],v[200:203],a[204:207],v[84:87]\n" \
"ds_read_b64_tr_b16 v[200:201],v241 offset:41472\ns_waitcnt lgkmcnt(5)\nM v[24:27],v[204:207],a[192:195],v[24:27]\nds_read_b64_tr_b16 v[202:203],v241 offset:41728\nv_cvt_pk_bf16_f32 v152,0,v152\nM v[28:31],v[204:207],a[196:199],v[28:31]\nv_cvt_pk_bf16_f32 v153,0,v153\nv_cvt_pk_bf16_f32 v154,0,v154\n" \
"M v[88:91],v[204:207],a[200:203],v[88:91]\nv_cvt_pk_bf16_f32 v155,0,v155\nv_add_u32 v171,s71,v240\nM v[92:95],v[204:207],a[204:207],v[92:95]\nds_read_b64_tr_b16 v[204:205],v242 offset:41472\nds_read_b64_tr_b16 v[206:207],v242 offset:41728\nM v[32:35],v[208:211],a[192:195],v[32:35]\nds_write_b128 v171,v[152:155] offset:16384\n" \
"M v[36:39],v[208:211],a[196:199],v[36:39]\nbuffer_load_dwordx4 v243,s[40:43],s69 offen offset:0 lds\nM v[96:99],v[208:211],a[200:203],v[96:99]\ns_lshl_b32 s70,s7,8\nv_lshl_add_u32 v251,v251,5,s70\nM v[100:103],v[208:211],a[204:207],v[100:103]\nds_read_b64_tr_b16 v[208:209],v241 offset:41984\nds_read_b64_tr_b16 v[210:211],v241 offset:42240\n" \
"M v[40:43],v[212:215],a[192:195],v[40:43]\ns_lshl_b32 s71,s6,5\ns_and_b32 s71,s71,0xfffffe00\nM v[44:47],v[212:215],a[196:199],v[44:47]\nglobal_load_dword v247,v253,s[12:13]\nM v[104:107],v[212:215],a[200:203],v[104:107]\nv_add_u32 v253,0x100000,v253\ns_xor_b32 s70,s63,16\n" \
"M v[108:111],v[212:215],a[204:207],v[108:111]\nds_read_b64_tr_b16 v[212:213],v242 offset:41984\nds_read_b64_tr_b16 v[214:215],v242 offset:42240\nM v[48:51],v[216:219],a[192:195],v[48:51]\nbuffer_load_dwordx4 v244,s[40:43],s69 offen offset:1024 lds\nM v[52:55],v[216:219],a[196:199],v[52:55]\ns_lshr_b32 s70,s70,2\ns_lshl3_add_u32 s70,s62,s70\n" \
"M v[112:115],v[216:219],a[200:203],v[112:115]\ns_lshl_b32 s70,s70,15\ns_add_u32 s70,s70,s64\nM v[116:119],v[216:219],a[204:207],v[116:119]\nds_read_b64_tr_b16 v[216:217],v241 offset:42496\nds_read_b64_tr_b16 v[218:219],v241 offset:42752\nM v[56:59],v[220:223],a[192:195],v[56:59]\nbuffer_load_dwordx4 v245,s[40:43],s69 offen offset:2048 lds\n" \
"M v[60:63],v[220:223],a[196:199],v[60:63]\nM v[120:123],v[220:223],a[200:203],v[120:123]\nM v[124:127],v[220:223],a[204:207],v[124:127]\nds_read_b64_tr_b16 v[220:221],v242 offset:42496\ns_waitcnt lgkmcnt(5)\nM v[0:3],v[192:195],a[208:211],v[0:3]\nbuffer_load_dwordx4 v246,s[40:43],s69 offen offset:3072 lds\nM v[4:7],v[192:195],a[212:215],v[4:7]\n" \
"ds_read_b64_tr_b16 v[222:223],v242 offset:42752\ns_add_u32 m0,s64,114688\nM v[64:67],v[192:195],a[216:219],v[64:67]\ns_add_u32 s69,s68,524288\nM v[68:71],v[192:195],a[220:223],v[68:71]\nM v[8:11],v[196:199],a[208:211],v[8:11]\nbuffer_store_dwordx2 v[232:233],off,s[52:55],s72 sc1\nM v[12:15],v[196:199],a[212:215],v[12:15]\n" \
"s_and_b32 s72,s6,1\ns_lshl_b32 s72,s72,8\nM v[72:75],v[196:199],a[216:219],v[72:75]\ns_add_u32 s71,s71,s72\ns_and_b32 s72,s6,14\nM v[76:79],v[196:199],a[220:223],v[76:79]\ns_lshl1_add_u32 s71,s72,s71\ns_or_b32 s71,s71,s75\n" \
"M v[16:19],v[200:203],a[208:211],v[16:19]\nbuffer_load_dwordx4 v243,s[40:43],s69 offen offset:0 lds\nM v[20:23],v[200:203],a[212:215],v[20:23]\ns_mov_b32 s72,0x3e0293ee\nM v[80:83],v[200:203],a[216:219],v[80:83]\nM v[84:87],v[200:203],a[220:223],v[84:87]\nM v[24:27],v[204:207],a[208:211],v[24:27]\nbuffer_load_dwordx4 v244,s[40:43],s69 offen offset:1024 lds\n" \
"M v[28:31],v[204:207],a[212:215],v[28:31]\nM v[88:91],v[204:207],a[216:219],v[88:91]\ns_waitcnt vmcnt(9) lgkmcnt(0)\ns_barrier\nM v[92:95],v[204:207],a[220:223],v[92:95]\nds_read_b128 a[224:227],v228 offset:8192\nds_read_b64_tr_b16 v[192:193],v241 offset:49152\nM v[32:35],v[208:211],a[208:211],v[32:35]\n" \
"ds_read_b64_tr_b16 v[194:195],v241 offset:49408\nds_read_b128 a[228:231],v228 offset:10240\nM v[36:39],v[208:211],a[212:215],v[36:39]\nbuffer_load_dwordx4 v245,s[40:43],s69 offen offset:2048 lds\nM v[96:99],v[208:211],a[216:219],v[96:99]\nds_read_b128 a[232:235],v228 offset:12288\nds_read_b128 a[236:239],v228 offset:14336\nM v[100:103],v[208:211],a[220:223],v[100:103]\n" \
"ds_read_b64_tr_b16 v[196:197],v242 offset:49152\nv_and_b32 v228,15,v249\nM v[40:43],v[212:215],a[208:211],v[40:43]\nds_read_b64_tr_b16 v[198:199],v242 offset:49408\nds_read_b64_tr_b16 v[200:201],v241 offset:49664\nM v[44:47],v[212:215],a[212:215],v[44:47]\nds_read_b64_tr_b16 v[202:203],v241 offset:49920\nds_read_b64_tr_b16 v[204:205],v242 offset:49664\n" \
"M v[104:107],v[212:215],a[216:219],v[104:107]\nds_read_b64_tr_b16 v[206:207],v242 offset:49920\nds_read_b64_tr_b16 v[208:209],v241 offset:50176\nM v[108:111],v[212:215],a[220:223],v[108:111]\nds_read_b64_tr_b16 v[210:211],v241 offset:50432\nds_read_b64_tr_b16 v[212:213],v242 offset:50176\nM v[48:51],v[216:219],a[208:211],v[48:51]\nds_read_b64_tr_b16 v[214:215],v242 offset:50432\n" \
"ds_read_b128 a[240:243],v229 offset:8192\nM v[52:55],v[216:219],a[212:215],v[52:55]\nds_read_b128 a[244:247],v229 offset:10240\nv_lshlrev_b32 v228,2,v228\nM v[112:115],v[216:219],a[216:219],v[112:115]\nglobal_load_dword v247,v253,s[12:13]\nM v[116:119],v[216:219],a[220:223],v[116:119]\nds_read_b64_tr_b16 v[216:217],v241 offset:50688\n" \
"ds_read_b64_tr_b16 v[218:219],v241 offset:50944\nM v[56:59],v[220:223],a[208:211],v[56:59]\nds_read_b128 a[248:251],v229 offset:12288\nds_read_b128 a[252:255],v229 offset:14336\nM v[60:63],v[220:223],a[212:215],v[60:63]\nds_read_b128 v[176:179],v251 offset:19456\nv_and_b32 v229,16,v249\nM v[120:123],v[220:223],a[216:219],v[120:123]\n" \
"ds_read_b128 v[160:163],v251 offset:17408\nv_lshl_or_b32 v228,v229,4,v228\nM v[124:127],v[220:223],a[220:223],v[124:127]\nds_read_b64_tr_b16 v[220:221],v242 offset:50688\ns_waitcnt lgkmcnt(15)\nM v[0:3],v[192:195],a[224:227],v[0:3]\nds_read_b64_tr_b16 v[222:223],v242 offset:50944\nds_read_b128 v[172:175],v251 offset:18448\n" \
"M v[4:7],v[192:195],a[228:231],v[4:7]\nds_read_b128 v[180:183],v251 offset:19472\nv_and_b32 v229,15,v249\nM v[64:67],v[192:195],a[232:235],v[64:67]\nds_read_b128 v[152:155],v251 offset:16384\nv_lshlrev_b32 v229,7,v229\nM v[68:71],v[192:195],a[236:239],v[68:71]\nds_read_b64_tr_b16 v[192:193],v241 offset:57344\n" \
"v_add_u32 v253,0xffd00000,v253\nM v[8:11],v[196:199],a[224:227],v[8:11]\nds_read_b64_tr_b16 v[194:195],v241 offset:57600\nds_read_b128 v[156:159],v251 offset:16400\nM v[12:15],v[196:199],a[228:231],v[12:15]\nds_read_b128 v[164:167],v251 offset:17424\nv_lshrrev_b32 v253,4,v249\nM v[72:75],v[196:199],a[232:235],v[72:75]\n" \
"buffer_load_dwordx4 v246,s[40:43],s69 offen offset:3072 lds\nM v[76:79],v[196:199],a[236:239],v[76:79]\nds_read_b64_tr_b16 v[196:197],v242 offset:57344\nds_read_b64_tr_b16 v[198:199],v242 offset:57600\nM v[16:19],v[200:203],a[224:227],v[16:19]\ns_waitcnt vmcnt(11)\nv_add_f32_dpp v168,v168,v168 quad_perm:[1,0,3,2] row_mask:0xf bank_mask:0xf\nM v[20:23],v[200:203],a[228:231],v[20:23]\n" \
"s_waitcnt lgkmcnt(11)\nv_mul_f32 v144,v144,v176\nM v[80:83],v[200:203],a[232:235],v[80:83]\nv_add_f32_dpp v168,v168,v168 quad_perm:[2,3,0,1] row_mask:0xf bank_mask:0xf\nv_cvt_pk_bf16_f32 v168,0,v168\nM v[84:87],v[200:203],a[236:239],v[84:87]\nds_read_b64_tr_b16 v[200:201],v241 offset:57856\nds_read_b64_tr_b16 v[202:203],v241 offset:58112\n" \
"M v[24:27],v[204:207],a[224:227],v[24:27]\nbuffer_store_dword v168,v170,s[52:55],s71 offen sc1\nM v[28:31],v[204:207],a[228:231],v[28:31]\nds_read_b128 v[168:171],v251 offset:18432\nv_mul_f32 v145,v145,v177\nM v[88:91],v[204:207],a[232:235],v[88:91]\nv_cvt_pk_bf16_f32 v144,0,v144\nv_cvt_pk_bf16_f32 v145,0,v145\n" \
"M v[92:95],v[204:207],a[236:239],v[92:95]\nds_read_b64_tr_b16 v[204:205],v242 offset:57856\nds_read_b64_tr_b16 v[206:207],v242 offset:58112\nM v[32:35],v[208:211],a[224:227],v[32:35]\nv_add_f32 v144,v144,v160\nv_add_f32 v145,v145,v161\nM v[36:39],v[208:211],a[228:231],v[36:39]\nv_cvt_pk_bf16_f32 v144,0,v144\n" \
"v_cvt_pk_bf16_f32 v145,0,v145\nM v[96:99],v[208:211],a[232:235],v[96:99]\nv_mul_f32 v144,s72,v144\nv_mul_f32 v145,s72,v145\nM v[100:103],v[208:211],a[236:239],v[100:103]\nds_read_b64_tr_b16 v[208:209],v241 offset:58368\nds_read_b64_tr_b16 v[210:211],v241 offset:58624\nM v[40:43],v[212:215],a[224:227],v[40:43]\n" \
"v_cvt_pk_bf16_f32 v236,v144,v145\nv_mul_f32 v146,v146,v178\nM v[44:47],v[212:215],a[228:231],v[44:47]\nv_mul_f32 v147,v147,v179\nv_cvt_pk_bf16_f32 v146,0,v146\nM v[104:107],v[212:215],a[232:235],v[104:107]\nv_cvt_pk_bf16_f32 v147,0,v147\nv_add_f32 v146,v146,v162\n" \
"M v[108:111],v[212:215],a[236:239],v[108:111]\nds_read_b64_tr_b16 v[212:213],v242 offset:58368\nds_read_b64_tr_b16 v[214:215],v242 offset:58624\nM v[48:51],v[216:219],a[224:227],v[48:51]\nv_add_f32 v147,v147,v163\nv_cvt_pk_bf16_f32 v146,0,v146\nM v[52:55],v[216:219],a[228:231],v[52:55]\nv_cvt_pk_bf16_f32 v147,0,v147\n" \
"v_mul_f32 v146,s72,v146\nM v[112:115],v[216:219],a[232:235],v[112:115]\nv_mul_f32 v147,s72,v147\nv_cvt_pk_bf16_f32 v237,v146,v147\nM v[116:119],v[216:219],a[236:239],v[116:119]\nds_read_b64_tr_b16 v[216:217],v241 offset:58880\ns_waitcnt lgkmcnt(5)\nM v[56:59],v[220:223],a[224:227],v[56:59]\n" \
"ds_read_b64_tr_b16 v[218:219],v241 offset:59136\ns_and_b32 s71,s63,2\nM v[60:63],v[220:223],a[228:231],v[60:63]\nv_mul_f32 v136,v136,v168\nv_mul_f32 v137,v137,v169\nM v[120:123],v[220:223],a[232:235],v[120:123]\nv_mul_f32 v138,v138,v170\nv_mul_f32 v139,v139,v171\n" \
"M v[124:127],v[220:223],a[236:239],v[124:127]\nds_read_b64_tr_b16 v[220:221],v242 offset:58880\nds_read_b64_tr_b16 v[222:223],v242 offset:59136\nM v[0:3],v[192:195],a[240:243],v[0:3]\nv_mul_f32 v140,v140,v172\nv_mul_f32 v141,v141,v173\nM v[4:7],v[192:195],a[244:247],v[4:7]\nv_mul_f32 v142,v142,v174\n" \
"v_mul_f32 v143,v143,v175\nM v[64:67],v[192:195],a[248:251],v[64:67]\nv_mul_f32 v148,v148,v180\nv_mul_f32 v149,v149,v181\nM v[68:71],v[192:195],a[252:255],v[68:71]\nv_cvt_pk_bf16_f32 v128,v0,v1\nv_cvt_pk_bf16_f32 v129,v2,v3\nM v[8:11],v[196:199],a[240:243],v[8:11]\n" \
"v_cvt_pk_bf16_f32 v144,v4,v5\nv_cvt_pk_bf16_f32 v145,v6,v7\nM v[12:15],v[196:199],a[244:247],v[12:15]\nv_cvt_pk_bf16_f32 v160,v64,v65\nv_cvt_pk_bf16_f32 v161,v66,v67\nM v[72:75],v[196:199],a[248:251],v[72:75]\nv_cvt_pk_bf16_f32 v176,v68,v69\nv_cvt_pk_bf16_f32 v177,v70,v71\n" \
"M v[76:79],v[196:199],a[252:255],v[76:79]\nv_cvt_pk_bf16_f32 v130,v8,v9\nv_cvt_pk_bf16_f32 v131,v10,v11\nM v[16:19],v[200:203],a[240:243],v[16:19]\nv_cvt_pk_bf16_f32 v146,v12,v13\nv_cvt_pk_bf16_f32 v147,v14,v15\nM v[20:23],v[200:203],a[244:247],v[20:23]\ns_lshl_b32 s71,s71,9\n" \
"v_cvt_pk_bf16_f32 v162,v72,v73\nM v[80:83],v[200:203],a[248:251],v[80:83]\nv_cvt_pk_bf16_f32 v163,v74,v75\nv_cvt_pk_bf16_f32 v136,0,v136\nM v[84:87],v[200:203],a[252:255],v[84:87]\nv_cvt_pk_bf16_f32 v137,0,v137\nv_cvt_pk_bf16_f32 v138,0,v138\nM v[24:27],v[204:207],a[240:243],v[24:27]\n" \
"v_cvt_pk_bf16_f32 v139,0,v139\nv_cvt_pk_bf16_f32 v140,0,v140\nM v[28:31],v[204:207],a[244:247],v[28:31]\nv_cvt_pk_bf16_f32 v141,0,v141\nv_cvt_pk_bf16_f32 v142,0,v142\nM v[88:91],v[204:207],a[248:251],v[88:91]\ns_waitcnt vmcnt(7) lgkmcnt(0)\ns_barrier\n" \
"M v[92:95],v[204:207],a[252:255],v[92:95]\nds_read_b64_tr_b16 v[192:193],v241 offset:0\nds_read_b64_tr_b16 v[194:195],v241 offset:256\nM v[32:35],v[208:211],a[240:243],v[32:35]\nds_read_b64_tr_b16 v[196:197],v242 offset:0\nds_read_b64_tr_b16 v[198:199],v242 offset:256\nM v[36:39],v[208:211],a[244:247],v[36:39]\nds_read_b64_tr_b16 v[200:201],v241 offset:512\n" \
"ds_read_b64_tr_b16 v[202:203],v241 offset:768\nM v[96:99],v[208:211],a[248:251],v[96:99]\nds_read_b64_tr_b16 v[204:205],v242 offset:512\nds_read_b64_tr_b16 v[206:207],v242 offset:768\nM v[100:103],v[208:211],a[252:255],v[100:103]\nds_read_b64_tr_b16 v[208:209],v241 offset:1024\nds_read_b64_tr_b16 v[210:211],v241 offset:1280\nM v[40:43],v[212:215],a[240:243],v[40:43]\n" \
"v_cvt_pk_bf16_f32 v143,0,v143\nv_cvt_pk_bf16_f32 v148,0,v148\nM v[44:47],v[212:215],a[244:247],v[44:47]\nv_cvt_pk_bf16_f32 v149,0,v149\nv_lshrrev_b32 v251,5,v249\nM v[104:107],v[212:215],a[248:251],v[104:107]\ns_add_u32 s70,s70,s71\nv_add_f32 v136,v136,v152\n" \
"M v[108:111],v[212:215],a[252:255],v[108:111]\nv_add_f32 v137,v137,v153\nv_add_f32 v138,v138,v154\nM v[48:51],v[216:219],a[240:243],v[48:51]\nv_add_f32 v139,v139,v155\nv_add_f32 v140,v140,v156\nM v[52:55],v[216:219],a[244:247],v[52:55]\nv_add_f32 v141,v141,v157\n" \
"v_add_f32 v142,v142,v158\nM v[112:115],v[216:219],a[248:251],v[112:115]\nv_add_f32 v143,v143,v159\nv_add_f32 v148,v148,v164\nM v[116:119],v[216:219],a[252:255],v[116:119]\nv_add_f32 v149,v149,v165\nv_lshlrev_b32 v251,11,v251\nM v[56:59],v[220:223],a[240:243],v[56:59]\n" \
"s_and_b32 s71,s63,1\nv_cvt_pk_bf16_f32 v178,v76,v77\nM v[60:63],v[220:223],a[244:247],v[60:63]\nv_cvt_pk_bf16_f32 v179,v78,v79\nv_cvt_pk_bf16_f32 v136,0,v136\nM v[120:123],v[220:223],a[248:251],v[120:123]\nv_cvt_pk_bf16_f32 v137,0,v137\nv_cvt_pk_bf16_f32 v138,0,v138\n" \
"M v[124:127],v[220:223],a[252:255],v[124:127]\nv_cvt_pk_bf16_f32 v139,0,v139\ns_waitcnt lgkmcnt(0)\nM v[0:3],a[0:3],v[192:195],0\nv_cvt_pk_bf16_f32 v140,0,v140\nv_cvt_pk_bf16_f32 v141,0,v141\nM v[4:7],a[4:7],v[192:195],0\nv_cvt_pk_bf16_f32 v142,0,v142\n" \
"v_cvt_pk_bf16_f32 v143,0,v143\nM v[64:67],a[8:11],v[192:195],0\nv_cvt_pk_bf16_f32 v148,0,v148\nv_cvt_pk_bf16_f32 v149,0,v149\nM v[68:71],a[12:15],v[192:195],0\nv_lshl_or_b32 v251,v250,8,v251\ns_lshl_b32 s71,s71,7\nM v[8:11],a[0:3],v[196:199],0\n" \
"v_mul_f32 v136,s72,v136\nv_mul_f32 v137,s72,v137\nM v[12:15],a[4:7],v[196:199],0\nv_mul_f32 v138,s72,v138\nv_mul_f32 v139,s72,v139\nM v[72:75],a[8:11],v[196:199],0\nv_mul_f32 v140,s72,v140\nv_mul_f32 v141,s72,v141\n" \
"M v[76:79],a[12:15],v[196:199],0\nv_mul_f32 v142,s72,v142\nv_mul_f32 v143,s72,v143\nv_mul_f32 v148,s72,v148\nv_mul_f32 v149,s72,v149\nv_and_b32 v250,0x70,v240\ns_add_u32 s70,s70,s71\nv_cvt_pk_bf16_f32 v132,v16,v17\n" \
"v_cvt_pk_bf16_f32 v133,v18,v19\nM v[16:19],a[0:3],v[200:203],0\nv_cvt_pk_bf16_f32 v232,v136,v137\nv_cvt_pk_bf16_f32 v233,v138,v139\nv_cvt_pk_bf16_f32 v234,v140,v141\nv_cvt_pk_bf16_f32 v235,v142,v143\nv_cvt_pk_bf16_f32 v238,v148,v149\nv_or_b32 v251,v251,v250\n" \
"s_add_u32 s70,s70,1048576\nbuffer_store_dwordx4 v[232:235],v251,s[52:55],s70 offen sc1\nv_cvt_pk_bf16_f32 v148,v20,v21\nv_cvt_pk_bf16_f32 v149,v22,v23\nM v[20:23],a[4:7],v[200:203],0\nv_mul_f32 v150,v150,v182\nv_mul_f32 v151,v151,v183\nv_cvt_pk_bf16_f32 v164,v80,v81\n" \
"v_cvt_pk_bf16_f32 v165,v82,v83\nds_read_b64_tr_b16 v[212:213],v242 offset:1024\nds_read_b64_tr_b16 v[214:215],v242 offset:1280\nM v[80:83],a[8:11],v[200:203],0\nv_cvt_pk_bf16_f32 v150,0,v150\nv_cvt_pk_bf16_f32 v151,0,v151\nv_add_f32 v150,v150,v166\nv_add_f32 v151,v151,v167\n" \
"v_cvt_pk_bf16_f32 v180,v84,v85\nv_cvt_pk_bf16_f32 v181,v86,v87\nM v[84:87],a[12:15],v[200:203],0\nv_cvt_pk_bf16_f32 v150,0,v150\nv_cvt_pk_bf16_f32 v151,0,v151\nv_mul_f32 v150,s72,v150\nv_mul_f32 v151,s72,v151\nv_cvt_pk_bf16_f32 v134,v24,v25\n" \
"v_cvt_pk_bf16_f32 v135,v26,v27\nM v[24:27],a[0:3],v[204:207],0\nv_cvt_pk_bf16_f32 v239,v150,v151\ns_add_u32 s71,s70,16384\nbuffer_store_dwordx4 v[236:239],v251,s[52:55],s71 offen sc1\nv_cvt_pk_bf16_f32 v150,v28,v29\nv_cvt_pk_bf16_f32 v151,v30,v31\nM v[28:31],a[4:7],v[204:207],0\n" \
"v_cvt_pk_bf16_f32 v166,v88,v89\nv_cvt_pk_bf16_f32 v167,v90,v91\nds_read_b64_tr_b16 v[216:217],v241 offset:1536\nds_read_b64_tr_b16 v[218:219],v241 offset:1792\nM v[88:91],a[8:11],v[204:207],0\nv_cvt_pk_bf16_f32 v182,v92,v93\nv_cvt_pk_bf16_f32 v183,v94,v95\nM v[92:95],a[12:15],v[204:207],0\n" \
"v_cvt_pk_bf16_f32 v136,v32,v33\nv_cvt_pk_bf16_f32 v137,v34,v35\nM v[32:35],a[0:3],v[208:211],0\ns_lshl_b32 s70,s62,5\ns_add_u32 s70,s70,s63\nv_cvt_pk_bf16_f32 v152,v36,v37\nv_cvt_pk_bf16_f32 v153,v38,v39\nM v[36:39],a[4:7],v[208:211],0\n" \
"s_lshl2_add_u32 s70,s70,s7\ns_lshl2_add_u32 s79,s70,90112\nv_cvt_pk_bf16_f32 v168,v96,v97\nv_cvt_pk_bf16_f32 v169,v98,v99\nds_read_b64_tr_b16 v[220:221],v242 offset:1536\nds_read_b64_tr_b16 v[222:223],v242 offset:1792\nM v[96:99],a[8:11],v[208:211],0\ns_lshl_b32 s70,s60,4\n" \
"v_cvt_pk_bf16_f32 v184,v100,v101\nv_cvt_pk_bf16_f32 v185,v102,v103\ns_sub_u32 s70,s70,s62\nM v[100:103],a[12:15],v[208:211],0\ns_add_u32 s70,s70,15\nv_cvt_pk_bf16_f32 v138,v40,v41\nv_cvt_pk_bf16_f32 v139,v42,v43\ns_lshl2_add_u32 s70,s70,s7\n" \
"s_waitcnt lgkmcnt(4)\nM v[40:43],a[0:3],v[212:215],0\ns_lshl_b32 s70,s70,15\ns_lshl_b32 s71,s61,8\nv_cvt_pk_bf16_f32 v154,v44,v45\nv_cvt_pk_bf16_f32 v155,v46,v47\ns_add_u32 s70,s70,s71\nM v[44:47],a[4:7],v[212:215],0\n" \
"s_add_u32 s78,s70,54525952\nv_cvt_pk_bf16_f32 v170,v104,v105\nv_cvt_pk_bf16_f32 v171,v106,v107\ns_lshl3_add_u32 s70,s62,s7\nds_read_b64_tr_b16 v[192:193],v241 offset:8192\nds_read_b64_tr_b16 v[194:195],v241 offset:8448\nM v[104:107],a[8:11],v[212:215],0\ns_lshl_b32 s80,s70,6\n" \
"s_lshl_b32 s76,s70,15\nv_cvt_pk_bf16_f32 v186,v108,v109\nv_cvt_pk_bf16_f32 v187,v110,v111\ns_lshl_b32 s70,s63,5\nM v[108:111],a[12:15],v[212:215],0\ns_add_u32 s70,s70,s74\nv_cvt_pk_bf16_f32 v140,v48,v49\nv_cvt_pk_bf16_f32 v141,v50,v51\n" \
"s_lshl_b32 s74,s70,14\ns_waitcnt lgkmcnt(2)\nM v[48:51],a[0:3],v[216:219],0\ns_add_u32 s74,s74,3145728\nv_cvt_pk_bf16_f32 v156,v52,v53\nv_cvt_pk_bf16_f32 v157,v54,v55\nbuffer_store_dwordx4 v[128:131],v240,s[52:55],s74 offen offset:0 sc1\nM v[52:55],a[4:7],v[216:219],0\n" \
"v_cvt_pk_bf16_f32 v172,v112,v113\nv_cvt_pk_bf16_f32 v173,v114,v115\nds_read_b64_tr_b16 v[196:197],v242 offset:8192\nds_read_b64_tr_b16 v[198:199],v242 offset:8448\nM v[112:115],a[8:11],v[216:219],0\nv_cvt_pk_bf16_f32 v188,v116,v117\nv_cvt_pk_bf16_f32 v189,v118,v119\nM v[116:119],a[12:15],v[216:219],0\n" \
"v_cvt_pk_bf16_f32 v142,v56,v57\nv_cvt_pk_bf16_f32 v143,v58,v59\nM v[56:59],a[0:3],v[220:223],0\nv_cvt_pk_bf16_f32 v158,v60,v61\nv_cvt_pk_bf16_f32 v159,v62,v63\nM v[60:63],a[4:7],v[220:223],0\nv_cvt_pk_bf16_f32 v174,v120,v121\nv_cvt_pk_bf16_f32 v175,v122,v123\n" \
"ds_read_b64_tr_b16 v[200:201],v241 offset:8704\nds_read_b64_tr_b16 v[202:203],v241 offset:8960\nM v[120:123],a[8:11],v[220:223],0\ns_add_u32 m0,s64,131072\ns_add_u32 s69,s68,1048576\nv_cvt_pk_bf16_f32 v190,v124,v125\nv_cvt_pk_bf16_f32 v191,v126,v127\nM v[124:127],a[12:15],v[220:223],0\n" \
"ds_read_b64_tr_b16 v[204:205],v242 offset:8704\ns_waitcnt lgkmcnt(5)\nM v[0:3],a[16:19],v[192:195],v[0:3]\nds_read_b64_tr_b16 v[206:207],v242 offset:8960\nds_read_b64_tr_b16 v[208:209],v241 offset:9216\nM v[4:7],a[20:23],v[192:195],v[4:7]\nds_read_b64_tr_b16 v[210:211],v241 offset:9472\nds_read_b64_tr_b16 v[212:213],v242 offset:9216\n" \
"M v[64:67],a[24:27],v[192:195],v[64:67]\nds_read_b64_tr_b16 v[214:215],v242 offset:9472\nds_read_b64_tr_b16 v[216:217],v241 offset:9728\nM v[68:71],a[28:31],v[192:195],v[68:71]\nds_read_b64_tr_b16 v[218:219],v241 offset:9984\ns_waitcnt lgkmcnt(10)\nM v[8:11],a[16:19],v[196:199],v[8:11]\nds_read_b64_tr_b16 v[220:221],v242 offset:9728\n" \
"ds_read_b64_tr_b16 v[222:223],v242 offset:9984\nM v[12:15],a[20:23],v[196:199],v[12:15]\nbuffer_load_dwordx4 v243,s[40:43],s69 offen offset:0 lds\nM v[72:75],a[24:27],v[196:199],v[72:75]\ns_add_u32 s80,s80,90112\ns_add_u32 s76,s76,1048576\nM v[76:79],a[28:31],v[196:199],v[76:79]\ns_add_u32 s77,s76,131072\n" \
"s_waitcnt lgkmcnt(10)\nM v[16:19],a[16:19],v[200:203],v[16:19]\ns_lshl2_add_u32 s29,s70,81920\ns_lshl_b32 s70,s7,14\nM v[20:23],a[20:23],v[200:203],v[20:23]\nbuffer_store_dwordx4 v[132:135],v240,s[52:55],s74 offen offset:1024 sc1\nM v[80:83],a[24:27],v[200:203],v[80:83]\nv_add_u32 v250,s70,v240\n" \
"s_lshl_b32 s70,s63,19\nM v[84:87],a[28:31],v[200:203],v[84:87]\ns_add_u32 s72,s78,16384\ns_waitcnt lgkmcnt(6)\nM v[24:27],a[16:19],v[204:207],v[24:27]\ns_add_u32 s44,s34,s70\ns_addc_u32 s45,s35,0\nM v[28:31],a[20:23],v[204:207],v[28:31]\n" \
"buffer_load_dwordx4 v244,s[40:43],s69 offen offset:1024 lds\nM v[88:91],a[24:27],v[204:207],v[88:91]\ns_waitcnt vmcnt(7) lgkmcnt(0)\ns_barrier\nM v[92:95],a[28:31],v[204:207],v[92:95]\nds_read_b64_tr_b16 v[192:193],v241 offset:16384\nds_read_b64_tr_b16 v[194:195],v241 offset:16640\nM v[32:35],a[16:19],v[208:211],v[32:35]\n" \
"ds_read_b64_tr_b16 v[196:197],v242 offset:16384\nds_read_b64_tr_b16 v[198:199],v242 offset:16640\nM v[36:39],a[20:23],v[208:211],v[36:39]\nds_read_b64_tr_b16 v[200:201],v241 offset:16896\nds_read_b64_tr_b16 v[202:203],v241 offset:17152\nM v[96:99],a[24:27],v[208:211],v[96:99]\nds_read_b64_tr_b16 v[204:205],v242 offset:16896\nds_read_b64_tr_b16 v[206:207],v242 offset:17152\n" \
"M v[100:103],a[28:31],v[208:211],v[100:103]\nds_read_b64_tr_b16 v[208:209],v241 offset:17408\nds_read_b64_tr_b16 v[210:211],v241 offset:17664\nM v[40:43],a[16:19],v[212:215],v[40:43]\nbuffer_load_dwordx4 v245,s[40:43],s69 offen offset:2048 lds\nM v[44:47],a[20:23],v[212:215],v[44:47]\ns_lshl_b32 s70,s62,15\ns_add_u32 s96,s70,s64\n" \
"M v[104:107],a[24:27],v[212:215],v[104:107]\ns_sub_u32 s96,s96,16384\ns_sub_u32 s46,491520,s70\nM v[108:111],a[28:31],v[212:215],v[108:111]\nds_read_b64_tr_b16 v[212:213],v242 offset:17408\nds_read_b64_tr_b16 v[214:215],v242 offset:17664\nM v[48:51],a[16:19],v[216:219],v[48:51]\nbuffer_store_dwordx4 v[136:139],v240,s[52:55],s74 offen offset:2048 sc1\n" \
"M v[52:55],a[20:23],v[216:219],v[52:55]\nds_write_b64 v250,v[128:129] offset:0\ns_lshl2_add_u32 s70,s62,0\nM v[112:115],a[24:27],v[216:219],v[112:115]\ns_cmp_eq_u32 s36,s96\ns_cselect_b32 s72,0xc000,s98\ns_add_u32 s65,s36,s72\nM v[116:119],a[28:31],v[216:219],v[116:119]\n" \
"ds_read_b64_tr_b16 v[216:217],v241 offset:17920\nds_read_b64_tr_b16 v[218:219],v241 offset:18176\nM v[56:59],a[16:19],v[220:223],v[56:59]\nbuffer_load_dwordx4 v246,s[40:43],s69 offen offset:3072 lds\nM v[60:63],a[20:23],v[220:223],v[60:63]\ns_add_u32 m0,s64,147456\ns_add_u32 s69,s68,1572864\nM v[120:123],a[24:27],v[220:223],v[120:123]\n" \
"s_cmp_eq_u32 s65,s96\ns_cselect_b32 s72,0xc000,s98\ns_add_u32 s66,s65,s72\nM v[124:127],a[28:31],v[220:223],v[124:127]\nds_read_b64_tr_b16 v[220:221],v242 offset:17920\ns_waitcnt lgkmcnt(6)\nM v[0:3],a[32:35],v[192:195],v[0:3]\nds_read_b64_tr_b16 v[222:223],v242 offset:18176\n" \
"s_sub_u32 s70,s70,s7\nM v[4:7],a[36:39],v[192:195],v[4:7]\nbuffer_load_dwordx4 v243,s[40:43],s69 offen offset:0 lds\nM v[64:67],a[40:43],v[192:195],v[64:67]\ns_lshl_b32 s70,s70,16\ns_add_u32 s33,s78,s70\nM v[68:71],a[44:47],v[192:195],v[68:71]\nds_read_b64_tr_b16 v[192:193],v241 offset:24576\n" \
"ds_read_b64_tr_b16 v[194:195],v241 offset:24832\nM v[8:11],a[32:35],v[196:199],v[8:11]\nds_write_b64 v250,v[130:131] offset:8\ns_lshl3_add_u32 s70,s62,s7\nM v[12:15],a[36:39],v[196:199],v[12:15]\nbuffer_load_dwordx4 v244,s[40:43],s69 offen offset:1024 lds\nM v[72:75],a[40:43],v[196:199],v[72:75]\ns_cmp_eq_u32 s66,s96\n" \
"s_cselect_b32 s72,0xc000,s98\ns_add_u32 s95,s66,s72\nM v[76:79],a[44:47],v[196:199],v[76:79]\nds_read_b64_tr_b16 v[196:197],v242 offset:24576\nds_read_b64_tr_b16 v[198:199],v242 offset:24832\nM v[16:19],a[32:35],v[200:203],v[16:19]\ns_cmp_eq_u32 s62,7\ns_cselect_b32 s46,229376,s46\n" \
"M v[20:23],a[36:39],v[200:203],v[20:23]\nbuffer_load_dwordx4 v245,s[40:43],s69 offen offset:2048 lds\nM v[80:83],a[40:43],v[200:203],v[80:83]\ns_lshl3_add_u32 s70,s70,s60\ns_lshl_b32 s67,s70,15\nM v[84:87],a[44:47],v[200:203],v[84:87]\nds_read_b64_tr_b16 v[200:201],v241 offset:25088\nds_read_b64_tr_b16 v[202:203],v241 offset:25344\n" \
"M v[24:27],a[32:35],v[204:207],v[24:27]\ns_cmp_gt_i32 s62,-1\ns_cselect_b32 s72,s98,0xfffe4000\nM v[28:31],a[36:39],v[204:207],v[28:31]\nbuffer_store_dwordx4 v[140:143],v240,s[52:55],s74 offen offset:3072 sc1\nM v[88:91],a[40:43],v[204:207],v[88:91]\ns_add_u32 s74,s74,4096\ns_add_u32 s15,s64,s46\n" \
"M v[92:95],a[44:47],v[204:207],v[92:95]\nds_read_b64_tr_b16 v[204:205],v242 offset:25088\nds_read_b64_tr_b16 v[206:207],v242 offset:25344\nM v[32:35],a[32:35],v[208:211],v[32:35]\nds_write_b64 v250,v[132:133] offset:1024\ns_lshl3_add_u32 s70,s62,s60\nM v[36:39],a[36:39],v[208:211],v[36:39]\nbuffer_load_dwordx4 v246,s[40:43],s69 offen offset:3072 lds\n" \
"M v[96:99],a[40:43],v[208:211],v[96:99]\ns_add_u32 m0,s64,98304\ns_add_u32 s69,s68,2097152\nM v[100:103],a[44:47],v[208:211],v[100:103]\nds_read_b64_tr_b16 v[208:209],v241 offset:25600\ns_waitcnt lgkmcnt(6)\nM v[40:43],a[32:35],v[212:215],v[40:43]\nds_read_b64_tr_b16 v[210:211],v241 offset:25856\n" \
"s_add_u32 s15,s15,s72\nM v[44:47],a[36:39],v[212:215],v[44:47]\nbuffer_load_dwordx4 v243,s[40:43],s69 offen offset:0 lds\nM v[104:107],a[40:43],v[212:215],v[104:107]\ns_lshl2_add_u32 s70,s70,s7\ns_add_u32 s97,s96,65536\nM v[108:111],a[44:47],v[212:215],v[108:111]\nds_read_b64_tr_b16 v[212:213],v242 offset:25600\n" \
"ds_read_b64_tr_b16 v[214:215],v242 offset:25856\nM v[48:51],a[32:35],v[216:219],v[48:51]\ns_cmp_eq_u32 s62,7\ns_cselect_b32 s72,98304,0\nM v[52:55],a[36:39],v[216:219],v[52:55]\nbuffer_load_dwordx4 v244,s[40:43],s69 offen offset:1024 lds\nM v[112:115],a[40:43],v[216:219],v[112:115]\ns_sub_u32 s97,s97,s72\n" \
"s_mul_i32 s71,s60,14\nM v[116:119],a[44:47],v[216:219],v[116:119]\nds_read_b64_tr_b16 v[216:217],v241 offset:26112\nds_read_b64_tr_b16 v[218:219],v241 offset:26368\nM v[56:59],a[32:35],v[220:223],v[56:59]\nds_write_b64 v250,v[134:135] offset:1032\ns_lshl_b32 s72,s60,1\nM v[60:63],a[36:39],v[220:223],v[60:63]\n" \
"buffer_store_dwordx4 v[144:147],v240,s[52:55],s74 offen offset:0 sc1\nM v[120:123],a[40:43],v[220:223],v[120:123]\ns_add_u32 s72,s72,s62\ns_sub_u32 s72,s72,6\nM v[124:127],a[44:47],v[220:223],v[124:127]\nds_read_b64_tr_b16 v[220:221],v242 offset:26112\nds_read_b64_tr_b16 v[222:223],v242 offset:26368\nM v[0:3],a[48:51],v[192:195],v[0:3]\n" \
"s_lshl_b32 s72,s72,18\nv_and_b32 v251,0xf0,v240\nM v[4:7],a[52:55],v[192:195],v[4:7]\nbuffer_load_dwordx4 v245,s[40:43],s69 offen offset:2048 lds\nM v[64:67],a[56:59],v[192:195],v[64:67]\ns_add_u32 s48,s44,19922944\ns_addc_u32 s49,s45,0\nM v[68:71],a[60:63],v[192:195],v[68:71]\n" \
"v_lshl_or_b32 v253,v253,12,v251\ns_mov_b32 s50,s46\nM v[8:11],a[48:51],v[196:199],v[8:11]\ns_add_u32 s44,s44,3145728\ns_addc_u32 s45,s45,0\nM v[12:15],a[52:55],v[196:199],v[12:15]\nbuffer_load_dwordx4 v246,s[40:43],s69 offen offset:3072 lds\nM v[72:75],a[56:59],v[196:199],v[72:75]\n" \
"s_sub_u32 s33,s33,1867776\ns_add_u32 s67,s67,36700160\nM v[76:79],a[60:63],v[196:199],v[76:79]\ns_waitcnt lgkmcnt(2)\nM v[16:19],a[48:51],v[200:203],v[16:19]\nds_write_b64 v250,v[136:137] offset:2048\nM v[20:23],a[52:55],v[200:203],v[20:23]\nM v[80:83],a[56:59],v[200:203],v[80:83]\n" \
"M v[84:87],a[60:63],v[200:203],v[84:87]\nM v[24:27],a[48:51],v[204:207],v[24:27]\ns_add_u32 m0,s64,114688\ns_add_u32 s69,s68,2621440\nM v[28:31],a[52:55],v[204:207],v[28:31]\nM v[88:91],a[56:59],v[204:207],v[88:91]\ns_waitcnt vmcnt(10) lgkmcnt(1)\ns_barrier\n" \
"M v[92:95],a[60:63],v[204:207],v[92:95]\nbuffer_store_dwordx4 v[148:151],v240,s[52:55],s74 offen offset:1024 sc1\nM v[32:35],a[48:51],v[208:211],v[32:35]\nds_read_b64_tr_b16 v[192:193],v241 offset:32768\nds_read_b64_tr_b16 v[194:195],v241 offset:33024\nM v[36:39],a[52:55],v[208:211],v[36:39]\nds_read_b64_tr_b16 v[196:197],v242 offset:32768\nds_read_b64_tr_b16 v[198:199],v242 offset:33024\n" \
"M v[96:99],a[56:59],v[208:211],v[96:99]\nds_read_b64_tr_b16 v[200:201],v241 offset:33280\nds_read_b64_tr_b16 v[202:203],v241 offset:33536\nM v[100:103],a[60:63],v[208:211],v[100:103]\nbuffer_store_dword v252,off,s[52:55],s79 sc1\nM v[40:43],a[48:51],v[212:215],v[40:43]\nds_read_b64_tr_b16 v[204:205],v242 offset:33280\nds_read_b64_tr_b16 v[206:207],v242 offset:33536\n" \
"M v[44:47],a[52:55],v[212:215],v[44:47]\nds_read_b64_tr_b16 v[208:209],v241 offset:33792\nds_read_b64_tr_b16 v[210:211],v241 offset:34048\nM v[104:107],a[56:59],v[212:215],v[104:107]\nbuffer_load_dwordx4 v243,s[40:43],s69 offen offset:0 lds\nM v[108:111],a[60:63],v[212:215],v[108:111]\nds_read_b64_tr_b16 v[212:213],v242 offset:33792\nds_read_b64_tr_b16 v[214:215],v242 offset:34048\n" \
"M v[48:51],a[48:51],v[216:219],v[48:51]\nds_write_b64 v250,v[138:139] offset:2056\ns_lshl4_add_u32 s79,s70,65536\nM v[52:55],a[52:55],v[216:219],v[52:55]\ns_lshr_b32 s70,s6,3\ns_sub_u32 s70,s70,16\nM v[112:115],a[56:59],v[216:219],v[112:115]\nbuffer_load_dwordx4 v244,s[40:43],s69 offen offset:1024 lds\n" \
"M v[116:119],a[60:63],v[216:219],v[116:119]\nds_read_b64_tr_b16 v[216:217],v241 offset:34304\nds_read_b64_tr_b16 v[218:219],v241 offset:34560\nM v[56:59],a[48:51],v[220:223],v[56:59]\ns_add_u32 s2,s70,s71\ns_mul_i32 s71,s60,448\nM v[60:63],a[52:55],v[220:223],v[60:63]\ns_and_b32 s71,s71,0xffffff80\n" \
"s_sub_u32 s58,1835008,s71\nM v[120:123],a[56:59],v[220:223],v[120:123]\nbuffer_load_dwordx4 v245,s[40:43],s69 offen offset:2048 lds\nM v[124:127],a[60:63],v[220:223],v[124:127]\nds_read_b64_tr_b16 v[220:221],v242 offset:34304\ns_waitcnt lgkmcnt(7)\nM v[0:3],a[64:67],v[192:195],v[0:3]\nds_read_b64_tr_b16 v[222:223],v242 offset:34560\n" \
"s_lshl_b32 s2,s2,5\nM v[4:7],a[68:71],v[192:195],v[4:7]\nds_write_b64 v250,v[140:141] offset:3072\nM v[64:67],a[72:75],v[192:195],v[64:67]\nbuffer_store_dwordx4 v[152:155],v240,s[52:55],s74 offen offset:2048 sc1\nM v[68:71],a[76:79],v[192:195],v[68:71]\nds_read_b64_tr_b16 v[192:193],v241 offset:40960\nds_read_b64_tr_b16 v[194:195],v241 offset:41216\n" \
"M v[8:11],a[64:67],v[196:199],v[8:11]\ns_cmp_ge_u32 s62,6\ns_cselect_b64 s[56:57],s[18:19],s[56:57]\ns_cselect_b32 s71,s72,s71\ns_cselect_b32 s58,0x40000,s58\ns_cselect_b32 s0,2048,s0\nM v[12:15],a[68:71],v[196:199],v[12:15]\ns_mul_i32 s72,s60,229376\n" \
"M v[72:75],a[72:75],v[196:199],v[72:75]\nbuffer_load_dwordx4 v246,s[40:43],s69 offen offset:3072 lds\nM v[76:79],a[76:79],v[196:199],v[76:79]\nds_read_b64_tr_b16 v[196:197],v242 offset:40960\nds_read_b64_tr_b16 v[198:199],v242 offset:41216\nM v[16:19],a[64:67],v[200:203],v[16:19]\ns_cmp_eq_u32 s62,3\ns_cselect_b64 s[56:57],s[26:27],s[56:57]\n" \
"s_cselect_b32 s71,s72,s71\ns_cselect_b32 s58,229376,s58\ns_cselect_b32 s0,4096,s0\nM v[20:23],a[68:71],v[200:203],v[20:23]\ns_cmp_lt_u32 s70,14\ns_cselect_b32 s2,s2,0\ns_cselect_b32 s3,-1,0\nM v[80:83],a[72:75],v[200:203],v[80:83]\n" \
"s_lshl2_add_u32 s70,s61,s7\ns_mul_i32 s1,s70,s0\nM v[84:87],a[76:79],v[200:203],v[84:87]\nds_read_b64_tr_b16 v[200:201],v241 offset:41472\nds_read_b64_tr_b16 v[202:203],v241 offset:41728\nM v[24:27],a[64:67],v[204:207],v[24:27]\nds_write_b64 v250,v[142:143] offset:3080\ns_lshl_b32 s70,s61,8\n" \
"M v[28:31],a[68:71],v[204:207],v[28:31]\ns_cmp_lt_u32 s62,3\ns_cselect_b32 s58,0,s58\nM v[88:91],a[72:75],v[204:207],v[88:91]\ns_add_u32 s56,s56,s71\ns_addc_u32 s57,s57,0\nM v[92:95],a[76:79],v[204:207],v[92:95]\nds_read_b64_tr_b16 v[204:205],v242 offset:41472\n" \
"s_waitcnt lgkmcnt(6)\nM v[32:35],a[64:67],v[208:211],v[32:35]\nds_read_b64_tr_b16 v[206:207],v242 offset:41728\ns_lshl_b32 s0,s0,4\nM v[36:39],a[68:71],v[208:211],v[36:39]\nM v[96:99],a[72:75],v[208:211],v[96:99]\nbuffer_store_dwordx4 v[156:159],v240,s[52:55],s74 offen offset:3072 sc1\nM v[100:103],a[76:79],v[208:211],v[100:103]\n" \
"ds_read_b64_tr_b16 v[208:209],v241 offset:41984\nds_read_b64_tr_b16 v[210:211],v241 offset:42240\nM v[40:43],a[64:67],v[212:215],v[40:43]\ns_add_u32 s74,s74,4096\nM v[44:47],a[68:71],v[212:215],v[44:47]\nM v[104:107],a[72:75],v[212:215],v[104:107]\nM v[108:111],a[76:79],v[212:215],v[108:111]\nds_read_b64_tr_b16 v[212:213],v242 offset:41984\n" \
"ds_read_b64_tr_b16 v[214:215],v242 offset:42240\nM v[48:51],a[64:67],v[216:219],v[48:51]\nds_write_b64 v250,v[144:145] offset:4096\nM v[52:55],a[68:71],v[216:219],v[52:55]\nM v[112:115],a[72:75],v[216:219],v[112:115]\nM v[116:119],a[76:79],v[216:219],v[116:119]\nds_read_b64_tr_b16 v[216:217],v241 offset:42496\nds_read_b64_tr_b16 v[218:219],v241 offset:42752\n" \
"M v[56:59],a[64:67],v[220:223],v[56:59]\nM v[60:63],a[68:71],v[220:223],v[60:63]\nM v[120:123],a[72:75],v[220:223],v[120:123]\nM v[124:127],a[76:79],v[220:223],v[124:127]\nds_read_b64_tr_b16 v[220:221],v242 offset:42496\nds_read_b64_tr_b16 v[222:223],v242 offset:42752\nM v[0:3],a[80:83],v[192:195],v[0:3]\nM v[4:7],a[84:87],v[192:195],v[4:7]\n" \
"M v[64:67],a[88:91],v[192:195],v[64:67]\nM v[68:71],a[92:95],v[192:195],v[68:71]\ns_waitcnt lgkmcnt(7)\nM v[8:11],a[80:83],v[196:199],v[8:11]\nbuffer_store_dwordx4 v[160:163],v240,s[52:55],s74 offen offset:0 sc1\nM v[12:15],a[84:87],v[196:199],v[12:15]\nds_write_b64 v250,v[146:147] offset:4104\nM v[72:75],a[88:91],v[196:199],v[72:75]\n" \
"M v[76:79],a[92:95],v[196:199],v[76:79]\nM v[16:19],a[80:83],v[200:203],v[16:19]\nM v[20:23],a[84:87],v[200:203],v[20:23]\nM v[80:83],a[88:91],v[200:203],v[80:83]\nM v[84:87],a[92:95],v[200:203],v[84:87]\nM v[24:27],a[80:83],v[204:207],v[24:27]\ns_add_u32 m0,s64,131072\ns_add_u32 s69,s68,3145728\n" \
"M v[28:31],a[84:87],v[204:207],v[28:31]\nM v[88:91],a[88:91],v[204:207],v[88:91]\ns_waitcnt vmcnt(14) lgkmcnt(1)\ns_barrier\nM v[92:95],a[92:95],v[204:207],v[92:95]\nds_read_b64_tr_b16 v[192:193],v241 offset:49152\nds_read_b64_tr_b16 v[194:195],v241 offset:49408\nM v[32:35],a[80:83],v[208:211],v[32:35]\n" \
"ds_read_b64_tr_b16 v[196:197],v242 offset:49152\nds_read_b64_tr_b16 v[198:199],v242 offset:49408\nM v[36:39],a[84:87],v[208:211],v[36:39]\nds_read_b64_tr_b16 v[200:201],v241 offset:49664\nds_read_b64_tr_b16 v[202:203],v241 offset:49920\nM v[96:99],a[88:91],v[208:211],v[96:99]\nds_read_b64_tr_b16 v[204:205],v242 offset:49664\nds_read_b64_tr_b16 v[206:207],v242 offset:49920\n" \
"M v[100:103],a[92:95],v[208:211],v[100:103]\nds_read_b64_tr_b16 v[208:209],v241 offset:50176\nds_read_b64_tr_b16 v[210:211],v241 offset:50432\nM v[40:43],a[80:83],v[212:215],v[40:43]\nbuffer_load_dwordx4 v243,s[40:43],s69 offen offset:0 lds\nM v[44:47],a[84:87],v[212:215],v[44:47]\nds_write_b64 v250,v[148:149] offset:5120\nM v[104:107],a[88:91],v[212:215],v[104:107]\n" \
"M v[108:111],a[92:95],v[212:215],v[108:111]\nds_read_b64_tr_b16 v[212:213],v242 offset:50176\nds_read_b64_tr_b16 v[214:215],v242 offset:50432\nM v[48:51],a[80:83],v[216:219],v[48:51]\nbuffer_store_dwordx4 v[164:167],v240,s[52:55],s74 offen offset:1024 sc1\nM v[52:55],a[84:87],v[216:219],v[52:55]\nM v[112:115],a[88:91],v[216:219],v[112:115]\nM v[116:119],a[92:95],v[216:219],v[116:119]\n" \
"ds_read_b64_tr_b16 v[216:217],v241 offset:50688\nds_read_b64_tr_b16 v[218:219],v241 offset:50944\nM v[56:59],a[80:83],v[220:223],v[56:59]\nbuffer_load_dwordx4 v244,s[40:43],s69 offen offset:1024 lds\nM v[60:63],a[84:87],v[220:223],v[60:63]\nds_write_b64 v250,v[150:151] offset:5128\nM v[120:123],a[88:91],v[220:223],v[120:123]\nM v[124:127],a[92:95],v[220:223],v[124:127]\n" \
"ds_read_b64_tr_b16 v[220:221],v242 offset:50688\ns_waitcnt lgkmcnt(9)\nM v[0:3],a[96:99],v[192:195],v[0:3]\nbuffer_load_dwordx4 v245,s[40:43],s69 offen offset:2048 lds\nM v[4:7],a[100:103],v[192:195],v[4:7]\nds_read_b64_tr_b16 v[222:223],v242 offset:50944\nM v[64:67],a[104:107],v[192:195],v[64:67]\nM v[68:71],a[108:111],v[192:195],v[68:71]\n" \
"ds_read_b64_tr_b16 v[192:193],v241 offset:57344\nds_read_b64_tr_b16 v[194:195],v241 offset:57600\nM v[8:11],a[96:99],v[196:199],v[8:11]\nbuffer_load_dwordx4 v246,s[40:43],s69 offen offset:3072 lds\nM v[12:15],a[100:103],v[196:199],v[12:15]\nM v[72:75],a[104:107],v[196:199],v[72:75]\nM v[76:79],a[108:111],v[196:199],v[76:79]\nds_read_b64_tr_b16 v[196:197],v242 offset:57344\n" \
"ds_read_b64_tr_b16 v[198:199],v242 offset:57600\nM v[16:19],a[96:99],v[200:203],v[16:19]\nbuffer_store_dwordx4 v[168:171],v240,s[52:55],s74 offen offset:2048 sc1\nM v[20:23],a[100:103],v[200:203],v[20:23]\nds_write_b64 v250,v[152:153] offset:6144\nM v[80:83],a[104:107],v[200:203],v[80:83]\nM v[84:87],a[108:111],v[200:203],v[84:87]\nds_read_b64_tr_b16 v[200:201],v241 offset:57856\n" \
"ds_read_b64_tr_b16 v[202:203],v241 offset:58112\nM v[24:27],a[96:99],v[204:207],v[24:27]\nM v[28:31],a[100:103],v[204:207],v[28:31]\nM v[88:91],a[104:107],v[204:207],v[88:91]\nM v[92:95],a[108:111],v[204:207],v[92:95]\nds_read_b64_tr_b16 v[204:205],v242 offset:57856\ns_waitcnt lgkmcnt(8)\nM v[32:35],a[96:99],v[208:211],v[32:35]\n" \
"ds_read_b64_tr_b16 v[206:207],v242 offset:58112\nM v[36:39],a[100:103],v[208:211],v[36:39]\nM v[96:99],a[104:107],v[208:211],v[96:99]\nM v[100:103],a[108:111],v[208:211],v[100:103]\nds_read_b64_tr_b16 v[208:209],v241 offset:58368\nds_read_b64_tr_b16 v[210:211],v241 offset:58624\nM v[40:43],a[96:99],v[212:215],v[40:43]\nds_write_b64 v250,v[154:155] offset:6152\n" \
"M v[44:47],a[100:103],v[212:215],v[44:47]\nM v[104:107],a[104:107],v[212:215],v[104:107]\nM v[108:111],a[108:111],v[212:215],v[108:111]\nds_read_b64_tr_b16 v[212:213],v242 offset:58368\nds_read_b64_tr_b16 v[214:215],v242 offset:58624\nM v[48:51],a[96:99],v[216:219],v[48:51]\nM v[52:55],a[100:103],v[216:219],v[52:55]\nM v[112:115],a[104:107],v[216:219],v[112:115]\n" \
"buffer_store_dwordx4 v[172:175],v240,s[52:55],s74 offen offset:3072 sc1\nM v[116:119],a[108:111],v[216:219],v[116:119]\nds_read_b64_tr_b16 v[216:217],v241 offset:58880\nds_read_b64_tr_b16 v[218:219],v241 offset:59136\nM v[56:59],a[96:99],v[220:223],v[56:59]\ns_add_u32 s74,s74,4096\nM v[60:63],a[100:103],v[220:223],v[60:63]\nM v[120:123],a[104:107],v[220:223],v[120:123]\n" \
"M v[124:127],a[108:111],v[220:223],v[124:127]\nds_read_b64_tr_b16 v[220:221],v242 offset:58880\ns_waitcnt lgkmcnt(8)\nM v[0:3],a[112:115],v[192:195],v[0:3]\nds_write_b64 v250,v[156:157] offset:7168\nds_read_b64_tr_b16 v[222:223],v242 offset:59136\nM v[4:7],a[116:119],v[192:195],v[4:7]\nM v[64:67],a[120:123],v[192:195],v[64:67]\n" \
"M v[68:71],a[124:127],v[192:195],v[68:71]\nM v[8:11],a[112:115],v[196:199],v[8:11]\nM v[12:15],a[116:119],v[196:199],v[12:15]\nM v[72:75],a[120:123],v[196:199],v[72:75]\nM v[76:79],a[124:127],v[196:199],v[76:79]\nM v[16:19],a[112:115],v[200:203],v[16:19]\nM v[20:23],a[116:119],v[200:203],v[20:23]\nM v[80:83],a[120:123],v[200:203],v[80:83]\n" \
"M v[84:87],a[124:127],v[200:203],v[84:87]\nM v[24:27],a[112:115],v[204:207],v[24:27]\nbuffer_store_dwordx4 v[176:179],v240,s[52:55],s74 offen offset:0 sc1\nM v[28:31],a[116:119],v[204:207],v[28:31]\ns_add_u32 m0,s64,147456\ns_add_u32 s69,s68,3670016\nM v[88:91],a[120:123],v[204:207],v[88:91]\ns_waitcnt vmcnt(17) lgkmcnt(0)\n" \
"s_barrier\nM v[92:95],a[124:127],v[204:207],v[92:95]\nds_read_b64_tr_b16 v[192:193],v241 offset:0\nds_read_b64_tr_b16 v[194:195],v241 offset:256\nM v[32:35],a[112:115],v[208:211],v[32:35]\nds_read_b64_tr_b16 v[196:197],v242 offset:0\nds_read_b64_tr_b16 v[198:199],v242 offset:256\nM v[36:39],a[116:119],v[208:211],v[36:39]\n" \
"ds_read_b64_tr_b16 v[200:201],v241 offset:512\nds_read_b64_tr_b16 v[202:203],v241 offset:768\nM v[96:99],a[120:123],v[208:211],v[96:99]\nds_read_b64_tr_b16 v[204:205],v242 offset:512\nds_read_b64_tr_b16 v[206:207],v242 offset:768\nM v[100:103],a[124:127],v[208:211],v[100:103]\nds_read_b64_tr_b16 v[208:209],v241 offset:1024\nds_read_b64_tr_b16 v[210:211],v241 offset:1280\n" \
"M v[40:43],a[112:115],v[212:215],v[40:43]\nbuffer_load_dwordx4 v243,s[40:43],s69 offen offset:0 lds\nM v[44:47],a[116:119],v[212:215],v[44:47]\nds_write_b64 v250,v[158:159] offset:7176\nM v[104:107],a[120:123],v[212:215],v[104:107]\nM v[108:111],a[124:127],v[212:215],v[108:111]\nds_read_b64_tr_b16 v[212:213],v242 offset:1024\nds_read_b64_tr_b16 v[214:215],v242 offset:1280\n" \
"M v[48:51],a[112:115],v[216:219],v[48:51]\nbuffer_load_dwordx4 v244,s[40:43],s69 offen offset:1024 lds\nM v[52:55],a[116:119],v[216:219],v[52:55]\nds_write_b64 v250,v[160:161] offset:8192\nM v[112:115],a[120:123],v[216:219],v[112:115]\nM v[116:119],a[124:127],v[216:219],v[116:119]\nds_read_b64_tr_b16 v[216:217],v241 offset:1536\nds_read_b64_tr_b16 v[218:219],v241 offset:1792\n" \
"M v[56:59],a[112:115],v[220:223],v[56:59]\nbuffer_load_dwordx4 v245,s[40:43],s69 offen offset:2048 lds\nM v[60:63],a[116:119],v[220:223],v[60:63]\nM v[120:123],a[120:123],v[220:223],v[120:123]\nM v[124:127],a[124:127],v[220:223],v[124:127]\nds_read_b64_tr_b16 v[220:221],v242 offset:1536\ns_waitcnt lgkmcnt(9)\nM v[0:3],a[128:131],v[192:195],v[0:3]\n" \
"buffer_store_dwordx4 v[180:183],v240,s[52:55],s74 offen offset:1024 sc1\nM v[4:7],a[132:135],v[192:195],v[4:7]\nds_read_b64_tr_b16 v[222:223],v242 offset:1792\nM v[64:67],a[136:139],v[192:195],v[64:67]\nM v[68:71],a[140:143],v[192:195],v[68:71]\nds_read_b64_tr_b16 v[192:193],v241 offset:8192\nds_read_b64_tr_b16 v[194:195],v241 offset:8448\nM v[8:11],a[128:131],v[196:199],v[8:11]\n" \
"buffer_load_dwordx4 v246,s[40:43],s69 offen offset:3072 lds\nM v[12:15],a[132:135],v[196:199],v[12:15]\nds_write_b64 v250,v[162:163] offset:8200\nM v[72:75],a[136:139],v[196:199],v[72:75]\nM v[76:79],a[140:143],v[196:199],v[76:79]\nds_read_b64_tr_b16 v[196:197],v242 offset:8192\nds_read_b64_tr_b16 v[198:199],v242 offset:8448\nM v[16:19],a[128:131],v[200:203],v[16:19]\n" \
"M v[20:23],a[132:135],v[200:203],v[20:23]\nM v[80:83],a[136:139],v[200:203],v[80:83]\nM v[84:87],a[140:143],v[200:203],v[84:87]\nds_read_b64_tr_b16 v[200:201],v241 offset:8704\nds_read_b64_tr_b16 v[202:203],v241 offset:8960\nM v[24:27],a[128:131],v[204:207],v[24:27]\nM v[28:31],a[132:135],v[204:207],v[28:31]\nM v[88:91],a[136:139],v[204:207],v[88:91]\n" \
"M v[92:95],a[140:143],v[204:207],v[92:95]\nds_read_b64_tr_b16 v[204:205],v242 offset:8704\ns_waitcnt lgkmcnt(9)\nM v[32:35],a[128:131],v[208:211],v[32:35]\nbuffer_store_dwordx4 v[184:187],v240,s[52:55],s74 offen offset:2048 sc1\nM v[36:39],a[132:135],v[208:211],v[36:39]\nds_write_b64 v250,v[164:165] offset:9216\nds_read_b64_tr_b16 v[206:207],v242 offset:8960\n" \
"M v[96:99],a[136:139],v[208:211],v[96:99]\nM v[100:103],a[140:143],v[208:211],v[100:103]\nds_read_b64_tr_b16 v[208:209],v241 offset:9216\nds_read_b64_tr_b16 v[210:211],v241 offset:9472\nM v[40:43],a[128:131],v[212:215],v[40:43]\nM v[44:47],a[132:135],v[212:215],v[44:47]\nM v[104:107],a[136:139],v[212:215],v[104:107]\nbuffer_load_dword v248,v228,s[52:55],s80 offen sc1\n" \
"M v[108:111],a[140:143],v[212:215],v[108:111]\nds_read_b64_tr_b16 v[212:213],v242 offset:9216\nds_read_b64_tr_b16 v[214:215],v242 offset:9472\nM v[48:51],a[128:131],v[216:219],v[48:51]\nM v[52:55],a[132:135],v[216:219],v[52:55]\nM v[112:115],a[136:139],v[216:219],v[112:115]\nM v[116:119],a[140:143],v[216:219],v[116:119]\nds_read_b64_tr_b16 v[216:217],v241 offset:9728\n" \
"s_waitcnt lgkmcnt(8)\nM v[56:59],a[128:131],v[220:223],v[56:59]\nds_write_b64 v250,v[166:167] offset:9224\nds_read_b64_tr_b16 v[218:219],v241 offset:9984\nM v[60:63],a[132:135],v[220:223],v[60:63]\nM v[120:123],a[136:139],v[220:223],v[120:123]\nM v[124:127],a[140:143],v[220:223],v[124:127]\nds_read_b64_tr_b16 v[220:221],v242 offset:9728\n" \
"ds_read_b64_tr_b16 v[222:223],v242 offset:9984\nM v[0:3],a[144:147],v[192:195],v[0:3]\nM v[4:7],a[148:151],v[192:195],v[4:7]\nM v[64:67],a[152:155],v[192:195],v[64:67]\nbuffer_store_dwordx4 v[188:191],v240,s[52:55],s74 offen offset:3072 sc1\nM v[68:71],a[156:159],v[192:195],v[68:71]\ns_add_u32 s74,s74,16764928\nM v[8:11],a[144:147],v[196:199],v[8:11]\n" \
"M v[12:15],a[148:151],v[196:199],v[12:15]\nM v[72:75],a[152:155],v[196:199],v[72:75]\nM v[76:79],a[156:159],v[196:199],v[76:79]\nM v[16:19],a[144:147],v[200:203],v[16:19]\nds_write_b64 v250,v[168:169] offset:10240\nM v[20:23],a[148:151],v[200:203],v[20:23]\nM v[80:83],a[152:155],v[200:203],v[80:83]\nM v[84:87],a[156:159],v[200:203],v[84:87]\n" \
"s_waitcnt lgkmcnt(5)\nM v[24:27],a[144:147],v[204:207],v[24:27]\ns_sub_u32 s40,s12,0x1000\ns_subb_u32 s41,s13,0\nM v[28:31],a[148:151],v[204:207],v[28:31]\ns_add_u32 m0,s64,98304\ns_add_u32 s69,s68,0\nM v[88:91],a[152:155],v[204:207],v[88:91]\n" \
"s_waitcnt vmcnt(18) lgkmcnt(1)\ns_barrier\nM v[92:95],a[156:159],v[204:207],v[92:95]\nds_read_b64_tr_b16 v[192:193],v241 offset:16384\nds_read_b64_tr_b16 v[194:195],v241 offset:16640\nM v[32:35],a[144:147],v[208:211],v[32:35]\nds_read_b64_tr_b16 v[196:197],v242 offset:16384\nds_read_b64_tr_b16 v[198:199],v242 offset:16640\n" \
"M v[36:39],a[148:151],v[208:211],v[36:39]\nds_read_b64_tr_b16 v[200:201],v241 offset:16896\nds_read_b64_tr_b16 v[202:203],v241 offset:17152\nM v[96:99],a[152:155],v[208:211],v[96:99]\nds_read_b64_tr_b16 v[204:205],v242 offset:16896\nds_read_b64_tr_b16 v[206:207],v242 offset:17152\nM v[100:103],a[156:159],v[208:211],v[100:103]\nds_read_b64_tr_b16 v[208:209],v241 offset:17408\n" \
"ds_read_b64_tr_b16 v[210:211],v241 offset:17664\nM v[40:43],a[144:147],v[212:215],v[40:43]\nbuffer_load_dwordx4 v243,s[40:43],s69 offen offset:0 lds\nM v[44:47],a[148:151],v[212:215],v[44:47]\nds_write_b64 v250,v[170:171] offset:10248\ns_lshl_b32 s12,s61,13\nM v[104:107],a[152:155],v[212:215],v[104:107]\nM v[108:111],a[156:159],v[212:215],v[108:111]\n" \
"ds_read_b64_tr_b16 v[212:213],v242 offset:17408\nds_read_b64_tr_b16 v[214:215],v242 offset:17664\nM v[48:51],a[144:147],v[216:219],v[48:51]\nbuffer_load_dwordx4 v244,s[40:43],s69 offen offset:1024 lds\nM v[52:55],a[148:151],v[216:219],v[52:55]\nM v[112:115],a[152:155],v[216:219],v[112:115]\nM v[116:119],a[156:159],v[216:219],v[116:119]\nds_read_b64_tr_b16 v[216:217],v241 offset:17920\n" \
"ds_read_b64_tr_b16 v[218:219],v241 offset:18176\nM v[56:59],a[144:147],v[220:223],v[56:59]\nbuffer_load_dwordx4 v245,s[40:43],s69 offen offset:2048 lds\nM v[60:63],a[148:151],v[220:223],v[60:63]\nM v[120:123],a[152:155],v[220:223],v[120:123]\nM v[124:127],a[156:159],v[220:223],v[124:127]\nds_read_b64_tr_b16 v[220:221],v242 offset:17920\ns_waitcnt lgkmcnt(8)\n" \
"M v[0:3],a[160:163],v[192:195],v[0:3]\nbuffer_load_dwordx4 v246,s[40:43],s69 offen offset:3072 lds\nM v[4:7],a[164:167],v[192:195],v[4:7]\nds_write_b64 v250,v[172:173] offset:11264\nds_read_b64_tr_b16 v[222:223],v242 offset:18176\nM v[64:67],a[168:171],v[192:195],v[64:67]\nM v[68:71],a[172:175],v[192:195],v[68:71]\nds_read_b64_tr_b16 v[192:193],v241 offset:24576\n" \
"ds_read_b64_tr_b16 v[194:195],v241 offset:24832\nM v[8:11],a[160:163],v[196:199],v[8:11]\nM v[12:15],a[164:167],v[196:199],v[12:15]\nM v[72:75],a[168:171],v[196:199],v[72:75]\nM v[76:79],a[172:175],v[196:199],v[76:79]\nds_read_b64_tr_b16 v[196:197],v242 offset:24576\nds_read_b64_tr_b16 v[198:199],v242 offset:24832\nM v[16:19],a[160:163],v[200:203],v[16:19]\n" \
"M v[20:23],a[164:167],v[200:203],v[20:23]\nM v[80:83],a[168:171],v[200:203],v[80:83]\nM v[84:87],a[172:175],v[200:203],v[84:87]\nds_read_b64_tr_b16 v[200:201],v241 offset:25088\nds_read_b64_tr_b16 v[202:203],v241 offset:25344\nM v[24:27],a[160:163],v[204:207],v[24:27]\nds_write_b64 v250,v[174:175] offset:11272\nM v[28:31],a[164:167],v[204:207],v[28:31]\n" \
"s_waitcnt vmcnt(5)\nv_cmp_eq_u32 vcc,0,v248\nM v[88:91],a[168:171],v[204:207],v[88:91]\ns_cbranch_vccnz .Lps0\n.Lpb0:\ns_lshl_b32 s80,s63,7\nM v[92:95],a[172:175],v[204:207],v[92:95]\nds_read_b64_tr_b16 v[204:205],v242 offset:25088\n" \
"s_waitcnt lgkmcnt(8)\nM v[32:35],a[160:163],v[208:211],v[32:35]\nds_read_b64_tr_b16 v[206:207],v242 offset:25344\ns_add_u32 s80,s80,81920\nM v[36:39],a[164:167],v[208:211],v[36:39]\nbuffer_load_dwordx4 a[0:3],v240,s[52:55],s76 offen offset:0 sc1\nM v[96:99],a[168:171],v[208:211],v[96:99]\nv_and_b32 v228,31,v249\n" \
"v_lshlrev_b32 v228,2,v228\nM v[100:103],a[172:175],v[208:211],v[100:103]\nds_read_b64_tr_b16 v[208:209],v241 offset:25600\nds_read_b64_tr_b16 v[210:211],v241 offset:25856\nM v[40:43],a[160:163],v[212:215],v[40:43]\nv_mov_b32 v251,v228\nM v[44:47],a[164:167],v[212:215],v[44:47]\nbuffer_load_dwordx4 a[4:7],v240,s[52:55],s76 offen offset:1024 sc1\n" \
"M v[104:107],a[168:171],v[212:215],v[104:107]\nM v[108:111],a[172:175],v[212:215],v[108:111]\nds_read_b64_tr_b16 v[212:213],v242 offset:25600\nds_read_b64_tr_b16 v[214:215],v242 offset:25856\nM v[48:51],a[160:163],v[216:219],v[48:51]\nds_write_b64 v250,v[176:177] offset:12288\nM v[52:55],a[164:167],v[216:219],v[52:55]\nbuffer_load_dwordx4 a[8:11],v240,s[52:55],s77 offen offset:0 sc1\n" \
"M v[112:115],a[168:171],v[216:219],v[112:115]\nM v[116:119],a[172:175],v[216:219],v[116:119]\nds_read_b64_tr_b16 v[216:217],v241 offset:26112\nds_read_b64_tr_b16 v[218:219],v241 offset:26368\nM v[56:59],a[160:163],v[220:223],v[56:59]\nM v[60:63],a[164:167],v[220:223],v[60:63]\nbuffer_load_dwordx4 a[12:15],v240,s[52:55],s77 offen offset:1024 sc1\nM v[120:123],a[168:171],v[220:223],v[120:123]\n" \
"M v[124:127],a[172:175],v[220:223],v[124:127]\nds_read_b64_tr_b16 v[220:221],v242 offset:26112\ns_waitcnt lgkmcnt(8)\nM v[0:3],a[176:179],v[192:195],v[0:3]\nds_read_b64_tr_b16 v[222:223],v242 offset:26368\nM v[4:7],a[180:183],v[192:195],v[4:7]\nM v[64:67],a[184:187],v[192:195],v[64:67]\nbuffer_load_dwordx4 a[16:19],v240,s[52:55],s76 offen offset:2048 sc1\n" \
"M v[68:71],a[188:191],v[192:195],v[68:71]\nM v[8:11],a[176:179],v[196:199],v[8:11]\nds_write_b64 v250,v[178:179] offset:12296\nM v[12:15],a[180:183],v[196:199],v[12:15]\nM v[72:75],a[184:187],v[196:199],v[72:75]\nM v[76:79],a[188:191],v[196:199],v[76:79]\nM v[16:19],a[176:179],v[200:203],v[16:19]\nM v[20:23],a[180:183],v[200:203],v[20:23]\n" \
"M v[80:83],a[184:187],v[200:203],v[80:83]\nbuffer_load_dwordx4 a[20:23],v240,s[52:55],s76 offen offset:3072 sc1\nM v[84:87],a[188:191],v[200:203],v[84:87]\ns_add_u32 s76,s76,4096\nM v[24:27],a[176:179],v[204:207],v[24:27]\ns_add_u32 m0,s64,114688\ns_add_u32 s69,s68,524288\nM v[28:31],a[180:183],v[204:207],v[28:31]\n" \
"M v[88:91],a[184:187],v[204:207],v[88:91]\ns_waitcnt lgkmcnt(1)\ns_barrier\nM v[92:95],a[188:191],v[204:207],v[92:95]\nds_read_b64_tr_b16 v[192:193],v241 offset:32768\nds_read_b64_tr_b16 v[194:195],v241 offset:33024\nM v[32:35],a[176:179],v[208:211],v[32:35]\nds_read_b64_tr_b16 v[196:197],v242 offset:32768\n" \
"ds_read_b64_tr_b16 v[198:199],v242 offset:33024\nM v[36:39],a[180:183],v[208:211],v[36:39]\nds_read_b64_tr_b16 v[200:201],v241 offset:33280\nds_read_b64_tr_b16 v[202:203],v241 offset:33536\nM v[96:99],a[184:187],v[208:211],v[96:99]\nds_read_b64_tr_b16 v[204:205],v242 offset:33280\nds_read_b64_tr_b16 v[206:207],v242 offset:33536\nM v[100:103],a[188:191],v[208:211],v[100:103]\n" \
"ds_read_b64_tr_b16 v[208:209],v241 offset:33792\nds_read_b64_tr_b16 v[210:211],v241 offset:34048\nM v[40:43],a[176:179],v[212:215],v[40:43]\nbuffer_load_dwordx4 a[24:27],v240,s[52:55],s77 offen offset:2048 sc1\nM v[44:47],a[180:183],v[212:215],v[44:47]\nds_write_b64 v250,v[180:181] offset:13312\nM v[104:107],a[184:187],v[212:215],v[104:107]\nM v[108:111],a[188:191],v[212:215],v[108:111]\n" \
"ds_read_b64_tr_b16 v[212:213],v242 offset:33792\nds_read_b64_tr_b16 v[214:215],v242 offset:34048\nM v[48:51],a[176:179],v[216:219],v[48:51]\nbuffer_load_dwordx4 v243,s[40:43],s69 offen offset:0 lds\nM v[52:55],a[180:183],v[216:219],v[52:55]\nM v[112:115],a[184:187],v[216:219],v[112:115]\nM v[116:119],a[188:191],v[216:219],v[116:119]\nds_read_b64_tr_b16 v[216:217],v241 offset:34304\n" \
"ds_read_b64_tr_b16 v[218:219],v241 offset:34560\nM v[56:59],a[176:179],v[220:223],v[56:59]\nbuffer_load_dwordx4 a[28:31],v240,s[52:55],s77 offen offset:3072 sc1\nM v[60:63],a[180:183],v[220:223],v[60:63]\nds_write_b64 v250,v[182:183] offset:13320\ns_add_u32 s77,s77,4096\nM v[120:123],a[184:187],v[220:223],v[120:123]\nM v[124:127],a[188:191],v[220:223],v[124:127]\n" \
"ds_read_b64_tr_b16 v[220:221],v242 offset:34304\ns_waitcnt lgkmcnt(9)\nM v[0:3],a[192:195],v[192:195],v[0:3]\nbuffer_load_dwordx4 v244,s[40:43],s69 offen offset:1024 lds\nM v[4:7],a[196:199],v[192:195],v[4:7]\nds_read_b64_tr_b16 v[222:223],v242 offset:34560\nM v[64:67],a[200:203],v[192:195],v[64:67]\nM v[68:71],a[204:207],v[192:195],v[68:71]\n" \
"ds_read_b64_tr_b16 v[192:193],v241 offset:40960\nds_read_b64_tr_b16 v[194:195],v241 offset:41216\nM v[8:11],a[192:195],v[196:199],v[8:11]\nbuffer_load_dwordx4 a[32:35],v240,s[52:55],s76 offen offset:0 sc1\nM v[12:15],a[196:199],v[196:199],v[12:15]\nM v[72:75],a[200:203],v[196:199],v[72:75]\nM v[76:79],a[204:207],v[196:199],v[76:79]\nds_read_b64_tr_b16 v[196:197],v242 offset:40960\n" \
"ds_read_b64_tr_b16 v[198:199],v242 offset:41216\nM v[16:19],a[192:195],v[200:203],v[16:19]\nbuffer_load_dwordx4 v245,s[40:43],s69 offen offset:2048 lds\nM v[20:23],a[196:199],v[200:203],v[20:23]\nds_write_b64 v250,v[184:185] offset:14336\nM v[80:83],a[200:203],v[200:203],v[80:83]\nM v[84:87],a[204:207],v[200:203],v[84:87]\nds_read_b64_tr_b16 v[200:201],v241 offset:41472\n" \
"ds_read_b64_tr_b16 v[202:203],v241 offset:41728\nM v[24:27],a[192:195],v[204:207],v[24:27]\nbuffer_load_dwordx4 a[36:39],v240,s[52:55],s76 offen offset:1024 sc1\nM v[28:31],a[196:199],v[204:207],v[28:31]\nM v[88:91],a[200:203],v[204:207],v[88:91]\nM v[92:95],a[204:207],v[204:207],v[92:95]\nds_read_b64_tr_b16 v[204:205],v242 offset:41472\ns_waitcnt lgkmcnt(8)\n" \
"M v[32:35],a[192:195],v[208:211],v[32:35]\nbuffer_load_dwordx4 v246,s[40:43],s69 offen offset:3072 lds\nM v[36:39],a[196:199],v[208:211],v[36:39]\nds_read_b64_tr_b16 v[206:207],v242 offset:41728\nM v[96:99],a[200:203],v[208:211],v[96:99]\nM v[100:103],a[204:207],v[208:211],v[100:103]\nds_read_b64_tr_b16 v[208:209],v241 offset:41984\nds_read_b64_tr_b16 v[210:211],v241 offset:42240\n" \
"M v[40:43],a[192:195],v[212:215],v[40:43]\nbuffer_load_dwordx4 a[40:43],v240,s[52:55],s77 offen offset:0 sc1\nM v[44:47],a[196:199],v[212:215],v[44:47]\nds_write_b64 v250,v[186:187] offset:14344\nM v[104:107],a[200:203],v[212:215],v[104:107]\nM v[108:111],a[204:207],v[212:215],v[108:111]\nds_read_b64_tr_b16 v[212:213],v242 offset:41984\nds_read_b64_tr_b16 v[214:215],v242 offset:42240\n" \
"M v[48:51],a[192:195],v[216:219],v[48:51]\ns_waitcnt vmcnt(19)\nbuffer_store_dword v252,off,s[52:55],s29 sc1\nM v[52:55],a[196:199],v[216:219],v[52:55]\ns_add_u32 s29,s29,4096\nM v[112:115],a[200:203],v[216:219],v[112:115]\nbuffer_load_dwordx4 a[44:47],v240,s[52:55],s77 offen offset:1024 sc1\nM v[116:119],a[204:207],v[216:219],v[116:119]\n" \
"ds_read_b64_tr_b16 v[216:217],v241 offset:42496\nds_read_b64_tr_b16 v[218:219],v241 offset:42752\nM v[56:59],a[192:195],v[220:223],v[56:59]\nM v[60:63],a[196:199],v[220:223],v[60:63]\nM v[120:123],a[200:203],v[220:223],v[120:123]\nM v[124:127],a[204:207],v[220:223],v[124:127]\nds_read_b64_tr_b16 v[220:221],v242 offset:42496\ns_waitcnt lgkmcnt(8)\n" \
"M v[0:3],a[208:211],v[192:195],v[0:3]\nds_write_b64 v250,v[188:189] offset:15360\nds_read_b64_tr_b16 v[222:223],v242 offset:42752\nM v[4:7],a[212:215],v[192:195],v[4:7]\nM v[64:67],a[216:219],v[192:195],v[64:67]\nbuffer_load_dwordx4 a[48:51],v240,s[52:55],s76 offen offset:2048 sc1\nM v[68:71],a[220:223],v[192:195],v[68:71]\nM v[8:11],a[208:211],v[196:199],v[8:11]\n" \
"M v[12:15],a[212:215],v[196:199],v[12:15]\nM v[72:75],a[216:219],v[196:199],v[72:75]\nM v[76:79],a[220:223],v[196:199],v[76:79]\nM v[16:19],a[208:211],v[200:203],v[16:19]\nM v[20:23],a[212:215],v[200:203],v[20:23]\nM v[80:83],a[216:219],v[200:203],v[80:83]\nbuffer_load_dwordx4 a[52:55],v240,s[52:55],s76 offen offset:3072 sc1\nM v[84:87],a[220:223],v[200:203],v[84:87]\n" \
"s_add_u32 s76,s76,4096\nM v[24:27],a[208:211],v[204:207],v[24:27]\nds_write_b64 v250,v[190:191] offset:15368\ns_add_u32 m0,s64,131072\nM v[28:31],a[212:215],v[204:207],v[28:31]\ns_add_u32 s69,s68,1048576\nv_add_u32 v250,65536,v250\nM v[88:91],a[216:219],v[204:207],v[88:91]\n" \
"s_waitcnt lgkmcnt(1)\ns_barrier\nM v[92:95],a[220:223],v[204:207],v[92:95]\nds_read_b64_tr_b16 v[192:193],v241 offset:49152\nds_read_b64_tr_b16 v[194:195],v241 offset:49408\nM v[32:35],a[208:211],v[208:211],v[32:35]\nds_read_b64_tr_b16 v[196:197],v242 offset:49152\nds_read_b64_tr_b16 v[198:199],v242 offset:49408\n" \
"M v[36:39],a[212:215],v[208:211],v[36:39]\nds_read_b64_tr_b16 v[200:201],v241 offset:49664\nds_read_b64_tr_b16 v[202:203],v241 offset:49920\nM v[96:99],a[216:219],v[208:211],v[96:99]\nds_read_b64_tr_b16 v[204:205],v242 offset:49664\nds_read_b64_tr_b16 v[206:207],v242 offset:49920\nM v[100:103],a[220:223],v[208:211],v[100:103]\nds_read_b64_tr_b16 v[208:209],v241 offset:50176\n" \
"ds_read_b64_tr_b16 v[210:211],v241 offset:50432\nM v[40:43],a[208:211],v[212:215],v[40:43]\nbuffer_load_dwordx4 a[56:59],v240,s[52:55],s77 offen offset:2048 sc1\nM v[44:47],a[212:215],v[212:215],v[44:47]\nM v[104:107],a[216:219],v[212:215],v[104:107]\nM v[108:111],a[220:223],v[212:215],v[108:111]\nds_read_b64_tr_b16 v[212:213],v242 offset:50176\nds_read_b64_tr_b16 v[214:215],v242 offset:50432\n" \
"M v[48:51],a[208:211],v[216:219],v[48:51]\nbuffer_load_dwordx4 v243,s[40:43],s69 offen offset:0 lds\nM v[52:55],a[212:215],v[216:219],v[52:55]\nM v[112:115],a[216:219],v[216:219],v[112:115]\nM v[116:119],a[220:223],v[216:219],v[116:119]\nds_read_b64_tr_b16 v[216:217],v241 offset:50688\nds_read_b64_tr_b16 v[218:219],v241 offset:50944\nM v[56:59],a[208:211],v[220:223],v[56:59]\n" \
"buffer_load_dwordx4 a[60:63],v240,s[52:55],s77 offen offset:3072 sc1\nM v[60:63],a[212:215],v[220:223],v[60:63]\ns_add_u32 s77,s77,4096\nM v[120:123],a[216:219],v[220:223],v[120:123]\nM v[124:127],a[220:223],v[220:223],v[124:127]\nds_read_b64_tr_b16 v[220:221],v242 offset:50688\ns_waitcnt lgkmcnt(7)\nM v[0:3],a[224:227],v[192:195],v[0:3]\n" \
"buffer_load_dwordx4 v244,s[40:43],s69 offen offset:1024 lds\nM v[4:7],a[228:231],v[192:195],v[4:7]\nds_read_b64_tr_b16 v[222:223],v242 offset:50944\nM v[64:67],a[232:235],v[192:195],v[64:67]\nM v[68:71],a[236:239],v[192:195],v[68:71]\nds_read_b64_tr_b16 v[192:193],v241 offset:57344\nds_read_b64_tr_b16 v[194:195],v241 offset:57600\nM v[8:11],a[224:227],v[196:199],v[8:11]\n" \
"buffer_load_dwordx4 a[64:67],v240,s[52:55],s76 offen offset:0 sc1\nM v[12:15],a[228:231],v[196:199],v[12:15]\nM v[72:75],a[232:235],v[196:199],v[72:75]\nM v[76:79],a[236:239],v[196:199],v[76:79]\nds_read_b64_tr_b16 v[196:197],v242 offset:57344\nds_read_b64_tr_b16 v[198:199],v242 offset:57600\nM v[16:19],a[224:227],v[200:203],v[16:19]\nbuffer_load_dwordx4 v245,s[40:43],s69 offen offset:2048 lds\n" \
"M v[20:23],a[228:231],v[200:203],v[20:23]\nM v[80:83],a[232:235],v[200:203],v[80:83]\nM v[84:87],a[236:239],v[200:203],v[84:87]\nds_read_b64_tr_b16 v[200:201],v241 offset:57856\nds_read_b64_tr_b16 v[202:203],v241 offset:58112\nM v[24:27],a[224:227],v[204:207],v[24:27]\nbuffer_load_dwordx4 a[68:71],v240,s[52:55],s76 offen offset:1024 sc1\nM v[28:31],a[228:231],v[204:207],v[28:31]\n" \
"M v[88:91],a[232:235],v[204:207],v[88:91]\nM v[92:95],a[236:239],v[204:207],v[92:95]\nds_read_b64_tr_b16 v[204:205],v242 offset:57856\ns_waitcnt lgkmcnt(7)\nM v[32:35],a[224:227],v[208:211],v[32:35]\nbuffer_load_dwordx4 v246,s[40:43],s69 offen offset:3072 lds\nM v[36:39],a[228:231],v[208:211],v[36:39]\nds_read_b64_tr_b16 v[206:207],v242 offset:58112\n" \
"M v[96:99],a[232:235],v[208:211],v[96:99]\nM v[100:103],a[236:239],v[208:211],v[100:103]\nds_read_b64_tr_b16 v[208:209],v241 offset:58368\nds_read_b64_tr_b16 v[210:211],v241 offset:58624\nM v[40:43],a[224:227],v[212:215],v[40:43]\nbuffer_load_dwordx4 a[72:75],v240,s[52:55],s77 offen offset:0 sc1\nM v[44:47],a[228:231],v[212:215],v[44:47]\nM v[104:107],a[232:235],v[212:215],v[104:107]\n" \
"M v[108:111],a[236:239],v[212:215],v[108:111]\nds_read_b64_tr_b16 v[212:213],v242 offset:58368\nds_read_b64_tr_b16 v[214:215],v242 offset:58624\nM v[48:51],a[224:227],v[216:219],v[48:51]\nM v[52:55],a[228:231],v[216:219],v[52:55]\nM v[112:115],a[232:235],v[216:219],v[112:115]\nbuffer_load_dwordx4 a[76:79],v240,s[52:55],s77 offen offset:1024 sc1\nM v[116:119],a[236:239],v[216:219],v[116:119]\n" \
"ds_read_b64_tr_b16 v[216:217],v241 offset:58880\nds_read_b64_tr_b16 v[218:219],v241 offset:59136\nM v[56:59],a[224:227],v[220:223],v[56:59]\nM v[60:63],a[228:231],v[220:223],v[60:63]\nM v[120:123],a[232:235],v[220:223],v[120:123]\nM v[124:127],a[236:239],v[220:223],v[124:127]\nds_read_b64_tr_b16 v[220:221],v242 offset:58880\ns_waitcnt lgkmcnt(8)\n" \
"M v[0:3],a[240:243],v[192:195],v[0:3]\nds_read_b64_tr_b16 v[222:223],v242 offset:59136\nM v[4:7],a[244:247],v[192:195],v[4:7]\nM v[64:67],a[248:251],v[192:195],v[64:67]\nbuffer_load_dwordx4 a[80:83],v240,s[52:55],s76 offen offset:2048 sc1\nM v[68:71],a[252:255],v[192:195],v[68:71]\nM v[8:11],a[240:243],v[196:199],v[8:11]\nM v[12:15],a[244:247],v[196:199],v[12:15]\n" \
"M v[72:75],a[248:251],v[196:199],v[72:75]\nv_cvt_pk_bf16_f32 v128,v0,v1\nv_cvt_pk_bf16_f32 v129,v2,v3\nM v[76:79],a[252:255],v[196:199],v[76:79]\nv_cvt_pk_bf16_f32 v130,v4,v5\nv_cvt_pk_bf16_f32 v131,v6,v7\nM v[16:19],a[240:243],v[200:203],v[16:19]\nv_cvt_pk_bf16_f32 v160,v64,v65\n" \
"v_cvt_pk_bf16_f32 v161,v66,v67\nM v[20:23],a[244:247],v[200:203],v[20:23]\nv_cvt_pk_bf16_f32 v162,v68,v69\nv_cvt_pk_bf16_f32 v163,v70,v71\nM v[80:83],a[248:251],v[200:203],v[80:83]\nv_cvt_pk_bf16_f32 v132,v8,v9\nv_cvt_pk_bf16_f32 v133,v10,v11\nM v[84:87],a[252:255],v[200:203],v[84:87]\n" \
"v_cvt_pk_bf16_f32 v134,v12,v13\ns_waitcnt lgkmcnt(2)\nM v[24:27],a[240:243],v[204:207],v[24:27]\nv_cvt_pk_bf16_f32 v135,v14,v15\nv_cvt_pk_bf16_f32 v164,v72,v73\nM v[28:31],a[244:247],v[204:207],v[28:31]\nv_cvt_pk_bf16_f32 v165,v74,v75\nv_cvt_pk_bf16_f32 v166,v76,v77\n" \
"M v[88:91],a[248:251],v[204:207],v[88:91]\ns_waitcnt vmcnt(23) lgkmcnt(0)\ns_barrier\nM v[92:95],a[252:255],v[204:207],v[92:95]\nds_read_b64_tr_b16 v[192:193],v241 offset:0\nds_read_b64_tr_b16 v[194:195],v241 offset:256\nM v[32:35],a[240:243],v[208:211],v[32:35]\nds_read_b64_tr_b16 v[196:197],v242 offset:0\n" \
"ds_read_b64_tr_b16 v[198:199],v242 offset:256\nM v[36:39],a[244:247],v[208:211],v[36:39]\nds_read_b64_tr_b16 v[200:201],v241 offset:512\nds_read_b64_tr_b16 v[202:203],v241 offset:768\nM v[96:99],a[248:251],v[208:211],v[96:99]\nds_read_b64_tr_b16 v[204:205],v242 offset:512\nds_read_b64_tr_b16 v[206:207],v242 offset:768\nM v[100:103],a[252:255],v[208:211],v[100:103]\n" \
"ds_read_b64_tr_b16 v[208:209],v241 offset:1024\nds_read_b64_tr_b16 v[210:211],v241 offset:1280\nM v[40:43],a[240:243],v[212:215],v[40:43]\nv_cvt_pk_bf16_f32 v167,v78,v79\nv_cvt_pk_bf16_f32 v136,v16,v17\nM v[44:47],a[244:247],v[212:215],v[44:47]\nv_cvt_pk_bf16_f32 v137,v18,v19\nv_cvt_pk_bf16_f32 v138,v20,v21\n" \
"M v[104:107],a[248:251],v[212:215],v[104:107]\nv_cvt_pk_bf16_f32 v139,v22,v23\nv_cvt_pk_bf16_f32 v168,v80,v81\nM v[108:111],a[252:255],v[212:215],v[108:111]\nv_cvt_pk_bf16_f32 v169,v82,v83\nds_read_b64_tr_b16 v[212:213],v242 offset:1024\nM v[48:51],a[240:243],v[216:219],v[48:51]\nds_read_b64_tr_b16 v[214:215],v242 offset:1280\n" \
"v_cvt_pk_bf16_f32 v170,v84,v85\nM v[52:55],a[244:247],v[216:219],v[52:55]\nv_cvt_pk_bf16_f32 v171,v86,v87\nv_cvt_pk_bf16_f32 v140,v24,v25\nM v[112:115],a[248:251],v[216:219],v[112:115]\nv_cvt_pk_bf16_f32 v141,v26,v27\nv_cvt_pk_bf16_f32 v142,v28,v29\nM v[116:119],a[252:255],v[216:219],v[116:119]\n" \
"v_cvt_pk_bf16_f32 v143,v30,v31\nv_cvt_pk_bf16_f32 v172,v88,v89\nM v[56:59],a[240:243],v[220:223],v[56:59]\nv_cvt_pk_bf16_f32 v173,v90,v91\nds_read_b64_tr_b16 v[216:217],v241 offset:1536\nM v[60:63],a[244:247],v[220:223],v[60:63]\nds_read_b64_tr_b16 v[218:219],v241 offset:1792\nv_cvt_pk_bf16_f32 v174,v92,v93\n" \
"M v[120:123],a[248:251],v[220:223],v[120:123]\nv_cvt_pk_bf16_f32 v175,v94,v95\nv_cvt_pk_bf16_f32 v144,v32,v33\nM v[124:127],a[252:255],v[220:223],v[124:127]\nv_cvt_pk_bf16_f32 v145,v34,v35\ns_waitcnt lgkmcnt(4)\nM v[0:3],v[192:195],a[0:3],0\nv_cvt_pk_bf16_f32 v146,v36,v37\n" \
"v_cvt_pk_bf16_f32 v147,v38,v39\nM v[4:7],v[192:195],a[4:7],0\nv_cvt_pk_bf16_f32 v176,v96,v97\nv_cvt_pk_bf16_f32 v177,v98,v99\nM v[64:67],v[192:195],a[8:11],0\nds_read_b64_tr_b16 v[220:221],v242 offset:1536\nds_read_b64_tr_b16 v[222:223],v242 offset:1792\nM v[68:71],v[192:195],a[12:15],0\n" \
"v_cvt_pk_bf16_f32 v178,v100,v101\nv_cvt_pk_bf16_f32 v179,v102,v103\nM v[8:11],v[196:199],a[0:3],0\nv_cvt_pk_bf16_f32 v148,v40,v41\nv_cvt_pk_bf16_f32 v149,v42,v43\nM v[12:15],v[196:199],a[4:7],0\nv_cvt_pk_bf16_f32 v150,v44,v45\nv_cvt_pk_bf16_f32 v151,v46,v47\n" \
"M v[72:75],v[196:199],a[8:11],0\nv_cvt_pk_bf16_f32 v180,v104,v105\nv_cvt_pk_bf16_f32 v181,v106,v107\nM v[76:79],v[196:199],a[12:15],0\nds_read_b64_tr_b16 v[192:193],v241 offset:8192\nds_read_b64_tr_b16 v[194:195],v241 offset:8448\nM v[16:19],v[200:203],a[0:3],0\nv_cvt_pk_bf16_f32 v182,v108,v109\n" \
"v_cvt_pk_bf16_f32 v183,v110,v111\nM v[20:23],v[200:203],a[4:7],0\nv_cvt_pk_bf16_f32 v152,v48,v49\nv_cvt_pk_bf16_f32 v153,v50,v51\nM v[80:83],v[200:203],a[8:11],0\nv_cvt_pk_bf16_f32 v154,v52,v53\nv_cvt_pk_bf16_f32 v155,v54,v55\nM v[84:87],v[200:203],a[12:15],0\n" \
"v_cvt_pk_bf16_f32 v184,v112,v113\nv_cvt_pk_bf16_f32 v185,v114,v115\nM v[24:27],v[204:207],a[0:3],0\nds_read_b64_tr_b16 v[196:197],v242 offset:8192\nds_read_b64_tr_b16 v[198:199],v242 offset:8448\nM v[28:31],v[204:207],a[4:7],0\nv_cvt_pk_bf16_f32 v186,v116,v117\nv_cvt_pk_bf16_f32 v187,v118,v119\n" \
"M v[88:91],v[204:207],a[8:11],0\nv_cvt_pk_bf16_f32 v156,v56,v57\nv_cvt_pk_bf16_f32 v157,v58,v59\nM v[92:95],v[204:207],a[12:15],0\nv_cvt_pk_bf16_f32 v158,v60,v61\nv_cvt_pk_bf16_f32 v159,v62,v63\nM v[32:35],v[208:211],a[0:3],0\nv_cvt_pk_bf16_f32 v188,v120,v121\n" \
"v_cvt_pk_bf16_f32 v189,v122,v123\nM v[36:39],v[208:211],a[4:7],0\nds_read_b64_tr_b16 v[200:201],v241 offset:8704\nds_read_b64_tr_b16 v[202:203],v241 offset:8960\nM v[96:99],v[208:211],a[8:11],0\nv_cvt_pk_bf16_f32 v190,v124,v125\nv_cvt_pk_bf16_f32 v191,v126,v127\nM v[100:103],v[208:211],a[12:15],0\n" \
"ds_read_b64_tr_b16 v[204:205],v242 offset:8704\ns_waitcnt lgkmcnt(5)\nM v[40:43],v[212:215],a[0:3],0\nbuffer_store_dwordx4 v[128:131],v240,s[52:55],s74 offen offset:0 sc1\nM v[44:47],v[212:215],a[4:7],0\nds_read_b64_tr_b16 v[206:207],v242 offset:8960\nds_read_b64_tr_b16 v[208:209],v241 offset:9216\nM v[104:107],v[212:215],a[8:11],0\n" \
"ds_read_b64_tr_b16 v[210:211],v241 offset:9472\ns_add_u32 m0,s64,147456\nM v[108:111],v[212:215],a[12:15],0\nds_read_b64_tr_b16 v[212:213],v242 offset:9216\nds_read_b64_tr_b16 v[214:215],v242 offset:9472\nM v[48:51],v[216:219],a[0:3],0\nbuffer_load_dwordx4 a[84:87],v240,s[52:55],s76 offen offset:3072 sc1\nM v[52:55],v[216:219],a[4:7],0\n" \
"s_add_u32 s69,s68,1572864\ns_add_u32 s76,s76,4096\nM v[112:115],v[216:219],a[8:11],0\nM v[116:119],v[216:219],a[12:15],0\nds_read_b64_tr_b16 v[216:217],v241 offset:9728\nds_read_b64_tr_b16 v[218:219],v241 offset:9984\nM v[56:59],v[220:223],a[0:3],0\nbuffer_load_dwordx4 a[88:91],v240,s[52:55],s77 offen offset:2048 sc1\n" \
"M v[60:63],v[220:223],a[4:7],0\nM v[120:123],v[220:223],a[8:11],0\nM v[124:127],v[220:223],a[12:15],0\nds_read_b64_tr_b16 v[220:221],v242 offset:9728\nds_read_b64_tr_b16 v[222:223],v242 offset:9984\nM v[0:3],v[192:195],a[16:19],v[0:3]\nbuffer_load_dwordx4 a[92:95],v240,s[52:55],s77 offen offset:3072 sc1\nM v[4:7],v[192:195],a[20:23],v[4:7]\n" \
"s_add_u32 s77,s77,4096\nM v[64:67],v[192:195],a[24:27],v[64:67]\ns_waitcnt vmcnt(25)\nM v[68:71],v[192:195],a[28:31],v[68:71]\ns_waitcnt lgkmcnt(6)\nM v[8:11],v[196:199],a[16:19],v[8:11]\nbuffer_load_dwordx4 a[96:99],v240,s[52:55],s76 offen offset:0 sc1\nM v[12:15],v[196:199],a[20:23],v[12:15]\n" \
"M v[72:75],v[196:199],a[24:27],v[72:75]\nM v[76:79],v[196:199],a[28:31],v[76:79]\nM v[16:19],v[200:203],a[16:19],v[16:19]\nbuffer_load_dwordx4 a[100:103],v240,s[52:55],s76 offen offset:1024 sc1\nM v[20:23],v[200:203],a[20:23],v[20:23]\nM v[80:83],v[200:203],a[24:27],v[80:83]\nM v[84:87],v[200:203],a[28:31],v[84:87]\nM v[24:27],v[204:207],a[16:19],v[24:27]\n" \
"buffer_load_dwordx4 a[104:107],v240,s[52:55],s77 offen offset:0 sc1\nM v[28:31],v[204:207],a[20:23],v[28:31]\nM v[88:91],v[204:207],a[24:27],v[88:91]\ns_waitcnt vmcnt(22) lgkmcnt(0)\ns_barrier\nM v[92:95],v[204:207],a[28:31],v[92:95]\nds_read_b64_tr_b16 v[192:193],v241 offset:16384\nds_read_b64_tr_b16 v[194:195],v241 offset:16640\n" \
"M v[32:35],v[208:211],a[16:19],v[32:35]\nds_read_b64_tr_b16 v[196:197],v242 offset:16384\nds_read_b64_tr_b16 v[198:199],v242 offset:16640\nM v[36:39],v[208:211],a[20:23],v[36:39]\nds_read_b64_tr_b16 v[200:201],v241 offset:16896\nds_read_b64_tr_b16 v[202:203],v241 offset:17152\nM v[96:99],v[208:211],a[24:27],v[96:99]\nbuffer_store_dwordx4 v[132:135],v240,s[52:55],s74 offen offset:1024 sc1\n" \
"M v[100:103],v[208:211],a[28:31],v[100:103]\nds_read_b64_tr_b16 v[204:205],v242 offset:16896\nds_read_b64_tr_b16 v[206:207],v242 offset:17152\nM v[40:43],v[212:215],a[16:19],v[40:43]\nds_read_b64_tr_b16 v[208:209],v241 offset:17408\nds_read_b64_tr_b16 v[210:211],v241 offset:17664\nM v[44:47],v[212:215],a[20:23],v[44:47]\nM v[104:107],v[212:215],a[24:27],v[104:107]\n" \
"buffer_load_dwordx4 a[108:111],v240,s[52:55],s77 offen offset:1024 sc1\nM v[108:111],v[212:215],a[28:31],v[108:111]\nds_read_b64_tr_b16 v[212:213],v242 offset:17408\nds_read_b64_tr_b16 v[214:215],v242 offset:17664\nM v[48:51],v[216:219],a[16:19],v[48:51]\nM v[52:55],v[216:219],a[20:23],v[52:55]\nM v[112:115],v[216:219],a[24:27],v[112:115]\nbuffer_load_dwordx4 a[112:115],v240,s[52:55],s76 offen offset:2048 sc1\n" \
"M v[116:119],v[216:219],a[28:31],v[116:119]\nds_read_b64_tr_b16 v[216:217],v241 offset:17920\nds_read_b64_tr_b16 v[218:219],v241 offset:18176\nM v[56:59],v[220:223],a[16:19],v[56:59]\nM v[60:63],v[220:223],a[20:23],v[60:63]\nM v[120:123],v[220:223],a[24:27],v[120:123]\nbuffer_load_dwordx4 v243,s[40:43],s69 offen offset:0 lds\nM v[124:127],v[220:223],a[28:31],v[124:127]\n" \
"ds_read_b64_tr_b16 v[220:221],v242 offset:17920\ns_waitcnt lgkmcnt(9)\nM v[0:3],v[192:195],a[32:35],v[0:3]\nds_read_b64_tr_b16 v[222:223],v242 offset:18176\nM v[4:7],v[192:195],a[36:39],v[4:7]\nM v[64:67],v[192:195],a[40:43],v[64:67]\ns_waitcnt vmcnt(21)\nM v[68:71],v[192:195],a[44:47],v[68:71]\n" \
"ds_read_b64_tr_b16 v[192:193],v241 offset:24576\nds_read_b64_tr_b16 v[194:195],v241 offset:24832\nM v[8:11],v[196:199],a[32:35],v[8:11]\nbuffer_load_dwordx4 v244,s[40:43],s69 offen offset:1024 lds\nM v[12:15],v[196:199],a[36:39],v[12:15]\nM v[72:75],v[196:199],a[40:43],v[72:75]\nM v[76:79],v[196:199],a[44:47],v[76:79]\nds_read_b64_tr_b16 v[196:197],v242 offset:24576\n" \
"ds_read_b64_tr_b16 v[198:199],v242 offset:24832\nM v[16:19],v[200:203],a[32:35],v[16:19]\nbuffer_load_dwordx4 v245,s[40:43],s69 offen offset:2048 lds\nM v[20:23],v[200:203],a[36:39],v[20:23]\nM v[80:83],v[200:203],a[40:43],v[80:83]\nM v[84:87],v[200:203],a[44:47],v[84:87]\nds_read_b64_tr_b16 v[200:201],v241 offset:25088\ns_waitcnt lgkmcnt(7)\n" \
"M v[24:27],v[204:207],a[32:35],v[24:27]\nbuffer_load_dwordx4 v246,s[40:43],s69 offen offset:3072 lds\nM v[28:31],v[204:207],a[36:39],v[28:31]\nds_read_b64_tr_b16 v[202:203],v241 offset:25344\ns_add_u32 m0,s64,98304\nM v[88:91],v[204:207],a[40:43],v[88:91]\ns_add_u32 s69,s68,2097152\nM v[92:95],v[204:207],a[44:47],v[92:95]\n" \
"ds_read_b64_tr_b16 v[204:205],v242 offset:25088\nds_read_b64_tr_b16 v[206:207],v242 offset:25344\nM v[32:35],v[208:211],a[32:35],v[32:35]\nbuffer_store_dwordx4 v[136:139],v240,s[52:55],s74 offen offset:2048 sc1\nM v[36:39],v[208:211],a[36:39],v[36:39]\nM v[96:99],v[208:211],a[40:43],v[96:99]\nM v[100:103],v[208:211],a[44:47],v[100:103]\nds_read_b64_tr_b16 v[208:209],v241 offset:25600\n" \
"ds_read_b64_tr_b16 v[210:211],v241 offset:25856\nM v[40:43],v[212:215],a[32:35],v[40:43]\nbuffer_load_dwordx4 a[116:119],v240,s[52:55],s76 offen offset:3072 sc1\nM v[44:47],v[212:215],a[36:39],v[44:47]\ns_add_u32 s76,s76,4096\nM v[104:107],v[212:215],a[40:43],v[104:107]\nM v[108:111],v[212:215],a[44:47],v[108:111]\nds_read_b64_tr_b16 v[212:213],v242 offset:25600\n" \
"ds_read_b64_tr_b16 v[214:215],v242 offset:25856\nM v[48:51],v[216:219],a[32:35],v[48:51]\nbuffer_load_dwordx4 a[120:123],v240,s[52:55],s77 offen offset:2048 sc1\nM v[52:55],v[216:219],a[36:39],v[52:55]\nM v[112:115],v[216:219],a[40:43],v[112:115]\nM v[116:119],v[216:219],a[44:47],v[116:119]\nds_read_b64_tr_b16 v[216:217],v241 offset:26112\ns_waitcnt lgkmcnt(7)\n" \
"M v[56:59],v[220:223],a[32:35],v[56:59]\nbuffer_load_dwordx4 a[124:127],v240,s[52:55],s77 offen offset:3072 sc1\nM v[60:63],v[220:223],a[36:39],v[60:63]\nds_read_b64_tr_b16 v[218:219],v241 offset:26368\ns_add_u32 s77,s77,4096\nM v[120:123],v[220:223],a[40:43],v[120:123]\nM v[124:127],v[220:223],a[44:47],v[124:127]\nds_read_b64_tr_b16 v[220:221],v242 offset:26112\n" \
"ds_read_b64_tr_b16 v[222:223],v242 offset:26368\nM v[0:3],v[192:195],a[48:51],v[0:3]\nbuffer_load_dwordx4 a[128:131],v240,s[52:55],s76 offen offset:0 sc1\nM v[4:7],v[192:195],a[52:55],v[4:7]\nM v[64:67],v[192:195],a[56:59],v[64:67]\ns_waitcnt vmcnt(27)\nM v[68:71],v[192:195],a[60:63],v[68:71]\nM v[8:11],v[196:199],a[48:51],v[8:11]\n" \
"buffer_load_dwordx4 a[132:135],v240,s[52:55],s76 offen offset:1024 sc1\nM v[12:15],v[196:199],a[52:55],v[12:15]\nM v[72:75],v[196:199],a[56:59],v[72:75]\nM v[76:79],v[196:199],a[60:63],v[76:79]\nM v[16:19],v[200:203],a[48:51],v[16:19]\nbuffer_load_dwordx4 a[136:139],v240,s[52:55],s77 offen offset:0 sc1\nM v[20:23],v[200:203],a[52:55],v[20:23]\nM v[80:83],v[200:203],a[56:59],v[80:83]\n" \
"M v[84:87],v[200:203],a[60:63],v[84:87]\ns_waitcnt lgkmcnt(3)\nM v[24:27],v[204:207],a[48:51],v[24:27]\nbuffer_load_dwordx4 a[140:143],v240,s[52:55],s77 offen offset:1024 sc1\nM v[28:31],v[204:207],a[52:55],v[28:31]\nM v[88:91],v[204:207],a[56:59],v[88:91]\ns_waitcnt vmcnt(22) lgkmcnt(0)\ns_barrier\n" \
"M v[92:95],v[204:207],a[60:63],v[92:95]\nds_read_b64_tr_b16 v[192:193],v241 offset:32768\nds_read_b64_tr_b16 v[194:195],v241 offset:33024\nM v[32:35],v[208:211],a[48:51],v[32:35]\nbuffer_store_dwordx4 v[140:143],v240,s[52:55],s74 offen offset:3072 sc1\nM v[36:39],v[208:211],a[52:55],v[36:39]\nds_read_b64_tr_b16 v[196:197],v242 offset:32768\nds_read_b64_tr_b16 v[198:199],v242 offset:33024\n" \
"M v[96:99],v[208:211],a[56:59],v[96:99]\nds_read_b64_tr_b16 v[200:201],v241 offset:33280\nds_read_b64_tr_b16 v[202:203],v241 offset:33536\nM v[100:103],v[208:211],a[60:63],v[100:103]\nds_read_b64_tr_b16 v[204:205],v242 offset:33280\nds_read_b64_tr_b16 v[206:207],v242 offset:33536\nM v[40:43],v[212:215],a[48:51],v[40:43]\nds_read_b64_tr_b16 v[208:209],v241 offset:33792\n" \
"ds_read_b64_tr_b16 v[210:211],v241 offset:34048\nM v[44:47],v[212:215],a[52:55],v[44:47]\nbuffer_load_dwordx4 a[144:147],v240,s[52:55],s76 offen offset:2048 sc1\nM v[104:107],v[212:215],a[56:59],v[104:107]\ns_add_u32 s74,s74,4096\nM v[108:111],v[212:215],a[60:63],v[108:111]\nds_read_b64_tr_b16 v[212:213],v242 offset:33792\nds_read_b64_tr_b16 v[214:215],v242 offset:34048\n" \
"M v[48:51],v[216:219],a[48:51],v[48:51]\nM v[52:55],v[216:219],a[52:55],v[52:55]\nbuffer_load_dwordx4 v243,s[40:43],s69 offen offset:0 lds\nM v[112:115],v[216:219],a[56:59],v[112:115]\nM v[116:119],v[216:219],a[60:63],v[116:119]\nds_read_b64_tr_b16 v[216:217],v241 offset:34304\nds_read_b64_tr_b16 v[218:219],v241 offset:34560\nM v[56:59],v[220:223],a[48:51],v[56:59]\n" \
"M v[60:63],v[220:223],a[52:55],v[60:63]\nbuffer_load_dwordx4 v244,s[40:43],s69 offen offset:1024 lds\nM v[120:123],v[220:223],a[56:59],v[120:123]\nM v[124:127],v[220:223],a[60:63],v[124:127]\nds_read_b64_tr_b16 v[220:221],v242 offset:34304\ns_waitcnt lgkmcnt(9)\nM v[0:3],v[192:195],a[64:67],v[0:3]\nds_read_b64_tr_b16 v[222:223],v242 offset:34560\n" \
"M v[4:7],v[192:195],a[68:71],v[4:7]\nbuffer_load_dwordx4 v245,s[40:43],s69 offen offset:2048 lds\nM v[64:67],v[192:195],a[72:75],v[64:67]\nM v[68:71],v[192:195],a[76:79],v[68:71]\nds_read_b64_tr_b16 v[192:193],v241 offset:40960\nds_read_b64_tr_b16 v[194:195],v241 offset:41216\nM v[8:11],v[196:199],a[64:67],v[8:11]\nM v[12:15],v[196:199],a[68:71],v[12:15]\n" \
"buffer_load_dwordx4 v246,s[40:43],s69 offen offset:3072 lds\nM v[72:75],v[196:199],a[72:75],v[72:75]\ns_add_u32 m0,s64,114688\ns_add_u32 s69,s68,2621440\nM v[76:79],v[196:199],a[76:79],v[76:79]\nds_read_b64_tr_b16 v[196:197],v242 offset:40960\nds_read_b64_tr_b16 v[198:199],v242 offset:41216\nM v[16:19],v[200:203],a[64:67],v[16:19]\n" \
"M v[20:23],v[200:203],a[68:71],v[20:23]\nbuffer_load_dwordx4 a[148:151],v240,s[52:55],s76 offen offset:3072 sc1\nM v[80:83],v[200:203],a[72:75],v[80:83]\ns_add_u32 s76,s76,4096\nM v[84:87],v[200:203],a[76:79],v[84:87]\nds_read_b64_tr_b16 v[200:201],v241 offset:41472\ns_waitcnt lgkmcnt(6)\nM v[24:27],v[204:207],a[64:67],v[24:27]\n" \
"ds_read_b64_tr_b16 v[202:203],v241 offset:41728\nM v[28:31],v[204:207],a[68:71],v[28:31]\nbuffer_store_dwordx4 v[144:147],v240,s[52:55],s74 offen offset:0 sc1\nM v[88:91],v[204:207],a[72:75],v[88:91]\nM v[92:95],v[204:207],a[76:79],v[92:95]\nds_read_b64_tr_b16 v[204:205],v242 offset:41472\nds_read_b64_tr_b16 v[206:207],v242 offset:41728\nM v[32:35],v[208:211],a[64:67],v[32:35]\n" \
"M v[36:39],v[208:211],a[68:71],v[36:39]\nbuffer_load_dwordx4 a[152:155],v240,s[52:55],s77 offen offset:2048 sc1\nM v[96:99],v[208:211],a[72:75],v[96:99]\nM v[100:103],v[208:211],a[76:79],v[100:103]\nds_read_b64_tr_b16 v[208:209],v241 offset:41984\nds_read_b64_tr_b16 v[210:211],v241 offset:42240\nM v[40:43],v[212:215],a[64:67],v[40:43]\nM v[44:47],v[212:215],a[68:71],v[44:47]\n" \
"buffer_load_dwordx4 a[156:159],v240,s[52:55],s77 offen offset:3072 sc1\nM v[104:107],v[212:215],a[72:75],v[104:107]\ns_add_u32 s77,s77,4096\nM v[108:111],v[212:215],a[76:79],v[108:111]\nds_read_b64_tr_b16 v[212:213],v242 offset:41984\nds_read_b64_tr_b16 v[214:215],v242 offset:42240\nM v[48:51],v[216:219],a[64:67],v[48:51]\nM v[52:55],v[216:219],a[68:71],v[52:55]\n" \
"buffer_load_dwordx4 a[160:163],v240,s[52:55],s76 offen offset:0 sc1\nM v[112:115],v[216:219],a[72:75],v[112:115]\nM v[116:119],v[216:219],a[76:79],v[116:119]\nds_read_b64_tr_b16 v[216:217],v241 offset:42496\ns_waitcnt lgkmcnt(7)\nM v[56:59],v[220:223],a[64:67],v[56:59]\nds_read_b64_tr_b16 v[218:219],v241 offset:42752\nM v[60:63],v[220:223],a[68:71],v[60:63]\n" \
"buffer_load_dwordx4 a[164:167],v240,s[52:55],s76 offen offset:1024 sc1\nM v[120:123],v[220:223],a[72:75],v[120:123]\nM v[124:127],v[220:223],a[76:79],v[124:127]\nds_read_b64_tr_b16 v[220:221],v242 offset:42496\nds_read_b64_tr_b16 v[222:223],v242 offset:42752\nM v[0:3],v[192:195],a[80:83],v[0:3]\ns_waitcnt vmcnt(27)\nM v[4:7],v[192:195],a[84:87],v[4:7]\n" \
"buffer_load_dwordx4 a[168:171],v240,s[52:55],s77 offen offset:0 sc1\nM v[64:67],v[192:195],a[88:91],v[64:67]\nM v[68:71],v[192:195],a[92:95],v[68:71]\nM v[8:11],v[196:199],a[80:83],v[8:11]\nM v[12:15],v[196:199],a[84:87],v[12:15]\nbuffer_load_dwordx4 a[172:175],v240,s[52:55],s77 offen offset:1024 sc1\nM v[72:75],v[196:199],a[88:91],v[72:75]\nM v[76:79],v[196:199],a[92:95],v[76:79]\n" \
"M v[16:19],v[200:203],a[80:83],v[16:19]\nM v[20:23],v[200:203],a[84:87],v[20:23]\nbuffer_load_dwordx4 a[176:179],v240,s[52:55],s76 offen offset:2048 sc1\nM v[80:83],v[200:203],a[88:91],v[80:83]\nM v[84:87],v[200:203],a[92:95],v[84:87]\ns_waitcnt lgkmcnt(2)\nM v[24:27],v[204:207],a[80:83],v[24:27]\nM v[28:31],v[204:207],a[84:87],v[28:31]\n" \
"buffer_store_dwordx4 v[148:151],v240,s[52:55],s74 offen offset:1024 sc1\nM v[88:91],v[204:207],a[88:91],v[88:91]\ns_waitcnt vmcnt(24) lgkmcnt(0)\ns_barrier\nM v[92:95],v[204:207],a[92:95],v[92:95]\nds_read_b64_tr_b16 v[192:193],v241 offset:49152\nds_read_b64_tr_b16 v[194:195],v241 offset:49408\nM v[32:35],v[208:211],a[80:83],v[32:35]\n" \
"ds_read_b64_tr_b16 v[196:197],v242 offset:49152\nds_read_b64_tr_b16 v[198:199],v242 offset:49408\nM v[36:39],v[208:211],a[84:87],v[36:39]\nds_read_b64_tr_b16 v[200:201],v241 offset:49664\nds_read_b64_tr_b16 v[202:203],v241 offset:49920\nM v[96:99],v[208:211],a[88:91],v[96:99]\nds_read_b64_tr_b16 v[204:205],v242 offset:49664\nds_read_b64_tr_b16 v[206:207],v242 offset:49920\n" \
"M v[100:103],v[208:211],a[92:95],v[100:103]\nds_read_b64_tr_b16 v[208:209],v241 offset:50176\nds_read_b64_tr_b16 v[210:211],v241 offset:50432\nM v[40:43],v[212:215],a[80:83],v[40:43]\nbuffer_load_dwordx4 v243,s[40:43],s69 offen offset:0 lds\nM v[44:47],v[212:215],a[84:87],v[44:47]\nM v[104:107],v[212:215],a[88:91],v[104:107]\nM v[108:111],v[212:215],a[92:95],v[108:111]\n" \
"ds_read_b64_tr_b16 v[212:213],v242 offset:50176\nds_read_b64_tr_b16 v[214:215],v242 offset:50432\nM v[48:51],v[216:219],a[80:83],v[48:51]\nbuffer_load_dwordx4 v244,s[40:43],s69 offen offset:1024 lds\nM v[52:55],v[216:219],a[84:87],v[52:55]\nM v[112:115],v[216:219],a[88:91],v[112:115]\nM v[116:119],v[216:219],a[92:95],v[116:119]\nds_read_b64_tr_b16 v[216:217],v241 offset:50688\n" \
"ds_read_b64_tr_b16 v[218:219],v241 offset:50944\nM v[56:59],v[220:223],a[80:83],v[56:59]\nbuffer_load_dwordx4 v245,s[40:43],s69 offen offset:2048 lds\nM v[60:63],v[220:223],a[84:87],v[60:63]\nM v[120:123],v[220:223],a[88:91],v[120:123]\nM v[124:127],v[220:223],a[92:95],v[124:127]\nds_read_b64_tr_b16 v[220:221],v242 offset:50688\ns_waitcnt lgkmcnt(8)\n" \
"M v[0:3],v[192:195],a[96:99],v[0:3]\nbuffer_load_dwordx4 v246,s[40:43],s69 offen offset:3072 lds\nM v[4:7],v[192:195],a[100:103],v[4:7]\nds_read_b64_tr_b16 v[222:223],v242 offset:50944\ns_add_u32 m0,s64,131072\nM v[64:67],v[192:195],a[104:107],v[64:67]\ns_add_u32 s69,s68,3145728\nM v[68:71],v[192:195],a[108:111],v[68:71]\n" \
"ds_read_b64_tr_b16 v[192:193],v241 offset:57344\nds_read_b64_tr_b16 v[194:195],v241 offset:57600\nM v[8:11],v[196:199],a[96:99],v[8:11]\nbuffer_load_dwordx4 a[180:183],v240,s[52:55],s76 offen offset:3072 sc1\nM v[12:15],v[196:199],a[100:103],v[12:15]\ns_add_u32 s76,s76,4096\nM v[72:75],v[196:199],a[104:107],v[72:75]\nM v[76:79],v[196:199],a[108:111],v[76:79]\n" \
"ds_read_b64_tr_b16 v[196:197],v242 offset:57344\nds_read_b64_tr_b16 v[198:199],v242 offset:57600\nM v[16:19],v[200:203],a[96:99],v[16:19]\nbuffer_store_dwordx4 v[152:155],v240,s[52:55],s74 offen offset:2048 sc1\nM v[20:23],v[200:203],a[100:103],v[20:23]\nM v[80:83],v[200:203],a[104:107],v[80:83]\nM v[84:87],v[200:203],a[108:111],v[84:87]\nds_read_b64_tr_b16 v[200:201],v241 offset:57856\n" \
"s_waitcnt lgkmcnt(6)\nM v[24:27],v[204:207],a[96:99],v[24:27]\nbuffer_load_dwordx4 a[184:187],v240,s[52:55],s77 offen offset:2048 sc1\nM v[28:31],v[204:207],a[100:103],v[28:31]\nds_read_b64_tr_b16 v[202:203],v241 offset:58112\nM v[88:91],v[204:207],a[104:107],v[88:91]\nM v[92:95],v[204:207],a[108:111],v[92:95]\nds_read_b64_tr_b16 v[204:205],v242 offset:57856\n" \
"ds_read_b64_tr_b16 v[206:207],v242 offset:58112\nM v[32:35],v[208:211],a[96:99],v[32:35]\nbuffer_load_dwordx4 a[188:191],v240,s[52:55],s77 offen offset:3072 sc1\nM v[36:39],v[208:211],a[100:103],v[36:39]\ns_add_u32 s77,s77,4096\nM v[96:99],v[208:211],a[104:107],v[96:99]\nM v[100:103],v[208:211],a[108:111],v[100:103]\nds_read_b64_tr_b16 v[208:209],v241 offset:58368\n" \
"ds_read_b64_tr_b16 v[210:211],v241 offset:58624\nM v[40:43],v[212:215],a[96:99],v[40:43]\nbuffer_load_dwordx4 a[192:195],v240,s[52:55],s76 offen offset:0 sc1\nM v[44:47],v[212:215],a[100:103],v[44:47]\nM v[104:107],v[212:215],a[104:107],v[104:107]\nM v[108:111],v[212:215],a[108:111],v[108:111]\nds_read_b64_tr_b16 v[212:213],v242 offset:58368\nds_read_b64_tr_b16 v[214:215],v242 offset:58624\n" \
"M v[48:51],v[216:219],a[96:99],v[48:51]\nbuffer_load_dwordx4 a[196:199],v240,s[52:55],s76 offen offset:1024 sc1\nM v[52:55],v[216:219],a[100:103],v[52:55]\nM v[112:115],v[216:219],a[104:107],v[112:115]\nM v[116:119],v[216:219],a[108:111],v[116:119]\nds_read_b64_tr_b16 v[216:217],v241 offset:58880\ns_waitcnt lgkmcnt(7)\nM v[56:59],v[220:223],a[96:99],v[56:59]\n" \
"buffer_load_dwordx4 a[200:203],v240,s[52:55],s77 offen offset:0 sc1\nM v[60:63],v[220:223],a[100:103],v[60:63]\nds_read_b64_tr_b16 v[218:219],v241 offset:59136\nM v[120:123],v[220:223],a[104:107],v[120:123]\nM v[124:127],v[220:223],a[108:111],v[124:127]\nds_read_b64_tr_b16 v[220:221],v242 offset:58880\nds_read_b64_tr_b16 v[222:223],v242 offset:59136\nM v[0:3],v[192:195],a[112:115],v[0:3]\n" \
"s_waitcnt vmcnt(27)\nM v[4:7],v[192:195],a[116:119],v[4:7]\nbuffer_load_dwordx4 a[204:207],v240,s[52:55],s77 offen offset:1024 sc1\nM v[64:67],v[192:195],a[120:123],v[64:67]\nM v[68:71],v[192:195],a[124:127],v[68:71]\nM v[8:11],v[196:199],a[112:115],v[8:11]\nM v[12:15],v[196:199],a[116:119],v[12:15]\nbuffer_load_dwordx4 a[208:211],v240,s[52:55],s76 offen offset:2048 sc1\n" \
"M v[72:75],v[196:199],a[120:123],v[72:75]\nM v[76:79],v[196:199],a[124:127],v[76:79]\nM v[16:19],v[200:203],a[112:115],v[16:19]\nM v[20:23],v[200:203],a[116:119],v[20:23]\nbuffer_store_dwordx4 v[156:159],v240,s[52:55],s74 offen offset:3072 sc1\nM v[80:83],v[200:203],a[120:123],v[80:83]\ns_add_u32 s74,s74,4096\nM v[84:87],v[200:203],a[124:127],v[84:87]\n" \
"s_waitcnt lgkmcnt(3)\nM v[24:27],v[204:207],a[112:115],v[24:27]\nM v[28:31],v[204:207],a[116:119],v[28:31]\nbuffer_load_dwordx4 v243,s[40:43],s69 offen offset:0 lds\nM v[88:91],v[204:207],a[120:123],v[88:91]\ns_waitcnt vmcnt(24) lgkmcnt(0)\ns_barrier\nM v[92:95],v[204:207],a[124:127],v[92:95]\n" \
"ds_read_b64_tr_b16 v[192:193],v241 offset:0\nds_read_b64_tr_b16 v[194:195],v241 offset:256\nM v[32:35],v[208:211],a[112:115],v[32:35]\nds_read_b64_tr_b16 v[196:197],v242 offset:0\nds_read_b64_tr_b16 v[198:199],v242 offset:256\nM v[36:39],v[208:211],a[116:119],v[36:39]\nds_read_b64_tr_b16 v[200:201],v241 offset:512\nds_read_b64_tr_b16 v[202:203],v241 offset:768\n" \
"M v[96:99],v[208:211],a[120:123],v[96:99]\nds_read_b64_tr_b16 v[204:205],v242 offset:512\nds_read_b64_tr_b16 v[206:207],v242 offset:768\nM v[100:103],v[208:211],a[124:127],v[100:103]\nds_read_b64_tr_b16 v[208:209],v241 offset:1024\nds_read_b64_tr_b16 v[210:211],v241 offset:1280\nM v[40:43],v[212:215],a[112:115],v[40:43]\nbuffer_load_dwordx4 v244,s[40:43],s69 offen offset:1024 lds\n" \
"M v[44:47],v[212:215],a[116:119],v[44:47]\nM v[104:107],v[212:215],a[120:123],v[104:107]\nM v[108:111],v[212:215],a[124:127],v[108:111]\nds_read_b64_tr_b16 v[212:213],v242 offset:1024\nds_read_b64_tr_b16 v[214:215],v242 offset:1280\nM v[48:51],v[216:219],a[112:115],v[48:51]\nbuffer_load_dwordx4 v245,s[40:43],s69 offen offset:2048 lds\nM v[52:55],v[216:219],a[116:119],v[52:55]\n" \
"M v[112:115],v[216:219],a[120:123],v[112:115]\nM v[116:119],v[216:219],a[124:127],v[116:119]\nds_read_b64_tr_b16 v[216:217],v241 offset:1536\nds_read_b64_tr_b16 v[218:219],v241 offset:1792\nM v[56:59],v[220:223],a[112:115],v[56:59]\nbuffer_load_dwordx4 v246,s[40:43],s69 offen offset:3072 lds\nM v[60:63],v[220:223],a[116:119],v[60:63]\ns_add_u32 m0,s64,147456\n" \
"s_add_u32 s69,s68,3670016\nM v[120:123],v[220:223],a[120:123],v[120:123]\ns_lshl_b32 s68,s60,19\ns_lshl2_add_u32 s68,s64,s68\nM v[124:127],v[220:223],a[124:127],v[124:127]\nds_read_b64_tr_b16 v[220:221],v242 offset:1536\ns_waitcnt lgkmcnt(6)\nM v[0:3],v[192:195],a[128:131],v[0:3]\n" \
"ds_read_b64_tr_b16 v[222:223],v242 offset:1792\ns_add_u32 s68,s68,s70\nM v[4:7],v[192:195],a[132:135],v[4:7]\nbuffer_load_dwordx4 a[212:215],v240,s[52:55],s76 offen offset:3072 sc1\nM v[64:67],v[192:195],a[136:139],v[64:67]\ns_add_u32 s76,s76,4096\nM v[68:71],v[192:195],a[140:143],v[68:71]\nds_read_b64_tr_b16 v[192:193],v241 offset:8192\n" \
"ds_read_b64_tr_b16 v[194:195],v241 offset:8448\nM v[8:11],v[196:199],a[128:131],v[8:11]\nM v[12:15],v[196:199],a[132:135],v[12:15]\nbuffer_store_dwordx4 v[160:163],v240,s[52:55],s74 offen offset:0 sc1\nM v[72:75],v[196:199],a[136:139],v[72:75]\nM v[76:79],v[196:199],a[140:143],v[76:79]\nds_read_b64_tr_b16 v[196:197],v242 offset:8192\nds_read_b64_tr_b16 v[198:199],v242 offset:8448\n" \
"M v[16:19],v[200:203],a[128:131],v[16:19]\nM v[20:23],v[200:203],a[132:135],v[20:23]\nbuffer_load_dwordx4 a[216:219],v240,s[52:55],s77 offen offset:2048 sc1\nM v[80:83],v[200:203],a[136:139],v[80:83]\nM v[84:87],v[200:203],a[140:143],v[84:87]\nds_read_b64_tr_b16 v[200:201],v241 offset:8704\nds_read_b64_tr_b16 v[202:203],v241 offset:8960\nM v[24:27],v[204:207],a[128:131],v[24:27]\n" \
"M v[28:31],v[204:207],a[132:135],v[28:31]\nbuffer_load_dwordx4 a[220:223],v240,s[52:55],s77 offen offset:3072 sc1\nM v[88:91],v[204:207],a[136:139],v[88:91]\ns_add_u32 s77,s77,4096\nM v[92:95],v[204:207],a[140:143],v[92:95]\nds_read_b64_tr_b16 v[204:205],v242 offset:8704\ns_waitcnt lgkmcnt(7)\nM v[32:35],v[208:211],a[128:131],v[32:35]\n" \
"ds_read_b64_tr_b16 v[206:207],v242 offset:8960\nM v[36:39],v[208:211],a[132:135],v[36:39]\nbuffer_load_dwordx4 a[224:227],v240,s[52:55],s76 offen offset:0 sc1\nM v[96:99],v[208:211],a[136:139],v[96:99]\nM v[100:103],v[208:211],a[140:143],v[100:103]\nds_read_b64_tr_b16 v[208:209],v241 offset:9216\nds_read_b64_tr_b16 v[210:211],v241 offset:9472\nM v[40:43],v[212:215],a[128:131],v[40:43]\n" \
"ds_read_b128 a[0:3],v240 offset:0\nM v[44:47],v[212:215],a[132:135],v[44:47]\nbuffer_load_dword v248,v228,s[52:55],s80 offen sc1\nM v[104:107],v[212:215],a[136:139],v[104:107]\nbuffer_load_dwordx4 a[228:231],v240,s[52:55],s76 offen offset:1024 sc1\nM v[108:111],v[212:215],a[140:143],v[108:111]\nds_read_b64_tr_b16 v[212:213],v242 offset:9216\nds_read_b64_tr_b16 v[214:215],v242 offset:9472\n" \
"M v[48:51],v[216:219],a[128:131],v[48:51]\nM v[52:55],v[216:219],a[132:135],v[52:55]\nM v[112:115],v[216:219],a[136:139],v[112:115]\nbuffer_load_dwordx4 a[232:235],v240,s[52:55],s77 offen offset:0 sc1\nM v[116:119],v[216:219],a[140:143],v[116:119]\nds_read_b64_tr_b16 v[216:217],v241 offset:9728\nds_read_b64_tr_b16 v[218:219],v241 offset:9984\nM v[56:59],v[220:223],a[128:131],v[56:59]\n" \
"ds_read_b128 a[4:7],v240 offset:1024\nM v[60:63],v[220:223],a[132:135],v[60:63]\nM v[120:123],v[220:223],a[136:139],v[120:123]\nbuffer_load_dwordx4 a[236:239],v240,s[52:55],s77 offen offset:1024 sc1\nM v[124:127],v[220:223],a[140:143],v[124:127]\nds_read_b64_tr_b16 v[220:221],v242 offset:9728\ns_waitcnt lgkmcnt(8)\nM v[0:3],v[192:195],a[144:147],v[0:3]\n" \
"ds_read_b64_tr_b16 v[222:223],v242 offset:9984\nds_read_b128 a[8:11],v240 offset:2048\nM v[4:7],v[192:195],a[148:151],v[4:7]\ns_waitcnt vmcnt(28)\nM v[64:67],v[192:195],a[152:155],v[64:67]\nbuffer_store_dwordx4 v[164:167],v240,s[52:55],s74 offen offset:1024 sc1\nM v[68:71],v[192:195],a[156:159],v[68:71]\nM v[8:11],v[196:199],a[144:147],v[8:11]\n" \
"M v[12:15],v[196:199],a[148:151],v[12:15]\nM v[72:75],v[196:199],a[152:155],v[72:75]\nbuffer_load_dwordx4 a[240:243],v240,s[52:55],s76 offen offset:2048 sc1\nM v[76:79],v[196:199],a[156:159],v[76:79]\nds_read_b128 a[12:15],v240 offset:3072\nM v[16:19],v[200:203],a[144:147],v[16:19]\nM v[20:23],v[200:203],a[148:151],v[20:23]\nM v[80:83],v[200:203],a[152:155],v[80:83]\n" \
"buffer_load_dwordx4 v243,s[40:43],s69 offen offset:0 lds\nM v[84:87],v[200:203],a[156:159],v[84:87]\nM v[24:27],v[204:207],a[144:147],v[24:27]\nds_read_b128 a[16:19],v240 offset:4096\nM v[28:31],v[204:207],a[148:151],v[28:31]\nM v[88:91],v[204:207],a[152:155],v[88:91]\ns_waitcnt vmcnt(25) lgkmcnt(3)\ns_barrier\n" \
"M v[92:95],v[204:207],a[156:159],v[92:95]\nds_read_b64_tr_b16 v[192:193],v241 offset:16384\nds_read_b64_tr_b16 v[194:195],v241 offset:16640\nM v[32:35],v[208:211],a[144:147],v[32:35]\nds_read_b64_tr_b16 v[196:197],v242 offset:16384\nds_read_b64_tr_b16 v[198:199],v242 offset:16640\nM v[36:39],v[208:211],a[148:151],v[36:39]\nds_read_b64_tr_b16 v[200:201],v241 offset:16896\n" \
"ds_read_b64_tr_b16 v[202:203],v241 offset:17152\nM v[96:99],v[208:211],a[152:155],v[96:99]\nds_read_b64_tr_b16 v[204:205],v242 offset:16896\nds_read_b64_tr_b16 v[206:207],v242 offset:17152\nM v[100:103],v[208:211],a[156:159],v[100:103]\nds_read_b64_tr_b16 v[208:209],v241 offset:17408\nds_read_b64_tr_b16 v[210:211],v241 offset:17664\nM v[40:43],v[212:215],a[144:147],v[40:43]\n" \
"buffer_load_dwordx4 v244,s[40:43],s69 offen offset:1024 lds\nM v[44:47],v[212:215],a[148:151],v[44:47]\nds_read_b128 a[20:23],v240 offset:5120\nM v[104:107],v[212:215],a[152:155],v[104:107]\nM v[108:111],v[212:215],a[156:159],v[108:111]\nds_read_b64_tr_b16 v[212:213],v242 offset:17408\nds_read_b64_tr_b16 v[214:215],v242 offset:17664\nM v[48:51],v[216:219],a[144:147],v[48:51]\n" \
"buffer_load_dwordx4 v245,s[40:43],s69 offen offset:2048 lds\nM v[52:55],v[216:219],a[148:151],v[52:55]\nds_read_b128 a[24:27],v240 offset:6144\nM v[112:115],v[216:219],a[152:155],v[112:115]\nM v[116:119],v[216:219],a[156:159],v[116:119]\nds_read_b64_tr_b16 v[216:217],v241 offset:17920\nds_read_b64_tr_b16 v[218:219],v241 offset:18176\nM v[56:59],v[220:223],a[144:147],v[56:59]\n" \
"buffer_load_dwordx4 v246,s[40:43],s69 offen offset:3072 lds\nM v[60:63],v[220:223],a[148:151],v[60:63]\ns_sub_u32 s40,s18,0x1000\ns_subb_u32 s41,s19,0\nM v[120:123],v[220:223],a[152:155],v[120:123]\nds_read_b128 a[28:31],v240 offset:7168\nM v[124:127],v[220:223],a[156:159],v[124:127]\nds_read_b64_tr_b16 v[220:221],v242 offset:17920\n" \
"s_waitcnt lgkmcnt(8)\nM v[0:3],v[192:195],a[160:163],v[0:3]\nbuffer_store_dwordx4 v[168:171],v240,s[52:55],s74 offen offset:2048 sc1\nM v[4:7],v[192:195],a[164:167],v[4:7]\nds_read_b64_tr_b16 v[222:223],v242 offset:18176\nM v[64:67],v[192:195],a[168:171],v[64:67]\nM v[68:71],v[192:195],a[172:175],v[68:71]\nds_read_b64_tr_b16 v[192:193],v241 offset:24576\n" \
"ds_read_b64_tr_b16 v[194:195],v241 offset:24832\nM v[8:11],v[196:199],a[160:163],v[8:11]\nbuffer_load_dwordx4 a[244:247],v240,s[52:55],s76 offen offset:3072 sc1\nM v[12:15],v[196:199],a[164:167],v[12:15]\nds_read_b128 a[32:35],v240 offset:8192\nM v[72:75],v[196:199],a[168:171],v[72:75]\nM v[76:79],v[196:199],a[172:175],v[76:79]\nds_read_b64_tr_b16 v[196:197],v242 offset:24576\n" \
"ds_read_b64_tr_b16 v[198:199],v242 offset:24832\nM v[16:19],v[200:203],a[160:163],v[16:19]\nbuffer_load_dwordx4 a[248:251],v240,s[52:55],s77 offen offset:2048 sc1\nM v[20:23],v[200:203],a[164:167],v[20:23]\nM v[80:83],v[200:203],a[168:171],v[80:83]\nds_read_b128 a[36:39],v240 offset:9216\nM v[84:87],v[200:203],a[172:175],v[84:87]\nds_read_b64_tr_b16 v[200:201],v241 offset:25088\n" \
"ds_read_b64_tr_b16 v[202:203],v241 offset:25344\nM v[24:27],v[204:207],a[160:163],v[24:27]\nbuffer_load_dwordx4 a[252:255],v240,s[52:55],s77 offen offset:3072 sc1\nM v[28:31],v[204:207],a[164:167],v[28:31]\nM v[88:91],v[204:207],a[168:171],v[88:91]\nM v[92:95],v[204:207],a[172:175],v[92:95]\nds_read_b64_tr_b16 v[204:205],v242 offset:25088\nds_read_b64_tr_b16 v[206:207],v242 offset:25344\n" \
"M v[32:35],v[208:211],a[160:163],v[32:35]\nds_read_b128 a[40:43],v240 offset:10240\nM v[36:39],v[208:211],a[164:167],v[36:39]\ns_waitcnt vmcnt(13)\nv_cmp_eq_u32 vcc,0,v248\nM v[96:99],v[208:211],a[168:171],v[96:99]\ns_cbranch_vccnz .Lps1\n.Lpb1:\n" \
"s_add_u32 s80,s80,4096\nM v[100:103],v[208:211],a[172:175],v[100:103]\nds_read_b64_tr_b16 v[208:209],v241 offset:25600\ns_waitcnt lgkmcnt(8)\nM v[40:43],v[212:215],a[160:163],v[40:43]\ns_cmp_ge_u32 s62,6\ns_cselect_b64 vcc,-1,0\nM v[44:47],v[212:215],a[164:167],v[44:47]\n" \
"ds_read_b64_tr_b16 v[210:211],v241 offset:25856\nv_cndmask_b32 v254,v231,v229,vcc\nM v[104:107],v[212:215],a[168:171],v[104:107]\ns_cmp_eq_u32 s62,3\ns_cselect_b64 vcc,-1,0\nM v[108:111],v[212:215],a[172:175],v[108:111]\nds_read_b64_tr_b16 v[212:213],v242 offset:25600\nds_read_b64_tr_b16 v[214:215],v242 offset:25856\n" \
"M v[48:51],v[216:219],a[160:163],v[48:51]\nv_cndmask_b32 v254,v254,v230,vcc\nv_bfe_u32 v230,v249,2,3\nM v[52:55],v[216:219],a[164:167],v[52:55]\nv_and_b32 v231,0x70,v243\nv_lshl_or_b32 v230,v230,10,v231\nM v[112:115],v[216:219],a[168:171],v[112:115]\nv_and_b32 v231,16,v249\n" \
"v_lshlrev_b32 v231,1,v231\nM v[116:119],v[216:219],a[172:175],v[116:119]\nds_read_b64_tr_b16 v[216:217],v241 offset:26112\nds_read_b64_tr_b16 v[218:219],v241 offset:26368\nM v[56:59],v[220:223],a[160:163],v[56:59]\nv_xor_b32 v230,v230,v231\nv_add_u32 v243,4096,v230\nM v[60:63],v[220:223],a[164:167],v[60:63]\n" \
"v_add_u32 v244,3200,v230\nv_add_u32 v245,10240,v230\nM v[120:123],v[220:223],a[168:171],v[120:123]\nbuffer_store_dwordx4 v[172:175],v240,s[52:55],s74 offen offset:3072 sc1\nM v[124:127],v[220:223],a[172:175],v[124:127]\nds_read_b64_tr_b16 v[220:221],v242 offset:26112\nds_read_b64_tr_b16 v[222:223],v242 offset:26368\nM v[0:3],v[192:195],a[176:179],v[0:3]\n" \
"s_add_u32 s74,s74,4096\nv_add_u32 v246,9344,v230\nM v[4:7],v[192:195],a[180:183],v[4:7]\nv_and_b32 v229,3,v249\nv_cmp_eq_u32 vcc,0,v229\nM v[64:67],v[192:195],a[184:187],v[64:67]\nv_mov_b32 v230,0x3f803f80\nv_cndmask_b32 v229,0,v230,vcc\n" \
"M v[68:71],v[192:195],a[188:191],v[68:71]\nv_and_b32 v230,0xff,v241\ns_waitcnt lgkmcnt(4)\nM v[8:11],v[196:199],a[176:179],v[8:11]\nv_and_b32 v231,16,v249\nv_lshl_or_b32 v230,v231,4,v230\nM v[12:15],v[196:199],a[180:183],v[12:15]\nv_and_b32 v231,32,v249\n" \
"ds_read_b128 a[44:47],v240 offset:11264\nM v[72:75],v[196:199],a[184:187],v[72:75]\nds_read_b128 a[48:51],v240 offset:12288\nds_read_b128 a[52:55],v240 offset:13312\nM v[76:79],v[196:199],a[188:191],v[76:79]\nM v[16:19],v[200:203],a[176:179],v[16:19]\nds_read_b128 a[56:59],v240 offset:14336\nM v[20:23],v[200:203],a[180:183],v[20:23]\n" \
"M v[80:83],v[200:203],a[184:187],v[80:83]\nM v[84:87],v[200:203],a[188:191],v[84:87]\nM v[24:27],v[204:207],a[176:179],v[24:27]\nM v[28:31],v[204:207],a[180:183],v[28:31]\nM v[88:91],v[204:207],a[184:187],v[88:91]\ns_waitcnt lgkmcnt(4)\ns_barrier\nM v[92:95],v[204:207],a[188:191],v[92:95]\n" \
"ds_read_b64_tr_b16 v[192:193],v241 offset:32768\nds_read_b64_tr_b16 v[194:195],v241 offset:33024\nM v[32:35],v[208:211],a[176:179],v[32:35]\nds_read_b64_tr_b16 v[196:197],v242 offset:32768\nds_read_b64_tr_b16 v[198:199],v242 offset:33024\nM v[36:39],v[208:211],a[180:183],v[36:39]\nds_read_b64_tr_b16 v[200:201],v241 offset:33280\nds_read_b64_tr_b16 v[202:203],v241 offset:33536\n" \
"M v[96:99],v[208:211],a[184:187],v[96:99]\nds_read_b64_tr_b16 v[204:205],v242 offset:33280\nds_read_b64_tr_b16 v[206:207],v242 offset:33536\nM v[100:103],v[208:211],a[188:191],v[100:103]\nds_read_b64_tr_b16 v[208:209],v241 offset:33792\nds_read_b64_tr_b16 v[210:211],v241 offset:34048\nM v[40:43],v[212:215],a[176:179],v[40:43]\nds_write_b64 v250,v[128:129] offset:0\n" \
"ds_read_b128 a[60:63],v240 offset:15360\nM v[44:47],v[212:215],a[180:183],v[44:47]\nds_write_b64 v250,v[130:131] offset:8\nM v[104:107],v[212:215],a[184:187],v[104:107]\nds_write_b64 v250,v[132:133] offset:1024\nM v[108:111],v[212:215],a[188:191],v[108:111]\nds_read_b64_tr_b16 v[212:213],v242 offset:33792\nds_read_b64_tr_b16 v[214:215],v242 offset:34048\n" \
"M v[48:51],v[216:219],a[176:179],v[48:51]\nM v[52:55],v[216:219],a[180:183],v[52:55]\nds_write_b64 v250,v[134:135] offset:1032\nM v[112:115],v[216:219],a[184:187],v[112:115]\nM v[116:119],v[216:219],a[188:191],v[116:119]\nds_read_b64_tr_b16 v[216:217],v241 offset:34304\nds_read_b64_tr_b16 v[218:219],v241 offset:34560\nM v[56:59],v[220:223],a[176:179],v[56:59]\n" \
"buffer_store_dwordx4 v[176:179],v240,s[52:55],s74 offen offset:0 sc1\nM v[60:63],v[220:223],a[180:183],v[60:63]\nds_write_b64 v250,v[136:137] offset:2048\nM v[120:123],v[220:223],a[184:187],v[120:123]\nM v[124:127],v[220:223],a[188:191],v[124:127]\nds_read_b64_tr_b16 v[220:221],v242 offset:34304\ns_waitcnt lgkmcnt(13)\nM v[0:3],v[192:195],a[192:195],v[0:3]\n" \
"ds_write_b64 v250,v[138:139] offset:2056\nds_read_b64_tr_b16 v[222:223],v242 offset:34560\nM v[4:7],v[192:195],a[196:199],v[4:7]\nM v[64:67],v[192:195],a[200:203],v[64:67]\nds_write_b64 v250,v[140:141] offset:3072\nM v[68:71],v[192:195],a[204:207],v[68:71]\nds_read_b64_tr_b16 v[192:193],v241 offset:40960\nds_read_b64_tr_b16 v[194:195],v241 offset:41216\n" \
"M v[8:11],v[196:199],a[192:195],v[8:11]\nM v[12:15],v[196:199],a[196:199],v[12:15]\nds_write_b64 v250,v[142:143] offset:3080\nM v[72:75],v[196:199],a[200:203],v[72:75]\nM v[76:79],v[196:199],a[204:207],v[76:79]\nds_read_b64_tr_b16 v[196:197],v242 offset:40960\nds_read_b64_tr_b16 v[198:199],v242 offset:41216\nM v[16:19],v[200:203],a[192:195],v[16:19]\n" \
"ds_write_b64 v250,v[144:145] offset:4096\nM v[20:23],v[200:203],a[196:199],v[20:23]\nM v[80:83],v[200:203],a[200:203],v[80:83]\nM v[84:87],v[200:203],a[204:207],v[84:87]\nds_read_b64_tr_b16 v[200:201],v241 offset:41472\nds_read_b64_tr_b16 v[202:203],v241 offset:41728\nM v[24:27],v[204:207],a[192:195],v[24:27]\nds_write_b64 v250,v[146:147] offset:4104\n" \
"M v[28:31],v[204:207],a[196:199],v[28:31]\nM v[88:91],v[204:207],a[200:203],v[88:91]\nds_write_b64 v250,v[148:149] offset:5120\nM v[92:95],v[204:207],a[204:207],v[92:95]\nds_read_b64_tr_b16 v[204:205],v242 offset:41472\ns_waitcnt lgkmcnt(12)\nM v[32:35],v[208:211],a[192:195],v[32:35]\nds_read_b64_tr_b16 v[206:207],v242 offset:41728\n" \
"M v[36:39],v[208:211],a[196:199],v[36:39]\nds_write_b64 v250,v[150:151] offset:5128\nM v[96:99],v[208:211],a[200:203],v[96:99]\nM v[100:103],v[208:211],a[204:207],v[100:103]\nds_read_b64_tr_b16 v[208:209],v241 offset:41984\nds_read_b64_tr_b16 v[210:211],v241 offset:42240\nM v[40:43],v[212:215],a[192:195],v[40:43]\nds_write_b64 v250,v[152:153] offset:6144\n" \
"M v[44:47],v[212:215],a[196:199],v[44:47]\nM v[104:107],v[212:215],a[200:203],v[104:107]\nM v[108:111],v[212:215],a[204:207],v[108:111]\nds_read_b64_tr_b16 v[212:213],v242 offset:41984\nds_read_b64_tr_b16 v[214:215],v242 offset:42240\nM v[48:51],v[216:219],a[192:195],v[48:51]\nds_write_b64 v250,v[154:155] offset:6152\nM v[52:55],v[216:219],a[196:199],v[52:55]\n" \
"M v[112:115],v[216:219],a[200:203],v[112:115]\nbuffer_store_dwordx4 v[180:183],v240,s[52:55],s74 offen offset:1024 sc1\nM v[116:119],v[216:219],a[204:207],v[116:119]\nds_read_b64_tr_b16 v[216:217],v241 offset:42496\nds_read_b64_tr_b16 v[218:219],v241 offset:42752\nM v[56:59],v[220:223],a[192:195],v[56:59]\nds_write_b64 v250,v[156:157] offset:7168\nM v[60:63],v[220:223],a[196:199],v[60:63]\n" \
"ds_write_b64 v250,v[158:159] offset:7176\nM v[120:123],v[220:223],a[200:203],v[120:123]\nM v[124:127],v[220:223],a[204:207],v[124:127]\nds_read_b64_tr_b16 v[220:221],v242 offset:42496\ns_waitcnt lgkmcnt(11)\nM v[0:3],v[192:195],a[208:211],v[0:3]\nds_write_b64 v250,v[160:161] offset:8192\nds_read_b64_tr_b16 v[222:223],v242 offset:42752\n" \
"M v[4:7],v[192:195],a[212:215],v[4:7]\nM v[64:67],v[192:195],a[216:219],v[64:67]\nM v[68:71],v[192:195],a[220:223],v[68:71]\nds_write_b64 v250,v[162:163] offset:8200\nM v[8:11],v[196:199],a[208:211],v[8:11]\nM v[12:15],v[196:199],a[212:215],v[12:15]\nM v[72:75],v[196:199],a[216:219],v[72:75]\nds_write_b64 v250,v[164:165] offset:9216\n" \
"M v[76:79],v[196:199],a[220:223],v[76:79]\nM v[16:19],v[200:203],a[208:211],v[16:19]\nM v[20:23],v[200:203],a[212:215],v[20:23]\nds_write_b64 v250,v[166:167] offset:9224\nM v[80:83],v[200:203],a[216:219],v[80:83]\nM v[84:87],v[200:203],a[220:223],v[84:87]\nM v[24:27],v[204:207],a[208:211],v[24:27]\nds_write_b64 v250,v[168:169] offset:10240\n" \
"M v[28:31],v[204:207],a[212:215],v[28:31]\nM v[88:91],v[204:207],a[216:219],v[88:91]\ns_waitcnt vmcnt(7) lgkmcnt(4)\ns_barrier\nM v[92:95],v[204:207],a[220:223],v[92:95]\nds_read_b64_tr_b16 v[192:193],v241 offset:49152\nds_read_b64_tr_b16 v[194:195],v241 offset:49408\nM v[32:35],v[208:211],a[208:211],v[32:35]\n" \
"ds_read_b64_tr_b16 v[196:197],v242 offset:49152\nds_read_b64_tr_b16 v[198:199],v242 offset:49408\nM v[36:39],v[208:211],a[212:215],v[36:39]\nds_read_b64_tr_b16 v[200:201],v241 offset:49664\nds_read_b64_tr_b16 v[202:203],v241 offset:49920\nM v[96:99],v[208:211],a[216:219],v[96:99]\nds_read_b64_tr_b16 v[204:205],v242 offset:49664\nds_read_b64_tr_b16 v[206:207],v242 offset:49920\n" \
"M v[100:103],v[208:211],a[220:223],v[100:103]\nds_read_b64_tr_b16 v[208:209],v241 offset:50176\nds_read_b64_tr_b16 v[210:211],v241 offset:50432\nM v[40:43],v[212:215],a[208:211],v[40:43]\nds_write_b64 v250,v[170:171] offset:10248\nM v[44:47],v[212:215],a[212:215],v[44:47]\nds_write_b64 v250,v[172:173] offset:11264\nM v[104:107],v[212:215],a[216:219],v[104:107]\n" \
"ds_write_b64 v250,v[174:175] offset:11272\nM v[108:111],v[212:215],a[220:223],v[108:111]\nds_read_b64_tr_b16 v[212:213],v242 offset:50176\nds_read_b64_tr_b16 v[214:215],v242 offset:50432\nM v[48:51],v[216:219],a[208:211],v[48:51]\nbuffer_store_dwordx4 v[184:187],v240,s[52:55],s74 offen offset:2048 sc1\nM v[52:55],v[216:219],a[212:215],v[52:55]\nds_write_b64 v250,v[176:177] offset:12288\n" \
"M v[112:115],v[216:219],a[216:219],v[112:115]\nM v[116:119],v[216:219],a[220:223],v[116:119]\nds_read_b64_tr_b16 v[216:217],v241 offset:50688\nds_read_b64_tr_b16 v[218:219],v241 offset:50944\nM v[56:59],v[220:223],a[208:211],v[56:59]\nds_write_b64 v250,v[178:179] offset:12296\nv_accvgpr_write_b32 a208,v229\nM v[60:63],v[220:223],a[212:215],v[60:63]\n" \
"v_accvgpr_write_b32 a209,v229\nv_accvgpr_write_b32 a210,v229\nM v[120:123],v[220:223],a[216:219],v[120:123]\nds_write_b64 v250,v[180:181] offset:13312\nv_accvgpr_write_b32 a211,v229\nM v[124:127],v[220:223],a[220:223],v[124:127]\nds_read_b64_tr_b16 v[220:221],v242 offset:50688\ns_waitcnt lgkmcnt(11)\n" \
"M v[0:3],v[192:195],a[224:227],v[0:3]\nds_read_b64_tr_b16 v[222:223],v242 offset:50944\nM v[4:7],v[192:195],a[228:231],v[4:7]\nds_write_b64 v250,v[182:183] offset:13320\nM v[64:67],v[192:195],a[232:235],v[64:67]\nM v[68:71],v[192:195],a[236:239],v[68:71]\nds_read_b64_tr_b16 v[192:193],v241 offset:57344\nds_read_b64_tr_b16 v[194:195],v241 offset:57600\n" \
"M v[8:11],v[196:199],a[224:227],v[8:11]\nds_write_b64 v250,v[184:185] offset:14336\nM v[12:15],v[196:199],a[228:231],v[12:15]\nM v[72:75],v[196:199],a[232:235],v[72:75]\nM v[76:79],v[196:199],a[236:239],v[76:79]\nds_read_b64_tr_b16 v[196:197],v242 offset:57344\nds_read_b64_tr_b16 v[198:199],v242 offset:57600\nM v[16:19],v[200:203],a[224:227],v[16:19]\n" \
"ds_write_b64 v250,v[186:187] offset:14344\nM v[20:23],v[200:203],a[228:231],v[20:23]\nM v[80:83],v[200:203],a[232:235],v[80:83]\nds_write_b64 v250,v[188:189] offset:15360\nM v[84:87],v[200:203],a[236:239],v[84:87]\nds_read_b64_tr_b16 v[200:201],v241 offset:57856\nds_read_b64_tr_b16 v[202:203],v241 offset:58112\nM v[24:27],v[204:207],a[224:227],v[24:27]\n" \
"M v[28:31],v[204:207],a[228:231],v[28:31]\nds_write_b64 v250,v[190:191] offset:15368\nv_add_u32 v250,114688,v240\nM v[88:91],v[204:207],a[232:235],v[88:91]\nM v[92:95],v[204:207],a[236:239],v[92:95]\nds_read_b64_tr_b16 v[204:205],v242 offset:57856\nds_read_b64_tr_b16 v[206:207],v242 offset:58112\nM v[32:35],v[208:211],a[224:227],v[32:35]\n" \
"M v[36:39],v[208:211],a[228:231],v[36:39]\nM v[96:99],v[208:211],a[232:235],v[96:99]\nM v[100:103],v[208:211],a[236:239],v[100:103]\nds_read_b64_tr_b16 v[208:209],v241 offset:58368\ns_waitcnt lgkmcnt(10)\nM v[40:43],v[212:215],a[224:227],v[40:43]\nds_read_b64_tr_b16 v[210:211],v241 offset:58624\nM v[44:47],v[212:215],a[228:231],v[44:47]\n" \
"M v[104:107],v[212:215],a[232:235],v[104:107]\nbuffer_store_dwordx4 v[188:191],v240,s[52:55],s74 offen offset:3072 sc1\nM v[108:111],v[212:215],a[236:239],v[108:111]\nds_read_b64_tr_b16 v[212:213],v242 offset:58368\nds_read_b64_tr_b16 v[214:215],v242 offset:58624\nM v[48:51],v[216:219],a[224:227],v[48:51]\nM v[52:55],v[216:219],a[228:231],v[52:55]\nM v[112:115],v[216:219],a[232:235],v[112:115]\n" \
"M v[116:119],v[216:219],a[236:239],v[116:119]\nds_read_b64_tr_b16 v[216:217],v241 offset:58880\nds_read_b64_tr_b16 v[218:219],v241 offset:59136\nM v[56:59],v[220:223],a[224:227],v[56:59]\nv_add_u32 v241,65536,v240\nM v[60:63],v[220:223],a[228:231],v[60:63]\nM v[120:123],v[220:223],a[232:235],v[120:123]\nM v[124:127],v[220:223],a[236:239],v[124:127]\n" \
"ds_read_b64_tr_b16 v[220:221],v242 offset:58880\nds_read_b64_tr_b16 v[222:223],v242 offset:59136\nM v[0:3],v[192:195],a[240:243],v[0:3]\nv_lshl_or_b32 v242,v231,6,v230\ns_waitcnt lgkmcnt(8)\nM v[8:11],v[196:199],a[240:243],v[8:11]\nM v[16:19],v[200:203],a[240:243],v[16:19]\nM v[24:27],v[204:207],a[240:243],v[24:27]\n" \
"s_waitcnt lgkmcnt(6)\nM v[32:35],v[208:211],a[240:243],v[32:35]\ns_waitcnt lgkmcnt(4)\nM v[40:43],v[212:215],a[240:243],v[40:43]\nv_cvt_pk_bf16_f32 v0,v0,v1\ns_waitcnt lgkmcnt(2)\nM v[48:51],v[216:219],a[240:243],v[48:51]\nv_cvt_pk_bf16_f32 v16,v16,v17\n" \
"s_waitcnt lgkmcnt(0)\nM v[56:59],v[220:223],a[240:243],v[56:59]\nv_cvt_pk_bf16_f32 v32,v32,v33\ns_waitcnt vmcnt(5)\nM v[4:7],v[192:195],a[244:247],v[4:7]\nv_cvt_pk_bf16_f32 v1,v2,v3\nv_cvt_pk_bf16_f32 v17,v18,v19\nM v[12:15],v[196:199],a[244:247],v[12:15]\n" \
"v_cvt_pk_bf16_f32 v48,v48,v49\nv_cvt_pk_bf16_f32 v33,v34,v35\nM v[20:23],v[200:203],a[244:247],v[20:23]\nv_cvt_pk_bf16_f32 v49,v50,v51\nv_cvt_pk_bf16_f32 v2,v8,v9\nM v[28:31],v[204:207],a[244:247],v[28:31]\nv_cvt_pk_bf16_f32 v4,v4,v5\nv_cvt_pk_bf16_f32 v5,v6,v7\n" \
"M v[36:39],v[208:211],a[244:247],v[36:39]\nv_cvt_pk_bf16_f32 v3,v10,v11\nv_cvt_pk_bf16_f32 v18,v24,v25\nM v[44:47],v[212:215],a[244:247],v[44:47]\nv_cvt_pk_bf16_f32 v20,v20,v21\nv_cvt_pk_bf16_f32 v21,v22,v23\nM v[52:55],v[216:219],a[244:247],v[52:55]\nv_cvt_pk_bf16_f32 v19,v26,v27\n" \
"v_cvt_pk_bf16_f32 v34,v40,v41\nM v[60:63],v[220:223],a[244:247],v[60:63]\nv_cvt_pk_bf16_f32 v36,v36,v37\nv_cvt_pk_bf16_f32 v37,v38,v39\nM v[64:67],v[192:195],a[248:251],v[64:67]\nv_cvt_pk_bf16_f32 v35,v42,v43\nv_cvt_pk_bf16_f32 v50,v56,v57\nM v[72:75],v[196:199],a[248:251],v[72:75]\n" \
"v_cvt_pk_bf16_f32 v52,v52,v53\nv_cvt_pk_bf16_f32 v53,v54,v55\nM v[80:83],v[200:203],a[248:251],v[80:83]\nv_cvt_pk_bf16_f32 v51,v58,v59\nv_cvt_pk_bf16_f32 v6,v12,v13\nM v[88:91],v[204:207],a[248:251],v[88:91]\nv_cvt_pk_bf16_f32 v64,v64,v65\nv_cvt_pk_bf16_f32 v65,v66,v67\n" \
"M v[96:99],v[208:211],a[248:251],v[96:99]\nv_cvt_pk_bf16_f32 v7,v14,v15\nv_cvt_pk_bf16_f32 v22,v28,v29\nM v[104:107],v[212:215],a[248:251],v[104:107]\nv_cvt_pk_bf16_f32 v80,v80,v81\nv_cvt_pk_bf16_f32 v81,v82,v83\nM v[112:115],v[216:219],a[248:251],v[112:115]\nv_cvt_pk_bf16_f32 v23,v30,v31\n" \
"v_cvt_pk_bf16_f32 v38,v44,v45\nM v[120:123],v[220:223],a[248:251],v[120:123]\nv_cvt_pk_bf16_f32 v96,v96,v97\nv_cvt_pk_bf16_f32 v97,v98,v99\nM v[68:71],v[192:195],a[252:255],v[68:71]\nv_cvt_pk_bf16_f32 v39,v46,v47\nv_cvt_pk_bf16_f32 v54,v60,v61\nM v[76:79],v[196:199],a[252:255],v[76:79]\n" \
"v_cvt_pk_bf16_f32 v112,v112,v113\nv_cvt_pk_bf16_f32 v113,v114,v115\nM v[84:87],v[200:203],a[252:255],v[84:87]\nv_cvt_pk_bf16_f32 v55,v62,v63\nv_cvt_pk_bf16_f32 v66,v72,v73\nM v[92:95],v[204:207],a[252:255],v[92:95]\nv_cvt_pk_bf16_f32 v68,v68,v69\nv_cvt_pk_bf16_f32 v69,v70,v71\n" \
"M v[100:103],v[208:211],a[252:255],v[100:103]\nv_cvt_pk_bf16_f32 v67,v74,v75\nv_cvt_pk_bf16_f32 v82,v88,v89\nM v[108:111],v[212:215],a[252:255],v[108:111]\nv_cvt_pk_bf16_f32 v84,v84,v85\nv_cvt_pk_bf16_f32 v85,v86,v87\nM v[116:119],v[216:219],a[252:255],v[116:119]\nv_cvt_pk_bf16_f32 v83,v90,v91\n" \
"v_cvt_pk_bf16_f32 v98,v104,v105\nv_cvt_pk_bf16_f32 v100,v100,v101\nv_cvt_pk_bf16_f32 v101,v102,v103\nM v[124:127],v[220:223],a[252:255],v[124:127]\nv_cvt_pk_bf16_f32 v99,v106,v107\nv_cvt_pk_bf16_f32 v114,v120,v121\nv_cvt_pk_bf16_f32 v115,v122,v123\nv_cvt_pk_bf16_f32 v116,v116,v117\n" \
"v_cvt_pk_bf16_f32 v117,v118,v119\nv_cvt_pk_bf16_f32 v70,v76,v77\nv_cvt_pk_bf16_f32 v71,v78,v79\nv_cvt_pk_bf16_f32 v86,v92,v93\nv_cvt_pk_bf16_f32 v87,v94,v95\nv_cvt_pk_bf16_f32 v102,v108,v109\nv_cvt_pk_bf16_f32 v103,v110,v111\nv_cvt_pk_bf16_f32 v118,v124,v125\n" \
"v_cvt_pk_bf16_f32 v119,v126,v127\ns_waitcnt vmcnt(0)\nbuffer_store_dword v252,off,s[52:55],s29 sc1\nv_accvgpr_write_b32 a244,0\nv_accvgpr_write_b32 a248,-1\nv_accvgpr_write_b32 a245,0\nv_accvgpr_write_b32 a249,-1\nv_accvgpr_write_b32 a246,0\n" \
"v_accvgpr_write_b32 a250,-1\nv_accvgpr_write_b32 a247,0\nv_accvgpr_write_b32 a251,-1\nv_accvgpr_write_b32 a128,v0\nv_accvgpr_write_b32 a129,v1\nv_accvgpr_write_b32 a130,v2\nv_accvgpr_write_b32 a131,v3\nv_accvgpr_write_b32 a132,v16\n" \
"v_accvgpr_write_b32 a133,v17\nv_accvgpr_write_b32 a134,v18\nv_accvgpr_write_b32 a135,v19\nv_accvgpr_write_b32 a136,v32\nv_accvgpr_write_b32 a137,v33\nv_accvgpr_write_b32 a138,v34\nv_accvgpr_write_b32 a139,v35\nv_accvgpr_write_b32 a140,v48\n" \
"v_accvgpr_write_b32 a141,v49\nv_accvgpr_write_b32 a142,v50\nv_accvgpr_write_b32 a143,v51\nv_accvgpr_write_b32 a144,v4\nv_accvgpr_write_b32 a145,v5\nv_accvgpr_write_b32 a146,v6\nv_accvgpr_write_b32 a147,v7\nv_accvgpr_write_b32 a148,v20\n" \
"v_accvgpr_write_b32 a149,v21\nv_accvgpr_write_b32 a150,v22\nv_accvgpr_write_b32 a151,v23\nv_accvgpr_write_b32 a152,v36\nv_accvgpr_write_b32 a153,v37\nv_accvgpr_write_b32 a154,v38\nv_accvgpr_write_b32 a155,v39\nv_accvgpr_write_b32 a156,v52\n" \
"v_accvgpr_write_b32 a157,v53\nv_accvgpr_write_b32 a158,v54\nv_accvgpr_write_b32 a159,v55\nv_accvgpr_write_b32 a160,v64\nv_accvgpr_write_b32 a161,v65\nv_accvgpr_write_b32 a162,v66\nv_accvgpr_write_b32 a163,v67\nv_accvgpr_write_b32 a164,v80\n" \
"v_accvgpr_write_b32 a165,v81\nv_accvgpr_write_b32 a166,v82\nv_accvgpr_write_b32 a167,v83\nv_accvgpr_write_b32 a168,v96\nv_accvgpr_write_b32 a169,v97\nv_accvgpr_write_b32 a170,v98\nv_accvgpr_write_b32 a171,v99\nv_accvgpr_write_b32 a172,v112\n" \
"v_accvgpr_write_b32 a173,v113\nv_accvgpr_write_b32 a174,v114\nv_accvgpr_write_b32 a175,v115\nv_accvgpr_write_b32 a176,v68\nv_accvgpr_write_b32 a177,v69\nv_accvgpr_write_b32 a178,v70\nv_accvgpr_write_b32 a179,v71\nv_accvgpr_write_b32 a180,v84\n" \
"v_accvgpr_write_b32 a181,v85\nv_accvgpr_write_b32 a182,v86\nv_accvgpr_write_b32 a183,v87\nv_accvgpr_write_b32 a184,v100\nv_accvgpr_write_b32 a185,v101\nv_accvgpr_write_b32 a186,v102\nv_accvgpr_write_b32 a187,v103\nv_accvgpr_write_b32 a188,v116\n" \
"v_accvgpr_write_b32 a189,v117\nv_accvgpr_write_b32 a190,v118\nv_accvgpr_write_b32 a191,v119\ns_waitcnt lgkmcnt(0)\n.Lattn:\ns_cmp_lt_u32 s7,2\ns_cbranch_scc0 .Lp23\ns_cmp_eq_u32 s7,0\n" \
"s_cbranch_scc0 .Lpro1\n.Lpro0:\ns_barrier\nM v[64:67],a[0:3],a[128:131],0\nds_read_b128 a[32:35],v240 offset:40960\nds_read_b128 a[36:39],v240 offset:41984\nM v[64:67],a[4:7],a[132:135],v[64:67]\nds_read_b128 a[40:43],v240 offset:43008\n" \
"s_mov_b32 m0,s64\nv_mov_b32 v218,0\nM v[64:67],a[8:11],a[136:139],v[64:67]\nds_read_b128 a[44:47],v240 offset:44032\nv_mov_b32 v219,0\nM v[64:67],a[12:15],a[140:143],v[64:67]\nds_read_b128 a[48:51],v240 offset:45056\nbuffer_load_dwordx4 v240,s[44:47],s36 offen offset:0 sc1 lds\n" \
"M v[68:71],a[0:3],a[144:147],0\nds_read_b128 a[0:3],v240 offset:32768\nM v[68:71],a[4:7],a[148:151],v[68:71]\nds_read_b128 a[4:7],v240 offset:33792\nM v[68:71],a[8:11],a[152:155],v[68:71]\nds_read_b128 a[8:11],v240 offset:34816\nv_max3_f32 v225,v64,v65,v66\nM v[68:71],a[12:15],a[156:159],v[68:71]\n" \
"ds_read_b128 a[12:15],v240 offset:35840\nv_max_f32 v225,v225,v67\nv_cndmask_b32 v225,v224,v225,s[90:91]\nM v[76:79],a[16:19],a[144:147],0\nds_read_b128 a[16:19],v240 offset:36864\nv_cndmask_b32 v64,v64,v252,s[82:83]\nM v[76:79],a[20:23],a[148:151],v[76:79]\nds_read_b128 a[20:23],v240 offset:37888\n" \
"v_max3_f32 v229,v68,v69,v70\nv_cndmask_b32 v65,v65,v252,s[84:85]\nM v[76:79],a[24:27],a[152:155],v[76:79]\nds_read_b128 a[24:27],v240 offset:38912\nv_cndmask_b32 v66,v66,v252,s[86:87]\nM v[76:79],a[28:31],a[156:159],v[76:79]\nds_read_b128 a[28:31],v240 offset:39936\nv_cndmask_b32 v67,v67,v252,s[88:89]\n" \
"s_waitcnt lgkmcnt(7)\nM v[136:139],a[0:3],a[160:163],0\nds_read_b128 a[52:55],v240 offset:46080\ns_waitcnt lgkmcnt(7)\nM v[136:139],a[4:7],a[164:167],v[136:139]\nbuffer_load_dwordx4 v240,s[44:47],s36 offen offset:1024 sc1 lds\ns_waitcnt lgkmcnt(6)\nM v[136:139],a[8:11],a[168:171],v[136:139]\n" \
"v_max3_f32 v229,v229,v71,v76\nv_max3_f32 v229,v229,v77,v78\ns_waitcnt lgkmcnt(5)\nM v[136:139],a[12:15],a[172:175],v[136:139]\nv_max_f32 v229,v229,v79\nv_cndmask_b32 v229,v228,v229,s[90:91]\nM v[140:143],a[0:3],a[176:179],0\nds_read_b128 a[56:59],v240 offset:47104\n" \
"v_permlane16_swap_b32 v225,v229\nv_max_f32 v225,v225,v229\nM v[140:143],a[4:7],a[180:183],v[140:143]\nv_mov_b32 v229,v225\nds_read_b128 a[60:63],v240 offset:48128\nM v[140:143],a[8:11],a[184:187],v[140:143]\nv_permlane32_swap_b32 v225,v229\nv_max_f32 v225,v225,v229\n" \
"v_mov_b32 v229,v225\nM v[140:143],a[12:15],a[188:191],v[140:143]\nv_max3_f32 v233,v136,v137,v138\ns_waitcnt lgkmcnt(4)\nM v[152:155],a[16:19],a[160:163],0\nv_permlane16_swap_b32 v225,v229\nv_sub_f32 v224,0,v225\nv_sub_f32 v226,0,v225\n" \
"M v[152:155],a[20:23],a[164:167],v[152:155]\nv_sub_f32 v227,0,v225\nv_sub_f32 v225,0,v225\nM v[152:155],a[24:27],a[168:171],v[152:155]\nv_sub_f32 v228,0,v229\nv_sub_f32 v230,0,v229\ns_waitcnt lgkmcnt(2)\nM v[152:155],a[28:31],a[172:175],v[152:155]\n" \
"v_sub_f32 v231,0,v229\nv_sub_f32 v229,0,v229\nM v[156:159],a[16:19],a[176:179],0\nv_max3_f32 v237,v140,v141,v142\nds_read_b128 a[64:67],v241 offset:0\nds_read_b128 a[68:71],v241 offset:1024\nM v[156:159],a[20:23],a[180:183],v[156:159]\nds_read_b128 a[72:75],v241 offset:2048\n" \
"v_max3_f32 v233,v233,v139,v152\nM v[156:159],a[24:27],a[184:187],v[156:159]\nv_max3_f32 v233,v233,v153,v154\nv_max_f32 v233,v233,v155\nv_cndmask_b32 v233,v232,v233,s[90:91]\nM v[156:159],a[28:31],a[188:191],v[156:159]\nds_read_b128 a[76:79],v241 offset:3072\nv_add_f32 v64,v64,v224\n" \
"v_add_f32 v65,v65,v224\nM v[128:131],a[0:3],a[128:131],v[224:227]\nv_add_f32 v66,v66,v224\nv_add_f32 v67,v67,v224\nM v[128:131],a[4:7],a[132:135],v[128:131]\nv_exp_f32 v64,v64\nv_max3_f32 v237,v237,v143,v156\nM v[128:131],a[8:11],a[136:139],v[128:131]\n" \
"v_max3_f32 v237,v237,v157,v158\nv_max_f32 v237,v237,v159\nM v[128:131],a[12:15],a[140:143],v[128:131]\nv_cndmask_b32 v237,v236,v237,s[90:91]\nv_exp_f32 v65,v65\nM v[132:135],a[0:3],a[144:147],v[228:231]\nv_permlane16_swap_b32 v233,v237\nv_max_f32 v233,v233,v237\n" \
"M v[132:135],a[4:7],a[148:151],v[132:135]\nv_mov_b32 v237,v233\nv_exp_f32 v66,v66\nM v[132:135],a[8:11],a[152:155],v[132:135]\nv_permlane32_swap_b32 v233,v237\nv_max_f32 v233,v233,v237\nM v[132:135],a[12:15],a[156:159],v[132:135]\nv_mov_b32 v237,v233\n" \
"v_exp_f32 v67,v67\nM v[144:147],a[16:19],a[128:131],v[224:227]\nv_permlane16_swap_b32 v233,v237\nv_sub_f32 v232,0,v233\nM v[144:147],a[20:23],a[132:135],v[144:147]\nv_sub_f32 v234,0,v233\nv_sub_f32 v235,0,v233\nv_sub_f32 v233,0,v233\n" \
"M v[144:147],a[24:27],a[136:139],v[144:147]\nv_sub_f32 v236,0,v237\nv_sub_f32 v238,0,v237\nM v[144:147],a[28:31],a[140:143],v[144:147]\nv_sub_f32 v239,0,v237\nv_sub_f32 v237,0,v237\nv_add_f32 v68,v68,v228\nM v[148:151],a[16:19],a[144:147],v[228:231]\n" \
"v_add_f32 v69,v69,v228\nv_cndmask_b32 v76,v76,v252,s[82:83]\nv_cndmask_b32 v77,v77,v252,s[84:85]\nM v[148:151],a[20:23],a[148:151],v[148:151]\nv_cndmask_b32 v78,v78,v252,s[86:87]\nv_cndmask_b32 v79,v79,v252,s[88:89]\nM v[148:151],a[24:27],a[152:155],v[148:151]\nv_add_f32 v70,v70,v228\n" \
"v_add_f32 v71,v71,v228\nv_cvt_pk_bf16_f32 v64,v64,v65\nM v[148:151],a[28:31],a[156:159],v[148:151]\nv_exp_f32 v68,v68\nM v[160:163],a[32:35],a[128:131],v[224:227]\nv_exp_f32 v69,v69\nds_read_b128 a[80:83],v241 offset:4096\nM v[160:163],a[36:39],a[132:135],v[160:163]\n" \
"v_exp_f32 v70,v70\nM v[160:163],a[40:43],a[136:139],v[160:163]\nv_exp_f32 v71,v71\nv_add_f32 v76,v76,v228\nM v[160:163],a[44:47],a[140:143],v[160:163]\nv_add_f32 v77,v77,v228\nv_add_f32 v78,v78,v228\nM v[164:167],a[32:35],a[144:147],v[228:231]\n" \
"v_add_f32 v79,v79,v228\nv_cvt_pk_bf16_f32 v65,v66,v67\nv_mov_b32 v66,0\nM v[164:167],a[36:39],a[148:151],v[164:167]\nv_exp_f32 v76,v76\nM v[164:167],a[40:43],a[152:155],v[164:167]\nv_exp_f32 v77,v77\nv_mov_b32 v67,0\n" \
"M v[164:167],a[44:47],a[156:159],v[164:167]\nv_exp_f32 v78,v78\nM v[176:179],a[48:51],a[128:131],v[224:227]\nv_exp_f32 v79,v79\nv_cvt_pk_bf16_f32 v68,v68,v69\nM v[176:179],a[52:55],a[132:135],v[176:179]\nv_cvt_pk_bf16_f32 v69,v70,v71\nv_cvt_pk_bf16_f32 v70,v76,v77\n" \
"s_waitcnt lgkmcnt(0)\nM v[176:179],a[56:59],a[136:139],v[176:179]\nv_cvt_pk_bf16_f32 v71,v78,v79\nds_read_b128 a[84:87],v241 offset:5120\nM v[176:179],a[60:63],a[140:143],v[176:179]\nbuffer_load_dwordx4 v240,s[44:47],s36 offen offset:2048 sc1 lds\nds_read_b128 a[88:91],v241 offset:6144\nM v[180:183],a[48:51],a[144:147],v[228:231]\n" \
"ds_read_b128 a[92:95],v241 offset:7168\nds_read_b128 a[0:3],v240 offset:49152\nM v[180:183],a[52:55],a[148:151],v[180:183]\nds_read_b128 a[4:7],v240 offset:50176\nv_exp_f32 v128,v128\nM v[180:183],a[56:59],a[152:155],v[180:183]\nds_read_b128 a[8:11],v240 offset:51200\nv_add_f32 v136,v136,v232\n" \
"M v[180:183],a[60:63],a[156:159],v[180:183]\nbuffer_load_dwordx4 v240,s[44:47],s36 offen offset:3072 sc1 lds\nds_read_b128 a[12:15],v240 offset:52224\nM v[168:171],a[32:35],a[160:163],v[232:235]\nds_read_b128 a[16:19],v240 offset:53248\nv_add_f32 v137,v137,v232\nM v[168:171],a[36:39],a[164:167],v[168:171]\nv_exp_f32 v129,v129\n" \
"ds_read_b128 a[20:23],v240 offset:54272\nM v[168:171],a[40:43],a[168:171],v[168:171]\nv_exp_f32 v130,v130\nM v[168:171],a[44:47],a[172:175],v[168:171]\nv_exp_f32 v131,v131\nv_cvt_pk_bf16_f32 v192,v128,v129\nM v[172:175],a[32:35],a[176:179],v[236:239]\nv_cvt_pk_bf16_f32 v193,v130,v131\n" \
"ds_read_b128 a[24:27],v240 offset:55296\nM v[172:175],a[36:39],a[180:183],v[172:175]\nds_read_b128 a[28:31],v240 offset:56320\nv_exp_f32 v132,v132\nM v[172:175],a[40:43],a[184:187],v[172:175]\nv_exp_f32 v133,v133\nv_add_f32 v138,v138,v232\nM v[172:175],a[44:47],a[188:191],v[172:175]\n" \
"v_exp_f32 v134,v134\nM v[188:191],a[48:51],a[176:179],v[236:239]\nv_exp_f32 v135,v135\nv_cvt_pk_bf16_f32 v196,v132,v133\nM v[188:191],a[52:55],a[180:183],v[188:191]\nv_cvt_pk_bf16_f32 v197,v134,v135\nv_add_f32 v139,v139,v232\nM v[188:191],a[56:59],a[184:187],v[188:191]\n" \
"v_exp_f32 v144,v144\nv_add_f32 v140,v140,v236\nM v[188:191],a[60:63],a[188:191],v[188:191]\nv_exp_f32 v145,v145\nM v[0:3],a[64:67],v[64:67],0\nv_exp_f32 v146,v146\nv_cvt_pk_bf16_f32 v194,v144,v145\nM v[4:7],a[64:67],v[68:71],0\n" \
"v_exp_f32 v147,v147\nM v[8:11],a[68:71],v[64:67],0\nbuffer_load_dword v248,v251,s[52:55],s80 offen sc1\nv_cvt_pk_bf16_f32 v195,v146,v147\nM v[12:15],a[68:71],v[68:71],0\nv_exp_f32 v148,v148\nM v[16:19],a[72:75],v[64:67],0\nv_exp_f32 v149,v149\n" \
"v_add_f32 v141,v141,v236\nM v[20:23],a[72:75],v[68:71],0\nv_exp_f32 v150,v150\nM v[24:27],a[76:79],v[64:67],0\nv_exp_f32 v151,v151\nv_cvt_pk_bf16_f32 v198,v148,v149\nM v[28:31],a[76:79],v[68:71],0\nv_cvt_pk_bf16_f32 v199,v150,v151\n" \
"v_exp_f32 v136,v136\nM v[32:35],a[80:83],v[64:67],0\nv_exp_f32 v137,v137\nM v[36:39],a[80:83],v[68:71],0\nv_exp_f32 v138,v138\ns_waitcnt lgkmcnt(0)\nM v[40:43],a[84:87],v[64:67],0\nv_exp_f32 v139,v139\n" \
"M v[44:47],a[84:87],v[68:71],0\nv_cvt_pk_bf16_f32 v200,v136,v137\nv_cvt_pk_bf16_f32 v201,v138,v139\nv_add_f32 v142,v142,v236\nM v[48:51],a[88:91],v[64:67],0\nv_add_f32 v143,v143,v236\nds_read_b128 a[32:35],v240 offset:57344\nM v[52:55],a[88:91],v[68:71],0\n" \
"v_exp_f32 v140,v140\nv_add_f32 v152,v152,v232\nM v[56:59],a[92:95],v[64:67],0\nv_exp_f32 v141,v141\nM v[60:63],a[92:95],v[68:71],0\nv_exp_f32 v142,v142\nv_cvt_pk_bf16_f32 v204,v140,v141\nM a[192:195],a[208:211],v[64:67],0\n" \
"v_exp_f32 v143,v143\nM a[196:199],a[208:211],v[68:71],0\nv_cvt_pk_bf16_f32 v205,v142,v143\nv_add_f32 v153,v153,v232\nv_add_f32 v154,v154,v232\nM v[128:131],a[0:3],a[128:131],v[224:227]\nv_add_f32 v155,v155,v232\nds_read_b128 a[36:39],v240 offset:58368\n" \
"M v[128:131],a[4:7],a[132:135],v[128:131]\nv_exp_f32 v152,v152\nds_read_b128 a[40:43],v240 offset:59392\nM v[128:131],a[8:11],a[136:139],v[128:131]\nv_exp_f32 v153,v153\nM v[128:131],a[12:15],a[140:143],v[128:131]\nv_exp_f32 v154,v154\nv_cvt_pk_bf16_f32 v202,v152,v153\n" \
"M v[132:135],a[0:3],a[144:147],v[228:231]\nv_exp_f32 v155,v155\nds_read_b128 a[44:47],v240 offset:60416\nM v[132:135],a[4:7],a[148:151],v[132:135]\nv_cvt_pk_bf16_f32 v203,v154,v155\nv_add_f32 v156,v156,v236\nM v[132:135],a[8:11],a[152:155],v[132:135]\nv_add_f32 v157,v157,v236\n" \
"v_add_f32 v158,v158,v236\nv_add_f32 v159,v159,v236\nM v[132:135],a[12:15],a[156:159],v[132:135]\nv_exp_f32 v156,v156\nM v[144:147],a[16:19],a[128:131],v[224:227]\nv_exp_f32 v157,v157\nds_read_b128 a[48:51],v240 offset:61440\nM v[144:147],a[20:23],a[132:135],v[144:147]\n" \
"v_exp_f32 v158,v158\nM v[144:147],a[24:27],a[136:139],v[144:147]\nv_exp_f32 v159,v159\nv_cvt_pk_bf16_f32 v206,v156,v157\nM v[144:147],a[28:31],a[140:143],v[144:147]\nv_cvt_pk_bf16_f32 v207,v158,v159\nds_read_b128 a[52:55],v240 offset:62464\nM v[148:151],a[16:19],a[144:147],v[228:231]\n" \
"v_exp_f32 v160,v160\nds_read_b128 a[56:59],v240 offset:63488\nM v[148:151],a[20:23],a[148:151],v[148:151]\nv_exp_f32 v161,v161\nM v[148:151],a[24:27],a[152:155],v[148:151]\nv_exp_f32 v162,v162\nv_cvt_pk_bf16_f32 v208,v160,v161\nM v[148:151],a[28:31],a[156:159],v[148:151]\n" \
"v_exp_f32 v163,v163\nM v[136:139],a[0:3],a[160:163],v[232:235]\nv_cvt_pk_bf16_f32 v209,v162,v163\nds_read_b128 a[60:63],v240 offset:64512\nv_cndmask_b32 v168,v168,v252,s[82:83]\nM v[136:139],a[4:7],a[164:167],v[136:139]\nv_exp_f32 v164,v164\nv_cndmask_b32 v169,v169,v252,s[84:85]\n" \
"M v[136:139],a[8:11],a[168:171],v[136:139]\nv_exp_f32 v165,v165\nM v[136:139],a[12:15],a[172:175],v[136:139]\nv_exp_f32 v166,v166\nv_cvt_pk_bf16_f32 v212,v164,v165\nM v[140:143],a[0:3],a[176:179],v[236:239]\nv_exp_f32 v167,v167\nM v[140:143],a[4:7],a[180:183],v[140:143]\n" \
"v_cvt_pk_bf16_f32 v213,v166,v167\nv_exp_f32 v176,v176\nM v[140:143],a[8:11],a[184:187],v[140:143]\nv_exp_f32 v177,v177\nM v[140:143],a[12:15],a[188:191],v[140:143]\nv_exp_f32 v178,v178\nv_cvt_pk_bf16_f32 v210,v176,v177\nM v[152:155],a[16:19],a[160:163],v[232:235]\n" \
"v_exp_f32 v179,v179\nM v[152:155],a[20:23],a[164:167],v[152:155]\nv_cvt_pk_bf16_f32 v211,v178,v179\nv_exp_f32 v180,v180\nM v[152:155],a[24:27],a[168:171],v[152:155]\nv_exp_f32 v181,v181\nM v[152:155],a[28:31],a[172:175],v[152:155]\nv_exp_f32 v182,v182\n" \
"v_cvt_pk_bf16_f32 v214,v180,v181\nM v[156:159],a[16:19],a[176:179],v[236:239]\nv_exp_f32 v183,v183\nM v[156:159],a[20:23],a[180:183],v[156:159]\nv_cvt_pk_bf16_f32 v215,v182,v183\nv_cndmask_b32 v170,v170,v252,s[86:87]\nv_cndmask_b32 v171,v171,v252,s[88:89]\nM v[156:159],a[24:27],a[184:187],v[156:159]\n" \
"v_exp_f32 v168,v168\nds_read_b128 a[64:67],v241 offset:32768\nM v[156:159],a[28:31],a[188:191],v[156:159]\nds_read_b128 a[68:71],v241 offset:33792\ns_waitcnt lgkmcnt(2)\nM v[160:163],a[32:35],a[128:131],v[224:227]\nv_exp_f32 v169,v169\nv_cndmask_b32 v188,v188,v252,s[82:83]\n" \
"M v[160:163],a[36:39],a[132:135],v[160:163]\nv_exp_f32 v170,v170\nM v[160:163],a[40:43],a[136:139],v[160:163]\ns_waitcnt vmcnt(1)\ns_barrier\nv_cvt_pk_bf16_f32 v216,v168,v169\nM v[160:163],a[44:47],a[140:143],v[160:163]\nv_exp_f32 v171,v171\n" \
"M v[164:167],a[32:35],a[144:147],v[228:231]\nv_cvt_pk_bf16_f32 v217,v170,v171\nv_exp_f32 v172,v172\nM v[164:167],a[36:39],a[148:151],v[164:167]\nv_exp_f32 v173,v173\nM v[164:167],a[40:43],a[152:155],v[164:167]\nv_exp_f32 v174,v174\nv_cvt_pk_bf16_f32 v220,v172,v173\n" \
"M v[164:167],a[44:47],a[156:159],v[164:167]\nv_exp_f32 v175,v175\nM v[176:179],a[48:51],a[128:131],v[224:227]\nv_cvt_pk_bf16_f32 v221,v174,v175\nv_cndmask_b32 v189,v189,v252,s[84:85]\nv_cndmask_b32 v190,v190,v252,s[86:87]\nM v[176:179],a[52:55],a[132:135],v[176:179]\nv_cndmask_b32 v191,v191,v252,s[88:89]\n" \
"ds_read_b128 a[72:75],v241 offset:34816\nM v[176:179],a[56:59],a[136:139],v[176:179]\nv_exp_f32 v188,v188\nds_read_b128 a[76:79],v241 offset:35840\nM v[176:179],a[60:63],a[140:143],v[176:179]\nv_exp_f32 v189,v189\nds_read_b128 a[80:83],v241 offset:36864\nM v[180:183],a[48:51],a[144:147],v[228:231]\n" \
"v_exp_f32 v190,v190\nM v[180:183],a[52:55],a[148:151],v[180:183]\nv_exp_f32 v191,v191\nv_cvt_pk_bf16_f32 v222,v188,v189\nM v[180:183],a[56:59],a[152:155],v[180:183]\nv_cvt_pk_bf16_f32 v223,v190,v191\nds_read_b128 a[84:87],v241 offset:37888\nM v[180:183],a[60:63],a[156:159],v[180:183]\n" \
"ds_read_b128 a[88:91],v241 offset:38912\nds_read_b128 a[92:95],v241 offset:39936\ns_waitcnt vmcnt(0)\nv_cmp_eq_u32 vcc,0,v248\nM v[168:171],a[32:35],a[160:163],v[232:235]\nds_read_b128 a[96:99],v241 offset:40960\ns_cbranch_vccnz .Lps2\n.Lpb2:\n" \
"M v[168:171],a[36:39],a[164:167],v[168:171]\nds_read_b128 a[100:103],v241 offset:41984\nv_exp_f32 v128,v128\nM v[168:171],a[40:43],a[168:171],v[168:171]\nds_read_b128 a[104:107],v241 offset:43008\ns_add_u32 m0,s64,65536\nM v[168:171],a[44:47],a[172:175],v[168:171]\nds_read_b128 a[108:111],v241 offset:44032\n" \
"buffer_load_dwordx4 v240,s[48:51],s36 offen offset:0 sc1 lds\nM v[172:175],a[32:35],a[176:179],v[236:239]\nv_exp_f32 v129,v129\nM v[172:175],a[36:39],a[180:183],v[172:175]\nds_read_b128 a[112:115],v241 offset:45056\nv_exp_f32 v130,v130\nM v[172:175],a[40:43],a[184:187],v[172:175]\nds_read_b128 a[116:119],v241 offset:46080\n" \
"ds_read_b128 a[120:123],v241 offset:47104\nM v[172:175],a[44:47],a[188:191],v[172:175]\nds_read_b128 a[124:127],v241 offset:48128\nbuffer_load_dwordx4 v240,s[48:51],s36 offen offset:1024 sc1 lds\nM v[184:187],a[48:51],a[160:163],v[232:235]\nds_read_b128 a[0:3],v240 offset:0\nv_exp_f32 v131,v131\nM v[184:187],a[52:55],a[164:167],v[184:187]\n" \
"v_exp_f32 v132,v132\nM v[184:187],a[56:59],a[168:171],v[184:187]\nds_read_b128 a[4:7],v240 offset:1024\nv_exp_f32 v133,v133\nM v[184:187],a[60:63],a[172:175],v[184:187]\nds_read_b128 a[8:11],v240 offset:2048\nds_read_b128 a[12:15],v240 offset:3072\nM v[188:191],a[48:51],a[176:179],v[236:239]\n" \
"ds_read_b128 a[16:19],v240 offset:4096\nbuffer_load_dwordx4 v240,s[48:51],s36 offen offset:2048 sc1 lds\nM v[188:191],a[52:55],a[180:183],v[188:191]\nv_exp_f32 v134,v134\nM v[188:191],a[56:59],a[184:187],v[188:191]\nds_read_b128 a[20:23],v240 offset:5120\nv_exp_f32 v135,v135\nM v[188:191],a[60:63],a[188:191],v[188:191]\n" \
"ds_read_b128 a[24:27],v240 offset:6144\ns_waitcnt lgkmcnt(8)\nM v[0:3],a[64:67],v[192:195],v[0:3]\nds_read_b128 a[28:31],v240 offset:7168\nbuffer_load_dwordx4 v240,s[48:51],s36 offen offset:3072 sc1 lds\nM v[4:7],a[64:67],v[196:199],v[4:7]\nds_read_b128 a[32:35],v240 offset:8192\nds_read_b128 a[36:39],v240 offset:9216\n" \
"M v[64:67],a[64:67],v[200:203],0\nds_read_b128 a[40:43],v240 offset:10240\nv_exp_f32 v144,v144\nM v[68:71],a[64:67],v[204:207],0\ns_add_u32 m0,s64,16384\nds_read_b128 a[44:47],v240 offset:11264\nM v[8:11],a[68:71],v[192:195],v[8:11]\nbuffer_load_dwordx4 v240,s[44:47],s65 offen offset:0 sc1 lds\n" \
"ds_read_b128 a[48:51],v240 offset:12288\nM v[12:15],a[68:71],v[196:199],v[12:15]\nds_read_b128 a[52:55],v240 offset:13312\nv_exp_f32 v145,v145\nM v[72:75],a[68:71],v[200:203],0\nv_exp_f32 v146,v146\nM v[76:79],a[68:71],v[204:207],0\nds_read_b128 a[56:59],v240 offset:14336\n" \
"v_exp_f32 v147,v147\nM v[16:19],a[72:75],v[192:195],v[16:19]\nbuffer_load_dwordx4 v240,s[44:47],s65 offen offset:1024 sc1 lds\nM v[20:23],a[72:75],v[196:199],v[20:23]\nds_read_b128 a[60:63],v240 offset:15360\nv_exp_f32 v148,v148\nM v[80:83],a[72:75],v[200:203],0\nv_exp_f32 v149,v149\n" \
"M v[84:87],a[72:75],v[204:207],0\nv_exp_f32 v150,v150\nM v[24:27],a[76:79],v[192:195],v[24:27]\nbuffer_load_dwordx4 v240,s[44:47],s65 offen offset:2048 sc1 lds\nM v[28:31],a[76:79],v[196:199],v[28:31]\nv_exp_f32 v151,v151\nM v[88:91],a[76:79],v[200:203],0\nv_exp_f32 v136,v136\n" \
"M v[92:95],a[76:79],v[204:207],0\nv_exp_f32 v137,v137\nM v[32:35],a[80:83],v[192:195],v[32:35]\nbuffer_load_dwordx4 v240,s[44:47],s65 offen offset:3072 sc1 lds\nM v[36:39],a[80:83],v[196:199],v[36:39]\ns_add_u32 m0,s64,81920\nv_exp_f32 v138,v138\nM v[96:99],a[80:83],v[200:203],0\n" \
"v_exp_f32 v139,v139\nM v[100:103],a[80:83],v[204:207],0\nv_exp_f32 v140,v140\nM v[40:43],a[84:87],v[192:195],v[40:43]\nbuffer_load_dwordx4 v240,s[48:51],s65 offen offset:0 sc1 lds\nM v[44:47],a[84:87],v[196:199],v[44:47]\nv_exp_f32 v141,v141\nM v[104:107],a[84:87],v[200:203],0\n" \
"v_exp_f32 v142,v142\nM v[108:111],a[84:87],v[204:207],0\nv_exp_f32 v143,v143\nM v[48:51],a[88:91],v[192:195],v[48:51]\nbuffer_load_dwordx4 v240,s[48:51],s65 offen offset:1024 sc1 lds\nM v[52:55],a[88:91],v[196:199],v[52:55]\nv_exp_f32 v152,v152\nM v[112:115],a[88:91],v[200:203],0\n" \
"v_exp_f32 v153,v153\nM v[116:119],a[88:91],v[204:207],0\nv_exp_f32 v154,v154\nM v[56:59],a[92:95],v[192:195],v[56:59]\nbuffer_load_dwordx4 v240,s[48:51],s65 offen offset:2048 sc1 lds\nM v[60:63],a[92:95],v[196:199],v[60:63]\nv_exp_f32 v155,v155\nM v[120:123],a[92:95],v[200:203],0\n" \
"v_exp_f32 v156,v156\nM v[124:127],a[92:95],v[204:207],0\nv_exp_f32 v157,v157\nM a[192:195],a[208:211],v[192:195],a[192:195]\nbuffer_load_dwordx4 v240,s[48:51],s65 offen offset:3072 sc1 lds\ns_add_u32 m0,s64,32768\nM a[196:199],a[208:211],v[196:199],a[196:199]\nv_exp_f32 v158,v158\n" \
"v_cvt_pk_bf16_f32 v192,v128,v129\nM a[200:203],a[208:211],v[200:203],0\nv_exp_f32 v159,v159\nM a[204:207],a[208:211],v[204:207],0\nv_exp_f32 v160,v160\nv_cvt_pk_bf16_f32 v193,v130,v131\nM v[0:3],a[96:99],v[208:211],v[0:3]\nbuffer_load_dwordx4 v240,s[44:47],s66 offen offset:0 sc1 lds\n" \
"M v[4:7],a[96:99],v[212:215],v[4:7]\nv_exp_f32 v161,v161\nv_cvt_pk_bf16_f32 v194,v144,v145\nM v[64:67],a[96:99],v[216:219],v[64:67]\nv_exp_f32 v162,v162\nM v[68:71],a[96:99],v[220:223],v[68:71]\nv_exp_f32 v163,v163\nv_cvt_pk_bf16_f32 v195,v146,v147\n" \
"M v[8:11],a[100:103],v[208:211],v[8:11]\nbuffer_load_dwordx4 v240,s[44:47],s66 offen offset:1024 sc1 lds\nM v[12:15],a[100:103],v[212:215],v[12:15]\nv_exp_f32 v164,v164\nv_cvt_pk_bf16_f32 v196,v132,v133\nM v[72:75],a[100:103],v[216:219],v[72:75]\ns_waitcnt lgkmcnt(0)\ns_barrier\n" \
"M v[76:79],a[100:103],v[220:223],v[76:79]\nv_exp_f32 v165,v165\nv_cvt_pk_bf16_f32 v197,v134,v135\nM v[16:19],a[104:107],v[208:211],v[16:19]\nbuffer_load_dwordx4 v240,s[44:47],s66 offen offset:2048 sc1 lds\nM v[20:23],a[104:107],v[212:215],v[20:23]\nv_exp_f32 v166,v166\nv_cvt_pk_bf16_f32 v198,v148,v149\n" \
"M v[80:83],a[104:107],v[216:219],v[80:83]\nv_exp_f32 v167,v167\nv_cvt_pk_bf16_f32 v199,v150,v151\nM v[84:87],a[104:107],v[220:223],v[84:87]\nv_exp_f32 v176,v176\nM v[24:27],a[108:111],v[208:211],v[24:27]\nbuffer_load_dwordx4 v240,s[44:47],s66 offen offset:3072 sc1 lds\ns_add_u32 m0,s64,98304\n" \
"M v[28:31],a[108:111],v[212:215],v[28:31]\nv_exp_f32 v177,v177\nM v[88:91],a[108:111],v[216:219],v[88:91]\nv_exp_f32 v178,v178\nv_cvt_pk_bf16_f32 v200,v136,v137\nM v[92:95],a[108:111],v[220:223],v[92:95]\nv_exp_f32 v179,v179\nM v[32:35],a[112:115],v[208:211],v[32:35]\n" \
"v_exp_f32 v180,v180\nv_cvt_pk_bf16_f32 v201,v138,v139\nM v[36:39],a[112:115],v[212:215],v[36:39]\nv_exp_f32 v181,v181\nM v[96:99],a[112:115],v[216:219],v[96:99]\nv_exp_f32 v182,v182\nv_cvt_pk_bf16_f32 v202,v152,v153\nM v[100:103],a[112:115],v[220:223],v[100:103]\n" \
"v_exp_f32 v183,v183\nM v[40:43],a[116:119],v[208:211],v[40:43]\nv_exp_f32 v168,v168\nv_cvt_pk_bf16_f32 v203,v154,v155\nM v[44:47],a[116:119],v[212:215],v[44:47]\nv_exp_f32 v169,v169\nM v[104:107],a[116:119],v[216:219],v[104:107]\nv_exp_f32 v170,v170\n" \
"v_cvt_pk_bf16_f32 v204,v140,v141\nM v[108:111],a[116:119],v[220:223],v[108:111]\nv_exp_f32 v171,v171\nv_cvt_pk_bf16_f32 v205,v142,v143\nM v[48:51],a[120:123],v[208:211],v[48:51]\nv_cvt_pk_bf16_f32 v206,v156,v157\nv_cvt_pk_bf16_f32 v207,v158,v159\nM v[52:55],a[120:123],v[212:215],v[52:55]\n" \
"buffer_load_dwordx4 v240,s[48:51],s66 offen offset:0 sc1 lds\nM v[112:115],a[120:123],v[216:219],v[112:115]\nM v[116:119],a[120:123],v[220:223],v[116:119]\nM v[56:59],a[124:127],v[208:211],v[56:59]\nM v[60:63],a[124:127],v[212:215],v[60:63]\nbuffer_load_dwordx4 v240,s[48:51],s66 offen offset:1024 sc1 lds\nM v[120:123],a[124:127],v[216:219],v[120:123]\nM v[124:127],a[124:127],v[220:223],v[124:127]\n" \
"M a[192:195],a[208:211],v[208:211],a[192:195]\nM a[196:199],a[208:211],v[212:215],a[196:199]\nbuffer_load_dwordx4 v240,s[48:51],s66 offen offset:2048 sc1 lds\nM a[200:203],a[208:211],v[216:219],a[200:203]\nbuffer_load_dwordx4 v240,s[48:51],s66 offen offset:3072 sc1 lds\nM a[204:207],a[208:211],v[220:223],a[204:207]\ns_cmp_lt_u32 s62,2\ns_cbranch_scc1 .Lx01\n" \
"s_branch .Ldual\n.Lpro1:\ns_barrier\nM v[64:67],a[0:3],a[128:131],0\nds_read_b128 a[64:67],v241 offset:0\nds_read_b128 a[68:71],v241 offset:1024\nM v[64:67],a[4:7],a[132:135],v[64:67]\nds_read_b128 a[72:75],v241 offset:2048\n" \
"s_mov_b32 m0,s64\nv_mov_b32 v202,0\nM v[64:67],a[8:11],a[136:139],v[64:67]\nds_read_b128 a[76:79],v241 offset:3072\nv_mov_b32 v203,0\nM v[64:67],a[12:15],a[140:143],v[64:67]\nds_read_b128 a[80:83],v241 offset:4096\nbuffer_load_dwordx4 v240,s[44:47],s36 offen offset:0 sc1 lds\n" \
"M v[68:71],a[0:3],a[144:147],0\nds_read_b128 a[0:3],v240 offset:32768\nM v[68:71],a[4:7],a[148:151],v[68:71]\nds_read_b128 a[4:7],v240 offset:33792\nM v[68:71],a[8:11],a[152:155],v[68:71]\nds_read_b128 a[8:11],v240 offset:34816\nv_max3_f32 v225,v64,v65,v66\nM v[68:71],a[12:15],a[156:159],v[68:71]\n" \
"ds_read_b128 a[12:15],v240 offset:35840\nbuffer_load_dwordx4 v240,s[44:47],s36 offen offset:1024 sc1 lds\nM v[72:75],a[16:19],a[128:131],0\nds_read_b128 a[84:87],v241 offset:5120\nM v[72:75],a[20:23],a[132:135],v[72:75]\nds_read_b128 a[88:91],v241 offset:6144\nM v[72:75],a[24:27],a[136:139],v[72:75]\nds_read_b128 a[92:95],v241 offset:7168\n" \
"v_max3_f32 v229,v68,v69,v70\nM v[72:75],a[28:31],a[140:143],v[72:75]\nbuffer_load_dwordx4 v240,s[44:47],s36 offen offset:2048 sc1 lds\nds_read_b128 a[96:99],v241 offset:8192\nM v[76:79],a[16:19],a[144:147],0\nds_read_b128 a[16:19],v240 offset:36864\nM v[76:79],a[20:23],a[148:151],v[76:79]\nds_read_b128 a[20:23],v240 offset:37888\n" \
"M v[76:79],a[24:27],a[152:155],v[76:79]\nds_read_b128 a[24:27],v240 offset:38912\nv_max3_f32 v225,v225,v67,v72\nv_max3_f32 v225,v225,v73,v74\nM v[76:79],a[28:31],a[156:159],v[76:79]\nds_read_b128 a[28:31],v240 offset:39936\nv_max_f32 v225,v225,v75\nv_cndmask_b32 v225,v224,v225,s[90:91]\n" \
"M v[80:83],a[32:35],a[128:131],0\nbuffer_load_dwordx4 v240,s[44:47],s36 offen offset:3072 sc1 lds\nM v[80:83],a[36:39],a[132:135],v[80:83]\nds_read_b128 a[100:103],v241 offset:9216\nds_read_b128 a[104:107],v241 offset:10240\nv_max3_f32 v229,v229,v71,v76\nM v[80:83],a[40:43],a[136:139],v[80:83]\nv_max3_f32 v229,v229,v77,v78\n" \
"v_max_f32 v229,v229,v79\nv_cndmask_b32 v229,v228,v229,s[90:91]\nM v[80:83],a[44:47],a[140:143],v[80:83]\nds_read_b128 a[108:111],v241 offset:11264\nv_permlane16_swap_b32 v225,v229\nM v[84:87],a[32:35],a[144:147],0\nv_max_f32 v225,v225,v229\nv_mov_b32 v229,v225\n" \
"ds_read_b128 a[32:35],v240 offset:40960\nM v[84:87],a[36:39],a[148:151],v[84:87]\nv_permlane32_swap_b32 v225,v229\nv_max_f32 v225,v225,v229\nM v[84:87],a[40:43],a[152:155],v[84:87]\nv_mov_b32 v229,v225\nds_read_b128 a[36:39],v240 offset:41984\nds_read_b128 a[40:43],v240 offset:43008\n" \
"M v[84:87],a[44:47],a[156:159],v[84:87]\nv_permlane16_swap_b32 v225,v229\nv_sub_f32 v224,0,v225\nv_sub_f32 v226,0,v225\nM v[92:95],a[48:51],a[144:147],0\nv_sub_f32 v227,0,v225\nv_sub_f32 v225,0,v225\nM v[92:95],a[52:55],a[148:151],v[92:95]\n" \
"v_sub_f32 v228,0,v229\nv_sub_f32 v230,0,v229\nv_sub_f32 v231,0,v229\nM v[92:95],a[56:59],a[152:155],v[92:95]\nv_sub_f32 v229,0,v229\nds_read_b128 a[44:47],v240 offset:44032\nds_read_b128 a[48:51],v240 offset:45056\nM v[92:95],a[60:63],a[156:159],v[92:95]\n" \
"ds_read_b128 a[52:55],v240 offset:46080\ns_waitcnt lgkmcnt(7)\nM v[136:139],a[0:3],a[160:163],0\nds_read_b128 a[56:59],v240 offset:47104\nv_add_f32 v64,v64,v224\nv_add_f32 v65,v65,v224\nM v[136:139],a[4:7],a[164:167],v[136:139]\nds_read_b128 a[60:63],v240 offset:48128\n" \
"v_add_f32 v66,v66,v224\nM v[136:139],a[8:11],a[168:171],v[136:139]\nv_add_f32 v67,v67,v224\nv_exp_f32 v64,v64\nM v[136:139],a[12:15],a[172:175],v[136:139]\nv_exp_f32 v65,v65\nv_add_f32 v68,v68,v228\nM v[140:143],a[0:3],a[176:179],0\n" \
"v_exp_f32 v66,v66\nM v[140:143],a[4:7],a[180:183],v[140:143]\nv_exp_f32 v67,v67\nv_add_f32 v69,v69,v228\nM v[140:143],a[8:11],a[184:187],v[140:143]\nv_add_f32 v72,v72,v224\nv_add_f32 v73,v73,v224\nv_add_f32 v74,v74,v224\n" \
"M v[140:143],a[12:15],a[188:191],v[140:143]\nv_add_f32 v75,v75,v224\nv_add_f32 v70,v70,v228\nM v[156:159],a[16:19],a[176:179],0\nv_add_f32 v71,v71,v228\nv_exp_f32 v68,v68\nM v[156:159],a[20:23],a[180:183],v[156:159]\nv_exp_f32 v69,v69\n" \
"v_cvt_pk_bf16_f32 v64,v64,v65\nM v[156:159],a[24:27],a[184:187],v[156:159]\nv_exp_f32 v72,v72\nM v[156:159],a[28:31],a[188:191],v[156:159]\nv_exp_f32 v73,v73\nv_add_f32 v76,v76,v228\nM v[128:131],a[0:3],a[128:131],v[224:227]\nv_exp_f32 v74,v74\n" \
"M v[128:131],a[4:7],a[132:135],v[128:131]\nv_exp_f32 v75,v75\nv_add_f32 v77,v77,v228\nM v[128:131],a[8:11],a[136:139],v[128:131]\nv_exp_f32 v70,v70\nv_add_f32 v78,v78,v228\nM v[128:131],a[12:15],a[140:143],v[128:131]\nv_exp_f32 v71,v71\n" \
"M v[132:135],a[0:3],a[144:147],v[228:231]\nv_add_f32 v79,v79,v228\nv_cvt_pk_bf16_f32 v65,v66,v67\nv_cvt_pk_bf16_f32 v66,v72,v73\nM v[132:135],a[4:7],a[148:151],v[132:135]\nv_exp_f32 v76,v76\nv_cvt_pk_bf16_f32 v67,v74,v75\nM v[132:135],a[8:11],a[152:155],v[132:135]\n" \
"v_exp_f32 v77,v77\nM v[132:135],a[12:15],a[156:159],v[132:135]\nv_exp_f32 v78,v78\nv_cvt_pk_bf16_f32 v68,v68,v69\nM v[144:147],a[16:19],a[128:131],v[224:227]\nv_exp_f32 v79,v79\nv_cvt_pk_bf16_f32 v69,v70,v71\nM v[144:147],a[20:23],a[132:135],v[144:147]\n" \
"v_cvt_pk_bf16_f32 v70,v76,v77\nv_cvt_pk_bf16_f32 v71,v78,v79\nM v[144:147],a[24:27],a[136:139],v[144:147]\nv_cndmask_b32 v80,v80,v252,s[82:83]\nv_cndmask_b32 v81,v81,v252,s[84:85]\nv_cndmask_b32 v82,v82,v252,s[86:87]\nM v[144:147],a[28:31],a[140:143],v[144:147]\nv_cndmask_b32 v83,v83,v252,s[88:89]\n" \
"v_add_f32 v80,v80,v224\nM v[148:151],a[16:19],a[144:147],v[228:231]\nv_add_f32 v81,v81,v224\nv_add_f32 v82,v82,v224\nv_add_f32 v83,v83,v224\nM v[148:151],a[20:23],a[148:151],v[148:151]\nv_exp_f32 v80,v80\nv_add_f32 v84,v84,v228\n" \
"M v[148:151],a[24:27],a[152:155],v[148:151]\nv_exp_f32 v81,v81\nM v[148:151],a[28:31],a[156:159],v[148:151]\nv_exp_f32 v82,v82\ns_waitcnt lgkmcnt(0)\nM v[160:163],a[32:35],a[128:131],v[224:227]\nv_exp_f32 v83,v83\nv_add_f32 v85,v85,v228\n" \
"M v[160:163],a[36:39],a[132:135],v[160:163]\nv_cndmask_b32 v92,v92,v252,s[82:83]\nv_cndmask_b32 v93,v93,v252,s[84:85]\nM v[160:163],a[40:43],a[136:139],v[160:163]\nv_cndmask_b32 v94,v94,v252,s[86:87]\nv_cndmask_b32 v95,v95,v252,s[88:89]\nv_add_f32 v86,v86,v228\nM v[160:163],a[44:47],a[140:143],v[160:163]\n" \
"v_add_f32 v87,v87,v228\nv_cvt_pk_bf16_f32 v80,v80,v81\nM v[164:167],a[32:35],a[144:147],v[228:231]\nv_exp_f32 v84,v84\nds_read_b128 a[112:115],v241 offset:12288\nM v[164:167],a[36:39],a[148:151],v[164:167]\nv_exp_f32 v85,v85\nv_add_f32 v92,v92,v228\n" \
"M v[164:167],a[40:43],a[152:155],v[164:167]\nv_exp_f32 v86,v86\nM v[164:167],a[44:47],a[156:159],v[164:167]\nv_exp_f32 v87,v87\nv_add_f32 v93,v93,v228\nM v[176:179],a[48:51],a[128:131],v[224:227]\nv_add_f32 v94,v94,v228\nv_add_f32 v95,v95,v228\n" \
"v_cvt_pk_bf16_f32 v81,v82,v83\nM v[176:179],a[52:55],a[132:135],v[176:179]\nv_exp_f32 v92,v92\nM v[176:179],a[56:59],a[136:139],v[176:179]\nv_exp_f32 v93,v93\nv_mov_b32 v82,0\nM v[176:179],a[60:63],a[140:143],v[176:179]\nv_exp_f32 v94,v94\n" \
"v_mov_b32 v83,0\nM v[180:183],a[48:51],a[144:147],v[228:231]\nv_exp_f32 v95,v95\nM v[180:183],a[52:55],a[148:151],v[180:183]\nv_cvt_pk_bf16_f32 v84,v84,v85\nv_cvt_pk_bf16_f32 v85,v86,v87\nv_cvt_pk_bf16_f32 v86,v92,v93\nM v[180:183],a[56:59],a[152:155],v[180:183]\n" \
"v_cvt_pk_bf16_f32 v87,v94,v95\nds_read_b128 a[116:119],v241 offset:13312\nM v[180:183],a[60:63],a[156:159],v[180:183]\nds_read_b128 a[120:123],v241 offset:14336\nds_read_b128 a[124:127],v241 offset:15360\nv_max3_f32 v237,v140,v141,v142\nM v[0:3],a[64:67],v[64:67],0\nds_read_b128 a[0:3],v240 offset:49152\n" \
"v_exp_f32 v128,v128\nM v[4:7],a[64:67],v[68:71],0\nds_read_b128 a[4:7],v240 offset:50176\nv_max3_f32 v237,v237,v143,v156\nM v[8:11],a[68:71],v[64:67],0\nbuffer_load_dword v248,v251,s[52:55],s80 offen sc1\nds_read_b128 a[8:11],v240 offset:51200\nM v[12:15],a[68:71],v[68:71],0\n" \
"ds_read_b128 a[12:15],v240 offset:52224\nv_exp_f32 v129,v129\nM v[16:19],a[72:75],v[64:67],0\nds_read_b128 a[16:19],v240 offset:53248\nv_cvt_pk_bf16_f32 v192,v128,v129\nM v[20:23],a[72:75],v[68:71],0\nv_exp_f32 v130,v130\nds_read_b128 a[20:23],v240 offset:54272\n" \
"M v[24:27],a[76:79],v[64:67],0\nv_exp_f32 v131,v131\nds_read_b128 a[24:27],v240 offset:55296\nM v[28:31],a[76:79],v[68:71],0\nv_cvt_pk_bf16_f32 v193,v130,v131\nds_read_b128 a[28:31],v240 offset:56320\nM v[32:35],a[80:83],v[64:67],0\nv_exp_f32 v132,v132\n" \
"v_max3_f32 v233,v136,v137,v138\nM v[36:39],a[80:83],v[68:71],0\nv_exp_f32 v133,v133\nM v[40:43],a[84:87],v[64:67],0\nv_exp_f32 v134,v134\nv_cvt_pk_bf16_f32 v196,v132,v133\nM v[44:47],a[84:87],v[68:71],0\nv_exp_f32 v135,v135\n" \
"v_max3_f32 v237,v237,v157,v158\nM v[48:51],a[88:91],v[64:67],0\nv_cvt_pk_bf16_f32 v197,v134,v135\nv_max_f32 v233,v233,v139\nM v[52:55],a[88:91],v[68:71],0\nv_exp_f32 v144,v144\nv_max_f32 v237,v237,v159\nM v[56:59],a[92:95],v[64:67],0\n" \
"v_exp_f32 v145,v145\nv_cndmask_b32 v233,v232,v233,s[90:91]\nM v[60:63],a[92:95],v[68:71],0\nv_exp_f32 v146,v146\nM a[192:195],a[208:211],v[64:67],0\nv_exp_f32 v147,v147\nv_cvt_pk_bf16_f32 v194,v144,v145\nM a[196:199],a[208:211],v[68:71],0\n" \
"v_cvt_pk_bf16_f32 v195,v146,v147\nv_cndmask_b32 v237,v236,v237,s[90:91]\nM v[0:3],a[96:99],v[80:83],v[0:3]\nv_exp_f32 v148,v148\nv_permlane16_swap_b32 v233,v237\nM v[4:7],a[96:99],v[84:87],v[4:7]\nv_max_f32 v233,v233,v237\nv_exp_f32 v149,v149\n" \
"M v[8:11],a[100:103],v[80:83],v[8:11]\nv_exp_f32 v150,v150\nM v[12:15],a[100:103],v[84:87],v[12:15]\nv_exp_f32 v151,v151\nv_mov_b32 v237,v233\nM v[16:19],a[104:107],v[80:83],v[16:19]\nv_cvt_pk_bf16_f32 v198,v148,v149\nv_permlane32_swap_b32 v233,v237\n" \
"v_cvt_pk_bf16_f32 v199,v150,v151\nM v[20:23],a[104:107],v[84:87],v[20:23]\nv_max_f32 v233,v233,v237\nv_mov_b32 v237,v233\nM v[24:27],a[108:111],v[80:83],v[24:27]\nv_cndmask_b32 v136,v136,v252,s[82:83]\nv_permlane16_swap_b32 v233,v237\nv_cndmask_b32 v137,v137,v252,s[84:85]\n" \
"M v[28:31],a[108:111],v[84:87],v[28:31]\nv_cndmask_b32 v138,v138,v252,s[86:87]\nv_cndmask_b32 v139,v139,v252,s[88:89]\ns_waitcnt lgkmcnt(0)\nM v[32:35],a[112:115],v[80:83],v[32:35]\nv_sub_f32 v232,0,v233\nv_sub_f32 v234,0,v233\nM v[36:39],a[112:115],v[84:87],v[36:39]\n" \
"v_sub_f32 v235,0,v233\nv_add_f32 v136,v136,v232\nv_add_f32 v137,v137,v232\nM v[40:43],a[116:119],v[80:83],v[40:43]\nv_add_f32 v138,v138,v232\nv_add_f32 v139,v139,v232\nM v[44:47],a[116:119],v[84:87],v[44:47]\nv_sub_f32 v233,0,v233\n" \
"v_exp_f32 v136,v136\nM v[48:51],a[120:123],v[80:83],v[48:51]\nv_exp_f32 v137,v137\nv_sub_f32 v236,0,v237\nM v[52:55],a[120:123],v[84:87],v[52:55]\nv_exp_f32 v138,v138\nM v[56:59],a[124:127],v[80:83],v[56:59]\nv_exp_f32 v139,v139\n" \
"v_cvt_pk_bf16_f32 v200,v136,v137\nM v[60:63],a[124:127],v[84:87],v[60:63]\nv_cvt_pk_bf16_f32 v201,v138,v139\nv_sub_f32 v238,0,v237\nv_sub_f32 v239,0,v237\nM a[192:195],a[208:211],v[80:83],a[192:195]\nv_add_f32 v140,v140,v236\nv_add_f32 v141,v141,v236\n" \
"M a[196:199],a[208:211],v[84:87],a[196:199]\nv_add_f32 v142,v142,v236\nv_add_f32 v143,v143,v236\nv_sub_f32 v237,0,v237\nM v[128:131],a[0:3],a[128:131],v[224:227]\nv_exp_f32 v140,v140\nds_read_b128 a[32:35],v240 offset:57344\nM v[128:131],a[4:7],a[132:135],v[128:131]\n" \
"v_exp_f32 v141,v141\nM v[128:131],a[8:11],a[136:139],v[128:131]\nv_exp_f32 v142,v142\nv_cvt_pk_bf16_f32 v204,v140,v141\nM v[128:131],a[12:15],a[140:143],v[128:131]\nv_exp_f32 v143,v143\nM v[132:135],a[0:3],a[144:147],v[228:231]\nv_cvt_pk_bf16_f32 v205,v142,v143\n" \
"ds_read_b128 a[36:39],v240 offset:58368\nds_read_b128 a[40:43],v240 offset:59392\nM v[132:135],a[4:7],a[148:151],v[132:135]\nds_read_b128 a[44:47],v240 offset:60416\nv_cndmask_b32 v156,v156,v252,s[82:83]\nv_cndmask_b32 v157,v157,v252,s[84:85]\nM v[132:135],a[8:11],a[152:155],v[132:135]\nv_cndmask_b32 v158,v158,v252,s[86:87]\n" \
"v_cndmask_b32 v159,v159,v252,s[88:89]\nM v[132:135],a[12:15],a[156:159],v[132:135]\nv_add_f32 v156,v156,v236\nv_add_f32 v157,v157,v236\nv_add_f32 v158,v158,v236\nM v[144:147],a[16:19],a[128:131],v[224:227]\nv_add_f32 v159,v159,v236\nv_exp_f32 v156,v156\n" \
"M v[144:147],a[20:23],a[132:135],v[144:147]\nv_exp_f32 v157,v157\nM v[144:147],a[24:27],a[136:139],v[144:147]\nv_exp_f32 v158,v158\nv_cvt_pk_bf16_f32 v206,v156,v157\nM v[144:147],a[28:31],a[140:143],v[144:147]\nv_exp_f32 v159,v159\nM v[148:151],a[16:19],a[144:147],v[228:231]\n" \
"v_cvt_pk_bf16_f32 v207,v158,v159\nds_read_b128 a[48:51],v240 offset:61440\nds_read_b128 a[52:55],v240 offset:62464\nM v[148:151],a[20:23],a[148:151],v[148:151]\nv_exp_f32 v160,v160\nds_read_b128 a[56:59],v240 offset:63488\nM v[148:151],a[24:27],a[152:155],v[148:151]\nv_exp_f32 v161,v161\n" \
"M v[148:151],a[28:31],a[156:159],v[148:151]\nv_exp_f32 v162,v162\nv_cvt_pk_bf16_f32 v208,v160,v161\nM v[136:139],a[0:3],a[160:163],v[232:235]\ns_waitcnt vmcnt(1)\ns_barrier\nds_read_b128 a[60:63],v240 offset:64512\nM v[136:139],a[4:7],a[164:167],v[136:139]\n" \
"v_exp_f32 v163,v163\nM v[136:139],a[8:11],a[168:171],v[136:139]\nv_cvt_pk_bf16_f32 v209,v162,v163\nv_exp_f32 v164,v164\nM v[136:139],a[12:15],a[172:175],v[136:139]\nv_exp_f32 v165,v165\nds_read_b128 a[64:67],v241 offset:32768\nM v[140:143],a[0:3],a[176:179],v[236:239]\n" \
"v_exp_f32 v166,v166\nM v[140:143],a[4:7],a[180:183],v[140:143]\nv_exp_f32 v167,v167\nv_cvt_pk_bf16_f32 v212,v164,v165\nM v[140:143],a[8:11],a[184:187],v[140:143]\nv_cvt_pk_bf16_f32 v213,v166,v167\nds_read_b128 a[68:71],v241 offset:33792\nM v[140:143],a[12:15],a[188:191],v[140:143]\n" \
"v_exp_f32 v176,v176\nds_read_b128 a[72:75],v241 offset:34816\nM v[152:155],a[16:19],a[160:163],v[232:235]\nv_exp_f32 v177,v177\nds_read_b128 a[76:79],v241 offset:35840\nM v[152:155],a[20:23],a[164:167],v[152:155]\nv_exp_f32 v178,v178\nM v[152:155],a[24:27],a[168:171],v[152:155]\n" \
"v_exp_f32 v179,v179\nv_cvt_pk_bf16_f32 v210,v176,v177\nM v[152:155],a[28:31],a[172:175],v[152:155]\nv_cvt_pk_bf16_f32 v211,v178,v179\nv_exp_f32 v180,v180\nM v[156:159],a[16:19],a[176:179],v[236:239]\nv_exp_f32 v181,v181\nM v[156:159],a[20:23],a[180:183],v[156:159]\n" \
"v_exp_f32 v182,v182\nv_cvt_pk_bf16_f32 v214,v180,v181\nM v[156:159],a[24:27],a[184:187],v[156:159]\nv_exp_f32 v183,v183\nds_read_b128 a[80:83],v241 offset:36864\nM v[156:159],a[28:31],a[188:191],v[156:159]\nv_cvt_pk_bf16_f32 v215,v182,v183\ns_waitcnt lgkmcnt(3)\n" \
"M v[160:163],a[32:35],a[128:131],v[224:227]\nds_read_b128 a[84:87],v241 offset:37888\nds_read_b128 a[88:91],v241 offset:38912\ns_waitcnt vmcnt(0)\nv_cmp_eq_u32 vcc,0,v248\nM v[160:163],a[36:39],a[132:135],v[160:163]\nds_read_b128 a[92:95],v241 offset:39936\ns_cbranch_vccnz .Lps3\n" \
".Lpb3:\nM v[160:163],a[40:43],a[136:139],v[160:163]\nds_read_b128 a[96:99],v241 offset:40960\nv_exp_f32 v128,v128\nM v[160:163],a[44:47],a[140:143],v[160:163]\nds_read_b128 a[100:103],v241 offset:41984\nv_exp_f32 v129,v129\nM v[164:167],a[32:35],a[144:147],v[228:231]\n" \
"ds_read_b128 a[104:107],v241 offset:43008\ns_add_u32 m0,s64,65536\nM v[164:167],a[36:39],a[148:151],v[164:167]\nds_read_b128 a[108:111],v241 offset:44032\nbuffer_load_dwordx4 v240,s[48:51],s36 offen offset:0 sc1 lds\nM v[164:167],a[40:43],a[152:155],v[164:167]\nds_read_b128 a[112:115],v241 offset:45056\nv_exp_f32 v130,v130\n" \
"M v[164:167],a[44:47],a[156:159],v[164:167]\nv_exp_f32 v131,v131\nM v[176:179],a[48:51],a[128:131],v[224:227]\nds_read_b128 a[116:119],v241 offset:46080\nv_exp_f32 v132,v132\nM v[176:179],a[52:55],a[132:135],v[176:179]\nds_read_b128 a[120:123],v241 offset:47104\nds_read_b128 a[124:127],v241 offset:48128\n" \
"M v[176:179],a[56:59],a[136:139],v[176:179]\nds_read_b128 a[0:3],v240 offset:0\nbuffer_load_dwordx4 v240,s[48:51],s36 offen offset:1024 sc1 lds\nM v[176:179],a[60:63],a[140:143],v[176:179]\nds_read_b128 a[4:7],v240 offset:1024\nv_exp_f32 v133,v133\nM v[180:183],a[48:51],a[144:147],v[228:231]\nv_exp_f32 v134,v134\n" \
"M v[180:183],a[52:55],a[148:151],v[180:183]\nds_read_b128 a[8:11],v240 offset:2048\nv_exp_f32 v135,v135\nM v[180:183],a[56:59],a[152:155],v[180:183]\nds_read_b128 a[12:15],v240 offset:3072\nbuffer_load_dwordx4 v240,s[48:51],s36 offen offset:2048 sc1 lds\nM v[180:183],a[60:63],a[156:159],v[180:183]\nds_read_b128 a[16:19],v240 offset:4096\n" \
"ds_read_b128 a[20:23],v240 offset:5120\nM v[168:171],a[32:35],a[160:163],v[232:235]\nds_read_b128 a[24:27],v240 offset:6144\nv_exp_f32 v144,v144\nM v[168:171],a[36:39],a[164:167],v[168:171]\nds_read_b128 a[28:31],v240 offset:7168\nv_exp_f32 v145,v145\nM v[168:171],a[40:43],a[168:171],v[168:171]\n" \
"buffer_load_dwordx4 v240,s[48:51],s36 offen offset:3072 sc1 lds\nM v[168:171],a[44:47],a[172:175],v[168:171]\ns_add_u32 m0,s64,16384\nv_exp_f32 v146,v146\nM v[172:175],a[32:35],a[176:179],v[236:239]\nv_exp_f32 v147,v147\nM v[172:175],a[36:39],a[180:183],v[172:175]\nds_read_b128 a[32:35],v240 offset:8192\n" \
"v_exp_f32 v148,v148\nM v[172:175],a[40:43],a[184:187],v[172:175]\nds_read_b128 a[36:39],v240 offset:9216\nbuffer_load_dwordx4 v240,s[44:47],s65 offen offset:0 sc1 lds\nM v[172:175],a[44:47],a[188:191],v[172:175]\nds_read_b128 a[40:43],v240 offset:10240\nds_read_b128 a[44:47],v240 offset:11264\nM v[184:187],a[48:51],a[160:163],v[232:235]\n" \
"v_exp_f32 v149,v149\nM v[184:187],a[52:55],a[164:167],v[184:187]\nv_exp_f32 v150,v150\nM v[184:187],a[56:59],a[168:171],v[184:187]\nbuffer_load_dwordx4 v240,s[44:47],s65 offen offset:1024 sc1 lds\nM v[184:187],a[60:63],a[172:175],v[184:187]\nv_exp_f32 v151,v151\nM v[188:191],a[48:51],a[176:179],v[236:239]\n" \
"v_exp_f32 v136,v136\nM v[188:191],a[52:55],a[180:183],v[188:191]\nds_read_b128 a[48:51],v240 offset:12288\nv_exp_f32 v137,v137\nM v[188:191],a[56:59],a[184:187],v[188:191]\nbuffer_load_dwordx4 v240,s[44:47],s65 offen offset:2048 sc1 lds\nds_read_b128 a[52:55],v240 offset:13312\nM v[188:191],a[60:63],a[188:191],v[188:191]\n" \
"ds_read_b128 a[56:59],v240 offset:14336\nds_read_b128 a[60:63],v240 offset:15360\nM v[0:3],a[64:67],v[192:195],v[0:3]\nv_exp_f32 v138,v138\nM v[4:7],a[64:67],v[196:199],v[4:7]\nv_exp_f32 v139,v139\nM v[64:67],a[64:67],v[200:203],0\nbuffer_load_dwordx4 v240,s[44:47],s65 offen offset:3072 sc1 lds\n" \
"M v[68:71],a[64:67],v[204:207],0\ns_add_u32 m0,s64,81920\nv_exp_f32 v140,v140\nM v[8:11],a[68:71],v[192:195],v[8:11]\nv_exp_f32 v141,v141\nM v[12:15],a[68:71],v[196:199],v[12:15]\nv_exp_f32 v142,v142\nM v[72:75],a[68:71],v[200:203],0\n" \
"buffer_load_dwordx4 v240,s[48:51],s65 offen offset:0 sc1 lds\nM v[76:79],a[68:71],v[204:207],0\ns_waitcnt lgkmcnt(3)\nM v[16:19],a[72:75],v[192:195],v[16:19]\nv_exp_f32 v143,v143\nM v[20:23],a[72:75],v[196:199],v[20:23]\nv_exp_f32 v152,v152\nM v[80:83],a[72:75],v[200:203],0\n" \
"buffer_load_dwordx4 v240,s[48:51],s65 offen offset:1024 sc1 lds\nM v[84:87],a[72:75],v[204:207],0\nv_exp_f32 v153,v153\nM v[24:27],a[76:79],v[192:195],v[24:27]\nv_exp_f32 v154,v154\nM v[28:31],a[76:79],v[196:199],v[28:31]\nv_exp_f32 v155,v155\nM v[88:91],a[76:79],v[200:203],0\n" \
"buffer_load_dwordx4 v240,s[48:51],s65 offen offset:2048 sc1 lds\nM v[92:95],a[76:79],v[204:207],0\nv_exp_f32 v156,v156\nM v[32:35],a[80:83],v[192:195],v[32:35]\nv_exp_f32 v157,v157\nM v[36:39],a[80:83],v[196:199],v[36:39]\nv_exp_f32 v158,v158\nM v[96:99],a[80:83],v[200:203],0\n" \
"buffer_load_dwordx4 v240,s[48:51],s65 offen offset:3072 sc1 lds\nM v[100:103],a[80:83],v[204:207],0\ns_add_u32 m0,s64,32768\nv_exp_f32 v159,v159\nM v[40:43],a[84:87],v[192:195],v[40:43]\nv_exp_f32 v160,v160\nM v[44:47],a[84:87],v[196:199],v[44:47]\nv_exp_f32 v161,v161\n" \
"M v[104:107],a[84:87],v[200:203],0\nbuffer_load_dwordx4 v240,s[44:47],s66 offen offset:0 sc1 lds\nM v[108:111],a[84:87],v[204:207],0\nv_exp_f32 v162,v162\nM v[48:51],a[88:91],v[192:195],v[48:51]\ns_barrier\nM v[52:55],a[88:91],v[196:199],v[52:55]\nv_exp_f32 v163,v163\n" \
"M v[112:115],a[88:91],v[200:203],0\nbuffer_load_dwordx4 v240,s[44:47],s66 offen offset:1024 sc1 lds\nM v[116:119],a[88:91],v[204:207],0\nv_exp_f32 v164,v164\nM v[56:59],a[92:95],v[192:195],v[56:59]\nv_exp_f32 v165,v165\nM v[60:63],a[92:95],v[196:199],v[60:63]\nv_exp_f32 v166,v166\n" \
"M v[120:123],a[92:95],v[200:203],0\nbuffer_load_dwordx4 v240,s[44:47],s66 offen offset:2048 sc1 lds\nM v[124:127],a[92:95],v[204:207],0\nv_exp_f32 v167,v167\nM a[192:195],a[208:211],v[192:195],a[192:195]\nv_exp_f32 v176,v176\nM a[196:199],a[208:211],v[196:199],a[196:199]\nv_exp_f32 v177,v177\n" \
"v_cvt_pk_bf16_f32 v192,v128,v129\nM a[200:203],a[208:211],v[200:203],0\nbuffer_load_dwordx4 v240,s[44:47],s66 offen offset:3072 sc1 lds\ns_add_u32 m0,s64,98304\nM a[204:207],a[208:211],v[204:207],0\nv_exp_f32 v178,v178\nM v[0:3],a[96:99],v[208:211],v[0:3]\nv_exp_f32 v179,v179\n" \
"v_cvt_pk_bf16_f32 v193,v130,v131\nM v[4:7],a[96:99],v[212:215],v[4:7]\nv_exp_f32 v180,v180\nM v[8:11],a[100:103],v[208:211],v[8:11]\nv_exp_f32 v181,v181\nv_cvt_pk_bf16_f32 v194,v144,v145\nM v[12:15],a[100:103],v[212:215],v[12:15]\nv_exp_f32 v182,v182\n" \
"v_cvt_pk_bf16_f32 v195,v146,v147\nM v[16:19],a[104:107],v[208:211],v[16:19]\nv_exp_f32 v183,v183\nM v[20:23],a[104:107],v[212:215],v[20:23]\nv_exp_f32 v168,v168\nv_cvt_pk_bf16_f32 v196,v132,v133\nM v[24:27],a[108:111],v[208:211],v[24:27]\nv_exp_f32 v169,v169\n" \
"v_cvt_pk_bf16_f32 v197,v134,v135\nM v[28:31],a[108:111],v[212:215],v[28:31]\nv_exp_f32 v170,v170\nM v[32:35],a[112:115],v[208:211],v[32:35]\nv_exp_f32 v171,v171\nv_cvt_pk_bf16_f32 v198,v148,v149\nM v[36:39],a[112:115],v[212:215],v[36:39]\nv_cvt_pk_bf16_f32 v199,v150,v151\n" \
"v_cvt_pk_bf16_f32 v200,v136,v137\nv_cvt_pk_bf16_f32 v201,v138,v139\nM v[40:43],a[116:119],v[208:211],v[40:43]\nv_cvt_pk_bf16_f32 v202,v152,v153\nv_cvt_pk_bf16_f32 v203,v154,v155\nM v[44:47],a[116:119],v[212:215],v[44:47]\nv_cvt_pk_bf16_f32 v204,v140,v141\nv_cvt_pk_bf16_f32 v205,v142,v143\n" \
"v_cvt_pk_bf16_f32 v206,v156,v157\nM v[48:51],a[120:123],v[208:211],v[48:51]\nbuffer_load_dwordx4 v240,s[48:51],s66 offen offset:0 sc1 lds\nM v[52:55],a[120:123],v[212:215],v[52:55]\nv_cvt_pk_bf16_f32 v207,v158,v159\nM v[56:59],a[124:127],v[208:211],v[56:59]\nM v[60:63],a[124:127],v[212:215],v[60:63]\nM a[192:195],a[208:211],v[208:211],a[192:195]\n" \
"buffer_load_dwordx4 v240,s[48:51],s66 offen offset:1024 sc1 lds\nbuffer_load_dwordx4 v240,s[48:51],s66 offen offset:2048 sc1 lds\nbuffer_load_dwordx4 v240,s[48:51],s66 offen offset:3072 sc1 lds\nM a[196:199],a[208:211],v[212:215],a[196:199]\ns_cmp_lt_u32 s62,2\ns_cbranch_scc1 .Lx01\ns_branch .Ldual\n.Lp23:\n" \
"s_cmp_eq_u32 s7,2\ns_cbranch_scc0 .Lpro3\n.Lpro2:\ns_barrier\nM v[64:67],a[0:3],a[128:131],0\nds_read_b128 a[64:67],v241 offset:0\nds_read_b128 a[68:71],v241 offset:1024\nM v[64:67],a[4:7],a[132:135],v[64:67]\n" \
"ds_read_b128 a[72:75],v241 offset:2048\ns_mov_b32 m0,s64\nv_mov_b32 v184,0xff800000\nM v[64:67],a[8:11],a[136:139],v[64:67]\nds_read_b128 a[76:79],v241 offset:3072\nv_mov_b32 v185,0xff800000\nM v[64:67],a[12:15],a[140:143],v[64:67]\nds_read_b128 a[80:83],v241 offset:4096\n" \
"buffer_load_dwordx4 v240,s[44:47],s36 offen offset:0 sc1 lds\nM v[68:71],a[0:3],a[144:147],0\nds_read_b128 a[0:3],v240 offset:16384\nv_mov_b32 v186,0xff800000\nv_mov_b32 v187,0xff800000\nM v[68:71],a[4:7],a[148:151],v[68:71]\nds_read_b128 a[4:7],v240 offset:17408\nv_max3_f32 v225,v64,v65,v66\n" \
"M v[68:71],a[8:11],a[152:155],v[68:71]\nds_read_b128 a[8:11],v240 offset:18432\nM v[68:71],a[12:15],a[156:159],v[68:71]\nbuffer_load_dwordx4 v240,s[44:47],s36 offen offset:1024 sc1 lds\nM v[72:75],a[16:19],a[128:131],0\nds_read_b128 a[12:15],v240 offset:19456\nds_read_b128 a[84:87],v241 offset:5120\nM v[72:75],a[20:23],a[132:135],v[72:75]\n" \
"ds_read_b128 a[88:91],v241 offset:6144\nM v[72:75],a[24:27],a[136:139],v[72:75]\nds_read_b128 a[92:95],v241 offset:7168\nv_max3_f32 v229,v68,v69,v70\nM v[72:75],a[28:31],a[140:143],v[72:75]\nds_read_b128 a[96:99],v241 offset:8192\nbuffer_load_dwordx4 v240,s[44:47],s36 offen offset:2048 sc1 lds\nM v[76:79],a[16:19],a[144:147],0\n" \
"ds_read_b128 a[16:19],v240 offset:20480\nM v[76:79],a[20:23],a[148:151],v[76:79]\nds_read_b128 a[20:23],v240 offset:21504\nM v[76:79],a[24:27],a[152:155],v[76:79]\nds_read_b128 a[24:27],v240 offset:22528\nv_max3_f32 v225,v225,v67,v72\nv_max3_f32 v225,v225,v73,v74\nM v[76:79],a[28:31],a[156:159],v[76:79]\n" \
"v_max_f32 v225,v225,v75\nv_cndmask_b32 v225,v224,v225,s[90:91]\nM v[80:83],a[32:35],a[128:131],0\nds_read_b128 a[28:31],v240 offset:23552\nbuffer_load_dwordx4 v240,s[44:47],s36 offen offset:3072 sc1 lds\nM v[80:83],a[36:39],a[132:135],v[80:83]\nds_read_b128 a[100:103],v241 offset:9216\nds_read_b128 a[104:107],v241 offset:10240\n" \
"M v[80:83],a[40:43],a[136:139],v[80:83]\nv_max3_f32 v229,v229,v71,v76\nv_max3_f32 v229,v229,v77,v78\nv_max_f32 v229,v229,v79\nM v[80:83],a[44:47],a[140:143],v[80:83]\nv_cndmask_b32 v229,v228,v229,s[90:91]\nds_read_b128 a[108:111],v241 offset:11264\nds_read_b128 a[112:115],v241 offset:12288\n" \
"M v[84:87],a[32:35],a[144:147],0\nv_permlane16_swap_b32 v225,v229\nv_max_f32 v225,v225,v229\nM v[84:87],a[36:39],a[148:151],v[84:87]\nv_mov_b32 v229,v225\nds_read_b128 a[116:119],v241 offset:13312\nds_read_b128 a[120:123],v241 offset:14336\nM v[84:87],a[40:43],a[152:155],v[84:87]\n" \
"v_permlane32_swap_b32 v225,v229\nv_max_f32 v225,v225,v229\nM v[84:87],a[44:47],a[156:159],v[84:87]\nv_mov_b32 v229,v225\nds_read_b128 a[124:127],v241 offset:15360\nds_read_b128 a[32:35],v240 offset:40960\nM v[88:91],a[48:51],a[128:131],0\nv_permlane16_swap_b32 v225,v229\n" \
"v_sub_f32 v224,0,v225\nv_sub_f32 v226,0,v225\nM v[88:91],a[52:55],a[132:135],v[88:91]\nv_sub_f32 v227,0,v225\nv_sub_f32 v225,0,v225\nM v[88:91],a[56:59],a[136:139],v[88:91]\nv_sub_f32 v228,0,v229\nv_sub_f32 v230,0,v229\n" \
"v_sub_f32 v231,0,v229\nM v[88:91],a[60:63],a[140:143],v[88:91]\nv_sub_f32 v229,0,v229\nv_add_f32 v64,v64,v224\nM v[92:95],a[48:51],a[144:147],0\nv_add_f32 v65,v65,v224\nv_add_f32 v66,v66,v224\nv_add_f32 v67,v67,v224\n" \
"M v[92:95],a[52:55],a[148:151],v[92:95]\nv_exp_f32 v64,v64\nv_add_f32 v68,v68,v228\nM v[92:95],a[56:59],a[152:155],v[92:95]\nv_exp_f32 v65,v65\nM v[92:95],a[60:63],a[156:159],v[92:95]\nv_exp_f32 v66,v66\ns_waitcnt lgkmcnt(2)\n" \
"M v[96:99],a[0:3],a[128:131],v[224:227]\nv_exp_f32 v67,v67\nv_add_f32 v69,v69,v228\nM v[96:99],a[4:7],a[132:135],v[96:99]\nv_add_f32 v72,v72,v224\nv_add_f32 v73,v73,v224\nM v[96:99],a[8:11],a[136:139],v[96:99]\nv_add_f32 v74,v74,v224\n" \
"v_add_f32 v75,v75,v224\nv_add_f32 v70,v70,v228\nM v[96:99],a[12:15],a[140:143],v[96:99]\nv_add_f32 v71,v71,v228\nv_cvt_pk_bf16_f32 v64,v64,v65\nM v[100:103],a[0:3],a[144:147],v[228:231]\nv_exp_f32 v68,v68\nv_add_f32 v76,v76,v228\n" \
"M v[100:103],a[4:7],a[148:151],v[100:103]\nv_exp_f32 v69,v69\nv_add_f32 v77,v77,v228\nM v[100:103],a[8:11],a[152:155],v[100:103]\nv_exp_f32 v72,v72\nM v[100:103],a[12:15],a[156:159],v[100:103]\nv_exp_f32 v73,v73\nv_add_f32 v78,v78,v228\n" \
"M v[108:111],a[16:19],a[144:147],v[228:231]\nv_exp_f32 v74,v74\nM v[108:111],a[20:23],a[148:151],v[108:111]\nv_exp_f32 v75,v75\nv_add_f32 v79,v79,v228\nM v[108:111],a[24:27],a[152:155],v[108:111]\nv_exp_f32 v70,v70\nv_exp_f32 v71,v71\n" \
"v_cvt_pk_bf16_f32 v65,v66,v67\nv_exp_f32 v76,v76\nv_exp_f32 v77,v77\nv_exp_f32 v78,v78\nv_exp_f32 v79,v79\nv_cvt_pk_bf16_f32 v66,v72,v73\nv_cvt_pk_bf16_f32 v67,v74,v75\nv_cvt_pk_bf16_f32 v68,v68,v69\n" \
"M v[108:111],a[28:31],a[156:159],v[108:111]\nv_cvt_pk_bf16_f32 v69,v70,v71\nv_cvt_pk_bf16_f32 v70,v76,v77\nv_cvt_pk_bf16_f32 v71,v78,v79\nM v[0:3],a[64:67],v[64:67],0\nv_add_f32 v80,v80,v224\nv_add_f32 v81,v81,v224\nv_add_f32 v82,v82,v224\n" \
"M v[4:7],a[64:67],v[68:71],0\nv_add_f32 v83,v83,v224\nv_add_f32 v84,v84,v228\nM v[8:11],a[68:71],v[64:67],0\nv_exp_f32 v80,v80\nv_add_f32 v85,v85,v228\nM v[12:15],a[68:71],v[68:71],0\nv_exp_f32 v81,v81\n" \
"v_add_f32 v88,v88,v224\nM v[16:19],a[72:75],v[64:67],0\nv_exp_f32 v82,v82\nM v[20:23],a[72:75],v[68:71],0\nv_exp_f32 v83,v83\nv_add_f32 v89,v89,v224\nM v[24:27],a[76:79],v[64:67],0\nv_add_f32 v90,v90,v224\n" \
"v_add_f32 v91,v91,v224\nM v[28:31],a[76:79],v[68:71],0\nv_add_f32 v86,v86,v228\nv_add_f32 v87,v87,v228\nv_cvt_pk_bf16_f32 v80,v80,v81\nM v[32:35],a[80:83],v[64:67],0\nv_exp_f32 v84,v84\nv_add_f32 v92,v92,v228\n" \
"M v[36:39],a[80:83],v[68:71],0\nv_exp_f32 v85,v85\nM v[40:43],a[84:87],v[64:67],0\nv_exp_f32 v88,v88\nv_add_f32 v93,v93,v228\nM v[44:47],a[84:87],v[68:71],0\nv_exp_f32 v89,v89\nM v[48:51],a[88:91],v[64:67],0\n" \
"v_exp_f32 v90,v90\nv_add_f32 v94,v94,v228\nM v[52:55],a[88:91],v[68:71],0\nv_exp_f32 v91,v91\nv_add_f32 v95,v95,v228\nM v[56:59],a[92:95],v[64:67],0\nv_exp_f32 v86,v86\nM v[60:63],a[92:95],v[68:71],0\n" \
"v_exp_f32 v87,v87\nv_cvt_pk_bf16_f32 v81,v82,v83\nM a[192:195],a[208:211],v[64:67],0\nv_exp_f32 v92,v92\nv_exp_f32 v93,v93\nv_exp_f32 v94,v94\nv_exp_f32 v95,v95\nv_cvt_pk_bf16_f32 v82,v88,v89\n" \
"v_cvt_pk_bf16_f32 v83,v90,v91\nv_cvt_pk_bf16_f32 v84,v84,v85\nM a[196:199],a[208:211],v[68:71],0\nv_cvt_pk_bf16_f32 v85,v86,v87\nv_cvt_pk_bf16_f32 v86,v92,v93\nv_cvt_pk_bf16_f32 v87,v94,v95\nM v[0:3],a[96:99],v[80:83],v[0:3]\nds_read_b128 a[0:3],v240 offset:32768\n" \
"ds_read_b128 a[4:7],v240 offset:33792\nv_cndmask_b32 v96,v96,v252,s[82:83]\nM v[4:7],a[96:99],v[84:87],v[4:7]\nds_read_b128 a[8:11],v240 offset:34816\nv_cndmask_b32 v97,v97,v252,s[84:85]\nM v[8:11],a[100:103],v[80:83],v[8:11]\nds_read_b128 a[12:15],v240 offset:35840\nv_cndmask_b32 v98,v98,v252,s[86:87]\n" \
"v_cndmask_b32 v99,v99,v252,s[88:89]\nM v[12:15],a[100:103],v[84:87],v[12:15]\nds_read_b128 a[16:19],v240 offset:36864\nv_exp_f32 v96,v96\nM v[16:19],a[104:107],v[80:83],v[16:19]\nds_read_b128 a[20:23],v240 offset:37888\nv_cndmask_b32 v108,v108,v252,s[82:83]\nM v[20:23],a[104:107],v[84:87],v[20:23]\n" \
"ds_read_b128 a[24:27],v240 offset:38912\nv_exp_f32 v97,v97\nM v[24:27],a[108:111],v[80:83],v[24:27]\nds_read_b128 a[28:31],v240 offset:39936\nv_cvt_pk_bf16_f32 v96,v96,v97\nM v[28:31],a[108:111],v[84:87],v[28:31]\nds_read_b128 a[36:39],v240 offset:41984\nv_exp_f32 v98,v98\n" \
"M v[32:35],a[112:115],v[80:83],v[32:35]\nds_read_b128 a[40:43],v240 offset:43008\nv_exp_f32 v99,v99\nM v[36:39],a[112:115],v[84:87],v[36:39]\nds_read_b128 a[44:47],v240 offset:44032\nv_cndmask_b32 v109,v109,v252,s[84:85]\nM v[40:43],a[116:119],v[80:83],v[40:43]\nds_read_b128 a[48:51],v240 offset:45056\n" \
"v_exp_f32 v100,v100\nM v[44:47],a[116:119],v[84:87],v[44:47]\nds_read_b128 a[52:55],v240 offset:46080\nv_cndmask_b32 v110,v110,v252,s[86:87]\nM v[48:51],a[120:123],v[80:83],v[48:51]\nbuffer_load_dword v248,v251,s[52:55],s80 offen sc1\nds_read_b128 a[56:59],v240 offset:47104\nM v[52:55],a[120:123],v[84:87],v[52:55]\n" \
"ds_read_b128 a[60:63],v240 offset:48128\nv_cndmask_b32 v111,v111,v252,s[88:89]\ns_waitcnt lgkmcnt(9)\nM v[56:59],a[124:127],v[80:83],v[56:59]\nds_read_b128 a[64:67],v241 offset:16384\nv_cvt_pk_bf16_f32 v97,v98,v99\nM v[60:63],a[124:127],v[84:87],v[60:63]\nds_read_b128 a[68:71],v241 offset:17408\n" \
"v_exp_f32 v101,v101\nM a[192:195],a[208:211],v[80:83],a[192:195]\nds_read_b128 a[72:75],v241 offset:18432\nv_mov_b32 v98,0\nM a[196:199],a[208:211],v[84:87],a[196:199]\nds_read_b128 a[76:79],v241 offset:19456\nv_exp_f32 v102,v102\nM v[128:131],a[0:3],a[128:131],v[224:227]\n" \
"ds_read_b128 a[80:83],v241 offset:20480\nv_exp_f32 v103,v103\nM v[128:131],a[4:7],a[132:135],v[128:131]\nv_exp_f32 v108,v108\nM v[128:131],a[8:11],a[136:139],v[128:131]\nv_exp_f32 v109,v109\nv_mov_b32 v99,0\nM v[128:131],a[12:15],a[140:143],v[128:131]\n" \
"v_exp_f32 v110,v110\nM v[132:135],a[0:3],a[144:147],v[228:231]\nv_exp_f32 v111,v111\nv_cvt_pk_bf16_f32 v100,v100,v101\nM v[132:135],a[4:7],a[148:151],v[132:135]\nv_cvt_pk_bf16_f32 v101,v102,v103\nv_cvt_pk_bf16_f32 v102,v108,v109\nv_cvt_pk_bf16_f32 v103,v110,v111\n" \
"M v[132:135],a[8:11],a[152:155],v[132:135]\nds_read_b128 a[84:87],v241 offset:21504\nds_read_b128 a[88:91],v241 offset:22528\nM v[132:135],a[12:15],a[156:159],v[132:135]\nds_read_b128 a[92:95],v241 offset:23552\nv_exp_f32 v128,v128\nM v[144:147],a[16:19],a[128:131],v[224:227]\nds_read_b128 a[0:3],v240 offset:49152\n" \
"v_exp_f32 v129,v129\nM v[144:147],a[20:23],a[132:135],v[144:147]\nds_read_b128 a[4:7],v240 offset:50176\ns_waitcnt lgkmcnt(5)\nM v[144:147],a[24:27],a[136:139],v[144:147]\nds_read_b128 a[8:11],v240 offset:51200\nv_exp_f32 v130,v130\nM v[144:147],a[28:31],a[140:143],v[144:147]\n" \
"ds_read_b128 a[12:15],v240 offset:52224\nv_cvt_pk_bf16_f32 v192,v128,v129\nM v[148:151],a[16:19],a[144:147],v[228:231]\nds_read_b128 a[16:19],v240 offset:53248\nv_exp_f32 v131,v131\nM v[148:151],a[20:23],a[148:151],v[148:151]\nds_read_b128 a[20:23],v240 offset:54272\nv_exp_f32 v132,v132\n" \
"M v[148:151],a[24:27],a[152:155],v[148:151]\nds_read_b128 a[24:27],v240 offset:55296\nv_cvt_pk_bf16_f32 v193,v130,v131\nM v[148:151],a[28:31],a[156:159],v[148:151]\nds_read_b128 a[28:31],v240 offset:56320\nv_exp_f32 v133,v133\nM v[160:163],a[32:35],a[128:131],v[224:227]\nv_exp_f32 v134,v134\n" \
"M v[160:163],a[36:39],a[132:135],v[160:163]\nv_exp_f32 v135,v135\nv_cvt_pk_bf16_f32 v196,v132,v133\nM v[160:163],a[40:43],a[136:139],v[160:163]\nv_cvt_pk_bf16_f32 v197,v134,v135\nv_exp_f32 v144,v144\nM v[160:163],a[44:47],a[140:143],v[160:163]\nv_exp_f32 v145,v145\n" \
"M v[164:167],a[32:35],a[144:147],v[228:231]\nds_read_b128 a[32:35],v240 offset:57344\nv_exp_f32 v146,v146\nM v[164:167],a[36:39],a[148:151],v[164:167]\nv_exp_f32 v147,v147\nM v[164:167],a[40:43],a[152:155],v[164:167]\nds_read_b128 a[36:39],v240 offset:58368\nv_cvt_pk_bf16_f32 v194,v144,v145\n" \
"v_cvt_pk_bf16_f32 v195,v146,v147\nM v[164:167],a[44:47],a[156:159],v[164:167]\nds_read_b128 a[40:43],v240 offset:59392\nv_exp_f32 v148,v148\nM v[176:179],a[48:51],a[128:131],v[224:227]\nds_read_b128 a[44:47],v240 offset:60416\nds_read_b128 a[96:99],v241 offset:40960\nM v[176:179],a[52:55],a[132:135],v[176:179]\n" \
"v_exp_f32 v149,v149\nds_read_b128 a[100:103],v241 offset:41984\nM v[176:179],a[56:59],a[136:139],v[176:179]\nv_exp_f32 v150,v150\nM v[176:179],a[60:63],a[140:143],v[176:179]\nv_exp_f32 v151,v151\nv_cvt_pk_bf16_f32 v198,v148,v149\nM v[180:183],a[48:51],a[144:147],v[228:231]\n" \
"v_cvt_pk_bf16_f32 v199,v150,v151\nds_read_b128 a[48:51],v240 offset:61440\nds_read_b128 a[104:107],v241 offset:43008\nM v[180:183],a[52:55],a[148:151],v[180:183]\nv_exp_f32 v160,v160\nM v[180:183],a[56:59],a[152:155],v[180:183]\nv_exp_f32 v161,v161\nds_read_b128 a[52:55],v240 offset:62464\n" \
"M v[180:183],a[60:63],a[156:159],v[180:183]\nv_exp_f32 v162,v162\nM v[0:3],a[64:67],v[96:99],v[0:3]\nv_exp_f32 v163,v163\nv_cvt_pk_bf16_f32 v208,v160,v161\nM v[4:7],a[64:67],v[100:103],v[4:7]\nv_cvt_pk_bf16_f32 v209,v162,v163\nds_read_b128 a[56:59],v240 offset:63488\n" \
"ds_read_b128 a[60:63],v240 offset:64512\nM v[8:11],a[68:71],v[96:99],v[8:11]\nv_exp_f32 v164,v164\nM v[12:15],a[68:71],v[100:103],v[12:15]\nv_exp_f32 v165,v165\nds_read_b128 a[64:67],v241 offset:32768\nM v[16:19],a[72:75],v[96:99],v[16:19]\nv_exp_f32 v166,v166\n" \
"v_cvt_pk_bf16_f32 v212,v164,v165\nM v[20:23],a[72:75],v[100:103],v[20:23]\nv_exp_f32 v167,v167\nM v[24:27],a[76:79],v[96:99],v[24:27]\nv_cvt_pk_bf16_f32 v213,v166,v167\nv_exp_f32 v176,v176\nM v[28:31],a[76:79],v[100:103],v[28:31]\nv_exp_f32 v177,v177\n" \
"M v[32:35],a[80:83],v[96:99],v[32:35]\nv_exp_f32 v178,v178\nv_cvt_pk_bf16_f32 v210,v176,v177\nM v[36:39],a[80:83],v[100:103],v[36:39]\nv_exp_f32 v179,v179\ns_waitcnt lgkmcnt(3)\nM v[40:43],a[84:87],v[96:99],v[40:43]\nv_cvt_pk_bf16_f32 v211,v178,v179\n" \
"ds_read_b128 a[68:71],v241 offset:33792\nM v[44:47],a[84:87],v[100:103],v[44:47]\nv_exp_f32 v180,v180\nds_read_b128 a[72:75],v241 offset:34816\nM v[48:51],a[88:91],v[96:99],v[48:51]\ns_waitcnt vmcnt(1)\ns_barrier\nM v[52:55],a[88:91],v[100:103],v[52:55]\n" \
"v_exp_f32 v181,v181\nds_read_b128 a[76:79],v241 offset:35840\nM v[56:59],a[92:95],v[96:99],v[56:59]\nv_exp_f32 v182,v182\nv_cvt_pk_bf16_f32 v214,v180,v181\nM v[60:63],a[92:95],v[100:103],v[60:63]\nv_exp_f32 v183,v183\nM a[192:195],a[208:211],v[96:99],a[192:195]\n" \
"v_cvt_pk_bf16_f32 v215,v182,v183\nds_read_b128 a[80:83],v241 offset:36864\nds_read_b128 a[84:87],v241 offset:37888\nM a[196:199],a[208:211],v[100:103],a[196:199]\nds_read_b128 a[88:91],v241 offset:38912\ns_waitcnt vmcnt(0)\nv_cmp_eq_u32 vcc,0,v248\nM v[64:67],a[244:247],a[244:247],0\n" \
"ds_read_b128 a[92:95],v241 offset:39936\ns_cbranch_vccnz .Lps4\n.Lpb4:\ns_add_u32 m0,s64,65536\nM v[68:71],a[244:247],a[244:247],0\nds_read_b128 a[108:111],v241 offset:44032\nbuffer_load_dwordx4 v240,s[48:51],s36 offen offset:0 sc1 lds\nM v[72:75],a[244:247],a[244:247],0\n" \
"ds_read_b128 a[112:115],v241 offset:45056\nM v[76:79],a[244:247],a[244:247],0\nds_read_b128 a[116:119],v241 offset:46080\nM v[80:83],a[244:247],a[244:247],0\nds_read_b128 a[120:123],v241 offset:47104\nM v[84:87],a[244:247],a[244:247],0\nds_read_b128 a[124:127],v241 offset:48128\nbuffer_load_dwordx4 v240,s[48:51],s36 offen offset:1024 sc1 lds\n" \
"M v[88:91],a[244:247],a[244:247],0\nM v[92:95],a[244:247],a[244:247],0\nM v[96:99],a[244:247],a[244:247],0\nM v[100:103],a[244:247],a[244:247],0\nbuffer_load_dwordx4 v240,s[48:51],s36 offen offset:2048 sc1 lds\nM v[104:107],a[244:247],a[244:247],0\nM v[108:111],a[244:247],a[244:247],0\nM v[112:115],a[244:247],a[244:247],0\n" \
"M v[116:119],a[244:247],a[244:247],0\nbuffer_load_dwordx4 v240,s[48:51],s36 offen offset:3072 sc1 lds\ns_add_u32 m0,s64,16384\nM v[120:123],a[244:247],a[244:247],0\nM v[124:127],a[244:247],a[244:247],0\nM a[200:203],a[244:247],a[244:247],0\nM a[204:207],a[244:247],a[244:247],0\nbuffer_load_dwordx4 v240,s[44:47],s65 offen offset:0 sc1 lds\n" \
"M v[136:139],a[0:3],a[160:163],0\nM v[136:139],a[4:7],a[164:167],v[136:139]\nM v[136:139],a[8:11],a[168:171],v[136:139]\nM v[136:139],a[12:15],a[172:175],v[136:139]\nbuffer_load_dwordx4 v240,s[44:47],s65 offen offset:1024 sc1 lds\nM v[140:143],a[0:3],a[176:179],0\nM v[140:143],a[4:7],a[180:183],v[140:143]\nM v[140:143],a[8:11],a[184:187],v[140:143]\n" \
"M v[140:143],a[12:15],a[188:191],v[140:143]\nbuffer_load_dwordx4 v240,s[44:47],s65 offen offset:2048 sc1 lds\nM v[152:155],a[16:19],a[160:163],0\nM v[152:155],a[20:23],a[164:167],v[152:155]\nv_max3_f32 v233,v136,v137,v138\nM v[152:155],a[24:27],a[168:171],v[152:155]\nM v[152:155],a[28:31],a[172:175],v[152:155]\nbuffer_load_dwordx4 v240,s[44:47],s65 offen offset:3072 sc1 lds\n" \
"s_add_u32 m0,s64,81920\nM v[156:159],a[16:19],a[176:179],0\nv_max3_f32 v237,v140,v141,v142\nM v[156:159],a[20:23],a[180:183],v[156:159]\nM v[156:159],a[24:27],a[184:187],v[156:159]\nM v[156:159],a[28:31],a[188:191],v[156:159]\nbuffer_load_dwordx4 v240,s[48:51],s65 offen offset:0 sc1 lds\nv_max3_f32 v233,v233,v139,v152\n" \
"M v[128:131],a[0:3],a[128:131],v[224:227]\nv_max3_f32 v233,v233,v153,v154\nv_max_f32 v233,v233,v155\nv_cndmask_b32 v233,v232,v233,s[90:91]\nM v[128:131],a[4:7],a[132:135],v[128:131]\nM v[128:131],a[8:11],a[136:139],v[128:131]\nv_max3_f32 v237,v237,v143,v156\nv_max3_f32 v237,v237,v157,v158\n" \
"v_max_f32 v237,v237,v159\nM v[128:131],a[12:15],a[140:143],v[128:131]\nbuffer_load_dwordx4 v240,s[48:51],s65 offen offset:1024 sc1 lds\nM v[132:135],a[0:3],a[144:147],v[228:231]\nv_cndmask_b32 v237,v236,v237,s[90:91]\nds_read_b128 a[0:3],v240 offset:0\nM v[132:135],a[4:7],a[148:151],v[132:135]\nv_permlane16_swap_b32 v233,v237\n" \
"v_max_f32 v233,v233,v237\nv_mov_b32 v237,v233\nM v[132:135],a[8:11],a[152:155],v[132:135]\nds_read_b128 a[4:7],v240 offset:1024\nv_permlane32_swap_b32 v233,v237\nM v[132:135],a[12:15],a[156:159],v[132:135]\nv_max_f32 v233,v233,v237\nv_mov_b32 v237,v233\n" \
"ds_read_b128 a[8:11],v240 offset:2048\nM v[144:147],a[16:19],a[128:131],v[224:227]\nv_permlane16_swap_b32 v233,v237\nv_sub_f32 v232,0,v233\nv_sub_f32 v234,0,v233\nM v[144:147],a[20:23],a[132:135],v[144:147]\nv_sub_f32 v235,0,v233\nv_sub_f32 v233,0,v233\n" \
"M v[144:147],a[24:27],a[136:139],v[144:147]\nv_sub_f32 v236,0,v237\nv_sub_f32 v238,0,v237\nv_sub_f32 v239,0,v237\nM v[144:147],a[28:31],a[140:143],v[144:147]\nv_sub_f32 v237,0,v237\nds_read_b128 a[12:15],v240 offset:3072\nM v[148:151],a[16:19],a[144:147],v[228:231]\n" \
"ds_read_b128 a[16:19],v240 offset:4096\nbuffer_load_dwordx4 v240,s[48:51],s65 offen offset:2048 sc1 lds\nM v[148:151],a[20:23],a[148:151],v[148:151]\nds_read_b128 a[20:23],v240 offset:5120\nv_add_f32 v136,v136,v232\nv_add_f32 v137,v137,v232\nM v[148:151],a[24:27],a[152:155],v[148:151]\nds_read_b128 a[24:27],v240 offset:6144\n" \
"v_add_f32 v138,v138,v232\nM v[148:151],a[28:31],a[156:159],v[148:151]\nds_read_b128 a[28:31],v240 offset:7168\nv_add_f32 v139,v139,v232\nv_add_f32 v140,v140,v236\nM v[160:163],a[32:35],a[128:131],v[224:227]\nbuffer_load_dwordx4 v240,s[48:51],s65 offen offset:3072 sc1 lds\nM v[160:163],a[36:39],a[132:135],v[160:163]\n" \
"s_add_u32 m0,s64,32768\nv_add_f32 v141,v141,v236\nv_add_f32 v142,v142,v236\nM v[160:163],a[40:43],a[136:139],v[160:163]\nv_add_f32 v143,v143,v236\nv_add_f32 v152,v152,v232\nv_add_f32 v153,v153,v232\nM v[160:163],a[44:47],a[140:143],v[160:163]\n" \
"v_add_f32 v154,v154,v232\nv_add_f32 v155,v155,v232\nM v[164:167],a[32:35],a[144:147],v[228:231]\nv_add_f32 v156,v156,v236\nv_add_f32 v157,v157,v236\nv_add_f32 v158,v158,v236\nM v[164:167],a[36:39],a[148:151],v[164:167]\nbuffer_load_dwordx4 v240,s[44:47],s66 offen offset:0 sc1 lds\n" \
"M v[164:167],a[40:43],a[152:155],v[164:167]\nv_add_f32 v159,v159,v236\nv_exp_f32 v136,v136\nM v[164:167],a[44:47],a[156:159],v[164:167]\nv_exp_f32 v137,v137\nM v[176:179],a[48:51],a[128:131],v[224:227]\nv_exp_f32 v138,v138\nM v[176:179],a[52:55],a[132:135],v[176:179]\n" \
"buffer_load_dwordx4 v240,s[44:47],s66 offen offset:1024 sc1 lds\ns_waitcnt lgkmcnt(0)\nM v[176:179],a[56:59],a[136:139],v[176:179]\nv_exp_f32 v139,v139\nM v[176:179],a[60:63],a[140:143],v[176:179]\nv_exp_f32 v140,v140\nv_cvt_pk_bf16_f32 v200,v136,v137\nM v[180:183],a[48:51],a[144:147],v[228:231]\n" \
"v_exp_f32 v141,v141\nv_cvt_pk_bf16_f32 v201,v138,v139\nM v[180:183],a[52:55],a[148:151],v[180:183]\nbuffer_load_dwordx4 v240,s[44:47],s66 offen offset:2048 sc1 lds\nM v[180:183],a[56:59],a[152:155],v[180:183]\nv_exp_f32 v142,v142\nv_cvt_pk_bf16_f32 v204,v140,v141\nM v[180:183],a[60:63],a[156:159],v[180:183]\n" \
"v_exp_f32 v143,v143\nM v[168:171],a[32:35],a[160:163],v[232:235]\ns_barrier\nv_exp_f32 v152,v152\nM v[168:171],a[36:39],a[164:167],v[168:171]\nbuffer_load_dwordx4 v240,s[44:47],s66 offen offset:3072 sc1 lds\ns_add_u32 m0,s64,98304\nM v[168:171],a[40:43],a[168:171],v[168:171]\n" \
"v_exp_f32 v153,v153\nM v[168:171],a[44:47],a[172:175],v[168:171]\nv_exp_f32 v154,v154\nv_cvt_pk_bf16_f32 v202,v152,v153\nM v[172:175],a[32:35],a[176:179],v[236:239]\nds_read_b128 a[32:35],v240 offset:8192\nv_exp_f32 v155,v155\nM v[172:175],a[36:39],a[180:183],v[172:175]\n" \
"ds_read_b128 a[36:39],v240 offset:9216\nv_cvt_pk_bf16_f32 v203,v154,v155\nM v[172:175],a[40:43],a[184:187],v[172:175]\nds_read_b128 a[40:43],v240 offset:10240\nv_exp_f32 v156,v156\nM v[172:175],a[44:47],a[188:191],v[172:175]\nds_read_b128 a[44:47],v240 offset:11264\nv_cndmask_b32 v168,v168,v252,s[82:83]\n" \
"M v[188:191],a[48:51],a[176:179],v[236:239]\nds_read_b128 a[48:51],v240 offset:12288\nv_exp_f32 v157,v157\nM v[188:191],a[52:55],a[180:183],v[188:191]\nds_read_b128 a[52:55],v240 offset:13312\nv_exp_f32 v158,v158\nM v[188:191],a[56:59],a[184:187],v[188:191]\nds_read_b128 a[56:59],v240 offset:14336\n" \
"v_cndmask_b32 v169,v169,v252,s[84:85]\nM v[188:191],a[60:63],a[188:191],v[188:191]\nds_read_b128 a[60:63],v240 offset:15360\nv_exp_f32 v159,v159\nM v[0:3],a[64:67],v[192:195],v[0:3]\nv_exp_f32 v128,v128\nM v[4:7],a[64:67],v[196:199],v[4:7]\nv_exp_f32 v129,v129\n" \
"v_cndmask_b32 v170,v170,v252,s[86:87]\nM v[8:11],a[68:71],v[192:195],v[8:11]\nv_exp_f32 v130,v130\nv_cndmask_b32 v171,v171,v252,s[88:89]\nM v[12:15],a[68:71],v[196:199],v[12:15]\nv_exp_f32 v131,v131\nM v[16:19],a[72:75],v[192:195],v[16:19]\nv_exp_f32 v132,v132\n" \
"v_cndmask_b32 v188,v188,v252,s[82:83]\nM v[20:23],a[72:75],v[196:199],v[20:23]\nv_exp_f32 v133,v133\nM v[24:27],a[76:79],v[192:195],v[24:27]\nv_exp_f32 v134,v134\nv_cndmask_b32 v189,v189,v252,s[84:85]\nM v[28:31],a[76:79],v[196:199],v[28:31]\nv_exp_f32 v135,v135\n" \
"v_cndmask_b32 v190,v190,v252,s[86:87]\nM v[32:35],a[80:83],v[192:195],v[32:35]\nv_exp_f32 v144,v144\nM v[36:39],a[80:83],v[196:199],v[36:39]\nv_exp_f32 v145,v145\nv_cndmask_b32 v191,v191,v252,s[88:89]\nM v[40:43],a[84:87],v[192:195],v[40:43]\nv_exp_f32 v146,v146\n" \
"M v[44:47],a[84:87],v[196:199],v[44:47]\nv_exp_f32 v147,v147\nv_cvt_pk_bf16_f32 v205,v142,v143\nM v[48:51],a[88:91],v[192:195],v[48:51]\nv_exp_f32 v148,v148\nv_cvt_pk_bf16_f32 v206,v156,v157\nM v[52:55],a[88:91],v[196:199],v[52:55]\nv_exp_f32 v149,v149\n" \
"M v[56:59],a[92:95],v[192:195],v[56:59]\nv_exp_f32 v150,v150\nv_cvt_pk_bf16_f32 v207,v158,v159\nM v[60:63],a[92:95],v[196:199],v[60:63]\nv_exp_f32 v151,v151\nM a[192:195],a[208:211],v[192:195],a[192:195]\nv_exp_f32 v160,v160\nv_cvt_pk_bf16_f32 v192,v128,v129\n" \
"M a[196:199],a[208:211],v[196:199],a[196:199]\nv_exp_f32 v161,v161\nv_cvt_pk_bf16_f32 v193,v130,v131\nM v[0:3],a[96:99],v[208:211],v[0:3]\nv_exp_f32 v162,v162\nM v[4:7],a[96:99],v[212:215],v[4:7]\nv_exp_f32 v163,v163\nv_cvt_pk_bf16_f32 v194,v144,v145\n" \
"M v[8:11],a[100:103],v[208:211],v[8:11]\nv_exp_f32 v164,v164\nv_cvt_pk_bf16_f32 v195,v146,v147\nM v[12:15],a[100:103],v[212:215],v[12:15]\nv_exp_f32 v165,v165\nM v[16:19],a[104:107],v[208:211],v[16:19]\nv_exp_f32 v166,v166\nv_cvt_pk_bf16_f32 v196,v132,v133\n" \
"M v[20:23],a[104:107],v[212:215],v[20:23]\nv_exp_f32 v167,v167\nM v[24:27],a[108:111],v[208:211],v[24:27]\nv_exp_f32 v176,v176\nv_cvt_pk_bf16_f32 v197,v134,v135\nM v[28:31],a[108:111],v[212:215],v[28:31]\nv_exp_f32 v177,v177\nv_cvt_pk_bf16_f32 v198,v148,v149\n" \
"M v[32:35],a[112:115],v[208:211],v[32:35]\nv_exp_f32 v178,v178\nM v[36:39],a[112:115],v[212:215],v[36:39]\nv_exp_f32 v179,v179\nv_cvt_pk_bf16_f32 v199,v150,v151\nM v[40:43],a[116:119],v[208:211],v[40:43]\nv_exp_f32 v180,v180\nM v[44:47],a[116:119],v[212:215],v[44:47]\n" \
"v_exp_f32 v181,v181\nM v[48:51],a[120:123],v[208:211],v[48:51]\nv_exp_f32 v182,v182\nM v[52:55],a[120:123],v[212:215],v[52:55]\nv_exp_f32 v183,v183\nM v[56:59],a[124:127],v[208:211],v[56:59]\nv_exp_f32 v168,v168\nM v[60:63],a[124:127],v[212:215],v[60:63]\n" \
"v_exp_f32 v169,v169\nM a[192:195],a[208:211],v[208:211],a[192:195]\nv_exp_f32 v170,v170\nv_exp_f32 v171,v171\nbuffer_load_dwordx4 v240,s[48:51],s66 offen offset:0 sc1 lds\nbuffer_load_dwordx4 v240,s[48:51],s66 offen offset:1024 sc1 lds\nbuffer_load_dwordx4 v240,s[48:51],s66 offen offset:2048 sc1 lds\nbuffer_load_dwordx4 v240,s[48:51],s66 offen offset:3072 sc1 lds\n" \
"M a[196:199],a[208:211],v[212:215],a[196:199]\ns_cmp_lt_u32 s62,2\ns_cbranch_scc1 .Lx01\ns_branch .Ldual\n.Lpro3:\ns_barrier\nM v[64:67],a[0:3],a[128:131],0\nds_read_b128 a[64:67],v241 offset:0\n" \
"ds_read_b128 a[68:71],v241 offset:1024\nM v[64:67],a[4:7],a[132:135],v[64:67]\nds_read_b128 a[72:75],v241 offset:2048\ns_mov_b32 m0,s64\nv_mov_b32 v168,0\nM v[64:67],a[8:11],a[136:139],v[64:67]\nds_read_b128 a[76:79],v241 offset:3072\nv_mov_b32 v169,0\n" \
"M v[64:67],a[12:15],a[140:143],v[64:67]\nds_read_b128 a[80:83],v241 offset:4096\nbuffer_load_dwordx4 v240,s[44:47],s36 offen offset:0 sc1 lds\nM v[68:71],a[0:3],a[144:147],0\nds_read_b128 a[0:3],v240 offset:16384\nv_mov_b32 v170,0\nv_mov_b32 v171,0\nM v[68:71],a[4:7],a[148:151],v[68:71]\n" \
"ds_read_b128 a[4:7],v240 offset:17408\nv_max3_f32 v225,v64,v65,v66\nM v[68:71],a[8:11],a[152:155],v[68:71]\nds_read_b128 a[8:11],v240 offset:18432\nv_mov_b32 v172,0xff800000\nv_mov_b32 v173,0xff800000\nM v[68:71],a[12:15],a[156:159],v[68:71]\nds_read_b128 a[12:15],v240 offset:19456\n" \
"v_mov_b32 v174,0xff800000\nM v[72:75],a[16:19],a[128:131],0\nds_read_b128 a[84:87],v241 offset:5120\nbuffer_load_dwordx4 v240,s[44:47],s36 offen offset:1024 sc1 lds\nM v[72:75],a[20:23],a[132:135],v[72:75]\nds_read_b128 a[88:91],v241 offset:6144\nv_mov_b32 v175,0xff800000\nv_max3_f32 v229,v68,v69,v70\n" \
"M v[72:75],a[24:27],a[136:139],v[72:75]\nds_read_b128 a[92:95],v241 offset:7168\nv_mov_b32 v184,0xff800000\nM v[72:75],a[28:31],a[140:143],v[72:75]\nds_read_b128 a[96:99],v241 offset:8192\nv_mov_b32 v185,0xff800000\nv_mov_b32 v186,0xff800000\nM v[76:79],a[16:19],a[144:147],0\n" \
"ds_read_b128 a[16:19],v240 offset:20480\nbuffer_load_dwordx4 v240,s[44:47],s36 offen offset:2048 sc1 lds\nM v[76:79],a[20:23],a[148:151],v[76:79]\nds_read_b128 a[20:23],v240 offset:21504\nv_max3_f32 v225,v225,v67,v72\nM v[76:79],a[24:27],a[152:155],v[76:79]\nv_max3_f32 v225,v225,v73,v74\nv_max_f32 v225,v225,v75\n" \
"v_cndmask_b32 v225,v224,v225,s[90:91]\nM v[76:79],a[28:31],a[156:159],v[76:79]\nds_read_b128 a[24:27],v240 offset:22528\nds_read_b128 a[28:31],v240 offset:23552\nM v[80:83],a[32:35],a[128:131],0\nds_read_b128 a[100:103],v241 offset:9216\nbuffer_load_dwordx4 v240,s[44:47],s36 offen offset:3072 sc1 lds\nM v[80:83],a[36:39],a[132:135],v[80:83]\n" \
"ds_read_b128 a[104:107],v241 offset:10240\nv_mov_b32 v187,0xff800000\nv_max3_f32 v229,v229,v71,v76\nM v[80:83],a[40:43],a[136:139],v[80:83]\nv_max3_f32 v229,v229,v77,v78\nv_max_f32 v229,v229,v79\nM v[80:83],a[44:47],a[140:143],v[80:83]\nv_cndmask_b32 v229,v228,v229,s[90:91]\n" \
"ds_read_b128 a[108:111],v241 offset:11264\nds_read_b128 a[112:115],v241 offset:12288\nM v[84:87],a[32:35],a[144:147],0\nv_permlane16_swap_b32 v225,v229\nv_max_f32 v225,v225,v229\nM v[84:87],a[36:39],a[148:151],v[84:87]\nv_mov_b32 v229,v225\nds_read_b128 a[32:35],v240 offset:24576\n" \
"ds_read_b128 a[36:39],v240 offset:25600\nM v[84:87],a[40:43],a[152:155],v[84:87]\nv_permlane32_swap_b32 v225,v229\nv_max_f32 v225,v225,v229\nv_mov_b32 v229,v225\nM v[84:87],a[44:47],a[156:159],v[84:87]\nds_read_b128 a[40:43],v240 offset:26624\nv_permlane16_swap_b32 v225,v229\n" \
"M v[88:91],a[48:51],a[128:131],0\nv_sub_f32 v224,0,v225\nv_sub_f32 v226,0,v225\nv_sub_f32 v227,0,v225\nM v[88:91],a[52:55],a[132:135],v[88:91]\nv_sub_f32 v225,0,v225\nv_sub_f32 v228,0,v229\nv_sub_f32 v230,0,v229\n" \
"M v[88:91],a[56:59],a[136:139],v[88:91]\nv_sub_f32 v231,0,v229\nv_sub_f32 v229,0,v229\nM v[88:91],a[60:63],a[140:143],v[88:91]\nds_read_b128 a[44:47],v240 offset:27648\nv_add_f32 v64,v64,v224\nv_add_f32 v65,v65,v224\nM v[92:95],a[48:51],a[144:147],0\n" \
"ds_read_b128 a[48:51],v240 offset:28672\nv_add_f32 v66,v66,v224\nM v[92:95],a[52:55],a[148:151],v[92:95]\nds_read_b128 a[52:55],v240 offset:29696\nv_add_f32 v67,v67,v224\nv_add_f32 v68,v68,v228\nM v[92:95],a[56:59],a[152:155],v[92:95]\nds_read_b128 a[56:59],v240 offset:30720\n" \
"v_exp_f32 v64,v64\nM v[92:95],a[60:63],a[156:159],v[92:95]\nds_read_b128 a[60:63],v240 offset:31744\ns_waitcnt lgkmcnt(6)\nM v[96:99],a[0:3],a[128:131],v[224:227]\nv_exp_f32 v65,v65\nv_add_f32 v69,v69,v228\nM v[96:99],a[4:7],a[132:135],v[96:99]\n" \
"v_exp_f32 v66,v66\nM v[96:99],a[8:11],a[136:139],v[96:99]\nv_exp_f32 v67,v67\nv_add_f32 v72,v72,v224\nM v[96:99],a[12:15],a[140:143],v[96:99]\nv_add_f32 v73,v73,v224\nv_add_f32 v74,v74,v224\nv_add_f32 v75,v75,v224\n" \
"M v[100:103],a[0:3],a[144:147],v[228:231]\nv_add_f32 v70,v70,v228\nv_add_f32 v71,v71,v228\nM v[100:103],a[4:7],a[148:151],v[100:103]\nv_exp_f32 v68,v68\nv_cvt_pk_bf16_f32 v64,v64,v65\nM v[100:103],a[8:11],a[152:155],v[100:103]\nv_exp_f32 v69,v69\n" \
"v_add_f32 v76,v76,v228\nM v[100:103],a[12:15],a[156:159],v[100:103]\nv_exp_f32 v72,v72\nM v[104:107],a[16:19],a[128:131],v[224:227]\nv_exp_f32 v73,v73\nv_add_f32 v77,v77,v228\nM v[104:107],a[20:23],a[132:135],v[104:107]\nv_exp_f32 v74,v74\n" \
"M v[104:107],a[24:27],a[136:139],v[104:107]\nv_exp_f32 v75,v75\nv_add_f32 v78,v78,v228\nM v[104:107],a[28:31],a[140:143],v[104:107]\nv_exp_f32 v70,v70\nv_add_f32 v79,v79,v228\nM v[108:111],a[16:19],a[144:147],v[228:231]\nv_exp_f32 v71,v71\n" \
"M v[108:111],a[20:23],a[148:151],v[108:111]\nv_cvt_pk_bf16_f32 v65,v66,v67\nv_exp_f32 v76,v76\nM v[108:111],a[24:27],a[152:155],v[108:111]\nv_exp_f32 v77,v77\nv_cvt_pk_bf16_f32 v66,v72,v73\nM v[108:111],a[28:31],a[156:159],v[108:111]\nv_exp_f32 v78,v78\n" \
"M v[112:115],a[32:35],a[128:131],v[224:227]\nv_exp_f32 v79,v79\nv_cvt_pk_bf16_f32 v67,v74,v75\nM v[112:115],a[36:39],a[132:135],v[112:115]\nv_cvt_pk_bf16_f32 v68,v68,v69\ns_waitcnt lgkmcnt(0)\nM v[112:115],a[40:43],a[136:139],v[112:115]\nv_cvt_pk_bf16_f32 v69,v70,v71\n" \
"v_cvt_pk_bf16_f32 v70,v76,v77\nv_cvt_pk_bf16_f32 v71,v78,v79\nM v[112:115],a[44:47],a[140:143],v[112:115]\nv_add_f32 v80,v80,v224\nv_add_f32 v81,v81,v224\nv_add_f32 v82,v82,v224\nM v[116:119],a[32:35],a[144:147],v[228:231]\nv_add_f32 v83,v83,v224\n" \
"v_add_f32 v84,v84,v228\nM v[116:119],a[36:39],a[148:151],v[116:119]\nv_exp_f32 v80,v80\nv_add_f32 v85,v85,v228\nM v[116:119],a[40:43],a[152:155],v[116:119]\nv_exp_f32 v81,v81\nM v[116:119],a[44:47],a[156:159],v[116:119]\nv_exp_f32 v82,v82\n" \
"v_add_f32 v88,v88,v224\nM v[124:127],a[48:51],a[144:147],v[228:231]\nv_exp_f32 v83,v83\nv_add_f32 v89,v89,v224\nM v[124:127],a[52:55],a[148:151],v[124:127]\nv_add_f32 v90,v90,v224\nv_add_f32 v91,v91,v224\nM v[124:127],a[56:59],a[152:155],v[124:127]\n" \
"v_add_f32 v86,v86,v228\nv_add_f32 v87,v87,v228\nv_cvt_pk_bf16_f32 v80,v80,v81\nM v[124:127],a[60:63],a[156:159],v[124:127]\nv_exp_f32 v84,v84\nv_add_f32 v92,v92,v228\nM v[0:3],a[64:67],v[64:67],0\nv_exp_f32 v85,v85\n" \
"M v[4:7],a[64:67],v[68:71],0\nv_exp_f32 v88,v88\nv_add_f32 v93,v93,v228\nM v[8:11],a[68:71],v[64:67],0\nv_exp_f32 v89,v89\nM v[12:15],a[68:71],v[68:71],0\nv_exp_f32 v90,v90\nv_add_f32 v94,v94,v228\n" \
"M v[16:19],a[72:75],v[64:67],0\nv_exp_f32 v91,v91\nv_add_f32 v95,v95,v228\nM v[20:23],a[72:75],v[68:71],0\nv_exp_f32 v86,v86\nM v[24:27],a[76:79],v[64:67],0\nv_exp_f32 v87,v87\nv_cvt_pk_bf16_f32 v81,v82,v83\n" \
"M v[28:31],a[76:79],v[68:71],0\nv_exp_f32 v92,v92\nM v[32:35],a[80:83],v[64:67],0\nv_exp_f32 v93,v93\nv_cvt_pk_bf16_f32 v82,v88,v89\nM v[36:39],a[80:83],v[68:71],0\nv_exp_f32 v94,v94\nv_cvt_pk_bf16_f32 v83,v90,v91\n" \
"M v[40:43],a[84:87],v[64:67],0\nv_exp_f32 v95,v95\nM v[44:47],a[84:87],v[68:71],0\nv_cvt_pk_bf16_f32 v84,v84,v85\nv_cvt_pk_bf16_f32 v85,v86,v87\nv_cvt_pk_bf16_f32 v86,v92,v93\nM v[48:51],a[88:91],v[64:67],0\nv_cvt_pk_bf16_f32 v87,v94,v95\n" \
"ds_read_b128 a[116:119],v241 offset:13312\nds_read_b128 a[120:123],v241 offset:14336\nM v[52:55],a[88:91],v[68:71],0\nds_read_b128 a[124:127],v241 offset:15360\nv_cndmask_b32 v112,v112,v252,s[82:83]\nM v[56:59],a[92:95],v[64:67],0\nbuffer_load_dword v248,v251,s[52:55],s80 offen sc1\nds_read_b128 a[0:3],v240 offset:32768\n" \
"M v[60:63],a[92:95],v[68:71],0\nds_read_b128 a[4:7],v240 offset:33792\nv_cndmask_b32 v113,v113,v252,s[84:85]\nM a[192:195],a[208:211],v[64:67],0\nds_read_b128 a[8:11],v240 offset:34816\nv_exp_f32 v96,v96\nM a[196:199],a[208:211],v[68:71],0\nds_read_b128 a[12:15],v240 offset:35840\n" \
"v_exp_f32 v97,v97\nM v[0:3],a[96:99],v[80:83],v[0:3]\nds_read_b128 a[16:19],v240 offset:36864\nv_cvt_pk_bf16_f32 v96,v96,v97\nM v[4:7],a[96:99],v[84:87],v[4:7]\nds_read_b128 a[20:23],v240 offset:37888\nv_exp_f32 v98,v98\nM v[8:11],a[100:103],v[80:83],v[8:11]\n" \
"ds_read_b128 a[24:27],v240 offset:38912\nv_exp_f32 v99,v99\nM v[12:15],a[100:103],v[84:87],v[12:15]\nds_read_b128 a[28:31],v240 offset:39936\nv_cvt_pk_bf16_f32 v97,v98,v99\nM v[16:19],a[104:107],v[80:83],v[16:19]\nds_read_b128 a[32:35],v240 offset:40960\nv_exp_f32 v100,v100\n" \
"M v[20:23],a[104:107],v[84:87],v[20:23]\nds_read_b128 a[36:39],v240 offset:41984\nv_cndmask_b32 v114,v114,v252,s[86:87]\nM v[24:27],a[108:111],v[80:83],v[24:27]\nds_read_b128 a[40:43],v240 offset:43008\nv_exp_f32 v101,v101\nM v[28:31],a[108:111],v[84:87],v[28:31]\nds_read_b128 a[44:47],v240 offset:44032\n" \
"v_exp_f32 v104,v104\nM v[32:35],a[112:115],v[80:83],v[32:35]\nds_read_b128 a[48:51],v240 offset:45056\nv_cvt_pk_bf16_f32 v100,v100,v101\nM v[36:39],a[112:115],v[84:87],v[36:39]\nds_read_b128 a[52:55],v240 offset:46080\nv_cndmask_b32 v115,v115,v252,s[88:89]\ns_waitcnt lgkmcnt(9)\n" \
"M v[40:43],a[116:119],v[80:83],v[40:43]\nds_read_b128 a[56:59],v240 offset:47104\nv_cndmask_b32 v124,v124,v252,s[82:83]\nM v[44:47],a[116:119],v[84:87],v[44:47]\nds_read_b128 a[60:63],v240 offset:48128\nv_exp_f32 v105,v105\nM v[48:51],a[120:123],v[80:83],v[48:51]\nds_read_b128 a[64:67],v241 offset:16384\n" \
"v_exp_f32 v106,v106\nM v[52:55],a[120:123],v[84:87],v[52:55]\nds_read_b128 a[68:71],v241 offset:17408\nv_cvt_pk_bf16_f32 v98,v104,v105\nM v[56:59],a[124:127],v[80:83],v[56:59]\nds_read_b128 a[72:75],v241 offset:18432\nv_exp_f32 v107,v107\nM v[60:63],a[124:127],v[84:87],v[60:63]\n" \
"ds_read_b128 a[76:79],v241 offset:19456\nv_exp_f32 v102,v102\nM a[192:195],a[208:211],v[80:83],a[192:195]\nds_read_b128 a[80:83],v241 offset:20480\nv_cvt_pk_bf16_f32 v99,v106,v107\nM a[196:199],a[208:211],v[84:87],a[196:199]\nds_read_b128 a[84:87],v241 offset:21504\nv_exp_f32 v103,v103\n" \
"M v[128:131],a[0:3],a[128:131],v[224:227]\nds_read_b128 a[88:91],v241 offset:22528\nv_cvt_pk_bf16_f32 v101,v102,v103\nM v[128:131],a[4:7],a[132:135],v[128:131]\nds_read_b128 a[92:95],v241 offset:23552\nv_exp_f32 v108,v108\nM v[128:131],a[8:11],a[136:139],v[128:131]\nds_read_b128 a[96:99],v241 offset:24576\n" \
"v_exp_f32 v109,v109\nM v[128:131],a[12:15],a[140:143],v[128:131]\nds_read_b128 a[100:103],v241 offset:25600\nv_cvt_pk_bf16_f32 v102,v108,v109\nM v[132:135],a[0:3],a[144:147],v[228:231]\nds_read_b128 a[104:107],v241 offset:26624\nv_exp_f32 v110,v110\nM v[132:135],a[4:7],a[148:151],v[132:135]\n" \
"ds_read_b128 a[108:111],v241 offset:27648\nv_cndmask_b32 v125,v125,v252,s[84:85]\nM v[132:135],a[8:11],a[152:155],v[132:135]\nds_read_b128 a[112:115],v241 offset:28672\nv_exp_f32 v111,v111\nM v[132:135],a[12:15],a[156:159],v[132:135]\nds_read_b128 a[116:119],v241 offset:29696\nv_cvt_pk_bf16_f32 v103,v110,v111\n" \
"v_cndmask_b32 v126,v126,v252,s[86:87]\nM v[144:147],a[16:19],a[128:131],v[224:227]\nds_read_b128 a[120:123],v241 offset:30720\ns_waitcnt lgkmcnt(9)\nM v[144:147],a[20:23],a[132:135],v[144:147]\nds_read_b128 a[124:127],v241 offset:31744\nv_exp_f32 v112,v112\nM v[144:147],a[24:27],a[136:139],v[144:147]\n" \
"v_exp_f32 v113,v113\nv_cndmask_b32 v127,v127,v252,s[88:89]\nM v[144:147],a[28:31],a[140:143],v[144:147]\nv_exp_f32 v114,v114\nM v[148:151],a[16:19],a[144:147],v[228:231]\nv_exp_f32 v115,v115\nv_cvt_pk_bf16_f32 v112,v112,v113\nM v[148:151],a[20:23],a[148:151],v[148:151]\n" \
"v_exp_f32 v116,v116\nM v[148:151],a[24:27],a[152:155],v[148:151]\nv_exp_f32 v117,v117\nv_cvt_pk_bf16_f32 v113,v114,v115\nM v[148:151],a[28:31],a[156:159],v[148:151]\nv_exp_f32 v118,v118\nv_mov_b32 v114,0\nM v[160:163],a[32:35],a[128:131],v[224:227]\n" \
"v_exp_f32 v119,v119\nM v[160:163],a[36:39],a[132:135],v[160:163]\nv_exp_f32 v124,v124\nv_mov_b32 v115,0\nM v[160:163],a[40:43],a[136:139],v[160:163]\nv_exp_f32 v125,v125\nv_cvt_pk_bf16_f32 v116,v116,v117\nM v[160:163],a[44:47],a[140:143],v[160:163]\n" \
"v_exp_f32 v126,v126\nM v[164:167],a[32:35],a[144:147],v[228:231]\nv_exp_f32 v127,v127\nv_cvt_pk_bf16_f32 v117,v118,v119\nM v[164:167],a[36:39],a[148:151],v[164:167]\nv_cvt_pk_bf16_f32 v118,v124,v125\nv_cvt_pk_bf16_f32 v119,v126,v127\nM v[164:167],a[40:43],a[152:155],v[164:167]\n" \
"ds_read_b128 a[0:3],v240 offset:49152\nds_read_b128 a[4:7],v240 offset:50176\nv_mov_b32 v188,0xff800000\nM v[164:167],a[44:47],a[156:159],v[164:167]\nds_read_b128 a[8:11],v240 offset:51200\nv_exp_f32 v128,v128\nM v[176:179],a[48:51],a[128:131],v[224:227]\nds_read_b128 a[12:15],v240 offset:52224\n" \
"s_waitcnt vmcnt(0)\nv_cmp_eq_u32 vcc,0,v248\nM v[176:179],a[52:55],a[132:135],v[176:179]\nds_read_b128 a[16:19],v240 offset:53248\nv_exp_f32 v129,v129\nM v[176:179],a[56:59],a[136:139],v[176:179]\nds_read_b128 a[20:23],v240 offset:54272\nv_cvt_pk_bf16_f32 v192,v128,v129\n" \
"M v[176:179],a[60:63],a[140:143],v[176:179]\nds_read_b128 a[24:27],v240 offset:55296\nv_exp_f32 v130,v130\nM v[180:183],a[48:51],a[144:147],v[228:231]\ns_waitcnt lgkmcnt(7)\ns_barrier\nds_read_b128 a[28:31],v240 offset:56320\nM v[180:183],a[52:55],a[148:151],v[180:183]\n" \
"v_exp_f32 v131,v131\nM v[180:183],a[56:59],a[152:155],v[180:183]\nv_cvt_pk_bf16_f32 v193,v130,v131\nds_read_b128 a[32:35],v240 offset:57344\nds_read_b128 a[36:39],v240 offset:58368\nM v[180:183],a[60:63],a[156:159],v[180:183]\nds_read_b128 a[40:43],v240 offset:59392\nv_exp_f32 v132,v132\n" \
"M v[0:3],a[64:67],v[96:99],v[0:3]\nds_read_b128 a[44:47],v240 offset:60416\ns_cbranch_vccnz .Lps5\n.Lpb5:\nM v[4:7],a[64:67],v[100:103],v[4:7]\nds_read_b128 a[48:51],v240 offset:61440\nv_exp_f32 v133,v133\nM v[8:11],a[68:71],v[96:99],v[8:11]\n" \
"ds_read_b128 a[52:55],v240 offset:62464\nv_cvt_pk_bf16_f32 v196,v132,v133\nM v[12:15],a[68:71],v[100:103],v[12:15]\nds_read_b128 a[56:59],v240 offset:63488\nv_exp_f32 v134,v134\nM v[16:19],a[72:75],v[96:99],v[16:19]\nds_read_b128 a[60:63],v240 offset:64512\nv_exp_f32 v135,v135\n" \
"M v[20:23],a[72:75],v[100:103],v[20:23]\nds_read_b128 a[64:67],v241 offset:32768\nv_cvt_pk_bf16_f32 v197,v134,v135\nM v[24:27],a[76:79],v[96:99],v[24:27]\nds_read_b128 a[68:71],v241 offset:33792\nv_exp_f32 v144,v144\nM v[28:31],a[76:79],v[100:103],v[28:31]\nds_read_b128 a[72:75],v241 offset:34816\n" \
"s_add_u32 m0,s64,65536\nM v[32:35],a[80:83],v[96:99],v[32:35]\nds_read_b128 a[76:79],v241 offset:35840\nv_exp_f32 v145,v145\nM v[36:39],a[80:83],v[100:103],v[36:39]\nds_read_b128 a[80:83],v241 offset:36864\nv_exp_f32 v146,v146\nM v[40:43],a[84:87],v[96:99],v[40:43]\n" \
"v_exp_f32 v147,v147\nM v[44:47],a[84:87],v[100:103],v[44:47]\nds_read_b128 a[84:87],v241 offset:37888\nv_cvt_pk_bf16_f32 v194,v144,v145\nv_cvt_pk_bf16_f32 v195,v146,v147\nM v[48:51],a[88:91],v[96:99],v[48:51]\nv_exp_f32 v148,v148\nv_mov_b32 v189,0xff800000\n" \
"M v[52:55],a[88:91],v[100:103],v[52:55]\nds_read_b128 a[88:91],v241 offset:38912\nv_mov_b32 v190,0xff800000\nM v[56:59],a[92:95],v[96:99],v[56:59]\nv_exp_f32 v149,v149\nv_mov_b32 v191,0xff800000\nM v[60:63],a[92:95],v[100:103],v[60:63]\nds_read_b128 a[92:95],v241 offset:39936\n" \
"v_cvt_pk_bf16_f32 v198,v148,v149\nM a[192:195],a[208:211],v[96:99],a[192:195]\nv_exp_f32 v150,v150\nv_mov_b32 v202,0\nM a[196:199],a[208:211],v[100:103],a[196:199]\nv_exp_f32 v151,v151\nv_mov_b32 v203,0\nM v[0:3],a[96:99],v[112:115],v[0:3]\n" \
"v_exp_f32 v160,v160\nM v[4:7],a[96:99],v[116:119],v[4:7]\nds_read_b128 a[96:99],v241 offset:40960\nv_exp_f32 v161,v161\nM v[8:11],a[100:103],v[112:115],v[8:11]\nv_cvt_pk_bf16_f32 v199,v150,v151\nv_exp_f32 v162,v162\nM v[12:15],a[100:103],v[116:119],v[12:15]\n" \
"ds_read_b128 a[100:103],v241 offset:41984\nv_cvt_pk_bf16_f32 v208,v160,v161\nM v[16:19],a[104:107],v[112:115],v[16:19]\nv_exp_f32 v163,v163\nM v[20:23],a[104:107],v[116:119],v[20:23]\nds_read_b128 a[104:107],v241 offset:43008\nv_cvt_pk_bf16_f32 v209,v162,v163\nM v[24:27],a[108:111],v[112:115],v[24:27]\n" \
"v_exp_f32 v164,v164\nM v[28:31],a[108:111],v[116:119],v[28:31]\nds_read_b128 a[108:111],v241 offset:44032\nv_exp_f32 v165,v165\nM v[32:35],a[112:115],v[112:115],v[32:35]\nv_exp_f32 v166,v166\nM v[36:39],a[112:115],v[116:119],v[36:39]\nds_read_b128 a[112:115],v241 offset:45056\n" \
"v_exp_f32 v167,v167\nM v[40:43],a[116:119],v[112:115],v[40:43]\nv_cvt_pk_bf16_f32 v212,v164,v165\nv_cvt_pk_bf16_f32 v213,v166,v167\nM v[44:47],a[116:119],v[116:119],v[44:47]\nds_read_b128 a[116:119],v241 offset:46080\nv_exp_f32 v176,v176\nM v[48:51],a[120:123],v[112:115],v[48:51]\n" \
"v_exp_f32 v177,v177\nM v[52:55],a[120:123],v[116:119],v[52:55]\nds_read_b128 a[120:123],v241 offset:47104\nv_cvt_pk_bf16_f32 v210,v176,v177\nM v[56:59],a[124:127],v[112:115],v[56:59]\nv_exp_f32 v178,v178\nM v[60:63],a[124:127],v[116:119],v[60:63]\nds_read_b128 a[124:127],v241 offset:48128\n" \
"v_exp_f32 v179,v179\nM a[192:195],a[208:211],v[112:115],a[192:195]\nv_exp_f32 v180,v180\nM a[196:199],a[208:211],v[116:119],a[196:199]\nv_cvt_pk_bf16_f32 v211,v178,v179\nv_exp_f32 v181,v181\nM v[64:67],a[244:247],a[244:247],0\nv_exp_f32 v182,v182\n" \
"M v[68:71],a[244:247],a[244:247],0\nv_exp_f32 v183,v183\nv_cvt_pk_bf16_f32 v214,v180,v181\nM v[72:75],a[244:247],a[244:247],0\nv_cvt_pk_bf16_f32 v215,v182,v183\nbuffer_load_dwordx4 v240,s[48:51],s36 offen offset:0 sc1 lds\nM v[76:79],a[244:247],a[244:247],0\nM v[80:83],a[244:247],a[244:247],0\n" \
"M v[84:87],a[244:247],a[244:247],0\nM v[88:91],a[244:247],a[244:247],0\nbuffer_load_dwordx4 v240,s[48:51],s36 offen offset:1024 sc1 lds\nM v[92:95],a[244:247],a[244:247],0\nM v[96:99],a[244:247],a[244:247],0\nM v[100:103],a[244:247],a[244:247],0\nM v[104:107],a[244:247],a[244:247],0\nbuffer_load_dwordx4 v240,s[48:51],s36 offen offset:2048 sc1 lds\n" \
"M v[108:111],a[244:247],a[244:247],0\nM v[112:115],a[244:247],a[244:247],0\nM v[116:119],a[244:247],a[244:247],0\nM v[120:123],a[244:247],a[244:247],0\nbuffer_load_dwordx4 v240,s[48:51],s36 offen offset:3072 sc1 lds\ns_add_u32 m0,s64,16384\nM v[124:127],a[244:247],a[244:247],0\nM a[200:203],a[244:247],a[244:247],0\n" \
"M a[204:207],a[244:247],a[244:247],0\ns_waitcnt lgkmcnt(0)\nM v[136:139],a[0:3],a[160:163],0\nbuffer_load_dwordx4 v240,s[44:47],s65 offen offset:0 sc1 lds\nM v[136:139],a[4:7],a[164:167],v[136:139]\nM v[136:139],a[8:11],a[168:171],v[136:139]\nM v[136:139],a[12:15],a[172:175],v[136:139]\nM v[140:143],a[0:3],a[176:179],0\n" \
"buffer_load_dwordx4 v240,s[44:47],s65 offen offset:1024 sc1 lds\nM v[140:143],a[4:7],a[180:183],v[140:143]\nM v[140:143],a[8:11],a[184:187],v[140:143]\nM v[140:143],a[12:15],a[188:191],v[140:143]\nM v[156:159],a[16:19],a[176:179],0\nbuffer_load_dwordx4 v240,s[44:47],s65 offen offset:2048 sc1 lds\nM v[156:159],a[20:23],a[180:183],v[156:159]\nv_max3_f32 v233,v136,v137,v138\n" \
"v_max_f32 v233,v233,v139\nv_cndmask_b32 v233,v232,v233,s[90:91]\nM v[156:159],a[24:27],a[184:187],v[156:159]\nv_cndmask_b32 v136,v136,v252,s[82:83]\nv_max3_f32 v237,v140,v141,v142\nv_cndmask_b32 v137,v137,v252,s[84:85]\nM v[156:159],a[28:31],a[188:191],v[156:159]\nv_cndmask_b32 v138,v138,v252,s[86:87]\n" \
"v_cndmask_b32 v139,v139,v252,s[88:89]\nM v[128:131],a[0:3],a[128:131],v[224:227]\nbuffer_load_dwordx4 v240,s[44:47],s65 offen offset:3072 sc1 lds\ns_add_u32 m0,s64,81920\nM v[128:131],a[4:7],a[132:135],v[128:131]\nM v[128:131],a[8:11],a[136:139],v[128:131]\ns_barrier\nv_max3_f32 v237,v237,v143,v156\n" \
"M v[128:131],a[12:15],a[140:143],v[128:131]\nv_max3_f32 v237,v237,v157,v158\nv_max_f32 v237,v237,v159\nv_cndmask_b32 v237,v236,v237,s[90:91]\nM v[132:135],a[0:3],a[144:147],v[228:231]\nds_read_b128 a[0:3],v240 offset:0\nv_permlane16_swap_b32 v233,v237\nM v[132:135],a[4:7],a[148:151],v[132:135]\n" \
"ds_read_b128 a[4:7],v240 offset:1024\nv_max_f32 v233,v233,v237\nv_mov_b32 v237,v233\nM v[132:135],a[8:11],a[152:155],v[132:135]\nds_read_b128 a[8:11],v240 offset:2048\nv_permlane32_swap_b32 v233,v237\nv_max_f32 v233,v233,v237\nM v[132:135],a[12:15],a[156:159],v[132:135]\n" \
"ds_read_b128 a[12:15],v240 offset:3072\nv_mov_b32 v237,v233\nM v[144:147],a[16:19],a[128:131],v[224:227]\nbuffer_load_dwordx4 v240,s[48:51],s65 offen offset:0 sc1 lds\nv_permlane16_swap_b32 v233,v237\nM v[144:147],a[20:23],a[132:135],v[144:147]\nv_cndmask_b32 v156,v156,v252,s[82:83]\nv_cndmask_b32 v157,v157,v252,s[84:85]\n" \
"M v[144:147],a[24:27],a[136:139],v[144:147]\nv_cndmask_b32 v158,v158,v252,s[86:87]\nv_cndmask_b32 v159,v159,v252,s[88:89]\nv_sub_f32 v232,0,v233\nM v[144:147],a[28:31],a[140:143],v[144:147]\nv_sub_f32 v236,0,v237\nv_add_f32 v136,v136,v232\nv_add_f32 v137,v137,v232\n" \
"M v[148:151],a[16:19],a[144:147],v[228:231]\nds_read_b128 a[16:19],v240 offset:4096\nv_add_f32 v138,v138,v232\nM v[148:151],a[20:23],a[148:151],v[148:151]\nds_read_b128 a[20:23],v240 offset:5120\nbuffer_load_dwordx4 v240,s[48:51],s65 offen offset:1024 sc1 lds\nM v[148:151],a[24:27],a[152:155],v[148:151]\nds_read_b128 a[24:27],v240 offset:6144\n" \
"v_add_f32 v139,v139,v232\nv_add_f32 v140,v140,v236\nM v[148:151],a[28:31],a[156:159],v[148:151]\nds_read_b128 a[28:31],v240 offset:7168\nv_add_f32 v141,v141,v236\nM v[160:163],a[32:35],a[128:131],v[224:227]\nv_add_f32 v142,v142,v236\nv_add_f32 v143,v143,v236\n" \
"v_add_f32 v156,v156,v236\nM v[160:163],a[36:39],a[132:135],v[160:163]\nbuffer_load_dwordx4 v240,s[48:51],s65 offen offset:2048 sc1 lds\nM v[160:163],a[40:43],a[136:139],v[160:163]\nv_add_f32 v157,v157,v236\nv_add_f32 v158,v158,v236\nv_add_f32 v159,v159,v236\nM v[160:163],a[44:47],a[140:143],v[160:163]\n" \
"v_exp_f32 v136,v136\nv_sub_f32 v234,0,v233\nM v[164:167],a[32:35],a[144:147],v[228:231]\nds_read_b128 a[32:35],v240 offset:8192\nv_sub_f32 v235,0,v233\nM v[164:167],a[36:39],a[148:151],v[164:167]\nds_read_b128 a[36:39],v240 offset:9216\nbuffer_load_dwordx4 v240,s[48:51],s65 offen offset:3072 sc1 lds\n" \
"M v[164:167],a[40:43],a[152:155],v[164:167]\nds_read_b128 a[40:43],v240 offset:10240\ns_add_u32 m0,s64,32768\nv_sub_f32 v238,0,v237\nM v[164:167],a[44:47],a[156:159],v[164:167]\nds_read_b128 a[44:47],v240 offset:11264\nv_sub_f32 v239,0,v237\nM v[176:179],a[48:51],a[128:131],v[224:227]\n" \
"v_exp_f32 v137,v137\nv_sub_f32 v233,0,v233\nM v[176:179],a[52:55],a[132:135],v[176:179]\nbuffer_load_dwordx4 v240,s[44:47],s66 offen offset:0 sc1 lds\nM v[176:179],a[56:59],a[136:139],v[176:179]\nv_exp_f32 v138,v138\nv_sub_f32 v237,0,v237\nM v[176:179],a[60:63],a[140:143],v[176:179]\n" \
"v_exp_f32 v139,v139\nv_cvt_pk_bf16_f32 v200,v136,v137\nM v[180:183],a[48:51],a[144:147],v[228:231]\nds_read_b128 a[48:51],v240 offset:12288\nv_cvt_pk_bf16_f32 v201,v138,v139\nM v[180:183],a[52:55],a[148:151],v[180:183]\nds_read_b128 a[52:55],v240 offset:13312\nbuffer_load_dwordx4 v240,s[44:47],s66 offen offset:1024 sc1 lds\n" \
"M v[180:183],a[56:59],a[152:155],v[180:183]\nv_exp_f32 v140,v140\nM v[180:183],a[60:63],a[156:159],v[180:183]\nds_read_b128 a[56:59],v240 offset:14336\nv_exp_f32 v141,v141\nM v[0:3],a[64:67],v[192:195],v[0:3]\nds_read_b128 a[60:63],v240 offset:15360\nv_exp_f32 v142,v142\n" \
"M v[4:7],a[64:67],v[196:199],v[4:7]\nbuffer_load_dwordx4 v240,s[44:47],s66 offen offset:2048 sc1 lds\nM v[8:11],a[68:71],v[192:195],v[8:11]\nv_exp_f32 v143,v143\nv_cvt_pk_bf16_f32 v204,v140,v141\nM v[12:15],a[68:71],v[196:199],v[12:15]\nv_exp_f32 v156,v156\nv_cvt_pk_bf16_f32 v205,v142,v143\n" \
"M v[16:19],a[72:75],v[192:195],v[16:19]\nv_exp_f32 v157,v157\nM v[20:23],a[72:75],v[196:199],v[20:23]\nbuffer_load_dwordx4 v240,s[44:47],s66 offen offset:3072 sc1 lds\ns_add_u32 m0,s64,98304\nM v[24:27],a[76:79],v[192:195],v[24:27]\nv_exp_f32 v158,v158\nM v[28:31],a[76:79],v[196:199],v[28:31]\n" \
"v_exp_f32 v159,v159\nv_cvt_pk_bf16_f32 v206,v156,v157\nM v[32:35],a[80:83],v[192:195],v[32:35]\nv_exp_f32 v128,v128\nv_cvt_pk_bf16_f32 v207,v158,v159\nM v[36:39],a[80:83],v[196:199],v[36:39]\nv_exp_f32 v129,v129\nM v[40:43],a[84:87],v[192:195],v[40:43]\n" \
"v_exp_f32 v130,v130\nM v[44:47],a[84:87],v[196:199],v[44:47]\nv_exp_f32 v131,v131\nM v[48:51],a[88:91],v[192:195],v[48:51]\nv_exp_f32 v132,v132\nM v[52:55],a[88:91],v[196:199],v[52:55]\nv_exp_f32 v133,v133\nM v[56:59],a[92:95],v[192:195],v[56:59]\n" \
"v_exp_f32 v134,v134\nM v[60:63],a[92:95],v[196:199],v[60:63]\nv_exp_f32 v135,v135\nM a[192:195],a[208:211],v[192:195],a[192:195]\nv_exp_f32 v144,v144\nv_cvt_pk_bf16_f32 v192,v128,v129\nM a[196:199],a[208:211],v[196:199],a[196:199]\nv_exp_f32 v145,v145\n" \
"M v[0:3],a[96:99],v[208:211],v[0:3]\nv_exp_f32 v146,v146\nv_cvt_pk_bf16_f32 v193,v130,v131\nM v[4:7],a[96:99],v[212:215],v[4:7]\nv_exp_f32 v147,v147\nM v[8:11],a[100:103],v[208:211],v[8:11]\nv_exp_f32 v148,v148\nv_cvt_pk_bf16_f32 v194,v144,v145\n" \
"M v[12:15],a[100:103],v[212:215],v[12:15]\nv_exp_f32 v149,v149\nv_cvt_pk_bf16_f32 v195,v146,v147\nM v[16:19],a[104:107],v[208:211],v[16:19]\nv_exp_f32 v150,v150\nM v[20:23],a[104:107],v[212:215],v[20:23]\nv_exp_f32 v151,v151\nv_cvt_pk_bf16_f32 v196,v132,v133\n" \
"M v[24:27],a[108:111],v[208:211],v[24:27]\nv_exp_f32 v160,v160\nv_cvt_pk_bf16_f32 v197,v134,v135\nM v[28:31],a[108:111],v[212:215],v[28:31]\nv_exp_f32 v161,v161\nM v[32:35],a[112:115],v[208:211],v[32:35]\nv_exp_f32 v162,v162\nv_cvt_pk_bf16_f32 v198,v148,v149\n" \
"M v[36:39],a[112:115],v[212:215],v[36:39]\nv_exp_f32 v163,v163\nM v[40:43],a[116:119],v[208:211],v[40:43]\nv_exp_f32 v164,v164\nv_cvt_pk_bf16_f32 v199,v150,v151\nM v[44:47],a[116:119],v[212:215],v[44:47]\nv_exp_f32 v165,v165\nM v[48:51],a[120:123],v[208:211],v[48:51]\n" \
"v_exp_f32 v166,v166\nM v[52:55],a[120:123],v[212:215],v[52:55]\nv_exp_f32 v167,v167\nM v[56:59],a[124:127],v[208:211],v[56:59]\nv_exp_f32 v176,v176\nM v[60:63],a[124:127],v[212:215],v[60:63]\nv_exp_f32 v177,v177\nM a[192:195],a[208:211],v[208:211],a[192:195]\n" \
"v_exp_f32 v178,v178\nv_exp_f32 v179,v179\nv_exp_f32 v180,v180\nv_exp_f32 v181,v181\nv_exp_f32 v182,v182\nv_exp_f32 v183,v183\nbuffer_load_dwordx4 v240,s[48:51],s66 offen offset:0 sc1 lds\nbuffer_load_dwordx4 v240,s[48:51],s66 offen offset:1024 sc1 lds\n" \
"buffer_load_dwordx4 v240,s[48:51],s66 offen offset:2048 sc1 lds\nbuffer_load_dwordx4 v240,s[48:51],s66 offen offset:3072 sc1 lds\nM a[196:199],a[208:211],v[212:215],a[196:199]\ns_cmp_lt_u32 s62,2\ns_cbranch_scc1 .Lx01\n.Ldual:\ns_waitcnt lgkmcnt(4)\nM v[128:131],a[0:3],a[128:131],v[224:227]\n" \
"s_add_u32 m0,s94,s64\nv_cvt_pk_bf16_f32 v208,v160,v161\nM v[128:131],a[4:7],a[132:135],v[128:131]\nds_read_b128 a[64:67],v250 offset:0\nds_read_b128 a[68:71],v250 offset:1024\nM v[128:131],a[8:11],a[136:139],v[128:131]\nv_cvt_pk_bf16_f32 v209,v162,v163\nv_cvt_pk_bf16_f32 v212,v164,v165\n" \
"M v[128:131],a[12:15],a[140:143],v[128:131]\nv_cvt_pk_bf16_f32 v213,v166,v167\nv_cvt_pk_bf16_f32 v216,v168,v169\nM v[132:135],a[0:3],a[144:147],v[228:231]\nbuffer_load_dwordx4 v240,s[44:47],s95 offen offset:0 sc1 lds\nM v[132:135],a[4:7],a[148:151],v[132:135]\nv_cvt_pk_bf16_f32 v217,v170,v171\nv_cvt_pk_bf16_f32 v210,v176,v177\n" \
"M v[132:135],a[8:11],a[152:155],v[132:135]\nv_exp_f32 v172,v172\nM v[132:135],a[12:15],a[156:159],v[132:135]\nds_read_b128 a[72:75],v250 offset:2048\nds_read_b128 a[76:79],v250 offset:3072\nM v[136:139],a[0:3],a[160:163],v[232:235]\nv_exp_f32 v173,v173\nM v[136:139],a[4:7],a[164:167],v[136:139]\n" \
"v_exp_f32 v174,v174\nM v[136:139],a[8:11],a[168:171],v[136:139]\nv_exp_f32 v175,v175\nM v[136:139],a[12:15],a[172:175],v[136:139]\nv_cvt_pk_bf16_f32 v220,v172,v173\nv_cvt_pk_bf16_f32 v221,v174,v175\nM v[140:143],a[0:3],a[176:179],v[236:239]\nv_cvt_pk_bf16_f32 v211,v178,v179\n" \
"v_cvt_pk_bf16_f32 v214,v180,v181\nM v[140:143],a[4:7],a[180:183],v[140:143]\nds_read_b128 a[80:83],v250 offset:4096\nds_read_b128 a[84:87],v250 offset:5120\nM v[140:143],a[8:11],a[184:187],v[140:143]\nv_cvt_pk_bf16_f32 v215,v182,v183\ns_add_u32 s94,s94,0x4000\nM v[140:143],a[12:15],a[188:191],v[140:143]\n" \
"v_exp_f32 v184,v184\nM v[144:147],a[16:19],a[128:131],v[224:227]\nv_exp_f32 v185,v185\nM v[144:147],a[20:23],a[132:135],v[144:147]\nv_exp_f32 v186,v186\nM v[144:147],a[24:27],a[136:139],v[144:147]\nv_exp_f32 v187,v187\nM v[144:147],a[28:31],a[140:143],v[144:147]\n" \
"ds_read_b128 a[88:91],v250 offset:6144\nds_read_b128 a[92:95],v250 offset:7168\nM v[148:151],a[16:19],a[144:147],v[228:231]\nv_cvt_pk_bf16_f32 v218,v184,v185\nv_cvt_pk_bf16_f32 v219,v186,v187\nM v[148:151],a[20:23],a[148:151],v[148:151]\nv_exp_f32 v188,v188\nM v[148:151],a[24:27],a[152:155],v[148:151]\n" \
"buffer_load_dwordx4 v240,s[44:47],s95 offen offset:1024 sc1 lds\nM v[148:151],a[28:31],a[156:159],v[148:151]\nv_exp_f32 v189,v189\nM v[152:155],a[16:19],a[160:163],v[232:235]\nv_exp_f32 v190,v190\nM v[152:155],a[20:23],a[164:167],v[152:155]\nds_read_b128 a[96:99],v250 offset:8192\nds_read_b128 a[100:103],v250 offset:9216\n" \
"M v[152:155],a[24:27],a[168:171],v[152:155]\nv_exp_f32 v191,v191\nM v[152:155],a[28:31],a[172:175],v[152:155]\nv_cvt_pk_bf16_f32 v222,v188,v189\nv_cvt_pk_bf16_f32 v223,v190,v191\nM v[156:159],a[16:19],a[176:179],v[236:239]\nv_exp_f32 v128,v128\nM v[156:159],a[20:23],a[180:183],v[156:159]\n" \
"v_exp_f32 v129,v129\nM v[156:159],a[24:27],a[184:187],v[156:159]\nv_exp_f32 v130,v130\nM v[156:159],a[28:31],a[188:191],v[156:159]\nds_read_b128 a[104:107],v250 offset:10240\nds_read_b128 a[108:111],v250 offset:11264\nM v[160:163],a[32:35],a[128:131],v[224:227]\nv_exp_f32 v131,v131\n" \
"M v[160:163],a[36:39],a[132:135],v[160:163]\nv_exp_f32 v144,v144\nM v[160:163],a[40:43],a[136:139],v[160:163]\nv_exp_f32 v145,v145\nM v[160:163],a[44:47],a[140:143],v[160:163]\nv_exp_f32 v146,v146\nM v[164:167],a[32:35],a[144:147],v[228:231]\nv_exp_f32 v147,v147\n" \
"M v[164:167],a[36:39],a[148:151],v[164:167]\nds_read_b128 a[112:115],v250 offset:12288\nds_read_b128 a[116:119],v250 offset:13312\nM v[164:167],a[40:43],a[152:155],v[164:167]\nv_exp_f32 v132,v132\nM v[164:167],a[44:47],a[156:159],v[164:167]\nv_exp_f32 v133,v133\nM v[168:171],a[32:35],a[160:163],v[232:235]\n" \
"buffer_load_dwordx4 v240,s[44:47],s95 offen offset:2048 sc1 lds\nM v[168:171],a[36:39],a[164:167],v[168:171]\nv_exp_f32 v134,v134\nM v[168:171],a[40:43],a[168:171],v[168:171]\nv_exp_f32 v135,v135\nM v[168:171],a[44:47],a[172:175],v[168:171]\nds_read_b128 a[120:123],v250 offset:14336\nds_read_b128 a[124:127],v250 offset:15360\n" \
"M v[172:175],a[32:35],a[176:179],v[236:239]\nv_xor_b32 v250,0x18000,v250\ns_and_b32 s94,s94,0xffff\nM v[172:175],a[36:39],a[180:183],v[172:175]\nv_exp_f32 v148,v148\nM v[172:175],a[40:43],a[184:187],v[172:175]\nv_exp_f32 v149,v149\nM v[172:175],a[44:47],a[188:191],v[172:175]\n" \
"s_waitcnt lgkmcnt(4)\nM v[176:179],a[48:51],a[128:131],v[224:227]\nv_exp_f32 v150,v150\nM v[176:179],a[52:55],a[132:135],v[176:179]\nv_exp_f32 v151,v151\nM v[176:179],a[56:59],a[136:139],v[176:179]\nv_exp_f32 v136,v136\nM v[176:179],a[60:63],a[140:143],v[176:179]\n" \
"v_exp_f32 v137,v137\nM v[180:183],a[48:51],a[144:147],v[228:231]\nv_exp_f32 v138,v138\nM v[180:183],a[52:55],a[148:151],v[180:183]\nv_exp_f32 v139,v139\nM v[180:183],a[56:59],a[152:155],v[180:183]\nv_exp_f32 v152,v152\nM v[180:183],a[60:63],a[156:159],v[180:183]\n" \
"v_exp_f32 v153,v153\nM v[184:187],a[48:51],a[160:163],v[232:235]\nv_exp_f32 v154,v154\nM v[184:187],a[52:55],a[164:167],v[184:187]\nv_exp_f32 v155,v155\nM v[184:187],a[56:59],a[168:171],v[184:187]\nbuffer_load_dwordx4 v240,s[44:47],s95 offen offset:3072 sc1 lds\nM v[184:187],a[60:63],a[172:175],v[184:187]\n" \
"v_exp_f32 v140,v140\nM v[188:191],a[48:51],a[176:179],v[236:239]\nv_exp_f32 v141,v141\nM v[188:191],a[52:55],a[180:183],v[188:191]\nv_exp_f32 v142,v142\nM v[188:191],a[56:59],a[184:187],v[188:191]\nv_exp_f32 v143,v143\nM v[188:191],a[60:63],a[188:191],v[188:191]\n" \
"v_exp_f32 v156,v156\nM v[0:3],a[64:67],v[192:195],v[0:3]\ns_waitcnt vmcnt(16) lgkmcnt(0)\ns_barrier\nM v[4:7],a[64:67],v[196:199],v[4:7]\nds_read_b128 a[0:3],v250 offset:0\nds_read_b128 a[4:7],v250 offset:1024\nM v[64:67],a[64:67],v[200:203],v[64:67]\n" \
"v_exp_f32 v157,v157\nM v[68:71],a[64:67],v[204:207],v[68:71]\nv_exp_f32 v158,v158\nM v[8:11],a[68:71],v[192:195],v[8:11]\ns_add_u32 m0,m0,0x10000\nM v[12:15],a[68:71],v[196:199],v[12:15]\nbuffer_load_dwordx4 v240,s[48:51],s95 offen offset:0 sc1 lds\nM v[72:75],a[68:71],v[200:203],v[72:75]\n" \
"v_exp_f32 v159,v159\nM v[76:79],a[68:71],v[204:207],v[76:79]\nv_exp_f32 v160,v160\nM v[16:19],a[72:75],v[192:195],v[16:19]\nv_exp_f32 v161,v161\nM v[20:23],a[72:75],v[196:199],v[20:23]\nds_read_b128 a[8:11],v250 offset:2048\nds_read_b128 a[12:15],v250 offset:3072\n" \
"M v[80:83],a[72:75],v[200:203],v[80:83]\nv_exp_f32 v162,v162\nM v[84:87],a[72:75],v[204:207],v[84:87]\nv_exp_f32 v163,v163\nM v[24:27],a[76:79],v[192:195],v[24:27]\nv_exp_f32 v176,v176\nM v[28:31],a[76:79],v[196:199],v[28:31]\nv_exp_f32 v177,v177\n" \
"M v[88:91],a[76:79],v[200:203],v[88:91]\nv_exp_f32 v178,v178\nM v[92:95],a[76:79],v[204:207],v[92:95]\nv_exp_f32 v179,v179\nM v[32:35],a[80:83],v[192:195],v[32:35]\nv_exp_f32 v164,v164\nM v[36:39],a[80:83],v[196:199],v[36:39]\nds_read_b128 a[16:19],v250 offset:4096\n" \
"ds_read_b128 a[20:23],v250 offset:5120\nM v[96:99],a[80:83],v[200:203],v[96:99]\nv_exp_f32 v165,v165\nM v[100:103],a[80:83],v[204:207],v[100:103]\nv_exp_f32 v166,v166\nM v[40:43],a[84:87],v[192:195],v[40:43]\nv_exp_f32 v167,v167\nM v[44:47],a[84:87],v[196:199],v[44:47]\n" \
"buffer_load_dwordx4 v240,s[48:51],s95 offen offset:1024 sc1 lds\nM v[104:107],a[84:87],v[200:203],v[104:107]\nv_exp_f32 v180,v180\nM v[108:111],a[84:87],v[204:207],v[108:111]\nv_exp_f32 v181,v181\nM v[48:51],a[88:91],v[192:195],v[48:51]\nv_exp_f32 v182,v182\nM v[52:55],a[88:91],v[196:199],v[52:55]\n" \
"ds_read_b128 a[24:27],v250 offset:6144\nds_read_b128 a[28:31],v250 offset:7168\nM v[112:115],a[88:91],v[200:203],v[112:115]\nv_exp_f32 v183,v183\nM v[116:119],a[88:91],v[204:207],v[116:119]\nv_exp_f32 v168,v168\nM v[56:59],a[92:95],v[192:195],v[56:59]\nv_exp_f32 v169,v169\n" \
"M v[60:63],a[92:95],v[196:199],v[60:63]\nv_exp_f32 v170,v170\nM v[120:123],a[92:95],v[200:203],v[120:123]\nv_exp_f32 v171,v171\nM v[124:127],a[92:95],v[204:207],v[124:127]\nM a[192:195],a[208:211],v[192:195],a[192:195]\nv_cvt_pk_bf16_f32 v192,v128,v129\nv_cvt_pk_bf16_f32 v193,v130,v131\n" \
"M a[196:199],a[208:211],v[196:199],a[196:199]\nds_read_b128 a[32:35],v250 offset:8192\nds_read_b128 a[36:39],v250 offset:9216\nM a[200:203],a[208:211],v[200:203],a[200:203]\nv_cvt_pk_bf16_f32 v194,v144,v145\nv_cvt_pk_bf16_f32 v195,v146,v147\nM a[204:207],a[208:211],v[204:207],a[204:207]\nv_cvt_pk_bf16_f32 v196,v132,v133\n" \
"v_cvt_pk_bf16_f32 v197,v134,v135\nM v[0:3],a[96:99],v[208:211],v[0:3]\nv_cvt_pk_bf16_f32 v198,v148,v149\nv_cvt_pk_bf16_f32 v199,v150,v151\nM v[4:7],a[96:99],v[212:215],v[4:7]\nbuffer_load_dwordx4 v240,s[48:51],s95 offen offset:2048 sc1 lds\nM v[64:67],a[96:99],v[216:219],v[64:67]\nv_cvt_pk_bf16_f32 v200,v136,v137\n" \
"v_cvt_pk_bf16_f32 v201,v138,v139\nM v[68:71],a[96:99],v[220:223],v[68:71]\nv_cvt_pk_bf16_f32 v202,v152,v153\nv_cvt_pk_bf16_f32 v203,v154,v155\nM v[8:11],a[100:103],v[208:211],v[8:11]\nv_cvt_pk_bf16_f32 v204,v140,v141\nv_cvt_pk_bf16_f32 v205,v142,v143\nM v[12:15],a[100:103],v[212:215],v[12:15]\n" \
"ds_read_b128 a[40:43],v250 offset:10240\nds_read_b128 a[44:47],v250 offset:11264\nM v[72:75],a[100:103],v[216:219],v[72:75]\nv_cvt_pk_bf16_f32 v206,v156,v157\nv_cvt_pk_bf16_f32 v207,v158,v159\nM v[76:79],a[100:103],v[220:223],v[76:79]\nM v[16:19],a[104:107],v[208:211],v[16:19]\nM v[20:23],a[104:107],v[212:215],v[20:23]\n" \
"M v[80:83],a[104:107],v[216:219],v[80:83]\nM v[84:87],a[104:107],v[220:223],v[84:87]\nM v[24:27],a[108:111],v[208:211],v[24:27]\nM v[28:31],a[108:111],v[212:215],v[28:31]\nds_read_b128 a[48:51],v250 offset:12288\nds_read_b128 a[52:55],v250 offset:13312\nM v[88:91],a[108:111],v[216:219],v[88:91]\nM v[92:95],a[108:111],v[220:223],v[92:95]\n" \
"M v[32:35],a[112:115],v[208:211],v[32:35]\nM v[36:39],a[112:115],v[212:215],v[36:39]\nbuffer_load_dwordx4 v240,s[48:51],s95 offen offset:3072 sc1 lds\nM v[96:99],a[112:115],v[216:219],v[96:99]\ns_cmp_eq_u32 s95,s96\ns_cselect_b32 s72,0xc000,s98\ns_add_u32 s95,s95,s72\nM v[100:103],a[112:115],v[220:223],v[100:103]\n" \
"M v[40:43],a[116:119],v[208:211],v[40:43]\nM v[44:47],a[116:119],v[212:215],v[44:47]\nds_read_b128 a[56:59],v250 offset:14336\nds_read_b128 a[60:63],v250 offset:15360\nM v[104:107],a[116:119],v[216:219],v[104:107]\nv_add_u32 v250,s94,v241\nM v[108:111],a[116:119],v[220:223],v[108:111]\nM v[48:51],a[120:123],v[208:211],v[48:51]\n" \
"M v[52:55],a[120:123],v[212:215],v[52:55]\nM v[112:115],a[120:123],v[216:219],v[112:115]\nM v[116:119],a[120:123],v[220:223],v[116:119]\nM v[56:59],a[124:127],v[208:211],v[56:59]\nM v[60:63],a[124:127],v[212:215],v[60:63]\nM v[120:123],a[124:127],v[216:219],v[120:123]\nbuffer_load_dword v247,v254,s[56:59],s1 offen\nM v[124:127],a[124:127],v[220:223],v[124:127]\n" \
"s_add_u32 s1,s1,s0\nM a[192:195],a[208:211],v[208:211],a[192:195]\nM a[196:199],a[208:211],v[212:215],a[196:199]\nM a[200:203],a[208:211],v[216:219],a[200:203]\nM a[204:207],a[208:211],v[220:223],a[204:207]\ns_cmp_lg_u32 s95,s97\ns_cbranch_scc1 .Ldual\n.Ldf:\n" \
"s_waitcnt lgkmcnt(4)\nM v[128:131],a[0:3],a[128:131],v[224:227]\ns_add_u32 m0,s94,s64\nv_lshrrev_b32 v248,2,v249\nM v[128:131],a[4:7],a[132:135],v[128:131]\nds_read_b128 a[64:67],v250 offset:0\nds_read_b128 a[68:71],v250 offset:1024\nM v[128:131],a[8:11],a[136:139],v[128:131]\n" \
"v_mul_u32_u24 v248,3584,v248\nv_and_b32 v251,3,v249\nM v[128:131],a[12:15],a[140:143],v[128:131]\ns_mul_i32 s70,s7,458752\nv_lshl_add_u32 v248,v251,3,v248\nM v[132:135],a[0:3],a[144:147],v[228:231]\nbuffer_load_dwordx4 v240,s[44:47],s95 offen offset:0 sc1 lds\nM v[132:135],a[4:7],a[148:151],v[132:135]\n" \
"s_add_u32 s70,s70,s2\nv_add_u32 v248,s70,v248\nM v[132:135],a[8:11],a[152:155],v[132:135]\nv_and_b32 v248,s3,v248\nv_cvt_pk_bf16_f32 v208,v160,v161\nM v[132:135],a[12:15],a[156:159],v[132:135]\nds_read_b128 a[72:75],v250 offset:2048\nds_read_b128 a[76:79],v250 offset:3072\n" \
"M v[136:139],a[0:3],a[160:163],v[232:235]\nv_cvt_pk_bf16_f32 v209,v162,v163\nv_cvt_pk_bf16_f32 v212,v164,v165\nM v[136:139],a[4:7],a[164:167],v[136:139]\nv_cvt_pk_bf16_f32 v213,v166,v167\nv_cvt_pk_bf16_f32 v216,v168,v169\nM v[136:139],a[8:11],a[168:171],v[136:139]\nv_cvt_pk_bf16_f32 v217,v170,v171\n" \
"v_cvt_pk_bf16_f32 v210,v176,v177\nM v[136:139],a[12:15],a[172:175],v[136:139]\nv_exp_f32 v172,v172\nM v[140:143],a[0:3],a[176:179],v[236:239]\nv_exp_f32 v173,v173\nM v[140:143],a[4:7],a[180:183],v[140:143]\nds_read_b128 a[80:83],v250 offset:4096\nds_read_b128 a[84:87],v250 offset:5120\n" \
"M v[140:143],a[8:11],a[184:187],v[140:143]\nglobal_load_dwordx2 a[212:213],v248,s[22:23]\nM v[140:143],a[12:15],a[188:191],v[140:143]\nv_exp_f32 v174,v174\nM v[144:147],a[16:19],a[128:131],v[224:227]\nv_exp_f32 v175,v175\nM v[144:147],a[20:23],a[132:135],v[144:147]\nv_cvt_pk_bf16_f32 v220,v172,v173\n" \
"v_cvt_pk_bf16_f32 v221,v174,v175\nM v[144:147],a[24:27],a[136:139],v[144:147]\nv_cvt_pk_bf16_f32 v211,v178,v179\ns_and_b32 s71,s3,57344\nM v[144:147],a[28:31],a[140:143],v[144:147]\nds_read_b128 a[88:91],v250 offset:6144\nds_read_b128 a[92:95],v250 offset:7168\nM v[148:151],a[16:19],a[144:147],v[228:231]\n" \
"v_cvt_pk_bf16_f32 v214,v180,v181\nv_cvt_pk_bf16_f32 v215,v182,v183\nM v[148:151],a[20:23],a[148:151],v[148:151]\nv_exp_f32 v184,v184\nM v[148:151],a[24:27],a[152:155],v[148:151]\nbuffer_load_dwordx4 v240,s[44:47],s95 offen offset:1024 sc1 lds\nM v[148:151],a[28:31],a[156:159],v[148:151]\nv_exp_f32 v185,v185\n" \
"M v[152:155],a[16:19],a[160:163],v[232:235]\nv_exp_f32 v186,v186\nM v[152:155],a[20:23],a[164:167],v[152:155]\nds_read_b128 a[96:99],v250 offset:8192\nds_read_b128 a[100:103],v250 offset:9216\nM v[152:155],a[24:27],a[168:171],v[152:155]\nv_exp_f32 v187,v187\nM v[152:155],a[28:31],a[172:175],v[152:155]\n" \
"v_cvt_pk_bf16_f32 v218,v184,v185\nv_cvt_pk_bf16_f32 v219,v186,v187\nM v[156:159],a[16:19],a[176:179],v[236:239]\nv_exp_f32 v188,v188\nM v[156:159],a[20:23],a[180:183],v[156:159]\nv_exp_f32 v189,v189\nM v[156:159],a[24:27],a[184:187],v[156:159]\nv_exp_f32 v190,v190\n" \
"M v[156:159],a[28:31],a[188:191],v[156:159]\nds_read_b128 a[104:107],v250 offset:10240\nds_read_b128 a[108:111],v250 offset:11264\nM v[160:163],a[32:35],a[128:131],v[224:227]\nglobal_load_dwordx2 a[228:229],v248,s[24:25]\nM v[160:163],a[36:39],a[132:135],v[160:163]\nv_add_u32 v248,s71,v248\nv_cvt_pk_bf16_f32 v222,v188,v189\n" \
"M v[160:163],a[40:43],a[136:139],v[160:163]\nv_exp_f32 v191,v191\nM v[160:163],a[44:47],a[140:143],v[160:163]\nv_cvt_pk_bf16_f32 v223,v190,v191\ns_add_u32 s94,s94,0x4000\nM v[164:167],a[32:35],a[144:147],v[228:231]\nv_exp_f32 v128,v128\nM v[164:167],a[36:39],a[148:151],v[164:167]\n" \
"ds_read_b128 a[112:115],v250 offset:12288\nds_read_b128 a[116:119],v250 offset:13312\nM v[164:167],a[40:43],a[152:155],v[164:167]\nv_exp_f32 v129,v129\nM v[164:167],a[44:47],a[156:159],v[164:167]\nv_exp_f32 v130,v130\nM v[168:171],a[32:35],a[160:163],v[232:235]\nbuffer_load_dwordx4 v240,s[44:47],s95 offen offset:2048 sc1 lds\n" \
"M v[168:171],a[36:39],a[164:167],v[168:171]\nv_exp_f32 v131,v131\nM v[168:171],a[40:43],a[168:171],v[168:171]\nv_exp_f32 v144,v144\nM v[168:171],a[44:47],a[172:175],v[168:171]\nds_read_b128 a[120:123],v250 offset:14336\nds_read_b128 a[124:127],v250 offset:15360\nM v[172:175],a[32:35],a[176:179],v[236:239]\n" \
"v_xor_b32 v250,0x18000,v250\ns_and_b32 s94,s94,0xffff\nM v[172:175],a[36:39],a[180:183],v[172:175]\nv_exp_f32 v145,v145\nM v[172:175],a[40:43],a[184:187],v[172:175]\nv_exp_f32 v146,v146\nM v[172:175],a[44:47],a[188:191],v[172:175]\ns_waitcnt lgkmcnt(4)\n" \
"M v[176:179],a[48:51],a[128:131],v[224:227]\nv_exp_f32 v147,v147\nM v[176:179],a[52:55],a[132:135],v[176:179]\nv_exp_f32 v132,v132\nM v[176:179],a[56:59],a[136:139],v[176:179]\nglobal_load_dwordx2 a[214:215],v248,s[22:23]\nM v[176:179],a[60:63],a[140:143],v[176:179]\nv_exp_f32 v133,v133\n" \
"M v[180:183],a[48:51],a[144:147],v[228:231]\nv_exp_f32 v134,v134\nM v[180:183],a[52:55],a[148:151],v[180:183]\nv_exp_f32 v135,v135\nM v[180:183],a[56:59],a[152:155],v[180:183]\nv_exp_f32 v148,v148\nM v[180:183],a[60:63],a[156:159],v[180:183]\nv_exp_f32 v149,v149\n" \
"M v[184:187],a[48:51],a[160:163],v[232:235]\nv_exp_f32 v150,v150\nM v[184:187],a[52:55],a[164:167],v[184:187]\nv_exp_f32 v151,v151\nM v[184:187],a[56:59],a[168:171],v[184:187]\nbuffer_load_dwordx4 v240,s[44:47],s95 offen offset:3072 sc1 lds\nM v[184:187],a[60:63],a[172:175],v[184:187]\nv_exp_f32 v136,v136\n" \
"M v[188:191],a[48:51],a[176:179],v[236:239]\nv_exp_f32 v137,v137\nM v[188:191],a[52:55],a[180:183],v[188:191]\nv_exp_f32 v138,v138\nM v[188:191],a[56:59],a[184:187],v[188:191]\nglobal_load_dwordx2 a[230:231],v248,s[24:25]\nM v[188:191],a[60:63],a[188:191],v[188:191]\ns_add_u32 m0,m0,0x10000\n" \
"v_add_u32 v248,s71,v248\nM v[0:3],a[64:67],v[192:195],v[0:3]\ns_waitcnt vmcnt(16) lgkmcnt(0)\ns_barrier\nM v[4:7],a[64:67],v[196:199],v[4:7]\nds_read_b128 a[0:3],v250 offset:0\nds_read_b128 a[4:7],v250 offset:1024\nM v[64:67],a[64:67],v[200:203],v[64:67]\n" \
"v_exp_f32 v139,v139\nM v[68:71],a[64:67],v[204:207],v[68:71]\nv_exp_f32 v152,v152\nM v[8:11],a[68:71],v[192:195],v[8:11]\nv_exp_f32 v153,v153\nM v[12:15],a[68:71],v[196:199],v[12:15]\nbuffer_load_dwordx4 v240,s[48:51],s95 offen offset:0 sc1 lds\nM v[72:75],a[68:71],v[200:203],v[72:75]\n" \
"v_exp_f32 v154,v154\nM v[76:79],a[68:71],v[204:207],v[76:79]\nv_exp_f32 v155,v155\nM v[16:19],a[72:75],v[192:195],v[16:19]\nv_exp_f32 v140,v140\nM v[20:23],a[72:75],v[196:199],v[20:23]\nds_read_b128 a[8:11],v250 offset:2048\nds_read_b128 a[12:15],v250 offset:3072\n" \
"M v[80:83],a[72:75],v[200:203],v[80:83]\nv_exp_f32 v141,v141\nM v[84:87],a[72:75],v[204:207],v[84:87]\nv_exp_f32 v142,v142\nM v[24:27],a[76:79],v[192:195],v[24:27]\nv_exp_f32 v143,v143\nM v[28:31],a[76:79],v[196:199],v[28:31]\nglobal_load_dwordx2 a[216:217],v248,s[22:23]\n" \
"M v[88:91],a[76:79],v[200:203],v[88:91]\nv_exp_f32 v156,v156\nM v[92:95],a[76:79],v[204:207],v[92:95]\nv_exp_f32 v157,v157\nM v[32:35],a[80:83],v[192:195],v[32:35]\nv_exp_f32 v158,v158\nM v[36:39],a[80:83],v[196:199],v[36:39]\nds_read_b128 a[16:19],v250 offset:4096\n" \
"ds_read_b128 a[20:23],v250 offset:5120\nM v[96:99],a[80:83],v[200:203],v[96:99]\nv_exp_f32 v159,v159\nM v[100:103],a[80:83],v[204:207],v[100:103]\nv_exp_f32 v160,v160\nM v[40:43],a[84:87],v[192:195],v[40:43]\nv_exp_f32 v161,v161\nM v[44:47],a[84:87],v[196:199],v[44:47]\n" \
"buffer_load_dwordx4 v240,s[48:51],s95 offen offset:1024 sc1 lds\nM v[104:107],a[84:87],v[200:203],v[104:107]\nv_exp_f32 v162,v162\nM v[108:111],a[84:87],v[204:207],v[108:111]\nv_exp_f32 v163,v163\nM v[48:51],a[88:91],v[192:195],v[48:51]\nv_exp_f32 v176,v176\nM v[52:55],a[88:91],v[196:199],v[52:55]\n" \
"ds_read_b128 a[24:27],v250 offset:6144\nds_read_b128 a[28:31],v250 offset:7168\nM v[112:115],a[88:91],v[200:203],v[112:115]\nv_exp_f32 v177,v177\nM v[116:119],a[88:91],v[204:207],v[116:119]\nv_exp_f32 v178,v178\nM v[56:59],a[92:95],v[192:195],v[56:59]\nv_exp_f32 v179,v179\n" \
"M v[60:63],a[92:95],v[196:199],v[60:63]\nglobal_load_dwordx2 a[232:233],v248,s[24:25]\nM v[120:123],a[92:95],v[200:203],v[120:123]\nv_exp_f32 v164,v164\nM v[124:127],a[92:95],v[204:207],v[124:127]\nv_exp_f32 v165,v165\nM a[192:195],a[208:211],v[192:195],a[192:195]\nv_add_u32 v248,s71,v248\n" \
"v_cvt_pk_bf16_f32 v192,v128,v129\nM a[196:199],a[208:211],v[196:199],a[196:199]\nds_read_b128 a[32:35],v250 offset:8192\nds_read_b128 a[36:39],v250 offset:9216\nM a[200:203],a[208:211],v[200:203],a[200:203]\nv_cvt_pk_bf16_f32 v193,v130,v131\nv_cvt_pk_bf16_f32 v194,v144,v145\nM a[204:207],a[208:211],v[204:207],a[204:207]\n" \
"v_cvt_pk_bf16_f32 v195,v146,v147\nv_cvt_pk_bf16_f32 v196,v132,v133\nM v[0:3],a[96:99],v[208:211],v[0:3]\nv_cvt_pk_bf16_f32 v197,v134,v135\nv_cvt_pk_bf16_f32 v198,v148,v149\nM v[4:7],a[96:99],v[212:215],v[4:7]\nbuffer_load_dwordx4 v240,s[48:51],s95 offen offset:2048 sc1 lds\nM v[64:67],a[96:99],v[216:219],v[64:67]\n" \
"v_cvt_pk_bf16_f32 v199,v150,v151\nv_cvt_pk_bf16_f32 v200,v136,v137\nM v[68:71],a[96:99],v[220:223],v[68:71]\nv_cvt_pk_bf16_f32 v201,v138,v139\nv_cvt_pk_bf16_f32 v202,v152,v153\nM v[8:11],a[100:103],v[208:211],v[8:11]\nv_cvt_pk_bf16_f32 v203,v154,v155\nv_cvt_pk_bf16_f32 v204,v140,v141\n" \
"M v[12:15],a[100:103],v[212:215],v[12:15]\nds_read_b128 a[40:43],v250 offset:10240\nds_read_b128 a[44:47],v250 offset:11264\nM v[72:75],a[100:103],v[216:219],v[72:75]\nv_cvt_pk_bf16_f32 v205,v142,v143\nv_cvt_pk_bf16_f32 v206,v156,v157\nM v[76:79],a[100:103],v[220:223],v[76:79]\nv_exp_f32 v166,v166\n" \
"M v[16:19],a[104:107],v[208:211],v[16:19]\nv_exp_f32 v167,v167\nM v[20:23],a[104:107],v[212:215],v[20:23]\nglobal_load_dwordx2 a[218:219],v248,s[22:23]\nM v[80:83],a[104:107],v[216:219],v[80:83]\nv_exp_f32 v180,v180\nM v[84:87],a[104:107],v[220:223],v[84:87]\nv_exp_f32 v181,v181\n" \
"M v[24:27],a[108:111],v[208:211],v[24:27]\nv_exp_f32 v182,v182\nM v[28:31],a[108:111],v[212:215],v[28:31]\nds_read_b128 a[48:51],v250 offset:12288\nds_read_b128 a[52:55],v250 offset:13312\nM v[88:91],a[108:111],v[216:219],v[88:91]\nv_exp_f32 v183,v183\nM v[92:95],a[108:111],v[220:223],v[92:95]\n" \
"v_exp_f32 v168,v168\nM v[32:35],a[112:115],v[208:211],v[32:35]\nv_exp_f32 v169,v169\nM v[36:39],a[112:115],v[212:215],v[36:39]\nbuffer_load_dwordx4 v240,s[48:51],s95 offen offset:3072 sc1 lds\nM v[96:99],a[112:115],v[216:219],v[96:99]\nv_cvt_pk_bf16_f32 v207,v158,v159\ns_add_u32 s95,s95,0x4000\n" \
"M v[100:103],a[112:115],v[220:223],v[100:103]\nv_exp_f32 v170,v170\nM v[40:43],a[116:119],v[208:211],v[40:43]\nv_exp_f32 v171,v171\nM v[44:47],a[116:119],v[212:215],v[44:47]\nds_read_b128 a[56:59],v250 offset:14336\nds_read_b128 a[60:63],v250 offset:15360\nM v[104:107],a[116:119],v[216:219],v[104:107]\n" \
"v_add_u32 v250,s94,v241\nM v[108:111],a[116:119],v[220:223],v[108:111]\nM v[48:51],a[120:123],v[208:211],v[48:51]\nM v[52:55],a[120:123],v[212:215],v[52:55]\nglobal_load_dwordx2 a[234:235],v248,s[24:25]\nM v[112:115],a[120:123],v[216:219],v[112:115]\nv_add_u32 v248,s71,v248\nM v[116:119],a[120:123],v[220:223],v[116:119]\n" \
"M v[56:59],a[124:127],v[208:211],v[56:59]\nM v[60:63],a[124:127],v[212:215],v[60:63]\nM v[120:123],a[124:127],v[216:219],v[120:123]\nM v[124:127],a[124:127],v[220:223],v[124:127]\nM a[192:195],a[208:211],v[208:211],a[192:195]\nM a[196:199],a[208:211],v[212:215],a[196:199]\nM a[200:203],a[208:211],v[216:219],a[200:203]\nM a[204:207],a[208:211],v[220:223],a[204:207]\n" \
"s_waitcnt lgkmcnt(4)\nM v[128:131],a[0:3],a[128:131],v[224:227]\ns_add_u32 m0,s94,s64\nv_cvt_pk_bf16_f32 v208,v160,v161\nM v[128:131],a[4:7],a[132:135],v[128:131]\nds_read_b128 a[64:67],v250 offset:0\nds_read_b128 a[68:71],v250 offset:1024\nM v[128:131],a[8:11],a[136:139],v[128:131]\n" \
"v_cvt_pk_bf16_f32 v209,v162,v163\nv_cvt_pk_bf16_f32 v212,v164,v165\nM v[128:131],a[12:15],a[140:143],v[128:131]\nv_cvt_pk_bf16_f32 v213,v166,v167\nv_cvt_pk_bf16_f32 v216,v168,v169\nM v[132:135],a[0:3],a[144:147],v[228:231]\nbuffer_load_dwordx4 v240,s[44:47],s95 offen offset:0 sc1 lds\nM v[132:135],a[4:7],a[148:151],v[132:135]\n" \
"v_cvt_pk_bf16_f32 v217,v170,v171\nv_cvt_pk_bf16_f32 v210,v176,v177\nM v[132:135],a[8:11],a[152:155],v[132:135]\nv_exp_f32 v172,v172\nM v[132:135],a[12:15],a[156:159],v[132:135]\nds_read_b128 a[72:75],v250 offset:2048\nds_read_b128 a[76:79],v250 offset:3072\nM v[136:139],a[0:3],a[160:163],v[232:235]\n" \
"v_exp_f32 v173,v173\nM v[136:139],a[4:7],a[164:167],v[136:139]\nv_exp_f32 v174,v174\nM v[136:139],a[8:11],a[168:171],v[136:139]\nv_exp_f32 v175,v175\nM v[136:139],a[12:15],a[172:175],v[136:139]\nv_cvt_pk_bf16_f32 v220,v172,v173\nv_cvt_pk_bf16_f32 v221,v174,v175\n" \
"M v[140:143],a[0:3],a[176:179],v[236:239]\nv_cvt_pk_bf16_f32 v211,v178,v179\nv_cvt_pk_bf16_f32 v214,v180,v181\nM v[140:143],a[4:7],a[180:183],v[140:143]\nds_read_b128 a[80:83],v250 offset:4096\nds_read_b128 a[84:87],v250 offset:5120\nM v[140:143],a[8:11],a[184:187],v[140:143]\nglobal_load_dwordx2 a[220:221],v248,s[22:23]\n" \
"M v[140:143],a[12:15],a[188:191],v[140:143]\nv_cvt_pk_bf16_f32 v215,v182,v183\ns_add_u32 s94,s94,0x4000\nM v[144:147],a[16:19],a[128:131],v[224:227]\nv_exp_f32 v184,v184\nM v[144:147],a[20:23],a[132:135],v[144:147]\nv_exp_f32 v185,v185\nM v[144:147],a[24:27],a[136:139],v[144:147]\n" \
"v_exp_f32 v186,v186\nM v[144:147],a[28:31],a[140:143],v[144:147]\nds_read_b128 a[88:91],v250 offset:6144\nds_read_b128 a[92:95],v250 offset:7168\nM v[148:151],a[16:19],a[144:147],v[228:231]\nv_exp_f32 v187,v187\nM v[148:151],a[20:23],a[148:151],v[148:151]\nv_cvt_pk_bf16_f32 v218,v184,v185\n" \
"v_cvt_pk_bf16_f32 v219,v186,v187\nM v[148:151],a[24:27],a[152:155],v[148:151]\nbuffer_load_dwordx4 v240,s[44:47],s95 offen offset:1024 sc1 lds\nM v[148:151],a[28:31],a[156:159],v[148:151]\nv_exp_f32 v188,v188\nM v[152:155],a[16:19],a[160:163],v[232:235]\nv_exp_f32 v189,v189\nM v[152:155],a[20:23],a[164:167],v[152:155]\n" \
"ds_read_b128 a[96:99],v250 offset:8192\nds_read_b128 a[100:103],v250 offset:9216\nM v[152:155],a[24:27],a[168:171],v[152:155]\nv_exp_f32 v190,v190\nM v[152:155],a[28:31],a[172:175],v[152:155]\nv_exp_f32 v191,v191\nM v[156:159],a[16:19],a[176:179],v[236:239]\nv_cvt_pk_bf16_f32 v222,v188,v189\n" \
"v_cvt_pk_bf16_f32 v223,v190,v191\nM v[156:159],a[20:23],a[180:183],v[156:159]\nv_exp_f32 v128,v128\nM v[156:159],a[24:27],a[184:187],v[156:159]\nv_exp_f32 v129,v129\nM v[156:159],a[28:31],a[188:191],v[156:159]\nds_read_b128 a[104:107],v250 offset:10240\nds_read_b128 a[108:111],v250 offset:11264\n" \
"M v[160:163],a[32:35],a[128:131],v[224:227]\nglobal_load_dwordx2 a[236:237],v248,s[24:25]\nM v[160:163],a[36:39],a[132:135],v[160:163]\nv_add_u32 v248,s71,v248\ns_and_b32 s94,s94,0xffff\nM v[160:163],a[40:43],a[136:139],v[160:163]\nv_exp_f32 v130,v130\nM v[160:163],a[44:47],a[140:143],v[160:163]\n" \
"v_exp_f32 v131,v131\nM v[164:167],a[32:35],a[144:147],v[228:231]\nv_exp_f32 v144,v144\nM v[164:167],a[36:39],a[148:151],v[164:167]\nds_read_b128 a[112:115],v250 offset:12288\nds_read_b128 a[116:119],v250 offset:13312\nM v[164:167],a[40:43],a[152:155],v[164:167]\nv_exp_f32 v145,v145\n" \
"M v[164:167],a[44:47],a[156:159],v[164:167]\nv_exp_f32 v146,v146\nM v[168:171],a[32:35],a[160:163],v[232:235]\nbuffer_load_dwordx4 v240,s[44:47],s95 offen offset:2048 sc1 lds\nM v[168:171],a[36:39],a[164:167],v[168:171]\nv_exp_f32 v147,v147\nM v[168:171],a[40:43],a[168:171],v[168:171]\nv_exp_f32 v132,v132\n" \
"M v[168:171],a[44:47],a[172:175],v[168:171]\nds_read_b128 a[120:123],v250 offset:14336\nds_read_b128 a[124:127],v250 offset:15360\nM v[172:175],a[32:35],a[176:179],v[236:239]\nv_exp_f32 v133,v133\nM v[172:175],a[36:39],a[180:183],v[172:175]\nv_exp_f32 v134,v134\nM v[172:175],a[40:43],a[184:187],v[172:175]\n" \
"v_exp_f32 v135,v135\nM v[172:175],a[44:47],a[188:191],v[172:175]\nv_xor_b32 v250,0x18000,v250\ns_waitcnt lgkmcnt(3)\nM v[176:179],a[48:51],a[128:131],v[224:227]\nv_exp_f32 v148,v148\nM v[176:179],a[52:55],a[132:135],v[176:179]\nv_exp_f32 v149,v149\n" \
"M v[176:179],a[56:59],a[136:139],v[176:179]\nglobal_load_dwordx2 a[222:223],v248,s[22:23]\nM v[176:179],a[60:63],a[140:143],v[176:179]\nv_exp_f32 v150,v150\nM v[180:183],a[48:51],a[144:147],v[228:231]\nv_exp_f32 v151,v151\nM v[180:183],a[52:55],a[148:151],v[180:183]\nv_exp_f32 v136,v136\n" \
"M v[180:183],a[56:59],a[152:155],v[180:183]\nv_exp_f32 v137,v137\nM v[180:183],a[60:63],a[156:159],v[180:183]\nv_exp_f32 v138,v138\nM v[184:187],a[48:51],a[160:163],v[232:235]\nv_exp_f32 v139,v139\nM v[184:187],a[52:55],a[164:167],v[184:187]\nv_exp_f32 v152,v152\n" \
"M v[184:187],a[56:59],a[168:171],v[184:187]\nbuffer_load_dwordx4 v240,s[44:47],s95 offen offset:3072 sc1 lds\nM v[184:187],a[60:63],a[172:175],v[184:187]\nv_exp_f32 v153,v153\nM v[188:191],a[48:51],a[176:179],v[236:239]\nv_exp_f32 v154,v154\nM v[188:191],a[52:55],a[180:183],v[188:191]\nv_exp_f32 v155,v155\n" \
"M v[188:191],a[56:59],a[184:187],v[188:191]\nglobal_load_dwordx2 a[238:239],v248,s[24:25]\nM v[188:191],a[60:63],a[188:191],v[188:191]\ns_add_u32 m0,m0,0x10000\nv_add_u32 v248,s71,v248\nM v[0:3],a[64:67],v[192:195],v[0:3]\ns_waitcnt vmcnt(16) lgkmcnt(0)\ns_barrier\n" \
"M v[4:7],a[64:67],v[196:199],v[4:7]\nds_read_b128 a[0:3],v250 offset:0\nds_read_b128 a[4:7],v250 offset:1024\nM v[64:67],a[64:67],v[200:203],v[64:67]\nv_exp_f32 v140,v140\nM v[68:71],a[64:67],v[204:207],v[68:71]\nv_exp_f32 v141,v141\nM v[8:11],a[68:71],v[192:195],v[8:11]\n" \
"v_exp_f32 v142,v142\nM v[12:15],a[68:71],v[196:199],v[12:15]\nbuffer_load_dwordx4 v240,s[48:51],s95 offen offset:0 sc1 lds\nM v[72:75],a[68:71],v[200:203],v[72:75]\nv_exp_f32 v143,v143\nM v[76:79],a[68:71],v[204:207],v[76:79]\nv_exp_f32 v156,v156\nM v[16:19],a[72:75],v[192:195],v[16:19]\n" \
"v_exp_f32 v157,v157\nM v[20:23],a[72:75],v[196:199],v[20:23]\nds_read_b128 a[8:11],v250 offset:2048\nds_read_b128 a[12:15],v250 offset:3072\nM v[80:83],a[72:75],v[200:203],v[80:83]\nv_exp_f32 v158,v158\nM v[84:87],a[72:75],v[204:207],v[84:87]\nv_exp_f32 v159,v159\n" \
"M v[24:27],a[76:79],v[192:195],v[24:27]\nv_exp_f32 v160,v160\nM v[28:31],a[76:79],v[196:199],v[28:31]\nglobal_load_dwordx2 a[224:225],v248,s[22:23]\nM v[88:91],a[76:79],v[200:203],v[88:91]\nv_exp_f32 v161,v161\nM v[92:95],a[76:79],v[204:207],v[92:95]\nv_exp_f32 v162,v162\n" \
"M v[32:35],a[80:83],v[192:195],v[32:35]\nv_exp_f32 v163,v163\nM v[36:39],a[80:83],v[196:199],v[36:39]\nds_read_b128 a[16:19],v250 offset:4096\nds_read_b128 a[20:23],v250 offset:5120\nM v[96:99],a[80:83],v[200:203],v[96:99]\nv_exp_f32 v176,v176\nM v[100:103],a[80:83],v[204:207],v[100:103]\n" \
"v_exp_f32 v177,v177\nM v[40:43],a[84:87],v[192:195],v[40:43]\nv_exp_f32 v178,v178\nM v[44:47],a[84:87],v[196:199],v[44:47]\nbuffer_load_dwordx4 v240,s[48:51],s95 offen offset:1024 sc1 lds\nM v[104:107],a[84:87],v[200:203],v[104:107]\nv_exp_f32 v179,v179\nM v[108:111],a[84:87],v[204:207],v[108:111]\n" \
"v_exp_f32 v164,v164\nM v[48:51],a[88:91],v[192:195],v[48:51]\nv_exp_f32 v165,v165\nM v[52:55],a[88:91],v[196:199],v[52:55]\nds_read_b128 a[24:27],v250 offset:6144\nds_read_b128 a[28:31],v250 offset:7168\nM v[112:115],a[88:91],v[200:203],v[112:115]\nv_exp_f32 v166,v166\n" \
"M v[116:119],a[88:91],v[204:207],v[116:119]\nv_exp_f32 v167,v167\nM v[56:59],a[92:95],v[192:195],v[56:59]\nv_exp_f32 v180,v180\nM v[60:63],a[92:95],v[196:199],v[60:63]\nglobal_load_dwordx2 a[240:241],v248,s[24:25]\nM v[120:123],a[92:95],v[200:203],v[120:123]\nv_exp_f32 v181,v181\n" \
"M v[124:127],a[92:95],v[204:207],v[124:127]\nv_exp_f32 v182,v182\nM a[192:195],a[208:211],v[192:195],a[192:195]\nv_add_u32 v248,s71,v248\nv_cvt_pk_bf16_f32 v192,v128,v129\nM a[196:199],a[208:211],v[196:199],a[196:199]\nds_read_b128 a[32:35],v250 offset:8192\nds_read_b128 a[36:39],v250 offset:9216\n" \
"M a[200:203],a[208:211],v[200:203],a[200:203]\nv_cvt_pk_bf16_f32 v193,v130,v131\nv_cvt_pk_bf16_f32 v194,v144,v145\nM a[204:207],a[208:211],v[204:207],a[204:207]\nv_cvt_pk_bf16_f32 v195,v146,v147\nv_cvt_pk_bf16_f32 v196,v132,v133\nM v[0:3],a[96:99],v[208:211],v[0:3]\nv_cvt_pk_bf16_f32 v197,v134,v135\n" \
"v_cvt_pk_bf16_f32 v198,v148,v149\nM v[4:7],a[96:99],v[212:215],v[4:7]\nbuffer_load_dwordx4 v240,s[48:51],s95 offen offset:2048 sc1 lds\nM v[64:67],a[96:99],v[216:219],v[64:67]\nv_cvt_pk_bf16_f32 v199,v150,v151\nv_cvt_pk_bf16_f32 v200,v136,v137\nM v[68:71],a[96:99],v[220:223],v[68:71]\nv_cvt_pk_bf16_f32 v201,v138,v139\n" \
"v_cvt_pk_bf16_f32 v202,v152,v153\nM v[8:11],a[100:103],v[208:211],v[8:11]\nv_cvt_pk_bf16_f32 v203,v154,v155\nv_cvt_pk_bf16_f32 v204,v140,v141\nM v[12:15],a[100:103],v[212:215],v[12:15]\nds_read_b128 a[40:43],v250 offset:10240\nds_read_b128 a[44:47],v250 offset:11264\nM v[72:75],a[100:103],v[216:219],v[72:75]\n" \
"v_cvt_pk_bf16_f32 v205,v142,v143\nv_cvt_pk_bf16_f32 v206,v156,v157\nM v[76:79],a[100:103],v[220:223],v[76:79]\nv_exp_f32 v183,v183\nM v[16:19],a[104:107],v[208:211],v[16:19]\nv_exp_f32 v168,v168\nM v[20:23],a[104:107],v[212:215],v[20:23]\nglobal_load_dwordx2 a[226:227],v248,s[22:23]\n" \
"M v[80:83],a[104:107],v[216:219],v[80:83]\nv_exp_f32 v169,v169\nM v[84:87],a[104:107],v[220:223],v[84:87]\nv_exp_f32 v170,v170\nM v[24:27],a[108:111],v[208:211],v[24:27]\nv_exp_f32 v171,v171\nM v[28:31],a[108:111],v[212:215],v[28:31]\nds_read_b128 a[48:51],v250 offset:12288\n" \
"ds_read_b128 a[52:55],v250 offset:13312\nM v[88:91],a[108:111],v[216:219],v[88:91]\nv_cvt_pk_bf16_f32 v207,v158,v159\nM v[92:95],a[108:111],v[220:223],v[92:95]\nM v[32:35],a[112:115],v[208:211],v[32:35]\nM v[36:39],a[112:115],v[212:215],v[36:39]\nbuffer_load_dwordx4 v240,s[48:51],s95 offen offset:3072 sc1 lds\nM v[96:99],a[112:115],v[216:219],v[96:99]\n" \
"s_add_u32 s95,s95,0x4000\nM v[100:103],a[112:115],v[220:223],v[100:103]\nM v[40:43],a[116:119],v[208:211],v[40:43]\nM v[44:47],a[116:119],v[212:215],v[44:47]\nds_read_b128 a[56:59],v250 offset:14336\nds_read_b128 a[60:63],v250 offset:15360\nM v[104:107],a[116:119],v[216:219],v[104:107]\nv_add_u32 v250,s94,v241\n" \
"M v[108:111],a[116:119],v[220:223],v[108:111]\nM v[48:51],a[120:123],v[208:211],v[48:51]\nM v[52:55],a[120:123],v[212:215],v[52:55]\nglobal_load_dwordx2 a[242:243],v248,s[24:25]\nM v[112:115],a[120:123],v[216:219],v[112:115]\nv_add_u32 v248,s71,v248\nM v[116:119],a[120:123],v[220:223],v[116:119]\nM v[56:59],a[124:127],v[208:211],v[56:59]\n" \
"M v[60:63],a[124:127],v[212:215],v[60:63]\nM v[120:123],a[124:127],v[216:219],v[120:123]\nM v[124:127],a[124:127],v[220:223],v[124:127]\nM a[192:195],a[208:211],v[208:211],a[192:195]\nM a[196:199],a[208:211],v[212:215],a[196:199]\nM a[200:203],a[208:211],v[216:219],a[200:203]\nM a[204:207],a[208:211],v[220:223],a[204:207]\ns_cmp_eq_u32 s62,7\n" \
"s_cbranch_scc0 .Ltrans\ns_waitcnt lgkmcnt(4)\nM v[136:139],a[0:3],a[160:163],v[232:235]\ns_add_u32 m0,s94,s64\nv_cvt_pk_bf16_f32 v216,v168,v169\nM v[136:139],a[4:7],a[164:167],v[136:139]\nds_read_b128 a[64:67],v250 offset:0\nds_read_b128 a[68:71],v250 offset:1024\n" \
"M v[136:139],a[8:11],a[168:171],v[136:139]\nbuffer_load_dwordx4 v240,s[44:47],s95 offen offset:0 sc1 lds\nM v[136:139],a[12:15],a[172:175],v[136:139]\nv_cvt_pk_bf16_f32 v217,v170,v171\ns_add_u32 s71,s33,16384\nM v[140:143],a[0:3],a[176:179],v[236:239]\nds_read_b128 a[72:75],v250 offset:2048\nds_read_b128 a[76:79],v250 offset:3072\n" \
"M v[140:143],a[4:7],a[180:183],v[140:143]\nbuffer_store_dwordx4 a[248:251],v253,s[52:55],s33 offen offset:0 sc1\nM v[140:143],a[8:11],a[184:187],v[140:143]\nv_exp_f32 v172,v172\nM v[140:143],a[12:15],a[188:191],v[140:143]\nds_read_b128 a[80:83],v250 offset:4096\nds_read_b128 a[84:87],v250 offset:5120\nM v[152:155],a[16:19],a[160:163],v[232:235]\n" \
"v_exp_f32 v173,v173\nM v[152:155],a[20:23],a[164:167],v[152:155]\nv_exp_f32 v174,v174\nM v[152:155],a[24:27],a[168:171],v[152:155]\nds_read_b128 a[88:91],v250 offset:6144\nds_read_b128 a[92:95],v250 offset:7168\nM v[152:155],a[28:31],a[172:175],v[152:155]\nbuffer_load_dwordx4 v240,s[44:47],s95 offen offset:1024 sc1 lds\n" \
"M v[156:159],a[16:19],a[176:179],v[236:239]\nv_exp_f32 v175,v175\nM v[156:159],a[20:23],a[180:183],v[156:159]\nds_read_b128 a[96:99],v250 offset:8192\nds_read_b128 a[100:103],v250 offset:9216\nM v[156:159],a[24:27],a[184:187],v[156:159]\nbuffer_store_dwordx4 a[248:251],v253,s[52:55],s33 offen offset:1024 sc1\nM v[156:159],a[28:31],a[188:191],v[156:159]\n" \
"v_cvt_pk_bf16_f32 v220,v172,v173\nv_cvt_pk_bf16_f32 v221,v174,v175\nM v[168:171],a[32:35],a[160:163],v[232:235]\nds_read_b128 a[104:107],v250 offset:10240\nds_read_b128 a[108:111],v250 offset:11264\nM v[168:171],a[36:39],a[164:167],v[168:171]\nv_exp_f32 v184,v184\nM v[168:171],a[40:43],a[168:171],v[168:171]\n" \
"v_exp_f32 v185,v185\nM v[168:171],a[44:47],a[172:175],v[168:171]\nds_read_b128 a[112:115],v250 offset:12288\nds_read_b128 a[116:119],v250 offset:13312\nM v[172:175],a[32:35],a[176:179],v[236:239]\nbuffer_load_dwordx4 v240,s[44:47],s95 offen offset:2048 sc1 lds\nM v[172:175],a[36:39],a[180:183],v[172:175]\nv_exp_f32 v186,v186\n" \
"M v[172:175],a[40:43],a[184:187],v[172:175]\nv_exp_f32 v187,v187\nds_read_b128 a[120:123],v250 offset:14336\nds_read_b128 a[124:127],v250 offset:15360\nM v[172:175],a[44:47],a[188:191],v[172:175]\nbuffer_store_dwordx4 a[248:251],v253,s[52:55],s33 offen offset:2048 sc1\nv_cvt_pk_bf16_f32 v218,v184,v185\nv_cvt_pk_bf16_f32 v219,v186,v187\n" \
"s_waitcnt lgkmcnt(6)\nM v[184:187],a[48:51],a[160:163],v[232:235]\nv_exp_f32 v188,v188\nM v[184:187],a[52:55],a[164:167],v[184:187]\nv_exp_f32 v189,v189\nM v[184:187],a[56:59],a[168:171],v[184:187]\nv_exp_f32 v190,v190\nv_exp_f32 v191,v191\n" \
"M v[184:187],a[60:63],a[172:175],v[184:187]\nv_cvt_pk_bf16_f32 v222,v188,v189\nv_cvt_pk_bf16_f32 v223,v190,v191\nM v[188:191],a[48:51],a[176:179],v[236:239]\nv_xor_b32 v250,0x18000,v250\nv_cvt_pk_bf16_f32 v208,v160,v161\nM v[188:191],a[52:55],a[180:183],v[188:191]\nbuffer_load_dwordx4 v240,s[44:47],s95 offen offset:3072 sc1 lds\n" \
"M v[188:191],a[56:59],a[184:187],v[188:191]\ns_add_u32 m0,m0,0x10000\nv_cvt_pk_bf16_f32 v209,v162,v163\nM v[188:191],a[60:63],a[188:191],v[188:191]\nv_cvt_pk_bf16_f32 v210,v176,v177\nv_cvt_pk_bf16_f32 v211,v178,v179\nM v[0:3],a[64:67],v[192:195],v[0:3]\nbuffer_store_dwordx4 a[248:251],v253,s[52:55],s33 offen offset:3072 sc1\n" \
"M v[4:7],a[64:67],v[196:199],v[4:7]\nv_cvt_pk_bf16_f32 v212,v164,v165\nv_cvt_pk_bf16_f32 v213,v166,v167\nM v[64:67],a[64:67],v[200:203],v[64:67]\ns_waitcnt vmcnt(16) lgkmcnt(0)\ns_barrier\nM v[68:71],a[64:67],v[204:207],v[68:71]\nds_read_b128 a[0:3],v250 offset:0\n" \
"ds_read_b128 a[4:7],v250 offset:1024\nM v[8:11],a[68:71],v[192:195],v[8:11]\nv_cvt_pk_bf16_f32 v214,v180,v181\nv_cvt_pk_bf16_f32 v215,v182,v183\nM v[12:15],a[68:71],v[196:199],v[12:15]\nbuffer_store_dwordx4 a[248:251],v253,s[52:55],s71 offen offset:0 sc1\nM v[72:75],a[68:71],v[200:203],v[72:75]\ns_add_u32 s70,s78,16384\n" \
"s_add_u32 s94,s94,0x4000\nM v[76:79],a[68:71],v[204:207],v[76:79]\nbuffer_load_dwordx4 v240,s[48:51],s95 offen offset:0 sc1 lds\nM v[16:19],a[72:75],v[192:195],v[16:19]\nv_exp_f32 v136,v136\nM v[20:23],a[72:75],v[196:199],v[20:23]\nbuffer_store_dwordx4 a[248:251],v253,s[52:55],s71 offen offset:1024 sc1\nM v[80:83],a[72:75],v[200:203],v[80:83]\n" \
"v_exp_f32 v137,v137\nM v[84:87],a[72:75],v[204:207],v[84:87]\nds_read_b128 a[8:11],v250 offset:2048\nds_read_b128 a[12:15],v250 offset:3072\nM v[24:27],a[76:79],v[192:195],v[24:27]\nv_exp_f32 v138,v138\nM v[28:31],a[76:79],v[196:199],v[28:31]\nbuffer_store_dwordx4 a[248:251],v253,s[52:55],s71 offen offset:2048 sc1\n" \
"M v[88:91],a[76:79],v[200:203],v[88:91]\nv_exp_f32 v139,v139\nM v[92:95],a[76:79],v[204:207],v[92:95]\nv_exp_f32 v152,v152\nM v[32:35],a[80:83],v[192:195],v[32:35]\nv_exp_f32 v153,v153\nM v[36:39],a[80:83],v[196:199],v[36:39]\nbuffer_store_dwordx4 a[248:251],v253,s[52:55],s71 offen offset:3072 sc1\n" \
"M v[96:99],a[80:83],v[200:203],v[96:99]\nv_exp_f32 v154,v154\nM v[100:103],a[80:83],v[204:207],v[100:103]\nds_read_b128 a[16:19],v250 offset:4096\nds_read_b128 a[20:23],v250 offset:5120\nM v[40:43],a[84:87],v[192:195],v[40:43]\nv_exp_f32 v155,v155\nM v[44:47],a[84:87],v[196:199],v[44:47]\n" \
"v_exp_f32 v140,v140\nM v[104:107],a[84:87],v[200:203],v[104:107]\nv_exp_f32 v141,v141\nM v[108:111],a[84:87],v[204:207],v[108:111]\nbuffer_load_dwordx4 v240,s[48:51],s95 offen offset:1024 sc1 lds\nM v[48:51],a[88:91],v[192:195],v[48:51]\nv_exp_f32 v142,v142\nM v[52:55],a[88:91],v[196:199],v[52:55]\n" \
"buffer_store_dwordx4 a[248:251],v253,s[52:55],s78 offen offset:0 sc1\nM v[112:115],a[88:91],v[200:203],v[112:115]\nv_exp_f32 v143,v143\nM v[116:119],a[88:91],v[204:207],v[116:119]\nds_read_b128 a[24:27],v250 offset:6144\nds_read_b128 a[28:31],v250 offset:7168\nM v[56:59],a[92:95],v[192:195],v[56:59]\nv_exp_f32 v156,v156\n" \
"M v[60:63],a[92:95],v[196:199],v[60:63]\nbuffer_store_dwordx4 a[248:251],v253,s[52:55],s78 offen offset:1024 sc1\nM v[120:123],a[92:95],v[200:203],v[120:123]\nv_exp_f32 v157,v157\nM v[124:127],a[92:95],v[204:207],v[124:127]\nv_exp_f32 v158,v158\nM a[192:195],a[208:211],v[192:195],a[192:195]\nv_exp_f32 v159,v159\n" \
"M a[196:199],a[208:211],v[196:199],a[196:199]\nbuffer_store_dwordx4 a[248:251],v253,s[52:55],s78 offen offset:2048 sc1\nM a[200:203],a[208:211],v[200:203],a[200:203]\ns_and_b32 s94,s94,0xffff\nv_cvt_pk_bf16_f32 v200,v136,v137\nM a[204:207],a[208:211],v[204:207],a[204:207]\nds_read_b128 a[32:35],v250 offset:8192\nds_read_b128 a[36:39],v250 offset:9216\n" \
"M v[0:3],a[96:99],v[208:211],v[0:3]\nv_cvt_pk_bf16_f32 v201,v138,v139\nv_cvt_pk_bf16_f32 v202,v152,v153\nM v[4:7],a[96:99],v[212:215],v[4:7]\nv_cvt_pk_bf16_f32 v203,v154,v155\nv_cvt_pk_bf16_f32 v204,v140,v141\nM v[64:67],a[96:99],v[216:219],v[64:67]\nv_cvt_pk_bf16_f32 v205,v142,v143\n" \
"v_cvt_pk_bf16_f32 v206,v156,v157\nM v[68:71],a[96:99],v[220:223],v[68:71]\nbuffer_load_dwordx4 v240,s[48:51],s95 offen offset:2048 sc1 lds\nM v[8:11],a[100:103],v[208:211],v[8:11]\nv_exp_f32 v168,v168\nM v[12:15],a[100:103],v[212:215],v[12:15]\nbuffer_store_dwordx4 a[248:251],v253,s[52:55],s78 offen offset:3072 sc1\nM v[72:75],a[100:103],v[216:219],v[72:75]\n" \
"v_exp_f32 v169,v169\nM v[76:79],a[100:103],v[220:223],v[76:79]\nds_read_b128 a[40:43],v250 offset:10240\nds_read_b128 a[44:47],v250 offset:11264\nM v[16:19],a[104:107],v[208:211],v[16:19]\nv_exp_f32 v170,v170\nM v[20:23],a[104:107],v[212:215],v[20:23]\nbuffer_store_dwordx4 a[248:251],v253,s[52:55],s70 offen offset:0 sc1\n" \
"M v[80:83],a[104:107],v[216:219],v[80:83]\nv_exp_f32 v171,v171\nM v[84:87],a[104:107],v[220:223],v[84:87]\nv_exp_f32 v184,v184\nM v[24:27],a[108:111],v[208:211],v[24:27]\nv_exp_f32 v185,v185\nM v[28:31],a[108:111],v[212:215],v[28:31]\nbuffer_store_dwordx4 a[248:251],v253,s[52:55],s70 offen offset:1024 sc1\n" \
"M v[88:91],a[108:111],v[216:219],v[88:91]\nv_exp_f32 v186,v186\nM v[92:95],a[108:111],v[220:223],v[92:95]\nds_read_b128 a[48:51],v250 offset:12288\nds_read_b128 a[52:55],v250 offset:13312\nM v[32:35],a[112:115],v[208:211],v[32:35]\nv_exp_f32 v187,v187\nM v[36:39],a[112:115],v[212:215],v[36:39]\n" \
"v_cvt_pk_bf16_f32 v207,v158,v159\nM v[96:99],a[112:115],v[216:219],v[96:99]\nM v[100:103],a[112:115],v[220:223],v[100:103]\nbuffer_load_dwordx4 v240,s[48:51],s95 offen offset:3072 sc1 lds\nM v[40:43],a[116:119],v[208:211],v[40:43]\ns_add_u32 s95,s95,0xffff4000\nM v[44:47],a[116:119],v[212:215],v[44:47]\nbuffer_store_dwordx4 a[248:251],v253,s[52:55],s70 offen offset:2048 sc1\n" \
"M v[104:107],a[116:119],v[216:219],v[104:107]\nM v[108:111],a[116:119],v[220:223],v[108:111]\nds_read_b128 a[56:59],v250 offset:14336\nds_read_b128 a[60:63],v250 offset:15360\nM v[48:51],a[120:123],v[208:211],v[48:51]\nv_add_u32 v250,s94,v241\nM v[52:55],a[120:123],v[212:215],v[52:55]\nbuffer_store_dwordx4 a[248:251],v253,s[52:55],s70 offen offset:3072 sc1\n" \
"M v[112:115],a[120:123],v[216:219],v[112:115]\nM v[116:119],a[120:123],v[220:223],v[116:119]\nM v[56:59],a[124:127],v[208:211],v[56:59]\nM v[60:63],a[124:127],v[212:215],v[60:63]\nM v[120:123],a[124:127],v[216:219],v[120:123]\nM v[124:127],a[124:127],v[220:223],v[124:127]\nM a[192:195],a[208:211],v[208:211],a[192:195]\nM a[196:199],a[208:211],v[212:215],a[196:199]\n" \
"M a[200:203],a[208:211],v[216:219],a[200:203]\nM a[204:207],a[208:211],v[220:223],a[204:207]\n.Lsl:\ns_waitcnt lgkmcnt(4)\nM v[136:139],a[0:3],a[160:163],v[232:235]\ns_add_u32 m0,s94,s64\nv_cvt_pk_bf16_f32 v216,v168,v169\nM v[136:139],a[4:7],a[164:167],v[136:139]\n" \
"ds_read_b128 a[64:67],v250 offset:0\nds_read_b128 a[68:71],v250 offset:1024\nM v[136:139],a[8:11],a[168:171],v[136:139]\nbuffer_load_dwordx4 v240,s[44:47],s95 offen offset:0 sc1 lds\nM v[136:139],a[12:15],a[172:175],v[136:139]\nv_cvt_pk_bf16_f32 v217,v170,v171\nv_cvt_pk_bf16_f32 v218,v184,v185\nM v[140:143],a[0:3],a[176:179],v[236:239]\n" \
"ds_read_b128 a[72:75],v250 offset:2048\nds_read_b128 a[76:79],v250 offset:3072\nM v[140:143],a[4:7],a[180:183],v[140:143]\nv_exp_f32 v172,v172\nM v[140:143],a[8:11],a[184:187],v[140:143]\nv_exp_f32 v173,v173\nM v[140:143],a[12:15],a[188:191],v[140:143]\nds_read_b128 a[80:83],v250 offset:4096\n" \
"ds_read_b128 a[84:87],v250 offset:5120\nM v[152:155],a[16:19],a[160:163],v[232:235]\nv_exp_f32 v174,v174\nM v[152:155],a[20:23],a[164:167],v[152:155]\nv_exp_f32 v175,v175\nM v[152:155],a[24:27],a[168:171],v[152:155]\nds_read_b128 a[88:91],v250 offset:6144\nds_read_b128 a[92:95],v250 offset:7168\n" \
"M v[152:155],a[28:31],a[172:175],v[152:155]\nbuffer_load_dwordx4 v240,s[44:47],s95 offen offset:1024 sc1 lds\nM v[156:159],a[16:19],a[176:179],v[236:239]\nv_cvt_pk_bf16_f32 v220,v172,v173\nv_cvt_pk_bf16_f32 v221,v174,v175\nM v[156:159],a[20:23],a[180:183],v[156:159]\nds_read_b128 a[96:99],v250 offset:8192\nds_read_b128 a[100:103],v250 offset:9216\n" \
"M v[156:159],a[24:27],a[184:187],v[156:159]\nv_cvt_pk_bf16_f32 v219,v186,v187\ns_add_u32 s94,s94,0x4000\nM v[156:159],a[28:31],a[188:191],v[156:159]\nv_exp_f32 v188,v188\nM v[168:171],a[32:35],a[160:163],v[232:235]\nds_read_b128 a[104:107],v250 offset:10240\nds_read_b128 a[108:111],v250 offset:11264\n" \
"M v[168:171],a[36:39],a[164:167],v[168:171]\nv_exp_f32 v189,v189\nM v[168:171],a[40:43],a[168:171],v[168:171]\nv_exp_f32 v190,v190\nM v[168:171],a[44:47],a[172:175],v[168:171]\nds_read_b128 a[112:115],v250 offset:12288\nds_read_b128 a[116:119],v250 offset:13312\nM v[172:175],a[32:35],a[176:179],v[236:239]\n" \
"buffer_load_dwordx4 v240,s[44:47],s95 offen offset:2048 sc1 lds\nM v[172:175],a[36:39],a[180:183],v[172:175]\nv_exp_f32 v191,v191\nM v[172:175],a[40:43],a[184:187],v[172:175]\nds_read_b128 a[120:123],v250 offset:14336\nds_read_b128 a[124:127],v250 offset:15360\nM v[172:175],a[44:47],a[188:191],v[172:175]\nv_cvt_pk_bf16_f32 v222,v188,v189\n" \
"s_waitcnt lgkmcnt(7)\nM v[184:187],a[48:51],a[160:163],v[232:235]\nv_cvt_pk_bf16_f32 v223,v190,v191\nv_xor_b32 v250,0x18000,v250\nM v[184:187],a[52:55],a[164:167],v[184:187]\nv_exp_f32 v136,v136\nM v[184:187],a[56:59],a[168:171],v[184:187]\nv_exp_f32 v137,v137\n" \
"M v[184:187],a[60:63],a[172:175],v[184:187]\nv_exp_f32 v138,v138\nM v[188:191],a[48:51],a[176:179],v[236:239]\nv_exp_f32 v139,v139\nM v[188:191],a[52:55],a[180:183],v[188:191]\nbuffer_load_dwordx4 v240,s[44:47],s95 offen offset:3072 sc1 lds\nM v[188:191],a[56:59],a[184:187],v[188:191]\ns_add_u32 m0,m0,0x10000\n" \
"s_and_b32 s94,s94,0xffff\nM v[188:191],a[60:63],a[188:191],v[188:191]\nv_exp_f32 v152,v152\nM v[64:67],a[64:67],v[200:203],v[64:67]\nv_exp_f32 v153,v153\nM v[68:71],a[64:67],v[204:207],v[68:71]\nv_exp_f32 v154,v154\nM v[72:75],a[68:71],v[200:203],v[72:75]\n" \
"s_waitcnt vmcnt(16) lgkmcnt(0)\ns_barrier\nM v[76:79],a[68:71],v[204:207],v[76:79]\nds_read_b128 a[0:3],v250 offset:0\nds_read_b128 a[4:7],v250 offset:1024\nM v[80:83],a[72:75],v[200:203],v[80:83]\nv_exp_f32 v155,v155\nM v[84:87],a[72:75],v[204:207],v[84:87]\n" \
"buffer_load_dwordx4 v240,s[48:51],s95 offen offset:0 sc1 lds\nM v[88:91],a[76:79],v[200:203],v[88:91]\nv_exp_f32 v140,v140\nM v[92:95],a[76:79],v[204:207],v[92:95]\nds_read_b128 a[8:11],v250 offset:2048\nds_read_b128 a[12:15],v250 offset:3072\nM v[96:99],a[80:83],v[200:203],v[96:99]\nv_exp_f32 v141,v141\n" \
"M v[100:103],a[80:83],v[204:207],v[100:103]\nv_exp_f32 v142,v142\nM v[104:107],a[84:87],v[200:203],v[104:107]\nv_exp_f32 v143,v143\nM v[108:111],a[84:87],v[204:207],v[108:111]\nds_read_b128 a[16:19],v250 offset:4096\nds_read_b128 a[20:23],v250 offset:5120\nM v[112:115],a[88:91],v[200:203],v[112:115]\n" \
"v_exp_f32 v156,v156\nM v[116:119],a[88:91],v[204:207],v[116:119]\nbuffer_load_dwordx4 v240,s[48:51],s95 offen offset:1024 sc1 lds\nM v[120:123],a[92:95],v[200:203],v[120:123]\nv_exp_f32 v157,v157\nM v[124:127],a[92:95],v[204:207],v[124:127]\nds_read_b128 a[24:27],v250 offset:6144\nds_read_b128 a[28:31],v250 offset:7168\n" \
"M a[200:203],a[208:211],v[200:203],a[200:203]\nv_exp_f32 v158,v158\nM a[204:207],a[208:211],v[204:207],a[204:207]\nv_exp_f32 v159,v159\nM v[64:67],a[96:99],v[216:219],v[64:67]\nv_cvt_pk_bf16_f32 v200,v136,v137\nv_cvt_pk_bf16_f32 v201,v138,v139\nM v[68:71],a[96:99],v[220:223],v[68:71]\n" \
"ds_read_b128 a[32:35],v250 offset:8192\nds_read_b128 a[36:39],v250 offset:9216\nM v[72:75],a[100:103],v[216:219],v[72:75]\nv_cvt_pk_bf16_f32 v202,v152,v153\nv_cvt_pk_bf16_f32 v203,v154,v155\nM v[76:79],a[100:103],v[220:223],v[76:79]\nbuffer_load_dwordx4 v240,s[48:51],s95 offen offset:2048 sc1 lds\nM v[80:83],a[104:107],v[216:219],v[80:83]\n" \
"v_cvt_pk_bf16_f32 v204,v140,v141\nv_cvt_pk_bf16_f32 v205,v142,v143\nM v[84:87],a[104:107],v[220:223],v[84:87]\nds_read_b128 a[40:43],v250 offset:10240\nds_read_b128 a[44:47],v250 offset:11264\nM v[88:91],a[108:111],v[216:219],v[88:91]\nv_cvt_pk_bf16_f32 v206,v156,v157\nv_cvt_pk_bf16_f32 v207,v158,v159\n" \
"M v[92:95],a[108:111],v[220:223],v[92:95]\nv_exp_f32 v168,v168\nM v[96:99],a[112:115],v[216:219],v[96:99]\nv_exp_f32 v169,v169\nM v[100:103],a[112:115],v[220:223],v[100:103]\nds_read_b128 a[48:51],v250 offset:12288\nds_read_b128 a[52:55],v250 offset:13312\nM v[104:107],a[116:119],v[216:219],v[104:107]\n" \
"v_exp_f32 v170,v170\nM v[108:111],a[116:119],v[220:223],v[108:111]\nbuffer_load_dwordx4 v240,s[48:51],s95 offen offset:3072 sc1 lds\nM v[112:115],a[120:123],v[216:219],v[112:115]\nv_exp_f32 v171,v171\nM v[116:119],a[120:123],v[220:223],v[116:119]\nds_read_b128 a[56:59],v250 offset:14336\nds_read_b128 a[60:63],v250 offset:15360\n" \
"M v[120:123],a[124:127],v[216:219],v[120:123]\nv_exp_f32 v184,v184\nM v[124:127],a[124:127],v[220:223],v[124:127]\nv_exp_f32 v185,v185\nM a[200:203],a[208:211],v[216:219],a[200:203]\nv_exp_f32 v186,v186\nv_exp_f32 v187,v187\nv_add_u32 v250,s94,v241\n" \
"s_add_u32 s95,s95,0x4000\nM a[204:207],a[208:211],v[220:223],a[204:207]\ns_cmp_lg_u32 s95,s96\ns_cbranch_scc1 .Lsl\ns_waitcnt lgkmcnt(4)\nM v[128:131],a[0:3],a[128:131],v[224:227]\ns_add_u32 m0,s94,s64\nv_cvt_pk_bf16_f32 v216,v168,v169\n" \
"M v[128:131],a[4:7],a[132:135],v[128:131]\nds_read_b128 a[64:67],v250 offset:0\nds_read_b128 a[68:71],v250 offset:1024\nM v[128:131],a[8:11],a[136:139],v[128:131]\nbuffer_load_dwordx4 v240,s[44:47],s95 offen offset:0 sc1 lds\nM v[128:131],a[12:15],a[140:143],v[128:131]\nv_exp_f32 v188,v188\nM v[132:135],a[0:3],a[144:147],v[228:231]\n" \
"ds_read_b128 a[72:75],v250 offset:2048\nds_read_b128 a[76:79],v250 offset:3072\nM v[132:135],a[4:7],a[148:151],v[132:135]\nv_exp_f32 v189,v189\nM v[132:135],a[8:11],a[152:155],v[132:135]\nv_exp_f32 v190,v190\nM v[132:135],a[12:15],a[156:159],v[132:135]\nds_read_b128 a[80:83],v250 offset:4096\n" \
"ds_read_b128 a[84:87],v250 offset:5120\nM v[144:147],a[16:19],a[128:131],v[224:227]\nv_exp_f32 v191,v191\nM v[144:147],a[20:23],a[132:135],v[144:147]\nv_exp_f32 v172,v172\nM v[144:147],a[24:27],a[136:139],v[144:147]\nds_read_b128 a[88:91],v250 offset:6144\nds_read_b128 a[92:95],v250 offset:7168\n" \
"M v[144:147],a[28:31],a[140:143],v[144:147]\nbuffer_load_dwordx4 v240,s[44:47],s95 offen offset:1024 sc1 lds\nM v[148:151],a[16:19],a[144:147],v[228:231]\nv_exp_f32 v173,v173\nM v[148:151],a[20:23],a[148:151],v[148:151]\nds_read_b128 a[96:99],v250 offset:8192\nds_read_b128 a[100:103],v250 offset:9216\nM v[148:151],a[24:27],a[152:155],v[148:151]\n" \
"v_exp_f32 v174,v174\nM v[148:151],a[28:31],a[156:159],v[148:151]\nv_exp_f32 v175,v175\nM v[160:163],a[32:35],a[128:131],v[224:227]\nds_read_b128 a[104:107],v250 offset:10240\nds_read_b128 a[108:111],v250 offset:11264\nM v[160:163],a[36:39],a[132:135],v[160:163]\nv_cvt_pk_bf16_f32 v217,v170,v171\n" \
"v_cvt_pk_bf16_f32 v218,v184,v185\nM v[160:163],a[40:43],a[136:139],v[160:163]\nv_cvt_pk_bf16_f32 v219,v186,v187\nv_cvt_pk_bf16_f32 v220,v172,v173\nM v[160:163],a[44:47],a[140:143],v[160:163]\nds_read_b128 a[112:115],v250 offset:12288\nds_read_b128 a[116:119],v250 offset:13312\nM v[164:167],a[32:35],a[144:147],v[228:231]\n" \
"buffer_load_dwordx4 v240,s[44:47],s95 offen offset:2048 sc1 lds\nM v[164:167],a[36:39],a[148:151],v[164:167]\nv_cvt_pk_bf16_f32 v221,v174,v175\nv_cvt_pk_bf16_f32 v222,v188,v189\nM v[164:167],a[40:43],a[152:155],v[164:167]\nds_read_b128 a[120:123],v250 offset:14336\nds_read_b128 a[124:127],v250 offset:15360\nM v[164:167],a[44:47],a[156:159],v[164:167]\n" \
"v_xor_b32 v250,0x18000,v250\ns_waitcnt lgkmcnt(7)\nM v[176:179],a[48:51],a[128:131],v[224:227]\nv_cvt_pk_bf16_f32 v223,v190,v191\ns_add_u32 s94,s94,0x4000\nM v[176:179],a[52:55],a[132:135],v[176:179]\nv_exp_f32 v128,v128\nM v[176:179],a[56:59],a[136:139],v[176:179]\n" \
"v_exp_f32 v129,v129\nM v[176:179],a[60:63],a[140:143],v[176:179]\nv_exp_f32 v130,v130\nM v[180:183],a[48:51],a[144:147],v[228:231]\nv_exp_f32 v131,v131\nM v[180:183],a[52:55],a[148:151],v[180:183]\nbuffer_load_dwordx4 v240,s[44:47],s95 offen offset:3072 sc1 lds\nM v[180:183],a[56:59],a[152:155],v[180:183]\n" \
"s_add_u32 m0,m0,0x10000\ns_and_b32 s94,s94,0xffff\nM v[180:183],a[60:63],a[156:159],v[180:183]\nv_exp_f32 v144,v144\nM v[64:67],a[64:67],v[200:203],v[64:67]\nv_exp_f32 v145,v145\nM v[68:71],a[64:67],v[204:207],v[68:71]\nv_exp_f32 v146,v146\n" \
"M v[72:75],a[68:71],v[200:203],v[72:75]\ns_waitcnt vmcnt(16) lgkmcnt(0)\ns_barrier\nM v[76:79],a[68:71],v[204:207],v[76:79]\nds_read_b128 a[0:3],v250 offset:0\nds_read_b128 a[4:7],v250 offset:1024\nM v[80:83],a[72:75],v[200:203],v[80:83]\nv_exp_f32 v147,v147\n" \
"M v[84:87],a[72:75],v[204:207],v[84:87]\nbuffer_load_dwordx4 v240,s[48:51],s95 offen offset:0 sc1 lds\nM v[88:91],a[76:79],v[200:203],v[88:91]\nv_exp_f32 v132,v132\nM v[92:95],a[76:79],v[204:207],v[92:95]\nds_read_b128 a[8:11],v250 offset:2048\nds_read_b128 a[12:15],v250 offset:3072\nM v[96:99],a[80:83],v[200:203],v[96:99]\n" \
"v_exp_f32 v133,v133\nM v[100:103],a[80:83],v[204:207],v[100:103]\nv_exp_f32 v134,v134\nM v[104:107],a[84:87],v[200:203],v[104:107]\nv_exp_f32 v135,v135\nM v[108:111],a[84:87],v[204:207],v[108:111]\nds_read_b128 a[16:19],v250 offset:4096\nds_read_b128 a[20:23],v250 offset:5120\n" \
"M v[112:115],a[88:91],v[200:203],v[112:115]\nv_exp_f32 v148,v148\nM v[116:119],a[88:91],v[204:207],v[116:119]\nbuffer_load_dwordx4 v240,s[48:51],s95 offen offset:1024 sc1 lds\nM v[120:123],a[92:95],v[200:203],v[120:123]\nv_exp_f32 v149,v149\nM v[124:127],a[92:95],v[204:207],v[124:127]\nds_read_b128 a[24:27],v250 offset:6144\n" \
"ds_read_b128 a[28:31],v250 offset:7168\nM a[200:203],a[208:211],v[200:203],a[200:203]\nv_exp_f32 v150,v150\nM a[204:207],a[208:211],v[204:207],a[204:207]\nv_exp_f32 v151,v151\nM v[64:67],a[96:99],v[216:219],v[64:67]\nv_cvt_pk_bf16_f32 v192,v128,v129\nv_cvt_pk_bf16_f32 v193,v130,v131\n" \
"M v[68:71],a[96:99],v[220:223],v[68:71]\nds_read_b128 a[32:35],v250 offset:8192\nds_read_b128 a[36:39],v250 offset:9216\nM v[72:75],a[100:103],v[216:219],v[72:75]\nv_cvt_pk_bf16_f32 v194,v144,v145\nv_cvt_pk_bf16_f32 v195,v146,v147\nM v[76:79],a[100:103],v[220:223],v[76:79]\nbuffer_load_dwordx4 v240,s[48:51],s95 offen offset:2048 sc1 lds\n" \
"M v[80:83],a[104:107],v[216:219],v[80:83]\nv_cvt_pk_bf16_f32 v196,v132,v133\nv_cvt_pk_bf16_f32 v197,v134,v135\nM v[84:87],a[104:107],v[220:223],v[84:87]\nds_read_b128 a[40:43],v250 offset:10240\nds_read_b128 a[44:47],v250 offset:11264\nM v[88:91],a[108:111],v[216:219],v[88:91]\nv_cvt_pk_bf16_f32 v198,v148,v149\n" \
"v_cvt_pk_bf16_f32 v199,v150,v151\nM v[92:95],a[108:111],v[220:223],v[92:95]\nv_exp_f32 v160,v160\nM v[96:99],a[112:115],v[216:219],v[96:99]\nv_exp_f32 v161,v161\nM v[100:103],a[112:115],v[220:223],v[100:103]\nds_read_b128 a[48:51],v250 offset:12288\nds_read_b128 a[52:55],v250 offset:13312\n" \
"M v[104:107],a[116:119],v[216:219],v[104:107]\nv_exp_f32 v162,v162\nM v[108:111],a[116:119],v[220:223],v[108:111]\nbuffer_load_dwordx4 v240,s[48:51],s95 offen offset:3072 sc1 lds\nM v[112:115],a[120:123],v[216:219],v[112:115]\nv_exp_f32 v163,v163\nM v[116:119],a[120:123],v[220:223],v[116:119]\nds_read_b128 a[56:59],v250 offset:14336\n" \
"ds_read_b128 a[60:63],v250 offset:15360\nM v[120:123],a[124:127],v[216:219],v[120:123]\nv_exp_f32 v176,v176\nM v[124:127],a[124:127],v[220:223],v[124:127]\nv_exp_f32 v177,v177\nM a[200:203],a[208:211],v[216:219],a[200:203]\nv_exp_f32 v178,v178\nv_exp_f32 v179,v179\n" \
"v_add_u32 v250,s94,v241\ns_add_u32 s95,s95,0x4000\nM a[204:207],a[208:211],v[220:223],a[204:207]\ns_branch .Lse\n.Ltrans:\ns_waitcnt lgkmcnt(4)\nM v[128:131],a[0:3],a[128:131],v[224:227]\ns_add_u32 m0,s94,s64\n" \
"v_cvt_pk_bf16_f32 v208,v160,v161\nM v[128:131],a[4:7],a[132:135],v[128:131]\nds_read_b128 a[64:67],v250 offset:0\nds_read_b128 a[68:71],v250 offset:1024\nM v[128:131],a[8:11],a[136:139],v[128:131]\nbuffer_load_dwordx4 v240,s[44:47],s95 offen offset:0 sc1 lds\nM v[128:131],a[12:15],a[140:143],v[128:131]\nv_cvt_pk_bf16_f32 v209,v162,v163\n" \
"v_cvt_pk_bf16_f32 v212,v164,v165\nM v[132:135],a[0:3],a[144:147],v[228:231]\nds_read_b128 a[72:75],v250 offset:2048\nds_read_b128 a[76:79],v250 offset:3072\nM v[132:135],a[4:7],a[148:151],v[132:135]\nbuffer_store_dwordx4 a[248:251],v253,s[52:55],s33 offen offset:0 sc1\nM v[132:135],a[8:11],a[152:155],v[132:135]\nv_cvt_pk_bf16_f32 v213,v166,v167\n" \
"v_cvt_pk_bf16_f32 v210,v176,v177\nM v[132:135],a[12:15],a[156:159],v[132:135]\nds_read_b128 a[80:83],v250 offset:4096\nds_read_b128 a[84:87],v250 offset:5120\nM v[144:147],a[16:19],a[128:131],v[224:227]\nv_cvt_pk_bf16_f32 v211,v178,v179\nv_cvt_pk_bf16_f32 v214,v180,v181\nM v[144:147],a[20:23],a[132:135],v[144:147]\n" \
"v_cvt_pk_bf16_f32 v215,v182,v183\ns_add_u32 s71,s33,16384\nM v[144:147],a[24:27],a[136:139],v[144:147]\nds_read_b128 a[88:91],v250 offset:6144\nds_read_b128 a[92:95],v250 offset:7168\nM v[144:147],a[28:31],a[140:143],v[144:147]\nbuffer_load_dwordx4 v240,s[44:47],s95 offen offset:1024 sc1 lds\nM v[148:151],a[16:19],a[144:147],v[228:231]\n" \
"v_exp_f32 v184,v184\nM v[148:151],a[20:23],a[148:151],v[148:151]\nds_read_b128 a[96:99],v250 offset:8192\nds_read_b128 a[100:103],v250 offset:9216\nM v[148:151],a[24:27],a[152:155],v[148:151]\nbuffer_store_dwordx4 a[248:251],v253,s[52:55],s33 offen offset:1024 sc1\nM v[148:151],a[28:31],a[156:159],v[148:151]\nv_exp_f32 v185,v185\n" \
"M v[160:163],a[32:35],a[128:131],v[224:227]\nds_read_b128 a[104:107],v250 offset:10240\nds_read_b128 a[108:111],v250 offset:11264\nM v[160:163],a[36:39],a[132:135],v[160:163]\nv_exp_f32 v186,v186\nM v[160:163],a[40:43],a[136:139],v[160:163]\nv_exp_f32 v187,v187\nM v[160:163],a[44:47],a[140:143],v[160:163]\n" \
"ds_read_b128 a[112:115],v250 offset:12288\nds_read_b128 a[116:119],v250 offset:13312\nM v[164:167],a[32:35],a[144:147],v[228:231]\nbuffer_load_dwordx4 v240,s[44:47],s95 offen offset:2048 sc1 lds\nM v[164:167],a[36:39],a[148:151],v[164:167]\nv_exp_f32 v172,v172\nM v[164:167],a[40:43],a[152:155],v[164:167]\nds_read_b128 a[120:123],v250 offset:14336\n" \
"ds_read_b128 a[124:127],v250 offset:15360\nM v[164:167],a[44:47],a[156:159],v[164:167]\nbuffer_store_dwordx4 a[248:251],v253,s[52:55],s33 offen offset:2048 sc1\ns_waitcnt lgkmcnt(6)\nM v[176:179],a[48:51],a[128:131],v[224:227]\nv_xor_b32 v250,0x18000,v250\nv_cvt_pk_bf16_f32 v216,v168,v169\nM v[176:179],a[52:55],a[132:135],v[176:179]\n" \
"v_exp_f32 v173,v173\nM v[176:179],a[56:59],a[136:139],v[176:179]\nv_exp_f32 v174,v174\nM v[176:179],a[60:63],a[140:143],v[176:179]\nv_exp_f32 v175,v175\nM v[180:183],a[48:51],a[144:147],v[228:231]\nv_exp_f32 v188,v188\nM v[180:183],a[52:55],a[148:151],v[180:183]\n" \
"buffer_load_dwordx4 v240,s[44:47],s95 offen offset:3072 sc1 lds\nM v[180:183],a[56:59],a[152:155],v[180:183]\ns_add_u32 m0,m0,0x10000\nv_cvt_pk_bf16_f32 v217,v170,v171\nM v[180:183],a[60:63],a[156:159],v[180:183]\nv_exp_f32 v189,v189\nM v[0:3],a[64:67],v[192:195],v[0:3]\nbuffer_store_dwordx4 a[248:251],v253,s[52:55],s33 offen offset:3072 sc1\n" \
"M v[4:7],a[64:67],v[196:199],v[4:7]\nv_exp_f32 v190,v190\nM v[64:67],a[64:67],v[200:203],v[64:67]\ns_waitcnt vmcnt(16) lgkmcnt(0)\ns_barrier\nM v[68:71],a[64:67],v[204:207],v[68:71]\nds_read_b128 a[0:3],v250 offset:0\nds_read_b128 a[4:7],v250 offset:1024\n" \
"M v[8:11],a[68:71],v[192:195],v[8:11]\nv_exp_f32 v191,v191\nM v[12:15],a[68:71],v[196:199],v[12:15]\nbuffer_store_dwordx4 a[248:251],v253,s[52:55],s71 offen offset:0 sc1\nM v[72:75],a[68:71],v[200:203],v[72:75]\nv_cvt_pk_bf16_f32 v218,v184,v185\nv_cvt_pk_bf16_f32 v219,v186,v187\nM v[76:79],a[68:71],v[204:207],v[76:79]\n" \
"buffer_load_dwordx4 v240,s[48:51],s95 offen offset:0 sc1 lds\nM v[16:19],a[72:75],v[192:195],v[16:19]\nv_cvt_pk_bf16_f32 v220,v172,v173\nv_cvt_pk_bf16_f32 v221,v174,v175\nM v[20:23],a[72:75],v[196:199],v[20:23]\nbuffer_store_dwordx4 a[248:251],v253,s[52:55],s71 offen offset:1024 sc1\nM v[80:83],a[72:75],v[200:203],v[80:83]\nv_cvt_pk_bf16_f32 v222,v188,v189\n" \
"v_cvt_pk_bf16_f32 v223,v190,v191\nM v[84:87],a[72:75],v[204:207],v[84:87]\nds_read_b128 a[8:11],v250 offset:2048\nds_read_b128 a[12:15],v250 offset:3072\nM v[24:27],a[76:79],v[192:195],v[24:27]\ns_add_u32 s70,s78,16384\ns_add_u32 s94,s94,0x4000\nM v[28:31],a[76:79],v[196:199],v[28:31]\n" \
"buffer_store_dwordx4 a[248:251],v253,s[52:55],s71 offen offset:2048 sc1\nM v[88:91],a[76:79],v[200:203],v[88:91]\nv_exp_f32 v128,v128\nM v[92:95],a[76:79],v[204:207],v[92:95]\nv_exp_f32 v129,v129\nM v[32:35],a[80:83],v[192:195],v[32:35]\nv_exp_f32 v130,v130\nM v[36:39],a[80:83],v[196:199],v[36:39]\n" \
"buffer_store_dwordx4 a[248:251],v253,s[52:55],s71 offen offset:3072 sc1\nM v[96:99],a[80:83],v[200:203],v[96:99]\nv_exp_f32 v131,v131\nM v[100:103],a[80:83],v[204:207],v[100:103]\nds_read_b128 a[16:19],v250 offset:4096\nds_read_b128 a[20:23],v250 offset:5120\nM v[40:43],a[84:87],v[192:195],v[40:43]\nv_exp_f32 v144,v144\n" \
"M v[44:47],a[84:87],v[196:199],v[44:47]\nv_exp_f32 v145,v145\nM v[104:107],a[84:87],v[200:203],v[104:107]\nv_exp_f32 v146,v146\nM v[108:111],a[84:87],v[204:207],v[108:111]\nbuffer_load_dwordx4 v240,s[48:51],s95 offen offset:1024 sc1 lds\nM v[48:51],a[88:91],v[192:195],v[48:51]\nv_exp_f32 v147,v147\n" \
"M v[52:55],a[88:91],v[196:199],v[52:55]\nbuffer_store_dwordx4 a[248:251],v253,s[52:55],s78 offen offset:0 sc1\nM v[112:115],a[88:91],v[200:203],v[112:115]\nv_exp_f32 v132,v132\nM v[116:119],a[88:91],v[204:207],v[116:119]\nds_read_b128 a[24:27],v250 offset:6144\nds_read_b128 a[28:31],v250 offset:7168\nM v[56:59],a[92:95],v[192:195],v[56:59]\n" \
"v_exp_f32 v133,v133\nM v[60:63],a[92:95],v[196:199],v[60:63]\nbuffer_store_dwordx4 a[248:251],v253,s[52:55],s78 offen offset:1024 sc1\nM v[120:123],a[92:95],v[200:203],v[120:123]\nv_exp_f32 v134,v134\nM v[124:127],a[92:95],v[204:207],v[124:127]\nv_exp_f32 v135,v135\nM a[192:195],a[208:211],v[192:195],a[192:195]\n" \
"v_exp_f32 v148,v148\nM a[196:199],a[208:211],v[196:199],a[196:199]\nbuffer_store_dwordx4 a[248:251],v253,s[52:55],s78 offen offset:2048 sc1\nM a[200:203],a[208:211],v[200:203],a[200:203]\nv_exp_f32 v149,v149\nM a[204:207],a[208:211],v[204:207],a[204:207]\nds_read_b128 a[32:35],v250 offset:8192\nds_read_b128 a[36:39],v250 offset:9216\n" \
"M v[0:3],a[96:99],v[208:211],v[0:3]\nv_exp_f32 v150,v150\nM v[4:7],a[96:99],v[212:215],v[4:7]\nv_exp_f32 v151,v151\nM v[64:67],a[96:99],v[216:219],v[64:67]\ns_and_b32 s94,s94,0xffff\nv_cvt_pk_bf16_f32 v192,v128,v129\nM v[68:71],a[96:99],v[220:223],v[68:71]\n" \
"buffer_load_dwordx4 v240,s[48:51],s95 offen offset:2048 sc1 lds\nM v[8:11],a[100:103],v[208:211],v[8:11]\nv_cvt_pk_bf16_f32 v193,v130,v131\nv_cvt_pk_bf16_f32 v194,v144,v145\nM v[12:15],a[100:103],v[212:215],v[12:15]\nbuffer_store_dwordx4 a[248:251],v253,s[52:55],s78 offen offset:3072 sc1\nM v[72:75],a[100:103],v[216:219],v[72:75]\nv_cvt_pk_bf16_f32 v195,v146,v147\n" \
"v_cvt_pk_bf16_f32 v196,v132,v133\nM v[76:79],a[100:103],v[220:223],v[76:79]\nds_read_b128 a[40:43],v250 offset:10240\nds_read_b128 a[44:47],v250 offset:11264\nM v[16:19],a[104:107],v[208:211],v[16:19]\nv_cvt_pk_bf16_f32 v197,v134,v135\nv_cvt_pk_bf16_f32 v198,v148,v149\nM v[20:23],a[104:107],v[212:215],v[20:23]\n" \
"buffer_store_dwordx4 a[248:251],v253,s[52:55],s70 offen offset:0 sc1\nM v[80:83],a[104:107],v[216:219],v[80:83]\nv_exp_f32 v160,v160\nM v[84:87],a[104:107],v[220:223],v[84:87]\nv_exp_f32 v161,v161\nM v[24:27],a[108:111],v[208:211],v[24:27]\nv_exp_f32 v162,v162\nM v[28:31],a[108:111],v[212:215],v[28:31]\n" \
"buffer_store_dwordx4 a[248:251],v253,s[52:55],s70 offen offset:1024 sc1\nM v[88:91],a[108:111],v[216:219],v[88:91]\nv_exp_f32 v163,v163\nM v[92:95],a[108:111],v[220:223],v[92:95]\nds_read_b128 a[48:51],v250 offset:12288\nds_read_b128 a[52:55],v250 offset:13312\nM v[32:35],a[112:115],v[208:211],v[32:35]\nv_exp_f32 v176,v176\n" \
"M v[36:39],a[112:115],v[212:215],v[36:39]\nv_exp_f32 v177,v177\nM v[96:99],a[112:115],v[216:219],v[96:99]\nv_exp_f32 v178,v178\nM v[100:103],a[112:115],v[220:223],v[100:103]\nbuffer_load_dwordx4 v240,s[48:51],s95 offen offset:3072 sc1 lds\nM v[40:43],a[116:119],v[208:211],v[40:43]\nv_cvt_pk_bf16_f32 v199,v150,v151\n" \
"s_add_u32 s95,s95,0x4000\nM v[44:47],a[116:119],v[212:215],v[44:47]\nbuffer_store_dwordx4 a[248:251],v253,s[52:55],s70 offen offset:2048 sc1\nM v[104:107],a[116:119],v[216:219],v[104:107]\nv_exp_f32 v179,v179\nM v[108:111],a[116:119],v[220:223],v[108:111]\nds_read_b128 a[56:59],v250 offset:14336\nds_read_b128 a[60:63],v250 offset:15360\n" \
"M v[48:51],a[120:123],v[208:211],v[48:51]\nv_add_u32 v250,s94,v241\nM v[52:55],a[120:123],v[212:215],v[52:55]\nbuffer_store_dwordx4 a[248:251],v253,s[52:55],s70 offen offset:3072 sc1\nM v[112:115],a[120:123],v[216:219],v[112:115]\nM v[116:119],a[120:123],v[220:223],v[116:119]\nM v[56:59],a[124:127],v[208:211],v[56:59]\nM v[60:63],a[124:127],v[212:215],v[60:63]\n" \
"M v[120:123],a[124:127],v[216:219],v[120:123]\nM v[124:127],a[124:127],v[220:223],v[124:127]\nM a[192:195],a[208:211],v[208:211],a[192:195]\nM a[196:199],a[208:211],v[212:215],a[196:199]\nM a[200:203],a[208:211],v[216:219],a[200:203]\nM a[204:207],a[208:211],v[220:223],a[204:207]\n.Lse:\ns_waitcnt lgkmcnt(4)\n" \
"M v[128:131],a[0:3],a[128:131],v[224:227]\ns_add_u32 m0,s94,s64\ns_add_u32 s37,s12,s67\ns_add_u32 s37,s37,1048576\nv_cvt_pk_bf16_f32 v208,v160,v161\nM v[128:131],a[4:7],a[132:135],v[128:131]\nds_read_b128 a[64:67],v250 offset:0\nds_read_b128 a[68:71],v250 offset:1024\n" \
"v_accvgpr_read_b32 v232,a200\nv_accvgpr_read_b32 v236,a204\nM v[128:131],a[8:11],a[136:139],v[128:131]\nbuffer_load_dwordx4 v240,s[44:47],s95 offen offset:0 sc1 lds\nv_add_f32 v233,v232,v236\nv_cvt_pk_bf16_f32 v209,v162,v163\nM v[128:131],a[12:15],a[140:143],v[128:131]\nv_rcp_f32 v232,v232\n" \
"v_rcp_f32 v236,v236\nM v[132:135],a[0:3],a[144:147],v[228:231]\nds_read_b128 a[72:75],v250 offset:2048\nds_read_b128 a[76:79],v250 offset:3072\nv_add_f32 v233,v233,v232\nv_add_f32 v233,v233,v236\nM v[132:135],a[4:7],a[148:151],v[132:135]\nv_cmp_ngt_f32 vcc,s28,v233\n" \
"v_mul_f32 v64,v64,v232\nv_mul_f32 v65,v65,v232\nv_mul_f32 v66,v66,v232\nv_mul_f32 v67,v67,v232\nM v[132:135],a[8:11],a[152:155],v[132:135]\ns_and_b64 vcc,vcc,s[90:91]\ns_cselect_b32 s39,0x8000000,s16\ns_cselect_b32 s16,s16,0x8000000\n" \
"s_cselect_b32 s71,2,1\ns_addc_u32 s99,s99,0\nM v[132:135],a[12:15],a[156:159],v[132:135]\nds_read_b128 a[80:83],v250 offset:4096\nds_read_b128 a[84:87],v250 offset:5120\nv_cvt_pk_bf16_f32 v64,v64,v65\nv_mul_f32 v72,v72,v232\nM v[144:147],a[16:19],a[128:131],v[224:227]\n" \
"v_mul_f32 v73,v73,v232\nv_mul_f32 v74,v74,v232\nv_mul_f32 v75,v75,v232\nv_cvt_pk_bf16_f32 v65,v66,v67\nM v[144:147],a[20:23],a[132:135],v[144:147]\ns_or_b32 s37,s37,s39\nv_cvt_pk_bf16_f32 v66,v72,v73\nv_cvt_pk_bf16_f32 v67,v74,v75\n" \
"buffer_store_dwordx4 v[64:67],v240,s[52:55],s37 offen offset:0 sc1\nM v[144:147],a[24:27],a[136:139],v[144:147]\nds_read_b128 a[88:91],v250 offset:6144\nds_read_b128 a[92:95],v250 offset:7168\nv_mul_f32 v68,v68,v236\nv_mul_f32 v69,v69,v236\nv_mul_f32 v70,v70,v236\nM v[144:147],a[28:31],a[140:143],v[144:147]\n" \
"buffer_load_dwordx4 v240,s[44:47],s95 offen offset:1024 sc1 lds\nv_mul_f32 v71,v71,v236\nv_cvt_pk_bf16_f32 v68,v68,v69\nM v[148:151],a[16:19],a[144:147],v[228:231]\nv_mul_f32 v76,v76,v236\nv_mul_f32 v77,v77,v236\nv_mul_f32 v78,v78,v236\nv_mul_f32 v79,v79,v236\n" \
"M v[148:151],a[20:23],a[148:151],v[148:151]\nds_read_b128 a[96:99],v250 offset:8192\nds_read_b128 a[100:103],v250 offset:9216\nv_cvt_pk_bf16_f32 v69,v70,v71\nv_cvt_pk_bf16_f32 v70,v76,v77\nM v[148:151],a[24:27],a[152:155],v[148:151]\nv_cvt_pk_bf16_f32 v71,v78,v79\nv_exp_f32 v164,v164\n" \
"v_mul_f32 v80,v80,v232\nM v[148:151],a[28:31],a[156:159],v[148:151]\nv_exp_f32 v165,v165\nv_exp_f32 v166,v166\nM v[160:163],a[32:35],a[128:131],v[224:227]\nds_read_b128 a[104:107],v250 offset:10240\nds_read_b128 a[108:111],v250 offset:11264\nv_exp_f32 v167,v167\n" \
"v_cvt_pk_bf16_f32 v212,v164,v165\nM v[160:163],a[36:39],a[132:135],v[160:163]\nbuffer_store_dwordx4 v[68:71],v240,s[52:55],s37 offen offset:1024 sc1\nv_cvt_pk_bf16_f32 v213,v166,v167\nv_mul_f32 v81,v81,v232\nM v[160:163],a[40:43],a[136:139],v[160:163]\nv_cvt_pk_bf16_f32 v210,v176,v177\nv_cvt_pk_bf16_f32 v211,v178,v179\n" \
"v_mul_f32 v82,v82,v232\nv_mul_f32 v83,v83,v232\nM v[160:163],a[44:47],a[140:143],v[160:163]\nds_read_b128 a[112:115],v250 offset:12288\nds_read_b128 a[116:119],v250 offset:13312\nv_cvt_pk_bf16_f32 v80,v80,v81\nv_mul_f32 v88,v88,v232\nM v[164:167],a[32:35],a[144:147],v[228:231]\n" \
"buffer_load_dwordx4 v240,s[44:47],s95 offen offset:2048 sc1 lds\nv_mul_f32 v89,v89,v232\nv_mul_f32 v90,v90,v232\nM v[164:167],a[36:39],a[148:151],v[164:167]\nv_mul_f32 v91,v91,v232\nv_cvt_pk_bf16_f32 v81,v82,v83\nv_cvt_pk_bf16_f32 v82,v88,v89\nv_cvt_pk_bf16_f32 v83,v90,v91\n" \
"v_mul_f32 v84,v84,v236\nM v[164:167],a[40:43],a[152:155],v[164:167]\nds_read_b128 a[120:123],v250 offset:14336\nds_read_b128 a[124:127],v250 offset:15360\nv_exp_f32 v180,v180\nM v[164:167],a[44:47],a[156:159],v[164:167]\nv_exp_f32 v181,v181\nv_mul_f32 v85,v85,v236\n" \
"s_waitcnt lgkmcnt(4)\nM v[176:179],a[48:51],a[128:131],v[224:227]\nv_exp_f32 v182,v182\nv_exp_f32 v183,v183\nM v[176:179],a[52:55],a[132:135],v[176:179]\nbuffer_store_dwordx4 v[80:83],v240,s[52:55],s37 offen offset:2048 sc1\nv_cvt_pk_bf16_f32 v214,v180,v181\nv_cvt_pk_bf16_f32 v215,v182,v183\n" \
"M v[176:179],a[56:59],a[136:139],v[176:179]\nv_mul_f32 v86,v86,v236\nv_mul_f32 v87,v87,v236\nv_cvt_pk_bf16_f32 v84,v84,v85\nv_mul_f32 v92,v92,v236\nM v[176:179],a[60:63],a[140:143],v[176:179]\nv_mul_f32 v93,v93,v236\nv_mul_f32 v94,v94,v236\n" \
"v_mul_f32 v95,v95,v236\nv_cvt_pk_bf16_f32 v85,v86,v87\nv_cvt_pk_bf16_f32 v86,v92,v93\nM v[180:183],a[48:51],a[144:147],v[228:231]\nv_cvt_pk_bf16_f32 v87,v94,v95\nv_xor_b32 v250,0x18000,v250\nv_mul_f32 v96,v96,v232\nv_mul_f32 v97,v97,v232\n" \
"M v[180:183],a[52:55],a[148:151],v[180:183]\nbuffer_load_dwordx4 v240,s[44:47],s95 offen offset:3072 sc1 lds\ns_add_u32 m0,m0,0x10000\nv_mul_f32 v98,v98,v232\nM v[180:183],a[56:59],a[152:155],v[180:183]\nv_mul_f32 v99,v99,v232\nv_cvt_pk_bf16_f32 v96,v96,v97\nv_mul_f32 v104,v104,v232\n" \
"v_mul_f32 v105,v105,v232\nM v[180:183],a[60:63],a[156:159],v[180:183]\nv_mul_f32 v106,v106,v232\nv_mul_f32 v107,v107,v232\nv_cvt_pk_bf16_f32 v97,v98,v99\ns_add_u32 s38,s37,4096\nM v[0:3],a[64:67],v[192:195],v[0:3]\nv_cvt_pk_bf16_f32 v98,v104,v105\n" \
"v_cvt_pk_bf16_f32 v99,v106,v107\nv_mul_f32 v100,v100,v236\nv_mul_f32 v101,v101,v236\nv_mul_f32 v102,v102,v236\nM v[4:7],a[64:67],v[196:199],v[4:7]\nbuffer_store_dwordx4 v[84:87],v240,s[52:55],s37 offen offset:3072 sc1\nv_mul_f32 v103,v103,v236\nv_cvt_pk_bf16_f32 v100,v100,v101\n" \
"M v[8:11],a[68:71],v[192:195],v[8:11]\ns_waitcnt vmcnt(16) lgkmcnt(0)\ns_barrier\nv_mul_f32 v108,v108,v236\nv_mul_f32 v109,v109,v236\nM v[12:15],a[68:71],v[196:199],v[12:15]\nds_read_b128 a[0:3],v250 offset:0\nds_read_b128 a[4:7],v250 offset:1024\n" \
"v_mul_f32 v110,v110,v236\nv_mul_f32 v111,v111,v236\nM v[16:19],a[72:75],v[192:195],v[16:19]\nv_cvt_pk_bf16_f32 v101,v102,v103\nv_cvt_pk_bf16_f32 v102,v108,v109\nv_cvt_pk_bf16_f32 v103,v110,v111\nv_mul_f32 v112,v112,v232\nM v[20:23],a[72:75],v[196:199],v[20:23]\n" \
"buffer_load_dwordx4 v240,s[48:51],s95 offen offset:0 sc1 lds\nv_mul_f32 v113,v113,v232\nv_mul_f32 v114,v114,v232\nv_mul_f32 v115,v115,v232\nM v[24:27],a[76:79],v[192:195],v[24:27]\nv_cvt_pk_bf16_f32 v112,v112,v113\nv_mul_f32 v120,v120,v232\nv_mul_f32 v121,v121,v232\n" \
"v_mul_f32 v122,v122,v232\nM v[28:31],a[76:79],v[196:199],v[28:31]\nds_read_b128 a[8:11],v250 offset:2048\nds_read_b128 a[12:15],v250 offset:3072\nv_mul_f32 v123,v123,v232\nv_cvt_pk_bf16_f32 v113,v114,v115\nM v[32:35],a[80:83],v[192:195],v[32:35]\nv_cvt_pk_bf16_f32 v114,v120,v121\n" \
"v_cvt_pk_bf16_f32 v115,v122,v123\nv_mul_f32 v116,v116,v236\nv_mul_f32 v117,v117,v236\nM v[36:39],a[80:83],v[196:199],v[36:39]\nbuffer_store_dwordx4 v[96:99],v240,s[52:55],s38 offen offset:0 sc1\nv_mul_f32 v118,v118,v236\nv_mul_f32 v119,v119,v236\nM v[40:43],a[84:87],v[192:195],v[40:43]\n" \
"v_cvt_pk_bf16_f32 v116,v116,v117\nv_mul_f32 v124,v124,v236\nv_mul_f32 v125,v125,v236\nv_mul_f32 v126,v126,v236\nM v[44:47],a[84:87],v[196:199],v[44:47]\nds_read_b128 a[16:19],v250 offset:4096\nds_read_b128 a[20:23],v250 offset:5120\nv_mul_f32 v127,v127,v236\n" \
"v_cvt_pk_bf16_f32 v117,v118,v119\nv_cvt_pk_bf16_f32 v118,v124,v125\nM v[48:51],a[88:91],v[192:195],v[48:51]\nv_cvt_pk_bf16_f32 v119,v126,v127\nv_exp_f32 v128,v128\ns_add_u32 s94,s94,0x4000\nM v[52:55],a[88:91],v[196:199],v[52:55]\nbuffer_load_dwordx4 v240,s[48:51],s95 offen offset:1024 sc1 lds\n" \
"v_exp_f32 v129,v129\nM v[56:59],a[92:95],v[192:195],v[56:59]\nv_exp_f32 v130,v130\nv_exp_f32 v131,v131\nM v[60:63],a[92:95],v[196:199],v[60:63]\nds_read_b128 a[24:27],v250 offset:6144\nds_read_b128 a[28:31],v250 offset:7168\nv_exp_f32 v144,v144\n" \
"M a[192:195],a[208:211],v[192:195],a[192:195]\nv_exp_f32 v145,v145\nv_exp_f32 v146,v146\ns_lshl2_add_u32 s77,s61,s79\nM a[196:199],a[208:211],v[196:199],a[196:199]\nbuffer_store_dwordx4 v[100:103],v240,s[52:55],s38 offen offset:1024 sc1\nv_exp_f32 v147,v147\nM v[0:3],a[96:99],v[208:211],v[0:3]\n" \
"v_exp_f32 v132,v132\nv_exp_f32 v133,v133\nM v[4:7],a[96:99],v[212:215],v[4:7]\nds_read_b128 a[32:35],v250 offset:8192\nds_read_b128 a[36:39],v250 offset:9216\nv_exp_f32 v134,v134\nM v[8:11],a[100:103],v[208:211],v[8:11]\nv_exp_f32 v135,v135\n" \
"v_exp_f32 v148,v148\nM v[12:15],a[100:103],v[212:215],v[12:15]\nbuffer_load_dwordx4 v240,s[48:51],s95 offen offset:2048 sc1 lds\nv_exp_f32 v149,v149\nM v[16:19],a[104:107],v[208:211],v[16:19]\nv_exp_f32 v150,v150\nv_exp_f32 v151,v151\ns_and_b32 s94,s94,0xffff\n" \
"M v[20:23],a[104:107],v[212:215],v[20:23]\nds_read_b128 a[40:43],v250 offset:10240\nds_read_b128 a[44:47],v250 offset:11264\ns_add_u32 s77,s77,4096\nv_mov_b32 v233,s71\nM v[24:27],a[108:111],v[208:211],v[24:27]\nv_cvt_pk_bf16_f32 v192,v128,v129\nv_cvt_pk_bf16_f32 v193,v130,v131\n" \
"v_cvt_pk_bf16_f32 v194,v144,v145\nv_cvt_pk_bf16_f32 v195,v146,v147\nM v[28:31],a[108:111],v[212:215],v[28:31]\nbuffer_store_dwordx4 v[112:115],v240,s[52:55],s38 offen offset:2048 sc1\nv_cvt_pk_bf16_f32 v196,v132,v133\nv_cvt_pk_bf16_f32 v197,v134,v135\nM v[32:35],a[112:115],v[208:211],v[32:35]\nv_cvt_pk_bf16_f32 v198,v148,v149\n" \
"v_cvt_pk_bf16_f32 v199,v150,v151\nv_exp_f32 v160,v160\nM v[36:39],a[112:115],v[212:215],v[36:39]\nds_read_b128 a[48:51],v250 offset:12288\nds_read_b128 a[52:55],v250 offset:13312\nv_exp_f32 v161,v161\nM v[40:43],a[116:119],v[208:211],v[40:43]\nv_exp_f32 v162,v162\n" \
"v_exp_f32 v163,v163\nM v[44:47],a[116:119],v[212:215],v[44:47]\nbuffer_load_dwordx4 v240,s[48:51],s95 offen offset:3072 sc1 lds\nv_exp_f32 v176,v176\nM v[48:51],a[120:123],v[208:211],v[48:51]\nv_exp_f32 v177,v177\nv_exp_f32 v178,v178\nM v[52:55],a[120:123],v[212:215],v[52:55]\n" \
"ds_read_b128 a[56:59],v250 offset:14336\nds_read_b128 a[60:63],v250 offset:15360\nv_exp_f32 v179,v179\nM v[56:59],a[124:127],v[208:211],v[56:59]\nv_add_u32 v250,s94,v241\ns_add_u32 s95,s95,0x4000\nM v[60:63],a[124:127],v[212:215],v[60:63]\nbuffer_store_dwordx4 v[116:119],v240,s[52:55],s38 offen offset:3072 sc1\n" \
"M a[192:195],a[208:211],v[208:211],a[192:195]\nM a[196:199],a[208:211],v[212:215],a[196:199]\ns_cmp_ge_u32 s95,s15\ns_cbranch_scc1 .Lsx\n.Lsingle:\ns_waitcnt lgkmcnt(4)\nM v[128:131],a[0:3],a[128:131],v[224:227]\ns_add_u32 m0,s94,s64\n" \
"v_cvt_pk_bf16_f32 v208,v160,v161\nM v[128:131],a[4:7],a[132:135],v[128:131]\nds_read_b128 a[64:67],v250 offset:0\nds_read_b128 a[68:71],v250 offset:1024\nM v[128:131],a[8:11],a[136:139],v[128:131]\nbuffer_load_dwordx4 v240,s[44:47],s95 offen offset:0 sc1 lds\nM v[128:131],a[12:15],a[140:143],v[128:131]\nv_cvt_pk_bf16_f32 v209,v162,v163\n" \
"v_cvt_pk_bf16_f32 v210,v176,v177\nM v[132:135],a[0:3],a[144:147],v[228:231]\nds_read_b128 a[72:75],v250 offset:2048\nds_read_b128 a[76:79],v250 offset:3072\nM v[132:135],a[4:7],a[148:151],v[132:135]\nv_exp_f32 v164,v164\nM v[132:135],a[8:11],a[152:155],v[132:135]\nv_exp_f32 v165,v165\n" \
"M v[132:135],a[12:15],a[156:159],v[132:135]\nds_read_b128 a[80:83],v250 offset:4096\nds_read_b128 a[84:87],v250 offset:5120\nM v[144:147],a[16:19],a[128:131],v[224:227]\nv_exp_f32 v166,v166\nM v[144:147],a[20:23],a[132:135],v[144:147]\nv_exp_f32 v167,v167\nM v[144:147],a[24:27],a[136:139],v[144:147]\n" \
"ds_read_b128 a[88:91],v250 offset:6144\nds_read_b128 a[92:95],v250 offset:7168\nM v[144:147],a[28:31],a[140:143],v[144:147]\nbuffer_load_dwordx4 v240,s[44:47],s95 offen offset:1024 sc1 lds\nM v[148:151],a[16:19],a[144:147],v[228:231]\nv_cvt_pk_bf16_f32 v212,v164,v165\nv_cvt_pk_bf16_f32 v213,v166,v167\nM v[148:151],a[20:23],a[148:151],v[148:151]\n" \
"ds_read_b128 a[96:99],v250 offset:8192\nds_read_b128 a[100:103],v250 offset:9216\nM v[148:151],a[24:27],a[152:155],v[148:151]\nv_cvt_pk_bf16_f32 v211,v178,v179\ns_add_u32 s94,s94,0x4000\nM v[148:151],a[28:31],a[156:159],v[148:151]\nv_exp_f32 v180,v180\nM v[160:163],a[32:35],a[128:131],v[224:227]\n" \
"ds_read_b128 a[104:107],v250 offset:10240\nds_read_b128 a[108:111],v250 offset:11264\nM v[160:163],a[36:39],a[132:135],v[160:163]\nv_exp_f32 v181,v181\nM v[160:163],a[40:43],a[136:139],v[160:163]\nv_exp_f32 v182,v182\nM v[160:163],a[44:47],a[140:143],v[160:163]\nds_read_b128 a[112:115],v250 offset:12288\n" \
"ds_read_b128 a[116:119],v250 offset:13312\nM v[164:167],a[32:35],a[144:147],v[228:231]\nbuffer_load_dwordx4 v240,s[44:47],s95 offen offset:2048 sc1 lds\nM v[164:167],a[36:39],a[148:151],v[164:167]\nv_exp_f32 v183,v183\nM v[164:167],a[40:43],a[152:155],v[164:167]\nds_read_b128 a[120:123],v250 offset:14336\nds_read_b128 a[124:127],v250 offset:15360\n" \
"M v[164:167],a[44:47],a[156:159],v[164:167]\nv_cvt_pk_bf16_f32 v214,v180,v181\ns_waitcnt lgkmcnt(7)\nM v[176:179],a[48:51],a[128:131],v[224:227]\nv_cvt_pk_bf16_f32 v215,v182,v183\nv_xor_b32 v250,0x18000,v250\nM v[176:179],a[52:55],a[132:135],v[176:179]\nv_exp_f32 v128,v128\n" \
"M v[176:179],a[56:59],a[136:139],v[176:179]\nv_exp_f32 v129,v129\nM v[176:179],a[60:63],a[140:143],v[176:179]\nv_exp_f32 v130,v130\nM v[180:183],a[48:51],a[144:147],v[228:231]\nv_exp_f32 v131,v131\nM v[180:183],a[52:55],a[148:151],v[180:183]\nbuffer_load_dwordx4 v240,s[44:47],s95 offen offset:3072 sc1 lds\n" \
"M v[180:183],a[56:59],a[152:155],v[180:183]\ns_add_u32 m0,m0,0x10000\ns_and_b32 s94,s94,0xffff\nM v[180:183],a[60:63],a[156:159],v[180:183]\nv_exp_f32 v144,v144\nM v[0:3],a[64:67],v[192:195],v[0:3]\nv_exp_f32 v145,v145\nM v[4:7],a[64:67],v[196:199],v[4:7]\n" \
"v_exp_f32 v146,v146\nM v[8:11],a[68:71],v[192:195],v[8:11]\ns_waitcnt vmcnt(16) lgkmcnt(0)\ns_barrier\nM v[12:15],a[68:71],v[196:199],v[12:15]\nds_read_b128 a[0:3],v250 offset:0\nds_read_b128 a[4:7],v250 offset:1024\nM v[16:19],a[72:75],v[192:195],v[16:19]\n" \
"v_exp_f32 v147,v147\nM v[20:23],a[72:75],v[196:199],v[20:23]\nbuffer_load_dwordx4 v240,s[48:51],s95 offen offset:0 sc1 lds\nM v[24:27],a[76:79],v[192:195],v[24:27]\nv_exp_f32 v132,v132\nM v[28:31],a[76:79],v[196:199],v[28:31]\nds_read_b128 a[8:11],v250 offset:2048\nds_read_b128 a[12:15],v250 offset:3072\n" \
"M v[32:35],a[80:83],v[192:195],v[32:35]\nv_exp_f32 v133,v133\nM v[36:39],a[80:83],v[196:199],v[36:39]\nv_exp_f32 v134,v134\nM v[40:43],a[84:87],v[192:195],v[40:43]\nv_exp_f32 v135,v135\nM v[44:47],a[84:87],v[196:199],v[44:47]\nds_read_b128 a[16:19],v250 offset:4096\n" \
"ds_read_b128 a[20:23],v250 offset:5120\nM v[48:51],a[88:91],v[192:195],v[48:51]\nv_exp_f32 v148,v148\nM v[52:55],a[88:91],v[196:199],v[52:55]\nbuffer_load_dwordx4 v240,s[48:51],s95 offen offset:1024 sc1 lds\nM v[56:59],a[92:95],v[192:195],v[56:59]\nv_exp_f32 v149,v149\nM v[60:63],a[92:95],v[196:199],v[60:63]\n" \
"ds_read_b128 a[24:27],v250 offset:6144\nds_read_b128 a[28:31],v250 offset:7168\nM a[192:195],a[208:211],v[192:195],a[192:195]\nv_exp_f32 v150,v150\nM a[196:199],a[208:211],v[196:199],a[196:199]\nv_exp_f32 v151,v151\nM v[0:3],a[96:99],v[208:211],v[0:3]\nv_cvt_pk_bf16_f32 v192,v128,v129\n" \
"v_cvt_pk_bf16_f32 v193,v130,v131\nM v[4:7],a[96:99],v[212:215],v[4:7]\nds_read_b128 a[32:35],v250 offset:8192\nds_read_b128 a[36:39],v250 offset:9216\nM v[8:11],a[100:103],v[208:211],v[8:11]\nv_cvt_pk_bf16_f32 v194,v144,v145\nv_cvt_pk_bf16_f32 v195,v146,v147\nM v[12:15],a[100:103],v[212:215],v[12:15]\n" \
"buffer_load_dwordx4 v240,s[48:51],s95 offen offset:2048 sc1 lds\nM v[16:19],a[104:107],v[208:211],v[16:19]\nv_cvt_pk_bf16_f32 v196,v132,v133\nv_cvt_pk_bf16_f32 v197,v134,v135\nM v[20:23],a[104:107],v[212:215],v[20:23]\nds_read_b128 a[40:43],v250 offset:10240\nds_read_b128 a[44:47],v250 offset:11264\nM v[24:27],a[108:111],v[208:211],v[24:27]\n" \
"v_cvt_pk_bf16_f32 v198,v148,v149\nv_cvt_pk_bf16_f32 v199,v150,v151\nM v[28:31],a[108:111],v[212:215],v[28:31]\nv_exp_f32 v160,v160\nM v[32:35],a[112:115],v[208:211],v[32:35]\nv_exp_f32 v161,v161\nM v[36:39],a[112:115],v[212:215],v[36:39]\nds_read_b128 a[48:51],v250 offset:12288\n" \
"ds_read_b128 a[52:55],v250 offset:13312\nM v[40:43],a[116:119],v[208:211],v[40:43]\nv_exp_f32 v162,v162\nM v[44:47],a[116:119],v[212:215],v[44:47]\nbuffer_load_dwordx4 v240,s[48:51],s95 offen offset:3072 sc1 lds\nM v[48:51],a[120:123],v[208:211],v[48:51]\nv_exp_f32 v163,v163\nM v[52:55],a[120:123],v[212:215],v[52:55]\n" \
"ds_read_b128 a[56:59],v250 offset:14336\nds_read_b128 a[60:63],v250 offset:15360\nM v[56:59],a[124:127],v[208:211],v[56:59]\nv_exp_f32 v176,v176\nM v[60:63],a[124:127],v[212:215],v[60:63]\nv_exp_f32 v177,v177\nM a[192:195],a[208:211],v[208:211],a[192:195]\nv_exp_f32 v178,v178\n" \
"v_exp_f32 v179,v179\nv_add_u32 v250,s94,v241\ns_add_u32 s95,s95,0x4000\nM a[196:199],a[208:211],v[212:215],a[196:199]\ns_cmp_lg_u32 s95,s15\ns_cbranch_scc1 .Lsingle\n.Lsx:\n.Lsw:\n" \
"s_waitcnt lgkmcnt(4)\nM v[128:131],a[0:3],a[128:131],v[224:227]\ns_lshl4_add_u32 s69,s12,s68\ns_add_u32 m0,s64,131072\nM v[128:131],a[4:7],a[132:135],v[128:131]\nds_read_b128 a[64:67],v250 offset:0\nds_read_b128 a[68:71],v250 offset:1024\nv_cvt_pk_bf16_f32 v208,v160,v161\n" \
"M v[128:131],a[8:11],a[136:139],v[128:131]\nbuffer_load_dwordx4 v243,s[40:43],s69 offen offset:0 lds\nM v[128:131],a[12:15],a[140:143],v[128:131]\nv_cvt_pk_bf16_f32 v209,v162,v163\nv_exp_f32 v164,v164\nM v[132:135],a[0:3],a[144:147],v[228:231]\nds_read_b128 a[72:75],v250 offset:2048\nds_read_b128 a[76:79],v250 offset:3072\n" \
"M v[132:135],a[4:7],a[148:151],v[132:135]\nbuffer_load_dwordx4 v244,s[40:43],s69 offen offset:1024 lds\nv_cvt_pk_bf16_f32 v210,v176,v177\nM v[132:135],a[8:11],a[152:155],v[132:135]\nv_exp_f32 v165,v165\nM v[132:135],a[12:15],a[156:159],v[132:135]\nds_read_b128 a[80:83],v250 offset:4096\nds_read_b128 a[84:87],v250 offset:5120\n" \
"v_cvt_pk_bf16_f32 v212,v164,v165\nM v[144:147],a[16:19],a[128:131],v[224:227]\nbuffer_load_dwordx4 v245,s[40:43],s69 offen offset:2048 lds\nM v[144:147],a[20:23],a[132:135],v[144:147]\nv_exp_f32 v166,v166\nv_cvt_pk_bf16_f32 v211,v178,v179\nM v[144:147],a[24:27],a[136:139],v[144:147]\nds_read_b128 a[88:91],v250 offset:6144\n" \
"ds_read_b128 a[92:95],v250 offset:7168\nM v[144:147],a[28:31],a[140:143],v[144:147]\nbuffer_load_dwordx4 v246,s[40:43],s69 offen offset:3072 lds\ns_lshl4_add_u32 s69,s12,s68\nM v[148:151],a[16:19],a[144:147],v[228:231]\ns_add_u32 s69,s69,0x10000\ns_add_u32 m0,s64,147456\nM v[148:151],a[20:23],a[148:151],v[148:151]\n" \
"ds_read_b128 a[96:99],v250 offset:8192\nds_read_b128 a[100:103],v250 offset:9216\ns_add_u32 s94,s94,0x4000\nM v[148:151],a[24:27],a[152:155],v[148:151]\nv_exp_f32 v167,v167\nM v[148:151],a[28:31],a[156:159],v[148:151]\nv_cvt_pk_bf16_f32 v213,v166,v167\nv_exp_f32 v180,v180\n" \
"M v[160:163],a[32:35],a[128:131],v[224:227]\nds_read_b128 a[104:107],v250 offset:10240\nds_read_b128 a[108:111],v250 offset:11264\ns_and_b32 s94,s94,0xffff\nM v[160:163],a[36:39],a[132:135],v[160:163]\nbuffer_load_dwordx4 v243,s[40:43],s69 offen offset:0 lds\nM v[160:163],a[40:43],a[136:139],v[160:163]\nv_exp_f32 v181,v181\n" \
"s_add_u32 s95,s95,0x4000\nM v[160:163],a[44:47],a[140:143],v[160:163]\nds_read_b128 a[112:115],v250 offset:12288\nds_read_b128 a[116:119],v250 offset:13312\nM v[164:167],a[32:35],a[144:147],v[228:231]\nv_exp_f32 v182,v182\nv_cvt_pk_bf16_f32 v214,v180,v181\nM v[164:167],a[36:39],a[148:151],v[164:167]\n" \
"buffer_load_dwordx4 v244,s[40:43],s69 offen offset:1024 lds\nM v[164:167],a[40:43],a[152:155],v[164:167]\nds_read_b128 a[120:123],v250 offset:14336\nds_read_b128 a[124:127],v250 offset:15360\nv_xor_b32 v250,0x18000,v250\nM v[164:167],a[44:47],a[156:159],v[164:167]\ns_waitcnt lgkmcnt(6)\nM v[176:179],a[48:51],a[128:131],v[224:227]\n" \
"s_waitcnt vmcnt(6)\nbuffer_store_dword v233,off,s[52:55],s77 sc1\nM v[176:179],a[52:55],a[132:135],v[176:179]\nbuffer_load_dwordx4 v245,s[40:43],s69 offen offset:2048 lds\nM v[176:179],a[56:59],a[136:139],v[176:179]\nv_exp_f32 v183,v183\nM v[176:179],a[60:63],a[140:143],v[176:179]\nv_cvt_pk_bf16_f32 v215,v182,v183\n" \
"M v[180:183],a[48:51],a[144:147],v[228:231]\nv_exp_f32 v128,v128\nM v[180:183],a[52:55],a[148:151],v[180:183]\nbuffer_load_dwordx4 v246,s[40:43],s69 offen offset:3072 lds\nM v[180:183],a[56:59],a[152:155],v[180:183]\nv_exp_f32 v129,v129\nM v[180:183],a[60:63],a[156:159],v[180:183]\ns_lshl4_add_u32 s69,s10,s68\n" \
"v_exp_f32 v130,v130\nM v[0:3],a[64:67],v[192:195],v[0:3]\nv_exp_f32 v131,v131\nM v[4:7],a[64:67],v[196:199],v[4:7]\ns_add_u32 m0,s64,0\nv_exp_f32 v144,v144\nM v[8:11],a[68:71],v[192:195],v[8:11]\ns_waitcnt vmcnt(16) lgkmcnt(0)\n" \
"s_barrier\nM v[12:15],a[68:71],v[196:199],v[12:15]\nds_read_b128 a[0:3],v250 offset:0\nds_read_b128 a[4:7],v250 offset:1024\nM v[16:19],a[72:75],v[192:195],v[16:19]\nv_exp_f32 v145,v145\nM v[20:23],a[72:75],v[196:199],v[20:23]\nbuffer_load_dwordx4 v243,s[40:43],s69 offen offset:0 lds\n" \
"M v[24:27],a[76:79],v[192:195],v[24:27]\nv_exp_f32 v146,v146\nM v[28:31],a[76:79],v[196:199],v[28:31]\nds_read_b128 a[8:11],v250 offset:2048\nds_read_b128 a[12:15],v250 offset:3072\nM v[32:35],a[80:83],v[192:195],v[32:35]\nv_exp_f32 v147,v147\nM v[36:39],a[80:83],v[196:199],v[36:39]\n" \
"buffer_load_dwordx4 v244,s[40:43],s69 offen offset:1024 lds\nM v[40:43],a[84:87],v[192:195],v[40:43]\nv_exp_f32 v132,v132\nM v[44:47],a[84:87],v[196:199],v[44:47]\nds_read_b128 a[16:19],v250 offset:4096\nds_read_b128 a[20:23],v250 offset:5120\nM v[48:51],a[88:91],v[192:195],v[48:51]\nv_exp_f32 v133,v133\n" \
"M v[52:55],a[88:91],v[196:199],v[52:55]\nbuffer_load_dwordx4 v245,s[40:43],s69 offen offset:2048 lds\nM v[56:59],a[92:95],v[192:195],v[56:59]\nv_exp_f32 v134,v134\nM v[60:63],a[92:95],v[196:199],v[60:63]\nds_read_b128 a[24:27],v250 offset:6144\nds_read_b128 a[28:31],v250 offset:7168\nM a[192:195],a[208:211],v[192:195],a[192:195]\n" \
"v_exp_f32 v135,v135\nv_cvt_pk_bf16_f32 v192,v128,v129\nM a[196:199],a[208:211],v[196:199],a[196:199]\nbuffer_load_dwordx4 v246,s[40:43],s69 offen offset:3072 lds\nM v[0:3],a[96:99],v[208:211],v[0:3]\ns_lshl4_add_u32 s69,s10,s68\ns_add_u32 s69,s69,0x10000\ns_add_u32 m0,s64,32768\n" \
"M v[4:7],a[96:99],v[212:215],v[4:7]\nds_read_b128 a[32:35],v250 offset:8192\nds_read_b128 a[36:39],v250 offset:9216\nM v[8:11],a[100:103],v[208:211],v[8:11]\nv_exp_f32 v148,v148\nv_cvt_pk_bf16_f32 v193,v130,v131\nM v[12:15],a[100:103],v[212:215],v[12:15]\nbuffer_load_dwordx4 v243,s[40:43],s69 offen offset:0 lds\n" \
"M v[16:19],a[104:107],v[208:211],v[16:19]\nv_exp_f32 v149,v149\nv_cvt_pk_bf16_f32 v194,v144,v145\nM v[20:23],a[104:107],v[212:215],v[20:23]\nds_read_b128 a[40:43],v250 offset:10240\nds_read_b128 a[44:47],v250 offset:11264\nM v[24:27],a[108:111],v[208:211],v[24:27]\nv_exp_f32 v150,v150\n" \
"v_cvt_pk_bf16_f32 v195,v146,v147\nM v[28:31],a[108:111],v[212:215],v[28:31]\nbuffer_load_dwordx4 v244,s[40:43],s69 offen offset:1024 lds\nM v[32:35],a[112:115],v[208:211],v[32:35]\nv_exp_f32 v151,v151\nv_cvt_pk_bf16_f32 v196,v132,v133\nM v[36:39],a[112:115],v[212:215],v[36:39]\nds_read_b128 a[48:51],v250 offset:12288\n" \
"ds_read_b128 a[52:55],v250 offset:13312\nM v[40:43],a[116:119],v[208:211],v[40:43]\nv_cvt_pk_bf16_f32 v197,v134,v135\nv_cvt_pk_bf16_f32 v198,v148,v149\nv_cvt_pk_bf16_f32 v199,v150,v151\nM v[44:47],a[116:119],v[212:215],v[44:47]\nbuffer_load_dwordx4 v245,s[40:43],s69 offen offset:2048 lds\nM v[48:51],a[120:123],v[208:211],v[48:51]\n" \
"v_exp_f32 v160,v160\nM v[52:55],a[120:123],v[212:215],v[52:55]\nds_read_b128 a[56:59],v250 offset:14336\nds_read_b128 a[60:63],v250 offset:15360\nv_add_u32 v250,s94,v241\nM v[56:59],a[124:127],v[208:211],v[56:59]\nv_exp_f32 v161,v161\nM v[60:63],a[124:127],v[212:215],v[60:63]\n" \
"buffer_load_dwordx4 v246,s[40:43],s69 offen offset:3072 lds\nM a[192:195],a[208:211],v[208:211],a[192:195]\nv_exp_f32 v162,v162\nM a[196:199],a[208:211],v[212:215],a[196:199]\nv_exp_f32 v163,v163\nv_exp_f32 v176,v176\nv_exp_f32 v177,v177\nv_exp_f32 v178,v178\n" \
"v_exp_f32 v179,v179\n.Lod:\ns_waitcnt lgkmcnt(2)\nM v[128:131],a[0:3],a[128:131],v[224:227]\ns_lshl2_add_u32 s39,s62,s7\ns_lshl_b32 s39,s39,5\nM v[128:131],a[4:7],a[132:135],v[128:131]\nds_read_b128 a[64:67],v250 offset:0\n" \
"ds_read_b128 a[68:71],v250 offset:1024\nM v[128:131],a[8:11],a[136:139],v[128:131]\ns_add_u32 s39,s39,s63\ns_lshl2_add_u32 s39,s39,73728\nM v[128:131],a[12:15],a[140:143],v[128:131]\nv_cvt_pk_bf16_f32 v208,v160,v161\nv_cvt_pk_bf16_f32 v209,v162,v163\nv_and_b32 v251,3,v249\n" \
"M v[132:135],a[0:3],a[144:147],v[228:231]\nds_read_b128 a[72:75],v250 offset:2048\nds_read_b128 a[76:79],v250 offset:3072\nM v[132:135],a[4:7],a[148:151],v[132:135]\nv_exp_f32 v164,v164\nM v[132:135],a[8:11],a[152:155],v[132:135]\ns_waitcnt vmcnt(16)\nbuffer_store_dword v252,off,s[52:55],s39 sc1\n" \
"M v[132:135],a[12:15],a[156:159],v[132:135]\nds_read_b128 a[80:83],v250 offset:4096\nds_read_b128 a[84:87],v250 offset:5120\nv_lshlrev_b32 v251,2,v251\nM v[144:147],a[16:19],a[128:131],v[224:227]\nv_exp_f32 v165,v165\nM v[144:147],a[20:23],a[132:135],v[144:147]\nv_exp_f32 v166,v166\n" \
"M v[144:147],a[24:27],a[136:139],v[144:147]\nds_read_b128 a[88:91],v250 offset:6144\nds_read_b128 a[92:95],v250 offset:7168\nM v[144:147],a[28:31],a[140:143],v[144:147]\nv_exp_f32 v167,v167\nv_cvt_pk_bf16_f32 v212,v164,v165\nM v[148:151],a[16:19],a[144:147],v[228:231]\nv_cvt_pk_bf16_f32 v213,v166,v167\n" \
"s_add_u32 s80,s79,4096\nM v[148:151],a[20:23],a[148:151],v[148:151]\nds_read_b128 a[96:99],v250 offset:8192\nds_read_b128 a[100:103],v250 offset:9216\nM v[148:151],a[24:27],a[152:155],v[148:151]\nv_cvt_pk_bf16_f32 v210,v176,v177\nv_cvt_pk_bf16_f32 v211,v178,v179\nM v[148:151],a[28:31],a[156:159],v[148:151]\n" \
"v_exp_f32 v180,v180\ns_add_u32 s94,s94,0x4000\nM v[160:163],a[32:35],a[128:131],v[224:227]\nds_read_b128 a[104:107],v250 offset:10240\nds_read_b128 a[108:111],v250 offset:11264\nM v[160:163],a[36:39],a[132:135],v[160:163]\nv_exp_f32 v181,v181\nM v[160:163],a[40:43],a[136:139],v[160:163]\n" \
"v_exp_f32 v182,v182\nM v[160:163],a[44:47],a[140:143],v[160:163]\nds_read_b128 a[112:115],v250 offset:12288\nds_read_b128 a[116:119],v250 offset:13312\nv_cvt_pk_bf16_f32 v214,v180,v181\nM v[164:167],a[32:35],a[144:147],v[228:231]\nbuffer_load_dword v248,v251,s[52:55],s80 offen sc1\nM v[164:167],a[36:39],a[148:151],v[164:167]\n" \
"v_exp_f32 v183,v183\nM v[164:167],a[40:43],a[152:155],v[164:167]\nds_read_b128 a[120:123],v250 offset:14336\nds_read_b128 a[124:127],v250 offset:15360\nM v[164:167],a[44:47],a[156:159],v[164:167]\nv_cvt_pk_bf16_f32 v215,v182,v183\ns_and_b32 s94,s94,0xffff\nv_add_u32 v250,s94,v241\n" \
"M v[176:179],a[48:51],a[128:131],v[224:227]\nv_exp_f32 v128,v128\nM v[176:179],a[52:55],a[132:135],v[176:179]\ns_add_u32 s65,s10,s67\ns_waitcnt lgkmcnt(4)\nM v[176:179],a[56:59],a[136:139],v[176:179]\nv_exp_f32 v129,v129\nM v[176:179],a[60:63],a[140:143],v[176:179]\n" \
"v_exp_f32 v130,v130\ns_add_u32 s65,s65,1048576\nM v[180:183],a[48:51],a[144:147],v[228:231]\nv_exp_f32 v131,v131\nM v[180:183],a[52:55],a[148:151],v[180:183]\nv_exp_f32 v144,v144\nM v[180:183],a[56:59],a[152:155],v[180:183]\nv_exp_f32 v145,v145\n" \
"M v[180:183],a[60:63],a[156:159],v[180:183]\nv_exp_f32 v146,v146\nv_add_u32 v226,131072,v242\nM v[0:3],a[64:67],v[192:195],v[0:3]\nv_exp_f32 v147,v147\nM v[4:7],a[64:67],v[196:199],v[4:7]\nv_exp_f32 v132,v132\nM v[8:11],a[68:71],v[192:195],v[8:11]\n" \
"s_waitcnt lgkmcnt(0)\ns_barrier\nM v[12:15],a[68:71],v[196:199],v[12:15]\ns_lshl4_add_u32 s69,s11,s68\ns_add_u32 m0,s64,16384\nds_read_b128 a[64:67],v250 offset:0\nM v[16:19],a[72:75],v[192:195],v[16:19]\nds_read_b128 a[68:71],v250 offset:1024\n" \
"v_xor_b32 v227,32,v226\nM v[20:23],a[72:75],v[196:199],v[20:23]\nds_read_b128 a[72:75],v250 offset:2048\ns_add_u32 s37,s12,s67\nM v[24:27],a[76:79],v[192:195],v[24:27]\nv_exp_f32 v133,v133\nM v[28:31],a[76:79],v[196:199],v[28:31]\nds_read_b128 a[76:79],v250 offset:3072\n" \
"v_exp_f32 v134,v134\nM v[32:35],a[80:83],v[192:195],v[32:35]\nbuffer_load_dwordx4 v243,s[40:43],s69 offen offset:0 lds\nM v[36:39],a[80:83],v[196:199],v[36:39]\nds_read_b128 a[80:83],v250 offset:4096\ns_add_u32 s38,s37,4096\nM v[40:43],a[84:87],v[192:195],v[40:43]\nv_exp_f32 v135,v135\n" \
"M v[44:47],a[84:87],v[196:199],v[44:47]\nv_exp_f32 v148,v148\nds_read_b128 a[84:87],v250 offset:5120\nM v[48:51],a[88:91],v[192:195],v[48:51]\nv_exp_f32 v149,v149\nM v[52:55],a[88:91],v[196:199],v[52:55]\nv_exp_f32 v150,v150\nM v[56:59],a[92:95],v[192:195],v[56:59]\n" \
"v_exp_f32 v151,v151\nM v[60:63],a[92:95],v[196:199],v[60:63]\nds_read_b128 a[88:91],v250 offset:6144\nds_read_b128 a[92:95],v250 offset:7168\ns_lshl2_add_u32 s76,s61,s79\nM a[192:195],a[208:211],v[192:195],a[192:195]\nbuffer_load_dwordx4 v244,s[40:43],s69 offen offset:1024 lds\nM a[196:199],a[208:211],v[196:199],a[196:199]\n" \
"v_cvt_pk_bf16_f32 v192,v128,v129\nv_cvt_pk_bf16_f32 v193,v130,v131\nM v[0:3],a[96:99],v[208:211],v[0:3]\nv_cvt_pk_bf16_f32 v194,v144,v145\nv_cvt_pk_bf16_f32 v195,v146,v147\nM v[4:7],a[96:99],v[212:215],v[4:7]\nv_cvt_pk_bf16_f32 v196,v132,v133\nv_cvt_pk_bf16_f32 v197,v134,v135\n" \
"v_cvt_pk_bf16_f32 v198,v148,v149\nM v[8:11],a[100:103],v[208:211],v[8:11]\nv_cvt_pk_bf16_f32 v199,v150,v151\nds_read_b128 a[96:99],v250 offset:8192\nM v[12:15],a[100:103],v[212:215],v[12:15]\nds_read_b128 a[100:103],v250 offset:9216\nv_lshlrev_b32 v238,1,v240\nM v[16:19],a[104:107],v[208:211],v[16:19]\n" \
"v_exp_f32 v160,v160\nM v[20:23],a[104:107],v[212:215],v[20:23]\nds_read_b128 a[104:107],v250 offset:10240\nv_exp_f32 v161,v161\nM v[24:27],a[108:111],v[208:211],v[24:27]\nbuffer_load_dwordx4 v245,s[40:43],s69 offen offset:2048 lds\nM v[28:31],a[108:111],v[212:215],v[28:31]\nds_read_b128 a[108:111],v250 offset:11264\n" \
"v_and_b32 v254,32,v249\nM v[32:35],a[112:115],v[208:211],v[32:35]\nv_exp_f32 v162,v162\nM v[36:39],a[112:115],v[212:215],v[36:39]\nv_exp_f32 v163,v163\nds_read_b128 a[112:115],v250 offset:12288\nM v[40:43],a[116:119],v[208:211],v[40:43]\nv_exp_f32 v176,v176\n" \
"M v[44:47],a[116:119],v[212:215],v[44:47]\nv_exp_f32 v177,v177\nM v[48:51],a[120:123],v[208:211],v[48:51]\nv_exp_f32 v178,v178\nM v[52:55],a[120:123],v[212:215],v[52:55]\nv_exp_f32 v179,v179\nds_read_b128 a[116:119],v250 offset:13312\nM v[56:59],a[124:127],v[208:211],v[56:59]\n" \
"buffer_load_dwordx4 v246,s[40:43],s69 offen offset:3072 lds\nM v[60:63],a[124:127],v[212:215],v[60:63]\ns_waitcnt vmcnt(4)\nv_and_b32 v248,s17,v248\nM a[192:195],a[208:211],v[208:211],a[192:195]\nv_cmp_eq_u32 vcc,0,v248\ns_lshl4_add_u32 s69,s11,s68\nM a[196:199],a[208:211],v[212:215],a[196:199]\n" \
"s_cbranch_vccnz .Lps6\n.Lpb6:\ns_add_u32 s69,s69,0x10000\ns_waitcnt lgkmcnt(1)\nM v[0:3],a[64:67],v[192:195],v[0:3]\nbuffer_load_dwordx4 v[72:75],v240,s[52:55],s65 offen offset:0 sc1\nM v[4:7],a[64:67],v[196:199],v[4:7]\ns_add_u32 m0,s64,49152\n" \
"v_cvt_pk_bf16_f32 v208,v160,v161\nM v[8:11],a[68:71],v[192:195],v[8:11]\nv_exp_f32 v164,v164\nM v[12:15],a[68:71],v[196:199],v[12:15]\nv_exp_f32 v165,v165\nv_cvt_pk_bf16_f32 v209,v162,v163\nM v[16:19],a[72:75],v[192:195],v[16:19]\nbuffer_load_dwordx4 v243,s[40:43],s69 offen offset:0 lds\n" \
"M v[20:23],a[72:75],v[196:199],v[20:23]\nv_exp_f32 v166,v166\nM v[24:27],a[76:79],v[192:195],v[24:27]\nv_exp_f32 v167,v167\nM v[28:31],a[76:79],v[196:199],v[28:31]\nv_exp_f32 v180,v180\nv_cvt_pk_bf16_f32 v210,v176,v177\nM v[32:35],a[80:83],v[192:195],v[32:35]\n" \
"buffer_load_dwordx4 v[76:79],v240,s[52:55],s65 offen offset:1024 sc1\nM v[36:39],a[80:83],v[196:199],v[36:39]\nv_exp_f32 v181,v181\nM v[40:43],a[84:87],v[192:195],v[40:43]\nv_exp_f32 v182,v182\nM v[44:47],a[84:87],v[196:199],v[44:47]\nv_exp_f32 v183,v183\nv_cvt_pk_bf16_f32 v211,v178,v179\n" \
"M v[48:51],a[88:91],v[192:195],v[48:51]\nbuffer_load_dwordx4 v244,s[40:43],s69 offen offset:1024 lds\nM v[52:55],a[88:91],v[196:199],v[52:55]\nv_cvt_pk_bf16_f32 v212,v164,v165\nv_cvt_pk_bf16_f32 v213,v166,v167\nM v[56:59],a[92:95],v[192:195],v[56:59]\nv_cvt_pk_bf16_f32 v214,v180,v181\nv_cvt_pk_bf16_f32 v215,v182,v183\n" \
"M v[60:63],a[92:95],v[196:199],v[60:63]\nds_read_b128 a[120:123],v250 offset:14336\nds_read_b128 a[124:127],v250 offset:15360\nv_cmp_eq_u32 vcc,2,v248\nM a[192:195],a[208:211],v[192:195],a[192:195]\nbuffer_load_dwordx4 v[88:91],v240,s[52:55],s65 offen offset:2048 sc1\nM a[196:199],a[208:211],v[196:199],a[196:199]\ns_cmp_lg_u64 vcc,0\n" \
"s_addc_u32 s99,s99,0\nM v[0:3],a[96:99],v[208:211],v[0:3]\ns_barrier\nds_read_b64_tr_b16 v[200:201],v226 offset:0\nM v[4:7],a[96:99],v[212:215],v[4:7]\nds_read_b64_tr_b16 v[202:203],v226 offset:4096\nds_read_b64_tr_b16 v[204:205],v227 offset:0\nds_read_b64_tr_b16 v[206:207],v227 offset:4096\n" \
"M v[8:11],a[100:103],v[208:211],v[8:11]\nbuffer_load_dwordx4 v245,s[40:43],s69 offen offset:2048 lds\nM v[12:15],a[100:103],v[212:215],v[12:15]\nds_read_b64_tr_b16 v[216:217],v226 offset:512\nds_read_b64_tr_b16 v[218:219],v226 offset:4608\nM v[16:19],a[104:107],v[208:211],v[16:19]\nds_read_b64_tr_b16 v[220:221],v227 offset:512\nds_read_b64_tr_b16 v[222:223],v227 offset:4608\n" \
"M v[20:23],a[104:107],v[212:215],v[20:23]\nds_read_b64_tr_b16 v[192:193],v226 offset:1024\nds_read_b64_tr_b16 v[194:195],v226 offset:5120\nds_read_b64_tr_b16 v[196:197],v227 offset:1024\nM v[24:27],a[108:111],v[208:211],v[24:27]\nbuffer_load_dwordx4 v[92:95],v240,s[52:55],s65 offen offset:3072 sc1\nM v[28:31],a[108:111],v[212:215],v[28:31]\ns_add_u32 s65,s65,4096\n" \
"ds_read_b64_tr_b16 v[198:199],v227 offset:5120\nM v[32:35],a[112:115],v[208:211],v[32:35]\nv_xor_b32 v250,0x8000,v250\nv_add_u32 v229,s64,v250\nM v[36:39],a[112:115],v[212:215],v[36:39]\ns_lshr_b32 s70,s60,1\nv_lshlrev_b32 v254,3,v254\ns_waitcnt lgkmcnt(8)\n" \
"M v[40:43],a[116:119],v[208:211],v[40:43]\nbuffer_load_dwordx4 v246,s[40:43],s69 offen offset:3072 lds\nM v[44:47],a[116:119],v[212:215],v[44:47]\ns_lshl_b32 s70,s70,7\nv_lshl_add_u32 v254,v249,2,v254\nM v[48:51],a[120:123],v[208:211],v[48:51]\nv_xor_b32 v254,s70,v254\ns_mov_b32 s97,4096\n" \
"M v[52:55],a[120:123],v[212:215],v[52:55]\ns_lshl_b32 s80,s62,9\ns_add_u32 s80,s80,73728\nv_and_b32 v236,15,v249\nM v[56:59],a[124:127],v[208:211],v[56:59]\nbuffer_load_dwordx4 v[104:107],v240,s[52:55],s65 offen offset:0 sc1\nM v[60:63],a[124:127],v[212:215],v[60:63]\ns_lshl2_add_u32 s74,s63,s7\n" \
"s_lshl_b32 s72,s62,7\nM a[192:195],a[208:211],v[208:211],a[192:195]\nds_read_b64_tr_b16 v[208:209],v226 offset:1536\nds_read_b64_tr_b16 v[210:211],v226 offset:5632\nM a[196:199],a[208:211],v[212:215],a[196:199]\nds_read_b64_tr_b16 v[212:213],v227 offset:1536\nds_read_b64_tr_b16 v[214:215],v227 offset:5632\nv_lshrrev_b32 v235,4,v249\n" \
"M v[128:131],v[64:67],v[200:203],0\nv_lshlrev_b32 v236,1,v236\nv_accvgpr_read_b32 v224,a192\ns_add_u32 s95,s74,s72\nv_lshl_or_b32 v235,v235,8,v236\nM v[132:135],v[68:71],v[200:203],0\nv_accvgpr_read_b32 v228,a196\nv_add_f32 v225,v224,v228\n" \
"v_rcp_f32 v224,v224\nds_read_b64_tr_b16 v[200:201],v226 offset:8192\nM v[136:139],v[64:67],v[204:207],0\nv_mul_f32 v0,v0,v224\nv_mul_f32 v1,v1,v224\nv_mul_f32 v2,v2,v224\nv_mul_f32 v3,v3,v224\nM v[140:143],v[68:71],v[204:207],0\n" \
"v_cvt_pk_bf16_f32 v0,v0,v1\nv_mul_f32 v8,v8,v224\nv_mul_f32 v9,v9,v224\nv_mul_f32 v10,v10,v224\ns_waitcnt lgkmcnt(3)\nM v[144:147],v[64:67],v[216:219],0\nbuffer_load_dwordx4 v[108:111],v240,s[52:55],s65 offen offset:1024 sc1\nv_mul_f32 v11,v11,v224\n" \
"v_cvt_pk_bf16_f32 v1,v2,v3\nM v[148:151],v[68:71],v[216:219],0\nds_read_b64_tr_b16 v[202:203],v226 offset:12288\nv_cvt_pk_bf16_f32 v2,v8,v9\nv_cvt_pk_bf16_f32 v3,v10,v11\nds_read_b64_tr_b16 v[204:205],v227 offset:8192\nds_read_b64_tr_b16 v[206:207],v227 offset:12288\nM v[152:155],v[64:67],v[220:223],0\n" \
"buffer_store_dwordx4 v[0:3],v240,s[52:55],s37 offen offset:0 sc1\nv_rcp_f32 v228,v228\nM v[156:159],v[68:71],v[220:223],0\nv_mul_f32 v4,v4,v228\nv_mul_f32 v5,v5,v228\nds_read_b64_tr_b16 v[216:217],v226 offset:8704\nds_read_b64_tr_b16 v[218:219],v226 offset:12800\nv_mul_f32 v6,v6,v228\n" \
"M v[160:163],v[64:67],v[192:195],0\nv_mul_f32 v7,v7,v228\nv_cvt_pk_bf16_f32 v4,v4,v5\nv_mul_f32 v12,v12,v228\nv_mul_f32 v13,v13,v228\nM v[164:167],v[68:71],v[192:195],0\nv_mul_f32 v14,v14,v228\nv_mul_f32 v15,v15,v228\n" \
"v_cvt_pk_bf16_f32 v5,v6,v7\nv_cvt_pk_bf16_f32 v6,v12,v13\nv_cvt_pk_bf16_f32 v7,v14,v15\nM v[168:171],v[64:67],v[196:199],0\nds_read_b64_tr_b16 v[220:221],v227 offset:8704\nds_read_b64_tr_b16 v[222:223],v227 offset:12800\nds_read_b64_tr_b16 v[192:193],v226 offset:9216\nv_mul_f32 v16,v16,v224\n" \
"M v[172:175],v[68:71],v[196:199],0\nbuffer_store_dwordx4 v[4:7],v240,s[52:55],s37 offen offset:1024 sc1\nds_read_b64_tr_b16 v[194:195],v226 offset:13312\nv_mul_f32 v17,v17,v224\nv_mul_f32 v18,v18,v224\nM v[176:179],v[64:67],v[208:211],0\nbuffer_load_dwordx4 v[120:123],v240,s[52:55],s65 offen offset:2048 sc1\nv_mul_f32 v19,v19,v224\n" \
"v_cvt_pk_bf16_f32 v16,v16,v17\nM v[180:183],v[68:71],v[208:211],0\nv_mul_f32 v24,v24,v224\nv_mul_f32 v25,v25,v224\nv_mul_f32 v26,v26,v224\nv_mul_f32 v27,v27,v224\ns_waitcnt lgkmcnt(4)\nM v[184:187],v[64:67],v[212:215],0\n" \
"v_cvt_pk_bf16_f32 v17,v18,v19\nds_read_b64_tr_b16 v[196:197],v227 offset:9216\nds_read_b64_tr_b16 v[198:199],v227 offset:13312\nv_cvt_pk_bf16_f32 v18,v24,v25\nM v[188:191],v[68:71],v[212:215],0\nv_cvt_pk_bf16_f32 v19,v26,v27\nds_read_b64_tr_b16 v[208:209],v226 offset:9728\nds_read_b64_tr_b16 v[210:211],v226 offset:13824\n" \
"v_mul_f32 v20,v20,v228\nv_mul_f32 v21,v21,v228\nM v[128:131],v[80:83],v[200:203],v[128:131]\nbuffer_store_dwordx4 v[16:19],v240,s[52:55],s37 offen offset:2048 sc1\nds_read_b64_tr_b16 v[212:213],v227 offset:9728\nds_read_b64_tr_b16 v[214:215],v227 offset:13824\nM v[132:135],v[84:87],v[200:203],v[132:135]\nv_mul_f32 v22,v22,v228\n" \
"v_mul_f32 v23,v23,v228\nv_cvt_pk_bf16_f32 v20,v20,v21\nv_mul_f32 v28,v28,v228\nv_mul_f32 v29,v29,v228\nM v[136:139],v[80:83],v[204:207],v[136:139]\nv_mul_f32 v30,v30,v228\nv_mul_f32 v31,v31,v228\nv_cvt_pk_bf16_f32 v21,v22,v23\n" \
"v_cvt_pk_bf16_f32 v22,v28,v29\nM v[140:143],v[84:87],v[204:207],v[140:143]\nv_cvt_pk_bf16_f32 v23,v30,v31\nds_read_b64_tr_b16 v[200:201],v226 offset:16384\nds_read_b64_tr_b16 v[202:203],v226 offset:20480\nds_read_b64_tr_b16 v[204:205],v227 offset:16384\nv_mul_f32 v32,v32,v224\nM v[144:147],v[80:83],v[216:219],v[144:147]\n" \
"buffer_load_dwordx4 v[124:127],v240,s[52:55],s65 offen offset:3072 sc1\nds_read_b64_tr_b16 v[206:207],v227 offset:20480\nv_mul_f32 v33,v33,v224\nM v[148:151],v[84:87],v[216:219],v[148:151]\nbuffer_store_dwordx4 v[20:23],v240,s[52:55],s37 offen offset:3072 sc1\nv_mul_f32 v34,v34,v224\nv_mul_f32 v35,v35,v224\ns_waitcnt lgkmcnt(4)\n" \
"M v[152:155],v[80:83],v[220:223],v[152:155]\nv_cvt_pk_bf16_f32 v32,v32,v33\nv_mul_f32 v40,v40,v224\nv_mul_f32 v41,v41,v224\nv_mul_f32 v42,v42,v224\nM v[156:159],v[84:87],v[220:223],v[156:159]\nv_mul_f32 v43,v43,v224\nv_cvt_pk_bf16_f32 v33,v34,v35\n" \
"ds_read_b64_tr_b16 v[216:217],v226 offset:16896\nds_read_b64_tr_b16 v[218:219],v226 offset:20992\nv_cvt_pk_bf16_f32 v34,v40,v41\nM v[160:163],v[80:83],v[192:195],v[160:163]\nv_cvt_pk_bf16_f32 v35,v42,v43\ns_add_u32 s65,s11,s67\nds_read_b64_tr_b16 v[220:221],v227 offset:16896\nds_read_b64_tr_b16 v[222:223],v227 offset:20992\n" \
"M v[164:167],v[84:87],v[192:195],v[164:167]\ns_add_u32 s65,s65,1048576\nv_mul_f32 v36,v36,v228\nv_mul_f32 v37,v37,v228\nds_read_b64_tr_b16 v[192:193],v226 offset:17408\nds_read_b64_tr_b16 v[194:195],v226 offset:21504\nM v[168:171],v[80:83],v[196:199],v[168:171]\nbuffer_store_dwordx4 v[32:35],v240,s[52:55],s38 offen offset:0 sc1\n" \
"v_mul_f32 v38,v38,v228\nv_mul_f32 v39,v39,v228\nM v[172:175],v[84:87],v[196:199],v[172:175]\nv_cvt_pk_bf16_f32 v36,v36,v37\nv_mul_f32 v44,v44,v228\nv_mul_f32 v45,v45,v228\nv_mul_f32 v46,v46,v228\nv_mul_f32 v47,v47,v228\n" \
"M v[176:179],v[80:83],v[208:211],v[176:179]\nbuffer_load_dwordx4 a[0:3],v240,s[52:55],s65 offen offset:0 sc1\nv_cvt_pk_bf16_f32 v37,v38,v39\nv_cvt_pk_bf16_f32 v38,v44,v45\nM v[180:183],v[84:87],v[208:211],v[180:183]\nv_cvt_pk_bf16_f32 v39,v46,v47\nds_read_b64_tr_b16 v[196:197],v227 offset:17408\nds_read_b64_tr_b16 v[198:199],v227 offset:21504\n" \
"v_add_f32 v225,v225,v224\nds_read_b64_tr_b16 v[208:209],v226 offset:17920\nM v[184:187],v[80:83],v[212:215],v[184:187]\nds_read_b64_tr_b16 v[210:211],v226 offset:22016\nv_add_f32 v225,v225,v228\nv_cmp_ngt_f32 vcc,s28,v225\nv_mul_f32 v48,v48,v224\nM v[188:191],v[84:87],v[212:215],v[188:191]\n" \
"buffer_store_dwordx4 v[36:39],v240,s[52:55],s38 offen offset:1024 sc1\nv_mul_f32 v49,v49,v224\nv_mul_f32 v50,v50,v224\ns_waitcnt lgkmcnt(4)\nM v[128:131],v[96:99],v[200:203],v[128:131]\ns_and_b64 vcc,vcc,s[90:91]\ns_addc_u32 s99,s99,0\nv_mul_f32 v51,v51,v224\n" \
"v_cvt_pk_bf16_f32 v48,v48,v49\nM v[132:135],v[100:103],v[200:203],v[132:135]\nv_mul_f32 v56,v56,v224\nv_mul_f32 v57,v57,v224\nv_mul_f32 v58,v58,v224\nv_mul_f32 v59,v59,v224\nv_cvt_pk_bf16_f32 v49,v50,v51\nM v[136:139],v[96:99],v[204:207],v[136:139]\n" \
"ds_read_b64_tr_b16 v[212:213],v227 offset:17920\nds_read_b64_tr_b16 v[214:215],v227 offset:22016\nv_cvt_pk_bf16_f32 v50,v56,v57\nv_cvt_pk_bf16_f32 v51,v58,v59\nM v[140:143],v[100:103],v[204:207],v[140:143]\nv_mov_b32 v230,s99\nds_write_b32 v229,v230\nds_read_b64_tr_b16 v[200:201],v226 offset:24576\n" \
"ds_read_b64_tr_b16 v[202:203],v226 offset:28672\nv_mul_f32 v52,v52,v228\nM v[144:147],v[96:99],v[216:219],v[144:147]\nbuffer_store_dwordx4 v[48:51],v240,s[52:55],s38 offen offset:2048 sc1\nbuffer_load_dwordx4 a[4:7],v240,s[52:55],s65 offen offset:1024 sc1\nM v[148:151],v[100:103],v[216:219],v[148:151]\nv_mul_f32 v53,v53,v228\nds_read_b64_tr_b16 v[204:205],v227 offset:24576\n" \
"ds_read_b64_tr_b16 v[206:207],v227 offset:28672\nv_mul_f32 v54,v54,v228\nv_mul_f32 v55,v55,v228\nM v[152:155],v[96:99],v[220:223],v[152:155]\nv_cvt_pk_bf16_f32 v52,v52,v53\nv_mul_f32 v60,v60,v228\nv_mul_f32 v61,v61,v228\nv_mul_f32 v62,v62,v228\n" \
"M v[156:159],v[100:103],v[220:223],v[156:159]\nv_mul_f32 v63,v63,v228\nv_cvt_pk_bf16_f32 v53,v54,v55\nv_cvt_pk_bf16_f32 v54,v60,v61\nv_cvt_pk_bf16_f32 v55,v62,v63\nds_read_b64_tr_b16 v[216:217],v226 offset:25088\nM v[160:163],v[96:99],v[192:195],v[160:163]\nds_read_b64_tr_b16 v[218:219],v226 offset:29184\n" \
"ds_read_b64_tr_b16 v[220:221],v227 offset:25088\nds_read_b64_tr_b16 v[222:223],v227 offset:29184\ns_lshl4_add_u32 s69,s14,s68\nM v[164:167],v[100:103],v[192:195],v[164:167]\nbuffer_store_dwordx4 v[52:55],v240,s[52:55],s38 offen offset:3072 sc1\nds_read_b64_tr_b16 v[192:193],v226 offset:25600\nds_read_b64_tr_b16 v[194:195],v226 offset:29696\ns_waitcnt lgkmcnt(7)\n" \
"M v[168:171],v[96:99],v[196:199],v[168:171]\ns_add_u32 m0,s64,65536\nv_lshl_add_u32 v229,v251,10,v250\ns_lshl_b32 s95,s95,10\nv_add_u32 v236,s95,v235\nM v[172:175],v[100:103],v[196:199],v[172:175]\nds_read_b64_tr_b16 v[196:197],v227 offset:25600\nds_read_b64_tr_b16 v[198:199],v227 offset:29696\n" \
"s_sub_u32 s74,s74,s72\ns_add_u32 s74,s74,1920\ns_lshl_b32 s74,s74,10\nM v[176:179],v[96:99],v[208:211],v[176:179]\nbuffer_load_dwordx4 a[8:11],v240,s[52:55],s65 offen offset:2048 sc1\nv_add_u32 v237,s74,v235\ns_lshl_b32 s72,s7,9\nM v[180:183],v[100:103],v[208:211],v[180:183]\n" \
"ds_read_b64_tr_b16 v[208:209],v226 offset:26112\nds_read_b64_tr_b16 v[210:211],v226 offset:30208\nv_add_u32 v226,0,v242\nv_lshl_add_u32 v239,v249,3,s72\ns_add_u32 s38,s33,16384\nM v[184:187],v[96:99],v[212:215],v[184:187]\ns_barrier\nbuffer_load_dwordx4 v243,s[40:43],s69 offen offset:0 lds\n" \
"ds_read_b32 v230,v229\nM v[188:191],v[100:103],v[212:215],v[188:191]\nds_read_b64_tr_b16 v[212:213],v227 offset:26112\nds_read_b64_tr_b16 v[214:215],v227 offset:30208\nv_xor_b32 v227,32,v226\nv_bfe_u32 v228,v249,2,2\nv_lshrrev_b32 v229,4,v249\nM v[128:131],v[112:115],v[200:203],v[128:131]\n" \
"s_add_u32 s95,s95,54525952\ns_add_u32 s74,s74,54525952\nM v[132:135],v[116:119],v[200:203],v[132:135]\nds_read_b64_tr_b16 v[200:201],v226 offset:0\nds_read_b64_tr_b16 v[202:203],v226 offset:4096\ns_waitcnt lgkmcnt(7)\nM v[136:139],v[112:115],v[204:207],v[136:139]\nM v[140:143],v[116:119],v[204:207],v[140:143]\n" \
"ds_read_b64_tr_b16 v[204:205],v227 offset:0\nds_read_b64_tr_b16 v[206:207],v227 offset:4096\nM v[144:147],v[112:115],v[216:219],v[144:147]\nbuffer_load_dwordx4 a[12:15],v240,s[52:55],s65 offen offset:3072 sc1\nM v[148:151],v[116:119],v[216:219],v[148:151]\nds_read_b64_tr_b16 v[216:217],v226 offset:512\nds_read_b64_tr_b16 v[218:219],v226 offset:4608\nM v[152:155],v[112:115],v[220:223],v[152:155]\n" \
"buffer_load_dwordx4 v244,s[40:43],s69 offen offset:1024 lds\nM v[156:159],v[116:119],v[220:223],v[156:159]\nds_read_b64_tr_b16 v[220:221],v227 offset:512\nds_read_b64_tr_b16 v[222:223],v227 offset:4608\nM v[160:163],v[112:115],v[192:195],v[160:163]\ns_waitcnt lgkmcnt(8)\nv_cmp_ne_u32 vcc,0,v230\nM v[164:167],v[116:119],v[192:195],v[164:167]\n" \
"s_add_u32 s65,s65,4096\ns_cbranch_vccnz .Lredo\nM v[168:171],v[112:115],v[196:199],v[168:171]\nds_read_b64_tr_b16 v[192:193],v226 offset:1024\nds_read_b64_tr_b16 v[194:195],v226 offset:5120\nM v[172:175],v[116:119],v[196:199],v[172:175]\nds_read_b64_tr_b16 v[196:197],v227 offset:1024\nds_read_b64_tr_b16 v[198:199],v227 offset:5120\n" \
"M v[176:179],v[112:115],v[208:211],v[176:179]\nbuffer_load_dwordx4 a[16:19],v240,s[52:55],s65 offen offset:0 sc1\nM v[180:183],v[116:119],v[208:211],v[180:183]\nds_read_b64_tr_b16 v[208:209],v226 offset:1536\nds_read_b64_tr_b16 v[210:211],v226 offset:5632\nM v[184:187],v[112:115],v[212:215],v[184:187]\nbuffer_load_dwordx4 v245,s[40:43],s69 offen offset:2048 lds\nM v[188:191],v[116:119],v[212:215],v[188:191]\n" \
"ds_read_b64_tr_b16 v[212:213],v227 offset:1536\ns_waitcnt vmcnt(27) lgkmcnt(7)\nM v[128:131],v[72:75],v[200:203],v[128:131]\nds_read_b64_tr_b16 v[214:215],v227 offset:5632\ns_waitcnt vmcnt(25)\nM v[132:135],v[76:79],v[200:203],v[132:135]\nds_read_b64_tr_b16 v[200:201],v226 offset:8192\nds_read_b64_tr_b16 v[202:203],v226 offset:12288\n" \
"M v[136:139],v[72:75],v[204:207],v[136:139]\ns_lshl_b32 s70,s2,9\ns_lshl_b32 s71,s7,8\nM v[140:143],v[76:79],v[204:207],v[140:143]\nds_read_b64_tr_b16 v[204:205],v227 offset:8192\nds_read_b64_tr_b16 v[206:207],v227 offset:12288\nM v[144:147],v[72:75],v[216:219],v[144:147]\nbuffer_load_dwordx4 a[20:23],v240,s[52:55],s65 offen offset:1024 sc1\n" \
"M v[148:151],v[76:79],v[216:219],v[148:151]\nds_read_b64_tr_b16 v[216:217],v226 offset:8704\nds_read_b64_tr_b16 v[218:219],v226 offset:12800\nM v[152:155],v[72:75],v[220:223],v[152:155]\nbuffer_load_dwordx4 v246,s[40:43],s69 offen offset:3072 lds\nM v[156:159],v[76:79],v[220:223],v[156:159]\nds_read_b64_tr_b16 v[220:221],v227 offset:8704\ns_waitcnt lgkmcnt(7)\n" \
"M v[160:163],v[72:75],v[192:195],v[160:163]\nds_read_b64_tr_b16 v[222:223],v227 offset:12800\ns_lshl4_add_u32 s69,s14,s68\nM v[164:167],v[76:79],v[192:195],v[164:167]\nds_read_b64_tr_b16 v[192:193],v226 offset:9216\nds_read_b64_tr_b16 v[194:195],v226 offset:13312\nM v[168:171],v[72:75],v[196:199],v[168:171]\ns_add_u32 s69,s69,0x10000\n" \
"s_add_u32 m0,s64,98304\nM v[172:175],v[76:79],v[196:199],v[172:175]\nds_read_b64_tr_b16 v[196:197],v227 offset:9216\nds_read_b64_tr_b16 v[198:199],v227 offset:13312\nM v[176:179],v[72:75],v[208:211],v[176:179]\nbuffer_load_dwordx4 a[24:27],v240,s[52:55],s65 offen offset:2048 sc1\nM v[180:183],v[76:79],v[208:211],v[180:183]\nds_read_b64_tr_b16 v[208:209],v226 offset:9728\n" \
"ds_read_b64_tr_b16 v[210:211],v226 offset:13824\nM v[184:187],v[72:75],v[212:215],v[184:187]\nbuffer_load_dwordx4 v243,s[40:43],s69 offen offset:0 lds\nM v[188:191],v[76:79],v[212:215],v[188:191]\nds_read_b64_tr_b16 v[212:213],v227 offset:9728\ns_waitcnt vmcnt(27) lgkmcnt(7)\nM v[128:131],v[88:91],v[200:203],v[128:131]\nds_read_b64_tr_b16 v[214:215],v227 offset:13824\n" \
"s_waitcnt vmcnt(25)\nM v[132:135],v[92:95],v[200:203],v[132:135]\nds_read_b64_tr_b16 v[200:201],v226 offset:32768\nds_read_b64_tr_b16 v[202:203],v226 offset:36864\nM v[136:139],v[88:91],v[204:207],v[136:139]\nv_lshl_add_u32 v241,v249,2,s71\ns_add_u32 s70,s70,4096\nM v[140:143],v[92:95],v[204:207],v[140:143]\n" \
"ds_read_b64_tr_b16 v[204:205],v227 offset:32768\nds_read_b64_tr_b16 v[206:207],v227 offset:36864\nM v[144:147],v[88:91],v[216:219],v[144:147]\nbuffer_load_dwordx4 a[28:31],v240,s[52:55],s65 offen offset:3072 sc1\nM v[148:151],v[92:95],v[216:219],v[148:151]\nds_read_b64_tr_b16 v[216:217],v226 offset:33280\nds_read_b64_tr_b16 v[218:219],v226 offset:37376\nM v[152:155],v[88:91],v[220:223],v[152:155]\n" \
"buffer_load_dwordx4 v244,s[40:43],s69 offen offset:1024 lds\nM v[156:159],v[92:95],v[220:223],v[156:159]\ns_add_u32 s65,s14,s67\ns_waitcnt lgkmcnt(6)\nM v[160:163],v[88:91],v[192:195],v[160:163]\nds_read_b64_tr_b16 v[220:221],v227 offset:33280\nds_read_b64_tr_b16 v[222:223],v227 offset:37376\nM v[164:167],v[92:95],v[192:195],v[164:167]\n" \
"s_add_u32 s65,s65,1048576\nds_read_b64_tr_b16 v[192:193],v226 offset:33792\nM v[168:171],v[88:91],v[196:199],v[168:171]\nds_read_b64_tr_b16 v[194:195],v226 offset:37888\nv_add_u32 v230,s70,v241\nM v[172:175],v[92:95],v[196:199],v[172:175]\nds_read_b64_tr_b16 v[196:197],v227 offset:33792\nds_read_b64_tr_b16 v[198:199],v227 offset:37888\n" \
"M v[176:179],v[88:91],v[208:211],v[176:179]\nbuffer_load_dwordx4 a[32:35],v240,s[52:55],s65 offen offset:0 sc1\nM v[180:183],v[92:95],v[208:211],v[180:183]\nds_read_b64_tr_b16 v[208:209],v226 offset:34304\nds_read_b64_tr_b16 v[210:211],v226 offset:38400\nM v[184:187],v[88:91],v[212:215],v[184:187]\nbuffer_load_dwordx4 v245,s[40:43],s69 offen offset:2048 lds\nM v[188:191],v[92:95],v[212:215],v[188:191]\n" \
"ds_read_b64_tr_b16 v[212:213],v227 offset:34304\ns_waitcnt vmcnt(26) lgkmcnt(8)\nM v[128:131],v[104:107],v[200:203],v[128:131]\nds_read_b64_tr_b16 v[214:215],v227 offset:38400\nv_add_u32 v231,8192,v230\nM v[132:135],v[108:111],v[200:203],v[132:135]\nds_read_b64_tr_b16 v[200:201],v226 offset:40960\nds_read_b64_tr_b16 v[202:203],v226 offset:45056\n" \
"M v[136:139],v[104:107],v[204:207],v[136:139]\nv_mov_b32 v250,83968\nv_accvgpr_write_b32 a194,0\nM v[140:143],v[108:111],v[204:207],v[140:143]\nds_read_b64_tr_b16 v[204:205],v227 offset:40960\nds_read_b64_tr_b16 v[206:207],v227 offset:45056\nM v[144:147],v[104:107],v[216:219],v[144:147]\nbuffer_load_dwordx4 a[36:39],v240,s[52:55],s65 offen offset:1024 sc1\n" \
"M v[148:151],v[108:111],v[216:219],v[148:151]\nds_read_b64_tr_b16 v[216:217],v226 offset:41472\ns_waitcnt lgkmcnt(7)\nM v[152:155],v[104:107],v[220:223],v[152:155]\nbuffer_load_dwordx4 v246,s[40:43],s69 offen offset:3072 lds\nM v[156:159],v[108:111],v[220:223],v[156:159]\nds_read_b64_tr_b16 v[218:219],v226 offset:45568\nds_read_b64_tr_b16 v[220:221],v227 offset:41472\n" \
"M v[160:163],v[104:107],v[192:195],v[160:163]\nds_read_b64_tr_b16 v[222:223],v227 offset:45568\nv_accvgpr_write_b32 a195,0\nM v[164:167],v[108:111],v[192:195],v[164:167]\nds_read_b64_tr_b16 v[192:193],v226 offset:41984\nds_read_b64_tr_b16 v[194:195],v226 offset:46080\nM v[168:171],v[104:107],v[196:199],v[168:171]\nds_write_b128 v250,a[244:247]\n" \
"M v[172:175],v[108:111],v[196:199],v[172:175]\nds_read_b64_tr_b16 v[196:197],v227 offset:41984\nds_read_b64_tr_b16 v[198:199],v227 offset:46080\nM v[176:179],v[104:107],v[208:211],v[176:179]\nbuffer_load_dwordx4 a[40:43],v240,s[52:55],s65 offen offset:2048 sc1\nM v[180:183],v[108:111],v[208:211],v[180:183]\nds_read_b64_tr_b16 v[208:209],v226 offset:42496\ns_waitcnt lgkmcnt(9)\n" \
"M v[184:187],v[104:107],v[212:215],v[184:187]\ns_waitcnt vmcnt(16)\nbuffer_store_dword v252,off,s[52:55],s76 sc1\ns_barrier\nM v[188:191],v[108:111],v[212:215],v[188:191]\nds_read_b64_tr_b16 v[210:211],v226 offset:46592\nds_read_b64_tr_b16 v[212:213],v227 offset:42496\nM v[128:131],v[120:123],v[200:203],v[128:131]\n" \
"ds_read_b64_tr_b16 v[214:215],v227 offset:46592\nv_accvgpr_write_b32 a196,0\nM v[132:135],v[124:127],v[200:203],v[132:135]\nds_read_b64_tr_b16 v[200:201],v226 offset:16384\nds_read_b64_tr_b16 v[202:203],v226 offset:20480\nM v[136:139],v[120:123],v[204:207],v[136:139]\nv_accvgpr_write_b32 a197,0\ns_load_dwordx2 s[44:45],s[4:5],0x68\n" \
"M v[140:143],v[124:127],v[204:207],v[140:143]\nds_read_b64_tr_b16 v[204:205],v227 offset:16384\ns_waitcnt lgkmcnt(7)\nM v[144:147],v[120:123],v[216:219],v[144:147]\nbuffer_load_dwordx4 a[44:47],v240,s[52:55],s65 offen offset:3072 sc1\nM v[148:151],v[124:127],v[216:219],v[148:151]\nds_read_b64_tr_b16 v[206:207],v227 offset:20480\nds_read_b64_tr_b16 v[216:217],v226 offset:16896\n" \
"M v[152:155],v[120:123],v[220:223],v[152:155]\nds_read_b64_tr_b16 v[218:219],v226 offset:20992\ns_add_u32 s65,s65,4096\nM v[156:159],v[124:127],v[220:223],v[156:159]\nds_read_b64_tr_b16 v[220:221],v227 offset:16896\nds_read_b64_tr_b16 v[222:223],v227 offset:20992\nM v[160:163],v[120:123],v[192:195],v[160:163]\ns_mov_b32 s46,2097152\n" \
"M v[164:167],v[124:127],v[192:195],v[164:167]\nds_read_b64_tr_b16 v[192:193],v226 offset:17408\nds_read_b64_tr_b16 v[194:195],v226 offset:21504\nM v[168:171],v[120:123],v[196:199],v[168:171]\nM v[172:175],v[124:127],v[196:199],v[172:175]\nds_read_b64_tr_b16 v[196:197],v227 offset:17408\ns_waitcnt lgkmcnt(8)\nM v[176:179],v[120:123],v[208:211],v[176:179]\n" \
"buffer_load_dwordx4 a[48:51],v240,s[52:55],s65 offen offset:0 sc1\nM v[180:183],v[124:127],v[208:211],v[180:183]\nds_read_b64_tr_b16 v[198:199],v227 offset:21504\nds_read_b64_tr_b16 v[208:209],v226 offset:17920\nM v[184:187],v[120:123],v[212:215],v[184:187]\nds_read_b64_tr_b16 v[210:211],v226 offset:22016\nM v[188:191],v[124:127],v[212:215],v[188:191]\nds_read_b64_tr_b16 v[212:213],v227 offset:17920\n" \
"ds_read_b64_tr_b16 v[214:215],v227 offset:22016\nM v[128:131],a[0:3],v[200:203],v[128:131]\nM v[132:135],a[4:7],v[200:203],v[132:135]\nds_read_b64_tr_b16 v[200:201],v226 offset:24576\ns_waitcnt lgkmcnt(9)\nM v[136:139],a[0:3],v[204:207],v[136:139]\nbuffer_load_dwordx4 a[160:163],v238,s[52:55],0 offen offset:0 sc1\nM v[140:143],a[4:7],v[204:207],v[140:143]\n" \
"ds_read_b64_tr_b16 v[202:203],v226 offset:28672\nds_read_b64_tr_b16 v[204:205],v227 offset:24576\nM v[144:147],a[0:3],v[216:219],v[144:147]\nbuffer_load_dwordx4 a[52:55],v240,s[52:55],s65 offen offset:1024 sc1\nM v[148:151],a[4:7],v[216:219],v[148:151]\nds_read_b64_tr_b16 v[206:207],v227 offset:28672\nds_read_b64_tr_b16 v[216:217],v226 offset:25088\nM v[152:155],a[0:3],v[220:223],v[152:155]\n" \
"ds_read_b64_tr_b16 v[218:219],v226 offset:29184\nM v[156:159],a[4:7],v[220:223],v[156:159]\nglobal_load_dword a200,v230,s[26:27] offset:-4096\ns_waitcnt lgkmcnt(6)\nM v[160:163],a[0:3],v[192:195],v[160:163]\nbuffer_load_dwordx4 a[164:167],v238,s[52:55],0 offen offset:16 sc1\nM v[164:167],a[4:7],v[192:195],v[164:167]\nglobal_load_dword a201,v230,s[26:27] offset:-3072\n" \
"M v[168:171],a[0:3],v[196:199],v[168:171]\nds_read_b64_tr_b16 v[220:221],v227 offset:25088\nds_read_b64_tr_b16 v[222:223],v227 offset:29184\nM v[172:175],a[4:7],v[196:199],v[172:175]\nglobal_load_dword a202,v230,s[26:27] offset:-2048\nM v[176:179],a[0:3],v[208:211],v[176:179]\nbuffer_load_dwordx4 a[56:59],v240,s[52:55],s65 offen offset:2048 sc1\nM v[180:183],a[4:7],v[208:211],v[180:183]\n" \
"ds_read_b64_tr_b16 v[192:193],v226 offset:25600\nds_read_b64_tr_b16 v[194:195],v226 offset:29696\nglobal_load_dword a203,v230,s[26:27] offset:-1024\nM v[184:187],a[0:3],v[212:215],v[184:187]\nbuffer_load_dwordx4 a[168:171],v238,s[52:55],0 offen offset:2048 sc1\nM v[188:191],a[4:7],v[212:215],v[188:191]\nds_read_b64_tr_b16 v[196:197],v227 offset:25600\nds_read_b64_tr_b16 v[198:199],v227 offset:29696\n" \
"global_load_dword a204,v230,s[26:27] offset:0\ns_waitcnt lgkmcnt(6)\nM v[128:131],a[8:11],v[200:203],v[128:131]\nds_read_b64_tr_b16 v[208:209],v226 offset:26112\ns_waitcnt vmcnt(27)\nM v[132:135],a[12:15],v[200:203],v[132:135]\nds_read_b64_tr_b16 v[210:211],v226 offset:30208\nglobal_load_dword a205,v230,s[26:27] offset:1024\n" \
"M v[136:139],a[8:11],v[204:207],v[136:139]\nds_read_b64_tr_b16 v[212:213],v227 offset:26112\nds_read_b64_tr_b16 v[214:215],v227 offset:30208\nM v[140:143],a[12:15],v[204:207],v[140:143]\nglobal_load_dword a206,v230,s[26:27] offset:2048\nM v[144:147],a[8:11],v[216:219],v[144:147]\nbuffer_load_dwordx4 a[60:63],v240,s[52:55],s65 offen offset:3072 sc1\nbuffer_load_dwordx4 a[172:175],v238,s[52:55],0 offen offset:2064 sc1\n" \
"M v[148:151],a[12:15],v[216:219],v[148:151]\nds_read_b64_tr_b16 v[200:201],v226 offset:49152\nds_read_b64_tr_b16 v[202:203],v226 offset:53248\nglobal_load_dword a207,v230,s[26:27] offset:3072\ns_waitcnt lgkmcnt(6)\nM v[152:155],a[8:11],v[220:223],v[152:155]\nds_read_b64_tr_b16 v[204:205],v227 offset:49152\nds_read_b64_tr_b16 v[206:207],v227 offset:53248\n" \
"M v[156:159],a[12:15],v[220:223],v[156:159]\nglobal_load_dword a208,v231,s[26:27] offset:-4096\nM v[160:163],a[8:11],v[192:195],v[160:163]\nds_read_b64_tr_b16 v[216:217],v226 offset:49664\nds_read_b64_tr_b16 v[218:219],v226 offset:53760\nM v[164:167],a[12:15],v[192:195],v[164:167]\nglobal_load_dword a209,v231,s[26:27] offset:-3072\nM v[168:171],a[8:11],v[196:199],v[168:171]\n" \
"buffer_load_dwordx4 a[176:179],v238,s[52:55],s97 offen offset:0 sc1\nM v[172:175],a[12:15],v[196:199],v[172:175]\nds_read_b64_tr_b16 v[220:221],v227 offset:49664\nds_read_b64_tr_b16 v[222:223],v227 offset:53760\nglobal_load_dword a210,v231,s[26:27] offset:-2048\ns_waitcnt lgkmcnt(6)\nM v[176:179],a[8:11],v[208:211],v[176:179]\nds_read_b64_tr_b16 v[192:193],v226 offset:50176\n" \
"ds_read_b64_tr_b16 v[194:195],v226 offset:54272\nM v[180:183],a[12:15],v[208:211],v[180:183]\nglobal_load_dword a211,v231,s[26:27] offset:-1024\nM v[184:187],a[8:11],v[212:215],v[184:187]\nds_read_b64_tr_b16 v[196:197],v227 offset:50176\nds_read_b64_tr_b16 v[198:199],v227 offset:54272\nM v[188:191],a[12:15],v[212:215],v[188:191]\nglobal_load_dword a252,v231,s[26:27] offset:0\n" \
"s_waitcnt vmcnt(36)\nM v[128:131],a[16:19],v[200:203],v[128:131]\nbuffer_load_dwordx4 a[180:183],v238,s[52:55],s97 offen offset:16 sc1\ns_waitcnt vmcnt(35)\nM v[132:135],a[20:23],v[200:203],v[132:135]\nds_read_b64_tr_b16 v[208:209],v226 offset:50688\nds_read_b64_tr_b16 v[210:211],v226 offset:54784\nglobal_load_dword a253,v231,s[26:27] offset:1024\n" \
"s_waitcnt lgkmcnt(6)\nM v[136:139],a[16:19],v[204:207],v[136:139]\nds_read_b64_tr_b16 v[212:213],v227 offset:50688\nds_read_b64_tr_b16 v[214:215],v227 offset:54784\nM v[140:143],a[20:23],v[204:207],v[140:143]\nglobal_load_dword a254,v231,s[26:27] offset:2048\nM v[144:147],a[16:19],v[216:219],v[144:147]\nds_read_b64_tr_b16 v[200:201],v226 offset:57344\n" \
"ds_read_b64_tr_b16 v[202:203],v226 offset:61440\nM v[148:151],a[20:23],v[216:219],v[148:151]\nglobal_load_dword a255,v231,s[26:27] offset:3072\nM v[152:155],a[16:19],v[220:223],v[152:155]\nbuffer_load_dwordx4 a[184:187],v238,s[52:55],s97 offen offset:2048 sc1\nM v[156:159],a[20:23],v[220:223],v[156:159]\nds_read_b64_tr_b16 v[204:205],v227 offset:57344\nds_read_b64_tr_b16 v[206:207],v227 offset:61440\n" \
"s_waitcnt lgkmcnt(7)\nM v[160:163],a[16:19],v[192:195],v[160:163]\nbuffer_load_dword v248,v254,s[52:55],s80 offen sc1\nM v[164:167],a[20:23],v[192:195],v[164:167]\nds_read_b64_tr_b16 v[216:217],v226 offset:57856\nds_read_b64_tr_b16 v[218:219],v226 offset:61952\nM v[168:171],a[16:19],v[196:199],v[168:171]\nds_read_b64_tr_b16 v[220:221],v227 offset:57856\n" \
"ds_read_b64_tr_b16 v[222:223],v227 offset:61952\nM v[172:175],a[20:23],v[196:199],v[172:175]\nglobal_load_ushort v64,v236,s[8:9] offset:0\ns_waitcnt lgkmcnt(6)\nM v[176:179],a[16:19],v[208:211],v[176:179]\nbuffer_load_dwordx4 a[188:191],v238,s[52:55],s97 offen offset:2064 sc1\nM v[180:183],a[20:23],v[208:211],v[180:183]\nds_read_b64_tr_b16 v[192:193],v226 offset:58368\n" \
"ds_read_b64_tr_b16 v[194:195],v226 offset:62464\nglobal_load_ushort v80,v237,s[8:9] offset:0\nM v[184:187],a[16:19],v[212:215],v[184:187]\ns_waitcnt vmcnt(33)\ns_barrier\nM v[188:191],a[20:23],v[212:215],v[188:191]\nds_read_b64_tr_b16 v[196:197],v227 offset:58368\nds_read_b64_tr_b16 v[198:199],v227 offset:62464\n" \
"global_load_ushort v96,v235,s[20:21] offset:0\nM v[128:131],a[24:27],v[200:203],v[128:131]\nds_read_b64_tr_b16 v[208:209],v226 offset:58880\nds_read_b64_tr_b16 v[210:211],v226 offset:62976\nM v[132:135],a[28:31],v[200:203],v[132:135]\nglobal_load_ushort v65,v236,s[8:9] offset:32\ns_waitcnt lgkmcnt(6)\nM v[136:139],a[24:27],v[204:207],v[136:139]\n" \
"ds_read_b64_tr_b16 v[212:213],v227 offset:58880\nds_read_b64_tr_b16 v[214:215],v227 offset:62976\nM v[140:143],a[28:31],v[204:207],v[140:143]\nglobal_load_ushort v81,v237,s[8:9] offset:32\nM v[144:147],a[24:27],v[216:219],v[144:147]\nv_add_u32 v226,65536,v242\nds_read_b64_tr_b16 v[200:201],v226 offset:0\nM v[148:151],a[28:31],v[216:219],v[148:151]\n" \
"ds_read_b64_tr_b16 v[202:203],v226 offset:4096\nglobal_load_ushort v97,v235,s[20:21] offset:32\nM v[152:155],a[24:27],v[220:223],v[152:155]\nv_xor_b32 v227,32,v226\nds_read_b64_tr_b16 v[204:205],v227 offset:0\nM v[156:159],a[28:31],v[220:223],v[156:159]\nds_read_b64_tr_b16 v[206:207],v227 offset:4096\nglobal_load_ushort v66,v236,s[8:9] offset:64\n" \
"s_waitcnt lgkmcnt(6)\nM v[160:163],a[24:27],v[192:195],v[160:163]\nds_read_b64_tr_b16 v[216:217],v226 offset:512\nds_read_b64_tr_b16 v[218:219],v226 offset:4608\nM v[164:167],a[28:31],v[192:195],v[164:167]\nglobal_load_ushort v82,v237,s[8:9] offset:64\nM v[168:171],a[24:27],v[196:199],v[168:171]\nds_read_b64_tr_b16 v[220:221],v227 offset:512\n" \
"ds_read_b64_tr_b16 v[222:223],v227 offset:4608\nM v[172:175],a[28:31],v[196:199],v[172:175]\nglobal_load_ushort v98,v235,s[20:21] offset:64\nM v[176:179],a[24:27],v[208:211],v[176:179]\nds_read_b64_tr_b16 v[192:193],v226 offset:1024\nds_read_b64_tr_b16 v[194:195],v226 offset:5120\nM v[180:183],a[28:31],v[208:211],v[180:183]\nglobal_load_ushort v67,v236,s[8:9] offset:96\n" \
"s_waitcnt lgkmcnt(7)\nM v[184:187],a[24:27],v[212:215],v[184:187]\nds_read_b64_tr_b16 v[196:197],v227 offset:1024\nds_read_b64_tr_b16 v[198:199],v227 offset:5120\nM v[188:191],a[28:31],v[212:215],v[188:191]\nglobal_load_ushort v83,v237,s[8:9] offset:96\nM v[128:131],a[32:35],v[200:203],v[128:131]\nds_read_b64_tr_b16 v[208:209],v226 offset:1536\n" \
"ds_read_b64_tr_b16 v[210:211],v226 offset:5632\nM v[132:135],a[36:39],v[200:203],v[132:135]\nglobal_load_ushort v99,v235,s[20:21] offset:96\ns_waitcnt lgkmcnt(6)\nM v[136:139],a[32:35],v[204:207],v[136:139]\nds_read_b64_tr_b16 v[212:213],v227 offset:1536\nds_read_b64_tr_b16 v[214:215],v227 offset:5632\nM v[140:143],a[36:39],v[204:207],v[140:143]\n" \
"global_load_ushort v68,v236,s[8:9] offset:128\nM v[144:147],a[32:35],v[216:219],v[144:147]\nds_read_b64_tr_b16 v[200:201],v226 offset:8192\nds_read_b64_tr_b16 v[202:203],v226 offset:12288\nM v[148:151],a[36:39],v[216:219],v[148:151]\nglobal_load_ushort v84,v237,s[8:9] offset:128\nM v[152:155],a[32:35],v[220:223],v[152:155]\nds_read_b64_tr_b16 v[204:205],v227 offset:8192\n" \
"ds_read_b64_tr_b16 v[206:207],v227 offset:12288\nM v[156:159],a[36:39],v[220:223],v[156:159]\nglobal_load_ushort v100,v235,s[20:21] offset:128\ns_waitcnt lgkmcnt(6)\nM v[160:163],a[32:35],v[192:195],v[160:163]\nds_read_b64_tr_b16 v[216:217],v226 offset:8704\nds_read_b64_tr_b16 v[218:219],v226 offset:12800\nM v[164:167],a[36:39],v[192:195],v[164:167]\n" \
"global_load_ushort v69,v236,s[8:9] offset:160\nM v[168:171],a[32:35],v[196:199],v[168:171]\nds_read_b64_tr_b16 v[220:221],v227 offset:8704\nds_read_b64_tr_b16 v[222:223],v227 offset:12800\nM v[172:175],a[36:39],v[196:199],v[172:175]\nglobal_load_ushort v85,v237,s[8:9] offset:160\nM v[176:179],a[32:35],v[208:211],v[176:179]\nds_read_b64_tr_b16 v[192:193],v226 offset:9216\n" \
"ds_read_b64_tr_b16 v[194:195],v226 offset:13312\nM v[180:183],a[36:39],v[208:211],v[180:183]\nglobal_load_ushort v101,v235,s[20:21] offset:160\ns_waitcnt lgkmcnt(6)\nM v[184:187],a[32:35],v[212:215],v[184:187]\nds_read_b64_tr_b16 v[196:197],v227 offset:9216\nds_read_b64_tr_b16 v[198:199],v227 offset:13312\nM v[188:191],a[36:39],v[212:215],v[188:191]\n" \
"global_load_ushort v70,v236,s[8:9] offset:192\nM v[128:131],a[40:43],v[200:203],v[128:131]\nds_read_b64_tr_b16 v[208:209],v226 offset:9728\ns_waitcnt vmcnt(30)\nM v[132:135],a[44:47],v[200:203],v[132:135]\nds_read_b64_tr_b16 v[210:211],v226 offset:13824\nglobal_load_ushort v86,v237,s[8:9] offset:192\nM v[136:139],a[40:43],v[204:207],v[136:139]\n" \
"s_waitcnt vmcnt(21)\nv_cmp_eq_u32 vcc,0,v248\nM v[140:143],a[44:47],v[204:207],v[140:143]\nds_read_b64_tr_b16 v[212:213],v227 offset:9728\nds_read_b64_tr_b16 v[214:215],v227 offset:13824\nglobal_load_ushort v102,v235,s[20:21] offset:192\ns_waitcnt lgkmcnt(6)\nM v[144:147],a[40:43],v[216:219],v[144:147]\n" \
"s_cbranch_vccnz .Lps7\n.Lpb7:\nbuffer_load_dword v248,v251,s[52:55],s79 offen sc1\nM v[148:151],a[44:47],v[216:219],v[148:151]\nds_read_b64_tr_b16 v[200:201],v226 offset:32768\nds_read_b64_tr_b16 v[202:203],v226 offset:36864\nglobal_load_ushort v71,v236,s[8:9] offset:224\nM v[152:155],a[40:43],v[220:223],v[152:155]\n" \
"ds_read_b64_tr_b16 v[204:205],v227 offset:32768\nds_read_b64_tr_b16 v[206:207],v227 offset:36864\nM v[156:159],a[44:47],v[220:223],v[156:159]\nglobal_load_ushort v87,v237,s[8:9] offset:224\nM v[160:163],a[40:43],v[192:195],v[160:163]\nds_read_b64_tr_b16 v[216:217],v226 offset:33280\nds_read_b64_tr_b16 v[218:219],v226 offset:37376\nM v[164:167],a[44:47],v[192:195],v[164:167]\n" \
"global_load_ushort v103,v235,s[20:21] offset:224\ns_waitcnt lgkmcnt(6)\nM v[168:171],a[40:43],v[196:199],v[168:171]\nds_read_b64_tr_b16 v[220:221],v227 offset:33280\nds_read_b64_tr_b16 v[222:223],v227 offset:37376\nM v[172:175],a[44:47],v[196:199],v[172:175]\nbuffer_load_dwordx2 v[112:113],v239,s[52:55],0 offen offset:0 sc1\nM v[176:179],a[40:43],v[208:211],v[176:179]\n" \
"ds_read_b64_tr_b16 v[192:193],v226 offset:33792\nds_read_b64_tr_b16 v[194:195],v226 offset:37888\nM v[180:183],a[44:47],v[208:211],v[180:183]\nbuffer_load_dwordx2 v[114:115],v239,s[52:55],0 offen offset:2048 sc1\nM v[184:187],a[40:43],v[212:215],v[184:187]\nds_read_b64_tr_b16 v[196:197],v227 offset:33792\nds_read_b64_tr_b16 v[198:199],v227 offset:37888\nM v[188:191],a[44:47],v[212:215],v[188:191]\n" \
"buffer_load_dwordx2 v[116:117],v239,s[52:55],s97 offen offset:0 sc1\ns_waitcnt lgkmcnt(6)\nM v[128:131],a[48:51],v[200:203],v[128:131]\nds_read_b64_tr_b16 v[208:209],v226 offset:34304\nds_read_b64_tr_b16 v[210:211],v226 offset:38400\nM v[132:135],a[52:55],v[200:203],v[132:135]\nbuffer_load_dwordx2 v[118:119],v239,s[52:55],s97 offen offset:2048 sc1\nM v[136:139],a[48:51],v[204:207],v[136:139]\n" \
"ds_read_b64_tr_b16 v[212:213],v227 offset:34304\nds_read_b64_tr_b16 v[214:215],v227 offset:38400\nM v[140:143],a[52:55],v[204:207],v[140:143]\nds_read_b64_tr_b16 v[200:201],v226 offset:40960\nds_read_b64_tr_b16 v[202:203],v226 offset:45056\nM v[144:147],a[48:51],v[216:219],v[144:147]\nds_read_b64_tr_b16 v[204:205],v227 offset:40960\nds_read_b64_tr_b16 v[206:207],v227 offset:45056\n" \
"M v[148:151],a[52:55],v[216:219],v[148:151]\nds_read_b64_tr_b16 v[216:217],v226 offset:41472\ns_waitcnt lgkmcnt(9)\nM v[152:155],a[48:51],v[220:223],v[152:155]\nds_read_b64_tr_b16 v[218:219],v226 offset:45568\ns_add_u32 s65,s10,s67\nM v[156:159],a[52:55],v[220:223],v[156:159]\nds_read_b64_tr_b16 v[220:221],v227 offset:41472\n" \
"ds_read_b64_tr_b16 v[222:223],v227 offset:45568\nM v[160:163],a[48:51],v[192:195],v[160:163]\nM v[164:167],a[52:55],v[192:195],v[164:167]\nds_read_b64_tr_b16 v[192:193],v226 offset:41984\nds_read_b64_tr_b16 v[194:195],v226 offset:46080\nM v[168:171],a[48:51],v[196:199],v[168:171]\nM v[172:175],a[52:55],v[196:199],v[172:175]\nds_read_b64_tr_b16 v[196:197],v227 offset:41984\n" \
"s_waitcnt lgkmcnt(10)\nM v[176:179],a[48:51],v[208:211],v[176:179]\nds_read_b64_tr_b16 v[198:199],v227 offset:46080\nM v[180:183],a[52:55],v[208:211],v[180:183]\nds_read_b64_tr_b16 v[208:209],v226 offset:42496\nds_read_b64_tr_b16 v[210:211],v226 offset:46592\nM v[184:187],a[48:51],v[212:215],v[184:187]\nv_add_u32 v226,131072,v242\n" \
"M v[188:191],a[52:55],v[212:215],v[188:191]\nds_read_b64_tr_b16 v[212:213],v227 offset:42496\ns_waitcnt lgkmcnt(9)\nM v[128:131],a[56:59],v[200:203],v[128:131]\nds_read_b64_tr_b16 v[214:215],v227 offset:46592\nv_xor_b32 v227,32,v226\nM v[132:135],a[60:63],v[200:203],v[132:135]\nds_read_b64_tr_b16 v[200:201],v226 offset:0\n" \
"ds_read_b64_tr_b16 v[202:203],v226 offset:4096\nM v[136:139],a[56:59],v[204:207],v[136:139]\nM v[140:143],a[60:63],v[204:207],v[140:143]\nds_read_b64_tr_b16 v[204:205],v227 offset:0\nds_read_b64_tr_b16 v[206:207],v227 offset:4096\nM v[144:147],a[56:59],v[216:219],v[144:147]\nM v[148:151],a[60:63],v[216:219],v[148:151]\nds_read_b64_tr_b16 v[216:217],v226 offset:512\n" \
"s_waitcnt lgkmcnt(9)\nM v[152:155],a[56:59],v[220:223],v[152:155]\ns_waitcnt vmcnt(7)\nv_cmp_eq_u32 vcc,0,v248\nM v[156:159],a[60:63],v[220:223],v[156:159]\ns_cbranch_vccnz .Lps8\n.Lpb8:\nds_read_b64_tr_b16 v[218:219],v226 offset:4608\n" \
"M v[160:163],a[56:59],v[192:195],v[160:163]\nbuffer_load_dwordx4 a[64:67],v240,s[52:55],s65 offen offset:0 sc1\nM v[164:167],a[60:63],v[192:195],v[164:167]\nds_read_b64_tr_b16 v[220:221],v227 offset:512\nds_read_b64_tr_b16 v[222:223],v227 offset:4608\nM v[168:171],a[56:59],v[196:199],v[168:171]\nds_read_b64_tr_b16 v[192:193],v226 offset:1024\nds_read_b64_tr_b16 v[194:195],v226 offset:5120\n" \
"M v[172:175],a[60:63],v[196:199],v[172:175]\nds_read_b64_tr_b16 v[196:197],v227 offset:1024\ns_waitcnt lgkmcnt(7)\nM v[176:179],a[56:59],v[208:211],v[176:179]\nbuffer_load_dwordx4 a[68:71],v240,s[52:55],s65 offen offset:1024 sc1\nM v[180:183],a[60:63],v[208:211],v[180:183]\nds_read_b64_tr_b16 v[198:199],v227 offset:5120\nds_read_b64_tr_b16 v[208:209],v226 offset:1536\n" \
"M v[184:187],a[56:59],v[212:215],v[184:187]\nds_read_b64_tr_b16 v[210:211],v226 offset:5632\nv_cvt_pk_bf16_f32 v128,v128,v136\nM v[188:191],a[60:63],v[212:215],v[188:191]\nds_read_b64_tr_b16 v[212:213],v227 offset:1536\nds_read_b64_tr_b16 v[214:215],v227 offset:5632\nM v[8:11],v[0:3],v[200:203],0\nbuffer_load_dwordx4 a[72:75],v240,s[52:55],s65 offen offset:2048 sc1\n" \
"M v[12:15],v[4:7],v[200:203],0\nds_read_b64_tr_b16 v[200:201],v226 offset:8192\nds_read_b64_tr_b16 v[202:203],v226 offset:12288\nM v[24:27],v[0:3],v[204:207],0\nv_cvt_pk_bf16_f32 v129,v129,v137\nv_cvt_pk_bf16_f32 v130,v130,v138\nM v[28:31],v[4:7],v[204:207],0\nds_read_b64_tr_b16 v[204:205],v227 offset:8192\n" \
"s_waitcnt lgkmcnt(8)\nM v[40:43],v[0:3],v[216:219],0\nbuffer_load_dwordx4 a[76:79],v240,s[52:55],s65 offen offset:3072 sc1\nM v[44:47],v[4:7],v[216:219],0\nds_read_b64_tr_b16 v[206:207],v227 offset:12288\ns_add_u32 s65,s65,4096\nM v[56:59],v[0:3],v[220:223],0\nds_read_b64_tr_b16 v[216:217],v226 offset:8704\n" \
"ds_read_b64_tr_b16 v[218:219],v226 offset:12800\nM v[60:63],v[4:7],v[220:223],0\nds_read_b64_tr_b16 v[220:221],v227 offset:8704\nds_read_b64_tr_b16 v[222:223],v227 offset:12800\nM v[72:75],v[0:3],v[192:195],0\nbuffer_load_dwordx4 a[80:83],v240,s[52:55],s65 offen offset:0 sc1\nM v[76:79],v[4:7],v[192:195],0\nds_read_b64_tr_b16 v[192:193],v226 offset:9216\n" \
"s_waitcnt lgkmcnt(7)\nM v[88:91],v[0:3],v[196:199],0\nds_read_b64_tr_b16 v[194:195],v226 offset:13312\nv_cvt_pk_bf16_f32 v131,v131,v139\nM v[92:95],v[4:7],v[196:199],0\nv_cvt_pk_bf16_f32 v144,v144,v152\nv_cvt_pk_bf16_f32 v160,v160,v168\nM v[104:107],v[0:3],v[208:211],0\n" \
"buffer_load_dwordx4 a[84:87],v240,s[52:55],s65 offen offset:1024 sc1\nM v[108:111],v[4:7],v[208:211],0\nv_cvt_pk_bf16_f32 v176,v176,v184\nv_min_u32 v136,-2,v128\nM v[120:123],v[0:3],v[212:215],0\nv_min_u32 v137,-2,v144\nv_min_u32 v138,-2,v160\nv_min_u32 v139,-2,v176\n" \
"buffer_store_dwordx4 v[136:139],v253,s[52:55],s33 offen offset:0 sc1\nM v[124:127],v[4:7],v[212:215],0\nds_read_b64_tr_b16 v[196:197],v227 offset:9216\nds_read_b64_tr_b16 v[198:199],v227 offset:13312\nM v[8:11],v[16:19],v[200:203],v[8:11]\nbuffer_load_dwordx4 a[88:91],v240,s[52:55],s65 offen offset:2048 sc1\nM v[12:15],v[20:23],v[200:203],v[12:15]\nds_read_b64_tr_b16 v[208:209],v226 offset:9728\n" \
"ds_read_b64_tr_b16 v[210:211],v226 offset:13824\ns_waitcnt lgkmcnt(4)\nM v[24:27],v[16:19],v[204:207],v[24:27]\nds_read_b64_tr_b16 v[212:213],v227 offset:9728\nds_read_b64_tr_b16 v[214:215],v227 offset:13824\nM v[28:31],v[20:23],v[204:207],v[28:31]\nds_read_b64_tr_b16 v[200:201],v226 offset:16384\nds_read_b64_tr_b16 v[202:203],v226 offset:20480\n" \
"M v[40:43],v[16:19],v[216:219],v[40:43]\nbuffer_load_dwordx4 a[92:95],v240,s[52:55],s65 offen offset:3072 sc1\nM v[44:47],v[20:23],v[216:219],v[44:47]\nds_read_b64_tr_b16 v[204:205],v227 offset:16384\nds_read_b64_tr_b16 v[206:207],v227 offset:20480\nM v[56:59],v[16:19],v[220:223],v[56:59]\ns_add_u32 s65,s11,s67\nds_read_b64_tr_b16 v[216:217],v226 offset:16896\n" \
"M v[60:63],v[20:23],v[220:223],v[60:63]\nds_read_b64_tr_b16 v[218:219],v226 offset:20992\nds_read_b64_tr_b16 v[220:221],v227 offset:16896\nM v[72:75],v[16:19],v[192:195],v[72:75]\nbuffer_load_dwordx4 a[96:99],v240,s[52:55],s65 offen offset:0 sc1\nM v[76:79],v[20:23],v[192:195],v[76:79]\nds_read_b64_tr_b16 v[222:223],v227 offset:20992\ns_waitcnt lgkmcnt(9)\n" \
"M v[88:91],v[16:19],v[196:199],v[88:91]\nds_read_b64_tr_b16 v[192:193],v226 offset:17408\nds_read_b64_tr_b16 v[194:195],v226 offset:21504\nM v[92:95],v[20:23],v[196:199],v[92:95]\nv_cvt_pk_bf16_f32 v145,v145,v153\nv_cvt_pk_bf16_f32 v146,v146,v154\nM v[104:107],v[16:19],v[208:211],v[104:107]\nbuffer_load_dwordx4 a[100:103],v240,s[52:55],s65 offen offset:1024 sc1\n" \
"M v[108:111],v[20:23],v[208:211],v[108:111]\nv_cvt_pk_bf16_f32 v147,v147,v155\nv_cvt_pk_bf16_f32 v161,v161,v169\nv_cvt_pk_bf16_f32 v177,v177,v185\ns_waitcnt lgkmcnt(5)\nM v[120:123],v[16:19],v[212:215],v[120:123]\nv_min_u32 v152,-2,v129\nv_min_u32 v153,-2,v145\n" \
"v_min_u32 v154,-2,v161\nv_min_u32 v155,-2,v177\nbuffer_store_dwordx4 v[152:155],v253,s[52:55],s33 offen offset:1024 sc1\nM v[124:127],v[20:23],v[212:215],v[124:127]\nds_read_b64_tr_b16 v[196:197],v227 offset:17408\nds_read_b64_tr_b16 v[198:199],v227 offset:21504\nM v[8:11],v[32:35],v[200:203],v[8:11]\nbuffer_load_dwordx4 a[104:107],v240,s[52:55],s65 offen offset:2048 sc1\n" \
"M v[12:15],v[36:39],v[200:203],v[12:15]\nds_read_b64_tr_b16 v[208:209],v226 offset:17920\nds_read_b64_tr_b16 v[210:211],v226 offset:22016\nM v[24:27],v[32:35],v[204:207],v[24:27]\nds_read_b64_tr_b16 v[212:213],v227 offset:17920\nds_read_b64_tr_b16 v[214:215],v227 offset:22016\nM v[28:31],v[36:39],v[204:207],v[28:31]\nds_read_b64_tr_b16 v[200:201],v226 offset:24576\n" \
"s_waitcnt lgkmcnt(7)\nM v[40:43],v[32:35],v[216:219],v[40:43]\nbuffer_load_dwordx4 a[108:111],v240,s[52:55],s65 offen offset:3072 sc1\nM v[44:47],v[36:39],v[216:219],v[44:47]\nds_read_b64_tr_b16 v[202:203],v226 offset:28672\nds_read_b64_tr_b16 v[204:205],v227 offset:24576\nM v[56:59],v[32:35],v[220:223],v[56:59]\nds_read_b64_tr_b16 v[206:207],v227 offset:28672\n" \
"s_add_u32 s65,s65,4096\nM v[60:63],v[36:39],v[220:223],v[60:63]\nds_read_b64_tr_b16 v[216:217],v226 offset:25088\nds_read_b64_tr_b16 v[218:219],v226 offset:29184\nM v[72:75],v[32:35],v[192:195],v[72:75]\nbuffer_load_dwordx4 a[112:115],v240,s[52:55],s65 offen offset:0 sc1\nM v[76:79],v[36:39],v[192:195],v[76:79]\nds_read_b64_tr_b16 v[220:221],v227 offset:25088\n" \
"s_waitcnt lgkmcnt(8)\nM v[88:91],v[32:35],v[196:199],v[88:91]\nds_read_b64_tr_b16 v[222:223],v227 offset:29184\nds_read_b64_tr_b16 v[192:193],v226 offset:25600\nM v[92:95],v[36:39],v[196:199],v[92:95]\nds_read_b64_tr_b16 v[194:195],v226 offset:29696\nv_cvt_pk_bf16_f32 v162,v162,v170\nM v[104:107],v[32:35],v[208:211],v[104:107]\n" \
"buffer_load_dwordx4 a[116:119],v240,s[52:55],s65 offen offset:1024 sc1\nM v[108:111],v[36:39],v[208:211],v[108:111]\nv_cvt_pk_bf16_f32 v163,v163,v171\nv_cvt_pk_bf16_f32 v178,v178,v186\ns_waitcnt lgkmcnt(6)\nM v[120:123],v[32:35],v[212:215],v[120:123]\nv_min_u32 v168,-2,v130\nv_min_u32 v169,-2,v146\n" \
"v_min_u32 v170,-2,v162\nv_min_u32 v171,-2,v178\nbuffer_store_dwordx4 v[168:171],v253,s[52:55],s33 offen offset:2048 sc1\nM v[124:127],v[36:39],v[212:215],v[124:127]\nds_read_b64_tr_b16 v[196:197],v227 offset:25600\nds_read_b64_tr_b16 v[198:199],v227 offset:29696\nM v[8:11],v[48:51],v[200:203],v[8:11]\nbuffer_load_dwordx4 a[120:123],v240,s[52:55],s65 offen offset:2048 sc1\n" \
"M v[12:15],v[52:55],v[200:203],v[12:15]\nds_read_b64_tr_b16 v[208:209],v226 offset:26112\nds_read_b64_tr_b16 v[210:211],v226 offset:30208\nM v[24:27],v[48:51],v[204:207],v[24:27]\nds_read_b64_tr_b16 v[212:213],v227 offset:26112\nds_read_b64_tr_b16 v[214:215],v227 offset:30208\nM v[28:31],v[52:55],v[204:207],v[28:31]\nv_add_u32 v226,0,v242\n" \
"s_waitcnt lgkmcnt(6)\nM v[40:43],v[48:51],v[216:219],v[40:43]\nbuffer_load_dwordx4 a[124:127],v240,s[52:55],s65 offen offset:3072 sc1\nM v[44:47],v[52:55],v[216:219],v[44:47]\nds_read_b64_tr_b16 v[200:201],v226 offset:0\nds_read_b64_tr_b16 v[202:203],v226 offset:4096\nM v[56:59],v[48:51],v[220:223],v[56:59]\nv_xor_b32 v227,32,v226\n" \
"ds_read_b64_tr_b16 v[204:205],v227 offset:0\nM v[60:63],v[52:55],v[220:223],v[60:63]\nds_read_b64_tr_b16 v[206:207],v227 offset:4096\ns_add_u32 s65,s14,s67\nM v[72:75],v[48:51],v[192:195],v[72:75]\nbuffer_load_dwordx4 a[128:131],v240,s[52:55],s65 offen offset:0 sc1\nM v[76:79],v[52:55],v[192:195],v[76:79]\nds_read_b64_tr_b16 v[216:217],v226 offset:512\n" \
"ds_read_b64_tr_b16 v[218:219],v226 offset:4608\ns_waitcnt lgkmcnt(6)\nM v[88:91],v[48:51],v[196:199],v[88:91]\nds_read_b64_tr_b16 v[220:221],v227 offset:512\nds_read_b64_tr_b16 v[222:223],v227 offset:4608\nM v[92:95],v[52:55],v[196:199],v[92:95]\nv_cvt_pk_bf16_f32 v179,v179,v187\nds_read_b64_tr_b16 v[192:193],v226 offset:1024\n" \
"M v[104:107],v[48:51],v[208:211],v[104:107]\nbuffer_load_dwordx4 a[132:135],v240,s[52:55],s65 offen offset:1024 sc1\nM v[108:111],v[52:55],v[208:211],v[108:111]\nds_read_b64_tr_b16 v[194:195],v226 offset:5120\nv_min_u32 v184,-2,v131\nM v[120:123],v[48:51],v[212:215],v[120:123]\nv_min_u32 v185,-2,v147\nv_min_u32 v186,-2,v163\n" \
"v_min_u32 v187,-2,v179\nbuffer_store_dwordx4 v[184:187],v253,s[52:55],s33 offen offset:3072 sc1\nM v[124:127],v[52:55],v[212:215],v[124:127]\nds_read_b64_tr_b16 v[196:197],v227 offset:1024\nds_read_b64_tr_b16 v[198:199],v227 offset:5120\ns_waitcnt vmcnt(18) lgkmcnt(6)\nM v[8:11],a[64:67],v[200:203],v[8:11]\nbuffer_load_dwordx4 a[136:139],v240,s[52:55],s65 offen offset:2048 sc1\n" \
"M v[12:15],a[68:71],v[200:203],v[12:15]\nds_read_b64_tr_b16 v[208:209],v226 offset:1536\nds_read_b64_tr_b16 v[210:211],v226 offset:5632\nM v[24:27],a[64:67],v[204:207],v[24:27]\nds_read_b64_tr_b16 v[212:213],v227 offset:1536\nds_read_b64_tr_b16 v[214:215],v227 offset:5632\nM v[28:31],a[68:71],v[204:207],v[28:31]\nds_read_b64_tr_b16 v[200:201],v226 offset:8192\n" \
"ds_read_b64_tr_b16 v[202:203],v226 offset:12288\nM v[40:43],a[64:67],v[216:219],v[40:43]\nbuffer_load_dwordx4 a[140:143],v240,s[52:55],s65 offen offset:3072 sc1\nM v[44:47],a[68:71],v[216:219],v[44:47]\nds_read_b64_tr_b16 v[204:205],v227 offset:8192\ns_waitcnt lgkmcnt(9)\nM v[56:59],a[64:67],v[220:223],v[56:59]\nds_read_b64_tr_b16 v[206:207],v227 offset:12288\n" \
"s_add_u32 s65,s65,4096\nM v[60:63],a[68:71],v[220:223],v[60:63]\nds_read_b64_tr_b16 v[216:217],v226 offset:8704\nds_read_b64_tr_b16 v[218:219],v226 offset:12800\nM v[72:75],a[64:67],v[192:195],v[72:75]\nbuffer_load_dwordx4 a[144:147],v240,s[52:55],s65 offen offset:0 sc1\nM v[76:79],a[68:71],v[192:195],v[76:79]\nds_read_b64_tr_b16 v[220:221],v227 offset:8704\n" \
"s_waitcnt lgkmcnt(8)\nM v[88:91],a[64:67],v[196:199],v[88:91]\nds_read_b64_tr_b16 v[222:223],v227 offset:12800\nv_cvt_pk_bf16_f32 v132,v132,v140\nM v[92:95],a[68:71],v[196:199],v[92:95]\nv_cvt_pk_bf16_f32 v133,v133,v141\nv_cvt_pk_bf16_f32 v134,v134,v142\nM v[104:107],a[64:67],v[208:211],v[104:107]\n" \
"buffer_load_dwordx4 a[148:151],v240,s[52:55],s65 offen offset:1024 sc1\nM v[108:111],a[68:71],v[208:211],v[108:111]\nv_cvt_pk_bf16_f32 v135,v135,v143\nv_cvt_pk_bf16_f32 v148,v148,v156\nv_cvt_pk_bf16_f32 v164,v164,v172\nv_cvt_pk_bf16_f32 v180,v180,v188\nds_read_b64_tr_b16 v[192:193],v226 offset:9216\nds_read_b64_tr_b16 v[194:195],v226 offset:13312\n" \
"s_waitcnt lgkmcnt(4)\nM v[120:123],a[64:67],v[212:215],v[120:123]\nv_min_u32 v140,-2,v132\nv_min_u32 v141,-2,v148\nv_min_u32 v142,-2,v164\nv_min_u32 v143,-2,v180\nbuffer_store_dwordx4 v[140:143],v253,s[52:55],s38 offen offset:0 sc1\nM v[124:127],a[68:71],v[212:215],v[124:127]\n" \
"ds_read_b64_tr_b16 v[196:197],v227 offset:9216\nds_read_b64_tr_b16 v[198:199],v227 offset:13312\nM v[8:11],a[72:75],v[200:203],v[8:11]\nbuffer_load_dwordx4 a[152:155],v240,s[52:55],s65 offen offset:2048 sc1\nM v[12:15],a[76:79],v[200:203],v[12:15]\nds_read_b64_tr_b16 v[208:209],v226 offset:9728\nds_read_b64_tr_b16 v[210:211],v226 offset:13824\nM v[24:27],a[72:75],v[204:207],v[24:27]\n" \
"ds_read_b64_tr_b16 v[212:213],v227 offset:9728\nds_read_b64_tr_b16 v[214:215],v227 offset:13824\nM v[28:31],a[76:79],v[204:207],v[28:31]\nds_read_b64_tr_b16 v[200:201],v226 offset:32768\nds_read_b64_tr_b16 v[202:203],v226 offset:36864\nM v[40:43],a[72:75],v[216:219],v[40:43]\nbuffer_load_dwordx4 a[156:159],v240,s[52:55],s65 offen offset:3072 sc1\nM v[44:47],a[76:79],v[216:219],v[44:47]\n" \
"ds_read_b64_tr_b16 v[204:205],v227 offset:32768\ns_waitcnt lgkmcnt(9)\nM v[56:59],a[72:75],v[220:223],v[56:59]\nds_read_b64_tr_b16 v[206:207],v227 offset:36864\nds_read_b64_tr_b16 v[216:217],v226 offset:33280\nM v[60:63],a[76:79],v[220:223],v[60:63]\nds_read_b64_tr_b16 v[218:219],v226 offset:37376\nds_read_b64_tr_b16 v[220:221],v227 offset:33280\n" \
"M v[72:75],a[72:75],v[192:195],v[72:75]\nds_read_b64_tr_b16 v[222:223],v227 offset:37376\nv_cvt_pk_bf16_f32 v149,v149,v157\nM v[76:79],a[76:79],v[192:195],v[76:79]\nv_cvt_pk_bf16_f32 v150,v150,v158\ns_waitcnt lgkmcnt(9)\nM v[88:91],a[72:75],v[196:199],v[88:91]\nv_cvt_pk_bf16_f32 v151,v151,v159\n" \
"v_cvt_pk_bf16_f32 v165,v165,v173\nM v[92:95],a[76:79],v[196:199],v[92:95]\nv_cvt_pk_bf16_f32 v181,v181,v189\nds_read_b64_tr_b16 v[192:193],v226 offset:33792\nM v[104:107],a[72:75],v[208:211],v[104:107]\nds_read_b64_tr_b16 v[194:195],v226 offset:37888\nv_min_u32 v156,-2,v133\nM v[108:111],a[76:79],v[208:211],v[108:111]\n" \
"v_min_u32 v157,-2,v149\ns_waitcnt lgkmcnt(6)\nM v[120:123],a[72:75],v[212:215],v[120:123]\nv_min_u32 v158,-2,v165\nv_min_u32 v159,-2,v181\nbuffer_store_dwordx4 v[156:159],v253,s[52:55],s38 offen offset:1024 sc1\nM v[124:127],a[76:79],v[212:215],v[124:127]\nds_read_b64_tr_b16 v[196:197],v227 offset:33792\n" \
"ds_read_b64_tr_b16 v[198:199],v227 offset:37888\ns_waitcnt vmcnt(24)\nM v[8:11],a[80:83],v[200:203],v[8:11]\nds_read_b64_tr_b16 v[208:209],v226 offset:34304\nds_read_b64_tr_b16 v[210:211],v226 offset:38400\nM v[12:15],a[84:87],v[200:203],v[12:15]\nds_read_b64_tr_b16 v[212:213],v227 offset:34304\nds_read_b64_tr_b16 v[214:215],v227 offset:38400\n" \
"M v[24:27],a[80:83],v[204:207],v[24:27]\nds_read_b64_tr_b16 v[200:201],v226 offset:40960\nds_read_b64_tr_b16 v[202:203],v226 offset:45056\nM v[28:31],a[84:87],v[204:207],v[28:31]\nds_read_b64_tr_b16 v[204:205],v227 offset:40960\ns_waitcnt lgkmcnt(9)\nM v[40:43],a[80:83],v[216:219],v[40:43]\nds_read_b64_tr_b16 v[206:207],v227 offset:45056\n" \
"v_cvt_pk_bf16_f32 v166,v166,v174\nM v[44:47],a[84:87],v[216:219],v[44:47]\nds_read_b64_tr_b16 v[216:217],v226 offset:41472\nds_read_b64_tr_b16 v[218:219],v226 offset:45568\nM v[56:59],a[80:83],v[220:223],v[56:59]\nv_cvt_pk_bf16_f32 v167,v167,v175\nv_cvt_pk_bf16_f32 v182,v182,v190\nM v[60:63],a[84:87],v[220:223],v[60:63]\n" \
"ds_read_b64_tr_b16 v[220:221],v227 offset:41472\nds_read_b64_tr_b16 v[222:223],v227 offset:45568\nM v[72:75],a[80:83],v[192:195],v[72:75]\nv_min_u32 v172,-2,v134\nv_min_u32 v173,-2,v150\nM v[76:79],a[84:87],v[192:195],v[76:79]\nds_read_b64_tr_b16 v[192:193],v226 offset:41984\ns_waitcnt lgkmcnt(8)\n" \
"M v[88:91],a[80:83],v[196:199],v[88:91]\nds_read_b64_tr_b16 v[194:195],v226 offset:46080\nv_min_u32 v174,-2,v166\nM v[92:95],a[84:87],v[196:199],v[92:95]\nv_min_u32 v175,-2,v182\nds_read_b64_tr_b16 v[196:197],v227 offset:41984\nM v[104:107],a[80:83],v[208:211],v[104:107]\nds_read_b64_tr_b16 v[198:199],v227 offset:46080\n" \
"v_cvt_pk_bf16_f32 v183,v183,v191\nM v[108:111],a[84:87],v[208:211],v[108:111]\nds_read_b64_tr_b16 v[208:209],v226 offset:42496\nds_read_b64_tr_b16 v[210:211],v226 offset:46592\nM v[120:123],a[80:83],v[212:215],v[120:123]\nbuffer_store_dwordx4 v[172:175],v253,s[52:55],s38 offen offset:2048 sc1\nM v[124:127],a[84:87],v[212:215],v[124:127]\nds_read_b64_tr_b16 v[212:213],v227 offset:42496\n" \
"s_waitcnt vmcnt(20) lgkmcnt(7)\nM v[8:11],a[88:91],v[200:203],v[8:11]\nds_read_b64_tr_b16 v[214:215],v227 offset:46592\nv_min_u32 v188,-2,v135\nM v[12:15],a[92:95],v[200:203],v[12:15]\nds_read_b64_tr_b16 v[200:201],v226 offset:16384\nds_read_b64_tr_b16 v[202:203],v226 offset:20480\nM v[24:27],a[88:91],v[204:207],v[24:27]\n" \
"v_min_u32 v189,-2,v151\nv_min_u32 v190,-2,v167\nM v[28:31],a[92:95],v[204:207],v[28:31]\nds_read_b64_tr_b16 v[204:205],v227 offset:16384\nds_read_b64_tr_b16 v[206:207],v227 offset:20480\nM v[40:43],a[88:91],v[216:219],v[40:43]\nv_min_u32 v191,-2,v183\nv_cmp_eq_u32 vcc,v228,v229\n" \
"M v[44:47],a[92:95],v[216:219],v[44:47]\nds_read_b64_tr_b16 v[216:217],v226 offset:16896\nds_read_b64_tr_b16 v[218:219],v226 offset:20992\nM v[56:59],a[88:91],v[220:223],v[56:59]\nv_and_b32 v228,1,v249\nv_lshlrev_b32 v228,4,v228\nM v[60:63],a[92:95],v[220:223],v[60:63]\nds_read_b64_tr_b16 v[220:221],v227 offset:16896\n" \
"s_waitcnt lgkmcnt(7)\nM v[72:75],a[88:91],v[192:195],v[72:75]\nds_read_b64_tr_b16 v[222:223],v227 offset:20992\nv_mov_b32 v229,0x3f80\nM v[76:79],a[92:95],v[192:195],v[76:79]\nds_read_b64_tr_b16 v[192:193],v226 offset:17408\nds_read_b64_tr_b16 v[194:195],v226 offset:21504\nM v[88:91],a[88:91],v[196:199],v[88:91]\n" \
"v_lshlrev_b32 v228,v228,v229\nv_cndmask_b32 v228,0,v228,vcc\nM v[92:95],a[92:95],v[196:199],v[92:95]\nds_read_b64_tr_b16 v[196:197],v227 offset:17408\nds_read_b64_tr_b16 v[198:199],v227 offset:21504\nM v[104:107],a[88:91],v[208:211],v[104:107]\nv_and_b32 v229,2,v249\nv_cmp_eq_u32 vcc,0,v229\n" \
"M v[108:111],a[92:95],v[208:211],v[108:111]\nds_read_b64_tr_b16 v[208:209],v226 offset:17920\nds_read_b64_tr_b16 v[210:211],v226 offset:22016\nM v[120:123],a[88:91],v[212:215],v[120:123]\nbuffer_store_dwordx4 v[188:191],v253,s[52:55],s38 offen offset:3072 sc1\nM v[124:127],a[92:95],v[212:215],v[124:127]\nds_read_b64_tr_b16 v[212:213],v227 offset:17920\ns_waitcnt lgkmcnt(7)\n" \
"M v[8:11],a[96:99],v[200:203],v[8:11]\nds_read_b64_tr_b16 v[214:215],v227 offset:22016\nv_cndmask_b32 v229,0,v228,vcc\nM v[12:15],a[100:103],v[200:203],v[12:15]\nds_read_b64_tr_b16 v[200:201],v226 offset:24576\nds_read_b64_tr_b16 v[202:203],v226 offset:28672\nM v[24:27],a[96:99],v[204:207],v[24:27]\nv_and_b32 v244,15,v249\n" \
"v_and_b32 v245,0x300,v240\nM v[28:31],a[100:103],v[204:207],v[28:31]\nds_read_b64_tr_b16 v[204:205],v227 offset:24576\nds_read_b64_tr_b16 v[206:207],v227 offset:28672\nM v[40:43],a[96:99],v[216:219],v[40:43]\nv_lshlrev_b32 v246,11,v249\nv_cmp_lt_u32 vcc,7,v249\nM v[44:47],a[100:103],v[216:219],v[44:47]\n" \
"ds_read_b64_tr_b16 v[216:217],v226 offset:25088\nds_read_b64_tr_b16 v[218:219],v226 offset:29184\nM v[56:59],a[96:99],v[220:223],v[56:59]\nv_mov_b32 v230,0x40000000\nv_lshl_or_b32 v244,v244,1,v245\nM v[60:63],a[100:103],v[220:223],v[60:63]\nds_read_b64_tr_b16 v[220:221],v227 offset:25088\ns_waitcnt lgkmcnt(7)\n" \
"M v[72:75],a[96:99],v[192:195],v[72:75]\nds_read_b64_tr_b16 v[222:223],v227 offset:29184\ns_add_u32 s70,s64,114688\nM v[76:79],a[100:103],v[192:195],v[76:79]\nds_read_b64_tr_b16 v[192:193],v226 offset:25600\nds_read_b64_tr_b16 v[194:195],v226 offset:29696\nM v[88:91],a[96:99],v[196:199],v[88:91]\nv_cndmask_b32 v246,v246,v230,vcc\n" \
"v_add_u32 v244,s70,v244\nM v[92:95],a[100:103],v[196:199],v[92:95]\nds_read_b64_tr_b16 v[196:197],v227 offset:25600\nds_read_b64_tr_b16 v[198:199],v227 offset:29696\nM v[104:107],a[96:99],v[208:211],v[104:107]\nv_add_u32 v245,s70,v240\ns_lshl_b32 s80,s60,11\nM v[108:111],a[100:103],v[208:211],v[108:111]\n" \
"ds_read_b64_tr_b16 v[208:209],v226 offset:26112\nds_read_b64_tr_b16 v[210:211],v226 offset:30208\nM v[120:123],a[96:99],v[212:215],v[120:123]\nv_and_b32 v230,3,v249\nv_lshrrev_b32 v231,2,v249\nM v[124:127],a[100:103],v[212:215],v[124:127]\nds_read_b64_tr_b16 v[212:213],v227 offset:26112\ns_waitcnt vmcnt(16) lgkmcnt(7)\n" \
"M v[8:11],a[104:107],v[200:203],v[8:11]\nds_read_b64_tr_b16 v[214:215],v227 offset:30208\nv_accvgpr_read_b32 v32,a176\nM v[12:15],a[108:111],v[200:203],v[12:15]\nds_read_b64_tr_b16 v[200:201],v226 offset:49152\nds_read_b64_tr_b16 v[202:203],v226 offset:53248\nM v[24:27],a[104:107],v[204:207],v[24:27]\nv_accvgpr_read_b32 v33,a177\n" \
"v_accvgpr_read_b32 v34,a178\nM v[28:31],a[108:111],v[204:207],v[28:31]\nds_read_b64_tr_b16 v[204:205],v227 offset:49152\nds_read_b64_tr_b16 v[206:207],v227 offset:53248\nM v[40:43],a[104:107],v[216:219],v[40:43]\nv_accvgpr_read_b32 v35,a179\nv_accvgpr_read_b32 v36,a180\nM v[44:47],a[108:111],v[216:219],v[44:47]\n" \
"ds_read_b64_tr_b16 v[216:217],v226 offset:49664\nds_read_b64_tr_b16 v[218:219],v226 offset:53760\nM v[56:59],a[104:107],v[220:223],v[56:59]\nv_accvgpr_read_b32 v37,a181\nv_accvgpr_read_b32 v38,a182\nM v[60:63],a[108:111],v[220:223],v[60:63]\nds_read_b64_tr_b16 v[220:221],v227 offset:49664\ns_waitcnt lgkmcnt(7)\n" \
"M v[72:75],a[104:107],v[192:195],v[72:75]\nds_read_b64_tr_b16 v[222:223],v227 offset:53760\nv_accvgpr_read_b32 v39,a183\nM v[76:79],a[108:111],v[192:195],v[76:79]\nds_read_b64_tr_b16 v[192:193],v226 offset:50176\nds_read_b64_tr_b16 v[194:195],v226 offset:54272\nM v[88:91],a[104:107],v[196:199],v[88:91]\ns_lshl3_add_u32 s37,s6,s75\n" \
"s_add_u32 s80,s80,557056\nM v[92:95],a[108:111],v[196:199],v[92:95]\nds_read_b64_tr_b16 v[196:197],v227 offset:50176\nds_read_b64_tr_b16 v[198:199],v227 offset:54272\nM v[104:107],a[104:107],v[208:211],v[104:107]\nv_lshl_add_u32 v231,v230,5,v231\ns_lshl_b32 s70,s7,8\nM v[108:111],a[108:111],v[208:211],v[108:111]\n" \
"ds_read_b64_tr_b16 v[208:209],v226 offset:50688\nds_read_b64_tr_b16 v[210:211],v226 offset:54784\nM v[120:123],a[104:107],v[212:215],v[120:123]\nv_add_f32 v32,1.0,v32\nv_add_f32 v33,1.0,v33\nM v[124:127],a[108:111],v[212:215],v[124:127]\nds_read_b64_tr_b16 v[212:213],v227 offset:50688\ns_waitcnt lgkmcnt(7)\n" \
"M v[8:11],a[112:115],v[200:203],v[8:11]\nds_read_b64_tr_b16 v[214:215],v227 offset:54784\nv_add_f32 v34,1.0,v34\nM v[12:15],a[116:119],v[200:203],v[12:15]\nds_read_b64_tr_b16 v[200:201],v226 offset:57344\nds_read_b64_tr_b16 v[202:203],v226 offset:61440\nM v[24:27],a[112:115],v[204:207],v[24:27]\nv_add_f32 v35,1.0,v35\n" \
"v_add_f32 v36,1.0,v36\nM v[28:31],a[116:119],v[204:207],v[28:31]\nds_read_b64_tr_b16 v[204:205],v227 offset:57344\nds_read_b64_tr_b16 v[206:207],v227 offset:61440\nM v[40:43],a[112:115],v[216:219],v[40:43]\nv_add_f32 v37,1.0,v37\nv_add_f32 v38,1.0,v38\nM v[44:47],a[116:119],v[216:219],v[44:47]\n" \
"ds_read_b64_tr_b16 v[216:217],v226 offset:57856\nds_read_b64_tr_b16 v[218:219],v226 offset:61952\nM v[56:59],a[112:115],v[220:223],v[56:59]\nv_add_f32 v39,1.0,v39\nv_add_f32 v116,1.0,v116\nM v[60:63],a[116:119],v[220:223],v[60:63]\nds_read_b64_tr_b16 v[220:221],v227 offset:57856\ns_waitcnt lgkmcnt(7)\n" \
"M v[72:75],a[112:115],v[192:195],v[72:75]\nds_read_b64_tr_b16 v[222:223],v227 offset:61952\nv_add_f32 v117,1.0,v117\nM v[76:79],a[116:119],v[192:195],v[76:79]\nds_read_b64_tr_b16 v[192:193],v226 offset:58368\nds_read_b64_tr_b16 v[194:195],v226 offset:62464\nM v[88:91],a[112:115],v[196:199],v[88:91]\ns_add_u32 s37,s37,557056\n" \
"v_lshlrev_b32 v251,5,v249\nM v[92:95],a[116:119],v[196:199],v[92:95]\nds_read_b64_tr_b16 v[196:197],v227 offset:58368\nds_read_b64_tr_b16 v[198:199],v227 offset:62464\nM v[104:107],a[112:115],v[208:211],v[104:107]\ns_or_b32 s80,s80,s75\nv_lshl_add_u32 v248,v231,1,s70\nM v[108:111],a[116:119],v[208:211],v[108:111]\n" \
"ds_read_b64_tr_b16 v[208:209],v226 offset:58880\nds_read_b64_tr_b16 v[210:211],v226 offset:62976\nM v[120:123],a[112:115],v[212:215],v[120:123]\nv_add_u32 v226,65536,v242\nv_accvgpr_read_b32 v0,a160\nM v[124:127],a[116:119],v[212:215],v[124:127]\nds_read_b64_tr_b16 v[212:213],v227 offset:58880\ns_waitcnt vmcnt(11) lgkmcnt(7)\n" \
"M v[8:11],a[120:123],v[200:203],v[8:11]\nds_read_b64_tr_b16 v[214:215],v227 offset:62976\nv_xor_b32 v227,32,v226\nM v[12:15],a[124:127],v[200:203],v[12:15]\nds_read_b64_tr_b16 v[200:201],v226 offset:0\nds_read_b64_tr_b16 v[202:203],v226 offset:4096\nM v[24:27],a[120:123],v[204:207],v[24:27]\nv_accvgpr_read_b32 v1,a161\n" \
"v_accvgpr_read_b32 v2,a162\nM v[28:31],a[124:127],v[204:207],v[28:31]\nds_read_b64_tr_b16 v[204:205],v227 offset:0\nds_read_b64_tr_b16 v[206:207],v227 offset:4096\nM v[40:43],a[120:123],v[216:219],v[40:43]\nv_accvgpr_read_b32 v3,a163\nv_accvgpr_read_b32 v4,a164\nM v[44:47],a[124:127],v[216:219],v[44:47]\n" \
"ds_read_b64_tr_b16 v[216:217],v226 offset:512\nds_read_b64_tr_b16 v[218:219],v226 offset:4608\nM v[56:59],a[120:123],v[220:223],v[56:59]\nv_accvgpr_read_b32 v5,a165\nv_accvgpr_read_b32 v6,a166\nM v[60:63],a[124:127],v[220:223],v[60:63]\nds_read_b64_tr_b16 v[220:221],v227 offset:512\ns_waitcnt lgkmcnt(7)\n" \
"M v[72:75],a[120:123],v[192:195],v[72:75]\nds_read_b64_tr_b16 v[222:223],v227 offset:4608\nv_accvgpr_read_b32 v7,a167\nM v[76:79],a[124:127],v[192:195],v[76:79]\nds_read_b64_tr_b16 v[192:193],v226 offset:1024\nds_read_b64_tr_b16 v[194:195],v226 offset:5120\nM v[88:91],a[120:123],v[196:199],v[88:91]\nv_accvgpr_read_b32 v16,a168\n" \
"v_accvgpr_read_b32 v17,a169\nM v[92:95],a[124:127],v[196:199],v[92:95]\nds_read_b64_tr_b16 v[196:197],v227 offset:1024\nds_read_b64_tr_b16 v[198:199],v227 offset:5120\nM v[104:107],a[120:123],v[208:211],v[104:107]\nv_accvgpr_read_b32 v18,a170\nv_accvgpr_read_b32 v19,a171\nM v[108:111],a[124:127],v[208:211],v[108:111]\n" \
"ds_read_b64_tr_b16 v[208:209],v226 offset:1536\nds_read_b64_tr_b16 v[210:211],v226 offset:5632\nM v[120:123],a[120:123],v[212:215],v[120:123]\nv_accvgpr_read_b32 v20,a172\nv_accvgpr_read_b32 v21,a173\nM v[124:127],a[124:127],v[212:215],v[124:127]\nds_read_b64_tr_b16 v[212:213],v227 offset:1536\ns_waitcnt lgkmcnt(7)\n" \
"M v[8:11],a[128:131],v[200:203],v[8:11]\nds_read_b64_tr_b16 v[214:215],v227 offset:5632\nv_accvgpr_read_b32 v22,a174\nM v[12:15],a[132:135],v[200:203],v[12:15]\nds_read_b64_tr_b16 v[200:201],v226 offset:8192\nds_read_b64_tr_b16 v[202:203],v226 offset:12288\nM v[24:27],a[128:131],v[204:207],v[24:27]\nv_accvgpr_read_b32 v23,a175\n" \
"v_accvgpr_read_b32 v48,a184\nM v[28:31],a[132:135],v[204:207],v[28:31]\nds_read_b64_tr_b16 v[204:205],v227 offset:8192\nds_read_b64_tr_b16 v[206:207],v227 offset:12288\nM v[40:43],a[128:131],v[216:219],v[40:43]\nv_accvgpr_read_b32 v49,a185\nv_accvgpr_read_b32 v50,a186\nM v[44:47],a[132:135],v[216:219],v[44:47]\n" \
"ds_read_b64_tr_b16 v[216:217],v226 offset:8704\nds_read_b64_tr_b16 v[218:219],v226 offset:12800\nM v[56:59],a[128:131],v[220:223],v[56:59]\nv_accvgpr_read_b32 v51,a187\nv_accvgpr_read_b32 v52,a188\nM v[60:63],a[132:135],v[220:223],v[60:63]\nds_read_b64_tr_b16 v[220:221],v227 offset:8704\ns_waitcnt lgkmcnt(7)\n" \
"M v[72:75],a[128:131],v[192:195],v[72:75]\nds_read_b64_tr_b16 v[222:223],v227 offset:12800\nv_accvgpr_read_b32 v53,a189\nM v[76:79],a[132:135],v[192:195],v[76:79]\nds_read_b64_tr_b16 v[192:193],v226 offset:9216\nds_read_b64_tr_b16 v[194:195],v226 offset:13312\nM v[88:91],a[128:131],v[196:199],v[88:91]\nv_accvgpr_read_b32 v54,a190\n" \
"v_accvgpr_read_b32 v55,a191\nM v[92:95],a[132:135],v[196:199],v[92:95]\nds_read_b64_tr_b16 v[196:197],v227 offset:9216\nds_read_b64_tr_b16 v[198:199],v227 offset:13312\nM v[104:107],a[128:131],v[208:211],v[104:107]\nv_lshlrev_b32 v64,16,v64\nv_lshlrev_b32 v65,16,v65\nM v[108:111],a[132:135],v[208:211],v[108:111]\n" \
"ds_read_b64_tr_b16 v[208:209],v226 offset:9728\nds_read_b64_tr_b16 v[210:211],v226 offset:13824\nM v[120:123],a[128:131],v[212:215],v[120:123]\nv_lshlrev_b32 v66,16,v66\nv_lshlrev_b32 v67,16,v67\nM v[124:127],a[132:135],v[212:215],v[124:127]\nds_read_b64_tr_b16 v[212:213],v227 offset:9728\ns_waitcnt vmcnt(6) lgkmcnt(7)\n" \
"M v[8:11],a[136:139],v[200:203],v[8:11]\nds_read_b64_tr_b16 v[214:215],v227 offset:13824\nv_lshlrev_b32 v68,16,v68\nM v[12:15],a[140:143],v[200:203],v[12:15]\nds_read_b64_tr_b16 v[200:201],v226 offset:32768\nds_read_b64_tr_b16 v[202:203],v226 offset:36864\nM v[24:27],a[136:139],v[204:207],v[24:27]\nv_lshlrev_b32 v69,16,v69\n" \
"v_lshlrev_b32 v70,16,v70\nM v[28:31],a[140:143],v[204:207],v[28:31]\nds_read_b64_tr_b16 v[204:205],v227 offset:32768\nds_read_b64_tr_b16 v[206:207],v227 offset:36864\nM v[40:43],a[136:139],v[216:219],v[40:43]\nv_lshlrev_b32 v71,16,v71\nv_lshlrev_b32 v80,16,v80\nM v[44:47],a[140:143],v[216:219],v[44:47]\n" \
"ds_read_b64_tr_b16 v[216:217],v226 offset:33280\nds_read_b64_tr_b16 v[218:219],v226 offset:37376\nM v[56:59],a[136:139],v[220:223],v[56:59]\nv_lshlrev_b32 v81,16,v81\nv_lshlrev_b32 v82,16,v82\nM v[60:63],a[140:143],v[220:223],v[60:63]\nds_read_b64_tr_b16 v[220:221],v227 offset:33280\ns_waitcnt lgkmcnt(7)\n" \
"M v[72:75],a[136:139],v[192:195],v[72:75]\nds_read_b64_tr_b16 v[222:223],v227 offset:37376\nv_lshlrev_b32 v83,16,v83\nM v[76:79],a[140:143],v[192:195],v[76:79]\nds_read_b64_tr_b16 v[192:193],v226 offset:33792\nds_read_b64_tr_b16 v[194:195],v226 offset:37888\nM v[88:91],a[136:139],v[196:199],v[88:91]\nv_lshlrev_b32 v84,16,v84\n" \
"v_lshlrev_b32 v85,16,v85\nM v[92:95],a[140:143],v[196:199],v[92:95]\nds_read_b64_tr_b16 v[196:197],v227 offset:33792\nds_read_b64_tr_b16 v[198:199],v227 offset:37888\nM v[104:107],a[136:139],v[208:211],v[104:107]\nv_lshlrev_b32 v86,16,v86\nv_lshlrev_b32 v87,16,v87\nM v[108:111],a[140:143],v[208:211],v[108:111]\n" \
"ds_read_b64_tr_b16 v[208:209],v226 offset:34304\nds_read_b64_tr_b16 v[210:211],v226 offset:38400\nM v[120:123],a[136:139],v[212:215],v[120:123]\nv_lshlrev_b32 v96,16,v96\nv_lshlrev_b32 v97,16,v97\nM v[124:127],a[140:143],v[212:215],v[124:127]\nds_read_b64_tr_b16 v[212:213],v227 offset:34304\ns_waitcnt lgkmcnt(7)\n" \
"M v[8:11],a[144:147],v[200:203],v[8:11]\nds_read_b64_tr_b16 v[214:215],v227 offset:38400\nv_lshlrev_b32 v98,16,v98\nM v[12:15],a[148:151],v[200:203],v[12:15]\nds_read_b64_tr_b16 v[200:201],v226 offset:40960\nds_read_b64_tr_b16 v[202:203],v226 offset:45056\nM v[24:27],a[144:147],v[204:207],v[24:27]\nv_lshlrev_b32 v99,16,v99\n" \
"v_lshlrev_b32 v100,16,v100\nM v[28:31],a[148:151],v[204:207],v[28:31]\nds_read_b64_tr_b16 v[204:205],v227 offset:40960\nds_read_b64_tr_b16 v[206:207],v227 offset:45056\nM v[40:43],a[144:147],v[216:219],v[40:43]\nv_lshlrev_b32 v101,16,v101\nv_lshlrev_b32 v102,16,v102\nM v[44:47],a[148:151],v[216:219],v[44:47]\n" \
"v_lshlrev_b32 v103,16,v103\nv_cvt_pk_bf16_f32 v32,0,v32\nM v[56:59],a[144:147],v[220:223],v[56:59]\nv_cvt_pk_bf16_f32 v33,0,v33\nv_cvt_pk_bf16_f32 v34,0,v34\nM v[60:63],a[148:151],v[220:223],v[60:63]\nv_cvt_pk_bf16_f32 v35,0,v35\ns_waitcnt lgkmcnt(4)\n" \
"M v[72:75],a[144:147],v[192:195],v[72:75]\nv_cvt_pk_bf16_f32 v36,0,v36\nv_cvt_pk_bf16_f32 v37,0,v37\nv_cvt_pk_bf16_f32 v38,0,v38\nv_cvt_pk_bf16_f32 v39,0,v39\nv_cvt_pk_bf16_f32 v116,0,v116\nv_cvt_pk_bf16_f32 v117,0,v117\nM v[76:79],a[148:151],v[192:195],v[76:79]\n" \
"ds_read_b64_tr_b16 v[216:217],v226 offset:41472\nds_read_b64_tr_b16 v[218:219],v226 offset:45568\nM v[88:91],a[144:147],v[196:199],v[88:91]\nds_read_b64_tr_b16 v[220:221],v227 offset:41472\nds_read_b64_tr_b16 v[222:223],v227 offset:45568\nM v[92:95],a[148:151],v[196:199],v[92:95]\nds_read_b64_tr_b16 v[192:193],v226 offset:41984\nds_read_b64_tr_b16 v[194:195],v226 offset:46080\n" \
"M v[104:107],a[144:147],v[208:211],v[104:107]\nds_read_b64_tr_b16 v[196:197],v227 offset:41984\nds_read_b64_tr_b16 v[198:199],v227 offset:46080\nM v[108:111],a[148:151],v[208:211],v[108:111]\nds_read_b64_tr_b16 v[208:209],v226 offset:42496\nds_read_b64_tr_b16 v[210:211],v226 offset:46592\nM v[120:123],a[144:147],v[212:215],v[120:123]\nv_xor_b32 v228,v228,v229\n" \
"s_add_u32 s38,s78,16384\nM v[124:127],a[148:151],v[212:215],v[124:127]\nds_read_b64_tr_b16 v[212:213],v227 offset:42496\ns_waitcnt vmcnt(3) lgkmcnt(11)\nM v[8:11],a[152:155],v[200:203],v[8:11]\nds_read_b64_tr_b16 v[214:215],v227 offset:46592\nv_accvgpr_write_b32 a192,v229\nM v[12:15],a[156:159],v[200:203],v[12:15]\n" \
"v_accvgpr_write_b32 a193,v228\nv_accvgpr_write_b32 a198,v229\nM v[24:27],a[152:155],v[204:207],v[24:27]\nv_accvgpr_write_b32 a199,v228\nM v[28:31],a[156:159],v[204:207],v[28:31]\ns_waitcnt lgkmcnt(6)\nM v[40:43],a[152:155],v[216:219],v[40:43]\nM v[44:47],a[156:159],v[216:219],v[44:47]\n" \
"M v[56:59],a[152:155],v[220:223],v[56:59]\nM v[60:63],a[156:159],v[220:223],v[60:63]\nM v[72:75],a[152:155],v[192:195],v[72:75]\nv_cvt_pk_bf16_f32 v8,v8,v24\nv_cvt_pk_bf16_f32 v9,v9,v25\nM v[76:79],a[156:159],v[192:195],v[76:79]\nv_cvt_pk_bf16_f32 v10,v10,v26\ns_waitcnt lgkmcnt(1)\n" \
"M v[88:91],a[152:155],v[196:199],v[88:91]\nv_cvt_pk_bf16_f32 v11,v11,v27\nv_cvt_pk_bf16_f32 v40,v40,v56\nM v[92:95],a[156:159],v[196:199],v[92:95]\nv_cvt_pk_bf16_f32 v41,v41,v57\nv_cvt_pk_bf16_f32 v42,v42,v58\nM v[104:107],a[152:155],v[208:211],v[104:107]\nv_cvt_pk_bf16_f32 v43,v43,v59\n" \
"v_cvt_pk_bf16_f32 v12,v12,v28\nM v[108:111],a[156:159],v[208:211],v[108:111]\nv_cvt_pk_bf16_f32 v72,v72,v88\ns_waitcnt lgkmcnt(0)\nM v[120:123],a[152:155],v[212:215],v[120:123]\nv_cvt_pk_bf16_f32 v73,v73,v89\nv_cvt_pk_bf16_f32 v74,v74,v90\nv_cvt_pk_bf16_f32 v75,v75,v91\n" \
"v_cvt_pk_bf16_f32 v13,v13,v29\nv_cvt_pk_bf16_f32 v14,v14,v30\nv_cvt_pk_bf16_f32 v15,v15,v31\nv_cvt_pk_bf16_f32 v44,v44,v60\nv_cvt_pk_bf16_f32 v45,v45,v61\nv_cvt_pk_bf16_f32 v104,v104,v120\nv_cvt_pk_bf16_f32 v105,v105,v121\nv_cvt_pk_bf16_f32 v106,v106,v122\n" \
"v_cvt_pk_bf16_f32 v107,v107,v123\nv_cvt_pk_bf16_f32 v46,v46,v62\nv_cvt_pk_bf16_f32 v47,v47,v63\nv_cvt_pk_bf16_f32 v76,v76,v92\nv_cvt_pk_bf16_f32 v77,v77,v93\nv_cvt_pk_bf16_f32 v78,v78,v94\nv_cvt_pk_bf16_f32 v79,v79,v95\nM v[124:127],a[156:159],v[212:215],v[124:127]\n" \
"v_min_u32 v24,-2,v8\nv_min_u32 v25,-2,v40\nv_min_u32 v26,-2,v72\nv_min_u32 v27,-2,v104\nv_min_u32 v56,-2,v9\nv_min_u32 v57,-2,v41\nv_min_u32 v58,-2,v73\nv_min_u32 v59,-2,v105\n" \
"v_cvt_pk_bf16_f32 v108,v108,v124\nv_cvt_pk_bf16_f32 v109,v109,v125\nv_cvt_pk_bf16_f32 v110,v110,v126\nv_cvt_pk_bf16_f32 v111,v111,v127\nv_min_u32 v88,-2,v10\nv_min_u32 v89,-2,v42\nv_min_u32 v90,-2,v74\nv_min_u32 v91,-2,v106\n" \
"v_min_u32 v120,-2,v11\nv_min_u32 v121,-2,v43\nv_min_u32 v122,-2,v75\nv_min_u32 v123,-2,v107\nv_min_u32 v28,-2,v12\nv_min_u32 v29,-2,v44\nv_min_u32 v30,-2,v76\nv_min_u32 v31,-2,v108\n" \
"v_min_u32 v60,-2,v13\nv_min_u32 v61,-2,v45\nv_min_u32 v62,-2,v77\nv_min_u32 v63,-2,v109\nv_min_u32 v92,-2,v14\nv_min_u32 v93,-2,v46\nv_min_u32 v94,-2,v78\nv_min_u32 v95,-2,v110\n" \
"v_min_u32 v124,-2,v15\nv_min_u32 v125,-2,v47\nv_min_u32 v126,-2,v79\nv_min_u32 v127,-2,v111\nbuffer_store_dwordx4 v[24:27],v253,s[52:55],s78 offen offset:0 sc1\nbuffer_store_dwordx4 v[56:59],v253,s[52:55],s78 offen offset:1024 sc1\nbuffer_store_dwordx4 v[88:91],v253,s[52:55],s78 offen offset:2048 sc1\nbuffer_store_dwordx4 v[120:123],v253,s[52:55],s78 offen offset:3072 sc1\n" \
"buffer_store_dwordx4 v[28:31],v253,s[52:55],s38 offen offset:0 sc1\nbuffer_store_dwordx4 v[60:63],v253,s[52:55],s38 offen offset:1024 sc1\nbuffer_store_dwordx4 v[92:95],v253,s[52:55],s38 offen offset:2048 sc1\nbuffer_store_dwordx4 v[124:127],v253,s[52:55],s38 offen offset:3072 sc1\n.Ltail:\ns_add_u32 s72,s95,0\nbuffer_load_dwordx4 v[160:163],v240,s[52:55],s72 offen sc1\ns_add_u32 s72,s95,2097152\n" \
"buffer_load_dwordx4 v[164:167],v240,s[52:55],s72 offen sc1\ns_add_u32 s72,s95,4194304\nbuffer_load_dwordx4 v[168:171],v240,s[52:55],s72 offen sc1\ns_add_u32 s72,s95,6291456\nbuffer_load_dwordx4 v[172:175],v240,s[52:55],s72 offen sc1\ns_add_u32 s72,s95,8388608\nbuffer_load_dwordx4 v[176:179],v240,s[52:55],s72 offen sc1\ns_add_u32 s72,s95,10485760\n" \
"buffer_load_dwordx4 v[180:183],v240,s[52:55],s72 offen sc1\ns_add_u32 s72,s95,12582912\nbuffer_load_dwordx4 v[184:187],v240,s[52:55],s72 offen sc1\ns_add_u32 s72,s95,14680064\nbuffer_load_dwordx4 v[188:191],v240,s[52:55],s72 offen sc1\n.Ltail2:\ns_add_u32 s72,s74,0\nbuffer_load_dwordx4 v[128:131],v240,s[52:55],s72 offen sc1\n" \
"s_add_u32 s72,s74,2097152\nbuffer_load_dwordx4 v[132:135],v240,s[52:55],s72 offen sc1\ns_add_u32 s72,s74,4194304\nbuffer_load_dwordx4 v[136:139],v240,s[52:55],s72 offen sc1\ns_add_u32 s72,s74,6291456\nbuffer_load_dwordx4 v[140:143],v240,s[52:55],s72 offen sc1\ns_add_u32 s72,s74,8388608\nbuffer_load_dwordx4 v[144:147],v240,s[52:55],s72 offen sc1\n" \
"s_add_u32 s72,s74,10485760\nbuffer_load_dwordx4 v[148:151],v240,s[52:55],s72 offen sc1\ns_add_u32 s72,s74,12582912\nbuffer_load_dwordx4 v[152:155],v240,s[52:55],s72 offen sc1\ns_add_u32 s72,s74,14680064\nbuffer_load_dwordx4 v[156:159],v240,s[52:55],s72 offen sc1\ns_cmp_eq_u32 s3,0\ns_cbranch_scc1 .Lnp\n" \
"s_mov_b32 s70,0x05040100\ns_mov_b32 s71,0x07060302\nv_accvgpr_read_b32 v228,a212\nv_accvgpr_read_b32 v229,a213\nv_accvgpr_read_b32 v230,a214\nv_accvgpr_read_b32 v231,a215\nv_perm_b32 v192,v230,v228,s70\nv_perm_b32 v193,v230,v228,s71\n" \
"v_perm_b32 v194,v231,v229,s70\nv_perm_b32 v195,v231,v229,s71\nv_accvgpr_read_b32 v228,a216\nv_accvgpr_read_b32 v229,a217\nv_accvgpr_read_b32 v230,a218\nv_accvgpr_read_b32 v231,a219\nv_perm_b32 v196,v230,v228,s70\nv_perm_b32 v197,v230,v228,s71\n" \
"v_perm_b32 v198,v231,v229,s70\nv_perm_b32 v199,v231,v229,s71\nv_accvgpr_read_b32 v228,a220\nv_accvgpr_read_b32 v229,a221\nv_accvgpr_read_b32 v230,a222\nv_accvgpr_read_b32 v231,a223\nv_perm_b32 v200,v230,v228,s70\nv_perm_b32 v201,v230,v228,s71\n" \
"v_perm_b32 v202,v231,v229,s70\nv_perm_b32 v203,v231,v229,s71\nv_accvgpr_read_b32 v228,a224\nv_accvgpr_read_b32 v229,a225\nv_accvgpr_read_b32 v230,a226\nv_accvgpr_read_b32 v231,a227\nv_perm_b32 v204,v230,v228,s70\nv_perm_b32 v205,v230,v228,s71\n" \
"v_perm_b32 v206,v231,v229,s70\nv_perm_b32 v207,v231,v229,s71\nv_accvgpr_read_b32 v228,a228\nv_accvgpr_read_b32 v229,a229\nv_accvgpr_read_b32 v230,a230\nv_accvgpr_read_b32 v231,a231\nv_perm_b32 v208,v230,v228,s70\nv_perm_b32 v209,v230,v228,s71\n" \
"v_perm_b32 v210,v231,v229,s70\nv_perm_b32 v211,v231,v229,s71\nv_accvgpr_read_b32 v228,a232\nv_accvgpr_read_b32 v229,a233\nv_accvgpr_read_b32 v230,a234\nv_accvgpr_read_b32 v231,a235\nv_perm_b32 v212,v230,v228,s70\nv_perm_b32 v213,v230,v228,s71\n" \
"v_perm_b32 v214,v231,v229,s70\nv_perm_b32 v215,v231,v229,s71\nv_accvgpr_read_b32 v228,a236\nv_accvgpr_read_b32 v229,a237\nv_accvgpr_read_b32 v230,a238\nv_accvgpr_read_b32 v231,a239\nv_perm_b32 v216,v230,v228,s70\nv_perm_b32 v217,v230,v228,s71\n" \
"v_perm_b32 v218,v231,v229,s70\nv_perm_b32 v219,v231,v229,s71\nv_accvgpr_read_b32 v228,a240\nv_accvgpr_read_b32 v229,a241\nv_accvgpr_read_b32 v230,a242\nv_accvgpr_read_b32 v231,a243\nv_perm_b32 v220,v230,v228,s70\nv_perm_b32 v221,v230,v228,s71\n" \
"v_perm_b32 v222,v231,v229,s70\nv_perm_b32 v223,v231,v229,s71\nv_and_b32 v228,3,v249\nv_lshrrev_b32 v229,4,v249\nv_and_b32 v230,1,v229\nv_lshrrev_b32 v229,1,v229\nv_lshlrev_b32 v230,5,v230\nv_lshl_or_b32 v230,v228,6,v230\n" \
"v_lshl_or_b32 v230,v229,8,v230\ns_lshl2_add_u32 s70,s7,84032\nv_add_u32 v252,s70,v230\nv_and_b32 v230,0xf0,v240\nv_add_u32 v253,84032,v230\n.Lnp:\n.Lrow1:\ns_waitcnt vmcnt(15)\n" \
"M v[72:75],a[192:195],v[160:163],0\nM v[76:79],a[196:199],v[160:163],0\ns_waitcnt vmcnt(14)\nM v[72:75],a[192:195],v[164:167],v[72:75]\nM v[76:79],a[196:199],v[164:167],v[76:79]\ns_waitcnt vmcnt(13)\nM v[72:75],a[192:195],v[168:171],v[72:75]\nM v[76:79],a[196:199],v[168:171],v[76:79]\n" \
"s_waitcnt vmcnt(12)\nM v[72:75],a[192:195],v[172:175],v[72:75]\nM v[76:79],a[196:199],v[172:175],v[76:79]\ns_waitcnt vmcnt(11)\nM v[72:75],a[192:195],v[176:179],v[72:75]\nM v[76:79],a[196:199],v[176:179],v[76:79]\ns_waitcnt vmcnt(10)\nM v[72:75],a[192:195],v[180:183],v[72:75]\n" \
"M v[76:79],a[196:199],v[180:183],v[76:79]\ns_waitcnt vmcnt(9)\nM v[72:75],a[192:195],v[184:187],v[72:75]\nM v[76:79],a[196:199],v[184:187],v[76:79]\ns_waitcnt vmcnt(8)\nM v[72:75],a[192:195],v[188:191],v[72:75]\nM v[76:79],a[196:199],v[188:191],v[76:79]\ns_sub_u32 s38,56622112,s95\n" \
"s_lshr_b32 s38,s38,5\ns_nop 4\nv_cvt_pk_bf16_f32 v72,0,v72\nv_cvt_pk_bf16_f32 v73,0,v73\nv_cvt_pk_bf16_f32 v74,0,v74\nv_cvt_pk_bf16_f32 v75,0,v75\nv_cvt_pk_bf16_f32 v76,0,v76\nv_cvt_pk_bf16_f32 v77,0,v77\n" \
"v_cvt_pk_bf16_f32 v78,0,v78\nv_cvt_pk_bf16_f32 v79,0,v79\nv_pk_mul_f32 v[72:73],v[72:73],v[0:1]\nv_pk_mul_f32 v[74:75],v[74:75],v[2:3]\nv_pk_mul_f32 v[76:77],v[76:77],v[4:5]\nv_pk_mul_f32 v[78:79],v[78:79],v[6:7]\nv_cvt_pk_bf16_f32 v72,0,v72\nv_cvt_pk_bf16_f32 v73,0,v73\n" \
"v_cvt_pk_bf16_f32 v74,0,v74\nv_cvt_pk_bf16_f32 v75,0,v75\nv_cvt_pk_bf16_f32 v76,0,v76\nv_cvt_pk_bf16_f32 v77,0,v77\nv_cvt_pk_bf16_f32 v78,0,v78\nv_cvt_pk_bf16_f32 v79,0,v79\nv_pk_add_f32 v[72:73],v[72:73],v[64:65]\nv_pk_add_f32 v[74:75],v[74:75],v[66:67]\n" \
"v_pk_add_f32 v[76:77],v[76:77],v[68:69]\nv_pk_add_f32 v[78:79],v[78:79],v[70:71]\nv_cvt_pk_bf16_f32 v72,0,v72\nv_cvt_pk_bf16_f32 v73,0,v73\nv_cvt_pk_bf16_f32 v74,0,v74\nv_cvt_pk_bf16_f32 v75,0,v75\nv_cvt_pk_bf16_f32 v76,0,v76\nv_cvt_pk_bf16_f32 v77,0,v77\n" \
"v_cvt_pk_bf16_f32 v78,0,v78\nv_cvt_pk_bf16_f32 v79,0,v79\nv_readfirstlane_b32 s70,v72\ns_nop 1\nv_subrev_f32 v104,s70,v72\nv_subrev_f32 v105,s70,v73\nv_subrev_f32 v106,s70,v74\nv_subrev_f32 v107,s70,v75\n" \
"v_subrev_f32 v108,s70,v76\nv_subrev_f32 v109,s70,v77\nv_subrev_f32 v110,s70,v78\nv_subrev_f32 v111,s70,v79\nv_pk_mul_f32 v[124:125],v[104:105],v[104:105]\nv_pk_add_f32 v[120:121],v[104:105],v[106:107]\nv_pk_fma_f32 v[124:125],v[106:107],v[106:107],v[124:125]\nv_pk_fma_f32 v[124:125],v[108:109],v[108:109],v[124:125]\n" \
"v_pk_fma_f32 v[124:125],v[110:111],v[110:111],v[124:125]\nv_pk_add_f32 v[108:109],v[108:109],v[110:111]\nv_pk_add_f32 v[120:121],v[120:121],v[108:109]\nv_add_f32 v120,v120,v121\nv_add_f32 v121,v124,v125\ns_nop 1\nv_permlane16_swap_b32 v120,v121\nv_add_f32 v120,v120,v121\n" \
"v_mov_b32 v121,v120\ns_nop 1\nv_permlane32_swap_b32 v120,v121\nv_add_f32 v120,v120,v121\ns_nop 1\nv_add_f32_dpp v120,v120,v120 quad_perm:[1,0,3,2] row_mask:0xf bank_mask:0xf\ns_nop 1\nv_add_f32_dpp v120,v120,v120 quad_perm:[2,3,0,1] row_mask:0xf bank_mask:0xf\n" \
"s_nop 1\nv_add_f32_dpp v120,v120,v120 row_half_mirror row_mask:0xf bank_mask:0xf\ns_nop 1\nv_add_f32_dpp v120,v120,v120 row_mirror row_mask:0xf bank_mask:0xf\nv_mul_f32 v120,0x3b000000,v120\nv_mov_b32 v121,v120\nv_cmp_u_f32 vcc,v120,v120\ns_nop 0\n" \
"v_permlane16_swap_b32 v120,v121\ns_cbranch_vccnz .Lrs1\n.Lrb1:\nv_fma_f32 v121,-v120,v120,v121\nv_add_f32 v122,s70,v120\nv_max_f32 v121,0,v121\nv_cvt_pk_bf16_f32 v122,0,v122\nv_cvt_pk_bf16_f32 v121,0,v121\n" \
"v_add_f32 v121,0x3727c5ac,v121\nv_cvt_pk_bf16_f32 v121,0,v121\nv_sqrt_f32 v121,v121\nv_pk_add_f32 v[88:89],v[72:73],v[122:123] op_sel_hi:[1,0] neg_lo:[0,1] neg_hi:[0,1]\nv_cvt_pk_bf16_f32 v121,0,v121\nv_rcp_f32 v123,v121\nv_cvt_pk_bf16_f32 v88,0,v88\nv_pk_add_f32 v[90:91],v[74:75],v[122:123] op_sel_hi:[1,0] neg_lo:[0,1] neg_hi:[0,1]\n" \
"v_pk_add_f32 v[92:93],v[76:77],v[122:123] op_sel_hi:[1,0] neg_lo:[0,1] neg_hi:[0,1]\nv_pk_add_f32 v[94:95],v[78:79],v[122:123] op_sel_hi:[1,0] neg_lo:[0,1] neg_hi:[0,1]\nv_cvt_pk_bf16_f32 v89,0,v89\nv_pk_mul_f32 v[88:89],v[88:89],v[122:123] op_sel:[0,1]\nv_cvt_pk_bf16_f32 v88,0,v88\nv_cvt_pk_bf16_f32 v89,0,v89\nv_pk_mul_f32 v[88:89],v[88:89],v[32:33]\nv_cvt_pk_bf16_f32 v88,0,v88\n" \
"v_cvt_pk_bf16_f32 v89,0,v89\nv_pk_add_f32 v[88:89],v[88:89],v[16:17]\nv_cvt_pk_bf16_f32 v88,0,v88\nv_cvt_pk_bf16_f32 v89,0,v89\nv_pk_mul_f32 v[124:125],v[88:89],v[96:97]\nv_cvt_pk_bf16_f32 v90,0,v90\nv_cvt_pk_bf16_f32 v91,0,v91\nv_pk_mul_f32 v[90:91],v[90:91],v[122:123] op_sel:[0,1]\n" \
"v_cvt_pk_bf16_f32 v90,0,v90\nv_cvt_pk_bf16_f32 v91,0,v91\nv_pk_mul_f32 v[90:91],v[90:91],v[34:35]\nv_cvt_pk_bf16_f32 v90,0,v90\nv_cvt_pk_bf16_f32 v91,0,v91\nv_pk_add_f32 v[90:91],v[90:91],v[18:19]\nv_cvt_pk_bf16_f32 v90,0,v90\nv_cvt_pk_bf16_f32 v91,0,v91\n" \
"v_pk_fma_f32 v[124:125],v[90:91],v[98:99],v[124:125]\nv_cvt_pk_bf16_f32 v92,0,v92\nv_cvt_pk_bf16_f32 v93,0,v93\nv_pk_mul_f32 v[92:93],v[92:93],v[122:123] op_sel:[0,1]\nv_cvt_pk_bf16_f32 v92,0,v92\nv_cvt_pk_bf16_f32 v93,0,v93\nv_pk_mul_f32 v[92:93],v[92:93],v[36:37]\nv_cvt_pk_bf16_f32 v92,0,v92\n" \
"v_cvt_pk_bf16_f32 v93,0,v93\nv_pk_add_f32 v[92:93],v[92:93],v[20:21]\nv_cvt_pk_bf16_f32 v92,0,v92\nv_cvt_pk_bf16_f32 v93,0,v93\nv_pk_fma_f32 v[124:125],v[92:93],v[100:101],v[124:125]\nv_cvt_pk_bf16_f32 v94,0,v94\nv_cvt_pk_bf16_f32 v95,0,v95\nv_pk_mul_f32 v[94:95],v[94:95],v[122:123] op_sel:[0,1]\n" \
"v_cvt_pk_bf16_f32 v94,0,v94\nv_cvt_pk_bf16_f32 v95,0,v95\nv_pk_mul_f32 v[94:95],v[94:95],v[38:39]\nv_cvt_pk_bf16_f32 v94,0,v94\nv_cvt_pk_bf16_f32 v95,0,v95\nv_pk_add_f32 v[94:95],v[94:95],v[22:23]\nv_cvt_pk_bf16_f32 v94,0,v94\nv_cvt_pk_bf16_f32 v95,0,v95\n" \
"v_pk_fma_f32 v[124:125],v[94:95],v[102:103],v[124:125]\nv_add_f32 v124,v124,v125\nv_mov_b32 v125,v124\nv_lshrrev_b32 v104,16,v121\nv_or_b32 v104,v104,v122\nv_permlane16_swap_b32 v124,v125\nv_add_f32 v124,v124,v125\nv_mov_b32 v125,v124\n" \
"s_nop 1\nv_permlane32_swap_b32 v124,v125\nv_add_f32 v124,v124,v125\ns_nop 1\nv_add_f32_dpp v124,v124,v124 quad_perm:[1,0,3,2] row_mask:0xf bank_mask:0xf\ns_nop 1\nv_add_f32_dpp v124,v124,v124 quad_perm:[2,3,0,1] row_mask:0xf bank_mask:0xf\ns_nop 1\n" \
"v_add_f32_dpp v124,v124,v124 row_half_mirror row_mask:0xf bank_mask:0xf\ns_nop 1\nv_add_f32_dpp v124,v124,v124 row_mirror row_mask:0xf bank_mask:0xf\nv_cvt_pk_bf16_f32 v124,0,v124\ns_nop 0\nv_readfirstlane_b32 s77,v124\ns_nop 0\ns_ashr_i32 s71,s77,31\n" \
"s_and_b32 s72,s77,0x7fffffff\ns_or_b32 s71,s71,0x80000000\ns_xor_b32 s77,s77,s71\ns_and_b32 s77,s77,0xffff0000\ns_cmp_gt_u32 s72,0x7f800000\ns_cselect_b32 s77,0,s77\ns_or_b32 s77,s77,s38\nv_mov_b32 v105,s77\n" \
"ds_max_u64 v250,v[104:105]\n.Lrow0:\ns_waitcnt vmcnt(7)\nM v[8:11],a[192:195],v[128:131],0\nM v[12:15],a[196:199],v[128:131],0\ns_waitcnt vmcnt(6)\nM v[8:11],a[192:195],v[132:135],v[8:11]\nM v[12:15],a[196:199],v[132:135],v[12:15]\n" \
"s_waitcnt vmcnt(5)\nM v[8:11],a[192:195],v[136:139],v[8:11]\nM v[12:15],a[196:199],v[136:139],v[12:15]\ns_waitcnt vmcnt(4)\nM v[8:11],a[192:195],v[140:143],v[8:11]\nM v[12:15],a[196:199],v[140:143],v[12:15]\ns_waitcnt vmcnt(3)\nM v[8:11],a[192:195],v[144:147],v[8:11]\n" \
"M v[12:15],a[196:199],v[144:147],v[12:15]\ns_waitcnt vmcnt(2)\nM v[8:11],a[192:195],v[148:151],v[8:11]\nM v[12:15],a[196:199],v[148:151],v[12:15]\ns_waitcnt vmcnt(1)\nM v[8:11],a[192:195],v[152:155],v[8:11]\nM v[12:15],a[196:199],v[152:155],v[12:15]\ns_waitcnt vmcnt(0)\n" \
"M v[8:11],a[192:195],v[156:159],v[8:11]\nM v[12:15],a[196:199],v[156:159],v[12:15]\ns_sub_u32 s39,56622112,s74\ns_lshr_b32 s39,s39,5\ns_nop 4\nv_cvt_pk_bf16_f32 v8,0,v8\nv_cvt_pk_bf16_f32 v9,0,v9\nv_cvt_pk_bf16_f32 v10,0,v10\n" \
"v_cvt_pk_bf16_f32 v11,0,v11\nv_cvt_pk_bf16_f32 v12,0,v12\nv_cvt_pk_bf16_f32 v13,0,v13\nv_cvt_pk_bf16_f32 v14,0,v14\nv_cvt_pk_bf16_f32 v15,0,v15\nv_pk_mul_f32 v[8:9],v[8:9],v[0:1]\nv_pk_mul_f32 v[10:11],v[10:11],v[2:3]\nv_pk_mul_f32 v[12:13],v[12:13],v[4:5]\n" \
"v_pk_mul_f32 v[14:15],v[14:15],v[6:7]\nv_cvt_pk_bf16_f32 v8,0,v8\nv_cvt_pk_bf16_f32 v9,0,v9\nv_cvt_pk_bf16_f32 v10,0,v10\nv_cvt_pk_bf16_f32 v11,0,v11\nv_cvt_pk_bf16_f32 v12,0,v12\nv_cvt_pk_bf16_f32 v13,0,v13\nv_cvt_pk_bf16_f32 v14,0,v14\n" \
"v_cvt_pk_bf16_f32 v15,0,v15\nv_pk_add_f32 v[8:9],v[8:9],v[80:81]\nv_pk_add_f32 v[10:11],v[10:11],v[82:83]\nv_pk_add_f32 v[12:13],v[12:13],v[84:85]\nv_pk_add_f32 v[14:15],v[14:15],v[86:87]\nv_cvt_pk_bf16_f32 v8,0,v8\nv_cvt_pk_bf16_f32 v9,0,v9\nv_cvt_pk_bf16_f32 v10,0,v10\n" \
"v_cvt_pk_bf16_f32 v11,0,v11\nv_cvt_pk_bf16_f32 v12,0,v12\nv_cvt_pk_bf16_f32 v13,0,v13\nv_cvt_pk_bf16_f32 v14,0,v14\nv_cvt_pk_bf16_f32 v15,0,v15\nv_readfirstlane_b32 s70,v8\ns_nop 1\nv_subrev_f32 v40,s70,v8\n" \
"v_subrev_f32 v41,s70,v9\nv_subrev_f32 v42,s70,v10\nv_subrev_f32 v43,s70,v11\nv_subrev_f32 v44,s70,v12\nv_subrev_f32 v45,s70,v13\nv_subrev_f32 v46,s70,v14\nv_subrev_f32 v47,s70,v15\nv_pk_mul_f32 v[60:61],v[40:41],v[40:41]\n" \
"v_pk_add_f32 v[56:57],v[40:41],v[42:43]\nv_pk_fma_f32 v[60:61],v[42:43],v[42:43],v[60:61]\nv_pk_fma_f32 v[60:61],v[44:45],v[44:45],v[60:61]\nv_pk_fma_f32 v[60:61],v[46:47],v[46:47],v[60:61]\nv_pk_add_f32 v[44:45],v[44:45],v[46:47]\nv_pk_add_f32 v[56:57],v[56:57],v[44:45]\nv_add_f32 v56,v56,v57\nv_add_f32 v57,v60,v61\n" \
"s_nop 1\nv_permlane16_swap_b32 v56,v57\nv_add_f32 v56,v56,v57\nv_mov_b32 v57,v56\ns_nop 1\nv_permlane32_swap_b32 v56,v57\nv_add_f32 v56,v56,v57\ns_nop 1\n" \
"v_add_f32_dpp v56,v56,v56 quad_perm:[1,0,3,2] row_mask:0xf bank_mask:0xf\ns_nop 1\nv_add_f32_dpp v56,v56,v56 quad_perm:[2,3,0,1] row_mask:0xf bank_mask:0xf\ns_nop 1\nv_add_f32_dpp v56,v56,v56 row_half_mirror row_mask:0xf bank_mask:0xf\ns_nop 1\nv_add_f32_dpp v56,v56,v56 row_mirror row_mask:0xf bank_mask:0xf\nv_mul_f32 v56,0x3b000000,v56\n" \
"v_mov_b32 v57,v56\nv_cmp_u_f32 vcc,v56,v56\ns_nop 0\nv_permlane16_swap_b32 v56,v57\ns_cbranch_vccnz .Lrs0\n.Lrb0:\nbuffer_load_dwordx4 v[232:235],v251,s[52:55],s80 offen sc1\nbuffer_load_dwordx4 v[236:239],v251,s[52:55],s80 offen offset:16 sc1\n" \
"v_fma_f32 v57,-v56,v56,v57\nv_add_f32 v58,s70,v56\nv_max_f32 v57,0,v57\nv_cvt_pk_bf16_f32 v58,0,v58\nv_cvt_pk_bf16_f32 v57,0,v57\nv_add_f32 v57,0x3727c5ac,v57\nv_cvt_pk_bf16_f32 v57,0,v57\nv_sqrt_f32 v57,v57\n" \
"v_pk_add_f32 v[24:25],v[8:9],v[58:59] op_sel_hi:[1,0] neg_lo:[0,1] neg_hi:[0,1]\nv_cvt_pk_bf16_f32 v57,0,v57\nv_rcp_f32 v59,v57\nv_cvt_pk_bf16_f32 v24,0,v24\nv_pk_add_f32 v[26:27],v[10:11],v[58:59] op_sel_hi:[1,0] neg_lo:[0,1] neg_hi:[0,1]\nv_pk_add_f32 v[28:29],v[12:13],v[58:59] op_sel_hi:[1,0] neg_lo:[0,1] neg_hi:[0,1]\nv_pk_add_f32 v[30:31],v[14:15],v[58:59] op_sel_hi:[1,0] neg_lo:[0,1] neg_hi:[0,1]\nv_cvt_pk_bf16_f32 v25,0,v25\n" \
"v_pk_mul_f32 v[24:25],v[24:25],v[58:59] op_sel:[0,1]\nv_cvt_pk_bf16_f32 v24,0,v24\nv_cvt_pk_bf16_f32 v25,0,v25\nv_pk_mul_f32 v[24:25],v[24:25],v[32:33]\nv_cvt_pk_bf16_f32 v24,0,v24\nv_cvt_pk_bf16_f32 v25,0,v25\nv_pk_add_f32 v[24:25],v[24:25],v[16:17]\nv_cvt_pk_bf16_f32 v24,0,v24\n" \
"v_cvt_pk_bf16_f32 v25,0,v25\nv_pk_mul_f32 v[60:61],v[24:25],v[96:97]\nv_cvt_pk_bf16_f32 v26,0,v26\nv_cvt_pk_bf16_f32 v27,0,v27\nv_pk_mul_f32 v[26:27],v[26:27],v[58:59] op_sel:[0,1]\nv_cvt_pk_bf16_f32 v26,0,v26\nv_cvt_pk_bf16_f32 v27,0,v27\nv_pk_mul_f32 v[26:27],v[26:27],v[34:35]\n" \
"v_cvt_pk_bf16_f32 v26,0,v26\nv_cvt_pk_bf16_f32 v27,0,v27\nv_pk_add_f32 v[26:27],v[26:27],v[18:19]\nv_cvt_pk_bf16_f32 v26,0,v26\nv_cvt_pk_bf16_f32 v27,0,v27\nv_pk_fma_f32 v[60:61],v[26:27],v[98:99],v[60:61]\nv_cvt_pk_bf16_f32 v28,0,v28\nv_cvt_pk_bf16_f32 v29,0,v29\n" \
"v_pk_mul_f32 v[28:29],v[28:29],v[58:59] op_sel:[0,1]\nv_cvt_pk_bf16_f32 v28,0,v28\nv_cvt_pk_bf16_f32 v29,0,v29\nv_pk_mul_f32 v[28:29],v[28:29],v[36:37]\nv_cvt_pk_bf16_f32 v28,0,v28\nv_cvt_pk_bf16_f32 v29,0,v29\nv_pk_add_f32 v[28:29],v[28:29],v[20:21]\nv_cvt_pk_bf16_f32 v28,0,v28\n" \
"v_cvt_pk_bf16_f32 v29,0,v29\nv_pk_fma_f32 v[60:61],v[28:29],v[100:101],v[60:61]\nv_cvt_pk_bf16_f32 v30,0,v30\nv_cvt_pk_bf16_f32 v31,0,v31\nv_pk_mul_f32 v[30:31],v[30:31],v[58:59] op_sel:[0,1]\nv_cvt_pk_bf16_f32 v30,0,v30\nv_cvt_pk_bf16_f32 v31,0,v31\nv_pk_mul_f32 v[30:31],v[30:31],v[38:39]\n" \
"v_cvt_pk_bf16_f32 v30,0,v30\nv_cvt_pk_bf16_f32 v31,0,v31\nv_pk_add_f32 v[30:31],v[30:31],v[22:23]\nv_cvt_pk_bf16_f32 v30,0,v30\nv_cvt_pk_bf16_f32 v31,0,v31\nv_pk_fma_f32 v[60:61],v[30:31],v[102:103],v[60:61]\nv_add_f32 v60,v60,v61\nv_mov_b32 v61,v60\n" \
"v_lshrrev_b32 v40,16,v57\nv_or_b32 v40,v40,v58\nv_permlane16_swap_b32 v60,v61\nv_add_f32 v60,v60,v61\nv_mov_b32 v61,v60\ns_nop 1\nv_permlane32_swap_b32 v60,v61\nv_add_f32 v60,v60,v61\n" \
"s_nop 1\nv_add_f32_dpp v60,v60,v60 quad_perm:[1,0,3,2] row_mask:0xf bank_mask:0xf\ns_nop 1\nv_add_f32_dpp v60,v60,v60 quad_perm:[2,3,0,1] row_mask:0xf bank_mask:0xf\ns_nop 1\nv_add_f32_dpp v60,v60,v60 row_half_mirror row_mask:0xf bank_mask:0xf\ns_nop 1\nv_add_f32_dpp v60,v60,v60 row_mirror row_mask:0xf bank_mask:0xf\n" \
"v_cvt_pk_bf16_f32 v60,0,v60\ns_nop 0\nv_readfirstlane_b32 s76,v60\ns_nop 0\ns_ashr_i32 s71,s76,31\ns_and_b32 s72,s76,0x7fffffff\ns_or_b32 s71,s71,0x80000000\ns_xor_b32 s76,s76,s71\n" \
"s_and_b32 s76,s76,0xffff0000\ns_cmp_gt_u32 s72,0x7f800000\ns_cselect_b32 s76,0,s76\ns_or_b32 s76,s76,s39\nv_mov_b32 v41,s76\nds_max_u64 v250,v[40:41]\ns_waitcnt lgkmcnt(0)\ns_barrier\n" \
"ds_read_b64 v[224:225],v250\ns_waitcnt lgkmcnt(0)\nbuffer_store_dwordx2 v[224:225],v246,s[52:55],s37 offen sc1\nv_readfirstlane_b32 s97,v225\ns_sub_u32 s95,s95,54525952\ns_sub_u32 s74,s74,54525952\ns_mov_b32 s94,0\ns_cmp_eq_u32 s97,s77\n" \
"s_cselect_b32 s65,s95,0x40000000\ns_cselect_b32 s95,0x40000000,s95\ns_cmp_eq_u32 s97,s76\ns_cselect_b32 s66,s74,0x40000000\ns_cselect_b32 s74,0x40000000,s74\ns_lshr_b32 s70,s6,7\ns_or_b32 s38,s7,s70\ns_cmp_lg_u32 s3,0\n" \
"s_waitcnt vmcnt(1)\ns_cbranch_scc1 .Lkp\nv_pk_mul_f32 v[88:89],v[88:89],v[48:49]\nv_pk_mul_f32 v[90:91],v[90:91],v[50:51]\nv_pk_mul_f32 v[92:93],v[92:93],v[52:53]\nv_pk_mul_f32 v[94:95],v[94:95],v[54:55]\nv_cvt_pk_bf16_f32 v88,0,v88\nv_cvt_pk_bf16_f32 v89,0,v89\n" \
"v_cvt_pk_bf16_f32 v90,0,v90\nv_cvt_pk_bf16_f32 v91,0,v91\nv_cvt_pk_bf16_f32 v92,0,v92\nv_cvt_pk_bf16_f32 v93,0,v93\nv_cvt_pk_bf16_f32 v94,0,v94\nv_cvt_pk_bf16_f32 v95,0,v95\nv_pk_add_f32 v[88:89],v[88:89],v[72:73]\nv_pk_add_f32 v[90:91],v[90:91],v[74:75]\n" \
"v_pk_add_f32 v[92:93],v[92:93],v[76:77]\nv_pk_add_f32 v[94:95],v[94:95],v[78:79]\nv_cvt_pk_bf16_f32 v88,0,v88\nds_write_b16_d16_hi v244,v88 offset:0\nv_cvt_pk_bf16_f32 v89,0,v89\nds_write_b16_d16_hi v244,v89 offset:32\nv_cvt_pk_bf16_f32 v90,0,v90\nds_write_b16_d16_hi v244,v90 offset:64\n" \
"v_cvt_pk_bf16_f32 v91,0,v91\nds_write_b16_d16_hi v244,v91 offset:96\nv_cvt_pk_bf16_f32 v92,0,v92\nds_write_b16_d16_hi v244,v92 offset:128\nv_cvt_pk_bf16_f32 v93,0,v93\nds_write_b16_d16_hi v244,v93 offset:160\nv_cvt_pk_bf16_f32 v94,0,v94\nds_write_b16_d16_hi v244,v94 offset:192\n" \
"v_cvt_pk_bf16_f32 v95,0,v95\nds_write_b16_d16_hi v244,v95 offset:224\nds_read_b128 v[64:67],v245 offset:0\nv_pk_mul_f32 v[24:25],v[24:25],v[48:49]\nv_pk_mul_f32 v[26:27],v[26:27],v[50:51]\nv_pk_mul_f32 v[28:29],v[28:29],v[52:53]\nv_pk_mul_f32 v[30:31],v[30:31],v[54:55]\nv_cvt_pk_bf16_f32 v24,0,v24\n" \
"v_cvt_pk_bf16_f32 v25,0,v25\nv_cvt_pk_bf16_f32 v26,0,v26\nv_cvt_pk_bf16_f32 v27,0,v27\nv_cvt_pk_bf16_f32 v28,0,v28\nv_cvt_pk_bf16_f32 v29,0,v29\nv_cvt_pk_bf16_f32 v30,0,v30\nv_cvt_pk_bf16_f32 v31,0,v31\nv_pk_add_f32 v[24:25],v[24:25],v[8:9]\n" \
"v_pk_add_f32 v[26:27],v[26:27],v[10:11]\nv_pk_add_f32 v[28:29],v[28:29],v[12:13]\nv_pk_add_f32 v[30:31],v[30:31],v[14:15]\nv_cvt_pk_bf16_f32 v24,0,v24\nds_write_b16_d16_hi v244,v24 offset:1024\nv_cvt_pk_bf16_f32 v25,0,v25\nds_write_b16_d16_hi v244,v25 offset:1056\nv_cvt_pk_bf16_f32 v26,0,v26\n" \
"ds_write_b16_d16_hi v244,v26 offset:1088\nv_cvt_pk_bf16_f32 v27,0,v27\nds_write_b16_d16_hi v244,v27 offset:1120\nv_cvt_pk_bf16_f32 v28,0,v28\nds_write_b16_d16_hi v244,v28 offset:1152\nv_cvt_pk_bf16_f32 v29,0,v29\nds_write_b16_d16_hi v244,v29 offset:1184\nv_cvt_pk_bf16_f32 v30,0,v30\n" \
"ds_write_b16_d16_hi v244,v30 offset:1216\nv_cvt_pk_bf16_f32 v31,0,v31\nds_write_b16_d16_hi v244,v31 offset:1248\nds_read_b128 v[68:71],v245 offset:1024\ns_waitcnt lgkmcnt(0)\nbuffer_store_dwordx4 v[64:67],v240,s[44:47],s95 offen sc1\nbuffer_store_dwordx4 v[68:71],v240,s[44:47],s74 offen sc1\ns_cmp_eq_u32 s7,0\n" \
"s_cselect_b32 s70,1,0\ns_xor_b32 s71,s65,s66\ns_or_b32 s70,s70,s71\ns_cbranch_scc0 .Lfin\ns_lshr_b32 s70,s6,3\ns_and_b32 s70,s70,15\ns_lshl4_add_u32 s98,s60,s70\nv_lshrrev_b32 v242,4,v249\n" \
"s_lshl_b32 s71,s98,2\nv_add_u32 v242,s71,v242\nv_and_b32 v244,15,v242\nv_bfe_u32 v245,v242,4,3\nv_lshl_add_u32 v244,v244,3,v245\nv_and_b32 v245,0x180,v242\nv_add_lshl_u32 v243,v244,v245,1\nv_lshlrev_b32 v244,1,v243\n" \
"s_mov_b32 s70,4096\nv_lshlrev_b32 v242,1,v242\nbuffer_load_dword v62,v244,s[52:55],0 offen offset:0 sc1\nbuffer_load_dword v63,v244,s[52:55],0 offen offset:2048 sc1\nbuffer_load_dword v126,v244,s[52:55],s70 offen offset:0 sc1\nbuffer_load_dword v127,v244,s[52:55],s70 offen offset:2048 sc1\n.Lkp:\ns_cmp_lg_u32 s7,0\n" \
"s_cbranch_scc1 .Lkl\nv_max3_u32 v228,v233,v235,v237\nv_min3_u32 v229,v233,v235,v237\nv_max_u32 v228,v228,v239\nv_min_u32 v229,v229,v239\nv_cmp_lt_u32 vcc,s94,v228\nv_cmp_eq_u32 s[90:91],0,v229\ns_cbranch_vccnz .Lnew\n" \
"s_cmp_eq_u64 s[90:91],0\ns_cbranch_scc1 .Lv0\ns_sub_u32 s73,s73,1\ns_cselect_b32 s73,0,s73\ns_cbranch_scc1 .Lv0\ns_sleep 3\nbuffer_load_dwordx4 v[232:235],v251,s[52:55],s80 offen sc1\nbuffer_load_dwordx4 v[236:239],v251,s[52:55],s80 offen offset:16 sc1\n" \
"s_waitcnt vmcnt(0)\ns_branch .Lkp\n.Lv0:\nv_mov_b32 v230,s96\ns_or_b32 s72,s94,2\nv_mov_b32 v231,s72\nds_write_b64 v250,v[230:231] offset:8\ns_branch .Lver\n" \
".Lkl:\nds_read_b64 v[230:231],v250 offset:8\ns_mov_b32 s91,0\ns_waitcnt lgkmcnt(0)\nv_readfirstlane_b32 s72,v231\nv_readfirstlane_b32 s96,v230\ns_and_b32 s90,s72,2\ns_xor_b32 s72,s72,s90\n" \
"s_xor_b32 s90,s90,2\ns_cmp_lg_u32 s72,s94\ns_mov_b32 s94,s72\ns_cbranch_scc1 .Lsp\ns_cmp_eq_u32 s90,0\ns_cbranch_scc1 .Lver\ns_sub_u32 s73,s73,1\ns_cselect_b32 s73,0,s73\n" \
"s_cbranch_scc1 .Lver\ns_sleep 1\n.Lyield:\ns_branch .Lkl\n.Lnew:\nv_mov_b32 v230,v228\nv_cmp_eq_u32 vcc,v228,v233\ns_cmp_eq_u64 s[90:91],0\n" \
"s_cselect_b32 s71,2,0\nv_max_u32_dpp v230,v230,v230 quad_perm:[1,0,3,2] row_mask:0xf bank_mask:0xf\nv_cndmask_b32 v229,v238,v232,vcc\nv_cmp_eq_u32 vcc,v228,v235\nv_max_u32_dpp v230,v230,v230 quad_perm:[2,3,0,1] row_mask:0xf bank_mask:0xf\ns_nop 1\nv_max_u32_dpp v230,v230,v230 row_half_mirror row_mask:0xf bank_mask:0xf\nv_cndmask_b32 v229,v229,v234,vcc\n" \
"v_cmp_eq_u32 vcc,v228,v237\nv_max_u32_dpp v230,v230,v230 row_mirror row_mask:0xf bank_mask:0xf\nv_mov_b32 v231,v230\nv_cndmask_b32 v229,v229,v236,vcc\ns_nop 0\nv_permlane16_swap_b32 v230,v231\nv_max_u32 v230,v230,v231\nv_mov_b32 v231,v230\n" \
"s_nop 1\nv_permlane32_swap_b32 v230,v231\nv_max_u32 v230,v230,v231\ns_nop 0\nv_readfirstlane_b32 s94,v230\ns_nop 1\nv_cmp_eq_u32 vcc,s94,v228\ns_nop 0\n" \
"s_ff1_i32_b64 s72,vcc\nv_readlane_b32 s96,v229,s72\ns_nop 1\nv_mov_b32 v230,s96\ns_or_b32 s72,s94,s71\nv_mov_b32 v231,s72\nds_write_b64 v250,v[230:231] offset:8\n.Lsp:\n" \
"s_not_b32 s15,s94\ns_bfe_u32 s15,s15,0xb0005\ns_lshl_b32 s15,s15,10\ns_cmp_eq_u32 s3,0\ns_cbranch_scc1 .Lkp\ns_add_u32 s72,s15,54525952\nbuffer_load_dword v129,v241,s[52:55],s72 offen sc1\ns_add_u32 s72,s15,56623104\n" \
"buffer_load_dword v131,v241,s[52:55],s72 offen sc1\ns_add_u32 s72,s15,58720256\nbuffer_load_dword v133,v241,s[52:55],s72 offen sc1\ns_add_u32 s72,s15,60817408\nbuffer_load_dword v135,v241,s[52:55],s72 offen sc1\ns_add_u32 s72,s15,62914560\nbuffer_load_dword v137,v241,s[52:55],s72 offen sc1\ns_add_u32 s72,s15,65011712\n" \
"buffer_load_dword v139,v241,s[52:55],s72 offen sc1\ns_add_u32 s72,s15,67108864\nbuffer_load_dword v141,v241,s[52:55],s72 offen sc1\ns_add_u32 s72,s15,69206016\nbuffer_load_dword v143,v241,s[52:55],s72 offen sc1\nv_add_u32 v146,s15,v248\nglobal_load_ushort v144,v146,s[8:9]\nglobal_load_ushort v145,v146,s[8:9] offset:32\n" \
"buffer_load_dwordx4 v[232:235],v251,s[52:55],s80 offen sc1\nbuffer_load_dwordx4 v[236:239],v251,s[52:55],s80 offen offset:16 sc1\nv_accvgpr_read_b32 v161,a200\nv_lshlrev_b32 v160,16,v161\nv_and_b32 v161,0xffff0000,v161\nv_accvgpr_read_b32 v163,a201\nv_lshlrev_b32 v162,16,v163\nv_and_b32 v163,0xffff0000,v163\n" \
"v_accvgpr_read_b32 v165,a202\nv_lshlrev_b32 v164,16,v165\nv_and_b32 v165,0xffff0000,v165\nv_accvgpr_read_b32 v167,a203\nv_lshlrev_b32 v166,16,v167\nv_and_b32 v167,0xffff0000,v167\nv_accvgpr_read_b32 v169,a204\nv_lshlrev_b32 v168,16,v169\n" \
"v_and_b32 v169,0xffff0000,v169\nv_accvgpr_read_b32 v171,a205\nv_lshlrev_b32 v170,16,v171\nv_and_b32 v171,0xffff0000,v171\nv_accvgpr_read_b32 v173,a206\nv_lshlrev_b32 v172,16,v173\nv_and_b32 v173,0xffff0000,v173\nv_accvgpr_read_b32 v175,a207\n" \
"v_lshlrev_b32 v174,16,v175\nv_and_b32 v175,0xffff0000,v175\nv_accvgpr_read_b32 v177,a208\nv_lshlrev_b32 v176,16,v177\nv_and_b32 v177,0xffff0000,v177\nv_accvgpr_read_b32 v179,a209\nv_lshlrev_b32 v178,16,v179\nv_and_b32 v179,0xffff0000,v179\n" \
"v_accvgpr_read_b32 v181,a210\nv_lshlrev_b32 v180,16,v181\nv_and_b32 v181,0xffff0000,v181\nv_accvgpr_read_b32 v183,a211\nv_lshlrev_b32 v182,16,v183\nv_and_b32 v183,0xffff0000,v183\nv_accvgpr_read_b32 v185,a252\nv_lshlrev_b32 v184,16,v185\n" \
"v_and_b32 v185,0xffff0000,v185\nv_accvgpr_read_b32 v187,a253\nv_lshlrev_b32 v186,16,v187\nv_and_b32 v187,0xffff0000,v187\nv_accvgpr_read_b32 v189,a254\nv_lshlrev_b32 v188,16,v189\nv_and_b32 v189,0xffff0000,v189\nv_accvgpr_read_b32 v191,a255\n" \
"v_lshlrev_b32 v190,16,v191\nv_and_b32 v191,0xffff0000,v191\ns_and_b32 s82,s96,0xffff0000\ns_lshl_b32 s83,s96,16\nv_rcp_f32 v148,s83\ns_waitcnt vmcnt(11)\nv_lshlrev_b32 v128,16,v129\nv_and_b32 v129,0xffff0000,v129\n" \
"s_waitcnt vmcnt(10)\nv_lshlrev_b32 v130,16,v131\nv_and_b32 v131,0xffff0000,v131\nv_pk_add_f32 v[128:129],v[128:129],v[130:131]\ns_waitcnt vmcnt(9)\nv_lshlrev_b32 v132,16,v133\nv_and_b32 v133,0xffff0000,v133\ns_waitcnt vmcnt(8)\n" \
"v_lshlrev_b32 v134,16,v135\nv_and_b32 v135,0xffff0000,v135\nv_pk_add_f32 v[132:133],v[132:133],v[134:135]\nv_pk_add_f32 v[128:129],v[128:129],v[132:133]\ns_waitcnt vmcnt(7)\nv_lshlrev_b32 v136,16,v137\nv_and_b32 v137,0xffff0000,v137\ns_waitcnt vmcnt(6)\n" \
"v_lshlrev_b32 v138,16,v139\nv_and_b32 v139,0xffff0000,v139\nv_pk_add_f32 v[136:137],v[136:137],v[138:139]\ns_waitcnt vmcnt(5)\nv_lshlrev_b32 v140,16,v141\nv_and_b32 v141,0xffff0000,v141\ns_waitcnt vmcnt(4)\nv_lshlrev_b32 v142,16,v143\n" \
"v_and_b32 v143,0xffff0000,v143\nv_pk_add_f32 v[140:141],v[140:141],v[142:143]\nv_pk_add_f32 v[136:137],v[136:137],v[140:141]\nv_pk_add_f32 v[128:129],v[128:129],v[136:137]\nv_cvt_pk_bf16_f32 v128,0,v128\nv_cvt_pk_bf16_f32 v129,0,v129\nv_pk_mul_f32 v[128:129],v[128:129],v[112:113]\nv_cvt_pk_bf16_f32 v128,0,v128\n" \
"v_cvt_pk_bf16_f32 v129,0,v129\ns_waitcnt vmcnt(3)\nv_lshlrev_b32 v144,16,v144\ns_waitcnt vmcnt(2)\nv_lshlrev_b32 v145,16,v145\nv_pk_add_f32 v[128:129],v[128:129],v[144:145]\nv_cvt_pk_bf16_f32 v128,0,v128\nv_cvt_pk_bf16_f32 v129,0,v129\n" \
"v_pk_add_f32 v[128:129],v[128:129],s[82:83] op_sel_hi:[1,0] neg_lo:[0,1] neg_hi:[0,1]\nv_cvt_pk_bf16_f32 v128,0,v128\nv_cvt_pk_bf16_f32 v129,0,v129\nv_pk_mul_f32 v[128:129],v[128:129],v[148:149] op_sel_hi:[1,0]\nv_cvt_pk_bf16_f32 v128,0,v128\nv_cvt_pk_bf16_f32 v129,0,v129\nv_pk_mul_f32 v[128:129],v[128:129],v[116:117]\nv_cvt_pk_bf16_f32 v128,0,v128\n" \
"v_cvt_pk_bf16_f32 v129,0,v129\nv_pk_add_f32 v[128:129],v[128:129],v[114:115]\nv_cvt_pk_bf16_f32 v128,v128,v129\ns_nop 1\nv_mov_b32_dpp v132,v128 quad_perm:[0,0,0,0] row_mask:0xf bank_mask:0xf\nv_mov_b32_dpp v133,v128 quad_perm:[1,1,1,1] row_mask:0xf bank_mask:0xf\nv_mov_b32_dpp v134,v128 quad_perm:[2,2,2,2] row_mask:0xf bank_mask:0xf\nv_mov_b32_dpp v135,v128 quad_perm:[3,3,3,3] row_mask:0xf bank_mask:0xf\n" \
"v_dot2_f32_bf16 v136,v192,v132,0\nv_dot2_f32_bf16 v137,v193,v132,0\nv_dot2_f32_bf16 v138,v194,v132,0\nv_dot2_f32_bf16 v139,v195,v132,0\nv_dot2_f32_bf16 v140,v208,v132,0\nv_dot2_f32_bf16 v141,v209,v132,0\nv_dot2_f32_bf16 v142,v210,v132,0\nv_dot2_f32_bf16 v143,v211,v132,0\n" \
"v_dot2_f32_bf16 v136,v196,v133,v136\nv_dot2_f32_bf16 v137,v197,v133,v137\nv_dot2_f32_bf16 v138,v198,v133,v138\nv_dot2_f32_bf16 v139,v199,v133,v139\nv_dot2_f32_bf16 v140,v212,v133,v140\nv_dot2_f32_bf16 v141,v213,v133,v141\nv_dot2_f32_bf16 v142,v214,v133,v142\nv_dot2_f32_bf16 v143,v215,v133,v143\n" \
"v_dot2_f32_bf16 v136,v200,v134,v136\nv_dot2_f32_bf16 v137,v201,v134,v137\nv_dot2_f32_bf16 v138,v202,v134,v138\nv_dot2_f32_bf16 v139,v203,v134,v139\nv_dot2_f32_bf16 v140,v216,v134,v140\nv_dot2_f32_bf16 v141,v217,v134,v141\nv_dot2_f32_bf16 v142,v218,v134,v142\nv_dot2_f32_bf16 v143,v219,v134,v143\n" \
"v_dot2_f32_bf16 v136,v204,v135,v136\nv_dot2_f32_bf16 v137,v205,v135,v137\nv_dot2_f32_bf16 v138,v206,v135,v138\nv_dot2_f32_bf16 v139,v207,v135,v139\nv_dot2_f32_bf16 v140,v220,v135,v140\nv_dot2_f32_bf16 v141,v221,v135,v141\nv_dot2_f32_bf16 v142,v222,v135,v142\nv_dot2_f32_bf16 v143,v223,v135,v143\n" \
"v_permlane32_swap_b32 v136,v140\nv_add_f32 v136,v136,v140\nv_permlane32_swap_b32 v137,v141\nv_add_f32 v137,v137,v141\nv_permlane32_swap_b32 v138,v142\nv_add_f32 v138,v138,v142\nv_permlane32_swap_b32 v139,v143\nv_add_f32 v139,v139,v143\n" \
"v_permlane16_swap_b32 v136,v138\nv_add_f32 v136,v136,v138\nv_permlane16_swap_b32 v137,v139\nv_add_f32 v137,v137,v139\nv_add_f32_dpp v136,v136,v136 row_ror:4 row_mask:0xf bank_mask:0xf\ns_nop 1\nv_add_f32_dpp v136,v136,v136 row_ror:8 row_mask:0xf bank_mask:0xf\nv_add_f32_dpp v137,v137,v137 row_ror:4 row_mask:0xf bank_mask:0xf\n" \
"s_nop 1\nv_add_f32_dpp v137,v137,v137 row_ror:8 row_mask:0xf bank_mask:0xf\ns_cmp_eq_u64 s[90:91],0\ns_cbranch_scc1 .Lver\ns_waitcnt vmcnt(0)\ns_branch .Lkp\n.Lver:\ns_cmp_eq_u32 s3,0\n" \
"s_cbranch_scc1 .Lnf\nds_write_b32 v252,v136\nds_write_b32 v252,v137 offset:16\ns_lshl_b32 s70,s2,6\ns_add_u32 s70,s70,98304\nv_lshlrev_b32 v241,1,v241\ns_waitcnt lgkmcnt(0)\ns_barrier\n" \
"ds_read_b128 v[128:131],v253\nds_read_b128 v[132:135],v253 offset:256\ns_waitcnt lgkmcnt(1)\nv_pk_add_f32 v[128:129],v[128:129],v[130:131]\nv_add_f32 v128,v128,v129\nv_cvt_pk_bf16_f32 v128,0,v128\nv_mul_f32 v129,0xbfb8aa3b,v128\nv_exp_f32 v129,v129\n" \
"s_waitcnt lgkmcnt(0)\nv_pk_add_f32 v[132:133],v[132:133],v[134:135]\nv_add_f32 v129,1.0,v129\nv_rcp_f32 v129,v129\nv_add_f32 v132,v132,v133\nv_cvt_pk_bf16_f32 v132,0,v132\nv_mul_f32 v128,v128,v129\nv_cvt_pk_bf16_f32 v128,0,v128\n" \
"v_mul_f32 v128,v128,v132\nv_cvt_pk_bf16_f32 v128,0,v128\ns_nop 0\nv_readlane_b32 s8,v128,0\nv_readlane_b32 s9,v128,1\nv_readlane_b32 s10,v128,2\nv_readlane_b32 s11,v128,3\nv_readlane_b32 s12,v128,4\n" \
"v_readlane_b32 s13,v128,5\nv_readlane_b32 s14,v128,6\nv_readlane_b32 s15,v128,7\nv_readlane_b32 s16,v128,8\nv_readlane_b32 s17,v128,9\nv_readlane_b32 s18,v128,10\nv_readlane_b32 s19,v128,11\nv_readlane_b32 s20,v128,12\n" \
"v_readlane_b32 s21,v128,13\nv_readlane_b32 s22,v128,14\nv_readlane_b32 s23,v128,15\nv_pk_mul_f32 v[136:137],v[160:161],s[8:9] op_sel_hi:[1,0]\nv_pk_mul_f32 v[138:139],v[162:163],s[8:9] op_sel:[0,1]\nv_pk_fma_f32 v[136:137],v[164:165],s[10:11],v[136:137] op_sel_hi:[1,0,1]\nv_pk_fma_f32 v[138:139],v[166:167],s[10:11],v[138:139] op_sel:[0,1,0]\nv_pk_fma_f32 v[136:137],v[168:169],s[12:13],v[136:137] op_sel_hi:[1,0,1]\n" \
"v_pk_fma_f32 v[138:139],v[170:171],s[12:13],v[138:139] op_sel:[0,1,0]\nv_pk_fma_f32 v[136:137],v[172:173],s[14:15],v[136:137] op_sel_hi:[1,0,1]\nv_pk_fma_f32 v[138:139],v[174:175],s[14:15],v[138:139] op_sel:[0,1,0]\nv_pk_fma_f32 v[136:137],v[176:177],s[16:17],v[136:137] op_sel_hi:[1,0,1]\nv_pk_fma_f32 v[138:139],v[178:179],s[16:17],v[138:139] op_sel:[0,1,0]\nv_pk_fma_f32 v[136:137],v[180:181],s[18:19],v[136:137] op_sel_hi:[1,0,1]\nv_pk_fma_f32 v[138:139],v[182:183],s[18:19],v[138:139] op_sel:[0,1,0]\nv_pk_fma_f32 v[136:137],v[184:185],s[20:21],v[136:137] op_sel_hi:[1,0,1]\n" \
"v_pk_fma_f32 v[138:139],v[186:187],s[20:21],v[138:139] op_sel:[0,1,0]\nv_pk_fma_f32 v[136:137],v[188:189],s[22:23],v[136:137] op_sel_hi:[1,0,1]\nv_pk_fma_f32 v[138:139],v[190:191],s[22:23],v[138:139] op_sel:[0,1,0]\nv_pk_add_f32 v[136:137],v[136:137],v[138:139]\nv_min_u32 v136,-2,v136\nv_min_u32 v137,-2,v137\nbuffer_store_dwordx2 v[136:137],v241,s[52:55],s70 offen sc1\nv_pk_mul_f32 v[88:89],v[88:89],v[48:49]\n" \
"v_pk_mul_f32 v[90:91],v[90:91],v[50:51]\nv_pk_mul_f32 v[92:93],v[92:93],v[52:53]\nv_pk_mul_f32 v[94:95],v[94:95],v[54:55]\nv_cvt_pk_bf16_f32 v88,0,v88\nv_cvt_pk_bf16_f32 v89,0,v89\nv_cvt_pk_bf16_f32 v90,0,v90\nv_cvt_pk_bf16_f32 v91,0,v91\nv_cvt_pk_bf16_f32 v92,0,v92\n" \
"v_cvt_pk_bf16_f32 v93,0,v93\nv_cvt_pk_bf16_f32 v94,0,v94\nv_cvt_pk_bf16_f32 v95,0,v95\nv_pk_add_f32 v[88:89],v[88:89],v[72:73]\nv_pk_add_f32 v[90:91],v[90:91],v[74:75]\nv_pk_add_f32 v[92:93],v[92:93],v[76:77]\nv_pk_add_f32 v[94:95],v[94:95],v[78:79]\nv_cvt_pk_bf16_f32 v88,0,v88\n" \
"ds_write_b16_d16_hi v244,v88 offset:0\nv_cvt_pk_bf16_f32 v89,0,v89\nds_write_b16_d16_hi v244,v89 offset:32\nv_cvt_pk_bf16_f32 v90,0,v90\nds_write_b16_d16_hi v244,v90 offset:64\nv_cvt_pk_bf16_f32 v91,0,v91\nds_write_b16_d16_hi v244,v91 offset:96\nv_cvt_pk_bf16_f32 v92,0,v92\n" \
"ds_write_b16_d16_hi v244,v92 offset:128\nv_cvt_pk_bf16_f32 v93,0,v93\nds_write_b16_d16_hi v244,v93 offset:160\nv_cvt_pk_bf16_f32 v94,0,v94\nds_write_b16_d16_hi v244,v94 offset:192\nv_cvt_pk_bf16_f32 v95,0,v95\nds_write_b16_d16_hi v244,v95 offset:224\nds_read_b128 v[64:67],v245 offset:0\n" \
"v_pk_mul_f32 v[24:25],v[24:25],v[48:49]\nv_pk_mul_f32 v[26:27],v[26:27],v[50:51]\nv_pk_mul_f32 v[28:29],v[28:29],v[52:53]\nv_pk_mul_f32 v[30:31],v[30:31],v[54:55]\nv_cvt_pk_bf16_f32 v24,0,v24\nv_cvt_pk_bf16_f32 v25,0,v25\nv_cvt_pk_bf16_f32 v26,0,v26\nv_cvt_pk_bf16_f32 v27,0,v27\n" \
"v_cvt_pk_bf16_f32 v28,0,v28\nv_cvt_pk_bf16_f32 v29,0,v29\nv_cvt_pk_bf16_f32 v30,0,v30\nv_cvt_pk_bf16_f32 v31,0,v31\nv_pk_add_f32 v[24:25],v[24:25],v[8:9]\nv_pk_add_f32 v[26:27],v[26:27],v[10:11]\nv_pk_add_f32 v[28:29],v[28:29],v[12:13]\nv_pk_add_f32 v[30:31],v[30:31],v[14:15]\n" \
"v_cvt_pk_bf16_f32 v24,0,v24\nds_write_b16_d16_hi v244,v24 offset:1024\nv_cvt_pk_bf16_f32 v25,0,v25\nds_write_b16_d16_hi v244,v25 offset:1056\nv_cvt_pk_bf16_f32 v26,0,v26\nds_write_b16_d16_hi v244,v26 offset:1088\nv_cvt_pk_bf16_f32 v27,0,v27\nds_write_b16_d16_hi v244,v27 offset:1120\n" \
"v_cvt_pk_bf16_f32 v28,0,v28\nds_write_b16_d16_hi v244,v28 offset:1152\nv_cvt_pk_bf16_f32 v29,0,v29\nds_write_b16_d16_hi v244,v29 offset:1184\nv_cvt_pk_bf16_f32 v30,0,v30\nds_write_b16_d16_hi v244,v30 offset:1216\nv_cvt_pk_bf16_f32 v31,0,v31\nds_write_b16_d16_hi v244,v31 offset:1248\n" \
"ds_read_b128 v[68:71],v245 offset:1024\ns_waitcnt lgkmcnt(0)\nbuffer_store_dwordx4 v[64:67],v240,s[44:47],s95 offen sc1\nbuffer_store_dwordx4 v[68:71],v240,s[44:47],s74 offen sc1\n.Lnf:\ns_cmp_eq_u32 s94,s97\ns_cbranch_scc1 .Lwin\nbuffer_store_dwordx4 v[64:67],v240,s[44:47],s65 offen sc1\n" \
"buffer_store_dwordx4 v[68:71],v240,s[44:47],s66 offen sc1\n.Lwin:\ns_cmp_lg_u32 s38,0\ns_cbranch_scc1 .Lfin\ns_add_u32 s72,s15,54525952\nbuffer_load_ushort v128,v243,s[52:55],s72 offen sc1\ns_add_u32 s72,s15,56623104\nbuffer_load_ushort v129,v243,s[52:55],s72 offen sc1\n" \
"s_add_u32 s72,s15,58720256\nbuffer_load_ushort v130,v243,s[52:55],s72 offen sc1\ns_add_u32 s72,s15,60817408\nbuffer_load_ushort v131,v243,s[52:55],s72 offen sc1\ns_add_u32 s72,s15,62914560\nbuffer_load_ushort v132,v243,s[52:55],s72 offen sc1\ns_add_u32 s72,s15,65011712\nbuffer_load_ushort v133,v243,s[52:55],s72 offen sc1\n" \
"s_add_u32 s72,s15,67108864\nbuffer_load_ushort v134,v243,s[52:55],s72 offen sc1\ns_add_u32 s72,s15,69206016\nbuffer_load_ushort v135,v243,s[52:55],s72 offen sc1\nv_add_u32 v242,s15,v242\nglobal_load_ushort v144,v242,s[8:9]\nv_min_u32 v168,55,v249\nv_lshlrev_b32 v168,11,v168\n" \
"s_lshl4_add_u32 s70,s98,98304\ns_add_u32 s71,s70,114688\ns_and_b32 s82,s96,0xffff0000\ns_lshl_b32 s83,s96,16\nv_rcp_f32 v148,s83\ns_ashr_i32 s37,s94,31\ns_not_b32 s37,s37\ns_or_b32 s37,s37,0x80000000\n" \
"s_xor_b32 s37,s37,s94\ns_and_b32 s37,s37,0xffff0000\nv_mov_b32 v149,0xbfb8aa3b\nv_mul_f32 v149,s37,v149\nv_exp_f32 v149,v149\nv_and_b32 v150,15,v249\nv_add_f32 v149,1.0,v149\nv_rcp_f32 v149,v149\n" \
"v_cmp_eq_u32 vcc,0,v150\nv_cvt_pk_bf16_f32 v149,0,v149\nv_mov_b32 v150,0x40000000\nv_cndmask_b32 v150,v150,v242,vcc\nv_cmp_gt_u32 s[90:91],56,v249\ns_waitcnt vmcnt(8)\nv_lshlrev_b32 v128,16,v128\ns_waitcnt vmcnt(7)\n" \
"v_lshlrev_b32 v129,16,v129\nv_add_f32 v128,v128,v129\ns_waitcnt vmcnt(6)\nv_lshlrev_b32 v130,16,v130\ns_waitcnt vmcnt(5)\nv_lshlrev_b32 v131,16,v131\nv_add_f32 v130,v130,v131\nv_add_f32 v128,v128,v130\n" \
"s_waitcnt vmcnt(4)\nv_lshlrev_b32 v132,16,v132\ns_waitcnt vmcnt(3)\nv_lshlrev_b32 v133,16,v133\nv_add_f32 v132,v132,v133\ns_waitcnt vmcnt(2)\nv_lshlrev_b32 v134,16,v134\ns_waitcnt vmcnt(1)\n" \
"v_lshlrev_b32 v135,16,v135\nv_add_f32 v134,v134,v135\nv_add_f32 v132,v132,v134\nv_add_f32 v128,v128,v132\nv_cvt_pk_bf16_f32 v128,0,v128\nv_mul_f32 v128,v128,v62\nv_cvt_pk_bf16_f32 v128,0,v128\ns_waitcnt vmcnt(0)\n" \
"v_add_f32 v126,1.0,v126\nv_cvt_pk_bf16_f32 v126,0,v126\nv_lshlrev_b32 v144,16,v144\nv_add_f32 v128,v128,v144\nv_cvt_pk_bf16_f32 v128,0,v128\nv_subrev_f32 v129,s82,v128\nv_cvt_pk_bf16_f32 v129,0,v129\nv_mul_f32 v129,v129,v148\n" \
"v_cvt_pk_bf16_f32 v129,0,v129\nv_mul_f32 v129,v129,v126\nv_cvt_pk_bf16_f32 v129,0,v129\nv_add_f32 v129,v129,v63\nv_cvt_pk_bf16_f32 v129,0,v129\n.Lp2:\nbuffer_load_dwordx4 v[160:163],v168,s[52:55],s70 offen sc1\nbuffer_load_dwordx4 v[164:167],v168,s[52:55],s71 offen sc1\n" \
"s_waitcnt vmcnt(0)\nv_max3_u32 v228,v160,v161,v162\nv_max3_u32 v229,v163,v164,v165\nv_max3_u32 v228,v228,v166,v167\nv_max_u32 v228,v228,v229\nv_pk_add_f32 v[160:161],v[160:161],v[164:165]\nv_cmp_eq_u32 vcc,-1,v228\nv_pk_add_f32 v[162:163],v[162:163],v[166:167]\n" \
"s_sub_u32 s73,s73,1\ns_cselect_b32 s73,0,s73\ns_cselect_b64 vcc,0,vcc\ns_cmp_lg_u64 vcc,0\ns_cselect_b32 s72,0x8000000,0\nv_cndmask_b32 v160,0,v160,s[90:91]\nv_cndmask_b32 v161,0,v161,s[90:91]\nv_cndmask_b32 v162,0,v162,s[90:91]\n" \
"v_cndmask_b32 v163,0,v163,s[90:91]\ns_nop 0\nv_permlane32_swap_b32 v160,v162\nv_permlane32_swap_b32 v161,v163\nv_pk_add_f32 v[160:161],v[160:161],v[162:163]\ns_nop 1\nv_permlane16_swap_b32 v160,v161\nv_add_f32 v160,v160,v161\n" \
"s_nop 1\nv_add_f32_dpp v160,v160,v160 quad_perm:[1,0,3,2] row_mask:0xf bank_mask:0xf\ns_nop 1\nv_add_f32_dpp v160,v160,v160 quad_perm:[2,3,0,1] row_mask:0xf bank_mask:0xf\ns_nop 1\nv_add_f32_dpp v160,v160,v160 row_half_mirror row_mask:0xf bank_mask:0xf\ns_nop 1\nv_add_f32_dpp v160,v160,v160 row_mirror row_mask:0xf bank_mask:0xf\n" \
"v_cvt_pk_bf16_f32 v160,0,v160\nv_mul_f32 v160,v160,v149\nv_cvt_pk_bf16_f32 v160,0,v160\nv_add_f32 v160,v160,v129\nv_cvt_pk_bf16_f32 v160,0,v160\nv_mul_f32 v160,v160,v127\nv_cvt_pk_bf16_f32 v160,0,v160\nv_add_f32 v160,v160,v128\n" \
"v_cvt_pk_bf16_f32 v160,0,v160\nbuffer_store_short_d16_hi v160,v150,s[44:47],s72 offen sc1\n.Lp2s:\ns_cbranch_scc1 .Lp2\ns_branch .Lfin\n.Lhs:\ns_sub_u32 s73,s73,1\ns_cselect_b32 s73,0,s73\n" \
"s_cbranch_scc1 .Lhb\ns_sleep 2\nbuffer_load_dwordx2 v[166:167],v165,s[52:55],s80 offen sc1\ns_waitcnt vmcnt(0)\nv_xor_b32 v166,v166,v230\nv_xor_b32 v167,v167,v231\nv_or_b32 v166,v166,v167\nv_cmp_ne_u32 vcc,0,v166\n" \
"s_nop 0\ns_cbranch_vccnz .Lhs\ns_branch .Lhb\n.Lps0:\ns_sub_u32 s73,s73,1\ns_cselect_b32 s73,0,s73\ns_cbranch_scc1 .Lpb0\ns_sleep 1\n" \
"buffer_load_dword v248,v228,s[52:55],s80 offen sc1\ns_waitcnt vmcnt(0)\nv_cmp_eq_u32 vcc,0,v248\ns_nop 0\ns_cbranch_vccnz .Lps0\ns_branch .Lpb0\n.Lps1:\ns_sub_u32 s73,s73,1\n" \
"s_cselect_b32 s73,0,s73\ns_cbranch_scc1 .Lpb1\ns_sleep 1\nbuffer_load_dword v248,v228,s[52:55],s80 offen sc1\ns_waitcnt vmcnt(0)\nv_cmp_eq_u32 vcc,0,v248\ns_nop 0\ns_cbranch_vccnz .Lps1\n" \
"s_branch .Lpb1\n.Lps2:\ns_sub_u32 s73,s73,1\ns_cselect_b32 s73,0,s73\ns_cbranch_scc1 .Lpb2\ns_sleep 1\nbuffer_load_dword v248,v251,s[52:55],s80 offen sc1\ns_waitcnt vmcnt(0)\n" \
"v_cmp_eq_u32 vcc,0,v248\ns_nop 0\ns_cbranch_vccnz .Lps2\ns_branch .Lpb2\n.Lps3:\ns_sub_u32 s73,s73,1\ns_cselect_b32 s73,0,s73\ns_cbranch_scc1 .Lpb3\n" \
"s_sleep 1\nbuffer_load_dword v248,v251,s[52:55],s80 offen sc1\ns_waitcnt vmcnt(0)\nv_cmp_eq_u32 vcc,0,v248\ns_nop 0\ns_cbranch_vccnz .Lps3\ns_branch .Lpb3\n.Lps4:\n" \
"s_sub_u32 s73,s73,1\ns_cselect_b32 s73,0,s73\ns_cbranch_scc1 .Lpb4\ns_sleep 1\nbuffer_load_dword v248,v251,s[52:55],s80 offen sc1\ns_waitcnt vmcnt(0)\nv_cmp_eq_u32 vcc,0,v248\ns_nop 0\n" \
"s_cbranch_vccnz .Lps4\ns_branch .Lpb4\n.Lps5:\ns_sub_u32 s73,s73,1\ns_cselect_b32 s73,0,s73\ns_cbranch_scc1 .Lpb5\ns_sleep 1\nbuffer_load_dword v248,v251,s[52:55],s80 offen sc1\n" \
"s_waitcnt vmcnt(0)\nv_cmp_eq_u32 vcc,0,v248\ns_nop 0\ns_cbranch_vccnz .Lps5\ns_branch .Lpb5\n.Lx01:\ns_cmp_eq_u32 s62,0\ns_cbranch_scc1 .Ltrans\n" \
"s_branch .Ldf\n.Lps6:\ns_sub_u32 s73,s73,1\ns_cselect_b32 s73,0,s73\ns_cbranch_scc1 .Lpb6\ns_sleep 1\nbuffer_load_dword v248,v251,s[52:55],s80 offen sc1\ns_waitcnt vmcnt(0)\n" \
"v_and_b32 v248,s17,v248\nv_cmp_eq_u32 vcc,0,v248\ns_nop 0\ns_cbranch_vccnz .Lps6\ns_branch .Lpb6\n.Lps7:\ns_sub_u32 s73,s73,1\ns_cselect_b32 s73,0,s73\n" \
"s_cbranch_scc1 .Lpb7\ns_sleep 1\nbuffer_load_dword v248,v254,s[52:55],s80 offen sc1\ns_waitcnt vmcnt(0)\nv_cmp_eq_u32 vcc,0,v248\ns_nop 0\ns_cbranch_vccnz .Lps7\ns_branch .Lpb7\n" \
".Lps8:\ns_sub_u32 s73,s73,1\ns_cselect_b32 s73,0,s73\ns_cbranch_scc1 .Lpb8\ns_sleep 1\nbuffer_load_dword v248,v251,s[52:55],s79 offen sc1\ns_waitcnt vmcnt(0)\nv_cmp_eq_u32 vcc,0,v248\n" \
"s_nop 0\ns_cbranch_vccnz .Lps8\ns_branch .Lpb8\n.Lrs1:\ns_mov_b32 s71,0\n.Lrl1:\ns_mov_b32 s97,0\nv_max3_u32 v124,v160,v161,v162\n" \
"v_max3_u32 v124,v124,v163,v163\nv_cmp_eq_u32 vcc,-1,v124\ns_add_u32 s72,s95,0\ns_cbranch_vccz .Lrz10\ns_mov_b64 exec,vcc\ns_mov_b32 s97,1\nbuffer_load_dwordx4 v[160:163],v240,s[52:55],s72 offen sc1\ns_mov_b64 exec,-1\n" \
".Lrz10:\nv_max3_u32 v124,v164,v165,v166\nv_max3_u32 v124,v124,v167,v167\nv_cmp_eq_u32 vcc,-1,v124\ns_add_u32 s72,s95,2097152\ns_cbranch_vccz .Lrz11\ns_mov_b64 exec,vcc\ns_mov_b32 s97,1\n" \
"buffer_load_dwordx4 v[164:167],v240,s[52:55],s72 offen sc1\ns_mov_b64 exec,-1\n.Lrz11:\nv_max3_u32 v124,v168,v169,v170\nv_max3_u32 v124,v124,v171,v171\nv_cmp_eq_u32 vcc,-1,v124\ns_add_u32 s72,s95,4194304\ns_cbranch_vccz .Lrz12\n" \
"s_mov_b64 exec,vcc\ns_mov_b32 s97,1\nbuffer_load_dwordx4 v[168:171],v240,s[52:55],s72 offen sc1\ns_mov_b64 exec,-1\n.Lrz12:\nv_max3_u32 v124,v172,v173,v174\nv_max3_u32 v124,v124,v175,v175\nv_cmp_eq_u32 vcc,-1,v124\n" \
"s_add_u32 s72,s95,6291456\ns_cbranch_vccz .Lrz13\ns_mov_b64 exec,vcc\ns_mov_b32 s97,1\nbuffer_load_dwordx4 v[172:175],v240,s[52:55],s72 offen sc1\ns_mov_b64 exec,-1\n.Lrz13:\nv_max3_u32 v124,v176,v177,v178\n" \
"v_max3_u32 v124,v124,v179,v179\nv_cmp_eq_u32 vcc,-1,v124\ns_add_u32 s72,s95,8388608\ns_cbranch_vccz .Lrz14\ns_mov_b64 exec,vcc\ns_mov_b32 s97,1\nbuffer_load_dwordx4 v[176:179],v240,s[52:55],s72 offen sc1\ns_mov_b64 exec,-1\n" \
".Lrz14:\nv_max3_u32 v124,v180,v181,v182\nv_max3_u32 v124,v124,v183,v183\nv_cmp_eq_u32 vcc,-1,v124\ns_add_u32 s72,s95,10485760\ns_cbranch_vccz .Lrz15\ns_mov_b64 exec,vcc\ns_mov_b32 s97,1\n" \
"buffer_load_dwordx4 v[180:183],v240,s[52:55],s72 offen sc1\ns_mov_b64 exec,-1\n.Lrz15:\nv_max3_u32 v124,v184,v185,v186\nv_max3_u32 v124,v124,v187,v187\nv_cmp_eq_u32 vcc,-1,v124\ns_add_u32 s72,s95,12582912\ns_cbranch_vccz .Lrz16\n" \
"s_mov_b64 exec,vcc\ns_mov_b32 s97,1\nbuffer_load_dwordx4 v[184:187],v240,s[52:55],s72 offen sc1\ns_mov_b64 exec,-1\n.Lrz16:\nv_max3_u32 v124,v188,v189,v190\nv_max3_u32 v124,v124,v191,v191\nv_cmp_eq_u32 vcc,-1,v124\n" \
"s_add_u32 s72,s95,14680064\ns_cbranch_vccz .Lrz17\ns_mov_b64 exec,vcc\ns_mov_b32 s97,1\nbuffer_load_dwordx4 v[188:191],v240,s[52:55],s72 offen sc1\ns_mov_b64 exec,-1\n.Lrz17:\ns_cmp_lg_u32 s97,0\n" \
"s_cbranch_scc1 .Lrm1\ns_cmp_eq_u32 s71,0\ns_cbranch_scc1 .Lrb1\ns_branch .Lrow1\n.Lrm1:\ns_mov_b32 s71,1\ns_waitcnt vmcnt(0)\ns_sub_u32 s73,s73,1\n" \
"s_cselect_b32 s73,0,s73\ns_cbranch_scc1 .Lrb1\ns_sleep 2\ns_branch .Lrl1\n.Lrs0:\ns_mov_b32 s71,0\n.Lrl0:\ns_mov_b32 s97,0\n" \
"v_max3_u32 v60,v128,v129,v130\nv_max3_u32 v60,v60,v131,v131\nv_cmp_eq_u32 vcc,-1,v60\ns_add_u32 s72,s74,0\ns_cbranch_vccz .Lrz00\ns_mov_b64 exec,vcc\ns_mov_b32 s97,1\nbuffer_load_dwordx4 v[128:131],v240,s[52:55],s72 offen sc1\n" \
"s_mov_b64 exec,-1\n.Lrz00:\nv_max3_u32 v60,v132,v133,v134\nv_max3_u32 v60,v60,v135,v135\nv_cmp_eq_u32 vcc,-1,v60\ns_add_u32 s72,s74,2097152\ns_cbranch_vccz .Lrz01\ns_mov_b64 exec,vcc\n" \
"s_mov_b32 s97,1\nbuffer_load_dwordx4 v[132:135],v240,s[52:55],s72 offen sc1\ns_mov_b64 exec,-1\n.Lrz01:\nv_max3_u32 v60,v136,v137,v138\nv_max3_u32 v60,v60,v139,v139\nv_cmp_eq_u32 vcc,-1,v60\ns_add_u32 s72,s74,4194304\n" \
"s_cbranch_vccz .Lrz02\ns_mov_b64 exec,vcc\ns_mov_b32 s97,1\nbuffer_load_dwordx4 v[136:139],v240,s[52:55],s72 offen sc1\ns_mov_b64 exec,-1\n.Lrz02:\nv_max3_u32 v60,v140,v141,v142\nv_max3_u32 v60,v60,v143,v143\n" \
"v_cmp_eq_u32 vcc,-1,v60\ns_add_u32 s72,s74,6291456\ns_cbranch_vccz .Lrz03\ns_mov_b64 exec,vcc\ns_mov_b32 s97,1\nbuffer_load_dwordx4 v[140:143],v240,s[52:55],s72 offen sc1\ns_mov_b64 exec,-1\n.Lrz03:\n" \
"v_max3_u32 v60,v144,v145,v146\nv_max3_u32 v60,v60,v147,v147\nv_cmp_eq_u32 vcc,-1,v60\ns_add_u32 s72,s74,8388608\ns_cbranch_vccz .Lrz04\ns_mov_b64 exec,vcc\ns_mov_b32 s97,1\nbuffer_load_dwordx4 v[144:147],v240,s[52:55],s72 offen sc1\n" \
"s_mov_b64 exec,-1\n.Lrz04:\nv_max3_u32 v60,v148,v149,v150\nv_max3_u32 v60,v60,v151,v151\nv_cmp_eq_u32 vcc,-1,v60\ns_add_u32 s72,s74,10485760\ns_cbranch_vccz .Lrz05\ns_mov_b64 exec,vcc\n" \
"s_mov_b32 s97,1\nbuffer_load_dwordx4 v[148:151],v240,s[52:55],s72 offen sc1\ns_mov_b64 exec,-1\n.Lrz05:\nv_max3_u32 v60,v152,v153,v154\nv_max3_u32 v60,v60,v155,v155\nv_cmp_eq_u32 vcc,-1,v60\ns_add_u32 s72,s74,12582912\n" \
"s_cbranch_vccz .Lrz06\ns_mov_b64 exec,vcc\ns_mov_b32 s97,1\nbuffer_load_dwordx4 v[152:155],v240,s[52:55],s72 offen sc1\ns_mov_b64 exec,-1\n.Lrz06:\nv_max3_u32 v60,v156,v157,v158\nv_max3_u32 v60,v60,v159,v159\n" \
"v_cmp_eq_u32 vcc,-1,v60\ns_add_u32 s72,s74,14680064\ns_cbranch_vccz .Lrz07\ns_mov_b64 exec,vcc\ns_mov_b32 s97,1\nbuffer_load_dwordx4 v[156:159],v240,s[52:55],s72 offen sc1\ns_mov_b64 exec,-1\n.Lrz07:\n" \
"s_cmp_lg_u32 s97,0\ns_cbranch_scc1 .Lrm0\ns_cmp_eq_u32 s71,0\ns_cbranch_scc1 .Lrb0\ns_branch .Lrow0\n.Lrm0:\ns_mov_b32 s71,1\ns_waitcnt vmcnt(0)\n" \
"s_sub_u32 s73,s73,1\ns_cselect_b32 s73,0,s73\ns_cbranch_scc1 .Lrb0\ns_sleep 2\ns_branch .Lrl0\n.Lredo:\ns_waitcnt vmcnt(0) lgkmcnt(0)\ns_barrier\n" \
"s_mov_b64 s[90:91],0\ns_mov_b32 s99,0\ns_mov_b32 s17,1\ns_lshl_b32 s70,s63,19\ns_add_u32 s37,s70,3145728\ns_lshl_b32 s71,s62,15\ns_add_u32 s72,s37,s64\ns_add_u32 s72,s72,491520\n" \
"s_sub_u32 s72,s72,s71\ns_add_u32 m0,s64,0\ns_nop 0\nbuffer_load_dwordx4 v240,s[52:55],s72 offen offset:0 sc1 lds\nbuffer_load_dwordx4 v240,s[52:55],s72 offen offset:1024 sc1 lds\nbuffer_load_dwordx4 v240,s[52:55],s72 offen offset:2048 sc1 lds\nbuffer_load_dwordx4 v240,s[52:55],s72 offen offset:3072 sc1 lds\ns_add_u32 m0,s64,65536\n" \
"s_add_u32 s72,s72,16777216\nbuffer_load_dwordx4 v240,s[52:55],s72 offen offset:0 sc1 lds\nbuffer_load_dwordx4 v240,s[52:55],s72 offen offset:1024 sc1 lds\nbuffer_load_dwordx4 v240,s[52:55],s72 offen offset:2048 sc1 lds\nbuffer_load_dwordx4 v240,s[52:55],s72 offen offset:3072 sc1 lds\ns_add_u32 s72,s37,s64\ns_add_u32 s72,s72,507904\ns_sub_u32 s72,s72,s71\n" \
"s_add_u32 m0,s64,16384\ns_nop 0\nbuffer_load_dwordx4 v240,s[52:55],s72 offen offset:0 sc1 lds\nbuffer_load_dwordx4 v240,s[52:55],s72 offen offset:1024 sc1 lds\nbuffer_load_dwordx4 v240,s[52:55],s72 offen offset:2048 sc1 lds\nbuffer_load_dwordx4 v240,s[52:55],s72 offen offset:3072 sc1 lds\ns_add_u32 m0,s64,81920\ns_add_u32 s72,s72,16777216\n" \
"buffer_load_dwordx4 v240,s[52:55],s72 offen offset:0 sc1 lds\nbuffer_load_dwordx4 v240,s[52:55],s72 offen offset:1024 sc1 lds\nbuffer_load_dwordx4 v240,s[52:55],s72 offen offset:2048 sc1 lds\nbuffer_load_dwordx4 v240,s[52:55],s72 offen offset:3072 sc1 lds\ns_add_u32 s72,s37,s64\ns_add_u32 s72,s72,16384\ns_add_u32 s72,s72,s71\ns_add_u32 m0,s64,32768\n" \
"s_nop 0\nbuffer_load_dwordx4 v240,s[52:55],s72 offen offset:0 sc1 lds\nbuffer_load_dwordx4 v240,s[52:55],s72 offen offset:1024 sc1 lds\nbuffer_load_dwordx4 v240,s[52:55],s72 offen offset:2048 sc1 lds\nbuffer_load_dwordx4 v240,s[52:55],s72 offen offset:3072 sc1 lds\ns_add_u32 m0,s64,98304\ns_add_u32 s72,s72,16777216\nbuffer_load_dwordx4 v240,s[52:55],s72 offen offset:0 sc1 lds\n" \
"buffer_load_dwordx4 v240,s[52:55],s72 offen offset:1024 sc1 lds\nbuffer_load_dwordx4 v240,s[52:55],s72 offen offset:2048 sc1 lds\nbuffer_load_dwordx4 v240,s[52:55],s72 offen offset:3072 sc1 lds\ns_add_u32 s72,s37,s64\ns_add_u32 s72,s72,0\ns_add_u32 s72,s72,s71\ns_add_u32 m0,s64,49152\ns_nop 0\n" \
"buffer_load_dwordx4 v240,s[52:55],s72 offen offset:0 sc1 lds\nbuffer_load_dwordx4 v240,s[52:55],s72 offen offset:1024 sc1 lds\nbuffer_load_dwordx4 v240,s[52:55],s72 offen offset:2048 sc1 lds\nbuffer_load_dwordx4 v240,s[52:55],s72 offen offset:3072 sc1 lds\ns_add_u32 m0,s64,114688\ns_add_u32 s72,s72,16777216\nbuffer_load_dwordx4 v240,s[52:55],s72 offen offset:0 sc1 lds\nbuffer_load_dwordx4 v240,s[52:55],s72 offen offset:1024 sc1 lds\n" \
"buffer_load_dwordx4 v240,s[52:55],s72 offen offset:2048 sc1 lds\nbuffer_load_dwordx4 v240,s[52:55],s72 offen offset:3072 sc1 lds\nv_and_b32 v251,15,v249\nv_lshrrev_b32 v250,4,v249\nv_lshlrev_b32 v250,2,v250\nv_sub_u32 v251,v251,v250\ns_lshl_b32 s70,s62,7\ns_lshl_b32 s72,s7,5\n" \
"s_sub_u32 s38,s72,s70\ns_add_u32 s38,s38,1920\ns_sub_u32 s39,s70,s72\ns_add_u32 s39,s39,96\nv_add_u32 v225,s38,v251\nv_add_u32 v225,0,v225\nv_mov_b32 v224,0xff800000\nv_add_u32 v229,s38,v251\n" \
"v_add_u32 v229,16,v229\nv_mov_b32 v228,0xff800000\nv_add_u32 v233,s39,v251\nv_add_u32 v233,0,v233\nv_mov_b32 v232,0xff800000\nv_add_u32 v237,s39,v251\nv_add_u32 v237,16,v237\nv_mov_b32 v236,0xff800000\n" \
"s_sub_u32 s38,524288,s71\ns_add_u32 s38,s38,s37\n.Lrl:\nbuffer_load_dwordx4 a[0:3],v240,s[52:55],s37 offen offset:0 sc1\nbuffer_load_dwordx4 a[4:7],v240,s[52:55],s37 offen offset:1024 sc1\nbuffer_load_dwordx4 a[8:11],v240,s[52:55],s37 offen offset:2048 sc1\nbuffer_load_dwordx4 a[12:15],v240,s[52:55],s37 offen offset:3072 sc1\ns_add_u32 s37,s37,4096\n" \
"buffer_load_dwordx4 a[16:19],v240,s[52:55],s37 offen offset:0 sc1\nbuffer_load_dwordx4 a[20:23],v240,s[52:55],s37 offen offset:1024 sc1\nbuffer_load_dwordx4 a[24:27],v240,s[52:55],s37 offen offset:2048 sc1\nbuffer_load_dwordx4 a[28:31],v240,s[52:55],s37 offen offset:3072 sc1\ns_add_u32 s37,s37,4096\nbuffer_load_dwordx4 a[32:35],v240,s[52:55],s37 offen offset:0 sc1\nbuffer_load_dwordx4 a[36:39],v240,s[52:55],s37 offen offset:1024 sc1\nbuffer_load_dwordx4 a[40:43],v240,s[52:55],s37 offen offset:2048 sc1\n" \
"buffer_load_dwordx4 a[44:47],v240,s[52:55],s37 offen offset:3072 sc1\ns_add_u32 s37,s37,4096\nbuffer_load_dwordx4 a[48:51],v240,s[52:55],s37 offen offset:0 sc1\nbuffer_load_dwordx4 a[52:55],v240,s[52:55],s37 offen offset:1024 sc1\nbuffer_load_dwordx4 a[56:59],v240,s[52:55],s37 offen offset:2048 sc1\nbuffer_load_dwordx4 a[60:63],v240,s[52:55],s37 offen offset:3072 sc1\ns_add_u32 s37,s37,4096\ns_waitcnt vmcnt(15)\n" \
"M v[128:131],a[0:3],a[128:131],0\ns_waitcnt vmcnt(14)\nM v[128:131],a[4:7],a[132:135],v[128:131]\ns_waitcnt vmcnt(13)\nM v[128:131],a[8:11],a[136:139],v[128:131]\ns_waitcnt vmcnt(12)\nM v[128:131],a[12:15],a[140:143],v[128:131]\nM v[132:135],a[0:3],a[144:147],0\n" \
"M v[132:135],a[4:7],a[148:151],v[132:135]\nM v[132:135],a[8:11],a[152:155],v[132:135]\nM v[132:135],a[12:15],a[156:159],v[132:135]\nM v[136:139],a[0:3],a[160:163],0\nM v[136:139],a[4:7],a[164:167],v[136:139]\nM v[136:139],a[8:11],a[168:171],v[136:139]\nM v[136:139],a[12:15],a[172:175],v[136:139]\nM v[140:143],a[0:3],a[176:179],0\n" \
"M v[140:143],a[4:7],a[180:183],v[140:143]\nM v[140:143],a[8:11],a[184:187],v[140:143]\nM v[140:143],a[12:15],a[188:191],v[140:143]\ns_waitcnt vmcnt(11)\nM v[144:147],a[16:19],a[128:131],0\ns_waitcnt vmcnt(10)\nM v[144:147],a[20:23],a[132:135],v[144:147]\ns_waitcnt vmcnt(9)\n" \
"M v[144:147],a[24:27],a[136:139],v[144:147]\ns_waitcnt vmcnt(8)\nM v[144:147],a[28:31],a[140:143],v[144:147]\nM v[148:151],a[16:19],a[144:147],0\nM v[148:151],a[20:23],a[148:151],v[148:151]\nM v[148:151],a[24:27],a[152:155],v[148:151]\nM v[148:151],a[28:31],a[156:159],v[148:151]\nM v[152:155],a[16:19],a[160:163],0\n" \
"M v[152:155],a[20:23],a[164:167],v[152:155]\nM v[152:155],a[24:27],a[168:171],v[152:155]\nM v[152:155],a[28:31],a[172:175],v[152:155]\nM v[156:159],a[16:19],a[176:179],0\nM v[156:159],a[20:23],a[180:183],v[156:159]\nM v[156:159],a[24:27],a[184:187],v[156:159]\nM v[156:159],a[28:31],a[188:191],v[156:159]\ns_waitcnt vmcnt(7)\n" \
"M v[160:163],a[32:35],a[128:131],0\ns_waitcnt vmcnt(6)\nM v[160:163],a[36:39],a[132:135],v[160:163]\ns_waitcnt vmcnt(5)\nM v[160:163],a[40:43],a[136:139],v[160:163]\ns_waitcnt vmcnt(4)\nM v[160:163],a[44:47],a[140:143],v[160:163]\nM v[164:167],a[32:35],a[144:147],0\n" \
"M v[164:167],a[36:39],a[148:151],v[164:167]\nM v[164:167],a[40:43],a[152:155],v[164:167]\nM v[164:167],a[44:47],a[156:159],v[164:167]\nM v[168:171],a[32:35],a[160:163],0\nM v[168:171],a[36:39],a[164:167],v[168:171]\nM v[168:171],a[40:43],a[168:171],v[168:171]\nM v[168:171],a[44:47],a[172:175],v[168:171]\nM v[172:175],a[32:35],a[176:179],0\n" \
"M v[172:175],a[36:39],a[180:183],v[172:175]\nM v[172:175],a[40:43],a[184:187],v[172:175]\nM v[172:175],a[44:47],a[188:191],v[172:175]\ns_waitcnt vmcnt(3)\nM v[176:179],a[48:51],a[128:131],0\ns_waitcnt vmcnt(2)\nM v[176:179],a[52:55],a[132:135],v[176:179]\ns_waitcnt vmcnt(1)\n" \
"M v[176:179],a[56:59],a[136:139],v[176:179]\ns_waitcnt vmcnt(0)\nM v[176:179],a[60:63],a[140:143],v[176:179]\nM v[180:183],a[48:51],a[144:147],0\nM v[180:183],a[52:55],a[148:151],v[180:183]\nM v[180:183],a[56:59],a[152:155],v[180:183]\nM v[180:183],a[60:63],a[156:159],v[180:183]\nM v[184:187],a[48:51],a[160:163],0\n" \
"M v[184:187],a[52:55],a[164:167],v[184:187]\nM v[184:187],a[56:59],a[168:171],v[184:187]\nM v[184:187],a[60:63],a[172:175],v[184:187]\nM v[188:191],a[48:51],a[176:179],0\nM v[188:191],a[52:55],a[180:183],v[188:191]\nM v[188:191],a[56:59],a[184:187],v[188:191]\nM v[188:191],a[60:63],a[188:191],v[188:191]\nv_cmp_le_i32 vcc,0,v225\n" \
"s_nop 1\nv_cndmask_b32 v128,v252,v128,vcc\nv_max_f32 v224,v224,v128\nv_cmp_le_i32 vcc,1,v225\ns_nop 1\nv_cndmask_b32 v129,v252,v129,vcc\nv_max_f32 v224,v224,v129\nv_cmp_le_i32 vcc,2,v225\n" \
"s_nop 1\nv_cndmask_b32 v130,v252,v130,vcc\nv_max_f32 v224,v224,v130\nv_cmp_le_i32 vcc,3,v225\ns_nop 1\nv_cndmask_b32 v131,v252,v131,vcc\nv_max_f32 v224,v224,v131\nv_cmp_le_i32 vcc,0,v229\n" \
"s_nop 1\nv_cndmask_b32 v132,v252,v132,vcc\nv_max_f32 v228,v228,v132\nv_cmp_le_i32 vcc,1,v229\ns_nop 1\nv_cndmask_b32 v133,v252,v133,vcc\nv_max_f32 v228,v228,v133\nv_cmp_le_i32 vcc,2,v229\n" \
"s_nop 1\nv_cndmask_b32 v134,v252,v134,vcc\nv_max_f32 v228,v228,v134\nv_cmp_le_i32 vcc,3,v229\ns_nop 1\nv_cndmask_b32 v135,v252,v135,vcc\nv_max_f32 v228,v228,v135\nv_cmp_le_i32 vcc,0,v233\n" \
"s_nop 1\nv_cndmask_b32 v136,v252,v136,vcc\nv_max_f32 v232,v232,v136\nv_cmp_le_i32 vcc,1,v233\ns_nop 1\nv_cndmask_b32 v137,v252,v137,vcc\nv_max_f32 v232,v232,v137\nv_cmp_le_i32 vcc,2,v233\n" \
"s_nop 1\nv_cndmask_b32 v138,v252,v138,vcc\nv_max_f32 v232,v232,v138\nv_cmp_le_i32 vcc,3,v233\ns_nop 1\nv_cndmask_b32 v139,v252,v139,vcc\nv_max_f32 v232,v232,v139\nv_cmp_le_i32 vcc,0,v237\n" \
"s_nop 1\nv_cndmask_b32 v140,v252,v140,vcc\nv_max_f32 v236,v236,v140\nv_cmp_le_i32 vcc,1,v237\ns_nop 1\nv_cndmask_b32 v141,v252,v141,vcc\nv_max_f32 v236,v236,v141\nv_cmp_le_i32 vcc,2,v237\n" \
"s_nop 1\nv_cndmask_b32 v142,v252,v142,vcc\nv_max_f32 v236,v236,v142\nv_cmp_le_i32 vcc,3,v237\ns_nop 1\nv_cndmask_b32 v143,v252,v143,vcc\nv_max_f32 v236,v236,v143\nv_cmp_le_i32 vcc,16,v225\n" \
"s_nop 1\nv_cndmask_b32 v144,v252,v144,vcc\nv_max_f32 v224,v224,v144\nv_cmp_le_i32 vcc,17,v225\ns_nop 1\nv_cndmask_b32 v145,v252,v145,vcc\nv_max_f32 v224,v224,v145\nv_cmp_le_i32 vcc,18,v225\n" \
"s_nop 1\nv_cndmask_b32 v146,v252,v146,vcc\nv_max_f32 v224,v224,v146\nv_cmp_le_i32 vcc,19,v225\ns_nop 1\nv_cndmask_b32 v147,v252,v147,vcc\nv_max_f32 v224,v224,v147\nv_cmp_le_i32 vcc,16,v229\n" \
"s_nop 1\nv_cndmask_b32 v148,v252,v148,vcc\nv_max_f32 v228,v228,v148\nv_cmp_le_i32 vcc,17,v229\ns_nop 1\nv_cndmask_b32 v149,v252,v149,vcc\nv_max_f32 v228,v228,v149\nv_cmp_le_i32 vcc,18,v229\n" \
"s_nop 1\nv_cndmask_b32 v150,v252,v150,vcc\nv_max_f32 v228,v228,v150\nv_cmp_le_i32 vcc,19,v229\ns_nop 1\nv_cndmask_b32 v151,v252,v151,vcc\nv_max_f32 v228,v228,v151\nv_cmp_le_i32 vcc,16,v233\n" \
"s_nop 1\nv_cndmask_b32 v152,v252,v152,vcc\nv_max_f32 v232,v232,v152\nv_cmp_le_i32 vcc,17,v233\ns_nop 1\nv_cndmask_b32 v153,v252,v153,vcc\nv_max_f32 v232,v232,v153\nv_cmp_le_i32 vcc,18,v233\n" \
"s_nop 1\nv_cndmask_b32 v154,v252,v154,vcc\nv_max_f32 v232,v232,v154\nv_cmp_le_i32 vcc,19,v233\ns_nop 1\nv_cndmask_b32 v155,v252,v155,vcc\nv_max_f32 v232,v232,v155\nv_cmp_le_i32 vcc,16,v237\n" \
"s_nop 1\nv_cndmask_b32 v156,v252,v156,vcc\nv_max_f32 v236,v236,v156\nv_cmp_le_i32 vcc,17,v237\ns_nop 1\nv_cndmask_b32 v157,v252,v157,vcc\nv_max_f32 v236,v236,v157\nv_cmp_le_i32 vcc,18,v237\n" \
"s_nop 1\nv_cndmask_b32 v158,v252,v158,vcc\nv_max_f32 v236,v236,v158\nv_cmp_le_i32 vcc,19,v237\ns_nop 1\nv_cndmask_b32 v159,v252,v159,vcc\nv_max_f32 v236,v236,v159\nv_cmp_le_i32 vcc,32,v225\n" \
"s_nop 1\nv_cndmask_b32 v160,v252,v160,vcc\nv_max_f32 v224,v224,v160\nv_cmp_le_i32 vcc,33,v225\ns_nop 1\nv_cndmask_b32 v161,v252,v161,vcc\nv_max_f32 v224,v224,v161\nv_cmp_le_i32 vcc,34,v225\n" \
"s_nop 1\nv_cndmask_b32 v162,v252,v162,vcc\nv_max_f32 v224,v224,v162\nv_cmp_le_i32 vcc,35,v225\ns_nop 1\nv_cndmask_b32 v163,v252,v163,vcc\nv_max_f32 v224,v224,v163\nv_cmp_le_i32 vcc,32,v229\n" \
"s_nop 1\nv_cndmask_b32 v164,v252,v164,vcc\nv_max_f32 v228,v228,v164\nv_cmp_le_i32 vcc,33,v229\ns_nop 1\nv_cndmask_b32 v165,v252,v165,vcc\nv_max_f32 v228,v228,v165\nv_cmp_le_i32 vcc,34,v229\n" \
"s_nop 1\nv_cndmask_b32 v166,v252,v166,vcc\nv_max_f32 v228,v228,v166\nv_cmp_le_i32 vcc,35,v229\ns_nop 1\nv_cndmask_b32 v167,v252,v167,vcc\nv_max_f32 v228,v228,v167\nv_cmp_le_i32 vcc,32,v233\n" \
"s_nop 1\nv_cndmask_b32 v168,v252,v168,vcc\nv_max_f32 v232,v232,v168\nv_cmp_le_i32 vcc,33,v233\ns_nop 1\nv_cndmask_b32 v169,v252,v169,vcc\nv_max_f32 v232,v232,v169\nv_cmp_le_i32 vcc,34,v233\n" \
"s_nop 1\nv_cndmask_b32 v170,v252,v170,vcc\nv_max_f32 v232,v232,v170\nv_cmp_le_i32 vcc,35,v233\ns_nop 1\nv_cndmask_b32 v171,v252,v171,vcc\nv_max_f32 v232,v232,v171\nv_cmp_le_i32 vcc,32,v237\n" \
"s_nop 1\nv_cndmask_b32 v172,v252,v172,vcc\nv_max_f32 v236,v236,v172\nv_cmp_le_i32 vcc,33,v237\ns_nop 1\nv_cndmask_b32 v173,v252,v173,vcc\nv_max_f32 v236,v236,v173\nv_cmp_le_i32 vcc,34,v237\n" \
"s_nop 1\nv_cndmask_b32 v174,v252,v174,vcc\nv_max_f32 v236,v236,v174\nv_cmp_le_i32 vcc,35,v237\ns_nop 1\nv_cndmask_b32 v175,v252,v175,vcc\nv_max_f32 v236,v236,v175\nv_cmp_le_i32 vcc,48,v225\n" \
"s_nop 1\nv_cndmask_b32 v176,v252,v176,vcc\nv_max_f32 v224,v224,v176\nv_cmp_le_i32 vcc,49,v225\ns_nop 1\nv_cndmask_b32 v177,v252,v177,vcc\nv_max_f32 v224,v224,v177\nv_cmp_le_i32 vcc,50,v225\n" \
"s_nop 1\nv_cndmask_b32 v178,v252,v178,vcc\nv_max_f32 v224,v224,v178\nv_cmp_le_i32 vcc,51,v225\ns_nop 1\nv_cndmask_b32 v179,v252,v179,vcc\nv_max_f32 v224,v224,v179\nv_cmp_le_i32 vcc,48,v229\n" \
"s_nop 1\nv_cndmask_b32 v180,v252,v180,vcc\nv_max_f32 v228,v228,v180\nv_cmp_le_i32 vcc,49,v229\ns_nop 1\nv_cndmask_b32 v181,v252,v181,vcc\nv_max_f32 v228,v228,v181\nv_cmp_le_i32 vcc,50,v229\n" \
"s_nop 1\nv_cndmask_b32 v182,v252,v182,vcc\nv_max_f32 v228,v228,v182\nv_cmp_le_i32 vcc,51,v229\ns_nop 1\nv_cndmask_b32 v183,v252,v183,vcc\nv_max_f32 v228,v228,v183\nv_cmp_le_i32 vcc,48,v233\n" \
"s_nop 1\nv_cndmask_b32 v184,v252,v184,vcc\nv_max_f32 v232,v232,v184\nv_cmp_le_i32 vcc,49,v233\ns_nop 1\nv_cndmask_b32 v185,v252,v185,vcc\nv_max_f32 v232,v232,v185\nv_cmp_le_i32 vcc,50,v233\n" \
"s_nop 1\nv_cndmask_b32 v186,v252,v186,vcc\nv_max_f32 v232,v232,v186\nv_cmp_le_i32 vcc,51,v233\ns_nop 1\nv_cndmask_b32 v187,v252,v187,vcc\nv_max_f32 v232,v232,v187\nv_cmp_le_i32 vcc,48,v237\n" \
"s_nop 1\nv_cndmask_b32 v188,v252,v188,vcc\nv_max_f32 v236,v236,v188\nv_cmp_le_i32 vcc,49,v237\ns_nop 1\nv_cndmask_b32 v189,v252,v189,vcc\nv_max_f32 v236,v236,v189\nv_cmp_le_i32 vcc,50,v237\n" \
"s_nop 1\nv_cndmask_b32 v190,v252,v190,vcc\nv_max_f32 v236,v236,v190\nv_cmp_le_i32 vcc,51,v237\ns_nop 1\nv_cndmask_b32 v191,v252,v191,vcc\nv_max_f32 v236,v236,v191\nv_subrev_u32 v225,64,v225\n" \
"v_subrev_u32 v229,64,v229\nv_subrev_u32 v233,64,v233\nv_subrev_u32 v237,64,v237\ns_cmp_lt_u32 s37,s38\ns_cbranch_scc1 .Lrl\ns_waitcnt vmcnt(0)\ns_barrier\nds_read_b128 a[0:3],v240 offset:0\n" \
"ds_read_b128 a[4:7],v240 offset:1024\nds_read_b128 a[8:11],v240 offset:2048\nds_read_b128 a[12:15],v240 offset:3072\nds_read_b128 a[16:19],v240 offset:4096\nds_read_b128 a[20:23],v240 offset:5120\nds_read_b128 a[24:27],v240 offset:6144\nds_read_b128 a[28:31],v240 offset:7168\nds_read_b128 a[32:35],v240 offset:8192\n" \
"ds_read_b128 a[36:39],v240 offset:9216\nds_read_b128 a[40:43],v240 offset:10240\nds_read_b128 a[44:47],v240 offset:11264\nds_read_b128 a[48:51],v240 offset:12288\nds_read_b128 a[52:55],v240 offset:13312\nds_read_b128 a[56:59],v240 offset:14336\nds_read_b128 a[60:63],v240 offset:15360\ns_cmp_eq_u32 s62,0\n" \
"s_cselect_b32 s36,0x8000,0\ns_add_u32 s36,s36,s64\ns_cmp_eq_u32 s36,s96\ns_cselect_b32 s72,0xc000,s98\ns_add_u32 s65,s36,s72\ns_cmp_eq_u32 s65,s96\ns_cselect_b32 s72,0xc000,s98\ns_add_u32 s66,s65,s72\n" \
"s_cmp_eq_u32 s66,s96\ns_cselect_b32 s72,0xc000,s98\ns_add_u32 s95,s66,s72\ns_add_u32 s97,s96,65536\ns_add_u32 s15,s64,s46\ns_cmp_gt_i32 s62,-1\ns_cselect_b32 s72,s98,0xfffe4000\ns_add_u32 s15,s15,s72\n" \
"s_mov_b32 s94,0xc000\ns_cmp_eq_u32 s62,7\ns_cselect_b32 s72,98304,0\ns_sub_u32 s97,s97,s72\nv_and_b32 v251,31,v249\nv_lshlrev_b32 v251,2,v251\ns_lshl_b32 s80,s63,7\ns_add_u32 s80,s80,86016\n" \
"v_add_u32 v250,0xc000,v241\ns_waitcnt lgkmcnt(0)\ns_branch .Lattn\n.Lfin:\n" \
""
constexpr size_t WS_DBG = (size_t)51 << 20;
constexpr unsigned LDS_BYTES = 163840;
struct Args {
    const u16 *x, *ckv, *w_q, *w_uk, *w_uv, *w_o, *w_r, *wg, *wu, *wd, *cvec, *w_mod;
    char* ws; u16* out;
};
typedef const __attribute__((address_space(4))) Args* kargs_t;
#define STAMPD(i) do {} while (0)
#define C8(p, a) p #a "0", p #a "1", p #a "2", p #a "3", p #a "4", p #a "5", p #a "6", p #a "7", p #a "8", p #a "9"
#define C100(p, a) C8(p, a##0), C8(p, a##1), C8(p, a##2), C8(p, a##3), C8(p, a##4), C8(p, a##5), C8(p, a##6), C8(p, a##7), C8(p, a##8), C8(p, a##9)
#define C10_99(p) C8(p, 1), C8(p, 2), C8(p, 3), C8(p, 4), C8(p, 5), C8(p, 6), C8(p, 7), C8(p, 8), C8(p, 9)
#define C200_249(p) C8(p, 20), C8(p, 21), C8(p, 22), C8(p, 23), C8(p, 24)
#define BODY_CLOB "memory", "vcc", "scc", "s0", "s1", "s2", "s3", "s8", "s9", C8("s", 1), C8("s", 2), "s30", "s31", "s33", "s34", "s35", "s36", "s37", "s38", "s39", C8("s", 4), C8("s", 5), C8("s", 6), C8("s", 7), C8("s", 8), C8("s", 9), \
    C8("v", ), C10_99("v"), C100("v", 1), C200_249("v"), "v250", "v251", "v252", "v253", "v254", C8("a", ), C10_99("a"), C100("a", 1), C200_249("a"), "a250", "a251", "a252", "a253", "a254", "a255"

__global__ __launch_bounds__(256, 1) void k_all(const Args a_) {
    kargs_t ap = (kargs_t)__builtin_amdgcn_kernarg_segment_ptr(); unsigned bid = blockIdx.x, wid = rfl(threadIdx.x >> 6), sv_;
    asm volatile(BODY_ASM : "+{s[4:5]}"(ap), "+{s6}"(bid), "+{s7}"(wid), "={v255}"(sv_) :: BODY_CLOB);
    asm volatile(".Lcode_e:");
}
extern "C" void kernel_launch(void* const* d_in, const int* in_sizes, int n_in, void* d_out, int out_size, void* d_ws, size_t ws_size, hipStream_t stream) {
    Args a;
    a.x = (const u16*)d_in[0]; a.ckv = (const u16*)d_in[1]; a.w_q = (const u16*)d_in[2]; a.w_uk = (const u16*)d_in[3]; a.w_uv = (const u16*)d_in[4]; a.w_o = (const u16*)d_in[5];
    a.w_r = (const u16*)d_in[7]; a.wg = (const u16*)d_in[8]; a.wu = (const u16*)d_in[9]; a.wd = (const u16*)d_in[10]; a.cvec = (const u16*)d_in[11]; a.w_mod = (const u16*)d_in[12];
    a.ws = (char*)d_ws;
    a.out = (u16*)d_out;
    (void)hipFuncSetAttribute((const void*)k_all, hipFuncAttributeMaxDynamicSharedMemorySize, LDS_BYTES);
    k_all<<<256, 256, LDS_BYTES, stream>>>(a);
}
```

```cpp
#include <hip/hip_runtime.h>
#include <hip/hip_bf16.h>
#include <stdint.h>
typedef unsigned short u16;
#define DI __device__ __forceinline__
DI unsigned lane_id() { unsigned l; asm volatile("v_mbcnt_lo_u32_b32 %0, -1, 0\n\tv_mbcnt_hi_u32_b32 %0, -1, %0" : "=v"(l)); return l; }
DI unsigned rfl(unsigned x) { return __builtin_amdgcn_readfirstlane(x); }
#define BODY_ASM ".macro M a,b,c,d\nv_mfma_f32_16x16x32_bf16 \\a,\\b,\\c,\\d\n.endm\n" \
".Lcode_b:\ns_load_dwordx16 s[8:23],s[4:5],0x0\ns_load_dwordx8 s[24:31],s[4:5],0x40\ns_load_dwordx2 s[34:35],s[4:5],0x60\nv_mbcnt_lo_u32_b32 v249,-1,0\nv_mbcnt_hi_u32_b32 v249,-1,v249\ns_getpc_b64 s[56:57]\n.Lpc0:\n" \
"s_and_b32 s57,s57,0xffff\ns_mov_b32 s58,.Lcode_e-.Lpc0\ns_mov_b32 s59,0x20000\ns_lshr_b32 s70,s6,3\nv_and_b32 v251,1,v249\ns_lshl2_add_u32 s78,s70,s7\nv_lshlrev_b32 v251,14,v251\ns_lshl_b32 s78,s78,7\n" \
"buffer_load_dword v247,v251,s[56:59],s78 offen\ns_bfe_u32 s61,s6,0x20003\ns_lshl_b32 s64,s7,12\ns_and_b32 s60,s6,7\ns_lshr_b32 s62,s6,5\ns_lshl2_add_u32 s63,s60,s61\nv_lshlrev_b32 v240,4,v249\ns_cmp_lt_u32 s6,192\n" \
"s_cselect_b32 s70,s6,0\ns_mul_i32 s71,s70,171\ns_lshr_b32 s71,s71,13\ns_mul_i32 s72,s71,48\ns_sub_u32 s70,s70,s72\ns_lshl2_add_u32 s72,s71,s7\ns_lshl_b32 s72,s72,5\nv_lshrrev_b32 v0,3,v249\n" \
"v_lshl_add_u32 v1,v0,2,s72\nv_and_b32 v2,7,v249\nv_mul_u32_u24 v3,6144,v1\nv_lshl_add_u32 v3,v2,4,v3\ns_lshl_b32 s72,s70,7\ns_add_u32 s72,s72,3072\nv_add_u32 v3,s72,v3\nv_add_u32 v4,12288,v3\n" \
"v_lshlrev_b32 v1,1,v1\ns_mul_i32 s71,s71,12288\ns_lshl_b32 s70,s70,8\ns_add_u32 s79,s70,s71\ns_add_u32 s79,s79,786432\ns_cmp_eq_u32 s7,0\ns_cselect_b32 s75,0,0x8000000\ns_cmp_lt_u32 s6,192\n" \
"s_cselect_b32 s70,s75,0x8000000\ns_or_b32 s79,s79,s70\ns_lshl_b32 s70,s62,7\ns_xor_b32 s71,s63,12\ns_lshl3_add_u32 s71,s71,s70\ns_lshl_b32 s72,s63,3\ns_sub_u32 s72,s72,s70\ns_add_u32 s72,s72,1792\n" \
"s_cmp_lt_u32 s63,16\ns_cselect_b32 s71,s71,s72\ns_lshl_b32 s71,s71,10\ns_lshl_b32 s72,s7,7\ns_add_u32 s71,s71,s72\nv_lshlrev_b32 v5,10,v2\nv_lshl_add_u32 v5,v0,4,v5\nv_add_u32 v5,s71,v5\n" \
"v_bfe_u32 v6,v249,2,3\nv_lshrrev_b32 v7,5,v249\nv_and_b32 v8,48,v240\nv_lshlrev_b32 v6,13,v6\nv_lshl_or_b32 v7,v7,6,v8\nv_or_b32 v6,0x1000,v6\nv_or_b32 v243,v6,v7\nv_add_u32 v244,0xfffffc80,v243\n" \
"v_xor_b32 v6,32,v243\nv_add_u32 v245,63488,v6\nv_add_u32 v246,62592,v6\nv_lshrrev_b32 v6,4,v249\nv_xor_b32 v7,v6,v2\nv_lshlrev_b32 v7,4,v7\nv_lshl_or_b32 v224,v0,10,v7\nv_xor_b32 v7,64,v224\n" \
"v_add_u32 v225,7168,v7\nv_add_u32 v226,14336,v224\nv_add_u32 v227,21504,v7\ns_lshl_b32 s70,s62,1\ns_sub_u32 s71,s7,s70\ns_add_u32 s71,s71,30\ns_sub_u32 s72,s70,s7\ns_add_u32 s72,s72,3\n" \
"s_cmp_lt_u32 s7,2\ns_cselect_b32 s74,s71,s72\ns_lshl_b32 s66,s74,16\ns_mul_i32 s65,s7,24576\ns_lshl_b32 s68,s7,17\ns_lshl_b32 s70,s63,8\ns_add_u32 s68,s68,s70\ns_mov_b32 s38,2097152\n" \
"s_mov_b32 s39,0x20000\ns_mov_b32 s42,4198400\ns_mov_b32 s43,0x20000\ns_mov_b32 s54,71303168\ns_mov_b32 s55,0x20000\ns_mov_b32 s70,94208\ns_waitcnt lgkmcnt(0)\ns_mov_b64 s[52:53],s[34:35]\n" \
"s_mov_b64 s[36:37],s[10:11]\ns_sub_u32 s40,s14,0x1000\ns_subb_u32 s41,s15,0\nbuffer_load_dwordx2 v[230:231],off,s[52:55],s70 sc1\nglobal_load_dwordx2 v[10:11],v1,s[28:29]\nglobal_load_dwordx4 v[12:15],v3,s[30:31] offset:-3072\nglobal_load_dwordx4 v[16:19],v3,s[30:31] offset:3072\nglobal_load_dwordx4 v[20:23],v4,s[30:31] offset:-3072\n" \
"global_load_dwordx4 v[24:27],v4,s[30:31] offset:3072\nglobal_load_dwordx4 v[128:131],v5,s[8:9]\nglobal_load_dwordx4 v[132:135],v5,s[8:9] offset:512\ns_add_u32 m0,s64,98304\ns_add_u32 s69,s68,0\nbuffer_load_dwordx4 v243,s[40:43],s69 offen offset:0 lds\nbuffer_load_dwordx4 v244,s[40:43],s69 offen offset:1024 lds\nbuffer_load_dwordx4 v245,s[40:43],s69 offen offset:2048 lds\n" \
"buffer_load_dwordx4 v246,s[40:43],s69 offen offset:3072 lds\ns_add_u32 m0,s65,0\ns_add_u32 s67,s66,0\nbuffer_load_dwordx4 v224,s[36:39],s67 offen offset:0 lds\nbuffer_load_dwordx4 v225,s[36:39],s67 offen offset:1024 lds\nbuffer_load_dwordx4 v226,s[36:39],s67 offen offset:2048 lds\nbuffer_load_dwordx4 v227,s[36:39],s67 offen offset:3072 lds\ns_add_u32 m0,s65,4096\n" \
"s_add_u32 s67,s66,32768\nbuffer_load_dwordx4 v224,s[36:39],s67 offen offset:0 lds\nbuffer_load_dwordx4 v225,s[36:39],s67 offen offset:1024 lds\nbuffer_load_dwordx4 v226,s[36:39],s67 offen offset:2048 lds\nbuffer_load_dwordx4 v227,s[36:39],s67 offen offset:3072 lds\ns_lshl_b32 s70,s6,11\ns_cmp_lt_u32 s6,112\ns_cselect_b32 s70,s70,0x8000000\n" \
"s_lshl_b32 s71,s7,9\ns_add_u32 s70,s70,s71\ns_add_u32 s70,s70,98304\nv_mov_b32 v28,-1\nv_mov_b32 v29,-1\nv_lshlrev_b32 v30,3,v249\nbuffer_store_dwordx2 v[28:29],v30,s[52:55],s70 offen sc1\ns_lshl_b32 s70,s6,7\n" \
"s_cmp_lt_u32 s6,224\ns_cselect_b32 s71,65536,0x40000000\ns_cmp_lt_u32 s6,128\ns_cselect_b32 s72,556928,0x40000000\nv_mov_b32 v31,s71\nv_mov_b32 v32,s72\nv_cmp_lt_u32 vcc,31,v249\ns_or_b32 s70,s70,s75\n" \
"v_mov_b32 v33,0\nv_cndmask_b32 v31,v31,v32,vcc\nv_lshl_add_u32 v31,v249,2,v31\nbuffer_store_dword v33,v31,s[52:55],s70 offen sc1\nv_and_b32 v34,15,v249\nv_lshrrev_b32 v35,1,v34\nv_xor_b32 v35,v35,v6\nv_lshlrev_b32 v35,4,v35\n" \
"v_lshl_or_b32 v35,v34,7,v35\nv_add_u32 v228,s65,v35\nv_xor_b32 v229,64,v228\nv_bfe_u32 v34,v249,2,2\nv_and_b32 v35,3,v249\nv_lshlrev_b32 v35,3,v35\nv_lshl_or_b32 v35,v34,6,v35\nv_lshl_or_b32 v35,v6,11,v35\n" \
"v_and_b32 v34,16,v249\nv_lshl_or_b32 v35,v34,1,v35\nv_add_u32 v241,98304,v35\nv_xor_b32 v242,32,v241\ns_mov_b32 s73,0x400000\nv_mov_b32 v252,0xff800000\ns_mov_b32 s70,0x05040100\ns_mov_b32 s71,0x07060302\n" \
"s_waitcnt vmcnt(20)\nv_lshlrev_b32 v36,16,v10\nv_and_b32 v37,0xffff0000,v10\nv_lshlrev_b32 v38,16,v11\nv_and_b32 v39,0xffff0000,v11\nv_mul_f32 v40,0xbfb8aa3b,v36\nv_mul_f32 v41,0xbfb8aa3b,v37\nv_mul_f32 v42,0xbfb8aa3b,v38\n" \
"v_mul_f32 v43,0xbfb8aa3b,v39\nv_exp_f32 v40,v40\nv_exp_f32 v41,v41\nv_exp_f32 v42,v42\nv_exp_f32 v43,v43\nv_add_f32 v40,1.0,v40\nv_add_f32 v41,1.0,v41\nv_add_f32 v42,1.0,v42\n" \
"v_add_f32 v43,1.0,v43\nv_rcp_f32 v40,v40\nv_rcp_f32 v41,v41\nv_rcp_f32 v42,v42\nv_rcp_f32 v43,v43\nv_mul_f32 v36,v36,v40\nv_mul_f32 v37,v37,v41\nv_mul_f32 v38,v38,v42\n" \
"v_mul_f32 v39,v39,v43\nv_cvt_pk_bf16_f32 v36,v36,v37\nv_cvt_pk_bf16_f32 v37,v38,v39\ns_waitcnt vmcnt(18)\nv_perm_b32 v48,v16,v12,s70\nv_perm_b32 v49,v16,v12,s71\nv_perm_b32 v50,v17,v13,s70\nv_perm_b32 v51,v17,v13,s71\n" \
"v_perm_b32 v52,v18,v14,s70\nv_perm_b32 v53,v18,v14,s71\nv_perm_b32 v54,v19,v15,s70\nv_perm_b32 v55,v19,v15,s71\nv_dot2_f32_bf16 v40,v48,v36,0\nv_dot2_f32_bf16 v41,v49,v36,0\nv_dot2_f32_bf16 v42,v50,v36,0\nv_dot2_f32_bf16 v43,v51,v36,0\n" \
"v_dot2_f32_bf16 v44,v52,v36,0\nv_dot2_f32_bf16 v45,v53,v36,0\nv_dot2_f32_bf16 v46,v54,v36,0\nv_dot2_f32_bf16 v47,v55,v36,0\ns_waitcnt vmcnt(16)\nv_perm_b32 v48,v24,v20,s70\nv_perm_b32 v49,v24,v20,s71\nv_perm_b32 v50,v25,v21,s70\n" \
"v_perm_b32 v51,v25,v21,s71\nv_perm_b32 v52,v26,v22,s70\nv_perm_b32 v53,v26,v22,s71\nv_perm_b32 v54,v27,v23,s70\nv_perm_b32 v55,v27,v23,s71\nv_dot2_f32_bf16 v40,v48,v37,v40\nv_dot2_f32_bf16 v41,v49,v37,v41\nv_dot2_f32_bf16 v42,v50,v37,v42\n" \
"v_dot2_f32_bf16 v43,v51,v37,v43\nv_dot2_f32_bf16 v44,v52,v37,v44\nv_dot2_f32_bf16 v45,v53,v37,v45\nv_dot2_f32_bf16 v46,v54,v37,v46\nv_dot2_f32_bf16 v47,v55,v37,v47\ns_waitcnt vmcnt(15)\nv_lshlrev_b32 v136,16,v128\nv_and_b32 v137,0xffff0000,v128\n" \
"v_lshlrev_b32 v138,16,v129\nv_and_b32 v139,0xffff0000,v129\nv_lshlrev_b32 v140,16,v130\nv_and_b32 v141,0xffff0000,v130\nv_lshlrev_b32 v142,16,v131\nv_and_b32 v143,0xffff0000,v131\ns_waitcnt vmcnt(14)\nv_lshlrev_b32 v144,16,v132\n" \
"v_and_b32 v145,0xffff0000,v132\nv_lshlrev_b32 v146,16,v133\nv_and_b32 v147,0xffff0000,v133\nv_lshlrev_b32 v148,16,v134\nv_and_b32 v149,0xffff0000,v134\nv_lshlrev_b32 v150,16,v135\nv_and_b32 v151,0xffff0000,v135\nv_permlane32_swap_b32 v40,v44\n" \
"v_permlane32_swap_b32 v41,v45\nv_permlane32_swap_b32 v42,v46\nv_permlane32_swap_b32 v43,v47\nv_add_f32 v40,v40,v44\nv_add_f32 v41,v41,v45\nv_add_f32 v42,v42,v46\nv_add_f32 v43,v43,v47\nv_add_f32 v48,v136,v140\n" \
"v_add_f32 v49,v137,v141\nv_add_f32 v50,v138,v142\nv_add_f32 v51,v139,v143\nv_permlane16_swap_b32 v40,v42\nv_permlane16_swap_b32 v41,v43\nv_add_f32 v52,v144,v148\nv_add_f32 v53,v145,v149\nv_add_f32 v54,v146,v150\n" \
"v_add_f32 v55,v147,v151\nv_add_f32 v40,v40,v42\nv_add_f32 v41,v41,v43\nv_add_f32 v48,v48,v52\nv_add_f32 v49,v49,v53\nv_add_f32 v50,v50,v54\nv_add_f32 v51,v51,v55\nv_add_f32_dpp v40,v40,v40 row_ror:8 row_mask:0xf bank_mask:0xf\n" \
"v_add_f32_dpp v41,v41,v41 row_ror:8 row_mask:0xf bank_mask:0xf\nv_add_f32 v48,v48,v49\nv_add_f32 v50,v50,v51\nv_lshlrev_b32 v42,5,v2\nv_add_f32 v48,v48,v50\nv_lshl_or_b32 v42,v6,3,v42\ns_lshl_b32 s72,s7,8\ns_add_u32 s72,s72,147456\n" \
"v_add_u32 v42,s72,v42\nds_write_b64 v42,v[40:41]\nv_add_f32_dpp v48,v48,v48 row_ror:8 row_mask:0xf bank_mask:0xf\nv_mov_b32 v50,v48\ns_nop 1\nv_permlane16_swap_b32 v48,v50\nv_add_f32 v48,v48,v50\nv_mov_b32 v50,v48\n" \
"s_nop 1\nv_permlane32_swap_b32 v48,v50\nv_add_f32 v48,v48,v50\nv_mul_f32 v48,0x3c000000,v48\nv_sub_f32 v56,v136,v48\nv_sub_f32 v57,v137,v48\nv_sub_f32 v58,v138,v48\nv_sub_f32 v59,v139,v48\n" \
"v_sub_f32 v60,v140,v48\nv_sub_f32 v61,v141,v48\nv_sub_f32 v62,v142,v48\nv_sub_f32 v63,v143,v48\nv_sub_f32 v64,v144,v48\nv_sub_f32 v65,v145,v48\nv_sub_f32 v66,v146,v48\nv_sub_f32 v67,v147,v48\n" \
"v_sub_f32 v68,v148,v48\nv_sub_f32 v69,v149,v48\nv_sub_f32 v70,v150,v48\nv_sub_f32 v71,v151,v48\nv_mul_f32 v52,v56,v56\nv_mul_f32 v53,v57,v57\nv_mul_f32 v54,v58,v58\nv_mul_f32 v55,v59,v59\n" \
"v_fmac_f32 v52,v60,v60\nv_fmac_f32 v53,v61,v61\nv_fmac_f32 v54,v62,v62\nv_fmac_f32 v55,v63,v63\nv_fmac_f32 v52,v64,v64\nv_fmac_f32 v53,v65,v65\nv_fmac_f32 v54,v66,v66\nv_fmac_f32 v55,v67,v67\n" \
"v_fmac_f32 v52,v68,v68\nv_fmac_f32 v53,v69,v69\nv_fmac_f32 v54,v70,v70\nv_fmac_f32 v55,v71,v71\nv_add_f32 v52,v52,v53\nv_add_f32 v54,v54,v55\ns_lshl_b32 s72,s7,3\nv_add_f32 v49,v52,v54\n" \
"s_add_u32 s72,s72,148480\nv_lshl_add_u32 v42,v2,5,s72\nv_add_f32_dpp v49,v49,v49 row_ror:8 row_mask:0xf bank_mask:0xf\nv_mov_b32 v50,v49\ns_nop 1\nv_permlane16_swap_b32 v49,v50\nv_add_f32 v49,v49,v50\nv_mov_b32 v50,v49\n" \
"s_nop 1\nv_permlane32_swap_b32 v49,v50\nv_add_f32 v49,v49,v50\nds_write_b64 v42,v[48:49]\nv_lshlrev_b32 v251,5,v2\nv_add_u32 v251,148480,v251\nv_lshlrev_b32 v250,2,v249\nv_add_u32 v250,147456,v250\n" \
"s_add_u32 m0,s64,114688\ns_add_u32 s69,s68,524288\nbuffer_load_dwordx4 v243,s[40:43],s69 offen offset:0 lds\nbuffer_load_dwordx4 v244,s[40:43],s69 offen offset:1024 lds\nbuffer_load_dwordx4 v245,s[40:43],s69 offen offset:2048 lds\nbuffer_load_dwordx4 v246,s[40:43],s69 offen offset:3072 lds\ns_add_u32 m0,s65,8192\ns_add_u32 s67,s66,128\n" \
"buffer_load_dwordx4 v224,s[36:39],s67 offen offset:0 lds\nbuffer_load_dwordx4 v225,s[36:39],s67 offen offset:1024 lds\nbuffer_load_dwordx4 v226,s[36:39],s67 offen offset:2048 lds\nbuffer_load_dwordx4 v227,s[36:39],s67 offen offset:3072 lds\ns_add_u32 m0,s65,12288\ns_add_u32 s67,s66,32896\nbuffer_load_dwordx4 v224,s[36:39],s67 offen offset:0 lds\nbuffer_load_dwordx4 v225,s[36:39],s67 offen offset:1024 lds\n" \
"buffer_load_dwordx4 v226,s[36:39],s67 offen offset:2048 lds\nbuffer_load_dwordx4 v227,s[36:39],s67 offen offset:3072 lds\ns_waitcnt lgkmcnt(0)\ns_waitcnt vmcnt(14)\ns_barrier\ns_add_u32 m0,s64,131072\ns_add_u32 s69,s68,1048576\nds_read_b128 a[0:3],v228 offset:0\n" \
"ds_read_b128 a[4:7],v228 offset:2048\nds_read_b128 a[8:11],v228 offset:4096\nds_read_b128 a[12:15],v228 offset:6144\nds_read_b128 a[16:19],v229 offset:0\nds_read_b128 a[20:23],v229 offset:2048\nds_read_b128 a[24:27],v229 offset:4096\nds_read_b128 a[28:31],v229 offset:6144\nds_read_b32 v152,v250 offset:0\n" \
"ds_read_b32 v153,v250 offset:256\nds_read_b32 v154,v250 offset:512\nds_read_b32 v155,v250 offset:768\ns_waitcnt lgkmcnt(2)\nv_add_f32 v152,v152,v153\ns_waitcnt lgkmcnt(0)\nv_add_f32 v154,v154,v155\nv_lshlrev_b32 v153,2,v249\n" \
"v_add_f32 v152,v152,v154\nds_read_b64_tr_b16 v[192:193],v241 offset:0\nds_read_b64_tr_b16 v[194:195],v241 offset:256\nds_read_b64_tr_b16 v[196:197],v241 offset:8192\nds_read_b64_tr_b16 v[198:199],v241 offset:8448\nds_read_b64_tr_b16 v[200:201],v242 offset:0\nds_read_b64_tr_b16 v[202:203],v242 offset:256\nds_read_b64_tr_b16 v[204:205],v242 offset:8192\n" \
"ds_read_b64_tr_b16 v[206:207],v242 offset:8448\nds_read_b64_tr_b16 v[208:209],v241 offset:512\nds_read_b64_tr_b16 v[210:211],v241 offset:768\nds_read_b64_tr_b16 v[212:213],v241 offset:8704\nds_read_b64_tr_b16 v[214:215],v241 offset:8960\nds_read_b64_tr_b16 v[216:217],v242 offset:512\nds_read_b64_tr_b16 v[218:219],v242 offset:768\nds_read_b64_tr_b16 v[220:221],v242 offset:8704\n" \
"ds_read_b64_tr_b16 v[222:223],v242 offset:8960\nbuffer_store_dword v152,v153,s[52:55],s79 offen sc1\nds_read_b128 v[156:159],v251\nds_read_b128 v[160:163],v251 offset:16\ns_waitcnt lgkmcnt(15)\nM v[0:3],v[192:195],a[0:3],0\nbuffer_load_dwordx4 v243,s[40:43],s69 offen offset:0 lds\ns_waitcnt lgkmcnt(14)\n" \
"M v[0:3],v[196:199],a[16:19],v[0:3]\nv_bfe_u32 v253,v249,1,2\ns_lshl2_add_u32 s76,s62,s7\nv_and_b32 v248,1,v249\nv_lshlrev_b32 v253,13,v253\ns_mul_i32 s76,s76,32768\ns_lshl_b32 s77,s63,8\nM v[4:7],v[192:195],a[4:7],0\n" \
"v_lshl_or_b32 v253,v248,7,v253\ns_add_u32 s76,s76,s77\nv_and_b32 v254,7,v249\nv_add_u32 v253,s76,v253\nM v[4:7],v[196:199],a[20:23],v[4:7]\ns_lshl3_add_u32 s72,s6,s75\ns_add_u32 s72,s72,573440\nM v[64:67],v[192:195],a[8:11],0\n" \
"global_load_dword v247,v253,s[16:17]\nM v[64:67],v[196:199],a[24:27],v[64:67]\nbuffer_load_dwordx4 v244,s[40:43],s69 offen offset:1024 lds\nM v[68:71],v[192:195],a[12:15],0\nds_read_b64_tr_b16 v[192:193],v241 offset:1024\nds_read_b64_tr_b16 v[194:195],v241 offset:1280\nM v[68:71],v[196:199],a[28:31],v[68:71]\nds_read_b64_tr_b16 v[196:197],v241 offset:9216\n" \
"ds_read_b64_tr_b16 v[198:199],v241 offset:9472\ns_waitcnt lgkmcnt(4)\nM v[8:11],v[200:203],a[0:3],0\nv_add_u32 v253,0x100000,v253\ns_lshl2_add_u32 s80,s60,s7\nM v[8:11],v[204:207],a[16:19],v[8:11]\nv_readfirstlane_b32 s92,v230\nv_readfirstlane_b32 s93,v231\n" \
"M v[12:15],v[200:203],a[4:7],0\nbuffer_load_dwordx4 v245,s[40:43],s69 offen offset:2048 lds\nM v[12:15],v[204:207],a[20:23],v[12:15]\ns_add_u32 s70,s92,1\ns_addc_u32 s71,s93,0\nM v[72:75],v[200:203],a[8:11],0\nv_add_f32 v164,v156,v158\nv_add_f32 v165,v160,v162\n" \
"M v[72:75],v[204:207],a[24:27],v[72:75]\nv_add_f32 v164,v164,v165\nv_and_b32 v165,7,v249\nM v[76:79],v[200:203],a[12:15],0\nds_read_b64_tr_b16 v[200:201],v242 offset:1024\nds_read_b64_tr_b16 v[202:203],v242 offset:1280\nM v[76:79],v[204:207],a[28:31],v[76:79]\nbuffer_load_dwordx4 v246,s[40:43],s69 offen offset:3072 lds\n" \
"ds_read_b64_tr_b16 v[204:205],v242 offset:9216\nds_read_b64_tr_b16 v[206:207],v242 offset:9472\nM v[16:19],v[208:211],a[0:3],0\ns_add_u32 s92,s92,0x7f4a7c15\ns_addc_u32 s93,s93,0x9e3779b9\nM v[16:19],v[212:215],a[16:19],v[16:19]\ns_add_u32 m0,s65,16384\ns_add_u32 s67,s66,256\n" \
"M v[20:23],v[208:211],a[4:7],0\nv_mov_b32 v230,s92\nv_mov_b32 v231,s93\nM v[20:23],v[212:215],a[20:23],v[20:23]\nv_lshlrev_b32 v165,11,v165\ns_lshl_b32 s80,s80,9\nM v[80:83],v[208:211],a[8:11],0\nbuffer_load_dwordx4 v224,s[36:39],s67 offen offset:0 lds\n" \
"M v[80:83],v[212:215],a[24:27],v[80:83]\ns_add_u32 s80,s80,573440\nv_mul_f32 v164,0x3e800000,v164\nM v[84:87],v[208:211],a[12:15],0\nds_read_b64_tr_b16 v[208:209],v241 offset:1536\nds_read_b64_tr_b16 v[210:211],v241 offset:1792\nM v[84:87],v[212:215],a[28:31],v[84:87]\nds_read_b64_tr_b16 v[212:213],v241 offset:9728\n" \
"ds_read_b64_tr_b16 v[214:215],v241 offset:9984\nM v[24:27],v[216:219],a[0:3],0\nv_sub_f32 v156,v156,v164\nv_sub_f32 v158,v158,v164\nM v[24:27],v[220:223],a[16:19],v[24:27]\nbuffer_load_dwordx4 v225,s[36:39],s67 offen offset:1024 lds\nM v[28:31],v[216:219],a[4:7],0\nv_mul_f32 v156,v156,v156\n" \
"v_sub_f32 v160,v160,v164\nM v[28:31],v[220:223],a[20:23],v[28:31]\nv_fmac_f32 v156,v158,v158\nv_add_f32 v157,v157,v159\nM v[88:91],v[216:219],a[8:11],0\nv_add_f32 v161,v161,v163\nv_sub_f32 v162,v162,v164\nM v[88:91],v[220:223],a[24:27],v[88:91]\n" \
"v_fmac_f32 v156,v160,v160\nv_add_f32 v157,v157,v161\nM v[92:95],v[216:219],a[12:15],0\nbuffer_load_dwordx4 v226,s[36:39],s67 offen offset:2048 lds\nds_read_b64_tr_b16 v[216:217],v242 offset:1536\nds_read_b64_tr_b16 v[218:219],v242 offset:1792\nM v[92:95],v[220:223],a[28:31],v[92:95]\nds_read_b64_tr_b16 v[220:221],v242 offset:9728\n" \
"ds_read_b64_tr_b16 v[222:223],v242 offset:9984\ns_waitcnt lgkmcnt(8)\nM v[32:35],v[192:195],a[0:3],0\nv_fmac_f32 v156,v162,v162\nv_fmac_f32 v157,0x43000000,v156\nM v[32:35],v[196:199],a[16:19],v[32:35]\nv_mul_f32 v157,0x3b000000,v157\nv_cvt_pk_bf16_f32 v157,0,v157\n" \
"M v[36:39],v[192:195],a[4:7],0\nv_add_f32 v157,0x3727c5ac,v157\nv_cvt_pk_bf16_f32 v157,0,v157\nM v[36:39],v[196:199],a[20:23],v[36:39]\nbuffer_load_dwordx4 v227,s[36:39],s67 offen offset:3072 lds\nM v[96:99],v[192:195],a[8:11],0\nv_sqrt_f32 v157,v157\nM v[96:99],v[196:199],a[24:27],v[96:99]\n" \
"s_add_u32 m0,s65,20480\ns_add_u32 s67,s66,33024\nM v[100:103],v[192:195],a[12:15],0\nv_mov_b32 v232,s70\nv_mov_b32 v233,s71\nM v[100:103],v[196:199],a[28:31],v[100:103]\ns_lshr_b32 s70,s64,2\nv_cvt_pk_bf16_f32 v164,0,v164\n" \
"M v[40:43],v[200:203],a[0:3],0\nbuffer_load_dwordx4 v224,s[36:39],s67 offen offset:0 lds\nM v[40:43],v[204:207],a[16:19],v[40:43]\nv_cvt_pk_bf16_f32 v157,0,v157\ns_add_u32 s71,s70,786432\nM v[44:47],v[200:203],a[4:7],0\nv_rcp_f32 v157,v157\nM v[44:47],v[204:207],a[20:23],v[44:47]\n" \
"v_sub_f32 v136,v136,v164\nv_sub_f32 v137,v137,v164\nM v[104:107],v[200:203],a[8:11],0\nglobal_load_dword v247,v253,s[16:17]\nM v[104:107],v[204:207],a[24:27],v[104:107]\nbuffer_load_dwordx4 v225,s[36:39],s67 offen offset:1024 lds\nM v[108:111],v[200:203],a[12:15],0\nv_add_u32 v253,0x100000,v253\n" \
"v_sub_f32 v138,v138,v164\nM v[108:111],v[204:207],a[28:31],v[108:111]\nv_sub_f32 v139,v139,v164\ns_waitcnt lgkmcnt(0)\nM v[48:51],v[208:211],a[0:3],0\nv_sub_f32 v140,v140,v164\nv_sub_f32 v141,v141,v164\nM v[48:51],v[212:215],a[16:19],v[48:51]\n" \
"v_sub_f32 v142,v142,v164\nv_sub_f32 v143,v143,v164\nM v[52:55],v[208:211],a[4:7],0\nbuffer_load_dwordx4 v226,s[36:39],s67 offen offset:2048 lds\nM v[52:55],v[212:215],a[20:23],v[52:55]\nv_sub_f32 v144,v144,v164\nv_sub_f32 v145,v145,v164\nM v[112:115],v[208:211],a[8:11],0\n" \
"s_waitcnt vmcnt(13)\ns_barrier\nds_read_b128 a[32:35],v228 offset:8192\nds_read_b128 a[36:39],v228 offset:10240\nds_read_b128 a[40:43],v228 offset:12288\nds_read_b128 a[44:47],v228 offset:14336\nds_read_b128 a[48:51],v229 offset:8192\nds_read_b128 a[52:55],v229 offset:10240\n" \
"ds_read_b128 a[56:59],v229 offset:12288\nds_read_b128 a[60:63],v229 offset:14336\nds_read_b64_tr_b16 v[192:193],v241 offset:16384\nds_read_b64_tr_b16 v[194:195],v241 offset:16640\nds_read_b64_tr_b16 v[196:197],v241 offset:24576\nds_read_b64_tr_b16 v[198:199],v241 offset:24832\nds_read_b64_tr_b16 v[200:201],v242 offset:16384\nds_read_b64_tr_b16 v[202:203],v242 offset:16640\n" \
"ds_read_b64_tr_b16 v[204:205],v242 offset:24576\nds_read_b64_tr_b16 v[206:207],v242 offset:24832\nM v[112:115],v[212:215],a[24:27],v[112:115]\nv_sub_f32 v146,v146,v164\nv_sub_f32 v147,v147,v164\nM v[116:119],v[208:211],a[12:15],0\nds_read_b64_tr_b16 v[208:209],v241 offset:16896\nds_read_b64_tr_b16 v[210:211],v241 offset:17152\n" \
"buffer_store_dwordx2 v[230:231],v165,s[52:55],s72 offen sc1\nM v[116:119],v[212:215],a[28:31],v[116:119]\nbuffer_load_dwordx4 v227,s[36:39],s67 offen offset:3072 lds\nds_read_b64_tr_b16 v[212:213],v241 offset:25088\nds_read_b64_tr_b16 v[214:215],v241 offset:25344\nM v[56:59],v[216:219],a[0:3],0\nv_lshlrev_b32 v165,3,v249\nv_sub_f32 v148,v148,v164\n" \
"M v[56:59],v[220:223],a[16:19],v[56:59]\ns_add_u32 m0,s65,0\ns_add_u32 s67,s66,384\nM v[60:63],v[216:219],a[4:7],0\nv_sub_f32 v149,v149,v164\nv_sub_f32 v150,v150,v164\nM v[60:63],v[220:223],a[20:23],v[60:63]\nv_sub_f32 v151,v151,v164\n" \
"v_cvt_pk_bf16_f32 v136,0,v136\nM v[120:123],v[216:219],a[8:11],0\nbuffer_load_dwordx4 v224,s[36:39],s67 offen offset:0 lds\nM v[120:123],v[220:223],a[24:27],v[120:123]\nv_cvt_pk_bf16_f32 v137,0,v137\nv_cvt_pk_bf16_f32 v138,0,v138\nM v[124:127],v[216:219],a[12:15],0\nds_read_b64_tr_b16 v[216:217],v242 offset:16896\n" \
"ds_read_b64_tr_b16 v[218:219],v242 offset:17152\nM v[124:127],v[220:223],a[28:31],v[124:127]\nds_read_b64_tr_b16 v[220:221],v242 offset:25088\nds_read_b64_tr_b16 v[222:223],v242 offset:25344\ns_waitcnt lgkmcnt(6)\nM v[0:3],v[192:195],a[32:35],v[0:3]\nv_cvt_pk_bf16_f32 v139,0,v139\nv_cvt_pk_bf16_f32 v140,0,v140\n" \
"M v[0:3],v[196:199],a[48:51],v[0:3]\nbuffer_load_dwordx4 v225,s[36:39],s67 offen offset:1024 lds\nM v[4:7],v[192:195],a[36:39],v[4:7]\nv_cvt_pk_bf16_f32 v141,0,v141\nv_cvt_pk_bf16_f32 v142,0,v142\nM v[4:7],v[196:199],a[52:55],v[4:7]\nv_cvt_pk_bf16_f32 v143,0,v143\nv_cvt_pk_bf16_f32 v144,0,v144\n" \
"M v[64:67],v[192:195],a[40:43],v[64:67]\nv_cvt_pk_bf16_f32 v145,0,v145\nv_cvt_pk_bf16_f32 v146,0,v146\nM v[64:67],v[196:199],a[56:59],v[64:67]\nv_cvt_pk_bf16_f32 v147,0,v147\nv_cvt_pk_bf16_f32 v148,0,v148\nM v[68:71],v[192:195],a[44:47],v[68:71]\nbuffer_load_dwordx4 v226,s[36:39],s67 offen offset:2048 lds\n" \
"ds_read_b64_tr_b16 v[192:193],v241 offset:17408\nds_read_b64_tr_b16 v[194:195],v241 offset:17664\nM v[68:71],v[196:199],a[60:63],v[68:71]\nds_read_b64_tr_b16 v[196:197],v241 offset:25600\nds_read_b64_tr_b16 v[198:199],v241 offset:25856\nM v[8:11],v[200:203],a[32:35],v[8:11]\nv_cvt_pk_bf16_f32 v149,0,v149\nv_cvt_pk_bf16_f32 v150,0,v150\n" \
"M v[8:11],v[204:207],a[48:51],v[8:11]\nv_cvt_pk_bf16_f32 v151,0,v151\nv_mul_f32 v136,v136,v157\nM v[12:15],v[200:203],a[36:39],v[12:15]\nv_mul_f32 v137,v137,v157\nv_mul_f32 v138,v138,v157\nM v[12:15],v[204:207],a[52:55],v[12:15]\nbuffer_load_dwordx4 v227,s[36:39],s67 offen offset:3072 lds\n" \
"M v[72:75],v[200:203],a[40:43],v[72:75]\nv_mul_f32 v139,v139,v157\nv_mul_f32 v140,v140,v157\nM v[72:75],v[204:207],a[56:59],v[72:75]\ns_add_u32 m0,s65,4096\ns_add_u32 s67,s66,33152\nM v[76:79],v[200:203],a[44:47],v[76:79]\nds_read_b64_tr_b16 v[200:201],v242 offset:17408\n" \
"ds_read_b64_tr_b16 v[202:203],v242 offset:17664\nM v[76:79],v[204:207],a[60:63],v[76:79]\nds_read_b64_tr_b16 v[204:205],v242 offset:25600\nds_read_b64_tr_b16 v[206:207],v242 offset:25856\nM v[16:19],v[208:211],a[32:35],v[16:19]\nbuffer_load_dwordx4 v224,s[36:39],s67 offen offset:0 lds\ns_waitcnt lgkmcnt(6)\nM v[16:19],v[212:215],a[48:51],v[16:19]\n" \
"v_mul_f32 v141,v141,v157\nv_mul_f32 v142,v142,v157\nM v[20:23],v[208:211],a[36:39],v[20:23]\nv_mul_f32 v143,v143,v157\nv_mul_f32 v144,v144,v157\nM v[20:23],v[212:215],a[52:55],v[20:23]\nv_mul_f32 v145,v145,v157\nv_mul_f32 v146,v146,v157\n" \
"M v[80:83],v[208:211],a[40:43],v[80:83]\nglobal_load_dword v247,v253,s[16:17]\nM v[80:83],v[212:215],a[56:59],v[80:83]\nbuffer_load_dwordx4 v225,s[36:39],s67 offen offset:1024 lds\nM v[84:87],v[208:211],a[44:47],v[84:87]\nds_read_b64_tr_b16 v[208:209],v241 offset:17920\nds_read_b64_tr_b16 v[210:211],v241 offset:18176\nM v[84:87],v[212:215],a[60:63],v[84:87]\n" \
"ds_read_b64_tr_b16 v[212:213],v241 offset:26112\nds_read_b64_tr_b16 v[214:215],v241 offset:26368\nM v[24:27],v[216:219],a[32:35],v[24:27]\nv_add_u32 v253,0x100000,v253\nv_mul_f32 v147,v147,v157\nM v[24:27],v[220:223],a[48:51],v[24:27]\nv_mul_f32 v148,v148,v157\nv_mul_f32 v149,v149,v157\n" \
"M v[28:31],v[216:219],a[36:39],v[28:31]\nbuffer_load_dwordx4 v226,s[36:39],s67 offen offset:2048 lds\nM v[28:31],v[220:223],a[52:55],v[28:31]\nv_mul_f32 v150,v150,v157\nv_mul_f32 v151,v151,v157\nM v[88:91],v[216:219],a[40:43],v[88:91]\nv_lshlrev_b32 v254,14,v254\ns_add_u32 s78,s78,32768\n" \
"M v[88:91],v[220:223],a[56:59],v[88:91]\nv_and_b32 v169,3,v249\nv_mul_u32_u24 v169,12288,v169\nM v[92:95],v[216:219],a[44:47],v[92:95]\nds_read_b64_tr_b16 v[216:217],v242 offset:17920\nds_read_b64_tr_b16 v[218:219],v242 offset:18176\nM v[92:95],v[220:223],a[60:63],v[92:95]\nbuffer_load_dwordx4 v227,s[36:39],s67 offen offset:3072 lds\n" \
"ds_read_b64_tr_b16 v[220:221],v242 offset:26112\nds_read_b64_tr_b16 v[222:223],v242 offset:26368\nM v[32:35],v[192:195],a[32:35],v[32:35]\nv_and_b32 v170,28,v249\ns_waitcnt lgkmcnt(6)\nM v[32:35],v[196:199],a[48:51],v[32:35]\ns_add_u32 m0,s64,147456\ns_add_u32 s69,s68,1572864\n" \
"M v[36:39],v[192:195],a[36:39],v[36:39]\nv_add_u32 v169,v169,v170\nv_lshrrev_b32 v251,3,v249\nM v[36:39],v[196:199],a[52:55],v[36:39]\ns_cmp_eq_u32 s6,0\ns_cselect_b32 s72,s75,0x8000000\nM v[96:99],v[192:195],a[40:43],v[96:99]\nbuffer_load_dwordx4 v243,s[40:43],s69 offen offset:0 lds\n" \
"M v[96:99],v[196:199],a[56:59],v[96:99]\ns_add_u32 s72,s72,94208\nv_lshlrev_b32 v170,3,v170\nM v[100:103],v[192:195],a[44:47],v[100:103]\nv_cvt_pk_bf16_f32 v144,0,v144\nv_cvt_pk_bf16_f32 v145,0,v145\nM v[100:103],v[196:199],a[60:63],v[100:103]\nv_cvt_pk_bf16_f32 v136,0,v136\n" \
"v_cvt_pk_bf16_f32 v137,0,v137\nM v[40:43],v[200:203],a[32:35],v[40:43]\nv_cvt_pk_bf16_f32 v138,0,v138\nv_cvt_pk_bf16_f32 v139,0,v139\nM v[40:43],v[204:207],a[48:51],v[40:43]\nbuffer_load_dwordx4 v244,s[40:43],s69 offen offset:1024 lds\nM v[44:47],v[200:203],a[36:39],v[44:47]\nv_cvt_pk_bf16_f32 v140,0,v140\n" \
"v_cvt_pk_bf16_f32 v141,0,v141\nM v[44:47],v[204:207],a[52:55],v[44:47]\nv_cvt_pk_bf16_f32 v142,0,v142\nv_cvt_pk_bf16_f32 v143,0,v143\nM v[104:107],v[200:203],a[40:43],v[104:107]\nv_cvt_pk_bf16_f32 v146,0,v146\nv_cvt_pk_bf16_f32 v147,0,v147\nM v[104:107],v[204:207],a[56:59],v[104:107]\n" \
"v_bfe_u32 v250,v249,3,2\nv_cvt_pk_bf16_f32 v148,0,v148\nM v[108:111],v[200:203],a[44:47],v[108:111]\nbuffer_load_dwordx4 v245,s[40:43],s69 offen offset:2048 lds\nM v[108:111],v[204:207],a[60:63],v[108:111]\nv_cvt_pk_bf16_f32 v149,0,v149\nv_cvt_pk_bf16_f32 v150,0,v150\nM v[48:51],v[208:211],a[32:35],v[48:51]\n" \
"v_cvt_pk_bf16_f32 v151,0,v151\ns_waitcnt lgkmcnt(0)\nM v[48:51],v[212:215],a[48:51],v[48:51]\ns_mov_b32 s98,0x4000\ns_mov_b32 s0,28672\nM v[52:55],v[208:211],a[36:39],v[52:55]\ns_add_u32 s10,s61,1\ns_add_u32 s11,s61,2\n" \
"M v[52:55],v[212:215],a[52:55],v[52:55]\nbuffer_load_dwordx4 v246,s[40:43],s69 offen offset:3072 lds\nM v[112:115],v[208:211],a[40:43],v[112:115]\ns_waitcnt vmcnt(13)\ns_barrier\nds_read_b128 a[64:67],v228 offset:16384\nds_read_b128 a[68:71],v228 offset:18432\nds_read_b128 a[72:75],v228 offset:20480\n" \
"ds_read_b128 a[76:79],v228 offset:22528\nds_read_b128 a[80:83],v229 offset:16384\nds_read_b128 a[84:87],v229 offset:18432\nds_read_b128 a[88:91],v229 offset:20480\nds_read_b128 a[92:95],v229 offset:22528\nM v[112:115],v[212:215],a[56:59],v[112:115]\ns_add_u32 m0,s65,8192\ns_add_u32 s67,s66,512\n" \
"ds_read_b64_tr_b16 v[192:193],v241 offset:32768\nds_read_b64_tr_b16 v[194:195],v241 offset:33024\nM v[116:119],v[208:211],a[44:47],v[116:119]\nds_read_b64_tr_b16 v[196:197],v241 offset:40960\nds_read_b64_tr_b16 v[198:199],v241 offset:41216\nM v[116:119],v[212:215],a[60:63],v[116:119]\ns_add_u32 s14,s61,3\ns_and_b32 s10,s10,3\n" \
"M v[56:59],v[216:219],a[32:35],v[56:59]\nbuffer_load_dwordx4 v224,s[36:39],s67 offen offset:0 lds\nM v[56:59],v[220:223],a[48:51],v[56:59]\ns_and_b32 s11,s11,3\ns_and_b32 s14,s14,3\nM v[60:63],v[216:219],a[36:39],v[60:63]\ns_mov_b32 s47,0x20000\ns_mov_b32 s51,0x20000\n" \
"M v[60:63],v[220:223],a[52:55],v[60:63]\ns_mov_b32 s94,0xc000\ns_lshl_b32 s10,s10,13\nM v[120:123],v[216:219],a[40:43],v[120:123]\nglobal_load_dword v247,v253,s[16:17]\nM v[120:123],v[220:223],a[56:59],v[120:123]\nbuffer_load_dwordx4 v225,s[36:39],s67 offen offset:1024 lds\nds_read_b64_tr_b16 v[200:201],v242 offset:32768\n" \
"ds_read_b64_tr_b16 v[202:203],v242 offset:33024\nM v[124:127],v[216:219],a[44:47],v[124:127]\nds_read_b64_tr_b16 v[204:205],v242 offset:40960\nds_read_b64_tr_b16 v[206:207],v242 offset:41216\nM v[124:127],v[220:223],a[60:63],v[124:127]\nds_read_b64_tr_b16 v[208:209],v241 offset:33280\ns_waitcnt lgkmcnt(7)\nM v[0:3],v[192:195],a[64:67],v[0:3]\n" \
"ds_read_b64_tr_b16 v[210:211],v241 offset:33536\ns_waitcnt lgkmcnt(6)\nM v[0:3],v[196:199],a[80:83],v[0:3]\nds_read_b64_tr_b16 v[212:213],v241 offset:41472\nds_read_b64_tr_b16 v[214:215],v241 offset:41728\nM v[4:7],v[192:195],a[68:71],v[4:7]\nbuffer_load_dwordx4 v226,s[36:39],s67 offen offset:2048 lds\nM v[4:7],v[196:199],a[84:87],v[4:7]\n" \
"ds_read_b64_tr_b16 v[216:217],v242 offset:33280\nds_read_b64_tr_b16 v[218:219],v242 offset:33536\nM v[64:67],v[192:195],a[72:75],v[64:67]\nds_read_b64_tr_b16 v[220:221],v242 offset:41472\nds_read_b64_tr_b16 v[222:223],v242 offset:41728\nM v[64:67],v[196:199],a[88:91],v[64:67]\nv_add_u32 v253,0xffd00000,v253\ns_lshl_b32 s11,s11,13\n" \
"M v[68:71],v[192:195],a[76:79],v[68:71]\nds_read_b64_tr_b16 v[192:193],v241 offset:33792\nds_read_b64_tr_b16 v[194:195],v241 offset:34048\nM v[68:71],v[196:199],a[92:95],v[68:71]\nbuffer_load_dwordx4 v227,s[36:39],s67 offen offset:3072 lds\ns_waitcnt lgkmcnt(12)\nM v[8:11],v[200:203],a[64:67],v[8:11]\nds_read_b64_tr_b16 v[196:197],v241 offset:41984\n" \
"s_waitcnt lgkmcnt(11)\nM v[8:11],v[204:207],a[80:83],v[8:11]\ns_add_u32 m0,s65,12288\ns_add_u32 s67,s66,33280\nM v[12:15],v[200:203],a[68:71],v[12:15]\nds_read_b64_tr_b16 v[198:199],v241 offset:42240\ns_lshl_b32 s14,s14,13\nM v[12:15],v[204:207],a[84:87],v[12:15]\n" \
"s_mov_b32 s82,0xf0000\ns_mov_b32 s83,0xfff00ff\nM v[72:75],v[200:203],a[72:75],v[72:75]\nbuffer_load_dwordx4 v224,s[36:39],s67 offen offset:0 lds\nM v[72:75],v[204:207],a[88:91],v[72:75]\ns_mov_b32 s84,0x1f0001\ns_mov_b32 s85,0x1fff01ff\nM v[76:79],v[200:203],a[76:79],v[76:79]\n" \
"ds_read_b64_tr_b16 v[200:201],v242 offset:33792\nds_read_b64_tr_b16 v[202:203],v242 offset:34048\nM v[76:79],v[204:207],a[92:95],v[76:79]\nds_read_b64_tr_b16 v[204:205],v242 offset:41984\ns_waitcnt lgkmcnt(7)\nM v[16:19],v[208:211],a[64:67],v[16:19]\nds_read_b64_tr_b16 v[206:207],v242 offset:42240\ns_mov_b32 s86,0x3f0003\n" \
"M v[16:19],v[212:215],a[80:83],v[16:19]\nbuffer_load_dwordx4 v225,s[36:39],s67 offen offset:1024 lds\nM v[20:23],v[208:211],a[68:71],v[20:23]\ns_mov_b32 s87,0x3fff03ff\ns_mov_b32 s88,0x7f0007\nM v[20:23],v[212:215],a[84:87],v[20:23]\ns_mov_b32 s89,0x7fff07ff\ns_mov_b64 s[90:91],-1\n" \
"M v[80:83],v[208:211],a[72:75],v[80:83]\ns_mov_b32 s28,0x5f800000\ns_mov_b32 s99,0\nM v[80:83],v[212:215],a[88:91],v[80:83]\nM v[84:87],v[208:211],a[76:79],v[84:87]\nbuffer_load_dwordx4 v226,s[36:39],s67 offen offset:2048 lds\nM v[84:87],v[212:215],a[92:95],v[84:87]\nds_read_b64_tr_b16 v[208:209],v241 offset:34304\n" \
"ds_read_b64_tr_b16 v[210:211],v241 offset:34560\nM v[24:27],v[216:219],a[64:67],v[24:27]\nds_read_b64_tr_b16 v[212:213],v241 offset:42496\nds_read_b64_tr_b16 v[214:215],v241 offset:42752\nM v[24:27],v[220:223],a[80:83],v[24:27]\nM v[28:31],v[216:219],a[68:71],v[28:31]\nM v[28:31],v[220:223],a[84:87],v[28:31]\nbuffer_load_dwordx4 v227,s[36:39],s67 offen offset:3072 lds\n" \
"M v[88:91],v[216:219],a[72:75],v[88:91]\nM v[88:91],v[220:223],a[88:91],v[88:91]\ns_add_u32 m0,s64,98304\ns_add_u32 s69,s68,2097152\nM v[92:95],v[216:219],a[76:79],v[92:95]\nds_read_b64_tr_b16 v[216:217],v242 offset:34304\nds_read_b64_tr_b16 v[218:219],v242 offset:34560\nM v[92:95],v[220:223],a[92:95],v[92:95]\n" \
"ds_read_b64_tr_b16 v[220:221],v242 offset:42496\ns_waitcnt lgkmcnt(7)\nM v[32:35],v[192:195],a[64:67],v[32:35]\nbuffer_load_dwordx4 v243,s[40:43],s69 offen offset:0 lds\nM v[32:35],v[196:199],a[80:83],v[32:35]\nds_read_b64_tr_b16 v[222:223],v242 offset:42752\nM v[36:39],v[192:195],a[68:71],v[36:39]\nM v[36:39],v[196:199],a[84:87],v[36:39]\n" \
"M v[96:99],v[192:195],a[72:75],v[96:99]\nM v[96:99],v[196:199],a[88:91],v[96:99]\nbuffer_load_dwordx4 v244,s[40:43],s69 offen offset:1024 lds\nM v[100:103],v[192:195],a[76:79],v[100:103]\nM v[100:103],v[196:199],a[92:95],v[100:103]\nM v[40:43],v[200:203],a[64:67],v[40:43]\nM v[40:43],v[204:207],a[80:83],v[40:43]\nM v[44:47],v[200:203],a[68:71],v[44:47]\n" \
"buffer_load_dwordx4 v245,s[40:43],s69 offen offset:2048 lds\nM v[44:47],v[204:207],a[84:87],v[44:47]\nM v[104:107],v[200:203],a[72:75],v[104:107]\nM v[104:107],v[204:207],a[88:91],v[104:107]\nM v[108:111],v[200:203],a[76:79],v[108:111]\nM v[108:111],v[204:207],a[92:95],v[108:111]\nbuffer_load_dwordx4 v246,s[40:43],s69 offen offset:3072 lds\ns_waitcnt lgkmcnt(1)\n" \
"M v[48:51],v[208:211],a[64:67],v[48:51]\nM v[48:51],v[212:215],a[80:83],v[48:51]\ns_add_u32 m0,s65,16384\ns_add_u32 s67,s66,640\nM v[52:55],v[208:211],a[68:71],v[52:55]\nM v[52:55],v[212:215],a[84:87],v[52:55]\nM v[112:115],v[208:211],a[72:75],v[112:115]\nbuffer_load_dwordx4 v224,s[36:39],s67 offen offset:0 lds\n" \
"s_waitcnt vmcnt(14) lgkmcnt(0)\ns_barrier\nds_read_b128 a[96:99],v228 offset:0\nds_read_b128 a[100:103],v228 offset:2048\nds_read_b128 a[104:107],v228 offset:4096\nds_read_b128 a[108:111],v228 offset:6144\nds_read_b128 a[112:115],v229 offset:0\nds_read_b128 a[116:119],v229 offset:2048\n" \
"ds_read_b128 a[120:123],v229 offset:4096\nds_read_b128 a[124:127],v229 offset:6144\nM v[112:115],v[212:215],a[88:91],v[112:115]\nds_read_b64_tr_b16 v[192:193],v241 offset:49152\nds_read_b64_tr_b16 v[194:195],v241 offset:49408\nM v[116:119],v[208:211],a[76:79],v[116:119]\nds_read_b64_tr_b16 v[196:197],v241 offset:57344\nds_read_b64_tr_b16 v[198:199],v241 offset:57600\n" \
"M v[116:119],v[212:215],a[92:95],v[116:119]\nM v[56:59],v[216:219],a[64:67],v[56:59]\nM v[56:59],v[220:223],a[80:83],v[56:59]\nbuffer_load_dwordx4 v225,s[36:39],s67 offen offset:1024 lds\nM v[60:63],v[216:219],a[68:71],v[60:63]\nM v[60:63],v[220:223],a[84:87],v[60:63]\nM v[120:123],v[216:219],a[72:75],v[120:123]\nM v[120:123],v[220:223],a[88:91],v[120:123]\n" \
"ds_read_b64_tr_b16 v[200:201],v242 offset:49152\nds_read_b64_tr_b16 v[202:203],v242 offset:49408\nM v[124:127],v[216:219],a[76:79],v[124:127]\nbuffer_load_dwordx4 v226,s[36:39],s67 offen offset:2048 lds\nds_read_b64_tr_b16 v[204:205],v242 offset:57344\nds_read_b64_tr_b16 v[206:207],v242 offset:57600\nM v[124:127],v[220:223],a[92:95],v[124:127]\nds_read_b64_tr_b16 v[208:209],v241 offset:49664\n" \
"s_waitcnt lgkmcnt(7)\nM v[0:3],v[192:195],a[96:99],v[0:3]\nds_read_b64_tr_b16 v[210:211],v241 offset:49920\ns_waitcnt lgkmcnt(6)\nM v[0:3],v[196:199],a[112:115],v[0:3]\nds_read_b64_tr_b16 v[212:213],v241 offset:57856\nds_read_b64_tr_b16 v[214:215],v241 offset:58112\nM v[4:7],v[192:195],a[100:103],v[4:7]\n" \
"ds_read_b64_tr_b16 v[216:217],v242 offset:49664\nds_read_b64_tr_b16 v[218:219],v242 offset:49920\nM v[4:7],v[196:199],a[116:119],v[4:7]\nbuffer_load_dwordx4 v227,s[36:39],s67 offen offset:3072 lds\nM v[64:67],v[192:195],a[104:107],v[64:67]\nds_read_b64_tr_b16 v[220:221],v242 offset:57856\nds_read_b64_tr_b16 v[222:223],v242 offset:58112\nM v[64:67],v[196:199],a[120:123],v[64:67]\n" \
"s_add_u32 m0,s65,20480\ns_add_u32 s67,s66,33408\nM v[68:71],v[192:195],a[108:111],v[68:71]\nds_read_b64_tr_b16 v[192:193],v241 offset:50176\nds_read_b64_tr_b16 v[194:195],v241 offset:50432\nM v[68:71],v[196:199],a[124:127],v[68:71]\nds_read_b64_tr_b16 v[196:197],v241 offset:58368\ns_waitcnt lgkmcnt(13)\n" \
"M v[8:11],v[200:203],a[96:99],v[8:11]\nbuffer_load_dwordx4 v224,s[36:39],s67 offen offset:0 lds\ns_waitcnt lgkmcnt(11)\nM v[8:11],v[204:207],a[112:115],v[8:11]\nds_read_b64_tr_b16 v[198:199],v241 offset:58624\nM v[12:15],v[200:203],a[100:103],v[12:15]\nM v[12:15],v[204:207],a[116:119],v[12:15]\nM v[72:75],v[200:203],a[104:107],v[72:75]\n" \
"M v[72:75],v[204:207],a[120:123],v[72:75]\nbuffer_load_dwordx4 v225,s[36:39],s67 offen offset:1024 lds\nM v[76:79],v[200:203],a[108:111],v[76:79]\nds_read_b64_tr_b16 v[200:201],v242 offset:50176\nds_read_b64_tr_b16 v[202:203],v242 offset:50432\nM v[76:79],v[204:207],a[124:127],v[76:79]\nds_read_b64_tr_b16 v[204:205],v242 offset:58368\ns_waitcnt lgkmcnt(9)\n" \
"M v[16:19],v[208:211],a[96:99],v[16:19]\nds_read_b64_tr_b16 v[206:207],v242 offset:58624\nM v[16:19],v[212:215],a[112:115],v[16:19]\nM v[20:23],v[208:211],a[100:103],v[20:23]\nbuffer_load_dwordx4 v226,s[36:39],s67 offen offset:2048 lds\nM v[20:23],v[212:215],a[116:119],v[20:23]\nM v[80:83],v[208:211],a[104:107],v[80:83]\nM v[80:83],v[212:215],a[120:123],v[80:83]\n" \
"M v[84:87],v[208:211],a[108:111],v[84:87]\nds_read_b64_tr_b16 v[208:209],v241 offset:50688\nds_read_b64_tr_b16 v[210:211],v241 offset:50944\nM v[84:87],v[212:215],a[124:127],v[84:87]\nbuffer_load_dwordx4 v227,s[36:39],s67 offen offset:3072 lds\nM v[24:27],v[216:219],a[96:99],v[24:27]\nds_read_b64_tr_b16 v[212:213],v241 offset:58880\ns_waitcnt lgkmcnt(7)\n" \
"M v[24:27],v[220:223],a[112:115],v[24:27]\ns_add_u32 m0,s64,114688\ns_add_u32 s69,s68,2621440\nM v[28:31],v[216:219],a[100:103],v[28:31]\nds_read_b64_tr_b16 v[214:215],v241 offset:59136\nM v[28:31],v[220:223],a[116:119],v[28:31]\nM v[88:91],v[216:219],a[104:107],v[88:91]\nbuffer_load_dwordx4 v243,s[40:43],s69 offen offset:0 lds\n" \
"M v[88:91],v[220:223],a[120:123],v[88:91]\nM v[92:95],v[216:219],a[108:111],v[92:95]\nds_read_b64_tr_b16 v[216:217],v242 offset:50688\nds_read_b64_tr_b16 v[218:219],v242 offset:50944\nM v[92:95],v[220:223],a[124:127],v[92:95]\nds_read_b64_tr_b16 v[220:221],v242 offset:58880\nds_read_b64_tr_b16 v[222:223],v242 offset:59136\nM v[32:35],v[192:195],a[96:99],v[32:35]\n" \
"buffer_load_dwordx2 v[166:167],v165,s[52:55],s80 offen sc1\nM v[32:35],v[196:199],a[112:115],v[32:35]\nbuffer_load_dwordx4 v244,s[40:43],s69 offen offset:1024 lds\nM v[36:39],v[192:195],a[100:103],v[36:39]\nM v[36:39],v[196:199],a[116:119],v[36:39]\nM v[96:99],v[192:195],a[104:107],v[96:99]\nM v[96:99],v[196:199],a[120:123],v[96:99]\nM v[100:103],v[192:195],a[108:111],v[100:103]\n" \
"buffer_load_dwordx4 v245,s[40:43],s69 offen offset:2048 lds\nM v[100:103],v[196:199],a[124:127],v[100:103]\ns_waitcnt lgkmcnt(5)\nM v[40:43],v[200:203],a[96:99],v[40:43]\nM v[40:43],v[204:207],a[112:115],v[40:43]\nM v[44:47],v[200:203],a[100:103],v[44:47]\nM v[44:47],v[204:207],a[116:119],v[44:47]\nbuffer_load_dwordx4 v246,s[40:43],s69 offen offset:3072 lds\n" \
"M v[104:107],v[200:203],a[104:107],v[104:107]\nM v[104:107],v[204:207],a[120:123],v[104:107]\ns_add_u32 m0,s65,0\ns_add_u32 s67,s66,768\nM v[108:111],v[200:203],a[108:111],v[108:111]\nM v[108:111],v[204:207],a[124:127],v[108:111]\nM v[48:51],v[208:211],a[96:99],v[48:51]\nbuffer_load_dwordx4 v224,s[36:39],s67 offen offset:0 lds\n" \
"s_waitcnt lgkmcnt(0)\nM v[48:51],v[212:215],a[112:115],v[48:51]\nM v[52:55],v[208:211],a[100:103],v[52:55]\nM v[52:55],v[212:215],a[116:119],v[52:55]\nM v[112:115],v[208:211],a[104:107],v[112:115]\ns_waitcnt vmcnt(14)\ns_barrier\nM v[112:115],v[212:215],a[120:123],v[112:115]\n" \
"buffer_load_dwordx4 v225,s[36:39],s67 offen offset:1024 lds\nds_read_b128 a[128:131],v228 offset:8192\nds_read_b64_tr_b16 v[192:193],v241 offset:0\nds_read_b64_tr_b16 v[194:195],v241 offset:256\nM v[116:119],v[208:211],a[108:111],v[116:119]\nds_read_b128 a[144:147],v229 offset:8192\nds_read_b64_tr_b16 v[196:197],v241 offset:8192\nds_read_b64_tr_b16 v[198:199],v241 offset:8448\n" \
"M v[116:119],v[212:215],a[124:127],v[116:119]\nds_read_b128 a[132:135],v228 offset:10240\nM v[56:59],v[216:219],a[96:99],v[56:59]\nds_read_b128 a[148:151],v229 offset:10240\nM v[56:59],v[220:223],a[112:115],v[56:59]\nds_read_b128 a[136:139],v228 offset:12288\nM v[60:63],v[216:219],a[100:103],v[60:63]\nbuffer_load_dwordx4 v226,s[36:39],s67 offen offset:2048 lds\n" \
"ds_read_b128 a[152:155],v229 offset:12288\nM v[60:63],v[220:223],a[116:119],v[60:63]\nds_read_b128 a[140:143],v228 offset:14336\nM v[120:123],v[216:219],a[104:107],v[120:123]\nds_read_b128 a[156:159],v229 offset:14336\nM v[120:123],v[220:223],a[120:123],v[120:123]\nds_read_b64_tr_b16 v[200:201],v242 offset:0\nds_read_b64_tr_b16 v[202:203],v242 offset:256\n" \
"M v[124:127],v[216:219],a[108:111],v[124:127]\nds_read_b64_tr_b16 v[204:205],v242 offset:8192\nds_read_b64_tr_b16 v[206:207],v242 offset:8448\nM v[124:127],v[220:223],a[124:127],v[124:127]\nbuffer_load_dwordx4 v227,s[36:39],s67 offen offset:3072 lds\ns_waitcnt lgkmcnt(13)\nM v[0:3],v[192:195],a[128:131],v[0:3]\nds_read_b64_tr_b16 v[208:209],v241 offset:512\n" \
"s_waitcnt lgkmcnt(11)\nM v[0:3],v[196:199],a[144:147],v[0:3]\ns_add_u32 m0,s65,4096\ns_add_u32 s67,s66,33536\ns_waitcnt lgkmcnt(10)\nM v[4:7],v[192:195],a[132:135],v[4:7]\nds_read_b64_tr_b16 v[210:211],v241 offset:768\ns_waitcnt lgkmcnt(10)\n" \
"M v[4:7],v[196:199],a[148:151],v[4:7]\nds_read_b64_tr_b16 v[212:213],v241 offset:8704\ns_waitcnt lgkmcnt(10)\nM v[64:67],v[192:195],a[136:139],v[64:67]\nbuffer_load_dwordx4 v224,s[36:39],s67 offen offset:0 lds\ns_waitcnt lgkmcnt(9)\nM v[64:67],v[196:199],a[152:155],v[64:67]\nds_read_b64_tr_b16 v[214:215],v241 offset:8960\n" \
"s_waitcnt lgkmcnt(9)\nM v[68:71],v[192:195],a[140:143],v[68:71]\nds_read_b64_tr_b16 v[216:217],v242 offset:512\ns_waitcnt lgkmcnt(9)\nM v[68:71],v[196:199],a[156:159],v[68:71]\nds_read_b64_tr_b16 v[218:219],v242 offset:768\ns_waitcnt lgkmcnt(8)\nM v[8:11],v[200:203],a[128:131],v[8:11]\n" \
"ds_read_b64_tr_b16 v[220:221],v242 offset:8704\ns_waitcnt lgkmcnt(7)\nM v[8:11],v[204:207],a[144:147],v[8:11]\nbuffer_load_dwordx4 v225,s[36:39],s67 offen offset:1024 lds\nM v[12:15],v[200:203],a[132:135],v[12:15]\nds_read_b64_tr_b16 v[222:223],v242 offset:8960\nds_read_b64_tr_b16 v[192:193],v241 offset:1024\nM v[12:15],v[204:207],a[148:151],v[12:15]\n" \
"ds_read_b64_tr_b16 v[194:195],v241 offset:1280\nds_read_b64_tr_b16 v[196:197],v241 offset:9216\nM v[72:75],v[200:203],a[136:139],v[72:75]\nds_read_b64_tr_b16 v[198:199],v241 offset:9472\nM v[72:75],v[204:207],a[152:155],v[72:75]\nM v[76:79],v[200:203],a[140:143],v[76:79]\nbuffer_load_dwordx4 v226,s[36:39],s67 offen offset:2048 lds\nM v[76:79],v[204:207],a[156:159],v[76:79]\n" \
"ds_read_b64_tr_b16 v[200:201],v242 offset:1024\ns_waitcnt lgkmcnt(10)\nM v[16:19],v[208:211],a[128:131],v[16:19]\nds_read_b64_tr_b16 v[202:203],v242 offset:1280\ns_waitcnt lgkmcnt(10)\nM v[16:19],v[212:215],a[144:147],v[16:19]\nds_read_b64_tr_b16 v[204:205],v242 offset:9216\nds_read_b64_tr_b16 v[206:207],v242 offset:9472\n" \
"M v[20:23],v[208:211],a[132:135],v[20:23]\nM v[20:23],v[212:215],a[148:151],v[20:23]\nbuffer_load_dwordx4 v227,s[36:39],s67 offen offset:3072 lds\nM v[80:83],v[208:211],a[136:139],v[80:83]\nM v[80:83],v[212:215],a[152:155],v[80:83]\ns_add_u32 m0,s64,131072\ns_add_u32 s69,s68,3145728\nM v[84:87],v[208:211],a[140:143],v[84:87]\n" \
"ds_read_b64_tr_b16 v[208:209],v241 offset:1536\nds_read_b64_tr_b16 v[210:211],v241 offset:1792\nM v[84:87],v[212:215],a[156:159],v[84:87]\nds_read_b64_tr_b16 v[212:213],v241 offset:9728\ns_waitcnt lgkmcnt(8)\nM v[24:27],v[216:219],a[128:131],v[24:27]\nbuffer_load_dwordx4 v243,s[40:43],s69 offen offset:0 lds\nM v[24:27],v[220:223],a[144:147],v[24:27]\n" \
"ds_read_b64_tr_b16 v[214:215],v241 offset:9984\nM v[28:31],v[216:219],a[132:135],v[28:31]\nM v[28:31],v[220:223],a[148:151],v[28:31]\nM v[88:91],v[216:219],a[136:139],v[88:91]\nM v[88:91],v[220:223],a[152:155],v[88:91]\nbuffer_load_dwordx4 v244,s[40:43],s69 offen offset:1024 lds\nM v[92:95],v[216:219],a[140:143],v[92:95]\nds_read_b64_tr_b16 v[216:217],v242 offset:1536\n" \
"ds_read_b64_tr_b16 v[218:219],v242 offset:1792\nM v[92:95],v[220:223],a[156:159],v[92:95]\nds_read_b64_tr_b16 v[220:221],v242 offset:9728\nds_read_b64_tr_b16 v[222:223],v242 offset:9984\nM v[32:35],v[192:195],a[128:131],v[32:35]\ns_waitcnt lgkmcnt(8)\nM v[32:35],v[196:199],a[144:147],v[32:35]\nM v[36:39],v[192:195],a[132:135],v[36:39]\n" \
"buffer_load_dwordx4 v245,s[40:43],s69 offen offset:2048 lds\nM v[36:39],v[196:199],a[148:151],v[36:39]\nM v[96:99],v[192:195],a[136:139],v[96:99]\ns_waitcnt vmcnt(14)\nv_xor_b32 v166,v166,v230\nM v[96:99],v[196:199],a[152:155],v[96:99]\nv_xor_b32 v167,v167,v231\nv_or_b32 v166,v166,v167\n" \
"M v[100:103],v[192:195],a[140:143],v[100:103]\nv_cmp_ne_u32 vcc,0,v166\nM v[100:103],v[196:199],a[156:159],v[100:103]\nbuffer_load_dwordx4 v246,s[40:43],s69 offen offset:3072 lds\nM v[40:43],v[200:203],a[128:131],v[40:43]\ns_cbranch_vccnz .Lhs\n.Lhb:\nv_bfe_u32 v231,v249,2,3\n" \
"M v[40:43],v[204:207],a[144:147],v[40:43]\ns_add_u32 m0,s65,8192\ns_add_u32 s67,s66,896\nM v[44:47],v[200:203],a[132:135],v[44:47]\nv_mul_u32_u24 v231,3584,v231\nv_and_b32 v230,31,v249\nM v[44:47],v[204:207],a[148:151],v[44:47]\nv_lshlrev_b32 v230,7,v230\n" \
"M v[104:107],v[200:203],a[136:139],v[104:107]\nbuffer_load_dwordx4 v224,s[36:39],s67 offen offset:0 lds\nM v[104:107],v[204:207],a[152:155],v[104:107]\nM v[108:111],v[200:203],a[140:143],v[108:111]\nM v[108:111],v[204:207],a[156:159],v[108:111]\ns_waitcnt lgkmcnt(0)\nM v[48:51],v[208:211],a[128:131],v[48:51]\nM v[48:51],v[212:215],a[144:147],v[48:51]\n" \
"buffer_load_dwordx4 v225,s[36:39],s67 offen offset:1024 lds\nM v[52:55],v[208:211],a[132:135],v[52:55]\nM v[52:55],v[212:215],a[148:151],v[52:55]\nM v[112:115],v[208:211],a[136:139],v[112:115]\ns_waitcnt vmcnt(14)\ns_barrier\nM v[112:115],v[212:215],a[152:155],v[112:115]\nds_read_b128 a[160:163],v228 offset:16384\n" \
"ds_read_b64_tr_b16 v[192:193],v241 offset:16384\nds_read_b64_tr_b16 v[194:195],v241 offset:16640\nM v[116:119],v[208:211],a[140:143],v[116:119]\nds_read_b128 a[176:179],v229 offset:16384\nds_read_b64_tr_b16 v[196:197],v241 offset:24576\nds_read_b64_tr_b16 v[198:199],v241 offset:24832\nM v[116:119],v[212:215],a[156:159],v[116:119]\nds_read_b128 a[164:167],v228 offset:18432\n" \
"M v[56:59],v[216:219],a[128:131],v[56:59]\nbuffer_load_dwordx4 v[152:155],v240,s[52:55],s71 offen sc1\nds_read_b128 a[180:183],v229 offset:18432\nM v[56:59],v[220:223],a[144:147],v[56:59]\nbuffer_load_dwordx4 v226,s[36:39],s67 offen offset:2048 lds\ns_add_u32 s71,s70,798720\nds_read_b128 a[168:171],v228 offset:20480\nbuffer_load_dwordx4 v[156:159],v240,s[52:55],s71 offen sc1\n" \
"M v[60:63],v[216:219],a[132:135],v[60:63]\nds_read_b128 a[184:187],v229 offset:20480\ns_add_u32 s71,s70,811008\nM v[60:63],v[220:223],a[148:151],v[60:63]\nbuffer_load_dwordx4 v227,s[36:39],s67 offen offset:3072 lds\nbuffer_load_dwordx4 v[160:163],v240,s[52:55],s71 offen sc1\nds_read_b128 a[172:175],v228 offset:22528\nM v[120:123],v[216:219],a[136:139],v[120:123]\n" \
"s_add_u32 s71,s70,823296\nds_read_b128 a[188:191],v229 offset:22528\nbuffer_load_dwordx4 v[164:167],v240,s[52:55],s71 offen sc1\nM v[120:123],v[220:223],a[152:155],v[120:123]\ns_add_u32 m0,s65,12288\ns_add_u32 s67,s66,33664\nds_read_b64_tr_b16 v[200:201],v242 offset:16384\nds_read_b64_tr_b16 v[202:203],v242 offset:16640\n" \
"M v[124:127],v[216:219],a[140:143],v[124:127]\nds_read_b64_tr_b16 v[204:205],v242 offset:24576\nds_read_b64_tr_b16 v[206:207],v242 offset:24832\nM v[124:127],v[220:223],a[156:159],v[124:127]\ns_lshl_b32 s71,s6,5\ns_waitcnt lgkmcnt(9)\nM v[0:3],v[192:195],a[160:163],v[0:3]\nds_read_b64_tr_b16 v[208:209],v241 offset:16896\n" \
"s_add_u32 s71,s71,790528\nM v[0:3],v[196:199],a[176:179],v[0:3]\nds_read_b64_tr_b16 v[210:211],v241 offset:17152\nds_read_b64_tr_b16 v[212:213],v241 offset:25088\nM v[4:7],v[192:195],a[164:167],v[4:7]\nds_read_b64_tr_b16 v[214:215],v241 offset:25344\ns_waitcnt lgkmcnt(11)\nM v[4:7],v[196:199],a[180:183],v[4:7]\n" \
"ds_read_b64_tr_b16 v[216:217],v242 offset:16896\nds_read_b64_tr_b16 v[218:219],v242 offset:17152\nM v[64:67],v[192:195],a[168:171],v[64:67]\nds_read_b64_tr_b16 v[220:221],v242 offset:25088\ns_waitcnt lgkmcnt(13)\nM v[64:67],v[196:199],a[184:187],v[64:67]\nds_read_b64_tr_b16 v[222:223],v242 offset:25344\ns_waitcnt lgkmcnt(13)\n" \
"M v[68:71],v[192:195],a[172:175],v[68:71]\nglobal_load_dword v247,v253,s[12:13]\ns_waitcnt lgkmcnt(12)\nM v[68:71],v[196:199],a[188:191],v[68:71]\nds_read_b64_tr_b16 v[192:193],v241 offset:17408\ns_waitcnt lgkmcnt(11)\nM v[8:11],v[200:203],a[160:163],v[8:11]\nds_read_b64_tr_b16 v[194:195],v241 offset:17664\n" \
"s_waitcnt lgkmcnt(10)\nM v[8:11],v[204:207],a[176:179],v[8:11]\nds_read_b64_tr_b16 v[196:197],v241 offset:25600\nds_read_b64_tr_b16 v[198:199],v241 offset:25856\nM v[12:15],v[200:203],a[164:167],v[12:15]\nbuffer_load_dword v247,v254,s[56:59],s78 offen\nM v[12:15],v[204:207],a[180:183],v[12:15]\nv_add_u32 v253,0x100000,v253\n" \
"v_and_b32 v254,3,v249\nM v[72:75],v[200:203],a[168:171],v[72:75]\ns_cmp_lt_u32 s7,2\ns_cselect_b32 s70,0,1.0\nM v[72:75],v[204:207],a[184:187],v[72:75]\ns_cmp_eq_u32 s62,5\ns_cselect_b64 s[56:57],s[22:23],s[24:25]\nM v[76:79],v[200:203],a[172:175],v[76:79]\n" \
"ds_read_b64_tr_b16 v[200:201],v242 offset:17408\nds_read_b64_tr_b16 v[202:203],v242 offset:17664\nM v[76:79],v[204:207],a[188:191],v[76:79]\nds_read_b64_tr_b16 v[204:205],v242 offset:25600\ns_waitcnt lgkmcnt(7)\nM v[16:19],v[208:211],a[160:163],v[16:19]\nds_read_b64_tr_b16 v[206:207],v242 offset:25856\nv_lshl_add_u32 v231,v254,7,v231\n" \
"M v[16:19],v[212:215],a[176:179],v[16:19]\ns_add_u32 s69,s68,3670016\nM v[20:23],v[208:211],a[164:167],v[20:23]\nbuffer_load_dwordx4 v224,s[36:39],s67 offen offset:0 lds\nM v[20:23],v[212:215],a[180:183],v[20:23]\nM v[80:83],v[208:211],a[168:171],v[80:83]\nM v[80:83],v[212:215],a[184:187],v[80:83]\nM v[84:87],v[208:211],a[172:175],v[84:87]\n" \
"buffer_load_dwordx4 v225,s[36:39],s67 offen offset:1024 lds\nM v[84:87],v[212:215],a[188:191],v[84:87]\nds_read_b64_tr_b16 v[208:209],v241 offset:17920\nds_read_b64_tr_b16 v[210:211],v241 offset:18176\nM v[24:27],v[216:219],a[160:163],v[24:27]\nds_read_b64_tr_b16 v[212:213],v241 offset:26112\nds_read_b64_tr_b16 v[214:215],v241 offset:26368\nM v[24:27],v[220:223],a[176:179],v[24:27]\n" \
"M v[28:31],v[216:219],a[164:167],v[28:31]\nbuffer_load_dwordx4 v226,s[36:39],s67 offen offset:2048 lds\nM v[28:31],v[220:223],a[180:183],v[28:31]\nM v[88:91],v[216:219],a[168:171],v[88:91]\nM v[88:91],v[220:223],a[184:187],v[88:91]\nM v[92:95],v[216:219],a[172:175],v[92:95]\nbuffer_load_dwordx4 v227,s[36:39],s67 offen offset:3072 lds\nM v[92:95],v[220:223],a[188:191],v[92:95]\n" \
"s_add_u32 m0,s64,147456\ns_waitcnt lgkmcnt(4)\nM v[32:35],v[192:195],a[160:163],v[32:35]\nds_read_b64_tr_b16 v[216:217],v242 offset:17920\nds_read_b64_tr_b16 v[218:219],v242 offset:18176\nM v[32:35],v[196:199],a[176:179],v[32:35]\nds_read_b64_tr_b16 v[220:221],v242 offset:26112\nds_read_b64_tr_b16 v[222:223],v242 offset:26368\n" \
"M v[36:39],v[192:195],a[164:167],v[36:39]\nbuffer_load_dwordx4 v243,s[40:43],s69 offen offset:0 lds\nM v[36:39],v[196:199],a[180:183],v[36:39]\ns_cmp_eq_u32 s62,0\ns_cselect_b32 s36,0x8000,0\nM v[96:99],v[192:195],a[168:171],v[96:99]\ns_add_u32 s36,s36,s64\nM v[96:99],v[196:199],a[184:187],v[96:99]\n" \
"M v[100:103],v[192:195],a[172:175],v[100:103]\nbuffer_load_dwordx4 v244,s[40:43],s69 offen offset:1024 lds\nM v[100:103],v[196:199],a[188:191],v[100:103]\nM v[40:43],v[200:203],a[160:163],v[40:43]\nM v[40:43],v[204:207],a[176:179],v[40:43]\nM v[44:47],v[200:203],a[164:167],v[44:47]\nglobal_load_dword v247,v253,s[12:13]\nM v[44:47],v[204:207],a[180:183],v[44:47]\n" \
"buffer_load_dwordx4 v245,s[40:43],s69 offen offset:2048 lds\nM v[104:107],v[200:203],a[168:171],v[104:107]\nv_add_u32 v253,0x100000,v253\nM v[104:107],v[204:207],a[184:187],v[104:107]\nM v[108:111],v[200:203],a[172:175],v[108:111]\nM v[108:111],v[204:207],a[188:191],v[108:111]\ns_waitcnt lgkmcnt(0)\nM v[48:51],v[208:211],a[160:163],v[48:51]\n" \
"buffer_load_dwordx4 v246,s[40:43],s69 offen offset:3072 lds\nM v[48:51],v[212:215],a[176:179],v[48:51]\ns_sub_u32 s40,s16,0x1000\ns_subb_u32 s41,s17,0\nM v[52:55],v[208:211],a[164:167],v[52:55]\ns_add_u32 m0,s64,98304\ns_add_u32 s69,s68,0\nM v[52:55],v[212:215],a[180:183],v[52:55]\n" \
"s_mov_b32 s16,0\ns_mov_b32 s17,3\nM v[112:115],v[208:211],a[168:171],v[112:115]\ns_waitcnt vmcnt(19)\ns_barrier\nM v[112:115],v[212:215],a[184:187],v[112:115]\nds_read_b128 a[192:195],v228 offset:0\nds_read_b64_tr_b16 v[192:193],v241 offset:32768\n" \
"ds_read_b64_tr_b16 v[194:195],v241 offset:33024\nM v[116:119],v[208:211],a[172:175],v[116:119]\nds_read_b128 a[208:211],v229 offset:0\nds_read_b64_tr_b16 v[196:197],v241 offset:40960\nds_read_b64_tr_b16 v[198:199],v241 offset:41216\nM v[116:119],v[212:215],a[188:191],v[116:119]\nds_read_b128 a[196:199],v228 offset:2048\nM v[56:59],v[216:219],a[160:163],v[56:59]\n" \
"ds_read_b128 a[212:215],v229 offset:2048\nM v[56:59],v[220:223],a[176:179],v[56:59]\nds_read_b128 a[200:203],v228 offset:4096\nM v[60:63],v[216:219],a[164:167],v[60:63]\nds_read_b128 a[216:219],v229 offset:4096\nM v[60:63],v[220:223],a[180:183],v[60:63]\nds_read_b128 a[204:207],v228 offset:6144\nM v[120:123],v[216:219],a[168:171],v[120:123]\n" \
"ds_read_b128 a[220:223],v229 offset:6144\nM v[120:123],v[220:223],a[184:187],v[120:123]\nds_read_b64_tr_b16 v[200:201],v242 offset:32768\nds_read_b64_tr_b16 v[202:203],v242 offset:33024\nM v[124:127],v[216:219],a[172:175],v[124:127]\nds_read_b64_tr_b16 v[204:205],v242 offset:40960\nds_read_b64_tr_b16 v[206:207],v242 offset:41216\nM v[124:127],v[220:223],a[188:191],v[124:127]\n" \
"s_waitcnt vmcnt(14)\nv_add_f32 v152,v152,v156\ns_waitcnt lgkmcnt(13)\nM v[0:3],v[192:195],a[192:195],v[0:3]\nds_read_b64_tr_b16 v[208:209],v241 offset:33280\ns_waitcnt lgkmcnt(11)\nM v[0:3],v[196:199],a[208:211],v[0:3]\nds_read_b64_tr_b16 v[210:211],v241 offset:33536\n" \
"s_waitcnt lgkmcnt(11)\nM v[4:7],v[192:195],a[196:199],v[4:7]\nds_read_b64_tr_b16 v[212:213],v241 offset:41472\ns_waitcnt lgkmcnt(11)\nM v[4:7],v[196:199],a[212:215],v[4:7]\nds_read_b64_tr_b16 v[214:215],v241 offset:41728\ns_waitcnt lgkmcnt(11)\nM v[64:67],v[192:195],a[200:203],v[64:67]\n" \
"ds_read_b64_tr_b16 v[216:217],v242 offset:33280\ns_waitcnt lgkmcnt(11)\nM v[64:67],v[196:199],a[216:219],v[64:67]\nds_read_b64_tr_b16 v[218:219],v242 offset:33536\ns_waitcnt lgkmcnt(11)\nM v[68:71],v[192:195],a[204:207],v[68:71]\nds_read_b64_tr_b16 v[220:221],v242 offset:41472\ns_waitcnt lgkmcnt(11)\n" \
"M v[68:71],v[196:199],a[220:223],v[68:71]\nds_read_b64_tr_b16 v[222:223],v242 offset:41728\ns_waitcnt lgkmcnt(10)\nM v[8:11],v[200:203],a[192:195],v[8:11]\nds_read_b64_tr_b16 v[192:193],v241 offset:33792\ns_waitcnt lgkmcnt(9)\nM v[8:11],v[204:207],a[208:211],v[8:11]\nds_read_b64_tr_b16 v[194:195],v241 offset:34048\n" \
"ds_read_b64_tr_b16 v[196:197],v241 offset:41984\nM v[12:15],v[200:203],a[196:199],v[12:15]\nds_read_b64_tr_b16 v[198:199],v241 offset:42240\ns_waitcnt vmcnt(11)\nv_add_f32 v160,v160,v164\nM v[12:15],v[204:207],a[212:215],v[12:15]\nv_add_f32 v153,v153,v157\nv_add_f32 v161,v161,v165\n" \
"M v[72:75],v[200:203],a[200:203],v[72:75]\nv_add_f32 v154,v154,v158\nv_add_f32 v162,v162,v166\nM v[72:75],v[204:207],a[216:219],v[72:75]\nv_add_f32 v155,v155,v159\nv_add_f32 v163,v163,v167\nM v[76:79],v[200:203],a[204:207],v[76:79]\nds_read_b64_tr_b16 v[200:201],v242 offset:33792\n" \
"ds_read_b64_tr_b16 v[202:203],v242 offset:34048\nM v[76:79],v[204:207],a[220:223],v[76:79]\nds_read_b64_tr_b16 v[204:205],v242 offset:41984\ns_waitcnt lgkmcnt(9)\nM v[16:19],v[208:211],a[192:195],v[16:19]\nds_read_b64_tr_b16 v[206:207],v242 offset:42240\nv_add_f32 v152,v152,v160\nM v[16:19],v[212:215],a[208:211],v[16:19]\n" \
"buffer_load_dword v168,v169,s[52:55],s71 offen sc1\nM v[20:23],v[208:211],a[196:199],v[20:23]\nglobal_load_dword v247,v253,s[12:13]\nM v[20:23],v[212:215],a[212:215],v[20:23]\nv_add_f32 v153,v153,v161\nv_add_f32 v154,v154,v162\nM v[80:83],v[208:211],a[200:203],v[80:83]\nv_add_f32 v155,v155,v163\n" \
"v_cvt_pk_bf16_f32 v152,0,v152\nM v[80:83],v[212:215],a[216:219],v[80:83]\nv_cvt_pk_bf16_f32 v153,0,v153\nv_cvt_pk_bf16_f32 v154,0,v154\nM v[84:87],v[208:211],a[204:207],v[84:87]\nds_read_b64_tr_b16 v[208:209],v241 offset:34304\nds_read_b64_tr_b16 v[210:211],v241 offset:34560\nM v[84:87],v[212:215],a[220:223],v[84:87]\n" \
"ds_read_b64_tr_b16 v[212:213],v241 offset:42496\nds_read_b64_tr_b16 v[214:215],v241 offset:42752\nM v[24:27],v[216:219],a[192:195],v[24:27]\nv_cvt_pk_bf16_f32 v155,0,v155\ns_waitcnt lgkmcnt(7)\nM v[24:27],v[220:223],a[208:211],v[24:27]\nv_add_f32 v152,s70,v152\nv_add_f32 v153,s70,v153\n" \
"M v[28:31],v[216:219],a[196:199],v[28:31]\nv_add_f32 v154,s70,v154\nv_add_f32 v155,s70,v155\nM v[28:31],v[220:223],a[212:215],v[28:31]\ns_lshr_b32 s71,s64,2\nv_cvt_pk_bf16_f32 v152,0,v152\nM v[88:91],v[216:219],a[200:203],v[88:91]\nv_cvt_pk_bf16_f32 v153,0,v153\n" \
"v_cvt_pk_bf16_f32 v154,0,v154\nM v[88:91],v[220:223],a[216:219],v[88:91]\nv_cvt_pk_bf16_f32 v155,0,v155\nv_add_u32 v171,s71,v240\nM v[92:95],v[216:219],a[204:207],v[92:95]\nds_write_b128 v171,v[152:155] offset:16384\nM v[92:95],v[220:223],a[220:223],v[92:95]\nds_read_b64_tr_b16 v[216:217],v242 offset:34304\n" \
"ds_read_b64_tr_b16 v[218:219],v242 offset:34560\nM v[32:35],v[192:195],a[192:195],v[32:35]\nds_read_b64_tr_b16 v[220:221],v242 offset:42496\nds_read_b64_tr_b16 v[222:223],v242 offset:42752\nM v[32:35],v[196:199],a[208:211],v[32:35]\nv_add_u32 v253,0x100000,v253\ns_lshl_b32 s70,s7,8\nM v[36:39],v[192:195],a[196:199],v[36:39]\n" \
"buffer_load_dwordx4 v243,s[40:43],s69 offen offset:0 lds\nM v[36:39],v[196:199],a[212:215],v[36:39]\nv_lshl_add_u32 v251,v251,5,s70\ns_lshl_b32 s71,s6,5\nM v[96:99],v[192:195],a[200:203],v[96:99]\ns_and_b32 s71,s71,0xfffffe00\ns_xor_b32 s70,s63,16\nM v[96:99],v[196:199],a[216:219],v[96:99]\n" \
"s_lshr_b32 s70,s70,2\ns_lshl3_add_u32 s70,s62,s70\nM v[100:103],v[192:195],a[204:207],v[100:103]\nbuffer_load_dwordx4 v244,s[40:43],s69 offen offset:1024 lds\nM v[100:103],v[196:199],a[220:223],v[100:103]\ns_lshl_b32 s70,s70,15\ns_waitcnt lgkmcnt(5)\nM v[40:43],v[200:203],a[192:195],v[40:43]\n" \
"s_add_u32 s70,s70,s64\nM v[40:43],v[204:207],a[208:211],v[40:43]\nM v[44:47],v[200:203],a[196:199],v[44:47]\nbuffer_load_dwordx4 v245,s[40:43],s69 offen offset:2048 lds\nM v[44:47],v[204:207],a[212:215],v[44:47]\nM v[104:107],v[200:203],a[200:203],v[104:107]\nM v[104:107],v[204:207],a[216:219],v[104:107]\nM v[108:111],v[200:203],a[204:207],v[108:111]\n" \
"buffer_load_dwordx4 v246,s[40:43],s69 offen offset:3072 lds\nM v[108:111],v[204:207],a[220:223],v[108:111]\ns_add_u32 m0,s64,114688\ns_add_u32 s69,s68,524288\nM v[48:51],v[208:211],a[192:195],v[48:51]\nM v[48:51],v[212:215],a[208:211],v[48:51]\nM v[52:55],v[208:211],a[196:199],v[52:55]\nbuffer_store_dwordx2 v[232:233],off,s[52:55],s72 sc1\n" \
"M v[52:55],v[212:215],a[212:215],v[52:55]\ns_and_b32 s72,s6,1\ns_lshl_b32 s72,s72,8\nM v[112:115],v[208:211],a[200:203],v[112:115]\ns_waitcnt vmcnt(7) lgkmcnt(0)\ns_barrier\nM v[112:115],v[212:215],a[216:219],v[112:115]\nds_read_b128 a[224:227],v228 offset:8192\n" \
"ds_read_b64_tr_b16 v[192:193],v241 offset:49152\nds_read_b64_tr_b16 v[194:195],v241 offset:49408\nM v[116:119],v[208:211],a[204:207],v[116:119]\nds_read_b128 a[240:243],v229 offset:8192\nds_read_b64_tr_b16 v[196:197],v241 offset:57344\nds_read_b64_tr_b16 v[198:199],v241 offset:57600\nM v[116:119],v[212:215],a[220:223],v[116:119]\nds_read_b128 a[228:231],v228 offset:10240\n" \
"s_add_u32 s71,s71,s72\nM v[56:59],v[216:219],a[192:195],v[56:59]\nds_read_b128 a[244:247],v229 offset:10240\ns_and_b32 s72,s6,14\nM v[56:59],v[220:223],a[208:211],v[56:59]\nds_read_b128 a[232:235],v228 offset:12288\ns_lshl1_add_u32 s71,s72,s71\nM v[60:63],v[216:219],a[196:199],v[60:63]\n" \
"ds_read_b128 a[248:251],v229 offset:12288\ns_or_b32 s71,s71,s75\nM v[60:63],v[220:223],a[212:215],v[60:63]\nds_read_b128 a[236:239],v228 offset:14336\ns_mov_b32 s72,0x3e0293ee\nM v[120:123],v[216:219],a[200:203],v[120:123]\nds_read_b128 a[252:255],v229 offset:14336\nv_and_b32 v228,15,v249\n" \
"M v[120:123],v[220:223],a[216:219],v[120:123]\nds_read_b64_tr_b16 v[200:201],v242 offset:49152\nds_read_b64_tr_b16 v[202:203],v242 offset:49408\nM v[124:127],v[216:219],a[204:207],v[124:127]\nglobal_load_dword v247,v253,s[12:13]\nds_read_b64_tr_b16 v[204:205],v242 offset:57344\nds_read_b64_tr_b16 v[206:207],v242 offset:57600\nM v[124:127],v[220:223],a[220:223],v[124:127]\n" \
"v_and_b32 v229,16,v249\ns_waitcnt lgkmcnt(13)\nM v[0:3],v[192:195],a[224:227],v[0:3]\nds_read_b64_tr_b16 v[208:209],v241 offset:49664\ns_waitcnt lgkmcnt(11)\nM v[0:3],v[196:199],a[240:243],v[0:3]\nds_read_b64_tr_b16 v[210:211],v241 offset:49920\ns_waitcnt lgkmcnt(11)\n" \
"M v[4:7],v[192:195],a[228:231],v[4:7]\nds_read_b64_tr_b16 v[212:213],v241 offset:57856\ns_waitcnt lgkmcnt(11)\nM v[4:7],v[196:199],a[244:247],v[4:7]\nds_read_b64_tr_b16 v[214:215],v241 offset:58112\ns_waitcnt lgkmcnt(11)\nM v[64:67],v[192:195],a[232:235],v[64:67]\nds_read_b64_tr_b16 v[216:217],v242 offset:49664\n" \
"s_waitcnt lgkmcnt(11)\nM v[64:67],v[196:199],a[248:251],v[64:67]\nds_read_b64_tr_b16 v[218:219],v242 offset:49920\ns_waitcnt lgkmcnt(11)\nM v[68:71],v[192:195],a[236:239],v[68:71]\nds_read_b64_tr_b16 v[220:221],v242 offset:57856\ns_waitcnt lgkmcnt(11)\nM v[68:71],v[196:199],a[252:255],v[68:71]\n" \
"ds_read_b64_tr_b16 v[222:223],v242 offset:58112\ns_waitcnt lgkmcnt(10)\nM v[8:11],v[200:203],a[224:227],v[8:11]\nds_read_b64_tr_b16 v[192:193],v241 offset:50176\ns_waitcnt lgkmcnt(9)\nM v[8:11],v[204:207],a[240:243],v[8:11]\nds_read_b64_tr_b16 v[194:195],v241 offset:50432\nds_read_b64_tr_b16 v[196:197],v241 offset:58368\n" \
"M v[12:15],v[200:203],a[228:231],v[12:15]\nds_read_b64_tr_b16 v[198:199],v241 offset:58624\nds_read_b128 v[176:179],v251 offset:19456\nM v[12:15],v[204:207],a[244:247],v[12:15]\nds_read_b128 v[160:163],v251 offset:17408\nv_cvt_pk_bf16_f32 v128,v0,v1\nM v[72:75],v[200:203],a[232:235],v[72:75]\nds_read_b128 v[172:175],v251 offset:18448\n" \
"v_cvt_pk_bf16_f32 v129,v2,v3\nM v[72:75],v[204:207],a[248:251],v[72:75]\nds_read_b128 v[152:155],v251 offset:16384\nv_cvt_pk_bf16_f32 v130,v8,v9\nM v[76:79],v[200:203],a[236:239],v[76:79]\nds_read_b64_tr_b16 v[200:201],v242 offset:50176\nds_read_b64_tr_b16 v[202:203],v242 offset:50432\nM v[76:79],v[204:207],a[252:255],v[76:79]\n" \
"ds_read_b64_tr_b16 v[204:205],v242 offset:58368\ns_waitcnt lgkmcnt(13)\nM v[16:19],v[208:211],a[224:227],v[16:19]\nds_read_b64_tr_b16 v[206:207],v242 offset:58624\nds_read_b128 v[156:159],v251 offset:16400\nM v[16:19],v[212:215],a[240:243],v[16:19]\nds_read_b128 v[180:183],v251 offset:19472\nv_cvt_pk_bf16_f32 v131,v10,v11\n" \
"M v[20:23],v[208:211],a[228:231],v[20:23]\nds_read_b128 v[164:167],v251 offset:17424\nv_lshlrev_b32 v228,2,v228\nM v[20:23],v[212:215],a[244:247],v[20:23]\nbuffer_load_dwordx4 v243,s[40:43],s69 offen offset:0 lds\nM v[80:83],v[208:211],a[232:235],v[80:83]\ns_waitcnt lgkmcnt(10)\nv_mul_f32 v144,v144,v176\n" \
"M v[80:83],v[212:215],a[248:251],v[80:83]\nv_mul_f32 v145,v145,v177\nv_cvt_pk_bf16_f32 v144,0,v144\nM v[84:87],v[208:211],a[236:239],v[84:87]\nds_read_b64_tr_b16 v[208:209],v241 offset:50688\nds_read_b64_tr_b16 v[210:211],v241 offset:50944\nM v[84:87],v[212:215],a[252:255],v[84:87]\nds_read_b64_tr_b16 v[212:213],v241 offset:58880\n" \
"ds_read_b64_tr_b16 v[214:215],v241 offset:59136\nM v[24:27],v[216:219],a[224:227],v[24:27]\ns_waitcnt vmcnt(7)\nv_add_f32_dpp v168,v168,v168 quad_perm:[1,0,3,2] row_mask:0xf bank_mask:0xf\nM v[24:27],v[220:223],a[240:243],v[24:27]\nv_cvt_pk_bf16_f32 v145,0,v145\nv_add_f32_dpp v168,v168,v168 quad_perm:[2,3,0,1] row_mask:0xf bank_mask:0xf\nM v[28:31],v[216:219],a[228:231],v[28:31]\n" \
"v_cvt_pk_bf16_f32 v168,0,v168\ns_waitcnt lgkmcnt(7)\nv_add_f32 v144,v144,v160\nM v[28:31],v[220:223],a[244:247],v[28:31]\nbuffer_store_dword v168,v170,s[52:55],s71 offen sc1\nM v[88:91],v[216:219],a[232:235],v[88:91]\nds_read_b128 v[168:171],v251 offset:18432\nv_add_f32 v145,v145,v161\n" \
"M v[88:91],v[220:223],a[248:251],v[88:91]\nv_cvt_pk_bf16_f32 v144,0,v144\nv_cvt_pk_bf16_f32 v145,0,v145\nM v[92:95],v[216:219],a[236:239],v[92:95]\nds_read_b64_tr_b16 v[216:217],v242 offset:50688\nds_read_b64_tr_b16 v[218:219],v242 offset:50944\nM v[92:95],v[220:223],a[252:255],v[92:95]\nds_read_b64_tr_b16 v[220:221],v242 offset:58880\n" \
"ds_read_b64_tr_b16 v[222:223],v242 offset:59136\nM v[32:35],v[192:195],a[224:227],v[32:35]\nv_mul_f32 v144,s72,v144\nv_mul_f32 v145,s72,v145\nM v[32:35],v[196:199],a[240:243],v[32:35]\nv_cvt_pk_bf16_f32 v236,v144,v145\nv_cvt_pk_bf16_f32 v144,v4,v5\nM v[36:39],v[192:195],a[228:231],v[36:39]\n" \
"v_cvt_pk_bf16_f32 v145,v6,v7\nv_cvt_pk_bf16_f32 v160,v64,v65\nM v[36:39],v[196:199],a[244:247],v[36:39]\nv_cvt_pk_bf16_f32 v161,v66,v67\ns_and_b32 s71,s63,2\nM v[96:99],v[192:195],a[232:235],v[96:99]\nv_mul_f32 v140,v140,v172\nv_mul_f32 v141,v141,v173\n" \
"M v[96:99],v[196:199],a[248:251],v[96:99]\nv_mul_f32 v142,v142,v174\nv_mul_f32 v143,v143,v175\nM v[100:103],v[192:195],a[236:239],v[100:103]\ns_waitcnt lgkmcnt(4)\nv_mul_f32 v136,v136,v168\nM v[100:103],v[196:199],a[252:255],v[100:103]\nv_mul_f32 v137,v137,v169\n" \
"v_mul_f32 v138,v138,v170\nM v[40:43],v[200:203],a[224:227],v[40:43]\nv_mul_f32 v139,v139,v171\ns_lshl_b32 s71,s71,9\nM v[40:43],v[204:207],a[240:243],v[40:43]\nv_cvt_pk_bf16_f32 v176,v68,v69\nv_cvt_pk_bf16_f32 v177,v70,v71\nM v[44:47],v[200:203],a[228:231],v[44:47]\n" \
"v_cvt_pk_bf16_f32 v136,0,v136\nv_cvt_pk_bf16_f32 v137,0,v137\nM v[44:47],v[204:207],a[244:247],v[44:47]\nv_cvt_pk_bf16_f32 v138,0,v138\nv_cvt_pk_bf16_f32 v139,0,v139\nM v[104:107],v[200:203],a[232:235],v[104:107]\nv_cvt_pk_bf16_f32 v140,0,v140\nv_cvt_pk_bf16_f32 v141,0,v141\n" \
"M v[104:107],v[204:207],a[248:251],v[104:107]\nv_cvt_pk_bf16_f32 v142,0,v142\nv_cvt_pk_bf16_f32 v143,0,v143\nM v[108:111],v[200:203],a[236:239],v[108:111]\nv_lshrrev_b32 v251,5,v249\ns_add_u32 s70,s70,s71\nM v[108:111],v[204:207],a[252:255],v[108:111]\nv_add_f32 v136,v136,v152\n" \
"v_add_f32 v137,v137,v153\nM v[48:51],v[208:211],a[224:227],v[48:51]\nv_add_f32 v138,v138,v154\nv_add_f32 v139,v139,v155\nM v[48:51],v[212:215],a[240:243],v[48:51]\nv_add_f32 v140,v140,v156\nv_add_f32 v141,v141,v157\nM v[52:55],v[208:211],a[228:231],v[52:55]\n" \
"v_add_f32 v142,v142,v158\nv_add_f32 v143,v143,v159\nM v[52:55],v[212:215],a[244:247],v[52:55]\nv_mul_f32 v146,v146,v178\nv_mul_f32 v147,v147,v179\nM v[112:115],v[208:211],a[232:235],v[112:115]\ns_waitcnt vmcnt(4) lgkmcnt(0)\ns_barrier\n" \
"M v[112:115],v[212:215],a[248:251],v[112:115]\nds_read_b64_tr_b16 v[192:193],v241 offset:0\nds_read_b64_tr_b16 v[194:195],v241 offset:256\nM v[116:119],v[208:211],a[236:239],v[116:119]\nds_read_b64_tr_b16 v[196:197],v241 offset:8192\nds_read_b64_tr_b16 v[198:199],v241 offset:8448\nM v[116:119],v[212:215],a[252:255],v[116:119]\nds_read_b64_tr_b16 v[200:201],v242 offset:0\n" \
"ds_read_b64_tr_b16 v[202:203],v242 offset:256\nM v[56:59],v[216:219],a[224:227],v[56:59]\nds_read_b64_tr_b16 v[204:205],v242 offset:8192\nds_read_b64_tr_b16 v[206:207],v242 offset:8448\nM v[56:59],v[220:223],a[240:243],v[56:59]\nds_read_b64_tr_b16 v[208:209],v241 offset:512\nds_read_b64_tr_b16 v[210:211],v241 offset:768\nM v[60:63],v[216:219],a[228:231],v[60:63]\n" \
"v_lshlrev_b32 v251,11,v251\ns_and_b32 s71,s63,1\nM v[60:63],v[220:223],a[244:247],v[60:63]\nds_read_b64_tr_b16 v[212:213],v241 offset:8704\nds_read_b64_tr_b16 v[214:215],v241 offset:8960\nM v[120:123],v[216:219],a[232:235],v[120:123]\nv_cvt_pk_bf16_f32 v136,0,v136\nv_cvt_pk_bf16_f32 v137,0,v137\n" \
"M v[120:123],v[220:223],a[248:251],v[120:123]\nv_cvt_pk_bf16_f32 v138,0,v138\nv_cvt_pk_bf16_f32 v139,0,v139\nM v[124:127],v[216:219],a[236:239],v[124:127]\nv_cvt_pk_bf16_f32 v140,0,v140\nv_cvt_pk_bf16_f32 v141,0,v141\nM v[124:127],v[220:223],a[252:255],v[124:127]\nv_cvt_pk_bf16_f32 v142,0,v142\n" \
"s_waitcnt lgkmcnt(10)\nM v[0:3],a[0:3],v[192:195],0\nv_cvt_pk_bf16_f32 v143,0,v143\ns_waitcnt lgkmcnt(8)\nM v[0:3],a[16:19],v[196:199],v[0:3]\nv_cvt_pk_bf16_f32 v146,0,v146\nv_cvt_pk_bf16_f32 v147,0,v147\nM v[4:7],a[4:7],v[192:195],0\n" \
"v_lshl_or_b32 v251,v250,8,v251\ns_lshl_b32 s71,s71,7\nM v[4:7],a[20:23],v[196:199],v[4:7]\nv_mul_f32 v136,s72,v136\nv_mul_f32 v137,s72,v137\nM v[64:67],a[8:11],v[192:195],0\nv_mul_f32 v138,s72,v138\nv_mul_f32 v139,s72,v139\n" \
"M v[64:67],a[24:27],v[196:199],v[64:67]\nv_mul_f32 v140,s72,v140\nv_mul_f32 v141,s72,v141\nM v[68:71],a[12:15],v[192:195],0\nv_mul_f32 v142,s72,v142\nv_mul_f32 v143,s72,v143\nM v[68:71],a[28:31],v[196:199],v[68:71]\nv_add_f32 v146,v146,v162\n" \
"v_add_f32 v147,v147,v163\nv_and_b32 v250,0x70,v240\ns_add_u32 s70,s70,s71\ns_waitcnt lgkmcnt(0)\nM v[8:11],a[0:3],v[200:203],0\nv_cvt_pk_bf16_f32 v232,v136,v137\nv_cvt_pk_bf16_f32 v233,v138,v139\nv_cvt_pk_bf16_f32 v234,v140,v141\n" \
"v_cvt_pk_bf16_f32 v235,v142,v143\nv_cvt_pk_bf16_f32 v146,0,v146\nv_cvt_pk_bf16_f32 v147,0,v147\nv_or_b32 v251,v251,v250\ns_add_u32 s70,s70,1048576\nv_mul_f32 v146,s72,v146\nv_mul_f32 v147,s72,v147\nbuffer_store_dwordx4 v[232:235],v251,s[52:55],s70 offen sc1\n" \
"M v[8:11],a[16:19],v[204:207],v[8:11]\nv_cvt_pk_bf16_f32 v237,v146,v147\nv_mul_f32 v148,v148,v180\nv_mul_f32 v149,v149,v181\nv_mul_f32 v150,v150,v182\nv_mul_f32 v151,v151,v183\nv_cvt_pk_bf16_f32 v146,v12,v13\nv_cvt_pk_bf16_f32 v147,v14,v15\n" \
"M v[12:15],a[4:7],v[200:203],0\nv_cvt_pk_bf16_f32 v148,0,v148\nv_cvt_pk_bf16_f32 v149,0,v149\nv_cvt_pk_bf16_f32 v150,0,v150\nv_cvt_pk_bf16_f32 v151,0,v151\nv_add_f32 v148,v148,v164\nv_add_f32 v149,v149,v165\nv_add_f32 v150,v150,v166\n" \
"v_add_f32 v151,v151,v167\nM v[12:15],a[20:23],v[204:207],v[12:15]\nv_cvt_pk_bf16_f32 v148,0,v148\nv_cvt_pk_bf16_f32 v149,0,v149\nv_cvt_pk_bf16_f32 v150,0,v150\nv_cvt_pk_bf16_f32 v151,0,v151\nv_mul_f32 v148,s72,v148\nv_mul_f32 v149,s72,v149\n" \
"v_mul_f32 v150,s72,v150\nv_mul_f32 v151,s72,v151\nv_cvt_pk_bf16_f32 v162,v72,v73\nv_cvt_pk_bf16_f32 v163,v74,v75\nM v[72:75],a[8:11],v[200:203],0\nv_cvt_pk_bf16_f32 v238,v148,v149\nv_cvt_pk_bf16_f32 v239,v150,v151\ns_add_u32 s71,s70,16384\n" \
"buffer_load_dwordx4 v244,s[40:43],s69 offen offset:1024 lds\nbuffer_load_dwordx4 v245,s[40:43],s69 offen offset:2048 lds\nbuffer_load_dwordx4 v246,s[40:43],s69 offen offset:3072 lds\nbuffer_store_dwordx4 v[236:239],v251,s[52:55],s71 offen sc1\nM v[72:75],a[24:27],v[204:207],v[72:75]\nv_cvt_pk_bf16_f32 v178,v76,v77\nv_cvt_pk_bf16_f32 v179,v78,v79\nds_read_b64_tr_b16 v[216:217],v242 offset:512\n" \
"ds_read_b64_tr_b16 v[218:219],v242 offset:768\nM v[76:79],a[12:15],v[200:203],0\nds_read_b64_tr_b16 v[220:221],v242 offset:8704\nds_read_b64_tr_b16 v[222:223],v242 offset:8960\nM v[76:79],a[28:31],v[204:207],v[76:79]\nv_cvt_pk_bf16_f32 v132,v16,v17\nv_cvt_pk_bf16_f32 v133,v18,v19\nM v[16:19],a[0:3],v[208:211],0\n" \
"s_lshl_b32 s70,s62,5\ns_add_u32 s70,s70,s63\nM v[16:19],a[16:19],v[212:215],v[16:19]\ns_lshl2_add_u32 s70,s70,s7\ns_lshl2_add_u32 s79,s70,90112\nv_cvt_pk_bf16_f32 v148,v20,v21\nv_cvt_pk_bf16_f32 v149,v22,v23\nM v[20:23],a[4:7],v[208:211],0\n" \
"s_lshl_b32 s70,s60,4\ns_sub_u32 s70,s70,s62\nM v[20:23],a[20:23],v[212:215],v[20:23]\ns_add_u32 s70,s70,15\nv_cvt_pk_bf16_f32 v164,v80,v81\nv_cvt_pk_bf16_f32 v165,v82,v83\ns_lshl2_add_u32 s70,s70,s7\nM v[80:83],a[8:11],v[208:211],0\n" \
"s_lshl_b32 s70,s70,15\ns_lshl_b32 s71,s61,8\ns_add_u32 s70,s70,s71\nM v[80:83],a[24:27],v[212:215],v[80:83]\ns_add_u32 s78,s70,54525952\nv_cvt_pk_bf16_f32 v180,v84,v85\nv_cvt_pk_bf16_f32 v181,v86,v87\ns_lshl3_add_u32 s70,s62,s7\n" \
"ds_read_b64_tr_b16 v[192:193],v241 offset:1024\nds_read_b64_tr_b16 v[194:195],v241 offset:1280\nM v[84:87],a[12:15],v[208:211],0\ns_lshl_b32 s80,s70,6\ns_lshl_b32 s76,s70,15\nds_read_b64_tr_b16 v[196:197],v241 offset:9216\nds_read_b64_tr_b16 v[198:199],v241 offset:9472\ns_lshl_b32 s70,s63,5\n" \
"M v[84:87],a[28:31],v[212:215],v[84:87]\ns_add_u32 s70,s70,s74\nv_cvt_pk_bf16_f32 v134,v24,v25\nv_cvt_pk_bf16_f32 v135,v26,v27\ns_lshl_b32 s74,s70,14\ns_waitcnt lgkmcnt(4)\nM v[24:27],a[0:3],v[216:219],0\ns_add_u32 s74,s74,3145728\n" \
"buffer_store_dwordx4 v[128:131],v240,s[52:55],s74 offen offset:0 sc1\nM v[24:27],a[16:19],v[220:223],v[24:27]\nv_cvt_pk_bf16_f32 v150,v28,v29\nv_cvt_pk_bf16_f32 v151,v30,v31\nM v[28:31],a[4:7],v[216:219],0\nv_cvt_pk_bf16_f32 v166,v88,v89\nv_cvt_pk_bf16_f32 v167,v90,v91\nM v[28:31],a[20:23],v[220:223],v[28:31]\n" \
"v_cvt_pk_bf16_f32 v182,v92,v93\nv_cvt_pk_bf16_f32 v183,v94,v95\nM v[88:91],a[8:11],v[216:219],0\nds_read_b64_tr_b16 v[200:201],v242 offset:1024\nds_read_b64_tr_b16 v[202:203],v242 offset:1280\nM v[88:91],a[24:27],v[220:223],v[88:91]\nds_read_b64_tr_b16 v[204:205],v242 offset:9216\nds_read_b64_tr_b16 v[206:207],v242 offset:9472\n" \
"M v[92:95],a[12:15],v[216:219],0\nv_cvt_pk_bf16_f32 v136,v32,v33\nv_cvt_pk_bf16_f32 v137,v34,v35\nM v[92:95],a[28:31],v[220:223],v[92:95]\nv_cvt_pk_bf16_f32 v152,v36,v37\ns_waitcnt lgkmcnt(4)\nM v[32:35],a[0:3],v[192:195],0\nv_cvt_pk_bf16_f32 v153,v38,v39\n" \
"v_cvt_pk_bf16_f32 v168,v96,v97\nM v[32:35],a[16:19],v[196:199],v[32:35]\nv_cvt_pk_bf16_f32 v169,v98,v99\ns_add_u32 m0,s64,131072\nM v[36:39],a[4:7],v[192:195],0\ns_add_u32 s69,s68,1048576\nv_cvt_pk_bf16_f32 v184,v100,v101\nM v[36:39],a[20:23],v[196:199],v[36:39]\n" \
"v_cvt_pk_bf16_f32 v185,v102,v103\nds_read_b64_tr_b16 v[208:209],v241 offset:1536\nM v[96:99],a[8:11],v[192:195],0\nds_read_b64_tr_b16 v[210:211],v241 offset:1792\nds_read_b64_tr_b16 v[212:213],v241 offset:9728\nM v[96:99],a[24:27],v[196:199],v[96:99]\nds_read_b64_tr_b16 v[214:215],v241 offset:9984\nv_cvt_pk_bf16_f32 v138,v40,v41\n" \
"M v[100:103],a[12:15],v[192:195],0\nv_cvt_pk_bf16_f32 v139,v42,v43\nv_cvt_pk_bf16_f32 v154,v44,v45\nM v[100:103],a[28:31],v[196:199],v[100:103]\nv_cvt_pk_bf16_f32 v155,v46,v47\ns_waitcnt lgkmcnt(5)\nM v[40:43],a[0:3],v[200:203],0\nds_read_b64_tr_b16 v[216:217],v242 offset:1536\n" \
"s_waitcnt lgkmcnt(5)\nM v[40:43],a[16:19],v[204:207],v[40:43]\nds_read_b64_tr_b16 v[218:219],v242 offset:1792\nds_read_b64_tr_b16 v[220:221],v242 offset:9728\nM v[44:47],a[4:7],v[200:203],0\nds_read_b64_tr_b16 v[222:223],v242 offset:9984\nbuffer_store_dwordx4 v[132:135],v240,s[52:55],s74 offen offset:1024 sc1\nM v[44:47],a[20:23],v[204:207],v[44:47]\n" \
"v_cvt_pk_bf16_f32 v170,v104,v105\nv_cvt_pk_bf16_f32 v171,v106,v107\nM v[104:107],a[8:11],v[200:203],0\nv_cvt_pk_bf16_f32 v186,v108,v109\nv_cvt_pk_bf16_f32 v187,v110,v111\nM v[104:107],a[24:27],v[204:207],v[104:107]\nv_cvt_pk_bf16_f32 v140,v48,v49\nv_cvt_pk_bf16_f32 v141,v50,v51\n" \
"M v[108:111],a[12:15],v[200:203],0\nv_cvt_pk_bf16_f32 v156,v52,v53\nv_cvt_pk_bf16_f32 v157,v54,v55\nM v[108:111],a[28:31],v[204:207],v[108:111]\nv_cvt_pk_bf16_f32 v172,v112,v113\ns_waitcnt lgkmcnt(4)\nM v[48:51],a[0:3],v[208:211],0\nv_cvt_pk_bf16_f32 v173,v114,v115\n" \
"v_cvt_pk_bf16_f32 v188,v116,v117\nM v[48:51],a[16:19],v[212:215],v[48:51]\nv_cvt_pk_bf16_f32 v189,v118,v119\nv_cvt_pk_bf16_f32 v142,v56,v57\nM v[52:55],a[4:7],v[208:211],0\nv_cvt_pk_bf16_f32 v143,v58,v59\nv_cvt_pk_bf16_f32 v158,v60,v61\nM v[52:55],a[20:23],v[212:215],v[52:55]\n" \
"v_cvt_pk_bf16_f32 v159,v62,v63\nv_cvt_pk_bf16_f32 v174,v120,v121\nM v[112:115],a[8:11],v[208:211],0\ns_waitcnt vmcnt(3) lgkmcnt(0)\ns_barrier\nM v[112:115],a[24:27],v[212:215],v[112:115]\nds_read_b64_tr_b16 v[192:193],v241 offset:16384\nds_read_b64_tr_b16 v[194:195],v241 offset:16640\n" \
"M v[116:119],a[12:15],v[208:211],0\nds_read_b64_tr_b16 v[196:197],v241 offset:24576\nds_read_b64_tr_b16 v[198:199],v241 offset:24832\nM v[116:119],a[28:31],v[212:215],v[116:119]\nv_cvt_pk_bf16_f32 v175,v122,v123\nv_cvt_pk_bf16_f32 v190,v124,v125\nM v[56:59],a[0:3],v[216:219],0\nbuffer_store_dwordx4 v[136:139],v240,s[52:55],s74 offen offset:2048 sc1\n" \
"M v[56:59],a[16:19],v[220:223],v[56:59]\nv_cvt_pk_bf16_f32 v191,v126,v127\nds_read_b64_tr_b16 v[200:201],v242 offset:16384\nM v[60:63],a[4:7],v[216:219],0\nds_read_b64_tr_b16 v[202:203],v242 offset:16640\nds_read_b64_tr_b16 v[204:205],v242 offset:24576\nM v[60:63],a[20:23],v[220:223],v[60:63]\nds_read_b64_tr_b16 v[206:207],v242 offset:24832\n" \
"ds_read_b64_tr_b16 v[208:209],v241 offset:16896\nM v[120:123],a[8:11],v[216:219],0\nds_read_b64_tr_b16 v[210:211],v241 offset:17152\nds_read_b64_tr_b16 v[212:213],v241 offset:25088\nM v[120:123],a[24:27],v[220:223],v[120:123]\nds_read_b64_tr_b16 v[214:215],v241 offset:25344\nv_lshl_or_b32 v228,v229,4,v228\nM v[124:127],a[12:15],v[216:219],0\n" \
"buffer_load_dwordx4 v243,s[40:43],s69 offen offset:0 lds\nM v[124:127],a[28:31],v[220:223],v[124:127]\nds_read_b64_tr_b16 v[216:217],v242 offset:16896\ns_waitcnt lgkmcnt(11)\nM v[0:3],a[32:35],v[192:195],v[0:3]\nds_read_b64_tr_b16 v[218:219],v242 offset:17152\ns_waitcnt lgkmcnt(10)\nM v[0:3],a[48:51],v[196:199],v[0:3]\n" \
"ds_read_b64_tr_b16 v[220:221],v242 offset:25088\nds_read_b64_tr_b16 v[222:223],v242 offset:25344\nM v[4:7],a[36:39],v[192:195],v[4:7]\nbuffer_load_dwordx4 v244,s[40:43],s69 offen offset:1024 lds\nM v[4:7],a[52:55],v[196:199],v[4:7]\ns_add_u32 s80,s80,90112\ns_add_u32 s76,s76,1048576\nM v[64:67],a[40:43],v[192:195],v[64:67]\n" \
"s_add_u32 s77,s76,131072\ns_lshl2_add_u32 s29,s70,81920\nM v[64:67],a[56:59],v[196:199],v[64:67]\ns_lshl_b32 s70,s7,14\nv_add_u32 v250,s70,v240\nM v[68:71],a[44:47],v[192:195],v[68:71]\nbuffer_load_dwordx4 v245,s[40:43],s69 offen offset:2048 lds\nM v[68:71],a[60:63],v[196:199],v[68:71]\n" \
"ds_read_b64_tr_b16 v[192:193],v241 offset:17408\ns_waitcnt lgkmcnt(5)\nM v[8:11],a[32:35],v[200:203],v[8:11]\nds_read_b64_tr_b16 v[194:195],v241 offset:17664\nds_read_b64_tr_b16 v[196:197],v241 offset:25600\nM v[8:11],a[48:51],v[204:207],v[8:11]\nds_read_b64_tr_b16 v[198:199],v241 offset:25856\ns_lshl_b32 s70,s63,19\n" \
"M v[12:15],a[36:39],v[200:203],v[12:15]\nbuffer_load_dwordx4 v246,s[40:43],s69 offen offset:3072 lds\nM v[12:15],a[52:55],v[204:207],v[12:15]\ns_add_u32 m0,s64,147456\ns_add_u32 s69,s68,1572864\nM v[72:75],a[40:43],v[200:203],v[72:75]\nds_write_b64 v250,v[128:129] offset:0\ns_add_u32 s72,s78,16384\n" \
"M v[72:75],a[56:59],v[204:207],v[72:75]\nds_write_b64 v250,v[130:131] offset:8\ns_mul_i32 s71,s60,14\nM v[76:79],a[44:47],v[200:203],v[76:79]\nbuffer_store_dwordx4 v[140:143],v240,s[52:55],s74 offen offset:3072 sc1\nM v[76:79],a[60:63],v[204:207],v[76:79]\nds_read_b64_tr_b16 v[200:201],v242 offset:17408\nds_read_b64_tr_b16 v[202:203],v242 offset:17664\n" \
"M v[16:19],a[32:35],v[208:211],v[16:19]\nds_read_b64_tr_b16 v[204:205],v242 offset:25600\nds_read_b64_tr_b16 v[206:207],v242 offset:25856\nM v[16:19],a[48:51],v[212:215],v[16:19]\ns_add_u32 s74,s74,4096\nv_and_b32 v229,15,v249\nM v[20:23],a[36:39],v[208:211],v[20:23]\nbuffer_load_dwordx4 v243,s[40:43],s69 offen offset:0 lds\n" \
"M v[20:23],a[52:55],v[212:215],v[20:23]\ns_add_u32 s44,s34,s70\ns_addc_u32 s45,s35,0\nM v[80:83],a[40:43],v[208:211],v[80:83]\ns_lshl_b32 s70,s62,15\ns_add_u32 s96,s70,s64\nM v[80:83],a[56:59],v[212:215],v[80:83]\ns_sub_u32 s96,s96,16384\n" \
"s_sub_u32 s46,491520,s70\nM v[84:87],a[44:47],v[208:211],v[84:87]\nds_read_b64_tr_b16 v[208:209],v241 offset:17920\nds_read_b64_tr_b16 v[210:211],v241 offset:18176\nM v[84:87],a[60:63],v[212:215],v[84:87]\nds_read_b64_tr_b16 v[212:213],v241 offset:26112\ns_waitcnt lgkmcnt(7)\nM v[24:27],a[32:35],v[216:219],v[24:27]\n" \
"ds_read_b64_tr_b16 v[214:215],v241 offset:26368\ns_lshl2_add_u32 s70,s62,0\nM v[24:27],a[48:51],v[220:223],v[24:27]\nbuffer_load_dwordx4 v244,s[40:43],s69 offen offset:1024 lds\nM v[28:31],a[36:39],v[216:219],v[28:31]\nds_write_b64 v250,v[132:133] offset:1024\ns_sub_u32 s70,s70,s7\nM v[28:31],a[52:55],v[220:223],v[28:31]\n" \
"s_cmp_eq_u32 s36,s96\ns_cselect_b32 s72,0xc000,s98\ns_add_u32 s65,s36,s72\nM v[88:91],a[40:43],v[216:219],v[88:91]\ns_cmp_eq_u32 s65,s96\ns_cselect_b32 s72,0xc000,s98\ns_add_u32 s66,s65,s72\nM v[88:91],a[56:59],v[220:223],v[88:91]\n" \
"buffer_store_dwordx4 v[144:147],v240,s[52:55],s74 offen offset:0 sc1\nM v[92:95],a[44:47],v[216:219],v[92:95]\nds_read_b64_tr_b16 v[216:217],v242 offset:17920\nds_read_b64_tr_b16 v[218:219],v242 offset:18176\nM v[92:95],a[60:63],v[220:223],v[92:95]\nds_read_b64_tr_b16 v[220:221],v242 offset:26112\nds_read_b64_tr_b16 v[222:223],v242 offset:26368\nM v[32:35],a[32:35],v[192:195],v[32:35]\n" \
"s_lshl_b32 s70,s70,16\ns_add_u32 s33,s78,s70\nM v[32:35],a[48:51],v[196:199],v[32:35]\nbuffer_load_dwordx4 v245,s[40:43],s69 offen offset:2048 lds\nM v[36:39],a[36:39],v[192:195],v[36:39]\nds_write_b64 v250,v[134:135] offset:1032\ns_lshl3_add_u32 s70,s62,s7\nM v[36:39],a[52:55],v[196:199],v[36:39]\n" \
"s_cmp_eq_u32 s66,s96\ns_cselect_b32 s72,0xc000,s98\ns_add_u32 s95,s66,s72\nM v[96:99],a[40:43],v[192:195],v[96:99]\ns_cmp_eq_u32 s62,7\ns_cselect_b32 s46,229376,s46\nM v[96:99],a[56:59],v[196:199],v[96:99]\nbuffer_load_dwordx4 v246,s[40:43],s69 offen offset:3072 lds\n" \
"M v[100:103],a[44:47],v[192:195],v[100:103]\ns_add_u32 m0,s64,98304\ns_add_u32 s69,s68,2097152\nM v[100:103],a[60:63],v[196:199],v[100:103]\ns_lshl3_add_u32 s70,s70,s60\ns_waitcnt lgkmcnt(5)\nM v[40:43],a[32:35],v[200:203],v[40:43]\ns_cmp_gt_i32 s62,-1\n" \
"s_cselect_b32 s72,s98,0xfffe4000\nM v[40:43],a[48:51],v[204:207],v[40:43]\nbuffer_load_dwordx4 v243,s[40:43],s69 offen offset:0 lds\nM v[44:47],a[36:39],v[200:203],v[44:47]\ns_lshl_b32 s67,s70,15\ns_add_u32 s15,s64,s46\nM v[44:47],a[52:55],v[204:207],v[44:47]\ns_lshl3_add_u32 s70,s62,s60\n" \
"s_add_u32 s15,s15,s72\nM v[104:107],a[40:43],v[200:203],v[104:107]\ns_lshl2_add_u32 s70,s70,s7\ns_add_u32 s97,s96,65536\nM v[104:107],a[56:59],v[204:207],v[104:107]\nbuffer_load_dwordx4 v244,s[40:43],s69 offen offset:1024 lds\nM v[108:111],a[44:47],v[200:203],v[108:111]\nds_write_b64 v250,v[136:137] offset:2048\n" \
"v_lshlrev_b32 v229,7,v229\nM v[108:111],a[60:63],v[204:207],v[108:111]\ns_cmp_eq_u32 s62,7\ns_cselect_b32 s72,98304,0\nM v[48:51],a[32:35],v[208:211],v[48:51]\ns_sub_u32 s97,s97,s72\ns_lshl_b32 s72,s60,1\nM v[48:51],a[48:51],v[212:215],v[48:51]\n" \
"buffer_store_dwordx4 v[148:151],v240,s[52:55],s74 offen offset:1024 sc1\nM v[52:55],a[36:39],v[208:211],v[52:55]\ns_add_u32 s72,s72,s62\ns_sub_u32 s72,s72,6\nM v[52:55],a[52:55],v[212:215],v[52:55]\ns_lshl_b32 s72,s72,18\nv_add_u32 v253,0xffd00000,v253\nM v[112:115],a[40:43],v[208:211],v[112:115]\n" \
"s_waitcnt vmcnt(9) lgkmcnt(1)\ns_barrier\nM v[112:115],a[56:59],v[212:215],v[112:115]\nds_read_b64_tr_b16 v[192:193],v241 offset:32768\nds_read_b64_tr_b16 v[194:195],v241 offset:33024\nM v[116:119],a[44:47],v[208:211],v[116:119]\nbuffer_store_dword v252,off,s[52:55],s79 sc1\nds_read_b64_tr_b16 v[196:197],v241 offset:40960\n" \
"ds_read_b64_tr_b16 v[198:199],v241 offset:41216\nM v[116:119],a[60:63],v[212:215],v[116:119]\nds_read_b64_tr_b16 v[200:201],v242 offset:32768\nds_read_b64_tr_b16 v[202:203],v242 offset:33024\nM v[56:59],a[32:35],v[216:219],v[56:59]\nds_read_b64_tr_b16 v[204:205],v242 offset:40960\nds_read_b64_tr_b16 v[206:207],v242 offset:41216\nM v[56:59],a[48:51],v[220:223],v[56:59]\n" \
"ds_read_b64_tr_b16 v[208:209],v241 offset:33280\nds_read_b64_tr_b16 v[210:211],v241 offset:33536\nM v[60:63],a[36:39],v[216:219],v[60:63]\nds_read_b64_tr_b16 v[212:213],v241 offset:41472\nds_read_b64_tr_b16 v[214:215],v241 offset:41728\nM v[60:63],a[52:55],v[220:223],v[60:63]\nbuffer_load_dwordx4 v245,s[40:43],s69 offen offset:2048 lds\nM v[120:123],a[40:43],v[216:219],v[120:123]\n" \
"ds_write_b64 v250,v[138:139] offset:2056\ns_lshl4_add_u32 s79,s70,65536\nM v[120:123],a[56:59],v[220:223],v[120:123]\ns_lshr_b32 s70,s6,3\ns_sub_u32 s70,s70,16\nM v[124:127],a[44:47],v[216:219],v[124:127]\nds_read_b64_tr_b16 v[216:217],v242 offset:33280\nds_read_b64_tr_b16 v[218:219],v242 offset:33536\n" \
"M v[124:127],a[60:63],v[220:223],v[124:127]\nds_read_b64_tr_b16 v[220:221],v242 offset:41472\ns_waitcnt lgkmcnt(14)\nM v[0:3],a[64:67],v[192:195],v[0:3]\nds_read_b64_tr_b16 v[222:223],v242 offset:41728\ns_waitcnt lgkmcnt(13)\nM v[0:3],a[80:83],v[196:199],v[0:3]\nbuffer_store_dwordx4 v[152:155],v240,s[52:55],s74 offen offset:2048 sc1\n" \
"M v[4:7],a[68:71],v[192:195],v[4:7]\ns_add_u32 s2,s70,s71\ns_mul_i32 s71,s60,448\nM v[4:7],a[84:87],v[196:199],v[4:7]\ns_and_b32 s71,s71,0xffffff80\ns_sub_u32 s58,1835008,s71\nM v[64:67],a[72:75],v[192:195],v[64:67]\ns_cmp_ge_u32 s62,6\n" \
"s_cselect_b64 s[56:57],s[18:19],s[56:57]\ns_cselect_b32 s71,s72,s71\ns_cselect_b32 s58,0x40000,s58\ns_cselect_b32 s0,2048,s0\nM v[64:67],a[88:91],v[196:199],v[64:67]\nbuffer_load_dwordx4 v246,s[40:43],s69 offen offset:3072 lds\nM v[68:71],a[76:79],v[192:195],v[68:71]\nds_read_b64_tr_b16 v[192:193],v241 offset:33792\n" \
"ds_read_b64_tr_b16 v[194:195],v241 offset:34048\nM v[68:71],a[92:95],v[196:199],v[68:71]\nds_read_b64_tr_b16 v[196:197],v241 offset:41984\ns_waitcnt lgkmcnt(7)\nM v[8:11],a[64:67],v[200:203],v[8:11]\nds_read_b64_tr_b16 v[198:199],v241 offset:42240\ns_add_u32 m0,s64,114688\nM v[8:11],a[80:83],v[204:207],v[8:11]\n" \
"s_add_u32 s69,s68,2621440\ns_mul_i32 s72,s60,229376\nM v[12:15],a[68:71],v[200:203],v[12:15]\nbuffer_load_dwordx4 v243,s[40:43],s69 offen offset:0 lds\nM v[12:15],a[84:87],v[204:207],v[12:15]\nds_write_b64 v250,v[140:141] offset:3072\ns_lshl_b32 s2,s2,5\nM v[72:75],a[72:75],v[200:203],v[72:75]\n" \
"s_cmp_eq_u32 s62,3\ns_cselect_b64 s[56:57],s[26:27],s[56:57]\ns_cselect_b32 s71,s72,s71\ns_cselect_b32 s58,229376,s58\ns_cselect_b32 s0,4096,s0\nM v[72:75],a[88:91],v[204:207],v[72:75]\ns_cmp_lt_u32 s70,14\ns_cselect_b32 s2,s2,0\n" \
"s_cselect_b32 s3,-1,0\nM v[76:79],a[76:79],v[200:203],v[76:79]\nds_read_b64_tr_b16 v[200:201],v242 offset:33792\nds_read_b64_tr_b16 v[202:203],v242 offset:34048\nM v[76:79],a[92:95],v[204:207],v[76:79]\nds_read_b64_tr_b16 v[204:205],v242 offset:41984\nds_read_b64_tr_b16 v[206:207],v242 offset:42240\nM v[16:19],a[64:67],v[208:211],v[16:19]\n" \
"buffer_load_dwordx4 v244,s[40:43],s69 offen offset:1024 lds\nM v[16:19],a[80:83],v[212:215],v[16:19]\ns_lshl2_add_u32 s70,s61,s7\nv_lshrrev_b32 v253,4,v249\nM v[20:23],a[68:71],v[208:211],v[20:23]\nds_write_b64 v250,v[142:143] offset:3080\nv_and_b32 v251,0xf0,v240\nM v[20:23],a[84:87],v[212:215],v[20:23]\n" \
"s_add_u32 s48,s44,19922944\ns_addc_u32 s49,s45,0\nM v[80:83],a[72:75],v[208:211],v[80:83]\nbuffer_store_dwordx4 v[156:159],v240,s[52:55],s74 offen offset:3072 sc1\nM v[80:83],a[88:91],v[212:215],v[80:83]\ns_add_u32 s74,s74,4096\ns_mul_i32 s1,s70,s0\nM v[84:87],a[76:79],v[208:211],v[84:87]\n" \
"ds_read_b64_tr_b16 v[208:209],v241 offset:34304\nds_read_b64_tr_b16 v[210:211],v241 offset:34560\nM v[84:87],a[92:95],v[212:215],v[84:87]\nds_read_b64_tr_b16 v[212:213],v241 offset:42496\ns_waitcnt lgkmcnt(8)\nM v[24:27],a[64:67],v[216:219],v[24:27]\nds_read_b64_tr_b16 v[214:215],v241 offset:42752\nv_lshl_or_b32 v253,v253,12,v251\n" \
"M v[24:27],a[80:83],v[220:223],v[24:27]\nbuffer_load_dwordx4 v245,s[40:43],s69 offen offset:2048 lds\nM v[28:31],a[68:71],v[216:219],v[28:31]\ns_lshl_b32 s70,s61,8\ns_mov_b32 s50,s46\nM v[28:31],a[84:87],v[220:223],v[28:31]\ns_add_u32 s44,s44,3145728\ns_addc_u32 s45,s45,0\n" \
"M v[88:91],a[72:75],v[216:219],v[88:91]\ns_sub_u32 s33,s33,1867776\ns_add_u32 s67,s67,36700160\nM v[88:91],a[88:91],v[220:223],v[88:91]\nbuffer_load_dwordx4 v246,s[40:43],s69 offen offset:3072 lds\nM v[92:95],a[76:79],v[216:219],v[92:95]\nds_read_b64_tr_b16 v[216:217],v242 offset:34304\nds_read_b64_tr_b16 v[218:219],v242 offset:34560\n" \
"M v[92:95],a[92:95],v[220:223],v[92:95]\nds_read_b64_tr_b16 v[220:221],v242 offset:42496\nds_read_b64_tr_b16 v[222:223],v242 offset:42752\nM v[32:35],a[64:67],v[192:195],v[32:35]\nds_write_b64 v250,v[144:145] offset:4096\ns_lshl_b32 s0,s0,4\nM v[32:35],a[80:83],v[196:199],v[32:35]\ns_cmp_lt_u32 s62,3\n" \
"s_cselect_b32 s58,0,s58\nM v[36:39],a[68:71],v[192:195],v[36:39]\ns_add_u32 s56,s56,s71\ns_addc_u32 s57,s57,0\nM v[36:39],a[84:87],v[196:199],v[36:39]\nM v[96:99],a[72:75],v[192:195],v[96:99]\nbuffer_store_dwordx4 v[160:163],v240,s[52:55],s74 offen offset:0 sc1\nM v[96:99],a[88:91],v[196:199],v[96:99]\n" \
"M v[100:103],a[76:79],v[192:195],v[100:103]\nM v[100:103],a[92:95],v[196:199],v[100:103]\ns_waitcnt lgkmcnt(5)\nM v[40:43],a[64:67],v[200:203],v[40:43]\nM v[40:43],a[80:83],v[204:207],v[40:43]\nM v[44:47],a[68:71],v[200:203],v[44:47]\nds_write_b64 v250,v[146:147] offset:4104\nM v[44:47],a[84:87],v[204:207],v[44:47]\n" \
"M v[104:107],a[72:75],v[200:203],v[104:107]\nM v[104:107],a[88:91],v[204:207],v[104:107]\nM v[108:111],a[76:79],v[200:203],v[108:111]\nM v[108:111],a[92:95],v[204:207],v[108:111]\nM v[48:51],a[64:67],v[208:211],v[48:51]\nM v[48:51],a[80:83],v[212:215],v[48:51]\nM v[52:55],a[68:71],v[208:211],v[52:55]\ns_add_u32 m0,s64,131072\n" \
"s_add_u32 s69,s68,3145728\nM v[52:55],a[84:87],v[212:215],v[52:55]\nM v[112:115],a[72:75],v[208:211],v[112:115]\ns_waitcnt vmcnt(13) lgkmcnt(1)\ns_barrier\nM v[112:115],a[88:91],v[212:215],v[112:115]\nds_read_b64_tr_b16 v[192:193],v241 offset:49152\nds_read_b64_tr_b16 v[194:195],v241 offset:49408\n" \
"M v[116:119],a[76:79],v[208:211],v[116:119]\nds_read_b64_tr_b16 v[196:197],v241 offset:57344\nds_read_b64_tr_b16 v[198:199],v241 offset:57600\nM v[116:119],a[92:95],v[212:215],v[116:119]\nbuffer_store_dwordx4 v[164:167],v240,s[52:55],s74 offen offset:1024 sc1\nM v[56:59],a[64:67],v[216:219],v[56:59]\nds_read_b64_tr_b16 v[200:201],v242 offset:49152\nds_read_b64_tr_b16 v[202:203],v242 offset:49408\n" \
"M v[56:59],a[80:83],v[220:223],v[56:59]\nds_read_b64_tr_b16 v[204:205],v242 offset:57344\nds_read_b64_tr_b16 v[206:207],v242 offset:57600\nM v[60:63],a[68:71],v[216:219],v[60:63]\nds_read_b64_tr_b16 v[208:209],v241 offset:49664\nds_read_b64_tr_b16 v[210:211],v241 offset:49920\nM v[60:63],a[84:87],v[220:223],v[60:63]\nds_read_b64_tr_b16 v[212:213],v241 offset:57856\n" \
"ds_read_b64_tr_b16 v[214:215],v241 offset:58112\nM v[120:123],a[72:75],v[216:219],v[120:123]\nbuffer_load_dwordx4 v243,s[40:43],s69 offen offset:0 lds\nM v[120:123],a[88:91],v[220:223],v[120:123]\nds_write_b64 v250,v[148:149] offset:5120\nM v[124:127],a[76:79],v[216:219],v[124:127]\nds_read_b64_tr_b16 v[216:217],v242 offset:49664\nds_read_b64_tr_b16 v[218:219],v242 offset:49920\n" \
"M v[124:127],a[92:95],v[220:223],v[124:127]\nds_read_b64_tr_b16 v[220:221],v242 offset:57856\ns_waitcnt lgkmcnt(14)\nM v[0:3],a[96:99],v[192:195],v[0:3]\nds_read_b64_tr_b16 v[222:223],v242 offset:58112\ns_waitcnt lgkmcnt(13)\nM v[0:3],a[112:115],v[196:199],v[0:3]\nbuffer_load_dwordx4 v244,s[40:43],s69 offen offset:1024 lds\n" \
"M v[4:7],a[100:103],v[192:195],v[4:7]\nds_write_b64 v250,v[150:151] offset:5128\nM v[4:7],a[116:119],v[196:199],v[4:7]\nM v[64:67],a[104:107],v[192:195],v[64:67]\nM v[64:67],a[120:123],v[196:199],v[64:67]\nbuffer_load_dwordx4 v245,s[40:43],s69 offen offset:2048 lds\nM v[68:71],a[108:111],v[192:195],v[68:71]\nds_read_b64_tr_b16 v[192:193],v241 offset:50176\n" \
"ds_read_b64_tr_b16 v[194:195],v241 offset:50432\nM v[68:71],a[124:127],v[196:199],v[68:71]\nds_read_b64_tr_b16 v[196:197],v241 offset:58368\ns_waitcnt lgkmcnt(9)\nM v[8:11],a[96:99],v[200:203],v[8:11]\nds_read_b64_tr_b16 v[198:199],v241 offset:58624\nM v[8:11],a[112:115],v[204:207],v[8:11]\nbuffer_store_dwordx4 v[168:171],v240,s[52:55],s74 offen offset:2048 sc1\n" \
"M v[12:15],a[100:103],v[200:203],v[12:15]\nM v[12:15],a[116:119],v[204:207],v[12:15]\nM v[72:75],a[104:107],v[200:203],v[72:75]\nM v[72:75],a[120:123],v[204:207],v[72:75]\nbuffer_load_dwordx4 v246,s[40:43],s69 offen offset:3072 lds\nM v[76:79],a[108:111],v[200:203],v[76:79]\nds_read_b64_tr_b16 v[200:201],v242 offset:50176\nds_read_b64_tr_b16 v[202:203],v242 offset:50432\n" \
"M v[76:79],a[124:127],v[204:207],v[76:79]\nds_read_b64_tr_b16 v[204:205],v242 offset:58368\nds_read_b64_tr_b16 v[206:207],v242 offset:58624\nM v[16:19],a[96:99],v[208:211],v[16:19]\nds_write_b64 v250,v[152:153] offset:6144\nM v[16:19],a[112:115],v[212:215],v[16:19]\nM v[20:23],a[100:103],v[208:211],v[20:23]\nM v[20:23],a[116:119],v[212:215],v[20:23]\n" \
"M v[80:83],a[104:107],v[208:211],v[80:83]\nM v[80:83],a[120:123],v[212:215],v[80:83]\nM v[84:87],a[108:111],v[208:211],v[84:87]\nds_read_b64_tr_b16 v[208:209],v241 offset:50688\nds_read_b64_tr_b16 v[210:211],v241 offset:50944\nM v[84:87],a[124:127],v[212:215],v[84:87]\nds_read_b64_tr_b16 v[212:213],v241 offset:58880\ns_waitcnt lgkmcnt(8)\n" \
"M v[24:27],a[96:99],v[216:219],v[24:27]\nds_read_b64_tr_b16 v[214:215],v241 offset:59136\nM v[24:27],a[112:115],v[220:223],v[24:27]\nM v[28:31],a[100:103],v[216:219],v[28:31]\nbuffer_store_dwordx4 v[172:175],v240,s[52:55],s74 offen offset:3072 sc1\nM v[28:31],a[116:119],v[220:223],v[28:31]\nds_write_b64 v250,v[154:155] offset:6152\ns_add_u32 s74,s74,4096\n" \
"M v[88:91],a[104:107],v[216:219],v[88:91]\nM v[88:91],a[120:123],v[220:223],v[88:91]\nM v[92:95],a[108:111],v[216:219],v[92:95]\nds_read_b64_tr_b16 v[216:217],v242 offset:50688\nds_read_b64_tr_b16 v[218:219],v242 offset:50944\nM v[92:95],a[124:127],v[220:223],v[92:95]\nds_read_b64_tr_b16 v[220:221],v242 offset:58880\nds_read_b64_tr_b16 v[222:223],v242 offset:59136\n" \
"M v[32:35],a[96:99],v[192:195],v[32:35]\nM v[32:35],a[112:115],v[196:199],v[32:35]\nM v[36:39],a[100:103],v[192:195],v[36:39]\nM v[36:39],a[116:119],v[196:199],v[36:39]\nM v[96:99],a[104:107],v[192:195],v[96:99]\nM v[96:99],a[120:123],v[196:199],v[96:99]\nM v[100:103],a[108:111],v[192:195],v[100:103]\nds_write_b64 v250,v[156:157] offset:7168\n" \
"M v[100:103],a[124:127],v[196:199],v[100:103]\ns_waitcnt lgkmcnt(7)\nM v[40:43],a[96:99],v[200:203],v[40:43]\nM v[40:43],a[112:115],v[204:207],v[40:43]\nM v[44:47],a[100:103],v[200:203],v[44:47]\nM v[44:47],a[116:119],v[204:207],v[44:47]\nM v[104:107],a[104:107],v[200:203],v[104:107]\nbuffer_store_dwordx4 v[176:179],v240,s[52:55],s74 offen offset:0 sc1\n" \
"M v[104:107],a[120:123],v[204:207],v[104:107]\nM v[108:111],a[108:111],v[200:203],v[108:111]\nM v[108:111],a[124:127],v[204:207],v[108:111]\nM v[48:51],a[96:99],v[208:211],v[48:51]\ns_waitcnt lgkmcnt(3)\nM v[48:51],a[112:115],v[212:215],v[48:51]\nM v[52:55],a[100:103],v[208:211],v[52:55]\ns_add_u32 m0,s64,147456\n" \
"s_add_u32 s69,s68,3670016\nM v[52:55],a[116:119],v[212:215],v[52:55]\nds_write_b64 v250,v[158:159] offset:7176\nM v[112:115],a[104:107],v[208:211],v[112:115]\ns_waitcnt vmcnt(14) lgkmcnt(2)\ns_barrier\nM v[112:115],a[120:123],v[212:215],v[112:115]\nds_read_b64_tr_b16 v[192:193],v241 offset:0\n" \
"ds_read_b64_tr_b16 v[194:195],v241 offset:256\nM v[116:119],a[108:111],v[208:211],v[116:119]\nds_read_b64_tr_b16 v[196:197],v241 offset:8192\nds_read_b64_tr_b16 v[198:199],v241 offset:8448\nM v[116:119],a[124:127],v[212:215],v[116:119]\nds_read_b64_tr_b16 v[200:201],v242 offset:0\nds_read_b64_tr_b16 v[202:203],v242 offset:256\nM v[56:59],a[96:99],v[216:219],v[56:59]\n" \
"ds_read_b64_tr_b16 v[204:205],v242 offset:8192\nds_read_b64_tr_b16 v[206:207],v242 offset:8448\nM v[56:59],a[112:115],v[220:223],v[56:59]\nds_read_b64_tr_b16 v[208:209],v241 offset:512\nds_read_b64_tr_b16 v[210:211],v241 offset:768\nM v[60:63],a[100:103],v[216:219],v[60:63]\nds_read_b64_tr_b16 v[212:213],v241 offset:8704\nds_read_b64_tr_b16 v[214:215],v241 offset:8960\n" \
"M v[60:63],a[116:119],v[220:223],v[60:63]\nbuffer_load_dwordx4 v243,s[40:43],s69 offen offset:0 lds\nM v[120:123],a[104:107],v[216:219],v[120:123]\nM v[120:123],a[120:123],v[220:223],v[120:123]\nM v[124:127],a[108:111],v[216:219],v[124:127]\nds_read_b64_tr_b16 v[216:217],v242 offset:512\nds_read_b64_tr_b16 v[218:219],v242 offset:768\nM v[124:127],a[124:127],v[220:223],v[124:127]\n" \
"ds_read_b64_tr_b16 v[220:221],v242 offset:8704\ns_waitcnt lgkmcnt(13)\nM v[0:3],a[128:131],v[192:195],v[0:3]\nds_read_b64_tr_b16 v[222:223],v242 offset:8960\ns_waitcnt lgkmcnt(12)\nM v[0:3],a[144:147],v[196:199],v[0:3]\nbuffer_store_dwordx4 v[180:183],v240,s[52:55],s74 offen offset:1024 sc1\nM v[4:7],a[132:135],v[192:195],v[4:7]\n" \
"ds_write_b64 v250,v[160:161] offset:8192\nM v[4:7],a[148:151],v[196:199],v[4:7]\nM v[64:67],a[136:139],v[192:195],v[64:67]\nM v[64:67],a[152:155],v[196:199],v[64:67]\nbuffer_load_dwordx4 v244,s[40:43],s69 offen offset:1024 lds\nM v[68:71],a[140:143],v[192:195],v[68:71]\nds_read_b64_tr_b16 v[192:193],v241 offset:1024\nds_read_b64_tr_b16 v[194:195],v241 offset:1280\n" \
"M v[68:71],a[156:159],v[196:199],v[68:71]\nds_read_b64_tr_b16 v[196:197],v241 offset:9216\ns_waitcnt lgkmcnt(8)\nM v[8:11],a[128:131],v[200:203],v[8:11]\nds_read_b64_tr_b16 v[198:199],v241 offset:9472\nM v[8:11],a[144:147],v[204:207],v[8:11]\nbuffer_load_dwordx4 v245,s[40:43],s69 offen offset:2048 lds\nM v[12:15],a[132:135],v[200:203],v[12:15]\n" \
"ds_write_b64 v250,v[162:163] offset:8200\nM v[12:15],a[148:151],v[204:207],v[12:15]\nM v[72:75],a[136:139],v[200:203],v[72:75]\nM v[72:75],a[152:155],v[204:207],v[72:75]\nbuffer_load_dwordx4 v246,s[40:43],s69 offen offset:3072 lds\nM v[76:79],a[140:143],v[200:203],v[76:79]\nds_read_b64_tr_b16 v[200:201],v242 offset:1024\nds_read_b64_tr_b16 v[202:203],v242 offset:1280\n" \
"M v[76:79],a[156:159],v[204:207],v[76:79]\nds_read_b64_tr_b16 v[204:205],v242 offset:9216\nds_read_b64_tr_b16 v[206:207],v242 offset:9472\nM v[16:19],a[128:131],v[208:211],v[16:19]\nM v[16:19],a[144:147],v[212:215],v[16:19]\nbuffer_store_dwordx4 v[184:187],v240,s[52:55],s74 offen offset:2048 sc1\nM v[20:23],a[132:135],v[208:211],v[20:23]\nM v[20:23],a[148:151],v[212:215],v[20:23]\n" \
"M v[80:83],a[136:139],v[208:211],v[80:83]\nM v[80:83],a[152:155],v[212:215],v[80:83]\nM v[84:87],a[140:143],v[208:211],v[84:87]\nbuffer_load_dword v248,v228,s[52:55],s80 offen sc1\nM v[84:87],a[156:159],v[212:215],v[84:87]\nds_read_b64_tr_b16 v[208:209],v241 offset:1536\ns_waitcnt lgkmcnt(6)\nM v[24:27],a[128:131],v[216:219],v[24:27]\n" \
"ds_read_b64_tr_b16 v[210:211],v241 offset:1792\nds_read_b64_tr_b16 v[212:213],v241 offset:9728\nM v[24:27],a[144:147],v[220:223],v[24:27]\nds_write_b64 v250,v[164:165] offset:9216\nds_read_b64_tr_b16 v[214:215],v241 offset:9984\nM v[28:31],a[132:135],v[216:219],v[28:31]\nM v[28:31],a[148:151],v[220:223],v[28:31]\nM v[88:91],a[136:139],v[216:219],v[88:91]\n" \
"M v[88:91],a[152:155],v[220:223],v[88:91]\nM v[92:95],a[140:143],v[216:219],v[92:95]\nds_read_b64_tr_b16 v[216:217],v242 offset:1536\nds_read_b64_tr_b16 v[218:219],v242 offset:1792\nM v[92:95],a[156:159],v[220:223],v[92:95]\nds_read_b64_tr_b16 v[220:221],v242 offset:9728\nds_read_b64_tr_b16 v[222:223],v242 offset:9984\nM v[32:35],a[128:131],v[192:195],v[32:35]\n" \
"M v[32:35],a[144:147],v[196:199],v[32:35]\nM v[36:39],a[132:135],v[192:195],v[36:39]\nbuffer_store_dwordx4 v[188:191],v240,s[52:55],s74 offen offset:3072 sc1\nM v[36:39],a[148:151],v[196:199],v[36:39]\nds_write_b64 v250,v[166:167] offset:9224\ns_add_u32 s74,s74,16764928\nM v[96:99],a[136:139],v[192:195],v[96:99]\nM v[96:99],a[152:155],v[196:199],v[96:99]\n" \
"M v[100:103],a[140:143],v[192:195],v[100:103]\nM v[100:103],a[156:159],v[196:199],v[100:103]\ns_waitcnt lgkmcnt(7)\nM v[40:43],a[128:131],v[200:203],v[40:43]\nM v[40:43],a[144:147],v[204:207],v[40:43]\nM v[44:47],a[132:135],v[200:203],v[44:47]\nM v[44:47],a[148:151],v[204:207],v[44:47]\nM v[104:107],a[136:139],v[200:203],v[104:107]\n" \
"M v[104:107],a[152:155],v[204:207],v[104:107]\nM v[108:111],a[140:143],v[200:203],v[108:111]\nds_write_b64 v250,v[168:169] offset:10240\nM v[108:111],a[156:159],v[204:207],v[108:111]\nM v[48:51],a[128:131],v[208:211],v[48:51]\ns_waitcnt lgkmcnt(2)\nM v[48:51],a[144:147],v[212:215],v[48:51]\nM v[52:55],a[132:135],v[208:211],v[52:55]\n" \
"s_sub_u32 s40,s12,0x1000\ns_subb_u32 s41,s13,0\nM v[52:55],a[148:151],v[212:215],v[52:55]\ns_add_u32 m0,s64,98304\ns_add_u32 s69,s68,0\nM v[112:115],a[136:139],v[208:211],v[112:115]\ns_waitcnt vmcnt(17)\ns_barrier\n" \
"M v[112:115],a[152:155],v[212:215],v[112:115]\nds_read_b64_tr_b16 v[192:193],v241 offset:16384\nds_read_b64_tr_b16 v[194:195],v241 offset:16640\nM v[116:119],a[140:143],v[208:211],v[116:119]\nds_read_b64_tr_b16 v[196:197],v241 offset:24576\nds_read_b64_tr_b16 v[198:199],v241 offset:24832\nM v[116:119],a[156:159],v[212:215],v[116:119]\nds_read_b64_tr_b16 v[200:201],v242 offset:16384\n" \
"ds_read_b64_tr_b16 v[202:203],v242 offset:16640\nM v[56:59],a[128:131],v[216:219],v[56:59]\nds_read_b64_tr_b16 v[204:205],v242 offset:24576\nds_read_b64_tr_b16 v[206:207],v242 offset:24832\nM v[56:59],a[144:147],v[220:223],v[56:59]\nds_read_b64_tr_b16 v[208:209],v241 offset:16896\nds_read_b64_tr_b16 v[210:211],v241 offset:17152\nM v[60:63],a[132:135],v[216:219],v[60:63]\n" \
"ds_read_b64_tr_b16 v[212:213],v241 offset:25088\nds_read_b64_tr_b16 v[214:215],v241 offset:25344\nM v[60:63],a[148:151],v[220:223],v[60:63]\nbuffer_load_dwordx4 v243,s[40:43],s69 offen offset:0 lds\nM v[120:123],a[136:139],v[216:219],v[120:123]\nds_write_b64 v250,v[170:171] offset:10248\ns_lshl_b32 s12,s61,13\nM v[120:123],a[152:155],v[220:223],v[120:123]\n" \
"M v[124:127],a[140:143],v[216:219],v[124:127]\nds_read_b64_tr_b16 v[216:217],v242 offset:16896\nds_read_b64_tr_b16 v[218:219],v242 offset:17152\nM v[124:127],a[156:159],v[220:223],v[124:127]\nds_read_b64_tr_b16 v[220:221],v242 offset:25088\ns_waitcnt lgkmcnt(14)\nM v[0:3],a[160:163],v[192:195],v[0:3]\nds_read_b64_tr_b16 v[222:223],v242 offset:25344\n" \
"s_waitcnt lgkmcnt(13)\nM v[0:3],a[176:179],v[196:199],v[0:3]\nbuffer_load_dwordx4 v244,s[40:43],s69 offen offset:1024 lds\nM v[4:7],a[164:167],v[192:195],v[4:7]\nM v[4:7],a[180:183],v[196:199],v[4:7]\nM v[64:67],a[168:171],v[192:195],v[64:67]\nM v[64:67],a[184:187],v[196:199],v[64:67]\nbuffer_load_dwordx4 v245,s[40:43],s69 offen offset:2048 lds\n" \
"M v[68:71],a[172:175],v[192:195],v[68:71]\nds_read_b64_tr_b16 v[192:193],v241 offset:17408\nds_read_b64_tr_b16 v[194:195],v241 offset:17664\nM v[68:71],a[188:191],v[196:199],v[68:71]\nds_read_b64_tr_b16 v[196:197],v241 offset:25600\ns_waitcnt lgkmcnt(8)\nM v[8:11],a[160:163],v[200:203],v[8:11]\nds_write_b64 v250,v[172:173] offset:11264\n" \
"ds_read_b64_tr_b16 v[198:199],v241 offset:25856\nM v[8:11],a[176:179],v[204:207],v[8:11]\nbuffer_load_dwordx4 v246,s[40:43],s69 offen offset:3072 lds\nM v[12:15],a[164:167],v[200:203],v[12:15]\nM v[12:15],a[180:183],v[204:207],v[12:15]\nM v[72:75],a[168:171],v[200:203],v[72:75]\nM v[72:75],a[184:187],v[204:207],v[72:75]\ns_waitcnt vmcnt(5)\n" \
"v_cmp_eq_u32 vcc,0,v248\nM v[76:79],a[172:175],v[200:203],v[76:79]\ns_cbranch_vccnz .Lps0\n.Lpb0:\nds_read_b64_tr_b16 v[200:201],v242 offset:17408\nM v[76:79],a[188:191],v[204:207],v[76:79]\nds_read_b64_tr_b16 v[202:203],v242 offset:17664\nds_read_b64_tr_b16 v[204:205],v242 offset:25600\n" \
"M v[16:19],a[160:163],v[208:211],v[16:19]\nds_read_b64_tr_b16 v[206:207],v242 offset:25856\ns_lshl_b32 s80,s63,7\nM v[16:19],a[176:179],v[212:215],v[16:19]\nbuffer_load_dwordx4 a[0:3],v240,s[52:55],s76 offen offset:0 sc1\nM v[20:23],a[164:167],v[208:211],v[20:23]\nds_write_b64 v250,v[174:175] offset:11272\nv_and_b32 v228,31,v249\n" \
"M v[20:23],a[180:183],v[212:215],v[20:23]\nv_lshlrev_b32 v228,2,v228\ns_add_u32 s80,s80,81920\nM v[80:83],a[168:171],v[208:211],v[80:83]\nv_mov_b32 v251,v228\nM v[80:83],a[184:187],v[212:215],v[80:83]\nbuffer_load_dwordx4 a[16:19],v240,s[52:55],s76 offen offset:2048 sc1\nM v[84:87],a[172:175],v[208:211],v[84:87]\n" \
"ds_read_b64_tr_b16 v[208:209],v241 offset:17920\nds_read_b64_tr_b16 v[210:211],v241 offset:18176\nM v[84:87],a[188:191],v[212:215],v[84:87]\nds_read_b64_tr_b16 v[212:213],v241 offset:26112\ns_waitcnt lgkmcnt(8)\nM v[24:27],a[160:163],v[216:219],v[24:27]\nds_read_b64_tr_b16 v[214:215],v241 offset:26368\nM v[24:27],a[176:179],v[220:223],v[24:27]\n" \
"buffer_load_dwordx4 a[4:7],v240,s[52:55],s76 offen offset:1024 sc1\nM v[28:31],a[164:167],v[216:219],v[28:31]\nM v[28:31],a[180:183],v[220:223],v[28:31]\nM v[88:91],a[168:171],v[216:219],v[88:91]\nM v[88:91],a[184:187],v[220:223],v[88:91]\nbuffer_load_dwordx4 a[20:23],v240,s[52:55],s76 offen offset:3072 sc1\nM v[92:95],a[172:175],v[216:219],v[92:95]\nds_read_b64_tr_b16 v[216:217],v242 offset:17920\n" \
"ds_read_b64_tr_b16 v[218:219],v242 offset:18176\nM v[92:95],a[188:191],v[220:223],v[92:95]\nds_read_b64_tr_b16 v[220:221],v242 offset:26112\nds_read_b64_tr_b16 v[222:223],v242 offset:26368\nM v[32:35],a[160:163],v[192:195],v[32:35]\nds_write_b64 v250,v[176:177] offset:12288\ns_add_u32 s76,s76,4096\nM v[32:35],a[176:179],v[196:199],v[32:35]\n" \
"buffer_load_dwordx4 a[8:11],v240,s[52:55],s77 offen offset:0 sc1\nM v[36:39],a[164:167],v[192:195],v[36:39]\nM v[36:39],a[180:183],v[196:199],v[36:39]\nM v[96:99],a[168:171],v[192:195],v[96:99]\nM v[96:99],a[184:187],v[196:199],v[96:99]\nbuffer_load_dwordx4 a[24:27],v240,s[52:55],s77 offen offset:2048 sc1\nM v[100:103],a[172:175],v[192:195],v[100:103]\nM v[100:103],a[188:191],v[196:199],v[100:103]\n" \
"s_waitcnt lgkmcnt(5)\nM v[40:43],a[160:163],v[200:203],v[40:43]\nM v[40:43],a[176:179],v[204:207],v[40:43]\nbuffer_load_dwordx4 a[12:15],v240,s[52:55],s77 offen offset:1024 sc1\nM v[44:47],a[164:167],v[200:203],v[44:47]\nds_write_b64 v250,v[178:179] offset:12296\nM v[44:47],a[180:183],v[204:207],v[44:47]\nM v[104:107],a[168:171],v[200:203],v[104:107]\n" \
"M v[104:107],a[184:187],v[204:207],v[104:107]\nbuffer_load_dwordx4 a[28:31],v240,s[52:55],s77 offen offset:3072 sc1\nM v[108:111],a[172:175],v[200:203],v[108:111]\ns_add_u32 s77,s77,4096\nM v[108:111],a[188:191],v[204:207],v[108:111]\nM v[48:51],a[160:163],v[208:211],v[48:51]\nM v[48:51],a[176:179],v[212:215],v[48:51]\nbuffer_load_dwordx4 a[32:35],v240,s[52:55],s76 offen offset:0 sc1\n" \
"M v[52:55],a[164:167],v[208:211],v[52:55]\ns_add_u32 m0,s64,114688\ns_add_u32 s69,s68,524288\nM v[52:55],a[180:183],v[212:215],v[52:55]\nM v[112:115],a[168:171],v[208:211],v[112:115]\ns_waitcnt lgkmcnt(1)\ns_barrier\nM v[112:115],a[184:187],v[212:215],v[112:115]\n" \
"ds_read_b64_tr_b16 v[192:193],v241 offset:32768\nds_read_b64_tr_b16 v[194:195],v241 offset:33024\nM v[116:119],a[172:175],v[208:211],v[116:119]\nds_read_b64_tr_b16 v[196:197],v241 offset:40960\nds_read_b64_tr_b16 v[198:199],v241 offset:41216\nM v[116:119],a[188:191],v[212:215],v[116:119]\nds_read_b64_tr_b16 v[200:201],v242 offset:32768\nds_read_b64_tr_b16 v[202:203],v242 offset:33024\n" \
"M v[56:59],a[160:163],v[216:219],v[56:59]\nds_read_b64_tr_b16 v[204:205],v242 offset:40960\nds_read_b64_tr_b16 v[206:207],v242 offset:41216\nM v[56:59],a[176:179],v[220:223],v[56:59]\nds_read_b64_tr_b16 v[208:209],v241 offset:33280\nds_read_b64_tr_b16 v[210:211],v241 offset:33536\nM v[60:63],a[164:167],v[216:219],v[60:63]\nds_read_b64_tr_b16 v[212:213],v241 offset:41472\n" \
"ds_read_b64_tr_b16 v[214:215],v241 offset:41728\nM v[60:63],a[180:183],v[220:223],v[60:63]\nbuffer_load_dwordx4 a[36:39],v240,s[52:55],s76 offen offset:1024 sc1\nM v[120:123],a[168:171],v[216:219],v[120:123]\nds_write_b64 v250,v[180:181] offset:13312\nM v[120:123],a[184:187],v[220:223],v[120:123]\nM v[124:127],a[172:175],v[216:219],v[124:127]\nds_read_b64_tr_b16 v[216:217],v242 offset:33280\n" \
"ds_read_b64_tr_b16 v[218:219],v242 offset:33536\nM v[124:127],a[188:191],v[220:223],v[124:127]\nds_read_b64_tr_b16 v[220:221],v242 offset:41472\ns_waitcnt lgkmcnt(14)\nM v[0:3],a[192:195],v[192:195],v[0:3]\nds_read_b64_tr_b16 v[222:223],v242 offset:41728\ns_waitcnt lgkmcnt(13)\nM v[0:3],a[208:211],v[196:199],v[0:3]\n" \
"buffer_load_dwordx4 a[40:43],v240,s[52:55],s77 offen offset:0 sc1\nM v[4:7],a[196:199],v[192:195],v[4:7]\nds_write_b64 v250,v[182:183] offset:13320\nM v[4:7],a[212:215],v[196:199],v[4:7]\nM v[64:67],a[200:203],v[192:195],v[64:67]\nM v[64:67],a[216:219],v[196:199],v[64:67]\nbuffer_load_dwordx4 a[48:51],v240,s[52:55],s76 offen offset:2048 sc1\nM v[68:71],a[204:207],v[192:195],v[68:71]\n" \
"ds_read_b64_tr_b16 v[192:193],v241 offset:33792\nds_read_b64_tr_b16 v[194:195],v241 offset:34048\nM v[68:71],a[220:223],v[196:199],v[68:71]\nds_read_b64_tr_b16 v[196:197],v241 offset:41984\ns_waitcnt lgkmcnt(9)\nM v[8:11],a[192:195],v[200:203],v[8:11]\nds_read_b64_tr_b16 v[198:199],v241 offset:42240\nM v[8:11],a[208:211],v[204:207],v[8:11]\n" \
"buffer_load_dwordx4 a[44:47],v240,s[52:55],s77 offen offset:1024 sc1\nM v[12:15],a[196:199],v[200:203],v[12:15]\nM v[12:15],a[212:215],v[204:207],v[12:15]\nM v[72:75],a[200:203],v[200:203],v[72:75]\nM v[72:75],a[216:219],v[204:207],v[72:75]\nbuffer_load_dwordx4 a[52:55],v240,s[52:55],s76 offen offset:3072 sc1\nM v[76:79],a[204:207],v[200:203],v[76:79]\nds_read_b64_tr_b16 v[200:201],v242 offset:33792\n" \
"ds_read_b64_tr_b16 v[202:203],v242 offset:34048\nM v[76:79],a[220:223],v[204:207],v[76:79]\nds_read_b64_tr_b16 v[204:205],v242 offset:41984\nds_read_b64_tr_b16 v[206:207],v242 offset:42240\nM v[16:19],a[192:195],v[208:211],v[16:19]\nds_write_b64 v250,v[184:185] offset:14336\ns_add_u32 s76,s76,4096\nM v[16:19],a[208:211],v[212:215],v[16:19]\n" \
"buffer_load_dwordx4 v243,s[40:43],s69 offen offset:0 lds\nM v[20:23],a[196:199],v[208:211],v[20:23]\nM v[20:23],a[212:215],v[212:215],v[20:23]\nM v[80:83],a[200:203],v[208:211],v[80:83]\nM v[80:83],a[216:219],v[212:215],v[80:83]\nbuffer_load_dwordx4 a[56:59],v240,s[52:55],s77 offen offset:2048 sc1\nM v[84:87],a[204:207],v[208:211],v[84:87]\nds_read_b64_tr_b16 v[208:209],v241 offset:34304\n" \
"ds_read_b64_tr_b16 v[210:211],v241 offset:34560\nM v[84:87],a[220:223],v[212:215],v[84:87]\nds_read_b64_tr_b16 v[212:213],v241 offset:42496\ns_waitcnt lgkmcnt(8)\nM v[24:27],a[192:195],v[216:219],v[24:27]\nds_read_b64_tr_b16 v[214:215],v241 offset:42752\nM v[24:27],a[208:211],v[220:223],v[24:27]\nbuffer_load_dwordx4 v244,s[40:43],s69 offen offset:1024 lds\n" \
"M v[28:31],a[196:199],v[216:219],v[28:31]\nds_write_b64 v250,v[186:187] offset:14344\nM v[28:31],a[212:215],v[220:223],v[28:31]\nM v[88:91],a[200:203],v[216:219],v[88:91]\nM v[88:91],a[216:219],v[220:223],v[88:91]\nbuffer_load_dwordx4 a[60:63],v240,s[52:55],s77 offen offset:3072 sc1\nM v[92:95],a[204:207],v[216:219],v[92:95]\nds_read_b64_tr_b16 v[216:217],v242 offset:34304\n" \
"ds_read_b64_tr_b16 v[218:219],v242 offset:34560\nM v[92:95],a[220:223],v[220:223],v[92:95]\nds_read_b64_tr_b16 v[220:221],v242 offset:42496\nds_read_b64_tr_b16 v[222:223],v242 offset:42752\nM v[32:35],a[192:195],v[192:195],v[32:35]\ns_add_u32 s77,s77,4096\nM v[32:35],a[208:211],v[196:199],v[32:35]\nbuffer_load_dwordx4 v245,s[40:43],s69 offen offset:2048 lds\n" \
"M v[36:39],a[196:199],v[192:195],v[36:39]\nM v[36:39],a[212:215],v[196:199],v[36:39]\nM v[96:99],a[200:203],v[192:195],v[96:99]\nM v[96:99],a[216:219],v[196:199],v[96:99]\nbuffer_load_dwordx4 v246,s[40:43],s69 offen offset:3072 lds\nM v[100:103],a[204:207],v[192:195],v[100:103]\nds_write_b64 v250,v[188:189] offset:15360\nM v[100:103],a[220:223],v[196:199],v[100:103]\n" \
"s_waitcnt lgkmcnt(6)\nM v[40:43],a[192:195],v[200:203],v[40:43]\nM v[40:43],a[208:211],v[204:207],v[40:43]\nbuffer_load_dwordx4 a[64:67],v240,s[52:55],s76 offen offset:0 sc1\nM v[44:47],a[196:199],v[200:203],v[44:47]\nM v[44:47],a[212:215],v[204:207],v[44:47]\nM v[104:107],a[200:203],v[200:203],v[104:107]\nM v[104:107],a[216:219],v[204:207],v[104:107]\n" \
"buffer_load_dwordx4 a[68:71],v240,s[52:55],s76 offen offset:1024 sc1\nM v[108:111],a[204:207],v[200:203],v[108:111]\nM v[108:111],a[220:223],v[204:207],v[108:111]\nM v[48:51],a[192:195],v[208:211],v[48:51]\nM v[48:51],a[208:211],v[212:215],v[48:51]\ns_waitcnt vmcnt(26)\nbuffer_store_dword v252,off,s[52:55],s29 sc1\nM v[52:55],a[196:199],v[208:211],v[52:55]\n" \
"s_add_u32 m0,s64,131072\ns_add_u32 s69,s68,1048576\nM v[52:55],a[212:215],v[212:215],v[52:55]\nds_write_b64 v250,v[190:191] offset:15368\nv_add_u32 v250,65536,v250\nM v[112:115],a[200:203],v[208:211],v[112:115]\ns_waitcnt lgkmcnt(1)\ns_barrier\n" \
"M v[112:115],a[216:219],v[212:215],v[112:115]\nds_read_b64_tr_b16 v[192:193],v241 offset:49152\nds_read_b64_tr_b16 v[194:195],v241 offset:49408\nM v[116:119],a[204:207],v[208:211],v[116:119]\nds_read_b64_tr_b16 v[196:197],v241 offset:57344\nds_read_b64_tr_b16 v[198:199],v241 offset:57600\nM v[116:119],a[220:223],v[212:215],v[116:119]\nds_read_b64_tr_b16 v[200:201],v242 offset:49152\n" \
"ds_read_b64_tr_b16 v[202:203],v242 offset:49408\nM v[56:59],a[192:195],v[216:219],v[56:59]\nds_read_b64_tr_b16 v[204:205],v242 offset:57344\nds_read_b64_tr_b16 v[206:207],v242 offset:57600\nM v[56:59],a[208:211],v[220:223],v[56:59]\nds_read_b64_tr_b16 v[208:209],v241 offset:49664\nds_read_b64_tr_b16 v[210:211],v241 offset:49920\nM v[60:63],a[196:199],v[216:219],v[60:63]\n" \
"ds_read_b64_tr_b16 v[212:213],v241 offset:57856\nds_read_b64_tr_b16 v[214:215],v241 offset:58112\nM v[60:63],a[212:215],v[220:223],v[60:63]\nbuffer_load_dwordx4 a[72:75],v240,s[52:55],s77 offen offset:0 sc1\nM v[120:123],a[200:203],v[216:219],v[120:123]\ns_add_u32 s29,s29,4096\nM v[120:123],a[216:219],v[220:223],v[120:123]\nM v[124:127],a[204:207],v[216:219],v[124:127]\n" \
"ds_read_b64_tr_b16 v[216:217],v242 offset:49664\nds_read_b64_tr_b16 v[218:219],v242 offset:49920\nM v[124:127],a[220:223],v[220:223],v[124:127]\nds_read_b64_tr_b16 v[220:221],v242 offset:57856\ns_waitcnt lgkmcnt(13)\nM v[0:3],a[224:227],v[192:195],v[0:3]\nds_read_b64_tr_b16 v[222:223],v242 offset:58112\ns_waitcnt lgkmcnt(12)\n" \
"M v[0:3],a[240:243],v[196:199],v[0:3]\nbuffer_load_dwordx4 a[76:79],v240,s[52:55],s77 offen offset:1024 sc1\nM v[4:7],a[228:231],v[192:195],v[4:7]\nM v[4:7],a[244:247],v[196:199],v[4:7]\nM v[64:67],a[232:235],v[192:195],v[64:67]\nM v[64:67],a[248:251],v[196:199],v[64:67]\nbuffer_load_dwordx4 a[80:83],v240,s[52:55],s76 offen offset:2048 sc1\nM v[68:71],a[236:239],v[192:195],v[68:71]\n" \
"ds_read_b64_tr_b16 v[192:193],v241 offset:50176\nds_read_b64_tr_b16 v[194:195],v241 offset:50432\nM v[68:71],a[252:255],v[196:199],v[68:71]\nds_read_b64_tr_b16 v[196:197],v241 offset:58368\ns_waitcnt lgkmcnt(7)\nM v[8:11],a[224:227],v[200:203],v[8:11]\nds_read_b64_tr_b16 v[198:199],v241 offset:58624\nv_cvt_pk_bf16_f32 v128,v0,v1\n" \
"M v[8:11],a[240:243],v[204:207],v[8:11]\nv_cvt_pk_bf16_f32 v129,v2,v3\nv_cvt_pk_bf16_f32 v130,v4,v5\nM v[12:15],a[228:231],v[200:203],v[12:15]\nv_cvt_pk_bf16_f32 v131,v6,v7\nv_cvt_pk_bf16_f32 v160,v64,v65\nM v[12:15],a[244:247],v[204:207],v[12:15]\nv_cvt_pk_bf16_f32 v161,v66,v67\n" \
"v_cvt_pk_bf16_f32 v162,v68,v69\nM v[72:75],a[232:235],v[200:203],v[72:75]\nv_cvt_pk_bf16_f32 v163,v70,v71\nv_cvt_pk_bf16_f32 v132,v8,v9\nM v[72:75],a[248:251],v[204:207],v[72:75]\nbuffer_load_dwordx4 a[84:87],v240,s[52:55],s76 offen offset:3072 sc1\nM v[76:79],a[236:239],v[200:203],v[76:79]\nds_read_b64_tr_b16 v[200:201],v242 offset:50176\n" \
"ds_read_b64_tr_b16 v[202:203],v242 offset:50432\nM v[76:79],a[252:255],v[204:207],v[76:79]\nds_read_b64_tr_b16 v[204:205],v242 offset:58368\nds_read_b64_tr_b16 v[206:207],v242 offset:58624\nM v[16:19],a[224:227],v[208:211],v[16:19]\nv_cvt_pk_bf16_f32 v133,v10,v11\nv_cvt_pk_bf16_f32 v134,v12,v13\nM v[16:19],a[240:243],v[212:215],v[16:19]\n" \
"v_cvt_pk_bf16_f32 v135,v14,v15\nv_cvt_pk_bf16_f32 v164,v72,v73\nM v[20:23],a[228:231],v[208:211],v[20:23]\nv_cvt_pk_bf16_f32 v165,v74,v75\nv_cvt_pk_bf16_f32 v166,v76,v77\nM v[20:23],a[244:247],v[212:215],v[20:23]\nv_cvt_pk_bf16_f32 v167,v78,v79\ns_add_u32 s76,s76,4096\n" \
"M v[80:83],a[232:235],v[208:211],v[80:83]\nv_cvt_pk_bf16_f32 v136,v16,v17\nv_cvt_pk_bf16_f32 v137,v18,v19\nM v[80:83],a[248:251],v[212:215],v[80:83]\nbuffer_load_dwordx4 a[88:91],v240,s[52:55],s77 offen offset:2048 sc1\nM v[84:87],a[236:239],v[208:211],v[84:87]\nds_read_b64_tr_b16 v[208:209],v241 offset:50688\nds_read_b64_tr_b16 v[210:211],v241 offset:50944\n" \
"M v[84:87],a[252:255],v[212:215],v[84:87]\nds_read_b64_tr_b16 v[212:213],v241 offset:58880\ns_waitcnt lgkmcnt(7)\nM v[24:27],a[224:227],v[216:219],v[24:27]\nds_read_b64_tr_b16 v[214:215],v241 offset:59136\nv_cvt_pk_bf16_f32 v138,v20,v21\nM v[24:27],a[240:243],v[220:223],v[24:27]\nv_cvt_pk_bf16_f32 v139,v22,v23\n" \
"v_cvt_pk_bf16_f32 v168,v80,v81\nM v[28:31],a[228:231],v[216:219],v[28:31]\nv_cvt_pk_bf16_f32 v169,v82,v83\nv_cvt_pk_bf16_f32 v170,v84,v85\nM v[28:31],a[244:247],v[220:223],v[28:31]\nbuffer_load_dwordx4 v243,s[40:43],s69 offen offset:0 lds\nM v[88:91],a[232:235],v[216:219],v[88:91]\nv_cvt_pk_bf16_f32 v171,v86,v87\n" \
"v_cvt_pk_bf16_f32 v140,v24,v25\nM v[88:91],a[248:251],v[220:223],v[88:91]\nv_cvt_pk_bf16_f32 v141,v26,v27\nM v[92:95],a[236:239],v[216:219],v[92:95]\nds_read_b64_tr_b16 v[216:217],v242 offset:50688\nds_read_b64_tr_b16 v[218:219],v242 offset:50944\nM v[92:95],a[252:255],v[220:223],v[92:95]\nds_read_b64_tr_b16 v[220:221],v242 offset:58880\n" \
"ds_read_b64_tr_b16 v[222:223],v242 offset:59136\nM v[32:35],a[224:227],v[192:195],v[32:35]\nv_cvt_pk_bf16_f32 v142,v28,v29\nv_cvt_pk_bf16_f32 v143,v30,v31\nM v[32:35],a[240:243],v[196:199],v[32:35]\nv_cvt_pk_bf16_f32 v172,v88,v89\nv_cvt_pk_bf16_f32 v173,v90,v91\nM v[36:39],a[228:231],v[192:195],v[36:39]\n" \
"v_cvt_pk_bf16_f32 v174,v92,v93\nv_cvt_pk_bf16_f32 v175,v94,v95\nM v[36:39],a[244:247],v[196:199],v[36:39]\nbuffer_load_dwordx4 a[92:95],v240,s[52:55],s77 offen offset:3072 sc1\nM v[96:99],a[232:235],v[192:195],v[96:99]\nv_cvt_pk_bf16_f32 v144,v32,v33\nv_cvt_pk_bf16_f32 v145,v34,v35\nM v[96:99],a[248:251],v[196:199],v[96:99]\n" \
"s_add_u32 s77,s77,4096\nM v[100:103],a[236:239],v[192:195],v[100:103]\nM v[100:103],a[252:255],v[196:199],v[100:103]\nv_cvt_pk_bf16_f32 v146,v36,v37\ns_waitcnt lgkmcnt(4)\nM v[40:43],a[224:227],v[200:203],v[40:43]\nbuffer_load_dwordx4 v244,s[40:43],s69 offen offset:1024 lds\nM v[40:43],a[240:243],v[204:207],v[40:43]\n" \
"v_cvt_pk_bf16_f32 v147,v38,v39\nv_cvt_pk_bf16_f32 v176,v96,v97\nM v[44:47],a[228:231],v[200:203],v[44:47]\nv_cvt_pk_bf16_f32 v177,v98,v99\nv_cvt_pk_bf16_f32 v178,v100,v101\nM v[44:47],a[244:247],v[204:207],v[44:47]\nv_cvt_pk_bf16_f32 v179,v102,v103\nM v[104:107],a[232:235],v[200:203],v[104:107]\n" \
"v_cvt_pk_bf16_f32 v148,v40,v41\nv_cvt_pk_bf16_f32 v149,v42,v43\nM v[104:107],a[248:251],v[204:207],v[104:107]\nbuffer_load_dwordx4 v245,s[40:43],s69 offen offset:2048 lds\nM v[108:111],a[236:239],v[200:203],v[108:111]\nM v[108:111],a[252:255],v[204:207],v[108:111]\nv_cvt_pk_bf16_f32 v150,v44,v45\nv_cvt_pk_bf16_f32 v151,v46,v47\n" \
"M v[48:51],a[224:227],v[208:211],v[48:51]\nM v[48:51],a[240:243],v[212:215],v[48:51]\nbuffer_load_dwordx4 v246,s[40:43],s69 offen offset:3072 lds\nM v[52:55],a[228:231],v[208:211],v[52:55]\nv_cvt_pk_bf16_f32 v180,v104,v105\nv_cvt_pk_bf16_f32 v181,v106,v107\nM v[52:55],a[244:247],v[212:215],v[52:55]\nv_cvt_pk_bf16_f32 v182,v108,v109\n" \
"v_cvt_pk_bf16_f32 v183,v110,v111\nM v[112:115],a[232:235],v[208:211],v[112:115]\ns_waitcnt vmcnt(19) lgkmcnt(0)\ns_barrier\nM v[112:115],a[248:251],v[212:215],v[112:115]\nds_read_b64_tr_b16 v[192:193],v241 offset:0\nds_read_b64_tr_b16 v[194:195],v241 offset:256\nM v[116:119],a[236:239],v[208:211],v[116:119]\n" \
"ds_read_b64_tr_b16 v[196:197],v241 offset:8192\nds_read_b64_tr_b16 v[198:199],v241 offset:8448\nM v[116:119],a[252:255],v[212:215],v[116:119]\nds_read_b64_tr_b16 v[200:201],v242 offset:0\nds_read_b64_tr_b16 v[202:203],v242 offset:256\nM v[56:59],a[224:227],v[216:219],v[56:59]\nds_read_b64_tr_b16 v[204:205],v242 offset:8192\nds_read_b64_tr_b16 v[206:207],v242 offset:8448\n" \
"M v[56:59],a[240:243],v[220:223],v[56:59]\nds_read_b64_tr_b16 v[208:209],v241 offset:512\nds_read_b64_tr_b16 v[210:211],v241 offset:768\nM v[60:63],a[228:231],v[216:219],v[60:63]\nds_read_b64_tr_b16 v[212:213],v241 offset:8704\nds_read_b64_tr_b16 v[214:215],v241 offset:8960\nM v[60:63],a[244:247],v[220:223],v[60:63]\nv_cvt_pk_bf16_f32 v152,v48,v49\n" \
"v_cvt_pk_bf16_f32 v153,v50,v51\nM v[120:123],a[232:235],v[216:219],v[120:123]\nv_cvt_pk_bf16_f32 v154,v52,v53\nv_cvt_pk_bf16_f32 v155,v54,v55\nM v[120:123],a[248:251],v[220:223],v[120:123]\nv_cvt_pk_bf16_f32 v184,v112,v113\nv_cvt_pk_bf16_f32 v185,v114,v115\nM v[124:127],a[236:239],v[216:219],v[124:127]\n" \
"ds_read_b64_tr_b16 v[216:217],v242 offset:512\nds_read_b64_tr_b16 v[218:219],v242 offset:768\nM v[124:127],a[252:255],v[220:223],v[124:127]\nds_read_b64_tr_b16 v[220:221],v242 offset:8704\ns_waitcnt lgkmcnt(13)\nM v[0:3],v[192:195],a[0:3],0\nds_read_b64_tr_b16 v[222:223],v242 offset:8960\ns_waitcnt lgkmcnt(12)\n" \
"M v[0:3],v[196:199],a[16:19],v[0:3]\nv_cvt_pk_bf16_f32 v186,v116,v117\nv_cvt_pk_bf16_f32 v187,v118,v119\nM v[4:7],v[192:195],a[4:7],0\nv_cvt_pk_bf16_f32 v156,v56,v57\nv_cvt_pk_bf16_f32 v157,v58,v59\nM v[4:7],v[196:199],a[20:23],v[4:7]\nv_cvt_pk_bf16_f32 v158,v60,v61\n" \
"v_cvt_pk_bf16_f32 v159,v62,v63\nM v[64:67],v[192:195],a[8:11],0\nv_cvt_pk_bf16_f32 v188,v120,v121\nv_cvt_pk_bf16_f32 v189,v122,v123\nM v[64:67],v[196:199],a[24:27],v[64:67]\nv_cvt_pk_bf16_f32 v190,v124,v125\nv_cvt_pk_bf16_f32 v191,v126,v127\nM v[68:71],v[192:195],a[12:15],0\n" \
"ds_read_b64_tr_b16 v[192:193],v241 offset:1024\nds_read_b64_tr_b16 v[194:195],v241 offset:1280\nM v[68:71],v[196:199],a[28:31],v[68:71]\nds_read_b64_tr_b16 v[196:197],v241 offset:9216\ns_waitcnt lgkmcnt(7)\nM v[8:11],v[200:203],a[0:3],0\nds_read_b64_tr_b16 v[198:199],v241 offset:9472\ns_add_u32 m0,s64,147456\n" \
"M v[8:11],v[204:207],a[16:19],v[8:11]\nbuffer_load_dwordx4 a[96:99],v240,s[52:55],s76 offen offset:0 sc1\nM v[12:15],v[200:203],a[4:7],0\ns_add_u32 s69,s68,1572864\nM v[12:15],v[204:207],a[20:23],v[12:15]\nM v[72:75],v[200:203],a[8:11],0\nM v[72:75],v[204:207],a[24:27],v[72:75]\nbuffer_load_dwordx4 a[112:115],v240,s[52:55],s76 offen offset:2048 sc1\n" \
"M v[76:79],v[200:203],a[12:15],0\nds_read_b64_tr_b16 v[200:201],v242 offset:1024\nds_read_b64_tr_b16 v[202:203],v242 offset:1280\nM v[76:79],v[204:207],a[28:31],v[76:79]\nds_read_b64_tr_b16 v[204:205],v242 offset:9216\nds_read_b64_tr_b16 v[206:207],v242 offset:9472\nM v[16:19],v[208:211],a[0:3],0\nM v[16:19],v[212:215],a[16:19],v[16:19]\n" \
"buffer_load_dwordx4 a[100:103],v240,s[52:55],s76 offen offset:1024 sc1\nM v[20:23],v[208:211],a[4:7],0\nM v[20:23],v[212:215],a[20:23],v[20:23]\nM v[80:83],v[208:211],a[8:11],0\nM v[80:83],v[212:215],a[24:27],v[80:83]\nbuffer_store_dwordx4 v[128:131],v240,s[52:55],s74 offen offset:0 sc1\nM v[84:87],v[208:211],a[12:15],0\nds_read_b64_tr_b16 v[208:209],v241 offset:1536\n" \
"ds_read_b64_tr_b16 v[210:211],v241 offset:1792\nM v[84:87],v[212:215],a[28:31],v[84:87]\nds_read_b64_tr_b16 v[212:213],v241 offset:9728\ns_waitcnt lgkmcnt(7)\nM v[24:27],v[216:219],a[0:3],0\nds_read_b64_tr_b16 v[214:215],v241 offset:9984\nM v[24:27],v[220:223],a[16:19],v[24:27]\nbuffer_load_dwordx4 a[116:119],v240,s[52:55],s76 offen offset:3072 sc1\n" \
"M v[28:31],v[216:219],a[4:7],0\ns_add_u32 s76,s76,4096\nM v[28:31],v[220:223],a[20:23],v[28:31]\nM v[88:91],v[216:219],a[8:11],0\nM v[88:91],v[220:223],a[24:27],v[88:91]\nbuffer_load_dwordx4 a[104:107],v240,s[52:55],s77 offen offset:0 sc1\nM v[92:95],v[216:219],a[12:15],0\nds_read_b64_tr_b16 v[216:217],v242 offset:1536\n" \
"ds_read_b64_tr_b16 v[218:219],v242 offset:1792\nM v[92:95],v[220:223],a[28:31],v[92:95]\nds_read_b64_tr_b16 v[220:221],v242 offset:9728\nds_read_b64_tr_b16 v[222:223],v242 offset:9984\nM v[32:35],v[192:195],a[0:3],0\nM v[32:35],v[196:199],a[16:19],v[32:35]\nbuffer_load_dwordx4 a[120:123],v240,s[52:55],s77 offen offset:2048 sc1\nM v[36:39],v[192:195],a[4:7],0\n" \
"M v[36:39],v[196:199],a[20:23],v[36:39]\nM v[96:99],v[192:195],a[8:11],0\nM v[96:99],v[196:199],a[24:27],v[96:99]\nbuffer_load_dwordx4 a[108:111],v240,s[52:55],s77 offen offset:1024 sc1\nM v[100:103],v[192:195],a[12:15],0\nM v[100:103],v[196:199],a[28:31],v[100:103]\ns_waitcnt lgkmcnt(4)\nM v[40:43],v[200:203],a[0:3],0\n" \
"M v[40:43],v[204:207],a[16:19],v[40:43]\nbuffer_load_dwordx4 a[124:127],v240,s[52:55],s77 offen offset:3072 sc1\nM v[44:47],v[200:203],a[4:7],0\ns_add_u32 s77,s77,4096\nM v[44:47],v[204:207],a[20:23],v[44:47]\nM v[104:107],v[200:203],a[8:11],0\nM v[104:107],v[204:207],a[24:27],v[104:107]\nbuffer_load_dwordx4 v243,s[40:43],s69 offen offset:0 lds\n" \
"M v[108:111],v[200:203],a[12:15],0\nM v[108:111],v[204:207],a[28:31],v[108:111]\nM v[48:51],v[208:211],a[0:3],0\nM v[48:51],v[212:215],a[16:19],v[48:51]\nbuffer_load_dwordx4 v244,s[40:43],s69 offen offset:1024 lds\nM v[52:55],v[208:211],a[4:7],0\nM v[52:55],v[212:215],a[20:23],v[52:55]\nM v[112:115],v[208:211],a[8:11],0\n" \
"s_waitcnt vmcnt(22) lgkmcnt(0)\ns_barrier\nM v[112:115],v[212:215],a[24:27],v[112:115]\nds_read_b64_tr_b16 v[192:193],v241 offset:16384\nds_read_b64_tr_b16 v[194:195],v241 offset:16640\nM v[116:119],v[208:211],a[12:15],0\nds_read_b64_tr_b16 v[196:197],v241 offset:24576\nds_read_b64_tr_b16 v[198:199],v241 offset:24832\n" \
"buffer_store_dwordx4 v[132:135],v240,s[52:55],s74 offen offset:1024 sc1\nM v[116:119],v[212:215],a[28:31],v[116:119]\nds_read_b64_tr_b16 v[200:201],v242 offset:16384\nds_read_b64_tr_b16 v[202:203],v242 offset:16640\nM v[56:59],v[216:219],a[0:3],0\nds_read_b64_tr_b16 v[204:205],v242 offset:24576\nds_read_b64_tr_b16 v[206:207],v242 offset:24832\nM v[56:59],v[220:223],a[16:19],v[56:59]\n" \
"ds_read_b64_tr_b16 v[208:209],v241 offset:16896\nds_read_b64_tr_b16 v[210:211],v241 offset:17152\nM v[60:63],v[216:219],a[4:7],0\nds_read_b64_tr_b16 v[212:213],v241 offset:25088\nds_read_b64_tr_b16 v[214:215],v241 offset:25344\nM v[60:63],v[220:223],a[20:23],v[60:63]\nbuffer_load_dwordx4 v245,s[40:43],s69 offen offset:2048 lds\nM v[120:123],v[216:219],a[8:11],0\n" \
"M v[120:123],v[220:223],a[24:27],v[120:123]\nM v[124:127],v[216:219],a[12:15],0\nds_read_b64_tr_b16 v[216:217],v242 offset:16896\nds_read_b64_tr_b16 v[218:219],v242 offset:17152\nM v[124:127],v[220:223],a[28:31],v[124:127]\nds_read_b64_tr_b16 v[220:221],v242 offset:25088\ns_waitcnt lgkmcnt(13)\nM v[0:3],v[192:195],a[32:35],v[0:3]\n" \
"ds_read_b64_tr_b16 v[222:223],v242 offset:25344\ns_waitcnt lgkmcnt(12)\nM v[0:3],v[196:199],a[48:51],v[0:3]\nbuffer_load_dwordx4 v246,s[40:43],s69 offen offset:3072 lds\nM v[4:7],v[192:195],a[36:39],v[4:7]\ns_add_u32 m0,s64,98304\ns_add_u32 s69,s68,2097152\nM v[4:7],v[196:199],a[52:55],v[4:7]\n" \
"M v[64:67],v[192:195],a[40:43],v[64:67]\nM v[64:67],v[196:199],a[56:59],v[64:67]\nbuffer_load_dwordx4 a[128:131],v240,s[52:55],s76 offen offset:0 sc1\nM v[68:71],v[192:195],a[44:47],v[68:71]\nds_read_b64_tr_b16 v[192:193],v241 offset:17408\nds_read_b64_tr_b16 v[194:195],v241 offset:17664\nM v[68:71],v[196:199],a[60:63],v[68:71]\nds_read_b64_tr_b16 v[196:197],v241 offset:25600\n" \
"s_waitcnt lgkmcnt(7)\nM v[8:11],v[200:203],a[32:35],v[8:11]\nds_read_b64_tr_b16 v[198:199],v241 offset:25856\nM v[8:11],v[204:207],a[48:51],v[8:11]\nbuffer_load_dwordx4 a[144:147],v240,s[52:55],s76 offen offset:2048 sc1\nM v[12:15],v[200:203],a[36:39],v[12:15]\nM v[12:15],v[204:207],a[52:55],v[12:15]\nM v[72:75],v[200:203],a[40:43],v[72:75]\n" \
"M v[72:75],v[204:207],a[56:59],v[72:75]\nbuffer_load_dwordx4 a[132:135],v240,s[52:55],s76 offen offset:1024 sc1\nM v[76:79],v[200:203],a[44:47],v[76:79]\nds_read_b64_tr_b16 v[200:201],v242 offset:17408\nds_read_b64_tr_b16 v[202:203],v242 offset:17664\nM v[76:79],v[204:207],a[60:63],v[76:79]\nds_read_b64_tr_b16 v[204:205],v242 offset:25600\nds_read_b64_tr_b16 v[206:207],v242 offset:25856\n" \
"M v[16:19],v[208:211],a[32:35],v[16:19]\nM v[16:19],v[212:215],a[48:51],v[16:19]\nbuffer_store_dwordx4 v[136:139],v240,s[52:55],s74 offen offset:2048 sc1\nM v[20:23],v[208:211],a[36:39],v[20:23]\nM v[20:23],v[212:215],a[52:55],v[20:23]\nM v[80:83],v[208:211],a[40:43],v[80:83]\nM v[80:83],v[212:215],a[56:59],v[80:83]\nbuffer_load_dwordx4 a[148:151],v240,s[52:55],s76 offen offset:3072 sc1\n" \
"M v[84:87],v[208:211],a[44:47],v[84:87]\nds_read_b64_tr_b16 v[208:209],v241 offset:17920\nds_read_b64_tr_b16 v[210:211],v241 offset:18176\nM v[84:87],v[212:215],a[60:63],v[84:87]\nds_read_b64_tr_b16 v[212:213],v241 offset:26112\ns_waitcnt lgkmcnt(7)\nM v[24:27],v[216:219],a[32:35],v[24:27]\nds_read_b64_tr_b16 v[214:215],v241 offset:26368\n" \
"s_add_u32 s76,s76,4096\nM v[24:27],v[220:223],a[48:51],v[24:27]\nbuffer_load_dwordx4 a[136:139],v240,s[52:55],s77 offen offset:0 sc1\nM v[28:31],v[216:219],a[36:39],v[28:31]\nM v[28:31],v[220:223],a[52:55],v[28:31]\nM v[88:91],v[216:219],a[40:43],v[88:91]\nM v[88:91],v[220:223],a[56:59],v[88:91]\nbuffer_load_dwordx4 a[152:155],v240,s[52:55],s77 offen offset:2048 sc1\n" \
"M v[92:95],v[216:219],a[44:47],v[92:95]\nds_read_b64_tr_b16 v[216:217],v242 offset:17920\nds_read_b64_tr_b16 v[218:219],v242 offset:18176\nM v[92:95],v[220:223],a[60:63],v[92:95]\nds_read_b64_tr_b16 v[220:221],v242 offset:26112\nds_read_b64_tr_b16 v[222:223],v242 offset:26368\nM v[32:35],v[192:195],a[32:35],v[32:35]\nM v[32:35],v[196:199],a[48:51],v[32:35]\n" \
"buffer_load_dwordx4 a[140:143],v240,s[52:55],s77 offen offset:1024 sc1\nM v[36:39],v[192:195],a[36:39],v[36:39]\nM v[36:39],v[196:199],a[52:55],v[36:39]\nM v[96:99],v[192:195],a[40:43],v[96:99]\nM v[96:99],v[196:199],a[56:59],v[96:99]\nbuffer_load_dwordx4 a[156:159],v240,s[52:55],s77 offen offset:3072 sc1\nM v[100:103],v[192:195],a[44:47],v[100:103]\ns_add_u32 s77,s77,4096\n" \
"M v[100:103],v[196:199],a[60:63],v[100:103]\ns_waitcnt lgkmcnt(4)\nM v[40:43],v[200:203],a[32:35],v[40:43]\nM v[40:43],v[204:207],a[48:51],v[40:43]\nbuffer_load_dwordx4 v243,s[40:43],s69 offen offset:0 lds\nM v[44:47],v[200:203],a[36:39],v[44:47]\nM v[44:47],v[204:207],a[52:55],v[44:47]\nM v[104:107],v[200:203],a[40:43],v[104:107]\n" \
"M v[104:107],v[204:207],a[56:59],v[104:107]\nbuffer_load_dwordx4 v244,s[40:43],s69 offen offset:1024 lds\nM v[108:111],v[200:203],a[44:47],v[108:111]\nM v[108:111],v[204:207],a[60:63],v[108:111]\nM v[48:51],v[208:211],a[32:35],v[48:51]\nM v[48:51],v[212:215],a[48:51],v[48:51]\nbuffer_store_dwordx4 v[140:143],v240,s[52:55],s74 offen offset:3072 sc1\nM v[52:55],v[208:211],a[36:39],v[52:55]\n" \
"s_add_u32 s74,s74,4096\nM v[52:55],v[212:215],a[52:55],v[52:55]\nM v[112:115],v[208:211],a[40:43],v[112:115]\ns_waitcnt vmcnt(23) lgkmcnt(0)\ns_barrier\nM v[112:115],v[212:215],a[56:59],v[112:115]\nds_read_b64_tr_b16 v[192:193],v241 offset:32768\nds_read_b64_tr_b16 v[194:195],v241 offset:33024\n" \
"M v[116:119],v[208:211],a[44:47],v[116:119]\nds_read_b64_tr_b16 v[196:197],v241 offset:40960\nds_read_b64_tr_b16 v[198:199],v241 offset:41216\nM v[116:119],v[212:215],a[60:63],v[116:119]\nds_read_b64_tr_b16 v[200:201],v242 offset:32768\nds_read_b64_tr_b16 v[202:203],v242 offset:33024\nM v[56:59],v[216:219],a[32:35],v[56:59]\nds_read_b64_tr_b16 v[204:205],v242 offset:40960\n" \
"ds_read_b64_tr_b16 v[206:207],v242 offset:41216\nM v[56:59],v[220:223],a[48:51],v[56:59]\nds_read_b64_tr_b16 v[208:209],v241 offset:33280\nds_read_b64_tr_b16 v[210:211],v241 offset:33536\nM v[60:63],v[216:219],a[36:39],v[60:63]\nds_read_b64_tr_b16 v[212:213],v241 offset:41472\nds_read_b64_tr_b16 v[214:215],v241 offset:41728\nM v[60:63],v[220:223],a[52:55],v[60:63]\n" \
"buffer_load_dwordx4 v245,s[40:43],s69 offen offset:2048 lds\nM v[120:123],v[216:219],a[40:43],v[120:123]\nM v[120:123],v[220:223],a[56:59],v[120:123]\nM v[124:127],v[216:219],a[44:47],v[124:127]\nds_read_b64_tr_b16 v[216:217],v242 offset:33280\nds_read_b64_tr_b16 v[218:219],v242 offset:33536\nM v[124:127],v[220:223],a[60:63],v[124:127]\nds_read_b64_tr_b16 v[220:221],v242 offset:41472\n" \
"s_waitcnt lgkmcnt(13)\nM v[0:3],v[192:195],a[64:67],v[0:3]\nds_read_b64_tr_b16 v[222:223],v242 offset:41728\ns_waitcnt lgkmcnt(12)\nM v[0:3],v[196:199],a[80:83],v[0:3]\nbuffer_load_dwordx4 v246,s[40:43],s69 offen offset:3072 lds\nM v[4:7],v[192:195],a[68:71],v[4:7]\ns_add_u32 m0,s64,114688\n" \
"s_add_u32 s69,s68,2621440\nM v[4:7],v[196:199],a[84:87],v[4:7]\nM v[64:67],v[192:195],a[72:75],v[64:67]\nM v[64:67],v[196:199],a[88:91],v[64:67]\nbuffer_load_dwordx4 a[160:163],v240,s[52:55],s76 offen offset:0 sc1\nM v[68:71],v[192:195],a[76:79],v[68:71]\nds_read_b64_tr_b16 v[192:193],v241 offset:33792\nds_read_b64_tr_b16 v[194:195],v241 offset:34048\n" \
"M v[68:71],v[196:199],a[92:95],v[68:71]\nds_read_b64_tr_b16 v[196:197],v241 offset:41984\ns_waitcnt lgkmcnt(7)\nM v[8:11],v[200:203],a[64:67],v[8:11]\nds_read_b64_tr_b16 v[198:199],v241 offset:42240\nM v[8:11],v[204:207],a[80:83],v[8:11]\nbuffer_load_dwordx4 a[176:179],v240,s[52:55],s76 offen offset:2048 sc1\nM v[12:15],v[200:203],a[68:71],v[12:15]\n" \
"M v[12:15],v[204:207],a[84:87],v[12:15]\nM v[72:75],v[200:203],a[72:75],v[72:75]\nM v[72:75],v[204:207],a[88:91],v[72:75]\nbuffer_store_dwordx4 v[144:147],v240,s[52:55],s74 offen offset:0 sc1\nM v[76:79],v[200:203],a[76:79],v[76:79]\nds_read_b64_tr_b16 v[200:201],v242 offset:33792\nds_read_b64_tr_b16 v[202:203],v242 offset:34048\nM v[76:79],v[204:207],a[92:95],v[76:79]\n" \
"ds_read_b64_tr_b16 v[204:205],v242 offset:41984\nds_read_b64_tr_b16 v[206:207],v242 offset:42240\nM v[16:19],v[208:211],a[64:67],v[16:19]\nM v[16:19],v[212:215],a[80:83],v[16:19]\nbuffer_load_dwordx4 a[164:167],v240,s[52:55],s76 offen offset:1024 sc1\nM v[20:23],v[208:211],a[68:71],v[20:23]\nM v[20:23],v[212:215],a[84:87],v[20:23]\nM v[80:83],v[208:211],a[72:75],v[80:83]\n" \
"M v[80:83],v[212:215],a[88:91],v[80:83]\nbuffer_load_dwordx4 a[180:183],v240,s[52:55],s76 offen offset:3072 sc1\nM v[84:87],v[208:211],a[76:79],v[84:87]\nds_read_b64_tr_b16 v[208:209],v241 offset:34304\nds_read_b64_tr_b16 v[210:211],v241 offset:34560\nM v[84:87],v[212:215],a[92:95],v[84:87]\nds_read_b64_tr_b16 v[212:213],v241 offset:42496\ns_waitcnt lgkmcnt(7)\n" \
"M v[24:27],v[216:219],a[64:67],v[24:27]\nds_read_b64_tr_b16 v[214:215],v241 offset:42752\ns_add_u32 s76,s76,4096\nM v[24:27],v[220:223],a[80:83],v[24:27]\nbuffer_load_dwordx4 a[168:171],v240,s[52:55],s77 offen offset:0 sc1\nM v[28:31],v[216:219],a[68:71],v[28:31]\nM v[28:31],v[220:223],a[84:87],v[28:31]\nM v[88:91],v[216:219],a[72:75],v[88:91]\n" \
"M v[88:91],v[220:223],a[88:91],v[88:91]\nbuffer_load_dwordx4 a[184:187],v240,s[52:55],s77 offen offset:2048 sc1\nM v[92:95],v[216:219],a[76:79],v[92:95]\nds_read_b64_tr_b16 v[216:217],v242 offset:34304\nds_read_b64_tr_b16 v[218:219],v242 offset:34560\nM v[92:95],v[220:223],a[92:95],v[92:95]\nds_read_b64_tr_b16 v[220:221],v242 offset:42496\nds_read_b64_tr_b16 v[222:223],v242 offset:42752\n" \
"M v[32:35],v[192:195],a[64:67],v[32:35]\nM v[32:35],v[196:199],a[80:83],v[32:35]\nbuffer_load_dwordx4 a[172:175],v240,s[52:55],s77 offen offset:1024 sc1\nM v[36:39],v[192:195],a[68:71],v[36:39]\nM v[36:39],v[196:199],a[84:87],v[36:39]\nM v[96:99],v[192:195],a[72:75],v[96:99]\nM v[96:99],v[196:199],a[88:91],v[96:99]\nbuffer_load_dwordx4 a[188:191],v240,s[52:55],s77 offen offset:3072 sc1\n" \
"M v[100:103],v[192:195],a[76:79],v[100:103]\ns_add_u32 s77,s77,4096\nM v[100:103],v[196:199],a[92:95],v[100:103]\ns_waitcnt lgkmcnt(4)\nM v[40:43],v[200:203],a[64:67],v[40:43]\nM v[40:43],v[204:207],a[80:83],v[40:43]\nbuffer_load_dwordx4 v243,s[40:43],s69 offen offset:0 lds\nM v[44:47],v[200:203],a[68:71],v[44:47]\n" \
"M v[44:47],v[204:207],a[84:87],v[44:47]\nM v[104:107],v[200:203],a[72:75],v[104:107]\nM v[104:107],v[204:207],a[88:91],v[104:107]\nbuffer_store_dwordx4 v[148:151],v240,s[52:55],s74 offen offset:1024 sc1\nM v[108:111],v[200:203],a[76:79],v[108:111]\nM v[108:111],v[204:207],a[92:95],v[108:111]\nM v[48:51],v[208:211],a[64:67],v[48:51]\nM v[48:51],v[212:215],a[80:83],v[48:51]\n" \
"buffer_load_dwordx4 v244,s[40:43],s69 offen offset:1024 lds\nM v[52:55],v[208:211],a[68:71],v[52:55]\nM v[52:55],v[212:215],a[84:87],v[52:55]\nM v[112:115],v[208:211],a[72:75],v[112:115]\ns_waitcnt vmcnt(23) lgkmcnt(0)\ns_barrier\nM v[112:115],v[212:215],a[88:91],v[112:115]\nds_read_b64_tr_b16 v[192:193],v241 offset:49152\n" \
"ds_read_b64_tr_b16 v[194:195],v241 offset:49408\nM v[116:119],v[208:211],a[76:79],v[116:119]\nds_read_b64_tr_b16 v[196:197],v241 offset:57344\nds_read_b64_tr_b16 v[198:199],v241 offset:57600\nM v[116:119],v[212:215],a[92:95],v[116:119]\nds_read_b64_tr_b16 v[200:201],v242 offset:49152\nds_read_b64_tr_b16 v[202:203],v242 offset:49408\nM v[56:59],v[216:219],a[64:67],v[56:59]\n" \
"ds_read_b64_tr_b16 v[204:205],v242 offset:57344\nds_read_b64_tr_b16 v[206:207],v242 offset:57600\nM v[56:59],v[220:223],a[80:83],v[56:59]\nds_read_b64_tr_b16 v[208:209],v241 offset:49664\nds_read_b64_tr_b16 v[210:211],v241 offset:49920\nM v[60:63],v[216:219],a[68:71],v[60:63]\nds_read_b64_tr_b16 v[212:213],v241 offset:57856\nds_read_b64_tr_b16 v[214:215],v241 offset:58112\n" \
"M v[60:63],v[220:223],a[84:87],v[60:63]\nbuffer_load_dwordx4 v245,s[40:43],s69 offen offset:2048 lds\nM v[120:123],v[216:219],a[72:75],v[120:123]\nM v[120:123],v[220:223],a[88:91],v[120:123]\nM v[124:127],v[216:219],a[76:79],v[124:127]\nds_read_b64_tr_b16 v[216:217],v242 offset:49664\nds_read_b64_tr_b16 v[218:219],v242 offset:49920\nM v[124:127],v[220:223],a[92:95],v[124:127]\n" \
"ds_read_b64_tr_b16 v[220:221],v242 offset:57856\ns_waitcnt lgkmcnt(13)\nM v[0:3],v[192:195],a[96:99],v[0:3]\nds_read_b64_tr_b16 v[222:223],v242 offset:58112\ns_waitcnt lgkmcnt(12)\nM v[0:3],v[196:199],a[112:115],v[0:3]\nbuffer_load_dwordx4 v246,s[40:43],s69 offen offset:3072 lds\nM v[4:7],v[192:195],a[100:103],v[4:7]\n" \
"s_add_u32 m0,s64,131072\ns_add_u32 s69,s68,3145728\nM v[4:7],v[196:199],a[116:119],v[4:7]\nM v[64:67],v[192:195],a[104:107],v[64:67]\nM v[64:67],v[196:199],a[120:123],v[64:67]\nbuffer_load_dwordx4 a[192:195],v240,s[52:55],s76 offen offset:0 sc1\nM v[68:71],v[192:195],a[108:111],v[68:71]\nds_read_b64_tr_b16 v[192:193],v241 offset:50176\n" \
"ds_read_b64_tr_b16 v[194:195],v241 offset:50432\nM v[68:71],v[196:199],a[124:127],v[68:71]\nds_read_b64_tr_b16 v[196:197],v241 offset:58368\ns_waitcnt lgkmcnt(7)\nM v[8:11],v[200:203],a[96:99],v[8:11]\nds_read_b64_tr_b16 v[198:199],v241 offset:58624\nM v[8:11],v[204:207],a[112:115],v[8:11]\nbuffer_store_dwordx4 v[152:155],v240,s[52:55],s74 offen offset:2048 sc1\n" \
"M v[12:15],v[200:203],a[100:103],v[12:15]\nM v[12:15],v[204:207],a[116:119],v[12:15]\nM v[72:75],v[200:203],a[104:107],v[72:75]\nM v[72:75],v[204:207],a[120:123],v[72:75]\nbuffer_load_dwordx4 a[208:211],v240,s[52:55],s76 offen offset:2048 sc1\nM v[76:79],v[200:203],a[108:111],v[76:79]\nds_read_b64_tr_b16 v[200:201],v242 offset:50176\nds_read_b64_tr_b16 v[202:203],v242 offset:50432\n" \
"M v[76:79],v[204:207],a[124:127],v[76:79]\nds_read_b64_tr_b16 v[204:205],v242 offset:58368\nds_read_b64_tr_b16 v[206:207],v242 offset:58624\nM v[16:19],v[208:211],a[96:99],v[16:19]\nM v[16:19],v[212:215],a[112:115],v[16:19]\nbuffer_load_dwordx4 a[196:199],v240,s[52:55],s76 offen offset:1024 sc1\nM v[20:23],v[208:211],a[100:103],v[20:23]\nM v[20:23],v[212:215],a[116:119],v[20:23]\n" \
"M v[80:83],v[208:211],a[104:107],v[80:83]\nM v[80:83],v[212:215],a[120:123],v[80:83]\nbuffer_load_dwordx4 a[212:215],v240,s[52:55],s76 offen offset:3072 sc1\nM v[84:87],v[208:211],a[108:111],v[84:87]\nds_read_b64_tr_b16 v[208:209],v241 offset:50688\nds_read_b64_tr_b16 v[210:211],v241 offset:50944\nM v[84:87],v[212:215],a[124:127],v[84:87]\nds_read_b64_tr_b16 v[212:213],v241 offset:58880\n" \
"s_waitcnt lgkmcnt(7)\nM v[24:27],v[216:219],a[96:99],v[24:27]\nds_read_b64_tr_b16 v[214:215],v241 offset:59136\ns_add_u32 s76,s76,4096\nM v[24:27],v[220:223],a[112:115],v[24:27]\nbuffer_load_dwordx4 a[200:203],v240,s[52:55],s77 offen offset:0 sc1\nM v[28:31],v[216:219],a[100:103],v[28:31]\nM v[28:31],v[220:223],a[116:119],v[28:31]\n" \
"M v[88:91],v[216:219],a[104:107],v[88:91]\nM v[88:91],v[220:223],a[120:123],v[88:91]\nbuffer_load_dwordx4 a[216:219],v240,s[52:55],s77 offen offset:2048 sc1\nM v[92:95],v[216:219],a[108:111],v[92:95]\nds_read_b64_tr_b16 v[216:217],v242 offset:50688\nds_read_b64_tr_b16 v[218:219],v242 offset:50944\nM v[92:95],v[220:223],a[124:127],v[92:95]\nds_read_b64_tr_b16 v[220:221],v242 offset:58880\n" \
"ds_read_b64_tr_b16 v[222:223],v242 offset:59136\nM v[32:35],v[192:195],a[96:99],v[32:35]\nM v[32:35],v[196:199],a[112:115],v[32:35]\nbuffer_load_dwordx4 a[204:207],v240,s[52:55],s77 offen offset:1024 sc1\nM v[36:39],v[192:195],a[100:103],v[36:39]\nM v[36:39],v[196:199],a[116:119],v[36:39]\nM v[96:99],v[192:195],a[104:107],v[96:99]\nM v[96:99],v[196:199],a[120:123],v[96:99]\n" \
"buffer_load_dwordx4 a[220:223],v240,s[52:55],s77 offen offset:3072 sc1\nM v[100:103],v[192:195],a[108:111],v[100:103]\ns_add_u32 s77,s77,4096\nM v[100:103],v[196:199],a[124:127],v[100:103]\ns_waitcnt lgkmcnt(4)\nM v[40:43],v[200:203],a[96:99],v[40:43]\nM v[40:43],v[204:207],a[112:115],v[40:43]\nbuffer_store_dwordx4 v[156:159],v240,s[52:55],s74 offen offset:3072 sc1\n" \
"M v[44:47],v[200:203],a[100:103],v[44:47]\ns_add_u32 s74,s74,4096\nM v[44:47],v[204:207],a[116:119],v[44:47]\nM v[104:107],v[200:203],a[104:107],v[104:107]\nM v[104:107],v[204:207],a[120:123],v[104:107]\nbuffer_load_dwordx4 v243,s[40:43],s69 offen offset:0 lds\nM v[108:111],v[200:203],a[108:111],v[108:111]\nM v[108:111],v[204:207],a[124:127],v[108:111]\n" \
"M v[48:51],v[208:211],a[96:99],v[48:51]\nM v[48:51],v[212:215],a[112:115],v[48:51]\nbuffer_load_dwordx4 v244,s[40:43],s69 offen offset:1024 lds\nM v[52:55],v[208:211],a[100:103],v[52:55]\nM v[52:55],v[212:215],a[116:119],v[52:55]\nM v[112:115],v[208:211],a[104:107],v[112:115]\ns_waitcnt vmcnt(24) lgkmcnt(0)\ns_barrier\n" \
"M v[112:115],v[212:215],a[120:123],v[112:115]\nds_read_b64_tr_b16 v[192:193],v241 offset:0\nds_read_b64_tr_b16 v[194:195],v241 offset:256\nM v[116:119],v[208:211],a[108:111],v[116:119]\nds_read_b64_tr_b16 v[196:197],v241 offset:8192\nds_read_b64_tr_b16 v[198:199],v241 offset:8448\nM v[116:119],v[212:215],a[124:127],v[116:119]\nds_read_b64_tr_b16 v[200:201],v242 offset:0\n" \
"ds_read_b64_tr_b16 v[202:203],v242 offset:256\nM v[56:59],v[216:219],a[96:99],v[56:59]\nds_read_b64_tr_b16 v[204:205],v242 offset:8192\nds_read_b64_tr_b16 v[206:207],v242 offset:8448\nM v[56:59],v[220:223],a[112:115],v[56:59]\nds_read_b64_tr_b16 v[208:209],v241 offset:512\nds_read_b64_tr_b16 v[210:211],v241 offset:768\nM v[60:63],v[216:219],a[100:103],v[60:63]\n" \
"ds_read_b64_tr_b16 v[212:213],v241 offset:8704\nds_read_b64_tr_b16 v[214:215],v241 offset:8960\nM v[60:63],v[220:223],a[116:119],v[60:63]\nbuffer_load_dwordx4 v245,s[40:43],s69 offen offset:2048 lds\nM v[120:123],v[216:219],a[104:107],v[120:123]\nM v[120:123],v[220:223],a[120:123],v[120:123]\nM v[124:127],v[216:219],a[108:111],v[124:127]\nds_read_b64_tr_b16 v[216:217],v242 offset:512\n" \
"ds_read_b64_tr_b16 v[218:219],v242 offset:768\nM v[124:127],v[220:223],a[124:127],v[124:127]\nds_read_b64_tr_b16 v[220:221],v242 offset:8704\ns_waitcnt lgkmcnt(13)\nM v[0:3],v[192:195],a[128:131],v[0:3]\nds_read_b64_tr_b16 v[222:223],v242 offset:8960\ns_waitcnt lgkmcnt(12)\nM v[0:3],v[196:199],a[144:147],v[0:3]\n" \
"buffer_load_dwordx4 v246,s[40:43],s69 offen offset:3072 lds\nM v[4:7],v[192:195],a[132:135],v[4:7]\ns_add_u32 m0,s64,147456\ns_add_u32 s69,s68,3670016\nM v[4:7],v[196:199],a[148:151],v[4:7]\ns_lshl_b32 s68,s60,19\ns_lshl2_add_u32 s68,s64,s68\nM v[64:67],v[192:195],a[136:139],v[64:67]\n" \
"s_add_u32 s68,s68,s70\nM v[64:67],v[196:199],a[152:155],v[64:67]\nbuffer_store_dwordx4 v[160:163],v240,s[52:55],s74 offen offset:0 sc1\nM v[68:71],v[192:195],a[140:143],v[68:71]\nds_read_b64_tr_b16 v[192:193],v241 offset:1024\nds_read_b64_tr_b16 v[194:195],v241 offset:1280\nM v[68:71],v[196:199],a[156:159],v[68:71]\nds_read_b64_tr_b16 v[196:197],v241 offset:9216\n" \
"s_waitcnt lgkmcnt(7)\nM v[8:11],v[200:203],a[128:131],v[8:11]\nds_read_b64_tr_b16 v[198:199],v241 offset:9472\nM v[8:11],v[204:207],a[144:147],v[8:11]\nbuffer_load_dwordx4 a[224:227],v240,s[52:55],s76 offen offset:0 sc1\nM v[12:15],v[200:203],a[132:135],v[12:15]\nM v[12:15],v[204:207],a[148:151],v[12:15]\nM v[72:75],v[200:203],a[136:139],v[72:75]\n" \
"M v[72:75],v[204:207],a[152:155],v[72:75]\nbuffer_load_dwordx4 a[240:243],v240,s[52:55],s76 offen offset:2048 sc1\nM v[76:79],v[200:203],a[140:143],v[76:79]\nds_read_b64_tr_b16 v[200:201],v242 offset:1024\nds_read_b64_tr_b16 v[202:203],v242 offset:1280\nM v[76:79],v[204:207],a[156:159],v[76:79]\nds_read_b64_tr_b16 v[204:205],v242 offset:9216\nds_read_b64_tr_b16 v[206:207],v242 offset:9472\n" \
"M v[16:19],v[208:211],a[128:131],v[16:19]\nM v[16:19],v[212:215],a[144:147],v[16:19]\nbuffer_load_dwordx4 a[228:231],v240,s[52:55],s76 offen offset:1024 sc1\nM v[20:23],v[208:211],a[132:135],v[20:23]\nM v[20:23],v[212:215],a[148:151],v[20:23]\nM v[80:83],v[208:211],a[136:139],v[80:83]\nds_read_b128 a[0:3],v240 offset:0\nM v[80:83],v[212:215],a[152:155],v[80:83]\n" \
"buffer_load_dword v248,v228,s[52:55],s80 offen sc1\nM v[84:87],v[208:211],a[140:143],v[84:87]\nds_read_b64_tr_b16 v[208:209],v241 offset:1536\nds_read_b64_tr_b16 v[210:211],v241 offset:1792\nM v[84:87],v[212:215],a[156:159],v[84:87]\nds_read_b64_tr_b16 v[212:213],v241 offset:9728\ns_waitcnt lgkmcnt(8)\nM v[24:27],v[216:219],a[128:131],v[24:27]\n" \
"buffer_load_dwordx4 a[244:247],v240,s[52:55],s76 offen offset:3072 sc1\nM v[24:27],v[220:223],a[144:147],v[24:27]\nds_read_b64_tr_b16 v[214:215],v241 offset:9984\nM v[28:31],v[216:219],a[132:135],v[28:31]\nds_read_b128 a[4:7],v240 offset:1024\nM v[28:31],v[220:223],a[148:151],v[28:31]\nM v[88:91],v[216:219],a[136:139],v[88:91]\nbuffer_load_dwordx4 a[232:235],v240,s[52:55],s77 offen offset:0 sc1\n" \
"M v[88:91],v[220:223],a[152:155],v[88:91]\nM v[92:95],v[216:219],a[140:143],v[92:95]\nds_read_b64_tr_b16 v[216:217],v242 offset:1536\nds_read_b64_tr_b16 v[218:219],v242 offset:1792\nM v[92:95],v[220:223],a[156:159],v[92:95]\nds_read_b64_tr_b16 v[220:221],v242 offset:9728\nds_read_b64_tr_b16 v[222:223],v242 offset:9984\nM v[32:35],v[192:195],a[128:131],v[32:35]\n" \
"buffer_load_dwordx4 a[248:251],v240,s[52:55],s77 offen offset:2048 sc1\nM v[32:35],v[196:199],a[144:147],v[32:35]\nds_read_b128 a[8:11],v240 offset:2048\nM v[36:39],v[192:195],a[132:135],v[36:39]\nM v[36:39],v[196:199],a[148:151],v[36:39]\nM v[96:99],v[192:195],a[136:139],v[96:99]\nbuffer_store_dwordx4 v[164:167],v240,s[52:55],s74 offen offset:1024 sc1\nM v[96:99],v[196:199],a[152:155],v[96:99]\n" \
"M v[100:103],v[192:195],a[140:143],v[100:103]\nds_read_b128 a[12:15],v240 offset:3072\nM v[100:103],v[196:199],a[156:159],v[100:103]\ns_waitcnt lgkmcnt(7)\nM v[40:43],v[200:203],a[128:131],v[40:43]\nbuffer_load_dwordx4 a[236:239],v240,s[52:55],s77 offen offset:1024 sc1\nM v[40:43],v[204:207],a[144:147],v[40:43]\nM v[44:47],v[200:203],a[132:135],v[44:47]\n" \
"M v[44:47],v[204:207],a[148:151],v[44:47]\nM v[104:107],v[200:203],a[136:139],v[104:107]\nbuffer_load_dwordx4 a[252:255],v240,s[52:55],s77 offen offset:3072 sc1\nM v[104:107],v[204:207],a[152:155],v[104:107]\nds_read_b128 a[16:19],v240 offset:4096\nM v[108:111],v[200:203],a[140:143],v[108:111]\nM v[108:111],v[204:207],a[156:159],v[108:111]\nM v[48:51],v[208:211],a[128:131],v[48:51]\n" \
"buffer_load_dwordx4 v243,s[40:43],s69 offen offset:0 lds\nM v[48:51],v[212:215],a[144:147],v[48:51]\nM v[52:55],v[208:211],a[132:135],v[52:55]\nds_read_b128 a[20:23],v240 offset:5120\nM v[52:55],v[212:215],a[148:151],v[52:55]\nM v[112:115],v[208:211],a[136:139],v[112:115]\ns_waitcnt vmcnt(25) lgkmcnt(3)\ns_barrier\n" \
"M v[112:115],v[212:215],a[152:155],v[112:115]\nds_read_b64_tr_b16 v[192:193],v241 offset:16384\nds_read_b64_tr_b16 v[194:195],v241 offset:16640\nM v[116:119],v[208:211],a[140:143],v[116:119]\nds_read_b64_tr_b16 v[196:197],v241 offset:24576\nds_read_b64_tr_b16 v[198:199],v241 offset:24832\nM v[116:119],v[212:215],a[156:159],v[116:119]\nds_read_b64_tr_b16 v[200:201],v242 offset:16384\n" \
"ds_read_b64_tr_b16 v[202:203],v242 offset:16640\nM v[56:59],v[216:219],a[128:131],v[56:59]\nds_read_b64_tr_b16 v[204:205],v242 offset:24576\nds_read_b64_tr_b16 v[206:207],v242 offset:24832\nM v[56:59],v[220:223],a[144:147],v[56:59]\nds_read_b64_tr_b16 v[208:209],v241 offset:16896\nds_read_b64_tr_b16 v[210:211],v241 offset:17152\nM v[60:63],v[216:219],a[132:135],v[60:63]\n" \
"ds_read_b64_tr_b16 v[212:213],v241 offset:25088\nds_read_b64_tr_b16 v[214:215],v241 offset:25344\nM v[60:63],v[220:223],a[148:151],v[60:63]\nbuffer_load_dwordx4 v244,s[40:43],s69 offen offset:1024 lds\nM v[120:123],v[216:219],a[136:139],v[120:123]\nds_read_b128 a[24:27],v240 offset:6144\nM v[120:123],v[220:223],a[152:155],v[120:123]\nM v[124:127],v[216:219],a[140:143],v[124:127]\n" \
"ds_read_b64_tr_b16 v[216:217],v242 offset:16896\nds_read_b64_tr_b16 v[218:219],v242 offset:17152\nM v[124:127],v[220:223],a[156:159],v[124:127]\nds_read_b64_tr_b16 v[220:221],v242 offset:25088\ns_waitcnt lgkmcnt(14)\nM v[0:3],v[192:195],a[160:163],v[0:3]\nds_read_b64_tr_b16 v[222:223],v242 offset:25344\ns_waitcnt lgkmcnt(13)\n" \
"M v[0:3],v[196:199],a[176:179],v[0:3]\nbuffer_store_dwordx4 v[168:171],v240,s[52:55],s74 offen offset:2048 sc1\nM v[4:7],v[192:195],a[164:167],v[4:7]\nds_read_b128 a[28:31],v240 offset:7168\nM v[4:7],v[196:199],a[180:183],v[4:7]\nM v[64:67],v[192:195],a[168:171],v[64:67]\nds_read_b128 a[32:35],v240 offset:8192\nM v[64:67],v[196:199],a[184:187],v[64:67]\n" \
"buffer_load_dwordx4 v245,s[40:43],s69 offen offset:2048 lds\nM v[68:71],v[192:195],a[172:175],v[68:71]\nds_read_b64_tr_b16 v[192:193],v241 offset:17408\nds_read_b64_tr_b16 v[194:195],v241 offset:17664\nM v[68:71],v[196:199],a[188:191],v[68:71]\nds_read_b64_tr_b16 v[196:197],v241 offset:25600\ns_waitcnt lgkmcnt(10)\nM v[8:11],v[200:203],a[160:163],v[8:11]\n" \
"ds_read_b64_tr_b16 v[198:199],v241 offset:25856\nM v[8:11],v[204:207],a[176:179],v[8:11]\nbuffer_load_dwordx4 v246,s[40:43],s69 offen offset:3072 lds\nM v[12:15],v[200:203],a[164:167],v[12:15]\ns_sub_u32 s40,s18,0x1000\ns_subb_u32 s41,s19,0\nM v[12:15],v[204:207],a[180:183],v[12:15]\nds_read_b128 a[36:39],v240 offset:9216\n" \
"M v[72:75],v[200:203],a[168:171],v[72:75]\nM v[72:75],v[204:207],a[184:187],v[72:75]\nM v[76:79],v[200:203],a[172:175],v[76:79]\nds_read_b64_tr_b16 v[200:201],v242 offset:17408\nds_read_b64_tr_b16 v[202:203],v242 offset:17664\nM v[76:79],v[204:207],a[188:191],v[76:79]\nds_read_b64_tr_b16 v[204:205],v242 offset:25600\nds_read_b64_tr_b16 v[206:207],v242 offset:25856\n" \
"M v[16:19],v[208:211],a[160:163],v[16:19]\nds_read_b128 a[40:43],v240 offset:10240\nM v[16:19],v[212:215],a[176:179],v[16:19]\ns_waitcnt vmcnt(11)\nv_cmp_eq_u32 vcc,0,v248\nM v[20:23],v[208:211],a[164:167],v[20:23]\ns_cbranch_vccnz .Lps1\n.Lpb1:\n" \
"s_add_u32 s80,s80,4096\nM v[20:23],v[212:215],a[180:183],v[20:23]\ns_cmp_ge_u32 s62,6\ns_cselect_b64 vcc,-1,0\nM v[80:83],v[208:211],a[168:171],v[80:83]\nv_cndmask_b32 v254,v231,v229,vcc\nv_and_b32 v231,0x70,v243\nM v[80:83],v[212:215],a[184:187],v[80:83]\n" \
"s_cmp_eq_u32 s62,3\ns_cselect_b64 vcc,-1,0\nM v[84:87],v[208:211],a[172:175],v[84:87]\nds_read_b64_tr_b16 v[208:209],v241 offset:17920\nds_read_b64_tr_b16 v[210:211],v241 offset:18176\nM v[84:87],v[212:215],a[188:191],v[84:87]\nds_read_b64_tr_b16 v[212:213],v241 offset:26112\ns_waitcnt lgkmcnt(8)\n" \
"M v[24:27],v[216:219],a[160:163],v[24:27]\nds_read_b64_tr_b16 v[214:215],v241 offset:26368\nv_cndmask_b32 v254,v254,v230,vcc\nM v[24:27],v[220:223],a[176:179],v[24:27]\nv_bfe_u32 v230,v249,2,3\nv_lshl_or_b32 v230,v230,10,v231\nM v[28:31],v[216:219],a[164:167],v[28:31]\nv_and_b32 v231,16,v249\n" \
"v_lshlrev_b32 v231,1,v231\nM v[28:31],v[220:223],a[180:183],v[28:31]\nv_xor_b32 v230,v230,v231\nv_add_u32 v243,4096,v230\nM v[88:91],v[216:219],a[168:171],v[88:91]\nv_add_u32 v244,3200,v230\nv_add_u32 v245,10240,v230\nM v[88:91],v[220:223],a[184:187],v[88:91]\n" \
"v_add_u32 v246,9344,v230\nv_and_b32 v229,3,v249\nM v[92:95],v[216:219],a[172:175],v[92:95]\nbuffer_store_dwordx4 v[172:175],v240,s[52:55],s74 offen offset:3072 sc1\nM v[92:95],v[220:223],a[188:191],v[92:95]\nds_read_b64_tr_b16 v[216:217],v242 offset:17920\nds_read_b64_tr_b16 v[218:219],v242 offset:18176\nM v[32:35],v[192:195],a[160:163],v[32:35]\n" \
"ds_read_b64_tr_b16 v[220:221],v242 offset:26112\nds_read_b64_tr_b16 v[222:223],v242 offset:26368\nM v[32:35],v[196:199],a[176:179],v[32:35]\ns_add_u32 s74,s74,4096\nv_cmp_eq_u32 vcc,0,v229\nM v[36:39],v[192:195],a[164:167],v[36:39]\nv_mov_b32 v230,0x3f803f80\nv_cndmask_b32 v229,0,v230,vcc\n" \
"M v[36:39],v[196:199],a[180:183],v[36:39]\nv_and_b32 v230,0xff,v241\nv_and_b32 v231,16,v249\nM v[96:99],v[192:195],a[168:171],v[96:99]\nv_lshl_or_b32 v230,v231,4,v230\nv_and_b32 v231,32,v249\nM v[96:99],v[196:199],a[184:187],v[96:99]\nds_read_b128 a[44:47],v240 offset:11264\n" \
"ds_read_b128 a[48:51],v240 offset:12288\nM v[100:103],v[192:195],a[172:175],v[100:103]\nds_read_b128 a[52:55],v240 offset:13312\nM v[100:103],v[196:199],a[188:191],v[100:103]\ns_waitcnt lgkmcnt(7)\nM v[40:43],v[200:203],a[160:163],v[40:43]\nds_read_b128 a[56:59],v240 offset:14336\nM v[40:43],v[204:207],a[176:179],v[40:43]\n" \
"M v[44:47],v[200:203],a[164:167],v[44:47]\nM v[44:47],v[204:207],a[180:183],v[44:47]\nM v[104:107],v[200:203],a[168:171],v[104:107]\nM v[104:107],v[204:207],a[184:187],v[104:107]\nM v[108:111],v[200:203],a[172:175],v[108:111]\nds_read_b128 a[60:63],v240 offset:15360\nM v[108:111],v[204:207],a[188:191],v[108:111]\nM v[48:51],v[208:211],a[160:163],v[48:51]\n" \
"M v[48:51],v[212:215],a[176:179],v[48:51]\nM v[52:55],v[208:211],a[164:167],v[52:55]\nM v[52:55],v[212:215],a[180:183],v[52:55]\nM v[112:115],v[208:211],a[168:171],v[112:115]\ns_waitcnt lgkmcnt(5)\ns_barrier\nM v[112:115],v[212:215],a[184:187],v[112:115]\nds_read_b64_tr_b16 v[192:193],v241 offset:32768\n" \
"ds_read_b64_tr_b16 v[194:195],v241 offset:33024\nM v[116:119],v[208:211],a[172:175],v[116:119]\nds_read_b64_tr_b16 v[196:197],v241 offset:40960\nds_read_b64_tr_b16 v[198:199],v241 offset:41216\nM v[116:119],v[212:215],a[188:191],v[116:119]\nds_read_b64_tr_b16 v[200:201],v242 offset:32768\nds_read_b64_tr_b16 v[202:203],v242 offset:33024\nM v[56:59],v[216:219],a[160:163],v[56:59]\n" \
"ds_read_b64_tr_b16 v[204:205],v242 offset:40960\nds_read_b64_tr_b16 v[206:207],v242 offset:41216\nM v[56:59],v[220:223],a[176:179],v[56:59]\nds_read_b64_tr_b16 v[208:209],v241 offset:33280\nds_read_b64_tr_b16 v[210:211],v241 offset:33536\nM v[60:63],v[216:219],a[164:167],v[60:63]\nds_read_b64_tr_b16 v[212:213],v241 offset:41472\nds_read_b64_tr_b16 v[214:215],v241 offset:41728\n" \
"M v[60:63],v[220:223],a[180:183],v[60:63]\nds_write_b64 v250,v[128:129] offset:0\nM v[120:123],v[216:219],a[168:171],v[120:123]\nbuffer_store_dwordx4 v[176:179],v240,s[52:55],s74 offen offset:0 sc1\nM v[120:123],v[220:223],a[184:187],v[120:123]\nds_write_b64 v250,v[130:131] offset:8\nM v[124:127],v[216:219],a[172:175],v[124:127]\nds_read_b64_tr_b16 v[216:217],v242 offset:33280\n" \
"ds_read_b64_tr_b16 v[218:219],v242 offset:33536\nM v[124:127],v[220:223],a[188:191],v[124:127]\nds_read_b64_tr_b16 v[220:221],v242 offset:41472\ns_waitcnt lgkmcnt(15)\nM v[0:3],v[192:195],a[192:195],v[0:3]\nds_read_b64_tr_b16 v[222:223],v242 offset:41728\ns_waitcnt lgkmcnt(14)\nM v[0:3],v[196:199],a[208:211],v[0:3]\n" \
"ds_write_b64 v250,v[132:133] offset:1024\nM v[4:7],v[192:195],a[196:199],v[4:7]\nds_write_b64 v250,v[134:135] offset:1032\nM v[4:7],v[196:199],a[212:215],v[4:7]\nds_write_b64 v250,v[136:137] offset:2048\nM v[64:67],v[192:195],a[200:203],v[64:67]\nM v[64:67],v[196:199],a[216:219],v[64:67]\nds_write_b64 v250,v[138:139] offset:2056\n" \
"M v[68:71],v[192:195],a[204:207],v[68:71]\nds_read_b64_tr_b16 v[192:193],v241 offset:33792\nds_read_b64_tr_b16 v[194:195],v241 offset:34048\nM v[68:71],v[196:199],a[220:223],v[68:71]\nds_read_b64_tr_b16 v[196:197],v241 offset:41984\ns_waitcnt lgkmcnt(12)\nM v[8:11],v[200:203],a[192:195],v[8:11]\nds_write_b64 v250,v[140:141] offset:3072\n" \
"ds_read_b64_tr_b16 v[198:199],v241 offset:42240\nM v[8:11],v[204:207],a[208:211],v[8:11]\nM v[12:15],v[200:203],a[196:199],v[12:15]\nM v[12:15],v[204:207],a[212:215],v[12:15]\nds_write_b64 v250,v[142:143] offset:3080\nM v[72:75],v[200:203],a[200:203],v[72:75]\nM v[72:75],v[204:207],a[216:219],v[72:75]\nM v[76:79],v[200:203],a[204:207],v[76:79]\n" \
"ds_read_b64_tr_b16 v[200:201],v242 offset:33792\nds_read_b64_tr_b16 v[202:203],v242 offset:34048\nM v[76:79],v[204:207],a[220:223],v[76:79]\nds_read_b64_tr_b16 v[204:205],v242 offset:41984\nds_read_b64_tr_b16 v[206:207],v242 offset:42240\nM v[16:19],v[208:211],a[192:195],v[16:19]\nds_write_b64 v250,v[144:145] offset:4096\nM v[16:19],v[212:215],a[208:211],v[16:19]\n" \
"ds_write_b64 v250,v[146:147] offset:4104\nM v[20:23],v[208:211],a[196:199],v[20:23]\nM v[20:23],v[212:215],a[212:215],v[20:23]\nM v[80:83],v[208:211],a[200:203],v[80:83]\nds_write_b64 v250,v[148:149] offset:5120\nM v[80:83],v[212:215],a[216:219],v[80:83]\nM v[84:87],v[208:211],a[204:207],v[84:87]\nds_read_b64_tr_b16 v[208:209],v241 offset:34304\n" \
"ds_read_b64_tr_b16 v[210:211],v241 offset:34560\nM v[84:87],v[212:215],a[220:223],v[84:87]\nds_read_b64_tr_b16 v[212:213],v241 offset:42496\ns_waitcnt lgkmcnt(11)\nM v[24:27],v[216:219],a[192:195],v[24:27]\nds_write_b64 v250,v[150:151] offset:5128\nds_read_b64_tr_b16 v[214:215],v241 offset:42752\nM v[24:27],v[220:223],a[208:211],v[24:27]\n" \
"M v[28:31],v[216:219],a[196:199],v[28:31]\nbuffer_store_dwordx4 v[180:183],v240,s[52:55],s74 offen offset:1024 sc1\nM v[28:31],v[220:223],a[212:215],v[28:31]\nds_write_b64 v250,v[152:153] offset:6144\nM v[88:91],v[216:219],a[200:203],v[88:91]\nM v[88:91],v[220:223],a[216:219],v[88:91]\nds_write_b64 v250,v[154:155] offset:6152\nM v[92:95],v[216:219],a[204:207],v[92:95]\n" \
"ds_read_b64_tr_b16 v[216:217],v242 offset:34304\nds_read_b64_tr_b16 v[218:219],v242 offset:34560\nM v[92:95],v[220:223],a[220:223],v[92:95]\nds_read_b64_tr_b16 v[220:221],v242 offset:42496\nds_read_b64_tr_b16 v[222:223],v242 offset:42752\nM v[32:35],v[192:195],a[192:195],v[32:35]\nds_write_b64 v250,v[156:157] offset:7168\nM v[32:35],v[196:199],a[208:211],v[32:35]\n" \
"M v[36:39],v[192:195],a[196:199],v[36:39]\nM v[36:39],v[196:199],a[212:215],v[36:39]\nds_write_b64 v250,v[158:159] offset:7176\nM v[96:99],v[192:195],a[200:203],v[96:99]\nM v[96:99],v[196:199],a[216:219],v[96:99]\nM v[100:103],v[192:195],a[204:207],v[100:103]\nds_write_b64 v250,v[160:161] offset:8192\nM v[100:103],v[196:199],a[220:223],v[100:103]\n" \
"s_waitcnt lgkmcnt(9)\nM v[40:43],v[200:203],a[192:195],v[40:43]\nM v[40:43],v[204:207],a[208:211],v[40:43]\nds_write_b64 v250,v[162:163] offset:8200\nM v[44:47],v[200:203],a[196:199],v[44:47]\nM v[44:47],v[204:207],a[212:215],v[44:47]\nM v[104:107],v[200:203],a[200:203],v[104:107]\nds_write_b64 v250,v[164:165] offset:9216\n" \
"M v[104:107],v[204:207],a[216:219],v[104:107]\nM v[108:111],v[200:203],a[204:207],v[108:111]\nM v[108:111],v[204:207],a[220:223],v[108:111]\nds_write_b64 v250,v[166:167] offset:9224\nM v[48:51],v[208:211],a[192:195],v[48:51]\nM v[48:51],v[212:215],a[208:211],v[48:51]\nM v[52:55],v[208:211],a[196:199],v[52:55]\nds_write_b64 v250,v[168:169] offset:10240\n" \
"M v[52:55],v[212:215],a[212:215],v[52:55]\nM v[112:115],v[208:211],a[200:203],v[112:115]\ns_waitcnt vmcnt(3) lgkmcnt(5)\ns_barrier\nM v[112:115],v[212:215],a[216:219],v[112:115]\nds_read_b64_tr_b16 v[192:193],v241 offset:49152\nds_read_b64_tr_b16 v[194:195],v241 offset:49408\nM v[116:119],v[208:211],a[204:207],v[116:119]\n" \
"ds_read_b64_tr_b16 v[196:197],v241 offset:57344\nds_read_b64_tr_b16 v[198:199],v241 offset:57600\nM v[116:119],v[212:215],a[220:223],v[116:119]\nds_read_b64_tr_b16 v[200:201],v242 offset:49152\nds_read_b64_tr_b16 v[202:203],v242 offset:49408\nM v[56:59],v[216:219],a[192:195],v[56:59]\nbuffer_store_dwordx4 v[184:187],v240,s[52:55],s74 offen offset:2048 sc1\nM v[56:59],v[220:223],a[208:211],v[56:59]\n" \
"ds_read_b64_tr_b16 v[204:205],v242 offset:57344\nds_read_b64_tr_b16 v[206:207],v242 offset:57600\nM v[60:63],v[216:219],a[196:199],v[60:63]\nds_read_b64_tr_b16 v[208:209],v241 offset:49664\nds_read_b64_tr_b16 v[210:211],v241 offset:49920\nM v[60:63],v[220:223],a[212:215],v[60:63]\nds_read_b64_tr_b16 v[212:213],v241 offset:57856\nds_read_b64_tr_b16 v[214:215],v241 offset:58112\n" \
"M v[120:123],v[216:219],a[200:203],v[120:123]\nds_write_b64 v250,v[170:171] offset:10248\nv_accvgpr_write_b32 a208,v229\nM v[120:123],v[220:223],a[216:219],v[120:123]\nds_write_b64 v250,v[172:173] offset:11264\nv_accvgpr_write_b32 a209,v229\nM v[124:127],v[216:219],a[204:207],v[124:127]\nds_read_b64_tr_b16 v[216:217],v242 offset:49664\n" \
"ds_read_b64_tr_b16 v[218:219],v242 offset:49920\nM v[124:127],v[220:223],a[220:223],v[124:127]\nds_read_b64_tr_b16 v[220:221],v242 offset:57856\ns_waitcnt lgkmcnt(15)\nM v[0:3],v[192:195],a[224:227],v[0:3]\nds_read_b64_tr_b16 v[222:223],v242 offset:58112\ns_waitcnt lgkmcnt(14)\nM v[0:3],v[196:199],a[240:243],v[0:3]\n" \
"ds_write_b64 v250,v[174:175] offset:11272\nv_accvgpr_write_b32 a210,v229\nM v[4:7],v[192:195],a[228:231],v[4:7]\nds_write_b64 v250,v[176:177] offset:12288\nv_accvgpr_write_b32 a211,v229\nM v[4:7],v[196:199],a[244:247],v[4:7]\nds_write_b64 v250,v[178:179] offset:12296\nM v[64:67],v[192:195],a[232:235],v[64:67]\n" \
"v_cvt_pk_bf16_f32 v0,v0,v1\nv_cvt_pk_bf16_f32 v1,v2,v3\nM v[64:67],v[196:199],a[248:251],v[64:67]\nds_write_b64 v250,v[180:181] offset:13312\nv_accvgpr_write_b32 a128,v0\nM v[68:71],v[192:195],a[236:239],v[68:71]\nds_read_b64_tr_b16 v[192:193],v241 offset:50176\nds_read_b64_tr_b16 v[194:195],v241 offset:50432\n" \
"M v[68:71],v[196:199],a[252:255],v[68:71]\nds_read_b64_tr_b16 v[196:197],v241 offset:58368\ns_waitcnt lgkmcnt(12)\nM v[8:11],v[200:203],a[224:227],v[8:11]\nds_read_b64_tr_b16 v[198:199],v241 offset:58624\nv_cvt_pk_bf16_f32 v4,v4,v5\nM v[8:11],v[204:207],a[240:243],v[8:11]\nv_cvt_pk_bf16_f32 v64,v64,v65\n" \
"v_cvt_pk_bf16_f32 v5,v6,v7\nM v[12:15],v[200:203],a[228:231],v[12:15]\nv_cvt_pk_bf16_f32 v68,v68,v69\nv_cvt_pk_bf16_f32 v65,v66,v67\nM v[12:15],v[204:207],a[244:247],v[12:15]\nds_write_b64 v250,v[182:183] offset:13320\nv_cvt_pk_bf16_f32 v69,v70,v71\nM v[72:75],v[200:203],a[232:235],v[72:75]\n" \
"ds_write_b64 v250,v[184:185] offset:14336\nv_cvt_pk_bf16_f32 v2,v8,v9\nM v[72:75],v[204:207],a[248:251],v[72:75]\nds_write_b64 v250,v[186:187] offset:14344\nv_cvt_pk_bf16_f32 v3,v10,v11\nM v[76:79],v[200:203],a[236:239],v[76:79]\nds_read_b64_tr_b16 v[200:201],v242 offset:50176\nds_read_b64_tr_b16 v[202:203],v242 offset:50432\n" \
"M v[76:79],v[204:207],a[252:255],v[76:79]\nds_read_b64_tr_b16 v[204:205],v242 offset:58368\nds_read_b64_tr_b16 v[206:207],v242 offset:58624\nM v[16:19],v[208:211],a[224:227],v[16:19]\nds_write_b64 v250,v[188:189] offset:15360\nv_cvt_pk_bf16_f32 v6,v12,v13\nM v[16:19],v[212:215],a[240:243],v[16:19]\nv_cvt_pk_bf16_f32 v7,v14,v15\n" \
"v_cvt_pk_bf16_f32 v66,v72,v73\nM v[20:23],v[208:211],a[228:231],v[20:23]\nv_cvt_pk_bf16_f32 v67,v74,v75\nv_cvt_pk_bf16_f32 v70,v76,v77\nM v[20:23],v[212:215],a[244:247],v[20:23]\nds_write_b64 v250,v[190:191] offset:15368\nv_cvt_pk_bf16_f32 v71,v78,v79\nM v[80:83],v[208:211],a[232:235],v[80:83]\n" \
"v_cvt_pk_bf16_f32 v16,v16,v17\nv_cvt_pk_bf16_f32 v17,v18,v19\nM v[80:83],v[212:215],a[248:251],v[80:83]\nv_add_u32 v250,114688,v240\nv_accvgpr_write_b32 a129,v1\nM v[84:87],v[208:211],a[236:239],v[84:87]\nbuffer_store_dwordx4 v[188:191],v240,s[52:55],s74 offen offset:3072 sc1\nM v[84:87],v[212:215],a[252:255],v[84:87]\n" \
"ds_read_b64_tr_b16 v[208:209],v241 offset:50688\ns_waitcnt lgkmcnt(7)\nM v[24:27],v[216:219],a[224:227],v[24:27]\nds_read_b64_tr_b16 v[210:211],v241 offset:50944\nds_read_b64_tr_b16 v[212:213],v241 offset:58880\nM v[24:27],v[220:223],a[240:243],v[24:27]\nds_read_b64_tr_b16 v[214:215],v241 offset:59136\nv_cvt_pk_bf16_f32 v20,v20,v21\n" \
"M v[28:31],v[216:219],a[228:231],v[28:31]\nv_cvt_pk_bf16_f32 v80,v80,v81\nv_cvt_pk_bf16_f32 v84,v84,v85\nM v[28:31],v[220:223],a[244:247],v[28:31]\nv_cvt_pk_bf16_f32 v21,v22,v23\nv_cvt_pk_bf16_f32 v81,v82,v83\nM v[88:91],v[216:219],a[232:235],v[88:91]\nv_cvt_pk_bf16_f32 v85,v86,v87\n" \
"v_cvt_pk_bf16_f32 v18,v24,v25\nM v[88:91],v[220:223],a[248:251],v[88:91]\nv_cvt_pk_bf16_f32 v19,v26,v27\nv_add_u32 v241,65536,v240\nM v[92:95],v[216:219],a[236:239],v[92:95]\nds_read_b64_tr_b16 v[216:217],v242 offset:50688\nds_read_b64_tr_b16 v[218:219],v242 offset:50944\nM v[92:95],v[220:223],a[252:255],v[92:95]\n" \
"ds_read_b64_tr_b16 v[220:221],v242 offset:58880\nds_read_b64_tr_b16 v[222:223],v242 offset:59136\nM v[32:35],v[192:195],a[224:227],v[32:35]\nv_cvt_pk_bf16_f32 v22,v28,v29\nv_cvt_pk_bf16_f32 v23,v30,v31\nM v[32:35],v[196:199],a[240:243],v[32:35]\nv_cvt_pk_bf16_f32 v82,v88,v89\nv_cvt_pk_bf16_f32 v83,v90,v91\n" \
"M v[36:39],v[192:195],a[228:231],v[36:39]\nv_cvt_pk_bf16_f32 v86,v92,v93\nv_cvt_pk_bf16_f32 v87,v94,v95\nM v[36:39],v[196:199],a[244:247],v[36:39]\nv_lshl_or_b32 v242,v231,6,v230\nv_accvgpr_write_b32 a130,v2\nM v[96:99],v[192:195],a[232:235],v[96:99]\nv_cvt_pk_bf16_f32 v32,v32,v33\n" \
"v_cvt_pk_bf16_f32 v33,v34,v35\nM v[96:99],v[196:199],a[248:251],v[96:99]\nv_accvgpr_write_b32 a131,v3\nv_accvgpr_write_b32 a144,v4\nM v[100:103],v[192:195],a[236:239],v[100:103]\nv_cvt_pk_bf16_f32 v36,v36,v37\nv_cvt_pk_bf16_f32 v37,v38,v39\nM v[100:103],v[196:199],a[252:255],v[100:103]\n" \
"v_accvgpr_write_b32 a145,v5\ns_waitcnt lgkmcnt(4)\nM v[40:43],v[200:203],a[224:227],v[40:43]\nv_cvt_pk_bf16_f32 v96,v96,v97\nv_cvt_pk_bf16_f32 v97,v98,v99\nM v[40:43],v[204:207],a[240:243],v[40:43]\nv_accvgpr_write_b32 a146,v6\nv_accvgpr_write_b32 a147,v7\n" \
"M v[44:47],v[200:203],a[228:231],v[44:47]\nv_cvt_pk_bf16_f32 v100,v100,v101\nv_cvt_pk_bf16_f32 v101,v102,v103\nM v[44:47],v[204:207],a[244:247],v[44:47]\nv_accvgpr_write_b32 a160,v64\nv_accvgpr_write_b32 a161,v65\nM v[104:107],v[200:203],a[232:235],v[104:107]\nv_cvt_pk_bf16_f32 v34,v40,v41\n" \
"v_cvt_pk_bf16_f32 v35,v42,v43\nM v[104:107],v[204:207],a[248:251],v[104:107]\nv_accvgpr_write_b32 a162,v66\nv_accvgpr_write_b32 a163,v67\nM v[108:111],v[200:203],a[236:239],v[108:111]\nv_cvt_pk_bf16_f32 v38,v44,v45\nv_cvt_pk_bf16_f32 v39,v46,v47\nM v[108:111],v[204:207],a[252:255],v[108:111]\n" \
"v_accvgpr_write_b32 a176,v68\nv_accvgpr_write_b32 a177,v69\nM v[48:51],v[208:211],a[224:227],v[48:51]\nv_cvt_pk_bf16_f32 v98,v104,v105\nv_cvt_pk_bf16_f32 v99,v106,v107\nM v[48:51],v[212:215],a[240:243],v[48:51]\nv_accvgpr_write_b32 a178,v70\nv_accvgpr_write_b32 a179,v71\n" \
"M v[52:55],v[208:211],a[228:231],v[52:55]\nv_cvt_pk_bf16_f32 v102,v108,v109\nv_cvt_pk_bf16_f32 v103,v110,v111\nM v[52:55],v[212:215],a[244:247],v[52:55]\nv_accvgpr_write_b32 a132,v16\nv_accvgpr_write_b32 a133,v17\nM v[112:115],v[208:211],a[232:235],v[112:115]\nv_cvt_pk_bf16_f32 v48,v48,v49\n" \
"v_cvt_pk_bf16_f32 v49,v50,v51\nM v[112:115],v[212:215],a[248:251],v[112:115]\nv_accvgpr_write_b32 a134,v18\nv_accvgpr_write_b32 a135,v19\nM v[116:119],v[208:211],a[236:239],v[116:119]\nv_cvt_pk_bf16_f32 v52,v52,v53\nv_cvt_pk_bf16_f32 v53,v54,v55\nM v[116:119],v[212:215],a[252:255],v[116:119]\n" \
"v_accvgpr_write_b32 a148,v20\ns_waitcnt lgkmcnt(0)\nM v[56:59],v[216:219],a[224:227],v[56:59]\nv_cvt_pk_bf16_f32 v112,v112,v113\nv_cvt_pk_bf16_f32 v113,v114,v115\nM v[56:59],v[220:223],a[240:243],v[56:59]\nv_accvgpr_write_b32 a149,v21\nv_accvgpr_write_b32 a150,v22\n" \
"M v[60:63],v[216:219],a[228:231],v[60:63]\nv_cvt_pk_bf16_f32 v116,v116,v117\nv_cvt_pk_bf16_f32 v117,v118,v119\nM v[60:63],v[220:223],a[244:247],v[60:63]\nv_accvgpr_write_b32 a244,0\nv_accvgpr_write_b32 a245,0\nM v[120:123],v[216:219],a[232:235],v[120:123]\nv_cvt_pk_bf16_f32 v50,v56,v57\n" \
"v_cvt_pk_bf16_f32 v51,v58,v59\nM v[120:123],v[220:223],a[248:251],v[120:123]\nv_accvgpr_write_b32 a248,-1\nv_accvgpr_write_b32 a249,-1\nM v[124:127],v[216:219],a[236:239],v[124:127]\nv_cvt_pk_bf16_f32 v54,v60,v61\nv_cvt_pk_bf16_f32 v55,v62,v63\nM v[124:127],v[220:223],a[252:255],v[124:127]\n" \
"s_waitcnt vmcnt(0)\nbuffer_store_dword v252,off,s[52:55],s29 sc1\nv_cvt_pk_bf16_f32 v114,v120,v121\nv_cvt_pk_bf16_f32 v115,v122,v123\nv_accvgpr_write_b32 a246,0\nv_accvgpr_write_b32 a250,-1\nv_accvgpr_write_b32 a247,0\nv_accvgpr_write_b32 a251,-1\n" \
"v_cvt_pk_bf16_f32 v118,v124,v125\nv_cvt_pk_bf16_f32 v119,v126,v127\nv_accvgpr_write_b32 a151,v23\nv_accvgpr_write_b32 a164,v80\nv_accvgpr_write_b32 a165,v81\nv_accvgpr_write_b32 a166,v82\nv_accvgpr_write_b32 a167,v83\nv_accvgpr_write_b32 a180,v84\n" \
"v_accvgpr_write_b32 a181,v85\nv_accvgpr_write_b32 a182,v86\nv_accvgpr_write_b32 a183,v87\nv_accvgpr_write_b32 a136,v32\nv_accvgpr_write_b32 a137,v33\nv_accvgpr_write_b32 a138,v34\nv_accvgpr_write_b32 a139,v35\nv_accvgpr_write_b32 a152,v36\n" \
"v_accvgpr_write_b32 a153,v37\nv_accvgpr_write_b32 a154,v38\nv_accvgpr_write_b32 a155,v39\nv_accvgpr_write_b32 a168,v96\nv_accvgpr_write_b32 a169,v97\nv_accvgpr_write_b32 a170,v98\nv_accvgpr_write_b32 a171,v99\nv_accvgpr_write_b32 a184,v100\n" \
"v_accvgpr_write_b32 a185,v101\nv_accvgpr_write_b32 a186,v102\nv_accvgpr_write_b32 a187,v103\nv_accvgpr_write_b32 a140,v48\nv_accvgpr_write_b32 a141,v49\nv_accvgpr_write_b32 a142,v50\nv_accvgpr_write_b32 a143,v51\nv_accvgpr_write_b32 a156,v52\n" \
"v_accvgpr_write_b32 a157,v53\nv_accvgpr_write_b32 a158,v54\nv_accvgpr_write_b32 a159,v55\nv_accvgpr_write_b32 a172,v112\nv_accvgpr_write_b32 a173,v113\nv_accvgpr_write_b32 a174,v114\nv_accvgpr_write_b32 a175,v115\nv_accvgpr_write_b32 a188,v116\n" \
"v_accvgpr_write_b32 a189,v117\nv_accvgpr_write_b32 a190,v118\nv_accvgpr_write_b32 a191,v119\ns_waitcnt lgkmcnt(0)\n.Lattn:\ns_cmp_lt_u32 s7,2\ns_cbranch_scc0 .Lp23\ns_cmp_eq_u32 s7,0\n" \
"s_cbranch_scc0 .Lpro1\n.Lpro0:\ns_barrier\nM v[64:67],a[0:3],a[128:131],0\nds_read_b128 a[32:35],v240 offset:40960\nds_read_b128 a[36:39],v240 offset:41984\nM v[64:67],a[4:7],a[132:135],v[64:67]\nds_read_b128 a[40:43],v240 offset:43008\n" \
"s_mov_b32 m0,s64\nv_mov_b32 v218,0\nM v[64:67],a[8:11],a[136:139],v[64:67]\nds_read_b128 a[44:47],v240 offset:44032\nv_mov_b32 v219,0\nM v[64:67],a[12:15],a[140:143],v[64:67]\nds_read_b128 a[48:51],v240 offset:45056\nbuffer_load_dwordx4 v240,s[44:47],s36 offen offset:0 sc1 lds\n" \
"M v[68:71],a[12:15],a[156:159],0\nds_read_b128 a[12:15],v240 offset:35840\nM v[68:71],a[8:11],a[152:155],v[68:71]\nds_read_b128 a[8:11],v240 offset:34816\nM v[68:71],a[4:7],a[148:151],v[68:71]\nds_read_b128 a[4:7],v240 offset:33792\nv_max3_f32 v225,v64,v65,v66\nM v[68:71],a[0:3],a[144:147],v[68:71]\n" \
"ds_read_b128 a[0:3],v240 offset:32768\nv_max_f32 v225,v225,v67\nv_cndmask_b32 v225,v224,v225,s[90:91]\nM v[76:79],a[28:31],a[156:159],0\nds_read_b128 a[28:31],v240 offset:39936\nv_cndmask_b32 v64,v64,v252,s[82:83]\nM v[76:79],a[24:27],a[152:155],v[76:79]\nds_read_b128 a[24:27],v240 offset:38912\n" \
"v_max3_f32 v229,v68,v69,v70\nv_cndmask_b32 v65,v65,v252,s[84:85]\nM v[76:79],a[20:23],a[148:151],v[76:79]\nds_read_b128 a[20:23],v240 offset:37888\nv_cndmask_b32 v66,v66,v252,s[86:87]\nM v[76:79],a[16:19],a[144:147],v[76:79]\nds_read_b128 a[16:19],v240 offset:36864\nv_cndmask_b32 v67,v67,v252,s[88:89]\n" \
"s_waitcnt lgkmcnt(4)\nM v[136:139],a[0:3],a[160:163],0\nbuffer_load_dwordx4 v240,s[44:47],s36 offen offset:1024 sc1 lds\nM v[136:139],a[4:7],a[164:167],v[136:139]\nds_read_b128 a[52:55],v240 offset:46080\nds_read_b128 a[56:59],v240 offset:47104\nv_max3_f32 v229,v229,v71,v76\nM v[136:139],a[8:11],a[168:171],v[136:139]\n" \
"v_max3_f32 v229,v229,v77,v78\nv_max_f32 v229,v229,v79\nv_cndmask_b32 v229,v228,v229,s[90:91]\nM v[136:139],a[12:15],a[172:175],v[136:139]\nds_read_b128 a[60:63],v240 offset:48128\nv_permlane16_swap_b32 v225,v229\nM v[140:143],a[12:15],a[188:191],0\nv_max_f32 v225,v225,v229\n" \
"v_mov_b32 v229,v225\nds_read_b128 a[64:67],v241 offset:0\nM v[140:143],a[8:11],a[184:187],v[140:143]\nv_permlane32_swap_b32 v225,v229\nv_max_f32 v225,v225,v229\nM v[140:143],a[4:7],a[180:183],v[140:143]\nv_mov_b32 v229,v225\nv_max3_f32 v233,v136,v137,v138\n" \
"ds_read_b128 a[68:71],v241 offset:1024\nM v[140:143],a[0:3],a[176:179],v[140:143]\nv_permlane16_swap_b32 v225,v229\ns_waitcnt lgkmcnt(5)\nM v[152:155],a[16:19],a[160:163],0\nv_sub_f32 v224,0,v225\nv_sub_f32 v226,0,v225\nv_sub_f32 v227,0,v225\n" \
"M v[152:155],a[20:23],a[164:167],v[152:155]\nv_sub_f32 v225,0,v225\nv_sub_f32 v228,0,v229\nM v[152:155],a[24:27],a[168:171],v[152:155]\nv_sub_f32 v230,0,v229\nv_sub_f32 v231,0,v229\nv_sub_f32 v229,0,v229\nM v[152:155],a[28:31],a[172:175],v[152:155]\n" \
"v_max3_f32 v237,v140,v141,v142\nds_read_b128 a[72:75],v241 offset:2048\nM v[156:159],a[28:31],a[188:191],0\nds_read_b128 a[76:79],v241 offset:3072\nds_read_b128 a[80:83],v241 offset:4096\nv_add_f32 v64,v64,v224\nM v[156:159],a[24:27],a[184:187],v[156:159]\nds_read_b128 a[84:87],v241 offset:5120\n" \
"v_max3_f32 v233,v233,v139,v152\nM v[156:159],a[20:23],a[180:183],v[156:159]\nv_max3_f32 v233,v233,v153,v154\nv_max_f32 v233,v233,v155\nv_cndmask_b32 v233,v232,v233,s[90:91]\nM v[156:159],a[16:19],a[176:179],v[156:159]\nds_read_b128 a[88:91],v241 offset:6144\nv_add_f32 v65,v65,v224\n" \
"v_add_f32 v66,v66,v224\nM v[128:131],a[0:3],a[128:131],v[224:227]\nv_add_f32 v67,v67,v224\nds_read_b128 a[92:95],v241 offset:7168\nM v[128:131],a[4:7],a[132:135],v[128:131]\nv_exp_f32 v64,v64\nv_max3_f32 v237,v237,v143,v156\nM v[128:131],a[8:11],a[136:139],v[128:131]\n" \
"v_max3_f32 v237,v237,v157,v158\nv_max_f32 v237,v237,v159\nM v[128:131],a[12:15],a[140:143],v[128:131]\nv_cndmask_b32 v237,v236,v237,s[90:91]\nv_exp_f32 v65,v65\nM v[132:135],a[12:15],a[156:159],v[228:231]\nv_permlane16_swap_b32 v233,v237\nv_max_f32 v233,v233,v237\n" \
"M v[132:135],a[8:11],a[152:155],v[132:135]\nv_mov_b32 v237,v233\nv_exp_f32 v66,v66\nM v[132:135],a[4:7],a[148:151],v[132:135]\nv_permlane32_swap_b32 v233,v237\nv_max_f32 v233,v233,v237\nM v[132:135],a[0:3],a[144:147],v[132:135]\nv_mov_b32 v237,v233\n" \
"v_exp_f32 v67,v67\nM v[144:147],a[16:19],a[128:131],v[224:227]\nv_permlane16_swap_b32 v233,v237\nv_sub_f32 v232,0,v233\nM v[144:147],a[20:23],a[132:135],v[144:147]\nv_sub_f32 v234,0,v233\nv_sub_f32 v235,0,v233\nv_sub_f32 v233,0,v233\n" \
"M v[144:147],a[24:27],a[136:139],v[144:147]\nv_sub_f32 v236,0,v237\nv_sub_f32 v238,0,v237\nM v[144:147],a[28:31],a[140:143],v[144:147]\nv_sub_f32 v239,0,v237\nv_sub_f32 v237,0,v237\nv_cvt_pk_bf16_f32 v64,v64,v65\nM v[148:151],a[28:31],a[156:159],v[228:231]\n" \
"v_cvt_pk_bf16_f32 v65,v66,v67\nv_mov_b32 v66,0\nv_mov_b32 v67,0\nM v[148:151],a[24:27],a[152:155],v[148:151]\nbuffer_load_dwordx4 v240,s[44:47],s36 offen offset:2048 sc1 lds\nM v[148:151],a[20:23],a[148:151],v[148:151]\nv_add_f32 v68,v68,v228\nv_add_f32 v69,v69,v228\n" \
"v_cndmask_b32 v76,v76,v252,s[82:83]\nM v[148:151],a[16:19],a[144:147],v[148:151]\nv_cndmask_b32 v77,v77,v252,s[84:85]\nv_cndmask_b32 v78,v78,v252,s[86:87]\nM v[160:163],a[32:35],a[128:131],v[224:227]\nv_cndmask_b32 v79,v79,v252,s[88:89]\nv_add_f32 v70,v70,v228\nv_add_f32 v71,v71,v228\n" \
"M v[160:163],a[36:39],a[132:135],v[160:163]\nbuffer_load_dwordx4 v240,s[44:47],s36 offen offset:3072 sc1 lds\nM v[160:163],a[40:43],a[136:139],v[160:163]\nv_exp_f32 v68,v68\nv_add_f32 v76,v76,v228\nM v[160:163],a[44:47],a[140:143],v[160:163]\nv_exp_f32 v69,v69\nM v[164:167],a[44:47],a[156:159],v[228:231]\n" \
"v_exp_f32 v70,v70\nv_add_f32 v77,v77,v228\nM v[164:167],a[40:43],a[152:155],v[164:167]\nv_exp_f32 v71,v71\nM v[164:167],a[36:39],a[148:151],v[164:167]\nv_add_f32 v78,v78,v228\nv_add_f32 v79,v79,v228\nv_cvt_pk_bf16_f32 v68,v68,v69\n" \
"M v[164:167],a[32:35],a[144:147],v[164:167]\nv_exp_f32 v76,v76\nM v[176:179],a[48:51],a[128:131],v[224:227]\nv_exp_f32 v77,v77\ns_waitcnt lgkmcnt(0)\nM v[176:179],a[52:55],a[132:135],v[176:179]\nv_exp_f32 v78,v78\nv_cvt_pk_bf16_f32 v69,v70,v71\n" \
"M v[176:179],a[56:59],a[136:139],v[176:179]\nv_exp_f32 v79,v79\nM v[176:179],a[60:63],a[140:143],v[176:179]\nv_cvt_pk_bf16_f32 v70,v76,v77\nv_cvt_pk_bf16_f32 v71,v78,v79\nds_read_b128 a[0:3],v240 offset:49152\nM v[180:183],a[60:63],a[156:159],v[228:231]\nds_read_b128 a[4:7],v240 offset:50176\n" \
"ds_read_b128 a[8:11],v240 offset:51200\nM v[180:183],a[56:59],a[152:155],v[180:183]\nds_read_b128 a[12:15],v240 offset:52224\nv_exp_f32 v128,v128\nM v[180:183],a[52:55],a[148:151],v[180:183]\nds_read_b128 a[16:19],v240 offset:53248\nv_add_f32 v136,v136,v232\nM v[180:183],a[48:51],a[144:147],v[180:183]\n" \
"v_exp_f32 v129,v129\nds_read_b128 a[20:23],v240 offset:54272\nM v[168:171],a[32:35],a[160:163],v[232:235]\nv_exp_f32 v130,v130\nM v[168:171],a[36:39],a[164:167],v[168:171]\nv_exp_f32 v131,v131\nv_cvt_pk_bf16_f32 v192,v128,v129\nM v[168:171],a[40:43],a[168:171],v[168:171]\n" \
"v_cvt_pk_bf16_f32 v193,v130,v131\nds_read_b128 a[24:27],v240 offset:55296\nM v[168:171],a[44:47],a[172:175],v[168:171]\nds_read_b128 a[28:31],v240 offset:56320\nv_exp_f32 v132,v132\nM v[172:175],a[44:47],a[188:191],v[236:239]\nv_exp_f32 v133,v133\nM v[172:175],a[40:43],a[184:187],v[172:175]\n" \
"v_exp_f32 v134,v134\nv_cvt_pk_bf16_f32 v196,v132,v133\nM v[172:175],a[36:39],a[180:183],v[172:175]\nv_exp_f32 v135,v135\nv_add_f32 v137,v137,v232\nM v[172:175],a[32:35],a[176:179],v[172:175]\nv_cvt_pk_bf16_f32 v197,v134,v135\nv_add_f32 v138,v138,v232\n" \
"M v[188:191],a[60:63],a[188:191],v[236:239]\nv_exp_f32 v144,v144\nv_add_f32 v139,v139,v232\nM v[188:191],a[56:59],a[184:187],v[188:191]\nv_exp_f32 v145,v145\nM v[188:191],a[52:55],a[180:183],v[188:191]\nv_exp_f32 v146,v146\nv_cvt_pk_bf16_f32 v194,v144,v145\n" \
"M v[188:191],a[48:51],a[176:179],v[188:191]\nv_exp_f32 v147,v147\nM v[0:3],a[64:67],v[64:67],0\nv_cvt_pk_bf16_f32 v195,v146,v147\nv_exp_f32 v148,v148\nM v[8:11],a[68:71],v[64:67],0\nv_exp_f32 v149,v149\nM v[16:19],a[72:75],v[64:67],0\n" \
"buffer_load_dword v248,v251,s[52:55],s80 offen sc1\nv_cvt_pk_bf16_f32 v198,v148,v149\nM v[24:27],a[76:79],v[64:67],0\nv_exp_f32 v150,v150\nM v[32:35],a[80:83],v[64:67],0\nv_exp_f32 v151,v151\nv_add_f32 v140,v140,v236\nM v[40:43],a[84:87],v[64:67],0\n" \
"v_cvt_pk_bf16_f32 v199,v150,v151\nv_add_f32 v141,v141,v236\nM v[48:51],a[88:91],v[64:67],0\nv_exp_f32 v136,v136\nv_add_f32 v142,v142,v236\nM v[56:59],a[92:95],v[64:67],0\nv_exp_f32 v137,v137\nv_add_f32 v143,v143,v236\n" \
"M a[192:195],a[208:211],v[64:67],0\nv_exp_f32 v138,v138\nM v[4:7],a[64:67],v[68:71],0\nv_exp_f32 v139,v139\nv_cvt_pk_bf16_f32 v200,v136,v137\nM v[12:15],a[68:71],v[68:71],0\nv_cvt_pk_bf16_f32 v201,v138,v139\nds_read_b128 a[32:35],v240 offset:57344\n" \
"M v[20:23],a[72:75],v[68:71],0\nv_exp_f32 v140,v140\nv_add_f32 v152,v152,v232\nM v[28:31],a[76:79],v[68:71],0\nv_exp_f32 v141,v141\nM v[36:39],a[80:83],v[68:71],0\nv_exp_f32 v142,v142\nv_cvt_pk_bf16_f32 v204,v140,v141\n" \
"M v[44:47],a[84:87],v[68:71],0\nv_exp_f32 v143,v143\nM v[52:55],a[88:91],v[68:71],0\nv_cvt_pk_bf16_f32 v205,v142,v143\nv_add_f32 v153,v153,v232\nv_add_f32 v154,v154,v232\nM v[60:63],a[92:95],v[68:71],0\nv_add_f32 v155,v155,v232\n" \
"ds_read_b128 a[36:39],v240 offset:58368\nM a[196:199],a[208:211],v[68:71],0\nv_exp_f32 v152,v152\ns_waitcnt lgkmcnt(2)\nM v[128:131],a[0:3],a[128:131],v[224:227]\nv_exp_f32 v153,v153\nM v[128:131],a[4:7],a[132:135],v[128:131]\nv_exp_f32 v154,v154\n" \
"v_cvt_pk_bf16_f32 v202,v152,v153\nM v[128:131],a[8:11],a[136:139],v[128:131]\nv_exp_f32 v155,v155\nM v[128:131],a[12:15],a[140:143],v[128:131]\nv_cvt_pk_bf16_f32 v203,v154,v155\nds_read_b128 a[40:43],v240 offset:59392\nds_read_b128 a[44:47],v240 offset:60416\nM v[132:135],a[12:15],a[156:159],v[228:231]\n" \
"v_add_f32 v156,v156,v236\nv_add_f32 v157,v157,v236\nv_add_f32 v158,v158,v236\nM v[132:135],a[8:11],a[152:155],v[132:135]\nv_add_f32 v159,v159,v236\nds_read_b128 a[48:51],v240 offset:61440\nM v[132:135],a[4:7],a[148:151],v[132:135]\nv_exp_f32 v156,v156\n" \
"ds_read_b128 a[52:55],v240 offset:62464\nM v[132:135],a[0:3],a[144:147],v[132:135]\nv_exp_f32 v157,v157\nM v[144:147],a[16:19],a[128:131],v[224:227]\nv_exp_f32 v158,v158\nv_cvt_pk_bf16_f32 v206,v156,v157\nM v[144:147],a[20:23],a[132:135],v[144:147]\nv_exp_f32 v159,v159\n" \
"M v[144:147],a[24:27],a[136:139],v[144:147]\nv_cvt_pk_bf16_f32 v207,v158,v159\nv_exp_f32 v160,v160\nM v[144:147],a[28:31],a[140:143],v[144:147]\nv_exp_f32 v161,v161\nM v[148:151],a[28:31],a[156:159],v[228:231]\nv_exp_f32 v162,v162\nv_cvt_pk_bf16_f32 v208,v160,v161\n" \
"M v[148:151],a[24:27],a[152:155],v[148:151]\nv_exp_f32 v163,v163\nM v[148:151],a[20:23],a[148:151],v[148:151]\nv_cvt_pk_bf16_f32 v209,v162,v163\nds_read_b128 a[56:59],v240 offset:63488\nds_read_b128 a[60:63],v240 offset:64512\nM v[148:151],a[16:19],a[144:147],v[148:151]\nv_exp_f32 v164,v164\n" \
"M v[136:139],a[0:3],a[160:163],v[232:235]\nv_exp_f32 v165,v165\nv_cndmask_b32 v168,v168,v252,s[82:83]\nM v[136:139],a[4:7],a[164:167],v[136:139]\nv_exp_f32 v166,v166\nv_cvt_pk_bf16_f32 v212,v164,v165\nM v[136:139],a[8:11],a[168:171],v[136:139]\nv_exp_f32 v167,v167\n" \
"M v[136:139],a[12:15],a[172:175],v[136:139]\nv_cvt_pk_bf16_f32 v213,v166,v167\nv_exp_f32 v176,v176\nM v[140:143],a[12:15],a[188:191],v[236:239]\nv_exp_f32 v177,v177\nM v[140:143],a[8:11],a[184:187],v[140:143]\nv_exp_f32 v178,v178\nv_cvt_pk_bf16_f32 v210,v176,v177\n" \
"M v[140:143],a[4:7],a[180:183],v[140:143]\nv_exp_f32 v179,v179\nM v[140:143],a[0:3],a[176:179],v[140:143]\nv_cvt_pk_bf16_f32 v211,v178,v179\nv_exp_f32 v180,v180\nM v[152:155],a[16:19],a[160:163],v[232:235]\nv_exp_f32 v181,v181\nM v[152:155],a[20:23],a[164:167],v[152:155]\n" \
"v_exp_f32 v182,v182\nv_cvt_pk_bf16_f32 v214,v180,v181\nM v[152:155],a[24:27],a[168:171],v[152:155]\nv_exp_f32 v183,v183\nM v[152:155],a[28:31],a[172:175],v[152:155]\nv_cvt_pk_bf16_f32 v215,v182,v183\nv_cndmask_b32 v169,v169,v252,s[84:85]\nv_cndmask_b32 v170,v170,v252,s[86:87]\n" \
"M v[156:159],a[28:31],a[188:191],v[236:239]\nv_cndmask_b32 v171,v171,v252,s[88:89]\nds_read_b128 a[64:67],v241 offset:32768\nM v[156:159],a[24:27],a[184:187],v[156:159]\nv_exp_f32 v168,v168\nds_read_b128 a[96:99],v241 offset:40960\nM v[156:159],a[20:23],a[180:183],v[156:159]\nv_exp_f32 v169,v169\n" \
"ds_read_b128 a[68:71],v241 offset:33792\nM v[156:159],a[16:19],a[176:179],v[156:159]\nv_cvt_pk_bf16_f32 v216,v168,v169\ns_waitcnt lgkmcnt(3)\nM v[160:163],a[32:35],a[128:131],v[224:227]\nv_exp_f32 v170,v170\nds_read_b128 a[100:103],v241 offset:41984\nM v[160:163],a[36:39],a[132:135],v[160:163]\n" \
"v_exp_f32 v171,v171\nM v[160:163],a[40:43],a[136:139],v[160:163]\ns_waitcnt vmcnt(1)\ns_barrier\nv_cvt_pk_bf16_f32 v217,v170,v171\nM v[160:163],a[44:47],a[140:143],v[160:163]\nv_exp_f32 v172,v172\nM v[164:167],a[44:47],a[156:159],v[228:231]\n" \
"v_exp_f32 v173,v173\nds_read_b128 a[72:75],v241 offset:34816\nM v[164:167],a[40:43],a[152:155],v[164:167]\nv_exp_f32 v174,v174\nM v[164:167],a[36:39],a[148:151],v[164:167]\nv_exp_f32 v175,v175\nv_cvt_pk_bf16_f32 v220,v172,v173\nM v[164:167],a[32:35],a[144:147],v[164:167]\n" \
"v_cvt_pk_bf16_f32 v221,v174,v175\nv_cndmask_b32 v188,v188,v252,s[82:83]\nM v[176:179],a[48:51],a[128:131],v[224:227]\nv_cndmask_b32 v189,v189,v252,s[84:85]\nv_cndmask_b32 v190,v190,v252,s[86:87]\nv_cndmask_b32 v191,v191,v252,s[88:89]\nM v[176:179],a[52:55],a[132:135],v[176:179]\nv_exp_f32 v188,v188\n" \
"M v[176:179],a[56:59],a[136:139],v[176:179]\nv_exp_f32 v189,v189\nds_read_b128 a[104:107],v241 offset:43008\nM v[176:179],a[60:63],a[140:143],v[176:179]\nv_exp_f32 v190,v190\nv_cvt_pk_bf16_f32 v222,v188,v189\nM v[180:183],a[60:63],a[156:159],v[228:231]\nv_exp_f32 v191,v191\n" \
"M v[180:183],a[56:59],a[152:155],v[180:183]\nv_cvt_pk_bf16_f32 v223,v190,v191\nds_read_b128 a[76:79],v241 offset:35840\nds_read_b128 a[108:111],v241 offset:44032\nM v[180:183],a[52:55],a[148:151],v[180:183]\nds_read_b128 a[80:83],v241 offset:36864\ns_waitcnt vmcnt(0)\nv_cmp_eq_u32 vcc,0,v248\n" \
"M v[180:183],a[48:51],a[144:147],v[180:183]\nds_read_b128 a[112:115],v241 offset:45056\ns_cbranch_vccnz .Lps2\n.Lpb2:\ns_add_u32 m0,s64,65536\nM v[168:171],a[32:35],a[160:163],v[232:235]\nbuffer_load_dwordx4 v240,s[48:51],s36 offen offset:0 sc1 lds\nM v[168:171],a[36:39],a[164:167],v[168:171]\n" \
"ds_read_b128 a[84:87],v241 offset:37888\nv_exp_f32 v128,v128\nM v[168:171],a[40:43],a[168:171],v[168:171]\nds_read_b128 a[116:119],v241 offset:46080\nds_read_b128 a[88:91],v241 offset:38912\nM v[168:171],a[44:47],a[172:175],v[168:171]\nds_read_b128 a[120:123],v241 offset:47104\nv_exp_f32 v129,v129\n" \
"M v[172:175],a[44:47],a[188:191],v[236:239]\nbuffer_load_dwordx4 v240,s[48:51],s36 offen offset:1024 sc1 lds\nM v[172:175],a[40:43],a[184:187],v[172:175]\nds_read_b128 a[92:95],v241 offset:39936\nv_exp_f32 v130,v130\nM v[172:175],a[36:39],a[180:183],v[172:175]\nds_read_b128 a[124:127],v241 offset:48128\nds_read_b128 a[0:3],v240 offset:0\n" \
"M v[172:175],a[32:35],a[176:179],v[172:175]\nds_read_b128 a[4:7],v240 offset:1024\nv_exp_f32 v131,v131\nM v[184:187],a[48:51],a[160:163],v[232:235]\nds_read_b128 a[8:11],v240 offset:2048\nbuffer_load_dwordx4 v240,s[48:51],s36 offen offset:2048 sc1 lds\nM v[184:187],a[52:55],a[164:167],v[184:187]\nv_exp_f32 v132,v132\n" \
"M v[184:187],a[56:59],a[168:171],v[184:187]\nds_read_b128 a[12:15],v240 offset:3072\nv_exp_f32 v133,v133\nM v[184:187],a[60:63],a[172:175],v[184:187]\nds_read_b128 a[16:19],v240 offset:4096\nds_read_b128 a[20:23],v240 offset:5120\nM v[188:191],a[60:63],a[188:191],v[236:239]\nds_read_b128 a[24:27],v240 offset:6144\n" \
"buffer_load_dwordx4 v240,s[48:51],s36 offen offset:3072 sc1 lds\nM v[188:191],a[56:59],a[184:187],v[188:191]\nds_read_b128 a[28:31],v240 offset:7168\ns_add_u32 m0,s64,16384\nM v[188:191],a[52:55],a[180:183],v[188:191]\nds_read_b128 a[32:35],v240 offset:8192\nv_exp_f32 v134,v134\nM v[188:191],a[48:51],a[176:179],v[188:191]\n" \
"ds_read_b128 a[36:39],v240 offset:9216\ns_waitcnt lgkmcnt(9)\nM v[0:3],a[64:67],v[192:195],v[0:3]\nds_read_b128 a[40:43],v240 offset:10240\nbuffer_load_dwordx4 v240,s[44:47],s65 offen offset:0 sc1 lds\nM v[0:3],a[96:99],v[208:211],v[0:3]\nv_exp_f32 v135,v135\nM v[8:11],a[68:71],v[192:195],v[8:11]\n" \
"ds_read_b128 a[44:47],v240 offset:11264\nv_exp_f32 v144,v144\nM v[8:11],a[100:103],v[208:211],v[8:11]\nds_read_b128 a[48:51],v240 offset:12288\nds_read_b128 a[52:55],v240 offset:13312\nM v[16:19],a[72:75],v[192:195],v[16:19]\nbuffer_load_dwordx4 v240,s[44:47],s65 offen offset:1024 sc1 lds\nds_read_b128 a[56:59],v240 offset:14336\n" \
"M v[16:19],a[104:107],v[208:211],v[16:19]\nds_read_b128 a[60:63],v240 offset:15360\nv_exp_f32 v145,v145\nM v[24:27],a[76:79],v[192:195],v[24:27]\nv_exp_f32 v146,v146\nM v[24:27],a[108:111],v[208:211],v[24:27]\nv_exp_f32 v147,v147\nM v[32:35],a[80:83],v[192:195],v[32:35]\n" \
"buffer_load_dwordx4 v240,s[44:47],s65 offen offset:2048 sc1 lds\nM v[32:35],a[112:115],v[208:211],v[32:35]\nv_exp_f32 v148,v148\nM v[40:43],a[84:87],v[192:195],v[40:43]\nv_exp_f32 v149,v149\nM v[40:43],a[116:119],v[208:211],v[40:43]\nv_exp_f32 v150,v150\nM v[48:51],a[88:91],v[192:195],v[48:51]\n" \
"buffer_load_dwordx4 v240,s[44:47],s65 offen offset:3072 sc1 lds\nM v[48:51],a[120:123],v[208:211],v[48:51]\ns_add_u32 m0,s64,81920\nv_exp_f32 v151,v151\nM v[56:59],a[92:95],v[192:195],v[56:59]\nv_exp_f32 v136,v136\nM v[56:59],a[124:127],v[208:211],v[56:59]\nv_exp_f32 v137,v137\n" \
"M a[192:195],a[208:211],v[192:195],a[192:195]\nbuffer_load_dwordx4 v240,s[48:51],s65 offen offset:0 sc1 lds\nM a[192:195],a[208:211],v[208:211],a[192:195]\nv_exp_f32 v138,v138\nv_cvt_pk_bf16_f32 v192,v128,v129\nM v[4:7],a[64:67],v[196:199],v[4:7]\nv_exp_f32 v139,v139\nM v[4:7],a[96:99],v[212:215],v[4:7]\n" \
"v_exp_f32 v152,v152\nv_cvt_pk_bf16_f32 v193,v130,v131\nM v[12:15],a[68:71],v[196:199],v[12:15]\nbuffer_load_dwordx4 v240,s[48:51],s65 offen offset:1024 sc1 lds\nv_cvt_pk_bf16_f32 v194,v144,v145\nM v[12:15],a[100:103],v[212:215],v[12:15]\nv_exp_f32 v153,v153\nM v[20:23],a[72:75],v[196:199],v[20:23]\n" \
"v_exp_f32 v154,v154\nv_cvt_pk_bf16_f32 v195,v146,v147\nM v[20:23],a[104:107],v[212:215],v[20:23]\nv_exp_f32 v155,v155\nM v[28:31],a[76:79],v[196:199],v[28:31]\nbuffer_load_dwordx4 v240,s[48:51],s65 offen offset:2048 sc1 lds\nM v[28:31],a[108:111],v[212:215],v[28:31]\nv_exp_f32 v160,v160\n" \
"M v[36:39],a[80:83],v[196:199],v[36:39]\nv_exp_f32 v161,v161\nM v[36:39],a[112:115],v[212:215],v[36:39]\nv_exp_f32 v162,v162\nM v[44:47],a[84:87],v[196:199],v[44:47]\nbuffer_load_dwordx4 v240,s[48:51],s65 offen offset:3072 sc1 lds\ns_add_u32 m0,s64,32768\nM v[44:47],a[116:119],v[212:215],v[44:47]\n" \
"v_exp_f32 v163,v163\nM v[52:55],a[88:91],v[196:199],v[52:55]\nv_exp_f32 v176,v176\nv_cvt_pk_bf16_f32 v208,v160,v161\nM v[52:55],a[120:123],v[212:215],v[52:55]\nv_exp_f32 v177,v177\nM v[60:63],a[92:95],v[196:199],v[60:63]\nbuffer_load_dwordx4 v240,s[44:47],s66 offen offset:0 sc1 lds\n" \
"v_cvt_pk_bf16_f32 v209,v162,v163\nM v[60:63],a[124:127],v[212:215],v[60:63]\nv_exp_f32 v178,v178\nv_cvt_pk_bf16_f32 v210,v176,v177\nM a[196:199],a[208:211],v[196:199],a[196:199]\nv_exp_f32 v179,v179\nM a[196:199],a[208:211],v[212:215],a[196:199]\nv_exp_f32 v140,v140\n" \
"v_cvt_pk_bf16_f32 v196,v132,v133\nM v[64:67],a[64:67],v[200:203],0\nbuffer_load_dwordx4 v240,s[44:47],s66 offen offset:1024 sc1 lds\nM v[64:67],a[96:99],v[216:219],v[64:67]\nv_exp_f32 v141,v141\nv_cvt_pk_bf16_f32 v197,v134,v135\nM v[72:75],a[68:71],v[200:203],0\nv_exp_f32 v142,v142\n" \
"M v[72:75],a[100:103],v[216:219],v[72:75]\nv_exp_f32 v143,v143\nv_cvt_pk_bf16_f32 v198,v148,v149\nM v[80:83],a[72:75],v[200:203],0\nbuffer_load_dwordx4 v240,s[44:47],s66 offen offset:2048 sc1 lds\nM v[80:83],a[104:107],v[216:219],v[80:83]\nv_exp_f32 v156,v156\nv_cvt_pk_bf16_f32 v199,v150,v151\n" \
"M v[88:91],a[76:79],v[200:203],0\ns_barrier\nv_cvt_pk_bf16_f32 v211,v178,v179\nM v[88:91],a[108:111],v[216:219],v[88:91]\nv_exp_f32 v157,v157\nM v[96:99],a[80:83],v[200:203],0\nbuffer_load_dwordx4 v240,s[44:47],s66 offen offset:3072 sc1 lds\nM v[96:99],a[112:115],v[216:219],v[96:99]\n" \
"s_add_u32 m0,s64,98304\nv_exp_f32 v158,v158\nM v[104:107],a[84:87],v[200:203],0\nv_exp_f32 v159,v159\nM v[104:107],a[116:119],v[216:219],v[104:107]\nv_exp_f32 v164,v164\nM v[112:115],a[88:91],v[200:203],0\nv_exp_f32 v165,v165\n" \
"M v[112:115],a[120:123],v[216:219],v[112:115]\nv_exp_f32 v166,v166\nM v[120:123],a[92:95],v[200:203],0\nv_exp_f32 v167,v167\nM v[120:123],a[124:127],v[216:219],v[120:123]\nv_exp_f32 v180,v180\nM a[200:203],a[208:211],v[200:203],0\nv_exp_f32 v181,v181\n" \
"v_cvt_pk_bf16_f32 v200,v136,v137\nM a[200:203],a[208:211],v[216:219],a[200:203]\nv_exp_f32 v182,v182\nM v[68:71],a[64:67],v[204:207],0\nv_exp_f32 v183,v183\nv_cvt_pk_bf16_f32 v201,v138,v139\nM v[68:71],a[96:99],v[220:223],v[68:71]\nv_exp_f32 v168,v168\n" \
"M v[76:79],a[68:71],v[204:207],0\nv_exp_f32 v169,v169\nv_cvt_pk_bf16_f32 v202,v152,v153\nM v[76:79],a[100:103],v[220:223],v[76:79]\nv_exp_f32 v170,v170\nM v[84:87],a[72:75],v[204:207],0\nv_exp_f32 v171,v171\nv_cvt_pk_bf16_f32 v203,v154,v155\n" \
"M v[84:87],a[104:107],v[220:223],v[84:87]\nbuffer_load_dwordx4 v240,s[48:51],s66 offen offset:0 sc1 lds\nM v[92:95],a[76:79],v[204:207],0\nM v[92:95],a[108:111],v[220:223],v[92:95]\nM v[100:103],a[80:83],v[204:207],0\nM v[100:103],a[112:115],v[220:223],v[100:103]\nbuffer_load_dwordx4 v240,s[48:51],s66 offen offset:1024 sc1 lds\nM v[108:111],a[84:87],v[204:207],0\n" \
"M v[108:111],a[116:119],v[220:223],v[108:111]\nM v[116:119],a[88:91],v[204:207],0\nM v[116:119],a[120:123],v[220:223],v[116:119]\nbuffer_load_dwordx4 v240,s[48:51],s66 offen offset:2048 sc1 lds\nM v[124:127],a[92:95],v[204:207],0\nM v[124:127],a[124:127],v[220:223],v[124:127]\nM a[204:207],a[208:211],v[204:207],0\nbuffer_load_dwordx4 v240,s[48:51],s66 offen offset:3072 sc1 lds\n" \
"M a[204:207],a[208:211],v[220:223],a[204:207]\ns_cmp_lt_u32 s62,2\ns_cbranch_scc1 .Lx01\ns_branch .Ldual\n.Lpro1:\ns_barrier\nM v[64:67],a[0:3],a[128:131],0\nds_read_b128 a[64:67],v241 offset:0\n" \
"ds_read_b128 a[96:99],v241 offset:8192\nM v[64:67],a[4:7],a[132:135],v[64:67]\nds_read_b128 a[68:71],v241 offset:1024\ns_mov_b32 m0,s64\nv_mov_b32 v202,0\nM v[64:67],a[8:11],a[136:139],v[64:67]\nds_read_b128 a[100:103],v241 offset:9216\nv_mov_b32 v203,0\n" \
"M v[64:67],a[12:15],a[140:143],v[64:67]\nds_read_b128 a[72:75],v241 offset:2048\nbuffer_load_dwordx4 v240,s[44:47],s36 offen offset:0 sc1 lds\nM v[68:71],a[12:15],a[156:159],0\nds_read_b128 a[12:15],v240 offset:35840\nM v[68:71],a[8:11],a[152:155],v[68:71]\nds_read_b128 a[8:11],v240 offset:34816\nM v[68:71],a[4:7],a[148:151],v[68:71]\n" \
"ds_read_b128 a[4:7],v240 offset:33792\nv_max3_f32 v225,v64,v65,v66\nM v[68:71],a[0:3],a[144:147],v[68:71]\nds_read_b128 a[0:3],v240 offset:32768\nbuffer_load_dwordx4 v240,s[44:47],s36 offen offset:1024 sc1 lds\nM v[72:75],a[16:19],a[128:131],0\nds_read_b128 a[104:107],v241 offset:10240\nM v[72:75],a[20:23],a[132:135],v[72:75]\n" \
"ds_read_b128 a[76:79],v241 offset:3072\nM v[72:75],a[24:27],a[136:139],v[72:75]\nds_read_b128 a[108:111],v241 offset:11264\nv_max3_f32 v229,v68,v69,v70\nM v[72:75],a[28:31],a[140:143],v[72:75]\nds_read_b128 a[80:83],v241 offset:4096\nbuffer_load_dwordx4 v240,s[44:47],s36 offen offset:2048 sc1 lds\nM v[76:79],a[28:31],a[156:159],0\n" \
"ds_read_b128 a[28:31],v240 offset:39936\nM v[76:79],a[24:27],a[152:155],v[76:79]\nds_read_b128 a[24:27],v240 offset:38912\nM v[76:79],a[20:23],a[148:151],v[76:79]\nds_read_b128 a[20:23],v240 offset:37888\nv_max3_f32 v225,v225,v67,v72\nv_max3_f32 v225,v225,v73,v74\nM v[76:79],a[16:19],a[144:147],v[76:79]\n" \
"ds_read_b128 a[16:19],v240 offset:36864\nv_max_f32 v225,v225,v75\nv_cndmask_b32 v225,v224,v225,s[90:91]\nM v[80:83],a[32:35],a[128:131],0\nbuffer_load_dwordx4 v240,s[44:47],s36 offen offset:3072 sc1 lds\nM v[80:83],a[36:39],a[132:135],v[80:83]\nds_read_b128 a[112:115],v241 offset:12288\nds_read_b128 a[84:87],v241 offset:5120\n" \
"v_max3_f32 v229,v229,v71,v76\nM v[80:83],a[40:43],a[136:139],v[80:83]\nv_max3_f32 v229,v229,v77,v78\nv_max_f32 v229,v229,v79\nv_cndmask_b32 v229,v228,v229,s[90:91]\nM v[80:83],a[44:47],a[140:143],v[80:83]\nds_read_b128 a[116:119],v241 offset:13312\nv_permlane16_swap_b32 v225,v229\n" \
"M v[84:87],a[44:47],a[156:159],0\nv_max_f32 v225,v225,v229\nv_mov_b32 v229,v225\nds_read_b128 a[44:47],v240 offset:44032\nM v[84:87],a[40:43],a[152:155],v[84:87]\nv_permlane32_swap_b32 v225,v229\nv_max_f32 v225,v225,v229\nM v[84:87],a[36:39],a[148:151],v[84:87]\n" \
"v_mov_b32 v229,v225\nds_read_b128 a[36:39],v240 offset:41984\nds_read_b128 a[40:43],v240 offset:43008\nM v[84:87],a[32:35],a[144:147],v[84:87]\nv_permlane16_swap_b32 v225,v229\nv_sub_f32 v224,0,v225\nv_sub_f32 v226,0,v225\nM v[92:95],a[60:63],a[156:159],0\n" \
"v_sub_f32 v227,0,v225\nv_sub_f32 v225,0,v225\nM v[92:95],a[56:59],a[152:155],v[92:95]\nv_sub_f32 v228,0,v229\nv_sub_f32 v230,0,v229\nv_sub_f32 v231,0,v229\nM v[92:95],a[52:55],a[148:151],v[92:95]\nv_sub_f32 v229,0,v229\n" \
"ds_read_b128 a[32:35],v240 offset:40960\nds_read_b128 a[52:55],v240 offset:46080\nM v[92:95],a[48:51],a[144:147],v[92:95]\nds_read_b128 a[48:51],v240 offset:45056\ns_waitcnt lgkmcnt(7)\nM v[136:139],a[0:3],a[160:163],0\nds_read_b128 a[56:59],v240 offset:47104\nv_add_f32 v64,v64,v224\n" \
"v_add_f32 v65,v65,v224\nM v[136:139],a[4:7],a[164:167],v[136:139]\nds_read_b128 a[60:63],v240 offset:48128\nv_add_f32 v66,v66,v224\nM v[136:139],a[8:11],a[168:171],v[136:139]\nv_add_f32 v67,v67,v224\nv_exp_f32 v64,v64\nM v[136:139],a[12:15],a[172:175],v[136:139]\n" \
"v_exp_f32 v65,v65\nv_cndmask_b32 v80,v80,v252,s[82:83]\nM v[140:143],a[12:15],a[188:191],0\nv_cndmask_b32 v81,v81,v252,s[84:85]\nv_add_f32 v72,v72,v224\nM v[140:143],a[8:11],a[184:187],v[140:143]\nv_exp_f32 v66,v66\nv_add_f32 v73,v73,v224\n" \
"M v[140:143],a[4:7],a[180:183],v[140:143]\nv_exp_f32 v67,v67\nv_add_f32 v74,v74,v224\nM v[140:143],a[0:3],a[176:179],v[140:143]\nv_add_f32 v75,v75,v224\nv_cndmask_b32 v82,v82,v252,s[86:87]\nM v[156:159],a[28:31],a[188:191],0\nv_cndmask_b32 v83,v83,v252,s[88:89]\n" \
"v_add_f32 v80,v80,v224\nv_add_f32 v81,v81,v224\nM v[156:159],a[24:27],a[184:187],v[156:159]\nv_exp_f32 v72,v72\nv_add_f32 v82,v82,v224\nM v[156:159],a[20:23],a[180:183],v[156:159]\nv_exp_f32 v73,v73\nM v[156:159],a[16:19],a[176:179],v[156:159]\n" \
"v_exp_f32 v74,v74\nv_add_f32 v83,v83,v224\nM v[128:131],a[0:3],a[128:131],v[224:227]\nv_exp_f32 v75,v75\nM v[128:131],a[4:7],a[132:135],v[128:131]\nv_exp_f32 v80,v80\nv_cvt_pk_bf16_f32 v64,v64,v65\nM v[128:131],a[8:11],a[136:139],v[128:131]\n" \
"v_exp_f32 v81,v81\nv_cvt_pk_bf16_f32 v65,v66,v67\nM v[128:131],a[12:15],a[140:143],v[128:131]\nv_exp_f32 v82,v82\nM v[132:135],a[12:15],a[156:159],v[228:231]\nv_exp_f32 v83,v83\nv_cvt_pk_bf16_f32 v66,v72,v73\nM v[132:135],a[8:11],a[152:155],v[132:135]\n" \
"v_cvt_pk_bf16_f32 v67,v74,v75\nv_cvt_pk_bf16_f32 v80,v80,v81\nv_cvt_pk_bf16_f32 v81,v82,v83\nM v[132:135],a[4:7],a[148:151],v[132:135]\nv_mov_b32 v82,0\nv_mov_b32 v83,0\nM v[132:135],a[0:3],a[144:147],v[132:135]\nds_read_b128 a[88:91],v241 offset:6144\n" \
"ds_read_b128 a[120:123],v241 offset:14336\nv_add_f32 v68,v68,v228\nM v[144:147],a[16:19],a[128:131],v[224:227]\nds_read_b128 a[92:95],v241 offset:7168\nv_add_f32 v69,v69,v228\nv_add_f32 v70,v70,v228\nM v[144:147],a[20:23],a[132:135],v[144:147]\nds_read_b128 a[124:127],v241 offset:15360\n" \
"v_add_f32 v71,v71,v228\nM v[144:147],a[24:27],a[136:139],v[144:147]\nv_exp_f32 v68,v68\nv_add_f32 v76,v76,v228\nM v[144:147],a[28:31],a[140:143],v[144:147]\nv_exp_f32 v69,v69\nM v[148:151],a[28:31],a[156:159],v[228:231]\nv_exp_f32 v70,v70\n" \
"v_add_f32 v77,v77,v228\nM v[148:151],a[24:27],a[152:155],v[148:151]\nv_exp_f32 v71,v71\nv_add_f32 v78,v78,v228\nM v[148:151],a[20:23],a[148:151],v[148:151]\nv_add_f32 v79,v79,v228\nv_add_f32 v84,v84,v228\nM v[148:151],a[16:19],a[144:147],v[148:151]\n" \
"v_add_f32 v85,v85,v228\nv_cndmask_b32 v92,v92,v252,s[82:83]\ns_waitcnt lgkmcnt(4)\nM v[160:163],a[32:35],a[128:131],v[224:227]\nv_exp_f32 v76,v76\nv_cndmask_b32 v93,v93,v252,s[84:85]\nM v[160:163],a[36:39],a[132:135],v[160:163]\nv_exp_f32 v77,v77\n" \
"M v[160:163],a[40:43],a[136:139],v[160:163]\nv_exp_f32 v78,v78\nv_cndmask_b32 v94,v94,v252,s[86:87]\nM v[160:163],a[44:47],a[140:143],v[160:163]\nv_exp_f32 v79,v79\nM v[164:167],a[44:47],a[156:159],v[228:231]\nv_cndmask_b32 v95,v95,v252,s[88:89]\nv_add_f32 v86,v86,v228\n" \
"v_add_f32 v87,v87,v228\nM v[164:167],a[40:43],a[152:155],v[164:167]\nv_exp_f32 v84,v84\nv_cvt_pk_bf16_f32 v68,v68,v69\nM v[164:167],a[36:39],a[148:151],v[164:167]\nv_exp_f32 v85,v85\nM v[164:167],a[32:35],a[144:147],v[164:167]\nv_exp_f32 v86,v86\n" \
"v_add_f32 v92,v92,v228\nM v[176:179],a[48:51],a[128:131],v[224:227]\nv_exp_f32 v87,v87\nv_add_f32 v93,v93,v228\nM v[176:179],a[52:55],a[132:135],v[176:179]\nv_add_f32 v94,v94,v228\nv_add_f32 v95,v95,v228\nM v[176:179],a[56:59],a[136:139],v[176:179]\n" \
"v_cvt_pk_bf16_f32 v69,v70,v71\nv_exp_f32 v92,v92\nM v[176:179],a[60:63],a[140:143],v[176:179]\nv_exp_f32 v93,v93\nv_cvt_pk_bf16_f32 v70,v76,v77\nM v[180:183],a[60:63],a[156:159],v[228:231]\nv_exp_f32 v94,v94\nM v[180:183],a[56:59],a[152:155],v[180:183]\n" \
"v_exp_f32 v95,v95\nv_cvt_pk_bf16_f32 v71,v78,v79\nM v[180:183],a[52:55],a[148:151],v[180:183]\nv_cvt_pk_bf16_f32 v84,v84,v85\nv_cvt_pk_bf16_f32 v85,v86,v87\nM v[180:183],a[48:51],a[144:147],v[180:183]\nv_cvt_pk_bf16_f32 v86,v92,v93\nv_cvt_pk_bf16_f32 v87,v94,v95\n" \
"ds_read_b128 a[0:3],v240 offset:49152\nM v[0:3],a[64:67],v[64:67],0\nds_read_b128 a[4:7],v240 offset:50176\nds_read_b128 a[8:11],v240 offset:51200\nv_max3_f32 v237,v140,v141,v142\nM v[0:3],a[96:99],v[80:83],v[0:3]\nds_read_b128 a[12:15],v240 offset:52224\nv_max3_f32 v237,v237,v143,v156\n" \
"M v[8:11],a[68:71],v[64:67],0\nbuffer_load_dword v248,v251,s[52:55],s80 offen sc1\nds_read_b128 a[16:19],v240 offset:53248\nM v[8:11],a[100:103],v[80:83],v[8:11]\nv_exp_f32 v128,v128\nds_read_b128 a[20:23],v240 offset:54272\nM v[16:19],a[72:75],v[64:67],0\nv_exp_f32 v129,v129\n" \
"M v[16:19],a[104:107],v[80:83],v[16:19]\nv_exp_f32 v130,v130\nv_cvt_pk_bf16_f32 v192,v128,v129\nM v[24:27],a[76:79],v[64:67],0\nv_exp_f32 v131,v131\nds_read_b128 a[24:27],v240 offset:55296\nM v[24:27],a[108:111],v[80:83],v[24:27]\nv_cvt_pk_bf16_f32 v193,v130,v131\n" \
"ds_read_b128 a[28:31],v240 offset:56320\nM v[32:35],a[80:83],v[64:67],0\nv_exp_f32 v132,v132\nv_max3_f32 v233,v136,v137,v138\nM v[32:35],a[112:115],v[80:83],v[32:35]\nv_exp_f32 v133,v133\nM v[40:43],a[84:87],v[64:67],0\nv_exp_f32 v134,v134\n" \
"v_cvt_pk_bf16_f32 v196,v132,v133\nM v[40:43],a[116:119],v[80:83],v[40:43]\nv_exp_f32 v135,v135\ns_waitcnt lgkmcnt(3)\nM v[48:51],a[88:91],v[64:67],0\nv_cvt_pk_bf16_f32 v197,v134,v135\nv_max3_f32 v237,v237,v157,v158\nM v[48:51],a[120:123],v[80:83],v[48:51]\n" \
"v_exp_f32 v144,v144\nv_max_f32 v233,v233,v139\nM v[56:59],a[92:95],v[64:67],0\nv_exp_f32 v145,v145\nv_max_f32 v237,v237,v159\nM v[56:59],a[124:127],v[80:83],v[56:59]\nv_exp_f32 v146,v146\nM a[192:195],a[208:211],v[64:67],0\n" \
"v_exp_f32 v147,v147\nv_cvt_pk_bf16_f32 v194,v144,v145\nM a[192:195],a[208:211],v[80:83],a[192:195]\nv_cvt_pk_bf16_f32 v195,v146,v147\nv_cndmask_b32 v233,v232,v233,s[90:91]\nM v[4:7],a[64:67],v[68:71],0\nv_cndmask_b32 v237,v236,v237,s[90:91]\nv_exp_f32 v148,v148\n" \
"M v[4:7],a[96:99],v[84:87],v[4:7]\nv_permlane16_swap_b32 v233,v237\nv_max_f32 v233,v233,v237\nv_mov_b32 v237,v233\nM v[12:15],a[68:71],v[68:71],0\nv_exp_f32 v149,v149\nM v[12:15],a[100:103],v[84:87],v[12:15]\nv_exp_f32 v150,v150\n" \
"v_permlane32_swap_b32 v233,v237\nM v[20:23],a[72:75],v[68:71],0\nv_exp_f32 v151,v151\nv_cvt_pk_bf16_f32 v198,v148,v149\nM v[20:23],a[104:107],v[84:87],v[20:23]\nv_cvt_pk_bf16_f32 v199,v150,v151\nv_max_f32 v233,v233,v237\nM v[28:31],a[76:79],v[68:71],0\n" \
"v_mov_b32 v237,v233\nv_cndmask_b32 v136,v136,v252,s[82:83]\nv_cndmask_b32 v137,v137,v252,s[84:85]\nM v[28:31],a[108:111],v[84:87],v[28:31]\nv_permlane16_swap_b32 v233,v237\nv_cndmask_b32 v138,v138,v252,s[86:87]\nv_cndmask_b32 v139,v139,v252,s[88:89]\nM v[36:39],a[80:83],v[68:71],0\n" \
"v_sub_f32 v232,0,v233\nv_sub_f32 v234,0,v233\nM v[36:39],a[112:115],v[84:87],v[36:39]\nv_sub_f32 v235,0,v233\nv_add_f32 v136,v136,v232\nv_add_f32 v137,v137,v232\nM v[44:47],a[84:87],v[68:71],0\nv_add_f32 v138,v138,v232\n" \
"v_add_f32 v139,v139,v232\nM v[44:47],a[116:119],v[84:87],v[44:47]\nv_sub_f32 v233,0,v233\nv_exp_f32 v136,v136\nM v[52:55],a[88:91],v[68:71],0\nv_exp_f32 v137,v137\nv_sub_f32 v236,0,v237\nM v[52:55],a[120:123],v[84:87],v[52:55]\n" \
"v_exp_f32 v138,v138\nM v[60:63],a[92:95],v[68:71],0\nv_exp_f32 v139,v139\nv_cvt_pk_bf16_f32 v200,v136,v137\nM v[60:63],a[124:127],v[84:87],v[60:63]\nv_cvt_pk_bf16_f32 v201,v138,v139\nv_sub_f32 v238,0,v237\nv_sub_f32 v239,0,v237\n" \
"M a[196:199],a[208:211],v[68:71],0\nv_add_f32 v140,v140,v236\nv_add_f32 v141,v141,v236\nM a[196:199],a[208:211],v[84:87],a[196:199]\nv_add_f32 v142,v142,v236\nv_add_f32 v143,v143,v236\nv_sub_f32 v237,0,v237\nM v[128:131],a[0:3],a[128:131],v[224:227]\n" \
"v_exp_f32 v140,v140\nds_read_b128 a[32:35],v240 offset:57344\nM v[128:131],a[4:7],a[132:135],v[128:131]\nv_exp_f32 v141,v141\nM v[128:131],a[8:11],a[136:139],v[128:131]\nv_exp_f32 v142,v142\nv_cvt_pk_bf16_f32 v204,v140,v141\nM v[128:131],a[12:15],a[140:143],v[128:131]\n" \
"v_exp_f32 v143,v143\nM v[132:135],a[12:15],a[156:159],v[228:231]\nv_cvt_pk_bf16_f32 v205,v142,v143\nds_read_b128 a[36:39],v240 offset:58368\nds_read_b128 a[40:43],v240 offset:59392\nM v[132:135],a[8:11],a[152:155],v[132:135]\nds_read_b128 a[44:47],v240 offset:60416\nv_cndmask_b32 v156,v156,v252,s[82:83]\n" \
"v_cndmask_b32 v157,v157,v252,s[84:85]\nM v[132:135],a[4:7],a[148:151],v[132:135]\nv_cndmask_b32 v158,v158,v252,s[86:87]\nv_cndmask_b32 v159,v159,v252,s[88:89]\nM v[132:135],a[0:3],a[144:147],v[132:135]\nv_add_f32 v156,v156,v236\nv_add_f32 v157,v157,v236\nv_add_f32 v158,v158,v236\n" \
"M v[144:147],a[16:19],a[128:131],v[224:227]\nv_add_f32 v159,v159,v236\nds_read_b128 a[48:51],v240 offset:61440\ns_waitcnt lgkmcnt(5)\nM v[144:147],a[20:23],a[132:135],v[144:147]\nv_exp_f32 v156,v156\nM v[144:147],a[24:27],a[136:139],v[144:147]\nv_exp_f32 v157,v157\n" \
"ds_read_b128 a[52:55],v240 offset:62464\nM v[144:147],a[28:31],a[140:143],v[144:147]\nv_exp_f32 v158,v158\nM v[148:151],a[28:31],a[156:159],v[228:231]\nv_exp_f32 v159,v159\nv_cvt_pk_bf16_f32 v206,v156,v157\nM v[148:151],a[24:27],a[152:155],v[148:151]\nv_cvt_pk_bf16_f32 v207,v158,v159\n" \
"v_exp_f32 v160,v160\nM v[148:151],a[20:23],a[148:151],v[148:151]\nv_exp_f32 v161,v161\nM v[148:151],a[16:19],a[144:147],v[148:151]\nv_exp_f32 v162,v162\nv_cvt_pk_bf16_f32 v208,v160,v161\nM v[136:139],a[0:3],a[160:163],v[232:235]\ns_waitcnt vmcnt(1)\n" \
"s_barrier\nds_read_b128 a[56:59],v240 offset:63488\nM v[136:139],a[4:7],a[164:167],v[136:139]\nv_exp_f32 v163,v163\nM v[136:139],a[8:11],a[168:171],v[136:139]\nv_cvt_pk_bf16_f32 v209,v162,v163\nds_read_b128 a[60:63],v240 offset:64512\nds_read_b128 a[64:67],v241 offset:32768\n" \
"M v[136:139],a[12:15],a[172:175],v[136:139]\nv_exp_f32 v164,v164\nds_read_b128 a[96:99],v241 offset:40960\nM v[140:143],a[12:15],a[188:191],v[236:239]\nv_exp_f32 v165,v165\nM v[140:143],a[8:11],a[184:187],v[140:143]\nv_exp_f32 v166,v166\nv_cvt_pk_bf16_f32 v212,v164,v165\n" \
"M v[140:143],a[4:7],a[180:183],v[140:143]\nv_exp_f32 v167,v167\nM v[140:143],a[0:3],a[176:179],v[140:143]\nv_cvt_pk_bf16_f32 v213,v166,v167\nv_exp_f32 v176,v176\nM v[152:155],a[16:19],a[160:163],v[232:235]\nv_exp_f32 v177,v177\nds_read_b128 a[68:71],v241 offset:33792\n" \
"M v[152:155],a[20:23],a[164:167],v[152:155]\nv_exp_f32 v178,v178\nM v[152:155],a[24:27],a[168:171],v[152:155]\nv_exp_f32 v179,v179\nv_cvt_pk_bf16_f32 v210,v176,v177\nM v[152:155],a[28:31],a[172:175],v[152:155]\nv_cvt_pk_bf16_f32 v211,v178,v179\nv_exp_f32 v180,v180\n" \
"M v[156:159],a[28:31],a[188:191],v[236:239]\nv_exp_f32 v181,v181\nM v[156:159],a[24:27],a[184:187],v[156:159]\nv_exp_f32 v182,v182\nv_cvt_pk_bf16_f32 v214,v180,v181\nM v[156:159],a[20:23],a[180:183],v[156:159]\nv_exp_f32 v183,v183\nds_read_b128 a[100:103],v241 offset:41984\n" \
"M v[156:159],a[16:19],a[176:179],v[156:159]\nv_cvt_pk_bf16_f32 v215,v182,v183\ns_waitcnt lgkmcnt(2)\nM v[160:163],a[32:35],a[128:131],v[224:227]\nds_read_b128 a[72:75],v241 offset:34816\nds_read_b128 a[104:107],v241 offset:43008\ns_waitcnt vmcnt(0)\nv_cmp_eq_u32 vcc,0,v248\n" \
"M v[160:163],a[36:39],a[132:135],v[160:163]\nds_read_b128 a[76:79],v241 offset:35840\ns_cbranch_vccnz .Lps3\n.Lpb3:\nM v[160:163],a[40:43],a[136:139],v[160:163]\nds_read_b128 a[108:111],v241 offset:44032\nv_exp_f32 v128,v128\nM v[160:163],a[44:47],a[140:143],v[160:163]\n" \
"ds_read_b128 a[80:83],v241 offset:36864\nv_exp_f32 v129,v129\nM v[164:167],a[44:47],a[156:159],v[228:231]\nds_read_b128 a[112:115],v241 offset:45056\ns_add_u32 m0,s64,65536\nM v[164:167],a[40:43],a[152:155],v[164:167]\nds_read_b128 a[84:87],v241 offset:37888\nbuffer_load_dwordx4 v240,s[48:51],s36 offen offset:0 sc1 lds\n" \
"M v[164:167],a[36:39],a[148:151],v[164:167]\nds_read_b128 a[116:119],v241 offset:46080\nv_exp_f32 v130,v130\nM v[164:167],a[32:35],a[144:147],v[164:167]\nv_exp_f32 v131,v131\nM v[176:179],a[48:51],a[128:131],v[224:227]\nds_read_b128 a[88:91],v241 offset:38912\nv_exp_f32 v132,v132\n" \
"M v[176:179],a[52:55],a[132:135],v[176:179]\nds_read_b128 a[120:123],v241 offset:47104\nds_read_b128 a[92:95],v241 offset:39936\nM v[176:179],a[56:59],a[136:139],v[176:179]\nds_read_b128 a[124:127],v241 offset:48128\nbuffer_load_dwordx4 v240,s[48:51],s36 offen offset:1024 sc1 lds\nM v[176:179],a[60:63],a[140:143],v[176:179]\nds_read_b128 a[0:3],v240 offset:0\n" \
"v_exp_f32 v133,v133\nM v[180:183],a[60:63],a[156:159],v[228:231]\nv_exp_f32 v134,v134\nM v[180:183],a[56:59],a[152:155],v[180:183]\nds_read_b128 a[4:7],v240 offset:1024\nv_exp_f32 v135,v135\nM v[180:183],a[52:55],a[148:151],v[180:183]\nds_read_b128 a[8:11],v240 offset:2048\n" \
"buffer_load_dwordx4 v240,s[48:51],s36 offen offset:2048 sc1 lds\nM v[180:183],a[48:51],a[144:147],v[180:183]\nds_read_b128 a[12:15],v240 offset:3072\nds_read_b128 a[16:19],v240 offset:4096\nM v[168:171],a[32:35],a[160:163],v[232:235]\nds_read_b128 a[20:23],v240 offset:5120\nv_exp_f32 v144,v144\nM v[168:171],a[36:39],a[164:167],v[168:171]\n" \
"ds_read_b128 a[24:27],v240 offset:6144\nv_exp_f32 v145,v145\nM v[168:171],a[40:43],a[168:171],v[168:171]\nbuffer_load_dwordx4 v240,s[48:51],s36 offen offset:3072 sc1 lds\nM v[168:171],a[44:47],a[172:175],v[168:171]\nds_read_b128 a[28:31],v240 offset:7168\nv_exp_f32 v146,v146\nM v[172:175],a[44:47],a[188:191],v[236:239]\n" \
"v_exp_f32 v147,v147\nM v[172:175],a[40:43],a[184:187],v[172:175]\ns_add_u32 m0,s64,16384\nv_exp_f32 v148,v148\nM v[172:175],a[36:39],a[180:183],v[172:175]\nbuffer_load_dwordx4 v240,s[44:47],s65 offen offset:0 sc1 lds\nM v[172:175],a[32:35],a[176:179],v[172:175]\nds_read_b128 a[32:35],v240 offset:8192\n" \
"ds_read_b128 a[36:39],v240 offset:9216\nM v[184:187],a[48:51],a[160:163],v[232:235]\nds_read_b128 a[40:43],v240 offset:10240\nv_exp_f32 v149,v149\nM v[184:187],a[52:55],a[164:167],v[184:187]\nds_read_b128 a[44:47],v240 offset:11264\nv_exp_f32 v150,v150\nM v[184:187],a[56:59],a[168:171],v[184:187]\n" \
"buffer_load_dwordx4 v240,s[44:47],s65 offen offset:1024 sc1 lds\nM v[184:187],a[60:63],a[172:175],v[184:187]\nv_exp_f32 v151,v151\nM v[188:191],a[60:63],a[188:191],v[236:239]\nv_exp_f32 v136,v136\nM v[188:191],a[56:59],a[184:187],v[188:191]\nv_exp_f32 v137,v137\nM v[188:191],a[52:55],a[180:183],v[188:191]\n" \
"buffer_load_dwordx4 v240,s[44:47],s65 offen offset:2048 sc1 lds\nM v[188:191],a[48:51],a[176:179],v[188:191]\nds_read_b128 a[48:51],v240 offset:12288\nds_read_b128 a[52:55],v240 offset:13312\nM v[0:3],a[64:67],v[192:195],v[0:3]\nds_read_b128 a[56:59],v240 offset:14336\nv_exp_f32 v138,v138\nM v[0:3],a[96:99],v[208:211],v[0:3]\n" \
"v_exp_f32 v139,v139\ns_waitcnt lgkmcnt(4)\nM v[8:11],a[68:71],v[192:195],v[8:11]\nbuffer_load_dwordx4 v240,s[44:47],s65 offen offset:3072 sc1 lds\nM v[8:11],a[100:103],v[208:211],v[8:11]\ns_add_u32 m0,s64,81920\nv_exp_f32 v152,v152\nM v[16:19],a[72:75],v[192:195],v[16:19]\n" \
"ds_read_b128 a[60:63],v240 offset:15360\nv_exp_f32 v153,v153\nM v[16:19],a[104:107],v[208:211],v[16:19]\nv_exp_f32 v154,v154\nM v[24:27],a[76:79],v[192:195],v[24:27]\nbuffer_load_dwordx4 v240,s[48:51],s65 offen offset:0 sc1 lds\nM v[24:27],a[108:111],v[208:211],v[24:27]\nv_exp_f32 v155,v155\n" \
"M v[32:35],a[80:83],v[192:195],v[32:35]\nv_exp_f32 v160,v160\nM v[32:35],a[112:115],v[208:211],v[32:35]\nv_exp_f32 v161,v161\nM v[40:43],a[84:87],v[192:195],v[40:43]\nbuffer_load_dwordx4 v240,s[48:51],s65 offen offset:1024 sc1 lds\nM v[40:43],a[116:119],v[208:211],v[40:43]\nv_exp_f32 v162,v162\n" \
"M v[48:51],a[88:91],v[192:195],v[48:51]\nv_exp_f32 v163,v163\nM v[48:51],a[120:123],v[208:211],v[48:51]\nv_exp_f32 v176,v176\nM v[56:59],a[92:95],v[192:195],v[56:59]\nbuffer_load_dwordx4 v240,s[48:51],s65 offen offset:2048 sc1 lds\nM v[56:59],a[124:127],v[208:211],v[56:59]\nv_exp_f32 v177,v177\n" \
"M a[192:195],a[208:211],v[192:195],a[192:195]\nv_exp_f32 v178,v178\nM a[192:195],a[208:211],v[208:211],a[192:195]\nv_exp_f32 v179,v179\nv_cvt_pk_bf16_f32 v192,v128,v129\nM v[4:7],a[64:67],v[196:199],v[4:7]\nbuffer_load_dwordx4 v240,s[48:51],s65 offen offset:3072 sc1 lds\nM v[4:7],a[96:99],v[212:215],v[4:7]\n" \
"s_add_u32 m0,s64,32768\nv_exp_f32 v140,v140\nM v[12:15],a[68:71],v[196:199],v[12:15]\nv_exp_f32 v141,v141\nv_cvt_pk_bf16_f32 v193,v130,v131\nM v[12:15],a[100:103],v[212:215],v[12:15]\nv_exp_f32 v142,v142\nM v[20:23],a[72:75],v[196:199],v[20:23]\n" \
"buffer_load_dwordx4 v240,s[44:47],s66 offen offset:0 sc1 lds\nv_cvt_pk_bf16_f32 v194,v144,v145\nM v[20:23],a[104:107],v[212:215],v[20:23]\nv_exp_f32 v143,v143\nv_cvt_pk_bf16_f32 v195,v146,v147\nM v[28:31],a[76:79],v[196:199],v[28:31]\ns_barrier\nv_cvt_pk_bf16_f32 v208,v160,v161\n" \
"M v[28:31],a[108:111],v[212:215],v[28:31]\nv_exp_f32 v156,v156\nv_cvt_pk_bf16_f32 v209,v162,v163\nM v[36:39],a[80:83],v[196:199],v[36:39]\nbuffer_load_dwordx4 v240,s[44:47],s66 offen offset:1024 sc1 lds\nM v[36:39],a[112:115],v[212:215],v[36:39]\nv_exp_f32 v157,v157\nv_cvt_pk_bf16_f32 v210,v176,v177\n" \
"M v[44:47],a[84:87],v[196:199],v[44:47]\nv_exp_f32 v158,v158\nv_cvt_pk_bf16_f32 v211,v178,v179\nM v[44:47],a[116:119],v[212:215],v[44:47]\nv_exp_f32 v159,v159\nM v[52:55],a[88:91],v[196:199],v[52:55]\nbuffer_load_dwordx4 v240,s[44:47],s66 offen offset:2048 sc1 lds\nM v[52:55],a[120:123],v[212:215],v[52:55]\n" \
"v_exp_f32 v164,v164\nM v[60:63],a[92:95],v[196:199],v[60:63]\nv_exp_f32 v165,v165\nM v[60:63],a[124:127],v[212:215],v[60:63]\nv_exp_f32 v166,v166\nM a[196:199],a[208:211],v[196:199],a[196:199]\nbuffer_load_dwordx4 v240,s[44:47],s66 offen offset:3072 sc1 lds\ns_add_u32 m0,s64,98304\n" \
"M a[196:199],a[208:211],v[212:215],a[196:199]\nv_exp_f32 v167,v167\nM v[64:67],a[64:67],v[200:203],0\nv_exp_f32 v180,v180\nv_cvt_pk_bf16_f32 v196,v132,v133\nM v[72:75],a[68:71],v[200:203],0\nv_exp_f32 v181,v181\nM v[80:83],a[72:75],v[200:203],0\n" \
"v_exp_f32 v182,v182\nv_cvt_pk_bf16_f32 v197,v134,v135\nM v[88:91],a[76:79],v[200:203],0\nv_exp_f32 v183,v183\nv_cvt_pk_bf16_f32 v198,v148,v149\nM v[96:99],a[80:83],v[200:203],0\nv_exp_f32 v168,v168\nM v[104:107],a[84:87],v[200:203],0\n" \
"v_exp_f32 v169,v169\nv_cvt_pk_bf16_f32 v199,v150,v151\nM v[112:115],a[88:91],v[200:203],0\nv_exp_f32 v170,v170\nM v[120:123],a[92:95],v[200:203],0\nv_exp_f32 v171,v171\nM a[200:203],a[208:211],v[200:203],0\nv_cvt_pk_bf16_f32 v200,v136,v137\n" \
"v_cvt_pk_bf16_f32 v201,v138,v139\nv_cvt_pk_bf16_f32 v202,v152,v153\nM v[68:71],a[64:67],v[204:207],0\nv_cvt_pk_bf16_f32 v203,v154,v155\nbuffer_load_dwordx4 v240,s[48:51],s66 offen offset:0 sc1 lds\nM v[76:79],a[68:71],v[204:207],0\nM v[84:87],a[72:75],v[204:207],0\nM v[92:95],a[76:79],v[204:207],0\n" \
"M v[100:103],a[80:83],v[204:207],0\nbuffer_load_dwordx4 v240,s[48:51],s66 offen offset:1024 sc1 lds\nM v[108:111],a[84:87],v[204:207],0\nM v[116:119],a[88:91],v[204:207],0\nM v[124:127],a[92:95],v[204:207],0\nbuffer_load_dwordx4 v240,s[48:51],s66 offen offset:2048 sc1 lds\nbuffer_load_dwordx4 v240,s[48:51],s66 offen offset:3072 sc1 lds\nM a[204:207],a[208:211],v[204:207],0\n" \
"s_cmp_lt_u32 s62,2\ns_cbranch_scc1 .Lx01\ns_branch .Ldual\n.Lp23:\ns_cmp_eq_u32 s7,2\ns_cbranch_scc0 .Lpro3\n.Lpro2:\ns_barrier\n" \
"M v[64:67],a[0:3],a[128:131],0\nds_read_b128 a[64:67],v241 offset:0\nds_read_b128 a[96:99],v241 offset:8192\nM v[64:67],a[4:7],a[132:135],v[64:67]\nds_read_b128 a[68:71],v241 offset:1024\ns_mov_b32 m0,s64\nv_mov_b32 v184,0xff800000\nM v[64:67],a[8:11],a[136:139],v[64:67]\n" \
"ds_read_b128 a[100:103],v241 offset:9216\nv_mov_b32 v185,0xff800000\nM v[64:67],a[12:15],a[140:143],v[64:67]\nds_read_b128 a[72:75],v241 offset:2048\nbuffer_load_dwordx4 v240,s[44:47],s36 offen offset:0 sc1 lds\nM v[68:71],a[12:15],a[156:159],0\nds_read_b128 a[12:15],v240 offset:19456\nv_mov_b32 v186,0xff800000\n" \
"v_mov_b32 v187,0xff800000\nM v[68:71],a[8:11],a[152:155],v[68:71]\nds_read_b128 a[8:11],v240 offset:18432\nv_max3_f32 v225,v64,v65,v66\nM v[68:71],a[4:7],a[148:151],v[68:71]\nds_read_b128 a[4:7],v240 offset:17408\nM v[68:71],a[0:3],a[144:147],v[68:71]\nbuffer_load_dwordx4 v240,s[44:47],s36 offen offset:1024 sc1 lds\n" \
"M v[72:75],a[16:19],a[128:131],0\nds_read_b128 a[0:3],v240 offset:16384\nds_read_b128 a[104:107],v241 offset:10240\nM v[72:75],a[20:23],a[132:135],v[72:75]\nds_read_b128 a[76:79],v241 offset:3072\nM v[72:75],a[24:27],a[136:139],v[72:75]\nds_read_b128 a[108:111],v241 offset:11264\nv_max3_f32 v229,v68,v69,v70\n" \
"M v[72:75],a[28:31],a[140:143],v[72:75]\nds_read_b128 a[80:83],v241 offset:4096\nbuffer_load_dwordx4 v240,s[44:47],s36 offen offset:2048 sc1 lds\nM v[76:79],a[28:31],a[156:159],0\nds_read_b128 a[28:31],v240 offset:23552\nM v[76:79],a[24:27],a[152:155],v[76:79]\nds_read_b128 a[24:27],v240 offset:22528\nM v[76:79],a[20:23],a[148:151],v[76:79]\n" \
"ds_read_b128 a[20:23],v240 offset:21504\nv_max3_f32 v225,v225,v67,v72\nv_max3_f32 v225,v225,v73,v74\nM v[76:79],a[16:19],a[144:147],v[76:79]\nv_max_f32 v225,v225,v75\nv_cndmask_b32 v225,v224,v225,s[90:91]\nM v[80:83],a[32:35],a[128:131],0\nds_read_b128 a[16:19],v240 offset:20480\n" \
"buffer_load_dwordx4 v240,s[44:47],s36 offen offset:3072 sc1 lds\nM v[80:83],a[36:39],a[132:135],v[80:83]\nds_read_b128 a[112:115],v241 offset:12288\nds_read_b128 a[84:87],v241 offset:5120\nM v[80:83],a[40:43],a[136:139],v[80:83]\nv_max3_f32 v229,v229,v71,v76\nv_max3_f32 v229,v229,v77,v78\nv_max_f32 v229,v229,v79\n" \
"M v[80:83],a[44:47],a[140:143],v[80:83]\nv_cndmask_b32 v229,v228,v229,s[90:91]\nds_read_b128 a[116:119],v241 offset:13312\nds_read_b128 a[88:91],v241 offset:6144\nM v[84:87],a[44:47],a[156:159],0\nv_permlane16_swap_b32 v225,v229\nv_max_f32 v225,v225,v229\nM v[84:87],a[40:43],a[152:155],v[84:87]\n" \
"v_mov_b32 v229,v225\nds_read_b128 a[120:123],v241 offset:14336\nds_read_b128 a[92:95],v241 offset:7168\nM v[84:87],a[36:39],a[148:151],v[84:87]\nv_permlane32_swap_b32 v225,v229\nv_max_f32 v225,v225,v229\nM v[84:87],a[32:35],a[144:147],v[84:87]\nv_mov_b32 v229,v225\n" \
"ds_read_b128 a[124:127],v241 offset:15360\nds_read_b128 a[32:35],v240 offset:40960\nM v[88:91],a[48:51],a[128:131],0\nv_permlane16_swap_b32 v225,v229\nv_sub_f32 v224,0,v225\nv_sub_f32 v226,0,v225\nM v[88:91],a[52:55],a[132:135],v[88:91]\nv_sub_f32 v227,0,v225\n" \
"v_sub_f32 v225,0,v225\nM v[88:91],a[56:59],a[136:139],v[88:91]\nv_sub_f32 v228,0,v229\nv_sub_f32 v230,0,v229\nv_sub_f32 v231,0,v229\nM v[88:91],a[60:63],a[140:143],v[88:91]\nv_sub_f32 v229,0,v229\nv_add_f32 v64,v64,v224\n" \
"M v[92:95],a[60:63],a[156:159],0\nv_add_f32 v65,v65,v224\nv_add_f32 v66,v66,v224\nv_add_f32 v67,v67,v224\nM v[92:95],a[56:59],a[152:155],v[92:95]\nv_exp_f32 v64,v64\nv_add_f32 v72,v72,v224\nM v[92:95],a[52:55],a[148:151],v[92:95]\n" \
"v_exp_f32 v65,v65\nM v[92:95],a[48:51],a[144:147],v[92:95]\nv_exp_f32 v66,v66\ns_waitcnt lgkmcnt(2)\nM v[96:99],a[0:3],a[128:131],v[224:227]\nv_exp_f32 v67,v67\nv_add_f32 v73,v73,v224\nM v[96:99],a[4:7],a[132:135],v[96:99]\n" \
"v_add_f32 v74,v74,v224\nv_add_f32 v75,v75,v224\nM v[96:99],a[8:11],a[136:139],v[96:99]\nv_add_f32 v80,v80,v224\nv_add_f32 v81,v81,v224\nv_add_f32 v82,v82,v224\nM v[96:99],a[12:15],a[140:143],v[96:99]\nv_exp_f32 v72,v72\n" \
"M v[100:103],a[12:15],a[156:159],v[228:231]\nv_exp_f32 v73,v73\nv_add_f32 v83,v83,v224\nM v[100:103],a[8:11],a[152:155],v[100:103]\nv_exp_f32 v74,v74\nv_cvt_pk_bf16_f32 v64,v64,v65\nM v[100:103],a[4:7],a[148:151],v[100:103]\nv_exp_f32 v75,v75\n" \
"M v[100:103],a[0:3],a[144:147],v[100:103]\nv_exp_f32 v80,v80\nv_add_f32 v88,v88,v224\nM v[108:111],a[28:31],a[156:159],v[228:231]\nv_exp_f32 v81,v81\nM v[108:111],a[24:27],a[152:155],v[108:111]\nv_exp_f32 v82,v82\nv_add_f32 v89,v89,v224\n" \
"M v[108:111],a[20:23],a[148:151],v[108:111]\nv_exp_f32 v83,v83\nv_add_f32 v90,v90,v224\nv_add_f32 v91,v91,v224\nv_cvt_pk_bf16_f32 v65,v66,v67\nv_exp_f32 v88,v88\nv_exp_f32 v89,v89\nv_exp_f32 v90,v90\n" \
"v_exp_f32 v91,v91\nv_cvt_pk_bf16_f32 v66,v72,v73\nv_cvt_pk_bf16_f32 v67,v74,v75\nv_cvt_pk_bf16_f32 v80,v80,v81\nM v[108:111],a[16:19],a[144:147],v[108:111]\nv_cvt_pk_bf16_f32 v81,v82,v83\nv_cvt_pk_bf16_f32 v82,v88,v89\nv_cvt_pk_bf16_f32 v83,v90,v91\n" \
"M v[0:3],a[64:67],v[64:67],0\nv_add_f32 v68,v68,v228\nv_add_f32 v69,v69,v228\nv_add_f32 v70,v70,v228\nM v[0:3],a[96:99],v[80:83],v[0:3]\nv_add_f32 v71,v71,v228\nv_add_f32 v76,v76,v228\nM v[8:11],a[68:71],v[64:67],0\n" \
"v_exp_f32 v68,v68\nv_add_f32 v77,v77,v228\nM v[8:11],a[100:103],v[80:83],v[8:11]\nv_exp_f32 v69,v69\nv_add_f32 v78,v78,v228\nM v[16:19],a[72:75],v[64:67],0\nv_exp_f32 v70,v70\nM v[16:19],a[104:107],v[80:83],v[16:19]\n" \
"v_exp_f32 v71,v71\nv_add_f32 v79,v79,v228\nM v[24:27],a[76:79],v[64:67],0\nv_add_f32 v84,v84,v228\nv_add_f32 v85,v85,v228\nM v[24:27],a[108:111],v[80:83],v[24:27]\nv_exp_f32 v76,v76\nv_add_f32 v86,v86,v228\n" \
"M v[32:35],a[80:83],v[64:67],0\nv_exp_f32 v77,v77\nv_add_f32 v87,v87,v228\nM v[32:35],a[112:115],v[80:83],v[32:35]\nv_exp_f32 v78,v78\nM v[40:43],a[84:87],v[64:67],0\nv_exp_f32 v79,v79\nv_cvt_pk_bf16_f32 v68,v68,v69\n" \
"M v[40:43],a[116:119],v[80:83],v[40:43]\nv_exp_f32 v84,v84\nM v[48:51],a[88:91],v[64:67],0\nv_exp_f32 v85,v85\nv_add_f32 v92,v92,v228\nM v[48:51],a[120:123],v[80:83],v[48:51]\nv_exp_f32 v86,v86\nv_add_f32 v93,v93,v228\n" \
"M v[56:59],a[92:95],v[64:67],0\nv_add_f32 v94,v94,v228\ns_waitcnt lgkmcnt(0)\nM v[56:59],a[124:127],v[80:83],v[56:59]\nv_exp_f32 v87,v87\nv_add_f32 v95,v95,v228\nM a[192:195],a[208:211],v[64:67],0\nv_cvt_pk_bf16_f32 v69,v70,v71\n" \
"v_exp_f32 v92,v92\nv_exp_f32 v93,v93\nv_exp_f32 v94,v94\nv_exp_f32 v95,v95\nv_cvt_pk_bf16_f32 v70,v76,v77\nv_cvt_pk_bf16_f32 v71,v78,v79\nv_cvt_pk_bf16_f32 v84,v84,v85\nM a[192:195],a[208:211],v[80:83],a[192:195]\n" \
"v_cvt_pk_bf16_f32 v85,v86,v87\nv_cvt_pk_bf16_f32 v86,v92,v93\nv_cvt_pk_bf16_f32 v87,v94,v95\nM v[4:7],a[64:67],v[68:71],0\nds_read_b128 a[0:3],v240 offset:32768\nds_read_b128 a[4:7],v240 offset:33792\nv_cndmask_b32 v96,v96,v252,s[82:83]\nM v[4:7],a[96:99],v[84:87],v[4:7]\n" \
"ds_read_b128 a[8:11],v240 offset:34816\nv_cndmask_b32 v97,v97,v252,s[84:85]\nM v[12:15],a[68:71],v[68:71],0\nds_read_b128 a[12:15],v240 offset:35840\nv_cndmask_b32 v98,v98,v252,s[86:87]\nv_cndmask_b32 v99,v99,v252,s[88:89]\nM v[12:15],a[100:103],v[84:87],v[12:15]\nds_read_b128 a[16:19],v240 offset:36864\n" \
"v_exp_f32 v96,v96\nM v[20:23],a[72:75],v[68:71],0\nds_read_b128 a[20:23],v240 offset:37888\nv_cndmask_b32 v108,v108,v252,s[82:83]\nM v[20:23],a[104:107],v[84:87],v[20:23]\nds_read_b128 a[24:27],v240 offset:38912\nv_exp_f32 v97,v97\nM v[28:31],a[76:79],v[68:71],0\n" \
"ds_read_b128 a[28:31],v240 offset:39936\nv_cvt_pk_bf16_f32 v96,v96,v97\nM v[28:31],a[108:111],v[84:87],v[28:31]\nds_read_b128 a[36:39],v240 offset:41984\nv_exp_f32 v98,v98\nM v[36:39],a[80:83],v[68:71],0\nds_read_b128 a[40:43],v240 offset:43008\nv_exp_f32 v99,v99\n" \
"M v[36:39],a[112:115],v[84:87],v[36:39]\nds_read_b128 a[44:47],v240 offset:44032\nv_cvt_pk_bf16_f32 v97,v98,v99\nM v[44:47],a[84:87],v[68:71],0\nds_read_b128 a[48:51],v240 offset:45056\nv_mov_b32 v98,0\nv_mov_b32 v99,0\nM v[44:47],a[116:119],v[84:87],v[44:47]\n" \
"ds_read_b128 a[52:55],v240 offset:46080\nv_cndmask_b32 v109,v109,v252,s[84:85]\nM v[52:55],a[88:91],v[68:71],0\nbuffer_load_dword v248,v251,s[52:55],s80 offen sc1\nds_read_b128 a[56:59],v240 offset:47104\nM v[52:55],a[120:123],v[84:87],v[52:55]\nds_read_b128 a[60:63],v240 offset:48128\nv_exp_f32 v100,v100\n" \
"M v[60:63],a[92:95],v[68:71],0\nds_read_b128 a[64:67],v241 offset:16384\nv_cndmask_b32 v110,v110,v252,s[86:87]\nM v[60:63],a[124:127],v[84:87],v[60:63]\nds_read_b128 a[68:71],v241 offset:17408\nv_exp_f32 v101,v101\nM a[196:199],a[208:211],v[68:71],0\nds_read_b128 a[72:75],v241 offset:18432\n" \
"v_cndmask_b32 v111,v111,v252,s[88:89]\nM a[196:199],a[208:211],v[84:87],a[196:199]\nds_read_b128 a[76:79],v241 offset:19456\nv_cvt_pk_bf16_f32 v100,v100,v101\ns_waitcnt lgkmcnt(9)\nM v[128:131],a[0:3],a[128:131],v[224:227]\nds_read_b128 a[80:83],v241 offset:20480\nv_exp_f32 v102,v102\n" \
"M v[128:131],a[4:7],a[132:135],v[128:131]\nv_exp_f32 v103,v103\nM v[128:131],a[8:11],a[136:139],v[128:131]\nds_read_b128 a[84:87],v241 offset:21504\nds_read_b128 a[88:91],v241 offset:22528\nv_cvt_pk_bf16_f32 v101,v102,v103\nM v[128:131],a[12:15],a[140:143],v[128:131]\nv_exp_f32 v108,v108\n" \
"M v[132:135],a[12:15],a[156:159],v[228:231]\nds_read_b128 a[92:95],v241 offset:23552\nv_exp_f32 v109,v109\nM v[132:135],a[8:11],a[152:155],v[132:135]\nv_exp_f32 v110,v110\nv_cvt_pk_bf16_f32 v102,v108,v109\nM v[132:135],a[4:7],a[148:151],v[132:135]\nv_exp_f32 v111,v111\n" \
"M v[132:135],a[0:3],a[144:147],v[132:135]\nv_cvt_pk_bf16_f32 v103,v110,v111\nds_read_b128 a[0:3],v240 offset:49152\nds_read_b128 a[4:7],v240 offset:50176\nM v[144:147],a[16:19],a[128:131],v[224:227]\nds_read_b128 a[8:11],v240 offset:51200\nv_exp_f32 v128,v128\nM v[144:147],a[20:23],a[132:135],v[144:147]\n" \
"v_exp_f32 v129,v129\nM v[144:147],a[24:27],a[136:139],v[144:147]\nds_read_b128 a[12:15],v240 offset:52224\nv_exp_f32 v130,v130\nM v[144:147],a[28:31],a[140:143],v[144:147]\nv_exp_f32 v131,v131\nM v[148:151],a[28:31],a[156:159],v[228:231]\nds_read_b128 a[28:31],v240 offset:56320\n" \
"v_cvt_pk_bf16_f32 v192,v128,v129\nv_cvt_pk_bf16_f32 v193,v130,v131\nM v[148:151],a[24:27],a[152:155],v[148:151]\nds_read_b128 a[24:27],v240 offset:55296\nv_exp_f32 v132,v132\nM v[148:151],a[20:23],a[148:151],v[148:151]\nds_read_b128 a[20:23],v240 offset:54272\nds_read_b128 a[96:99],v241 offset:40960\n" \
"M v[148:151],a[16:19],a[144:147],v[148:151]\nds_read_b128 a[16:19],v240 offset:53248\nv_exp_f32 v133,v133\nM v[160:163],a[32:35],a[128:131],v[224:227]\nv_exp_f32 v134,v134\nM v[160:163],a[36:39],a[132:135],v[160:163]\nv_exp_f32 v135,v135\nv_cvt_pk_bf16_f32 v196,v132,v133\n" \
"M v[160:163],a[40:43],a[136:139],v[160:163]\nv_cvt_pk_bf16_f32 v197,v134,v135\nds_read_b128 a[100:103],v241 offset:41984\ns_waitcnt lgkmcnt(7)\nM v[160:163],a[44:47],a[140:143],v[160:163]\nv_exp_f32 v144,v144\nM v[164:167],a[44:47],a[156:159],v[228:231]\nv_exp_f32 v145,v145\n" \
"ds_read_b128 a[44:47],v240 offset:60416\nM v[164:167],a[40:43],a[152:155],v[164:167]\nv_exp_f32 v146,v146\nM v[164:167],a[36:39],a[148:151],v[164:167]\nv_exp_f32 v147,v147\nds_read_b128 a[36:39],v240 offset:58368\nM v[164:167],a[32:35],a[144:147],v[164:167]\nds_read_b128 a[32:35],v240 offset:57344\n" \
"v_cvt_pk_bf16_f32 v194,v144,v145\nv_cvt_pk_bf16_f32 v195,v146,v147\nM v[176:179],a[48:51],a[128:131],v[224:227]\nds_read_b128 a[40:43],v240 offset:59392\nds_read_b128 a[104:107],v241 offset:43008\nM v[176:179],a[52:55],a[132:135],v[176:179]\nv_exp_f32 v148,v148\nds_read_b128 a[108:111],v241 offset:44032\n" \
"M v[176:179],a[56:59],a[136:139],v[176:179]\nv_exp_f32 v149,v149\nM v[176:179],a[60:63],a[140:143],v[176:179]\nv_exp_f32 v150,v150\nv_cvt_pk_bf16_f32 v198,v148,v149\nM v[180:183],a[60:63],a[156:159],v[228:231]\nv_exp_f32 v151,v151\nds_read_b128 a[60:63],v240 offset:64512\n" \
"M v[180:183],a[56:59],a[152:155],v[180:183]\nv_cvt_pk_bf16_f32 v199,v150,v151\nds_read_b128 a[56:59],v240 offset:63488\nM v[180:183],a[52:55],a[148:151],v[180:183]\nv_exp_f32 v160,v160\nds_read_b128 a[52:55],v240 offset:62464\nM v[180:183],a[48:51],a[144:147],v[180:183]\nds_read_b128 a[48:51],v240 offset:61440\n" \
"ds_read_b128 a[112:115],v241 offset:45056\nM v[0:3],a[64:67],v[96:99],v[0:3]\nv_exp_f32 v161,v161\nds_read_b128 a[116:119],v241 offset:46080\nM v[8:11],a[68:71],v[96:99],v[8:11]\nv_exp_f32 v162,v162\nv_cvt_pk_bf16_f32 v208,v160,v161\nM v[16:19],a[72:75],v[96:99],v[16:19]\n" \
"v_exp_f32 v163,v163\nM v[24:27],a[76:79],v[96:99],v[24:27]\nv_cvt_pk_bf16_f32 v209,v162,v163\nv_exp_f32 v164,v164\nM v[32:35],a[80:83],v[96:99],v[32:35]\nv_exp_f32 v165,v165\nds_read_b128 a[120:123],v241 offset:47104\nM v[40:43],a[84:87],v[96:99],v[40:43]\n" \
"v_exp_f32 v166,v166\nM v[48:51],a[88:91],v[96:99],v[48:51]\nv_exp_f32 v167,v167\nv_cvt_pk_bf16_f32 v212,v164,v165\nM v[56:59],a[92:95],v[96:99],v[56:59]\nv_cvt_pk_bf16_f32 v213,v166,v167\nds_read_b128 a[124:127],v241 offset:48128\nM a[192:195],a[208:211],v[96:99],a[192:195]\n" \
"v_exp_f32 v176,v176\ns_waitcnt vmcnt(0)\nv_cmp_eq_u32 vcc,0,v248\nM v[4:7],a[64:67],v[100:103],v[4:7]\nv_exp_f32 v177,v177\nds_read_b128 a[64:67],v241 offset:32768\nM v[12:15],a[68:71],v[100:103],v[12:15]\nv_exp_f32 v178,v178\n" \
"M v[20:23],a[72:75],v[100:103],v[20:23]\nv_exp_f32 v179,v179\nv_cvt_pk_bf16_f32 v210,v176,v177\nM v[28:31],a[76:79],v[100:103],v[28:31]\ns_barrier\nv_cvt_pk_bf16_f32 v211,v178,v179\nM v[36:39],a[80:83],v[100:103],v[36:39]\nv_exp_f32 v180,v180\n" \
"ds_read_b128 a[68:71],v241 offset:33792\nM v[44:47],a[84:87],v[100:103],v[44:47]\nv_exp_f32 v181,v181\nds_read_b128 a[72:75],v241 offset:34816\nM v[52:55],a[88:91],v[100:103],v[52:55]\nv_exp_f32 v182,v182\nM v[60:63],a[92:95],v[100:103],v[60:63]\nv_exp_f32 v183,v183\n" \
"v_cvt_pk_bf16_f32 v214,v180,v181\nM a[196:199],a[208:211],v[100:103],a[196:199]\nv_cvt_pk_bf16_f32 v215,v182,v183\nds_read_b128 a[76:79],v241 offset:35840\nM v[64:67],a[244:247],a[244:247],0\nds_read_b128 a[80:83],v241 offset:36864\nds_read_b128 a[84:87],v241 offset:37888\ns_cbranch_vccnz .Lps4\n" \
".Lpb4:\nM v[68:71],a[244:247],a[244:247],0\nds_read_b128 a[88:91],v241 offset:38912\ns_add_u32 m0,s64,65536\nM v[72:75],a[244:247],a[244:247],0\nbuffer_load_dwordx4 v240,s[48:51],s36 offen offset:0 sc1 lds\nM v[76:79],a[244:247],a[244:247],0\nds_read_b128 a[92:95],v241 offset:39936\n" \
"M v[80:83],a[244:247],a[244:247],0\nM v[84:87],a[244:247],a[244:247],0\nM v[88:91],a[244:247],a[244:247],0\nbuffer_load_dwordx4 v240,s[48:51],s36 offen offset:1024 sc1 lds\nM v[92:95],a[244:247],a[244:247],0\nM v[96:99],a[244:247],a[244:247],0\nM v[100:103],a[244:247],a[244:247],0\nM v[104:107],a[244:247],a[244:247],0\n" \
"buffer_load_dwordx4 v240,s[48:51],s36 offen offset:2048 sc1 lds\nM v[108:111],a[244:247],a[244:247],0\nM v[112:115],a[244:247],a[244:247],0\nM v[116:119],a[244:247],a[244:247],0\nM v[120:123],a[244:247],a[244:247],0\nbuffer_load_dwordx4 v240,s[48:51],s36 offen offset:3072 sc1 lds\ns_add_u32 m0,s64,16384\nM v[124:127],a[244:247],a[244:247],0\n" \
"M a[200:203],a[244:247],a[244:247],0\nM a[204:207],a[244:247],a[244:247],0\nM v[136:139],a[0:3],a[160:163],0\nbuffer_load_dwordx4 v240,s[44:47],s65 offen offset:0 sc1 lds\nM v[136:139],a[4:7],a[164:167],v[136:139]\nM v[136:139],a[8:11],a[168:171],v[136:139]\ns_waitcnt lgkmcnt(0)\nM v[136:139],a[12:15],a[172:175],v[136:139]\n" \
"M v[140:143],a[12:15],a[188:191],0\nbuffer_load_dwordx4 v240,s[44:47],s65 offen offset:1024 sc1 lds\nM v[140:143],a[8:11],a[184:187],v[140:143]\nM v[140:143],a[4:7],a[180:183],v[140:143]\nM v[140:143],a[0:3],a[176:179],v[140:143]\nM v[152:155],a[16:19],a[160:163],0\nbuffer_load_dwordx4 v240,s[44:47],s65 offen offset:2048 sc1 lds\nM v[152:155],a[20:23],a[164:167],v[152:155]\n" \
"v_max3_f32 v233,v136,v137,v138\nM v[152:155],a[24:27],a[168:171],v[152:155]\nM v[152:155],a[28:31],a[172:175],v[152:155]\nM v[156:159],a[28:31],a[188:191],0\nbuffer_load_dwordx4 v240,s[44:47],s65 offen offset:3072 sc1 lds\nM v[156:159],a[24:27],a[184:187],v[156:159]\nv_max3_f32 v237,v140,v141,v142\ns_add_u32 m0,s64,81920\n" \
"M v[156:159],a[20:23],a[180:183],v[156:159]\nM v[156:159],a[16:19],a[176:179],v[156:159]\nM v[128:131],a[0:3],a[128:131],v[224:227]\nv_max3_f32 v233,v233,v139,v152\nv_max3_f32 v233,v233,v153,v154\nv_max_f32 v233,v233,v155\nM v[128:131],a[4:7],a[132:135],v[128:131]\nbuffer_load_dwordx4 v240,s[48:51],s65 offen offset:0 sc1 lds\n" \
"M v[128:131],a[8:11],a[136:139],v[128:131]\nv_cndmask_b32 v233,v232,v233,s[90:91]\nv_max3_f32 v237,v237,v143,v156\nv_max3_f32 v237,v237,v157,v158\nM v[128:131],a[12:15],a[140:143],v[128:131]\nv_max_f32 v237,v237,v159\nv_cndmask_b32 v237,v236,v237,s[90:91]\nM v[132:135],a[12:15],a[156:159],v[228:231]\n" \
"M v[132:135],a[8:11],a[152:155],v[132:135]\nv_permlane16_swap_b32 v233,v237\nv_max_f32 v233,v233,v237\nv_mov_b32 v237,v233\nM v[132:135],a[4:7],a[148:151],v[132:135]\nbuffer_load_dwordx4 v240,s[48:51],s65 offen offset:1024 sc1 lds\nM v[132:135],a[0:3],a[144:147],v[132:135]\nv_permlane32_swap_b32 v233,v237\n" \
"v_max_f32 v233,v233,v237\nv_mov_b32 v237,v233\nM v[144:147],a[16:19],a[128:131],v[224:227]\nds_read_b128 a[0:3],v240 offset:0\nv_permlane16_swap_b32 v233,v237\nv_sub_f32 v232,0,v233\nM v[144:147],a[20:23],a[132:135],v[144:147]\nv_sub_f32 v234,0,v233\n" \
"v_sub_f32 v235,0,v233\nM v[144:147],a[24:27],a[136:139],v[144:147]\nv_sub_f32 v233,0,v233\nv_sub_f32 v236,0,v237\nv_sub_f32 v238,0,v237\nM v[144:147],a[28:31],a[140:143],v[144:147]\nv_sub_f32 v239,0,v237\nv_sub_f32 v237,0,v237\n" \
"M v[148:151],a[28:31],a[156:159],v[228:231]\nds_read_b128 a[4:7],v240 offset:1024\nds_read_b128 a[8:11],v240 offset:2048\nv_add_f32 v136,v136,v232\nM v[148:151],a[24:27],a[152:155],v[148:151]\nds_read_b128 a[12:15],v240 offset:3072\nbuffer_load_dwordx4 v240,s[48:51],s65 offen offset:2048 sc1 lds\nM v[148:151],a[20:23],a[148:151],v[148:151]\n" \
"v_add_f32 v137,v137,v232\nv_add_f32 v138,v138,v232\nM v[148:151],a[16:19],a[144:147],v[148:151]\nds_read_b128 a[16:19],v240 offset:4096\nds_read_b128 a[20:23],v240 offset:5120\nv_add_f32 v139,v139,v232\nM v[160:163],a[32:35],a[128:131],v[224:227]\nds_read_b128 a[24:27],v240 offset:6144\n" \
"v_add_f32 v152,v152,v232\nM v[160:163],a[36:39],a[132:135],v[160:163]\nds_read_b128 a[28:31],v240 offset:7168\nbuffer_load_dwordx4 v240,s[48:51],s65 offen offset:3072 sc1 lds\nM v[160:163],a[40:43],a[136:139],v[160:163]\ns_add_u32 m0,s64,32768\nv_add_f32 v153,v153,v232\nv_add_f32 v154,v154,v232\n" \
"M v[160:163],a[44:47],a[140:143],v[160:163]\nv_add_f32 v155,v155,v232\nv_add_f32 v140,v140,v236\nM v[164:167],a[44:47],a[156:159],v[228:231]\nv_exp_f32 v136,v136\nv_add_f32 v141,v141,v236\nM v[164:167],a[40:43],a[152:155],v[164:167]\nbuffer_load_dwordx4 v240,s[44:47],s66 offen offset:0 sc1 lds\n" \
"M v[164:167],a[36:39],a[148:151],v[164:167]\nv_exp_f32 v137,v137\nv_add_f32 v142,v142,v236\nM v[164:167],a[32:35],a[144:147],v[164:167]\nv_exp_f32 v138,v138\nv_add_f32 v143,v143,v236\nM v[176:179],a[48:51],a[128:131],v[224:227]\nv_exp_f32 v139,v139\n" \
"M v[176:179],a[52:55],a[132:135],v[176:179]\nbuffer_load_dwordx4 v240,s[44:47],s66 offen offset:1024 sc1 lds\nv_add_f32 v156,v156,v236\nM v[176:179],a[56:59],a[136:139],v[176:179]\nv_exp_f32 v152,v152\nM v[176:179],a[60:63],a[140:143],v[176:179]\nv_exp_f32 v153,v153\nv_add_f32 v157,v157,v236\n" \
"M v[180:183],a[60:63],a[156:159],v[228:231]\nv_exp_f32 v154,v154\nv_add_f32 v158,v158,v236\nM v[180:183],a[56:59],a[152:155],v[180:183]\nbuffer_load_dwordx4 v240,s[44:47],s66 offen offset:2048 sc1 lds\nM v[180:183],a[52:55],a[148:151],v[180:183]\nv_exp_f32 v155,v155\nv_add_f32 v159,v159,v236\n" \
"M v[180:183],a[48:51],a[144:147],v[180:183]\nv_exp_f32 v128,v128\nM v[168:171],a[32:35],a[160:163],v[232:235]\ns_barrier\nv_exp_f32 v129,v129\nM v[168:171],a[36:39],a[164:167],v[168:171]\nbuffer_load_dwordx4 v240,s[44:47],s66 offen offset:3072 sc1 lds\ns_add_u32 m0,s64,98304\n" \
"M v[168:171],a[40:43],a[168:171],v[168:171]\nv_exp_f32 v130,v130\nM v[168:171],a[44:47],a[172:175],v[168:171]\nv_exp_f32 v131,v131\nv_cvt_pk_bf16_f32 v200,v136,v137\nM v[172:175],a[44:47],a[188:191],v[236:239]\nv_exp_f32 v132,v132\nv_cvt_pk_bf16_f32 v201,v138,v139\n" \
"M v[172:175],a[40:43],a[184:187],v[172:175]\nv_exp_f32 v133,v133\nM v[172:175],a[36:39],a[180:183],v[172:175]\nv_exp_f32 v134,v134\nv_cndmask_b32 v168,v168,v252,s[82:83]\nM v[172:175],a[32:35],a[176:179],v[172:175]\nds_read_b128 a[32:35],v240 offset:8192\nds_read_b128 a[36:39],v240 offset:9216\n" \
"M v[188:191],a[60:63],a[188:191],v[236:239]\nds_read_b128 a[40:43],v240 offset:10240\nv_exp_f32 v135,v135\nM v[188:191],a[56:59],a[184:187],v[188:191]\nds_read_b128 a[44:47],v240 offset:11264\nv_exp_f32 v144,v144\nM v[188:191],a[52:55],a[180:183],v[188:191]\nv_exp_f32 v145,v145\n" \
"M v[188:191],a[48:51],a[176:179],v[188:191]\nds_read_b128 a[48:51],v240 offset:12288\nds_read_b128 a[52:55],v240 offset:13312\nv_cndmask_b32 v169,v169,v252,s[84:85]\nM v[0:3],a[64:67],v[192:195],v[0:3]\nds_read_b128 a[56:59],v240 offset:14336\nv_cndmask_b32 v170,v170,v252,s[86:87]\nM v[0:3],a[96:99],v[208:211],v[0:3]\n" \
"ds_read_b128 a[60:63],v240 offset:15360\nv_exp_f32 v146,v146\nM v[8:11],a[68:71],v[192:195],v[8:11]\nv_exp_f32 v147,v147\nv_cndmask_b32 v171,v171,v252,s[88:89]\nM v[8:11],a[100:103],v[208:211],v[8:11]\nv_exp_f32 v148,v148\nM v[16:19],a[72:75],v[192:195],v[16:19]\n" \
"v_exp_f32 v149,v149\nv_cndmask_b32 v188,v188,v252,s[82:83]\nM v[16:19],a[104:107],v[208:211],v[16:19]\nv_exp_f32 v150,v150\nM v[24:27],a[76:79],v[192:195],v[24:27]\nv_exp_f32 v151,v151\nv_cndmask_b32 v189,v189,v252,s[84:85]\nM v[24:27],a[108:111],v[208:211],v[24:27]\n" \
"v_exp_f32 v160,v160\nv_cndmask_b32 v190,v190,v252,s[86:87]\nM v[32:35],a[80:83],v[192:195],v[32:35]\nv_exp_f32 v161,v161\nM v[32:35],a[112:115],v[208:211],v[32:35]\nv_exp_f32 v162,v162\nv_cndmask_b32 v191,v191,v252,s[88:89]\nM v[40:43],a[84:87],v[192:195],v[40:43]\n" \
"v_exp_f32 v163,v163\nM v[40:43],a[116:119],v[208:211],v[40:43]\nv_exp_f32 v176,v176\nv_cvt_pk_bf16_f32 v202,v152,v153\nM v[48:51],a[88:91],v[192:195],v[48:51]\nv_exp_f32 v177,v177\nv_cvt_pk_bf16_f32 v203,v154,v155\nM v[48:51],a[120:123],v[208:211],v[48:51]\n" \
"v_exp_f32 v178,v178\nM v[56:59],a[92:95],v[192:195],v[56:59]\nv_exp_f32 v179,v179\nM v[56:59],a[124:127],v[208:211],v[56:59]\nv_exp_f32 v140,v140\nM a[192:195],a[208:211],v[192:195],a[192:195]\nv_exp_f32 v141,v141\nv_cvt_pk_bf16_f32 v192,v128,v129\n" \
"M a[192:195],a[208:211],v[208:211],a[192:195]\nv_exp_f32 v142,v142\nv_cvt_pk_bf16_f32 v193,v130,v131\nM v[4:7],a[64:67],v[196:199],v[4:7]\nv_exp_f32 v143,v143\nM v[4:7],a[96:99],v[212:215],v[4:7]\nv_exp_f32 v156,v156\nv_cvt_pk_bf16_f32 v194,v144,v145\n" \
"M v[12:15],a[68:71],v[196:199],v[12:15]\nv_exp_f32 v157,v157\nv_cvt_pk_bf16_f32 v195,v146,v147\nM v[12:15],a[100:103],v[212:215],v[12:15]\nv_exp_f32 v158,v158\nM v[20:23],a[72:75],v[196:199],v[20:23]\nv_exp_f32 v159,v159\nv_cvt_pk_bf16_f32 v208,v160,v161\n" \
"M v[20:23],a[104:107],v[212:215],v[20:23]\nv_exp_f32 v164,v164\nM v[28:31],a[76:79],v[196:199],v[28:31]\nv_exp_f32 v165,v165\nv_cvt_pk_bf16_f32 v209,v162,v163\nM v[28:31],a[108:111],v[212:215],v[28:31]\nv_exp_f32 v166,v166\nv_cvt_pk_bf16_f32 v210,v176,v177\n" \
"M v[36:39],a[80:83],v[196:199],v[36:39]\nv_exp_f32 v167,v167\nM v[36:39],a[112:115],v[212:215],v[36:39]\nv_exp_f32 v180,v180\nv_cvt_pk_bf16_f32 v211,v178,v179\nM v[44:47],a[84:87],v[196:199],v[44:47]\nv_exp_f32 v181,v181\nM v[44:47],a[116:119],v[212:215],v[44:47]\n" \
"v_exp_f32 v182,v182\nM v[52:55],a[88:91],v[196:199],v[52:55]\nv_exp_f32 v183,v183\nM v[52:55],a[120:123],v[212:215],v[52:55]\nv_exp_f32 v168,v168\nM v[60:63],a[92:95],v[196:199],v[60:63]\nv_exp_f32 v169,v169\nM v[60:63],a[124:127],v[212:215],v[60:63]\n" \
"v_exp_f32 v170,v170\nM a[196:199],a[208:211],v[196:199],a[196:199]\nv_exp_f32 v171,v171\nv_cvt_pk_bf16_f32 v196,v132,v133\nv_cvt_pk_bf16_f32 v197,v134,v135\nv_cvt_pk_bf16_f32 v198,v148,v149\nv_cvt_pk_bf16_f32 v199,v150,v151\nbuffer_load_dwordx4 v240,s[48:51],s66 offen offset:0 sc1 lds\n" \
"buffer_load_dwordx4 v240,s[48:51],s66 offen offset:1024 sc1 lds\nbuffer_load_dwordx4 v240,s[48:51],s66 offen offset:2048 sc1 lds\nbuffer_load_dwordx4 v240,s[48:51],s66 offen offset:3072 sc1 lds\nM a[196:199],a[208:211],v[212:215],a[196:199]\ns_cmp_lt_u32 s62,2\ns_cbranch_scc1 .Lx01\ns_branch .Ldual\n.Lpro3:\n" \
"s_barrier\nM v[64:67],a[0:3],a[128:131],0\nds_read_b128 a[64:67],v241 offset:0\nds_read_b128 a[96:99],v241 offset:8192\nM v[64:67],a[4:7],a[132:135],v[64:67]\nds_read_b128 a[68:71],v241 offset:1024\ns_mov_b32 m0,s64\nv_mov_b32 v168,0\n" \
"M v[64:67],a[8:11],a[136:139],v[64:67]\nds_read_b128 a[100:103],v241 offset:9216\nv_mov_b32 v169,0\nM v[64:67],a[12:15],a[140:143],v[64:67]\nds_read_b128 a[72:75],v241 offset:2048\nbuffer_load_dwordx4 v240,s[44:47],s36 offen offset:0 sc1 lds\nM v[68:71],a[12:15],a[156:159],0\nds_read_b128 a[12:15],v240 offset:19456\n" \
"v_mov_b32 v170,0\nv_mov_b32 v171,0\nM v[68:71],a[8:11],a[152:155],v[68:71]\nds_read_b128 a[8:11],v240 offset:18432\nv_max3_f32 v225,v64,v65,v66\nM v[68:71],a[4:7],a[148:151],v[68:71]\nds_read_b128 a[4:7],v240 offset:17408\nv_mov_b32 v172,0xff800000\n" \
"v_mov_b32 v173,0xff800000\nM v[68:71],a[0:3],a[144:147],v[68:71]\nds_read_b128 a[0:3],v240 offset:16384\nv_mov_b32 v174,0xff800000\nM v[72:75],a[16:19],a[128:131],0\nds_read_b128 a[104:107],v241 offset:10240\nbuffer_load_dwordx4 v240,s[44:47],s36 offen offset:1024 sc1 lds\nM v[72:75],a[20:23],a[132:135],v[72:75]\n" \
"ds_read_b128 a[76:79],v241 offset:3072\nv_mov_b32 v175,0xff800000\nv_max3_f32 v229,v68,v69,v70\nM v[72:75],a[24:27],a[136:139],v[72:75]\nds_read_b128 a[108:111],v241 offset:11264\nv_mov_b32 v184,0xff800000\nM v[72:75],a[28:31],a[140:143],v[72:75]\nds_read_b128 a[80:83],v241 offset:4096\n" \
"v_mov_b32 v185,0xff800000\nv_mov_b32 v186,0xff800000\nM v[76:79],a[28:31],a[156:159],0\nds_read_b128 a[28:31],v240 offset:23552\nbuffer_load_dwordx4 v240,s[44:47],s36 offen offset:2048 sc1 lds\nM v[76:79],a[24:27],a[152:155],v[76:79]\nds_read_b128 a[24:27],v240 offset:22528\nv_max3_f32 v225,v225,v67,v72\n" \
"M v[76:79],a[20:23],a[148:151],v[76:79]\nv_max3_f32 v225,v225,v73,v74\nv_max_f32 v225,v225,v75\nv_cndmask_b32 v225,v224,v225,s[90:91]\nM v[76:79],a[16:19],a[144:147],v[76:79]\nds_read_b128 a[16:19],v240 offset:20480\nds_read_b128 a[20:23],v240 offset:21504\nM v[80:83],a[32:35],a[128:131],0\n" \
"ds_read_b128 a[112:115],v241 offset:12288\nbuffer_load_dwordx4 v240,s[44:47],s36 offen offset:3072 sc1 lds\nM v[80:83],a[36:39],a[132:135],v[80:83]\nds_read_b128 a[84:87],v241 offset:5120\nv_mov_b32 v187,0xff800000\nv_max3_f32 v229,v229,v71,v76\nM v[80:83],a[40:43],a[136:139],v[80:83]\nv_max3_f32 v229,v229,v77,v78\n" \
"v_max_f32 v229,v229,v79\nM v[80:83],a[44:47],a[140:143],v[80:83]\nv_cndmask_b32 v229,v228,v229,s[90:91]\nds_read_b128 a[116:119],v241 offset:13312\nds_read_b128 a[88:91],v241 offset:6144\nM v[84:87],a[44:47],a[156:159],0\nv_permlane16_swap_b32 v225,v229\nv_max_f32 v225,v225,v229\n" \
"M v[84:87],a[40:43],a[152:155],v[84:87]\nv_mov_b32 v229,v225\nds_read_b128 a[40:43],v240 offset:26624\nds_read_b128 a[44:47],v240 offset:27648\nM v[84:87],a[36:39],a[148:151],v[84:87]\nv_permlane32_swap_b32 v225,v229\nv_max_f32 v225,v225,v229\nv_mov_b32 v229,v225\n" \
"M v[84:87],a[32:35],a[144:147],v[84:87]\nds_read_b128 a[32:35],v240 offset:24576\nv_permlane16_swap_b32 v225,v229\nM v[88:91],a[48:51],a[128:131],0\nv_sub_f32 v224,0,v225\nv_sub_f32 v226,0,v225\nv_sub_f32 v227,0,v225\nM v[88:91],a[52:55],a[132:135],v[88:91]\n" \
"v_sub_f32 v225,0,v225\nv_sub_f32 v228,0,v229\nv_sub_f32 v230,0,v229\nM v[88:91],a[56:59],a[136:139],v[88:91]\nv_sub_f32 v231,0,v229\nv_sub_f32 v229,0,v229\nM v[88:91],a[60:63],a[140:143],v[88:91]\nds_read_b128 a[36:39],v240 offset:25600\n" \
"v_add_f32 v64,v64,v224\nv_add_f32 v65,v65,v224\nM v[92:95],a[60:63],a[156:159],0\nds_read_b128 a[60:63],v240 offset:31744\nv_add_f32 v66,v66,v224\nM v[92:95],a[56:59],a[152:155],v[92:95]\nds_read_b128 a[56:59],v240 offset:30720\nv_add_f32 v67,v67,v224\n" \
"v_add_f32 v72,v72,v224\nM v[92:95],a[52:55],a[148:151],v[92:95]\nds_read_b128 a[52:55],v240 offset:29696\nv_exp_f32 v64,v64\nM v[92:95],a[48:51],a[144:147],v[92:95]\nds_read_b128 a[48:51],v240 offset:28672\ns_waitcnt lgkmcnt(6)\nM v[96:99],a[0:3],a[128:131],v[224:227]\n" \
"v_exp_f32 v65,v65\nv_add_f32 v73,v73,v224\nM v[96:99],a[4:7],a[132:135],v[96:99]\nv_exp_f32 v66,v66\nM v[96:99],a[8:11],a[136:139],v[96:99]\nv_exp_f32 v67,v67\nv_add_f32 v74,v74,v224\nM v[96:99],a[12:15],a[140:143],v[96:99]\n" \
"v_add_f32 v75,v75,v224\nv_add_f32 v80,v80,v224\nv_add_f32 v81,v81,v224\nM v[100:103],a[12:15],a[156:159],v[228:231]\nv_exp_f32 v72,v72\nM v[100:103],a[8:11],a[152:155],v[100:103]\nv_exp_f32 v73,v73\nv_add_f32 v82,v82,v224\n" \
"M v[100:103],a[4:7],a[148:151],v[100:103]\nv_exp_f32 v74,v74\nv_add_f32 v83,v83,v224\nM v[100:103],a[0:3],a[144:147],v[100:103]\nv_exp_f32 v75,v75\nM v[104:107],a[16:19],a[128:131],v[224:227]\nv_exp_f32 v80,v80\nv_cvt_pk_bf16_f32 v64,v64,v65\n" \
"M v[104:107],a[20:23],a[132:135],v[104:107]\nv_exp_f32 v81,v81\nM v[104:107],a[24:27],a[136:139],v[104:107]\nv_exp_f32 v82,v82\nv_add_f32 v88,v88,v224\nM v[104:107],a[28:31],a[140:143],v[104:107]\nv_exp_f32 v83,v83\nv_add_f32 v89,v89,v224\n" \
"M v[108:111],a[28:31],a[156:159],v[228:231]\nv_add_f32 v90,v90,v224\nv_add_f32 v91,v91,v224\nM v[108:111],a[24:27],a[152:155],v[108:111]\nv_cvt_pk_bf16_f32 v65,v66,v67\nv_exp_f32 v88,v88\nM v[108:111],a[20:23],a[148:151],v[108:111]\nv_exp_f32 v89,v89\n" \
"v_cvt_pk_bf16_f32 v66,v72,v73\nM v[108:111],a[16:19],a[144:147],v[108:111]\nv_cvt_pk_bf16_f32 v67,v74,v75\ns_waitcnt lgkmcnt(0)\nM v[112:115],a[32:35],a[128:131],v[224:227]\nv_exp_f32 v90,v90\nv_cvt_pk_bf16_f32 v80,v80,v81\nM v[112:115],a[36:39],a[132:135],v[112:115]\n" \
"v_exp_f32 v91,v91\nM v[112:115],a[40:43],a[136:139],v[112:115]\nv_cvt_pk_bf16_f32 v81,v82,v83\nv_cvt_pk_bf16_f32 v82,v88,v89\nv_cvt_pk_bf16_f32 v83,v90,v91\nM v[112:115],a[44:47],a[140:143],v[112:115]\nds_read_b128 a[120:123],v241 offset:14336\nds_read_b128 a[92:95],v241 offset:7168\n" \
"v_add_f32 v68,v68,v228\nM v[116:119],a[44:47],a[156:159],v[228:231]\nds_read_b128 a[124:127],v241 offset:15360\nv_add_f32 v69,v69,v228\nM v[116:119],a[40:43],a[152:155],v[116:119]\nv_add_f32 v70,v70,v228\nv_add_f32 v71,v71,v228\nv_add_f32 v76,v76,v228\n" \
"M v[116:119],a[36:39],a[148:151],v[116:119]\nv_exp_f32 v68,v68\nM v[116:119],a[32:35],a[144:147],v[116:119]\nv_exp_f32 v69,v69\nv_add_f32 v77,v77,v228\nM v[124:127],a[60:63],a[156:159],v[228:231]\nv_exp_f32 v70,v70\nv_add_f32 v78,v78,v228\n" \
"M v[124:127],a[56:59],a[152:155],v[124:127]\nv_exp_f32 v71,v71\nM v[124:127],a[52:55],a[148:151],v[124:127]\nv_add_f32 v79,v79,v228\nv_add_f32 v84,v84,v228\nv_add_f32 v85,v85,v228\nM v[124:127],a[48:51],a[144:147],v[124:127]\nv_exp_f32 v76,v76\n" \
"v_add_f32 v86,v86,v228\nM v[0:3],a[64:67],v[64:67],0\nv_exp_f32 v77,v77\nM v[0:3],a[96:99],v[80:83],v[0:3]\nv_exp_f32 v78,v78\nv_add_f32 v87,v87,v228\nM v[8:11],a[68:71],v[64:67],0\nv_exp_f32 v79,v79\n" \
"M v[8:11],a[100:103],v[80:83],v[8:11]\nv_exp_f32 v84,v84\nv_cvt_pk_bf16_f32 v68,v68,v69\nM v[16:19],a[72:75],v[64:67],0\nv_exp_f32 v85,v85\nv_add_f32 v92,v92,v228\nM v[16:19],a[104:107],v[80:83],v[16:19]\nv_exp_f32 v86,v86\n" \
"M v[24:27],a[76:79],v[64:67],0\nv_exp_f32 v87,v87\nv_add_f32 v93,v93,v228\nM v[24:27],a[108:111],v[80:83],v[24:27]\nv_add_f32 v94,v94,v228\nv_add_f32 v95,v95,v228\nM v[32:35],a[80:83],v[64:67],0\nv_cvt_pk_bf16_f32 v69,v70,v71\n" \
"v_exp_f32 v92,v92\nM v[32:35],a[112:115],v[80:83],v[32:35]\nv_exp_f32 v93,v93\nv_cvt_pk_bf16_f32 v70,v76,v77\nM v[40:43],a[84:87],v[64:67],0\nv_exp_f32 v94,v94\nM v[40:43],a[116:119],v[80:83],v[40:43]\nv_exp_f32 v95,v95\n" \
"v_cvt_pk_bf16_f32 v71,v78,v79\nM v[48:51],a[88:91],v[64:67],0\nv_cvt_pk_bf16_f32 v84,v84,v85\nv_cvt_pk_bf16_f32 v85,v86,v87\ns_waitcnt lgkmcnt(0)\nM v[48:51],a[120:123],v[80:83],v[48:51]\nv_cvt_pk_bf16_f32 v86,v92,v93\nv_cvt_pk_bf16_f32 v87,v94,v95\n" \
"M v[56:59],a[92:95],v[64:67],0\nbuffer_load_dword v248,v251,s[52:55],s80 offen sc1\nds_read_b128 a[0:3],v240 offset:32768\nM v[56:59],a[124:127],v[80:83],v[56:59]\nds_read_b128 a[4:7],v240 offset:33792\nds_read_b128 a[8:11],v240 offset:34816\nM a[192:195],a[208:211],v[64:67],0\nds_read_b128 a[12:15],v240 offset:35840\n" \
"v_exp_f32 v96,v96\nM a[192:195],a[208:211],v[80:83],a[192:195]\nds_read_b128 a[16:19],v240 offset:36864\nv_exp_f32 v97,v97\nM v[4:7],a[64:67],v[68:71],0\nds_read_b128 a[20:23],v240 offset:37888\nv_cndmask_b32 v112,v112,v252,s[82:83]\nM v[4:7],a[96:99],v[84:87],v[4:7]\n" \
"ds_read_b128 a[24:27],v240 offset:38912\nv_exp_f32 v98,v98\nM v[12:15],a[68:71],v[68:71],0\nds_read_b128 a[28:31],v240 offset:39936\nv_exp_f32 v99,v99\nM v[12:15],a[100:103],v[84:87],v[12:15]\nds_read_b128 a[32:35],v240 offset:40960\nv_cndmask_b32 v113,v113,v252,s[84:85]\n" \
"M v[20:23],a[72:75],v[68:71],0\nds_read_b128 a[36:39],v240 offset:41984\nv_exp_f32 v104,v104\nM v[20:23],a[104:107],v[84:87],v[20:23]\nds_read_b128 a[40:43],v240 offset:43008\nv_cndmask_b32 v114,v114,v252,s[86:87]\nM v[28:31],a[76:79],v[68:71],0\nds_read_b128 a[44:47],v240 offset:44032\n" \
"v_exp_f32 v105,v105\nM v[28:31],a[108:111],v[84:87],v[28:31]\nds_read_b128 a[48:51],v240 offset:45056\nv_exp_f32 v106,v106\nM v[36:39],a[80:83],v[68:71],0\nds_read_b128 a[52:55],v240 offset:46080\nv_cndmask_b32 v115,v115,v252,s[88:89]\nM v[36:39],a[112:115],v[84:87],v[36:39]\n" \
"ds_read_b128 a[56:59],v240 offset:47104\nv_exp_f32 v107,v107\nM v[44:47],a[84:87],v[68:71],0\nds_read_b128 a[60:63],v240 offset:48128\nv_cvt_pk_bf16_f32 v96,v96,v97\nM v[44:47],a[116:119],v[84:87],v[44:47]\nds_read_b128 a[64:67],v241 offset:16384\nv_exp_f32 v112,v112\n" \
"M v[52:55],a[88:91],v[68:71],0\nds_read_b128 a[96:99],v241 offset:24576\nv_exp_f32 v113,v113\nM v[52:55],a[120:123],v[84:87],v[52:55]\nds_read_b128 a[68:71],v241 offset:17408\nv_cvt_pk_bf16_f32 v97,v98,v99\nM v[60:63],a[92:95],v[68:71],0\nds_read_b128 a[100:103],v241 offset:25600\n" \
"v_exp_f32 v114,v114\nM v[60:63],a[124:127],v[84:87],v[60:63]\nds_read_b128 a[72:75],v241 offset:18432\nv_exp_f32 v115,v115\nM a[196:199],a[208:211],v[68:71],0\nds_read_b128 a[104:107],v241 offset:26624\nv_cvt_pk_bf16_f32 v98,v104,v105\nM a[196:199],a[208:211],v[84:87],a[196:199]\n" \
"ds_read_b128 a[76:79],v241 offset:19456\nv_cvt_pk_bf16_f32 v99,v106,v107\ns_waitcnt lgkmcnt(9)\nM v[128:131],a[0:3],a[128:131],v[224:227]\nds_read_b128 a[108:111],v241 offset:27648\nv_cvt_pk_bf16_f32 v112,v112,v113\nM v[128:131],a[4:7],a[132:135],v[128:131]\nds_read_b128 a[80:83],v241 offset:20480\n" \
"v_cvt_pk_bf16_f32 v113,v114,v115\nv_mov_b32 v114,0\nM v[128:131],a[8:11],a[136:139],v[128:131]\nds_read_b128 a[112:115],v241 offset:28672\nv_mov_b32 v115,0\nv_cndmask_b32 v124,v124,v252,s[82:83]\nM v[128:131],a[12:15],a[140:143],v[128:131]\nds_read_b128 a[84:87],v241 offset:21504\n" \
"v_cndmask_b32 v125,v125,v252,s[84:85]\nM v[132:135],a[12:15],a[156:159],v[228:231]\nds_read_b128 a[116:119],v241 offset:29696\nv_exp_f32 v100,v100\nM v[132:135],a[8:11],a[152:155],v[132:135]\nds_read_b128 a[88:91],v241 offset:22528\nv_cndmask_b32 v126,v126,v252,s[86:87]\nM v[132:135],a[4:7],a[148:151],v[132:135]\n" \
"ds_read_b128 a[120:123],v241 offset:30720\nv_exp_f32 v101,v101\nM v[132:135],a[0:3],a[144:147],v[132:135]\nds_read_b128 a[92:95],v241 offset:23552\nv_exp_f32 v102,v102\nM v[144:147],a[16:19],a[128:131],v[224:227]\nds_read_b128 a[124:127],v241 offset:31744\nv_cvt_pk_bf16_f32 v100,v100,v101\n" \
"M v[144:147],a[20:23],a[132:135],v[144:147]\nv_exp_f32 v103,v103\nv_cndmask_b32 v127,v127,v252,s[88:89]\nM v[144:147],a[24:27],a[136:139],v[144:147]\nv_exp_f32 v108,v108\nv_cvt_pk_bf16_f32 v101,v102,v103\nM v[144:147],a[28:31],a[140:143],v[144:147]\nv_exp_f32 v109,v109\n" \
"M v[148:151],a[28:31],a[156:159],v[228:231]\nv_exp_f32 v110,v110\nv_cvt_pk_bf16_f32 v102,v108,v109\nM v[148:151],a[24:27],a[152:155],v[148:151]\nv_exp_f32 v111,v111\nM v[148:151],a[20:23],a[148:151],v[148:151]\nv_exp_f32 v116,v116\nv_cvt_pk_bf16_f32 v103,v110,v111\n" \
"M v[148:151],a[16:19],a[144:147],v[148:151]\nv_exp_f32 v117,v117\nds_read_b128 a[0:3],v240 offset:49152\nM v[160:163],a[32:35],a[128:131],v[224:227]\nv_exp_f32 v118,v118\nM v[160:163],a[36:39],a[132:135],v[160:163]\nv_exp_f32 v119,v119\nv_cvt_pk_bf16_f32 v116,v116,v117\n" \
"M v[160:163],a[40:43],a[136:139],v[160:163]\nv_exp_f32 v124,v124\nv_cvt_pk_bf16_f32 v117,v118,v119\nM v[160:163],a[44:47],a[140:143],v[160:163]\nv_exp_f32 v125,v125\nM v[164:167],a[44:47],a[156:159],v[228:231]\nv_exp_f32 v126,v126\nv_cvt_pk_bf16_f32 v118,v124,v125\n" \
"M v[164:167],a[40:43],a[152:155],v[164:167]\nv_exp_f32 v127,v127\nM v[164:167],a[36:39],a[148:151],v[164:167]\nv_cvt_pk_bf16_f32 v119,v126,v127\nds_read_b128 a[4:7],v240 offset:50176\nds_read_b128 a[8:11],v240 offset:51200\nM v[164:167],a[32:35],a[144:147],v[164:167]\nds_read_b128 a[12:15],v240 offset:52224\n" \
"v_exp_f32 v128,v128\nM v[176:179],a[48:51],a[128:131],v[224:227]\nds_read_b128 a[28:31],v240 offset:56320\ns_waitcnt vmcnt(0)\nv_cmp_eq_u32 vcc,0,v248\nM v[176:179],a[52:55],a[132:135],v[176:179]\nds_read_b128 a[24:27],v240 offset:55296\ns_cbranch_vccnz .Lps5\n" \
".Lpb5:\ns_waitcnt lgkmcnt(5)\nM v[176:179],a[56:59],a[136:139],v[176:179]\nds_read_b128 a[20:23],v240 offset:54272\nv_mov_b32 v188,0xff800000\nM v[176:179],a[60:63],a[140:143],v[176:179]\nds_read_b128 a[16:19],v240 offset:53248\nv_exp_f32 v129,v129\n" \
"M v[180:183],a[60:63],a[156:159],v[228:231]\ns_barrier\nv_exp_f32 v130,v130\nM v[180:183],a[56:59],a[152:155],v[180:183]\nv_exp_f32 v131,v131\nM v[180:183],a[52:55],a[148:151],v[180:183]\nv_cvt_pk_bf16_f32 v192,v128,v129\nv_cvt_pk_bf16_f32 v193,v130,v131\n" \
"ds_read_b128 a[32:35],v240 offset:57344\nM v[180:183],a[48:51],a[144:147],v[180:183]\nds_read_b128 a[36:39],v240 offset:58368\nds_read_b128 a[40:43],v240 offset:59392\ns_add_u32 m0,s64,65536\nM v[0:3],a[64:67],v[96:99],v[0:3]\nds_read_b128 a[44:47],v240 offset:60416\nv_mov_b32 v189,0xff800000\n" \
"M v[0:3],a[96:99],v[112:115],v[0:3]\nds_read_b128 a[48:51],v240 offset:61440\nv_exp_f32 v132,v132\nM v[8:11],a[68:71],v[96:99],v[8:11]\nds_read_b128 a[52:55],v240 offset:62464\nv_mov_b32 v190,0xff800000\nM v[8:11],a[100:103],v[112:115],v[8:11]\nds_read_b128 a[56:59],v240 offset:63488\n" \
"v_exp_f32 v133,v133\nM v[16:19],a[72:75],v[96:99],v[16:19]\nds_read_b128 a[60:63],v240 offset:64512\nv_exp_f32 v134,v134\nM v[16:19],a[104:107],v[112:115],v[16:19]\nv_exp_f32 v135,v135\nM v[24:27],a[76:79],v[96:99],v[24:27]\nv_cvt_pk_bf16_f32 v196,v132,v133\n" \
"v_cvt_pk_bf16_f32 v197,v134,v135\nv_mov_b32 v191,0xff800000\nM v[24:27],a[108:111],v[112:115],v[24:27]\nv_exp_f32 v144,v144\nM v[32:35],a[80:83],v[96:99],v[32:35]\nv_exp_f32 v145,v145\nv_mov_b32 v202,0\nM v[32:35],a[112:115],v[112:115],v[32:35]\n" \
"v_exp_f32 v146,v146\nv_cvt_pk_bf16_f32 v194,v144,v145\nM v[40:43],a[84:87],v[96:99],v[40:43]\nv_exp_f32 v147,v147\nM v[40:43],a[116:119],v[112:115],v[40:43]\nv_cvt_pk_bf16_f32 v195,v146,v147\nv_exp_f32 v148,v148\nM v[48:51],a[88:91],v[96:99],v[48:51]\n" \
"v_exp_f32 v149,v149\nv_mov_b32 v203,0\nM v[48:51],a[120:123],v[112:115],v[48:51]\nv_exp_f32 v150,v150\nM v[56:59],a[92:95],v[96:99],v[56:59]\nv_exp_f32 v151,v151\nv_cvt_pk_bf16_f32 v198,v148,v149\nM v[56:59],a[124:127],v[112:115],v[56:59]\n" \
"v_exp_f32 v160,v160\nM a[192:195],a[208:211],v[96:99],a[192:195]\nv_cvt_pk_bf16_f32 v199,v150,v151\nv_exp_f32 v161,v161\nM a[192:195],a[208:211],v[112:115],a[192:195]\nv_exp_f32 v162,v162\nv_cvt_pk_bf16_f32 v208,v160,v161\nM v[4:7],a[64:67],v[100:103],v[4:7]\n" \
"v_exp_f32 v163,v163\nM v[4:7],a[96:99],v[116:119],v[4:7]\nds_read_b128 a[64:67],v241 offset:32768\nds_read_b128 a[96:99],v241 offset:40960\nv_cvt_pk_bf16_f32 v209,v162,v163\nM v[12:15],a[68:71],v[100:103],v[12:15]\nds_read_b128 a[68:71],v241 offset:33792\nv_exp_f32 v164,v164\n" \
"M v[12:15],a[100:103],v[116:119],v[12:15]\nv_exp_f32 v165,v165\nM v[20:23],a[72:75],v[100:103],v[20:23]\nds_read_b128 a[100:103],v241 offset:41984\nds_read_b128 a[72:75],v241 offset:34816\nv_cvt_pk_bf16_f32 v212,v164,v165\nM v[20:23],a[104:107],v[116:119],v[20:23]\nv_exp_f32 v166,v166\n" \
"M v[28:31],a[76:79],v[100:103],v[28:31]\nds_read_b128 a[104:107],v241 offset:43008\nv_exp_f32 v167,v167\nM v[28:31],a[108:111],v[116:119],v[28:31]\nds_read_b128 a[76:79],v241 offset:35840\nds_read_b128 a[108:111],v241 offset:44032\nv_cvt_pk_bf16_f32 v213,v166,v167\nM v[36:39],a[80:83],v[100:103],v[36:39]\n" \
"v_exp_f32 v176,v176\nM v[36:39],a[112:115],v[116:119],v[36:39]\nds_read_b128 a[80:83],v241 offset:36864\nv_exp_f32 v177,v177\nM v[44:47],a[84:87],v[100:103],v[44:47]\nds_read_b128 a[112:115],v241 offset:45056\nds_read_b128 a[84:87],v241 offset:37888\nM v[44:47],a[116:119],v[116:119],v[44:47]\n" \
"ds_read_b128 a[116:119],v241 offset:46080\nv_exp_f32 v178,v178\nM v[52:55],a[88:91],v[100:103],v[52:55]\nds_read_b128 a[88:91],v241 offset:38912\nv_exp_f32 v179,v179\nM v[52:55],a[120:123],v[116:119],v[52:55]\nds_read_b128 a[120:123],v241 offset:47104\nv_cvt_pk_bf16_f32 v210,v176,v177\n" \
"M v[60:63],a[92:95],v[100:103],v[60:63]\nds_read_b128 a[92:95],v241 offset:39936\nv_exp_f32 v180,v180\nM v[60:63],a[124:127],v[116:119],v[60:63]\nds_read_b128 a[124:127],v241 offset:48128\nv_exp_f32 v181,v181\nM a[196:199],a[208:211],v[100:103],a[196:199]\nv_cvt_pk_bf16_f32 v211,v178,v179\n" \
"v_cvt_pk_bf16_f32 v214,v180,v181\nM a[196:199],a[208:211],v[116:119],a[196:199]\nv_exp_f32 v182,v182\nM v[64:67],a[244:247],a[244:247],0\nv_exp_f32 v183,v183\nM v[68:71],a[244:247],a[244:247],0\nv_cvt_pk_bf16_f32 v215,v182,v183\nbuffer_load_dwordx4 v240,s[48:51],s36 offen offset:0 sc1 lds\n" \
"M v[72:75],a[244:247],a[244:247],0\nM v[76:79],a[244:247],a[244:247],0\nM v[80:83],a[244:247],a[244:247],0\nM v[84:87],a[244:247],a[244:247],0\nbuffer_load_dwordx4 v240,s[48:51],s36 offen offset:1024 sc1 lds\nM v[88:91],a[244:247],a[244:247],0\nM v[92:95],a[244:247],a[244:247],0\nM v[96:99],a[244:247],a[244:247],0\n" \
"M v[100:103],a[244:247],a[244:247],0\nbuffer_load_dwordx4 v240,s[48:51],s36 offen offset:2048 sc1 lds\nM v[104:107],a[244:247],a[244:247],0\nM v[108:111],a[244:247],a[244:247],0\nM v[112:115],a[244:247],a[244:247],0\nM v[116:119],a[244:247],a[244:247],0\nbuffer_load_dwordx4 v240,s[48:51],s36 offen offset:3072 sc1 lds\nM v[120:123],a[244:247],a[244:247],0\n" \
"s_add_u32 m0,s64,16384\nM v[124:127],a[244:247],a[244:247],0\nM a[200:203],a[244:247],a[244:247],0\nM a[204:207],a[244:247],a[244:247],0\nbuffer_load_dwordx4 v240,s[44:47],s65 offen offset:0 sc1 lds\nM v[136:139],a[0:3],a[160:163],0\ns_waitcnt lgkmcnt(0)\nM v[136:139],a[4:7],a[164:167],v[136:139]\n" \
"M v[136:139],a[8:11],a[168:171],v[136:139]\nM v[136:139],a[12:15],a[172:175],v[136:139]\nbuffer_load_dwordx4 v240,s[44:47],s65 offen offset:1024 sc1 lds\nM v[140:143],a[12:15],a[188:191],0\nM v[140:143],a[8:11],a[184:187],v[140:143]\nM v[140:143],a[4:7],a[180:183],v[140:143]\nM v[140:143],a[0:3],a[176:179],v[140:143]\nbuffer_load_dwordx4 v240,s[44:47],s65 offen offset:2048 sc1 lds\n" \
"M v[156:159],a[28:31],a[188:191],0\nM v[156:159],a[24:27],a[184:187],v[156:159]\nv_max3_f32 v233,v136,v137,v138\nv_max_f32 v233,v233,v139\nv_cndmask_b32 v233,v232,v233,s[90:91]\nM v[156:159],a[20:23],a[180:183],v[156:159]\nv_cndmask_b32 v136,v136,v252,s[82:83]\nv_max3_f32 v237,v140,v141,v142\n" \
"v_cndmask_b32 v137,v137,v252,s[84:85]\nM v[156:159],a[16:19],a[176:179],v[156:159]\nbuffer_load_dwordx4 v240,s[44:47],s65 offen offset:3072 sc1 lds\nM v[128:131],a[0:3],a[128:131],v[224:227]\ns_add_u32 m0,s64,81920\nv_cndmask_b32 v138,v138,v252,s[86:87]\nv_cndmask_b32 v139,v139,v252,s[88:89]\nM v[128:131],a[4:7],a[132:135],v[128:131]\n" \
"M v[128:131],a[8:11],a[136:139],v[128:131]\ns_barrier\nv_max3_f32 v237,v237,v143,v156\nM v[128:131],a[12:15],a[140:143],v[128:131]\nv_max3_f32 v237,v237,v157,v158\nv_max_f32 v237,v237,v159\nv_cndmask_b32 v237,v236,v237,s[90:91]\nM v[132:135],a[12:15],a[156:159],v[228:231]\n" \
"buffer_load_dwordx4 v240,s[48:51],s65 offen offset:0 sc1 lds\nM v[132:135],a[8:11],a[152:155],v[132:135]\nv_permlane16_swap_b32 v233,v237\nv_max_f32 v233,v233,v237\nv_mov_b32 v237,v233\nM v[132:135],a[4:7],a[148:151],v[132:135]\nv_cndmask_b32 v156,v156,v252,s[82:83]\nv_permlane32_swap_b32 v233,v237\n" \
"v_max_f32 v233,v233,v237\nM v[132:135],a[0:3],a[144:147],v[132:135]\nds_read_b128 a[0:3],v240 offset:0\nds_read_b128 a[4:7],v240 offset:1024\nM v[144:147],a[16:19],a[128:131],v[224:227]\nds_read_b128 a[8:11],v240 offset:2048\nv_mov_b32 v237,v233\nv_cndmask_b32 v157,v157,v252,s[84:85]\n" \
"M v[144:147],a[20:23],a[132:135],v[144:147]\nds_read_b128 a[12:15],v240 offset:3072\nv_permlane16_swap_b32 v233,v237\nM v[144:147],a[24:27],a[136:139],v[144:147]\nbuffer_load_dwordx4 v240,s[48:51],s65 offen offset:1024 sc1 lds\nv_sub_f32 v232,0,v233\nM v[144:147],a[28:31],a[140:143],v[144:147]\nv_add_f32 v136,v136,v232\n" \
"v_add_f32 v137,v137,v232\nv_add_f32 v138,v138,v232\nM v[148:151],a[28:31],a[156:159],v[228:231]\nv_add_f32 v139,v139,v232\nv_cndmask_b32 v158,v158,v252,s[86:87]\nM v[148:151],a[24:27],a[152:155],v[148:151]\nv_cndmask_b32 v159,v159,v252,s[88:89]\nv_sub_f32 v236,0,v237\n" \
"v_sub_f32 v234,0,v233\nM v[148:151],a[20:23],a[148:151],v[148:151]\nbuffer_load_dwordx4 v240,s[48:51],s65 offen offset:2048 sc1 lds\nv_sub_f32 v235,0,v233\nM v[148:151],a[16:19],a[144:147],v[148:151]\nds_read_b128 a[16:19],v240 offset:4096\nds_read_b128 a[20:23],v240 offset:5120\nM v[160:163],a[32:35],a[128:131],v[224:227]\n" \
"ds_read_b128 a[24:27],v240 offset:6144\nv_exp_f32 v136,v136\nM v[160:163],a[36:39],a[132:135],v[160:163]\nds_read_b128 a[28:31],v240 offset:7168\nv_sub_f32 v238,0,v237\nM v[160:163],a[40:43],a[136:139],v[160:163]\nbuffer_load_dwordx4 v240,s[48:51],s65 offen offset:3072 sc1 lds\ns_add_u32 m0,s64,32768\n" \
"M v[160:163],a[44:47],a[140:143],v[160:163]\nv_exp_f32 v137,v137\nv_sub_f32 v239,0,v237\nM v[164:167],a[44:47],a[156:159],v[228:231]\nv_exp_f32 v138,v138\nM v[164:167],a[40:43],a[152:155],v[164:167]\nv_exp_f32 v139,v139\nv_add_f32 v140,v140,v236\n" \
"M v[164:167],a[36:39],a[148:151],v[164:167]\nbuffer_load_dwordx4 v240,s[44:47],s66 offen offset:0 sc1 lds\nv_add_f32 v141,v141,v236\nM v[164:167],a[32:35],a[144:147],v[164:167]\nds_read_b128 a[32:35],v240 offset:8192\nds_read_b128 a[36:39],v240 offset:9216\nM v[176:179],a[48:51],a[128:131],v[224:227]\nds_read_b128 a[40:43],v240 offset:10240\n" \
"v_exp_f32 v128,v128\nM v[176:179],a[52:55],a[132:135],v[176:179]\nds_read_b128 a[44:47],v240 offset:11264\nv_add_f32 v142,v142,v236\nM v[176:179],a[56:59],a[136:139],v[176:179]\nbuffer_load_dwordx4 v240,s[44:47],s66 offen offset:1024 sc1 lds\nv_add_f32 v143,v143,v236\nM v[176:179],a[60:63],a[140:143],v[176:179]\n" \
"v_exp_f32 v129,v129\nv_add_f32 v156,v156,v236\nM v[180:183],a[60:63],a[156:159],v[228:231]\nv_exp_f32 v130,v130\nM v[180:183],a[56:59],a[152:155],v[180:183]\nv_exp_f32 v131,v131\nv_add_f32 v157,v157,v236\nM v[180:183],a[52:55],a[148:151],v[180:183]\n" \
"buffer_load_dwordx4 v240,s[44:47],s66 offen offset:2048 sc1 lds\nM v[180:183],a[48:51],a[144:147],v[180:183]\nds_read_b128 a[48:51],v240 offset:12288\nds_read_b128 a[52:55],v240 offset:13312\nv_add_f32 v158,v158,v236\nM v[0:3],a[64:67],v[192:195],v[0:3]\nds_read_b128 a[56:59],v240 offset:14336\nv_exp_f32 v132,v132\n" \
"M v[0:3],a[96:99],v[208:211],v[0:3]\nds_read_b128 a[60:63],v240 offset:15360\nv_add_f32 v159,v159,v236\nM v[8:11],a[68:71],v[192:195],v[8:11]\nbuffer_load_dwordx4 v240,s[44:47],s66 offen offset:3072 sc1 lds\ns_add_u32 m0,s64,98304\nM v[8:11],a[100:103],v[208:211],v[8:11]\nv_exp_f32 v133,v133\n" \
"v_sub_f32 v233,0,v233\nM v[16:19],a[72:75],v[192:195],v[16:19]\nv_exp_f32 v134,v134\nM v[16:19],a[104:107],v[208:211],v[16:19]\nv_exp_f32 v135,v135\nv_sub_f32 v237,0,v237\nM v[24:27],a[76:79],v[192:195],v[24:27]\nv_exp_f32 v144,v144\n" \
"M v[24:27],a[108:111],v[208:211],v[24:27]\nv_exp_f32 v145,v145\nv_cvt_pk_bf16_f32 v200,v136,v137\nM v[32:35],a[80:83],v[192:195],v[32:35]\nv_exp_f32 v146,v146\nv_cvt_pk_bf16_f32 v201,v138,v139\nM v[32:35],a[112:115],v[208:211],v[32:35]\nv_exp_f32 v147,v147\n" \
"M v[40:43],a[84:87],v[192:195],v[40:43]\nv_exp_f32 v148,v148\nM v[40:43],a[116:119],v[208:211],v[40:43]\nv_exp_f32 v149,v149\nM v[48:51],a[88:91],v[192:195],v[48:51]\nv_exp_f32 v150,v150\nM v[48:51],a[120:123],v[208:211],v[48:51]\nv_exp_f32 v151,v151\n" \
"M v[56:59],a[92:95],v[192:195],v[56:59]\nv_exp_f32 v160,v160\nM v[56:59],a[124:127],v[208:211],v[56:59]\nv_exp_f32 v161,v161\nM a[192:195],a[208:211],v[192:195],a[192:195]\nv_exp_f32 v162,v162\nv_cvt_pk_bf16_f32 v192,v128,v129\nM a[192:195],a[208:211],v[208:211],a[192:195]\n" \
"v_exp_f32 v163,v163\nM v[4:7],a[64:67],v[196:199],v[4:7]\nv_exp_f32 v176,v176\nv_cvt_pk_bf16_f32 v193,v130,v131\nM v[4:7],a[96:99],v[212:215],v[4:7]\nv_exp_f32 v177,v177\nM v[12:15],a[68:71],v[196:199],v[12:15]\nv_exp_f32 v178,v178\n" \
"v_cvt_pk_bf16_f32 v194,v144,v145\nM v[12:15],a[100:103],v[212:215],v[12:15]\nv_exp_f32 v179,v179\nv_cvt_pk_bf16_f32 v195,v146,v147\nM v[20:23],a[72:75],v[196:199],v[20:23]\nv_exp_f32 v140,v140\nM v[20:23],a[104:107],v[212:215],v[20:23]\nv_exp_f32 v141,v141\n" \
"v_cvt_pk_bf16_f32 v208,v160,v161\nM v[28:31],a[76:79],v[196:199],v[28:31]\nv_exp_f32 v142,v142\nv_cvt_pk_bf16_f32 v209,v162,v163\nM v[28:31],a[108:111],v[212:215],v[28:31]\nv_exp_f32 v143,v143\nM v[36:39],a[80:83],v[196:199],v[36:39]\nv_exp_f32 v156,v156\n" \
"v_cvt_pk_bf16_f32 v210,v176,v177\nM v[36:39],a[112:115],v[212:215],v[36:39]\nv_exp_f32 v157,v157\nM v[44:47],a[84:87],v[196:199],v[44:47]\nv_exp_f32 v158,v158\nv_cvt_pk_bf16_f32 v211,v178,v179\nM v[44:47],a[116:119],v[212:215],v[44:47]\nv_exp_f32 v159,v159\n" \
"M v[52:55],a[88:91],v[196:199],v[52:55]\nv_exp_f32 v164,v164\nM v[52:55],a[120:123],v[212:215],v[52:55]\nv_exp_f32 v165,v165\nM v[60:63],a[92:95],v[196:199],v[60:63]\nv_exp_f32 v166,v166\nM v[60:63],a[124:127],v[212:215],v[60:63]\nv_exp_f32 v167,v167\n" \
"M a[196:199],a[208:211],v[196:199],a[196:199]\nv_exp_f32 v180,v180\nv_exp_f32 v181,v181\nv_exp_f32 v182,v182\nv_exp_f32 v183,v183\nv_cvt_pk_bf16_f32 v196,v132,v133\nv_cvt_pk_bf16_f32 v197,v134,v135\nv_cvt_pk_bf16_f32 v198,v148,v149\n" \
"v_cvt_pk_bf16_f32 v199,v150,v151\nbuffer_load_dwordx4 v240,s[48:51],s66 offen offset:0 sc1 lds\nbuffer_load_dwordx4 v240,s[48:51],s66 offen offset:1024 sc1 lds\nbuffer_load_dwordx4 v240,s[48:51],s66 offen offset:2048 sc1 lds\nbuffer_load_dwordx4 v240,s[48:51],s66 offen offset:3072 sc1 lds\nM a[196:199],a[208:211],v[212:215],a[196:199]\ns_cmp_lt_u32 s62,2\ns_cbranch_scc1 .Lx01\n" \
".Ldual:\ns_waitcnt lgkmcnt(4)\nM v[128:131],a[0:3],a[128:131],v[224:227]\ns_add_u32 m0,s94,s64\nv_cvt_pk_bf16_f32 v204,v140,v141\nM v[128:131],a[4:7],a[132:135],v[128:131]\nds_read_b128 a[64:67],v250 offset:0\nds_read_b128 a[96:99],v250 offset:8192\n" \
"M v[128:131],a[8:11],a[136:139],v[128:131]\nv_cvt_pk_bf16_f32 v205,v142,v143\nv_cvt_pk_bf16_f32 v206,v156,v157\nM v[128:131],a[12:15],a[140:143],v[128:131]\nv_cvt_pk_bf16_f32 v207,v158,v159\nv_cvt_pk_bf16_f32 v212,v164,v165\nM v[132:135],a[12:15],a[156:159],v[228:231]\nbuffer_load_dwordx4 v240,s[44:47],s95 offen offset:0 sc1 lds\n" \
"M v[132:135],a[8:11],a[152:155],v[132:135]\nv_cvt_pk_bf16_f32 v213,v166,v167\nv_cvt_pk_bf16_f32 v216,v168,v169\nM v[132:135],a[4:7],a[148:151],v[132:135]\nv_cvt_pk_bf16_f32 v217,v170,v171\nv_cvt_pk_bf16_f32 v214,v180,v181\nM v[132:135],a[0:3],a[144:147],v[132:135]\nds_read_b128 a[68:71],v250 offset:1024\n" \
"ds_read_b128 a[100:103],v250 offset:9216\nM v[136:139],a[0:3],a[160:163],v[232:235]\nv_exp_f32 v172,v172\nM v[136:139],a[4:7],a[164:167],v[136:139]\nv_exp_f32 v173,v173\nM v[136:139],a[8:11],a[168:171],v[136:139]\nv_exp_f32 v174,v174\nM v[136:139],a[12:15],a[172:175],v[136:139]\n" \
"v_exp_f32 v175,v175\nM v[140:143],a[12:15],a[188:191],v[236:239]\nv_cvt_pk_bf16_f32 v220,v172,v173\nv_cvt_pk_bf16_f32 v221,v174,v175\nM v[140:143],a[8:11],a[184:187],v[140:143]\nds_read_b128 a[72:75],v250 offset:2048\nds_read_b128 a[104:107],v250 offset:10240\nM v[140:143],a[4:7],a[180:183],v[140:143]\n" \
"v_cvt_pk_bf16_f32 v215,v182,v183\ns_add_u32 s94,s94,0x4000\nM v[140:143],a[0:3],a[176:179],v[140:143]\nv_exp_f32 v184,v184\nM v[144:147],a[16:19],a[128:131],v[224:227]\nv_exp_f32 v185,v185\nM v[144:147],a[20:23],a[132:135],v[144:147]\nv_exp_f32 v186,v186\n" \
"M v[144:147],a[24:27],a[136:139],v[144:147]\nv_exp_f32 v187,v187\nM v[144:147],a[28:31],a[140:143],v[144:147]\nds_read_b128 a[76:79],v250 offset:3072\nds_read_b128 a[108:111],v250 offset:11264\nM v[148:151],a[28:31],a[156:159],v[228:231]\nv_cvt_pk_bf16_f32 v218,v184,v185\nv_cvt_pk_bf16_f32 v219,v186,v187\n" \
"M v[148:151],a[24:27],a[152:155],v[148:151]\nv_exp_f32 v188,v188\nM v[148:151],a[20:23],a[148:151],v[148:151]\nbuffer_load_dwordx4 v240,s[44:47],s95 offen offset:1024 sc1 lds\nM v[148:151],a[16:19],a[144:147],v[148:151]\nv_exp_f32 v189,v189\nM v[152:155],a[16:19],a[160:163],v[232:235]\nv_exp_f32 v190,v190\n" \
"M v[152:155],a[20:23],a[164:167],v[152:155]\nds_read_b128 a[80:83],v250 offset:4096\nds_read_b128 a[112:115],v250 offset:12288\nM v[152:155],a[24:27],a[168:171],v[152:155]\nv_exp_f32 v191,v191\nM v[152:155],a[28:31],a[172:175],v[152:155]\nv_cvt_pk_bf16_f32 v222,v188,v189\nv_cvt_pk_bf16_f32 v223,v190,v191\n" \
"M v[156:159],a[28:31],a[188:191],v[236:239]\nv_exp_f32 v128,v128\nM v[156:159],a[24:27],a[184:187],v[156:159]\nv_exp_f32 v129,v129\nM v[156:159],a[20:23],a[180:183],v[156:159]\nv_exp_f32 v130,v130\nM v[156:159],a[16:19],a[176:179],v[156:159]\nds_read_b128 a[84:87],v250 offset:5120\n" \
"ds_read_b128 a[116:119],v250 offset:13312\nM v[160:163],a[32:35],a[128:131],v[224:227]\nv_exp_f32 v131,v131\nM v[160:163],a[36:39],a[132:135],v[160:163]\nv_exp_f32 v144,v144\nM v[160:163],a[40:43],a[136:139],v[160:163]\nv_exp_f32 v145,v145\nM v[160:163],a[44:47],a[140:143],v[160:163]\n" \
"v_exp_f32 v146,v146\nM v[164:167],a[44:47],a[156:159],v[228:231]\nv_exp_f32 v147,v147\nM v[164:167],a[40:43],a[152:155],v[164:167]\nds_read_b128 a[88:91],v250 offset:6144\nds_read_b128 a[120:123],v250 offset:14336\nM v[164:167],a[36:39],a[148:151],v[164:167]\nv_exp_f32 v132,v132\n" \
"M v[164:167],a[32:35],a[144:147],v[164:167]\nv_exp_f32 v133,v133\nM v[168:171],a[32:35],a[160:163],v[232:235]\nbuffer_load_dwordx4 v240,s[44:47],s95 offen offset:2048 sc1 lds\nM v[168:171],a[36:39],a[164:167],v[168:171]\nv_exp_f32 v134,v134\nM v[168:171],a[40:43],a[168:171],v[168:171]\nv_exp_f32 v135,v135\n" \
"M v[168:171],a[44:47],a[172:175],v[168:171]\nds_read_b128 a[92:95],v250 offset:7168\nds_read_b128 a[124:127],v250 offset:15360\nM v[172:175],a[44:47],a[188:191],v[236:239]\nv_xor_b32 v250,0x18000,v250\ns_and_b32 s94,s94,0xffff\nM v[172:175],a[40:43],a[184:187],v[172:175]\nv_exp_f32 v148,v148\n" \
"M v[172:175],a[36:39],a[180:183],v[172:175]\nv_exp_f32 v149,v149\nM v[172:175],a[32:35],a[176:179],v[172:175]\ns_waitcnt lgkmcnt(4)\nM v[176:179],a[48:51],a[128:131],v[224:227]\nv_exp_f32 v150,v150\nM v[176:179],a[52:55],a[132:135],v[176:179]\nv_exp_f32 v151,v151\n" \
"M v[176:179],a[56:59],a[136:139],v[176:179]\nv_exp_f32 v136,v136\nM v[176:179],a[60:63],a[140:143],v[176:179]\nv_exp_f32 v137,v137\nM v[180:183],a[60:63],a[156:159],v[228:231]\nv_exp_f32 v138,v138\nM v[180:183],a[56:59],a[152:155],v[180:183]\nv_exp_f32 v139,v139\n" \
"M v[180:183],a[52:55],a[148:151],v[180:183]\nv_exp_f32 v152,v152\nM v[180:183],a[48:51],a[144:147],v[180:183]\nv_exp_f32 v153,v153\nM v[184:187],a[48:51],a[160:163],v[232:235]\nv_exp_f32 v154,v154\nM v[184:187],a[52:55],a[164:167],v[184:187]\nv_exp_f32 v155,v155\n" \
"M v[184:187],a[56:59],a[168:171],v[184:187]\nbuffer_load_dwordx4 v240,s[44:47],s95 offen offset:3072 sc1 lds\nM v[184:187],a[60:63],a[172:175],v[184:187]\nv_exp_f32 v160,v160\nM v[188:191],a[60:63],a[188:191],v[236:239]\nv_exp_f32 v161,v161\nM v[188:191],a[56:59],a[184:187],v[188:191]\nv_exp_f32 v162,v162\n" \
"M v[188:191],a[52:55],a[180:183],v[188:191]\nv_exp_f32 v163,v163\nM v[188:191],a[48:51],a[176:179],v[188:191]\nv_exp_f32 v176,v176\nM v[0:3],a[64:67],v[192:195],v[0:3]\ns_waitcnt vmcnt(16) lgkmcnt(0)\ns_barrier\nM v[0:3],a[96:99],v[208:211],v[0:3]\n" \
"ds_read_b128 a[0:3],v250 offset:0\nds_read_b128 a[4:7],v250 offset:1024\nM v[8:11],a[68:71],v[192:195],v[8:11]\nv_exp_f32 v177,v177\nM v[8:11],a[100:103],v[208:211],v[8:11]\nv_exp_f32 v178,v178\nM v[16:19],a[72:75],v[192:195],v[16:19]\ns_add_u32 m0,m0,0x10000\n" \
"M v[16:19],a[104:107],v[208:211],v[16:19]\nbuffer_load_dwordx4 v240,s[48:51],s95 offen offset:0 sc1 lds\nM v[24:27],a[76:79],v[192:195],v[24:27]\nv_exp_f32 v179,v179\nM v[24:27],a[108:111],v[208:211],v[24:27]\nv_exp_f32 v140,v140\nM v[32:35],a[80:83],v[192:195],v[32:35]\nv_exp_f32 v141,v141\n" \
"M v[32:35],a[112:115],v[208:211],v[32:35]\nds_read_b128 a[8:11],v250 offset:2048\nds_read_b128 a[12:15],v250 offset:3072\nM v[40:43],a[84:87],v[192:195],v[40:43]\nv_exp_f32 v142,v142\nM v[40:43],a[116:119],v[208:211],v[40:43]\nv_exp_f32 v143,v143\nM v[48:51],a[88:91],v[192:195],v[48:51]\n" \
"v_exp_f32 v156,v156\nM v[48:51],a[120:123],v[208:211],v[48:51]\nv_exp_f32 v157,v157\nM v[56:59],a[92:95],v[192:195],v[56:59]\nv_exp_f32 v158,v158\nM v[56:59],a[124:127],v[208:211],v[56:59]\nv_exp_f32 v159,v159\nM a[192:195],a[208:211],v[192:195],a[192:195]\n" \
"v_exp_f32 v164,v164\nM a[192:195],a[208:211],v[208:211],a[192:195]\nds_read_b128 a[16:19],v250 offset:4096\nds_read_b128 a[20:23],v250 offset:5120\nM v[4:7],a[64:67],v[196:199],v[4:7]\nv_exp_f32 v165,v165\nM v[4:7],a[96:99],v[212:215],v[4:7]\nv_exp_f32 v166,v166\n" \
"M v[12:15],a[68:71],v[196:199],v[12:15]\nv_exp_f32 v167,v167\nM v[12:15],a[100:103],v[212:215],v[12:15]\nbuffer_load_dwordx4 v240,s[48:51],s95 offen offset:1024 sc1 lds\nM v[20:23],a[72:75],v[196:199],v[20:23]\nv_exp_f32 v180,v180\nM v[20:23],a[104:107],v[212:215],v[20:23]\nv_exp_f32 v181,v181\n" \
"M v[28:31],a[76:79],v[196:199],v[28:31]\nv_exp_f32 v182,v182\nM v[28:31],a[108:111],v[212:215],v[28:31]\nds_read_b128 a[24:27],v250 offset:6144\nds_read_b128 a[28:31],v250 offset:7168\nM v[36:39],a[80:83],v[196:199],v[36:39]\nv_exp_f32 v183,v183\nM v[36:39],a[112:115],v[212:215],v[36:39]\n" \
"v_exp_f32 v168,v168\nM v[44:47],a[84:87],v[196:199],v[44:47]\nv_exp_f32 v169,v169\nM v[44:47],a[116:119],v[212:215],v[44:47]\nv_exp_f32 v170,v170\nM v[52:55],a[88:91],v[196:199],v[52:55]\nv_exp_f32 v171,v171\nM v[52:55],a[120:123],v[212:215],v[52:55]\n" \
"v_cvt_pk_bf16_f32 v192,v128,v129\nv_cvt_pk_bf16_f32 v193,v130,v131\nM v[60:63],a[92:95],v[196:199],v[60:63]\nv_cvt_pk_bf16_f32 v194,v144,v145\nv_cvt_pk_bf16_f32 v195,v146,v147\nM v[60:63],a[124:127],v[212:215],v[60:63]\nds_read_b128 a[32:35],v250 offset:8192\nds_read_b128 a[36:39],v250 offset:9216\n" \
"M a[196:199],a[208:211],v[196:199],a[196:199]\nv_cvt_pk_bf16_f32 v208,v160,v161\nv_cvt_pk_bf16_f32 v209,v162,v163\nM a[196:199],a[208:211],v[212:215],a[196:199]\nv_cvt_pk_bf16_f32 v210,v176,v177\nv_cvt_pk_bf16_f32 v211,v178,v179\nM v[64:67],a[64:67],v[200:203],v[64:67]\nv_cvt_pk_bf16_f32 v196,v132,v133\n" \
"v_cvt_pk_bf16_f32 v197,v134,v135\nM v[64:67],a[96:99],v[216:219],v[64:67]\nbuffer_load_dwordx4 v240,s[48:51],s95 offen offset:2048 sc1 lds\nM v[72:75],a[68:71],v[200:203],v[72:75]\nv_cvt_pk_bf16_f32 v198,v148,v149\nv_cvt_pk_bf16_f32 v199,v150,v151\nM v[72:75],a[100:103],v[216:219],v[72:75]\nM v[80:83],a[72:75],v[200:203],v[80:83]\n" \
"M v[80:83],a[104:107],v[216:219],v[80:83]\nds_read_b128 a[40:43],v250 offset:10240\nds_read_b128 a[44:47],v250 offset:11264\nM v[88:91],a[76:79],v[200:203],v[88:91]\nM v[88:91],a[108:111],v[216:219],v[88:91]\nM v[96:99],a[80:83],v[200:203],v[96:99]\nM v[96:99],a[112:115],v[216:219],v[96:99]\nM v[104:107],a[84:87],v[200:203],v[104:107]\n" \
"M v[104:107],a[116:119],v[216:219],v[104:107]\nM v[112:115],a[88:91],v[200:203],v[112:115]\nM v[112:115],a[120:123],v[216:219],v[112:115]\nds_read_b128 a[48:51],v250 offset:12288\nds_read_b128 a[52:55],v250 offset:13312\nM v[120:123],a[92:95],v[200:203],v[120:123]\nM v[120:123],a[124:127],v[216:219],v[120:123]\nM a[200:203],a[208:211],v[200:203],a[200:203]\n" \
"v_cvt_pk_bf16_f32 v200,v136,v137\nv_cvt_pk_bf16_f32 v201,v138,v139\nM a[200:203],a[208:211],v[216:219],a[200:203]\nbuffer_load_dwordx4 v240,s[48:51],s95 offen offset:3072 sc1 lds\nM v[68:71],a[64:67],v[204:207],v[68:71]\nv_cvt_pk_bf16_f32 v202,v152,v153\nv_cvt_pk_bf16_f32 v203,v154,v155\nM v[68:71],a[96:99],v[220:223],v[68:71]\n" \
"s_cmp_eq_u32 s95,s96\ns_cselect_b32 s72,0xc000,s98\ns_add_u32 s95,s95,s72\nM v[76:79],a[68:71],v[204:207],v[76:79]\nM v[76:79],a[100:103],v[220:223],v[76:79]\nds_read_b128 a[56:59],v250 offset:14336\nds_read_b128 a[60:63],v250 offset:15360\nM v[84:87],a[72:75],v[204:207],v[84:87]\n" \
"v_add_u32 v250,s94,v241\nM v[84:87],a[104:107],v[220:223],v[84:87]\nM v[92:95],a[76:79],v[204:207],v[92:95]\nM v[92:95],a[108:111],v[220:223],v[92:95]\nM v[100:103],a[80:83],v[204:207],v[100:103]\nM v[100:103],a[112:115],v[220:223],v[100:103]\nM v[108:111],a[84:87],v[204:207],v[108:111]\nM v[108:111],a[116:119],v[220:223],v[108:111]\n" \
"M v[116:119],a[88:91],v[204:207],v[116:119]\nbuffer_load_dword v247,v254,s[56:59],s1 offen\nM v[116:119],a[120:123],v[220:223],v[116:119]\ns_add_u32 s1,s1,s0\nM v[124:127],a[92:95],v[204:207],v[124:127]\nM v[124:127],a[124:127],v[220:223],v[124:127]\nM a[204:207],a[208:211],v[204:207],a[204:207]\nM a[204:207],a[208:211],v[220:223],a[204:207]\n" \
"s_cmp_lg_u32 s95,s97\ns_cbranch_scc1 .Ldual\n.Ldf:\ns_waitcnt lgkmcnt(4)\nM v[128:131],a[0:3],a[128:131],v[224:227]\ns_add_u32 m0,s94,s64\nv_cvt_pk_bf16_f32 v204,v140,v141\nM v[128:131],a[4:7],a[132:135],v[128:131]\n" \
"ds_read_b128 a[64:67],v250 offset:0\nds_read_b128 a[96:99],v250 offset:8192\nM v[128:131],a[8:11],a[136:139],v[128:131]\nv_cvt_pk_bf16_f32 v205,v142,v143\nv_lshrrev_b32 v248,2,v249\nM v[128:131],a[12:15],a[140:143],v[128:131]\nv_mul_u32_u24 v248,3584,v248\nv_and_b32 v251,3,v249\n" \
"M v[132:135],a[12:15],a[156:159],v[228:231]\nbuffer_load_dwordx4 v240,s[44:47],s95 offen offset:0 sc1 lds\nM v[132:135],a[8:11],a[152:155],v[132:135]\ns_mul_i32 s70,s7,458752\nv_lshl_add_u32 v248,v251,3,v248\nM v[132:135],a[4:7],a[148:151],v[132:135]\ns_add_u32 s70,s70,s2\nv_add_u32 v248,s70,v248\n" \
"M v[132:135],a[0:3],a[144:147],v[132:135]\nds_read_b128 a[68:71],v250 offset:1024\nds_read_b128 a[100:103],v250 offset:9216\nM v[136:139],a[0:3],a[160:163],v[232:235]\nv_and_b32 v248,s3,v248\nv_cvt_pk_bf16_f32 v206,v156,v157\nM v[136:139],a[4:7],a[164:167],v[136:139]\nv_cvt_pk_bf16_f32 v207,v158,v159\n" \
"v_cvt_pk_bf16_f32 v212,v164,v165\nM v[136:139],a[8:11],a[168:171],v[136:139]\nv_cvt_pk_bf16_f32 v213,v166,v167\nv_cvt_pk_bf16_f32 v216,v168,v169\nM v[136:139],a[12:15],a[172:175],v[136:139]\nv_cvt_pk_bf16_f32 v217,v170,v171\ns_and_b32 s71,s3,57344\nM v[140:143],a[12:15],a[188:191],v[236:239]\n" \
"v_exp_f32 v172,v172\nM v[140:143],a[8:11],a[184:187],v[140:143]\nds_read_b128 a[72:75],v250 offset:2048\nds_read_b128 a[104:107],v250 offset:10240\nM v[140:143],a[4:7],a[180:183],v[140:143]\nglobal_load_dwordx2 a[212:213],v248,s[22:23]\nM v[140:143],a[0:3],a[176:179],v[140:143]\nv_exp_f32 v173,v173\n" \
"M v[144:147],a[16:19],a[128:131],v[224:227]\nv_exp_f32 v174,v174\nM v[144:147],a[20:23],a[132:135],v[144:147]\nv_exp_f32 v175,v175\nM v[144:147],a[24:27],a[136:139],v[144:147]\nv_cvt_pk_bf16_f32 v220,v172,v173\nv_cvt_pk_bf16_f32 v221,v174,v175\nM v[144:147],a[28:31],a[140:143],v[144:147]\n" \
"ds_read_b128 a[76:79],v250 offset:3072\nds_read_b128 a[108:111],v250 offset:11264\nM v[148:151],a[28:31],a[156:159],v[228:231]\nv_cvt_pk_bf16_f32 v214,v180,v181\nv_cvt_pk_bf16_f32 v215,v182,v183\nM v[148:151],a[24:27],a[152:155],v[148:151]\nv_exp_f32 v184,v184\nM v[148:151],a[20:23],a[148:151],v[148:151]\n" \
"buffer_load_dwordx4 v240,s[44:47],s95 offen offset:1024 sc1 lds\nM v[148:151],a[16:19],a[144:147],v[148:151]\nv_exp_f32 v185,v185\nM v[152:155],a[16:19],a[160:163],v[232:235]\nv_exp_f32 v186,v186\nM v[152:155],a[20:23],a[164:167],v[152:155]\nds_read_b128 a[80:83],v250 offset:4096\nds_read_b128 a[112:115],v250 offset:12288\n" \
"M v[152:155],a[24:27],a[168:171],v[152:155]\nv_exp_f32 v187,v187\nM v[152:155],a[28:31],a[172:175],v[152:155]\nv_cvt_pk_bf16_f32 v218,v184,v185\nv_cvt_pk_bf16_f32 v219,v186,v187\nM v[156:159],a[28:31],a[188:191],v[236:239]\nv_exp_f32 v188,v188\nM v[156:159],a[24:27],a[184:187],v[156:159]\n" \
"v_exp_f32 v189,v189\nM v[156:159],a[20:23],a[180:183],v[156:159]\nv_exp_f32 v190,v190\nM v[156:159],a[16:19],a[176:179],v[156:159]\nds_read_b128 a[84:87],v250 offset:5120\nds_read_b128 a[116:119],v250 offset:13312\nM v[160:163],a[32:35],a[128:131],v[224:227]\nglobal_load_dwordx2 a[228:229],v248,s[24:25]\n" \
"M v[160:163],a[36:39],a[132:135],v[160:163]\nv_add_u32 v248,s71,v248\nv_cvt_pk_bf16_f32 v222,v188,v189\nM v[160:163],a[40:43],a[136:139],v[160:163]\nv_exp_f32 v191,v191\nM v[160:163],a[44:47],a[140:143],v[160:163]\nv_cvt_pk_bf16_f32 v223,v190,v191\ns_add_u32 s94,s94,0x4000\n" \
"M v[164:167],a[44:47],a[156:159],v[228:231]\nv_exp_f32 v128,v128\nM v[164:167],a[40:43],a[152:155],v[164:167]\nds_read_b128 a[88:91],v250 offset:6144\nds_read_b128 a[120:123],v250 offset:14336\nM v[164:167],a[36:39],a[148:151],v[164:167]\nv_exp_f32 v129,v129\nM v[164:167],a[32:35],a[144:147],v[164:167]\n" \
"v_exp_f32 v130,v130\nM v[168:171],a[32:35],a[160:163],v[232:235]\nbuffer_load_dwordx4 v240,s[44:47],s95 offen offset:2048 sc1 lds\nM v[168:171],a[36:39],a[164:167],v[168:171]\nv_exp_f32 v131,v131\nM v[168:171],a[40:43],a[168:171],v[168:171]\nv_exp_f32 v144,v144\nM v[168:171],a[44:47],a[172:175],v[168:171]\n" \
"ds_read_b128 a[92:95],v250 offset:7168\nds_read_b128 a[124:127],v250 offset:15360\nM v[172:175],a[44:47],a[188:191],v[236:239]\nv_xor_b32 v250,0x18000,v250\ns_and_b32 s94,s94,0xffff\nM v[172:175],a[40:43],a[184:187],v[172:175]\nv_exp_f32 v145,v145\nM v[172:175],a[36:39],a[180:183],v[172:175]\n" \
"v_exp_f32 v146,v146\nM v[172:175],a[32:35],a[176:179],v[172:175]\ns_waitcnt lgkmcnt(4)\nM v[176:179],a[48:51],a[128:131],v[224:227]\nv_exp_f32 v147,v147\nM v[176:179],a[52:55],a[132:135],v[176:179]\nv_exp_f32 v132,v132\nM v[176:179],a[56:59],a[136:139],v[176:179]\n" \
"global_load_dwordx2 a[214:215],v248,s[22:23]\nM v[176:179],a[60:63],a[140:143],v[176:179]\nv_exp_f32 v133,v133\nM v[180:183],a[60:63],a[156:159],v[228:231]\nv_exp_f32 v134,v134\nM v[180:183],a[56:59],a[152:155],v[180:183]\nv_exp_f32 v135,v135\nM v[180:183],a[52:55],a[148:151],v[180:183]\n" \
"v_exp_f32 v148,v148\nM v[180:183],a[48:51],a[144:147],v[180:183]\nv_exp_f32 v149,v149\nM v[184:187],a[48:51],a[160:163],v[232:235]\nv_exp_f32 v150,v150\nM v[184:187],a[52:55],a[164:167],v[184:187]\nv_exp_f32 v151,v151\nM v[184:187],a[56:59],a[168:171],v[184:187]\n" \
"buffer_load_dwordx4 v240,s[44:47],s95 offen offset:3072 sc1 lds\nM v[184:187],a[60:63],a[172:175],v[184:187]\nv_exp_f32 v136,v136\nM v[188:191],a[60:63],a[188:191],v[236:239]\nv_exp_f32 v137,v137\nM v[188:191],a[56:59],a[184:187],v[188:191]\nv_exp_f32 v138,v138\nM v[188:191],a[52:55],a[180:183],v[188:191]\n" \
"global_load_dwordx2 a[230:231],v248,s[24:25]\nM v[188:191],a[48:51],a[176:179],v[188:191]\ns_add_u32 m0,m0,0x10000\nv_add_u32 v248,s71,v248\nM v[0:3],a[64:67],v[192:195],v[0:3]\ns_waitcnt vmcnt(16) lgkmcnt(0)\ns_barrier\nM v[0:3],a[96:99],v[208:211],v[0:3]\n" \
"ds_read_b128 a[0:3],v250 offset:0\nds_read_b128 a[4:7],v250 offset:1024\nM v[8:11],a[68:71],v[192:195],v[8:11]\nv_exp_f32 v139,v139\nM v[8:11],a[100:103],v[208:211],v[8:11]\nv_exp_f32 v152,v152\nM v[16:19],a[72:75],v[192:195],v[16:19]\nv_exp_f32 v153,v153\n" \
"M v[16:19],a[104:107],v[208:211],v[16:19]\nbuffer_load_dwordx4 v240,s[48:51],s95 offen offset:0 sc1 lds\nM v[24:27],a[76:79],v[192:195],v[24:27]\nv_exp_f32 v154,v154\nM v[24:27],a[108:111],v[208:211],v[24:27]\nv_exp_f32 v155,v155\nM v[32:35],a[80:83],v[192:195],v[32:35]\nv_exp_f32 v160,v160\n" \
"M v[32:35],a[112:115],v[208:211],v[32:35]\nds_read_b128 a[8:11],v250 offset:2048\nds_read_b128 a[12:15],v250 offset:3072\nM v[40:43],a[84:87],v[192:195],v[40:43]\nv_exp_f32 v161,v161\nM v[40:43],a[116:119],v[208:211],v[40:43]\nv_exp_f32 v162,v162\nM v[48:51],a[88:91],v[192:195],v[48:51]\n" \
"v_exp_f32 v163,v163\nM v[48:51],a[120:123],v[208:211],v[48:51]\nglobal_load_dwordx2 a[216:217],v248,s[22:23]\nM v[56:59],a[92:95],v[192:195],v[56:59]\nv_exp_f32 v176,v176\nM v[56:59],a[124:127],v[208:211],v[56:59]\nv_exp_f32 v177,v177\nM a[192:195],a[208:211],v[192:195],a[192:195]\n" \
"v_exp_f32 v178,v178\nM a[192:195],a[208:211],v[208:211],a[192:195]\nds_read_b128 a[16:19],v250 offset:4096\nds_read_b128 a[20:23],v250 offset:5120\nM v[4:7],a[64:67],v[196:199],v[4:7]\nv_exp_f32 v179,v179\nM v[4:7],a[96:99],v[212:215],v[4:7]\nv_exp_f32 v140,v140\n" \
"M v[12:15],a[68:71],v[196:199],v[12:15]\nv_exp_f32 v141,v141\nM v[12:15],a[100:103],v[212:215],v[12:15]\nbuffer_load_dwordx4 v240,s[48:51],s95 offen offset:1024 sc1 lds\nM v[20:23],a[72:75],v[196:199],v[20:23]\nv_exp_f32 v142,v142\nM v[20:23],a[104:107],v[212:215],v[20:23]\nv_exp_f32 v143,v143\n" \
"M v[28:31],a[76:79],v[196:199],v[28:31]\nv_exp_f32 v156,v156\nM v[28:31],a[108:111],v[212:215],v[28:31]\nds_read_b128 a[24:27],v250 offset:6144\nds_read_b128 a[28:31],v250 offset:7168\nM v[36:39],a[80:83],v[196:199],v[36:39]\nv_exp_f32 v157,v157\nM v[36:39],a[112:115],v[212:215],v[36:39]\n" \
"v_exp_f32 v158,v158\nM v[44:47],a[84:87],v[196:199],v[44:47]\nv_exp_f32 v159,v159\nM v[44:47],a[116:119],v[212:215],v[44:47]\nglobal_load_dwordx2 a[232:233],v248,s[24:25]\nM v[52:55],a[88:91],v[196:199],v[52:55]\nv_add_u32 v248,s71,v248\nv_cvt_pk_bf16_f32 v192,v128,v129\n" \
"M v[52:55],a[120:123],v[212:215],v[52:55]\nv_exp_f32 v164,v164\nM v[60:63],a[92:95],v[196:199],v[60:63]\nv_exp_f32 v165,v165\nM v[60:63],a[124:127],v[212:215],v[60:63]\nds_read_b128 a[32:35],v250 offset:8192\nds_read_b128 a[36:39],v250 offset:9216\nM a[196:199],a[208:211],v[196:199],a[196:199]\n" \
"v_exp_f32 v166,v166\nM a[196:199],a[208:211],v[212:215],a[196:199]\nv_exp_f32 v167,v167\nM v[64:67],a[64:67],v[200:203],v[64:67]\nv_exp_f32 v180,v180\nM v[64:67],a[96:99],v[216:219],v[64:67]\nbuffer_load_dwordx4 v240,s[48:51],s95 offen offset:2048 sc1 lds\nM v[72:75],a[68:71],v[200:203],v[72:75]\n" \
"v_exp_f32 v181,v181\nM v[72:75],a[100:103],v[216:219],v[72:75]\nv_exp_f32 v182,v182\nM v[80:83],a[72:75],v[200:203],v[80:83]\nv_exp_f32 v183,v183\nM v[80:83],a[104:107],v[216:219],v[80:83]\nds_read_b128 a[40:43],v250 offset:10240\nds_read_b128 a[44:47],v250 offset:11264\n" \
"M v[88:91],a[76:79],v[200:203],v[88:91]\nv_exp_f32 v168,v168\nM v[88:91],a[108:111],v[216:219],v[88:91]\nv_exp_f32 v169,v169\nM v[96:99],a[80:83],v[200:203],v[96:99]\nv_exp_f32 v170,v170\nM v[96:99],a[112:115],v[216:219],v[96:99]\nglobal_load_dwordx2 a[218:219],v248,s[22:23]\n" \
"M v[104:107],a[84:87],v[200:203],v[104:107]\nv_exp_f32 v171,v171\nM v[104:107],a[116:119],v[216:219],v[104:107]\nv_cvt_pk_bf16_f32 v193,v130,v131\nv_cvt_pk_bf16_f32 v194,v144,v145\nM v[112:115],a[88:91],v[200:203],v[112:115]\nv_cvt_pk_bf16_f32 v195,v146,v147\nv_cvt_pk_bf16_f32 v208,v160,v161\n" \
"M v[112:115],a[120:123],v[216:219],v[112:115]\nds_read_b128 a[48:51],v250 offset:12288\nds_read_b128 a[52:55],v250 offset:13312\nM v[120:123],a[92:95],v[200:203],v[120:123]\nv_cvt_pk_bf16_f32 v209,v162,v163\nv_cvt_pk_bf16_f32 v210,v176,v177\nM v[120:123],a[124:127],v[216:219],v[120:123]\nv_cvt_pk_bf16_f32 v211,v178,v179\n" \
"v_cvt_pk_bf16_f32 v196,v132,v133\nM a[200:203],a[208:211],v[200:203],a[200:203]\nv_cvt_pk_bf16_f32 v197,v134,v135\nv_cvt_pk_bf16_f32 v198,v148,v149\nM a[200:203],a[208:211],v[216:219],a[200:203]\nbuffer_load_dwordx4 v240,s[48:51],s95 offen offset:3072 sc1 lds\nM v[68:71],a[64:67],v[204:207],v[68:71]\nv_cvt_pk_bf16_f32 v199,v150,v151\n" \
"v_cvt_pk_bf16_f32 v200,v136,v137\nM v[68:71],a[96:99],v[220:223],v[68:71]\nv_cvt_pk_bf16_f32 v201,v138,v139\nv_cvt_pk_bf16_f32 v202,v152,v153\nM v[76:79],a[68:71],v[204:207],v[76:79]\nv_cvt_pk_bf16_f32 v203,v154,v155\ns_add_u32 s95,s95,0x4000\nM v[76:79],a[100:103],v[220:223],v[76:79]\n" \
"ds_read_b128 a[56:59],v250 offset:14336\nds_read_b128 a[60:63],v250 offset:15360\nM v[84:87],a[72:75],v[204:207],v[84:87]\nv_add_u32 v250,s94,v241\nM v[84:87],a[104:107],v[220:223],v[84:87]\nM v[92:95],a[76:79],v[204:207],v[92:95]\nM v[92:95],a[108:111],v[220:223],v[92:95]\nglobal_load_dwordx2 a[234:235],v248,s[24:25]\n" \
"M v[100:103],a[80:83],v[204:207],v[100:103]\nv_add_u32 v248,s71,v248\nM v[100:103],a[112:115],v[220:223],v[100:103]\nM v[108:111],a[84:87],v[204:207],v[108:111]\nM v[108:111],a[116:119],v[220:223],v[108:111]\nM v[116:119],a[88:91],v[204:207],v[116:119]\nM v[116:119],a[120:123],v[220:223],v[116:119]\nM v[124:127],a[92:95],v[204:207],v[124:127]\n" \
"M v[124:127],a[124:127],v[220:223],v[124:127]\nM a[204:207],a[208:211],v[204:207],a[204:207]\nM a[204:207],a[208:211],v[220:223],a[204:207]\ns_waitcnt lgkmcnt(4)\nM v[128:131],a[0:3],a[128:131],v[224:227]\ns_add_u32 m0,s94,s64\nv_cvt_pk_bf16_f32 v204,v140,v141\nM v[128:131],a[4:7],a[132:135],v[128:131]\n" \
"ds_read_b128 a[64:67],v250 offset:0\nds_read_b128 a[96:99],v250 offset:8192\nM v[128:131],a[8:11],a[136:139],v[128:131]\nv_cvt_pk_bf16_f32 v205,v142,v143\nv_cvt_pk_bf16_f32 v206,v156,v157\nM v[128:131],a[12:15],a[140:143],v[128:131]\nv_cvt_pk_bf16_f32 v207,v158,v159\nv_cvt_pk_bf16_f32 v212,v164,v165\n" \
"M v[132:135],a[12:15],a[156:159],v[228:231]\nbuffer_load_dwordx4 v240,s[44:47],s95 offen offset:0 sc1 lds\nM v[132:135],a[8:11],a[152:155],v[132:135]\nv_cvt_pk_bf16_f32 v213,v166,v167\nv_cvt_pk_bf16_f32 v216,v168,v169\nM v[132:135],a[4:7],a[148:151],v[132:135]\nv_cvt_pk_bf16_f32 v217,v170,v171\nv_cvt_pk_bf16_f32 v214,v180,v181\n" \
"M v[132:135],a[0:3],a[144:147],v[132:135]\nds_read_b128 a[68:71],v250 offset:1024\nds_read_b128 a[100:103],v250 offset:9216\nM v[136:139],a[0:3],a[160:163],v[232:235]\nv_exp_f32 v172,v172\nM v[136:139],a[4:7],a[164:167],v[136:139]\nv_exp_f32 v173,v173\nM v[136:139],a[8:11],a[168:171],v[136:139]\n" \
"v_exp_f32 v174,v174\nM v[136:139],a[12:15],a[172:175],v[136:139]\nv_exp_f32 v175,v175\nM v[140:143],a[12:15],a[188:191],v[236:239]\nv_cvt_pk_bf16_f32 v220,v172,v173\nv_cvt_pk_bf16_f32 v221,v174,v175\nM v[140:143],a[8:11],a[184:187],v[140:143]\nds_read_b128 a[72:75],v250 offset:2048\n" \
"ds_read_b128 a[104:107],v250 offset:10240\nM v[140:143],a[4:7],a[180:183],v[140:143]\nglobal_load_dwordx2 a[220:221],v248,s[22:23]\nM v[140:143],a[0:3],a[176:179],v[140:143]\nv_cvt_pk_bf16_f32 v215,v182,v183\ns_add_u32 s94,s94,0x4000\nM v[144:147],a[16:19],a[128:131],v[224:227]\nv_exp_f32 v184,v184\n" \
"M v[144:147],a[20:23],a[132:135],v[144:147]\nv_exp_f32 v185,v185\nM v[144:147],a[24:27],a[136:139],v[144:147]\nv_exp_f32 v186,v186\nM v[144:147],a[28:31],a[140:143],v[144:147]\nds_read_b128 a[76:79],v250 offset:3072\nds_read_b128 a[108:111],v250 offset:11264\nM v[148:151],a[28:31],a[156:159],v[228:231]\n" \
"v_exp_f32 v187,v187\nM v[148:151],a[24:27],a[152:155],v[148:151]\nv_cvt_pk_bf16_f32 v218,v184,v185\nv_cvt_pk_bf16_f32 v219,v186,v187\nM v[148:151],a[20:23],a[148:151],v[148:151]\nbuffer_load_dwordx4 v240,s[44:47],s95 offen offset:1024 sc1 lds\nM v[148:151],a[16:19],a[144:147],v[148:151]\nv_exp_f32 v188,v188\n" \
"M v[152:155],a[16:19],a[160:163],v[232:235]\nv_exp_f32 v189,v189\nM v[152:155],a[20:23],a[164:167],v[152:155]\nds_read_b128 a[80:83],v250 offset:4096\nds_read_b128 a[112:115],v250 offset:12288\nM v[152:155],a[24:27],a[168:171],v[152:155]\nv_exp_f32 v190,v190\nM v[152:155],a[28:31],a[172:175],v[152:155]\n" \
"v_exp_f32 v191,v191\nM v[156:159],a[28:31],a[188:191],v[236:239]\nv_cvt_pk_bf16_f32 v222,v188,v189\nv_cvt_pk_bf16_f32 v223,v190,v191\nM v[156:159],a[24:27],a[184:187],v[156:159]\nv_exp_f32 v128,v128\nM v[156:159],a[20:23],a[180:183],v[156:159]\nv_exp_f32 v129,v129\n" \
"M v[156:159],a[16:19],a[176:179],v[156:159]\nds_read_b128 a[84:87],v250 offset:5120\nds_read_b128 a[116:119],v250 offset:13312\nM v[160:163],a[32:35],a[128:131],v[224:227]\nglobal_load_dwordx2 a[236:237],v248,s[24:25]\nM v[160:163],a[36:39],a[132:135],v[160:163]\nv_add_u32 v248,s71,v248\ns_and_b32 s94,s94,0xffff\n" \
"M v[160:163],a[40:43],a[136:139],v[160:163]\nv_exp_f32 v130,v130\nM v[160:163],a[44:47],a[140:143],v[160:163]\nv_exp_f32 v131,v131\nM v[164:167],a[44:47],a[156:159],v[228:231]\nv_exp_f32 v144,v144\nM v[164:167],a[40:43],a[152:155],v[164:167]\nds_read_b128 a[88:91],v250 offset:6144\n" \
"ds_read_b128 a[120:123],v250 offset:14336\nM v[164:167],a[36:39],a[148:151],v[164:167]\nv_exp_f32 v145,v145\nM v[164:167],a[32:35],a[144:147],v[164:167]\nv_exp_f32 v146,v146\nM v[168:171],a[32:35],a[160:163],v[232:235]\nbuffer_load_dwordx4 v240,s[44:47],s95 offen offset:2048 sc1 lds\nM v[168:171],a[36:39],a[164:167],v[168:171]\n" \
"v_exp_f32 v147,v147\nM v[168:171],a[40:43],a[168:171],v[168:171]\nv_exp_f32 v132,v132\nM v[168:171],a[44:47],a[172:175],v[168:171]\nds_read_b128 a[92:95],v250 offset:7168\nds_read_b128 a[124:127],v250 offset:15360\nM v[172:175],a[44:47],a[188:191],v[236:239]\nv_exp_f32 v133,v133\n" \
"M v[172:175],a[40:43],a[184:187],v[172:175]\nv_exp_f32 v134,v134\nM v[172:175],a[36:39],a[180:183],v[172:175]\nv_exp_f32 v135,v135\nM v[172:175],a[32:35],a[176:179],v[172:175]\nv_xor_b32 v250,0x18000,v250\ns_waitcnt lgkmcnt(3)\nM v[176:179],a[48:51],a[128:131],v[224:227]\n" \
"v_exp_f32 v148,v148\nM v[176:179],a[52:55],a[132:135],v[176:179]\nv_exp_f32 v149,v149\nM v[176:179],a[56:59],a[136:139],v[176:179]\nglobal_load_dwordx2 a[222:223],v248,s[22:23]\nM v[176:179],a[60:63],a[140:143],v[176:179]\nv_exp_f32 v150,v150\nM v[180:183],a[60:63],a[156:159],v[228:231]\n" \
"v_exp_f32 v151,v151\nM v[180:183],a[56:59],a[152:155],v[180:183]\nv_exp_f32 v136,v136\nM v[180:183],a[52:55],a[148:151],v[180:183]\nv_exp_f32 v137,v137\nM v[180:183],a[48:51],a[144:147],v[180:183]\nv_exp_f32 v138,v138\nM v[184:187],a[48:51],a[160:163],v[232:235]\n" \
"v_exp_f32 v139,v139\nM v[184:187],a[52:55],a[164:167],v[184:187]\nv_exp_f32 v152,v152\nM v[184:187],a[56:59],a[168:171],v[184:187]\nbuffer_load_dwordx4 v240,s[44:47],s95 offen offset:3072 sc1 lds\nM v[184:187],a[60:63],a[172:175],v[184:187]\nv_exp_f32 v153,v153\nM v[188:191],a[60:63],a[188:191],v[236:239]\n" \
"v_exp_f32 v154,v154\nM v[188:191],a[56:59],a[184:187],v[188:191]\nv_exp_f32 v155,v155\nM v[188:191],a[52:55],a[180:183],v[188:191]\nglobal_load_dwordx2 a[238:239],v248,s[24:25]\nM v[188:191],a[48:51],a[176:179],v[188:191]\ns_add_u32 m0,m0,0x10000\nv_add_u32 v248,s71,v248\n" \
"M v[0:3],a[64:67],v[192:195],v[0:3]\ns_waitcnt vmcnt(16) lgkmcnt(0)\ns_barrier\nM v[0:3],a[96:99],v[208:211],v[0:3]\nds_read_b128 a[0:3],v250 offset:0\nds_read_b128 a[4:7],v250 offset:1024\nM v[8:11],a[68:71],v[192:195],v[8:11]\nv_exp_f32 v160,v160\n" \
"M v[8:11],a[100:103],v[208:211],v[8:11]\nv_exp_f32 v161,v161\nM v[16:19],a[72:75],v[192:195],v[16:19]\nv_exp_f32 v162,v162\nM v[16:19],a[104:107],v[208:211],v[16:19]\nbuffer_load_dwordx4 v240,s[48:51],s95 offen offset:0 sc1 lds\nM v[24:27],a[76:79],v[192:195],v[24:27]\nv_exp_f32 v163,v163\n" \
"M v[24:27],a[108:111],v[208:211],v[24:27]\nv_exp_f32 v176,v176\nM v[32:35],a[80:83],v[192:195],v[32:35]\nv_exp_f32 v177,v177\nM v[32:35],a[112:115],v[208:211],v[32:35]\nds_read_b128 a[8:11],v250 offset:2048\nds_read_b128 a[12:15],v250 offset:3072\nM v[40:43],a[84:87],v[192:195],v[40:43]\n" \
"v_exp_f32 v178,v178\nM v[40:43],a[116:119],v[208:211],v[40:43]\nv_exp_f32 v179,v179\nM v[48:51],a[88:91],v[192:195],v[48:51]\nv_exp_f32 v140,v140\nM v[48:51],a[120:123],v[208:211],v[48:51]\nglobal_load_dwordx2 a[224:225],v248,s[22:23]\nM v[56:59],a[92:95],v[192:195],v[56:59]\n" \
"v_exp_f32 v141,v141\nM v[56:59],a[124:127],v[208:211],v[56:59]\nv_exp_f32 v142,v142\nM a[192:195],a[208:211],v[192:195],a[192:195]\nv_exp_f32 v143,v143\nM a[192:195],a[208:211],v[208:211],a[192:195]\nds_read_b128 a[16:19],v250 offset:4096\nds_read_b128 a[20:23],v250 offset:5120\n" \
"M v[4:7],a[64:67],v[196:199],v[4:7]\nv_exp_f32 v156,v156\nM v[4:7],a[96:99],v[212:215],v[4:7]\nv_exp_f32 v157,v157\nM v[12:15],a[68:71],v[196:199],v[12:15]\nv_exp_f32 v158,v158\nM v[12:15],a[100:103],v[212:215],v[12:15]\nbuffer_load_dwordx4 v240,s[48:51],s95 offen offset:1024 sc1 lds\n" \
"M v[20:23],a[72:75],v[196:199],v[20:23]\nv_exp_f32 v159,v159\nM v[20:23],a[104:107],v[212:215],v[20:23]\nv_exp_f32 v164,v164\nM v[28:31],a[76:79],v[196:199],v[28:31]\nv_exp_f32 v165,v165\nM v[28:31],a[108:111],v[212:215],v[28:31]\nds_read_b128 a[24:27],v250 offset:6144\n" \
"ds_read_b128 a[28:31],v250 offset:7168\nM v[36:39],a[80:83],v[196:199],v[36:39]\nv_exp_f32 v166,v166\nM v[36:39],a[112:115],v[212:215],v[36:39]\nv_exp_f32 v167,v167\nM v[44:47],a[84:87],v[196:199],v[44:47]\nv_exp_f32 v180,v180\nM v[44:47],a[116:119],v[212:215],v[44:47]\n" \
"global_load_dwordx2 a[240:241],v248,s[24:25]\nM v[52:55],a[88:91],v[196:199],v[52:55]\nv_add_u32 v248,s71,v248\nv_cvt_pk_bf16_f32 v192,v128,v129\nM v[52:55],a[120:123],v[212:215],v[52:55]\nv_exp_f32 v181,v181\nM v[60:63],a[92:95],v[196:199],v[60:63]\nv_exp_f32 v182,v182\n" \
"M v[60:63],a[124:127],v[212:215],v[60:63]\nds_read_b128 a[32:35],v250 offset:8192\nds_read_b128 a[36:39],v250 offset:9216\nM a[196:199],a[208:211],v[196:199],a[196:199]\nv_exp_f32 v183,v183\nM a[196:199],a[208:211],v[212:215],a[196:199]\nv_exp_f32 v168,v168\nM v[64:67],a[64:67],v[200:203],v[64:67]\n" \
"v_exp_f32 v169,v169\nM v[64:67],a[96:99],v[216:219],v[64:67]\nbuffer_load_dwordx4 v240,s[48:51],s95 offen offset:2048 sc1 lds\nM v[72:75],a[68:71],v[200:203],v[72:75]\nv_exp_f32 v170,v170\nM v[72:75],a[100:103],v[216:219],v[72:75]\nv_exp_f32 v171,v171\nM v[80:83],a[72:75],v[200:203],v[80:83]\n" \
"v_cvt_pk_bf16_f32 v193,v130,v131\nv_cvt_pk_bf16_f32 v194,v144,v145\nM v[80:83],a[104:107],v[216:219],v[80:83]\nds_read_b128 a[40:43],v250 offset:10240\nds_read_b128 a[44:47],v250 offset:11264\nM v[88:91],a[76:79],v[200:203],v[88:91]\nv_cvt_pk_bf16_f32 v195,v146,v147\nv_cvt_pk_bf16_f32 v208,v160,v161\n" \
"M v[88:91],a[108:111],v[216:219],v[88:91]\nv_cvt_pk_bf16_f32 v209,v162,v163\nv_cvt_pk_bf16_f32 v210,v176,v177\nM v[96:99],a[80:83],v[200:203],v[96:99]\nv_cvt_pk_bf16_f32 v211,v178,v179\nv_cvt_pk_bf16_f32 v196,v132,v133\nM v[96:99],a[112:115],v[216:219],v[96:99]\nglobal_load_dwordx2 a[226:227],v248,s[22:23]\n" \
"M v[104:107],a[84:87],v[200:203],v[104:107]\nv_cvt_pk_bf16_f32 v197,v134,v135\nv_cvt_pk_bf16_f32 v198,v148,v149\nM v[104:107],a[116:119],v[216:219],v[104:107]\nv_cvt_pk_bf16_f32 v199,v150,v151\nM v[112:115],a[88:91],v[200:203],v[112:115]\nM v[112:115],a[120:123],v[216:219],v[112:115]\nds_read_b128 a[48:51],v250 offset:12288\n" \
"ds_read_b128 a[52:55],v250 offset:13312\nM v[120:123],a[92:95],v[200:203],v[120:123]\nM v[120:123],a[124:127],v[216:219],v[120:123]\nM a[200:203],a[208:211],v[200:203],a[200:203]\nv_cvt_pk_bf16_f32 v200,v136,v137\nv_cvt_pk_bf16_f32 v201,v138,v139\nM a[200:203],a[208:211],v[216:219],a[200:203]\nbuffer_load_dwordx4 v240,s[48:51],s95 offen offset:3072 sc1 lds\n" \
"M v[68:71],a[64:67],v[204:207],v[68:71]\nv_cvt_pk_bf16_f32 v202,v152,v153\nv_cvt_pk_bf16_f32 v203,v154,v155\nM v[68:71],a[96:99],v[220:223],v[68:71]\ns_add_u32 s95,s95,0x4000\nM v[76:79],a[68:71],v[204:207],v[76:79]\nM v[76:79],a[100:103],v[220:223],v[76:79]\nds_read_b128 a[56:59],v250 offset:14336\n" \
"ds_read_b128 a[60:63],v250 offset:15360\nM v[84:87],a[72:75],v[204:207],v[84:87]\nv_add_u32 v250,s94,v241\nM v[84:87],a[104:107],v[220:223],v[84:87]\nM v[92:95],a[76:79],v[204:207],v[92:95]\nM v[92:95],a[108:111],v[220:223],v[92:95]\nglobal_load_dwordx2 a[242:243],v248,s[24:25]\nM v[100:103],a[80:83],v[204:207],v[100:103]\n" \
"v_add_u32 v248,s71,v248\nM v[100:103],a[112:115],v[220:223],v[100:103]\nM v[108:111],a[84:87],v[204:207],v[108:111]\nM v[108:111],a[116:119],v[220:223],v[108:111]\nM v[116:119],a[88:91],v[204:207],v[116:119]\nM v[116:119],a[120:123],v[220:223],v[116:119]\nM v[124:127],a[92:95],v[204:207],v[124:127]\nM v[124:127],a[124:127],v[220:223],v[124:127]\n" \
"M a[204:207],a[208:211],v[204:207],a[204:207]\nM a[204:207],a[208:211],v[220:223],a[204:207]\ns_cmp_eq_u32 s62,7\ns_cbranch_scc0 .Ltrans\ns_waitcnt lgkmcnt(4)\nM v[136:139],a[0:3],a[160:163],v[232:235]\ns_add_u32 m0,s94,s64\nv_cvt_pk_bf16_f32 v204,v140,v141\n" \
"M v[136:139],a[4:7],a[164:167],v[136:139]\nds_read_b128 a[64:67],v250 offset:0\nds_read_b128 a[96:99],v250 offset:8192\nM v[136:139],a[8:11],a[168:171],v[136:139]\nbuffer_load_dwordx4 v240,s[44:47],s95 offen offset:0 sc1 lds\nM v[136:139],a[12:15],a[172:175],v[136:139]\nv_cvt_pk_bf16_f32 v205,v142,v143\nv_cvt_pk_bf16_f32 v206,v156,v157\n" \
"M v[140:143],a[12:15],a[188:191],v[236:239]\nds_read_b128 a[68:71],v250 offset:1024\nds_read_b128 a[100:103],v250 offset:9216\nM v[140:143],a[8:11],a[184:187],v[140:143]\nbuffer_store_dwordx4 a[248:251],v253,s[52:55],s33 offen offset:0 sc1\nM v[140:143],a[4:7],a[180:183],v[140:143]\nv_cvt_pk_bf16_f32 v207,v158,v159\nv_cvt_pk_bf16_f32 v216,v168,v169\n" \
"M v[140:143],a[0:3],a[176:179],v[140:143]\nds_read_b128 a[72:75],v250 offset:2048\nds_read_b128 a[104:107],v250 offset:10240\nM v[152:155],a[16:19],a[160:163],v[232:235]\nv_cvt_pk_bf16_f32 v217,v170,v171\ns_add_u32 s71,s33,16384\nM v[152:155],a[20:23],a[164:167],v[152:155]\nv_exp_f32 v172,v172\n" \
"M v[152:155],a[24:27],a[168:171],v[152:155]\nds_read_b128 a[76:79],v250 offset:3072\nds_read_b128 a[108:111],v250 offset:11264\nM v[152:155],a[28:31],a[172:175],v[152:155]\nbuffer_load_dwordx4 v240,s[44:47],s95 offen offset:1024 sc1 lds\nM v[156:159],a[28:31],a[188:191],v[236:239]\nv_exp_f32 v173,v173\nM v[156:159],a[24:27],a[184:187],v[156:159]\n" \
"ds_read_b128 a[80:83],v250 offset:4096\nds_read_b128 a[112:115],v250 offset:12288\nM v[156:159],a[20:23],a[180:183],v[156:159]\nbuffer_store_dwordx4 a[248:251],v253,s[52:55],s33 offen offset:1024 sc1\nM v[156:159],a[16:19],a[176:179],v[156:159]\nv_exp_f32 v174,v174\nM v[168:171],a[32:35],a[160:163],v[232:235]\nds_read_b128 a[84:87],v250 offset:5120\n" \
"ds_read_b128 a[116:119],v250 offset:13312\nM v[168:171],a[36:39],a[164:167],v[168:171]\nv_exp_f32 v175,v175\nM v[168:171],a[40:43],a[168:171],v[168:171]\nv_cvt_pk_bf16_f32 v220,v172,v173\nv_cvt_pk_bf16_f32 v221,v174,v175\nM v[168:171],a[44:47],a[172:175],v[168:171]\nds_read_b128 a[88:91],v250 offset:6144\n" \
"ds_read_b128 a[120:123],v250 offset:14336\nM v[172:175],a[44:47],a[188:191],v[236:239]\nbuffer_load_dwordx4 v240,s[44:47],s95 offen offset:2048 sc1 lds\nM v[172:175],a[40:43],a[184:187],v[172:175]\nv_exp_f32 v184,v184\nM v[172:175],a[36:39],a[180:183],v[172:175]\nv_exp_f32 v185,v185\nv_exp_f32 v186,v186\n" \
"v_exp_f32 v187,v187\nds_read_b128 a[92:95],v250 offset:7168\nds_read_b128 a[124:127],v250 offset:15360\nM v[172:175],a[32:35],a[176:179],v[172:175]\nbuffer_store_dwordx4 a[248:251],v253,s[52:55],s33 offen offset:2048 sc1\nv_cvt_pk_bf16_f32 v218,v184,v185\nv_cvt_pk_bf16_f32 v219,v186,v187\ns_waitcnt lgkmcnt(6)\n" \
"M v[184:187],a[48:51],a[160:163],v[232:235]\nv_exp_f32 v188,v188\nM v[184:187],a[52:55],a[164:167],v[184:187]\nv_exp_f32 v189,v189\nM v[184:187],a[56:59],a[168:171],v[184:187]\nv_exp_f32 v190,v190\nv_exp_f32 v191,v191\nM v[184:187],a[60:63],a[172:175],v[184:187]\n" \
"v_cvt_pk_bf16_f32 v222,v188,v189\nv_cvt_pk_bf16_f32 v223,v190,v191\nM v[188:191],a[60:63],a[188:191],v[236:239]\nv_xor_b32 v250,0x18000,v250\ns_add_u32 s70,s78,16384\nM v[188:191],a[56:59],a[184:187],v[188:191]\nbuffer_load_dwordx4 v240,s[44:47],s95 offen offset:3072 sc1 lds\nM v[188:191],a[52:55],a[180:183],v[188:191]\n" \
"s_add_u32 m0,m0,0x10000\nv_cvt_pk_bf16_f32 v212,v164,v165\nM v[188:191],a[48:51],a[176:179],v[188:191]\nv_cvt_pk_bf16_f32 v213,v166,v167\nv_cvt_pk_bf16_f32 v214,v180,v181\nM v[64:67],a[64:67],v[200:203],v[64:67]\nbuffer_store_dwordx4 a[248:251],v253,s[52:55],s33 offen offset:3072 sc1\nM v[64:67],a[96:99],v[216:219],v[64:67]\n" \
"v_cvt_pk_bf16_f32 v215,v182,v183\ns_add_u32 s94,s94,0x4000\nM v[72:75],a[68:71],v[200:203],v[72:75]\ns_waitcnt vmcnt(16) lgkmcnt(0)\ns_barrier\nM v[72:75],a[100:103],v[216:219],v[72:75]\nds_read_b128 a[0:3],v250 offset:0\nds_read_b128 a[4:7],v250 offset:1024\n" \
"M v[80:83],a[72:75],v[200:203],v[80:83]\nv_exp_f32 v136,v136\nM v[80:83],a[104:107],v[216:219],v[80:83]\nbuffer_store_dwordx4 a[248:251],v253,s[52:55],s71 offen offset:0 sc1\nM v[88:91],a[76:79],v[200:203],v[88:91]\nv_exp_f32 v137,v137\nM v[88:91],a[108:111],v[216:219],v[88:91]\nbuffer_load_dwordx4 v240,s[48:51],s95 offen offset:0 sc1 lds\n" \
"M v[96:99],a[80:83],v[200:203],v[96:99]\nv_exp_f32 v138,v138\nM v[96:99],a[112:115],v[216:219],v[96:99]\nbuffer_store_dwordx4 a[248:251],v253,s[52:55],s71 offen offset:1024 sc1\nM v[104:107],a[84:87],v[200:203],v[104:107]\nv_exp_f32 v139,v139\nM v[104:107],a[116:119],v[216:219],v[104:107]\nds_read_b128 a[8:11],v250 offset:2048\n" \
"ds_read_b128 a[12:15],v250 offset:3072\nM v[112:115],a[88:91],v[200:203],v[112:115]\nv_exp_f32 v152,v152\nM v[112:115],a[120:123],v[216:219],v[112:115]\nbuffer_store_dwordx4 a[248:251],v253,s[52:55],s71 offen offset:2048 sc1\nM v[120:123],a[92:95],v[200:203],v[120:123]\nv_exp_f32 v153,v153\nM v[120:123],a[124:127],v[216:219],v[120:123]\n" \
"v_exp_f32 v154,v154\nM a[200:203],a[208:211],v[200:203],a[200:203]\nv_exp_f32 v155,v155\nM a[200:203],a[208:211],v[216:219],a[200:203]\nbuffer_store_dwordx4 a[248:251],v253,s[52:55],s71 offen offset:3072 sc1\nM v[68:71],a[64:67],v[204:207],v[68:71]\nv_exp_f32 v168,v168\nM v[68:71],a[96:99],v[220:223],v[68:71]\n" \
"ds_read_b128 a[16:19],v250 offset:4096\nds_read_b128 a[20:23],v250 offset:5120\nM v[76:79],a[68:71],v[204:207],v[76:79]\nv_exp_f32 v169,v169\nM v[76:79],a[100:103],v[220:223],v[76:79]\nv_exp_f32 v170,v170\nM v[84:87],a[72:75],v[204:207],v[84:87]\nv_exp_f32 v171,v171\n" \
"M v[84:87],a[104:107],v[220:223],v[84:87]\nbuffer_load_dwordx4 v240,s[48:51],s95 offen offset:1024 sc1 lds\nM v[92:95],a[76:79],v[204:207],v[92:95]\nv_exp_f32 v184,v184\nM v[92:95],a[108:111],v[220:223],v[92:95]\nbuffer_store_dwordx4 a[248:251],v253,s[52:55],s78 offen offset:0 sc1\nM v[100:103],a[80:83],v[204:207],v[100:103]\nv_exp_f32 v185,v185\n" \
"M v[100:103],a[112:115],v[220:223],v[100:103]\nds_read_b128 a[24:27],v250 offset:6144\nds_read_b128 a[28:31],v250 offset:7168\nM v[108:111],a[84:87],v[204:207],v[108:111]\nv_exp_f32 v186,v186\nM v[108:111],a[116:119],v[220:223],v[108:111]\nbuffer_store_dwordx4 a[248:251],v253,s[52:55],s78 offen offset:1024 sc1\nM v[116:119],a[88:91],v[204:207],v[116:119]\n" \
"v_exp_f32 v187,v187\nM v[116:119],a[120:123],v[220:223],v[116:119]\ns_and_b32 s94,s94,0xffff\nv_cvt_pk_bf16_f32 v200,v136,v137\nM v[124:127],a[92:95],v[204:207],v[124:127]\nv_exp_f32 v140,v140\nM v[124:127],a[124:127],v[220:223],v[124:127]\nbuffer_store_dwordx4 a[248:251],v253,s[52:55],s78 offen offset:2048 sc1\n" \
"M a[204:207],a[208:211],v[204:207],a[204:207]\nv_exp_f32 v141,v141\nM a[204:207],a[208:211],v[220:223],a[204:207]\nds_read_b128 a[32:35],v250 offset:8192\nds_read_b128 a[36:39],v250 offset:9216\nM v[0:3],a[64:67],v[192:195],v[0:3]\nv_exp_f32 v142,v142\nM v[0:3],a[96:99],v[208:211],v[0:3]\n" \
"v_exp_f32 v143,v143\nM v[8:11],a[68:71],v[192:195],v[8:11]\nv_exp_f32 v156,v156\nM v[8:11],a[100:103],v[208:211],v[8:11]\nbuffer_load_dwordx4 v240,s[48:51],s95 offen offset:2048 sc1 lds\nM v[16:19],a[72:75],v[192:195],v[16:19]\nv_exp_f32 v157,v157\nM v[16:19],a[104:107],v[208:211],v[16:19]\n" \
"buffer_store_dwordx4 a[248:251],v253,s[52:55],s78 offen offset:3072 sc1\nM v[24:27],a[76:79],v[192:195],v[24:27]\nv_exp_f32 v158,v158\nM v[24:27],a[108:111],v[208:211],v[24:27]\nds_read_b128 a[40:43],v250 offset:10240\nds_read_b128 a[44:47],v250 offset:11264\nM v[32:35],a[80:83],v[192:195],v[32:35]\nv_exp_f32 v159,v159\n" \
"M v[32:35],a[112:115],v[208:211],v[32:35]\nbuffer_store_dwordx4 a[248:251],v253,s[52:55],s70 offen offset:0 sc1\nM v[40:43],a[84:87],v[192:195],v[40:43]\nv_cvt_pk_bf16_f32 v201,v138,v139\nv_cvt_pk_bf16_f32 v202,v152,v153\nM v[40:43],a[116:119],v[208:211],v[40:43]\nv_cvt_pk_bf16_f32 v203,v154,v155\nv_cvt_pk_bf16_f32 v216,v168,v169\n" \
"M v[48:51],a[88:91],v[192:195],v[48:51]\nv_cvt_pk_bf16_f32 v217,v170,v171\nv_cvt_pk_bf16_f32 v218,v184,v185\nM v[48:51],a[120:123],v[208:211],v[48:51]\nbuffer_store_dwordx4 a[248:251],v253,s[52:55],s70 offen offset:1024 sc1\nM v[56:59],a[92:95],v[192:195],v[56:59]\nv_cvt_pk_bf16_f32 v219,v186,v187\nM v[56:59],a[124:127],v[208:211],v[56:59]\n" \
"ds_read_b128 a[48:51],v250 offset:12288\nds_read_b128 a[52:55],v250 offset:13312\nM a[192:195],a[208:211],v[192:195],a[192:195]\nM a[192:195],a[208:211],v[208:211],a[192:195]\nM v[4:7],a[64:67],v[196:199],v[4:7]\nM v[4:7],a[96:99],v[212:215],v[4:7]\nbuffer_load_dwordx4 v240,s[48:51],s95 offen offset:3072 sc1 lds\nM v[12:15],a[68:71],v[196:199],v[12:15]\n" \
"s_add_u32 s95,s95,0xffff4000\nM v[12:15],a[100:103],v[212:215],v[12:15]\nbuffer_store_dwordx4 a[248:251],v253,s[52:55],s70 offen offset:2048 sc1\nM v[20:23],a[72:75],v[196:199],v[20:23]\nM v[20:23],a[104:107],v[212:215],v[20:23]\nds_read_b128 a[56:59],v250 offset:14336\nds_read_b128 a[60:63],v250 offset:15360\nM v[28:31],a[76:79],v[196:199],v[28:31]\n" \
"v_add_u32 v250,s94,v241\nM v[28:31],a[108:111],v[212:215],v[28:31]\nbuffer_store_dwordx4 a[248:251],v253,s[52:55],s70 offen offset:3072 sc1\nM v[36:39],a[80:83],v[196:199],v[36:39]\nM v[36:39],a[112:115],v[212:215],v[36:39]\nM v[44:47],a[84:87],v[196:199],v[44:47]\nM v[44:47],a[116:119],v[212:215],v[44:47]\nM v[52:55],a[88:91],v[196:199],v[52:55]\n" \
"M v[52:55],a[120:123],v[212:215],v[52:55]\nM v[60:63],a[92:95],v[196:199],v[60:63]\nM v[60:63],a[124:127],v[212:215],v[60:63]\nM a[196:199],a[208:211],v[196:199],a[196:199]\nM a[196:199],a[208:211],v[212:215],a[196:199]\n.Lsl:\ns_waitcnt lgkmcnt(4)\nM v[136:139],a[0:3],a[160:163],v[232:235]\n" \
"s_add_u32 m0,s94,s64\nv_cvt_pk_bf16_f32 v204,v140,v141\nM v[136:139],a[4:7],a[164:167],v[136:139]\nds_read_b128 a[64:67],v250 offset:0\nds_read_b128 a[96:99],v250 offset:8192\nM v[136:139],a[8:11],a[168:171],v[136:139]\nbuffer_load_dwordx4 v240,s[44:47],s95 offen offset:0 sc1 lds\nM v[136:139],a[12:15],a[172:175],v[136:139]\n" \
"v_cvt_pk_bf16_f32 v205,v142,v143\nv_cvt_pk_bf16_f32 v206,v156,v157\nM v[140:143],a[12:15],a[188:191],v[236:239]\nds_read_b128 a[68:71],v250 offset:1024\nds_read_b128 a[100:103],v250 offset:9216\nM v[140:143],a[8:11],a[184:187],v[140:143]\nv_cvt_pk_bf16_f32 v207,v158,v159\ns_add_u32 s94,s94,0x4000\n" \
"M v[140:143],a[4:7],a[180:183],v[140:143]\nv_exp_f32 v172,v172\nM v[140:143],a[0:3],a[176:179],v[140:143]\nds_read_b128 a[72:75],v250 offset:2048\nds_read_b128 a[104:107],v250 offset:10240\nM v[152:155],a[16:19],a[160:163],v[232:235]\nv_exp_f32 v173,v173\nM v[152:155],a[20:23],a[164:167],v[152:155]\n" \
"v_exp_f32 v174,v174\nM v[152:155],a[24:27],a[168:171],v[152:155]\nds_read_b128 a[76:79],v250 offset:3072\nds_read_b128 a[108:111],v250 offset:11264\nM v[152:155],a[28:31],a[172:175],v[152:155]\nbuffer_load_dwordx4 v240,s[44:47],s95 offen offset:1024 sc1 lds\nM v[156:159],a[28:31],a[188:191],v[236:239]\nv_exp_f32 v175,v175\n" \
"M v[156:159],a[24:27],a[184:187],v[156:159]\nds_read_b128 a[80:83],v250 offset:4096\nds_read_b128 a[112:115],v250 offset:12288\nM v[156:159],a[20:23],a[180:183],v[156:159]\nv_cvt_pk_bf16_f32 v220,v172,v173\nv_cvt_pk_bf16_f32 v221,v174,v175\nM v[156:159],a[16:19],a[176:179],v[156:159]\nv_exp_f32 v188,v188\n" \
"M v[168:171],a[32:35],a[160:163],v[232:235]\nds_read_b128 a[84:87],v250 offset:5120\nds_read_b128 a[116:119],v250 offset:13312\nM v[168:171],a[36:39],a[164:167],v[168:171]\nv_exp_f32 v189,v189\nM v[168:171],a[40:43],a[168:171],v[168:171]\nv_exp_f32 v190,v190\nM v[168:171],a[44:47],a[172:175],v[168:171]\n" \
"ds_read_b128 a[88:91],v250 offset:6144\nds_read_b128 a[120:123],v250 offset:14336\nM v[172:175],a[44:47],a[188:191],v[236:239]\nbuffer_load_dwordx4 v240,s[44:47],s95 offen offset:2048 sc1 lds\nM v[172:175],a[40:43],a[184:187],v[172:175]\nv_exp_f32 v191,v191\nM v[172:175],a[36:39],a[180:183],v[172:175]\nds_read_b128 a[92:95],v250 offset:7168\n" \
"ds_read_b128 a[124:127],v250 offset:15360\nM v[172:175],a[32:35],a[176:179],v[172:175]\nv_cvt_pk_bf16_f32 v222,v188,v189\ns_waitcnt lgkmcnt(7)\nM v[184:187],a[48:51],a[160:163],v[232:235]\nv_cvt_pk_bf16_f32 v223,v190,v191\nv_xor_b32 v250,0x18000,v250\nM v[184:187],a[52:55],a[164:167],v[184:187]\n" \
"v_exp_f32 v136,v136\nM v[184:187],a[56:59],a[168:171],v[184:187]\nv_exp_f32 v137,v137\nM v[184:187],a[60:63],a[172:175],v[184:187]\nv_exp_f32 v138,v138\nM v[188:191],a[60:63],a[188:191],v[236:239]\nv_exp_f32 v139,v139\nM v[188:191],a[56:59],a[184:187],v[188:191]\n" \
"buffer_load_dwordx4 v240,s[44:47],s95 offen offset:3072 sc1 lds\nM v[188:191],a[52:55],a[180:183],v[188:191]\ns_add_u32 m0,m0,0x10000\ns_and_b32 s94,s94,0xffff\nM v[188:191],a[48:51],a[176:179],v[188:191]\nv_exp_f32 v152,v152\nM v[64:67],a[64:67],v[200:203],v[64:67]\nv_exp_f32 v153,v153\n" \
"M v[64:67],a[96:99],v[216:219],v[64:67]\nv_exp_f32 v154,v154\nM v[72:75],a[68:71],v[200:203],v[72:75]\ns_waitcnt vmcnt(16) lgkmcnt(0)\ns_barrier\nM v[72:75],a[100:103],v[216:219],v[72:75]\nds_read_b128 a[0:3],v250 offset:0\nds_read_b128 a[4:7],v250 offset:1024\n" \
"M v[80:83],a[72:75],v[200:203],v[80:83]\nv_exp_f32 v155,v155\nM v[80:83],a[104:107],v[216:219],v[80:83]\nbuffer_load_dwordx4 v240,s[48:51],s95 offen offset:0 sc1 lds\nM v[88:91],a[76:79],v[200:203],v[88:91]\nv_exp_f32 v168,v168\nM v[88:91],a[108:111],v[216:219],v[88:91]\nds_read_b128 a[8:11],v250 offset:2048\n" \
"ds_read_b128 a[12:15],v250 offset:3072\nM v[96:99],a[80:83],v[200:203],v[96:99]\nv_exp_f32 v169,v169\nM v[96:99],a[112:115],v[216:219],v[96:99]\nv_exp_f32 v170,v170\nM v[104:107],a[84:87],v[200:203],v[104:107]\nv_exp_f32 v171,v171\nM v[104:107],a[116:119],v[216:219],v[104:107]\n" \
"ds_read_b128 a[16:19],v250 offset:4096\nds_read_b128 a[20:23],v250 offset:5120\nM v[112:115],a[88:91],v[200:203],v[112:115]\nv_exp_f32 v184,v184\nM v[112:115],a[120:123],v[216:219],v[112:115]\nbuffer_load_dwordx4 v240,s[48:51],s95 offen offset:1024 sc1 lds\nM v[120:123],a[92:95],v[200:203],v[120:123]\nv_exp_f32 v185,v185\n" \
"M v[120:123],a[124:127],v[216:219],v[120:123]\nds_read_b128 a[24:27],v250 offset:6144\nds_read_b128 a[28:31],v250 offset:7168\nM a[200:203],a[208:211],v[200:203],a[200:203]\nv_exp_f32 v186,v186\nM a[200:203],a[208:211],v[216:219],a[200:203]\nv_exp_f32 v187,v187\nM v[68:71],a[64:67],v[204:207],v[68:71]\n" \
"v_exp_f32 v140,v140\nM v[68:71],a[96:99],v[220:223],v[68:71]\nds_read_b128 a[32:35],v250 offset:8192\nds_read_b128 a[36:39],v250 offset:9216\nM v[76:79],a[68:71],v[204:207],v[76:79]\nv_exp_f32 v141,v141\nM v[76:79],a[100:103],v[220:223],v[76:79]\nbuffer_load_dwordx4 v240,s[48:51],s95 offen offset:2048 sc1 lds\n" \
"M v[84:87],a[72:75],v[204:207],v[84:87]\nv_exp_f32 v142,v142\nM v[84:87],a[104:107],v[220:223],v[84:87]\nds_read_b128 a[40:43],v250 offset:10240\nds_read_b128 a[44:47],v250 offset:11264\nM v[92:95],a[76:79],v[204:207],v[92:95]\nv_exp_f32 v143,v143\nM v[92:95],a[108:111],v[220:223],v[92:95]\n" \
"v_exp_f32 v156,v156\nM v[100:103],a[80:83],v[204:207],v[100:103]\nv_exp_f32 v157,v157\nM v[100:103],a[112:115],v[220:223],v[100:103]\nds_read_b128 a[48:51],v250 offset:12288\nds_read_b128 a[52:55],v250 offset:13312\nM v[108:111],a[84:87],v[204:207],v[108:111]\nv_exp_f32 v158,v158\n" \
"M v[108:111],a[116:119],v[220:223],v[108:111]\nbuffer_load_dwordx4 v240,s[48:51],s95 offen offset:3072 sc1 lds\nM v[116:119],a[88:91],v[204:207],v[116:119]\nv_exp_f32 v159,v159\nM v[116:119],a[120:123],v[220:223],v[116:119]\nds_read_b128 a[56:59],v250 offset:14336\nds_read_b128 a[60:63],v250 offset:15360\nM v[124:127],a[92:95],v[204:207],v[124:127]\n" \
"v_cvt_pk_bf16_f32 v200,v136,v137\nv_cvt_pk_bf16_f32 v201,v138,v139\nM v[124:127],a[124:127],v[220:223],v[124:127]\nv_cvt_pk_bf16_f32 v202,v152,v153\nv_cvt_pk_bf16_f32 v203,v154,v155\nM a[204:207],a[208:211],v[204:207],a[204:207]\nv_cvt_pk_bf16_f32 v216,v168,v169\nv_cvt_pk_bf16_f32 v217,v170,v171\n" \
"v_cvt_pk_bf16_f32 v218,v184,v185\nv_cvt_pk_bf16_f32 v219,v186,v187\nv_add_u32 v250,s94,v241\ns_add_u32 s95,s95,0x4000\nM a[204:207],a[208:211],v[220:223],a[204:207]\ns_cmp_lg_u32 s95,s96\ns_cbranch_scc1 .Lsl\ns_waitcnt lgkmcnt(4)\n" \
"M v[128:131],a[0:3],a[128:131],v[224:227]\ns_add_u32 m0,s94,s64\nv_cvt_pk_bf16_f32 v204,v140,v141\nM v[128:131],a[4:7],a[132:135],v[128:131]\nds_read_b128 a[64:67],v250 offset:0\nds_read_b128 a[96:99],v250 offset:8192\nM v[128:131],a[8:11],a[136:139],v[128:131]\nbuffer_load_dwordx4 v240,s[44:47],s95 offen offset:0 sc1 lds\n" \
"M v[128:131],a[12:15],a[140:143],v[128:131]\nv_exp_f32 v188,v188\nM v[132:135],a[12:15],a[156:159],v[228:231]\nds_read_b128 a[68:71],v250 offset:1024\nds_read_b128 a[100:103],v250 offset:9216\nM v[132:135],a[8:11],a[152:155],v[132:135]\nv_exp_f32 v189,v189\nM v[132:135],a[4:7],a[148:151],v[132:135]\n" \
"v_exp_f32 v190,v190\nM v[132:135],a[0:3],a[144:147],v[132:135]\nds_read_b128 a[72:75],v250 offset:2048\nds_read_b128 a[104:107],v250 offset:10240\nM v[144:147],a[16:19],a[128:131],v[224:227]\nv_exp_f32 v191,v191\nM v[144:147],a[20:23],a[132:135],v[144:147]\nv_exp_f32 v172,v172\n" \
"M v[144:147],a[24:27],a[136:139],v[144:147]\nds_read_b128 a[76:79],v250 offset:3072\nds_read_b128 a[108:111],v250 offset:11264\nM v[144:147],a[28:31],a[140:143],v[144:147]\nbuffer_load_dwordx4 v240,s[44:47],s95 offen offset:1024 sc1 lds\nM v[148:151],a[28:31],a[156:159],v[228:231]\nv_exp_f32 v173,v173\nM v[148:151],a[24:27],a[152:155],v[148:151]\n" \
"ds_read_b128 a[80:83],v250 offset:4096\nds_read_b128 a[112:115],v250 offset:12288\nM v[148:151],a[20:23],a[148:151],v[148:151]\nv_exp_f32 v174,v174\nM v[148:151],a[16:19],a[144:147],v[148:151]\nv_exp_f32 v175,v175\nM v[160:163],a[32:35],a[128:131],v[224:227]\nds_read_b128 a[84:87],v250 offset:5120\n" \
"ds_read_b128 a[116:119],v250 offset:13312\nM v[160:163],a[36:39],a[132:135],v[160:163]\nv_cvt_pk_bf16_f32 v205,v142,v143\nv_cvt_pk_bf16_f32 v206,v156,v157\nM v[160:163],a[40:43],a[136:139],v[160:163]\nv_cvt_pk_bf16_f32 v207,v158,v159\nv_cvt_pk_bf16_f32 v220,v172,v173\nM v[160:163],a[44:47],a[140:143],v[160:163]\n" \
"ds_read_b128 a[88:91],v250 offset:6144\nds_read_b128 a[120:123],v250 offset:14336\nM v[164:167],a[44:47],a[156:159],v[228:231]\nbuffer_load_dwordx4 v240,s[44:47],s95 offen offset:2048 sc1 lds\nM v[164:167],a[40:43],a[152:155],v[164:167]\nv_cvt_pk_bf16_f32 v221,v174,v175\nv_cvt_pk_bf16_f32 v222,v188,v189\nM v[164:167],a[36:39],a[148:151],v[164:167]\n" \
"ds_read_b128 a[92:95],v250 offset:7168\nds_read_b128 a[124:127],v250 offset:15360\nM v[164:167],a[32:35],a[144:147],v[164:167]\nv_xor_b32 v250,0x18000,v250\ns_waitcnt lgkmcnt(7)\nM v[176:179],a[48:51],a[128:131],v[224:227]\nv_cvt_pk_bf16_f32 v223,v190,v191\ns_add_u32 s94,s94,0x4000\n" \
"M v[176:179],a[52:55],a[132:135],v[176:179]\nv_exp_f32 v128,v128\nM v[176:179],a[56:59],a[136:139],v[176:179]\nv_exp_f32 v129,v129\nM v[176:179],a[60:63],a[140:143],v[176:179]\nv_exp_f32 v130,v130\nM v[180:183],a[60:63],a[156:159],v[228:231]\nv_exp_f32 v131,v131\n" \
"M v[180:183],a[56:59],a[152:155],v[180:183]\nbuffer_load_dwordx4 v240,s[44:47],s95 offen offset:3072 sc1 lds\nM v[180:183],a[52:55],a[148:151],v[180:183]\ns_add_u32 m0,m0,0x10000\ns_and_b32 s94,s94,0xffff\nM v[180:183],a[48:51],a[144:147],v[180:183]\nv_exp_f32 v144,v144\nM v[64:67],a[64:67],v[200:203],v[64:67]\n" \
"v_exp_f32 v145,v145\nM v[64:67],a[96:99],v[216:219],v[64:67]\nv_exp_f32 v146,v146\nM v[72:75],a[68:71],v[200:203],v[72:75]\ns_waitcnt vmcnt(16) lgkmcnt(0)\ns_barrier\nM v[72:75],a[100:103],v[216:219],v[72:75]\nds_read_b128 a[0:3],v250 offset:0\n" \
"ds_read_b128 a[4:7],v250 offset:1024\nM v[80:83],a[72:75],v[200:203],v[80:83]\nv_exp_f32 v147,v147\nM v[80:83],a[104:107],v[216:219],v[80:83]\nbuffer_load_dwordx4 v240,s[48:51],s95 offen offset:0 sc1 lds\nM v[88:91],a[76:79],v[200:203],v[88:91]\nv_exp_f32 v160,v160\nM v[88:91],a[108:111],v[216:219],v[88:91]\n" \
"ds_read_b128 a[8:11],v250 offset:2048\nds_read_b128 a[12:15],v250 offset:3072\nM v[96:99],a[80:83],v[200:203],v[96:99]\nv_exp_f32 v161,v161\nM v[96:99],a[112:115],v[216:219],v[96:99]\nv_exp_f32 v162,v162\nM v[104:107],a[84:87],v[200:203],v[104:107]\nv_exp_f32 v163,v163\n" \
"M v[104:107],a[116:119],v[216:219],v[104:107]\nds_read_b128 a[16:19],v250 offset:4096\nds_read_b128 a[20:23],v250 offset:5120\nM v[112:115],a[88:91],v[200:203],v[112:115]\nv_exp_f32 v176,v176\nM v[112:115],a[120:123],v[216:219],v[112:115]\nbuffer_load_dwordx4 v240,s[48:51],s95 offen offset:1024 sc1 lds\nM v[120:123],a[92:95],v[200:203],v[120:123]\n" \
"v_exp_f32 v177,v177\nM v[120:123],a[124:127],v[216:219],v[120:123]\nds_read_b128 a[24:27],v250 offset:6144\nds_read_b128 a[28:31],v250 offset:7168\nM a[200:203],a[208:211],v[200:203],a[200:203]\nv_exp_f32 v178,v178\nM a[200:203],a[208:211],v[216:219],a[200:203]\nv_exp_f32 v179,v179\n" \
"M v[68:71],a[64:67],v[204:207],v[68:71]\nv_exp_f32 v132,v132\nM v[68:71],a[96:99],v[220:223],v[68:71]\nds_read_b128 a[32:35],v250 offset:8192\nds_read_b128 a[36:39],v250 offset:9216\nM v[76:79],a[68:71],v[204:207],v[76:79]\nv_exp_f32 v133,v133\nM v[76:79],a[100:103],v[220:223],v[76:79]\n" \
"buffer_load_dwordx4 v240,s[48:51],s95 offen offset:2048 sc1 lds\nM v[84:87],a[72:75],v[204:207],v[84:87]\nv_exp_f32 v134,v134\nM v[84:87],a[104:107],v[220:223],v[84:87]\nds_read_b128 a[40:43],v250 offset:10240\nds_read_b128 a[44:47],v250 offset:11264\nM v[92:95],a[76:79],v[204:207],v[92:95]\nv_exp_f32 v135,v135\n" \
"M v[92:95],a[108:111],v[220:223],v[92:95]\nv_exp_f32 v148,v148\nM v[100:103],a[80:83],v[204:207],v[100:103]\nv_exp_f32 v149,v149\nM v[100:103],a[112:115],v[220:223],v[100:103]\nds_read_b128 a[48:51],v250 offset:12288\nds_read_b128 a[52:55],v250 offset:13312\nM v[108:111],a[84:87],v[204:207],v[108:111]\n" \
"v_exp_f32 v150,v150\nM v[108:111],a[116:119],v[220:223],v[108:111]\nbuffer_load_dwordx4 v240,s[48:51],s95 offen offset:3072 sc1 lds\nM v[116:119],a[88:91],v[204:207],v[116:119]\nv_exp_f32 v151,v151\nM v[116:119],a[120:123],v[220:223],v[116:119]\nds_read_b128 a[56:59],v250 offset:14336\nds_read_b128 a[60:63],v250 offset:15360\n" \
"M v[124:127],a[92:95],v[204:207],v[124:127]\nv_cvt_pk_bf16_f32 v192,v128,v129\nv_cvt_pk_bf16_f32 v193,v130,v131\nM v[124:127],a[124:127],v[220:223],v[124:127]\nv_cvt_pk_bf16_f32 v194,v144,v145\nv_cvt_pk_bf16_f32 v195,v146,v147\nM a[204:207],a[208:211],v[204:207],a[204:207]\nv_cvt_pk_bf16_f32 v208,v160,v161\n" \
"v_cvt_pk_bf16_f32 v209,v162,v163\nv_cvt_pk_bf16_f32 v210,v176,v177\nv_cvt_pk_bf16_f32 v211,v178,v179\nv_add_u32 v250,s94,v241\ns_add_u32 s95,s95,0x4000\nM a[204:207],a[208:211],v[220:223],a[204:207]\ns_branch .Lse\n.Ltrans:\n" \
"s_waitcnt lgkmcnt(4)\nM v[128:131],a[0:3],a[128:131],v[224:227]\ns_add_u32 m0,s94,s64\nv_cvt_pk_bf16_f32 v212,v164,v165\nM v[128:131],a[4:7],a[132:135],v[128:131]\nds_read_b128 a[64:67],v250 offset:0\nds_read_b128 a[96:99],v250 offset:8192\nM v[128:131],a[8:11],a[136:139],v[128:131]\n" \
"buffer_load_dwordx4 v240,s[44:47],s95 offen offset:0 sc1 lds\nM v[128:131],a[12:15],a[140:143],v[128:131]\nv_cvt_pk_bf16_f32 v213,v166,v167\nv_cvt_pk_bf16_f32 v214,v180,v181\nM v[132:135],a[12:15],a[156:159],v[228:231]\nds_read_b128 a[68:71],v250 offset:1024\nds_read_b128 a[100:103],v250 offset:9216\nM v[132:135],a[8:11],a[152:155],v[132:135]\n" \
"buffer_store_dwordx4 a[248:251],v253,s[52:55],s33 offen offset:0 sc1\nM v[132:135],a[4:7],a[148:151],v[132:135]\nv_cvt_pk_bf16_f32 v215,v182,v183\ns_add_u32 s71,s33,16384\nM v[132:135],a[0:3],a[144:147],v[132:135]\nds_read_b128 a[72:75],v250 offset:2048\nds_read_b128 a[104:107],v250 offset:10240\nM v[144:147],a[16:19],a[128:131],v[224:227]\n" \
"v_exp_f32 v184,v184\nM v[144:147],a[20:23],a[132:135],v[144:147]\nv_exp_f32 v185,v185\nM v[144:147],a[24:27],a[136:139],v[144:147]\nds_read_b128 a[76:79],v250 offset:3072\nds_read_b128 a[108:111],v250 offset:11264\nM v[144:147],a[28:31],a[140:143],v[144:147]\nbuffer_load_dwordx4 v240,s[44:47],s95 offen offset:1024 sc1 lds\n" \
"M v[148:151],a[28:31],a[156:159],v[228:231]\nv_exp_f32 v186,v186\nM v[148:151],a[24:27],a[152:155],v[148:151]\nds_read_b128 a[80:83],v250 offset:4096\nds_read_b128 a[112:115],v250 offset:12288\nM v[148:151],a[20:23],a[148:151],v[148:151]\nbuffer_store_dwordx4 a[248:251],v253,s[52:55],s33 offen offset:1024 sc1\nM v[148:151],a[16:19],a[144:147],v[148:151]\n" \
"v_exp_f32 v187,v187\nM v[160:163],a[32:35],a[128:131],v[224:227]\nds_read_b128 a[84:87],v250 offset:5120\nds_read_b128 a[116:119],v250 offset:13312\nM v[160:163],a[36:39],a[132:135],v[160:163]\nv_cvt_pk_bf16_f32 v216,v168,v169\nv_cvt_pk_bf16_f32 v217,v170,v171\nM v[160:163],a[40:43],a[136:139],v[160:163]\n" \
"v_cvt_pk_bf16_f32 v218,v184,v185\nv_cvt_pk_bf16_f32 v219,v186,v187\nM v[160:163],a[44:47],a[140:143],v[160:163]\nds_read_b128 a[88:91],v250 offset:6144\nds_read_b128 a[120:123],v250 offset:14336\nM v[164:167],a[44:47],a[156:159],v[228:231]\nbuffer_load_dwordx4 v240,s[44:47],s95 offen offset:2048 sc1 lds\nM v[164:167],a[40:43],a[152:155],v[164:167]\n" \
"s_add_u32 s70,s78,16384\nv_cvt_pk_bf16_f32 v204,v140,v141\nM v[164:167],a[36:39],a[148:151],v[164:167]\nds_read_b128 a[92:95],v250 offset:7168\nds_read_b128 a[124:127],v250 offset:15360\nM v[164:167],a[32:35],a[144:147],v[164:167]\nbuffer_store_dwordx4 a[248:251],v253,s[52:55],s33 offen offset:2048 sc1\ns_waitcnt lgkmcnt(6)\n" \
"M v[176:179],a[48:51],a[128:131],v[224:227]\nv_xor_b32 v250,0x18000,v250\nv_cvt_pk_bf16_f32 v205,v142,v143\nM v[176:179],a[52:55],a[132:135],v[176:179]\nv_exp_f32 v172,v172\nM v[176:179],a[56:59],a[136:139],v[176:179]\nv_exp_f32 v173,v173\nM v[176:179],a[60:63],a[140:143],v[176:179]\n" \
"v_exp_f32 v174,v174\nM v[180:183],a[60:63],a[156:159],v[228:231]\nv_exp_f32 v175,v175\nM v[180:183],a[56:59],a[152:155],v[180:183]\nbuffer_load_dwordx4 v240,s[44:47],s95 offen offset:3072 sc1 lds\nM v[180:183],a[52:55],a[148:151],v[180:183]\ns_add_u32 m0,m0,0x10000\nv_cvt_pk_bf16_f32 v206,v156,v157\n" \
"M v[180:183],a[48:51],a[144:147],v[180:183]\nv_exp_f32 v188,v188\nM v[0:3],a[64:67],v[192:195],v[0:3]\nbuffer_store_dwordx4 a[248:251],v253,s[52:55],s33 offen offset:3072 sc1\nM v[0:3],a[96:99],v[208:211],v[0:3]\nv_exp_f32 v189,v189\nM v[8:11],a[68:71],v[192:195],v[8:11]\ns_waitcnt vmcnt(16) lgkmcnt(0)\n" \
"s_barrier\nM v[8:11],a[100:103],v[208:211],v[8:11]\nds_read_b128 a[0:3],v250 offset:0\nds_read_b128 a[4:7],v250 offset:1024\nM v[16:19],a[72:75],v[192:195],v[16:19]\nv_exp_f32 v190,v190\nM v[16:19],a[104:107],v[208:211],v[16:19]\nbuffer_store_dwordx4 a[248:251],v253,s[52:55],s71 offen offset:0 sc1\n" \
"M v[24:27],a[76:79],v[192:195],v[24:27]\nv_exp_f32 v191,v191\nM v[24:27],a[108:111],v[208:211],v[24:27]\nbuffer_load_dwordx4 v240,s[48:51],s95 offen offset:0 sc1 lds\nM v[32:35],a[80:83],v[192:195],v[32:35]\nv_cvt_pk_bf16_f32 v207,v158,v159\nv_cvt_pk_bf16_f32 v220,v172,v173\nM v[32:35],a[112:115],v[208:211],v[32:35]\n" \
"buffer_store_dwordx4 a[248:251],v253,s[52:55],s71 offen offset:1024 sc1\nM v[40:43],a[84:87],v[192:195],v[40:43]\nv_cvt_pk_bf16_f32 v221,v174,v175\nv_cvt_pk_bf16_f32 v222,v188,v189\nM v[40:43],a[116:119],v[208:211],v[40:43]\nds_read_b128 a[8:11],v250 offset:2048\nds_read_b128 a[12:15],v250 offset:3072\nM v[48:51],a[88:91],v[192:195],v[48:51]\n" \
"v_cvt_pk_bf16_f32 v223,v190,v191\ns_add_u32 s94,s94,0x4000\nM v[48:51],a[120:123],v[208:211],v[48:51]\nbuffer_store_dwordx4 a[248:251],v253,s[52:55],s71 offen offset:2048 sc1\nM v[56:59],a[92:95],v[192:195],v[56:59]\nv_exp_f32 v128,v128\nM v[56:59],a[124:127],v[208:211],v[56:59]\nv_exp_f32 v129,v129\n" \
"M a[192:195],a[208:211],v[192:195],a[192:195]\nv_exp_f32 v130,v130\nM a[192:195],a[208:211],v[208:211],a[192:195]\nbuffer_store_dwordx4 a[248:251],v253,s[52:55],s71 offen offset:3072 sc1\nM v[4:7],a[64:67],v[196:199],v[4:7]\nv_exp_f32 v131,v131\nM v[4:7],a[96:99],v[212:215],v[4:7]\nds_read_b128 a[16:19],v250 offset:4096\n" \
"ds_read_b128 a[20:23],v250 offset:5120\nM v[12:15],a[68:71],v[196:199],v[12:15]\nv_exp_f32 v144,v144\nM v[12:15],a[100:103],v[212:215],v[12:15]\nv_exp_f32 v145,v145\nM v[20:23],a[72:75],v[196:199],v[20:23]\nv_exp_f32 v146,v146\nM v[20:23],a[104:107],v[212:215],v[20:23]\n" \
"buffer_load_dwordx4 v240,s[48:51],s95 offen offset:1024 sc1 lds\nM v[28:31],a[76:79],v[196:199],v[28:31]\nv_exp_f32 v147,v147\nM v[28:31],a[108:111],v[212:215],v[28:31]\nbuffer_store_dwordx4 a[248:251],v253,s[52:55],s78 offen offset:0 sc1\nM v[36:39],a[80:83],v[196:199],v[36:39]\nv_exp_f32 v160,v160\nM v[36:39],a[112:115],v[212:215],v[36:39]\n" \
"ds_read_b128 a[24:27],v250 offset:6144\nds_read_b128 a[28:31],v250 offset:7168\nM v[44:47],a[84:87],v[196:199],v[44:47]\nv_exp_f32 v161,v161\nM v[44:47],a[116:119],v[212:215],v[44:47]\nbuffer_store_dwordx4 a[248:251],v253,s[52:55],s78 offen offset:1024 sc1\nM v[52:55],a[88:91],v[196:199],v[52:55]\nv_exp_f32 v162,v162\n" \
"M v[52:55],a[120:123],v[212:215],v[52:55]\nv_exp_f32 v163,v163\nM v[60:63],a[92:95],v[196:199],v[60:63]\nv_exp_f32 v176,v176\nM v[60:63],a[124:127],v[212:215],v[60:63]\nbuffer_store_dwordx4 a[248:251],v253,s[52:55],s78 offen offset:2048 sc1\nM a[196:199],a[208:211],v[196:199],a[196:199]\nv_exp_f32 v177,v177\n" \
"M a[196:199],a[208:211],v[212:215],a[196:199]\nds_read_b128 a[32:35],v250 offset:8192\nds_read_b128 a[36:39],v250 offset:9216\nM v[64:67],a[64:67],v[200:203],v[64:67]\nv_exp_f32 v178,v178\nM v[64:67],a[96:99],v[216:219],v[64:67]\nv_exp_f32 v179,v179\nM v[72:75],a[68:71],v[200:203],v[72:75]\n" \
"s_and_b32 s94,s94,0xffff\nv_cvt_pk_bf16_f32 v192,v128,v129\nM v[72:75],a[100:103],v[216:219],v[72:75]\nbuffer_load_dwordx4 v240,s[48:51],s95 offen offset:2048 sc1 lds\nM v[80:83],a[72:75],v[200:203],v[80:83]\nv_exp_f32 v132,v132\nM v[80:83],a[104:107],v[216:219],v[80:83]\nbuffer_store_dwordx4 a[248:251],v253,s[52:55],s78 offen offset:3072 sc1\n" \
"M v[88:91],a[76:79],v[200:203],v[88:91]\nv_exp_f32 v133,v133\nM v[88:91],a[108:111],v[216:219],v[88:91]\nds_read_b128 a[40:43],v250 offset:10240\nds_read_b128 a[44:47],v250 offset:11264\nM v[96:99],a[80:83],v[200:203],v[96:99]\nv_exp_f32 v134,v134\nM v[96:99],a[112:115],v[216:219],v[96:99]\n" \
"buffer_store_dwordx4 a[248:251],v253,s[52:55],s70 offen offset:0 sc1\nM v[104:107],a[84:87],v[200:203],v[104:107]\nv_exp_f32 v135,v135\nM v[104:107],a[116:119],v[216:219],v[104:107]\nv_exp_f32 v148,v148\nM v[112:115],a[88:91],v[200:203],v[112:115]\nv_exp_f32 v149,v149\nM v[112:115],a[120:123],v[216:219],v[112:115]\n" \
"buffer_store_dwordx4 a[248:251],v253,s[52:55],s70 offen offset:1024 sc1\nM v[120:123],a[92:95],v[200:203],v[120:123]\nv_exp_f32 v150,v150\nM v[120:123],a[124:127],v[216:219],v[120:123]\nds_read_b128 a[48:51],v250 offset:12288\nds_read_b128 a[52:55],v250 offset:13312\nM a[200:203],a[208:211],v[200:203],a[200:203]\nv_exp_f32 v151,v151\n" \
"M a[200:203],a[208:211],v[216:219],a[200:203]\nv_cvt_pk_bf16_f32 v193,v130,v131\nv_cvt_pk_bf16_f32 v194,v144,v145\nM v[68:71],a[64:67],v[204:207],v[68:71]\nv_cvt_pk_bf16_f32 v195,v146,v147\nv_cvt_pk_bf16_f32 v208,v160,v161\nM v[68:71],a[96:99],v[220:223],v[68:71]\nbuffer_load_dwordx4 v240,s[48:51],s95 offen offset:3072 sc1 lds\n" \
"M v[76:79],a[68:71],v[204:207],v[76:79]\nv_cvt_pk_bf16_f32 v209,v162,v163\nv_cvt_pk_bf16_f32 v210,v176,v177\nM v[76:79],a[100:103],v[220:223],v[76:79]\nbuffer_store_dwordx4 a[248:251],v253,s[52:55],s70 offen offset:2048 sc1\nM v[84:87],a[72:75],v[204:207],v[84:87]\nv_cvt_pk_bf16_f32 v211,v178,v179\ns_add_u32 s95,s95,0x4000\n" \
"M v[84:87],a[104:107],v[220:223],v[84:87]\nds_read_b128 a[56:59],v250 offset:14336\nds_read_b128 a[60:63],v250 offset:15360\nM v[92:95],a[76:79],v[204:207],v[92:95]\nv_add_u32 v250,s94,v241\nM v[92:95],a[108:111],v[220:223],v[92:95]\nbuffer_store_dwordx4 a[248:251],v253,s[52:55],s70 offen offset:3072 sc1\nM v[100:103],a[80:83],v[204:207],v[100:103]\n" \
"M v[100:103],a[112:115],v[220:223],v[100:103]\nM v[108:111],a[84:87],v[204:207],v[108:111]\nM v[108:111],a[116:119],v[220:223],v[108:111]\nM v[116:119],a[88:91],v[204:207],v[116:119]\nM v[116:119],a[120:123],v[220:223],v[116:119]\nM v[124:127],a[92:95],v[204:207],v[124:127]\nM v[124:127],a[124:127],v[220:223],v[124:127]\nM a[204:207],a[208:211],v[204:207],a[204:207]\n" \
"M a[204:207],a[208:211],v[220:223],a[204:207]\n.Lse:\ns_waitcnt lgkmcnt(4)\nM v[128:131],a[0:3],a[128:131],v[224:227]\ns_add_u32 m0,s94,s64\nv_accvgpr_read_b32 v232,a200\nv_cvt_pk_bf16_f32 v196,v132,v133\nv_cvt_pk_bf16_f32 v197,v134,v135\n" \
"M v[128:131],a[4:7],a[132:135],v[128:131]\nds_read_b128 a[64:67],v250 offset:0\nds_read_b128 a[96:99],v250 offset:8192\nv_accvgpr_read_b32 v236,a204\nv_add_f32 v233,v232,v236\nM v[128:131],a[8:11],a[136:139],v[128:131]\nbuffer_load_dwordx4 v240,s[44:47],s95 offen offset:0 sc1 lds\nv_rcp_f32 v232,v232\n" \
"M v[128:131],a[12:15],a[140:143],v[128:131]\nv_rcp_f32 v236,v236\nv_add_f32 v233,v233,v232\nv_add_f32 v233,v233,v236\nM v[132:135],a[12:15],a[156:159],v[228:231]\nds_read_b128 a[68:71],v250 offset:1024\nds_read_b128 a[100:103],v250 offset:9216\nv_cmp_ngt_f32 vcc,s28,v233\n" \
"v_mul_f32 v64,v64,v232\nM v[132:135],a[8:11],a[152:155],v[132:135]\ns_and_b64 vcc,vcc,s[90:91]\ns_cselect_b32 s39,0x8000000,s16\ns_cselect_b32 s16,s16,0x8000000\ns_cselect_b32 s71,2,1\ns_addc_u32 s99,s99,0\nM v[132:135],a[4:7],a[148:151],v[132:135]\n" \
"v_mul_f32 v65,v65,v232\ns_add_u32 s37,s12,s67\nv_mul_f32 v66,v66,v232\nv_mul_f32 v67,v67,v232\nM v[132:135],a[0:3],a[144:147],v[132:135]\nds_read_b128 a[72:75],v250 offset:2048\nds_read_b128 a[104:107],v250 offset:10240\nv_cvt_pk_bf16_f32 v64,v64,v65\n" \
"s_add_u32 s37,s37,1048576\nM v[144:147],a[16:19],a[128:131],v[224:227]\nv_mul_f32 v72,v72,v232\nv_mul_f32 v73,v73,v232\nv_mul_f32 v74,v74,v232\nv_mul_f32 v75,v75,v232\nv_cvt_pk_bf16_f32 v65,v66,v67\nM v[144:147],a[20:23],a[132:135],v[144:147]\n" \
"s_or_b32 s37,s37,s39\nv_cvt_pk_bf16_f32 v66,v72,v73\nv_cvt_pk_bf16_f32 v67,v74,v75\nbuffer_store_dwordx4 v[64:67],v240,s[52:55],s37 offen offset:0 sc1\nM v[144:147],a[24:27],a[136:139],v[144:147]\nds_read_b128 a[76:79],v250 offset:3072\nds_read_b128 a[108:111],v250 offset:11264\nv_cvt_pk_bf16_f32 v198,v148,v149\n" \
"v_cvt_pk_bf16_f32 v199,v150,v151\nv_mul_f32 v68,v68,v236\nM v[144:147],a[28:31],a[140:143],v[144:147]\nbuffer_load_dwordx4 v240,s[44:47],s95 offen offset:1024 sc1 lds\nv_mul_f32 v69,v69,v236\nv_mul_f32 v70,v70,v236\nM v[148:151],a[28:31],a[156:159],v[228:231]\nv_mul_f32 v71,v71,v236\n" \
"v_cvt_pk_bf16_f32 v68,v68,v69\nv_mul_f32 v76,v76,v236\nv_mul_f32 v77,v77,v236\nM v[148:151],a[24:27],a[152:155],v[148:151]\nds_read_b128 a[80:83],v250 offset:4096\nds_read_b128 a[112:115],v250 offset:12288\nv_mul_f32 v78,v78,v236\nv_mul_f32 v79,v79,v236\n" \
"M v[148:151],a[20:23],a[148:151],v[148:151]\nv_cvt_pk_bf16_f32 v69,v70,v71\nv_cvt_pk_bf16_f32 v70,v76,v77\nv_cvt_pk_bf16_f32 v71,v78,v79\nv_mul_f32 v80,v80,v232\nM v[148:151],a[16:19],a[144:147],v[148:151]\nv_exp_f32 v164,v164\nv_exp_f32 v165,v165\n" \
"M v[160:163],a[32:35],a[128:131],v[224:227]\nds_read_b128 a[84:87],v250 offset:5120\nds_read_b128 a[116:119],v250 offset:13312\nv_exp_f32 v166,v166\nv_cvt_pk_bf16_f32 v212,v164,v165\nM v[160:163],a[36:39],a[132:135],v[160:163]\nbuffer_store_dwordx4 v[68:71],v240,s[52:55],s37 offen offset:1024 sc1\nv_exp_f32 v167,v167\n" \
"M v[160:163],a[40:43],a[136:139],v[160:163]\nv_cvt_pk_bf16_f32 v213,v166,v167\nv_mul_f32 v81,v81,v232\nv_mul_f32 v82,v82,v232\nv_mul_f32 v83,v83,v232\nM v[160:163],a[44:47],a[140:143],v[160:163]\nds_read_b128 a[88:91],v250 offset:6144\nds_read_b128 a[120:123],v250 offset:14336\n" \
"v_cvt_pk_bf16_f32 v80,v80,v81\nv_mul_f32 v88,v88,v232\nM v[164:167],a[44:47],a[156:159],v[228:231]\nbuffer_load_dwordx4 v240,s[44:47],s95 offen offset:2048 sc1 lds\nv_mul_f32 v89,v89,v232\nv_mul_f32 v90,v90,v232\nM v[164:167],a[40:43],a[152:155],v[164:167]\nv_mul_f32 v91,v91,v232\n" \
"v_cvt_pk_bf16_f32 v81,v82,v83\nv_cvt_pk_bf16_f32 v82,v88,v89\nv_cvt_pk_bf16_f32 v83,v90,v91\nv_mul_f32 v84,v84,v236\nM v[164:167],a[36:39],a[148:151],v[164:167]\nds_read_b128 a[92:95],v250 offset:7168\nds_read_b128 a[124:127],v250 offset:15360\nv_exp_f32 v180,v180\n" \
"M v[164:167],a[32:35],a[144:147],v[164:167]\nv_exp_f32 v181,v181\nv_mul_f32 v85,v85,v236\ns_waitcnt lgkmcnt(4)\nM v[176:179],a[48:51],a[128:131],v[224:227]\nv_exp_f32 v182,v182\nv_exp_f32 v183,v183\nM v[176:179],a[52:55],a[132:135],v[176:179]\n" \
"buffer_store_dwordx4 v[80:83],v240,s[52:55],s37 offen offset:2048 sc1\nv_cvt_pk_bf16_f32 v214,v180,v181\nv_cvt_pk_bf16_f32 v215,v182,v183\nM v[176:179],a[56:59],a[136:139],v[176:179]\nv_mul_f32 v86,v86,v236\nv_mul_f32 v87,v87,v236\nv_cvt_pk_bf16_f32 v84,v84,v85\nv_mul_f32 v92,v92,v236\n" \
"M v[176:179],a[60:63],a[140:143],v[176:179]\nv_mul_f32 v93,v93,v236\nv_mul_f32 v94,v94,v236\nv_mul_f32 v95,v95,v236\nv_cvt_pk_bf16_f32 v85,v86,v87\nv_cvt_pk_bf16_f32 v86,v92,v93\nM v[180:183],a[60:63],a[156:159],v[228:231]\nv_cvt_pk_bf16_f32 v87,v94,v95\n" \
"v_xor_b32 v250,0x18000,v250\nv_mul_f32 v96,v96,v232\nv_mul_f32 v97,v97,v232\nM v[180:183],a[56:59],a[152:155],v[180:183]\nbuffer_load_dwordx4 v240,s[44:47],s95 offen offset:3072 sc1 lds\ns_add_u32 m0,m0,0x10000\nv_mul_f32 v98,v98,v232\nM v[180:183],a[52:55],a[148:151],v[180:183]\n" \
"v_mul_f32 v99,v99,v232\nv_cvt_pk_bf16_f32 v96,v96,v97\nv_mul_f32 v104,v104,v232\nv_mul_f32 v105,v105,v232\nM v[180:183],a[48:51],a[144:147],v[180:183]\nv_mul_f32 v106,v106,v232\nv_mul_f32 v107,v107,v232\nv_cvt_pk_bf16_f32 v97,v98,v99\n" \
"s_add_u32 s38,s37,4096\nM v[0:3],a[64:67],v[192:195],v[0:3]\nv_cvt_pk_bf16_f32 v98,v104,v105\nv_cvt_pk_bf16_f32 v99,v106,v107\nv_mul_f32 v100,v100,v236\nv_mul_f32 v101,v101,v236\nv_mul_f32 v102,v102,v236\nM v[0:3],a[96:99],v[208:211],v[0:3]\n" \
"buffer_store_dwordx4 v[84:87],v240,s[52:55],s37 offen offset:3072 sc1\nv_mul_f32 v103,v103,v236\nv_cvt_pk_bf16_f32 v100,v100,v101\nM v[8:11],a[68:71],v[192:195],v[8:11]\ns_waitcnt vmcnt(16) lgkmcnt(0)\ns_barrier\nv_mul_f32 v108,v108,v236\nv_mul_f32 v109,v109,v236\n" \
"M v[8:11],a[100:103],v[208:211],v[8:11]\nds_read_b128 a[0:3],v250 offset:0\nds_read_b128 a[4:7],v250 offset:1024\nv_mul_f32 v110,v110,v236\nv_mul_f32 v111,v111,v236\nM v[16:19],a[72:75],v[192:195],v[16:19]\nv_cvt_pk_bf16_f32 v101,v102,v103\nv_cvt_pk_bf16_f32 v102,v108,v109\n" \
"v_cvt_pk_bf16_f32 v103,v110,v111\nv_mul_f32 v112,v112,v232\nM v[16:19],a[104:107],v[208:211],v[16:19]\nbuffer_load_dwordx4 v240,s[48:51],s95 offen offset:0 sc1 lds\nv_mul_f32 v113,v113,v232\nv_mul_f32 v114,v114,v232\nv_mul_f32 v115,v115,v232\nM v[24:27],a[76:79],v[192:195],v[24:27]\n" \
"v_cvt_pk_bf16_f32 v112,v112,v113\nv_mul_f32 v120,v120,v232\nv_mul_f32 v121,v121,v232\nv_mul_f32 v122,v122,v232\nM v[24:27],a[108:111],v[208:211],v[24:27]\nds_read_b128 a[8:11],v250 offset:2048\nds_read_b128 a[12:15],v250 offset:3072\nv_mul_f32 v123,v123,v232\n" \
"v_cvt_pk_bf16_f32 v113,v114,v115\nM v[32:35],a[80:83],v[192:195],v[32:35]\nv_cvt_pk_bf16_f32 v114,v120,v121\nv_cvt_pk_bf16_f32 v115,v122,v123\nv_mul_f32 v116,v116,v236\nv_mul_f32 v117,v117,v236\nM v[32:35],a[112:115],v[208:211],v[32:35]\nbuffer_store_dwordx4 v[96:99],v240,s[52:55],s38 offen offset:0 sc1\n" \
"v_mul_f32 v118,v118,v236\nv_mul_f32 v119,v119,v236\nM v[40:43],a[84:87],v[192:195],v[40:43]\nv_cvt_pk_bf16_f32 v116,v116,v117\nv_mul_f32 v124,v124,v236\nv_mul_f32 v125,v125,v236\nv_mul_f32 v126,v126,v236\nM v[40:43],a[116:119],v[208:211],v[40:43]\n" \
"ds_read_b128 a[16:19],v250 offset:4096\nds_read_b128 a[20:23],v250 offset:5120\nv_mul_f32 v127,v127,v236\nv_cvt_pk_bf16_f32 v117,v118,v119\nv_cvt_pk_bf16_f32 v118,v124,v125\nM v[48:51],a[88:91],v[192:195],v[48:51]\nv_cvt_pk_bf16_f32 v119,v126,v127\nv_exp_f32 v128,v128\n" \
"s_add_u32 s94,s94,0x4000\nM v[48:51],a[120:123],v[208:211],v[48:51]\nbuffer_load_dwordx4 v240,s[48:51],s95 offen offset:1024 sc1 lds\nv_exp_f32 v129,v129\nM v[56:59],a[92:95],v[192:195],v[56:59]\nv_exp_f32 v130,v130\nv_exp_f32 v131,v131\nM v[56:59],a[124:127],v[208:211],v[56:59]\n" \
"ds_read_b128 a[24:27],v250 offset:6144\nds_read_b128 a[28:31],v250 offset:7168\nv_exp_f32 v144,v144\nM a[192:195],a[208:211],v[192:195],a[192:195]\nv_exp_f32 v145,v145\nv_exp_f32 v146,v146\ns_lshl2_add_u32 s77,s61,s79\nM a[192:195],a[208:211],v[208:211],a[192:195]\n" \
"buffer_store_dwordx4 v[100:103],v240,s[52:55],s38 offen offset:1024 sc1\nv_exp_f32 v147,v147\nM v[4:7],a[64:67],v[196:199],v[4:7]\nv_exp_f32 v160,v160\nv_exp_f32 v161,v161\nM v[4:7],a[96:99],v[212:215],v[4:7]\nds_read_b128 a[32:35],v250 offset:8192\nds_read_b128 a[36:39],v250 offset:9216\n" \
"v_exp_f32 v162,v162\nM v[12:15],a[68:71],v[196:199],v[12:15]\nv_exp_f32 v163,v163\nv_exp_f32 v176,v176\nM v[12:15],a[100:103],v[212:215],v[12:15]\nbuffer_load_dwordx4 v240,s[48:51],s95 offen offset:2048 sc1 lds\nv_exp_f32 v177,v177\nM v[20:23],a[72:75],v[196:199],v[20:23]\n" \
"v_exp_f32 v178,v178\nv_exp_f32 v179,v179\ns_and_b32 s94,s94,0xffff\nM v[20:23],a[104:107],v[212:215],v[20:23]\nds_read_b128 a[40:43],v250 offset:10240\nds_read_b128 a[44:47],v250 offset:11264\ns_add_u32 s77,s77,4096\nv_mov_b32 v233,s71\n" \
"M v[28:31],a[76:79],v[196:199],v[28:31]\nv_exp_f32 v132,v132\nv_exp_f32 v133,v133\nM v[28:31],a[108:111],v[212:215],v[28:31]\nbuffer_store_dwordx4 v[112:115],v240,s[52:55],s38 offen offset:2048 sc1\nv_exp_f32 v134,v134\nM v[36:39],a[80:83],v[196:199],v[36:39]\nv_exp_f32 v135,v135\n" \
"v_exp_f32 v148,v148\nM v[36:39],a[112:115],v[212:215],v[36:39]\nds_read_b128 a[48:51],v250 offset:12288\nds_read_b128 a[52:55],v250 offset:13312\nv_exp_f32 v149,v149\nv_cvt_pk_bf16_f32 v192,v128,v129\nM v[44:47],a[84:87],v[196:199],v[44:47]\nv_exp_f32 v150,v150\n" \
"v_exp_f32 v151,v151\nM v[44:47],a[116:119],v[212:215],v[44:47]\nbuffer_load_dwordx4 v240,s[48:51],s95 offen offset:3072 sc1 lds\nv_cvt_pk_bf16_f32 v193,v130,v131\nv_cvt_pk_bf16_f32 v194,v144,v145\nM v[52:55],a[88:91],v[196:199],v[52:55]\nv_cvt_pk_bf16_f32 v195,v146,v147\nv_cvt_pk_bf16_f32 v208,v160,v161\n" \
"v_cvt_pk_bf16_f32 v209,v162,v163\nv_cvt_pk_bf16_f32 v210,v176,v177\nM v[52:55],a[120:123],v[212:215],v[52:55]\nds_read_b128 a[56:59],v250 offset:14336\nds_read_b128 a[60:63],v250 offset:15360\nv_cvt_pk_bf16_f32 v211,v178,v179\nv_add_u32 v250,s94,v241\nM v[60:63],a[92:95],v[196:199],v[60:63]\n" \
"s_add_u32 s95,s95,0x4000\nM v[60:63],a[124:127],v[212:215],v[60:63]\nbuffer_store_dwordx4 v[116:119],v240,s[52:55],s38 offen offset:3072 sc1\nM a[196:199],a[208:211],v[196:199],a[196:199]\nM a[196:199],a[208:211],v[212:215],a[196:199]\ns_cmp_ge_u32 s95,s15\ns_cbranch_scc1 .Lsx\n.Lsingle:\n" \
"s_waitcnt lgkmcnt(4)\nM v[128:131],a[0:3],a[128:131],v[224:227]\ns_add_u32 m0,s94,s64\nv_cvt_pk_bf16_f32 v196,v132,v133\nM v[128:131],a[4:7],a[132:135],v[128:131]\nds_read_b128 a[64:67],v250 offset:0\nds_read_b128 a[96:99],v250 offset:8192\nM v[128:131],a[8:11],a[136:139],v[128:131]\n" \
"buffer_load_dwordx4 v240,s[44:47],s95 offen offset:0 sc1 lds\nM v[128:131],a[12:15],a[140:143],v[128:131]\nv_cvt_pk_bf16_f32 v197,v134,v135\nv_cvt_pk_bf16_f32 v198,v148,v149\nM v[132:135],a[12:15],a[156:159],v[228:231]\nds_read_b128 a[68:71],v250 offset:1024\nds_read_b128 a[100:103],v250 offset:9216\nM v[132:135],a[8:11],a[152:155],v[132:135]\n" \
"v_cvt_pk_bf16_f32 v199,v150,v151\ns_add_u32 s94,s94,0x4000\nM v[132:135],a[4:7],a[148:151],v[132:135]\nv_exp_f32 v164,v164\nM v[132:135],a[0:3],a[144:147],v[132:135]\nds_read_b128 a[72:75],v250 offset:2048\nds_read_b128 a[104:107],v250 offset:10240\nM v[144:147],a[16:19],a[128:131],v[224:227]\n" \
"v_exp_f32 v165,v165\nM v[144:147],a[20:23],a[132:135],v[144:147]\nv_exp_f32 v166,v166\nM v[144:147],a[24:27],a[136:139],v[144:147]\nds_read_b128 a[76:79],v250 offset:3072\nds_read_b128 a[108:111],v250 offset:11264\nM v[144:147],a[28:31],a[140:143],v[144:147]\nbuffer_load_dwordx4 v240,s[44:47],s95 offen offset:1024 sc1 lds\n" \
"M v[148:151],a[28:31],a[156:159],v[228:231]\nv_exp_f32 v167,v167\nM v[148:151],a[24:27],a[152:155],v[148:151]\nds_read_b128 a[80:83],v250 offset:4096\nds_read_b128 a[112:115],v250 offset:12288\nM v[148:151],a[20:23],a[148:151],v[148:151]\nv_cvt_pk_bf16_f32 v212,v164,v165\nv_cvt_pk_bf16_f32 v213,v166,v167\n" \
"M v[148:151],a[16:19],a[144:147],v[148:151]\nv_exp_f32 v180,v180\nM v[160:163],a[32:35],a[128:131],v[224:227]\nds_read_b128 a[84:87],v250 offset:5120\nds_read_b128 a[116:119],v250 offset:13312\nM v[160:163],a[36:39],a[132:135],v[160:163]\nv_exp_f32 v181,v181\nM v[160:163],a[40:43],a[136:139],v[160:163]\n" \
"v_exp_f32 v182,v182\nM v[160:163],a[44:47],a[140:143],v[160:163]\nds_read_b128 a[88:91],v250 offset:6144\nds_read_b128 a[120:123],v250 offset:14336\nM v[164:167],a[44:47],a[156:159],v[228:231]\nbuffer_load_dwordx4 v240,s[44:47],s95 offen offset:2048 sc1 lds\nM v[164:167],a[40:43],a[152:155],v[164:167]\nv_exp_f32 v183,v183\n" \
"M v[164:167],a[36:39],a[148:151],v[164:167]\nds_read_b128 a[92:95],v250 offset:7168\nds_read_b128 a[124:127],v250 offset:15360\nM v[164:167],a[32:35],a[144:147],v[164:167]\nv_cvt_pk_bf16_f32 v214,v180,v181\ns_waitcnt lgkmcnt(7)\nM v[176:179],a[48:51],a[128:131],v[224:227]\nv_cvt_pk_bf16_f32 v215,v182,v183\n" \
"v_xor_b32 v250,0x18000,v250\nM v[176:179],a[52:55],a[132:135],v[176:179]\nv_exp_f32 v128,v128\nM v[176:179],a[56:59],a[136:139],v[176:179]\nv_exp_f32 v129,v129\nM v[176:179],a[60:63],a[140:143],v[176:179]\nv_exp_f32 v130,v130\nM v[180:183],a[60:63],a[156:159],v[228:231]\n" \
"v_exp_f32 v131,v131\nM v[180:183],a[56:59],a[152:155],v[180:183]\nbuffer_load_dwordx4 v240,s[44:47],s95 offen offset:3072 sc1 lds\nM v[180:183],a[52:55],a[148:151],v[180:183]\ns_add_u32 m0,m0,0x10000\ns_and_b32 s94,s94,0xffff\nM v[180:183],a[48:51],a[144:147],v[180:183]\nv_exp_f32 v144,v144\n" \
"M v[0:3],a[64:67],v[192:195],v[0:3]\nv_exp_f32 v145,v145\nM v[0:3],a[96:99],v[208:211],v[0:3]\nv_exp_f32 v146,v146\nM v[8:11],a[68:71],v[192:195],v[8:11]\ns_waitcnt vmcnt(16) lgkmcnt(0)\ns_barrier\nM v[8:11],a[100:103],v[208:211],v[8:11]\n" \
"ds_read_b128 a[0:3],v250 offset:0\nds_read_b128 a[4:7],v250 offset:1024\nM v[16:19],a[72:75],v[192:195],v[16:19]\nv_exp_f32 v147,v147\nM v[16:19],a[104:107],v[208:211],v[16:19]\nbuffer_load_dwordx4 v240,s[48:51],s95 offen offset:0 sc1 lds\nM v[24:27],a[76:79],v[192:195],v[24:27]\nv_exp_f32 v160,v160\n" \
"M v[24:27],a[108:111],v[208:211],v[24:27]\nds_read_b128 a[8:11],v250 offset:2048\nds_read_b128 a[12:15],v250 offset:3072\nM v[32:35],a[80:83],v[192:195],v[32:35]\nv_exp_f32 v161,v161\nM v[32:35],a[112:115],v[208:211],v[32:35]\nv_exp_f32 v162,v162\nM v[40:43],a[84:87],v[192:195],v[40:43]\n" \
"v_exp_f32 v163,v163\nM v[40:43],a[116:119],v[208:211],v[40:43]\nds_read_b128 a[16:19],v250 offset:4096\nds_read_b128 a[20:23],v250 offset:5120\nM v[48:51],a[88:91],v[192:195],v[48:51]\nv_exp_f32 v176,v176\nM v[48:51],a[120:123],v[208:211],v[48:51]\nbuffer_load_dwordx4 v240,s[48:51],s95 offen offset:1024 sc1 lds\n" \
"M v[56:59],a[92:95],v[192:195],v[56:59]\nv_exp_f32 v177,v177\nM v[56:59],a[124:127],v[208:211],v[56:59]\nds_read_b128 a[24:27],v250 offset:6144\nds_read_b128 a[28:31],v250 offset:7168\nM a[192:195],a[208:211],v[192:195],a[192:195]\nv_exp_f32 v178,v178\nM a[192:195],a[208:211],v[208:211],a[192:195]\n" \
"v_exp_f32 v179,v179\nM v[4:7],a[64:67],v[196:199],v[4:7]\nv_exp_f32 v132,v132\nM v[4:7],a[96:99],v[212:215],v[4:7]\nds_read_b128 a[32:35],v250 offset:8192\nds_read_b128 a[36:39],v250 offset:9216\nM v[12:15],a[68:71],v[196:199],v[12:15]\nv_exp_f32 v133,v133\n" \
"M v[12:15],a[100:103],v[212:215],v[12:15]\nbuffer_load_dwordx4 v240,s[48:51],s95 offen offset:2048 sc1 lds\nM v[20:23],a[72:75],v[196:199],v[20:23]\nv_exp_f32 v134,v134\nM v[20:23],a[104:107],v[212:215],v[20:23]\nds_read_b128 a[40:43],v250 offset:10240\nds_read_b128 a[44:47],v250 offset:11264\nM v[28:31],a[76:79],v[196:199],v[28:31]\n" \
"v_exp_f32 v135,v135\nM v[28:31],a[108:111],v[212:215],v[28:31]\nv_exp_f32 v148,v148\nM v[36:39],a[80:83],v[196:199],v[36:39]\nv_exp_f32 v149,v149\nM v[36:39],a[112:115],v[212:215],v[36:39]\nds_read_b128 a[48:51],v250 offset:12288\nds_read_b128 a[52:55],v250 offset:13312\n" \
"M v[44:47],a[84:87],v[196:199],v[44:47]\nv_exp_f32 v150,v150\nM v[44:47],a[116:119],v[212:215],v[44:47]\nbuffer_load_dwordx4 v240,s[48:51],s95 offen offset:3072 sc1 lds\nM v[52:55],a[88:91],v[196:199],v[52:55]\nv_exp_f32 v151,v151\nM v[52:55],a[120:123],v[212:215],v[52:55]\nds_read_b128 a[56:59],v250 offset:14336\n" \
"ds_read_b128 a[60:63],v250 offset:15360\nM v[60:63],a[92:95],v[196:199],v[60:63]\nv_cvt_pk_bf16_f32 v192,v128,v129\nv_cvt_pk_bf16_f32 v193,v130,v131\nM v[60:63],a[124:127],v[212:215],v[60:63]\nv_cvt_pk_bf16_f32 v194,v144,v145\nv_cvt_pk_bf16_f32 v195,v146,v147\nM a[196:199],a[208:211],v[196:199],a[196:199]\n" \
"v_cvt_pk_bf16_f32 v208,v160,v161\nv_cvt_pk_bf16_f32 v209,v162,v163\nv_cvt_pk_bf16_f32 v210,v176,v177\nv_cvt_pk_bf16_f32 v211,v178,v179\nv_add_u32 v250,s94,v241\ns_add_u32 s95,s95,0x4000\nM a[196:199],a[208:211],v[212:215],a[196:199]\ns_cmp_lg_u32 s95,s15\n" \
"s_cbranch_scc1 .Lsingle\n.Lsx:\n.Lsw:\ns_waitcnt lgkmcnt(4)\nM v[128:131],a[0:3],a[128:131],v[224:227]\ns_lshl4_add_u32 s69,s12,s68\ns_add_u32 m0,s64,131072\nM v[128:131],a[4:7],a[132:135],v[128:131]\n" \
"ds_read_b128 a[64:67],v250 offset:0\nds_read_b128 a[96:99],v250 offset:8192\nv_cvt_pk_bf16_f32 v196,v132,v133\nM v[128:131],a[8:11],a[136:139],v[128:131]\nbuffer_load_dwordx4 v243,s[40:43],s69 offen offset:0 lds\nM v[128:131],a[12:15],a[140:143],v[128:131]\nv_cvt_pk_bf16_f32 v197,v134,v135\nv_cvt_pk_bf16_f32 v198,v148,v149\n" \
"v_cvt_pk_bf16_f32 v199,v150,v151\nM v[132:135],a[12:15],a[156:159],v[228:231]\nds_read_b128 a[68:71],v250 offset:1024\nds_read_b128 a[100:103],v250 offset:9216\nM v[132:135],a[8:11],a[152:155],v[132:135]\nbuffer_load_dwordx4 v244,s[40:43],s69 offen offset:1024 lds\ns_add_u32 s94,s94,0x4000\nM v[132:135],a[4:7],a[148:151],v[132:135]\n" \
"v_exp_f32 v164,v164\nM v[132:135],a[0:3],a[144:147],v[132:135]\nds_read_b128 a[72:75],v250 offset:2048\nds_read_b128 a[104:107],v250 offset:10240\ns_and_b32 s94,s94,0xffff\nM v[144:147],a[16:19],a[128:131],v[224:227]\nbuffer_load_dwordx4 v245,s[40:43],s69 offen offset:2048 lds\nM v[144:147],a[20:23],a[132:135],v[144:147]\n" \
"v_exp_f32 v165,v165\ns_add_u32 s95,s95,0x4000\nM v[144:147],a[24:27],a[136:139],v[144:147]\nds_read_b128 a[76:79],v250 offset:3072\nds_read_b128 a[108:111],v250 offset:11264\nM v[144:147],a[28:31],a[140:143],v[144:147]\nbuffer_load_dwordx4 v246,s[40:43],s69 offen offset:3072 lds\ns_lshl4_add_u32 s69,s12,s68\n" \
"M v[148:151],a[28:31],a[156:159],v[228:231]\ns_add_u32 s69,s69,0x10000\ns_add_u32 m0,s64,147456\nM v[148:151],a[24:27],a[152:155],v[148:151]\nds_read_b128 a[80:83],v250 offset:4096\nds_read_b128 a[112:115],v250 offset:12288\nv_cvt_pk_bf16_f32 v212,v164,v165\nM v[148:151],a[20:23],a[148:151],v[148:151]\n" \
"v_exp_f32 v166,v166\nM v[148:151],a[16:19],a[144:147],v[148:151]\nv_exp_f32 v167,v167\nM v[160:163],a[32:35],a[128:131],v[224:227]\nds_read_b128 a[84:87],v250 offset:5120\nds_read_b128 a[116:119],v250 offset:13312\nv_cvt_pk_bf16_f32 v213,v166,v167\nM v[160:163],a[36:39],a[132:135],v[160:163]\n" \
"buffer_load_dwordx4 v243,s[40:43],s69 offen offset:0 lds\nM v[160:163],a[40:43],a[136:139],v[160:163]\nv_exp_f32 v180,v180\nM v[160:163],a[44:47],a[140:143],v[160:163]\nds_read_b128 a[88:91],v250 offset:6144\nds_read_b128 a[120:123],v250 offset:14336\nM v[164:167],a[44:47],a[156:159],v[228:231]\nv_exp_f32 v181,v181\n" \
"M v[164:167],a[40:43],a[152:155],v[164:167]\nbuffer_load_dwordx4 v244,s[40:43],s69 offen offset:1024 lds\nM v[164:167],a[36:39],a[148:151],v[164:167]\nds_read_b128 a[92:95],v250 offset:7168\nds_read_b128 a[124:127],v250 offset:15360\nv_cvt_pk_bf16_f32 v214,v180,v181\nM v[164:167],a[32:35],a[144:147],v[164:167]\nv_xor_b32 v250,0x18000,v250\n" \
"s_waitcnt lgkmcnt(6)\nM v[176:179],a[48:51],a[128:131],v[224:227]\ns_waitcnt vmcnt(6)\nbuffer_store_dword v233,off,s[52:55],s77 sc1\nM v[176:179],a[52:55],a[132:135],v[176:179]\nbuffer_load_dwordx4 v245,s[40:43],s69 offen offset:2048 lds\nM v[176:179],a[56:59],a[136:139],v[176:179]\nv_exp_f32 v182,v182\n" \
"v_exp_f32 v183,v183\nM v[176:179],a[60:63],a[140:143],v[176:179]\nv_cvt_pk_bf16_f32 v215,v182,v183\nM v[180:183],a[60:63],a[156:159],v[228:231]\nv_exp_f32 v128,v128\nM v[180:183],a[56:59],a[152:155],v[180:183]\nbuffer_load_dwordx4 v246,s[40:43],s69 offen offset:3072 lds\nM v[180:183],a[52:55],a[148:151],v[180:183]\n" \
"v_exp_f32 v129,v129\nM v[180:183],a[48:51],a[144:147],v[180:183]\ns_lshl4_add_u32 s69,s10,s68\nv_exp_f32 v130,v130\nM v[0:3],a[64:67],v[192:195],v[0:3]\nv_exp_f32 v131,v131\nM v[0:3],a[96:99],v[208:211],v[0:3]\ns_add_u32 m0,s64,0\n" \
"v_exp_f32 v144,v144\nM v[8:11],a[68:71],v[192:195],v[8:11]\ns_waitcnt vmcnt(16) lgkmcnt(0)\ns_barrier\nM v[8:11],a[100:103],v[208:211],v[8:11]\nds_read_b128 a[0:3],v250 offset:0\nds_read_b128 a[4:7],v250 offset:1024\nM v[16:19],a[72:75],v[192:195],v[16:19]\n" \
"v_exp_f32 v145,v145\nM v[16:19],a[104:107],v[208:211],v[16:19]\nbuffer_load_dwordx4 v243,s[40:43],s69 offen offset:0 lds\nM v[24:27],a[76:79],v[192:195],v[24:27]\nv_exp_f32 v146,v146\nM v[24:27],a[108:111],v[208:211],v[24:27]\nds_read_b128 a[8:11],v250 offset:2048\nds_read_b128 a[12:15],v250 offset:3072\n" \
"M v[32:35],a[80:83],v[192:195],v[32:35]\nv_exp_f32 v147,v147\nM v[32:35],a[112:115],v[208:211],v[32:35]\nbuffer_load_dwordx4 v244,s[40:43],s69 offen offset:1024 lds\nM v[40:43],a[84:87],v[192:195],v[40:43]\nv_exp_f32 v160,v160\nM v[40:43],a[116:119],v[208:211],v[40:43]\nds_read_b128 a[16:19],v250 offset:4096\n" \
"ds_read_b128 a[20:23],v250 offset:5120\nM v[48:51],a[88:91],v[192:195],v[48:51]\nv_exp_f32 v161,v161\nM v[48:51],a[120:123],v[208:211],v[48:51]\nbuffer_load_dwordx4 v245,s[40:43],s69 offen offset:2048 lds\nM v[56:59],a[92:95],v[192:195],v[56:59]\nv_exp_f32 v162,v162\nM v[56:59],a[124:127],v[208:211],v[56:59]\n" \
"ds_read_b128 a[24:27],v250 offset:6144\nds_read_b128 a[28:31],v250 offset:7168\nM a[192:195],a[208:211],v[192:195],a[192:195]\nv_exp_f32 v163,v163\nv_cvt_pk_bf16_f32 v192,v128,v129\nM a[192:195],a[208:211],v[208:211],a[192:195]\nbuffer_load_dwordx4 v246,s[40:43],s69 offen offset:3072 lds\nM v[4:7],a[64:67],v[196:199],v[4:7]\n" \
"s_lshl4_add_u32 s69,s10,s68\ns_add_u32 s69,s69,0x10000\ns_add_u32 m0,s64,32768\nM v[4:7],a[96:99],v[212:215],v[4:7]\nds_read_b128 a[32:35],v250 offset:8192\nds_read_b128 a[36:39],v250 offset:9216\nM v[12:15],a[68:71],v[196:199],v[12:15]\nv_exp_f32 v176,v176\n" \
"v_cvt_pk_bf16_f32 v193,v130,v131\nM v[12:15],a[100:103],v[212:215],v[12:15]\nbuffer_load_dwordx4 v243,s[40:43],s69 offen offset:0 lds\nM v[20:23],a[72:75],v[196:199],v[20:23]\nv_exp_f32 v177,v177\nv_cvt_pk_bf16_f32 v194,v144,v145\nM v[20:23],a[104:107],v[212:215],v[20:23]\nds_read_b128 a[40:43],v250 offset:10240\n" \
"ds_read_b128 a[44:47],v250 offset:11264\nM v[28:31],a[76:79],v[196:199],v[28:31]\nv_exp_f32 v178,v178\nv_cvt_pk_bf16_f32 v195,v146,v147\nM v[28:31],a[108:111],v[212:215],v[28:31]\nbuffer_load_dwordx4 v244,s[40:43],s69 offen offset:1024 lds\nM v[36:39],a[80:83],v[196:199],v[36:39]\nv_exp_f32 v179,v179\n" \
"v_cvt_pk_bf16_f32 v208,v160,v161\nM v[36:39],a[112:115],v[212:215],v[36:39]\nds_read_b128 a[48:51],v250 offset:12288\nds_read_b128 a[52:55],v250 offset:13312\nM v[44:47],a[84:87],v[196:199],v[44:47]\nv_exp_f32 v132,v132\nv_cvt_pk_bf16_f32 v209,v162,v163\nM v[44:47],a[116:119],v[212:215],v[44:47]\n" \
"buffer_load_dwordx4 v245,s[40:43],s69 offen offset:2048 lds\nv_cvt_pk_bf16_f32 v210,v176,v177\nM v[52:55],a[88:91],v[196:199],v[52:55]\nv_exp_f32 v133,v133\nM v[52:55],a[120:123],v[212:215],v[52:55]\nds_read_b128 a[56:59],v250 offset:14336\nds_read_b128 a[60:63],v250 offset:15360\nv_cvt_pk_bf16_f32 v211,v178,v179\n" \
"M v[60:63],a[92:95],v[196:199],v[60:63]\nv_exp_f32 v134,v134\nM v[60:63],a[124:127],v[212:215],v[60:63]\nbuffer_load_dwordx4 v246,s[40:43],s69 offen offset:3072 lds\nv_add_u32 v250,s94,v241\nM a[196:199],a[208:211],v[196:199],a[196:199]\nv_exp_f32 v135,v135\nM a[196:199],a[208:211],v[212:215],a[196:199]\n" \
"v_exp_f32 v148,v148\nv_exp_f32 v149,v149\nv_exp_f32 v150,v150\nv_exp_f32 v151,v151\n.Lod:\ns_waitcnt lgkmcnt(2)\nM v[128:131],a[0:3],a[128:131],v[224:227]\nv_cvt_pk_bf16_f32 v196,v132,v133\n" \
"v_cvt_pk_bf16_f32 v197,v134,v135\nM v[128:131],a[4:7],a[132:135],v[128:131]\nds_read_b128 a[64:67],v250 offset:0\nds_read_b128 a[96:99],v250 offset:8192\nM v[128:131],a[8:11],a[136:139],v[128:131]\ns_lshl2_add_u32 s39,s62,s7\ns_lshl_b32 s39,s39,5\nM v[128:131],a[12:15],a[140:143],v[128:131]\n" \
"s_add_u32 s39,s39,s63\nv_cvt_pk_bf16_f32 v198,v148,v149\nv_cvt_pk_bf16_f32 v199,v150,v151\nM v[132:135],a[12:15],a[156:159],v[228:231]\nds_read_b128 a[68:71],v250 offset:1024\nds_read_b128 a[100:103],v250 offset:9216\nM v[132:135],a[8:11],a[152:155],v[132:135]\ns_lshl2_add_u32 s39,s39,73728\n" \
"v_and_b32 v251,3,v249\nM v[132:135],a[4:7],a[148:151],v[132:135]\ns_waitcnt vmcnt(16)\nbuffer_store_dword v252,off,s[52:55],s39 sc1\nM v[132:135],a[0:3],a[144:147],v[132:135]\nds_read_b128 a[72:75],v250 offset:2048\nds_read_b128 a[104:107],v250 offset:10240\nv_lshlrev_b32 v251,2,v251\n" \
"M v[144:147],a[16:19],a[128:131],v[224:227]\nv_exp_f32 v164,v164\nM v[144:147],a[20:23],a[132:135],v[144:147]\nv_exp_f32 v165,v165\nM v[144:147],a[24:27],a[136:139],v[144:147]\nds_read_b128 a[76:79],v250 offset:3072\nds_read_b128 a[108:111],v250 offset:11264\nM v[144:147],a[28:31],a[140:143],v[144:147]\n" \
"v_exp_f32 v166,v166\nv_cvt_pk_bf16_f32 v212,v164,v165\nM v[148:151],a[28:31],a[156:159],v[228:231]\nv_exp_f32 v167,v167\nM v[148:151],a[24:27],a[152:155],v[148:151]\nds_read_b128 a[80:83],v250 offset:4096\nds_read_b128 a[112:115],v250 offset:12288\nM v[148:151],a[20:23],a[148:151],v[148:151]\n" \
"v_cvt_pk_bf16_f32 v213,v166,v167\ns_add_u32 s80,s79,4096\nM v[148:151],a[16:19],a[144:147],v[148:151]\nv_exp_f32 v180,v180\ns_add_u32 s94,s94,0x4000\nM v[160:163],a[32:35],a[128:131],v[224:227]\nds_read_b128 a[84:87],v250 offset:5120\nds_read_b128 a[116:119],v250 offset:13312\n" \
"M v[160:163],a[36:39],a[132:135],v[160:163]\nv_exp_f32 v181,v181\nM v[160:163],a[40:43],a[136:139],v[160:163]\nv_exp_f32 v182,v182\nM v[160:163],a[44:47],a[140:143],v[160:163]\nds_read_b128 a[88:91],v250 offset:6144\nds_read_b128 a[120:123],v250 offset:14336\nv_cvt_pk_bf16_f32 v214,v180,v181\n" \
"M v[164:167],a[44:47],a[156:159],v[228:231]\nbuffer_load_dword v248,v251,s[52:55],s80 offen sc1\nM v[164:167],a[40:43],a[152:155],v[164:167]\nv_exp_f32 v183,v183\nM v[164:167],a[36:39],a[148:151],v[164:167]\nds_read_b128 a[92:95],v250 offset:7168\nds_read_b128 a[124:127],v250 offset:15360\nM v[164:167],a[32:35],a[144:147],v[164:167]\n" \
"v_cvt_pk_bf16_f32 v215,v182,v183\ns_and_b32 s94,s94,0xffff\nv_add_u32 v250,s94,v241\nM v[176:179],a[48:51],a[128:131],v[224:227]\nv_exp_f32 v128,v128\nM v[176:179],a[52:55],a[132:135],v[176:179]\ns_add_u32 s65,s10,s67\ns_waitcnt lgkmcnt(4)\n" \
"M v[176:179],a[56:59],a[136:139],v[176:179]\nv_exp_f32 v129,v129\nM v[176:179],a[60:63],a[140:143],v[176:179]\nv_exp_f32 v130,v130\ns_add_u32 s65,s65,1048576\nM v[180:183],a[60:63],a[156:159],v[228:231]\nv_exp_f32 v131,v131\nM v[180:183],a[56:59],a[152:155],v[180:183]\n" \
"v_exp_f32 v144,v144\nM v[180:183],a[52:55],a[148:151],v[180:183]\nv_exp_f32 v145,v145\nM v[180:183],a[48:51],a[144:147],v[180:183]\nv_exp_f32 v146,v146\nv_add_u32 v226,131072,v242\nM v[0:3],a[64:67],v[192:195],v[0:3]\nv_exp_f32 v147,v147\n" \
"M v[0:3],a[96:99],v[208:211],v[0:3]\nv_exp_f32 v160,v160\nM v[8:11],a[68:71],v[192:195],v[8:11]\ns_waitcnt lgkmcnt(0)\ns_barrier\nM v[8:11],a[100:103],v[208:211],v[8:11]\ns_lshl4_add_u32 s69,s11,s68\ns_add_u32 m0,s64,16384\n" \
"v_xor_b32 v227,32,v226\nM v[16:19],a[72:75],v[192:195],v[16:19]\nv_exp_f32 v161,v161\nM v[16:19],a[104:107],v[208:211],v[16:19]\nv_exp_f32 v162,v162\nM v[24:27],a[76:79],v[192:195],v[24:27]\nv_exp_f32 v163,v163\nM v[24:27],a[108:111],v[208:211],v[24:27]\n" \
"v_exp_f32 v176,v176\ns_add_u32 s37,s12,s67\nM v[32:35],a[80:83],v[192:195],v[32:35]\nbuffer_load_dwordx4 v243,s[40:43],s69 offen offset:0 lds\nM v[32:35],a[112:115],v[208:211],v[32:35]\nv_exp_f32 v177,v177\nM v[40:43],a[84:87],v[192:195],v[40:43]\nv_exp_f32 v178,v178\n" \
"M v[40:43],a[116:119],v[208:211],v[40:43]\nv_exp_f32 v179,v179\ns_add_u32 s38,s37,4096\nM v[48:51],a[88:91],v[192:195],v[48:51]\nv_exp_f32 v132,v132\nM v[48:51],a[120:123],v[208:211],v[48:51]\nv_exp_f32 v133,v133\nM v[56:59],a[92:95],v[192:195],v[56:59]\n" \
"v_exp_f32 v134,v134\nM v[56:59],a[124:127],v[208:211],v[56:59]\nv_exp_f32 v135,v135\ns_lshl2_add_u32 s76,s61,s79\nM a[192:195],a[208:211],v[192:195],a[192:195]\nbuffer_load_dwordx4 v244,s[40:43],s69 offen offset:1024 lds\nM a[192:195],a[208:211],v[208:211],a[192:195]\nv_cvt_pk_bf16_f32 v192,v128,v129\n" \
"v_cvt_pk_bf16_f32 v193,v130,v131\nM v[4:7],a[64:67],v[196:199],v[4:7]\nds_read_b128 a[64:67],v250 offset:0\nv_cvt_pk_bf16_f32 v194,v144,v145\nM v[4:7],a[96:99],v[212:215],v[4:7]\nds_read_b128 a[96:99],v250 offset:8192\nv_cvt_pk_bf16_f32 v195,v146,v147\nv_cvt_pk_bf16_f32 v208,v160,v161\n" \
"M v[12:15],a[68:71],v[196:199],v[12:15]\nds_read_b128 a[68:71],v250 offset:1024\nv_cvt_pk_bf16_f32 v209,v162,v163\nM v[12:15],a[100:103],v[212:215],v[12:15]\nds_read_b128 a[100:103],v250 offset:9216\nv_cvt_pk_bf16_f32 v210,v176,v177\nM v[20:23],a[72:75],v[196:199],v[20:23]\nds_read_b128 a[72:75],v250 offset:2048\n" \
"v_cvt_pk_bf16_f32 v211,v178,v179\nM v[20:23],a[104:107],v[212:215],v[20:23]\nds_read_b128 a[104:107],v250 offset:10240\nv_exp_f32 v148,v148\nM v[28:31],a[76:79],v[196:199],v[28:31]\nbuffer_load_dwordx4 v245,s[40:43],s69 offen offset:2048 lds\nM v[28:31],a[108:111],v[212:215],v[28:31]\nds_read_b128 a[76:79],v250 offset:3072\n" \
"ds_read_b128 a[108:111],v250 offset:11264\nM v[36:39],a[80:83],v[196:199],v[36:39]\nds_read_b128 a[80:83],v250 offset:4096\nv_lshlrev_b32 v238,1,v240\nM v[36:39],a[112:115],v[212:215],v[36:39]\nds_read_b128 a[112:115],v250 offset:12288\nv_exp_f32 v149,v149\nM v[44:47],a[84:87],v[196:199],v[44:47]\n" \
"ds_read_b128 a[84:87],v250 offset:5120\nv_and_b32 v254,32,v249\nM v[44:47],a[116:119],v[212:215],v[44:47]\nds_read_b128 a[116:119],v250 offset:13312\ns_lshr_b32 s70,s60,1\nM v[52:55],a[88:91],v[196:199],v[52:55]\nds_read_b128 a[88:91],v250 offset:6144\nv_lshlrev_b32 v254,3,v254\n" \
"M v[52:55],a[120:123],v[212:215],v[52:55]\nds_read_b128 a[120:123],v250 offset:14336\nv_exp_f32 v150,v150\nM v[60:63],a[92:95],v[196:199],v[60:63]\nbuffer_load_dwordx4 v246,s[40:43],s69 offen offset:3072 lds\nM v[60:63],a[124:127],v[212:215],v[60:63]\ns_waitcnt vmcnt(4)\nv_and_b32 v248,s17,v248\n" \
"M a[196:199],a[208:211],v[196:199],a[196:199]\nv_cmp_eq_u32 vcc,0,v248\nds_read_b128 a[92:95],v250 offset:7168\nM a[196:199],a[208:211],v[212:215],a[196:199]\ns_cbranch_vccnz .Lps6\n.Lpb6:\ns_lshl4_add_u32 s69,s11,s68\ns_waitcnt lgkmcnt(4)\n" \
"M v[0:3],a[64:67],v[192:195],v[0:3]\nbuffer_load_dwordx4 v[72:75],v240,s[52:55],s65 offen offset:0 sc1\nM v[0:3],a[96:99],v[208:211],v[0:3]\ns_add_u32 s69,s69,0x10000\ns_add_u32 m0,s64,49152\nM v[8:11],a[68:71],v[192:195],v[8:11]\nds_read_b128 a[124:127],v250 offset:15360\nv_cvt_pk_bf16_f32 v196,v132,v133\n" \
"M v[8:11],a[100:103],v[208:211],v[8:11]\nv_exp_f32 v151,v151\nv_cvt_pk_bf16_f32 v197,v134,v135\nM v[16:19],a[72:75],v[192:195],v[16:19]\nbuffer_load_dwordx4 v243,s[40:43],s69 offen offset:0 lds\nM v[16:19],a[104:107],v[208:211],v[16:19]\nv_exp_f32 v164,v164\nM v[24:27],a[76:79],v[192:195],v[24:27]\n" \
"v_exp_f32 v165,v165\nM v[24:27],a[108:111],v[208:211],v[24:27]\nv_exp_f32 v166,v166\nv_cvt_pk_bf16_f32 v198,v148,v149\nM v[32:35],a[80:83],v[192:195],v[32:35]\nbuffer_load_dwordx4 v[76:79],v240,s[52:55],s65 offen offset:1024 sc1\nM v[32:35],a[112:115],v[208:211],v[32:35]\nv_exp_f32 v167,v167\n" \
"M v[40:43],a[84:87],v[192:195],v[40:43]\nv_cvt_pk_bf16_f32 v199,v150,v151\ns_waitcnt lgkmcnt(0)\nM v[40:43],a[116:119],v[208:211],v[40:43]\nv_exp_f32 v180,v180\nv_cvt_pk_bf16_f32 v212,v164,v165\nM v[48:51],a[88:91],v[192:195],v[48:51]\nbuffer_load_dwordx4 v244,s[40:43],s69 offen offset:1024 lds\n" \
"M v[48:51],a[120:123],v[208:211],v[48:51]\nv_exp_f32 v181,v181\nM v[56:59],a[92:95],v[192:195],v[56:59]\nv_exp_f32 v182,v182\nM v[56:59],a[124:127],v[208:211],v[56:59]\nv_exp_f32 v183,v183\nv_cvt_pk_bf16_f32 v213,v166,v167\nM a[192:195],a[208:211],v[192:195],a[192:195]\n" \
"buffer_load_dwordx4 v[88:91],v240,s[52:55],s65 offen offset:2048 sc1\nM a[192:195],a[208:211],v[208:211],a[192:195]\nv_cvt_pk_bf16_f32 v214,v180,v181\nv_cvt_pk_bf16_f32 v215,v182,v183\nM v[4:7],a[64:67],v[196:199],v[4:7]\ns_barrier\nds_read_b64_tr_b16 v[200:201],v226 offset:0\nM v[4:7],a[96:99],v[212:215],v[4:7]\n" \
"ds_read_b64_tr_b16 v[202:203],v226 offset:4096\nds_read_b64_tr_b16 v[204:205],v226 offset:8192\nds_read_b64_tr_b16 v[206:207],v226 offset:12288\nM v[12:15],a[68:71],v[196:199],v[12:15]\nbuffer_load_dwordx4 v245,s[40:43],s69 offen offset:2048 lds\nM v[12:15],a[100:103],v[212:215],v[12:15]\nds_read_b64_tr_b16 v[216:217],v227 offset:0\nds_read_b64_tr_b16 v[218:219],v227 offset:4096\n" \
"M v[20:23],a[72:75],v[196:199],v[20:23]\nds_read_b64_tr_b16 v[220:221],v227 offset:8192\nds_read_b64_tr_b16 v[222:223],v227 offset:12288\nM v[20:23],a[104:107],v[212:215],v[20:23]\nds_read_b64_tr_b16 v[192:193],v226 offset:512\nds_read_b64_tr_b16 v[194:195],v226 offset:4608\nds_read_b64_tr_b16 v[208:209],v226 offset:8704\nM v[28:31],a[76:79],v[196:199],v[28:31]\n" \
"buffer_load_dwordx4 v[92:95],v240,s[52:55],s65 offen offset:3072 sc1\nM v[28:31],a[108:111],v[212:215],v[28:31]\ns_add_u32 s65,s65,4096\nds_read_b64_tr_b16 v[210:211],v226 offset:12800\nM v[36:39],a[80:83],v[196:199],v[36:39]\nv_accvgpr_read_b32 v224,a192\nv_cmp_eq_u32 vcc,2,v248\nM v[36:39],a[112:115],v[212:215],v[36:39]\n" \
"s_cmp_lg_u64 vcc,0\ns_addc_u32 s99,s99,0\nv_xor_b32 v250,0x8000,v250\nM v[44:47],a[84:87],v[196:199],v[44:47]\nbuffer_load_dwordx4 v246,s[40:43],s69 offen offset:3072 lds\nM v[44:47],a[116:119],v[212:215],v[44:47]\nv_add_u32 v229,s64,v250\ns_lshl_b32 s70,s70,7\n" \
"M v[52:55],a[88:91],v[196:199],v[52:55]\nv_lshl_add_u32 v254,v249,2,v254\nv_xor_b32 v254,s70,v254\nM v[52:55],a[120:123],v[212:215],v[52:55]\ns_mov_b32 s97,4096\ns_lshl_b32 s80,s62,9\ns_add_u32 s80,s80,73728\nM v[60:63],a[92:95],v[196:199],v[60:63]\n" \
"buffer_load_dwordx4 v[104:107],v240,s[52:55],s65 offen offset:0 sc1\nM v[60:63],a[124:127],v[212:215],v[60:63]\nv_and_b32 v236,15,v249\ns_lshl2_add_u32 s74,s63,s7\nM a[196:199],a[208:211],v[196:199],a[196:199]\nds_read_b64_tr_b16 v[196:197],v227 offset:512\nds_read_b64_tr_b16 v[198:199],v227 offset:4608\nM a[196:199],a[208:211],v[212:215],a[196:199]\n" \
"ds_read_b64_tr_b16 v[212:213],v227 offset:8704\nds_read_b64_tr_b16 v[214:215],v227 offset:12800\ns_waitcnt lgkmcnt(4)\nM v[128:131],v[64:67],v[200:203],0\ns_lshl_b32 s72,s62,7\nv_lshrrev_b32 v235,4,v249\nv_lshlrev_b32 v236,1,v236\ns_add_u32 s95,s74,s72\n" \
"M v[128:131],v[80:83],v[204:207],v[128:131]\nv_accvgpr_read_b32 v228,a196\nv_add_f32 v225,v224,v228\nv_rcp_f32 v224,v224\nv_lshl_or_b32 v235,v235,8,v236\nM v[132:135],v[68:71],v[200:203],0\nv_mul_f32 v0,v0,v224\nv_mul_f32 v1,v1,v224\n" \
"v_mul_f32 v2,v2,v224\nv_mul_f32 v3,v3,v224\nM v[132:135],v[84:87],v[204:207],v[132:135]\nv_cvt_pk_bf16_f32 v0,v0,v1\nv_mul_f32 v8,v8,v224\nv_mul_f32 v9,v9,v224\nv_mul_f32 v10,v10,v224\nv_mul_f32 v11,v11,v224\n" \
"M v[136:139],v[64:67],v[216:219],0\nbuffer_load_dwordx4 v[108:111],v240,s[52:55],s65 offen offset:1024 sc1\nv_cvt_pk_bf16_f32 v1,v2,v3\nds_read_b64_tr_b16 v[200:201],v226 offset:1024\nM v[136:139],v[80:83],v[220:223],v[136:139]\nds_read_b64_tr_b16 v[202:203],v226 offset:5120\nv_cvt_pk_bf16_f32 v2,v8,v9\nv_cvt_pk_bf16_f32 v3,v10,v11\n" \
"ds_read_b64_tr_b16 v[204:205],v226 offset:9216\nds_read_b64_tr_b16 v[206:207],v226 offset:13312\nM v[140:143],v[68:71],v[216:219],0\nbuffer_store_dwordx4 v[0:3],v240,s[52:55],s37 offen offset:0 sc1\nv_rcp_f32 v228,v228\nM v[140:143],v[84:87],v[220:223],v[140:143]\nv_mul_f32 v4,v4,v228\nv_mul_f32 v5,v5,v228\n" \
"ds_read_b64_tr_b16 v[216:217],v227 offset:1024\nds_read_b64_tr_b16 v[218:219],v227 offset:5120\nv_mul_f32 v6,v6,v228\nM v[144:147],v[64:67],v[192:195],0\nv_mul_f32 v7,v7,v228\nv_cvt_pk_bf16_f32 v4,v4,v5\nv_mul_f32 v12,v12,v228\nv_mul_f32 v13,v13,v228\n" \
"M v[144:147],v[80:83],v[208:211],v[144:147]\nv_mul_f32 v14,v14,v228\nv_mul_f32 v15,v15,v228\nv_cvt_pk_bf16_f32 v5,v6,v7\nds_read_b64_tr_b16 v[220:221],v227 offset:9216\nds_read_b64_tr_b16 v[222:223],v227 offset:13312\nM v[148:151],v[68:71],v[192:195],0\nv_cvt_pk_bf16_f32 v6,v12,v13\n" \
"v_cvt_pk_bf16_f32 v7,v14,v15\nds_read_b64_tr_b16 v[192:193],v226 offset:1536\nds_read_b64_tr_b16 v[194:195],v226 offset:5632\nM v[148:151],v[84:87],v[208:211],v[148:151]\nbuffer_store_dwordx4 v[4:7],v240,s[52:55],s37 offen offset:1024 sc1\nv_mul_f32 v16,v16,v224\nv_mul_f32 v17,v17,v224\ns_waitcnt lgkmcnt(6)\n" \
"M v[152:155],v[64:67],v[196:199],0\nbuffer_load_dwordx4 v[120:123],v240,s[52:55],s65 offen offset:2048 sc1\nds_read_b64_tr_b16 v[208:209],v226 offset:9728\nds_read_b64_tr_b16 v[210:211],v226 offset:13824\nM v[152:155],v[80:83],v[212:215],v[152:155]\nv_mul_f32 v18,v18,v224\nv_mul_f32 v19,v19,v224\nv_cvt_pk_bf16_f32 v16,v16,v17\n" \
"v_mul_f32 v24,v24,v224\nv_mul_f32 v25,v25,v224\nM v[156:159],v[68:71],v[196:199],0\nv_mul_f32 v26,v26,v224\nv_mul_f32 v27,v27,v224\nv_cvt_pk_bf16_f32 v17,v18,v19\nv_cvt_pk_bf16_f32 v18,v24,v25\nM v[156:159],v[84:87],v[212:215],v[156:159]\n" \
"v_cvt_pk_bf16_f32 v19,v26,v27\nds_read_b64_tr_b16 v[196:197],v227 offset:1536\nds_read_b64_tr_b16 v[198:199],v227 offset:5632\nds_read_b64_tr_b16 v[212:213],v227 offset:9728\nv_mul_f32 v20,v20,v228\nM v[160:163],v[64:67],v[200:203],0\nbuffer_store_dwordx4 v[16:19],v240,s[52:55],s37 offen offset:2048 sc1\nds_read_b64_tr_b16 v[214:215],v227 offset:13824\n" \
"v_mul_f32 v21,v21,v228\nM v[160:163],v[80:83],v[204:207],v[160:163]\nv_mul_f32 v22,v22,v228\nv_mul_f32 v23,v23,v228\nv_cvt_pk_bf16_f32 v20,v20,v21\nv_mul_f32 v28,v28,v228\nv_mul_f32 v29,v29,v228\nM v[164:167],v[68:71],v[200:203],0\n" \
"v_mul_f32 v30,v30,v228\nv_mul_f32 v31,v31,v228\nv_cvt_pk_bf16_f32 v21,v22,v23\nv_cvt_pk_bf16_f32 v22,v28,v29\nM v[164:167],v[84:87],v[204:207],v[164:167]\nv_cvt_pk_bf16_f32 v23,v30,v31\nds_read_b64_tr_b16 v[200:201],v226 offset:16384\nds_read_b64_tr_b16 v[202:203],v226 offset:20480\n" \
"ds_read_b64_tr_b16 v[204:205],v226 offset:24576\ns_waitcnt lgkmcnt(6)\nM v[168:171],v[64:67],v[216:219],0\nbuffer_load_dwordx4 v[124:127],v240,s[52:55],s65 offen offset:3072 sc1\nds_read_b64_tr_b16 v[206:207],v226 offset:28672\nv_mul_f32 v32,v32,v224\nM v[168:171],v[80:83],v[220:223],v[168:171]\nbuffer_store_dwordx4 v[20:23],v240,s[52:55],s37 offen offset:3072 sc1\n" \
"v_mul_f32 v33,v33,v224\nv_mul_f32 v34,v34,v224\nv_mul_f32 v35,v35,v224\nM v[172:175],v[68:71],v[216:219],0\nv_cvt_pk_bf16_f32 v32,v32,v33\nv_mul_f32 v40,v40,v224\nv_mul_f32 v41,v41,v224\nv_mul_f32 v42,v42,v224\n" \
"M v[172:175],v[84:87],v[220:223],v[172:175]\nv_mul_f32 v43,v43,v224\nv_cvt_pk_bf16_f32 v33,v34,v35\nds_read_b64_tr_b16 v[216:217],v227 offset:16384\nds_read_b64_tr_b16 v[218:219],v227 offset:20480\nv_cvt_pk_bf16_f32 v34,v40,v41\nM v[176:179],v[64:67],v[192:195],0\nv_cvt_pk_bf16_f32 v35,v42,v43\n" \
"ds_read_b64_tr_b16 v[220:221],v227 offset:24576\nds_read_b64_tr_b16 v[222:223],v227 offset:28672\ns_add_u32 s65,s11,s67\nM v[176:179],v[80:83],v[208:211],v[176:179]\ns_add_u32 s65,s65,1048576\nv_mul_f32 v36,v36,v228\nv_mul_f32 v37,v37,v228\nv_mul_f32 v38,v38,v228\n" \
"v_mul_f32 v39,v39,v228\nM v[180:183],v[68:71],v[192:195],0\nbuffer_store_dwordx4 v[32:35],v240,s[52:55],s38 offen offset:0 sc1\nds_read_b64_tr_b16 v[192:193],v226 offset:16896\nds_read_b64_tr_b16 v[194:195],v226 offset:20992\nM v[180:183],v[84:87],v[208:211],v[180:183]\nv_cvt_pk_bf16_f32 v36,v36,v37\nv_mul_f32 v44,v44,v228\n" \
"v_mul_f32 v45,v45,v228\nv_mul_f32 v46,v46,v228\ns_waitcnt lgkmcnt(6)\nM v[184:187],v[64:67],v[196:199],0\nbuffer_load_dwordx4 a[0:3],v240,s[52:55],s65 offen offset:0 sc1\nv_mul_f32 v47,v47,v228\nv_cvt_pk_bf16_f32 v37,v38,v39\nM v[184:187],v[80:83],v[212:215],v[184:187]\n" \
"ds_read_b64_tr_b16 v[208:209],v226 offset:25088\nds_read_b64_tr_b16 v[210:211],v226 offset:29184\nv_cvt_pk_bf16_f32 v38,v44,v45\nv_cvt_pk_bf16_f32 v39,v46,v47\nv_add_f32 v225,v225,v224\nM v[188:191],v[68:71],v[196:199],0\nds_read_b64_tr_b16 v[196:197],v227 offset:16896\nds_read_b64_tr_b16 v[198:199],v227 offset:20992\n" \
"v_add_f32 v225,v225,v228\nv_cmp_ngt_f32 vcc,s28,v225\nM v[188:191],v[84:87],v[212:215],v[188:191]\nbuffer_store_dwordx4 v[36:39],v240,s[52:55],s38 offen offset:1024 sc1\nv_mul_f32 v48,v48,v224\nv_mul_f32 v49,v49,v224\nds_read_b64_tr_b16 v[212:213],v227 offset:25088\nM v[128:131],v[96:99],v[200:203],v[128:131]\n" \
"ds_read_b64_tr_b16 v[214:215],v227 offset:29184\ns_and_b64 vcc,vcc,s[90:91]\ns_addc_u32 s99,s99,0\nv_mul_f32 v50,v50,v224\nM v[128:131],v[112:115],v[204:207],v[128:131]\nv_mul_f32 v51,v51,v224\nv_cvt_pk_bf16_f32 v48,v48,v49\nv_mul_f32 v56,v56,v224\n" \
"v_mul_f32 v57,v57,v224\nv_mul_f32 v58,v58,v224\nM v[132:135],v[100:103],v[200:203],v[132:135]\nv_mul_f32 v59,v59,v224\nv_cvt_pk_bf16_f32 v49,v50,v51\nv_cvt_pk_bf16_f32 v50,v56,v57\nv_cvt_pk_bf16_f32 v51,v58,v59\nM v[132:135],v[116:119],v[204:207],v[132:135]\n" \
"v_mov_b32 v230,s99\nds_write_b32 v229,v230\nds_read_b64_tr_b16 v[200:201],v226 offset:17408\nds_read_b64_tr_b16 v[202:203],v226 offset:21504\ns_waitcnt lgkmcnt(5)\nM v[136:139],v[96:99],v[216:219],v[136:139]\nbuffer_store_dwordx4 v[48:51],v240,s[52:55],s38 offen offset:2048 sc1\nbuffer_load_dwordx4 a[4:7],v240,s[52:55],s65 offen offset:1024 sc1\n" \
"M v[136:139],v[112:115],v[220:223],v[136:139]\nds_read_b64_tr_b16 v[204:205],v226 offset:25600\nds_read_b64_tr_b16 v[206:207],v226 offset:29696\nv_mul_f32 v52,v52,v228\nv_mul_f32 v53,v53,v228\nv_mul_f32 v54,v54,v228\nM v[140:143],v[100:103],v[216:219],v[140:143]\nv_mul_f32 v55,v55,v228\n" \
"v_cvt_pk_bf16_f32 v52,v52,v53\nv_mul_f32 v60,v60,v228\nv_mul_f32 v61,v61,v228\nM v[140:143],v[116:119],v[220:223],v[140:143]\nv_mul_f32 v62,v62,v228\nv_mul_f32 v63,v63,v228\nv_cvt_pk_bf16_f32 v53,v54,v55\nv_cvt_pk_bf16_f32 v54,v60,v61\n" \
"v_cvt_pk_bf16_f32 v55,v62,v63\nM v[144:147],v[96:99],v[192:195],v[144:147]\nds_read_b64_tr_b16 v[216:217],v227 offset:17408\nds_read_b64_tr_b16 v[218:219],v227 offset:21504\nds_read_b64_tr_b16 v[220:221],v227 offset:25600\ns_lshl4_add_u32 s69,s14,s68\nM v[144:147],v[112:115],v[208:211],v[144:147]\nbuffer_store_dwordx4 v[52:55],v240,s[52:55],s38 offen offset:3072 sc1\n" \
"ds_read_b64_tr_b16 v[222:223],v227 offset:29696\ns_add_u32 m0,s64,65536\nv_lshl_add_u32 v229,v251,10,v250\nM v[148:151],v[100:103],v[192:195],v[148:151]\nds_read_b64_tr_b16 v[192:193],v226 offset:17920\nds_read_b64_tr_b16 v[194:195],v226 offset:22016\ns_lshl_b32 s95,s95,10\nv_add_u32 v236,s95,v235\n" \
"M v[148:151],v[116:119],v[208:211],v[148:151]\nds_read_b64_tr_b16 v[208:209],v226 offset:26112\nds_read_b64_tr_b16 v[210:211],v226 offset:30208\nv_add_u32 v226,0,v242\ns_sub_u32 s74,s74,s72\ns_add_u32 s74,s74,1920\nM v[152:155],v[96:99],v[196:199],v[152:155]\nbuffer_load_dwordx4 a[8:11],v240,s[52:55],s65 offen offset:2048 sc1\n" \
"s_lshl_b32 s74,s74,10\ns_waitcnt lgkmcnt(7)\nM v[152:155],v[112:115],v[212:215],v[152:155]\nv_add_u32 v237,s74,v235\ns_lshl_b32 s72,s7,9\nv_lshl_add_u32 v239,v249,3,s72\ns_add_u32 s38,s33,16384\nv_bfe_u32 v228,v249,2,2\n" \
"M v[156:159],v[100:103],v[196:199],v[156:159]\ns_barrier\nbuffer_load_dwordx4 v243,s[40:43],s69 offen offset:0 lds\nds_read_b64_tr_b16 v[196:197],v227 offset:17920\nM v[156:159],v[116:119],v[212:215],v[156:159]\nds_read_b64_tr_b16 v[198:199],v227 offset:22016\nds_read_b32 v230,v229\nds_read_b64_tr_b16 v[212:213],v227 offset:26112\n" \
"v_lshrrev_b32 v229,4,v249\ns_add_u32 s95,s95,54525952\nM v[160:163],v[96:99],v[200:203],v[160:163]\nds_read_b64_tr_b16 v[214:215],v227 offset:30208\nv_xor_b32 v227,32,v226\ns_add_u32 s74,s74,54525952\nM v[160:163],v[112:115],v[204:207],v[160:163]\nM v[164:167],v[100:103],v[200:203],v[164:167]\n" \
"ds_read_b64_tr_b16 v[200:201],v226 offset:0\nds_read_b64_tr_b16 v[202:203],v226 offset:4096\nM v[164:167],v[116:119],v[204:207],v[164:167]\nds_read_b64_tr_b16 v[204:205],v226 offset:8192\ns_waitcnt lgkmcnt(8)\nM v[168:171],v[96:99],v[216:219],v[168:171]\nbuffer_load_dwordx4 a[12:15],v240,s[52:55],s65 offen offset:3072 sc1\nM v[168:171],v[112:115],v[220:223],v[168:171]\n" \
"ds_read_b64_tr_b16 v[206:207],v226 offset:12288\ns_add_u32 s65,s65,4096\nM v[172:175],v[100:103],v[216:219],v[172:175]\nbuffer_load_dwordx4 v244,s[40:43],s69 offen offset:1024 lds\nM v[172:175],v[116:119],v[220:223],v[172:175]\nds_read_b64_tr_b16 v[216:217],v227 offset:0\nds_read_b64_tr_b16 v[218:219],v227 offset:4096\nM v[176:179],v[96:99],v[192:195],v[176:179]\n" \
"ds_read_b64_tr_b16 v[220:221],v227 offset:8192\nds_read_b64_tr_b16 v[222:223],v227 offset:12288\nM v[176:179],v[112:115],v[208:211],v[176:179]\ns_waitcnt lgkmcnt(8)\nv_cmp_ne_u32 vcc,0,v230\nM v[180:183],v[100:103],v[192:195],v[180:183]\ns_cbranch_vccnz .Lredo\nds_read_b64_tr_b16 v[192:193],v226 offset:512\n" \
"M v[180:183],v[116:119],v[208:211],v[180:183]\nds_read_b64_tr_b16 v[194:195],v226 offset:4608\nds_read_b64_tr_b16 v[208:209],v226 offset:8704\nM v[184:187],v[96:99],v[196:199],v[184:187]\nbuffer_load_dwordx4 a[16:19],v240,s[52:55],s65 offen offset:0 sc1\nM v[184:187],v[112:115],v[212:215],v[184:187]\nds_read_b64_tr_b16 v[210:211],v226 offset:12800\ns_lshl_b32 s70,s2,9\n" \
"M v[188:191],v[100:103],v[196:199],v[188:191]\nbuffer_load_dwordx4 v245,s[40:43],s69 offen offset:2048 lds\nM v[188:191],v[116:119],v[212:215],v[188:191]\nds_read_b64_tr_b16 v[196:197],v227 offset:512\ns_waitcnt vmcnt(27) lgkmcnt(6)\nM v[128:131],v[72:75],v[200:203],v[128:131]\nds_read_b64_tr_b16 v[198:199],v227 offset:4608\ns_waitcnt vmcnt(23)\n" \
"M v[128:131],v[88:91],v[204:207],v[128:131]\nds_read_b64_tr_b16 v[212:213],v227 offset:8704\nds_read_b64_tr_b16 v[214:215],v227 offset:12800\nM v[132:135],v[76:79],v[200:203],v[132:135]\nds_read_b64_tr_b16 v[200:201],v226 offset:1024\ns_waitcnt vmcnt(21)\nM v[132:135],v[92:95],v[204:207],v[132:135]\nds_read_b64_tr_b16 v[202:203],v226 offset:5120\n" \
"ds_read_b64_tr_b16 v[204:205],v226 offset:9216\nM v[136:139],v[72:75],v[216:219],v[136:139]\nbuffer_load_dwordx4 a[20:23],v240,s[52:55],s65 offen offset:1024 sc1\ns_waitcnt lgkmcnt(7)\nM v[136:139],v[88:91],v[220:223],v[136:139]\nds_read_b64_tr_b16 v[206:207],v226 offset:13312\ns_lshl_b32 s71,s7,8\nM v[140:143],v[76:79],v[216:219],v[140:143]\n" \
"buffer_load_dwordx4 v246,s[40:43],s69 offen offset:3072 lds\nM v[140:143],v[92:95],v[220:223],v[140:143]\nds_read_b64_tr_b16 v[216:217],v227 offset:1024\nds_read_b64_tr_b16 v[218:219],v227 offset:5120\nM v[144:147],v[72:75],v[192:195],v[144:147]\nds_read_b64_tr_b16 v[220:221],v227 offset:9216\nds_read_b64_tr_b16 v[222:223],v227 offset:13312\nM v[144:147],v[88:91],v[208:211],v[144:147]\n" \
"s_lshl4_add_u32 s69,s14,s68\ns_add_u32 s69,s69,0x10000\nM v[148:151],v[76:79],v[192:195],v[148:151]\nds_read_b64_tr_b16 v[192:193],v226 offset:1536\nds_read_b64_tr_b16 v[194:195],v226 offset:5632\nM v[148:151],v[92:95],v[208:211],v[148:151]\ns_add_u32 m0,s64,98304\ns_waitcnt lgkmcnt(7)\n" \
"M v[152:155],v[72:75],v[196:199],v[152:155]\nbuffer_load_dwordx4 a[24:27],v240,s[52:55],s65 offen offset:2048 sc1\nM v[152:155],v[88:91],v[212:215],v[152:155]\nds_read_b64_tr_b16 v[208:209],v226 offset:9728\nds_read_b64_tr_b16 v[210:211],v226 offset:13824\nM v[156:159],v[76:79],v[196:199],v[156:159]\nbuffer_load_dwordx4 v243,s[40:43],s69 offen offset:0 lds\nM v[156:159],v[92:95],v[212:215],v[156:159]\n" \
"ds_read_b64_tr_b16 v[196:197],v227 offset:1536\nds_read_b64_tr_b16 v[198:199],v227 offset:5632\nM v[160:163],v[72:75],v[200:203],v[160:163]\nds_read_b64_tr_b16 v[212:213],v227 offset:9728\ns_waitcnt lgkmcnt(7)\nM v[160:163],v[88:91],v[204:207],v[160:163]\nds_read_b64_tr_b16 v[214:215],v227 offset:13824\nv_lshl_add_u32 v241,v249,2,s71\n" \
"M v[164:167],v[76:79],v[200:203],v[164:167]\nds_read_b64_tr_b16 v[200:201],v226 offset:32768\nds_read_b64_tr_b16 v[202:203],v226 offset:36864\nM v[164:167],v[92:95],v[204:207],v[164:167]\nds_read_b64_tr_b16 v[204:205],v226 offset:40960\nds_read_b64_tr_b16 v[206:207],v226 offset:45056\nM v[168:171],v[72:75],v[216:219],v[168:171]\nbuffer_load_dwordx4 a[28:31],v240,s[52:55],s65 offen offset:3072 sc1\n" \
"M v[168:171],v[88:91],v[220:223],v[168:171]\ns_add_u32 s65,s14,s67\ns_add_u32 s65,s65,1048576\nM v[172:175],v[76:79],v[216:219],v[172:175]\nbuffer_load_dwordx4 v244,s[40:43],s69 offen offset:1024 lds\nM v[172:175],v[92:95],v[220:223],v[172:175]\nds_read_b64_tr_b16 v[216:217],v227 offset:32768\ns_waitcnt lgkmcnt(6)\n" \
"M v[176:179],v[72:75],v[192:195],v[176:179]\nds_read_b64_tr_b16 v[218:219],v227 offset:36864\nds_read_b64_tr_b16 v[220:221],v227 offset:40960\nM v[176:179],v[88:91],v[208:211],v[176:179]\nds_read_b64_tr_b16 v[222:223],v227 offset:45056\ns_add_u32 s70,s70,4096\nM v[180:183],v[76:79],v[192:195],v[180:183]\nds_read_b64_tr_b16 v[192:193],v226 offset:33280\n" \
"ds_read_b64_tr_b16 v[194:195],v226 offset:37376\nM v[180:183],v[92:95],v[208:211],v[180:183]\nds_read_b64_tr_b16 v[208:209],v226 offset:41472\nds_read_b64_tr_b16 v[210:211],v226 offset:45568\nM v[184:187],v[72:75],v[196:199],v[184:187]\nbuffer_load_dwordx4 a[32:35],v240,s[52:55],s65 offen offset:0 sc1\ns_waitcnt lgkmcnt(8)\nM v[184:187],v[88:91],v[212:215],v[184:187]\n" \
"v_add_u32 v230,s70,v241\nv_add_u32 v231,8192,v230\nM v[188:191],v[76:79],v[196:199],v[188:191]\nbuffer_load_dwordx4 v245,s[40:43],s69 offen offset:2048 lds\nM v[188:191],v[92:95],v[212:215],v[188:191]\nds_read_b64_tr_b16 v[196:197],v227 offset:33280\ns_waitcnt vmcnt(26)\nM v[128:131],v[104:107],v[200:203],v[128:131]\n" \
"ds_read_b64_tr_b16 v[198:199],v227 offset:37376\ns_waitcnt vmcnt(23)\nM v[128:131],v[120:123],v[204:207],v[128:131]\nds_read_b64_tr_b16 v[212:213],v227 offset:41472\nds_read_b64_tr_b16 v[214:215],v227 offset:45568\nM v[132:135],v[108:111],v[200:203],v[132:135]\nds_read_b64_tr_b16 v[200:201],v226 offset:33792\ns_waitcnt vmcnt(21)\n" \
"M v[132:135],v[124:127],v[204:207],v[132:135]\nds_read_b64_tr_b16 v[202:203],v226 offset:37888\ns_waitcnt lgkmcnt(6)\nM v[136:139],v[104:107],v[216:219],v[136:139]\nbuffer_load_dwordx4 a[36:39],v240,s[52:55],s65 offen offset:1024 sc1\nM v[136:139],v[120:123],v[220:223],v[136:139]\nds_read_b64_tr_b16 v[204:205],v226 offset:41984\nds_read_b64_tr_b16 v[206:207],v226 offset:46080\n" \
"M v[140:143],v[108:111],v[216:219],v[140:143]\nbuffer_load_dwordx4 v246,s[40:43],s69 offen offset:3072 lds\nM v[140:143],v[124:127],v[220:223],v[140:143]\nds_read_b64_tr_b16 v[216:217],v227 offset:33792\nds_read_b64_tr_b16 v[218:219],v227 offset:37888\nM v[144:147],v[104:107],v[192:195],v[144:147]\nds_read_b64_tr_b16 v[220:221],v227 offset:41984\nds_read_b64_tr_b16 v[222:223],v227 offset:46080\n" \
"M v[144:147],v[120:123],v[208:211],v[144:147]\nv_mov_b32 v250,83968\nv_accvgpr_write_b32 a194,0\nM v[148:151],v[108:111],v[192:195],v[148:151]\nds_read_b64_tr_b16 v[192:193],v226 offset:34304\nds_read_b64_tr_b16 v[194:195],v226 offset:38400\nM v[148:151],v[124:127],v[208:211],v[148:151]\nds_read_b64_tr_b16 v[208:209],v226 offset:42496\n" \
"s_waitcnt lgkmcnt(9)\nM v[152:155],v[104:107],v[196:199],v[152:155]\nbuffer_load_dwordx4 a[40:43],v240,s[52:55],s65 offen offset:2048 sc1\nM v[152:155],v[120:123],v[212:215],v[152:155]\nds_read_b64_tr_b16 v[210:211],v226 offset:46592\nv_accvgpr_write_b32 a195,0\nM v[156:159],v[108:111],v[196:199],v[156:159]\ns_waitcnt vmcnt(16)\n" \
"buffer_store_dword v252,off,s[52:55],s76 sc1\ns_barrier\nM v[156:159],v[124:127],v[212:215],v[156:159]\nds_read_b64_tr_b16 v[196:197],v227 offset:34304\nds_read_b64_tr_b16 v[198:199],v227 offset:38400\nM v[160:163],v[104:107],v[200:203],v[160:163]\nds_read_b64_tr_b16 v[212:213],v227 offset:42496\ns_waitcnt lgkmcnt(7)\n" \
"M v[160:163],v[120:123],v[204:207],v[160:163]\nds_read_b64_tr_b16 v[214:215],v227 offset:46592\nv_accvgpr_write_b32 a196,0\nM v[164:167],v[108:111],v[200:203],v[164:167]\nds_read_b64_tr_b16 v[200:201],v226 offset:16384\nds_read_b64_tr_b16 v[202:203],v226 offset:20480\nM v[164:167],v[124:127],v[204:207],v[164:167]\nds_read_b64_tr_b16 v[204:205],v226 offset:24576\n" \
"ds_read_b64_tr_b16 v[206:207],v226 offset:28672\nM v[168:171],v[104:107],v[216:219],v[168:171]\nbuffer_load_dwordx4 a[44:47],v240,s[52:55],s65 offen offset:3072 sc1\nM v[168:171],v[120:123],v[220:223],v[168:171]\ns_add_u32 s65,s65,4096\nv_accvgpr_write_b32 a197,0\nM v[172:175],v[108:111],v[216:219],v[172:175]\nds_read_b64_tr_b16 v[216:217],v227 offset:16384\n" \
"ds_read_b64_tr_b16 v[218:219],v227 offset:20480\nM v[172:175],v[124:127],v[220:223],v[172:175]\nds_read_b64_tr_b16 v[220:221],v227 offset:24576\ns_waitcnt lgkmcnt(8)\nM v[176:179],v[104:107],v[192:195],v[176:179]\nds_read_b64_tr_b16 v[222:223],v227 offset:28672\ns_load_dwordx2 s[44:45],s[4:5],0x68\nM v[176:179],v[120:123],v[208:211],v[176:179]\n" \
"ds_write_b128 v250,a[244:247]\nM v[180:183],v[108:111],v[192:195],v[180:183]\nds_read_b64_tr_b16 v[192:193],v226 offset:16896\nds_read_b64_tr_b16 v[194:195],v226 offset:20992\nM v[180:183],v[124:127],v[208:211],v[180:183]\nds_read_b64_tr_b16 v[208:209],v226 offset:25088\nds_read_b64_tr_b16 v[210:211],v226 offset:29184\nM v[184:187],v[104:107],v[196:199],v[184:187]\n" \
"buffer_load_dwordx4 a[48:51],v240,s[52:55],s65 offen offset:0 sc1\ns_waitcnt lgkmcnt(9)\nM v[184:187],v[120:123],v[212:215],v[184:187]\ns_mov_b32 s46,2097152\nM v[188:191],v[108:111],v[196:199],v[188:191]\nds_read_b64_tr_b16 v[196:197],v227 offset:16896\nds_read_b64_tr_b16 v[198:199],v227 offset:20992\nM v[188:191],v[124:127],v[212:215],v[188:191]\n" \
"ds_read_b64_tr_b16 v[212:213],v227 offset:25088\nds_read_b64_tr_b16 v[214:215],v227 offset:29184\nM v[128:131],a[0:3],v[200:203],v[128:131]\nM v[128:131],a[8:11],v[204:207],v[128:131]\nM v[132:135],a[4:7],v[200:203],v[132:135]\nbuffer_load_dwordx4 a[160:163],v238,s[52:55],0 offen offset:0 sc1\ns_waitcnt vmcnt(18)\nM v[132:135],a[12:15],v[204:207],v[132:135]\n" \
"ds_read_b64_tr_b16 v[200:201],v226 offset:17408\ns_waitcnt lgkmcnt(5)\nM v[136:139],a[0:3],v[216:219],v[136:139]\nbuffer_load_dwordx4 a[52:55],v240,s[52:55],s65 offen offset:1024 sc1\nM v[136:139],a[8:11],v[220:223],v[136:139]\nds_read_b64_tr_b16 v[202:203],v226 offset:21504\nds_read_b64_tr_b16 v[204:205],v226 offset:25600\nM v[140:143],a[4:7],v[216:219],v[140:143]\n" \
"ds_read_b64_tr_b16 v[206:207],v226 offset:29696\nds_read_b64_tr_b16 v[216:217],v227 offset:17408\nM v[140:143],a[12:15],v[220:223],v[140:143]\nglobal_load_dword a200,v230,s[26:27] offset:-4096\nM v[144:147],a[0:3],v[192:195],v[144:147]\nbuffer_load_dwordx4 a[164:167],v238,s[52:55],0 offen offset:16 sc1\nM v[144:147],a[8:11],v[208:211],v[144:147]\nds_read_b64_tr_b16 v[218:219],v227 offset:21504\n" \
"global_load_dword a201,v230,s[26:27] offset:-3072\nM v[148:151],a[4:7],v[192:195],v[148:151]\nds_read_b64_tr_b16 v[220:221],v227 offset:25600\nds_read_b64_tr_b16 v[222:223],v227 offset:29696\nM v[148:151],a[12:15],v[208:211],v[148:151]\nglobal_load_dword a202,v230,s[26:27] offset:-2048\ns_waitcnt lgkmcnt(7)\nM v[152:155],a[0:3],v[196:199],v[152:155]\n" \
"buffer_load_dwordx4 a[56:59],v240,s[52:55],s65 offen offset:2048 sc1\nM v[152:155],a[8:11],v[212:215],v[152:155]\nds_read_b64_tr_b16 v[192:193],v226 offset:17920\nds_read_b64_tr_b16 v[194:195],v226 offset:22016\nglobal_load_dword a203,v230,s[26:27] offset:-1024\nM v[156:159],a[4:7],v[196:199],v[156:159]\nds_read_b64_tr_b16 v[208:209],v226 offset:26112\nds_read_b64_tr_b16 v[210:211],v226 offset:30208\n" \
"buffer_load_dwordx4 a[168:171],v238,s[52:55],0 offen offset:2048 sc1\nM v[156:159],a[12:15],v[212:215],v[156:159]\nglobal_load_dword a204,v230,s[26:27] offset:0\ns_waitcnt lgkmcnt(6)\nM v[160:163],a[0:3],v[200:203],v[160:163]\nds_read_b64_tr_b16 v[196:197],v227 offset:17920\nds_read_b64_tr_b16 v[198:199],v227 offset:22016\nM v[160:163],a[8:11],v[204:207],v[160:163]\n" \
"global_load_dword a205,v230,s[26:27] offset:1024\nM v[164:167],a[4:7],v[200:203],v[164:167]\nds_read_b64_tr_b16 v[212:213],v227 offset:26112\nds_read_b64_tr_b16 v[214:215],v227 offset:30208\nM v[164:167],a[12:15],v[204:207],v[164:167]\nglobal_load_dword a206,v230,s[26:27] offset:2048\nM v[168:171],a[0:3],v[216:219],v[168:171]\nbuffer_load_dwordx4 a[60:63],v240,s[52:55],s65 offen offset:3072 sc1\n" \
"buffer_load_dwordx4 a[172:175],v238,s[52:55],0 offen offset:2064 sc1\ns_waitcnt lgkmcnt(4)\nM v[168:171],a[8:11],v[220:223],v[168:171]\nds_read_b64_tr_b16 v[200:201],v226 offset:49152\nds_read_b64_tr_b16 v[202:203],v226 offset:53248\nglobal_load_dword a207,v230,s[26:27] offset:3072\nM v[172:175],a[4:7],v[216:219],v[172:175]\nds_read_b64_tr_b16 v[204:205],v226 offset:57344\n" \
"ds_read_b64_tr_b16 v[206:207],v226 offset:61440\nM v[172:175],a[12:15],v[220:223],v[172:175]\nglobal_load_dword a208,v231,s[26:27] offset:-4096\nM v[176:179],a[0:3],v[192:195],v[176:179]\nds_read_b64_tr_b16 v[216:217],v227 offset:49152\nds_read_b64_tr_b16 v[218:219],v227 offset:53248\nM v[176:179],a[8:11],v[208:211],v[176:179]\nglobal_load_dword a209,v231,s[26:27] offset:-3072\n" \
"M v[180:183],a[4:7],v[192:195],v[180:183]\nds_read_b64_tr_b16 v[220:221],v227 offset:57344\nds_read_b64_tr_b16 v[222:223],v227 offset:61440\nbuffer_load_dwordx4 a[176:179],v238,s[52:55],s97 offen offset:0 sc1\nM v[180:183],a[12:15],v[208:211],v[180:183]\nglobal_load_dword a210,v231,s[26:27] offset:-2048\ns_waitcnt lgkmcnt(6)\nM v[184:187],a[0:3],v[196:199],v[184:187]\n" \
"ds_read_b64_tr_b16 v[192:193],v226 offset:49664\nds_read_b64_tr_b16 v[194:195],v226 offset:53760\nM v[184:187],a[8:11],v[212:215],v[184:187]\nglobal_load_dword a211,v231,s[26:27] offset:-1024\nM v[188:191],a[4:7],v[196:199],v[188:191]\nds_read_b64_tr_b16 v[208:209],v226 offset:57856\nds_read_b64_tr_b16 v[210:211],v226 offset:61952\nM v[188:191],a[12:15],v[212:215],v[188:191]\n" \
"global_load_dword a252,v231,s[26:27] offset:0\ns_waitcnt vmcnt(36)\nM v[128:131],a[16:19],v[200:203],v[128:131]\nbuffer_load_dwordx4 a[180:183],v238,s[52:55],s97 offen offset:16 sc1\ns_waitcnt vmcnt(33) lgkmcnt(4)\nM v[128:131],a[24:27],v[204:207],v[128:131]\nds_read_b64_tr_b16 v[196:197],v227 offset:49664\nds_read_b64_tr_b16 v[198:199],v227 offset:53760\n" \
"global_load_dword a253,v231,s[26:27] offset:1024\nM v[132:135],a[20:23],v[200:203],v[132:135]\nds_read_b64_tr_b16 v[212:213],v227 offset:57856\nds_read_b64_tr_b16 v[214:215],v227 offset:61952\ns_waitcnt vmcnt(32)\nM v[132:135],a[28:31],v[204:207],v[132:135]\nglobal_load_dword a254,v231,s[26:27] offset:2048\nM v[136:139],a[16:19],v[216:219],v[136:139]\n" \
"ds_read_b64_tr_b16 v[200:201],v226 offset:50176\nds_read_b64_tr_b16 v[202:203],v226 offset:54272\nM v[136:139],a[24:27],v[220:223],v[136:139]\nglobal_load_dword a255,v231,s[26:27] offset:3072\nM v[140:143],a[20:23],v[216:219],v[140:143]\nds_read_b64_tr_b16 v[204:205],v226 offset:58368\nds_read_b64_tr_b16 v[206:207],v226 offset:62464\nbuffer_load_dwordx4 a[184:187],v238,s[52:55],s97 offen offset:2048 sc1\n" \
"M v[140:143],a[28:31],v[220:223],v[140:143]\nds_read_b64_tr_b16 v[216:217],v227 offset:50176\ns_waitcnt lgkmcnt(7)\nM v[144:147],a[16:19],v[192:195],v[144:147]\nbuffer_load_dword v248,v254,s[52:55],s80 offen sc1\nM v[144:147],a[24:27],v[208:211],v[144:147]\nds_read_b64_tr_b16 v[218:219],v227 offset:54272\nds_read_b64_tr_b16 v[220:221],v227 offset:58368\n" \
"M v[148:151],a[20:23],v[192:195],v[148:151]\nds_read_b64_tr_b16 v[222:223],v227 offset:62464\nds_read_b64_tr_b16 v[192:193],v226 offset:50688\nM v[148:151],a[28:31],v[208:211],v[148:151]\nglobal_load_ushort v64,v236,s[8:9] offset:0\nM v[152:155],a[16:19],v[196:199],v[152:155]\nbuffer_load_dwordx4 a[188:191],v238,s[52:55],s97 offen offset:2064 sc1\ns_waitcnt lgkmcnt(7)\n" \
"M v[152:155],a[24:27],v[212:215],v[152:155]\nds_read_b64_tr_b16 v[194:195],v226 offset:54784\nglobal_load_ushort v80,v237,s[8:9] offset:0\nM v[156:159],a[20:23],v[196:199],v[156:159]\ns_waitcnt vmcnt(33)\ns_barrier\nds_read_b64_tr_b16 v[208:209],v226 offset:58880\nds_read_b64_tr_b16 v[210:211],v226 offset:62976\n" \
"M v[156:159],a[28:31],v[212:215],v[156:159]\nglobal_load_ushort v96,v235,s[20:21] offset:0\nM v[160:163],a[16:19],v[200:203],v[160:163]\nds_read_b64_tr_b16 v[196:197],v227 offset:50688\ns_waitcnt lgkmcnt(5)\nM v[160:163],a[24:27],v[204:207],v[160:163]\nds_read_b64_tr_b16 v[198:199],v227 offset:54784\nglobal_load_ushort v65,v236,s[8:9] offset:32\n" \
"M v[164:167],a[20:23],v[200:203],v[164:167]\nds_read_b64_tr_b16 v[212:213],v227 offset:58880\nds_read_b64_tr_b16 v[214:215],v227 offset:62976\nM v[164:167],a[28:31],v[204:207],v[164:167]\nglobal_load_ushort v81,v237,s[8:9] offset:32\nM v[168:171],a[16:19],v[216:219],v[168:171]\nv_add_u32 v226,65536,v242\nds_read_b64_tr_b16 v[200:201],v226 offset:0\n" \
"M v[168:171],a[24:27],v[220:223],v[168:171]\nds_read_b64_tr_b16 v[202:203],v226 offset:4096\nglobal_load_ushort v97,v235,s[20:21] offset:32\nM v[172:175],a[20:23],v[216:219],v[172:175]\nds_read_b64_tr_b16 v[204:205],v226 offset:8192\nds_read_b64_tr_b16 v[206:207],v226 offset:12288\nM v[172:175],a[28:31],v[220:223],v[172:175]\nglobal_load_ushort v66,v236,s[8:9] offset:64\n" \
"s_waitcnt lgkmcnt(7)\nM v[176:179],a[16:19],v[192:195],v[176:179]\nv_xor_b32 v227,32,v226\nds_read_b64_tr_b16 v[216:217],v227 offset:0\nM v[176:179],a[24:27],v[208:211],v[176:179]\nds_read_b64_tr_b16 v[218:219],v227 offset:4096\nglobal_load_ushort v82,v237,s[8:9] offset:64\nM v[180:183],a[20:23],v[192:195],v[180:183]\n" \
"ds_read_b64_tr_b16 v[220:221],v227 offset:8192\nds_read_b64_tr_b16 v[222:223],v227 offset:12288\nM v[180:183],a[28:31],v[208:211],v[180:183]\nglobal_load_ushort v98,v235,s[20:21] offset:64\ns_waitcnt lgkmcnt(6)\nM v[184:187],a[16:19],v[196:199],v[184:187]\nds_read_b64_tr_b16 v[192:193],v226 offset:512\nds_read_b64_tr_b16 v[194:195],v226 offset:4608\n" \
"M v[184:187],a[24:27],v[212:215],v[184:187]\nglobal_load_ushort v67,v236,s[8:9] offset:96\nM v[188:191],a[20:23],v[196:199],v[188:191]\nds_read_b64_tr_b16 v[208:209],v226 offset:8704\nds_read_b64_tr_b16 v[210:211],v226 offset:12800\nM v[188:191],a[28:31],v[212:215],v[188:191]\nglobal_load_ushort v83,v237,s[8:9] offset:96\nM v[128:131],a[32:35],v[200:203],v[128:131]\n" \
"ds_read_b64_tr_b16 v[196:197],v227 offset:512\ns_waitcnt lgkmcnt(7)\nM v[128:131],a[40:43],v[204:207],v[128:131]\nds_read_b64_tr_b16 v[198:199],v227 offset:4608\nglobal_load_ushort v99,v235,s[20:21] offset:96\nM v[132:135],a[36:39],v[200:203],v[132:135]\nds_read_b64_tr_b16 v[212:213],v227 offset:8704\nds_read_b64_tr_b16 v[214:215],v227 offset:12800\n" \
"s_waitcnt vmcnt(32)\nM v[132:135],a[44:47],v[204:207],v[132:135]\nglobal_load_ushort v68,v236,s[8:9] offset:128\nM v[136:139],a[32:35],v[216:219],v[136:139]\nds_read_b64_tr_b16 v[200:201],v226 offset:1024\ns_waitcnt lgkmcnt(7)\nM v[136:139],a[40:43],v[220:223],v[136:139]\nds_read_b64_tr_b16 v[202:203],v226 offset:5120\n" \
"global_load_ushort v84,v237,s[8:9] offset:128\nM v[140:143],a[36:39],v[216:219],v[140:143]\nds_read_b64_tr_b16 v[204:205],v226 offset:9216\nds_read_b64_tr_b16 v[206:207],v226 offset:13312\nM v[140:143],a[44:47],v[220:223],v[140:143]\nglobal_load_ushort v100,v235,s[20:21] offset:128\nM v[144:147],a[32:35],v[192:195],v[144:147]\nds_read_b64_tr_b16 v[216:217],v227 offset:1024\n" \
"s_waitcnt lgkmcnt(7)\nM v[144:147],a[40:43],v[208:211],v[144:147]\nds_read_b64_tr_b16 v[218:219],v227 offset:5120\nglobal_load_ushort v69,v236,s[8:9] offset:160\nM v[148:151],a[36:39],v[192:195],v[148:151]\nds_read_b64_tr_b16 v[220:221],v227 offset:9216\nds_read_b64_tr_b16 v[222:223],v227 offset:13312\nM v[148:151],a[44:47],v[208:211],v[148:151]\n" \
"global_load_ushort v85,v237,s[8:9] offset:160\nM v[152:155],a[32:35],v[196:199],v[152:155]\nds_read_b64_tr_b16 v[192:193],v226 offset:1536\ns_waitcnt lgkmcnt(7)\nM v[152:155],a[40:43],v[212:215],v[152:155]\nds_read_b64_tr_b16 v[194:195],v226 offset:5632\nglobal_load_ushort v101,v235,s[20:21] offset:160\nM v[156:159],a[36:39],v[196:199],v[156:159]\n" \
"ds_read_b64_tr_b16 v[208:209],v226 offset:9728\nds_read_b64_tr_b16 v[210:211],v226 offset:13824\nM v[156:159],a[44:47],v[212:215],v[156:159]\nglobal_load_ushort v70,v236,s[8:9] offset:192\nM v[160:163],a[32:35],v[200:203],v[160:163]\nds_read_b64_tr_b16 v[196:197],v227 offset:1536\ns_waitcnt lgkmcnt(7)\nM v[160:163],a[40:43],v[204:207],v[160:163]\n" \
"ds_read_b64_tr_b16 v[198:199],v227 offset:5632\nglobal_load_ushort v86,v237,s[8:9] offset:192\nM v[164:167],a[36:39],v[200:203],v[164:167]\nds_read_b64_tr_b16 v[212:213],v227 offset:9728\nds_read_b64_tr_b16 v[214:215],v227 offset:13824\nM v[164:167],a[44:47],v[204:207],v[164:167]\ns_waitcnt vmcnt(21)\nv_cmp_eq_u32 vcc,0,v248\n" \
"global_load_ushort v102,v235,s[20:21] offset:192\nM v[168:171],a[32:35],v[216:219],v[168:171]\ns_cbranch_vccnz .Lps7\n.Lpb7:\nbuffer_load_dword v248,v251,s[52:55],s79 offen sc1\ns_waitcnt lgkmcnt(4)\nM v[168:171],a[40:43],v[220:223],v[168:171]\nds_read_b64_tr_b16 v[200:201],v226 offset:32768\n" \
"ds_read_b64_tr_b16 v[202:203],v226 offset:36864\nglobal_load_ushort v71,v236,s[8:9] offset:224\nM v[172:175],a[36:39],v[216:219],v[172:175]\nds_read_b64_tr_b16 v[204:205],v226 offset:40960\nds_read_b64_tr_b16 v[206:207],v226 offset:45056\nM v[172:175],a[44:47],v[220:223],v[172:175]\nglobal_load_ushort v87,v237,s[8:9] offset:224\nM v[176:179],a[32:35],v[192:195],v[176:179]\n" \
"ds_read_b64_tr_b16 v[216:217],v227 offset:32768\nds_read_b64_tr_b16 v[218:219],v227 offset:36864\nM v[176:179],a[40:43],v[208:211],v[176:179]\nglobal_load_ushort v103,v235,s[20:21] offset:224\nM v[180:183],a[36:39],v[192:195],v[180:183]\nds_read_b64_tr_b16 v[220:221],v227 offset:40960\nds_read_b64_tr_b16 v[222:223],v227 offset:45056\nM v[180:183],a[44:47],v[208:211],v[180:183]\n" \
"buffer_load_dwordx2 v[112:113],v239,s[52:55],0 offen offset:0 sc1\ns_waitcnt lgkmcnt(8)\nM v[184:187],a[32:35],v[196:199],v[184:187]\nds_read_b64_tr_b16 v[192:193],v226 offset:33280\nds_read_b64_tr_b16 v[194:195],v226 offset:37376\nM v[184:187],a[40:43],v[212:215],v[184:187]\nbuffer_load_dwordx2 v[114:115],v239,s[52:55],0 offen offset:2048 sc1\nM v[188:191],a[36:39],v[196:199],v[188:191]\n" \
"ds_read_b64_tr_b16 v[208:209],v226 offset:41472\nds_read_b64_tr_b16 v[210:211],v226 offset:45568\nM v[188:191],a[44:47],v[212:215],v[188:191]\nbuffer_load_dwordx2 v[116:117],v239,s[52:55],s97 offen offset:0 sc1\ns_waitcnt lgkmcnt(6)\nM v[128:131],a[48:51],v[200:203],v[128:131]\nds_read_b64_tr_b16 v[196:197],v227 offset:33280\nds_read_b64_tr_b16 v[198:199],v227 offset:37376\n" \
"M v[128:131],a[56:59],v[204:207],v[128:131]\nbuffer_load_dwordx2 v[118:119],v239,s[52:55],s97 offen offset:2048 sc1\nM v[132:135],a[52:55],v[200:203],v[132:135]\nds_read_b64_tr_b16 v[212:213],v227 offset:41472\nds_read_b64_tr_b16 v[214:215],v227 offset:45568\nM v[132:135],a[60:63],v[204:207],v[132:135]\nds_read_b64_tr_b16 v[200:201],v226 offset:33792\nds_read_b64_tr_b16 v[202:203],v226 offset:37888\n" \
"M v[136:139],a[48:51],v[216:219],v[136:139]\nds_read_b64_tr_b16 v[204:205],v226 offset:41984\ns_waitcnt lgkmcnt(9)\nM v[136:139],a[56:59],v[220:223],v[136:139]\nds_read_b64_tr_b16 v[206:207],v226 offset:46080\ns_add_u32 s65,s10,s67\nM v[140:143],a[52:55],v[216:219],v[140:143]\nds_read_b64_tr_b16 v[216:217],v227 offset:33792\n" \
"ds_read_b64_tr_b16 v[218:219],v227 offset:37888\nM v[140:143],a[60:63],v[220:223],v[140:143]\nds_read_b64_tr_b16 v[220:221],v227 offset:41984\nds_read_b64_tr_b16 v[222:223],v227 offset:46080\nM v[144:147],a[48:51],v[192:195],v[144:147]\nv_cvt_pk_bf16_f32 v128,v128,v136\ns_waitcnt lgkmcnt(10)\nM v[144:147],a[56:59],v[208:211],v[144:147]\n" \
"v_cvt_pk_bf16_f32 v129,v129,v137\nv_cvt_pk_bf16_f32 v130,v130,v138\nM v[148:151],a[52:55],v[192:195],v[148:151]\nds_read_b64_tr_b16 v[192:193],v226 offset:34304\nds_read_b64_tr_b16 v[194:195],v226 offset:38400\nM v[148:151],a[60:63],v[208:211],v[148:151]\nds_read_b64_tr_b16 v[208:209],v226 offset:42496\nds_read_b64_tr_b16 v[210:211],v226 offset:46592\n" \
"M v[152:155],a[48:51],v[196:199],v[152:155]\nv_add_u32 v226,131072,v242\ns_waitcnt lgkmcnt(8)\nM v[152:155],a[56:59],v[212:215],v[152:155]\nv_cvt_pk_bf16_f32 v131,v131,v139\nv_min_u32 v136,-2,v128\nM v[156:159],a[52:55],v[196:199],v[156:159]\nds_read_b64_tr_b16 v[196:197],v227 offset:34304\n" \
"ds_read_b64_tr_b16 v[198:199],v227 offset:38400\nM v[156:159],a[60:63],v[212:215],v[156:159]\nds_read_b64_tr_b16 v[212:213],v227 offset:42496\nds_read_b64_tr_b16 v[214:215],v227 offset:46592\nM v[160:163],a[48:51],v[200:203],v[160:163]\nv_xor_b32 v227,32,v226\nv_cvt_pk_bf16_f32 v144,v144,v152\nM v[160:163],a[56:59],v[204:207],v[160:163]\n" \
"v_min_u32 v137,-2,v144\nv_cvt_pk_bf16_f32 v145,v145,v153\nM v[164:167],a[52:55],v[200:203],v[164:167]\nds_read_b64_tr_b16 v[200:201],v226 offset:0\nds_read_b64_tr_b16 v[202:203],v226 offset:4096\nM v[164:167],a[60:63],v[204:207],v[164:167]\nds_read_b64_tr_b16 v[204:205],v226 offset:8192\ns_waitcnt lgkmcnt(7)\n" \
"M v[168:171],a[48:51],v[216:219],v[168:171]\nds_read_b64_tr_b16 v[206:207],v226 offset:12288\nv_cvt_pk_bf16_f32 v146,v146,v154\nM v[168:171],a[56:59],v[220:223],v[168:171]\nv_cvt_pk_bf16_f32 v147,v147,v155\nv_min_u32 v152,-2,v129\nM v[172:175],a[52:55],v[216:219],v[172:175]\ns_waitcnt vmcnt(7)\n" \
"v_cmp_eq_u32 vcc,0,v248\nM v[172:175],a[60:63],v[220:223],v[172:175]\ns_cbranch_vccnz .Lps8\n.Lpb8:\nds_read_b64_tr_b16 v[216:217],v227 offset:0\nM v[176:179],a[48:51],v[192:195],v[176:179]\nbuffer_load_dwordx4 a[64:67],v240,s[52:55],s65 offen offset:0 sc1\nM v[176:179],a[56:59],v[208:211],v[176:179]\n" \
"ds_read_b64_tr_b16 v[218:219],v227 offset:4096\nds_read_b64_tr_b16 v[220:221],v227 offset:8192\nM v[180:183],a[52:55],v[192:195],v[180:183]\nds_read_b64_tr_b16 v[222:223],v227 offset:12288\nds_read_b64_tr_b16 v[192:193],v226 offset:512\nM v[180:183],a[60:63],v[208:211],v[180:183]\nds_read_b64_tr_b16 v[194:195],v226 offset:4608\ns_waitcnt lgkmcnt(6)\n" \
"M v[184:187],a[48:51],v[196:199],v[184:187]\nbuffer_load_dwordx4 a[68:71],v240,s[52:55],s65 offen offset:1024 sc1\nM v[184:187],a[56:59],v[212:215],v[184:187]\nds_read_b64_tr_b16 v[208:209],v226 offset:8704\nds_read_b64_tr_b16 v[210:211],v226 offset:12800\nM v[188:191],a[52:55],v[196:199],v[188:191]\nds_read_b64_tr_b16 v[196:197],v227 offset:512\nds_read_b64_tr_b16 v[198:199],v227 offset:4608\n" \
"M v[188:191],a[60:63],v[212:215],v[188:191]\nds_read_b64_tr_b16 v[212:213],v227 offset:8704\nds_read_b64_tr_b16 v[214:215],v227 offset:12800\nM v[8:11],v[0:3],v[200:203],0\nbuffer_load_dwordx4 a[72:75],v240,s[52:55],s65 offen offset:2048 sc1\nM v[8:11],v[16:19],v[204:207],v[8:11]\nv_cvt_pk_bf16_f32 v160,v160,v168\nv_cvt_pk_bf16_f32 v176,v176,v184\n" \
"M v[12:15],v[4:7],v[200:203],0\nds_read_b64_tr_b16 v[200:201],v226 offset:1024\nds_read_b64_tr_b16 v[202:203],v226 offset:5120\nM v[12:15],v[20:23],v[204:207],v[12:15]\nds_read_b64_tr_b16 v[204:205],v226 offset:9216\ns_waitcnt lgkmcnt(9)\nM v[24:27],v[0:3],v[216:219],0\nbuffer_load_dwordx4 a[76:79],v240,s[52:55],s65 offen offset:3072 sc1\n" \
"M v[24:27],v[16:19],v[220:223],v[24:27]\nds_read_b64_tr_b16 v[206:207],v226 offset:13312\ns_add_u32 s65,s65,4096\nM v[28:31],v[4:7],v[216:219],0\nds_read_b64_tr_b16 v[216:217],v227 offset:1024\nds_read_b64_tr_b16 v[218:219],v227 offset:5120\nM v[28:31],v[20:23],v[220:223],v[28:31]\nds_read_b64_tr_b16 v[220:221],v227 offset:9216\n" \
"ds_read_b64_tr_b16 v[222:223],v227 offset:13312\nM v[40:43],v[0:3],v[192:195],0\nbuffer_load_dwordx4 a[80:83],v240,s[52:55],s65 offen offset:0 sc1\ns_waitcnt lgkmcnt(8)\nM v[40:43],v[16:19],v[208:211],v[40:43]\nv_min_u32 v138,-2,v160\nv_min_u32 v139,-2,v176\nM v[44:47],v[4:7],v[192:195],0\n" \
"ds_read_b64_tr_b16 v[192:193],v226 offset:1536\nds_read_b64_tr_b16 v[194:195],v226 offset:5632\nM v[44:47],v[20:23],v[208:211],v[44:47]\nds_read_b64_tr_b16 v[208:209],v226 offset:9728\nds_read_b64_tr_b16 v[210:211],v226 offset:13824\nM v[56:59],v[0:3],v[196:199],0\nbuffer_load_dwordx4 a[84:87],v240,s[52:55],s65 offen offset:1024 sc1\nM v[56:59],v[16:19],v[212:215],v[56:59]\n" \
"v_cvt_pk_bf16_f32 v161,v161,v169\nv_cvt_pk_bf16_f32 v177,v177,v185\nM v[60:63],v[4:7],v[196:199],0\nbuffer_store_dwordx4 v[136:139],v253,s[52:55],s33 offen offset:0 sc1\nM v[60:63],v[20:23],v[212:215],v[60:63]\nds_read_b64_tr_b16 v[196:197],v227 offset:1536\ns_waitcnt lgkmcnt(5)\nM v[72:75],v[0:3],v[200:203],0\n" \
"buffer_load_dwordx4 a[88:91],v240,s[52:55],s65 offen offset:2048 sc1\nM v[72:75],v[16:19],v[204:207],v[72:75]\nds_read_b64_tr_b16 v[198:199],v227 offset:5632\nds_read_b64_tr_b16 v[212:213],v227 offset:9728\nM v[76:79],v[4:7],v[200:203],0\nds_read_b64_tr_b16 v[214:215],v227 offset:13824\nds_read_b64_tr_b16 v[200:201],v226 offset:16384\nM v[76:79],v[20:23],v[204:207],v[76:79]\n" \
"ds_read_b64_tr_b16 v[202:203],v226 offset:20480\nds_read_b64_tr_b16 v[204:205],v226 offset:24576\nM v[88:91],v[0:3],v[216:219],0\nbuffer_load_dwordx4 a[92:95],v240,s[52:55],s65 offen offset:3072 sc1\nM v[88:91],v[16:19],v[220:223],v[88:91]\nds_read_b64_tr_b16 v[206:207],v226 offset:28672\ns_add_u32 s65,s11,s67\nM v[92:95],v[4:7],v[216:219],0\n" \
"ds_read_b64_tr_b16 v[216:217],v227 offset:16384\nds_read_b64_tr_b16 v[218:219],v227 offset:20480\nM v[92:95],v[20:23],v[220:223],v[92:95]\nds_read_b64_tr_b16 v[220:221],v227 offset:24576\ns_waitcnt lgkmcnt(10)\nM v[104:107],v[0:3],v[192:195],0\nbuffer_load_dwordx4 a[96:99],v240,s[52:55],s65 offen offset:0 sc1\nM v[104:107],v[16:19],v[208:211],v[104:107]\n" \
"ds_read_b64_tr_b16 v[222:223],v227 offset:28672\nv_min_u32 v153,-2,v145\nM v[108:111],v[4:7],v[192:195],0\nds_read_b64_tr_b16 v[192:193],v226 offset:16896\nds_read_b64_tr_b16 v[194:195],v226 offset:20992\nM v[108:111],v[20:23],v[208:211],v[108:111]\nv_min_u32 v154,-2,v161\ns_waitcnt lgkmcnt(7)\n" \
"M v[120:123],v[0:3],v[196:199],0\nbuffer_load_dwordx4 a[100:103],v240,s[52:55],s65 offen offset:1024 sc1\nM v[120:123],v[16:19],v[212:215],v[120:123]\nv_min_u32 v155,-2,v177\nds_read_b64_tr_b16 v[208:209],v226 offset:25088\nM v[124:127],v[4:7],v[196:199],0\nds_read_b64_tr_b16 v[210:211],v226 offset:29184\nbuffer_store_dwordx4 v[152:155],v253,s[52:55],s33 offen offset:1024 sc1\n" \
"M v[124:127],v[20:23],v[212:215],v[124:127]\nds_read_b64_tr_b16 v[196:197],v227 offset:16896\nds_read_b64_tr_b16 v[198:199],v227 offset:20992\nM v[8:11],v[32:35],v[200:203],v[8:11]\nbuffer_load_dwordx4 a[104:107],v240,s[52:55],s65 offen offset:2048 sc1\ns_waitcnt lgkmcnt(6)\nM v[8:11],v[48:51],v[204:207],v[8:11]\nds_read_b64_tr_b16 v[212:213],v227 offset:25088\n" \
"ds_read_b64_tr_b16 v[214:215],v227 offset:29184\nM v[12:15],v[36:39],v[200:203],v[12:15]\nds_read_b64_tr_b16 v[200:201],v226 offset:17408\nds_read_b64_tr_b16 v[202:203],v226 offset:21504\nM v[12:15],v[52:55],v[204:207],v[12:15]\nds_read_b64_tr_b16 v[204:205],v226 offset:25600\nds_read_b64_tr_b16 v[206:207],v226 offset:29696\nM v[24:27],v[32:35],v[216:219],v[24:27]\n" \
"buffer_load_dwordx4 a[108:111],v240,s[52:55],s65 offen offset:3072 sc1\nM v[24:27],v[48:51],v[220:223],v[24:27]\ns_add_u32 s65,s65,4096\nv_cvt_pk_bf16_f32 v162,v162,v170\nM v[28:31],v[36:39],v[216:219],v[28:31]\nds_read_b64_tr_b16 v[216:217],v227 offset:17408\nds_read_b64_tr_b16 v[218:219],v227 offset:21504\nM v[28:31],v[52:55],v[220:223],v[28:31]\n" \
"ds_read_b64_tr_b16 v[220:221],v227 offset:25600\ns_waitcnt lgkmcnt(9)\nM v[40:43],v[32:35],v[192:195],v[40:43]\nbuffer_load_dwordx4 a[112:115],v240,s[52:55],s65 offen offset:0 sc1\nM v[40:43],v[48:51],v[208:211],v[40:43]\nds_read_b64_tr_b16 v[222:223],v227 offset:29696\nv_cvt_pk_bf16_f32 v163,v163,v171\nM v[44:47],v[36:39],v[192:195],v[44:47]\n" \
"ds_read_b64_tr_b16 v[192:193],v226 offset:17920\nds_read_b64_tr_b16 v[194:195],v226 offset:22016\nM v[44:47],v[52:55],v[208:211],v[44:47]\nv_cvt_pk_bf16_f32 v178,v178,v186\nv_min_u32 v168,-2,v130\nM v[56:59],v[32:35],v[196:199],v[56:59]\nbuffer_load_dwordx4 a[116:119],v240,s[52:55],s65 offen offset:1024 sc1\ns_waitcnt lgkmcnt(6)\n" \
"M v[56:59],v[48:51],v[212:215],v[56:59]\nv_min_u32 v169,-2,v146\nv_min_u32 v170,-2,v162\nM v[60:63],v[36:39],v[196:199],v[60:63]\nv_min_u32 v171,-2,v178\nds_read_b64_tr_b16 v[208:209],v226 offset:26112\nds_read_b64_tr_b16 v[210:211],v226 offset:30208\nbuffer_store_dwordx4 v[168:171],v253,s[52:55],s33 offen offset:2048 sc1\n" \
"M v[60:63],v[52:55],v[212:215],v[60:63]\nds_read_b64_tr_b16 v[196:197],v227 offset:17920\nds_read_b64_tr_b16 v[198:199],v227 offset:22016\nM v[72:75],v[32:35],v[200:203],v[72:75]\nbuffer_load_dwordx4 a[120:123],v240,s[52:55],s65 offen offset:2048 sc1\nM v[72:75],v[48:51],v[204:207],v[72:75]\nds_read_b64_tr_b16 v[212:213],v227 offset:26112\nds_read_b64_tr_b16 v[214:215],v227 offset:30208\n" \
"M v[76:79],v[36:39],v[200:203],v[76:79]\nv_add_u32 v226,0,v242\nds_read_b64_tr_b16 v[200:201],v226 offset:0\nM v[76:79],v[52:55],v[204:207],v[76:79]\nds_read_b64_tr_b16 v[202:203],v226 offset:4096\ns_waitcnt lgkmcnt(8)\nM v[88:91],v[32:35],v[216:219],v[88:91]\nbuffer_load_dwordx4 a[124:127],v240,s[52:55],s65 offen offset:3072 sc1\n" \
"M v[88:91],v[48:51],v[220:223],v[88:91]\nds_read_b64_tr_b16 v[204:205],v226 offset:8192\nds_read_b64_tr_b16 v[206:207],v226 offset:12288\nM v[92:95],v[36:39],v[216:219],v[92:95]\ns_add_u32 s65,s14,s67\nv_xor_b32 v227,32,v226\nM v[92:95],v[52:55],v[220:223],v[92:95]\nds_read_b64_tr_b16 v[216:217],v227 offset:0\n" \
"ds_read_b64_tr_b16 v[218:219],v227 offset:4096\nM v[104:107],v[32:35],v[192:195],v[104:107]\nbuffer_load_dwordx4 a[128:131],v240,s[52:55],s65 offen offset:0 sc1\ns_waitcnt lgkmcnt(6)\nM v[104:107],v[48:51],v[208:211],v[104:107]\nds_read_b64_tr_b16 v[220:221],v227 offset:8192\nds_read_b64_tr_b16 v[222:223],v227 offset:12288\nM v[108:111],v[36:39],v[192:195],v[108:111]\n" \
"v_cvt_pk_bf16_f32 v179,v179,v187\nds_read_b64_tr_b16 v[192:193],v226 offset:512\nM v[108:111],v[52:55],v[208:211],v[108:111]\nds_read_b64_tr_b16 v[194:195],v226 offset:4608\nv_min_u32 v184,-2,v131\nM v[120:123],v[32:35],v[196:199],v[120:123]\nbuffer_load_dwordx4 a[132:135],v240,s[52:55],s65 offen offset:1024 sc1\nM v[120:123],v[48:51],v[212:215],v[120:123]\n" \
"v_min_u32 v185,-2,v147\nv_min_u32 v186,-2,v163\nM v[124:127],v[36:39],v[196:199],v[124:127]\nv_min_u32 v187,-2,v179\nbuffer_store_dwordx4 v[184:187],v253,s[52:55],s33 offen offset:3072 sc1\nds_read_b64_tr_b16 v[208:209],v226 offset:8704\nds_read_b64_tr_b16 v[210:211],v226 offset:12800\nM v[124:127],v[52:55],v[212:215],v[124:127]\n" \
"ds_read_b64_tr_b16 v[196:197],v227 offset:512\ns_waitcnt vmcnt(18) lgkmcnt(7)\nM v[8:11],a[64:67],v[200:203],v[8:11]\nbuffer_load_dwordx4 a[136:139],v240,s[52:55],s65 offen offset:2048 sc1\nM v[8:11],a[72:75],v[204:207],v[8:11]\nds_read_b64_tr_b16 v[198:199],v227 offset:4608\nds_read_b64_tr_b16 v[212:213],v227 offset:8704\nM v[12:15],a[68:71],v[200:203],v[12:15]\n" \
"ds_read_b64_tr_b16 v[214:215],v227 offset:12800\nds_read_b64_tr_b16 v[200:201],v226 offset:1024\nM v[12:15],a[76:79],v[204:207],v[12:15]\nds_read_b64_tr_b16 v[202:203],v226 offset:5120\nds_read_b64_tr_b16 v[204:205],v226 offset:9216\nM v[24:27],a[64:67],v[216:219],v[24:27]\nbuffer_load_dwordx4 a[140:143],v240,s[52:55],s65 offen offset:3072 sc1\ns_waitcnt lgkmcnt(9)\n" \
"M v[24:27],a[72:75],v[220:223],v[24:27]\nds_read_b64_tr_b16 v[206:207],v226 offset:13312\ns_add_u32 s65,s65,4096\nM v[28:31],a[68:71],v[216:219],v[28:31]\nds_read_b64_tr_b16 v[216:217],v227 offset:1024\nds_read_b64_tr_b16 v[218:219],v227 offset:5120\nM v[28:31],a[76:79],v[220:223],v[28:31]\nds_read_b64_tr_b16 v[220:221],v227 offset:9216\n" \
"ds_read_b64_tr_b16 v[222:223],v227 offset:13312\nM v[40:43],a[64:67],v[192:195],v[40:43]\nbuffer_load_dwordx4 a[144:147],v240,s[52:55],s65 offen offset:0 sc1\ns_waitcnt lgkmcnt(9)\nM v[40:43],a[72:75],v[208:211],v[40:43]\nv_cvt_pk_bf16_f32 v132,v132,v140\nv_cvt_pk_bf16_f32 v133,v133,v141\nM v[44:47],a[68:71],v[192:195],v[44:47]\n" \
"v_cvt_pk_bf16_f32 v134,v134,v142\nv_cvt_pk_bf16_f32 v135,v135,v143\nM v[44:47],a[76:79],v[208:211],v[44:47]\nv_cvt_pk_bf16_f32 v148,v148,v156\nv_cvt_pk_bf16_f32 v164,v164,v172\nM v[56:59],a[64:67],v[196:199],v[56:59]\nbuffer_load_dwordx4 a[148:151],v240,s[52:55],s65 offen offset:1024 sc1\ns_waitcnt lgkmcnt(4)\n" \
"M v[56:59],a[72:75],v[212:215],v[56:59]\nv_cvt_pk_bf16_f32 v180,v180,v188\nds_read_b64_tr_b16 v[192:193],v226 offset:1536\nds_read_b64_tr_b16 v[194:195],v226 offset:5632\nM v[60:63],a[68:71],v[196:199],v[60:63]\nv_min_u32 v140,-2,v132\nv_min_u32 v141,-2,v148\nv_min_u32 v142,-2,v164\n" \
"v_min_u32 v143,-2,v180\nbuffer_store_dwordx4 v[140:143],v253,s[52:55],s38 offen offset:0 sc1\nds_read_b64_tr_b16 v[208:209],v226 offset:9728\nds_read_b64_tr_b16 v[210:211],v226 offset:13824\nM v[60:63],a[76:79],v[212:215],v[60:63]\nds_read_b64_tr_b16 v[196:197],v227 offset:1536\nds_read_b64_tr_b16 v[198:199],v227 offset:5632\nM v[72:75],a[64:67],v[200:203],v[72:75]\n" \
"buffer_load_dwordx4 a[152:155],v240,s[52:55],s65 offen offset:2048 sc1\nM v[72:75],a[72:75],v[204:207],v[72:75]\nds_read_b64_tr_b16 v[212:213],v227 offset:9728\nds_read_b64_tr_b16 v[214:215],v227 offset:13824\nM v[76:79],a[68:71],v[200:203],v[76:79]\nds_read_b64_tr_b16 v[200:201],v226 offset:32768\nds_read_b64_tr_b16 v[202:203],v226 offset:36864\nM v[76:79],a[76:79],v[204:207],v[76:79]\n" \
"ds_read_b64_tr_b16 v[204:205],v226 offset:40960\ns_waitcnt lgkmcnt(9)\nM v[88:91],a[64:67],v[216:219],v[88:91]\nbuffer_load_dwordx4 a[156:159],v240,s[52:55],s65 offen offset:3072 sc1\nM v[88:91],a[72:75],v[220:223],v[88:91]\nds_read_b64_tr_b16 v[206:207],v226 offset:45056\nv_cvt_pk_bf16_f32 v149,v149,v157\nM v[92:95],a[68:71],v[216:219],v[92:95]\n" \
"ds_read_b64_tr_b16 v[216:217],v227 offset:32768\nds_read_b64_tr_b16 v[218:219],v227 offset:36864\nM v[92:95],a[76:79],v[220:223],v[92:95]\nds_read_b64_tr_b16 v[220:221],v227 offset:40960\nds_read_b64_tr_b16 v[222:223],v227 offset:45056\nM v[104:107],a[64:67],v[192:195],v[104:107]\nv_cvt_pk_bf16_f32 v150,v150,v158\ns_waitcnt lgkmcnt(9)\n" \
"M v[104:107],a[72:75],v[208:211],v[104:107]\nv_cvt_pk_bf16_f32 v151,v151,v159\nv_cvt_pk_bf16_f32 v165,v165,v173\nM v[108:111],a[68:71],v[192:195],v[108:111]\nv_cvt_pk_bf16_f32 v181,v181,v189\nds_read_b64_tr_b16 v[192:193],v226 offset:33280\nM v[108:111],a[76:79],v[208:211],v[108:111]\nds_read_b64_tr_b16 v[194:195],v226 offset:37376\n" \
"v_min_u32 v156,-2,v133\nM v[120:123],a[64:67],v[196:199],v[120:123]\nv_min_u32 v157,-2,v149\ns_waitcnt lgkmcnt(6)\nM v[120:123],a[72:75],v[212:215],v[120:123]\nv_min_u32 v158,-2,v165\nv_min_u32 v159,-2,v181\nM v[124:127],a[68:71],v[196:199],v[124:127]\n" \
"buffer_store_dwordx4 v[156:159],v253,s[52:55],s38 offen offset:1024 sc1\nds_read_b64_tr_b16 v[208:209],v226 offset:41472\nds_read_b64_tr_b16 v[210:211],v226 offset:45568\nM v[124:127],a[76:79],v[212:215],v[124:127]\nds_read_b64_tr_b16 v[196:197],v227 offset:33280\ns_waitcnt vmcnt(24)\nM v[8:11],a[80:83],v[200:203],v[8:11]\nds_read_b64_tr_b16 v[198:199],v227 offset:37376\n" \
"s_waitcnt vmcnt(19)\nM v[8:11],a[88:91],v[204:207],v[8:11]\nds_read_b64_tr_b16 v[212:213],v227 offset:41472\nds_read_b64_tr_b16 v[214:215],v227 offset:45568\nM v[12:15],a[84:87],v[200:203],v[12:15]\nds_read_b64_tr_b16 v[200:201],v226 offset:33792\nds_read_b64_tr_b16 v[202:203],v226 offset:37888\nM v[12:15],a[92:95],v[204:207],v[12:15]\n" \
"ds_read_b64_tr_b16 v[204:205],v226 offset:41984\ns_waitcnt lgkmcnt(9)\nM v[24:27],a[80:83],v[216:219],v[24:27]\nds_read_b64_tr_b16 v[206:207],v226 offset:46080\nv_cvt_pk_bf16_f32 v166,v166,v174\nM v[24:27],a[88:91],v[220:223],v[24:27]\nv_cvt_pk_bf16_f32 v167,v167,v175\nv_cvt_pk_bf16_f32 v182,v182,v190\n" \
"M v[28:31],a[84:87],v[216:219],v[28:31]\nds_read_b64_tr_b16 v[216:217],v227 offset:33792\nds_read_b64_tr_b16 v[218:219],v227 offset:37888\nM v[28:31],a[92:95],v[220:223],v[28:31]\nds_read_b64_tr_b16 v[220:221],v227 offset:41984\nds_read_b64_tr_b16 v[222:223],v227 offset:46080\nM v[40:43],a[80:83],v[192:195],v[40:43]\nv_min_u32 v172,-2,v134\n" \
"s_waitcnt lgkmcnt(9)\nM v[40:43],a[88:91],v[208:211],v[40:43]\nv_min_u32 v173,-2,v150\nv_min_u32 v174,-2,v166\nM v[44:47],a[84:87],v[192:195],v[44:47]\nds_read_b64_tr_b16 v[192:193],v226 offset:34304\nds_read_b64_tr_b16 v[194:195],v226 offset:38400\nM v[44:47],a[92:95],v[208:211],v[44:47]\n" \
"v_min_u32 v175,-2,v182\nds_read_b64_tr_b16 v[208:209],v226 offset:42496\nM v[56:59],a[80:83],v[196:199],v[56:59]\nds_read_b64_tr_b16 v[210:211],v226 offset:46592\ns_waitcnt lgkmcnt(8)\nM v[56:59],a[88:91],v[212:215],v[56:59]\nv_cvt_pk_bf16_f32 v183,v183,v191\nv_min_u32 v188,-2,v135\n" \
"M v[60:63],a[84:87],v[196:199],v[60:63]\nbuffer_store_dwordx4 v[172:175],v253,s[52:55],s38 offen offset:2048 sc1\nM v[60:63],a[92:95],v[212:215],v[60:63]\nds_read_b64_tr_b16 v[196:197],v227 offset:34304\nds_read_b64_tr_b16 v[198:199],v227 offset:38400\nM v[72:75],a[80:83],v[200:203],v[72:75]\nds_read_b64_tr_b16 v[212:213],v227 offset:42496\nds_read_b64_tr_b16 v[214:215],v227 offset:46592\n" \
"M v[72:75],a[88:91],v[204:207],v[72:75]\nv_min_u32 v189,-2,v151\nv_min_u32 v190,-2,v167\nM v[76:79],a[84:87],v[200:203],v[76:79]\nds_read_b64_tr_b16 v[200:201],v226 offset:16384\nds_read_b64_tr_b16 v[202:203],v226 offset:20480\nM v[76:79],a[92:95],v[204:207],v[76:79]\nds_read_b64_tr_b16 v[204:205],v226 offset:24576\n" \
"s_waitcnt lgkmcnt(7)\nM v[88:91],a[80:83],v[216:219],v[88:91]\nds_read_b64_tr_b16 v[206:207],v226 offset:28672\nv_min_u32 v191,-2,v183\nM v[88:91],a[88:91],v[220:223],v[88:91]\nv_cmp_eq_u32 vcc,v228,v229\nv_and_b32 v228,1,v249\nM v[92:95],a[84:87],v[216:219],v[92:95]\n" \
"ds_read_b64_tr_b16 v[216:217],v227 offset:16384\nds_read_b64_tr_b16 v[218:219],v227 offset:20480\nM v[92:95],a[92:95],v[220:223],v[92:95]\nds_read_b64_tr_b16 v[220:221],v227 offset:24576\nds_read_b64_tr_b16 v[222:223],v227 offset:28672\nM v[104:107],a[80:83],v[192:195],v[104:107]\nv_lshlrev_b32 v228,4,v228\nv_mov_b32 v229,0x3f80\n" \
"M v[104:107],a[88:91],v[208:211],v[104:107]\nv_lshlrev_b32 v228,v228,v229\nv_cndmask_b32 v228,0,v228,vcc\nM v[108:111],a[84:87],v[192:195],v[108:111]\nds_read_b64_tr_b16 v[192:193],v226 offset:16896\nds_read_b64_tr_b16 v[194:195],v226 offset:20992\nM v[108:111],a[92:95],v[208:211],v[108:111]\nds_read_b64_tr_b16 v[208:209],v226 offset:25088\n" \
"s_waitcnt lgkmcnt(7)\nM v[120:123],a[80:83],v[196:199],v[120:123]\nds_read_b64_tr_b16 v[210:211],v226 offset:29184\nv_and_b32 v229,2,v249\nM v[120:123],a[88:91],v[212:215],v[120:123]\nv_cmp_eq_u32 vcc,0,v229\nv_and_b32 v244,15,v249\nM v[124:127],a[84:87],v[196:199],v[124:127]\n" \
"buffer_store_dwordx4 v[188:191],v253,s[52:55],s38 offen offset:3072 sc1\nM v[124:127],a[92:95],v[212:215],v[124:127]\nds_read_b64_tr_b16 v[196:197],v227 offset:16896\nds_read_b64_tr_b16 v[198:199],v227 offset:20992\nM v[8:11],a[96:99],v[200:203],v[8:11]\nds_read_b64_tr_b16 v[212:213],v227 offset:25088\ns_waitcnt vmcnt(16)\nM v[8:11],a[104:107],v[204:207],v[8:11]\n" \
"ds_read_b64_tr_b16 v[214:215],v227 offset:29184\nv_cndmask_b32 v229,0,v228,vcc\nM v[12:15],a[100:103],v[200:203],v[12:15]\nds_read_b64_tr_b16 v[200:201],v226 offset:17408\nds_read_b64_tr_b16 v[202:203],v226 offset:21504\nM v[12:15],a[108:111],v[204:207],v[12:15]\nds_read_b64_tr_b16 v[204:205],v226 offset:25600\ns_waitcnt lgkmcnt(7)\n" \
"M v[24:27],a[96:99],v[216:219],v[24:27]\nds_read_b64_tr_b16 v[206:207],v226 offset:29696\nv_and_b32 v245,0x300,v240\nM v[24:27],a[104:107],v[220:223],v[24:27]\nv_lshlrev_b32 v246,11,v249\nv_cmp_lt_u32 vcc,7,v249\nM v[28:31],a[100:103],v[216:219],v[28:31]\nds_read_b64_tr_b16 v[216:217],v227 offset:17408\n" \
"ds_read_b64_tr_b16 v[218:219],v227 offset:21504\nM v[28:31],a[108:111],v[220:223],v[28:31]\nds_read_b64_tr_b16 v[220:221],v227 offset:25600\nds_read_b64_tr_b16 v[222:223],v227 offset:29696\nM v[40:43],a[96:99],v[192:195],v[40:43]\nv_mov_b32 v230,0x40000000\nv_lshl_or_b32 v244,v244,1,v245\nM v[40:43],a[104:107],v[208:211],v[40:43]\n" \
"s_add_u32 s70,s64,114688\nv_cndmask_b32 v246,v246,v230,vcc\nM v[44:47],a[100:103],v[192:195],v[44:47]\nds_read_b64_tr_b16 v[192:193],v226 offset:17920\nds_read_b64_tr_b16 v[194:195],v226 offset:22016\nM v[44:47],a[108:111],v[208:211],v[44:47]\nds_read_b64_tr_b16 v[208:209],v226 offset:26112\ns_waitcnt lgkmcnt(7)\n" \
"M v[56:59],a[96:99],v[196:199],v[56:59]\nds_read_b64_tr_b16 v[210:211],v226 offset:30208\nv_add_u32 v244,s70,v244\nM v[56:59],a[104:107],v[212:215],v[56:59]\nv_add_u32 v245,s70,v240\ns_lshl_b32 s80,s60,11\nM v[60:63],a[100:103],v[196:199],v[60:63]\nds_read_b64_tr_b16 v[196:197],v227 offset:17920\n" \
"ds_read_b64_tr_b16 v[198:199],v227 offset:22016\nM v[60:63],a[108:111],v[212:215],v[60:63]\nds_read_b64_tr_b16 v[212:213],v227 offset:26112\nds_read_b64_tr_b16 v[214:215],v227 offset:30208\nM v[72:75],a[96:99],v[200:203],v[72:75]\nv_and_b32 v230,3,v249\nv_lshrrev_b32 v231,2,v249\nM v[72:75],a[104:107],v[204:207],v[72:75]\n" \
"v_accvgpr_read_b32 v32,a176\nv_accvgpr_read_b32 v33,a177\nM v[76:79],a[100:103],v[200:203],v[76:79]\nds_read_b64_tr_b16 v[200:201],v226 offset:49152\nds_read_b64_tr_b16 v[202:203],v226 offset:53248\nM v[76:79],a[108:111],v[204:207],v[76:79]\nds_read_b64_tr_b16 v[204:205],v226 offset:57344\ns_waitcnt lgkmcnt(7)\n" \
"M v[88:91],a[96:99],v[216:219],v[88:91]\nds_read_b64_tr_b16 v[206:207],v226 offset:61440\nv_accvgpr_read_b32 v34,a178\nM v[88:91],a[104:107],v[220:223],v[88:91]\nv_accvgpr_read_b32 v35,a179\nv_accvgpr_read_b32 v36,a180\nM v[92:95],a[100:103],v[216:219],v[92:95]\nds_read_b64_tr_b16 v[216:217],v227 offset:49152\n" \
"ds_read_b64_tr_b16 v[218:219],v227 offset:53248\nM v[92:95],a[108:111],v[220:223],v[92:95]\nds_read_b64_tr_b16 v[220:221],v227 offset:57344\nds_read_b64_tr_b16 v[222:223],v227 offset:61440\nM v[104:107],a[96:99],v[192:195],v[104:107]\nv_accvgpr_read_b32 v37,a181\nv_accvgpr_read_b32 v38,a182\nM v[104:107],a[104:107],v[208:211],v[104:107]\n" \
"v_accvgpr_read_b32 v39,a183\ns_lshl3_add_u32 s37,s6,s75\nM v[108:111],a[100:103],v[192:195],v[108:111]\nds_read_b64_tr_b16 v[192:193],v226 offset:49664\nds_read_b64_tr_b16 v[194:195],v226 offset:53760\nM v[108:111],a[108:111],v[208:211],v[108:111]\nds_read_b64_tr_b16 v[208:209],v226 offset:57856\ns_waitcnt lgkmcnt(7)\n" \
"M v[120:123],a[96:99],v[196:199],v[120:123]\nds_read_b64_tr_b16 v[210:211],v226 offset:61952\ns_add_u32 s80,s80,557056\nM v[120:123],a[104:107],v[212:215],v[120:123]\nv_lshl_add_u32 v231,v230,5,v231\ns_lshl_b32 s70,s7,8\nM v[124:127],a[100:103],v[196:199],v[124:127]\nds_read_b64_tr_b16 v[196:197],v227 offset:49664\n" \
"ds_read_b64_tr_b16 v[198:199],v227 offset:53760\nM v[124:127],a[108:111],v[212:215],v[124:127]\nds_read_b64_tr_b16 v[212:213],v227 offset:57856\nds_read_b64_tr_b16 v[214:215],v227 offset:61952\nM v[8:11],a[112:115],v[200:203],v[8:11]\nv_add_f32 v32,1.0,v32\ns_waitcnt vmcnt(11)\nM v[8:11],a[120:123],v[204:207],v[8:11]\n" \
"v_add_f32 v33,1.0,v33\nv_add_f32 v34,1.0,v34\nM v[12:15],a[116:119],v[200:203],v[12:15]\nds_read_b64_tr_b16 v[200:201],v226 offset:50176\nds_read_b64_tr_b16 v[202:203],v226 offset:54272\nM v[12:15],a[124:127],v[204:207],v[12:15]\nds_read_b64_tr_b16 v[204:205],v226 offset:58368\ns_waitcnt lgkmcnt(7)\n" \
"M v[24:27],a[112:115],v[216:219],v[24:27]\nds_read_b64_tr_b16 v[206:207],v226 offset:62464\nv_add_f32 v35,1.0,v35\nM v[24:27],a[120:123],v[220:223],v[24:27]\nv_add_f32 v36,1.0,v36\nv_add_f32 v37,1.0,v37\nM v[28:31],a[116:119],v[216:219],v[28:31]\nds_read_b64_tr_b16 v[216:217],v227 offset:50176\n" \
"ds_read_b64_tr_b16 v[218:219],v227 offset:54272\nM v[28:31],a[124:127],v[220:223],v[28:31]\nds_read_b64_tr_b16 v[220:221],v227 offset:58368\nds_read_b64_tr_b16 v[222:223],v227 offset:62464\nM v[40:43],a[112:115],v[192:195],v[40:43]\nv_add_f32 v38,1.0,v38\nv_add_f32 v39,1.0,v39\nM v[40:43],a[120:123],v[208:211],v[40:43]\n" \
"v_add_f32 v116,1.0,v116\nv_add_f32 v117,1.0,v117\nM v[44:47],a[116:119],v[192:195],v[44:47]\nds_read_b64_tr_b16 v[192:193],v226 offset:50688\nds_read_b64_tr_b16 v[194:195],v226 offset:54784\nM v[44:47],a[124:127],v[208:211],v[44:47]\nds_read_b64_tr_b16 v[208:209],v226 offset:58880\ns_waitcnt lgkmcnt(7)\n" \
"M v[56:59],a[112:115],v[196:199],v[56:59]\nds_read_b64_tr_b16 v[210:211],v226 offset:62976\nv_add_u32 v226,65536,v242\nM v[56:59],a[120:123],v[212:215],v[56:59]\ns_add_u32 s37,s37,557056\nv_lshlrev_b32 v251,5,v249\nM v[60:63],a[116:119],v[196:199],v[60:63]\nds_read_b64_tr_b16 v[196:197],v227 offset:50688\n" \
"ds_read_b64_tr_b16 v[198:199],v227 offset:54784\nM v[60:63],a[124:127],v[212:215],v[60:63]\nds_read_b64_tr_b16 v[212:213],v227 offset:58880\nds_read_b64_tr_b16 v[214:215],v227 offset:62976\nM v[72:75],a[112:115],v[200:203],v[72:75]\nv_xor_b32 v227,32,v226\ns_or_b32 s80,s80,s75\nM v[72:75],a[120:123],v[204:207],v[72:75]\n" \
"v_lshl_add_u32 v248,v231,1,s70\nv_accvgpr_read_b32 v0,a160\nM v[76:79],a[116:119],v[200:203],v[76:79]\nds_read_b64_tr_b16 v[200:201],v226 offset:0\nds_read_b64_tr_b16 v[202:203],v226 offset:4096\nM v[76:79],a[124:127],v[204:207],v[76:79]\nds_read_b64_tr_b16 v[204:205],v226 offset:8192\ns_waitcnt lgkmcnt(7)\n" \
"M v[88:91],a[112:115],v[216:219],v[88:91]\nds_read_b64_tr_b16 v[206:207],v226 offset:12288\nv_accvgpr_read_b32 v1,a161\nM v[88:91],a[120:123],v[220:223],v[88:91]\nv_accvgpr_read_b32 v2,a162\nv_accvgpr_read_b32 v3,a163\nM v[92:95],a[116:119],v[216:219],v[92:95]\nds_read_b64_tr_b16 v[216:217],v227 offset:0\n" \
"ds_read_b64_tr_b16 v[218:219],v227 offset:4096\nM v[92:95],a[124:127],v[220:223],v[92:95]\nds_read_b64_tr_b16 v[220:221],v227 offset:8192\nds_read_b64_tr_b16 v[222:223],v227 offset:12288\nM v[104:107],a[112:115],v[192:195],v[104:107]\nv_accvgpr_read_b32 v4,a164\nv_accvgpr_read_b32 v5,a165\nM v[104:107],a[120:123],v[208:211],v[104:107]\n" \
"v_accvgpr_read_b32 v6,a166\nv_accvgpr_read_b32 v7,a167\nM v[108:111],a[116:119],v[192:195],v[108:111]\nds_read_b64_tr_b16 v[192:193],v226 offset:512\nds_read_b64_tr_b16 v[194:195],v226 offset:4608\nM v[108:111],a[124:127],v[208:211],v[108:111]\nds_read_b64_tr_b16 v[208:209],v226 offset:8704\ns_waitcnt lgkmcnt(7)\n" \
"M v[120:123],a[112:115],v[196:199],v[120:123]\nds_read_b64_tr_b16 v[210:211],v226 offset:12800\nv_accvgpr_read_b32 v16,a168\nM v[120:123],a[120:123],v[212:215],v[120:123]\nv_accvgpr_read_b32 v17,a169\nv_accvgpr_read_b32 v18,a170\nM v[124:127],a[116:119],v[196:199],v[124:127]\nds_read_b64_tr_b16 v[196:197],v227 offset:512\n" \
"ds_read_b64_tr_b16 v[198:199],v227 offset:4608\nM v[124:127],a[124:127],v[212:215],v[124:127]\nds_read_b64_tr_b16 v[212:213],v227 offset:8704\nds_read_b64_tr_b16 v[214:215],v227 offset:12800\nM v[8:11],a[128:131],v[200:203],v[8:11]\nv_accvgpr_read_b32 v19,a171\ns_waitcnt vmcnt(6)\nM v[8:11],a[136:139],v[204:207],v[8:11]\n" \
"v_accvgpr_read_b32 v20,a172\nv_accvgpr_read_b32 v21,a173\nM v[12:15],a[132:135],v[200:203],v[12:15]\nds_read_b64_tr_b16 v[200:201],v226 offset:1024\nds_read_b64_tr_b16 v[202:203],v226 offset:5120\nM v[12:15],a[140:143],v[204:207],v[12:15]\nds_read_b64_tr_b16 v[204:205],v226 offset:9216\ns_waitcnt lgkmcnt(7)\n" \
"M v[24:27],a[128:131],v[216:219],v[24:27]\nds_read_b64_tr_b16 v[206:207],v226 offset:13312\nv_accvgpr_read_b32 v22,a174\nM v[24:27],a[136:139],v[220:223],v[24:27]\nv_accvgpr_read_b32 v23,a175\nv_accvgpr_read_b32 v48,a184\nM v[28:31],a[132:135],v[216:219],v[28:31]\nds_read_b64_tr_b16 v[216:217],v227 offset:1024\n" \
"ds_read_b64_tr_b16 v[218:219],v227 offset:5120\nM v[28:31],a[140:143],v[220:223],v[28:31]\nds_read_b64_tr_b16 v[220:221],v227 offset:9216\nds_read_b64_tr_b16 v[222:223],v227 offset:13312\nM v[40:43],a[128:131],v[192:195],v[40:43]\nv_accvgpr_read_b32 v49,a185\nv_accvgpr_read_b32 v50,a186\nM v[40:43],a[136:139],v[208:211],v[40:43]\n" \
"v_accvgpr_read_b32 v51,a187\nv_accvgpr_read_b32 v52,a188\nM v[44:47],a[132:135],v[192:195],v[44:47]\nds_read_b64_tr_b16 v[192:193],v226 offset:1536\nds_read_b64_tr_b16 v[194:195],v226 offset:5632\nM v[44:47],a[140:143],v[208:211],v[44:47]\nds_read_b64_tr_b16 v[208:209],v226 offset:9728\ns_waitcnt lgkmcnt(7)\n" \
"M v[56:59],a[128:131],v[196:199],v[56:59]\nds_read_b64_tr_b16 v[210:211],v226 offset:13824\nv_accvgpr_read_b32 v53,a189\nM v[56:59],a[136:139],v[212:215],v[56:59]\nv_accvgpr_read_b32 v54,a190\nv_accvgpr_read_b32 v55,a191\nM v[60:63],a[132:135],v[196:199],v[60:63]\nds_read_b64_tr_b16 v[196:197],v227 offset:1536\n" \
"ds_read_b64_tr_b16 v[198:199],v227 offset:5632\nM v[60:63],a[140:143],v[212:215],v[60:63]\nds_read_b64_tr_b16 v[212:213],v227 offset:9728\nds_read_b64_tr_b16 v[214:215],v227 offset:13824\nM v[72:75],a[128:131],v[200:203],v[72:75]\nv_lshlrev_b32 v64,16,v64\nv_lshlrev_b32 v65,16,v65\nM v[72:75],a[136:139],v[204:207],v[72:75]\n" \
"v_lshlrev_b32 v66,16,v66\nv_lshlrev_b32 v67,16,v67\nM v[76:79],a[132:135],v[200:203],v[76:79]\nds_read_b64_tr_b16 v[200:201],v226 offset:32768\nds_read_b64_tr_b16 v[202:203],v226 offset:36864\nM v[76:79],a[140:143],v[204:207],v[76:79]\nds_read_b64_tr_b16 v[204:205],v226 offset:40960\ns_waitcnt lgkmcnt(7)\n" \
"M v[88:91],a[128:131],v[216:219],v[88:91]\nds_read_b64_tr_b16 v[206:207],v226 offset:45056\nv_lshlrev_b32 v68,16,v68\nM v[88:91],a[136:139],v[220:223],v[88:91]\nv_lshlrev_b32 v69,16,v69\nv_lshlrev_b32 v70,16,v70\nM v[92:95],a[132:135],v[216:219],v[92:95]\nds_read_b64_tr_b16 v[216:217],v227 offset:32768\n" \
"ds_read_b64_tr_b16 v[218:219],v227 offset:36864\nM v[92:95],a[140:143],v[220:223],v[92:95]\nds_read_b64_tr_b16 v[220:221],v227 offset:40960\nds_read_b64_tr_b16 v[222:223],v227 offset:45056\nM v[104:107],a[128:131],v[192:195],v[104:107]\nv_lshlrev_b32 v71,16,v71\nv_lshlrev_b32 v80,16,v80\nM v[104:107],a[136:139],v[208:211],v[104:107]\n" \
"v_lshlrev_b32 v81,16,v81\nv_lshlrev_b32 v82,16,v82\nM v[108:111],a[132:135],v[192:195],v[108:111]\nds_read_b64_tr_b16 v[192:193],v226 offset:33280\nds_read_b64_tr_b16 v[194:195],v226 offset:37376\nM v[108:111],a[140:143],v[208:211],v[108:111]\nds_read_b64_tr_b16 v[208:209],v226 offset:41472\ns_waitcnt lgkmcnt(7)\n" \
"M v[120:123],a[128:131],v[196:199],v[120:123]\nds_read_b64_tr_b16 v[210:211],v226 offset:45568\nv_lshlrev_b32 v83,16,v83\nM v[120:123],a[136:139],v[212:215],v[120:123]\nv_lshlrev_b32 v84,16,v84\nv_lshlrev_b32 v85,16,v85\nM v[124:127],a[132:135],v[196:199],v[124:127]\nds_read_b64_tr_b16 v[196:197],v227 offset:33280\n" \
"ds_read_b64_tr_b16 v[198:199],v227 offset:37376\nM v[124:127],a[140:143],v[212:215],v[124:127]\nds_read_b64_tr_b16 v[212:213],v227 offset:41472\nds_read_b64_tr_b16 v[214:215],v227 offset:45568\nM v[8:11],a[144:147],v[200:203],v[8:11]\nv_lshlrev_b32 v86,16,v86\ns_waitcnt vmcnt(3)\nM v[8:11],a[152:155],v[204:207],v[8:11]\n" \
"v_lshlrev_b32 v87,16,v87\nv_lshlrev_b32 v96,16,v96\nM v[12:15],a[148:151],v[200:203],v[12:15]\nds_read_b64_tr_b16 v[200:201],v226 offset:33792\nds_read_b64_tr_b16 v[202:203],v226 offset:37888\nM v[12:15],a[156:159],v[204:207],v[12:15]\nds_read_b64_tr_b16 v[204:205],v226 offset:41984\ns_waitcnt lgkmcnt(7)\n" \
"M v[24:27],a[144:147],v[216:219],v[24:27]\nds_read_b64_tr_b16 v[206:207],v226 offset:46080\nv_lshlrev_b32 v97,16,v97\nM v[24:27],a[152:155],v[220:223],v[24:27]\nv_lshlrev_b32 v98,16,v98\nv_lshlrev_b32 v99,16,v99\nM v[28:31],a[148:151],v[216:219],v[28:31]\nv_lshlrev_b32 v100,16,v100\n" \
"v_lshlrev_b32 v101,16,v101\nM v[28:31],a[156:159],v[220:223],v[28:31]\nv_lshlrev_b32 v102,16,v102\nv_lshlrev_b32 v103,16,v103\nM v[40:43],a[144:147],v[192:195],v[40:43]\nv_cvt_pk_bf16_f32 v32,0,v32\nv_cvt_pk_bf16_f32 v33,0,v33\nv_cvt_pk_bf16_f32 v34,0,v34\n" \
"v_cvt_pk_bf16_f32 v35,0,v35\nv_cvt_pk_bf16_f32 v36,0,v36\nv_cvt_pk_bf16_f32 v37,0,v37\nv_cvt_pk_bf16_f32 v38,0,v38\nv_cvt_pk_bf16_f32 v39,0,v39\nv_cvt_pk_bf16_f32 v116,0,v116\nv_cvt_pk_bf16_f32 v117,0,v117\nM v[40:43],a[152:155],v[208:211],v[40:43]\n" \
"ds_read_b64_tr_b16 v[216:217],v227 offset:33792\nds_read_b64_tr_b16 v[218:219],v227 offset:37888\nM v[44:47],a[148:151],v[192:195],v[44:47]\nds_read_b64_tr_b16 v[220:221],v227 offset:41984\nds_read_b64_tr_b16 v[222:223],v227 offset:46080\nM v[44:47],a[156:159],v[208:211],v[44:47]\nds_read_b64_tr_b16 v[192:193],v226 offset:34304\ns_waitcnt lgkmcnt(5)\n" \
"M v[56:59],a[144:147],v[196:199],v[56:59]\nds_read_b64_tr_b16 v[194:195],v226 offset:38400\nds_read_b64_tr_b16 v[208:209],v226 offset:42496\nM v[56:59],a[152:155],v[212:215],v[56:59]\nds_read_b64_tr_b16 v[210:211],v226 offset:46592\nv_cvt_pk_bf16_f32 v8,v8,v24\nM v[60:63],a[148:151],v[196:199],v[60:63]\nds_read_b64_tr_b16 v[196:197],v227 offset:34304\n" \
"ds_read_b64_tr_b16 v[198:199],v227 offset:38400\nM v[60:63],a[156:159],v[212:215],v[60:63]\nds_read_b64_tr_b16 v[212:213],v227 offset:42496\nds_read_b64_tr_b16 v[214:215],v227 offset:46592\nM v[72:75],a[144:147],v[200:203],v[72:75]\nv_cvt_pk_bf16_f32 v9,v9,v25\nv_cvt_pk_bf16_f32 v10,v10,v26\nM v[72:75],a[152:155],v[204:207],v[72:75]\n" \
"v_cvt_pk_bf16_f32 v11,v11,v27\nv_cvt_pk_bf16_f32 v40,v40,v56\nM v[76:79],a[148:151],v[200:203],v[76:79]\nv_cvt_pk_bf16_f32 v41,v41,v57\nv_cvt_pk_bf16_f32 v42,v42,v58\nM v[76:79],a[156:159],v[204:207],v[76:79]\nv_cvt_pk_bf16_f32 v43,v43,v59\ns_waitcnt lgkmcnt(6)\n" \
"M v[88:91],a[144:147],v[216:219],v[88:91]\nv_cvt_pk_bf16_f32 v12,v12,v28\nv_cvt_pk_bf16_f32 v13,v13,v29\nM v[88:91],a[152:155],v[220:223],v[88:91]\nv_cvt_pk_bf16_f32 v14,v14,v30\nv_cvt_pk_bf16_f32 v15,v15,v31\nM v[92:95],a[148:151],v[216:219],v[92:95]\nv_cvt_pk_bf16_f32 v44,v44,v60\n" \
"v_cvt_pk_bf16_f32 v45,v45,v61\nM v[92:95],a[156:159],v[220:223],v[92:95]\nv_cvt_pk_bf16_f32 v46,v46,v62\nv_cvt_pk_bf16_f32 v47,v47,v63\nM v[104:107],a[144:147],v[192:195],v[104:107]\nv_cvt_pk_bf16_f32 v72,v72,v88\ns_waitcnt lgkmcnt(0)\nM v[104:107],a[152:155],v[208:211],v[104:107]\n" \
"v_cvt_pk_bf16_f32 v73,v73,v89\nv_cvt_pk_bf16_f32 v74,v74,v90\nM v[108:111],a[148:151],v[192:195],v[108:111]\nv_cvt_pk_bf16_f32 v75,v75,v91\nv_cvt_pk_bf16_f32 v76,v76,v92\nM v[108:111],a[156:159],v[208:211],v[108:111]\nv_cvt_pk_bf16_f32 v77,v77,v93\nv_cvt_pk_bf16_f32 v78,v78,v94\n" \
"M v[120:123],a[144:147],v[196:199],v[120:123]\nv_cvt_pk_bf16_f32 v79,v79,v95\nv_xor_b32 v228,v228,v229\nM v[120:123],a[152:155],v[212:215],v[120:123]\ns_add_u32 s38,s78,16384\nv_min_u32 v24,-2,v8\nM v[124:127],a[148:151],v[196:199],v[124:127]\nv_min_u32 v25,-2,v40\n" \
"v_min_u32 v26,-2,v72\nM v[124:127],a[156:159],v[212:215],v[124:127]\nv_min_u32 v56,-2,v9\nv_min_u32 v57,-2,v41\nv_cvt_pk_bf16_f32 v104,v104,v120\nv_cvt_pk_bf16_f32 v105,v105,v121\nv_cvt_pk_bf16_f32 v106,v106,v122\nv_cvt_pk_bf16_f32 v107,v107,v123\n" \
"v_min_u32 v27,-2,v104\nv_min_u32 v58,-2,v73\nv_cvt_pk_bf16_f32 v108,v108,v124\nv_cvt_pk_bf16_f32 v109,v109,v125\nv_cvt_pk_bf16_f32 v110,v110,v126\nv_cvt_pk_bf16_f32 v111,v111,v127\nv_min_u32 v59,-2,v105\nv_min_u32 v88,-2,v10\n" \
"v_min_u32 v89,-2,v42\nv_min_u32 v90,-2,v74\nv_min_u32 v91,-2,v106\nv_min_u32 v120,-2,v11\nv_min_u32 v121,-2,v43\nv_min_u32 v122,-2,v75\nv_min_u32 v123,-2,v107\nv_min_u32 v28,-2,v12\n" \
"v_min_u32 v29,-2,v44\nv_min_u32 v30,-2,v76\nv_min_u32 v31,-2,v108\nv_min_u32 v60,-2,v13\nv_min_u32 v61,-2,v45\nv_min_u32 v62,-2,v77\nv_min_u32 v63,-2,v109\nv_min_u32 v92,-2,v14\n" \
"v_min_u32 v93,-2,v46\nv_min_u32 v94,-2,v78\nv_min_u32 v95,-2,v110\nv_min_u32 v124,-2,v15\nv_min_u32 v125,-2,v47\nv_min_u32 v126,-2,v79\nv_min_u32 v127,-2,v111\nv_accvgpr_write_b32 a192,v229\n" \
"v_accvgpr_write_b32 a193,v228\nv_accvgpr_write_b32 a198,v229\nv_accvgpr_write_b32 a199,v228\nbuffer_store_dwordx4 v[24:27],v253,s[52:55],s78 offen offset:0 sc1\nbuffer_store_dwordx4 v[56:59],v253,s[52:55],s78 offen offset:1024 sc1\nbuffer_store_dwordx4 v[88:91],v253,s[52:55],s78 offen offset:2048 sc1\nbuffer_store_dwordx4 v[120:123],v253,s[52:55],s78 offen offset:3072 sc1\nbuffer_store_dwordx4 v[28:31],v253,s[52:55],s38 offen offset:0 sc1\n" \
"buffer_store_dwordx4 v[60:63],v253,s[52:55],s38 offen offset:1024 sc1\nbuffer_store_dwordx4 v[92:95],v253,s[52:55],s38 offen offset:2048 sc1\nbuffer_store_dwordx4 v[124:127],v253,s[52:55],s38 offen offset:3072 sc1\n.Ltail:\ns_add_u32 s72,s95,0\nbuffer_load_dwordx4 v[160:163],v240,s[52:55],s72 offen sc1\ns_add_u32 s72,s95,2097152\nbuffer_load_dwordx4 v[164:167],v240,s[52:55],s72 offen sc1\n" \
"s_add_u32 s72,s95,4194304\nbuffer_load_dwordx4 v[168:171],v240,s[52:55],s72 offen sc1\ns_add_u32 s72,s95,6291456\nbuffer_load_dwordx4 v[172:175],v240,s[52:55],s72 offen sc1\ns_add_u32 s72,s95,8388608\nbuffer_load_dwordx4 v[176:179],v240,s[52:55],s72 offen sc1\ns_add_u32 s72,s95,10485760\nbuffer_load_dwordx4 v[180:183],v240,s[52:55],s72 offen sc1\n" \
"s_add_u32 s72,s95,12582912\nbuffer_load_dwordx4 v[184:187],v240,s[52:55],s72 offen sc1\ns_add_u32 s72,s95,14680064\nbuffer_load_dwordx4 v[188:191],v240,s[52:55],s72 offen sc1\n.Ltail2:\ns_add_u32 s72,s74,0\nbuffer_load_dwordx4 v[128:131],v240,s[52:55],s72 offen sc1\ns_add_u32 s72,s74,2097152\n" \
"buffer_load_dwordx4 v[132:135],v240,s[52:55],s72 offen sc1\ns_add_u32 s72,s74,4194304\nbuffer_load_dwordx4 v[136:139],v240,s[52:55],s72 offen sc1\ns_add_u32 s72,s74,6291456\nbuffer_load_dwordx4 v[140:143],v240,s[52:55],s72 offen sc1\ns_add_u32 s72,s74,8388608\nbuffer_load_dwordx4 v[144:147],v240,s[52:55],s72 offen sc1\ns_add_u32 s72,s74,10485760\n" \
"buffer_load_dwordx4 v[148:151],v240,s[52:55],s72 offen sc1\ns_add_u32 s72,s74,12582912\nbuffer_load_dwordx4 v[152:155],v240,s[52:55],s72 offen sc1\ns_add_u32 s72,s74,14680064\nbuffer_load_dwordx4 v[156:159],v240,s[52:55],s72 offen sc1\ns_cmp_eq_u32 s3,0\ns_cbranch_scc1 .Lnp\ns_mov_b32 s70,0x05040100\n" \
"s_mov_b32 s71,0x07060302\nv_accvgpr_read_b32 v228,a212\nv_accvgpr_read_b32 v229,a213\nv_accvgpr_read_b32 v230,a214\nv_accvgpr_read_b32 v231,a215\nv_perm_b32 v192,v230,v228,s70\nv_perm_b32 v193,v230,v228,s71\nv_perm_b32 v194,v231,v229,s70\n" \
"v_perm_b32 v195,v231,v229,s71\nv_accvgpr_read_b32 v228,a216\nv_accvgpr_read_b32 v229,a217\nv_accvgpr_read_b32 v230,a218\nv_accvgpr_read_b32 v231,a219\nv_perm_b32 v196,v230,v228,s70\nv_perm_b32 v197,v230,v228,s71\nv_perm_b32 v198,v231,v229,s70\n" \
"v_perm_b32 v199,v231,v229,s71\nv_accvgpr_read_b32 v228,a220\nv_accvgpr_read_b32 v229,a221\nv_accvgpr_read_b32 v230,a222\nv_accvgpr_read_b32 v231,a223\nv_perm_b32 v200,v230,v228,s70\nv_perm_b32 v201,v230,v228,s71\nv_perm_b32 v202,v231,v229,s70\n" \
"v_perm_b32 v203,v231,v229,s71\nv_accvgpr_read_b32 v228,a224\nv_accvgpr_read_b32 v229,a225\nv_accvgpr_read_b32 v230,a226\nv_accvgpr_read_b32 v231,a227\nv_perm_b32 v204,v230,v228,s70\nv_perm_b32 v205,v230,v228,s71\nv_perm_b32 v206,v231,v229,s70\n" \
"v_perm_b32 v207,v231,v229,s71\nv_accvgpr_read_b32 v228,a228\nv_accvgpr_read_b32 v229,a229\nv_accvgpr_read_b32 v230,a230\nv_accvgpr_read_b32 v231,a231\nv_perm_b32 v208,v230,v228,s70\nv_perm_b32 v209,v230,v228,s71\nv_perm_b32 v210,v231,v229,s70\n" \
"v_perm_b32 v211,v231,v229,s71\nv_accvgpr_read_b32 v228,a232\nv_accvgpr_read_b32 v229,a233\nv_accvgpr_read_b32 v230,a234\nv_accvgpr_read_b32 v231,a235\nv_perm_b32 v212,v230,v228,s70\nv_perm_b32 v213,v230,v228,s71\nv_perm_b32 v214,v231,v229,s70\n" \
"v_perm_b32 v215,v231,v229,s71\nv_accvgpr_read_b32 v228,a236\nv_accvgpr_read_b32 v229,a237\nv_accvgpr_read_b32 v230,a238\nv_accvgpr_read_b32 v231,a239\nv_perm_b32 v216,v230,v228,s70\nv_perm_b32 v217,v230,v228,s71\nv_perm_b32 v218,v231,v229,s70\n" \
"v_perm_b32 v219,v231,v229,s71\nv_accvgpr_read_b32 v228,a240\nv_accvgpr_read_b32 v229,a241\nv_accvgpr_read_b32 v230,a242\nv_accvgpr_read_b32 v231,a243\nv_perm_b32 v220,v230,v228,s70\nv_perm_b32 v221,v230,v228,s71\nv_perm_b32 v222,v231,v229,s70\n" \
"v_perm_b32 v223,v231,v229,s71\nv_and_b32 v228,3,v249\nv_lshrrev_b32 v229,4,v249\nv_and_b32 v230,1,v229\nv_lshrrev_b32 v229,1,v229\nv_lshlrev_b32 v230,5,v230\nv_lshl_or_b32 v230,v228,6,v230\nv_lshl_or_b32 v230,v229,8,v230\n" \
"s_lshl2_add_u32 s70,s7,84032\nv_add_u32 v252,s70,v230\nv_and_b32 v230,0xf0,v240\nv_add_u32 v253,84032,v230\n.Lnp:\n.Lrow1:\ns_waitcnt vmcnt(15)\nM v[72:75],a[192:195],v[160:163],0\n" \
"M v[76:79],a[196:199],v[160:163],0\ns_waitcnt vmcnt(14)\nM v[72:75],a[192:195],v[164:167],v[72:75]\nM v[76:79],a[196:199],v[164:167],v[76:79]\ns_waitcnt vmcnt(13)\nM v[72:75],a[192:195],v[168:171],v[72:75]\nM v[76:79],a[196:199],v[168:171],v[76:79]\ns_waitcnt vmcnt(12)\n" \
"M v[72:75],a[192:195],v[172:175],v[72:75]\nM v[76:79],a[196:199],v[172:175],v[76:79]\ns_waitcnt vmcnt(11)\nM v[72:75],a[192:195],v[176:179],v[72:75]\nM v[76:79],a[196:199],v[176:179],v[76:79]\ns_waitcnt vmcnt(10)\nM v[72:75],a[192:195],v[180:183],v[72:75]\nM v[76:79],a[196:199],v[180:183],v[76:79]\n" \
"s_waitcnt vmcnt(9)\nM v[72:75],a[192:195],v[184:187],v[72:75]\nM v[76:79],a[196:199],v[184:187],v[76:79]\ns_waitcnt vmcnt(8)\nM v[72:75],a[192:195],v[188:191],v[72:75]\nM v[76:79],a[196:199],v[188:191],v[76:79]\ns_sub_u32 s38,56622112,s95\ns_lshr_b32 s38,s38,5\n" \
"s_nop 4\nv_cvt_pk_bf16_f32 v72,0,v72\nv_cvt_pk_bf16_f32 v73,0,v73\nv_cvt_pk_bf16_f32 v74,0,v74\nv_cvt_pk_bf16_f32 v75,0,v75\nv_cvt_pk_bf16_f32 v76,0,v76\nv_cvt_pk_bf16_f32 v77,0,v77\nv_cvt_pk_bf16_f32 v78,0,v78\n" \
"v_cvt_pk_bf16_f32 v79,0,v79\nv_pk_mul_f32 v[72:73],v[72:73],v[0:1]\nv_pk_mul_f32 v[74:75],v[74:75],v[2:3]\nv_pk_mul_f32 v[76:77],v[76:77],v[4:5]\nv_pk_mul_f32 v[78:79],v[78:79],v[6:7]\nv_cvt_pk_bf16_f32 v72,0,v72\nv_cvt_pk_bf16_f32 v73,0,v73\nv_cvt_pk_bf16_f32 v74,0,v74\n" \
"v_cvt_pk_bf16_f32 v75,0,v75\nv_cvt_pk_bf16_f32 v76,0,v76\nv_cvt_pk_bf16_f32 v77,0,v77\nv_cvt_pk_bf16_f32 v78,0,v78\nv_cvt_pk_bf16_f32 v79,0,v79\nv_pk_add_f32 v[72:73],v[72:73],v[64:65]\nv_pk_add_f32 v[74:75],v[74:75],v[66:67]\nv_pk_add_f32 v[76:77],v[76:77],v[68:69]\n" \
"v_pk_add_f32 v[78:79],v[78:79],v[70:71]\nv_cvt_pk_bf16_f32 v72,0,v72\nv_cvt_pk_bf16_f32 v73,0,v73\nv_cvt_pk_bf16_f32 v74,0,v74\nv_cvt_pk_bf16_f32 v75,0,v75\nv_cvt_pk_bf16_f32 v76,0,v76\nv_cvt_pk_bf16_f32 v77,0,v77\nv_cvt_pk_bf16_f32 v78,0,v78\n" \
"v_cvt_pk_bf16_f32 v79,0,v79\nv_readfirstlane_b32 s70,v72\ns_nop 1\nv_subrev_f32 v104,s70,v72\nv_subrev_f32 v105,s70,v73\nv_subrev_f32 v106,s70,v74\nv_subrev_f32 v107,s70,v75\nv_subrev_f32 v108,s70,v76\n" \
"v_subrev_f32 v109,s70,v77\nv_subrev_f32 v110,s70,v78\nv_subrev_f32 v111,s70,v79\nv_pk_mul_f32 v[124:125],v[104:105],v[104:105]\nv_pk_add_f32 v[120:121],v[104:105],v[106:107]\nv_pk_fma_f32 v[124:125],v[106:107],v[106:107],v[124:125]\nv_pk_fma_f32 v[124:125],v[108:109],v[108:109],v[124:125]\nv_pk_fma_f32 v[124:125],v[110:111],v[110:111],v[124:125]\n" \
"v_pk_add_f32 v[108:109],v[108:109],v[110:111]\nv_pk_add_f32 v[120:121],v[120:121],v[108:109]\nv_add_f32 v120,v120,v121\nv_add_f32 v121,v124,v125\ns_nop 1\nv_permlane16_swap_b32 v120,v121\nv_add_f32 v120,v120,v121\nv_mov_b32 v121,v120\n" \
"s_nop 1\nv_permlane32_swap_b32 v120,v121\nv_add_f32 v120,v120,v121\ns_nop 1\nv_add_f32_dpp v120,v120,v120 quad_perm:[1,0,3,2] row_mask:0xf bank_mask:0xf\ns_nop 1\nv_add_f32_dpp v120,v120,v120 quad_perm:[2,3,0,1] row_mask:0xf bank_mask:0xf\ns_nop 1\n" \
"v_add_f32_dpp v120,v120,v120 row_half_mirror row_mask:0xf bank_mask:0xf\ns_nop 1\nv_add_f32_dpp v120,v120,v120 row_mirror row_mask:0xf bank_mask:0xf\nv_mul_f32 v120,0x3b000000,v120\nv_mov_b32 v121,v120\nv_cmp_u_f32 vcc,v120,v120\ns_nop 0\nv_permlane16_swap_b32 v120,v121\n" \
"s_cbranch_vccnz .Lrs1\n.Lrb1:\nv_fma_f32 v121,-v120,v120,v121\nv_add_f32 v122,s70,v120\nv_max_f32 v121,0,v121\nv_cvt_pk_bf16_f32 v122,0,v122\nv_cvt_pk_bf16_f32 v121,0,v121\nv_add_f32 v121,0x3727c5ac,v121\n" \
"v_cvt_pk_bf16_f32 v121,0,v121\nv_sqrt_f32 v121,v121\nv_pk_add_f32 v[88:89],v[72:73],v[122:123] op_sel_hi:[1,0] neg_lo:[0,1] neg_hi:[0,1]\nv_cvt_pk_bf16_f32 v121,0,v121\nv_rcp_f32 v123,v121\nv_cvt_pk_bf16_f32 v88,0,v88\nv_pk_add_f32 v[90:91],v[74:75],v[122:123] op_sel_hi:[1,0] neg_lo:[0,1] neg_hi:[0,1]\nv_pk_add_f32 v[92:93],v[76:77],v[122:123] op_sel_hi:[1,0] neg_lo:[0,1] neg_hi:[0,1]\n" \
"v_pk_add_f32 v[94:95],v[78:79],v[122:123] op_sel_hi:[1,0] neg_lo:[0,1] neg_hi:[0,1]\nv_cvt_pk_bf16_f32 v89,0,v89\nv_pk_mul_f32 v[88:89],v[88:89],v[122:123] op_sel:[0,1]\nv_cvt_pk_bf16_f32 v88,0,v88\nv_cvt_pk_bf16_f32 v89,0,v89\nv_pk_mul_f32 v[88:89],v[88:89],v[32:33]\nv_cvt_pk_bf16_f32 v88,0,v88\nv_cvt_pk_bf16_f32 v89,0,v89\n" \
"v_pk_add_f32 v[88:89],v[88:89],v[16:17]\nv_cvt_pk_bf16_f32 v88,0,v88\nv_cvt_pk_bf16_f32 v89,0,v89\nv_pk_mul_f32 v[124:125],v[88:89],v[96:97]\nv_cvt_pk_bf16_f32 v90,0,v90\nv_cvt_pk_bf16_f32 v91,0,v91\nv_pk_mul_f32 v[90:91],v[90:91],v[122:123] op_sel:[0,1]\nv_cvt_pk_bf16_f32 v90,0,v90\n" \
"v_cvt_pk_bf16_f32 v91,0,v91\nv_pk_mul_f32 v[90:91],v[90:91],v[34:35]\nv_cvt_pk_bf16_f32 v90,0,v90\nv_cvt_pk_bf16_f32 v91,0,v91\nv_pk_add_f32 v[90:91],v[90:91],v[18:19]\nv_cvt_pk_bf16_f32 v90,0,v90\nv_cvt_pk_bf16_f32 v91,0,v91\nv_pk_fma_f32 v[124:125],v[90:91],v[98:99],v[124:125]\n" \
"v_cvt_pk_bf16_f32 v92,0,v92\nv_cvt_pk_bf16_f32 v93,0,v93\nv_pk_mul_f32 v[92:93],v[92:93],v[122:123] op_sel:[0,1]\nv_cvt_pk_bf16_f32 v92,0,v92\nv_cvt_pk_bf16_f32 v93,0,v93\nv_pk_mul_f32 v[92:93],v[92:93],v[36:37]\nv_cvt_pk_bf16_f32 v92,0,v92\nv_cvt_pk_bf16_f32 v93,0,v93\n" \
"v_pk_add_f32 v[92:93],v[92:93],v[20:21]\nv_cvt_pk_bf16_f32 v92,0,v92\nv_cvt_pk_bf16_f32 v93,0,v93\nv_pk_fma_f32 v[124:125],v[92:93],v[100:101],v[124:125]\nv_cvt_pk_bf16_f32 v94,0,v94\nv_cvt_pk_bf16_f32 v95,0,v95\nv_pk_mul_f32 v[94:95],v[94:95],v[122:123] op_sel:[0,1]\nv_cvt_pk_bf16_f32 v94,0,v94\n" \
"v_cvt_pk_bf16_f32 v95,0,v95\nv_pk_mul_f32 v[94:95],v[94:95],v[38:39]\nv_cvt_pk_bf16_f32 v94,0,v94\nv_cvt_pk_bf16_f32 v95,0,v95\nv_pk_add_f32 v[94:95],v[94:95],v[22:23]\nv_cvt_pk_bf16_f32 v94,0,v94\nv_cvt_pk_bf16_f32 v95,0,v95\nv_pk_fma_f32 v[124:125],v[94:95],v[102:103],v[124:125]\n" \
"v_add_f32 v124,v124,v125\nv_mov_b32 v125,v124\nv_lshrrev_b32 v104,16,v121\nv_or_b32 v104,v104,v122\nv_permlane16_swap_b32 v124,v125\nv_add_f32 v124,v124,v125\nv_mov_b32 v125,v124\ns_nop 1\n" \
"v_permlane32_swap_b32 v124,v125\nv_add_f32 v124,v124,v125\ns_nop 1\nv_add_f32_dpp v124,v124,v124 quad_perm:[1,0,3,2] row_mask:0xf bank_mask:0xf\ns_nop 1\nv_add_f32_dpp v124,v124,v124 quad_perm:[2,3,0,1] row_mask:0xf bank_mask:0xf\ns_nop 1\nv_add_f32_dpp v124,v124,v124 row_half_mirror row_mask:0xf bank_mask:0xf\n" \
"s_nop 1\nv_add_f32_dpp v124,v124,v124 row_mirror row_mask:0xf bank_mask:0xf\nv_cvt_pk_bf16_f32 v124,0,v124\ns_nop 0\nv_readfirstlane_b32 s77,v124\ns_nop 0\ns_ashr_i32 s71,s77,31\ns_and_b32 s72,s77,0x7fffffff\n" \
"s_or_b32 s71,s71,0x80000000\ns_xor_b32 s77,s77,s71\ns_and_b32 s77,s77,0xffff0000\ns_cmp_gt_u32 s72,0x7f800000\ns_cselect_b32 s77,0,s77\ns_or_b32 s77,s77,s38\nv_mov_b32 v105,s77\nds_max_u64 v250,v[104:105]\n" \
".Lrow0:\ns_waitcnt vmcnt(7)\nM v[8:11],a[192:195],v[128:131],0\nM v[12:15],a[196:199],v[128:131],0\ns_waitcnt vmcnt(6)\nM v[8:11],a[192:195],v[132:135],v[8:11]\nM v[12:15],a[196:199],v[132:135],v[12:15]\ns_waitcnt vmcnt(5)\n" \
"M v[8:11],a[192:195],v[136:139],v[8:11]\nM v[12:15],a[196:199],v[136:139],v[12:15]\ns_waitcnt vmcnt(4)\nM v[8:11],a[192:195],v[140:143],v[8:11]\nM v[12:15],a[196:199],v[140:143],v[12:15]\ns_waitcnt vmcnt(3)\nM v[8:11],a[192:195],v[144:147],v[8:11]\nM v[12:15],a[196:199],v[144:147],v[12:15]\n" \
"s_waitcnt vmcnt(2)\nM v[8:11],a[192:195],v[148:151],v[8:11]\nM v[12:15],a[196:199],v[148:151],v[12:15]\ns_waitcnt vmcnt(1)\nM v[8:11],a[192:195],v[152:155],v[8:11]\nM v[12:15],a[196:199],v[152:155],v[12:15]\ns_waitcnt vmcnt(0)\nM v[8:11],a[192:195],v[156:159],v[8:11]\n" \
"M v[12:15],a[196:199],v[156:159],v[12:15]\ns_sub_u32 s39,56622112,s74\ns_lshr_b32 s39,s39,5\ns_nop 4\nv_cvt_pk_bf16_f32 v8,0,v8\nv_cvt_pk_bf16_f32 v9,0,v9\nv_cvt_pk_bf16_f32 v10,0,v10\nv_cvt_pk_bf16_f32 v11,0,v11\n" \
"v_cvt_pk_bf16_f32 v12,0,v12\nv_cvt_pk_bf16_f32 v13,0,v13\nv_cvt_pk_bf16_f32 v14,0,v14\nv_cvt_pk_bf16_f32 v15,0,v15\nv_pk_mul_f32 v[8:9],v[8:9],v[0:1]\nv_pk_mul_f32 v[10:11],v[10:11],v[2:3]\nv_pk_mul_f32 v[12:13],v[12:13],v[4:5]\nv_pk_mul_f32 v[14:15],v[14:15],v[6:7]\n" \
"v_cvt_pk_bf16_f32 v8,0,v8\nv_cvt_pk_bf16_f32 v9,0,v9\nv_cvt_pk_bf16_f32 v10,0,v10\nv_cvt_pk_bf16_f32 v11,0,v11\nv_cvt_pk_bf16_f32 v12,0,v12\nv_cvt_pk_bf16_f32 v13,0,v13\nv_cvt_pk_bf16_f32 v14,0,v14\nv_cvt_pk_bf16_f32 v15,0,v15\n" \
"v_pk_add_f32 v[8:9],v[8:9],v[80:81]\nv_pk_add_f32 v[10:11],v[10:11],v[82:83]\nv_pk_add_f32 v[12:13],v[12:13],v[84:85]\nv_pk_add_f32 v[14:15],v[14:15],v[86:87]\nv_cvt_pk_bf16_f32 v8,0,v8\nv_cvt_pk_bf16_f32 v9,0,v9\nv_cvt_pk_bf16_f32 v10,0,v10\nv_cvt_pk_bf16_f32 v11,0,v11\n" \
"v_cvt_pk_bf16_f32 v12,0,v12\nv_cvt_pk_bf16_f32 v13,0,v13\nv_cvt_pk_bf16_f32 v14,0,v14\nv_cvt_pk_bf16_f32 v15,0,v15\nv_readfirstlane_b32 s70,v8\ns_nop 1\nv_subrev_f32 v40,s70,v8\nv_subrev_f32 v41,s70,v9\n" \
"v_subrev_f32 v42,s70,v10\nv_subrev_f32 v43,s70,v11\nv_subrev_f32 v44,s70,v12\nv_subrev_f32 v45,s70,v13\nv_subrev_f32 v46,s70,v14\nv_subrev_f32 v47,s70,v15\nv_pk_mul_f32 v[60:61],v[40:41],v[40:41]\nv_pk_add_f32 v[56:57],v[40:41],v[42:43]\n" \
"v_pk_fma_f32 v[60:61],v[42:43],v[42:43],v[60:61]\nv_pk_fma_f32 v[60:61],v[44:45],v[44:45],v[60:61]\nv_pk_fma_f32 v[60:61],v[46:47],v[46:47],v[60:61]\nv_pk_add_f32 v[44:45],v[44:45],v[46:47]\nv_pk_add_f32 v[56:57],v[56:57],v[44:45]\nv_add_f32 v56,v56,v57\nv_add_f32 v57,v60,v61\ns_nop 1\n" \
"v_permlane16_swap_b32 v56,v57\nv_add_f32 v56,v56,v57\nv_mov_b32 v57,v56\ns_nop 1\nv_permlane32_swap_b32 v56,v57\nv_add_f32 v56,v56,v57\ns_nop 1\nv_add_f32_dpp v56,v56,v56 quad_perm:[1,0,3,2] row_mask:0xf bank_mask:0xf\n" \
"s_nop 1\nv_add_f32_dpp v56,v56,v56 quad_perm:[2,3,0,1] row_mask:0xf bank_mask:0xf\ns_nop 1\nv_add_f32_dpp v56,v56,v56 row_half_mirror row_mask:0xf bank_mask:0xf\ns_nop 1\nv_add_f32_dpp v56,v56,v56 row_mirror row_mask:0xf bank_mask:0xf\nv_mul_f32 v56,0x3b000000,v56\nv_mov_b32 v57,v56\n" \
"v_cmp_u_f32 vcc,v56,v56\ns_nop 0\nv_permlane16_swap_b32 v56,v57\ns_cbranch_vccnz .Lrs0\n.Lrb0:\nbuffer_load_dwordx4 v[232:235],v251,s[52:55],s80 offen sc1\nbuffer_load_dwordx4 v[236:239],v251,s[52:55],s80 offen offset:16 sc1\nv_fma_f32 v57,-v56,v56,v57\n" \
"v_add_f32 v58,s70,v56\nv_max_f32 v57,0,v57\nv_cvt_pk_bf16_f32 v58,0,v58\nv_cvt_pk_bf16_f32 v57,0,v57\nv_add_f32 v57,0x3727c5ac,v57\nv_cvt_pk_bf16_f32 v57,0,v57\nv_sqrt_f32 v57,v57\nv_pk_add_f32 v[24:25],v[8:9],v[58:59] op_sel_hi:[1,0] neg_lo:[0,1] neg_hi:[0,1]\n" \
"v_cvt_pk_bf16_f32 v57,0,v57\nv_rcp_f32 v59,v57\nv_cvt_pk_bf16_f32 v24,0,v24\nv_pk_add_f32 v[26:27],v[10:11],v[58:59] op_sel_hi:[1,0] neg_lo:[0,1] neg_hi:[0,1]\nv_pk_add_f32 v[28:29],v[12:13],v[58:59] op_sel_hi:[1,0] neg_lo:[0,1] neg_hi:[0,1]\nv_pk_add_f32 v[30:31],v[14:15],v[58:59] op_sel_hi:[1,0] neg_lo:[0,1] neg_hi:[0,1]\nv_cvt_pk_bf16_f32 v25,0,v25\nv_pk_mul_f32 v[24:25],v[24:25],v[58:59] op_sel:[0,1]\n" \
"v_cvt_pk_bf16_f32 v24,0,v24\nv_cvt_pk_bf16_f32 v25,0,v25\nv_pk_mul_f32 v[24:25],v[24:25],v[32:33]\nv_cvt_pk_bf16_f32 v24,0,v24\nv_cvt_pk_bf16_f32 v25,0,v25\nv_pk_add_f32 v[24:25],v[24:25],v[16:17]\nv_cvt_pk_bf16_f32 v24,0,v24\nv_cvt_pk_bf16_f32 v25,0,v25\n" \
"v_pk_mul_f32 v[60:61],v[24:25],v[96:97]\nv_cvt_pk_bf16_f32 v26,0,v26\nv_cvt_pk_bf16_f32 v27,0,v27\nv_pk_mul_f32 v[26:27],v[26:27],v[58:59] op_sel:[0,1]\nv_cvt_pk_bf16_f32 v26,0,v26\nv_cvt_pk_bf16_f32 v27,0,v27\nv_pk_mul_f32 v[26:27],v[26:27],v[34:35]\nv_cvt_pk_bf16_f32 v26,0,v26\n" \
"v_cvt_pk_bf16_f32 v27,0,v27\nv_pk_add_f32 v[26:27],v[26:27],v[18:19]\nv_cvt_pk_bf16_f32 v26,0,v26\nv_cvt_pk_bf16_f32 v27,0,v27\nv_pk_fma_f32 v[60:61],v[26:27],v[98:99],v[60:61]\nv_cvt_pk_bf16_f32 v28,0,v28\nv_cvt_pk_bf16_f32 v29,0,v29\nv_pk_mul_f32 v[28:29],v[28:29],v[58:59] op_sel:[0,1]\n" \
"v_cvt_pk_bf16_f32 v28,0,v28\nv_cvt_pk_bf16_f32 v29,0,v29\nv_pk_mul_f32 v[28:29],v[28:29],v[36:37]\nv_cvt_pk_bf16_f32 v28,0,v28\nv_cvt_pk_bf16_f32 v29,0,v29\nv_pk_add_f32 v[28:29],v[28:29],v[20:21]\nv_cvt_pk_bf16_f32 v28,0,v28\nv_cvt_pk_bf16_f32 v29,0,v29\n" \
"v_pk_fma_f32 v[60:61],v[28:29],v[100:101],v[60:61]\nv_cvt_pk_bf16_f32 v30,0,v30\nv_cvt_pk_bf16_f32 v31,0,v31\nv_pk_mul_f32 v[30:31],v[30:31],v[58:59] op_sel:[0,1]\nv_cvt_pk_bf16_f32 v30,0,v30\nv_cvt_pk_bf16_f32 v31,0,v31\nv_pk_mul_f32 v[30:31],v[30:31],v[38:39]\nv_cvt_pk_bf16_f32 v30,0,v30\n" \
"v_cvt_pk_bf16_f32 v31,0,v31\nv_pk_add_f32 v[30:31],v[30:31],v[22:23]\nv_cvt_pk_bf16_f32 v30,0,v30\nv_cvt_pk_bf16_f32 v31,0,v31\nv_pk_fma_f32 v[60:61],v[30:31],v[102:103],v[60:61]\nv_add_f32 v60,v60,v61\nv_mov_b32 v61,v60\nv_lshrrev_b32 v40,16,v57\n" \
"v_or_b32 v40,v40,v58\nv_permlane16_swap_b32 v60,v61\nv_add_f32 v60,v60,v61\nv_mov_b32 v61,v60\ns_nop 1\nv_permlane32_swap_b32 v60,v61\nv_add_f32 v60,v60,v61\ns_nop 1\n" \
"v_add_f32_dpp v60,v60,v60 quad_perm:[1,0,3,2] row_mask:0xf bank_mask:0xf\ns_nop 1\nv_add_f32_dpp v60,v60,v60 quad_perm:[2,3,0,1] row_mask:0xf bank_mask:0xf\ns_nop 1\nv_add_f32_dpp v60,v60,v60 row_half_mirror row_mask:0xf bank_mask:0xf\ns_nop 1\nv_add_f32_dpp v60,v60,v60 row_mirror row_mask:0xf bank_mask:0xf\nv_cvt_pk_bf16_f32 v60,0,v60\n" \
"s_nop 0\nv_readfirstlane_b32 s76,v60\ns_nop 0\ns_ashr_i32 s71,s76,31\ns_and_b32 s72,s76,0x7fffffff\ns_or_b32 s71,s71,0x80000000\ns_xor_b32 s76,s76,s71\ns_and_b32 s76,s76,0xffff0000\n" \
"s_cmp_gt_u32 s72,0x7f800000\ns_cselect_b32 s76,0,s76\ns_or_b32 s76,s76,s39\nv_mov_b32 v41,s76\nds_max_u64 v250,v[40:41]\ns_waitcnt lgkmcnt(0)\ns_barrier\nds_read_b64 v[224:225],v250\n" \
"s_waitcnt lgkmcnt(0)\nbuffer_store_dwordx2 v[224:225],v246,s[52:55],s37 offen sc1\nv_readfirstlane_b32 s97,v225\ns_sub_u32 s95,s95,54525952\ns_sub_u32 s74,s74,54525952\ns_mov_b32 s94,0\ns_cmp_eq_u32 s97,s77\ns_cselect_b32 s65,s95,0x40000000\n" \
"s_cselect_b32 s95,0x40000000,s95\ns_cmp_eq_u32 s97,s76\ns_cselect_b32 s66,s74,0x40000000\ns_cselect_b32 s74,0x40000000,s74\ns_lshr_b32 s70,s6,7\ns_or_b32 s38,s7,s70\ns_cmp_lg_u32 s3,0\ns_waitcnt vmcnt(1)\n" \
"s_cbranch_scc1 .Lkp\nv_pk_mul_f32 v[88:89],v[88:89],v[48:49]\nv_pk_mul_f32 v[90:91],v[90:91],v[50:51]\nv_pk_mul_f32 v[92:93],v[92:93],v[52:53]\nv_pk_mul_f32 v[94:95],v[94:95],v[54:55]\nv_cvt_pk_bf16_f32 v88,0,v88\nv_cvt_pk_bf16_f32 v89,0,v89\nv_cvt_pk_bf16_f32 v90,0,v90\n" \
"v_cvt_pk_bf16_f32 v91,0,v91\nv_cvt_pk_bf16_f32 v92,0,v92\nv_cvt_pk_bf16_f32 v93,0,v93\nv_cvt_pk_bf16_f32 v94,0,v94\nv_cvt_pk_bf16_f32 v95,0,v95\nv_pk_add_f32 v[88:89],v[88:89],v[72:73]\nv_pk_add_f32 v[90:91],v[90:91],v[74:75]\nv_pk_add_f32 v[92:93],v[92:93],v[76:77]\n" \
"v_pk_add_f32 v[94:95],v[94:95],v[78:79]\nv_cvt_pk_bf16_f32 v88,0,v88\nds_write_b16_d16_hi v244,v88 offset:0\nv_cvt_pk_bf16_f32 v89,0,v89\nds_write_b16_d16_hi v244,v89 offset:32\nv_cvt_pk_bf16_f32 v90,0,v90\nds_write_b16_d16_hi v244,v90 offset:64\nv_cvt_pk_bf16_f32 v91,0,v91\n" \
"ds_write_b16_d16_hi v244,v91 offset:96\nv_cvt_pk_bf16_f32 v92,0,v92\nds_write_b16_d16_hi v244,v92 offset:128\nv_cvt_pk_bf16_f32 v93,0,v93\nds_write_b16_d16_hi v244,v93 offset:160\nv_cvt_pk_bf16_f32 v94,0,v94\nds_write_b16_d16_hi v244,v94 offset:192\nv_cvt_pk_bf16_f32 v95,0,v95\n" \
"ds_write_b16_d16_hi v244,v95 offset:224\nds_read_b128 v[64:67],v245 offset:0\nv_pk_mul_f32 v[24:25],v[24:25],v[48:49]\nv_pk_mul_f32 v[26:27],v[26:27],v[50:51]\nv_pk_mul_f32 v[28:29],v[28:29],v[52:53]\nv_pk_mul_f32 v[30:31],v[30:31],v[54:55]\nv_cvt_pk_bf16_f32 v24,0,v24\nv_cvt_pk_bf16_f32 v25,0,v25\n" \
"v_cvt_pk_bf16_f32 v26,0,v26\nv_cvt_pk_bf16_f32 v27,0,v27\nv_cvt_pk_bf16_f32 v28,0,v28\nv_cvt_pk_bf16_f32 v29,0,v29\nv_cvt_pk_bf16_f32 v30,0,v30\nv_cvt_pk_bf16_f32 v31,0,v31\nv_pk_add_f32 v[24:25],v[24:25],v[8:9]\nv_pk_add_f32 v[26:27],v[26:27],v[10:11]\n" \
"v_pk_add_f32 v[28:29],v[28:29],v[12:13]\nv_pk_add_f32 v[30:31],v[30:31],v[14:15]\nv_cvt_pk_bf16_f32 v24,0,v24\nds_write_b16_d16_hi v244,v24 offset:1024\nv_cvt_pk_bf16_f32 v25,0,v25\nds_write_b16_d16_hi v244,v25 offset:1056\nv_cvt_pk_bf16_f32 v26,0,v26\nds_write_b16_d16_hi v244,v26 offset:1088\n" \
"v_cvt_pk_bf16_f32 v27,0,v27\nds_write_b16_d16_hi v244,v27 offset:1120\nv_cvt_pk_bf16_f32 v28,0,v28\nds_write_b16_d16_hi v244,v28 offset:1152\nv_cvt_pk_bf16_f32 v29,0,v29\nds_write_b16_d16_hi v244,v29 offset:1184\nv_cvt_pk_bf16_f32 v30,0,v30\nds_write_b16_d16_hi v244,v30 offset:1216\n" \
"v_cvt_pk_bf16_f32 v31,0,v31\nds_write_b16_d16_hi v244,v31 offset:1248\nds_read_b128 v[68:71],v245 offset:1024\ns_waitcnt lgkmcnt(0)\nbuffer_store_dwordx4 v[64:67],v240,s[44:47],s95 offen sc1\nbuffer_store_dwordx4 v[68:71],v240,s[44:47],s74 offen sc1\ns_cmp_eq_u32 s7,0\ns_cselect_b32 s70,1,0\n" \
"s_xor_b32 s71,s65,s66\ns_or_b32 s70,s70,s71\ns_cbranch_scc0 .Lfin\ns_lshr_b32 s70,s6,3\ns_and_b32 s70,s70,15\ns_lshl4_add_u32 s98,s60,s70\nv_lshrrev_b32 v242,4,v249\ns_lshl_b32 s71,s98,2\n" \
"v_add_u32 v242,s71,v242\nv_and_b32 v244,15,v242\nv_bfe_u32 v245,v242,4,3\nv_lshl_add_u32 v244,v244,3,v245\nv_and_b32 v245,0x180,v242\nv_add_lshl_u32 v243,v244,v245,1\nv_lshlrev_b32 v244,1,v243\ns_mov_b32 s70,4096\n" \
"v_lshlrev_b32 v242,1,v242\nbuffer_load_dword v62,v244,s[52:55],0 offen offset:0 sc1\nbuffer_load_dword v63,v244,s[52:55],0 offen offset:2048 sc1\nbuffer_load_dword v126,v244,s[52:55],s70 offen offset:0 sc1\nbuffer_load_dword v127,v244,s[52:55],s70 offen offset:2048 sc1\n.Lkp:\ns_cmp_lg_u32 s7,0\ns_cbranch_scc1 .Lkl\n" \
"v_max3_u32 v228,v233,v235,v237\nv_min3_u32 v229,v233,v235,v237\nv_max_u32 v228,v228,v239\nv_min_u32 v229,v229,v239\nv_cmp_lt_u32 vcc,s94,v228\nv_cmp_eq_u32 s[90:91],0,v229\ns_cbranch_vccnz .Lnew\ns_cmp_eq_u64 s[90:91],0\n" \
"s_cbranch_scc1 .Lv0\ns_sub_u32 s73,s73,1\ns_cselect_b32 s73,0,s73\ns_cbranch_scc1 .Lv0\ns_sleep 3\nbuffer_load_dwordx4 v[232:235],v251,s[52:55],s80 offen sc1\nbuffer_load_dwordx4 v[236:239],v251,s[52:55],s80 offen offset:16 sc1\ns_waitcnt vmcnt(0)\n" \
"s_branch .Lkp\n.Lv0:\nv_mov_b32 v230,s96\ns_or_b32 s72,s94,2\nv_mov_b32 v231,s72\nds_write_b64 v250,v[230:231] offset:8\ns_branch .Lver\n.Lkl:\n" \
"ds_read_b64 v[230:231],v250 offset:8\ns_mov_b32 s91,0\ns_waitcnt lgkmcnt(0)\nv_readfirstlane_b32 s72,v231\nv_readfirstlane_b32 s96,v230\ns_and_b32 s90,s72,2\ns_xor_b32 s72,s72,s90\ns_xor_b32 s90,s90,2\n" \
"s_cmp_lg_u32 s72,s94\ns_mov_b32 s94,s72\ns_cbranch_scc1 .Lsp\ns_cmp_eq_u32 s90,0\ns_cbranch_scc1 .Lver\ns_sub_u32 s73,s73,1\ns_cselect_b32 s73,0,s73\ns_cbranch_scc1 .Lver\n" \
"s_sleep 1\n.Lyield:\ns_branch .Lkl\n.Lnew:\nv_mov_b32 v230,v228\nv_cmp_eq_u32 vcc,v228,v233\ns_cmp_eq_u64 s[90:91],0\ns_cselect_b32 s71,2,0\n" \
"v_max_u32_dpp v230,v230,v230 quad_perm:[1,0,3,2] row_mask:0xf bank_mask:0xf\nv_cndmask_b32 v229,v238,v232,vcc\nv_cmp_eq_u32 vcc,v228,v235\nv_max_u32_dpp v230,v230,v230 quad_perm:[2,3,0,1] row_mask:0xf bank_mask:0xf\ns_nop 1\nv_max_u32_dpp v230,v230,v230 row_half_mirror row_mask:0xf bank_mask:0xf\nv_cndmask_b32 v229,v229,v234,vcc\nv_cmp_eq_u32 vcc,v228,v237\n" \
"v_max_u32_dpp v230,v230,v230 row_mirror row_mask:0xf bank_mask:0xf\nv_mov_b32 v231,v230\nv_cndmask_b32 v229,v229,v236,vcc\ns_nop 0\nv_permlane16_swap_b32 v230,v231\nv_max_u32 v230,v230,v231\nv_mov_b32 v231,v230\ns_nop 1\n" \
"v_permlane32_swap_b32 v230,v231\nv_max_u32 v230,v230,v231\ns_nop 0\nv_readfirstlane_b32 s94,v230\ns_nop 1\nv_cmp_eq_u32 vcc,s94,v228\ns_nop 0\ns_ff1_i32_b64 s72,vcc\n" \
"v_readlane_b32 s96,v229,s72\ns_nop 1\nv_mov_b32 v230,s96\ns_or_b32 s72,s94,s71\nv_mov_b32 v231,s72\nds_write_b64 v250,v[230:231] offset:8\n.Lsp:\ns_not_b32 s15,s94\n" \
"s_bfe_u32 s15,s15,0xb0005\ns_lshl_b32 s15,s15,10\ns_cmp_eq_u32 s3,0\ns_cbranch_scc1 .Lkp\ns_add_u32 s72,s15,54525952\nbuffer_load_dword v129,v241,s[52:55],s72 offen sc1\ns_add_u32 s72,s15,56623104\nbuffer_load_dword v131,v241,s[52:55],s72 offen sc1\n" \
"s_add_u32 s72,s15,58720256\nbuffer_load_dword v133,v241,s[52:55],s72 offen sc1\ns_add_u32 s72,s15,60817408\nbuffer_load_dword v135,v241,s[52:55],s72 offen sc1\ns_add_u32 s72,s15,62914560\nbuffer_load_dword v137,v241,s[52:55],s72 offen sc1\ns_add_u32 s72,s15,65011712\nbuffer_load_dword v139,v241,s[52:55],s72 offen sc1\n" \
"s_add_u32 s72,s15,67108864\nbuffer_load_dword v141,v241,s[52:55],s72 offen sc1\ns_add_u32 s72,s15,69206016\nbuffer_load_dword v143,v241,s[52:55],s72 offen sc1\nv_add_u32 v146,s15,v248\nglobal_load_ushort v144,v146,s[8:9]\nglobal_load_ushort v145,v146,s[8:9] offset:32\nbuffer_load_dwordx4 v[232:235],v251,s[52:55],s80 offen sc1\n" \
"buffer_load_dwordx4 v[236:239],v251,s[52:55],s80 offen offset:16 sc1\nv_accvgpr_read_b32 v161,a200\nv_lshlrev_b32 v160,16,v161\nv_and_b32 v161,0xffff0000,v161\nv_accvgpr_read_b32 v163,a201\nv_lshlrev_b32 v162,16,v163\nv_and_b32 v163,0xffff0000,v163\nv_accvgpr_read_b32 v165,a202\n" \
"v_lshlrev_b32 v164,16,v165\nv_and_b32 v165,0xffff0000,v165\nv_accvgpr_read_b32 v167,a203\nv_lshlrev_b32 v166,16,v167\nv_and_b32 v167,0xffff0000,v167\nv_accvgpr_read_b32 v169,a204\nv_lshlrev_b32 v168,16,v169\nv_and_b32 v169,0xffff0000,v169\n" \
"v_accvgpr_read_b32 v171,a205\nv_lshlrev_b32 v170,16,v171\nv_and_b32 v171,0xffff0000,v171\nv_accvgpr_read_b32 v173,a206\nv_lshlrev_b32 v172,16,v173\nv_and_b32 v173,0xffff0000,v173\nv_accvgpr_read_b32 v175,a207\nv_lshlrev_b32 v174,16,v175\n" \
"v_and_b32 v175,0xffff0000,v175\nv_accvgpr_read_b32 v177,a208\nv_lshlrev_b32 v176,16,v177\nv_and_b32 v177,0xffff0000,v177\nv_accvgpr_read_b32 v179,a209\nv_lshlrev_b32 v178,16,v179\nv_and_b32 v179,0xffff0000,v179\nv_accvgpr_read_b32 v181,a210\n" \
"v_lshlrev_b32 v180,16,v181\nv_and_b32 v181,0xffff0000,v181\nv_accvgpr_read_b32 v183,a211\nv_lshlrev_b32 v182,16,v183\nv_and_b32 v183,0xffff0000,v183\nv_accvgpr_read_b32 v185,a252\nv_lshlrev_b32 v184,16,v185\nv_and_b32 v185,0xffff0000,v185\n" \
"v_accvgpr_read_b32 v187,a253\nv_lshlrev_b32 v186,16,v187\nv_and_b32 v187,0xffff0000,v187\nv_accvgpr_read_b32 v189,a254\nv_lshlrev_b32 v188,16,v189\nv_and_b32 v189,0xffff0000,v189\nv_accvgpr_read_b32 v191,a255\nv_lshlrev_b32 v190,16,v191\n" \
"v_and_b32 v191,0xffff0000,v191\ns_and_b32 s82,s96,0xffff0000\ns_lshl_b32 s83,s96,16\nv_rcp_f32 v148,s83\ns_waitcnt vmcnt(11)\nv_lshlrev_b32 v128,16,v129\nv_and_b32 v129,0xffff0000,v129\ns_waitcnt vmcnt(10)\n" \
"v_lshlrev_b32 v130,16,v131\nv_and_b32 v131,0xffff0000,v131\nv_pk_add_f32 v[128:129],v[128:129],v[130:131]\ns_waitcnt vmcnt(9)\nv_lshlrev_b32 v132,16,v133\nv_and_b32 v133,0xffff0000,v133\ns_waitcnt vmcnt(8)\nv_lshlrev_b32 v134,16,v135\n" \
"v_and_b32 v135,0xffff0000,v135\nv_pk_add_f32 v[132:133],v[132:133],v[134:135]\nv_pk_add_f32 v[128:129],v[128:129],v[132:133]\ns_waitcnt vmcnt(7)\nv_lshlrev_b32 v136,16,v137\nv_and_b32 v137,0xffff0000,v137\ns_waitcnt vmcnt(6)\nv_lshlrev_b32 v138,16,v139\n" \
"v_and_b32 v139,0xffff0000,v139\nv_pk_add_f32 v[136:137],v[136:137],v[138:139]\ns_waitcnt vmcnt(5)\nv_lshlrev_b32 v140,16,v141\nv_and_b32 v141,0xffff0000,v141\ns_waitcnt vmcnt(4)\nv_lshlrev_b32 v142,16,v143\nv_and_b32 v143,0xffff0000,v143\n" \
"v_pk_add_f32 v[140:141],v[140:141],v[142:143]\nv_pk_add_f32 v[136:137],v[136:137],v[140:141]\nv_pk_add_f32 v[128:129],v[128:129],v[136:137]\nv_cvt_pk_bf16_f32 v128,0,v128\nv_cvt_pk_bf16_f32 v129,0,v129\nv_pk_mul_f32 v[128:129],v[128:129],v[112:113]\nv_cvt_pk_bf16_f32 v128,0,v128\nv_cvt_pk_bf16_f32 v129,0,v129\n" \
"s_waitcnt vmcnt(3)\nv_lshlrev_b32 v144,16,v144\ns_waitcnt vmcnt(2)\nv_lshlrev_b32 v145,16,v145\nv_pk_add_f32 v[128:129],v[128:129],v[144:145]\nv_cvt_pk_bf16_f32 v128,0,v128\nv_cvt_pk_bf16_f32 v129,0,v129\nv_pk_add_f32 v[128:129],v[128:129],s[82:83] op_sel_hi:[1,0] neg_lo:[0,1] neg_hi:[0,1]\n" \
"v_cvt_pk_bf16_f32 v128,0,v128\nv_cvt_pk_bf16_f32 v129,0,v129\nv_pk_mul_f32 v[128:129],v[128:129],v[148:149] op_sel_hi:[1,0]\nv_cvt_pk_bf16_f32 v128,0,v128\nv_cvt_pk_bf16_f32 v129,0,v129\nv_pk_mul_f32 v[128:129],v[128:129],v[116:117]\nv_cvt_pk_bf16_f32 v128,0,v128\nv_cvt_pk_bf16_f32 v129,0,v129\n" \
"v_pk_add_f32 v[128:129],v[128:129],v[114:115]\nv_cvt_pk_bf16_f32 v128,v128,v129\ns_nop 1\nv_mov_b32_dpp v132,v128 quad_perm:[0,0,0,0] row_mask:0xf bank_mask:0xf\nv_mov_b32_dpp v133,v128 quad_perm:[1,1,1,1] row_mask:0xf bank_mask:0xf\nv_mov_b32_dpp v134,v128 quad_perm:[2,2,2,2] row_mask:0xf bank_mask:0xf\nv_mov_b32_dpp v135,v128 quad_perm:[3,3,3,3] row_mask:0xf bank_mask:0xf\nv_dot2_f32_bf16 v136,v192,v132,0\n" \
"v_dot2_f32_bf16 v137,v193,v132,0\nv_dot2_f32_bf16 v138,v194,v132,0\nv_dot2_f32_bf16 v139,v195,v132,0\nv_dot2_f32_bf16 v140,v208,v132,0\nv_dot2_f32_bf16 v141,v209,v132,0\nv_dot2_f32_bf16 v142,v210,v132,0\nv_dot2_f32_bf16 v143,v211,v132,0\nv_dot2_f32_bf16 v136,v196,v133,v136\n" \
"v_dot2_f32_bf16 v137,v197,v133,v137\nv_dot2_f32_bf16 v138,v198,v133,v138\nv_dot2_f32_bf16 v139,v199,v133,v139\nv_dot2_f32_bf16 v140,v212,v133,v140\nv_dot2_f32_bf16 v141,v213,v133,v141\nv_dot2_f32_bf16 v142,v214,v133,v142\nv_dot2_f32_bf16 v143,v215,v133,v143\nv_dot2_f32_bf16 v136,v200,v134,v136\n" \
"v_dot2_f32_bf16 v137,v201,v134,v137\nv_dot2_f32_bf16 v138,v202,v134,v138\nv_dot2_f32_bf16 v139,v203,v134,v139\nv_dot2_f32_bf16 v140,v216,v134,v140\nv_dot2_f32_bf16 v141,v217,v134,v141\nv_dot2_f32_bf16 v142,v218,v134,v142\nv_dot2_f32_bf16 v143,v219,v134,v143\nv_dot2_f32_bf16 v136,v204,v135,v136\n" \
"v_dot2_f32_bf16 v137,v205,v135,v137\nv_dot2_f32_bf16 v138,v206,v135,v138\nv_dot2_f32_bf16 v139,v207,v135,v139\nv_dot2_f32_bf16 v140,v220,v135,v140\nv_dot2_f32_bf16 v141,v221,v135,v141\nv_dot2_f32_bf16 v142,v222,v135,v142\nv_dot2_f32_bf16 v143,v223,v135,v143\nv_permlane32_swap_b32 v136,v140\n" \
"v_add_f32 v136,v136,v140\nv_permlane32_swap_b32 v137,v141\nv_add_f32 v137,v137,v141\nv_permlane32_swap_b32 v138,v142\nv_add_f32 v138,v138,v142\nv_permlane32_swap_b32 v139,v143\nv_add_f32 v139,v139,v143\nv_permlane16_swap_b32 v136,v138\n" \
"v_add_f32 v136,v136,v138\nv_permlane16_swap_b32 v137,v139\nv_add_f32 v137,v137,v139\nv_add_f32_dpp v136,v136,v136 row_ror:4 row_mask:0xf bank_mask:0xf\ns_nop 1\nv_add_f32_dpp v136,v136,v136 row_ror:8 row_mask:0xf bank_mask:0xf\nv_add_f32_dpp v137,v137,v137 row_ror:4 row_mask:0xf bank_mask:0xf\ns_nop 1\n" \
"v_add_f32_dpp v137,v137,v137 row_ror:8 row_mask:0xf bank_mask:0xf\ns_cmp_eq_u64 s[90:91],0\ns_cbranch_scc1 .Lver\ns_waitcnt vmcnt(0)\ns_branch .Lkp\n.Lver:\ns_cmp_eq_u32 s3,0\ns_cbranch_scc1 .Lnf\n" \
"ds_write_b32 v252,v136\nds_write_b32 v252,v137 offset:16\ns_lshl_b32 s70,s2,6\ns_add_u32 s70,s70,98304\nv_lshlrev_b32 v241,1,v241\ns_waitcnt lgkmcnt(0)\ns_barrier\nds_read_b128 v[128:131],v253\n" \
"ds_read_b128 v[132:135],v253 offset:256\ns_waitcnt lgkmcnt(1)\nv_pk_add_f32 v[128:129],v[128:129],v[130:131]\nv_add_f32 v128,v128,v129\nv_cvt_pk_bf16_f32 v128,0,v128\nv_mul_f32 v129,0xbfb8aa3b,v128\nv_exp_f32 v129,v129\ns_waitcnt lgkmcnt(0)\n" \
"v_pk_add_f32 v[132:133],v[132:133],v[134:135]\nv_add_f32 v129,1.0,v129\nv_rcp_f32 v129,v129\nv_add_f32 v132,v132,v133\nv_cvt_pk_bf16_f32 v132,0,v132\nv_mul_f32 v128,v128,v129\nv_cvt_pk_bf16_f32 v128,0,v128\nv_mul_f32 v128,v128,v132\n" \
"v_cvt_pk_bf16_f32 v128,0,v128\ns_nop 0\nv_readlane_b32 s8,v128,0\nv_readlane_b32 s9,v128,1\nv_readlane_b32 s10,v128,2\nv_readlane_b32 s11,v128,3\nv_readlane_b32 s12,v128,4\nv_readlane_b32 s13,v128,5\n" \
"v_readlane_b32 s14,v128,6\nv_readlane_b32 s15,v128,7\nv_readlane_b32 s16,v128,8\nv_readlane_b32 s17,v128,9\nv_readlane_b32 s18,v128,10\nv_readlane_b32 s19,v128,11\nv_readlane_b32 s20,v128,12\nv_readlane_b32 s21,v128,13\n" \
"v_readlane_b32 s22,v128,14\nv_readlane_b32 s23,v128,15\nv_pk_mul_f32 v[136:137],v[160:161],s[8:9] op_sel_hi:[1,0]\nv_pk_mul_f32 v[138:139],v[162:163],s[8:9] op_sel:[0,1]\nv_pk_fma_f32 v[136:137],v[164:165],s[10:11],v[136:137] op_sel_hi:[1,0,1]\nv_pk_fma_f32 v[138:139],v[166:167],s[10:11],v[138:139] op_sel:[0,1,0]\nv_pk_fma_f32 v[136:137],v[168:169],s[12:13],v[136:137] op_sel_hi:[1,0,1]\nv_pk_fma_f32 v[138:139],v[170:171],s[12:13],v[138:139] op_sel:[0,1,0]\n" \
"v_pk_fma_f32 v[136:137],v[172:173],s[14:15],v[136:137] op_sel_hi:[1,0,1]\nv_pk_fma_f32 v[138:139],v[174:175],s[14:15],v[138:139] op_sel:[0,1,0]\nv_pk_fma_f32 v[136:137],v[176:177],s[16:17],v[136:137] op_sel_hi:[1,0,1]\nv_pk_fma_f32 v[138:139],v[178:179],s[16:17],v[138:139] op_sel:[0,1,0]\nv_pk_fma_f32 v[136:137],v[180:181],s[18:19],v[136:137] op_sel_hi:[1,0,1]\nv_pk_fma_f32 v[138:139],v[182:183],s[18:19],v[138:139] op_sel:[0,1,0]\nv_pk_fma_f32 v[136:137],v[184:185],s[20:21],v[136:137] op_sel_hi:[1,0,1]\nv_pk_fma_f32 v[138:139],v[186:187],s[20:21],v[138:139] op_sel:[0,1,0]\n" \
"v_pk_fma_f32 v[136:137],v[188:189],s[22:23],v[136:137] op_sel_hi:[1,0,1]\nv_pk_fma_f32 v[138:139],v[190:191],s[22:23],v[138:139] op_sel:[0,1,0]\nv_pk_add_f32 v[136:137],v[136:137],v[138:139]\nv_min_u32 v136,-2,v136\nv_min_u32 v137,-2,v137\nbuffer_store_dwordx2 v[136:137],v241,s[52:55],s70 offen sc1\nv_pk_mul_f32 v[88:89],v[88:89],v[48:49]\nv_pk_mul_f32 v[90:91],v[90:91],v[50:51]\n" \
"v_pk_mul_f32 v[92:93],v[92:93],v[52:53]\nv_pk_mul_f32 v[94:95],v[94:95],v[54:55]\nv_cvt_pk_bf16_f32 v88,0,v88\nv_cvt_pk_bf16_f32 v89,0,v89\nv_cvt_pk_bf16_f32 v90,0,v90\nv_cvt_pk_bf16_f32 v91,0,v91\nv_cvt_pk_bf16_f32 v92,0,v92\nv_cvt_pk_bf16_f32 v93,0,v93\n" \
"v_cvt_pk_bf16_f32 v94,0,v94\nv_cvt_pk_bf16_f32 v95,0,v95\nv_pk_add_f32 v[88:89],v[88:89],v[72:73]\nv_pk_add_f32 v[90:91],v[90:91],v[74:75]\nv_pk_add_f32 v[92:93],v[92:93],v[76:77]\nv_pk_add_f32 v[94:95],v[94:95],v[78:79]\nv_cvt_pk_bf16_f32 v88,0,v88\nds_write_b16_d16_hi v244,v88 offset:0\n" \
"v_cvt_pk_bf16_f32 v89,0,v89\nds_write_b16_d16_hi v244,v89 offset:32\nv_cvt_pk_bf16_f32 v90,0,v90\nds_write_b16_d16_hi v244,v90 offset:64\nv_cvt_pk_bf16_f32 v91,0,v91\nds_write_b16_d16_hi v244,v91 offset:96\nv_cvt_pk_bf16_f32 v92,0,v92\nds_write_b16_d16_hi v244,v92 offset:128\n" \
"v_cvt_pk_bf16_f32 v93,0,v93\nds_write_b16_d16_hi v244,v93 offset:160\nv_cvt_pk_bf16_f32 v94,0,v94\nds_write_b16_d16_hi v244,v94 offset:192\nv_cvt_pk_bf16_f32 v95,0,v95\nds_write_b16_d16_hi v244,v95 offset:224\nds_read_b128 v[64:67],v245 offset:0\nv_pk_mul_f32 v[24:25],v[24:25],v[48:49]\n" \
"v_pk_mul_f32 v[26:27],v[26:27],v[50:51]\nv_pk_mul_f32 v[28:29],v[28:29],v[52:53]\nv_pk_mul_f32 v[30:31],v[30:31],v[54:55]\nv_cvt_pk_bf16_f32 v24,0,v24\nv_cvt_pk_bf16_f32 v25,0,v25\nv_cvt_pk_bf16_f32 v26,0,v26\nv_cvt_pk_bf16_f32 v27,0,v27\nv_cvt_pk_bf16_f32 v28,0,v28\n" \
"v_cvt_pk_bf16_f32 v29,0,v29\nv_cvt_pk_bf16_f32 v30,0,v30\nv_cvt_pk_bf16_f32 v31,0,v31\nv_pk_add_f32 v[24:25],v[24:25],v[8:9]\nv_pk_add_f32 v[26:27],v[26:27],v[10:11]\nv_pk_add_f32 v[28:29],v[28:29],v[12:13]\nv_pk_add_f32 v[30:31],v[30:31],v[14:15]\nv_cvt_pk_bf16_f32 v24,0,v24\n" \
"ds_write_b16_d16_hi v244,v24 offset:1024\nv_cvt_pk_bf16_f32 v25,0,v25\nds_write_b16_d16_hi v244,v25 offset:1056\nv_cvt_pk_bf16_f32 v26,0,v26\nds_write_b16_d16_hi v244,v26 offset:1088\nv_cvt_pk_bf16_f32 v27,0,v27\nds_write_b16_d16_hi v244,v27 offset:1120\nv_cvt_pk_bf16_f32 v28,0,v28\n" \
"ds_write_b16_d16_hi v244,v28 offset:1152\nv_cvt_pk_bf16_f32 v29,0,v29\nds_write_b16_d16_hi v244,v29 offset:1184\nv_cvt_pk_bf16_f32 v30,0,v30\nds_write_b16_d16_hi v244,v30 offset:1216\nv_cvt_pk_bf16_f32 v31,0,v31\nds_write_b16_d16_hi v244,v31 offset:1248\nds_read_b128 v[68:71],v245 offset:1024\n" \
"s_waitcnt lgkmcnt(0)\nbuffer_store_dwordx4 v[64:67],v240,s[44:47],s95 offen sc1\nbuffer_store_dwordx4 v[68:71],v240,s[44:47],s74 offen sc1\n.Lnf:\ns_cmp_eq_u32 s94,s97\ns_cbranch_scc1 .Lwin\nbuffer_store_dwordx4 v[64:67],v240,s[44:47],s65 offen sc1\nbuffer_store_dwordx4 v[68:71],v240,s[44:47],s66 offen sc1\n" \
".Lwin:\ns_cmp_lg_u32 s38,0\ns_cbranch_scc1 .Lfin\ns_add_u32 s72,s15,54525952\nbuffer_load_ushort v128,v243,s[52:55],s72 offen sc1\ns_add_u32 s72,s15,56623104\nbuffer_load_ushort v129,v243,s[52:55],s72 offen sc1\ns_add_u32 s72,s15,58720256\n" \
"buffer_load_ushort v130,v243,s[52:55],s72 offen sc1\ns_add_u32 s72,s15,60817408\nbuffer_load_ushort v131,v243,s[52:55],s72 offen sc1\ns_add_u32 s72,s15,62914560\nbuffer_load_ushort v132,v243,s[52:55],s72 offen sc1\ns_add_u32 s72,s15,65011712\nbuffer_load_ushort v133,v243,s[52:55],s72 offen sc1\ns_add_u32 s72,s15,67108864\n" \
"buffer_load_ushort v134,v243,s[52:55],s72 offen sc1\ns_add_u32 s72,s15,69206016\nbuffer_load_ushort v135,v243,s[52:55],s72 offen sc1\nv_add_u32 v242,s15,v242\nglobal_load_ushort v144,v242,s[8:9]\nv_min_u32 v168,55,v249\nv_lshlrev_b32 v168,11,v168\ns_lshl4_add_u32 s70,s98,98304\n" \
"s_add_u32 s71,s70,114688\ns_and_b32 s82,s96,0xffff0000\ns_lshl_b32 s83,s96,16\nv_rcp_f32 v148,s83\ns_ashr_i32 s37,s94,31\ns_not_b32 s37,s37\ns_or_b32 s37,s37,0x80000000\ns_xor_b32 s37,s37,s94\n" \
"s_and_b32 s37,s37,0xffff0000\nv_mov_b32 v149,0xbfb8aa3b\nv_mul_f32 v149,s37,v149\nv_exp_f32 v149,v149\nv_and_b32 v150,15,v249\nv_add_f32 v149,1.0,v149\nv_rcp_f32 v149,v149\nv_cmp_eq_u32 vcc,0,v150\n" \
"v_cvt_pk_bf16_f32 v149,0,v149\nv_mov_b32 v150,0x40000000\nv_cndmask_b32 v150,v150,v242,vcc\nv_cmp_gt_u32 s[90:91],56,v249\ns_waitcnt vmcnt(8)\nv_lshlrev_b32 v128,16,v128\ns_waitcnt vmcnt(7)\nv_lshlrev_b32 v129,16,v129\n" \
"v_add_f32 v128,v128,v129\ns_waitcnt vmcnt(6)\nv_lshlrev_b32 v130,16,v130\ns_waitcnt vmcnt(5)\nv_lshlrev_b32 v131,16,v131\nv_add_f32 v130,v130,v131\nv_add_f32 v128,v128,v130\ns_waitcnt vmcnt(4)\n" \
"v_lshlrev_b32 v132,16,v132\ns_waitcnt vmcnt(3)\nv_lshlrev_b32 v133,16,v133\nv_add_f32 v132,v132,v133\ns_waitcnt vmcnt(2)\nv_lshlrev_b32 v134,16,v134\ns_waitcnt vmcnt(1)\nv_lshlrev_b32 v135,16,v135\n" \
"v_add_f32 v134,v134,v135\nv_add_f32 v132,v132,v134\nv_add_f32 v128,v128,v132\nv_cvt_pk_bf16_f32 v128,0,v128\nv_mul_f32 v128,v128,v62\nv_cvt_pk_bf16_f32 v128,0,v128\ns_waitcnt vmcnt(0)\nv_add_f32 v126,1.0,v126\n" \
"v_cvt_pk_bf16_f32 v126,0,v126\nv_lshlrev_b32 v144,16,v144\nv_add_f32 v128,v128,v144\nv_cvt_pk_bf16_f32 v128,0,v128\nv_subrev_f32 v129,s82,v128\nv_cvt_pk_bf16_f32 v129,0,v129\nv_mul_f32 v129,v129,v148\nv_cvt_pk_bf16_f32 v129,0,v129\n" \
"v_mul_f32 v129,v129,v126\nv_cvt_pk_bf16_f32 v129,0,v129\nv_add_f32 v129,v129,v63\nv_cvt_pk_bf16_f32 v129,0,v129\n.Lp2:\nbuffer_load_dwordx4 v[160:163],v168,s[52:55],s70 offen sc1\nbuffer_load_dwordx4 v[164:167],v168,s[52:55],s71 offen sc1\ns_waitcnt vmcnt(0)\n" \
"v_max3_u32 v228,v160,v161,v162\nv_max3_u32 v229,v163,v164,v165\nv_max3_u32 v228,v228,v166,v167\nv_max_u32 v228,v228,v229\nv_pk_add_f32 v[160:161],v[160:161],v[164:165]\nv_cmp_eq_u32 vcc,-1,v228\nv_pk_add_f32 v[162:163],v[162:163],v[166:167]\ns_sub_u32 s73,s73,1\n" \
"s_cselect_b32 s73,0,s73\ns_cselect_b64 vcc,0,vcc\ns_cmp_lg_u64 vcc,0\ns_cselect_b32 s72,0x8000000,0\nv_cndmask_b32 v160,0,v160,s[90:91]\nv_cndmask_b32 v161,0,v161,s[90:91]\nv_cndmask_b32 v162,0,v162,s[90:91]\nv_cndmask_b32 v163,0,v163,s[90:91]\n" \
"s_nop 0\nv_permlane32_swap_b32 v160,v162\nv_permlane32_swap_b32 v161,v163\nv_pk_add_f32 v[160:161],v[160:161],v[162:163]\ns_nop 1\nv_permlane16_swap_b32 v160,v161\nv_add_f32 v160,v160,v161\ns_nop 1\n" \
"v_add_f32_dpp v160,v160,v160 quad_perm:[1,0,3,2] row_mask:0xf bank_mask:0xf\ns_nop 1\nv_add_f32_dpp v160,v160,v160 quad_perm:[2,3,0,1] row_mask:0xf bank_mask:0xf\ns_nop 1\nv_add_f32_dpp v160,v160,v160 row_half_mirror row_mask:0xf bank_mask:0xf\ns_nop 1\nv_add_f32_dpp v160,v160,v160 row_mirror row_mask:0xf bank_mask:0xf\nv_cvt_pk_bf16_f32 v160,0,v160\n" \
"v_mul_f32 v160,v160,v149\nv_cvt_pk_bf16_f32 v160,0,v160\nv_add_f32 v160,v160,v129\nv_cvt_pk_bf16_f32 v160,0,v160\nv_mul_f32 v160,v160,v127\nv_cvt_pk_bf16_f32 v160,0,v160\nv_add_f32 v160,v160,v128\nv_cvt_pk_bf16_f32 v160,0,v160\n" \
"buffer_store_short_d16_hi v160,v150,s[44:47],s72 offen sc1\n.Lp2s:\ns_cbranch_scc1 .Lp2\ns_branch .Lfin\n.Lhs:\ns_sub_u32 s73,s73,1\ns_cselect_b32 s73,0,s73\ns_cbranch_scc1 .Lhb\n" \
"s_sleep 2\nbuffer_load_dwordx2 v[166:167],v165,s[52:55],s80 offen sc1\ns_waitcnt vmcnt(0)\nv_xor_b32 v166,v166,v230\nv_xor_b32 v167,v167,v231\nv_or_b32 v166,v166,v167\nv_cmp_ne_u32 vcc,0,v166\ns_nop 0\n" \
"s_cbranch_vccnz .Lhs\ns_branch .Lhb\n.Lps0:\ns_sub_u32 s73,s73,1\ns_cselect_b32 s73,0,s73\ns_cbranch_scc1 .Lpb0\ns_sleep 1\nbuffer_load_dword v248,v228,s[52:55],s80 offen sc1\n" \
"s_waitcnt vmcnt(0)\nv_cmp_eq_u32 vcc,0,v248\ns_nop 0\ns_cbranch_vccnz .Lps0\ns_branch .Lpb0\n.Lps1:\ns_sub_u32 s73,s73,1\ns_cselect_b32 s73,0,s73\n" \
"s_cbranch_scc1 .Lpb1\ns_sleep 1\nbuffer_load_dword v248,v228,s[52:55],s80 offen sc1\ns_waitcnt vmcnt(0)\nv_cmp_eq_u32 vcc,0,v248\ns_nop 0\ns_cbranch_vccnz .Lps1\ns_branch .Lpb1\n" \
".Lps2:\ns_sub_u32 s73,s73,1\ns_cselect_b32 s73,0,s73\ns_cbranch_scc1 .Lpb2\ns_sleep 1\nbuffer_load_dword v248,v251,s[52:55],s80 offen sc1\ns_waitcnt vmcnt(0)\nv_cmp_eq_u32 vcc,0,v248\n" \
"s_nop 0\ns_cbranch_vccnz .Lps2\ns_branch .Lpb2\n.Lps3:\ns_sub_u32 s73,s73,1\ns_cselect_b32 s73,0,s73\ns_cbranch_scc1 .Lpb3\ns_sleep 1\n" \
"buffer_load_dword v248,v251,s[52:55],s80 offen sc1\ns_waitcnt vmcnt(0)\nv_cmp_eq_u32 vcc,0,v248\ns_nop 0\ns_cbranch_vccnz .Lps3\ns_branch .Lpb3\n.Lps4:\ns_sub_u32 s73,s73,1\n" \
"s_cselect_b32 s73,0,s73\ns_cbranch_scc1 .Lpb4\ns_sleep 1\nbuffer_load_dword v248,v251,s[52:55],s80 offen sc1\ns_waitcnt vmcnt(0)\nv_cmp_eq_u32 vcc,0,v248\ns_nop 0\ns_cbranch_vccnz .Lps4\n" \
"s_branch .Lpb4\n.Lps5:\ns_sub_u32 s73,s73,1\ns_cselect_b32 s73,0,s73\ns_cbranch_scc1 .Lpb5\ns_sleep 1\nbuffer_load_dword v248,v251,s[52:55],s80 offen sc1\ns_waitcnt vmcnt(0)\n" \
"v_cmp_eq_u32 vcc,0,v248\ns_nop 0\ns_cbranch_vccnz .Lps5\ns_branch .Lpb5\n.Lx01:\ns_cmp_eq_u32 s62,0\ns_cbranch_scc1 .Ltrans\ns_branch .Ldf\n" \
".Lps6:\ns_sub_u32 s73,s73,1\ns_cselect_b32 s73,0,s73\ns_cbranch_scc1 .Lpb6\ns_sleep 1\nbuffer_load_dword v248,v251,s[52:55],s80 offen sc1\ns_waitcnt vmcnt(0)\nv_and_b32 v248,s17,v248\n" \
"v_cmp_eq_u32 vcc,0,v248\ns_nop 0\ns_cbranch_vccnz .Lps6\ns_branch .Lpb6\n.Lps7:\ns_sub_u32 s73,s73,1\ns_cselect_b32 s73,0,s73\ns_cbranch_scc1 .Lpb7\n" \
"s_sleep 1\nbuffer_load_dword v248,v254,s[52:55],s80 offen sc1\ns_waitcnt vmcnt(0)\nv_cmp_eq_u32 vcc,0,v248\ns_nop 0\ns_cbranch_vccnz .Lps7\ns_branch .Lpb7\n.Lps8:\n" \
"s_sub_u32 s73,s73,1\ns_cselect_b32 s73,0,s73\ns_cbranch_scc1 .Lpb8\ns_sleep 1\nbuffer_load_dword v248,v251,s[52:55],s79 offen sc1\ns_waitcnt vmcnt(0)\nv_cmp_eq_u32 vcc,0,v248\ns_nop 0\n" \
"s_cbranch_vccnz .Lps8\ns_branch .Lpb8\n.Lrs1:\ns_mov_b32 s71,0\n.Lrl1:\ns_mov_b32 s97,0\nv_max3_u32 v124,v160,v161,v162\nv_max3_u32 v124,v124,v163,v163\n" \
"v_cmp_eq_u32 vcc,-1,v124\ns_add_u32 s72,s95,0\ns_cbranch_vccz .Lrz10\ns_mov_b64 exec,vcc\ns_mov_b32 s97,1\nbuffer_load_dwordx4 v[160:163],v240,s[52:55],s72 offen sc1\ns_mov_b64 exec,-1\n.Lrz10:\n" \
"v_max3_u32 v124,v164,v165,v166\nv_max3_u32 v124,v124,v167,v167\nv_cmp_eq_u32 vcc,-1,v124\ns_add_u32 s72,s95,2097152\ns_cbranch_vccz .Lrz11\ns_mov_b64 exec,vcc\ns_mov_b32 s97,1\nbuffer_load_dwordx4 v[164:167],v240,s[52:55],s72 offen sc1\n" \
"s_mov_b64 exec,-1\n.Lrz11:\nv_max3_u32 v124,v168,v169,v170\nv_max3_u32 v124,v124,v171,v171\nv_cmp_eq_u32 vcc,-1,v124\ns_add_u32 s72,s95,4194304\ns_cbranch_vccz .Lrz12\ns_mov_b64 exec,vcc\n" \
"s_mov_b32 s97,1\nbuffer_load_dwordx4 v[168:171],v240,s[52:55],s72 offen sc1\ns_mov_b64 exec,-1\n.Lrz12:\nv_max3_u32 v124,v172,v173,v174\nv_max3_u32 v124,v124,v175,v175\nv_cmp_eq_u32 vcc,-1,v124\ns_add_u32 s72,s95,6291456\n" \
"s_cbranch_vccz .Lrz13\ns_mov_b64 exec,vcc\ns_mov_b32 s97,1\nbuffer_load_dwordx4 v[172:175],v240,s[52:55],s72 offen sc1\ns_mov_b64 exec,-1\n.Lrz13:\nv_max3_u32 v124,v176,v177,v178\nv_max3_u32 v124,v124,v179,v179\n" \
"v_cmp_eq_u32 vcc,-1,v124\ns_add_u32 s72,s95,8388608\ns_cbranch_vccz .Lrz14\ns_mov_b64 exec,vcc\ns_mov_b32 s97,1\nbuffer_load_dwordx4 v[176:179],v240,s[52:55],s72 offen sc1\ns_mov_b64 exec,-1\n.Lrz14:\n" \
"v_max3_u32 v124,v180,v181,v182\nv_max3_u32 v124,v124,v183,v183\nv_cmp_eq_u32 vcc,-1,v124\ns_add_u32 s72,s95,10485760\ns_cbranch_vccz .Lrz15\ns_mov_b64 exec,vcc\ns_mov_b32 s97,1\nbuffer_load_dwordx4 v[180:183],v240,s[52:55],s72 offen sc1\n" \
"s_mov_b64 exec,-1\n.Lrz15:\nv_max3_u32 v124,v184,v185,v186\nv_max3_u32 v124,v124,v187,v187\nv_cmp_eq_u32 vcc,-1,v124\ns_add_u32 s72,s95,12582912\ns_cbranch_vccz .Lrz16\ns_mov_b64 exec,vcc\n" \
"s_mov_b32 s97,1\nbuffer_load_dwordx4 v[184:187],v240,s[52:55],s72 offen sc1\ns_mov_b64 exec,-1\n.Lrz16:\nv_max3_u32 v124,v188,v189,v190\nv_max3_u32 v124,v124,v191,v191\nv_cmp_eq_u32 vcc,-1,v124\ns_add_u32 s72,s95,14680064\n" \
"s_cbranch_vccz .Lrz17\ns_mov_b64 exec,vcc\ns_mov_b32 s97,1\nbuffer_load_dwordx4 v[188:191],v240,s[52:55],s72 offen sc1\ns_mov_b64 exec,-1\n.Lrz17:\ns_cmp_lg_u32 s97,0\ns_cbranch_scc1 .Lrm1\n" \
"s_cmp_eq_u32 s71,0\ns_cbranch_scc1 .Lrb1\ns_branch .Lrow1\n.Lrm1:\ns_mov_b32 s71,1\ns_waitcnt vmcnt(0)\ns_sub_u32 s73,s73,1\ns_cselect_b32 s73,0,s73\n" \
"s_cbranch_scc1 .Lrb1\ns_sleep 2\ns_branch .Lrl1\n.Lrs0:\ns_mov_b32 s71,0\n.Lrl0:\ns_mov_b32 s97,0\nv_max3_u32 v60,v128,v129,v130\n" \
"v_max3_u32 v60,v60,v131,v131\nv_cmp_eq_u32 vcc,-1,v60\ns_add_u32 s72,s74,0\ns_cbranch_vccz .Lrz00\ns_mov_b64 exec,vcc\ns_mov_b32 s97,1\nbuffer_load_dwordx4 v[128:131],v240,s[52:55],s72 offen sc1\ns_mov_b64 exec,-1\n" \
".Lrz00:\nv_max3_u32 v60,v132,v133,v134\nv_max3_u32 v60,v60,v135,v135\nv_cmp_eq_u32 vcc,-1,v60\ns_add_u32 s72,s74,2097152\ns_cbranch_vccz .Lrz01\ns_mov_b64 exec,vcc\ns_mov_b32 s97,1\n" \
"buffer_load_dwordx4 v[132:135],v240,s[52:55],s72 offen sc1\ns_mov_b64 exec,-1\n.Lrz01:\nv_max3_u32 v60,v136,v137,v138\nv_max3_u32 v60,v60,v139,v139\nv_cmp_eq_u32 vcc,-1,v60\ns_add_u32 s72,s74,4194304\ns_cbranch_vccz .Lrz02\n" \
"s_mov_b64 exec,vcc\ns_mov_b32 s97,1\nbuffer_load_dwordx4 v[136:139],v240,s[52:55],s72 offen sc1\ns_mov_b64 exec,-1\n.Lrz02:\nv_max3_u32 v60,v140,v141,v142\nv_max3_u32 v60,v60,v143,v143\nv_cmp_eq_u32 vcc,-1,v60\n" \
"s_add_u32 s72,s74,6291456\ns_cbranch_vccz .Lrz03\ns_mov_b64 exec,vcc\ns_mov_b32 s97,1\nbuffer_load_dwordx4 v[140:143],v240,s[52:55],s72 offen sc1\ns_mov_b64 exec,-1\n.Lrz03:\nv_max3_u32 v60,v144,v145,v146\n" \
"v_max3_u32 v60,v60,v147,v147\nv_cmp_eq_u32 vcc,-1,v60\ns_add_u32 s72,s74,8388608\ns_cbranch_vccz .Lrz04\ns_mov_b64 exec,vcc\ns_mov_b32 s97,1\nbuffer_load_dwordx4 v[144:147],v240,s[52:55],s72 offen sc1\ns_mov_b64 exec,-1\n" \
".Lrz04:\nv_max3_u32 v60,v148,v149,v150\nv_max3_u32 v60,v60,v151,v151\nv_cmp_eq_u32 vcc,-1,v60\ns_add_u32 s72,s74,10485760\ns_cbranch_vccz .Lrz05\ns_mov_b64 exec,vcc\ns_mov_b32 s97,1\n" \
"buffer_load_dwordx4 v[148:151],v240,s[52:55],s72 offen sc1\ns_mov_b64 exec,-1\n.Lrz05:\nv_max3_u32 v60,v152,v153,v154\nv_max3_u32 v60,v60,v155,v155\nv_cmp_eq_u32 vcc,-1,v60\ns_add_u32 s72,s74,12582912\ns_cbranch_vccz .Lrz06\n" \
"s_mov_b64 exec,vcc\ns_mov_b32 s97,1\nbuffer_load_dwordx4 v[152:155],v240,s[52:55],s72 offen sc1\ns_mov_b64 exec,-1\n.Lrz06:\nv_max3_u32 v60,v156,v157,v158\nv_max3_u32 v60,v60,v159,v159\nv_cmp_eq_u32 vcc,-1,v60\n" \
"s_add_u32 s72,s74,14680064\ns_cbranch_vccz .Lrz07\ns_mov_b64 exec,vcc\ns_mov_b32 s97,1\nbuffer_load_dwordx4 v[156:159],v240,s[52:55],s72 offen sc1\ns_mov_b64 exec,-1\n.Lrz07:\ns_cmp_lg_u32 s97,0\n" \
"s_cbranch_scc1 .Lrm0\ns_cmp_eq_u32 s71,0\ns_cbranch_scc1 .Lrb0\ns_branch .Lrow0\n.Lrm0:\ns_mov_b32 s71,1\ns_waitcnt vmcnt(0)\ns_sub_u32 s73,s73,1\n" \
"s_cselect_b32 s73,0,s73\ns_cbranch_scc1 .Lrb0\ns_sleep 2\ns_branch .Lrl0\n.Lredo:\ns_waitcnt vmcnt(0) lgkmcnt(0)\ns_barrier\ns_mov_b64 s[90:91],0\n" \
"s_mov_b32 s99,0\ns_mov_b32 s17,1\ns_lshl_b32 s70,s63,19\ns_add_u32 s37,s70,3145728\ns_lshl_b32 s71,s62,15\ns_add_u32 s72,s37,s64\ns_add_u32 s72,s72,491520\ns_sub_u32 s72,s72,s71\n" \
"s_add_u32 m0,s64,0\ns_nop 0\nbuffer_load_dwordx4 v240,s[52:55],s72 offen offset:0 sc1 lds\nbuffer_load_dwordx4 v240,s[52:55],s72 offen offset:1024 sc1 lds\nbuffer_load_dwordx4 v240,s[52:55],s72 offen offset:2048 sc1 lds\nbuffer_load_dwordx4 v240,s[52:55],s72 offen offset:3072 sc1 lds\ns_add_u32 m0,s64,65536\ns_add_u32 s72,s72,16777216\n" \
"buffer_load_dwordx4 v240,s[52:55],s72 offen offset:0 sc1 lds\nbuffer_load_dwordx4 v240,s[52:55],s72 offen offset:1024 sc1 lds\nbuffer_load_dwordx4 v240,s[52:55],s72 offen offset:2048 sc1 lds\nbuffer_load_dwordx4 v240,s[52:55],s72 offen offset:3072 sc1 lds\ns_add_u32 s72,s37,s64\ns_add_u32 s72,s72,507904\ns_sub_u32 s72,s72,s71\ns_add_u32 m0,s64,16384\n" \
"s_nop 0\nbuffer_load_dwordx4 v240,s[52:55],s72 offen offset:0 sc1 lds\nbuffer_load_dwordx4 v240,s[52:55],s72 offen offset:1024 sc1 lds\nbuffer_load_dwordx4 v240,s[52:55],s72 offen offset:2048 sc1 lds\nbuffer_load_dwordx4 v240,s[52:55],s72 offen offset:3072 sc1 lds\ns_add_u32 m0,s64,81920\ns_add_u32 s72,s72,16777216\nbuffer_load_dwordx4 v240,s[52:55],s72 offen offset:0 sc1 lds\n" \
"buffer_load_dwordx4 v240,s[52:55],s72 offen offset:1024 sc1 lds\nbuffer_load_dwordx4 v240,s[52:55],s72 offen offset:2048 sc1 lds\nbuffer_load_dwordx4 v240,s[52:55],s72 offen offset:3072 sc1 lds\ns_add_u32 s72,s37,s64\ns_add_u32 s72,s72,16384\ns_add_u32 s72,s72,s71\ns_add_u32 m0,s64,32768\ns_nop 0\n" \
"buffer_load_dwordx4 v240,s[52:55],s72 offen offset:0 sc1 lds\nbuffer_load_dwordx4 v240,s[52:55],s72 offen offset:1024 sc1 lds\nbuffer_load_dwordx4 v240,s[52:55],s72 offen offset:2048 sc1 lds\nbuffer_load_dwordx4 v240,s[52:55],s72 offen offset:3072 sc1 lds\ns_add_u32 m0,s64,98304\ns_add_u32 s72,s72,16777216\nbuffer_load_dwordx4 v240,s[52:55],s72 offen offset:0 sc1 lds\nbuffer_load_dwordx4 v240,s[52:55],s72 offen offset:1024 sc1 lds\n" \
"buffer_load_dwordx4 v240,s[52:55],s72 offen offset:2048 sc1 lds\nbuffer_load_dwordx4 v240,s[52:55],s72 offen offset:3072 sc1 lds\ns_add_u32 s72,s37,s64\ns_add_u32 s72,s72,0\ns_add_u32 s72,s72,s71\ns_add_u32 m0,s64,49152\ns_nop 0\nbuffer_load_dwordx4 v240,s[52:55],s72 offen offset:0 sc1 lds\n" \
"buffer_load_dwordx4 v240,s[52:55],s72 offen offset:1024 sc1 lds\nbuffer_load_dwordx4 v240,s[52:55],s72 offen offset:2048 sc1 lds\nbuffer_load_dwordx4 v240,s[52:55],s72 offen offset:3072 sc1 lds\ns_add_u32 m0,s64,114688\ns_add_u32 s72,s72,16777216\nbuffer_load_dwordx4 v240,s[52:55],s72 offen offset:0 sc1 lds\nbuffer_load_dwordx4 v240,s[52:55],s72 offen offset:1024 sc1 lds\nbuffer_load_dwordx4 v240,s[52:55],s72 offen offset:2048 sc1 lds\n" \
"buffer_load_dwordx4 v240,s[52:55],s72 offen offset:3072 sc1 lds\nv_and_b32 v251,15,v249\nv_lshrrev_b32 v250,4,v249\nv_lshlrev_b32 v250,2,v250\nv_sub_u32 v251,v251,v250\ns_lshl_b32 s70,s62,7\ns_lshl_b32 s72,s7,5\ns_sub_u32 s38,s72,s70\n" \
"s_add_u32 s38,s38,1920\ns_sub_u32 s39,s70,s72\ns_add_u32 s39,s39,96\nv_add_u32 v225,s38,v251\nv_add_u32 v225,0,v225\nv_mov_b32 v224,0xff800000\nv_add_u32 v229,s38,v251\nv_add_u32 v229,16,v229\n" \
"v_mov_b32 v228,0xff800000\nv_add_u32 v233,s39,v251\nv_add_u32 v233,0,v233\nv_mov_b32 v232,0xff800000\nv_add_u32 v237,s39,v251\nv_add_u32 v237,16,v237\nv_mov_b32 v236,0xff800000\ns_sub_u32 s38,524288,s71\n" \
"s_add_u32 s38,s38,s37\n.Lrl:\nbuffer_load_dwordx4 a[0:3],v240,s[52:55],s37 offen offset:0 sc1\nbuffer_load_dwordx4 a[4:7],v240,s[52:55],s37 offen offset:1024 sc1\nbuffer_load_dwordx4 a[8:11],v240,s[52:55],s37 offen offset:2048 sc1\nbuffer_load_dwordx4 a[12:15],v240,s[52:55],s37 offen offset:3072 sc1\ns_add_u32 s37,s37,4096\nbuffer_load_dwordx4 a[16:19],v240,s[52:55],s37 offen offset:0 sc1\n" \
"buffer_load_dwordx4 a[20:23],v240,s[52:55],s37 offen offset:1024 sc1\nbuffer_load_dwordx4 a[24:27],v240,s[52:55],s37 offen offset:2048 sc1\nbuffer_load_dwordx4 a[28:31],v240,s[52:55],s37 offen offset:3072 sc1\ns_add_u32 s37,s37,4096\nbuffer_load_dwordx4 a[32:35],v240,s[52:55],s37 offen offset:0 sc1\nbuffer_load_dwordx4 a[36:39],v240,s[52:55],s37 offen offset:1024 sc1\nbuffer_load_dwordx4 a[40:43],v240,s[52:55],s37 offen offset:2048 sc1\nbuffer_load_dwordx4 a[44:47],v240,s[52:55],s37 offen offset:3072 sc1\n" \
"s_add_u32 s37,s37,4096\nbuffer_load_dwordx4 a[48:51],v240,s[52:55],s37 offen offset:0 sc1\nbuffer_load_dwordx4 a[52:55],v240,s[52:55],s37 offen offset:1024 sc1\nbuffer_load_dwordx4 a[56:59],v240,s[52:55],s37 offen offset:2048 sc1\nbuffer_load_dwordx4 a[60:63],v240,s[52:55],s37 offen offset:3072 sc1\ns_add_u32 s37,s37,4096\ns_waitcnt vmcnt(15)\nM v[128:131],a[0:3],a[128:131],0\n" \
"s_waitcnt vmcnt(14)\nM v[128:131],a[4:7],a[132:135],v[128:131]\ns_waitcnt vmcnt(13)\nM v[128:131],a[8:11],a[136:139],v[128:131]\ns_waitcnt vmcnt(12)\nM v[128:131],a[12:15],a[140:143],v[128:131]\nM v[132:135],a[0:3],a[144:147],0\nM v[132:135],a[4:7],a[148:151],v[132:135]\n" \
"M v[132:135],a[8:11],a[152:155],v[132:135]\nM v[132:135],a[12:15],a[156:159],v[132:135]\nM v[136:139],a[0:3],a[160:163],0\nM v[136:139],a[4:7],a[164:167],v[136:139]\nM v[136:139],a[8:11],a[168:171],v[136:139]\nM v[136:139],a[12:15],a[172:175],v[136:139]\nM v[140:143],a[0:3],a[176:179],0\nM v[140:143],a[4:7],a[180:183],v[140:143]\n" \
"M v[140:143],a[8:11],a[184:187],v[140:143]\nM v[140:143],a[12:15],a[188:191],v[140:143]\ns_waitcnt vmcnt(11)\nM v[144:147],a[16:19],a[128:131],0\ns_waitcnt vmcnt(10)\nM v[144:147],a[20:23],a[132:135],v[144:147]\ns_waitcnt vmcnt(9)\nM v[144:147],a[24:27],a[136:139],v[144:147]\n" \
"s_waitcnt vmcnt(8)\nM v[144:147],a[28:31],a[140:143],v[144:147]\nM v[148:151],a[16:19],a[144:147],0\nM v[148:151],a[20:23],a[148:151],v[148:151]\nM v[148:151],a[24:27],a[152:155],v[148:151]\nM v[148:151],a[28:31],a[156:159],v[148:151]\nM v[152:155],a[16:19],a[160:163],0\nM v[152:155],a[20:23],a[164:167],v[152:155]\n" \
"M v[152:155],a[24:27],a[168:171],v[152:155]\nM v[152:155],a[28:31],a[172:175],v[152:155]\nM v[156:159],a[16:19],a[176:179],0\nM v[156:159],a[20:23],a[180:183],v[156:159]\nM v[156:159],a[24:27],a[184:187],v[156:159]\nM v[156:159],a[28:31],a[188:191],v[156:159]\ns_waitcnt vmcnt(7)\nM v[160:163],a[32:35],a[128:131],0\n" \
"s_waitcnt vmcnt(6)\nM v[160:163],a[36:39],a[132:135],v[160:163]\ns_waitcnt vmcnt(5)\nM v[160:163],a[40:43],a[136:139],v[160:163]\ns_waitcnt vmcnt(4)\nM v[160:163],a[44:47],a[140:143],v[160:163]\nM v[164:167],a[32:35],a[144:147],0\nM v[164:167],a[36:39],a[148:151],v[164:167]\n" \
"M v[164:167],a[40:43],a[152:155],v[164:167]\nM v[164:167],a[44:47],a[156:159],v[164:167]\nM v[168:171],a[32:35],a[160:163],0\nM v[168:171],a[36:39],a[164:167],v[168:171]\nM v[168:171],a[40:43],a[168:171],v[168:171]\nM v[168:171],a[44:47],a[172:175],v[168:171]\nM v[172:175],a[32:35],a[176:179],0\nM v[172:175],a[36:39],a[180:183],v[172:175]\n" \
"M v[172:175],a[40:43],a[184:187],v[172:175]\nM v[172:175],a[44:47],a[188:191],v[172:175]\ns_waitcnt vmcnt(3)\nM v[176:179],a[48:51],a[128:131],0\ns_waitcnt vmcnt(2)\nM v[176:179],a[52:55],a[132:135],v[176:179]\ns_waitcnt vmcnt(1)\nM v[176:179],a[56:59],a[136:139],v[176:179]\n" \
"s_waitcnt vmcnt(0)\nM v[176:179],a[60:63],a[140:143],v[176:179]\nM v[180:183],a[48:51],a[144:147],0\nM v[180:183],a[52:55],a[148:151],v[180:183]\nM v[180:183],a[56:59],a[152:155],v[180:183]\nM v[180:183],a[60:63],a[156:159],v[180:183]\nM v[184:187],a[48:51],a[160:163],0\nM v[184:187],a[52:55],a[164:167],v[184:187]\n" \
"M v[184:187],a[56:59],a[168:171],v[184:187]\nM v[184:187],a[60:63],a[172:175],v[184:187]\nM v[188:191],a[48:51],a[176:179],0\nM v[188:191],a[52:55],a[180:183],v[188:191]\nM v[188:191],a[56:59],a[184:187],v[188:191]\nM v[188:191],a[60:63],a[188:191],v[188:191]\nv_cmp_le_i32 vcc,0,v225\ns_nop 1\n" \
"v_cndmask_b32 v128,v252,v128,vcc\nv_max_f32 v224,v224,v128\nv_cmp_le_i32 vcc,1,v225\ns_nop 1\nv_cndmask_b32 v129,v252,v129,vcc\nv_max_f32 v224,v224,v129\nv_cmp_le_i32 vcc,2,v225\ns_nop 1\n" \
"v_cndmask_b32 v130,v252,v130,vcc\nv_max_f32 v224,v224,v130\nv_cmp_le_i32 vcc,3,v225\ns_nop 1\nv_cndmask_b32 v131,v252,v131,vcc\nv_max_f32 v224,v224,v131\nv_cmp_le_i32 vcc,0,v229\ns_nop 1\n" \
"v_cndmask_b32 v132,v252,v132,vcc\nv_max_f32 v228,v228,v132\nv_cmp_le_i32 vcc,1,v229\ns_nop 1\nv_cndmask_b32 v133,v252,v133,vcc\nv_max_f32 v228,v228,v133\nv_cmp_le_i32 vcc,2,v229\ns_nop 1\n" \
"v_cndmask_b32 v134,v252,v134,vcc\nv_max_f32 v228,v228,v134\nv_cmp_le_i32 vcc,3,v229\ns_nop 1\nv_cndmask_b32 v135,v252,v135,vcc\nv_max_f32 v228,v228,v135\nv_cmp_le_i32 vcc,0,v233\ns_nop 1\n" \
"v_cndmask_b32 v136,v252,v136,vcc\nv_max_f32 v232,v232,v136\nv_cmp_le_i32 vcc,1,v233\ns_nop 1\nv_cndmask_b32 v137,v252,v137,vcc\nv_max_f32 v232,v232,v137\nv_cmp_le_i32 vcc,2,v233\ns_nop 1\n" \
"v_cndmask_b32 v138,v252,v138,vcc\nv_max_f32 v232,v232,v138\nv_cmp_le_i32 vcc,3,v233\ns_nop 1\nv_cndmask_b32 v139,v252,v139,vcc\nv_max_f32 v232,v232,v139\nv_cmp_le_i32 vcc,0,v237\ns_nop 1\n" \
"v_cndmask_b32 v140,v252,v140,vcc\nv_max_f32 v236,v236,v140\nv_cmp_le_i32 vcc,1,v237\ns_nop 1\nv_cndmask_b32 v141,v252,v141,vcc\nv_max_f32 v236,v236,v141\nv_cmp_le_i32 vcc,2,v237\ns_nop 1\n" \
"v_cndmask_b32 v142,v252,v142,vcc\nv_max_f32 v236,v236,v142\nv_cmp_le_i32 vcc,3,v237\ns_nop 1\nv_cndmask_b32 v143,v252,v143,vcc\nv_max_f32 v236,v236,v143\nv_cmp_le_i32 vcc,16,v225\ns_nop 1\n" \
"v_cndmask_b32 v144,v252,v144,vcc\nv_max_f32 v224,v224,v144\nv_cmp_le_i32 vcc,17,v225\ns_nop 1\nv_cndmask_b32 v145,v252,v145,vcc\nv_max_f32 v224,v224,v145\nv_cmp_le_i32 vcc,18,v225\ns_nop 1\n" \
"v_cndmask_b32 v146,v252,v146,vcc\nv_max_f32 v224,v224,v146\nv_cmp_le_i32 vcc,19,v225\ns_nop 1\nv_cndmask_b32 v147,v252,v147,vcc\nv_max_f32 v224,v224,v147\nv_cmp_le_i32 vcc,16,v229\ns_nop 1\n" \
"v_cndmask_b32 v148,v252,v148,vcc\nv_max_f32 v228,v228,v148\nv_cmp_le_i32 vcc,17,v229\ns_nop 1\nv_cndmask_b32 v149,v252,v149,vcc\nv_max_f32 v228,v228,v149\nv_cmp_le_i32 vcc,18,v229\ns_nop 1\n" \
"v_cndmask_b32 v150,v252,v150,vcc\nv_max_f32 v228,v228,v150\nv_cmp_le_i32 vcc,19,v229\ns_nop 1\nv_cndmask_b32 v151,v252,v151,vcc\nv_max_f32 v228,v228,v151\nv_cmp_le_i32 vcc,16,v233\ns_nop 1\n" \
"v_cndmask_b32 v152,v252,v152,vcc\nv_max_f32 v232,v232,v152\nv_cmp_le_i32 vcc,17,v233\ns_nop 1\nv_cndmask_b32 v153,v252,v153,vcc\nv_max_f32 v232,v232,v153\nv_cmp_le_i32 vcc,18,v233\ns_nop 1\n" \
"v_cndmask_b32 v154,v252,v154,vcc\nv_max_f32 v232,v232,v154\nv_cmp_le_i32 vcc,19,v233\ns_nop 1\nv_cndmask_b32 v155,v252,v155,vcc\nv_max_f32 v232,v232,v155\nv_cmp_le_i32 vcc,16,v237\ns_nop 1\n" \
"v_cndmask_b32 v156,v252,v156,vcc\nv_max_f32 v236,v236,v156\nv_cmp_le_i32 vcc,17,v237\ns_nop 1\nv_cndmask_b32 v157,v252,v157,vcc\nv_max_f32 v236,v236,v157\nv_cmp_le_i32 vcc,18,v237\ns_nop 1\n" \
"v_cndmask_b32 v158,v252,v158,vcc\nv_max_f32 v236,v236,v158\nv_cmp_le_i32 vcc,19,v237\ns_nop 1\nv_cndmask_b32 v159,v252,v159,vcc\nv_max_f32 v236,v236,v159\nv_cmp_le_i32 vcc,32,v225\ns_nop 1\n" \
"v_cndmask_b32 v160,v252,v160,vcc\nv_max_f32 v224,v224,v160\nv_cmp_le_i32 vcc,33,v225\ns_nop 1\nv_cndmask_b32 v161,v252,v161,vcc\nv_max_f32 v224,v224,v161\nv_cmp_le_i32 vcc,34,v225\ns_nop 1\n" \
"v_cndmask_b32 v162,v252,v162,vcc\nv_max_f32 v224,v224,v162\nv_cmp_le_i32 vcc,35,v225\ns_nop 1\nv_cndmask_b32 v163,v252,v163,vcc\nv_max_f32 v224,v224,v163\nv_cmp_le_i32 vcc,32,v229\ns_nop 1\n" \
"v_cndmask_b32 v164,v252,v164,vcc\nv_max_f32 v228,v228,v164\nv_cmp_le_i32 vcc,33,v229\ns_nop 1\nv_cndmask_b32 v165,v252,v165,vcc\nv_max_f32 v228,v228,v165\nv_cmp_le_i32 vcc,34,v229\ns_nop 1\n" \
"v_cndmask_b32 v166,v252,v166,vcc\nv_max_f32 v228,v228,v166\nv_cmp_le_i32 vcc,35,v229\ns_nop 1\nv_cndmask_b32 v167,v252,v167,vcc\nv_max_f32 v228,v228,v167\nv_cmp_le_i32 vcc,32,v233\ns_nop 1\n" \
"v_cndmask_b32 v168,v252,v168,vcc\nv_max_f32 v232,v232,v168\nv_cmp_le_i32 vcc,33,v233\ns_nop 1\nv_cndmask_b32 v169,v252,v169,vcc\nv_max_f32 v232,v232,v169\nv_cmp_le_i32 vcc,34,v233\ns_nop 1\n" \
"v_cndmask_b32 v170,v252,v170,vcc\nv_max_f32 v232,v232,v170\nv_cmp_le_i32 vcc,35,v233\ns_nop 1\nv_cndmask_b32 v171,v252,v171,vcc\nv_max_f32 v232,v232,v171\nv_cmp_le_i32 vcc,32,v237\ns_nop 1\n" \
"v_cndmask_b32 v172,v252,v172,vcc\nv_max_f32 v236,v236,v172\nv_cmp_le_i32 vcc,33,v237\ns_nop 1\nv_cndmask_b32 v173,v252,v173,vcc\nv_max_f32 v236,v236,v173\nv_cmp_le_i32 vcc,34,v237\ns_nop 1\n" \
"v_cndmask_b32 v174,v252,v174,vcc\nv_max_f32 v236,v236,v174\nv_cmp_le_i32 vcc,35,v237\ns_nop 1\nv_cndmask_b32 v175,v252,v175,vcc\nv_max_f32 v236,v236,v175\nv_cmp_le_i32 vcc,48,v225\ns_nop 1\n" \
"v_cndmask_b32 v176,v252,v176,vcc\nv_max_f32 v224,v224,v176\nv_cmp_le_i32 vcc,49,v225\ns_nop 1\nv_cndmask_b32 v177,v252,v177,vcc\nv_max_f32 v224,v224,v177\nv_cmp_le_i32 vcc,50,v225\ns_nop 1\n" \
"v_cndmask_b32 v178,v252,v178,vcc\nv_max_f32 v224,v224,v178\nv_cmp_le_i32 vcc,51,v225\ns_nop 1\nv_cndmask_b32 v179,v252,v179,vcc\nv_max_f32 v224,v224,v179\nv_cmp_le_i32 vcc,48,v229\ns_nop 1\n" \
"v_cndmask_b32 v180,v252,v180,vcc\nv_max_f32 v228,v228,v180\nv_cmp_le_i32 vcc,49,v229\ns_nop 1\nv_cndmask_b32 v181,v252,v181,vcc\nv_max_f32 v228,v228,v181\nv_cmp_le_i32 vcc,50,v229\ns_nop 1\n" \
"v_cndmask_b32 v182,v252,v182,vcc\nv_max_f32 v228,v228,v182\nv_cmp_le_i32 vcc,51,v229\ns_nop 1\nv_cndmask_b32 v183,v252,v183,vcc\nv_max_f32 v228,v228,v183\nv_cmp_le_i32 vcc,48,v233\ns_nop 1\n" \
"v_cndmask_b32 v184,v252,v184,vcc\nv_max_f32 v232,v232,v184\nv_cmp_le_i32 vcc,49,v233\ns_nop 1\nv_cndmask_b32 v185,v252,v185,vcc\nv_max_f32 v232,v232,v185\nv_cmp_le_i32 vcc,50,v233\ns_nop 1\n" \
"v_cndmask_b32 v186,v252,v186,vcc\nv_max_f32 v232,v232,v186\nv_cmp_le_i32 vcc,51,v233\ns_nop 1\nv_cndmask_b32 v187,v252,v187,vcc\nv_max_f32 v232,v232,v187\nv_cmp_le_i32 vcc,48,v237\ns_nop 1\n" \
"v_cndmask_b32 v188,v252,v188,vcc\nv_max_f32 v236,v236,v188\nv_cmp_le_i32 vcc,49,v237\ns_nop 1\nv_cndmask_b32 v189,v252,v189,vcc\nv_max_f32 v236,v236,v189\nv_cmp_le_i32 vcc,50,v237\ns_nop 1\n" \
"v_cndmask_b32 v190,v252,v190,vcc\nv_max_f32 v236,v236,v190\nv_cmp_le_i32 vcc,51,v237\ns_nop 1\nv_cndmask_b32 v191,v252,v191,vcc\nv_max_f32 v236,v236,v191\nv_subrev_u32 v225,64,v225\nv_subrev_u32 v229,64,v229\n" \
"v_subrev_u32 v233,64,v233\nv_subrev_u32 v237,64,v237\ns_cmp_lt_u32 s37,s38\ns_cbranch_scc1 .Lrl\ns_waitcnt vmcnt(0)\ns_barrier\nds_read_b128 a[0:3],v240 offset:0\nds_read_b128 a[4:7],v240 offset:1024\n" \
"ds_read_b128 a[8:11],v240 offset:2048\nds_read_b128 a[12:15],v240 offset:3072\nds_read_b128 a[16:19],v240 offset:4096\nds_read_b128 a[20:23],v240 offset:5120\nds_read_b128 a[24:27],v240 offset:6144\nds_read_b128 a[28:31],v240 offset:7168\nds_read_b128 a[32:35],v240 offset:8192\nds_read_b128 a[36:39],v240 offset:9216\n" \
"ds_read_b128 a[40:43],v240 offset:10240\nds_read_b128 a[44:47],v240 offset:11264\nds_read_b128 a[48:51],v240 offset:12288\nds_read_b128 a[52:55],v240 offset:13312\nds_read_b128 a[56:59],v240 offset:14336\nds_read_b128 a[60:63],v240 offset:15360\ns_cmp_eq_u32 s62,0\ns_cselect_b32 s36,0x8000,0\n" \
"s_add_u32 s36,s36,s64\ns_cmp_eq_u32 s36,s96\ns_cselect_b32 s72,0xc000,s98\ns_add_u32 s65,s36,s72\ns_cmp_eq_u32 s65,s96\ns_cselect_b32 s72,0xc000,s98\ns_add_u32 s66,s65,s72\ns_cmp_eq_u32 s66,s96\n" \
"s_cselect_b32 s72,0xc000,s98\ns_add_u32 s95,s66,s72\ns_add_u32 s97,s96,65536\ns_add_u32 s15,s64,s46\ns_cmp_gt_i32 s62,-1\ns_cselect_b32 s72,s98,0xfffe4000\ns_add_u32 s15,s15,s72\ns_mov_b32 s94,0xc000\n" \
"s_cmp_eq_u32 s62,7\ns_cselect_b32 s72,98304,0\ns_sub_u32 s97,s97,s72\nv_and_b32 v251,31,v249\nv_lshlrev_b32 v251,2,v251\ns_lshl_b32 s80,s63,7\ns_add_u32 s80,s80,86016\nv_add_u32 v250,0xc000,v241\n" \
"s_waitcnt lgkmcnt(0)\ns_branch .Lattn\n.Lfin:\n" \
""
constexpr size_t WS_DBG = (size_t)51 << 20;
constexpr unsigned LDS_BYTES = 163840;
struct Args {
    const u16 *x, *ckv, *w_q, *w_uk, *w_uv, *w_o, *w_r, *wg, *wu, *wd, *cvec, *w_mod;
    char* ws; u16* out;
};
typedef const __attribute__((address_space(4))) Args* kargs_t;
#define STAMPD(i) do {} while (0)
#define C8(p, a) p #a "0", p #a "1", p #a "2", p #a "3", p #a "4", p #a "5", p #a "6", p #a "7", p #a "8", p #a "9"
#define C100(p, a) C8(p, a##0), C8(p, a##1), C8(p, a##2), C8(p, a##3), C8(p, a##4), C8(p, a##5), C8(p, a##6), C8(p, a##7), C8(p, a##8), C8(p, a##9)
#define C10_99(p) C8(p, 1), C8(p, 2), C8(p, 3), C8(p, 4), C8(p, 5), C8(p, 6), C8(p, 7), C8(p, 8), C8(p, 9)
#define C200_249(p) C8(p, 20), C8(p, 21), C8(p, 22), C8(p, 23), C8(p, 24)
#define BODY_CLOB "memory", "vcc", "scc", "s0", "s1", "s2", "s3", "s8", "s9", C8("s", 1), C8("s", 2), "s30", "s31", "s33", "s34", "s35", "s36", "s37", "s38", "s39", C8("s", 4), C8("s", 5), C8("s", 6), C8("s", 7), C8("s", 8), C8("s", 9), \
    C8("v", ), C10_99("v"), C100("v", 1), C200_249("v"), "v250", "v251", "v252", "v253", "v254", C8("a", ), C10_99("a"), C100("a", 1), C200_249("a"), "a250", "a251", "a252", "a253", "a254", "a255"

__global__ __launch_bounds__(256, 1) void k_all(const Args a_) {
    kargs_t ap = (kargs_t)__builtin_amdgcn_kernarg_segment_ptr(); unsigned bid = blockIdx.x, wid = rfl(threadIdx.x >> 6), sv_;
    asm volatile(BODY_ASM : "+{s[4:5]}"(ap), "+{s6}"(bid), "+{s7}"(wid), "={v255}"(sv_) :: BODY_CLOB);
    asm volatile(".Lcode_e:");
}
extern "C" void kernel_launch(void* const* d_in, const int* in_sizes, int n_in, void* d_out, int out_size, void* d_ws, size_t ws_size, hipStream_t stream) {
    Args a;
    a.x = (const u16*)d_in[0]; a.ckv = (const u16*)d_in[1]; a.w_q = (const u16*)d_in[2]; a.w_uk = (const u16*)d_in[3]; a.w_uv = (const u16*)d_in[4]; a.w_o = (const u16*)d_in[5];
    a.w_r = (const u16*)d_in[7]; a.wg = (const u16*)d_in[8]; a.wu = (const u16*)d_in[9]; a.wd = (const u16*)d_in[10]; a.cvec = (const u16*)d_in[11]; a.w_mod = (const u16*)d_in[12];
    a.ws = (char*)d_ws;
    a.out = (u16*)d_out;
    (void)hipFuncSetAttribute((const void*)k_all, hipFuncAttributeMaxDynamicSharedMemorySize, LDS_BYTES);
    k_all<<<256, 256, LDS_BYTES, stream>>>(a);
}
```

```cpp
#include <hip/hip_runtime.h>
#include <hip/hip_bf16.h>
#include <stdint.h>
typedef unsigned short u16;
#define DI __device__ __forceinline__
DI unsigned lane_id() { unsigned l; asm volatile("v_mbcnt_lo_u32_b32 %0, -1, 0\n\tv_mbcnt_hi_u32_b32 %0, -1, %0" : "=v"(l)); return l; }
DI unsigned rfl(unsigned x) { return __builtin_amdgcn_readfirstlane(x); }
#define BODY_ASM ".macro M a,b,c,d\nv_mfma_f32_16x16x32_bf16 \\a,\\b,\\c,\\d\n.endm\n" \
".Lcode_b:\ns_load_dwordx16 s[8:23],s[4:5],0x0\ns_load_dwordx8 s[24:31],s[4:5],0x40\ns_load_dwordx2 s[34:35],s[4:5],0x60\nv_mbcnt_lo_u32_b32 v249,-1,0\nv_mbcnt_hi_u32_b32 v249,-1,v249\ns_getpc_b64 s[56:57]\n.Lpc0:\n" \
"s_and_b32 s57,s57,0xffff\ns_mov_b32 s58,.Lcode_e-.Lpc0\ns_mov_b32 s59,0x20000\ns_lshr_b32 s70,s6,3\nv_and_b32 v251,1,v249\ns_lshl2_add_u32 s78,s70,s7\nv_lshlrev_b32 v251,14,v251\ns_lshl_b32 s78,s78,7\n" \
"buffer_load_dword v247,v251,s[56:59],s78 offen\ns_bfe_u32 s61,s6,0x20003\ns_lshl_b32 s64,s7,12\ns_and_b32 s60,s6,7\ns_lshr_b32 s62,s6,5\ns_lshl2_add_u32 s63,s60,s61\nv_lshlrev_b32 v240,4,v249\ns_cmp_lt_u32 s6,192\n" \
"s_cselect_b32 s70,s6,0\ns_mul_i32 s71,s70,171\ns_lshr_b32 s71,s71,13\ns_mul_i32 s72,s71,48\ns_sub_u32 s70,s70,s72\ns_lshl2_add_u32 s72,s71,s7\ns_lshl_b32 s72,s72,5\nv_lshrrev_b32 v0,3,v249\n" \
"v_lshl_add_u32 v1,v0,2,s72\nv_and_b32 v2,7,v249\nv_mul_u32_u24 v3,6144,v1\nv_lshl_add_u32 v3,v2,4,v3\ns_lshl_b32 s72,s70,7\ns_add_u32 s72,s72,3072\nv_add_u32 v3,s72,v3\nv_add_u32 v4,12288,v3\n" \
"v_lshlrev_b32 v1,1,v1\ns_mul_i32 s71,s71,12288\ns_lshl_b32 s70,s70,8\ns_add_u32 s79,s70,s71\ns_add_u32 s79,s79,786432\ns_cmp_eq_u32 s7,0\ns_cselect_b32 s75,0,0x8000000\ns_cmp_lt_u32 s6,192\n" \
"s_cselect_b32 s70,s75,0x8000000\ns_or_b32 s79,s79,s70\ns_lshl_b32 s70,s62,7\ns_xor_b32 s71,s63,12\ns_lshl3_add_u32 s71,s71,s70\ns_lshl_b32 s72,s63,3\ns_sub_u32 s72,s72,s70\ns_add_u32 s72,s72,1792\n" \
"s_cmp_lt_u32 s63,16\ns_cselect_b32 s71,s71,s72\ns_lshl_b32 s71,s71,10\ns_lshl_b32 s72,s7,7\ns_add_u32 s71,s71,s72\nv_lshlrev_b32 v5,10,v2\nv_lshl_add_u32 v5,v0,4,v5\nv_add_u32 v5,s71,v5\n" \
"v_bfe_u32 v6,v249,2,3\nv_lshrrev_b32 v7,5,v249\nv_and_b32 v8,48,v240\nv_lshlrev_b32 v6,13,v6\nv_lshl_or_b32 v7,v7,6,v8\nv_or_b32 v6,0x1000,v6\nv_or_b32 v243,v6,v7\nv_add_u32 v244,0xfffffc80,v243\n" \
"v_xor_b32 v6,32,v243\nv_add_u32 v245,63488,v6\nv_add_u32 v246,62592,v6\nv_lshrrev_b32 v6,4,v249\nv_xor_b32 v7,v6,v2\nv_lshlrev_b32 v7,4,v7\nv_lshl_or_b32 v224,v0,10,v7\nv_xor_b32 v7,64,v224\n" \
"v_add_u32 v225,7168,v7\nv_add_u32 v226,14336,v224\nv_add_u32 v227,21504,v7\ns_lshl_b32 s70,s62,1\ns_sub_u32 s71,s7,s70\ns_add_u32 s71,s71,30\ns_sub_u32 s72,s70,s7\ns_add_u32 s72,s72,3\n" \
"s_cmp_lt_u32 s7,2\ns_cselect_b32 s74,s71,s72\ns_lshl_b32 s66,s74,16\ns_mul_i32 s65,s7,24576\ns_lshl_b32 s68,s7,17\ns_lshl_b32 s70,s63,8\ns_add_u32 s68,s68,s70\ns_mov_b32 s38,2097152\n" \
"s_mov_b32 s39,0x20000\ns_mov_b32 s42,4198400\ns_mov_b32 s43,0x20000\ns_mov_b32 s54,71303168\ns_mov_b32 s55,0x20000\ns_mov_b32 s70,94208\ns_waitcnt lgkmcnt(0)\ns_mov_b64 s[52:53],s[34:35]\n" \
"s_mov_b64 s[36:37],s[10:11]\ns_sub_u32 s40,s14,0x1000\ns_subb_u32 s41,s15,0\nbuffer_load_dwordx2 v[230:231],off,s[52:55],s70 sc1\nglobal_load_dwordx2 v[10:11],v1,s[28:29]\nglobal_load_dwordx4 v[12:15],v3,s[30:31] offset:-3072\nglobal_load_dwordx4 v[16:19],v3,s[30:31] offset:3072\nglobal_load_dwordx4 v[20:23],v4,s[30:31] offset:-3072\n" \
"global_load_dwordx4 v[24:27],v4,s[30:31] offset:3072\nglobal_load_dwordx4 v[128:131],v5,s[8:9]\nglobal_load_dwordx4 v[132:135],v5,s[8:9] offset:512\ns_add_u32 m0,s64,98304\ns_add_u32 s69,s68,0\nbuffer_load_dwordx4 v243,s[40:43],s69 offen offset:0 lds\nbuffer_load_dwordx4 v244,s[40:43],s69 offen offset:1024 lds\nbuffer_load_dwordx4 v245,s[40:43],s69 offen offset:2048 lds\n" \
"buffer_load_dwordx4 v246,s[40:43],s69 offen offset:3072 lds\ns_add_u32 m0,s65,0\ns_add_u32 s67,s66,0\nbuffer_load_dwordx4 v224,s[36:39],s67 offen offset:0 lds\nbuffer_load_dwordx4 v225,s[36:39],s67 offen offset:1024 lds\nbuffer_load_dwordx4 v226,s[36:39],s67 offen offset:2048 lds\nbuffer_load_dwordx4 v227,s[36:39],s67 offen offset:3072 lds\ns_add_u32 m0,s65,4096\n" \
"s_add_u32 s67,s66,32768\nbuffer_load_dwordx4 v224,s[36:39],s67 offen offset:0 lds\nbuffer_load_dwordx4 v225,s[36:39],s67 offen offset:1024 lds\nbuffer_load_dwordx4 v226,s[36:39],s67 offen offset:2048 lds\nbuffer_load_dwordx4 v227,s[36:39],s67 offen offset:3072 lds\ns_lshl_b32 s70,s6,11\ns_cmp_lt_u32 s6,112\ns_cselect_b32 s70,s70,0x8000000\n" \
"s_lshl_b32 s71,s7,9\ns_add_u32 s70,s70,s71\ns_add_u32 s70,s70,98304\nv_mov_b32 v28,-1\nv_mov_b32 v29,-1\nv_lshlrev_b32 v30,3,v249\nbuffer_store_dwordx2 v[28:29],v30,s[52:55],s70 offen sc1\ns_lshl_b32 s70,s6,7\n" \
"s_cmp_lt_u32 s6,224\ns_cselect_b32 s71,65536,0x40000000\ns_cmp_lt_u32 s6,128\ns_cselect_b32 s72,556928,0x40000000\nv_mov_b32 v31,s71\nv_mov_b32 v32,s72\nv_cmp_lt_u32 vcc,31,v249\ns_or_b32 s70,s70,s75\n" \
"v_mov_b32 v33,0\nv_cndmask_b32 v31,v31,v32,vcc\nv_lshl_add_u32 v31,v249,2,v31\nbuffer_store_dword v33,v31,s[52:55],s70 offen sc1\nv_and_b32 v34,15,v249\nv_lshrrev_b32 v35,1,v34\nv_xor_b32 v35,v35,v6\nv_lshlrev_b32 v35,4,v35\n" \
"v_lshl_or_b32 v35,v34,7,v35\nv_add_u32 v228,s65,v35\nv_xor_b32 v229,64,v228\nv_bfe_u32 v34,v249,2,2\nv_and_b32 v35,3,v249\nv_lshlrev_b32 v35,3,v35\nv_lshl_or_b32 v35,v34,6,v35\nv_lshl_or_b32 v35,v6,11,v35\n" \
"v_and_b32 v34,16,v249\nv_lshl_or_b32 v35,v34,1,v35\nv_add_u32 v241,98304,v35\nv_xor_b32 v242,32,v241\ns_mov_b32 s73,0x400000\nv_mov_b32 v252,0xff800000\ns_mov_b32 s70,0x05040100\ns_mov_b32 s71,0x07060302\n" \
"s_waitcnt vmcnt(20)\nv_lshlrev_b32 v36,16,v10\nv_and_b32 v37,0xffff0000,v10\nv_lshlrev_b32 v38,16,v11\nv_and_b32 v39,0xffff0000,v11\nv_mul_f32 v40,0xbfb8aa3b,v36\nv_mul_f32 v41,0xbfb8aa3b,v37\nv_mul_f32 v42,0xbfb8aa3b,v38\n" \
"v_mul_f32 v43,0xbfb8aa3b,v39\nv_exp_f32 v40,v40\nv_exp_f32 v41,v41\nv_exp_f32 v42,v42\nv_exp_f32 v43,v43\nv_add_f32 v40,1.0,v40\nv_add_f32 v41,1.0,v41\nv_add_f32 v42,1.0,v42\n" \
"v_add_f32 v43,1.0,v43\nv_rcp_f32 v40,v40\nv_rcp_f32 v41,v41\nv_rcp_f32 v42,v42\nv_rcp_f32 v43,v43\nv_mul_f32 v36,v36,v40\nv_mul_f32 v37,v37,v41\nv_mul_f32 v38,v38,v42\n" \
"v_mul_f32 v39,v39,v43\nv_cvt_pk_bf16_f32 v36,v36,v37\nv_cvt_pk_bf16_f32 v37,v38,v39\ns_waitcnt vmcnt(18)\nv_perm_b32 v48,v16,v12,s70\nv_perm_b32 v49,v16,v12,s71\nv_perm_b32 v50,v17,v13,s70\nv_perm_b32 v51,v17,v13,s71\n" \
"v_perm_b32 v52,v18,v14,s70\nv_perm_b32 v53,v18,v14,s71\nv_perm_b32 v54,v19,v15,s70\nv_perm_b32 v55,v19,v15,s71\nv_dot2_f32_bf16 v40,v48,v36,0\nv_dot2_f32_bf16 v41,v49,v36,0\nv_dot2_f32_bf16 v42,v50,v36,0\nv_dot2_f32_bf16 v43,v51,v36,0\n" \
"v_dot2_f32_bf16 v44,v52,v36,0\nv_dot2_f32_bf16 v45,v53,v36,0\nv_dot2_f32_bf16 v46,v54,v36,0\nv_dot2_f32_bf16 v47,v55,v36,0\ns_waitcnt vmcnt(16)\nv_perm_b32 v48,v24,v20,s70\nv_perm_b32 v49,v24,v20,s71\nv_perm_b32 v50,v25,v21,s70\n" \
"v_perm_b32 v51,v25,v21,s71\nv_perm_b32 v52,v26,v22,s70\nv_perm_b32 v53,v26,v22,s71\nv_perm_b32 v54,v27,v23,s70\nv_perm_b32 v55,v27,v23,s71\nv_dot2_f32_bf16 v40,v48,v37,v40\nv_dot2_f32_bf16 v41,v49,v37,v41\nv_dot2_f32_bf16 v42,v50,v37,v42\n" \
"v_dot2_f32_bf16 v43,v51,v37,v43\nv_dot2_f32_bf16 v44,v52,v37,v44\nv_dot2_f32_bf16 v45,v53,v37,v45\nv_dot2_f32_bf16 v46,v54,v37,v46\nv_dot2_f32_bf16 v47,v55,v37,v47\ns_waitcnt vmcnt(15)\nv_lshlrev_b32 v136,16,v128\nv_and_b32 v137,0xffff0000,v128\n" \
"v_lshlrev_b32 v138,16,v129\nv_and_b32 v139,0xffff0000,v129\nv_lshlrev_b32 v140,16,v130\nv_and_b32 v141,0xffff0000,v130\nv_lshlrev_b32 v142,16,v131\nv_and_b32 v143,0xffff0000,v131\ns_waitcnt vmcnt(14)\nv_lshlrev_b32 v144,16,v132\n" \
"v_and_b32 v145,0xffff0000,v132\nv_lshlrev_b32 v146,16,v133\nv_and_b32 v147,0xffff0000,v133\nv_lshlrev_b32 v148,16,v134\nv_and_b32 v149,0xffff0000,v134\nv_lshlrev_b32 v150,16,v135\nv_and_b32 v151,0xffff0000,v135\nv_permlane32_swap_b32 v40,v44\n" \
"v_permlane32_swap_b32 v41,v45\nv_permlane32_swap_b32 v42,v46\nv_permlane32_swap_b32 v43,v47\nv_add_f32 v40,v40,v44\nv_add_f32 v41,v41,v45\nv_add_f32 v42,v42,v46\nv_add_f32 v43,v43,v47\nv_add_f32 v48,v136,v140\n" \
"v_add_f32 v49,v137,v141\nv_add_f32 v50,v138,v142\nv_add_f32 v51,v139,v143\nv_permlane16_swap_b32 v40,v42\nv_permlane16_swap_b32 v41,v43\nv_add_f32 v52,v144,v148\nv_add_f32 v53,v145,v149\nv_add_f32 v54,v146,v150\n" \
"v_add_f32 v55,v147,v151\nv_add_f32 v40,v40,v42\nv_add_f32 v41,v41,v43\nv_add_f32 v48,v48,v52\nv_add_f32 v49,v49,v53\nv_add_f32 v50,v50,v54\nv_add_f32 v51,v51,v55\nv_add_f32_dpp v40,v40,v40 row_ror:8 row_mask:0xf bank_mask:0xf\n" \
"v_add_f32_dpp v41,v41,v41 row_ror:8 row_mask:0xf bank_mask:0xf\nv_add_f32 v48,v48,v49\nv_add_f32 v50,v50,v51\nv_lshlrev_b32 v42,5,v2\nv_add_f32 v48,v48,v50\nv_lshl_or_b32 v42,v6,3,v42\ns_lshl_b32 s72,s7,8\ns_add_u32 s72,s72,147456\n" \
"v_add_u32 v42,s72,v42\nds_write_b64 v42,v[40:41]\nv_add_f32_dpp v48,v48,v48 row_ror:8 row_mask:0xf bank_mask:0xf\nv_mov_b32 v50,v48\ns_nop 1\nv_permlane16_swap_b32 v48,v50\nv_add_f32 v48,v48,v50\nv_mov_b32 v50,v48\n" \
"s_nop 1\nv_permlane32_swap_b32 v48,v50\nv_add_f32 v48,v48,v50\nv_mul_f32 v48,0x3c000000,v48\nv_sub_f32 v56,v136,v48\nv_sub_f32 v57,v137,v48\nv_sub_f32 v58,v138,v48\nv_sub_f32 v59,v139,v48\n" \
"v_sub_f32 v60,v140,v48\nv_sub_f32 v61,v141,v48\nv_sub_f32 v62,v142,v48\nv_sub_f32 v63,v143,v48\nv_sub_f32 v64,v144,v48\nv_sub_f32 v65,v145,v48\nv_sub_f32 v66,v146,v48\nv_sub_f32 v67,v147,v48\n" \
"v_sub_f32 v68,v148,v48\nv_sub_f32 v69,v149,v48\nv_sub_f32 v70,v150,v48\nv_sub_f32 v71,v151,v48\nv_mul_f32 v52,v56,v56\nv_mul_f32 v53,v57,v57\nv_mul_f32 v54,v58,v58\nv_mul_f32 v55,v59,v59\n" \
"v_fmac_f32 v52,v60,v60\nv_fmac_f32 v53,v61,v61\nv_fmac_f32 v54,v62,v62\nv_fmac_f32 v55,v63,v63\nv_fmac_f32 v52,v64,v64\nv_fmac_f32 v53,v65,v65\nv_fmac_f32 v54,v66,v66\nv_fmac_f32 v55,v67,v67\n" \
"v_fmac_f32 v52,v68,v68\nv_fmac_f32 v53,v69,v69\nv_fmac_f32 v54,v70,v70\nv_fmac_f32 v55,v71,v71\nv_add_f32 v52,v52,v53\nv_add_f32 v54,v54,v55\ns_lshl_b32 s72,s7,3\nv_add_f32 v49,v52,v54\n" \
"s_add_u32 s72,s72,148480\nv_lshl_add_u32 v42,v2,5,s72\nv_add_f32_dpp v49,v49,v49 row_ror:8 row_mask:0xf bank_mask:0xf\nv_mov_b32 v50,v49\ns_nop 1\nv_permlane16_swap_b32 v49,v50\nv_add_f32 v49,v49,v50\nv_mov_b32 v50,v49\n" \
"s_nop 1\nv_permlane32_swap_b32 v49,v50\nv_add_f32 v49,v49,v50\nds_write_b64 v42,v[48:49]\nv_lshlrev_b32 v251,5,v2\nv_add_u32 v251,148480,v251\nv_lshlrev_b32 v250,2,v249\nv_add_u32 v250,147456,v250\n" \
"s_add_u32 m0,s64,114688\ns_add_u32 s69,s68,524288\nbuffer_load_dwordx4 v243,s[40:43],s69 offen offset:0 lds\nbuffer_load_dwordx4 v244,s[40:43],s69 offen offset:1024 lds\nbuffer_load_dwordx4 v245,s[40:43],s69 offen offset:2048 lds\nbuffer_load_dwordx4 v246,s[40:43],s69 offen offset:3072 lds\ns_add_u32 m0,s65,8192\ns_add_u32 s67,s66,128\n" \
"buffer_load_dwordx4 v224,s[36:39],s67 offen offset:0 lds\nbuffer_load_dwordx4 v225,s[36:39],s67 offen offset:1024 lds\nbuffer_load_dwordx4 v226,s[36:39],s67 offen offset:2048 lds\nbuffer_load_dwordx4 v227,s[36:39],s67 offen offset:3072 lds\ns_add_u32 m0,s65,12288\ns_add_u32 s67,s66,32896\nbuffer_load_dwordx4 v224,s[36:39],s67 offen offset:0 lds\nbuffer_load_dwordx4 v225,s[36:39],s67 offen offset:1024 lds\n" \
"buffer_load_dwordx4 v226,s[36:39],s67 offen offset:2048 lds\nbuffer_load_dwordx4 v227,s[36:39],s67 offen offset:3072 lds\ns_waitcnt lgkmcnt(0)\ns_waitcnt vmcnt(14)\ns_barrier\ns_add_u32 m0,s64,131072\ns_add_u32 s69,s68,1048576\nds_read_b128 a[0:3],v228 offset:0\n" \
"ds_read_b128 a[4:7],v228 offset:2048\nds_read_b128 a[8:11],v228 offset:4096\nds_read_b128 a[12:15],v228 offset:6144\nds_read_b128 a[16:19],v229 offset:0\nds_read_b128 a[20:23],v229 offset:2048\nds_read_b128 a[24:27],v229 offset:4096\nds_read_b128 a[28:31],v229 offset:6144\nds_read_b32 v152,v250 offset:0\n" \
"ds_read_b32 v153,v250 offset:256\nds_read_b32 v154,v250 offset:512\nds_read_b32 v155,v250 offset:768\ns_waitcnt lgkmcnt(2)\nv_add_f32 v152,v152,v153\ns_waitcnt lgkmcnt(0)\nv_add_f32 v154,v154,v155\nv_lshlrev_b32 v153,2,v249\n" \
"v_add_f32 v152,v152,v154\nds_read_b64_tr_b16 v[192:193],v241 offset:0\nds_read_b64_tr_b16 v[194:195],v241 offset:256\nds_read_b64_tr_b16 v[196:197],v241 offset:8192\nds_read_b64_tr_b16 v[198:199],v241 offset:8448\nds_read_b64_tr_b16 v[200:201],v242 offset:0\nds_read_b64_tr_b16 v[202:203],v242 offset:256\nds_read_b64_tr_b16 v[204:205],v242 offset:8192\n" \
"ds_read_b64_tr_b16 v[206:207],v242 offset:8448\nds_read_b64_tr_b16 v[208:209],v241 offset:512\nds_read_b64_tr_b16 v[210:211],v241 offset:768\nds_read_b64_tr_b16 v[212:213],v241 offset:8704\nds_read_b64_tr_b16 v[214:215],v241 offset:8960\nds_read_b64_tr_b16 v[216:217],v242 offset:512\nds_read_b64_tr_b16 v[218:219],v242 offset:768\nds_read_b64_tr_b16 v[220:221],v242 offset:8704\n" \
"ds_read_b64_tr_b16 v[222:223],v242 offset:8960\nbuffer_store_dword v152,v153,s[52:55],s79 offen sc1\nds_read_b128 v[156:159],v251\nds_read_b128 v[160:163],v251 offset:16\ns_waitcnt lgkmcnt(15)\nM v[0:3],v[192:195],a[0:3],0\nbuffer_load_dwordx4 v243,s[40:43],s69 offen offset:0 lds\ns_waitcnt lgkmcnt(14)\n" \
"M v[0:3],v[196:199],a[16:19],v[0:3]\nv_bfe_u32 v253,v249,1,2\ns_lshl2_add_u32 s76,s62,s7\nv_and_b32 v248,1,v249\nv_lshlrev_b32 v253,13,v253\ns_mul_i32 s76,s76,32768\ns_lshl_b32 s77,s63,8\nM v[4:7],v[192:195],a[4:7],0\n" \
"v_lshl_or_b32 v253,v248,7,v253\ns_add_u32 s76,s76,s77\nv_and_b32 v254,7,v249\nv_add_u32 v253,s76,v253\nM v[4:7],v[196:199],a[20:23],v[4:7]\ns_lshl3_add_u32 s72,s6,s75\ns_add_u32 s72,s72,573440\nM v[64:67],v[192:195],a[8:11],0\n" \
"global_load_dword v247,v253,s[16:17]\nM v[64:67],v[196:199],a[24:27],v[64:67]\nbuffer_load_dwordx4 v244,s[40:43],s69 offen offset:1024 lds\nM v[68:71],v[192:195],a[12:15],0\nds_read_b64_tr_b16 v[192:193],v241 offset:1024\nds_read_b64_tr_b16 v[194:195],v241 offset:1280\nM v[68:71],v[196:199],a[28:31],v[68:71]\nds_read_b64_tr_b16 v[196:197],v241 offset:9216\n" \
"ds_read_b64_tr_b16 v[198:199],v241 offset:9472\ns_waitcnt lgkmcnt(4)\nM v[8:11],v[200:203],a[0:3],0\nv_add_u32 v253,0x100000,v253\ns_lshl2_add_u32 s80,s60,s7\nM v[8:11],v[204:207],a[16:19],v[8:11]\nv_readfirstlane_b32 s92,v230\nv_readfirstlane_b32 s93,v231\n" \
"M v[12:15],v[200:203],a[4:7],0\nbuffer_load_dwordx4 v245,s[40:43],s69 offen offset:2048 lds\nM v[12:15],v[204:207],a[20:23],v[12:15]\ns_add_u32 s70,s92,1\ns_addc_u32 s71,s93,0\nM v[72:75],v[200:203],a[8:11],0\nv_add_f32 v164,v156,v158\nv_add_f32 v165,v160,v162\n" \
"M v[72:75],v[204:207],a[24:27],v[72:75]\nv_add_f32 v164,v164,v165\nv_and_b32 v165,7,v249\nM v[76:79],v[200:203],a[12:15],0\nds_read_b64_tr_b16 v[200:201],v242 offset:1024\nds_read_b64_tr_b16 v[202:203],v242 offset:1280\nM v[76:79],v[204:207],a[28:31],v[76:79]\nbuffer_load_dwordx4 v246,s[40:43],s69 offen offset:3072 lds\n" \
"ds_read_b64_tr_b16 v[204:205],v242 offset:9216\nds_read_b64_tr_b16 v[206:207],v242 offset:9472\nM v[16:19],v[208:211],a[0:3],0\ns_add_u32 s92,s92,0x7f4a7c15\ns_addc_u32 s93,s93,0x9e3779b9\nM v[16:19],v[212:215],a[16:19],v[16:19]\ns_add_u32 m0,s65,16384\ns_add_u32 s67,s66,256\n" \
"M v[20:23],v[208:211],a[4:7],0\nv_mov_b32 v230,s92\nv_mov_b32 v231,s93\nM v[20:23],v[212:215],a[20:23],v[20:23]\nv_lshlrev_b32 v165,11,v165\ns_lshl_b32 s80,s80,9\nM v[80:83],v[208:211],a[8:11],0\nbuffer_load_dwordx4 v224,s[36:39],s67 offen offset:0 lds\n" \
"M v[80:83],v[212:215],a[24:27],v[80:83]\ns_add_u32 s80,s80,573440\nv_mul_f32 v164,0x3e800000,v164\nM v[84:87],v[208:211],a[12:15],0\nds_read_b64_tr_b16 v[208:209],v241 offset:1536\nds_read_b64_tr_b16 v[210:211],v241 offset:1792\nM v[84:87],v[212:215],a[28:31],v[84:87]\nds_read_b64_tr_b16 v[212:213],v241 offset:9728\n" \
"ds_read_b64_tr_b16 v[214:215],v241 offset:9984\nM v[24:27],v[216:219],a[0:3],0\nv_sub_f32 v156,v156,v164\nv_sub_f32 v158,v158,v164\nM v[24:27],v[220:223],a[16:19],v[24:27]\nbuffer_load_dwordx4 v225,s[36:39],s67 offen offset:1024 lds\nM v[28:31],v[216:219],a[4:7],0\nv_mul_f32 v156,v156,v156\n" \
"v_sub_f32 v160,v160,v164\nM v[28:31],v[220:223],a[20:23],v[28:31]\nv_fmac_f32 v156,v158,v158\nv_add_f32 v157,v157,v159\nM v[88:91],v[216:219],a[8:11],0\nv_add_f32 v161,v161,v163\nv_sub_f32 v162,v162,v164\nM v[88:91],v[220:223],a[24:27],v[88:91]\n" \
"v_fmac_f32 v156,v160,v160\nv_add_f32 v157,v157,v161\nM v[92:95],v[216:219],a[12:15],0\nbuffer_load_dwordx4 v226,s[36:39],s67 offen offset:2048 lds\nds_read_b64_tr_b16 v[216:217],v242 offset:1536\nds_read_b64_tr_b16 v[218:219],v242 offset:1792\nM v[92:95],v[220:223],a[28:31],v[92:95]\nds_read_b64_tr_b16 v[220:221],v242 offset:9728\n" \
"ds_read_b64_tr_b16 v[222:223],v242 offset:9984\ns_waitcnt lgkmcnt(8)\nM v[32:35],v[192:195],a[0:3],0\nv_fmac_f32 v156,v162,v162\nv_fmac_f32 v157,0x43000000,v156\nM v[32:35],v[196:199],a[16:19],v[32:35]\nv_mul_f32 v157,0x3b000000,v157\nv_cvt_pk_bf16_f32 v157,0,v157\n" \
"M v[36:39],v[192:195],a[4:7],0\nv_add_f32 v157,0x3727c5ac,v157\nv_cvt_pk_bf16_f32 v157,0,v157\nM v[36:39],v[196:199],a[20:23],v[36:39]\nbuffer_load_dwordx4 v227,s[36:39],s67 offen offset:3072 lds\nM v[96:99],v[192:195],a[8:11],0\nv_sqrt_f32 v157,v157\nM v[96:99],v[196:199],a[24:27],v[96:99]\n" \
"s_add_u32 m0,s65,20480\ns_add_u32 s67,s66,33024\nM v[100:103],v[192:195],a[12:15],0\nv_mov_b32 v232,s70\nv_mov_b32 v233,s71\nM v[100:103],v[196:199],a[28:31],v[100:103]\ns_lshr_b32 s70,s64,2\nv_cvt_pk_bf16_f32 v164,0,v164\n" \
"M v[40:43],v[200:203],a[0:3],0\nbuffer_load_dwordx4 v224,s[36:39],s67 offen offset:0 lds\nM v[40:43],v[204:207],a[16:19],v[40:43]\nv_cvt_pk_bf16_f32 v157,0,v157\ns_add_u32 s71,s70,786432\nM v[44:47],v[200:203],a[4:7],0\nv_rcp_f32 v157,v157\nM v[44:47],v[204:207],a[20:23],v[44:47]\n" \
"v_sub_f32 v136,v136,v164\nv_sub_f32 v137,v137,v164\nM v[104:107],v[200:203],a[8:11],0\nglobal_load_dword v247,v253,s[16:17]\nM v[104:107],v[204:207],a[24:27],v[104:107]\nbuffer_load_dwordx4 v225,s[36:39],s67 offen offset:1024 lds\nM v[108:111],v[200:203],a[12:15],0\nv_add_u32 v253,0x100000,v253\n" \
"v_sub_f32 v138,v138,v164\nM v[108:111],v[204:207],a[28:31],v[108:111]\nv_sub_f32 v139,v139,v164\ns_waitcnt lgkmcnt(0)\nM v[48:51],v[208:211],a[0:3],0\nv_sub_f32 v140,v140,v164\nv_sub_f32 v141,v141,v164\nM v[48:51],v[212:215],a[16:19],v[48:51]\n" \
"v_sub_f32 v142,v142,v164\nv_sub_f32 v143,v143,v164\nM v[52:55],v[208:211],a[4:7],0\nbuffer_load_dwordx4 v226,s[36:39],s67 offen offset:2048 lds\nM v[52:55],v[212:215],a[20:23],v[52:55]\nv_sub_f32 v144,v144,v164\nv_sub_f32 v145,v145,v164\nM v[112:115],v[208:211],a[8:11],0\n" \
"s_waitcnt vmcnt(13)\ns_barrier\nds_read_b128 a[32:35],v228 offset:8192\nds_read_b128 a[36:39],v228 offset:10240\nds_read_b128 a[40:43],v228 offset:12288\nds_read_b128 a[44:47],v228 offset:14336\nds_read_b128 a[48:51],v229 offset:8192\nds_read_b128 a[52:55],v229 offset:10240\n" \
"ds_read_b128 a[56:59],v229 offset:12288\nds_read_b128 a[60:63],v229 offset:14336\nds_read_b64_tr_b16 v[192:193],v241 offset:16384\nds_read_b64_tr_b16 v[194:195],v241 offset:16640\nds_read_b64_tr_b16 v[196:197],v241 offset:24576\nds_read_b64_tr_b16 v[198:199],v241 offset:24832\nds_read_b64_tr_b16 v[200:201],v242 offset:16384\nds_read_b64_tr_b16 v[202:203],v242 offset:16640\n" \
"ds_read_b64_tr_b16 v[204:205],v242 offset:24576\nds_read_b64_tr_b16 v[206:207],v242 offset:24832\nM v[112:115],v[212:215],a[24:27],v[112:115]\nv_sub_f32 v146,v146,v164\nv_sub_f32 v147,v147,v164\nM v[116:119],v[208:211],a[12:15],0\nds_read_b64_tr_b16 v[208:209],v241 offset:16896\nds_read_b64_tr_b16 v[210:211],v241 offset:17152\n" \
"buffer_store_dwordx2 v[230:231],v165,s[52:55],s72 offen sc1\nM v[116:119],v[212:215],a[28:31],v[116:119]\nbuffer_load_dwordx4 v227,s[36:39],s67 offen offset:3072 lds\nds_read_b64_tr_b16 v[212:213],v241 offset:25088\nds_read_b64_tr_b16 v[214:215],v241 offset:25344\nM v[56:59],v[216:219],a[0:3],0\nv_lshlrev_b32 v165,3,v249\nv_sub_f32 v148,v148,v164\n" \
"M v[56:59],v[220:223],a[16:19],v[56:59]\ns_add_u32 m0,s65,0\ns_add_u32 s67,s66,384\nM v[60:63],v[216:219],a[4:7],0\nv_sub_f32 v149,v149,v164\nv_sub_f32 v150,v150,v164\nM v[60:63],v[220:223],a[20:23],v[60:63]\nv_sub_f32 v151,v151,v164\n" \
"v_cvt_pk_bf16_f32 v136,0,v136\nM v[120:123],v[216:219],a[8:11],0\nbuffer_load_dwordx4 v224,s[36:39],s67 offen offset:0 lds\nM v[120:123],v[220:223],a[24:27],v[120:123]\nv_cvt_pk_bf16_f32 v137,0,v137\nv_cvt_pk_bf16_f32 v138,0,v138\nM v[124:127],v[216:219],a[12:15],0\nds_read_b64_tr_b16 v[216:217],v242 offset:16896\n" \
"ds_read_b64_tr_b16 v[218:219],v242 offset:17152\nM v[124:127],v[220:223],a[28:31],v[124:127]\nds_read_b64_tr_b16 v[220:221],v242 offset:25088\nds_read_b64_tr_b16 v[222:223],v242 offset:25344\ns_waitcnt lgkmcnt(6)\nM v[0:3],v[192:195],a[32:35],v[0:3]\nv_cvt_pk_bf16_f32 v139,0,v139\nv_cvt_pk_bf16_f32 v140,0,v140\n" \
"M v[0:3],v[196:199],a[48:51],v[0:3]\nbuffer_load_dwordx4 v225,s[36:39],s67 offen offset:1024 lds\nM v[4:7],v[192:195],a[36:39],v[4:7]\nv_cvt_pk_bf16_f32 v141,0,v141\nv_cvt_pk_bf16_f32 v142,0,v142\nM v[4:7],v[196:199],a[52:55],v[4:7]\nv_cvt_pk_bf16_f32 v143,0,v143\nv_cvt_pk_bf16_f32 v144,0,v144\n" \
"M v[64:67],v[192:195],a[40:43],v[64:67]\nv_cvt_pk_bf16_f32 v145,0,v145\nv_cvt_pk_bf16_f32 v146,0,v146\nM v[64:67],v[196:199],a[56:59],v[64:67]\nv_cvt_pk_bf16_f32 v147,0,v147\nv_cvt_pk_bf16_f32 v148,0,v148\nM v[68:71],v[192:195],a[44:47],v[68:71]\nbuffer_load_dwordx4 v226,s[36:39],s67 offen offset:2048 lds\n" \
"ds_read_b64_tr_b16 v[192:193],v241 offset:17408\nds_read_b64_tr_b16 v[194:195],v241 offset:17664\nM v[68:71],v[196:199],a[60:63],v[68:71]\nds_read_b64_tr_b16 v[196:197],v241 offset:25600\nds_read_b64_tr_b16 v[198:199],v241 offset:25856\nM v[8:11],v[200:203],a[32:35],v[8:11]\nv_cvt_pk_bf16_f32 v149,0,v149\nv_cvt_pk_bf16_f32 v150,0,v150\n" \
"M v[8:11],v[204:207],a[48:51],v[8:11]\nv_cvt_pk_bf16_f32 v151,0,v151\nv_mul_f32 v136,v136,v157\nM v[12:15],v[200:203],a[36:39],v[12:15]\nv_mul_f32 v137,v137,v157\nv_mul_f32 v138,v138,v157\nM v[12:15],v[204:207],a[52:55],v[12:15]\nbuffer_load_dwordx4 v227,s[36:39],s67 offen offset:3072 lds\n" \
"M v[72:75],v[200:203],a[40:43],v[72:75]\nv_mul_f32 v139,v139,v157\nv_mul_f32 v140,v140,v157\nM v[72:75],v[204:207],a[56:59],v[72:75]\ns_add_u32 m0,s65,4096\ns_add_u32 s67,s66,33152\nM v[76:79],v[200:203],a[44:47],v[76:79]\nds_read_b64_tr_b16 v[200:201],v242 offset:17408\n" \
"ds_read_b64_tr_b16 v[202:203],v242 offset:17664\nM v[76:79],v[204:207],a[60:63],v[76:79]\nds_read_b64_tr_b16 v[204:205],v242 offset:25600\nds_read_b64_tr_b16 v[206:207],v242 offset:25856\nM v[16:19],v[208:211],a[32:35],v[16:19]\nbuffer_load_dwordx4 v224,s[36:39],s67 offen offset:0 lds\ns_waitcnt lgkmcnt(6)\nM v[16:19],v[212:215],a[48:51],v[16:19]\n" \
"v_mul_f32 v141,v141,v157\nv_mul_f32 v142,v142,v157\nM v[20:23],v[208:211],a[36:39],v[20:23]\nv_mul_f32 v143,v143,v157\nv_mul_f32 v144,v144,v157\nM v[20:23],v[212:215],a[52:55],v[20:23]\nv_mul_f32 v145,v145,v157\nv_mul_f32 v146,v146,v157\n" \
"M v[80:83],v[208:211],a[40:43],v[80:83]\nglobal_load_dword v247,v253,s[16:17]\nM v[80:83],v[212:215],a[56:59],v[80:83]\nbuffer_load_dwordx4 v225,s[36:39],s67 offen offset:1024 lds\nM v[84:87],v[208:211],a[44:47],v[84:87]\nds_read_b64_tr_b16 v[208:209],v241 offset:17920\nds_read_b64_tr_b16 v[210:211],v241 offset:18176\nM v[84:87],v[212:215],a[60:63],v[84:87]\n" \
"ds_read_b64_tr_b16 v[212:213],v241 offset:26112\nds_read_b64_tr_b16 v[214:215],v241 offset:26368\nM v[24:27],v[216:219],a[32:35],v[24:27]\nv_add_u32 v253,0x100000,v253\nv_mul_f32 v147,v147,v157\nM v[24:27],v[220:223],a[48:51],v[24:27]\nv_mul_f32 v148,v148,v157\nv_mul_f32 v149,v149,v157\n" \
"M v[28:31],v[216:219],a[36:39],v[28:31]\nbuffer_load_dwordx4 v226,s[36:39],s67 offen offset:2048 lds\nM v[28:31],v[220:223],a[52:55],v[28:31]\nv_mul_f32 v150,v150,v157\nv_mul_f32 v151,v151,v157\nM v[88:91],v[216:219],a[40:43],v[88:91]\nv_lshlrev_b32 v254,14,v254\ns_add_u32 s78,s78,32768\n" \
"M v[88:91],v[220:223],a[56:59],v[88:91]\nv_and_b32 v169,3,v249\nv_mul_u32_u24 v169,12288,v169\nM v[92:95],v[216:219],a[44:47],v[92:95]\nds_read_b64_tr_b16 v[216:217],v242 offset:17920\nds_read_b64_tr_b16 v[218:219],v242 offset:18176\nM v[92:95],v[220:223],a[60:63],v[92:95]\nbuffer_load_dwordx4 v227,s[36:39],s67 offen offset:3072 lds\n" \
"ds_read_b64_tr_b16 v[220:221],v242 offset:26112\nds_read_b64_tr_b16 v[222:223],v242 offset:26368\nM v[32:35],v[192:195],a[32:35],v[32:35]\nv_and_b32 v170,28,v249\ns_waitcnt lgkmcnt(6)\nM v[32:35],v[196:199],a[48:51],v[32:35]\ns_add_u32 m0,s64,147456\ns_add_u32 s69,s68,1572864\n" \
"M v[36:39],v[192:195],a[36:39],v[36:39]\nv_add_u32 v169,v169,v170\nv_lshrrev_b32 v251,3,v249\nM v[36:39],v[196:199],a[52:55],v[36:39]\ns_cmp_eq_u32 s6,0\ns_cselect_b32 s72,s75,0x8000000\nM v[96:99],v[192:195],a[40:43],v[96:99]\nbuffer_load_dwordx4 v243,s[40:43],s69 offen offset:0 lds\n" \
"M v[96:99],v[196:199],a[56:59],v[96:99]\ns_add_u32 s72,s72,94208\nv_lshlrev_b32 v170,3,v170\nM v[100:103],v[192:195],a[44:47],v[100:103]\nv_cvt_pk_bf16_f32 v144,0,v144\nv_cvt_pk_bf16_f32 v145,0,v145\nM v[100:103],v[196:199],a[60:63],v[100:103]\nv_cvt_pk_bf16_f32 v136,0,v136\n" \
"v_cvt_pk_bf16_f32 v137,0,v137\nM v[40:43],v[200:203],a[32:35],v[40:43]\nv_cvt_pk_bf16_f32 v138,0,v138\nv_cvt_pk_bf16_f32 v139,0,v139\nM v[40:43],v[204:207],a[48:51],v[40:43]\nbuffer_load_dwordx4 v244,s[40:43],s69 offen offset:1024 lds\nM v[44:47],v[200:203],a[36:39],v[44:47]\nv_cvt_pk_bf16_f32 v140,0,v140\n" \
"v_cvt_pk_bf16_f32 v141,0,v141\nM v[44:47],v[204:207],a[52:55],v[44:47]\nv_cvt_pk_bf16_f32 v142,0,v142\nv_cvt_pk_bf16_f32 v143,0,v143\nM v[104:107],v[200:203],a[40:43],v[104:107]\nv_cvt_pk_bf16_f32 v146,0,v146\nv_cvt_pk_bf16_f32 v147,0,v147\nM v[104:107],v[204:207],a[56:59],v[104:107]\n" \
"v_bfe_u32 v250,v249,3,2\nv_cvt_pk_bf16_f32 v148,0,v148\nM v[108:111],v[200:203],a[44:47],v[108:111]\nbuffer_load_dwordx4 v245,s[40:43],s69 offen offset:2048 lds\nM v[108:111],v[204:207],a[60:63],v[108:111]\nv_cvt_pk_bf16_f32 v149,0,v149\nv_cvt_pk_bf16_f32 v150,0,v150\nM v[48:51],v[208:211],a[32:35],v[48:51]\n" \
"v_cvt_pk_bf16_f32 v151,0,v151\ns_waitcnt lgkmcnt(0)\nM v[48:51],v[212:215],a[48:51],v[48:51]\ns_mov_b32 s98,0x4000\ns_mov_b32 s0,28672\nM v[52:55],v[208:211],a[36:39],v[52:55]\ns_add_u32 s10,s61,1\ns_add_u32 s11,s61,2\n" \
"M v[52:55],v[212:215],a[52:55],v[52:55]\nbuffer_load_dwordx4 v246,s[40:43],s69 offen offset:3072 lds\nM v[112:115],v[208:211],a[40:43],v[112:115]\ns_waitcnt vmcnt(13)\ns_barrier\nds_read_b128 a[64:67],v228 offset:16384\nds_read_b128 a[68:71],v228 offset:18432\nds_read_b128 a[72:75],v228 offset:20480\n" \
"ds_read_b128 a[76:79],v228 offset:22528\nds_read_b128 a[80:83],v229 offset:16384\nds_read_b128 a[84:87],v229 offset:18432\nds_read_b128 a[88:91],v229 offset:20480\nds_read_b128 a[92:95],v229 offset:22528\nM v[112:115],v[212:215],a[56:59],v[112:115]\ns_add_u32 m0,s65,8192\ns_add_u32 s67,s66,512\n" \
"ds_read_b64_tr_b16 v[192:193],v241 offset:32768\nds_read_b64_tr_b16 v[194:195],v241 offset:33024\nM v[116:119],v[208:211],a[44:47],v[116:119]\nds_read_b64_tr_b16 v[196:197],v241 offset:40960\nds_read_b64_tr_b16 v[198:199],v241 offset:41216\nM v[116:119],v[212:215],a[60:63],v[116:119]\ns_add_u32 s14,s61,3\ns_and_b32 s10,s10,3\n" \
"M v[56:59],v[216:219],a[32:35],v[56:59]\nbuffer_load_dwordx4 v224,s[36:39],s67 offen offset:0 lds\nM v[56:59],v[220:223],a[48:51],v[56:59]\ns_and_b32 s11,s11,3\ns_and_b32 s14,s14,3\nM v[60:63],v[216:219],a[36:39],v[60:63]\ns_mov_b32 s47,0x20000\ns_mov_b32 s51,0x20000\n" \
"M v[60:63],v[220:223],a[52:55],v[60:63]\ns_mov_b32 s94,0xc000\ns_lshl_b32 s10,s10,13\nM v[120:123],v[216:219],a[40:43],v[120:123]\nglobal_load_dword v247,v253,s[16:17]\nM v[120:123],v[220:223],a[56:59],v[120:123]\nbuffer_load_dwordx4 v225,s[36:39],s67 offen offset:1024 lds\nds_read_b64_tr_b16 v[200:201],v242 offset:32768\n" \
"ds_read_b64_tr_b16 v[202:203],v242 offset:33024\nM v[124:127],v[216:219],a[44:47],v[124:127]\nds_read_b64_tr_b16 v[204:205],v242 offset:40960\nds_read_b64_tr_b16 v[206:207],v242 offset:41216\nM v[124:127],v[220:223],a[60:63],v[124:127]\nds_read_b64_tr_b16 v[208:209],v241 offset:33280\ns_waitcnt lgkmcnt(7)\nM v[0:3],v[192:195],a[64:67],v[0:3]\n" \
"ds_read_b64_tr_b16 v[210:211],v241 offset:33536\ns_waitcnt lgkmcnt(6)\nM v[0:3],v[196:199],a[80:83],v[0:3]\nds_read_b64_tr_b16 v[212:213],v241 offset:41472\nds_read_b64_tr_b16 v[214:215],v241 offset:41728\nM v[4:7],v[192:195],a[68:71],v[4:7]\nbuffer_load_dwordx4 v226,s[36:39],s67 offen offset:2048 lds\nM v[4:7],v[196:199],a[84:87],v[4:7]\n" \
"ds_read_b64_tr_b16 v[216:217],v242 offset:33280\nds_read_b64_tr_b16 v[218:219],v242 offset:33536\nM v[64:67],v[192:195],a[72:75],v[64:67]\nds_read_b64_tr_b16 v[220:221],v242 offset:41472\nds_read_b64_tr_b16 v[222:223],v242 offset:41728\nM v[64:67],v[196:199],a[88:91],v[64:67]\nv_add_u32 v253,0xffd00000,v253\ns_lshl_b32 s11,s11,13\n" \
"M v[68:71],v[192:195],a[76:79],v[68:71]\nds_read_b64_tr_b16 v[192:193],v241 offset:33792\nds_read_b64_tr_b16 v[194:195],v241 offset:34048\nM v[68:71],v[196:199],a[92:95],v[68:71]\nbuffer_load_dwordx4 v227,s[36:39],s67 offen offset:3072 lds\ns_waitcnt lgkmcnt(12)\nM v[8:11],v[200:203],a[64:67],v[8:11]\nds_read_b64_tr_b16 v[196:197],v241 offset:41984\n" \
"s_waitcnt lgkmcnt(11)\nM v[8:11],v[204:207],a[80:83],v[8:11]\ns_add_u32 m0,s65,12288\ns_add_u32 s67,s66,33280\nM v[12:15],v[200:203],a[68:71],v[12:15]\nds_read_b64_tr_b16 v[198:199],v241 offset:42240\ns_lshl_b32 s14,s14,13\nM v[12:15],v[204:207],a[84:87],v[12:15]\n" \
"s_mov_b32 s82,0xf0000\ns_mov_b32 s83,0xfff00ff\nM v[72:75],v[200:203],a[72:75],v[72:75]\nbuffer_load_dwordx4 v224,s[36:39],s67 offen offset:0 lds\nM v[72:75],v[204:207],a[88:91],v[72:75]\ns_mov_b32 s84,0x1f0001\ns_mov_b32 s85,0x1fff01ff\nM v[76:79],v[200:203],a[76:79],v[76:79]\n" \
"ds_read_b64_tr_b16 v[200:201],v242 offset:33792\nds_read_b64_tr_b16 v[202:203],v242 offset:34048\nM v[76:79],v[204:207],a[92:95],v[76:79]\nds_read_b64_tr_b16 v[204:205],v242 offset:41984\ns_waitcnt lgkmcnt(7)\nM v[16:19],v[208:211],a[64:67],v[16:19]\nds_read_b64_tr_b16 v[206:207],v242 offset:42240\ns_mov_b32 s86,0x3f0003\n" \
"M v[16:19],v[212:215],a[80:83],v[16:19]\nbuffer_load_dwordx4 v225,s[36:39],s67 offen offset:1024 lds\nM v[20:23],v[208:211],a[68:71],v[20:23]\ns_mov_b32 s87,0x3fff03ff\ns_mov_b32 s88,0x7f0007\nM v[20:23],v[212:215],a[84:87],v[20:23]\ns_mov_b32 s89,0x7fff07ff\ns_mov_b64 s[90:91],-1\n" \
"M v[80:83],v[208:211],a[72:75],v[80:83]\ns_mov_b32 s28,0x5f800000\ns_mov_b32 s99,0\nM v[80:83],v[212:215],a[88:91],v[80:83]\nM v[84:87],v[208:211],a[76:79],v[84:87]\nbuffer_load_dwordx4 v226,s[36:39],s67 offen offset:2048 lds\nM v[84:87],v[212:215],a[92:95],v[84:87]\nds_read_b64_tr_b16 v[208:209],v241 offset:34304\n" \
"ds_read_b64_tr_b16 v[210:211],v241 offset:34560\nM v[24:27],v[216:219],a[64:67],v[24:27]\nds_read_b64_tr_b16 v[212:213],v241 offset:42496\nds_read_b64_tr_b16 v[214:215],v241 offset:42752\nM v[24:27],v[220:223],a[80:83],v[24:27]\nM v[28:31],v[216:219],a[68:71],v[28:31]\nM v[28:31],v[220:223],a[84:87],v[28:31]\nbuffer_load_dwordx4 v227,s[36:39],s67 offen offset:3072 lds\n" \
"M v[88:91],v[216:219],a[72:75],v[88:91]\nM v[88:91],v[220:223],a[88:91],v[88:91]\ns_add_u32 m0,s64,98304\ns_add_u32 s69,s68,2097152\nM v[92:95],v[216:219],a[76:79],v[92:95]\nds_read_b64_tr_b16 v[216:217],v242 offset:34304\nds_read_b64_tr_b16 v[218:219],v242 offset:34560\nM v[92:95],v[220:223],a[92:95],v[92:95]\n" \
"ds_read_b64_tr_b16 v[220:221],v242 offset:42496\ns_waitcnt lgkmcnt(7)\nM v[32:35],v[192:195],a[64:67],v[32:35]\nbuffer_load_dwordx4 v243,s[40:43],s69 offen offset:0 lds\nM v[32:35],v[196:199],a[80:83],v[32:35]\nds_read_b64_tr_b16 v[222:223],v242 offset:42752\nM v[36:39],v[192:195],a[68:71],v[36:39]\nM v[36:39],v[196:199],a[84:87],v[36:39]\n" \
"M v[96:99],v[192:195],a[72:75],v[96:99]\nM v[96:99],v[196:199],a[88:91],v[96:99]\nbuffer_load_dwordx4 v244,s[40:43],s69 offen offset:1024 lds\nM v[100:103],v[192:195],a[76:79],v[100:103]\nM v[100:103],v[196:199],a[92:95],v[100:103]\nM v[40:43],v[200:203],a[64:67],v[40:43]\nM v[40:43],v[204:207],a[80:83],v[40:43]\nM v[44:47],v[200:203],a[68:71],v[44:47]\n" \
"buffer_load_dwordx4 v245,s[40:43],s69 offen offset:2048 lds\nM v[44:47],v[204:207],a[84:87],v[44:47]\nM v[104:107],v[200:203],a[72:75],v[104:107]\nM v[104:107],v[204:207],a[88:91],v[104:107]\nM v[108:111],v[200:203],a[76:79],v[108:111]\nM v[108:111],v[204:207],a[92:95],v[108:111]\nbuffer_load_dwordx4 v246,s[40:43],s69 offen offset:3072 lds\ns_waitcnt lgkmcnt(1)\n" \
"M v[48:51],v[208:211],a[64:67],v[48:51]\nM v[48:51],v[212:215],a[80:83],v[48:51]\ns_add_u32 m0,s65,16384\ns_add_u32 s67,s66,640\nM v[52:55],v[208:211],a[68:71],v[52:55]\nM v[52:55],v[212:215],a[84:87],v[52:55]\nM v[112:115],v[208:211],a[72:75],v[112:115]\nbuffer_load_dwordx4 v224,s[36:39],s67 offen offset:0 lds\n" \
"s_waitcnt vmcnt(14) lgkmcnt(0)\ns_barrier\nds_read_b128 a[96:99],v228 offset:0\nds_read_b128 a[100:103],v228 offset:2048\nds_read_b128 a[104:107],v228 offset:4096\nds_read_b128 a[108:111],v228 offset:6144\nds_read_b128 a[112:115],v229 offset:0\nds_read_b128 a[116:119],v229 offset:2048\n" \
"ds_read_b128 a[120:123],v229 offset:4096\nds_read_b128 a[124:127],v229 offset:6144\nM v[112:115],v[212:215],a[88:91],v[112:115]\nds_read_b64_tr_b16 v[192:193],v241 offset:49152\nds_read_b64_tr_b16 v[194:195],v241 offset:49408\nM v[116:119],v[208:211],a[76:79],v[116:119]\nds_read_b64_tr_b16 v[196:197],v241 offset:57344\nds_read_b64_tr_b16 v[198:199],v241 offset:57600\n" \
"M v[116:119],v[212:215],a[92:95],v[116:119]\nM v[56:59],v[216:219],a[64:67],v[56:59]\nM v[56:59],v[220:223],a[80:83],v[56:59]\nbuffer_load_dwordx4 v225,s[36:39],s67 offen offset:1024 lds\nM v[60:63],v[216:219],a[68:71],v[60:63]\nM v[60:63],v[220:223],a[84:87],v[60:63]\nM v[120:123],v[216:219],a[72:75],v[120:123]\nM v[120:123],v[220:223],a[88:91],v[120:123]\n" \
"ds_read_b64_tr_b16 v[200:201],v242 offset:49152\nds_read_b64_tr_b16 v[202:203],v242 offset:49408\nM v[124:127],v[216:219],a[76:79],v[124:127]\nbuffer_load_dwordx4 v226,s[36:39],s67 offen offset:2048 lds\nds_read_b64_tr_b16 v[204:205],v242 offset:57344\nds_read_b64_tr_b16 v[206:207],v242 offset:57600\nM v[124:127],v[220:223],a[92:95],v[124:127]\nds_read_b64_tr_b16 v[208:209],v241 offset:49664\n" \
"s_waitcnt lgkmcnt(7)\nM v[0:3],v[192:195],a[96:99],v[0:3]\nds_read_b64_tr_b16 v[210:211],v241 offset:49920\ns_waitcnt lgkmcnt(6)\nM v[0:3],v[196:199],a[112:115],v[0:3]\nds_read_b64_tr_b16 v[212:213],v241 offset:57856\nds_read_b64_tr_b16 v[214:215],v241 offset:58112\nM v[4:7],v[192:195],a[100:103],v[4:7]\n" \
"ds_read_b64_tr_b16 v[216:217],v242 offset:49664\nds_read_b64_tr_b16 v[218:219],v242 offset:49920\nM v[4:7],v[196:199],a[116:119],v[4:7]\nbuffer_load_dwordx4 v227,s[36:39],s67 offen offset:3072 lds\nM v[64:67],v[192:195],a[104:107],v[64:67]\nds_read_b64_tr_b16 v[220:221],v242 offset:57856\nds_read_b64_tr_b16 v[222:223],v242 offset:58112\nM v[64:67],v[196:199],a[120:123],v[64:67]\n" \
"s_add_u32 m0,s65,20480\ns_add_u32 s67,s66,33408\nM v[68:71],v[192:195],a[108:111],v[68:71]\nds_read_b64_tr_b16 v[192:193],v241 offset:50176\nds_read_b64_tr_b16 v[194:195],v241 offset:50432\nM v[68:71],v[196:199],a[124:127],v[68:71]\nds_read_b64_tr_b16 v[196:197],v241 offset:58368\ns_waitcnt lgkmcnt(13)\n" \
"M v[8:11],v[200:203],a[96:99],v[8:11]\nbuffer_load_dwordx4 v224,s[36:39],s67 offen offset:0 lds\ns_waitcnt lgkmcnt(11)\nM v[8:11],v[204:207],a[112:115],v[8:11]\nds_read_b64_tr_b16 v[198:199],v241 offset:58624\nM v[12:15],v[200:203],a[100:103],v[12:15]\nM v[12:15],v[204:207],a[116:119],v[12:15]\nM v[72:75],v[200:203],a[104:107],v[72:75]\n" \
"M v[72:75],v[204:207],a[120:123],v[72:75]\nbuffer_load_dwordx4 v225,s[36:39],s67 offen offset:1024 lds\nM v[76:79],v[200:203],a[108:111],v[76:79]\nds_read_b64_tr_b16 v[200:201],v242 offset:50176\nds_read_b64_tr_b16 v[202:203],v242 offset:50432\nM v[76:79],v[204:207],a[124:127],v[76:79]\nds_read_b64_tr_b16 v[204:205],v242 offset:58368\ns_waitcnt lgkmcnt(9)\n" \
"M v[16:19],v[208:211],a[96:99],v[16:19]\nds_read_b64_tr_b16 v[206:207],v242 offset:58624\nM v[16:19],v[212:215],a[112:115],v[16:19]\nM v[20:23],v[208:211],a[100:103],v[20:23]\nbuffer_load_dwordx4 v226,s[36:39],s67 offen offset:2048 lds\nM v[20:23],v[212:215],a[116:119],v[20:23]\nM v[80:83],v[208:211],a[104:107],v[80:83]\nM v[80:83],v[212:215],a[120:123],v[80:83]\n" \
"M v[84:87],v[208:211],a[108:111],v[84:87]\nds_read_b64_tr_b16 v[208:209],v241 offset:50688\nds_read_b64_tr_b16 v[210:211],v241 offset:50944\nM v[84:87],v[212:215],a[124:127],v[84:87]\nbuffer_load_dwordx4 v227,s[36:39],s67 offen offset:3072 lds\nM v[24:27],v[216:219],a[96:99],v[24:27]\nds_read_b64_tr_b16 v[212:213],v241 offset:58880\ns_waitcnt lgkmcnt(7)\n" \
"M v[24:27],v[220:223],a[112:115],v[24:27]\ns_add_u32 m0,s64,114688\ns_add_u32 s69,s68,2621440\nM v[28:31],v[216:219],a[100:103],v[28:31]\nds_read_b64_tr_b16 v[214:215],v241 offset:59136\nM v[28:31],v[220:223],a[116:119],v[28:31]\nM v[88:91],v[216:219],a[104:107],v[88:91]\nbuffer_load_dwordx4 v243,s[40:43],s69 offen offset:0 lds\n" \
"M v[88:91],v[220:223],a[120:123],v[88:91]\nM v[92:95],v[216:219],a[108:111],v[92:95]\nds_read_b64_tr_b16 v[216:217],v242 offset:50688\nds_read_b64_tr_b16 v[218:219],v242 offset:50944\nM v[92:95],v[220:223],a[124:127],v[92:95]\nds_read_b64_tr_b16 v[220:221],v242 offset:58880\nds_read_b64_tr_b16 v[222:223],v242 offset:59136\nM v[32:35],v[192:195],a[96:99],v[32:35]\n" \
"buffer_load_dwordx2 v[166:167],v165,s[52:55],s80 offen sc1\nM v[32:35],v[196:199],a[112:115],v[32:35]\nbuffer_load_dwordx4 v244,s[40:43],s69 offen offset:1024 lds\nM v[36:39],v[192:195],a[100:103],v[36:39]\nM v[36:39],v[196:199],a[116:119],v[36:39]\nM v[96:99],v[192:195],a[104:107],v[96:99]\nM v[96:99],v[196:199],a[120:123],v[96:99]\nM v[100:103],v[192:195],a[108:111],v[100:103]\n" \
"buffer_load_dwordx4 v245,s[40:43],s69 offen offset:2048 lds\nM v[100:103],v[196:199],a[124:127],v[100:103]\ns_waitcnt lgkmcnt(5)\nM v[40:43],v[200:203],a[96:99],v[40:43]\nM v[40:43],v[204:207],a[112:115],v[40:43]\nM v[44:47],v[200:203],a[100:103],v[44:47]\nM v[44:47],v[204:207],a[116:119],v[44:47]\nbuffer_load_dwordx4 v246,s[40:43],s69 offen offset:3072 lds\n" \
"M v[104:107],v[200:203],a[104:107],v[104:107]\nM v[104:107],v[204:207],a[120:123],v[104:107]\ns_add_u32 m0,s65,0\ns_add_u32 s67,s66,768\nM v[108:111],v[200:203],a[108:111],v[108:111]\nM v[108:111],v[204:207],a[124:127],v[108:111]\nM v[48:51],v[208:211],a[96:99],v[48:51]\nbuffer_load_dwordx4 v224,s[36:39],s67 offen offset:0 lds\n" \
"s_waitcnt lgkmcnt(0)\nM v[48:51],v[212:215],a[112:115],v[48:51]\nM v[52:55],v[208:211],a[100:103],v[52:55]\nM v[52:55],v[212:215],a[116:119],v[52:55]\nM v[112:115],v[208:211],a[104:107],v[112:115]\ns_waitcnt vmcnt(14)\ns_barrier\nM v[112:115],v[212:215],a[120:123],v[112:115]\n" \
"buffer_load_dwordx4 v225,s[36:39],s67 offen offset:1024 lds\nds_read_b128 a[128:131],v228 offset:8192\nds_read_b64_tr_b16 v[192:193],v241 offset:0\nds_read_b64_tr_b16 v[194:195],v241 offset:256\nM v[116:119],v[208:211],a[108:111],v[116:119]\nds_read_b128 a[144:147],v229 offset:8192\nds_read_b64_tr_b16 v[196:197],v241 offset:8192\nds_read_b64_tr_b16 v[198:199],v241 offset:8448\n" \
"M v[116:119],v[212:215],a[124:127],v[116:119]\nds_read_b128 a[132:135],v228 offset:10240\nM v[56:59],v[216:219],a[96:99],v[56:59]\nds_read_b128 a[148:151],v229 offset:10240\nM v[56:59],v[220:223],a[112:115],v[56:59]\nds_read_b128 a[136:139],v228 offset:12288\nM v[60:63],v[216:219],a[100:103],v[60:63]\nbuffer_load_dwordx4 v226,s[36:39],s67 offen offset:2048 lds\n" \
"ds_read_b128 a[152:155],v229 offset:12288\nM v[60:63],v[220:223],a[116:119],v[60:63]\nds_read_b128 a[140:143],v228 offset:14336\nM v[120:123],v[216:219],a[104:107],v[120:123]\nds_read_b128 a[156:159],v229 offset:14336\nM v[120:123],v[220:223],a[120:123],v[120:123]\nds_read_b64_tr_b16 v[200:201],v242 offset:0\nds_read_b64_tr_b16 v[202:203],v242 offset:256\n" \
"M v[124:127],v[216:219],a[108:111],v[124:127]\nds_read_b64_tr_b16 v[204:205],v242 offset:8192\nds_read_b64_tr_b16 v[206:207],v242 offset:8448\nM v[124:127],v[220:223],a[124:127],v[124:127]\nbuffer_load_dwordx4 v227,s[36:39],s67 offen offset:3072 lds\ns_waitcnt lgkmcnt(13)\nM v[0:3],v[192:195],a[128:131],v[0:3]\nds_read_b64_tr_b16 v[208:209],v241 offset:512\n" \
"s_waitcnt lgkmcnt(11)\nM v[0:3],v[196:199],a[144:147],v[0:3]\ns_add_u32 m0,s65,4096\ns_add_u32 s67,s66,33536\ns_waitcnt lgkmcnt(10)\nM v[4:7],v[192:195],a[132:135],v[4:7]\nds_read_b64_tr_b16 v[210:211],v241 offset:768\ns_waitcnt lgkmcnt(10)\n" \
"M v[4:7],v[196:199],a[148:151],v[4:7]\nds_read_b64_tr_b16 v[212:213],v241 offset:8704\ns_waitcnt lgkmcnt(10)\nM v[64:67],v[192:195],a[136:139],v[64:67]\nbuffer_load_dwordx4 v224,s[36:39],s67 offen offset:0 lds\ns_waitcnt lgkmcnt(9)\nM v[64:67],v[196:199],a[152:155],v[64:67]\nds_read_b64_tr_b16 v[214:215],v241 offset:8960\n" \
"s_waitcnt lgkmcnt(9)\nM v[68:71],v[192:195],a[140:143],v[68:71]\nds_read_b64_tr_b16 v[216:217],v242 offset:512\ns_waitcnt lgkmcnt(9)\nM v[68:71],v[196:199],a[156:159],v[68:71]\nds_read_b64_tr_b16 v[218:219],v242 offset:768\ns_waitcnt lgkmcnt(8)\nM v[8:11],v[200:203],a[128:131],v[8:11]\n" \
"ds_read_b64_tr_b16 v[220:221],v242 offset:8704\ns_waitcnt lgkmcnt(7)\nM v[8:11],v[204:207],a[144:147],v[8:11]\nbuffer_load_dwordx4 v225,s[36:39],s67 offen offset:1024 lds\nM v[12:15],v[200:203],a[132:135],v[12:15]\nds_read_b64_tr_b16 v[222:223],v242 offset:8960\nds_read_b64_tr_b16 v[192:193],v241 offset:1024\nM v[12:15],v[204:207],a[148:151],v[12:15]\n" \
"ds_read_b64_tr_b16 v[194:195],v241 offset:1280\nds_read_b64_tr_b16 v[196:197],v241 offset:9216\nM v[72:75],v[200:203],a[136:139],v[72:75]\nds_read_b64_tr_b16 v[198:199],v241 offset:9472\nM v[72:75],v[204:207],a[152:155],v[72:75]\nM v[76:79],v[200:203],a[140:143],v[76:79]\nbuffer_load_dwordx4 v226,s[36:39],s67 offen offset:2048 lds\nM v[76:79],v[204:207],a[156:159],v[76:79]\n" \
"ds_read_b64_tr_b16 v[200:201],v242 offset:1024\ns_waitcnt lgkmcnt(10)\nM v[16:19],v[208:211],a[128:131],v[16:19]\nds_read_b64_tr_b16 v[202:203],v242 offset:1280\ns_waitcnt lgkmcnt(10)\nM v[16:19],v[212:215],a[144:147],v[16:19]\nds_read_b64_tr_b16 v[204:205],v242 offset:9216\nds_read_b64_tr_b16 v[206:207],v242 offset:9472\n" \
"M v[20:23],v[208:211],a[132:135],v[20:23]\nM v[20:23],v[212:215],a[148:151],v[20:23]\nbuffer_load_dwordx4 v227,s[36:39],s67 offen offset:3072 lds\nM v[80:83],v[208:211],a[136:139],v[80:83]\nM v[80:83],v[212:215],a[152:155],v[80:83]\ns_add_u32 m0,s64,131072\ns_add_u32 s69,s68,3145728\nM v[84:87],v[208:211],a[140:143],v[84:87]\n" \
"ds_read_b64_tr_b16 v[208:209],v241 offset:1536\nds_read_b64_tr_b16 v[210:211],v241 offset:1792\nM v[84:87],v[212:215],a[156:159],v[84:87]\nds_read_b64_tr_b16 v[212:213],v241 offset:9728\ns_waitcnt lgkmcnt(8)\nM v[24:27],v[216:219],a[128:131],v[24:27]\nbuffer_load_dwordx4 v243,s[40:43],s69 offen offset:0 lds\nM v[24:27],v[220:223],a[144:147],v[24:27]\n" \
"ds_read_b64_tr_b16 v[214:215],v241 offset:9984\nM v[28:31],v[216:219],a[132:135],v[28:31]\nM v[28:31],v[220:223],a[148:151],v[28:31]\nM v[88:91],v[216:219],a[136:139],v[88:91]\nM v[88:91],v[220:223],a[152:155],v[88:91]\nbuffer_load_dwordx4 v244,s[40:43],s69 offen offset:1024 lds\nM v[92:95],v[216:219],a[140:143],v[92:95]\nds_read_b64_tr_b16 v[216:217],v242 offset:1536\n" \
"ds_read_b64_tr_b16 v[218:219],v242 offset:1792\nM v[92:95],v[220:223],a[156:159],v[92:95]\nds_read_b64_tr_b16 v[220:221],v242 offset:9728\nds_read_b64_tr_b16 v[222:223],v242 offset:9984\nM v[32:35],v[192:195],a[128:131],v[32:35]\ns_waitcnt lgkmcnt(8)\nM v[32:35],v[196:199],a[144:147],v[32:35]\nM v[36:39],v[192:195],a[132:135],v[36:39]\n" \
"buffer_load_dwordx4 v245,s[40:43],s69 offen offset:2048 lds\nM v[36:39],v[196:199],a[148:151],v[36:39]\nM v[96:99],v[192:195],a[136:139],v[96:99]\ns_waitcnt vmcnt(14)\nv_xor_b32 v166,v166,v230\nM v[96:99],v[196:199],a[152:155],v[96:99]\nv_xor_b32 v167,v167,v231\nv_or_b32 v166,v166,v167\n" \
"M v[100:103],v[192:195],a[140:143],v[100:103]\nv_cmp_ne_u32 vcc,0,v166\nM v[100:103],v[196:199],a[156:159],v[100:103]\nbuffer_load_dwordx4 v246,s[40:43],s69 offen offset:3072 lds\nM v[40:43],v[200:203],a[128:131],v[40:43]\ns_cbranch_vccnz .Lhs\n.Lhb:\nv_bfe_u32 v231,v249,2,3\n" \
"M v[40:43],v[204:207],a[144:147],v[40:43]\ns_add_u32 m0,s65,8192\ns_add_u32 s67,s66,896\nM v[44:47],v[200:203],a[132:135],v[44:47]\nv_mul_u32_u24 v231,3584,v231\nv_and_b32 v230,31,v249\nM v[44:47],v[204:207],a[148:151],v[44:47]\nv_lshlrev_b32 v230,7,v230\n" \
"M v[104:107],v[200:203],a[136:139],v[104:107]\nbuffer_load_dwordx4 v224,s[36:39],s67 offen offset:0 lds\nM v[104:107],v[204:207],a[152:155],v[104:107]\nM v[108:111],v[200:203],a[140:143],v[108:111]\nM v[108:111],v[204:207],a[156:159],v[108:111]\ns_waitcnt lgkmcnt(0)\nM v[48:51],v[208:211],a[128:131],v[48:51]\nM v[48:51],v[212:215],a[144:147],v[48:51]\n" \
"buffer_load_dwordx4 v225,s[36:39],s67 offen offset:1024 lds\nM v[52:55],v[208:211],a[132:135],v[52:55]\nM v[52:55],v[212:215],a[148:151],v[52:55]\nM v[112:115],v[208:211],a[136:139],v[112:115]\ns_waitcnt vmcnt(14)\ns_barrier\nM v[112:115],v[212:215],a[152:155],v[112:115]\nds_read_b128 a[160:163],v228 offset:16384\n" \
"ds_read_b64_tr_b16 v[192:193],v241 offset:16384\nds_read_b64_tr_b16 v[194:195],v241 offset:16640\nM v[116:119],v[208:211],a[140:143],v[116:119]\nds_read_b128 a[176:179],v229 offset:16384\nds_read_b64_tr_b16 v[196:197],v241 offset:24576\nds_read_b64_tr_b16 v[198:199],v241 offset:24832\nM v[116:119],v[212:215],a[156:159],v[116:119]\nds_read_b128 a[164:167],v228 offset:18432\n" \
"M v[56:59],v[216:219],a[128:131],v[56:59]\nbuffer_load_dwordx4 v[152:155],v240,s[52:55],s71 offen sc1\nds_read_b128 a[180:183],v229 offset:18432\nM v[56:59],v[220:223],a[144:147],v[56:59]\nbuffer_load_dwordx4 v226,s[36:39],s67 offen offset:2048 lds\ns_add_u32 s71,s70,798720\nds_read_b128 a[168:171],v228 offset:20480\nbuffer_load_dwordx4 v[156:159],v240,s[52:55],s71 offen sc1\n" \
"M v[60:63],v[216:219],a[132:135],v[60:63]\nds_read_b128 a[184:187],v229 offset:20480\ns_add_u32 s71,s70,811008\nM v[60:63],v[220:223],a[148:151],v[60:63]\nbuffer_load_dwordx4 v227,s[36:39],s67 offen offset:3072 lds\nbuffer_load_dwordx4 v[160:163],v240,s[52:55],s71 offen sc1\nds_read_b128 a[172:175],v228 offset:22528\nM v[120:123],v[216:219],a[136:139],v[120:123]\n" \
"s_add_u32 s71,s70,823296\nds_read_b128 a[188:191],v229 offset:22528\nbuffer_load_dwordx4 v[164:167],v240,s[52:55],s71 offen sc1\nM v[120:123],v[220:223],a[152:155],v[120:123]\ns_add_u32 m0,s65,12288\ns_add_u32 s67,s66,33664\nds_read_b64_tr_b16 v[200:201],v242 offset:16384\nds_read_b64_tr_b16 v[202:203],v242 offset:16640\n" \
"M v[124:127],v[216:219],a[140:143],v[124:127]\nds_read_b64_tr_b16 v[204:205],v242 offset:24576\nds_read_b64_tr_b16 v[206:207],v242 offset:24832\nM v[124:127],v[220:223],a[156:159],v[124:127]\ns_lshl_b32 s71,s6,5\ns_waitcnt lgkmcnt(9)\nM v[0:3],v[192:195],a[160:163],v[0:3]\nds_read_b64_tr_b16 v[208:209],v241 offset:16896\n" \
"s_add_u32 s71,s71,790528\nM v[0:3],v[196:199],a[176:179],v[0:3]\nds_read_b64_tr_b16 v[210:211],v241 offset:17152\nds_read_b64_tr_b16 v[212:213],v241 offset:25088\nM v[4:7],v[192:195],a[164:167],v[4:7]\nds_read_b64_tr_b16 v[214:215],v241 offset:25344\ns_waitcnt lgkmcnt(11)\nM v[4:7],v[196:199],a[180:183],v[4:7]\n" \
"ds_read_b64_tr_b16 v[216:217],v242 offset:16896\nds_read_b64_tr_b16 v[218:219],v242 offset:17152\nM v[64:67],v[192:195],a[168:171],v[64:67]\nds_read_b64_tr_b16 v[220:221],v242 offset:25088\ns_waitcnt lgkmcnt(13)\nM v[64:67],v[196:199],a[184:187],v[64:67]\nds_read_b64_tr_b16 v[222:223],v242 offset:25344\ns_waitcnt lgkmcnt(13)\n" \
"M v[68:71],v[192:195],a[172:175],v[68:71]\nglobal_load_dword v247,v253,s[12:13]\ns_waitcnt lgkmcnt(12)\nM v[68:71],v[196:199],a[188:191],v[68:71]\nds_read_b64_tr_b16 v[192:193],v241 offset:17408\ns_waitcnt lgkmcnt(11)\nM v[8:11],v[200:203],a[160:163],v[8:11]\nds_read_b64_tr_b16 v[194:195],v241 offset:17664\n" \
"s_waitcnt lgkmcnt(10)\nM v[8:11],v[204:207],a[176:179],v[8:11]\nds_read_b64_tr_b16 v[196:197],v241 offset:25600\nds_read_b64_tr_b16 v[198:199],v241 offset:25856\nM v[12:15],v[200:203],a[164:167],v[12:15]\nbuffer_load_dword v247,v254,s[56:59],s78 offen\nM v[12:15],v[204:207],a[180:183],v[12:15]\nv_add_u32 v253,0x100000,v253\n" \
"v_and_b32 v254,3,v249\nM v[72:75],v[200:203],a[168:171],v[72:75]\ns_cmp_lt_u32 s7,2\ns_cselect_b32 s70,0,1.0\nM v[72:75],v[204:207],a[184:187],v[72:75]\ns_cmp_eq_u32 s62,5\ns_cselect_b64 s[56:57],s[22:23],s[24:25]\nM v[76:79],v[200:203],a[172:175],v[76:79]\n" \
"ds_read_b64_tr_b16 v[200:201],v242 offset:17408\nds_read_b64_tr_b16 v[202:203],v242 offset:17664\nM v[76:79],v[204:207],a[188:191],v[76:79]\nds_read_b64_tr_b16 v[204:205],v242 offset:25600\ns_waitcnt lgkmcnt(7)\nM v[16:19],v[208:211],a[160:163],v[16:19]\nds_read_b64_tr_b16 v[206:207],v242 offset:25856\nv_lshl_add_u32 v231,v254,7,v231\n" \
"M v[16:19],v[212:215],a[176:179],v[16:19]\ns_add_u32 s69,s68,3670016\nM v[20:23],v[208:211],a[164:167],v[20:23]\nbuffer_load_dwordx4 v224,s[36:39],s67 offen offset:0 lds\nM v[20:23],v[212:215],a[180:183],v[20:23]\nM v[80:83],v[208:211],a[168:171],v[80:83]\nM v[80:83],v[212:215],a[184:187],v[80:83]\nM v[84:87],v[208:211],a[172:175],v[84:87]\n" \
"buffer_load_dwordx4 v225,s[36:39],s67 offen offset:1024 lds\nM v[84:87],v[212:215],a[188:191],v[84:87]\nds_read_b64_tr_b16 v[208:209],v241 offset:17920\nds_read_b64_tr_b16 v[210:211],v241 offset:18176\nM v[24:27],v[216:219],a[160:163],v[24:27]\nds_read_b64_tr_b16 v[212:213],v241 offset:26112\nds_read_b64_tr_b16 v[214:215],v241 offset:26368\nM v[24:27],v[220:223],a[176:179],v[24:27]\n" \
"M v[28:31],v[216:219],a[164:167],v[28:31]\nbuffer_load_dwordx4 v226,s[36:39],s67 offen offset:2048 lds\nM v[28:31],v[220:223],a[180:183],v[28:31]\nM v[88:91],v[216:219],a[168:171],v[88:91]\nM v[88:91],v[220:223],a[184:187],v[88:91]\nM v[92:95],v[216:219],a[172:175],v[92:95]\nbuffer_load_dwordx4 v227,s[36:39],s67 offen offset:3072 lds\nM v[92:95],v[220:223],a[188:191],v[92:95]\n" \
"s_add_u32 m0,s64,147456\ns_waitcnt lgkmcnt(4)\nM v[32:35],v[192:195],a[160:163],v[32:35]\nds_read_b64_tr_b16 v[216:217],v242 offset:17920\nds_read_b64_tr_b16 v[218:219],v242 offset:18176\nM v[32:35],v[196:199],a[176:179],v[32:35]\nds_read_b64_tr_b16 v[220:221],v242 offset:26112\nds_read_b64_tr_b16 v[222:223],v242 offset:26368\n" \
"M v[36:39],v[192:195],a[164:167],v[36:39]\nbuffer_load_dwordx4 v243,s[40:43],s69 offen offset:0 lds\nM v[36:39],v[196:199],a[180:183],v[36:39]\ns_cmp_eq_u32 s62,0\ns_cselect_b32 s36,0x8000,0\nM v[96:99],v[192:195],a[168:171],v[96:99]\ns_add_u32 s36,s36,s64\nM v[96:99],v[196:199],a[184:187],v[96:99]\n" \
"M v[100:103],v[192:195],a[172:175],v[100:103]\nbuffer_load_dwordx4 v244,s[40:43],s69 offen offset:1024 lds\nM v[100:103],v[196:199],a[188:191],v[100:103]\nM v[40:43],v[200:203],a[160:163],v[40:43]\nM v[40:43],v[204:207],a[176:179],v[40:43]\nM v[44:47],v[200:203],a[164:167],v[44:47]\nglobal_load_dword v247,v253,s[12:13]\nM v[44:47],v[204:207],a[180:183],v[44:47]\n" \
"buffer_load_dwordx4 v245,s[40:43],s69 offen offset:2048 lds\nM v[104:107],v[200:203],a[168:171],v[104:107]\nv_add_u32 v253,0x100000,v253\nM v[104:107],v[204:207],a[184:187],v[104:107]\nM v[108:111],v[200:203],a[172:175],v[108:111]\nM v[108:111],v[204:207],a[188:191],v[108:111]\ns_waitcnt lgkmcnt(0)\nM v[48:51],v[208:211],a[160:163],v[48:51]\n" \
"buffer_load_dwordx4 v246,s[40:43],s69 offen offset:3072 lds\nM v[48:51],v[212:215],a[176:179],v[48:51]\ns_sub_u32 s40,s16,0x1000\ns_subb_u32 s41,s17,0\nM v[52:55],v[208:211],a[164:167],v[52:55]\ns_add_u32 m0,s64,98304\ns_add_u32 s69,s68,0\nM v[52:55],v[212:215],a[180:183],v[52:55]\n" \
"s_mov_b32 s16,0\ns_mov_b32 s17,3\nM v[112:115],v[208:211],a[168:171],v[112:115]\ns_waitcnt vmcnt(19)\ns_barrier\nM v[112:115],v[212:215],a[184:187],v[112:115]\nds_read_b128 a[192:195],v228 offset:0\nds_read_b64_tr_b16 v[192:193],v241 offset:32768\n" \
"ds_read_b64_tr_b16 v[194:195],v241 offset:33024\nM v[116:119],v[208:211],a[172:175],v[116:119]\nds_read_b128 a[208:211],v229 offset:0\nds_read_b64_tr_b16 v[196:197],v241 offset:40960\nds_read_b64_tr_b16 v[198:199],v241 offset:41216\nM v[116:119],v[212:215],a[188:191],v[116:119]\nds_read_b128 a[196:199],v228 offset:2048\nM v[56:59],v[216:219],a[160:163],v[56:59]\n" \
"ds_read_b128 a[212:215],v229 offset:2048\nM v[56:59],v[220:223],a[176:179],v[56:59]\nds_read_b128 a[200:203],v228 offset:4096\nM v[60:63],v[216:219],a[164:167],v[60:63]\nds_read_b128 a[216:219],v229 offset:4096\nM v[60:63],v[220:223],a[180:183],v[60:63]\nds_read_b128 a[204:207],v228 offset:6144\nM v[120:123],v[216:219],a[168:171],v[120:123]\n" \
"ds_read_b128 a[220:223],v229 offset:6144\nM v[120:123],v[220:223],a[184:187],v[120:123]\nds_read_b64_tr_b16 v[200:201],v242 offset:32768\nds_read_b64_tr_b16 v[202:203],v242 offset:33024\nM v[124:127],v[216:219],a[172:175],v[124:127]\nds_read_b64_tr_b16 v[204:205],v242 offset:40960\nds_read_b64_tr_b16 v[206:207],v242 offset:41216\nM v[124:127],v[220:223],a[188:191],v[124:127]\n" \
"s_waitcnt vmcnt(14)\nv_add_f32 v152,v152,v156\ns_waitcnt lgkmcnt(13)\nM v[0:3],v[192:195],a[192:195],v[0:3]\nds_read_b64_tr_b16 v[208:209],v241 offset:33280\ns_waitcnt lgkmcnt(11)\nM v[0:3],v[196:199],a[208:211],v[0:3]\nds_read_b64_tr_b16 v[210:211],v241 offset:33536\n" \
"s_waitcnt lgkmcnt(11)\nM v[4:7],v[192:195],a[196:199],v[4:7]\nds_read_b64_tr_b16 v[212:213],v241 offset:41472\ns_waitcnt lgkmcnt(11)\nM v[4:7],v[196:199],a[212:215],v[4:7]\nds_read_b64_tr_b16 v[214:215],v241 offset:41728\ns_waitcnt lgkmcnt(11)\nM v[64:67],v[192:195],a[200:203],v[64:67]\n" \
"ds_read_b64_tr_b16 v[216:217],v242 offset:33280\ns_waitcnt lgkmcnt(11)\nM v[64:67],v[196:199],a[216:219],v[64:67]\nds_read_b64_tr_b16 v[218:219],v242 offset:33536\ns_waitcnt lgkmcnt(11)\nM v[68:71],v[192:195],a[204:207],v[68:71]\nds_read_b64_tr_b16 v[220:221],v242 offset:41472\ns_waitcnt lgkmcnt(11)\n" \
"M v[68:71],v[196:199],a[220:223],v[68:71]\nds_read_b64_tr_b16 v[222:223],v242 offset:41728\ns_waitcnt lgkmcnt(10)\nM v[8:11],v[200:203],a[192:195],v[8:11]\nds_read_b64_tr_b16 v[192:193],v241 offset:33792\ns_waitcnt lgkmcnt(9)\nM v[8:11],v[204:207],a[208:211],v[8:11]\nds_read_b64_tr_b16 v[194:195],v241 offset:34048\n" \
"ds_read_b64_tr_b16 v[196:197],v241 offset:41984\nM v[12:15],v[200:203],a[196:199],v[12:15]\nds_read_b64_tr_b16 v[198:199],v241 offset:42240\ns_waitcnt vmcnt(11)\nv_add_f32 v160,v160,v164\nM v[12:15],v[204:207],a[212:215],v[12:15]\nv_add_f32 v153,v153,v157\nv_add_f32 v161,v161,v165\n" \
"M v[72:75],v[200:203],a[200:203],v[72:75]\nv_add_f32 v154,v154,v158\nv_add_f32 v162,v162,v166\nM v[72:75],v[204:207],a[216:219],v[72:75]\nv_add_f32 v155,v155,v159\nv_add_f32 v163,v163,v167\nM v[76:79],v[200:203],a[204:207],v[76:79]\nds_read_b64_tr_b16 v[200:201],v242 offset:33792\n" \
"ds_read_b64_tr_b16 v[202:203],v242 offset:34048\nM v[76:79],v[204:207],a[220:223],v[76:79]\nds_read_b64_tr_b16 v[204:205],v242 offset:41984\ns_waitcnt lgkmcnt(9)\nM v[16:19],v[208:211],a[192:195],v[16:19]\nds_read_b64_tr_b16 v[206:207],v242 offset:42240\nv_add_f32 v152,v152,v160\nM v[16:19],v[212:215],a[208:211],v[16:19]\n" \
"buffer_load_dword v168,v169,s[52:55],s71 offen sc1\nM v[20:23],v[208:211],a[196:199],v[20:23]\nglobal_load_dword v247,v253,s[12:13]\nM v[20:23],v[212:215],a[212:215],v[20:23]\nv_add_f32 v153,v153,v161\nv_add_f32 v154,v154,v162\nM v[80:83],v[208:211],a[200:203],v[80:83]\nv_add_f32 v155,v155,v163\n" \
"v_cvt_pk_bf16_f32 v152,0,v152\nM v[80:83],v[212:215],a[216:219],v[80:83]\nv_cvt_pk_bf16_f32 v153,0,v153\nv_cvt_pk_bf16_f32 v154,0,v154\nM v[84:87],v[208:211],a[204:207],v[84:87]\nds_read_b64_tr_b16 v[208:209],v241 offset:34304\nds_read_b64_tr_b16 v[210:211],v241 offset:34560\nM v[84:87],v[212:215],a[220:223],v[84:87]\n" \
"ds_read_b64_tr_b16 v[212:213],v241 offset:42496\nds_read_b64_tr_b16 v[214:215],v241 offset:42752\nM v[24:27],v[216:219],a[192:195],v[24:27]\nv_cvt_pk_bf16_f32 v155,0,v155\ns_waitcnt lgkmcnt(7)\nM v[24:27],v[220:223],a[208:211],v[24:27]\nv_add_f32 v152,s70,v152\nv_add_f32 v153,s70,v153\n" \
"M v[28:31],v[216:219],a[196:199],v[28:31]\nv_add_f32 v154,s70,v154\nv_add_f32 v155,s70,v155\nM v[28:31],v[220:223],a[212:215],v[28:31]\ns_lshr_b32 s71,s64,2\nv_cvt_pk_bf16_f32 v152,0,v152\nM v[88:91],v[216:219],a[200:203],v[88:91]\nv_cvt_pk_bf16_f32 v153,0,v153\n" \
"v_cvt_pk_bf16_f32 v154,0,v154\nM v[88:91],v[220:223],a[216:219],v[88:91]\nv_cvt_pk_bf16_f32 v155,0,v155\nv_add_u32 v171,s71,v240\nM v[92:95],v[216:219],a[204:207],v[92:95]\nds_write_b128 v171,v[152:155] offset:16384\nM v[92:95],v[220:223],a[220:223],v[92:95]\nds_read_b64_tr_b16 v[216:217],v242 offset:34304\n" \
"ds_read_b64_tr_b16 v[218:219],v242 offset:34560\nM v[32:35],v[192:195],a[192:195],v[32:35]\nds_read_b64_tr_b16 v[220:221],v242 offset:42496\nds_read_b64_tr_b16 v[222:223],v242 offset:42752\nM v[32:35],v[196:199],a[208:211],v[32:35]\nv_add_u32 v253,0x100000,v253\ns_lshl_b32 s70,s7,8\nM v[36:39],v[192:195],a[196:199],v[36:39]\n" \
"buffer_load_dwordx4 v243,s[40:43],s69 offen offset:0 lds\nM v[36:39],v[196:199],a[212:215],v[36:39]\nv_lshl_add_u32 v251,v251,5,s70\ns_lshl_b32 s71,s6,5\nM v[96:99],v[192:195],a[200:203],v[96:99]\ns_and_b32 s71,s71,0xfffffe00\ns_xor_b32 s70,s63,16\nM v[96:99],v[196:199],a[216:219],v[96:99]\n" \
"s_lshr_b32 s70,s70,2\ns_lshl3_add_u32 s70,s62,s70\nM v[100:103],v[192:195],a[204:207],v[100:103]\nbuffer_load_dwordx4 v244,s[40:43],s69 offen offset:1024 lds\nM v[100:103],v[196:199],a[220:223],v[100:103]\ns_lshl_b32 s70,s70,15\ns_waitcnt lgkmcnt(5)\nM v[40:43],v[200:203],a[192:195],v[40:43]\n" \
"s_add_u32 s70,s70,s64\nM v[40:43],v[204:207],a[208:211],v[40:43]\nM v[44:47],v[200:203],a[196:199],v[44:47]\nbuffer_load_dwordx4 v245,s[40:43],s69 offen offset:2048 lds\nM v[44:47],v[204:207],a[212:215],v[44:47]\nM v[104:107],v[200:203],a[200:203],v[104:107]\nM v[104:107],v[204:207],a[216:219],v[104:107]\nM v[108:111],v[200:203],a[204:207],v[108:111]\n" \
"buffer_load_dwordx4 v246,s[40:43],s69 offen offset:3072 lds\nM v[108:111],v[204:207],a[220:223],v[108:111]\ns_add_u32 m0,s64,114688\ns_add_u32 s69,s68,524288\nM v[48:51],v[208:211],a[192:195],v[48:51]\nM v[48:51],v[212:215],a[208:211],v[48:51]\nM v[52:55],v[208:211],a[196:199],v[52:55]\nbuffer_store_dwordx2 v[232:233],off,s[52:55],s72 sc1\n" \
"M v[52:55],v[212:215],a[212:215],v[52:55]\ns_and_b32 s72,s6,1\ns_lshl_b32 s72,s72,8\nM v[112:115],v[208:211],a[200:203],v[112:115]\ns_waitcnt vmcnt(7) lgkmcnt(0)\ns_barrier\nM v[112:115],v[212:215],a[216:219],v[112:115]\nds_read_b128 a[224:227],v228 offset:8192\n" \
"ds_read_b64_tr_b16 v[192:193],v241 offset:49152\nds_read_b64_tr_b16 v[194:195],v241 offset:49408\nM v[116:119],v[208:211],a[204:207],v[116:119]\nds_read_b128 a[240:243],v229 offset:8192\nds_read_b64_tr_b16 v[196:197],v241 offset:57344\nds_read_b64_tr_b16 v[198:199],v241 offset:57600\nM v[116:119],v[212:215],a[220:223],v[116:119]\nds_read_b128 a[228:231],v228 offset:10240\n" \
"s_add_u32 s71,s71,s72\nM v[56:59],v[216:219],a[192:195],v[56:59]\nds_read_b128 a[244:247],v229 offset:10240\ns_and_b32 s72,s6,14\nM v[56:59],v[220:223],a[208:211],v[56:59]\nds_read_b128 a[232:235],v228 offset:12288\ns_lshl1_add_u32 s71,s72,s71\nM v[60:63],v[216:219],a[196:199],v[60:63]\n" \
"ds_read_b128 a[248:251],v229 offset:12288\ns_or_b32 s71,s71,s75\nM v[60:63],v[220:223],a[212:215],v[60:63]\nds_read_b128 a[236:239],v228 offset:14336\ns_mov_b32 s72,0x3e0293ee\nM v[120:123],v[216:219],a[200:203],v[120:123]\nds_read_b128 a[252:255],v229 offset:14336\nv_and_b32 v228,15,v249\n" \
"M v[120:123],v[220:223],a[216:219],v[120:123]\nds_read_b64_tr_b16 v[200:201],v242 offset:49152\nds_read_b64_tr_b16 v[202:203],v242 offset:49408\nM v[124:127],v[216:219],a[204:207],v[124:127]\nglobal_load_dword v247,v253,s[12:13]\nds_read_b64_tr_b16 v[204:205],v242 offset:57344\nds_read_b64_tr_b16 v[206:207],v242 offset:57600\nM v[124:127],v[220:223],a[220:223],v[124:127]\n" \
"v_and_b32 v229,16,v249\ns_waitcnt lgkmcnt(13)\nM v[0:3],v[192:195],a[224:227],v[0:3]\nds_read_b64_tr_b16 v[208:209],v241 offset:49664\ns_waitcnt lgkmcnt(11)\nM v[0:3],v[196:199],a[240:243],v[0:3]\nds_read_b64_tr_b16 v[210:211],v241 offset:49920\ns_waitcnt lgkmcnt(11)\n" \
"M v[4:7],v[192:195],a[228:231],v[4:7]\nds_read_b64_tr_b16 v[212:213],v241 offset:57856\ns_waitcnt lgkmcnt(11)\nM v[4:7],v[196:199],a[244:247],v[4:7]\nds_read_b64_tr_b16 v[214:215],v241 offset:58112\ns_waitcnt lgkmcnt(11)\nM v[64:67],v[192:195],a[232:235],v[64:67]\nds_read_b64_tr_b16 v[216:217],v242 offset:49664\n" \
"s_waitcnt lgkmcnt(11)\nM v[64:67],v[196:199],a[248:251],v[64:67]\nds_read_b64_tr_b16 v[218:219],v242 offset:49920\ns_waitcnt lgkmcnt(11)\nM v[68:71],v[192:195],a[236:239],v[68:71]\nds_read_b64_tr_b16 v[220:221],v242 offset:57856\ns_waitcnt lgkmcnt(11)\nM v[68:71],v[196:199],a[252:255],v[68:71]\n" \
"ds_read_b64_tr_b16 v[222:223],v242 offset:58112\ns_waitcnt lgkmcnt(10)\nM v[8:11],v[200:203],a[224:227],v[8:11]\nds_read_b64_tr_b16 v[192:193],v241 offset:50176\ns_waitcnt lgkmcnt(9)\nM v[8:11],v[204:207],a[240:243],v[8:11]\nds_read_b64_tr_b16 v[194:195],v241 offset:50432\nds_read_b64_tr_b16 v[196:197],v241 offset:58368\n" \
"M v[12:15],v[200:203],a[228:231],v[12:15]\nds_read_b64_tr_b16 v[198:199],v241 offset:58624\nds_read_b128 v[176:179],v251 offset:19456\nM v[12:15],v[204:207],a[244:247],v[12:15]\nds_read_b128 v[160:163],v251 offset:17408\nv_cvt_pk_bf16_f32 v128,v0,v1\nM v[72:75],v[200:203],a[232:235],v[72:75]\nds_read_b128 v[172:175],v251 offset:18448\n" \
"v_cvt_pk_bf16_f32 v129,v2,v3\nM v[72:75],v[204:207],a[248:251],v[72:75]\nds_read_b128 v[152:155],v251 offset:16384\nv_cvt_pk_bf16_f32 v130,v8,v9\nM v[76:79],v[200:203],a[236:239],v[76:79]\nds_read_b64_tr_b16 v[200:201],v242 offset:50176\nds_read_b64_tr_b16 v[202:203],v242 offset:50432\nM v[76:79],v[204:207],a[252:255],v[76:79]\n" \
"ds_read_b64_tr_b16 v[204:205],v242 offset:58368\ns_waitcnt lgkmcnt(13)\nM v[16:19],v[208:211],a[224:227],v[16:19]\nds_read_b64_tr_b16 v[206:207],v242 offset:58624\nds_read_b128 v[156:159],v251 offset:16400\nM v[16:19],v[212:215],a[240:243],v[16:19]\nds_read_b128 v[180:183],v251 offset:19472\nv_cvt_pk_bf16_f32 v131,v10,v11\n" \
"M v[20:23],v[208:211],a[228:231],v[20:23]\nds_read_b128 v[164:167],v251 offset:17424\nv_lshlrev_b32 v228,2,v228\nM v[20:23],v[212:215],a[244:247],v[20:23]\nbuffer_load_dwordx4 v243,s[40:43],s69 offen offset:0 lds\nM v[80:83],v[208:211],a[232:235],v[80:83]\ns_waitcnt lgkmcnt(10)\nv_mul_f32 v144,v144,v176\n" \
"M v[80:83],v[212:215],a[248:251],v[80:83]\nv_mul_f32 v145,v145,v177\nv_cvt_pk_bf16_f32 v144,0,v144\nM v[84:87],v[208:211],a[236:239],v[84:87]\nds_read_b64_tr_b16 v[208:209],v241 offset:50688\nds_read_b64_tr_b16 v[210:211],v241 offset:50944\nM v[84:87],v[212:215],a[252:255],v[84:87]\nds_read_b64_tr_b16 v[212:213],v241 offset:58880\n" \
"ds_read_b64_tr_b16 v[214:215],v241 offset:59136\nM v[24:27],v[216:219],a[224:227],v[24:27]\ns_waitcnt vmcnt(7)\nv_add_f32_dpp v168,v168,v168 quad_perm:[1,0,3,2] row_mask:0xf bank_mask:0xf\nM v[24:27],v[220:223],a[240:243],v[24:27]\nv_cvt_pk_bf16_f32 v145,0,v145\nv_add_f32_dpp v168,v168,v168 quad_perm:[2,3,0,1] row_mask:0xf bank_mask:0xf\nM v[28:31],v[216:219],a[228:231],v[28:31]\n" \
"v_cvt_pk_bf16_f32 v168,0,v168\ns_waitcnt lgkmcnt(7)\nv_add_f32 v144,v144,v160\nM v[28:31],v[220:223],a[244:247],v[28:31]\nbuffer_store_dword v168,v170,s[52:55],s71 offen sc1\nM v[88:91],v[216:219],a[232:235],v[88:91]\nds_read_b128 v[168:171],v251 offset:18432\nv_add_f32 v145,v145,v161\n" \
"M v[88:91],v[220:223],a[248:251],v[88:91]\nv_cvt_pk_bf16_f32 v144,0,v144\nv_cvt_pk_bf16_f32 v145,0,v145\nM v[92:95],v[216:219],a[236:239],v[92:95]\nds_read_b64_tr_b16 v[216:217],v242 offset:50688\nds_read_b64_tr_b16 v[218:219],v242 offset:50944\nM v[92:95],v[220:223],a[252:255],v[92:95]\nds_read_b64_tr_b16 v[220:221],v242 offset:58880\n" \
"ds_read_b64_tr_b16 v[222:223],v242 offset:59136\nM v[32:35],v[192:195],a[224:227],v[32:35]\nv_mul_f32 v144,s72,v144\nv_mul_f32 v145,s72,v145\nM v[32:35],v[196:199],a[240:243],v[32:35]\nv_cvt_pk_bf16_f32 v236,v144,v145\nv_cvt_pk_bf16_f32 v144,v4,v5\nM v[36:39],v[192:195],a[228:231],v[36:39]\n" \
"v_cvt_pk_bf16_f32 v145,v6,v7\nv_cvt_pk_bf16_f32 v160,v64,v65\nM v[36:39],v[196:199],a[244:247],v[36:39]\nv_cvt_pk_bf16_f32 v161,v66,v67\ns_and_b32 s71,s63,2\nM v[96:99],v[192:195],a[232:235],v[96:99]\nv_mul_f32 v140,v140,v172\nv_mul_f32 v141,v141,v173\n" \
"M v[96:99],v[196:199],a[248:251],v[96:99]\nv_mul_f32 v142,v142,v174\nv_mul_f32 v143,v143,v175\nM v[100:103],v[192:195],a[236:239],v[100:103]\ns_waitcnt lgkmcnt(4)\nv_mul_f32 v136,v136,v168\nM v[100:103],v[196:199],a[252:255],v[100:103]\nv_mul_f32 v137,v137,v169\n" \
"v_mul_f32 v138,v138,v170\nM v[40:43],v[200:203],a[224:227],v[40:43]\nv_mul_f32 v139,v139,v171\ns_lshl_b32 s71,s71,9\nM v[40:43],v[204:207],a[240:243],v[40:43]\nv_cvt_pk_bf16_f32 v176,v68,v69\nv_cvt_pk_bf16_f32 v177,v70,v71\nM v[44:47],v[200:203],a[228:231],v[44:47]\n" \
"v_cvt_pk_bf16_f32 v136,0,v136\nv_cvt_pk_bf16_f32 v137,0,v137\nM v[44:47],v[204:207],a[244:247],v[44:47]\nv_cvt_pk_bf16_f32 v138,0,v138\nv_cvt_pk_bf16_f32 v139,0,v139\nM v[104:107],v[200:203],a[232:235],v[104:107]\nv_cvt_pk_bf16_f32 v140,0,v140\nv_cvt_pk_bf16_f32 v141,0,v141\n" \
"M v[104:107],v[204:207],a[248:251],v[104:107]\nv_cvt_pk_bf16_f32 v142,0,v142\nv_cvt_pk_bf16_f32 v143,0,v143\nM v[108:111],v[200:203],a[236:239],v[108:111]\nv_lshrrev_b32 v251,5,v249\ns_add_u32 s70,s70,s71\nM v[108:111],v[204:207],a[252:255],v[108:111]\nv_add_f32 v136,v136,v152\n" \
"v_add_f32 v137,v137,v153\nM v[48:51],v[208:211],a[224:227],v[48:51]\nv_add_f32 v138,v138,v154\nv_add_f32 v139,v139,v155\nM v[48:51],v[212:215],a[240:243],v[48:51]\nv_add_f32 v140,v140,v156\nv_add_f32 v141,v141,v157\nM v[52:55],v[208:211],a[228:231],v[52:55]\n" \
"v_add_f32 v142,v142,v158\nv_add_f32 v143,v143,v159\nM v[52:55],v[212:215],a[244:247],v[52:55]\nv_mul_f32 v146,v146,v178\nv_mul_f32 v147,v147,v179\nM v[112:115],v[208:211],a[232:235],v[112:115]\ns_waitcnt vmcnt(4) lgkmcnt(0)\ns_barrier\n" \
"M v[112:115],v[212:215],a[248:251],v[112:115]\nds_read_b64_tr_b16 v[192:193],v241 offset:0\nds_read_b64_tr_b16 v[194:195],v241 offset:256\nM v[116:119],v[208:211],a[236:239],v[116:119]\nds_read_b64_tr_b16 v[196:197],v241 offset:8192\nds_read_b64_tr_b16 v[198:199],v241 offset:8448\nM v[116:119],v[212:215],a[252:255],v[116:119]\nds_read_b64_tr_b16 v[200:201],v242 offset:0\n" \
"ds_read_b64_tr_b16 v[202:203],v242 offset:256\nM v[56:59],v[216:219],a[224:227],v[56:59]\nds_read_b64_tr_b16 v[204:205],v242 offset:8192\nds_read_b64_tr_b16 v[206:207],v242 offset:8448\nM v[56:59],v[220:223],a[240:243],v[56:59]\nds_read_b64_tr_b16 v[208:209],v241 offset:512\nds_read_b64_tr_b16 v[210:211],v241 offset:768\nM v[60:63],v[216:219],a[228:231],v[60:63]\n" \
"v_lshlrev_b32 v251,11,v251\ns_and_b32 s71,s63,1\nM v[60:63],v[220:223],a[244:247],v[60:63]\nds_read_b64_tr_b16 v[212:213],v241 offset:8704\nds_read_b64_tr_b16 v[214:215],v241 offset:8960\nM v[120:123],v[216:219],a[232:235],v[120:123]\nv_cvt_pk_bf16_f32 v136,0,v136\nv_cvt_pk_bf16_f32 v137,0,v137\n" \
"M v[120:123],v[220:223],a[248:251],v[120:123]\nv_cvt_pk_bf16_f32 v138,0,v138\nv_cvt_pk_bf16_f32 v139,0,v139\nM v[124:127],v[216:219],a[236:239],v[124:127]\nv_cvt_pk_bf16_f32 v140,0,v140\nv_cvt_pk_bf16_f32 v141,0,v141\nM v[124:127],v[220:223],a[252:255],v[124:127]\nv_cvt_pk_bf16_f32 v142,0,v142\n" \
"s_waitcnt lgkmcnt(10)\nM v[0:3],a[0:3],v[192:195],0\nv_cvt_pk_bf16_f32 v143,0,v143\ns_waitcnt lgkmcnt(8)\nM v[0:3],a[16:19],v[196:199],v[0:3]\nv_cvt_pk_bf16_f32 v146,0,v146\nv_cvt_pk_bf16_f32 v147,0,v147\nM v[4:7],a[4:7],v[192:195],0\n" \
"v_lshl_or_b32 v251,v250,8,v251\ns_lshl_b32 s71,s71,7\nM v[4:7],a[20:23],v[196:199],v[4:7]\nv_mul_f32 v136,s72,v136\nv_mul_f32 v137,s72,v137\nM v[64:67],a[8:11],v[192:195],0\nv_mul_f32 v138,s72,v138\nv_mul_f32 v139,s72,v139\n" \
"M v[64:67],a[24:27],v[196:199],v[64:67]\nv_mul_f32 v140,s72,v140\nv_mul_f32 v141,s72,v141\nM v[68:71],a[12:15],v[192:195],0\nv_mul_f32 v142,s72,v142\nv_mul_f32 v143,s72,v143\nM v[68:71],a[28:31],v[196:199],v[68:71]\nv_add_f32 v146,v146,v162\n" \
"v_add_f32 v147,v147,v163\nv_and_b32 v250,0x70,v240\ns_add_u32 s70,s70,s71\ns_waitcnt lgkmcnt(0)\nM v[8:11],a[0:3],v[200:203],0\nv_cvt_pk_bf16_f32 v232,v136,v137\nv_cvt_pk_bf16_f32 v233,v138,v139\nv_cvt_pk_bf16_f32 v234,v140,v141\n" \
"v_cvt_pk_bf16_f32 v235,v142,v143\nv_cvt_pk_bf16_f32 v146,0,v146\nv_cvt_pk_bf16_f32 v147,0,v147\nv_or_b32 v251,v251,v250\ns_add_u32 s70,s70,1048576\nv_mul_f32 v146,s72,v146\nv_mul_f32 v147,s72,v147\nbuffer_store_dwordx4 v[232:235],v251,s[52:55],s70 offen sc1\n" \
"M v[8:11],a[16:19],v[204:207],v[8:11]\nv_cvt_pk_bf16_f32 v237,v146,v147\nv_mul_f32 v148,v148,v180\nv_mul_f32 v149,v149,v181\nv_mul_f32 v150,v150,v182\nv_mul_f32 v151,v151,v183\nv_cvt_pk_bf16_f32 v146,v12,v13\nv_cvt_pk_bf16_f32 v147,v14,v15\n" \
"M v[12:15],a[4:7],v[200:203],0\nv_cvt_pk_bf16_f32 v148,0,v148\nv_cvt_pk_bf16_f32 v149,0,v149\nv_cvt_pk_bf16_f32 v150,0,v150\nv_cvt_pk_bf16_f32 v151,0,v151\nv_add_f32 v148,v148,v164\nv_add_f32 v149,v149,v165\nv_add_f32 v150,v150,v166\n" \
"v_add_f32 v151,v151,v167\nM v[12:15],a[20:23],v[204:207],v[12:15]\nv_cvt_pk_bf16_f32 v148,0,v148\nv_cvt_pk_bf16_f32 v149,0,v149\nv_cvt_pk_bf16_f32 v150,0,v150\nv_cvt_pk_bf16_f32 v151,0,v151\nv_mul_f32 v148,s72,v148\nv_mul_f32 v149,s72,v149\n" \
"v_mul_f32 v150,s72,v150\nv_mul_f32 v151,s72,v151\nv_cvt_pk_bf16_f32 v162,v72,v73\nv_cvt_pk_bf16_f32 v163,v74,v75\nM v[72:75],a[8:11],v[200:203],0\nv_cvt_pk_bf16_f32 v238,v148,v149\nv_cvt_pk_bf16_f32 v239,v150,v151\ns_add_u32 s71,s70,16384\n" \
"buffer_load_dwordx4 v244,s[40:43],s69 offen offset:1024 lds\nbuffer_load_dwordx4 v245,s[40:43],s69 offen offset:2048 lds\nbuffer_load_dwordx4 v246,s[40:43],s69 offen offset:3072 lds\nbuffer_store_dwordx4 v[236:239],v251,s[52:55],s71 offen sc1\nM v[72:75],a[24:27],v[204:207],v[72:75]\nv_cvt_pk_bf16_f32 v178,v76,v77\nv_cvt_pk_bf16_f32 v179,v78,v79\nds_read_b64_tr_b16 v[216:217],v242 offset:512\n" \
"ds_read_b64_tr_b16 v[218:219],v242 offset:768\nM v[76:79],a[12:15],v[200:203],0\nds_read_b64_tr_b16 v[220:221],v242 offset:8704\nds_read_b64_tr_b16 v[222:223],v242 offset:8960\nM v[76:79],a[28:31],v[204:207],v[76:79]\nv_cvt_pk_bf16_f32 v132,v16,v17\nv_cvt_pk_bf16_f32 v133,v18,v19\nM v[16:19],a[0:3],v[208:211],0\n" \
"v_cvt_pk_bf16_f32 v148,v20,v21\nv_cvt_pk_bf16_f32 v149,v22,v23\nM v[16:19],a[16:19],v[212:215],v[16:19]\nv_cvt_pk_bf16_f32 v164,v80,v81\nv_cvt_pk_bf16_f32 v165,v82,v83\nM v[20:23],a[4:7],v[208:211],0\nv_cvt_pk_bf16_f32 v180,v84,v85\nv_cvt_pk_bf16_f32 v181,v86,v87\n" \
"M v[20:23],a[20:23],v[212:215],v[20:23]\nds_read_b64_tr_b16 v[192:193],v241 offset:1024\nds_read_b64_tr_b16 v[194:195],v241 offset:1280\nM v[80:83],a[8:11],v[208:211],0\nds_read_b64_tr_b16 v[196:197],v241 offset:9216\nds_read_b64_tr_b16 v[198:199],v241 offset:9472\nM v[80:83],a[24:27],v[212:215],v[80:83]\nv_cvt_pk_bf16_f32 v134,v24,v25\n" \
"v_cvt_pk_bf16_f32 v135,v26,v27\nM v[84:87],a[12:15],v[208:211],0\nv_cvt_pk_bf16_f32 v150,v28,v29\nv_cvt_pk_bf16_f32 v151,v30,v31\nM v[84:87],a[28:31],v[212:215],v[84:87]\nv_cvt_pk_bf16_f32 v166,v88,v89\ns_waitcnt lgkmcnt(6)\nM v[24:27],a[0:3],v[216:219],0\n" \
"v_cvt_pk_bf16_f32 v167,v90,v91\ns_waitcnt lgkmcnt(4)\nM v[24:27],a[16:19],v[220:223],v[24:27]\nv_cvt_pk_bf16_f32 v182,v92,v93\nv_cvt_pk_bf16_f32 v183,v94,v95\nM v[28:31],a[4:7],v[216:219],0\nds_read_b64_tr_b16 v[200:201],v242 offset:1024\nds_read_b64_tr_b16 v[202:203],v242 offset:1280\n" \
"M v[28:31],a[20:23],v[220:223],v[28:31]\nds_read_b64_tr_b16 v[204:205],v242 offset:9216\nds_read_b64_tr_b16 v[206:207],v242 offset:9472\nM v[88:91],a[8:11],v[216:219],0\nv_cvt_pk_bf16_f32 v136,v32,v33\nv_cvt_pk_bf16_f32 v137,v34,v35\nM v[88:91],a[24:27],v[220:223],v[88:91]\nv_cvt_pk_bf16_f32 v152,v36,v37\n" \
"v_cvt_pk_bf16_f32 v153,v38,v39\nM v[92:95],a[12:15],v[216:219],0\nv_cvt_pk_bf16_f32 v168,v96,v97\nv_cvt_pk_bf16_f32 v169,v98,v99\nM v[92:95],a[28:31],v[220:223],v[92:95]\ns_add_u32 m0,s64,131072\ns_waitcnt lgkmcnt(4)\nM v[32:35],a[0:3],v[192:195],0\n" \
"s_add_u32 s69,s68,1048576\nv_cvt_pk_bf16_f32 v184,v100,v101\nM v[32:35],a[16:19],v[196:199],v[32:35]\nv_cvt_pk_bf16_f32 v185,v102,v103\nds_read_b64_tr_b16 v[208:209],v241 offset:1536\nM v[36:39],a[4:7],v[192:195],0\nds_read_b64_tr_b16 v[210:211],v241 offset:1792\nds_read_b64_tr_b16 v[212:213],v241 offset:9728\n" \
"M v[36:39],a[20:23],v[196:199],v[36:39]\nds_read_b64_tr_b16 v[214:215],v241 offset:9984\nv_cvt_pk_bf16_f32 v138,v40,v41\nM v[96:99],a[8:11],v[192:195],0\nv_cvt_pk_bf16_f32 v139,v42,v43\nv_cvt_pk_bf16_f32 v154,v44,v45\nM v[96:99],a[24:27],v[196:199],v[96:99]\nv_cvt_pk_bf16_f32 v155,v46,v47\n" \
"ds_read_b64_tr_b16 v[216:217],v242 offset:1536\nM v[100:103],a[12:15],v[192:195],0\nds_read_b64_tr_b16 v[218:219],v242 offset:1792\nds_read_b64_tr_b16 v[220:221],v242 offset:9728\nM v[100:103],a[28:31],v[196:199],v[100:103]\nds_read_b64_tr_b16 v[222:223],v242 offset:9984\ns_waitcnt lgkmcnt(8)\nM v[40:43],a[0:3],v[200:203],0\n" \
"v_cvt_pk_bf16_f32 v170,v104,v105\nv_cvt_pk_bf16_f32 v171,v106,v107\nM v[40:43],a[16:19],v[204:207],v[40:43]\nv_cvt_pk_bf16_f32 v186,v108,v109\nv_cvt_pk_bf16_f32 v187,v110,v111\nM v[44:47],a[4:7],v[200:203],0\nv_cvt_pk_bf16_f32 v140,v48,v49\nv_cvt_pk_bf16_f32 v141,v50,v51\n" \
"M v[44:47],a[20:23],v[204:207],v[44:47]\nv_cvt_pk_bf16_f32 v156,v52,v53\nv_cvt_pk_bf16_f32 v157,v54,v55\nM v[104:107],a[8:11],v[200:203],0\nv_cvt_pk_bf16_f32 v172,v112,v113\nv_cvt_pk_bf16_f32 v173,v114,v115\nM v[104:107],a[24:27],v[204:207],v[104:107]\nv_cvt_pk_bf16_f32 v188,v116,v117\n" \
"v_cvt_pk_bf16_f32 v189,v118,v119\nM v[108:111],a[12:15],v[200:203],0\nv_cvt_pk_bf16_f32 v142,v56,v57\nv_cvt_pk_bf16_f32 v143,v58,v59\nM v[108:111],a[28:31],v[204:207],v[108:111]\nv_cvt_pk_bf16_f32 v158,v60,v61\ns_waitcnt lgkmcnt(4)\nM v[48:51],a[0:3],v[208:211],0\n" \
"v_cvt_pk_bf16_f32 v159,v62,v63\nv_cvt_pk_bf16_f32 v174,v120,v121\nM v[48:51],a[16:19],v[212:215],v[48:51]\nv_cvt_pk_bf16_f32 v175,v122,v123\nv_cvt_pk_bf16_f32 v190,v124,v125\nM v[52:55],a[4:7],v[208:211],0\nv_cvt_pk_bf16_f32 v191,v126,v127\ns_lshl_b32 s70,s62,5\n" \
"M v[52:55],a[20:23],v[212:215],v[52:55]\nbuffer_load_dwordx4 v243,s[40:43],s69 offen offset:0 lds\nM v[112:115],a[8:11],v[208:211],0\ns_waitcnt vmcnt(2) lgkmcnt(0)\ns_barrier\nM v[112:115],a[24:27],v[212:215],v[112:115]\nds_read_b64_tr_b16 v[192:193],v241 offset:16384\nds_read_b64_tr_b16 v[194:195],v241 offset:16640\n" \
"M v[116:119],a[12:15],v[208:211],0\nds_read_b64_tr_b16 v[196:197],v241 offset:24576\nds_read_b64_tr_b16 v[198:199],v241 offset:24832\nM v[116:119],a[28:31],v[212:215],v[116:119]\nds_read_b64_tr_b16 v[200:201],v242 offset:16384\nds_read_b64_tr_b16 v[202:203],v242 offset:16640\nM v[56:59],a[0:3],v[216:219],0\nds_read_b64_tr_b16 v[204:205],v242 offset:24576\n" \
"ds_read_b64_tr_b16 v[206:207],v242 offset:24832\nM v[56:59],a[16:19],v[220:223],v[56:59]\nds_read_b64_tr_b16 v[208:209],v241 offset:16896\nds_read_b64_tr_b16 v[210:211],v241 offset:17152\nM v[60:63],a[4:7],v[216:219],0\nds_read_b64_tr_b16 v[212:213],v241 offset:25088\nds_read_b64_tr_b16 v[214:215],v241 offset:25344\nM v[60:63],a[20:23],v[220:223],v[60:63]\n" \
"buffer_load_dwordx4 v244,s[40:43],s69 offen offset:1024 lds\nM v[120:123],a[8:11],v[216:219],0\ns_add_u32 s70,s70,s63\ns_lshl2_add_u32 s70,s70,s7\nM v[120:123],a[24:27],v[220:223],v[120:123]\ns_lshl2_add_u32 s79,s70,90112\ns_lshl_b32 s70,s60,4\nM v[124:127],a[12:15],v[216:219],0\n" \
"ds_read_b64_tr_b16 v[216:217],v242 offset:16896\nds_read_b64_tr_b16 v[218:219],v242 offset:17152\nM v[124:127],a[28:31],v[220:223],v[124:127]\ns_sub_u32 s70,s70,s62\ns_waitcnt lgkmcnt(12)\nM v[0:3],a[32:35],v[192:195],v[0:3]\nds_read_b64_tr_b16 v[220:221],v242 offset:25088\ns_waitcnt lgkmcnt(11)\n" \
"M v[0:3],a[48:51],v[196:199],v[0:3]\nbuffer_load_dwordx4 v245,s[40:43],s69 offen offset:2048 lds\nM v[4:7],a[36:39],v[192:195],v[4:7]\nds_read_b64_tr_b16 v[222:223],v242 offset:25344\ns_add_u32 s70,s70,15\nM v[4:7],a[52:55],v[196:199],v[4:7]\ns_lshl2_add_u32 s70,s70,s7\ns_lshl_b32 s70,s70,15\n" \
"M v[64:67],a[40:43],v[192:195],v[64:67]\ns_lshl_b32 s71,s61,8\ns_add_u32 s70,s70,s71\nM v[64:67],a[56:59],v[196:199],v[64:67]\nbuffer_load_dwordx4 v246,s[40:43],s69 offen offset:3072 lds\nM v[68:71],a[44:47],v[192:195],v[68:71]\ns_add_u32 s78,s70,54525952\ns_lshl3_add_u32 s70,s62,s7\n" \
"M v[68:71],a[60:63],v[196:199],v[68:71]\ns_lshl_b32 s80,s70,6\ns_waitcnt lgkmcnt(4)\nM v[8:11],a[32:35],v[200:203],v[8:11]\ns_lshl_b32 s76,s70,15\ns_lshl_b32 s70,s63,5\nM v[8:11],a[48:51],v[204:207],v[8:11]\ns_add_u32 s70,s70,s74\n" \
"s_lshl_b32 s74,s70,14\nM v[12:15],a[36:39],v[200:203],v[12:15]\ns_add_u32 s74,s74,3145728\nds_read_b64_tr_b16 v[192:193],v241 offset:17408\nM v[12:15],a[52:55],v[204:207],v[12:15]\nds_read_b64_tr_b16 v[194:195],v241 offset:17664\nds_read_b64_tr_b16 v[196:197],v241 offset:25600\nM v[72:75],a[40:43],v[200:203],v[72:75]\n" \
"ds_read_b64_tr_b16 v[198:199],v241 offset:25856\ns_add_u32 m0,s64,147456\nM v[72:75],a[56:59],v[204:207],v[72:75]\ns_add_u32 s69,s68,1572864\nv_lshl_or_b32 v228,v229,4,v228\nM v[76:79],a[44:47],v[200:203],v[76:79]\nds_read_b64_tr_b16 v[200:201],v242 offset:17408\nds_read_b64_tr_b16 v[202:203],v242 offset:17664\n" \
"M v[76:79],a[60:63],v[204:207],v[76:79]\nds_read_b64_tr_b16 v[204:205],v242 offset:25600\nds_read_b64_tr_b16 v[206:207],v242 offset:25856\nM v[16:19],a[32:35],v[208:211],v[16:19]\nbuffer_store_dwordx4 v[128:131],v240,s[52:55],s74 offen offset:0 sc1\nM v[16:19],a[48:51],v[212:215],v[16:19]\ns_add_u32 s80,s80,90112\ns_add_u32 s76,s76,1048576\n" \
"M v[20:23],a[36:39],v[208:211],v[20:23]\ns_add_u32 s77,s76,131072\ns_lshl2_add_u32 s29,s70,81920\nM v[20:23],a[52:55],v[212:215],v[20:23]\ns_lshl_b32 s70,s7,14\nv_add_u32 v250,s70,v240\nM v[80:83],a[40:43],v[208:211],v[80:83]\nbuffer_load_dwordx4 v243,s[40:43],s69 offen offset:0 lds\n" \
"M v[80:83],a[56:59],v[212:215],v[80:83]\nds_write_b64 v250,v[128:129] offset:0\ns_lshl_b32 s70,s63,19\nM v[84:87],a[44:47],v[208:211],v[84:87]\nds_read_b64_tr_b16 v[208:209],v241 offset:17920\nds_read_b64_tr_b16 v[210:211],v241 offset:18176\nM v[84:87],a[60:63],v[212:215],v[84:87]\nds_read_b64_tr_b16 v[212:213],v241 offset:26112\n" \
"s_waitcnt lgkmcnt(8)\nM v[24:27],a[32:35],v[216:219],v[24:27]\nds_read_b64_tr_b16 v[214:215],v241 offset:26368\ns_add_u32 s72,s78,16384\nM v[24:27],a[48:51],v[220:223],v[24:27]\nbuffer_load_dwordx4 v244,s[40:43],s69 offen offset:1024 lds\nM v[28:31],a[36:39],v[216:219],v[28:31]\nds_write_b64 v250,v[130:131] offset:8\n" \
"s_mul_i32 s71,s60,14\nM v[28:31],a[52:55],v[220:223],v[28:31]\nds_write_b64 v250,v[132:133] offset:1024\nv_and_b32 v229,15,v249\nM v[88:91],a[40:43],v[216:219],v[88:91]\ns_add_u32 s44,s34,s70\ns_addc_u32 s45,s35,0\nM v[88:91],a[56:59],v[220:223],v[88:91]\n" \
"buffer_load_dwordx4 v245,s[40:43],s69 offen offset:2048 lds\nM v[92:95],a[44:47],v[216:219],v[92:95]\nds_read_b64_tr_b16 v[216:217],v242 offset:17920\nds_read_b64_tr_b16 v[218:219],v242 offset:18176\nM v[92:95],a[60:63],v[220:223],v[92:95]\nds_read_b64_tr_b16 v[220:221],v242 offset:26112\nds_read_b64_tr_b16 v[222:223],v242 offset:26368\nM v[32:35],a[32:35],v[192:195],v[32:35]\n" \
"s_lshl_b32 s70,s62,15\ns_add_u32 s96,s70,s64\nM v[32:35],a[48:51],v[196:199],v[32:35]\nbuffer_load_dwordx4 v246,s[40:43],s69 offen offset:3072 lds\nM v[36:39],a[36:39],v[192:195],v[36:39]\ns_add_u32 m0,s64,98304\ns_add_u32 s69,s68,2097152\nM v[36:39],a[52:55],v[196:199],v[36:39]\n" \
"ds_write_b64 v250,v[134:135] offset:1032\ns_sub_u32 s96,s96,16384\nM v[96:99],a[40:43],v[192:195],v[96:99]\ns_cmp_eq_u32 s36,s96\ns_cselect_b32 s72,0xc000,s98\ns_add_u32 s65,s36,s72\nM v[96:99],a[56:59],v[196:199],v[96:99]\nbuffer_store_dwordx4 v[132:135],v240,s[52:55],s74 offen offset:1024 sc1\n" \
"M v[100:103],a[44:47],v[192:195],v[100:103]\ns_sub_u32 s46,491520,s70\ns_lshl2_add_u32 s70,s62,0\nM v[100:103],a[60:63],v[196:199],v[100:103]\ns_cmp_eq_u32 s65,s96\ns_cselect_b32 s72,0xc000,s98\ns_add_u32 s66,s65,s72\ns_waitcnt lgkmcnt(5)\n" \
"M v[40:43],a[32:35],v[200:203],v[40:43]\ns_sub_u32 s70,s70,s7\ns_lshl_b32 s70,s70,16\nM v[40:43],a[48:51],v[204:207],v[40:43]\nbuffer_load_dwordx4 v243,s[40:43],s69 offen offset:0 lds\nM v[44:47],a[36:39],v[200:203],v[44:47]\ns_cmp_eq_u32 s66,s96\ns_cselect_b32 s72,0xc000,s98\n" \
"s_add_u32 s95,s66,s72\nM v[44:47],a[52:55],v[204:207],v[44:47]\ns_add_u32 s33,s78,s70\ns_lshl3_add_u32 s70,s62,s7\nM v[104:107],a[40:43],v[200:203],v[104:107]\ns_cmp_eq_u32 s62,7\ns_cselect_b32 s46,229376,s46\nM v[104:107],a[56:59],v[204:207],v[104:107]\n" \
"buffer_load_dwordx4 v244,s[40:43],s69 offen offset:1024 lds\nM v[108:111],a[44:47],v[200:203],v[108:111]\nds_write_b64 v250,v[136:137] offset:2048\ns_lshl3_add_u32 s70,s70,s60\nM v[108:111],a[60:63],v[204:207],v[108:111]\ns_cmp_gt_i32 s62,-1\ns_cselect_b32 s72,s98,0xfffe4000\nM v[48:51],a[32:35],v[208:211],v[48:51]\n" \
"s_lshl_b32 s67,s70,15\ns_add_u32 s15,s64,s46\nM v[48:51],a[48:51],v[212:215],v[48:51]\ns_waitcnt vmcnt(12)\nbuffer_store_dword v252,off,s[52:55],s79 sc1\nM v[52:55],a[36:39],v[208:211],v[52:55]\nbuffer_load_dwordx4 v245,s[40:43],s69 offen offset:2048 lds\nM v[52:55],a[52:55],v[212:215],v[52:55]\n" \
"s_lshl3_add_u32 s70,s62,s60\ns_add_u32 s15,s15,s72\nM v[112:115],a[40:43],v[208:211],v[112:115]\ns_waitcnt vmcnt(10) lgkmcnt(1)\ns_barrier\nM v[112:115],a[56:59],v[212:215],v[112:115]\nds_read_b64_tr_b16 v[192:193],v241 offset:32768\nds_read_b64_tr_b16 v[194:195],v241 offset:33024\n" \
"M v[116:119],a[44:47],v[208:211],v[116:119]\nds_read_b64_tr_b16 v[196:197],v241 offset:40960\nds_read_b64_tr_b16 v[198:199],v241 offset:41216\nM v[116:119],a[60:63],v[212:215],v[116:119]\nbuffer_store_dwordx4 v[136:139],v240,s[52:55],s74 offen offset:2048 sc1\nM v[56:59],a[32:35],v[216:219],v[56:59]\nds_read_b64_tr_b16 v[200:201],v242 offset:32768\nds_read_b64_tr_b16 v[202:203],v242 offset:33024\n" \
"M v[56:59],a[48:51],v[220:223],v[56:59]\nds_read_b64_tr_b16 v[204:205],v242 offset:40960\nds_read_b64_tr_b16 v[206:207],v242 offset:41216\nM v[60:63],a[36:39],v[216:219],v[60:63]\nds_read_b64_tr_b16 v[208:209],v241 offset:33280\nds_read_b64_tr_b16 v[210:211],v241 offset:33536\nM v[60:63],a[52:55],v[220:223],v[60:63]\nds_read_b64_tr_b16 v[212:213],v241 offset:41472\n" \
"ds_read_b64_tr_b16 v[214:215],v241 offset:41728\nM v[120:123],a[40:43],v[216:219],v[120:123]\nbuffer_load_dwordx4 v246,s[40:43],s69 offen offset:3072 lds\nM v[120:123],a[56:59],v[220:223],v[120:123]\ns_add_u32 m0,s64,114688\ns_add_u32 s69,s68,2621440\nM v[124:127],a[44:47],v[216:219],v[124:127]\nds_read_b64_tr_b16 v[216:217],v242 offset:33280\n" \
"ds_read_b64_tr_b16 v[218:219],v242 offset:33536\nM v[124:127],a[60:63],v[220:223],v[124:127]\nds_read_b64_tr_b16 v[220:221],v242 offset:41472\ns_waitcnt lgkmcnt(13)\nM v[0:3],a[64:67],v[192:195],v[0:3]\nds_read_b64_tr_b16 v[222:223],v242 offset:41728\ns_waitcnt lgkmcnt(12)\nM v[0:3],a[80:83],v[196:199],v[0:3]\n" \
"buffer_load_dwordx4 v243,s[40:43],s69 offen offset:0 lds\nM v[4:7],a[68:71],v[192:195],v[4:7]\nds_write_b64 v250,v[138:139] offset:2056\ns_lshl2_add_u32 s70,s70,s7\nM v[4:7],a[84:87],v[196:199],v[4:7]\ns_cmp_eq_u32 s62,7\ns_cselect_b32 s72,98304,0\nM v[64:67],a[72:75],v[192:195],v[64:67]\n" \
"s_lshl4_add_u32 s79,s70,65536\ns_add_u32 s97,s96,65536\nM v[64:67],a[88:91],v[196:199],v[64:67]\nbuffer_load_dwordx4 v244,s[40:43],s69 offen offset:1024 lds\nM v[68:71],a[76:79],v[192:195],v[68:71]\nds_read_b64_tr_b16 v[192:193],v241 offset:33792\nds_read_b64_tr_b16 v[194:195],v241 offset:34048\nM v[68:71],a[92:95],v[196:199],v[68:71]\n" \
"ds_read_b64_tr_b16 v[196:197],v241 offset:41984\ns_waitcnt lgkmcnt(8)\nM v[8:11],a[64:67],v[200:203],v[8:11]\nds_read_b64_tr_b16 v[198:199],v241 offset:42240\ns_lshr_b32 s70,s6,3\nM v[8:11],a[80:83],v[204:207],v[8:11]\nbuffer_store_dwordx4 v[140:143],v240,s[52:55],s74 offen offset:3072 sc1\nM v[12:15],a[68:71],v[200:203],v[12:15]\n" \
"s_add_u32 s74,s74,4096\ns_sub_u32 s97,s97,s72\nM v[12:15],a[84:87],v[204:207],v[12:15]\nds_write_b64 v250,v[140:141] offset:3072\ns_sub_u32 s70,s70,16\nM v[72:75],a[72:75],v[200:203],v[72:75]\ns_add_u32 s2,s70,s71\ns_lshl_b32 s72,s60,1\n" \
"M v[72:75],a[88:91],v[204:207],v[72:75]\nbuffer_load_dwordx4 v245,s[40:43],s69 offen offset:2048 lds\nM v[76:79],a[76:79],v[200:203],v[76:79]\nds_read_b64_tr_b16 v[200:201],v242 offset:33792\nds_read_b64_tr_b16 v[202:203],v242 offset:34048\nM v[76:79],a[92:95],v[204:207],v[76:79]\nds_read_b64_tr_b16 v[204:205],v242 offset:41984\nds_read_b64_tr_b16 v[206:207],v242 offset:42240\n" \
"M v[16:19],a[64:67],v[208:211],v[16:19]\nv_lshlrev_b32 v229,7,v229\ns_mul_i32 s71,s60,448\nM v[16:19],a[80:83],v[212:215],v[16:19]\nbuffer_load_dwordx4 v246,s[40:43],s69 offen offset:3072 lds\nM v[20:23],a[68:71],v[208:211],v[20:23]\nds_write_b64 v250,v[142:143] offset:3080\ns_add_u32 s72,s72,s62\n" \
"M v[20:23],a[84:87],v[212:215],v[20:23]\ns_and_b32 s71,s71,0xffffff80\ns_sub_u32 s72,s72,6\nM v[80:83],a[72:75],v[208:211],v[80:83]\ns_sub_u32 s58,1835008,s71\ns_lshl_b32 s72,s72,18\nM v[80:83],a[88:91],v[212:215],v[80:83]\ns_cmp_ge_u32 s62,6\n" \
"s_cselect_b64 s[56:57],s[18:19],s[56:57]\ns_cselect_b32 s71,s72,s71\ns_cselect_b32 s58,0x40000,s58\ns_cselect_b32 s0,2048,s0\nM v[84:87],a[76:79],v[208:211],v[84:87]\nds_read_b64_tr_b16 v[208:209],v241 offset:34304\nds_read_b64_tr_b16 v[210:211],v241 offset:34560\nM v[84:87],a[92:95],v[212:215],v[84:87]\n" \
"ds_read_b64_tr_b16 v[212:213],v241 offset:42496\ns_waitcnt lgkmcnt(8)\nM v[24:27],a[64:67],v[216:219],v[24:27]\nbuffer_store_dwordx4 v[144:147],v240,s[52:55],s74 offen offset:0 sc1\nM v[24:27],a[80:83],v[220:223],v[24:27]\nds_read_b64_tr_b16 v[214:215],v241 offset:42752\ns_mul_i32 s72,s60,229376\nM v[28:31],a[68:71],v[216:219],v[28:31]\n" \
"s_lshl_b32 s2,s2,5\nv_add_u32 v253,0xffd00000,v253\nM v[28:31],a[84:87],v[220:223],v[28:31]\ns_cmp_eq_u32 s62,3\ns_cselect_b64 s[56:57],s[26:27],s[56:57]\ns_cselect_b32 s71,s72,s71\ns_cselect_b32 s58,229376,s58\ns_cselect_b32 s0,4096,s0\n" \
"M v[88:91],a[72:75],v[216:219],v[88:91]\ns_cmp_lt_u32 s70,14\ns_cselect_b32 s2,s2,0\ns_cselect_b32 s3,-1,0\nM v[88:91],a[88:91],v[220:223],v[88:91]\ns_lshl2_add_u32 s70,s61,s7\nv_lshrrev_b32 v253,4,v249\nM v[92:95],a[76:79],v[216:219],v[92:95]\n" \
"ds_read_b64_tr_b16 v[216:217],v242 offset:34304\nds_read_b64_tr_b16 v[218:219],v242 offset:34560\nM v[92:95],a[92:95],v[220:223],v[92:95]\nds_read_b64_tr_b16 v[220:221],v242 offset:42496\nds_read_b64_tr_b16 v[222:223],v242 offset:42752\nM v[32:35],a[64:67],v[192:195],v[32:35]\nds_write_b64 v250,v[144:145] offset:4096\nv_and_b32 v251,0xf0,v240\n" \
"M v[32:35],a[80:83],v[196:199],v[32:35]\ns_add_u32 s48,s44,19922944\ns_addc_u32 s49,s45,0\nM v[36:39],a[68:71],v[192:195],v[36:39]\ns_mul_i32 s1,s70,s0\nv_lshl_or_b32 v253,v253,12,v251\nM v[36:39],a[84:87],v[196:199],v[36:39]\ns_lshl_b32 s70,s61,8\n" \
"s_mov_b32 s50,s46\nM v[96:99],a[72:75],v[192:195],v[96:99]\ns_add_u32 s44,s44,3145728\ns_addc_u32 s45,s45,0\nM v[96:99],a[88:91],v[196:199],v[96:99]\ns_sub_u32 s33,s33,1867776\ns_add_u32 s67,s67,36700160\nM v[100:103],a[76:79],v[192:195],v[100:103]\n" \
"s_cmp_lt_u32 s62,3\ns_cselect_b32 s58,0,s58\nM v[100:103],a[92:95],v[196:199],v[100:103]\ns_lshl_b32 s0,s0,4\ns_waitcnt lgkmcnt(5)\nM v[40:43],a[64:67],v[200:203],v[40:43]\ns_add_u32 s56,s56,s71\ns_addc_u32 s57,s57,0\n" \
"M v[40:43],a[80:83],v[204:207],v[40:43]\nM v[44:47],a[68:71],v[200:203],v[44:47]\nbuffer_store_dwordx4 v[148:151],v240,s[52:55],s74 offen offset:1024 sc1\nM v[44:47],a[84:87],v[204:207],v[44:47]\nds_write_b64 v250,v[146:147] offset:4104\nM v[104:107],a[72:75],v[200:203],v[104:107]\nM v[104:107],a[88:91],v[204:207],v[104:107]\nM v[108:111],a[76:79],v[200:203],v[108:111]\n" \
"M v[108:111],a[92:95],v[204:207],v[108:111]\nM v[48:51],a[64:67],v[208:211],v[48:51]\nM v[48:51],a[80:83],v[212:215],v[48:51]\nM v[52:55],a[68:71],v[208:211],v[52:55]\ns_add_u32 m0,s64,131072\ns_add_u32 s69,s68,3145728\nM v[52:55],a[84:87],v[212:215],v[52:55]\nM v[112:115],a[72:75],v[208:211],v[112:115]\n" \
"s_waitcnt vmcnt(14) lgkmcnt(1)\ns_barrier\nM v[112:115],a[88:91],v[212:215],v[112:115]\nds_read_b64_tr_b16 v[192:193],v241 offset:49152\nds_read_b64_tr_b16 v[194:195],v241 offset:49408\nM v[116:119],a[76:79],v[208:211],v[116:119]\nds_read_b64_tr_b16 v[196:197],v241 offset:57344\nds_read_b64_tr_b16 v[198:199],v241 offset:57600\n" \
"M v[116:119],a[92:95],v[212:215],v[116:119]\nds_read_b64_tr_b16 v[200:201],v242 offset:49152\nds_read_b64_tr_b16 v[202:203],v242 offset:49408\nM v[56:59],a[64:67],v[216:219],v[56:59]\nds_read_b64_tr_b16 v[204:205],v242 offset:57344\nds_read_b64_tr_b16 v[206:207],v242 offset:57600\nM v[56:59],a[80:83],v[220:223],v[56:59]\nds_read_b64_tr_b16 v[208:209],v241 offset:49664\n" \
"ds_read_b64_tr_b16 v[210:211],v241 offset:49920\nM v[60:63],a[68:71],v[216:219],v[60:63]\nds_read_b64_tr_b16 v[212:213],v241 offset:57856\nds_read_b64_tr_b16 v[214:215],v241 offset:58112\nM v[60:63],a[84:87],v[220:223],v[60:63]\nbuffer_load_dwordx4 v243,s[40:43],s69 offen offset:0 lds\nM v[120:123],a[72:75],v[216:219],v[120:123]\nds_write_b64 v250,v[148:149] offset:5120\n" \
"M v[120:123],a[88:91],v[220:223],v[120:123]\nM v[124:127],a[76:79],v[216:219],v[124:127]\nds_read_b64_tr_b16 v[216:217],v242 offset:49664\nds_read_b64_tr_b16 v[218:219],v242 offset:49920\nM v[124:127],a[92:95],v[220:223],v[124:127]\nds_read_b64_tr_b16 v[220:221],v242 offset:57856\ns_waitcnt lgkmcnt(14)\nM v[0:3],a[96:99],v[192:195],v[0:3]\n" \
"buffer_store_dwordx4 v[152:155],v240,s[52:55],s74 offen offset:2048 sc1\ns_waitcnt lgkmcnt(12)\nM v[0:3],a[112:115],v[196:199],v[0:3]\nds_read_b64_tr_b16 v[222:223],v242 offset:58112\nM v[4:7],a[100:103],v[192:195],v[4:7]\nds_write_b64 v250,v[150:151] offset:5128\nM v[4:7],a[116:119],v[196:199],v[4:7]\nM v[64:67],a[104:107],v[192:195],v[64:67]\n" \
"buffer_load_dwordx4 v244,s[40:43],s69 offen offset:1024 lds\nM v[64:67],a[120:123],v[196:199],v[64:67]\nM v[68:71],a[108:111],v[192:195],v[68:71]\nds_read_b64_tr_b16 v[192:193],v241 offset:50176\nds_read_b64_tr_b16 v[194:195],v241 offset:50432\nM v[68:71],a[124:127],v[196:199],v[68:71]\nds_read_b64_tr_b16 v[196:197],v241 offset:58368\ns_waitcnt lgkmcnt(9)\n" \
"M v[8:11],a[96:99],v[200:203],v[8:11]\nbuffer_load_dwordx4 v245,s[40:43],s69 offen offset:2048 lds\nM v[8:11],a[112:115],v[204:207],v[8:11]\nds_read_b64_tr_b16 v[198:199],v241 offset:58624\nM v[12:15],a[100:103],v[200:203],v[12:15]\nM v[12:15],a[116:119],v[204:207],v[12:15]\nM v[72:75],a[104:107],v[200:203],v[72:75]\nbuffer_load_dwordx4 v246,s[40:43],s69 offen offset:3072 lds\n" \
"M v[72:75],a[120:123],v[204:207],v[72:75]\nM v[76:79],a[108:111],v[200:203],v[76:79]\nds_read_b64_tr_b16 v[200:201],v242 offset:50176\nds_read_b64_tr_b16 v[202:203],v242 offset:50432\nM v[76:79],a[124:127],v[204:207],v[76:79]\nds_read_b64_tr_b16 v[204:205],v242 offset:58368\nds_read_b64_tr_b16 v[206:207],v242 offset:58624\nM v[16:19],a[96:99],v[208:211],v[16:19]\n" \
"buffer_store_dwordx4 v[156:159],v240,s[52:55],s74 offen offset:3072 sc1\nM v[16:19],a[112:115],v[212:215],v[16:19]\nds_write_b64 v250,v[152:153] offset:6144\ns_add_u32 s74,s74,4096\nM v[20:23],a[100:103],v[208:211],v[20:23]\nM v[20:23],a[116:119],v[212:215],v[20:23]\nM v[80:83],a[104:107],v[208:211],v[80:83]\nM v[80:83],a[120:123],v[212:215],v[80:83]\n" \
"M v[84:87],a[108:111],v[208:211],v[84:87]\nds_read_b64_tr_b16 v[208:209],v241 offset:50688\nds_read_b64_tr_b16 v[210:211],v241 offset:50944\nM v[84:87],a[124:127],v[212:215],v[84:87]\nds_read_b64_tr_b16 v[212:213],v241 offset:58880\ns_waitcnt lgkmcnt(8)\nM v[24:27],a[96:99],v[216:219],v[24:27]\nds_read_b64_tr_b16 v[214:215],v241 offset:59136\n" \
"M v[24:27],a[112:115],v[220:223],v[24:27]\nM v[28:31],a[100:103],v[216:219],v[28:31]\nds_write_b64 v250,v[154:155] offset:6152\nM v[28:31],a[116:119],v[220:223],v[28:31]\nM v[88:91],a[104:107],v[216:219],v[88:91]\nM v[88:91],a[120:123],v[220:223],v[88:91]\nM v[92:95],a[108:111],v[216:219],v[92:95]\nds_read_b64_tr_b16 v[216:217],v242 offset:50688\n" \
"ds_read_b64_tr_b16 v[218:219],v242 offset:50944\nM v[92:95],a[124:127],v[220:223],v[92:95]\nds_read_b64_tr_b16 v[220:221],v242 offset:58880\nds_read_b64_tr_b16 v[222:223],v242 offset:59136\nM v[32:35],a[96:99],v[192:195],v[32:35]\nbuffer_store_dwordx4 v[160:163],v240,s[52:55],s74 offen offset:0 sc1\nM v[32:35],a[112:115],v[196:199],v[32:35]\nM v[36:39],a[100:103],v[192:195],v[36:39]\n" \
"M v[36:39],a[116:119],v[196:199],v[36:39]\nM v[96:99],a[104:107],v[192:195],v[96:99]\nM v[96:99],a[120:123],v[196:199],v[96:99]\nM v[100:103],a[108:111],v[192:195],v[100:103]\nds_write_b64 v250,v[156:157] offset:7168\nM v[100:103],a[124:127],v[196:199],v[100:103]\ns_waitcnt lgkmcnt(7)\nM v[40:43],a[96:99],v[200:203],v[40:43]\n" \
"M v[40:43],a[112:115],v[204:207],v[40:43]\nM v[44:47],a[100:103],v[200:203],v[44:47]\nM v[44:47],a[116:119],v[204:207],v[44:47]\nM v[104:107],a[104:107],v[200:203],v[104:107]\nM v[104:107],a[120:123],v[204:207],v[104:107]\nM v[108:111],a[108:111],v[200:203],v[108:111]\nM v[108:111],a[124:127],v[204:207],v[108:111]\nM v[48:51],a[96:99],v[208:211],v[48:51]\n" \
"s_waitcnt lgkmcnt(3)\nM v[48:51],a[112:115],v[212:215],v[48:51]\nM v[52:55],a[100:103],v[208:211],v[52:55]\nbuffer_store_dwordx4 v[164:167],v240,s[52:55],s74 offen offset:1024 sc1\nM v[52:55],a[116:119],v[212:215],v[52:55]\ns_add_u32 m0,s64,147456\ns_add_u32 s69,s68,3670016\nM v[112:115],a[104:107],v[208:211],v[112:115]\n" \
"s_waitcnt vmcnt(15) lgkmcnt(1)\ns_barrier\nM v[112:115],a[120:123],v[212:215],v[112:115]\nds_read_b64_tr_b16 v[192:193],v241 offset:0\nds_read_b64_tr_b16 v[194:195],v241 offset:256\nM v[116:119],a[108:111],v[208:211],v[116:119]\nds_read_b64_tr_b16 v[196:197],v241 offset:8192\nds_read_b64_tr_b16 v[198:199],v241 offset:8448\n" \
"M v[116:119],a[124:127],v[212:215],v[116:119]\nds_read_b64_tr_b16 v[200:201],v242 offset:0\nds_read_b64_tr_b16 v[202:203],v242 offset:256\nM v[56:59],a[96:99],v[216:219],v[56:59]\nds_read_b64_tr_b16 v[204:205],v242 offset:8192\nds_read_b64_tr_b16 v[206:207],v242 offset:8448\nM v[56:59],a[112:115],v[220:223],v[56:59]\nds_read_b64_tr_b16 v[208:209],v241 offset:512\n" \
"ds_read_b64_tr_b16 v[210:211],v241 offset:768\nM v[60:63],a[100:103],v[216:219],v[60:63]\nds_read_b64_tr_b16 v[212:213],v241 offset:8704\nds_read_b64_tr_b16 v[214:215],v241 offset:8960\nM v[60:63],a[116:119],v[220:223],v[60:63]\nbuffer_load_dwordx4 v243,s[40:43],s69 offen offset:0 lds\nM v[120:123],a[104:107],v[216:219],v[120:123]\nds_write_b64 v250,v[158:159] offset:7176\n" \
"M v[120:123],a[120:123],v[220:223],v[120:123]\nM v[124:127],a[108:111],v[216:219],v[124:127]\nds_read_b64_tr_b16 v[216:217],v242 offset:512\nds_read_b64_tr_b16 v[218:219],v242 offset:768\nM v[124:127],a[124:127],v[220:223],v[124:127]\nds_read_b64_tr_b16 v[220:221],v242 offset:8704\ns_waitcnt lgkmcnt(14)\nM v[0:3],a[128:131],v[192:195],v[0:3]\n" \
"ds_read_b64_tr_b16 v[222:223],v242 offset:8960\ns_waitcnt lgkmcnt(13)\nM v[0:3],a[144:147],v[196:199],v[0:3]\nbuffer_load_dwordx4 v244,s[40:43],s69 offen offset:1024 lds\nM v[4:7],a[132:135],v[192:195],v[4:7]\nds_write_b64 v250,v[160:161] offset:8192\nM v[4:7],a[148:151],v[196:199],v[4:7]\nM v[64:67],a[136:139],v[192:195],v[64:67]\n" \
"M v[64:67],a[152:155],v[196:199],v[64:67]\nbuffer_store_dwordx4 v[168:171],v240,s[52:55],s74 offen offset:2048 sc1\nM v[68:71],a[140:143],v[192:195],v[68:71]\nds_read_b64_tr_b16 v[192:193],v241 offset:1024\nds_read_b64_tr_b16 v[194:195],v241 offset:1280\nM v[68:71],a[156:159],v[196:199],v[68:71]\nds_read_b64_tr_b16 v[196:197],v241 offset:9216\ns_waitcnt lgkmcnt(9)\n" \
"M v[8:11],a[128:131],v[200:203],v[8:11]\nds_read_b64_tr_b16 v[198:199],v241 offset:9472\nM v[8:11],a[144:147],v[204:207],v[8:11]\nbuffer_load_dwordx4 v245,s[40:43],s69 offen offset:2048 lds\nM v[12:15],a[132:135],v[200:203],v[12:15]\nds_write_b64 v250,v[162:163] offset:8200\nM v[12:15],a[148:151],v[204:207],v[12:15]\nM v[72:75],a[136:139],v[200:203],v[72:75]\n" \
"M v[72:75],a[152:155],v[204:207],v[72:75]\nbuffer_load_dwordx4 v246,s[40:43],s69 offen offset:3072 lds\nM v[76:79],a[140:143],v[200:203],v[76:79]\nds_read_b64_tr_b16 v[200:201],v242 offset:1024\nds_read_b64_tr_b16 v[202:203],v242 offset:1280\nM v[76:79],a[156:159],v[204:207],v[76:79]\nds_read_b64_tr_b16 v[204:205],v242 offset:9216\nds_read_b64_tr_b16 v[206:207],v242 offset:9472\n" \
"M v[16:19],a[128:131],v[208:211],v[16:19]\nM v[16:19],a[144:147],v[212:215],v[16:19]\nM v[20:23],a[132:135],v[208:211],v[20:23]\nM v[20:23],a[148:151],v[212:215],v[20:23]\nM v[80:83],a[136:139],v[208:211],v[80:83]\nM v[80:83],a[152:155],v[212:215],v[80:83]\nM v[84:87],a[140:143],v[208:211],v[84:87]\nbuffer_load_dword v248,v228,s[52:55],s80 offen sc1\n" \
"M v[84:87],a[156:159],v[212:215],v[84:87]\nds_read_b64_tr_b16 v[208:209],v241 offset:1536\ns_waitcnt lgkmcnt(6)\nM v[24:27],a[128:131],v[216:219],v[24:27]\nbuffer_store_dwordx4 v[172:175],v240,s[52:55],s74 offen offset:3072 sc1\nM v[24:27],a[144:147],v[220:223],v[24:27]\nds_read_b64_tr_b16 v[210:211],v241 offset:1792\nds_read_b64_tr_b16 v[212:213],v241 offset:9728\n" \
"M v[28:31],a[132:135],v[216:219],v[28:31]\nds_read_b64_tr_b16 v[214:215],v241 offset:9984\ns_add_u32 s74,s74,4096\nM v[28:31],a[148:151],v[220:223],v[28:31]\nds_write_b64 v250,v[164:165] offset:9216\nM v[88:91],a[136:139],v[216:219],v[88:91]\nM v[88:91],a[152:155],v[220:223],v[88:91]\nM v[92:95],a[140:143],v[216:219],v[92:95]\n" \
"ds_read_b64_tr_b16 v[216:217],v242 offset:1536\nds_read_b64_tr_b16 v[218:219],v242 offset:1792\nM v[92:95],a[156:159],v[220:223],v[92:95]\nds_read_b64_tr_b16 v[220:221],v242 offset:9728\nds_read_b64_tr_b16 v[222:223],v242 offset:9984\nM v[32:35],a[128:131],v[192:195],v[32:35]\nM v[32:35],a[144:147],v[196:199],v[32:35]\nM v[36:39],a[132:135],v[192:195],v[36:39]\n" \
"ds_write_b64 v250,v[166:167] offset:9224\nM v[36:39],a[148:151],v[196:199],v[36:39]\nM v[96:99],a[136:139],v[192:195],v[96:99]\nM v[96:99],a[152:155],v[196:199],v[96:99]\nM v[100:103],a[140:143],v[192:195],v[100:103]\nM v[100:103],a[156:159],v[196:199],v[100:103]\ns_waitcnt lgkmcnt(9)\nM v[40:43],a[128:131],v[200:203],v[40:43]\n" \
"buffer_store_dwordx4 v[176:179],v240,s[52:55],s74 offen offset:0 sc1\nM v[40:43],a[144:147],v[204:207],v[40:43]\nM v[44:47],a[132:135],v[200:203],v[44:47]\nM v[44:47],a[148:151],v[204:207],v[44:47]\nM v[104:107],a[136:139],v[200:203],v[104:107]\nM v[104:107],a[152:155],v[204:207],v[104:107]\nM v[108:111],a[140:143],v[200:203],v[108:111]\nds_write_b64 v250,v[168:169] offset:10240\n" \
"M v[108:111],a[156:159],v[204:207],v[108:111]\ns_waitcnt lgkmcnt(4)\nM v[48:51],a[128:131],v[208:211],v[48:51]\nM v[48:51],a[144:147],v[212:215],v[48:51]\nM v[52:55],a[132:135],v[208:211],v[52:55]\ns_sub_u32 s40,s12,0x1000\ns_subb_u32 s41,s13,0\nM v[52:55],a[148:151],v[212:215],v[52:55]\n" \
"s_add_u32 m0,s64,98304\ns_add_u32 s69,s68,0\nM v[112:115],a[136:139],v[208:211],v[112:115]\ns_waitcnt vmcnt(18) lgkmcnt(1)\ns_barrier\nM v[112:115],a[152:155],v[212:215],v[112:115]\nds_read_b64_tr_b16 v[192:193],v241 offset:16384\nds_read_b64_tr_b16 v[194:195],v241 offset:16640\n" \
"M v[116:119],a[140:143],v[208:211],v[116:119]\nds_read_b64_tr_b16 v[196:197],v241 offset:24576\nds_read_b64_tr_b16 v[198:199],v241 offset:24832\nM v[116:119],a[156:159],v[212:215],v[116:119]\nds_read_b64_tr_b16 v[200:201],v242 offset:16384\nds_read_b64_tr_b16 v[202:203],v242 offset:16640\nM v[56:59],a[128:131],v[216:219],v[56:59]\nds_read_b64_tr_b16 v[204:205],v242 offset:24576\n" \
"ds_read_b64_tr_b16 v[206:207],v242 offset:24832\nM v[56:59],a[144:147],v[220:223],v[56:59]\nds_read_b64_tr_b16 v[208:209],v241 offset:16896\nds_read_b64_tr_b16 v[210:211],v241 offset:17152\nM v[60:63],a[132:135],v[216:219],v[60:63]\nbuffer_store_dwordx4 v[180:183],v240,s[52:55],s74 offen offset:1024 sc1\nM v[60:63],a[148:151],v[220:223],v[60:63]\nds_read_b64_tr_b16 v[212:213],v241 offset:25088\n" \
"ds_read_b64_tr_b16 v[214:215],v241 offset:25344\nM v[120:123],a[136:139],v[216:219],v[120:123]\nds_write_b64 v250,v[170:171] offset:10248\ns_lshl_b32 s12,s61,13\nM v[120:123],a[152:155],v[220:223],v[120:123]\nM v[124:127],a[140:143],v[216:219],v[124:127]\nds_read_b64_tr_b16 v[216:217],v242 offset:16896\nds_read_b64_tr_b16 v[218:219],v242 offset:17152\n" \
"M v[124:127],a[156:159],v[220:223],v[124:127]\nds_read_b64_tr_b16 v[220:221],v242 offset:25088\ns_waitcnt lgkmcnt(14)\nM v[0:3],a[160:163],v[192:195],v[0:3]\nds_read_b64_tr_b16 v[222:223],v242 offset:25344\ns_waitcnt lgkmcnt(13)\nM v[0:3],a[176:179],v[196:199],v[0:3]\nbuffer_load_dwordx4 v243,s[40:43],s69 offen offset:0 lds\n" \
"M v[4:7],a[164:167],v[192:195],v[4:7]\nM v[4:7],a[180:183],v[196:199],v[4:7]\nM v[64:67],a[168:171],v[192:195],v[64:67]\nM v[64:67],a[184:187],v[196:199],v[64:67]\nbuffer_load_dwordx4 v244,s[40:43],s69 offen offset:1024 lds\nM v[68:71],a[172:175],v[192:195],v[68:71]\nds_read_b64_tr_b16 v[192:193],v241 offset:17408\nds_read_b64_tr_b16 v[194:195],v241 offset:17664\n" \
"M v[68:71],a[188:191],v[196:199],v[68:71]\nds_read_b64_tr_b16 v[196:197],v241 offset:25600\ns_waitcnt lgkmcnt(9)\nM v[8:11],a[160:163],v[200:203],v[8:11]\nds_write_b64 v250,v[172:173] offset:11264\nds_read_b64_tr_b16 v[198:199],v241 offset:25856\nM v[8:11],a[176:179],v[204:207],v[8:11]\nbuffer_load_dwordx4 v245,s[40:43],s69 offen offset:2048 lds\n" \
"M v[12:15],a[164:167],v[200:203],v[12:15]\nM v[12:15],a[180:183],v[204:207],v[12:15]\nM v[72:75],a[168:171],v[200:203],v[72:75]\nM v[72:75],a[184:187],v[204:207],v[72:75]\nbuffer_store_dwordx4 v[184:187],v240,s[52:55],s74 offen offset:2048 sc1\nM v[76:79],a[172:175],v[200:203],v[76:79]\ns_waitcnt vmcnt(7)\nv_cmp_eq_u32 vcc,0,v248\n" \
"M v[76:79],a[188:191],v[204:207],v[76:79]\ns_cbranch_vccnz .Lps0\n.Lpb0:\nds_read_b64_tr_b16 v[200:201],v242 offset:17408\nM v[16:19],a[160:163],v[208:211],v[16:19]\nds_read_b64_tr_b16 v[202:203],v242 offset:17664\ns_waitcnt lgkmcnt(7)\nM v[16:19],a[176:179],v[212:215],v[16:19]\n" \
"ds_read_b64_tr_b16 v[204:205],v242 offset:25600\nds_read_b64_tr_b16 v[206:207],v242 offset:25856\nM v[20:23],a[164:167],v[208:211],v[20:23]\nbuffer_load_dwordx4 a[0:3],v240,s[52:55],s76 offen offset:0 sc1\nM v[20:23],a[180:183],v[212:215],v[20:23]\nds_write_b64 v250,v[174:175] offset:11272\nv_and_b32 v228,31,v249\nM v[80:83],a[168:171],v[208:211],v[80:83]\n" \
"s_lshl_b32 s80,s63,7\nv_lshlrev_b32 v228,2,v228\nM v[80:83],a[184:187],v[212:215],v[80:83]\ns_add_u32 s80,s80,81920\nv_mov_b32 v251,v228\nM v[84:87],a[172:175],v[208:211],v[84:87]\nds_read_b64_tr_b16 v[208:209],v241 offset:17920\nds_read_b64_tr_b16 v[210:211],v241 offset:18176\n" \
"M v[84:87],a[188:191],v[212:215],v[84:87]\nds_read_b64_tr_b16 v[212:213],v241 offset:26112\nds_read_b64_tr_b16 v[214:215],v241 offset:26368\nM v[24:27],a[160:163],v[216:219],v[24:27]\nbuffer_load_dwordx4 a[16:19],v240,s[52:55],s76 offen offset:2048 sc1\nM v[24:27],a[176:179],v[220:223],v[24:27]\nM v[28:31],a[164:167],v[216:219],v[28:31]\nM v[28:31],a[180:183],v[220:223],v[28:31]\n" \
"M v[88:91],a[168:171],v[216:219],v[88:91]\nbuffer_load_dwordx4 a[4:7],v240,s[52:55],s76 offen offset:1024 sc1\nM v[88:91],a[184:187],v[220:223],v[88:91]\nM v[92:95],a[172:175],v[216:219],v[92:95]\nds_read_b64_tr_b16 v[216:217],v242 offset:17920\nds_read_b64_tr_b16 v[218:219],v242 offset:18176\nM v[92:95],a[188:191],v[220:223],v[92:95]\nds_read_b64_tr_b16 v[220:221],v242 offset:26112\n" \
"s_waitcnt lgkmcnt(7)\nM v[32:35],a[160:163],v[192:195],v[32:35]\nbuffer_store_dwordx4 v[188:191],v240,s[52:55],s74 offen offset:3072 sc1\nM v[32:35],a[176:179],v[196:199],v[32:35]\nds_read_b64_tr_b16 v[222:223],v242 offset:26368\ns_add_u32 s74,s74,16764928\nM v[36:39],a[164:167],v[192:195],v[36:39]\nds_write_b64 v250,v[176:177] offset:12288\n" \
"M v[36:39],a[180:183],v[196:199],v[36:39]\nM v[96:99],a[168:171],v[192:195],v[96:99]\nbuffer_load_dwordx4 a[20:23],v240,s[52:55],s76 offen offset:3072 sc1\nM v[96:99],a[184:187],v[196:199],v[96:99]\ns_add_u32 s76,s76,4096\nM v[100:103],a[172:175],v[192:195],v[100:103]\nM v[100:103],a[188:191],v[196:199],v[100:103]\nM v[40:43],a[160:163],v[200:203],v[40:43]\n" \
"buffer_load_dwordx4 a[8:11],v240,s[52:55],s77 offen offset:0 sc1\nM v[40:43],a[176:179],v[204:207],v[40:43]\nM v[44:47],a[164:167],v[200:203],v[44:47]\nds_write_b64 v250,v[178:179] offset:12296\nM v[44:47],a[180:183],v[204:207],v[44:47]\nM v[104:107],a[168:171],v[200:203],v[104:107]\nbuffer_load_dwordx4 a[24:27],v240,s[52:55],s77 offen offset:2048 sc1\nM v[104:107],a[184:187],v[204:207],v[104:107]\n" \
"M v[108:111],a[172:175],v[200:203],v[108:111]\nM v[108:111],a[188:191],v[204:207],v[108:111]\ns_waitcnt lgkmcnt(3)\nM v[48:51],a[160:163],v[208:211],v[48:51]\nbuffer_load_dwordx4 a[12:15],v240,s[52:55],s77 offen offset:1024 sc1\nM v[48:51],a[176:179],v[212:215],v[48:51]\nM v[52:55],a[164:167],v[208:211],v[52:55]\nM v[52:55],a[180:183],v[212:215],v[52:55]\n" \
"M v[112:115],a[168:171],v[208:211],v[112:115]\ns_waitcnt lgkmcnt(1)\ns_barrier\nM v[112:115],a[184:187],v[212:215],v[112:115]\nds_read_b64_tr_b16 v[192:193],v241 offset:32768\nds_read_b64_tr_b16 v[194:195],v241 offset:33024\nM v[116:119],a[172:175],v[208:211],v[116:119]\nds_read_b64_tr_b16 v[196:197],v241 offset:40960\n" \
"ds_read_b64_tr_b16 v[198:199],v241 offset:41216\nM v[116:119],a[188:191],v[212:215],v[116:119]\nds_read_b64_tr_b16 v[200:201],v242 offset:32768\nds_read_b64_tr_b16 v[202:203],v242 offset:33024\nM v[56:59],a[160:163],v[216:219],v[56:59]\nds_read_b64_tr_b16 v[204:205],v242 offset:40960\nds_read_b64_tr_b16 v[206:207],v242 offset:41216\nM v[56:59],a[176:179],v[220:223],v[56:59]\n" \
"ds_read_b64_tr_b16 v[208:209],v241 offset:33280\nds_read_b64_tr_b16 v[210:211],v241 offset:33536\nM v[60:63],a[164:167],v[216:219],v[60:63]\nds_read_b64_tr_b16 v[212:213],v241 offset:41472\nds_read_b64_tr_b16 v[214:215],v241 offset:41728\nM v[60:63],a[180:183],v[220:223],v[60:63]\nbuffer_load_dwordx4 a[28:31],v240,s[52:55],s77 offen offset:3072 sc1\nM v[120:123],a[168:171],v[216:219],v[120:123]\n" \
"ds_write_b64 v250,v[180:181] offset:13312\ns_add_u32 s77,s77,4096\nM v[120:123],a[184:187],v[220:223],v[120:123]\nM v[124:127],a[172:175],v[216:219],v[124:127]\nds_read_b64_tr_b16 v[216:217],v242 offset:33280\nds_read_b64_tr_b16 v[218:219],v242 offset:33536\nM v[124:127],a[188:191],v[220:223],v[124:127]\nds_read_b64_tr_b16 v[220:221],v242 offset:41472\n" \
"s_waitcnt lgkmcnt(14)\nM v[0:3],a[192:195],v[192:195],v[0:3]\nds_read_b64_tr_b16 v[222:223],v242 offset:41728\ns_waitcnt lgkmcnt(13)\nM v[0:3],a[208:211],v[196:199],v[0:3]\nbuffer_load_dwordx4 v246,s[40:43],s69 offen offset:3072 lds\nM v[4:7],a[196:199],v[192:195],v[4:7]\ns_add_u32 m0,s64,114688\n" \
"s_add_u32 s69,s68,524288\nM v[4:7],a[212:215],v[196:199],v[4:7]\nds_write_b64 v250,v[182:183] offset:13320\nM v[64:67],a[200:203],v[192:195],v[64:67]\nM v[64:67],a[216:219],v[196:199],v[64:67]\nbuffer_load_dwordx4 a[32:35],v240,s[52:55],s76 offen offset:0 sc1\nM v[68:71],a[204:207],v[192:195],v[68:71]\nds_read_b64_tr_b16 v[192:193],v241 offset:33792\n" \
"ds_read_b64_tr_b16 v[194:195],v241 offset:34048\nM v[68:71],a[220:223],v[196:199],v[68:71]\nds_read_b64_tr_b16 v[196:197],v241 offset:41984\ns_waitcnt lgkmcnt(9)\nM v[8:11],a[192:195],v[200:203],v[8:11]\nds_read_b64_tr_b16 v[198:199],v241 offset:42240\nM v[8:11],a[208:211],v[204:207],v[8:11]\nbuffer_load_dwordx4 a[48:51],v240,s[52:55],s76 offen offset:2048 sc1\n" \
"M v[12:15],a[196:199],v[200:203],v[12:15]\nM v[12:15],a[212:215],v[204:207],v[12:15]\nM v[72:75],a[200:203],v[200:203],v[72:75]\nM v[72:75],a[216:219],v[204:207],v[72:75]\nbuffer_load_dwordx4 a[36:39],v240,s[52:55],s76 offen offset:1024 sc1\nM v[76:79],a[204:207],v[200:203],v[76:79]\nds_read_b64_tr_b16 v[200:201],v242 offset:33792\nds_read_b64_tr_b16 v[202:203],v242 offset:34048\n" \
"M v[76:79],a[220:223],v[204:207],v[76:79]\nds_read_b64_tr_b16 v[204:205],v242 offset:41984\nds_read_b64_tr_b16 v[206:207],v242 offset:42240\nM v[16:19],a[192:195],v[208:211],v[16:19]\nds_write_b64 v250,v[184:185] offset:14336\nM v[16:19],a[208:211],v[212:215],v[16:19]\nbuffer_load_dwordx4 a[52:55],v240,s[52:55],s76 offen offset:3072 sc1\nM v[20:23],a[196:199],v[208:211],v[20:23]\n" \
"s_add_u32 s76,s76,4096\nM v[20:23],a[212:215],v[212:215],v[20:23]\nM v[80:83],a[200:203],v[208:211],v[80:83]\nM v[80:83],a[216:219],v[212:215],v[80:83]\nbuffer_load_dwordx4 a[40:43],v240,s[52:55],s77 offen offset:0 sc1\nM v[84:87],a[204:207],v[208:211],v[84:87]\nds_read_b64_tr_b16 v[208:209],v241 offset:34304\nds_read_b64_tr_b16 v[210:211],v241 offset:34560\n" \
"M v[84:87],a[220:223],v[212:215],v[84:87]\nds_read_b64_tr_b16 v[212:213],v241 offset:42496\ns_waitcnt lgkmcnt(8)\nM v[24:27],a[192:195],v[216:219],v[24:27]\nds_read_b64_tr_b16 v[214:215],v241 offset:42752\nM v[24:27],a[208:211],v[220:223],v[24:27]\nbuffer_load_dwordx4 a[56:59],v240,s[52:55],s77 offen offset:2048 sc1\nM v[28:31],a[196:199],v[216:219],v[28:31]\n" \
"ds_write_b64 v250,v[186:187] offset:14344\nM v[28:31],a[212:215],v[220:223],v[28:31]\nM v[88:91],a[200:203],v[216:219],v[88:91]\nM v[88:91],a[216:219],v[220:223],v[88:91]\nbuffer_load_dwordx4 a[44:47],v240,s[52:55],s77 offen offset:1024 sc1\nM v[92:95],a[204:207],v[216:219],v[92:95]\nds_read_b64_tr_b16 v[216:217],v242 offset:34304\nds_read_b64_tr_b16 v[218:219],v242 offset:34560\n" \
"M v[92:95],a[220:223],v[220:223],v[92:95]\nds_read_b64_tr_b16 v[220:221],v242 offset:42496\nds_read_b64_tr_b16 v[222:223],v242 offset:42752\nM v[32:35],a[192:195],v[192:195],v[32:35]\nM v[32:35],a[208:211],v[196:199],v[32:35]\nbuffer_load_dwordx4 a[60:63],v240,s[52:55],s77 offen offset:3072 sc1\nM v[36:39],a[196:199],v[192:195],v[36:39]\ns_add_u32 s77,s77,4096\n" \
"M v[36:39],a[212:215],v[196:199],v[36:39]\nM v[96:99],a[200:203],v[192:195],v[96:99]\nM v[96:99],a[216:219],v[196:199],v[96:99]\nbuffer_load_dwordx4 v243,s[40:43],s69 offen offset:0 lds\nM v[100:103],a[204:207],v[192:195],v[100:103]\nds_write_b64 v250,v[188:189] offset:15360\nM v[100:103],a[220:223],v[196:199],v[100:103]\ns_waitcnt lgkmcnt(6)\n" \
"M v[40:43],a[192:195],v[200:203],v[40:43]\nM v[40:43],a[208:211],v[204:207],v[40:43]\nbuffer_load_dwordx4 v244,s[40:43],s69 offen offset:1024 lds\nM v[44:47],a[196:199],v[200:203],v[44:47]\nM v[44:47],a[212:215],v[204:207],v[44:47]\nM v[104:107],a[200:203],v[200:203],v[104:107]\nM v[104:107],a[216:219],v[204:207],v[104:107]\nbuffer_load_dwordx4 v245,s[40:43],s69 offen offset:2048 lds\n" \
"M v[108:111],a[204:207],v[200:203],v[108:111]\nM v[108:111],a[220:223],v[204:207],v[108:111]\nM v[48:51],a[192:195],v[208:211],v[48:51]\nM v[48:51],a[208:211],v[212:215],v[48:51]\nbuffer_load_dwordx4 v246,s[40:43],s69 offen offset:3072 lds\nM v[52:55],a[196:199],v[208:211],v[52:55]\ns_add_u32 m0,s64,131072\ns_add_u32 s69,s68,1048576\n" \
"M v[52:55],a[212:215],v[212:215],v[52:55]\nds_write_b64 v250,v[190:191] offset:15368\nv_add_u32 v250,65536,v250\nM v[112:115],a[200:203],v[208:211],v[112:115]\ns_waitcnt lgkmcnt(2)\ns_barrier\nM v[112:115],a[216:219],v[212:215],v[112:115]\nds_read_b64_tr_b16 v[192:193],v241 offset:49152\n" \
"ds_read_b64_tr_b16 v[194:195],v241 offset:49408\nM v[116:119],a[204:207],v[208:211],v[116:119]\nds_read_b64_tr_b16 v[196:197],v241 offset:57344\nds_read_b64_tr_b16 v[198:199],v241 offset:57600\nM v[116:119],a[220:223],v[212:215],v[116:119]\nds_read_b64_tr_b16 v[200:201],v242 offset:49152\nds_read_b64_tr_b16 v[202:203],v242 offset:49408\nM v[56:59],a[192:195],v[216:219],v[56:59]\n" \
"ds_read_b64_tr_b16 v[204:205],v242 offset:57344\nds_read_b64_tr_b16 v[206:207],v242 offset:57600\nM v[56:59],a[208:211],v[220:223],v[56:59]\nds_read_b64_tr_b16 v[208:209],v241 offset:49664\nds_read_b64_tr_b16 v[210:211],v241 offset:49920\nM v[60:63],a[196:199],v[216:219],v[60:63]\nds_read_b64_tr_b16 v[212:213],v241 offset:57856\nds_read_b64_tr_b16 v[214:215],v241 offset:58112\n" \
"M v[60:63],a[212:215],v[220:223],v[60:63]\nbuffer_load_dwordx4 a[64:67],v240,s[52:55],s76 offen offset:0 sc1\nM v[120:123],a[200:203],v[216:219],v[120:123]\nM v[120:123],a[216:219],v[220:223],v[120:123]\nM v[124:127],a[204:207],v[216:219],v[124:127]\nds_read_b64_tr_b16 v[216:217],v242 offset:49664\nds_read_b64_tr_b16 v[218:219],v242 offset:49920\nM v[124:127],a[220:223],v[220:223],v[124:127]\n" \
"ds_read_b64_tr_b16 v[220:221],v242 offset:57856\ns_waitcnt lgkmcnt(13)\nM v[0:3],a[224:227],v[192:195],v[0:3]\nds_read_b64_tr_b16 v[222:223],v242 offset:58112\ns_waitcnt lgkmcnt(12)\nM v[0:3],a[240:243],v[196:199],v[0:3]\nbuffer_load_dwordx4 a[68:71],v240,s[52:55],s76 offen offset:1024 sc1\nM v[4:7],a[228:231],v[192:195],v[4:7]\n" \
"M v[4:7],a[244:247],v[196:199],v[4:7]\nM v[64:67],a[232:235],v[192:195],v[64:67]\nM v[64:67],a[248:251],v[196:199],v[64:67]\nbuffer_load_dwordx4 a[80:83],v240,s[52:55],s76 offen offset:2048 sc1\nM v[68:71],a[236:239],v[192:195],v[68:71]\nds_read_b64_tr_b16 v[192:193],v241 offset:50176\nds_read_b64_tr_b16 v[194:195],v241 offset:50432\nM v[68:71],a[252:255],v[196:199],v[68:71]\n" \
"ds_read_b64_tr_b16 v[196:197],v241 offset:58368\ns_waitcnt lgkmcnt(7)\nM v[8:11],a[224:227],v[200:203],v[8:11]\nds_read_b64_tr_b16 v[198:199],v241 offset:58624\nv_cvt_pk_bf16_f32 v128,v0,v1\nM v[8:11],a[240:243],v[204:207],v[8:11]\nv_cvt_pk_bf16_f32 v129,v2,v3\nv_cvt_pk_bf16_f32 v130,v4,v5\n" \
"M v[12:15],a[228:231],v[200:203],v[12:15]\nv_cvt_pk_bf16_f32 v131,v6,v7\nv_cvt_pk_bf16_f32 v160,v64,v65\nM v[12:15],a[244:247],v[204:207],v[12:15]\nv_cvt_pk_bf16_f32 v161,v66,v67\nv_cvt_pk_bf16_f32 v162,v68,v69\nM v[72:75],a[232:235],v[200:203],v[72:75]\nv_cvt_pk_bf16_f32 v163,v70,v71\n" \
"v_cvt_pk_bf16_f32 v132,v8,v9\nM v[72:75],a[248:251],v[204:207],v[72:75]\nbuffer_load_dwordx4 a[84:87],v240,s[52:55],s76 offen offset:3072 sc1\nM v[76:79],a[236:239],v[200:203],v[76:79]\nds_read_b64_tr_b16 v[200:201],v242 offset:50176\nds_read_b64_tr_b16 v[202:203],v242 offset:50432\nM v[76:79],a[252:255],v[204:207],v[76:79]\nds_read_b64_tr_b16 v[204:205],v242 offset:58368\n" \
"ds_read_b64_tr_b16 v[206:207],v242 offset:58624\nM v[16:19],a[224:227],v[208:211],v[16:19]\nv_cvt_pk_bf16_f32 v133,v10,v11\nv_cvt_pk_bf16_f32 v134,v12,v13\nM v[16:19],a[240:243],v[212:215],v[16:19]\nv_cvt_pk_bf16_f32 v135,v14,v15\nv_cvt_pk_bf16_f32 v164,v72,v73\nM v[20:23],a[228:231],v[208:211],v[20:23]\n" \
"v_cvt_pk_bf16_f32 v165,v74,v75\nv_cvt_pk_bf16_f32 v166,v76,v77\nM v[20:23],a[244:247],v[212:215],v[20:23]\nv_cvt_pk_bf16_f32 v167,v78,v79\ns_add_u32 s76,s76,4096\nM v[80:83],a[232:235],v[208:211],v[80:83]\nv_cvt_pk_bf16_f32 v136,v16,v17\nv_cvt_pk_bf16_f32 v137,v18,v19\n" \
"M v[80:83],a[248:251],v[212:215],v[80:83]\nbuffer_load_dwordx4 a[72:75],v240,s[52:55],s77 offen offset:0 sc1\nM v[84:87],a[236:239],v[208:211],v[84:87]\nds_read_b64_tr_b16 v[208:209],v241 offset:50688\nds_read_b64_tr_b16 v[210:211],v241 offset:50944\nM v[84:87],a[252:255],v[212:215],v[84:87]\nds_read_b64_tr_b16 v[212:213],v241 offset:58880\ns_waitcnt lgkmcnt(7)\n" \
"M v[24:27],a[224:227],v[216:219],v[24:27]\nds_read_b64_tr_b16 v[214:215],v241 offset:59136\nv_cvt_pk_bf16_f32 v138,v20,v21\nM v[24:27],a[240:243],v[220:223],v[24:27]\nv_cvt_pk_bf16_f32 v139,v22,v23\nv_cvt_pk_bf16_f32 v168,v80,v81\nM v[28:31],a[228:231],v[216:219],v[28:31]\nv_cvt_pk_bf16_f32 v169,v82,v83\n" \
"v_cvt_pk_bf16_f32 v170,v84,v85\nM v[28:31],a[244:247],v[220:223],v[28:31]\nbuffer_load_dwordx4 a[88:91],v240,s[52:55],s77 offen offset:2048 sc1\nM v[88:91],a[232:235],v[216:219],v[88:91]\nv_cvt_pk_bf16_f32 v171,v86,v87\nv_cvt_pk_bf16_f32 v140,v24,v25\nM v[88:91],a[248:251],v[220:223],v[88:91]\nv_cvt_pk_bf16_f32 v141,v26,v27\n" \
"M v[92:95],a[236:239],v[216:219],v[92:95]\nds_read_b64_tr_b16 v[216:217],v242 offset:50688\nds_read_b64_tr_b16 v[218:219],v242 offset:50944\nM v[92:95],a[252:255],v[220:223],v[92:95]\nds_read_b64_tr_b16 v[220:221],v242 offset:58880\nds_read_b64_tr_b16 v[222:223],v242 offset:59136\nM v[32:35],a[224:227],v[192:195],v[32:35]\nv_cvt_pk_bf16_f32 v142,v28,v29\n" \
"v_cvt_pk_bf16_f32 v143,v30,v31\nM v[32:35],a[240:243],v[196:199],v[32:35]\nv_cvt_pk_bf16_f32 v172,v88,v89\nv_cvt_pk_bf16_f32 v173,v90,v91\nM v[36:39],a[228:231],v[192:195],v[36:39]\nv_cvt_pk_bf16_f32 v174,v92,v93\nv_cvt_pk_bf16_f32 v175,v94,v95\nM v[36:39],a[244:247],v[196:199],v[36:39]\n" \
"buffer_load_dwordx4 a[76:79],v240,s[52:55],s77 offen offset:1024 sc1\nM v[96:99],a[232:235],v[192:195],v[96:99]\nv_cvt_pk_bf16_f32 v144,v32,v33\nv_cvt_pk_bf16_f32 v145,v34,v35\nM v[96:99],a[248:251],v[196:199],v[96:99]\nM v[100:103],a[236:239],v[192:195],v[100:103]\nM v[100:103],a[252:255],v[196:199],v[100:103]\ns_waitcnt vmcnt(20)\n" \
"buffer_store_dword v252,off,s[52:55],s29 sc1\ns_waitcnt lgkmcnt(4)\nM v[40:43],a[224:227],v[200:203],v[40:43]\nv_cvt_pk_bf16_f32 v146,v36,v37\nv_cvt_pk_bf16_f32 v147,v38,v39\nM v[40:43],a[240:243],v[204:207],v[40:43]\nv_cvt_pk_bf16_f32 v176,v96,v97\nv_cvt_pk_bf16_f32 v177,v98,v99\n" \
"M v[44:47],a[228:231],v[200:203],v[44:47]\nv_cvt_pk_bf16_f32 v178,v100,v101\nv_cvt_pk_bf16_f32 v179,v102,v103\nM v[44:47],a[244:247],v[204:207],v[44:47]\nbuffer_load_dwordx4 a[92:95],v240,s[52:55],s77 offen offset:3072 sc1\nM v[104:107],a[232:235],v[200:203],v[104:107]\nv_cvt_pk_bf16_f32 v148,v40,v41\nv_cvt_pk_bf16_f32 v149,v42,v43\n" \
"M v[104:107],a[248:251],v[204:207],v[104:107]\ns_add_u32 s77,s77,4096\ns_add_u32 s29,s29,4096\nM v[108:111],a[236:239],v[200:203],v[108:111]\nv_cvt_pk_bf16_f32 v150,v44,v45\nv_cvt_pk_bf16_f32 v151,v46,v47\nM v[108:111],a[252:255],v[204:207],v[108:111]\nbuffer_load_dwordx4 v243,s[40:43],s69 offen offset:0 lds\n" \
"M v[48:51],a[224:227],v[208:211],v[48:51]\nv_cvt_pk_bf16_f32 v180,v104,v105\nv_cvt_pk_bf16_f32 v181,v106,v107\nM v[48:51],a[240:243],v[212:215],v[48:51]\nM v[52:55],a[228:231],v[208:211],v[52:55]\nM v[52:55],a[244:247],v[212:215],v[52:55]\nbuffer_load_dwordx4 v244,s[40:43],s69 offen offset:1024 lds\nM v[112:115],a[232:235],v[208:211],v[112:115]\n" \
"s_waitcnt vmcnt(15) lgkmcnt(0)\ns_barrier\nM v[112:115],a[248:251],v[212:215],v[112:115]\nds_read_b64_tr_b16 v[192:193],v241 offset:0\nds_read_b64_tr_b16 v[194:195],v241 offset:256\nM v[116:119],a[236:239],v[208:211],v[116:119]\nds_read_b64_tr_b16 v[196:197],v241 offset:8192\nds_read_b64_tr_b16 v[198:199],v241 offset:8448\n" \
"M v[116:119],a[252:255],v[212:215],v[116:119]\nds_read_b64_tr_b16 v[200:201],v242 offset:0\nds_read_b64_tr_b16 v[202:203],v242 offset:256\nM v[56:59],a[224:227],v[216:219],v[56:59]\nds_read_b64_tr_b16 v[204:205],v242 offset:8192\nds_read_b64_tr_b16 v[206:207],v242 offset:8448\nM v[56:59],a[240:243],v[220:223],v[56:59]\nds_read_b64_tr_b16 v[208:209],v241 offset:512\n" \
"ds_read_b64_tr_b16 v[210:211],v241 offset:768\nM v[60:63],a[228:231],v[216:219],v[60:63]\nds_read_b64_tr_b16 v[212:213],v241 offset:8704\nds_read_b64_tr_b16 v[214:215],v241 offset:8960\nM v[60:63],a[244:247],v[220:223],v[60:63]\nv_cvt_pk_bf16_f32 v182,v108,v109\nv_cvt_pk_bf16_f32 v183,v110,v111\nM v[120:123],a[232:235],v[216:219],v[120:123]\n" \
"v_cvt_pk_bf16_f32 v152,v48,v49\nv_cvt_pk_bf16_f32 v153,v50,v51\nM v[120:123],a[248:251],v[220:223],v[120:123]\nv_cvt_pk_bf16_f32 v154,v52,v53\nv_cvt_pk_bf16_f32 v155,v54,v55\nM v[124:127],a[236:239],v[216:219],v[124:127]\nds_read_b64_tr_b16 v[216:217],v242 offset:512\nds_read_b64_tr_b16 v[218:219],v242 offset:768\n" \
"M v[124:127],a[252:255],v[220:223],v[124:127]\nds_read_b64_tr_b16 v[220:221],v242 offset:8704\ns_waitcnt lgkmcnt(13)\nM v[0:3],v[192:195],a[0:3],0\nds_read_b64_tr_b16 v[222:223],v242 offset:8960\ns_waitcnt lgkmcnt(12)\nM v[0:3],v[196:199],a[16:19],v[0:3]\nv_cvt_pk_bf16_f32 v184,v112,v113\n" \
"v_cvt_pk_bf16_f32 v185,v114,v115\nM v[4:7],v[192:195],a[4:7],0\nv_cvt_pk_bf16_f32 v186,v116,v117\nv_cvt_pk_bf16_f32 v187,v118,v119\nM v[4:7],v[196:199],a[20:23],v[4:7]\nv_cvt_pk_bf16_f32 v156,v56,v57\nv_cvt_pk_bf16_f32 v157,v58,v59\nM v[64:67],v[192:195],a[8:11],0\n" \
"v_cvt_pk_bf16_f32 v158,v60,v61\nv_cvt_pk_bf16_f32 v159,v62,v63\nM v[64:67],v[196:199],a[24:27],v[64:67]\nv_cvt_pk_bf16_f32 v188,v120,v121\nv_cvt_pk_bf16_f32 v189,v122,v123\nM v[68:71],v[192:195],a[12:15],0\nds_read_b64_tr_b16 v[192:193],v241 offset:1024\nds_read_b64_tr_b16 v[194:195],v241 offset:1280\n" \
"M v[68:71],v[196:199],a[28:31],v[68:71]\nds_read_b64_tr_b16 v[196:197],v241 offset:9216\ns_waitcnt lgkmcnt(7)\nM v[8:11],v[200:203],a[0:3],0\nds_read_b64_tr_b16 v[198:199],v241 offset:9472\nv_cvt_pk_bf16_f32 v190,v124,v125\nM v[8:11],v[204:207],a[16:19],v[8:11]\nbuffer_load_dwordx4 v245,s[40:43],s69 offen offset:2048 lds\n" \
"M v[12:15],v[200:203],a[4:7],0\nv_cvt_pk_bf16_f32 v191,v126,v127\nM v[12:15],v[204:207],a[20:23],v[12:15]\nM v[72:75],v[200:203],a[8:11],0\nM v[72:75],v[204:207],a[24:27],v[72:75]\nbuffer_load_dwordx4 v246,s[40:43],s69 offen offset:3072 lds\nM v[76:79],v[200:203],a[12:15],0\nds_read_b64_tr_b16 v[200:201],v242 offset:1024\n" \
"ds_read_b64_tr_b16 v[202:203],v242 offset:1280\nM v[76:79],v[204:207],a[28:31],v[76:79]\nds_read_b64_tr_b16 v[204:205],v242 offset:9216\nds_read_b64_tr_b16 v[206:207],v242 offset:9472\nM v[16:19],v[208:211],a[0:3],0\ns_add_u32 m0,s64,147456\ns_add_u32 s69,s68,1572864\nM v[16:19],v[212:215],a[16:19],v[16:19]\n" \
"buffer_load_dwordx4 a[96:99],v240,s[52:55],s76 offen offset:0 sc1\nM v[20:23],v[208:211],a[4:7],0\nM v[20:23],v[212:215],a[20:23],v[20:23]\nM v[80:83],v[208:211],a[8:11],0\nM v[80:83],v[212:215],a[24:27],v[80:83]\nbuffer_store_dwordx4 v[128:131],v240,s[52:55],s74 offen offset:0 sc1\nM v[84:87],v[208:211],a[12:15],0\nds_read_b64_tr_b16 v[208:209],v241 offset:1536\n" \
"ds_read_b64_tr_b16 v[210:211],v241 offset:1792\nM v[84:87],v[212:215],a[28:31],v[84:87]\nds_read_b64_tr_b16 v[212:213],v241 offset:9728\ns_waitcnt lgkmcnt(7)\nM v[24:27],v[216:219],a[0:3],0\nds_read_b64_tr_b16 v[214:215],v241 offset:9984\nM v[24:27],v[220:223],a[16:19],v[24:27]\nbuffer_load_dwordx4 a[112:115],v240,s[52:55],s76 offen offset:2048 sc1\n" \
"M v[28:31],v[216:219],a[4:7],0\nM v[28:31],v[220:223],a[20:23],v[28:31]\nM v[88:91],v[216:219],a[8:11],0\nM v[88:91],v[220:223],a[24:27],v[88:91]\nbuffer_load_dwordx4 a[100:103],v240,s[52:55],s76 offen offset:1024 sc1\nM v[92:95],v[216:219],a[12:15],0\nds_read_b64_tr_b16 v[216:217],v242 offset:1536\nds_read_b64_tr_b16 v[218:219],v242 offset:1792\n" \
"M v[92:95],v[220:223],a[28:31],v[92:95]\nds_read_b64_tr_b16 v[220:221],v242 offset:9728\nds_read_b64_tr_b16 v[222:223],v242 offset:9984\nM v[32:35],v[192:195],a[0:3],0\nM v[32:35],v[196:199],a[16:19],v[32:35]\nbuffer_load_dwordx4 a[116:119],v240,s[52:55],s76 offen offset:3072 sc1\nM v[36:39],v[192:195],a[4:7],0\ns_add_u32 s76,s76,4096\n" \
"M v[36:39],v[196:199],a[20:23],v[36:39]\nM v[96:99],v[192:195],a[8:11],0\nM v[96:99],v[196:199],a[24:27],v[96:99]\nbuffer_load_dwordx4 a[104:107],v240,s[52:55],s77 offen offset:0 sc1\nM v[100:103],v[192:195],a[12:15],0\nM v[100:103],v[196:199],a[28:31],v[100:103]\ns_waitcnt lgkmcnt(4)\nM v[40:43],v[200:203],a[0:3],0\n" \
"M v[40:43],v[204:207],a[16:19],v[40:43]\nbuffer_load_dwordx4 a[120:123],v240,s[52:55],s77 offen offset:2048 sc1\nM v[44:47],v[200:203],a[4:7],0\nM v[44:47],v[204:207],a[20:23],v[44:47]\nM v[104:107],v[200:203],a[8:11],0\nM v[104:107],v[204:207],a[24:27],v[104:107]\nbuffer_load_dwordx4 a[108:111],v240,s[52:55],s77 offen offset:1024 sc1\nM v[108:111],v[200:203],a[12:15],0\n" \
"M v[108:111],v[204:207],a[28:31],v[108:111]\nM v[48:51],v[208:211],a[0:3],0\nM v[48:51],v[212:215],a[16:19],v[48:51]\nbuffer_load_dwordx4 a[124:127],v240,s[52:55],s77 offen offset:3072 sc1\nM v[52:55],v[208:211],a[4:7],0\ns_add_u32 s77,s77,4096\nM v[52:55],v[212:215],a[20:23],v[52:55]\nM v[112:115],v[208:211],a[8:11],0\n" \
"s_waitcnt vmcnt(15) lgkmcnt(0)\ns_barrier\nM v[112:115],v[212:215],a[24:27],v[112:115]\nds_read_b64_tr_b16 v[192:193],v241 offset:16384\nds_read_b64_tr_b16 v[194:195],v241 offset:16640\nM v[116:119],v[208:211],a[12:15],0\nds_read_b64_tr_b16 v[196:197],v241 offset:24576\nds_read_b64_tr_b16 v[198:199],v241 offset:24832\n" \
"buffer_store_dwordx4 v[132:135],v240,s[52:55],s74 offen offset:1024 sc1\nM v[116:119],v[212:215],a[28:31],v[116:119]\nds_read_b64_tr_b16 v[200:201],v242 offset:16384\nds_read_b64_tr_b16 v[202:203],v242 offset:16640\nM v[56:59],v[216:219],a[0:3],0\nds_read_b64_tr_b16 v[204:205],v242 offset:24576\nds_read_b64_tr_b16 v[206:207],v242 offset:24832\nM v[56:59],v[220:223],a[16:19],v[56:59]\n" \
"ds_read_b64_tr_b16 v[208:209],v241 offset:16896\nds_read_b64_tr_b16 v[210:211],v241 offset:17152\nM v[60:63],v[216:219],a[4:7],0\nds_read_b64_tr_b16 v[212:213],v241 offset:25088\nds_read_b64_tr_b16 v[214:215],v241 offset:25344\nM v[60:63],v[220:223],a[20:23],v[60:63]\nbuffer_load_dwordx4 v243,s[40:43],s69 offen offset:0 lds\nM v[120:123],v[216:219],a[8:11],0\n" \
"M v[120:123],v[220:223],a[24:27],v[120:123]\nM v[124:127],v[216:219],a[12:15],0\nds_read_b64_tr_b16 v[216:217],v242 offset:16896\nds_read_b64_tr_b16 v[218:219],v242 offset:17152\nM v[124:127],v[220:223],a[28:31],v[124:127]\nds_read_b64_tr_b16 v[220:221],v242 offset:25088\ns_waitcnt lgkmcnt(13)\nM v[0:3],v[192:195],a[32:35],v[0:3]\n" \
"ds_read_b64_tr_b16 v[222:223],v242 offset:25344\ns_waitcnt lgkmcnt(12)\nM v[0:3],v[196:199],a[48:51],v[0:3]\nbuffer_load_dwordx4 v244,s[40:43],s69 offen offset:1024 lds\nM v[4:7],v[192:195],a[36:39],v[4:7]\nM v[4:7],v[196:199],a[52:55],v[4:7]\nM v[64:67],v[192:195],a[40:43],v[64:67]\nM v[64:67],v[196:199],a[56:59],v[64:67]\n" \
"buffer_load_dwordx4 v245,s[40:43],s69 offen offset:2048 lds\nM v[68:71],v[192:195],a[44:47],v[68:71]\nds_read_b64_tr_b16 v[192:193],v241 offset:17408\nds_read_b64_tr_b16 v[194:195],v241 offset:17664\nM v[68:71],v[196:199],a[60:63],v[68:71]\nds_read_b64_tr_b16 v[196:197],v241 offset:25600\ns_waitcnt lgkmcnt(7)\nM v[8:11],v[200:203],a[32:35],v[8:11]\n" \
"ds_read_b64_tr_b16 v[198:199],v241 offset:25856\nM v[8:11],v[204:207],a[48:51],v[8:11]\nbuffer_load_dwordx4 v246,s[40:43],s69 offen offset:3072 lds\nM v[12:15],v[200:203],a[36:39],v[12:15]\ns_add_u32 m0,s64,98304\ns_add_u32 s69,s68,2097152\nM v[12:15],v[204:207],a[52:55],v[12:15]\nM v[72:75],v[200:203],a[40:43],v[72:75]\n" \
"M v[72:75],v[204:207],a[56:59],v[72:75]\nbuffer_load_dwordx4 a[128:131],v240,s[52:55],s76 offen offset:0 sc1\nM v[76:79],v[200:203],a[44:47],v[76:79]\nds_read_b64_tr_b16 v[200:201],v242 offset:17408\nds_read_b64_tr_b16 v[202:203],v242 offset:17664\nM v[76:79],v[204:207],a[60:63],v[76:79]\nds_read_b64_tr_b16 v[204:205],v242 offset:25600\nds_read_b64_tr_b16 v[206:207],v242 offset:25856\n" \
"M v[16:19],v[208:211],a[32:35],v[16:19]\nM v[16:19],v[212:215],a[48:51],v[16:19]\nbuffer_store_dwordx4 v[136:139],v240,s[52:55],s74 offen offset:2048 sc1\nM v[20:23],v[208:211],a[36:39],v[20:23]\nM v[20:23],v[212:215],a[52:55],v[20:23]\nM v[80:83],v[208:211],a[40:43],v[80:83]\nM v[80:83],v[212:215],a[56:59],v[80:83]\nbuffer_load_dwordx4 a[144:147],v240,s[52:55],s76 offen offset:2048 sc1\n" \
"M v[84:87],v[208:211],a[44:47],v[84:87]\nds_read_b64_tr_b16 v[208:209],v241 offset:17920\nds_read_b64_tr_b16 v[210:211],v241 offset:18176\nM v[84:87],v[212:215],a[60:63],v[84:87]\nds_read_b64_tr_b16 v[212:213],v241 offset:26112\ns_waitcnt lgkmcnt(7)\nM v[24:27],v[216:219],a[32:35],v[24:27]\nds_read_b64_tr_b16 v[214:215],v241 offset:26368\n" \
"M v[24:27],v[220:223],a[48:51],v[24:27]\nbuffer_load_dwordx4 a[132:135],v240,s[52:55],s76 offen offset:1024 sc1\nM v[28:31],v[216:219],a[36:39],v[28:31]\nM v[28:31],v[220:223],a[52:55],v[28:31]\nM v[88:91],v[216:219],a[40:43],v[88:91]\nM v[88:91],v[220:223],a[56:59],v[88:91]\nbuffer_load_dwordx4 a[148:151],v240,s[52:55],s76 offen offset:3072 sc1\nM v[92:95],v[216:219],a[44:47],v[92:95]\n" \
"ds_read_b64_tr_b16 v[216:217],v242 offset:17920\nds_read_b64_tr_b16 v[218:219],v242 offset:18176\nM v[92:95],v[220:223],a[60:63],v[92:95]\nds_read_b64_tr_b16 v[220:221],v242 offset:26112\nds_read_b64_tr_b16 v[222:223],v242 offset:26368\nM v[32:35],v[192:195],a[32:35],v[32:35]\ns_add_u32 s76,s76,4096\nM v[32:35],v[196:199],a[48:51],v[32:35]\n" \
"buffer_load_dwordx4 a[136:139],v240,s[52:55],s77 offen offset:0 sc1\nM v[36:39],v[192:195],a[36:39],v[36:39]\nM v[36:39],v[196:199],a[52:55],v[36:39]\nM v[96:99],v[192:195],a[40:43],v[96:99]\nM v[96:99],v[196:199],a[56:59],v[96:99]\nbuffer_load_dwordx4 a[152:155],v240,s[52:55],s77 offen offset:2048 sc1\nM v[100:103],v[192:195],a[44:47],v[100:103]\nM v[100:103],v[196:199],a[60:63],v[100:103]\n" \
"s_waitcnt lgkmcnt(4)\nM v[40:43],v[200:203],a[32:35],v[40:43]\nM v[40:43],v[204:207],a[48:51],v[40:43]\nbuffer_load_dwordx4 a[140:143],v240,s[52:55],s77 offen offset:1024 sc1\nM v[44:47],v[200:203],a[36:39],v[44:47]\nM v[44:47],v[204:207],a[52:55],v[44:47]\nM v[104:107],v[200:203],a[40:43],v[104:107]\nM v[104:107],v[204:207],a[56:59],v[104:107]\n" \
"buffer_load_dwordx4 a[156:159],v240,s[52:55],s77 offen offset:3072 sc1\nM v[108:111],v[200:203],a[44:47],v[108:111]\ns_add_u32 s77,s77,4096\nM v[108:111],v[204:207],a[60:63],v[108:111]\nM v[48:51],v[208:211],a[32:35],v[48:51]\nM v[48:51],v[212:215],a[48:51],v[48:51]\nbuffer_store_dwordx4 v[140:143],v240,s[52:55],s74 offen offset:3072 sc1\nM v[52:55],v[208:211],a[36:39],v[52:55]\n" \
"s_add_u32 s74,s74,4096\nM v[52:55],v[212:215],a[52:55],v[52:55]\nM v[112:115],v[208:211],a[40:43],v[112:115]\ns_waitcnt vmcnt(23) lgkmcnt(0)\ns_barrier\nM v[112:115],v[212:215],a[56:59],v[112:115]\nds_read_b64_tr_b16 v[192:193],v241 offset:32768\nds_read_b64_tr_b16 v[194:195],v241 offset:33024\n" \
"M v[116:119],v[208:211],a[44:47],v[116:119]\nds_read_b64_tr_b16 v[196:197],v241 offset:40960\nds_read_b64_tr_b16 v[198:199],v241 offset:41216\nM v[116:119],v[212:215],a[60:63],v[116:119]\nds_read_b64_tr_b16 v[200:201],v242 offset:32768\nds_read_b64_tr_b16 v[202:203],v242 offset:33024\nM v[56:59],v[216:219],a[32:35],v[56:59]\nds_read_b64_tr_b16 v[204:205],v242 offset:40960\n" \
"ds_read_b64_tr_b16 v[206:207],v242 offset:41216\nM v[56:59],v[220:223],a[48:51],v[56:59]\nds_read_b64_tr_b16 v[208:209],v241 offset:33280\nds_read_b64_tr_b16 v[210:211],v241 offset:33536\nM v[60:63],v[216:219],a[36:39],v[60:63]\nds_read_b64_tr_b16 v[212:213],v241 offset:41472\nds_read_b64_tr_b16 v[214:215],v241 offset:41728\nM v[60:63],v[220:223],a[52:55],v[60:63]\n" \
"buffer_load_dwordx4 v243,s[40:43],s69 offen offset:0 lds\nM v[120:123],v[216:219],a[40:43],v[120:123]\nM v[120:123],v[220:223],a[56:59],v[120:123]\nM v[124:127],v[216:219],a[44:47],v[124:127]\nds_read_b64_tr_b16 v[216:217],v242 offset:33280\nds_read_b64_tr_b16 v[218:219],v242 offset:33536\nM v[124:127],v[220:223],a[60:63],v[124:127]\nds_read_b64_tr_b16 v[220:221],v242 offset:41472\n" \
"s_waitcnt lgkmcnt(13)\nM v[0:3],v[192:195],a[64:67],v[0:3]\nds_read_b64_tr_b16 v[222:223],v242 offset:41728\ns_waitcnt lgkmcnt(12)\nM v[0:3],v[196:199],a[80:83],v[0:3]\nbuffer_load_dwordx4 v244,s[40:43],s69 offen offset:1024 lds\nM v[4:7],v[192:195],a[68:71],v[4:7]\nM v[4:7],v[196:199],a[84:87],v[4:7]\n" \
"M v[64:67],v[192:195],a[72:75],v[64:67]\nM v[64:67],v[196:199],a[88:91],v[64:67]\nbuffer_load_dwordx4 v245,s[40:43],s69 offen offset:2048 lds\nM v[68:71],v[192:195],a[76:79],v[68:71]\nds_read_b64_tr_b16 v[192:193],v241 offset:33792\nds_read_b64_tr_b16 v[194:195],v241 offset:34048\nM v[68:71],v[196:199],a[92:95],v[68:71]\nds_read_b64_tr_b16 v[196:197],v241 offset:41984\n" \
"s_waitcnt lgkmcnt(7)\nM v[8:11],v[200:203],a[64:67],v[8:11]\nds_read_b64_tr_b16 v[198:199],v241 offset:42240\nM v[8:11],v[204:207],a[80:83],v[8:11]\nbuffer_load_dwordx4 v246,s[40:43],s69 offen offset:3072 lds\nM v[12:15],v[200:203],a[68:71],v[12:15]\ns_add_u32 m0,s64,114688\ns_add_u32 s69,s68,2621440\n" \
"M v[12:15],v[204:207],a[84:87],v[12:15]\nM v[72:75],v[200:203],a[72:75],v[72:75]\nM v[72:75],v[204:207],a[88:91],v[72:75]\nbuffer_store_dwordx4 v[144:147],v240,s[52:55],s74 offen offset:0 sc1\nM v[76:79],v[200:203],a[76:79],v[76:79]\nds_read_b64_tr_b16 v[200:201],v242 offset:33792\nds_read_b64_tr_b16 v[202:203],v242 offset:34048\nM v[76:79],v[204:207],a[92:95],v[76:79]\n" \
"ds_read_b64_tr_b16 v[204:205],v242 offset:41984\nds_read_b64_tr_b16 v[206:207],v242 offset:42240\nM v[16:19],v[208:211],a[64:67],v[16:19]\nM v[16:19],v[212:215],a[80:83],v[16:19]\nbuffer_load_dwordx4 a[160:163],v240,s[52:55],s76 offen offset:0 sc1\nM v[20:23],v[208:211],a[68:71],v[20:23]\nM v[20:23],v[212:215],a[84:87],v[20:23]\nM v[80:83],v[208:211],a[72:75],v[80:83]\n" \
"M v[80:83],v[212:215],a[88:91],v[80:83]\nbuffer_load_dwordx4 a[176:179],v240,s[52:55],s76 offen offset:2048 sc1\nM v[84:87],v[208:211],a[76:79],v[84:87]\nds_read_b64_tr_b16 v[208:209],v241 offset:34304\nds_read_b64_tr_b16 v[210:211],v241 offset:34560\nM v[84:87],v[212:215],a[92:95],v[84:87]\nds_read_b64_tr_b16 v[212:213],v241 offset:42496\ns_waitcnt lgkmcnt(7)\n" \
"M v[24:27],v[216:219],a[64:67],v[24:27]\nds_read_b64_tr_b16 v[214:215],v241 offset:42752\nM v[24:27],v[220:223],a[80:83],v[24:27]\nbuffer_load_dwordx4 a[164:167],v240,s[52:55],s76 offen offset:1024 sc1\nM v[28:31],v[216:219],a[68:71],v[28:31]\nM v[28:31],v[220:223],a[84:87],v[28:31]\nM v[88:91],v[216:219],a[72:75],v[88:91]\nM v[88:91],v[220:223],a[88:91],v[88:91]\n" \
"buffer_load_dwordx4 a[180:183],v240,s[52:55],s76 offen offset:3072 sc1\nM v[92:95],v[216:219],a[76:79],v[92:95]\nds_read_b64_tr_b16 v[216:217],v242 offset:34304\nds_read_b64_tr_b16 v[218:219],v242 offset:34560\nM v[92:95],v[220:223],a[92:95],v[92:95]\nds_read_b64_tr_b16 v[220:221],v242 offset:42496\nds_read_b64_tr_b16 v[222:223],v242 offset:42752\nM v[32:35],v[192:195],a[64:67],v[32:35]\n" \
"s_add_u32 s76,s76,4096\nM v[32:35],v[196:199],a[80:83],v[32:35]\nbuffer_load_dwordx4 a[168:171],v240,s[52:55],s77 offen offset:0 sc1\nM v[36:39],v[192:195],a[68:71],v[36:39]\nM v[36:39],v[196:199],a[84:87],v[36:39]\nM v[96:99],v[192:195],a[72:75],v[96:99]\nM v[96:99],v[196:199],a[88:91],v[96:99]\nbuffer_load_dwordx4 a[184:187],v240,s[52:55],s77 offen offset:2048 sc1\n" \
"M v[100:103],v[192:195],a[76:79],v[100:103]\nM v[100:103],v[196:199],a[92:95],v[100:103]\ns_waitcnt lgkmcnt(4)\nM v[40:43],v[200:203],a[64:67],v[40:43]\nM v[40:43],v[204:207],a[80:83],v[40:43]\nbuffer_load_dwordx4 a[172:175],v240,s[52:55],s77 offen offset:1024 sc1\nM v[44:47],v[200:203],a[68:71],v[44:47]\nM v[44:47],v[204:207],a[84:87],v[44:47]\n" \
"M v[104:107],v[200:203],a[72:75],v[104:107]\nM v[104:107],v[204:207],a[88:91],v[104:107]\nbuffer_store_dwordx4 v[148:151],v240,s[52:55],s74 offen offset:1024 sc1\nM v[108:111],v[200:203],a[76:79],v[108:111]\nM v[108:111],v[204:207],a[92:95],v[108:111]\nM v[48:51],v[208:211],a[64:67],v[48:51]\nM v[48:51],v[212:215],a[80:83],v[48:51]\nbuffer_load_dwordx4 a[188:191],v240,s[52:55],s77 offen offset:3072 sc1\n" \
"M v[52:55],v[208:211],a[68:71],v[52:55]\ns_add_u32 s77,s77,4096\nM v[52:55],v[212:215],a[84:87],v[52:55]\nM v[112:115],v[208:211],a[72:75],v[112:115]\ns_waitcnt vmcnt(23) lgkmcnt(0)\ns_barrier\nM v[112:115],v[212:215],a[88:91],v[112:115]\nds_read_b64_tr_b16 v[192:193],v241 offset:49152\n" \
"ds_read_b64_tr_b16 v[194:195],v241 offset:49408\nM v[116:119],v[208:211],a[76:79],v[116:119]\nds_read_b64_tr_b16 v[196:197],v241 offset:57344\nds_read_b64_tr_b16 v[198:199],v241 offset:57600\nM v[116:119],v[212:215],a[92:95],v[116:119]\nds_read_b64_tr_b16 v[200:201],v242 offset:49152\nds_read_b64_tr_b16 v[202:203],v242 offset:49408\nM v[56:59],v[216:219],a[64:67],v[56:59]\n" \
"ds_read_b64_tr_b16 v[204:205],v242 offset:57344\nds_read_b64_tr_b16 v[206:207],v242 offset:57600\nM v[56:59],v[220:223],a[80:83],v[56:59]\nds_read_b64_tr_b16 v[208:209],v241 offset:49664\nds_read_b64_tr_b16 v[210:211],v241 offset:49920\nM v[60:63],v[216:219],a[68:71],v[60:63]\nds_read_b64_tr_b16 v[212:213],v241 offset:57856\nds_read_b64_tr_b16 v[214:215],v241 offset:58112\n" \
"M v[60:63],v[220:223],a[84:87],v[60:63]\nbuffer_load_dwordx4 v243,s[40:43],s69 offen offset:0 lds\nM v[120:123],v[216:219],a[72:75],v[120:123]\nM v[120:123],v[220:223],a[88:91],v[120:123]\nM v[124:127],v[216:219],a[76:79],v[124:127]\nds_read_b64_tr_b16 v[216:217],v242 offset:49664\nds_read_b64_tr_b16 v[218:219],v242 offset:49920\nM v[124:127],v[220:223],a[92:95],v[124:127]\n" \
"ds_read_b64_tr_b16 v[220:221],v242 offset:57856\ns_waitcnt lgkmcnt(13)\nM v[0:3],v[192:195],a[96:99],v[0:3]\nds_read_b64_tr_b16 v[222:223],v242 offset:58112\ns_waitcnt lgkmcnt(12)\nM v[0:3],v[196:199],a[112:115],v[0:3]\nbuffer_load_dwordx4 v244,s[40:43],s69 offen offset:1024 lds\nM v[4:7],v[192:195],a[100:103],v[4:7]\n" \
"M v[4:7],v[196:199],a[116:119],v[4:7]\nM v[64:67],v[192:195],a[104:107],v[64:67]\nM v[64:67],v[196:199],a[120:123],v[64:67]\nbuffer_load_dwordx4 v245,s[40:43],s69 offen offset:2048 lds\nM v[68:71],v[192:195],a[108:111],v[68:71]\nds_read_b64_tr_b16 v[192:193],v241 offset:50176\nds_read_b64_tr_b16 v[194:195],v241 offset:50432\nM v[68:71],v[196:199],a[124:127],v[68:71]\n" \
"ds_read_b64_tr_b16 v[196:197],v241 offset:58368\ns_waitcnt lgkmcnt(7)\nM v[8:11],v[200:203],a[96:99],v[8:11]\nds_read_b64_tr_b16 v[198:199],v241 offset:58624\nM v[8:11],v[204:207],a[112:115],v[8:11]\nbuffer_store_dwordx4 v[152:155],v240,s[52:55],s74 offen offset:2048 sc1\nM v[12:15],v[200:203],a[100:103],v[12:15]\nM v[12:15],v[204:207],a[116:119],v[12:15]\n" \
"M v[72:75],v[200:203],a[104:107],v[72:75]\nM v[72:75],v[204:207],a[120:123],v[72:75]\nbuffer_load_dwordx4 v246,s[40:43],s69 offen offset:3072 lds\nM v[76:79],v[200:203],a[108:111],v[76:79]\nds_read_b64_tr_b16 v[200:201],v242 offset:50176\nds_read_b64_tr_b16 v[202:203],v242 offset:50432\nM v[76:79],v[204:207],a[124:127],v[76:79]\nds_read_b64_tr_b16 v[204:205],v242 offset:58368\n" \
"ds_read_b64_tr_b16 v[206:207],v242 offset:58624\nM v[16:19],v[208:211],a[96:99],v[16:19]\ns_add_u32 m0,s64,131072\ns_add_u32 s69,s68,3145728\nM v[16:19],v[212:215],a[112:115],v[16:19]\nbuffer_load_dwordx4 a[192:195],v240,s[52:55],s76 offen offset:0 sc1\nM v[20:23],v[208:211],a[100:103],v[20:23]\nM v[20:23],v[212:215],a[116:119],v[20:23]\n" \
"M v[80:83],v[208:211],a[104:107],v[80:83]\nM v[80:83],v[212:215],a[120:123],v[80:83]\nbuffer_load_dwordx4 a[208:211],v240,s[52:55],s76 offen offset:2048 sc1\nM v[84:87],v[208:211],a[108:111],v[84:87]\nds_read_b64_tr_b16 v[208:209],v241 offset:50688\nds_read_b64_tr_b16 v[210:211],v241 offset:50944\nM v[84:87],v[212:215],a[124:127],v[84:87]\nds_read_b64_tr_b16 v[212:213],v241 offset:58880\n" \
"s_waitcnt lgkmcnt(7)\nM v[24:27],v[216:219],a[96:99],v[24:27]\nds_read_b64_tr_b16 v[214:215],v241 offset:59136\nM v[24:27],v[220:223],a[112:115],v[24:27]\nbuffer_load_dwordx4 a[196:199],v240,s[52:55],s76 offen offset:1024 sc1\nM v[28:31],v[216:219],a[100:103],v[28:31]\nM v[28:31],v[220:223],a[116:119],v[28:31]\nM v[88:91],v[216:219],a[104:107],v[88:91]\n" \
"M v[88:91],v[220:223],a[120:123],v[88:91]\nbuffer_load_dwordx4 a[212:215],v240,s[52:55],s76 offen offset:3072 sc1\nM v[92:95],v[216:219],a[108:111],v[92:95]\nds_read_b64_tr_b16 v[216:217],v242 offset:50688\nds_read_b64_tr_b16 v[218:219],v242 offset:50944\nM v[92:95],v[220:223],a[124:127],v[92:95]\nds_read_b64_tr_b16 v[220:221],v242 offset:58880\nds_read_b64_tr_b16 v[222:223],v242 offset:59136\n" \
"M v[32:35],v[192:195],a[96:99],v[32:35]\ns_add_u32 s76,s76,4096\nM v[32:35],v[196:199],a[112:115],v[32:35]\nbuffer_load_dwordx4 a[200:203],v240,s[52:55],s77 offen offset:0 sc1\nM v[36:39],v[192:195],a[100:103],v[36:39]\nM v[36:39],v[196:199],a[116:119],v[36:39]\nM v[96:99],v[192:195],a[104:107],v[96:99]\nM v[96:99],v[196:199],a[120:123],v[96:99]\n" \
"buffer_load_dwordx4 a[216:219],v240,s[52:55],s77 offen offset:2048 sc1\nM v[100:103],v[192:195],a[108:111],v[100:103]\nM v[100:103],v[196:199],a[124:127],v[100:103]\ns_waitcnt lgkmcnt(4)\nM v[40:43],v[200:203],a[96:99],v[40:43]\nM v[40:43],v[204:207],a[112:115],v[40:43]\nbuffer_store_dwordx4 v[156:159],v240,s[52:55],s74 offen offset:3072 sc1\nM v[44:47],v[200:203],a[100:103],v[44:47]\n" \
"s_add_u32 s74,s74,4096\nM v[44:47],v[204:207],a[116:119],v[44:47]\nM v[104:107],v[200:203],a[104:107],v[104:107]\nM v[104:107],v[204:207],a[120:123],v[104:107]\nbuffer_load_dwordx4 a[204:207],v240,s[52:55],s77 offen offset:1024 sc1\nM v[108:111],v[200:203],a[108:111],v[108:111]\nM v[108:111],v[204:207],a[124:127],v[108:111]\nM v[48:51],v[208:211],a[96:99],v[48:51]\n" \
"M v[48:51],v[212:215],a[112:115],v[48:51]\nbuffer_load_dwordx4 a[220:223],v240,s[52:55],s77 offen offset:3072 sc1\nM v[52:55],v[208:211],a[100:103],v[52:55]\ns_add_u32 s77,s77,4096\nM v[52:55],v[212:215],a[116:119],v[52:55]\nM v[112:115],v[208:211],a[104:107],v[112:115]\ns_waitcnt vmcnt(24) lgkmcnt(0)\ns_barrier\n" \
"M v[112:115],v[212:215],a[120:123],v[112:115]\nds_read_b64_tr_b16 v[192:193],v241 offset:0\nds_read_b64_tr_b16 v[194:195],v241 offset:256\nM v[116:119],v[208:211],a[108:111],v[116:119]\nds_read_b64_tr_b16 v[196:197],v241 offset:8192\nds_read_b64_tr_b16 v[198:199],v241 offset:8448\nM v[116:119],v[212:215],a[124:127],v[116:119]\nds_read_b64_tr_b16 v[200:201],v242 offset:0\n" \
"ds_read_b64_tr_b16 v[202:203],v242 offset:256\nM v[56:59],v[216:219],a[96:99],v[56:59]\nds_read_b64_tr_b16 v[204:205],v242 offset:8192\nds_read_b64_tr_b16 v[206:207],v242 offset:8448\nM v[56:59],v[220:223],a[112:115],v[56:59]\nds_read_b64_tr_b16 v[208:209],v241 offset:512\nds_read_b64_tr_b16 v[210:211],v241 offset:768\nM v[60:63],v[216:219],a[100:103],v[60:63]\n" \
"ds_read_b64_tr_b16 v[212:213],v241 offset:8704\nds_read_b64_tr_b16 v[214:215],v241 offset:8960\nM v[60:63],v[220:223],a[116:119],v[60:63]\nbuffer_load_dwordx4 v243,s[40:43],s69 offen offset:0 lds\nM v[120:123],v[216:219],a[104:107],v[120:123]\nM v[120:123],v[220:223],a[120:123],v[120:123]\nM v[124:127],v[216:219],a[108:111],v[124:127]\nds_read_b64_tr_b16 v[216:217],v242 offset:512\n" \
"ds_read_b64_tr_b16 v[218:219],v242 offset:768\nM v[124:127],v[220:223],a[124:127],v[124:127]\nds_read_b64_tr_b16 v[220:221],v242 offset:8704\ns_waitcnt lgkmcnt(13)\nM v[0:3],v[192:195],a[128:131],v[0:3]\nds_read_b64_tr_b16 v[222:223],v242 offset:8960\ns_waitcnt lgkmcnt(12)\nM v[0:3],v[196:199],a[144:147],v[0:3]\n" \
"buffer_load_dwordx4 v244,s[40:43],s69 offen offset:1024 lds\nM v[4:7],v[192:195],a[132:135],v[4:7]\nM v[4:7],v[196:199],a[148:151],v[4:7]\nM v[64:67],v[192:195],a[136:139],v[64:67]\nM v[64:67],v[196:199],a[152:155],v[64:67]\nbuffer_store_dwordx4 v[160:163],v240,s[52:55],s74 offen offset:0 sc1\nM v[68:71],v[192:195],a[140:143],v[68:71]\nds_read_b64_tr_b16 v[192:193],v241 offset:1024\n" \
"ds_read_b64_tr_b16 v[194:195],v241 offset:1280\nM v[68:71],v[196:199],a[156:159],v[68:71]\nds_read_b64_tr_b16 v[196:197],v241 offset:9216\ns_waitcnt lgkmcnt(7)\nM v[8:11],v[200:203],a[128:131],v[8:11]\nds_read_b64_tr_b16 v[198:199],v241 offset:9472\nM v[8:11],v[204:207],a[144:147],v[8:11]\nbuffer_load_dwordx4 v245,s[40:43],s69 offen offset:2048 lds\n" \
"M v[12:15],v[200:203],a[132:135],v[12:15]\nM v[12:15],v[204:207],a[148:151],v[12:15]\nM v[72:75],v[200:203],a[136:139],v[72:75]\nM v[72:75],v[204:207],a[152:155],v[72:75]\nbuffer_load_dwordx4 v246,s[40:43],s69 offen offset:3072 lds\nM v[76:79],v[200:203],a[140:143],v[76:79]\nds_read_b64_tr_b16 v[200:201],v242 offset:1024\nds_read_b64_tr_b16 v[202:203],v242 offset:1280\n" \
"M v[76:79],v[204:207],a[156:159],v[76:79]\nds_read_b64_tr_b16 v[204:205],v242 offset:9216\nds_read_b64_tr_b16 v[206:207],v242 offset:9472\nM v[16:19],v[208:211],a[128:131],v[16:19]\ns_add_u32 m0,s64,147456\ns_add_u32 s69,s68,3670016\nM v[16:19],v[212:215],a[144:147],v[16:19]\nbuffer_load_dwordx4 a[224:227],v240,s[52:55],s76 offen offset:0 sc1\n" \
"M v[20:23],v[208:211],a[132:135],v[20:23]\ns_lshl_b32 s68,s60,19\ns_lshl2_add_u32 s68,s64,s68\nM v[20:23],v[212:215],a[148:151],v[20:23]\ns_add_u32 s68,s68,s70\nM v[80:83],v[208:211],a[136:139],v[80:83]\nds_read_b128 a[0:3],v240 offset:0\nM v[80:83],v[212:215],a[152:155],v[80:83]\n" \
"buffer_load_dword v248,v228,s[52:55],s80 offen sc1\nM v[84:87],v[208:211],a[140:143],v[84:87]\nds_read_b64_tr_b16 v[208:209],v241 offset:1536\nds_read_b64_tr_b16 v[210:211],v241 offset:1792\nM v[84:87],v[212:215],a[156:159],v[84:87]\nds_read_b64_tr_b16 v[212:213],v241 offset:9728\ns_waitcnt lgkmcnt(8)\nM v[24:27],v[216:219],a[128:131],v[24:27]\n" \
"buffer_load_dwordx4 a[240:243],v240,s[52:55],s76 offen offset:2048 sc1\nM v[24:27],v[220:223],a[144:147],v[24:27]\nds_read_b64_tr_b16 v[214:215],v241 offset:9984\nM v[28:31],v[216:219],a[132:135],v[28:31]\nds_read_b128 a[4:7],v240 offset:1024\nM v[28:31],v[220:223],a[148:151],v[28:31]\nM v[88:91],v[216:219],a[136:139],v[88:91]\nbuffer_load_dwordx4 a[228:231],v240,s[52:55],s76 offen offset:1024 sc1\n" \
"M v[88:91],v[220:223],a[152:155],v[88:91]\nM v[92:95],v[216:219],a[140:143],v[92:95]\nds_read_b64_tr_b16 v[216:217],v242 offset:1536\nds_read_b64_tr_b16 v[218:219],v242 offset:1792\nM v[92:95],v[220:223],a[156:159],v[92:95]\nds_read_b64_tr_b16 v[220:221],v242 offset:9728\nds_read_b64_tr_b16 v[222:223],v242 offset:9984\nM v[32:35],v[192:195],a[128:131],v[32:35]\n" \
"buffer_load_dwordx4 a[244:247],v240,s[52:55],s76 offen offset:3072 sc1\nM v[32:35],v[196:199],a[144:147],v[32:35]\nds_read_b128 a[8:11],v240 offset:2048\nM v[36:39],v[192:195],a[132:135],v[36:39]\nM v[36:39],v[196:199],a[148:151],v[36:39]\nM v[96:99],v[192:195],a[136:139],v[96:99]\nbuffer_store_dwordx4 v[164:167],v240,s[52:55],s74 offen offset:1024 sc1\nM v[96:99],v[196:199],a[152:155],v[96:99]\n" \
"M v[100:103],v[192:195],a[140:143],v[100:103]\nds_read_b128 a[12:15],v240 offset:3072\nM v[100:103],v[196:199],a[156:159],v[100:103]\ns_waitcnt lgkmcnt(7)\nM v[40:43],v[200:203],a[128:131],v[40:43]\nbuffer_load_dwordx4 a[232:235],v240,s[52:55],s77 offen offset:0 sc1\nM v[40:43],v[204:207],a[144:147],v[40:43]\nM v[44:47],v[200:203],a[132:135],v[44:47]\n" \
"M v[44:47],v[204:207],a[148:151],v[44:47]\nM v[104:107],v[200:203],a[136:139],v[104:107]\nbuffer_load_dwordx4 a[248:251],v240,s[52:55],s77 offen offset:2048 sc1\nM v[104:107],v[204:207],a[152:155],v[104:107]\nds_read_b128 a[16:19],v240 offset:4096\nM v[108:111],v[200:203],a[140:143],v[108:111]\nM v[108:111],v[204:207],a[156:159],v[108:111]\nM v[48:51],v[208:211],a[128:131],v[48:51]\n" \
"buffer_load_dwordx4 a[236:239],v240,s[52:55],s77 offen offset:1024 sc1\nM v[48:51],v[212:215],a[144:147],v[48:51]\nM v[52:55],v[208:211],a[132:135],v[52:55]\nds_read_b128 a[20:23],v240 offset:5120\nM v[52:55],v[212:215],a[148:151],v[52:55]\nM v[112:115],v[208:211],a[136:139],v[112:115]\ns_waitcnt vmcnt(17) lgkmcnt(3)\ns_barrier\n" \
"M v[112:115],v[212:215],a[152:155],v[112:115]\nds_read_b64_tr_b16 v[192:193],v241 offset:16384\nds_read_b64_tr_b16 v[194:195],v241 offset:16640\nM v[116:119],v[208:211],a[140:143],v[116:119]\nds_read_b64_tr_b16 v[196:197],v241 offset:24576\nds_read_b64_tr_b16 v[198:199],v241 offset:24832\nM v[116:119],v[212:215],a[156:159],v[116:119]\nds_read_b64_tr_b16 v[200:201],v242 offset:16384\n" \
"ds_read_b64_tr_b16 v[202:203],v242 offset:16640\nM v[56:59],v[216:219],a[128:131],v[56:59]\nds_read_b64_tr_b16 v[204:205],v242 offset:24576\nds_read_b64_tr_b16 v[206:207],v242 offset:24832\nM v[56:59],v[220:223],a[144:147],v[56:59]\nds_read_b64_tr_b16 v[208:209],v241 offset:16896\nds_read_b64_tr_b16 v[210:211],v241 offset:17152\nM v[60:63],v[216:219],a[132:135],v[60:63]\n" \
"ds_read_b64_tr_b16 v[212:213],v241 offset:25088\nds_read_b64_tr_b16 v[214:215],v241 offset:25344\nM v[60:63],v[220:223],a[148:151],v[60:63]\nbuffer_load_dwordx4 a[252:255],v240,s[52:55],s77 offen offset:3072 sc1\nM v[120:123],v[216:219],a[136:139],v[120:123]\nds_read_b128 a[24:27],v240 offset:6144\nM v[120:123],v[220:223],a[152:155],v[120:123]\nM v[124:127],v[216:219],a[140:143],v[124:127]\n" \
"ds_read_b64_tr_b16 v[216:217],v242 offset:16896\nds_read_b64_tr_b16 v[218:219],v242 offset:17152\nM v[124:127],v[220:223],a[156:159],v[124:127]\nds_read_b64_tr_b16 v[220:221],v242 offset:25088\ns_waitcnt lgkmcnt(14)\nM v[0:3],v[192:195],a[160:163],v[0:3]\nds_read_b64_tr_b16 v[222:223],v242 offset:25344\ns_waitcnt lgkmcnt(13)\n" \
"M v[0:3],v[196:199],a[176:179],v[0:3]\nbuffer_store_dwordx4 v[168:171],v240,s[52:55],s74 offen offset:2048 sc1\nM v[4:7],v[192:195],a[164:167],v[4:7]\nds_read_b128 a[28:31],v240 offset:7168\nM v[4:7],v[196:199],a[180:183],v[4:7]\nM v[64:67],v[192:195],a[168:171],v[64:67]\nds_read_b128 a[32:35],v240 offset:8192\nM v[64:67],v[196:199],a[184:187],v[64:67]\n" \
"buffer_load_dwordx4 v243,s[40:43],s69 offen offset:0 lds\nM v[68:71],v[192:195],a[172:175],v[68:71]\nds_read_b64_tr_b16 v[192:193],v241 offset:17408\nds_read_b64_tr_b16 v[194:195],v241 offset:17664\nM v[68:71],v[196:199],a[188:191],v[68:71]\nds_read_b64_tr_b16 v[196:197],v241 offset:25600\ns_waitcnt lgkmcnt(10)\nM v[8:11],v[200:203],a[160:163],v[8:11]\n" \
"ds_read_b64_tr_b16 v[198:199],v241 offset:25856\nM v[8:11],v[204:207],a[176:179],v[8:11]\nbuffer_load_dwordx4 v244,s[40:43],s69 offen offset:1024 lds\nM v[12:15],v[200:203],a[164:167],v[12:15]\nds_read_b128 a[36:39],v240 offset:9216\nM v[12:15],v[204:207],a[180:183],v[12:15]\nM v[72:75],v[200:203],a[168:171],v[72:75]\nM v[72:75],v[204:207],a[184:187],v[72:75]\n" \
"buffer_load_dwordx4 v245,s[40:43],s69 offen offset:2048 lds\nM v[76:79],v[200:203],a[172:175],v[76:79]\nds_read_b64_tr_b16 v[200:201],v242 offset:17408\nds_read_b64_tr_b16 v[202:203],v242 offset:17664\nM v[76:79],v[204:207],a[188:191],v[76:79]\nds_read_b64_tr_b16 v[204:205],v242 offset:25600\nds_read_b64_tr_b16 v[206:207],v242 offset:25856\nM v[16:19],v[208:211],a[160:163],v[16:19]\n" \
"ds_read_b128 a[40:43],v240 offset:10240\nM v[16:19],v[212:215],a[176:179],v[16:19]\nbuffer_load_dwordx4 v246,s[40:43],s69 offen offset:3072 lds\nM v[20:23],v[208:211],a[164:167],v[20:23]\ns_sub_u32 s40,s18,0x1000\ns_subb_u32 s41,s19,0\nM v[20:23],v[212:215],a[180:183],v[20:23]\ns_waitcnt vmcnt(13)\n" \
"v_cmp_eq_u32 vcc,0,v248\nM v[80:83],v[208:211],a[168:171],v[80:83]\ns_cbranch_vccnz .Lps1\n.Lpb1:\ns_add_u32 s80,s80,4096\nM v[80:83],v[212:215],a[184:187],v[80:83]\ns_cmp_ge_u32 s62,6\ns_cselect_b64 vcc,-1,0\n" \
"M v[84:87],v[208:211],a[172:175],v[84:87]\nds_read_b64_tr_b16 v[208:209],v241 offset:17920\nds_read_b64_tr_b16 v[210:211],v241 offset:18176\nM v[84:87],v[212:215],a[188:191],v[84:87]\nds_read_b64_tr_b16 v[212:213],v241 offset:26112\ns_waitcnt lgkmcnt(8)\nM v[24:27],v[216:219],a[160:163],v[24:27]\nds_read_b64_tr_b16 v[214:215],v241 offset:26368\n" \
"v_cndmask_b32 v254,v231,v229,vcc\nM v[24:27],v[220:223],a[176:179],v[24:27]\ns_cmp_eq_u32 s62,3\ns_cselect_b64 vcc,-1,0\nM v[28:31],v[216:219],a[164:167],v[28:31]\nv_cndmask_b32 v254,v254,v230,vcc\nv_bfe_u32 v230,v249,2,3\nM v[28:31],v[220:223],a[180:183],v[28:31]\n" \
"v_and_b32 v231,0x70,v243\nv_lshl_or_b32 v230,v230,10,v231\nM v[88:91],v[216:219],a[168:171],v[88:91]\nv_and_b32 v231,16,v249\nv_lshlrev_b32 v231,1,v231\nM v[88:91],v[220:223],a[184:187],v[88:91]\nv_xor_b32 v230,v230,v231\nv_add_u32 v243,4096,v230\n" \
"M v[92:95],v[216:219],a[172:175],v[92:95]\nbuffer_store_dwordx4 v[172:175],v240,s[52:55],s74 offen offset:3072 sc1\nM v[92:95],v[220:223],a[188:191],v[92:95]\nds_read_b64_tr_b16 v[216:217],v242 offset:17920\nds_read_b64_tr_b16 v[218:219],v242 offset:18176\nM v[32:35],v[192:195],a[160:163],v[32:35]\nds_read_b64_tr_b16 v[220:221],v242 offset:26112\nds_read_b64_tr_b16 v[222:223],v242 offset:26368\n" \
"M v[32:35],v[196:199],a[176:179],v[32:35]\ns_add_u32 s74,s74,4096\nv_add_u32 v244,3200,v230\nM v[36:39],v[192:195],a[164:167],v[36:39]\nv_add_u32 v245,10240,v230\nv_add_u32 v246,9344,v230\nM v[36:39],v[196:199],a[180:183],v[36:39]\nv_and_b32 v229,3,v249\n" \
"v_cmp_eq_u32 vcc,0,v229\nM v[96:99],v[192:195],a[168:171],v[96:99]\nv_mov_b32 v230,0x3f803f80\nv_cndmask_b32 v229,0,v230,vcc\nM v[96:99],v[196:199],a[184:187],v[96:99]\nv_and_b32 v230,0xff,v241\nv_and_b32 v231,16,v249\nM v[100:103],v[192:195],a[172:175],v[100:103]\n" \
"v_lshl_or_b32 v230,v231,4,v230\nv_and_b32 v231,32,v249\nM v[100:103],v[196:199],a[188:191],v[100:103]\nds_read_b128 a[44:47],v240 offset:11264\ns_waitcnt lgkmcnt(5)\nM v[40:43],v[200:203],a[160:163],v[40:43]\nds_read_b128 a[48:51],v240 offset:12288\nds_read_b128 a[52:55],v240 offset:13312\n" \
"M v[40:43],v[204:207],a[176:179],v[40:43]\nds_read_b128 a[56:59],v240 offset:14336\nM v[44:47],v[200:203],a[164:167],v[44:47]\nM v[44:47],v[204:207],a[180:183],v[44:47]\nM v[104:107],v[200:203],a[168:171],v[104:107]\nM v[104:107],v[204:207],a[184:187],v[104:107]\nM v[108:111],v[200:203],a[172:175],v[108:111]\nds_read_b128 a[60:63],v240 offset:15360\n" \
"M v[108:111],v[204:207],a[188:191],v[108:111]\nM v[48:51],v[208:211],a[160:163],v[48:51]\nM v[48:51],v[212:215],a[176:179],v[48:51]\nM v[52:55],v[208:211],a[164:167],v[52:55]\nM v[52:55],v[212:215],a[180:183],v[52:55]\nM v[112:115],v[208:211],a[168:171],v[112:115]\ns_waitcnt lgkmcnt(5)\ns_barrier\n" \
"M v[112:115],v[212:215],a[184:187],v[112:115]\nds_read_b64_tr_b16 v[192:193],v241 offset:32768\nds_read_b64_tr_b16 v[194:195],v241 offset:33024\nM v[116:119],v[208:211],a[172:175],v[116:119]\nds_read_b64_tr_b16 v[196:197],v241 offset:40960\nds_read_b64_tr_b16 v[198:199],v241 offset:41216\nM v[116:119],v[212:215],a[188:191],v[116:119]\nds_read_b64_tr_b16 v[200:201],v242 offset:32768\n" \
"ds_read_b64_tr_b16 v[202:203],v242 offset:33024\nM v[56:59],v[216:219],a[160:163],v[56:59]\nds_read_b64_tr_b16 v[204:205],v242 offset:40960\nds_read_b64_tr_b16 v[206:207],v242 offset:41216\nM v[56:59],v[220:223],a[176:179],v[56:59]\nds_read_b64_tr_b16 v[208:209],v241 offset:33280\nds_read_b64_tr_b16 v[210:211],v241 offset:33536\nM v[60:63],v[216:219],a[164:167],v[60:63]\n" \
"ds_read_b64_tr_b16 v[212:213],v241 offset:41472\nds_read_b64_tr_b16 v[214:215],v241 offset:41728\nM v[60:63],v[220:223],a[180:183],v[60:63]\nds_write_b64 v250,v[128:129] offset:0\nM v[120:123],v[216:219],a[168:171],v[120:123]\nbuffer_store_dwordx4 v[176:179],v240,s[52:55],s74 offen offset:0 sc1\nM v[120:123],v[220:223],a[184:187],v[120:123]\nds_write_b64 v250,v[130:131] offset:8\n" \
"M v[124:127],v[216:219],a[172:175],v[124:127]\nds_read_b64_tr_b16 v[216:217],v242 offset:33280\nds_read_b64_tr_b16 v[218:219],v242 offset:33536\nM v[124:127],v[220:223],a[188:191],v[124:127]\nds_read_b64_tr_b16 v[220:221],v242 offset:41472\ns_waitcnt lgkmcnt(15)\nM v[0:3],v[192:195],a[192:195],v[0:3]\nds_read_b64_tr_b16 v[222:223],v242 offset:41728\n" \
"s_waitcnt lgkmcnt(14)\nM v[0:3],v[196:199],a[208:211],v[0:3]\nds_write_b64 v250,v[132:133] offset:1024\nM v[4:7],v[192:195],a[196:199],v[4:7]\nds_write_b64 v250,v[134:135] offset:1032\nM v[4:7],v[196:199],a[212:215],v[4:7]\nds_write_b64 v250,v[136:137] offset:2048\nM v[64:67],v[192:195],a[200:203],v[64:67]\n" \
"M v[64:67],v[196:199],a[216:219],v[64:67]\nds_write_b64 v250,v[138:139] offset:2056\nM v[68:71],v[192:195],a[204:207],v[68:71]\nds_read_b64_tr_b16 v[192:193],v241 offset:33792\nds_read_b64_tr_b16 v[194:195],v241 offset:34048\nM v[68:71],v[196:199],a[220:223],v[68:71]\nds_read_b64_tr_b16 v[196:197],v241 offset:41984\ns_waitcnt lgkmcnt(12)\n" \
"M v[8:11],v[200:203],a[192:195],v[8:11]\nds_write_b64 v250,v[140:141] offset:3072\nds_read_b64_tr_b16 v[198:199],v241 offset:42240\nM v[8:11],v[204:207],a[208:211],v[8:11]\nM v[12:15],v[200:203],a[196:199],v[12:15]\nM v[12:15],v[204:207],a[212:215],v[12:15]\nds_write_b64 v250,v[142:143] offset:3080\nM v[72:75],v[200:203],a[200:203],v[72:75]\n" \
"M v[72:75],v[204:207],a[216:219],v[72:75]\nM v[76:79],v[200:203],a[204:207],v[76:79]\nds_read_b64_tr_b16 v[200:201],v242 offset:33792\nds_read_b64_tr_b16 v[202:203],v242 offset:34048\nM v[76:79],v[204:207],a[220:223],v[76:79]\nds_read_b64_tr_b16 v[204:205],v242 offset:41984\nds_read_b64_tr_b16 v[206:207],v242 offset:42240\nM v[16:19],v[208:211],a[192:195],v[16:19]\n" \
"ds_write_b64 v250,v[144:145] offset:4096\nM v[16:19],v[212:215],a[208:211],v[16:19]\nds_write_b64 v250,v[146:147] offset:4104\nM v[20:23],v[208:211],a[196:199],v[20:23]\nM v[20:23],v[212:215],a[212:215],v[20:23]\nM v[80:83],v[208:211],a[200:203],v[80:83]\nds_write_b64 v250,v[148:149] offset:5120\nM v[80:83],v[212:215],a[216:219],v[80:83]\n" \
"M v[84:87],v[208:211],a[204:207],v[84:87]\nds_read_b64_tr_b16 v[208:209],v241 offset:34304\nds_read_b64_tr_b16 v[210:211],v241 offset:34560\nM v[84:87],v[212:215],a[220:223],v[84:87]\nds_read_b64_tr_b16 v[212:213],v241 offset:42496\ns_waitcnt lgkmcnt(11)\nM v[24:27],v[216:219],a[192:195],v[24:27]\nds_write_b64 v250,v[150:151] offset:5128\n" \
"ds_read_b64_tr_b16 v[214:215],v241 offset:42752\nM v[24:27],v[220:223],a[208:211],v[24:27]\nM v[28:31],v[216:219],a[196:199],v[28:31]\nbuffer_store_dwordx4 v[180:183],v240,s[52:55],s74 offen offset:1024 sc1\nM v[28:31],v[220:223],a[212:215],v[28:31]\nds_write_b64 v250,v[152:153] offset:6144\nM v[88:91],v[216:219],a[200:203],v[88:91]\nM v[88:91],v[220:223],a[216:219],v[88:91]\n" \
"ds_write_b64 v250,v[154:155] offset:6152\nM v[92:95],v[216:219],a[204:207],v[92:95]\nds_read_b64_tr_b16 v[216:217],v242 offset:34304\nds_read_b64_tr_b16 v[218:219],v242 offset:34560\nM v[92:95],v[220:223],a[220:223],v[92:95]\nds_read_b64_tr_b16 v[220:221],v242 offset:42496\nds_read_b64_tr_b16 v[222:223],v242 offset:42752\nM v[32:35],v[192:195],a[192:195],v[32:35]\n" \
"ds_write_b64 v250,v[156:157] offset:7168\nM v[32:35],v[196:199],a[208:211],v[32:35]\nM v[36:39],v[192:195],a[196:199],v[36:39]\nM v[36:39],v[196:199],a[212:215],v[36:39]\nds_write_b64 v250,v[158:159] offset:7176\nM v[96:99],v[192:195],a[200:203],v[96:99]\nM v[96:99],v[196:199],a[216:219],v[96:99]\nM v[100:103],v[192:195],a[204:207],v[100:103]\n" \
"ds_write_b64 v250,v[160:161] offset:8192\nM v[100:103],v[196:199],a[220:223],v[100:103]\ns_waitcnt lgkmcnt(9)\nM v[40:43],v[200:203],a[192:195],v[40:43]\nM v[40:43],v[204:207],a[208:211],v[40:43]\nds_write_b64 v250,v[162:163] offset:8200\nM v[44:47],v[200:203],a[196:199],v[44:47]\nM v[44:47],v[204:207],a[212:215],v[44:47]\n" \
"M v[104:107],v[200:203],a[200:203],v[104:107]\nds_write_b64 v250,v[164:165] offset:9216\nM v[104:107],v[204:207],a[216:219],v[104:107]\nM v[108:111],v[200:203],a[204:207],v[108:111]\nM v[108:111],v[204:207],a[220:223],v[108:111]\nds_write_b64 v250,v[166:167] offset:9224\nM v[48:51],v[208:211],a[192:195],v[48:51]\nM v[48:51],v[212:215],a[208:211],v[48:51]\n" \
"M v[52:55],v[208:211],a[196:199],v[52:55]\nds_write_b64 v250,v[168:169] offset:10240\nM v[52:55],v[212:215],a[212:215],v[52:55]\nM v[112:115],v[208:211],a[200:203],v[112:115]\ns_waitcnt vmcnt(3) lgkmcnt(5)\ns_barrier\nM v[112:115],v[212:215],a[216:219],v[112:115]\nds_read_b64_tr_b16 v[192:193],v241 offset:49152\n" \
"ds_read_b64_tr_b16 v[194:195],v241 offset:49408\nM v[116:119],v[208:211],a[204:207],v[116:119]\nds_read_b64_tr_b16 v[196:197],v241 offset:57344\nds_read_b64_tr_b16 v[198:199],v241 offset:57600\nM v[116:119],v[212:215],a[220:223],v[116:119]\nds_read_b64_tr_b16 v[200:201],v242 offset:49152\nds_read_b64_tr_b16 v[202:203],v242 offset:49408\nM v[56:59],v[216:219],a[192:195],v[56:59]\n" \
"buffer_store_dwordx4 v[184:187],v240,s[52:55],s74 offen offset:2048 sc1\nM v[56:59],v[220:223],a[208:211],v[56:59]\nds_read_b64_tr_b16 v[204:205],v242 offset:57344\nds_read_b64_tr_b16 v[206:207],v242 offset:57600\nM v[60:63],v[216:219],a[196:199],v[60:63]\nds_read_b64_tr_b16 v[208:209],v241 offset:49664\nds_read_b64_tr_b16 v[210:211],v241 offset:49920\nM v[60:63],v[220:223],a[212:215],v[60:63]\n" \
"ds_read_b64_tr_b16 v[212:213],v241 offset:57856\nds_read_b64_tr_b16 v[214:215],v241 offset:58112\nM v[120:123],v[216:219],a[200:203],v[120:123]\nds_write_b64 v250,v[170:171] offset:10248\nv_accvgpr_write_b32 a208,v229\nM v[120:123],v[220:223],a[216:219],v[120:123]\nds_write_b64 v250,v[172:173] offset:11264\nv_accvgpr_write_b32 a209,v229\n" \
"M v[124:127],v[216:219],a[204:207],v[124:127]\nds_read_b64_tr_b16 v[216:217],v242 offset:49664\nds_read_b64_tr_b16 v[218:219],v242 offset:49920\nM v[124:127],v[220:223],a[220:223],v[124:127]\nds_read_b64_tr_b16 v[220:221],v242 offset:57856\ns_waitcnt lgkmcnt(15)\nM v[0:3],v[192:195],a[224:227],v[0:3]\nds_read_b64_tr_b16 v[222:223],v242 offset:58112\n" \
"s_waitcnt lgkmcnt(14)\nM v[0:3],v[196:199],a[240:243],v[0:3]\nds_write_b64 v250,v[174:175] offset:11272\nv_accvgpr_write_b32 a210,v229\nM v[4:7],v[192:195],a[228:231],v[4:7]\nds_write_b64 v250,v[176:177] offset:12288\nv_accvgpr_write_b32 a211,v229\nM v[4:7],v[196:199],a[244:247],v[4:7]\n" \
"ds_write_b64 v250,v[178:179] offset:12296\nM v[64:67],v[192:195],a[232:235],v[64:67]\nv_cvt_pk_bf16_f32 v0,v0,v1\nv_cvt_pk_bf16_f32 v1,v2,v3\nM v[64:67],v[196:199],a[248:251],v[64:67]\nds_write_b64 v250,v[180:181] offset:13312\nv_accvgpr_write_b32 a128,v0\nM v[68:71],v[192:195],a[236:239],v[68:71]\n" \
"ds_read_b64_tr_b16 v[192:193],v241 offset:50176\nds_read_b64_tr_b16 v[194:195],v241 offset:50432\nM v[68:71],v[196:199],a[252:255],v[68:71]\nds_read_b64_tr_b16 v[196:197],v241 offset:58368\ns_waitcnt lgkmcnt(12)\nM v[8:11],v[200:203],a[224:227],v[8:11]\nds_read_b64_tr_b16 v[198:199],v241 offset:58624\nv_cvt_pk_bf16_f32 v4,v4,v5\n" \
"M v[8:11],v[204:207],a[240:243],v[8:11]\nv_cvt_pk_bf16_f32 v64,v64,v65\nv_cvt_pk_bf16_f32 v5,v6,v7\nM v[12:15],v[200:203],a[228:231],v[12:15]\nv_cvt_pk_bf16_f32 v68,v68,v69\nv_cvt_pk_bf16_f32 v65,v66,v67\nM v[12:15],v[204:207],a[244:247],v[12:15]\nds_write_b64 v250,v[182:183] offset:13320\n" \
"v_cvt_pk_bf16_f32 v69,v70,v71\nM v[72:75],v[200:203],a[232:235],v[72:75]\nds_write_b64 v250,v[184:185] offset:14336\nv_cvt_pk_bf16_f32 v2,v8,v9\nM v[72:75],v[204:207],a[248:251],v[72:75]\nds_write_b64 v250,v[186:187] offset:14344\nv_cvt_pk_bf16_f32 v3,v10,v11\nM v[76:79],v[200:203],a[236:239],v[76:79]\n" \
"ds_read_b64_tr_b16 v[200:201],v242 offset:50176\nds_read_b64_tr_b16 v[202:203],v242 offset:50432\nM v[76:79],v[204:207],a[252:255],v[76:79]\nds_read_b64_tr_b16 v[204:205],v242 offset:58368\nds_read_b64_tr_b16 v[206:207],v242 offset:58624\nM v[16:19],v[208:211],a[224:227],v[16:19]\nds_write_b64 v250,v[188:189] offset:15360\nv_cvt_pk_bf16_f32 v6,v12,v13\n" \
"M v[16:19],v[212:215],a[240:243],v[16:19]\nv_cvt_pk_bf16_f32 v7,v14,v15\nv_cvt_pk_bf16_f32 v66,v72,v73\nM v[20:23],v[208:211],a[228:231],v[20:23]\nv_cvt_pk_bf16_f32 v67,v74,v75\nv_cvt_pk_bf16_f32 v70,v76,v77\nM v[20:23],v[212:215],a[244:247],v[20:23]\nds_write_b64 v250,v[190:191] offset:15368\n" \
"v_cvt_pk_bf16_f32 v71,v78,v79\nM v[80:83],v[208:211],a[232:235],v[80:83]\nv_cvt_pk_bf16_f32 v16,v16,v17\nv_cvt_pk_bf16_f32 v17,v18,v19\nM v[80:83],v[212:215],a[248:251],v[80:83]\nv_add_u32 v250,114688,v240\nv_accvgpr_write_b32 a129,v1\nM v[84:87],v[208:211],a[236:239],v[84:87]\n" \
"buffer_store_dwordx4 v[188:191],v240,s[52:55],s74 offen offset:3072 sc1\nM v[84:87],v[212:215],a[252:255],v[84:87]\nds_read_b64_tr_b16 v[208:209],v241 offset:50688\ns_waitcnt lgkmcnt(7)\nM v[24:27],v[216:219],a[224:227],v[24:27]\nds_read_b64_tr_b16 v[210:211],v241 offset:50944\nds_read_b64_tr_b16 v[212:213],v241 offset:58880\nM v[24:27],v[220:223],a[240:243],v[24:27]\n" \
"ds_read_b64_tr_b16 v[214:215],v241 offset:59136\nv_cvt_pk_bf16_f32 v20,v20,v21\nM v[28:31],v[216:219],a[228:231],v[28:31]\nv_cvt_pk_bf16_f32 v80,v80,v81\nv_cvt_pk_bf16_f32 v84,v84,v85\nM v[28:31],v[220:223],a[244:247],v[28:31]\nv_cvt_pk_bf16_f32 v21,v22,v23\nv_cvt_pk_bf16_f32 v81,v82,v83\n" \
"M v[88:91],v[216:219],a[232:235],v[88:91]\nv_cvt_pk_bf16_f32 v85,v86,v87\nv_cvt_pk_bf16_f32 v18,v24,v25\nM v[88:91],v[220:223],a[248:251],v[88:91]\nv_cvt_pk_bf16_f32 v19,v26,v27\nv_add_u32 v241,65536,v240\nM v[92:95],v[216:219],a[236:239],v[92:95]\nds_read_b64_tr_b16 v[216:217],v242 offset:50688\n" \
"ds_read_b64_tr_b16 v[218:219],v242 offset:50944\nM v[92:95],v[220:223],a[252:255],v[92:95]\nds_read_b64_tr_b16 v[220:221],v242 offset:58880\nds_read_b64_tr_b16 v[222:223],v242 offset:59136\nM v[32:35],v[192:195],a[224:227],v[32:35]\nv_cvt_pk_bf16_f32 v22,v28,v29\nv_cvt_pk_bf16_f32 v23,v30,v31\nM v[32:35],v[196:199],a[240:243],v[32:35]\n" \
"v_cvt_pk_bf16_f32 v82,v88,v89\nv_cvt_pk_bf16_f32 v83,v90,v91\nM v[36:39],v[192:195],a[228:231],v[36:39]\nv_cvt_pk_bf16_f32 v86,v92,v93\nv_cvt_pk_bf16_f32 v87,v94,v95\nM v[36:39],v[196:199],a[244:247],v[36:39]\nv_lshl_or_b32 v242,v231,6,v230\nv_accvgpr_write_b32 a130,v2\n" \
"M v[96:99],v[192:195],a[232:235],v[96:99]\nv_cvt_pk_bf16_f32 v32,v32,v33\nv_cvt_pk_bf16_f32 v33,v34,v35\nM v[96:99],v[196:199],a[248:251],v[96:99]\nv_accvgpr_write_b32 a131,v3\nv_accvgpr_write_b32 a144,v4\nM v[100:103],v[192:195],a[236:239],v[100:103]\nv_cvt_pk_bf16_f32 v36,v36,v37\n" \
"v_cvt_pk_bf16_f32 v37,v38,v39\nM v[100:103],v[196:199],a[252:255],v[100:103]\nv_accvgpr_write_b32 a145,v5\ns_waitcnt lgkmcnt(4)\nM v[40:43],v[200:203],a[224:227],v[40:43]\nv_cvt_pk_bf16_f32 v96,v96,v97\nv_cvt_pk_bf16_f32 v97,v98,v99\nM v[40:43],v[204:207],a[240:243],v[40:43]\n" \
"v_accvgpr_write_b32 a146,v6\nv_accvgpr_write_b32 a147,v7\nM v[44:47],v[200:203],a[228:231],v[44:47]\nv_cvt_pk_bf16_f32 v100,v100,v101\nv_cvt_pk_bf16_f32 v101,v102,v103\nM v[44:47],v[204:207],a[244:247],v[44:47]\nv_accvgpr_write_b32 a160,v64\nv_accvgpr_write_b32 a161,v65\n" \
"M v[104:107],v[200:203],a[232:235],v[104:107]\nv_cvt_pk_bf16_f32 v34,v40,v41\nv_cvt_pk_bf16_f32 v35,v42,v43\nM v[104:107],v[204:207],a[248:251],v[104:107]\nv_accvgpr_write_b32 a162,v66\nv_accvgpr_write_b32 a163,v67\nM v[108:111],v[200:203],a[236:239],v[108:111]\nv_cvt_pk_bf16_f32 v38,v44,v45\n" \
"v_cvt_pk_bf16_f32 v39,v46,v47\nM v[108:111],v[204:207],a[252:255],v[108:111]\nv_accvgpr_write_b32 a176,v68\nv_accvgpr_write_b32 a177,v69\nM v[48:51],v[208:211],a[224:227],v[48:51]\nv_cvt_pk_bf16_f32 v98,v104,v105\nv_cvt_pk_bf16_f32 v99,v106,v107\nM v[48:51],v[212:215],a[240:243],v[48:51]\n" \
"v_accvgpr_write_b32 a178,v70\nv_accvgpr_write_b32 a179,v71\nM v[52:55],v[208:211],a[228:231],v[52:55]\nv_cvt_pk_bf16_f32 v102,v108,v109\nv_cvt_pk_bf16_f32 v103,v110,v111\nM v[52:55],v[212:215],a[244:247],v[52:55]\nv_accvgpr_write_b32 a132,v16\nv_accvgpr_write_b32 a133,v17\n" \
"M v[112:115],v[208:211],a[232:235],v[112:115]\nv_cvt_pk_bf16_f32 v48,v48,v49\nv_cvt_pk_bf16_f32 v49,v50,v51\nM v[112:115],v[212:215],a[248:251],v[112:115]\nv_accvgpr_write_b32 a134,v18\nv_accvgpr_write_b32 a135,v19\nM v[116:119],v[208:211],a[236:239],v[116:119]\nv_cvt_pk_bf16_f32 v52,v52,v53\n" \
"v_cvt_pk_bf16_f32 v53,v54,v55\nM v[116:119],v[212:215],a[252:255],v[116:119]\nv_accvgpr_write_b32 a148,v20\ns_waitcnt lgkmcnt(0)\nM v[56:59],v[216:219],a[224:227],v[56:59]\nv_cvt_pk_bf16_f32 v112,v112,v113\nv_cvt_pk_bf16_f32 v113,v114,v115\nM v[56:59],v[220:223],a[240:243],v[56:59]\n" \
"v_accvgpr_write_b32 a149,v21\nv_accvgpr_write_b32 a150,v22\nM v[60:63],v[216:219],a[228:231],v[60:63]\nv_cvt_pk_bf16_f32 v116,v116,v117\nv_cvt_pk_bf16_f32 v117,v118,v119\nM v[60:63],v[220:223],a[244:247],v[60:63]\nv_accvgpr_write_b32 a244,0\nv_accvgpr_write_b32 a245,0\n" \
"M v[120:123],v[216:219],a[232:235],v[120:123]\nv_cvt_pk_bf16_f32 v50,v56,v57\nv_cvt_pk_bf16_f32 v51,v58,v59\nM v[120:123],v[220:223],a[248:251],v[120:123]\nv_accvgpr_write_b32 a248,-1\nv_accvgpr_write_b32 a249,-1\nM v[124:127],v[216:219],a[236:239],v[124:127]\nv_cvt_pk_bf16_f32 v54,v60,v61\n" \
"v_cvt_pk_bf16_f32 v55,v62,v63\nM v[124:127],v[220:223],a[252:255],v[124:127]\ns_waitcnt vmcnt(0)\nbuffer_store_dword v252,off,s[52:55],s29 sc1\nv_cvt_pk_bf16_f32 v114,v120,v121\nv_cvt_pk_bf16_f32 v115,v122,v123\nv_accvgpr_write_b32 a246,0\nv_accvgpr_write_b32 a250,-1\n" \
"v_accvgpr_write_b32 a247,0\nv_accvgpr_write_b32 a251,-1\nv_cvt_pk_bf16_f32 v118,v124,v125\nv_cvt_pk_bf16_f32 v119,v126,v127\nv_accvgpr_write_b32 a151,v23\nv_accvgpr_write_b32 a164,v80\nv_accvgpr_write_b32 a165,v81\nv_accvgpr_write_b32 a166,v82\n" \
"v_accvgpr_write_b32 a167,v83\nv_accvgpr_write_b32 a180,v84\nv_accvgpr_write_b32 a181,v85\nv_accvgpr_write_b32 a182,v86\nv_accvgpr_write_b32 a183,v87\nv_accvgpr_write_b32 a136,v32\nv_accvgpr_write_b32 a137,v33\nv_accvgpr_write_b32 a138,v34\n" \
"v_accvgpr_write_b32 a139,v35\nv_accvgpr_write_b32 a152,v36\nv_accvgpr_write_b32 a153,v37\nv_accvgpr_write_b32 a154,v38\nv_accvgpr_write_b32 a155,v39\nv_accvgpr_write_b32 a168,v96\nv_accvgpr_write_b32 a169,v97\nv_accvgpr_write_b32 a170,v98\n" \
"v_accvgpr_write_b32 a171,v99\nv_accvgpr_write_b32 a184,v100\nv_accvgpr_write_b32 a185,v101\nv_accvgpr_write_b32 a186,v102\nv_accvgpr_write_b32 a187,v103\nv_accvgpr_write_b32 a140,v48\nv_accvgpr_write_b32 a141,v49\nv_accvgpr_write_b32 a142,v50\n" \
"v_accvgpr_write_b32 a143,v51\nv_accvgpr_write_b32 a156,v52\nv_accvgpr_write_b32 a157,v53\nv_accvgpr_write_b32 a158,v54\nv_accvgpr_write_b32 a159,v55\nv_accvgpr_write_b32 a172,v112\nv_accvgpr_write_b32 a173,v113\nv_accvgpr_write_b32 a174,v114\n" \
"v_accvgpr_write_b32 a175,v115\nv_accvgpr_write_b32 a188,v116\nv_accvgpr_write_b32 a189,v117\nv_accvgpr_write_b32 a190,v118\nv_accvgpr_write_b32 a191,v119\ns_waitcnt lgkmcnt(0)\n.Lattn:\ns_cmp_lt_u32 s7,2\n" \
"s_cbranch_scc0 .Lp23\ns_cmp_eq_u32 s7,0\ns_cbranch_scc0 .Lpro1\n.Lpro0:\ns_barrier\nM v[64:67],a[0:3],a[128:131],0\nds_read_b128 a[32:35],v240 offset:40960\nds_read_b128 a[36:39],v240 offset:41984\n" \
"M v[64:67],a[4:7],a[132:135],v[64:67]\nds_read_b128 a[40:43],v240 offset:43008\ns_mov_b32 m0,s64\nv_mov_b32 v218,0\nM v[64:67],a[8:11],a[136:139],v[64:67]\nds_read_b128 a[44:47],v240 offset:44032\nv_mov_b32 v219,0\nM v[64:67],a[12:15],a[140:143],v[64:67]\n" \
"ds_read_b128 a[48:51],v240 offset:45056\nbuffer_load_dwordx4 v240,s[44:47],s36 offen offset:0 sc1 lds\nM v[68:71],a[12:15],a[156:159],0\nds_read_b128 a[12:15],v240 offset:35840\nM v[68:71],a[8:11],a[152:155],v[68:71]\nds_read_b128 a[8:11],v240 offset:34816\nM v[68:71],a[4:7],a[148:151],v[68:71]\nds_read_b128 a[4:7],v240 offset:33792\n" \
"v_max3_f32 v225,v64,v65,v66\nM v[68:71],a[0:3],a[144:147],v[68:71]\nds_read_b128 a[0:3],v240 offset:32768\nv_max_f32 v225,v225,v67\nv_cndmask_b32 v225,v224,v225,s[90:91]\nM v[76:79],a[28:31],a[156:159],0\nds_read_b128 a[28:31],v240 offset:39936\nv_cndmask_b32 v64,v64,v252,s[82:83]\n" \
"M v[76:79],a[24:27],a[152:155],v[76:79]\nds_read_b128 a[24:27],v240 offset:38912\nv_max3_f32 v229,v68,v69,v70\nv_cndmask_b32 v65,v65,v252,s[84:85]\nM v[76:79],a[20:23],a[148:151],v[76:79]\nds_read_b128 a[20:23],v240 offset:37888\nv_cndmask_b32 v66,v66,v252,s[86:87]\nM v[76:79],a[16:19],a[144:147],v[76:79]\n" \
"ds_read_b128 a[16:19],v240 offset:36864\nv_cndmask_b32 v67,v67,v252,s[88:89]\ns_waitcnt lgkmcnt(4)\nM v[136:139],a[0:3],a[160:163],0\nbuffer_load_dwordx4 v240,s[44:47],s36 offen offset:1024 sc1 lds\nM v[136:139],a[4:7],a[164:167],v[136:139]\nds_read_b128 a[52:55],v240 offset:46080\nds_read_b128 a[56:59],v240 offset:47104\n" \
"v_max3_f32 v229,v229,v71,v76\nM v[136:139],a[8:11],a[168:171],v[136:139]\nv_max3_f32 v229,v229,v77,v78\nv_max_f32 v229,v229,v79\nv_cndmask_b32 v229,v228,v229,s[90:91]\nM v[136:139],a[12:15],a[172:175],v[136:139]\nds_read_b128 a[60:63],v240 offset:48128\nv_permlane16_swap_b32 v225,v229\n" \
"M v[140:143],a[12:15],a[188:191],0\nv_max_f32 v225,v225,v229\nv_mov_b32 v229,v225\nds_read_b128 a[64:67],v241 offset:0\nM v[140:143],a[8:11],a[184:187],v[140:143]\nv_permlane32_swap_b32 v225,v229\nv_max_f32 v225,v225,v229\nM v[140:143],a[4:7],a[180:183],v[140:143]\n" \
"v_mov_b32 v229,v225\nv_max3_f32 v233,v136,v137,v138\nds_read_b128 a[68:71],v241 offset:1024\nM v[140:143],a[0:3],a[176:179],v[140:143]\nv_permlane16_swap_b32 v225,v229\ns_waitcnt lgkmcnt(5)\nM v[152:155],a[16:19],a[160:163],0\nv_sub_f32 v224,0,v225\n" \
"v_sub_f32 v226,0,v225\nv_sub_f32 v227,0,v225\nM v[152:155],a[20:23],a[164:167],v[152:155]\nv_sub_f32 v225,0,v225\nv_sub_f32 v228,0,v229\nM v[152:155],a[24:27],a[168:171],v[152:155]\nv_sub_f32 v230,0,v229\nv_sub_f32 v231,0,v229\n" \
"v_sub_f32 v229,0,v229\nM v[152:155],a[28:31],a[172:175],v[152:155]\nv_max3_f32 v237,v140,v141,v142\nds_read_b128 a[72:75],v241 offset:2048\nM v[156:159],a[28:31],a[188:191],0\nbuffer_load_dwordx4 v240,s[44:47],s36 offen offset:2048 sc1 lds\nds_read_b128 a[76:79],v241 offset:3072\nM v[156:159],a[24:27],a[184:187],v[156:159]\n" \
"ds_read_b128 a[80:83],v241 offset:4096\nds_read_b128 a[84:87],v241 offset:5120\nM v[156:159],a[20:23],a[180:183],v[156:159]\nv_max3_f32 v233,v233,v139,v152\nv_max3_f32 v233,v233,v153,v154\nv_max_f32 v233,v233,v155\nM v[156:159],a[16:19],a[176:179],v[156:159]\nv_cndmask_b32 v233,v232,v233,s[90:91]\n" \
"ds_read_b128 a[88:91],v241 offset:6144\nv_add_f32 v64,v64,v224\nM v[128:131],a[0:3],a[128:131],v[224:227]\nbuffer_load_dwordx4 v240,s[44:47],s36 offen offset:3072 sc1 lds\nM v[128:131],a[4:7],a[132:135],v[128:131]\nv_add_f32 v65,v65,v224\nv_add_f32 v66,v66,v224\nv_max3_f32 v237,v237,v143,v156\n" \
"M v[128:131],a[8:11],a[136:139],v[128:131]\nv_max3_f32 v237,v237,v157,v158\nv_max_f32 v237,v237,v159\nM v[128:131],a[12:15],a[140:143],v[128:131]\nv_cndmask_b32 v237,v236,v237,s[90:91]\nv_add_f32 v67,v67,v224\nds_read_b128 a[92:95],v241 offset:7168\nM v[132:135],a[12:15],a[156:159],v[228:231]\n" \
"v_permlane16_swap_b32 v233,v237\nv_max_f32 v233,v233,v237\nM v[132:135],a[8:11],a[152:155],v[132:135]\nv_mov_b32 v237,v233\nv_exp_f32 v64,v64\nM v[132:135],a[4:7],a[148:151],v[132:135]\nv_permlane32_swap_b32 v233,v237\nv_max_f32 v233,v233,v237\n" \
"M v[132:135],a[0:3],a[144:147],v[132:135]\nv_mov_b32 v237,v233\nv_exp_f32 v65,v65\nM v[144:147],a[16:19],a[128:131],v[224:227]\nv_permlane16_swap_b32 v233,v237\nv_sub_f32 v232,0,v233\nM v[144:147],a[20:23],a[132:135],v[144:147]\nv_sub_f32 v234,0,v233\n" \
"v_sub_f32 v235,0,v233\nv_sub_f32 v233,0,v233\nM v[144:147],a[24:27],a[136:139],v[144:147]\nv_sub_f32 v236,0,v237\nv_sub_f32 v238,0,v237\nM v[144:147],a[28:31],a[140:143],v[144:147]\nv_sub_f32 v239,0,v237\nv_sub_f32 v237,0,v237\n" \
"v_cvt_pk_bf16_f32 v64,v64,v65\nM v[148:151],a[28:31],a[156:159],v[228:231]\nv_exp_f32 v66,v66\nv_add_f32 v68,v68,v228\nM v[148:151],a[24:27],a[152:155],v[148:151]\nv_exp_f32 v67,v67\nM v[148:151],a[20:23],a[148:151],v[148:151]\nv_cvt_pk_bf16_f32 v65,v66,v67\n" \
"v_mov_b32 v66,0\nv_mov_b32 v67,0\nM v[148:151],a[16:19],a[144:147],v[148:151]\nv_add_f32 v69,v69,v228\nv_cndmask_b32 v76,v76,v252,s[82:83]\nM v[160:163],a[32:35],a[128:131],v[224:227]\nv_cndmask_b32 v77,v77,v252,s[84:85]\nv_cndmask_b32 v78,v78,v252,s[86:87]\n" \
"v_cndmask_b32 v79,v79,v252,s[88:89]\nM v[160:163],a[36:39],a[132:135],v[160:163]\nv_add_f32 v70,v70,v228\nv_add_f32 v71,v71,v228\nM v[160:163],a[40:43],a[136:139],v[160:163]\nv_exp_f32 v68,v68\nv_add_f32 v76,v76,v228\nM v[160:163],a[44:47],a[140:143],v[160:163]\n" \
"v_exp_f32 v69,v69\nM v[164:167],a[44:47],a[156:159],v[228:231]\nv_exp_f32 v70,v70\nv_add_f32 v77,v77,v228\nM v[164:167],a[40:43],a[152:155],v[164:167]\nv_exp_f32 v71,v71\nM v[164:167],a[36:39],a[148:151],v[164:167]\nv_add_f32 v78,v78,v228\n" \
"v_add_f32 v79,v79,v228\nv_cvt_pk_bf16_f32 v68,v68,v69\nM v[164:167],a[32:35],a[144:147],v[164:167]\nv_exp_f32 v76,v76\nM v[176:179],a[48:51],a[128:131],v[224:227]\nv_exp_f32 v77,v77\ns_waitcnt lgkmcnt(0)\nM v[176:179],a[52:55],a[132:135],v[176:179]\n" \
"v_exp_f32 v78,v78\nv_cvt_pk_bf16_f32 v69,v70,v71\nM v[176:179],a[56:59],a[136:139],v[176:179]\nv_exp_f32 v79,v79\nM v[176:179],a[60:63],a[140:143],v[176:179]\nv_cvt_pk_bf16_f32 v70,v76,v77\nv_cvt_pk_bf16_f32 v71,v78,v79\nds_read_b128 a[0:3],v240 offset:49152\n" \
"M v[180:183],a[60:63],a[156:159],v[228:231]\nds_read_b128 a[4:7],v240 offset:50176\nds_read_b128 a[8:11],v240 offset:51200\nM v[180:183],a[56:59],a[152:155],v[180:183]\nds_read_b128 a[12:15],v240 offset:52224\nv_exp_f32 v128,v128\nM v[180:183],a[52:55],a[148:151],v[180:183]\nds_read_b128 a[16:19],v240 offset:53248\n" \
"v_add_f32 v136,v136,v232\nM v[180:183],a[48:51],a[144:147],v[180:183]\nv_exp_f32 v129,v129\nds_read_b128 a[20:23],v240 offset:54272\nM v[168:171],a[32:35],a[160:163],v[232:235]\nv_exp_f32 v130,v130\nM v[168:171],a[36:39],a[164:167],v[168:171]\nv_exp_f32 v131,v131\n" \
"v_cvt_pk_bf16_f32 v192,v128,v129\nM v[168:171],a[40:43],a[168:171],v[168:171]\nv_cvt_pk_bf16_f32 v193,v130,v131\nds_read_b128 a[24:27],v240 offset:55296\nM v[168:171],a[44:47],a[172:175],v[168:171]\nds_read_b128 a[28:31],v240 offset:56320\nv_exp_f32 v132,v132\nM v[172:175],a[44:47],a[188:191],v[236:239]\n" \
"v_exp_f32 v133,v133\nM v[172:175],a[40:43],a[184:187],v[172:175]\nv_exp_f32 v134,v134\nv_cvt_pk_bf16_f32 v196,v132,v133\nM v[172:175],a[36:39],a[180:183],v[172:175]\nbuffer_load_dword v248,v251,s[52:55],s80 offen sc1\nv_add_f32 v137,v137,v232\nM v[172:175],a[32:35],a[176:179],v[172:175]\n" \
"v_exp_f32 v135,v135\nM v[188:191],a[60:63],a[188:191],v[236:239]\nv_cvt_pk_bf16_f32 v197,v134,v135\nv_exp_f32 v144,v144\nM v[188:191],a[56:59],a[184:187],v[188:191]\nv_exp_f32 v145,v145\nM v[188:191],a[52:55],a[180:183],v[188:191]\nv_exp_f32 v146,v146\n" \
"v_cvt_pk_bf16_f32 v194,v144,v145\nM v[188:191],a[48:51],a[176:179],v[188:191]\nv_exp_f32 v147,v147\nM v[0:3],a[64:67],v[64:67],0\nv_cvt_pk_bf16_f32 v195,v146,v147\nv_exp_f32 v148,v148\nM v[8:11],a[68:71],v[64:67],0\nv_exp_f32 v149,v149\n" \
"M v[16:19],a[72:75],v[64:67],0\nv_exp_f32 v150,v150\nv_cvt_pk_bf16_f32 v198,v148,v149\nM v[24:27],a[76:79],v[64:67],0\nv_exp_f32 v151,v151\nM v[32:35],a[80:83],v[64:67],0\nv_cvt_pk_bf16_f32 v199,v150,v151\nv_add_f32 v138,v138,v232\n" \
"v_add_f32 v139,v139,v232\nM v[40:43],a[84:87],v[64:67],0\nv_exp_f32 v136,v136\nM v[48:51],a[88:91],v[64:67],0\nv_exp_f32 v137,v137\nv_add_f32 v140,v140,v236\nM v[56:59],a[92:95],v[64:67],0\nv_exp_f32 v138,v138\n" \
"v_cvt_pk_bf16_f32 v200,v136,v137\nM a[192:195],a[208:211],v[64:67],0\nv_exp_f32 v139,v139\nM v[4:7],a[64:67],v[68:71],0\nv_cvt_pk_bf16_f32 v201,v138,v139\nv_add_f32 v141,v141,v236\nv_add_f32 v142,v142,v236\nM v[12:15],a[68:71],v[68:71],0\n" \
"v_add_f32 v143,v143,v236\nds_read_b128 a[32:35],v240 offset:57344\nM v[20:23],a[72:75],v[68:71],0\nv_exp_f32 v140,v140\nv_add_f32 v152,v152,v232\nM v[28:31],a[76:79],v[68:71],0\nv_exp_f32 v141,v141\nM v[36:39],a[80:83],v[68:71],0\n" \
"v_exp_f32 v142,v142\nv_cvt_pk_bf16_f32 v204,v140,v141\nM v[44:47],a[84:87],v[68:71],0\nv_exp_f32 v143,v143\nM v[52:55],a[88:91],v[68:71],0\nv_cvt_pk_bf16_f32 v205,v142,v143\nv_add_f32 v153,v153,v232\nv_add_f32 v154,v154,v232\n" \
"M v[60:63],a[92:95],v[68:71],0\nv_add_f32 v155,v155,v232\nds_read_b128 a[36:39],v240 offset:58368\nM a[196:199],a[208:211],v[68:71],0\nv_exp_f32 v152,v152\ns_waitcnt lgkmcnt(2)\nM v[128:131],a[0:3],a[128:131],v[224:227]\nv_exp_f32 v153,v153\n" \
"M v[128:131],a[4:7],a[132:135],v[128:131]\nv_exp_f32 v154,v154\nv_cvt_pk_bf16_f32 v202,v152,v153\nM v[128:131],a[8:11],a[136:139],v[128:131]\nv_exp_f32 v155,v155\nM v[128:131],a[12:15],a[140:143],v[128:131]\nv_cvt_pk_bf16_f32 v203,v154,v155\nds_read_b128 a[40:43],v240 offset:59392\n" \
"ds_read_b128 a[44:47],v240 offset:60416\nM v[132:135],a[12:15],a[156:159],v[228:231]\nv_add_f32 v156,v156,v236\nv_add_f32 v157,v157,v236\nv_add_f32 v158,v158,v236\nM v[132:135],a[8:11],a[152:155],v[132:135]\nv_add_f32 v159,v159,v236\nds_read_b128 a[48:51],v240 offset:61440\n" \
"M v[132:135],a[4:7],a[148:151],v[132:135]\nv_exp_f32 v156,v156\nds_read_b128 a[52:55],v240 offset:62464\nM v[132:135],a[0:3],a[144:147],v[132:135]\nv_exp_f32 v157,v157\nM v[144:147],a[16:19],a[128:131],v[224:227]\nv_exp_f32 v158,v158\nv_cvt_pk_bf16_f32 v206,v156,v157\n" \
"M v[144:147],a[20:23],a[132:135],v[144:147]\nv_exp_f32 v159,v159\nM v[144:147],a[24:27],a[136:139],v[144:147]\nv_cvt_pk_bf16_f32 v207,v158,v159\nv_exp_f32 v160,v160\nM v[144:147],a[28:31],a[140:143],v[144:147]\nv_exp_f32 v161,v161\nM v[148:151],a[28:31],a[156:159],v[228:231]\n" \
"v_exp_f32 v162,v162\nv_cvt_pk_bf16_f32 v208,v160,v161\nM v[148:151],a[24:27],a[152:155],v[148:151]\nv_exp_f32 v163,v163\nM v[148:151],a[20:23],a[148:151],v[148:151]\nv_cvt_pk_bf16_f32 v209,v162,v163\nds_read_b128 a[56:59],v240 offset:63488\nds_read_b128 a[60:63],v240 offset:64512\n" \
"M v[148:151],a[16:19],a[144:147],v[148:151]\nv_exp_f32 v164,v164\nM v[136:139],a[0:3],a[160:163],v[232:235]\nv_exp_f32 v165,v165\nv_cndmask_b32 v168,v168,v252,s[82:83]\nM v[136:139],a[4:7],a[164:167],v[136:139]\nv_exp_f32 v166,v166\nv_cvt_pk_bf16_f32 v212,v164,v165\n" \
"M v[136:139],a[8:11],a[168:171],v[136:139]\nv_exp_f32 v167,v167\nM v[136:139],a[12:15],a[172:175],v[136:139]\nv_cvt_pk_bf16_f32 v213,v166,v167\nv_exp_f32 v176,v176\nM v[140:143],a[12:15],a[188:191],v[236:239]\nv_exp_f32 v177,v177\nM v[140:143],a[8:11],a[184:187],v[140:143]\n" \
"v_exp_f32 v178,v178\nv_cvt_pk_bf16_f32 v210,v176,v177\nM v[140:143],a[4:7],a[180:183],v[140:143]\nv_exp_f32 v179,v179\nM v[140:143],a[0:3],a[176:179],v[140:143]\nv_cvt_pk_bf16_f32 v211,v178,v179\nv_exp_f32 v180,v180\nM v[152:155],a[16:19],a[160:163],v[232:235]\n" \
"v_exp_f32 v181,v181\nM v[152:155],a[20:23],a[164:167],v[152:155]\nv_exp_f32 v182,v182\nv_cvt_pk_bf16_f32 v214,v180,v181\nM v[152:155],a[24:27],a[168:171],v[152:155]\ns_waitcnt vmcnt(1)\ns_barrier\nM v[152:155],a[28:31],a[172:175],v[152:155]\n" \
"v_exp_f32 v183,v183\nv_cndmask_b32 v169,v169,v252,s[84:85]\nM v[156:159],a[28:31],a[188:191],v[236:239]\nv_cvt_pk_bf16_f32 v215,v182,v183\nv_cndmask_b32 v170,v170,v252,s[86:87]\nM v[156:159],a[24:27],a[184:187],v[156:159]\nv_cndmask_b32 v171,v171,v252,s[88:89]\nv_exp_f32 v168,v168\n" \
"M v[156:159],a[20:23],a[180:183],v[156:159]\nv_exp_f32 v169,v169\nds_read_b128 a[64:67],v241 offset:32768\nM v[156:159],a[16:19],a[176:179],v[156:159]\nv_cvt_pk_bf16_f32 v216,v168,v169\ns_waitcnt lgkmcnt(1)\nM v[160:163],a[32:35],a[128:131],v[224:227]\nv_exp_f32 v170,v170\n" \
"ds_read_b128 a[96:99],v241 offset:40960\nM v[160:163],a[36:39],a[132:135],v[160:163]\nv_exp_f32 v171,v171\nM v[160:163],a[40:43],a[136:139],v[160:163]\nv_cvt_pk_bf16_f32 v217,v170,v171\nv_exp_f32 v172,v172\nM v[160:163],a[44:47],a[140:143],v[160:163]\nv_exp_f32 v173,v173\n" \
"M v[164:167],a[44:47],a[156:159],v[228:231]\nv_exp_f32 v174,v174\nv_cvt_pk_bf16_f32 v220,v172,v173\nM v[164:167],a[40:43],a[152:155],v[164:167]\nv_exp_f32 v175,v175\nM v[164:167],a[36:39],a[148:151],v[164:167]\nv_cvt_pk_bf16_f32 v221,v174,v175\nds_read_b128 a[68:71],v241 offset:33792\n" \
"ds_read_b128 a[100:103],v241 offset:41984\nM v[164:167],a[32:35],a[144:147],v[164:167]\nds_read_b128 a[72:75],v241 offset:34816\nv_cndmask_b32 v188,v188,v252,s[82:83]\nM v[176:179],a[48:51],a[128:131],v[224:227]\nv_cndmask_b32 v189,v189,v252,s[84:85]\nv_cndmask_b32 v190,v190,v252,s[86:87]\nv_cndmask_b32 v191,v191,v252,s[88:89]\n" \
"M v[176:179],a[52:55],a[132:135],v[176:179]\nv_exp_f32 v188,v188\nM v[176:179],a[56:59],a[136:139],v[176:179]\nv_exp_f32 v189,v189\nds_read_b128 a[104:107],v241 offset:43008\nM v[176:179],a[60:63],a[140:143],v[176:179]\nv_exp_f32 v190,v190\nv_cvt_pk_bf16_f32 v222,v188,v189\n" \
"M v[180:183],a[60:63],a[156:159],v[228:231]\nv_exp_f32 v191,v191\nM v[180:183],a[56:59],a[152:155],v[180:183]\nv_cvt_pk_bf16_f32 v223,v190,v191\nds_read_b128 a[76:79],v241 offset:35840\nds_read_b128 a[108:111],v241 offset:44032\nM v[180:183],a[52:55],a[148:151],v[180:183]\nds_read_b128 a[80:83],v241 offset:36864\n" \
"s_waitcnt vmcnt(0)\nv_cmp_eq_u32 vcc,0,v248\nM v[180:183],a[48:51],a[144:147],v[180:183]\nds_read_b128 a[112:115],v241 offset:45056\ns_cbranch_vccnz .Lps2\n.Lpb2:\ns_add_u32 m0,s64,65536\nM v[168:171],a[32:35],a[160:163],v[232:235]\n" \
"buffer_load_dwordx4 v240,s[48:51],s36 offen offset:0 sc1 lds\nM v[168:171],a[36:39],a[164:167],v[168:171]\nds_read_b128 a[84:87],v241 offset:37888\nv_exp_f32 v128,v128\nM v[168:171],a[40:43],a[168:171],v[168:171]\nds_read_b128 a[116:119],v241 offset:46080\nds_read_b128 a[88:91],v241 offset:38912\nM v[168:171],a[44:47],a[172:175],v[168:171]\n" \
"ds_read_b128 a[120:123],v241 offset:47104\nv_exp_f32 v129,v129\nM v[172:175],a[44:47],a[188:191],v[236:239]\nbuffer_load_dwordx4 v240,s[48:51],s36 offen offset:1024 sc1 lds\nM v[172:175],a[40:43],a[184:187],v[172:175]\nds_read_b128 a[92:95],v241 offset:39936\nv_exp_f32 v130,v130\nM v[172:175],a[36:39],a[180:183],v[172:175]\n" \
"ds_read_b128 a[124:127],v241 offset:48128\nds_read_b128 a[0:3],v240 offset:0\nM v[172:175],a[32:35],a[176:179],v[172:175]\nds_read_b128 a[4:7],v240 offset:1024\nv_exp_f32 v131,v131\nM v[184:187],a[48:51],a[160:163],v[232:235]\nds_read_b128 a[8:11],v240 offset:2048\nbuffer_load_dwordx4 v240,s[48:51],s36 offen offset:2048 sc1 lds\n" \
"M v[184:187],a[52:55],a[164:167],v[184:187]\nv_exp_f32 v132,v132\nM v[184:187],a[56:59],a[168:171],v[184:187]\nds_read_b128 a[12:15],v240 offset:3072\nv_exp_f32 v133,v133\nM v[184:187],a[60:63],a[172:175],v[184:187]\nds_read_b128 a[16:19],v240 offset:4096\nds_read_b128 a[20:23],v240 offset:5120\n" \
"M v[188:191],a[60:63],a[188:191],v[236:239]\nds_read_b128 a[24:27],v240 offset:6144\nbuffer_load_dwordx4 v240,s[48:51],s36 offen offset:3072 sc1 lds\nM v[188:191],a[56:59],a[184:187],v[188:191]\nds_read_b128 a[28:31],v240 offset:7168\ns_add_u32 m0,s64,16384\nM v[188:191],a[52:55],a[180:183],v[188:191]\nds_read_b128 a[32:35],v240 offset:8192\n" \
"v_exp_f32 v134,v134\nM v[188:191],a[48:51],a[176:179],v[188:191]\nds_read_b128 a[36:39],v240 offset:9216\ns_waitcnt lgkmcnt(9)\nM v[0:3],a[64:67],v[192:195],v[0:3]\nds_read_b128 a[40:43],v240 offset:10240\nbuffer_load_dwordx4 v240,s[44:47],s65 offen offset:0 sc1 lds\nM v[0:3],a[96:99],v[208:211],v[0:3]\n" \
"v_exp_f32 v135,v135\nM v[8:11],a[68:71],v[192:195],v[8:11]\nds_read_b128 a[44:47],v240 offset:11264\nv_exp_f32 v144,v144\nM v[8:11],a[100:103],v[208:211],v[8:11]\nds_read_b128 a[48:51],v240 offset:12288\nds_read_b128 a[52:55],v240 offset:13312\nM v[16:19],a[72:75],v[192:195],v[16:19]\n" \
"buffer_load_dwordx4 v240,s[44:47],s65 offen offset:1024 sc1 lds\nds_read_b128 a[56:59],v240 offset:14336\nM v[16:19],a[104:107],v[208:211],v[16:19]\nds_read_b128 a[60:63],v240 offset:15360\nv_exp_f32 v145,v145\nM v[24:27],a[76:79],v[192:195],v[24:27]\nv_exp_f32 v146,v146\nM v[24:27],a[108:111],v[208:211],v[24:27]\n" \
"v_exp_f32 v147,v147\nM v[32:35],a[80:83],v[192:195],v[32:35]\nbuffer_load_dwordx4 v240,s[44:47],s65 offen offset:2048 sc1 lds\nM v[32:35],a[112:115],v[208:211],v[32:35]\nv_exp_f32 v148,v148\nM v[40:43],a[84:87],v[192:195],v[40:43]\nv_exp_f32 v149,v149\nM v[40:43],a[116:119],v[208:211],v[40:43]\n" \
"v_exp_f32 v150,v150\nM v[48:51],a[88:91],v[192:195],v[48:51]\nbuffer_load_dwordx4 v240,s[44:47],s65 offen offset:3072 sc1 lds\nM v[48:51],a[120:123],v[208:211],v[48:51]\ns_add_u32 m0,s64,81920\nv_exp_f32 v151,v151\nM v[56:59],a[92:95],v[192:195],v[56:59]\nv_exp_f32 v136,v136\n" \
"M v[56:59],a[124:127],v[208:211],v[56:59]\nv_exp_f32 v137,v137\nM a[192:195],a[208:211],v[192:195],a[192:195]\nbuffer_load_dwordx4 v240,s[48:51],s65 offen offset:0 sc1 lds\nM a[192:195],a[208:211],v[208:211],a[192:195]\nv_exp_f32 v138,v138\nv_cvt_pk_bf16_f32 v192,v128,v129\nM v[4:7],a[64:67],v[196:199],v[4:7]\n" \
"v_exp_f32 v139,v139\nM v[4:7],a[96:99],v[212:215],v[4:7]\nv_exp_f32 v152,v152\nv_cvt_pk_bf16_f32 v193,v130,v131\nM v[12:15],a[68:71],v[196:199],v[12:15]\nbuffer_load_dwordx4 v240,s[48:51],s65 offen offset:1024 sc1 lds\nv_cvt_pk_bf16_f32 v194,v144,v145\nM v[12:15],a[100:103],v[212:215],v[12:15]\n" \
"v_exp_f32 v153,v153\nM v[20:23],a[72:75],v[196:199],v[20:23]\nv_exp_f32 v154,v154\nv_cvt_pk_bf16_f32 v195,v146,v147\nM v[20:23],a[104:107],v[212:215],v[20:23]\nv_exp_f32 v155,v155\nM v[28:31],a[76:79],v[196:199],v[28:31]\nbuffer_load_dwordx4 v240,s[48:51],s65 offen offset:2048 sc1 lds\n" \
"M v[28:31],a[108:111],v[212:215],v[28:31]\nv_exp_f32 v160,v160\nM v[36:39],a[80:83],v[196:199],v[36:39]\ns_barrier\nv_exp_f32 v161,v161\nM v[36:39],a[112:115],v[212:215],v[36:39]\nv_exp_f32 v162,v162\nM v[44:47],a[84:87],v[196:199],v[44:47]\n" \
"buffer_load_dwordx4 v240,s[48:51],s65 offen offset:3072 sc1 lds\ns_add_u32 m0,s64,32768\nM v[44:47],a[116:119],v[212:215],v[44:47]\nv_exp_f32 v163,v163\nM v[52:55],a[88:91],v[196:199],v[52:55]\nv_exp_f32 v176,v176\nv_cvt_pk_bf16_f32 v208,v160,v161\nM v[52:55],a[120:123],v[212:215],v[52:55]\n" \
"v_exp_f32 v177,v177\nM v[60:63],a[92:95],v[196:199],v[60:63]\nbuffer_load_dwordx4 v240,s[44:47],s66 offen offset:0 sc1 lds\nv_cvt_pk_bf16_f32 v209,v162,v163\nM v[60:63],a[124:127],v[212:215],v[60:63]\nv_exp_f32 v178,v178\nv_cvt_pk_bf16_f32 v210,v176,v177\nM a[196:199],a[208:211],v[196:199],a[196:199]\n" \
"v_exp_f32 v179,v179\nM a[196:199],a[208:211],v[212:215],a[196:199]\nv_exp_f32 v140,v140\nv_cvt_pk_bf16_f32 v196,v132,v133\nM v[64:67],a[64:67],v[200:203],0\nbuffer_load_dwordx4 v240,s[44:47],s66 offen offset:1024 sc1 lds\nM v[64:67],a[96:99],v[216:219],v[64:67]\nv_exp_f32 v141,v141\n" \
"v_cvt_pk_bf16_f32 v197,v134,v135\nM v[72:75],a[68:71],v[200:203],0\nv_exp_f32 v142,v142\nM v[72:75],a[100:103],v[216:219],v[72:75]\nv_exp_f32 v143,v143\nv_cvt_pk_bf16_f32 v198,v148,v149\nM v[80:83],a[72:75],v[200:203],0\nbuffer_load_dwordx4 v240,s[44:47],s66 offen offset:2048 sc1 lds\n" \
"M v[80:83],a[104:107],v[216:219],v[80:83]\nv_exp_f32 v156,v156\nv_cvt_pk_bf16_f32 v199,v150,v151\nM v[88:91],a[76:79],v[200:203],0\nv_exp_f32 v157,v157\nM v[88:91],a[108:111],v[216:219],v[88:91]\nv_exp_f32 v158,v158\nv_cvt_pk_bf16_f32 v211,v178,v179\n" \
"M v[96:99],a[80:83],v[200:203],0\nbuffer_load_dwordx4 v240,s[44:47],s66 offen offset:3072 sc1 lds\nM v[96:99],a[112:115],v[216:219],v[96:99]\ns_add_u32 m0,s64,98304\nv_exp_f32 v159,v159\nM v[104:107],a[84:87],v[200:203],0\nv_exp_f32 v164,v164\nM v[104:107],a[116:119],v[216:219],v[104:107]\n" \
"v_exp_f32 v165,v165\nM v[112:115],a[88:91],v[200:203],0\nv_exp_f32 v166,v166\nM v[112:115],a[120:123],v[216:219],v[112:115]\nv_exp_f32 v167,v167\nM v[120:123],a[92:95],v[200:203],0\nv_exp_f32 v180,v180\nM v[120:123],a[124:127],v[216:219],v[120:123]\n" \
"v_exp_f32 v181,v181\nM a[200:203],a[208:211],v[200:203],0\nv_exp_f32 v182,v182\nv_cvt_pk_bf16_f32 v200,v136,v137\nM a[200:203],a[208:211],v[216:219],a[200:203]\nv_exp_f32 v183,v183\nM v[68:71],a[64:67],v[204:207],0\nv_exp_f32 v168,v168\n" \
"v_cvt_pk_bf16_f32 v201,v138,v139\nM v[68:71],a[96:99],v[220:223],v[68:71]\nv_exp_f32 v169,v169\nM v[76:79],a[68:71],v[204:207],0\nv_exp_f32 v170,v170\nv_cvt_pk_bf16_f32 v202,v152,v153\nM v[76:79],a[100:103],v[220:223],v[76:79]\nv_exp_f32 v171,v171\n" \
"M v[84:87],a[72:75],v[204:207],0\nv_cvt_pk_bf16_f32 v203,v154,v155\nbuffer_load_dwordx4 v240,s[48:51],s66 offen offset:0 sc1 lds\nM v[84:87],a[104:107],v[220:223],v[84:87]\nM v[92:95],a[76:79],v[204:207],0\nM v[92:95],a[108:111],v[220:223],v[92:95]\nM v[100:103],a[80:83],v[204:207],0\nbuffer_load_dwordx4 v240,s[48:51],s66 offen offset:1024 sc1 lds\n" \
"M v[100:103],a[112:115],v[220:223],v[100:103]\nM v[108:111],a[84:87],v[204:207],0\nM v[108:111],a[116:119],v[220:223],v[108:111]\nM v[116:119],a[88:91],v[204:207],0\nbuffer_load_dwordx4 v240,s[48:51],s66 offen offset:2048 sc1 lds\nM v[116:119],a[120:123],v[220:223],v[116:119]\nM v[124:127],a[92:95],v[204:207],0\nM v[124:127],a[124:127],v[220:223],v[124:127]\n" \
"M a[204:207],a[208:211],v[204:207],0\nbuffer_load_dwordx4 v240,s[48:51],s66 offen offset:3072 sc1 lds\nM a[204:207],a[208:211],v[220:223],a[204:207]\ns_cmp_lt_u32 s62,2\ns_cbranch_scc1 .Lx01\ns_branch .Ldual\n.Lpro1:\ns_barrier\n" \
"M v[64:67],a[0:3],a[128:131],0\nds_read_b128 a[64:67],v241 offset:0\nds_read_b128 a[96:99],v241 offset:8192\nM v[64:67],a[4:7],a[132:135],v[64:67]\nds_read_b128 a[68:71],v241 offset:1024\ns_mov_b32 m0,s64\nv_mov_b32 v202,0\nM v[64:67],a[8:11],a[136:139],v[64:67]\n" \
"buffer_load_dwordx4 v240,s[44:47],s36 offen offset:0 sc1 lds\nM v[64:67],a[12:15],a[140:143],v[64:67]\nds_read_b128 a[100:103],v241 offset:9216\nds_read_b128 a[72:75],v241 offset:2048\nv_mov_b32 v203,0\nM v[68:71],a[12:15],a[156:159],0\nds_read_b128 a[12:15],v240 offset:35840\nM v[68:71],a[8:11],a[152:155],v[68:71]\n" \
"ds_read_b128 a[8:11],v240 offset:34816\nM v[68:71],a[4:7],a[148:151],v[68:71]\nds_read_b128 a[4:7],v240 offset:33792\nbuffer_load_dwordx4 v240,s[44:47],s36 offen offset:1024 sc1 lds\nM v[68:71],a[0:3],a[144:147],v[68:71]\nds_read_b128 a[0:3],v240 offset:32768\nv_max3_f32 v225,v64,v65,v66\nM v[72:75],a[16:19],a[128:131],0\n" \
"ds_read_b128 a[104:107],v241 offset:10240\nM v[72:75],a[20:23],a[132:135],v[72:75]\nds_read_b128 a[76:79],v241 offset:3072\nM v[72:75],a[24:27],a[136:139],v[72:75]\nbuffer_load_dwordx4 v240,s[44:47],s36 offen offset:2048 sc1 lds\nM v[72:75],a[28:31],a[140:143],v[72:75]\nv_max3_f32 v229,v68,v69,v70\nds_read_b128 a[108:111],v241 offset:11264\n" \
"ds_read_b128 a[80:83],v241 offset:4096\nM v[76:79],a[28:31],a[156:159],0\nds_read_b128 a[28:31],v240 offset:39936\nM v[76:79],a[24:27],a[152:155],v[76:79]\nds_read_b128 a[24:27],v240 offset:38912\nM v[76:79],a[20:23],a[148:151],v[76:79]\nds_read_b128 a[20:23],v240 offset:37888\nv_max3_f32 v225,v225,v67,v72\n" \
"v_max3_f32 v225,v225,v73,v74\nM v[76:79],a[16:19],a[144:147],v[76:79]\nds_read_b128 a[16:19],v240 offset:36864\nv_max_f32 v225,v225,v75\nv_cndmask_b32 v225,v224,v225,s[90:91]\nM v[80:83],a[32:35],a[128:131],0\nbuffer_load_dwordx4 v240,s[44:47],s36 offen offset:3072 sc1 lds\nM v[80:83],a[36:39],a[132:135],v[80:83]\n" \
"ds_read_b128 a[112:115],v241 offset:12288\nds_read_b128 a[84:87],v241 offset:5120\nv_max3_f32 v229,v229,v71,v76\nM v[80:83],a[40:43],a[136:139],v[80:83]\nv_max3_f32 v229,v229,v77,v78\nv_max_f32 v229,v229,v79\nv_cndmask_b32 v229,v228,v229,s[90:91]\nM v[80:83],a[44:47],a[140:143],v[80:83]\n" \
"ds_read_b128 a[116:119],v241 offset:13312\nv_permlane16_swap_b32 v225,v229\nM v[84:87],a[44:47],a[156:159],0\nv_max_f32 v225,v225,v229\nv_mov_b32 v229,v225\nds_read_b128 a[44:47],v240 offset:44032\nM v[84:87],a[40:43],a[152:155],v[84:87]\nv_permlane32_swap_b32 v225,v229\n" \
"v_max_f32 v225,v225,v229\nM v[84:87],a[36:39],a[148:151],v[84:87]\nv_mov_b32 v229,v225\nds_read_b128 a[36:39],v240 offset:41984\nds_read_b128 a[40:43],v240 offset:43008\nM v[84:87],a[32:35],a[144:147],v[84:87]\nv_permlane16_swap_b32 v225,v229\nv_sub_f32 v224,0,v225\n" \
"v_sub_f32 v226,0,v225\nM v[92:95],a[60:63],a[156:159],0\nv_sub_f32 v227,0,v225\nv_sub_f32 v225,0,v225\nM v[92:95],a[56:59],a[152:155],v[92:95]\nv_sub_f32 v228,0,v229\nv_sub_f32 v230,0,v229\nv_sub_f32 v231,0,v229\n" \
"M v[92:95],a[52:55],a[148:151],v[92:95]\nv_sub_f32 v229,0,v229\nds_read_b128 a[32:35],v240 offset:40960\nds_read_b128 a[52:55],v240 offset:46080\nM v[92:95],a[48:51],a[144:147],v[92:95]\nds_read_b128 a[48:51],v240 offset:45056\ns_waitcnt lgkmcnt(7)\nM v[136:139],a[0:3],a[160:163],0\n" \
"ds_read_b128 a[56:59],v240 offset:47104\nv_add_f32 v64,v64,v224\nv_add_f32 v65,v65,v224\nM v[136:139],a[4:7],a[164:167],v[136:139]\nds_read_b128 a[60:63],v240 offset:48128\nv_add_f32 v66,v66,v224\nM v[136:139],a[8:11],a[168:171],v[136:139]\nv_add_f32 v67,v67,v224\n" \
"v_exp_f32 v64,v64\nM v[136:139],a[12:15],a[172:175],v[136:139]\nv_exp_f32 v65,v65\nv_cndmask_b32 v80,v80,v252,s[82:83]\nM v[140:143],a[12:15],a[188:191],0\nv_cndmask_b32 v81,v81,v252,s[84:85]\nv_add_f32 v72,v72,v224\nM v[140:143],a[8:11],a[184:187],v[140:143]\n" \
"v_exp_f32 v66,v66\nv_add_f32 v73,v73,v224\nM v[140:143],a[4:7],a[180:183],v[140:143]\nv_exp_f32 v67,v67\nv_add_f32 v74,v74,v224\nM v[140:143],a[0:3],a[176:179],v[140:143]\nv_add_f32 v75,v75,v224\nv_cndmask_b32 v82,v82,v252,s[86:87]\n" \
"M v[156:159],a[28:31],a[188:191],0\nv_cndmask_b32 v83,v83,v252,s[88:89]\nv_add_f32 v80,v80,v224\nv_add_f32 v81,v81,v224\nM v[156:159],a[24:27],a[184:187],v[156:159]\nv_exp_f32 v72,v72\nv_add_f32 v82,v82,v224\nM v[156:159],a[20:23],a[180:183],v[156:159]\n" \
"v_exp_f32 v73,v73\nM v[156:159],a[16:19],a[176:179],v[156:159]\nv_exp_f32 v74,v74\nv_add_f32 v83,v83,v224\nM v[128:131],a[0:3],a[128:131],v[224:227]\nv_exp_f32 v75,v75\nM v[128:131],a[4:7],a[132:135],v[128:131]\nv_exp_f32 v80,v80\n" \
"v_cvt_pk_bf16_f32 v64,v64,v65\nM v[128:131],a[8:11],a[136:139],v[128:131]\nv_exp_f32 v81,v81\nv_cvt_pk_bf16_f32 v65,v66,v67\nM v[128:131],a[12:15],a[140:143],v[128:131]\nv_exp_f32 v82,v82\nM v[132:135],a[12:15],a[156:159],v[228:231]\nv_exp_f32 v83,v83\n" \
"v_cvt_pk_bf16_f32 v66,v72,v73\nM v[132:135],a[8:11],a[152:155],v[132:135]\nv_cvt_pk_bf16_f32 v67,v74,v75\nv_cvt_pk_bf16_f32 v80,v80,v81\nv_cvt_pk_bf16_f32 v81,v82,v83\nM v[132:135],a[4:7],a[148:151],v[132:135]\nv_mov_b32 v82,0\nv_mov_b32 v83,0\n" \
"M v[132:135],a[0:3],a[144:147],v[132:135]\nds_read_b128 a[88:91],v241 offset:6144\nds_read_b128 a[120:123],v241 offset:14336\nv_add_f32 v68,v68,v228\nM v[144:147],a[16:19],a[128:131],v[224:227]\nds_read_b128 a[92:95],v241 offset:7168\nv_add_f32 v69,v69,v228\nv_add_f32 v70,v70,v228\n" \
"M v[144:147],a[20:23],a[132:135],v[144:147]\nds_read_b128 a[124:127],v241 offset:15360\nv_add_f32 v71,v71,v228\nM v[144:147],a[24:27],a[136:139],v[144:147]\nv_exp_f32 v68,v68\nv_add_f32 v76,v76,v228\nM v[144:147],a[28:31],a[140:143],v[144:147]\nv_exp_f32 v69,v69\n" \
"M v[148:151],a[28:31],a[156:159],v[228:231]\nv_exp_f32 v70,v70\nv_add_f32 v77,v77,v228\nM v[148:151],a[24:27],a[152:155],v[148:151]\nv_exp_f32 v71,v71\nv_add_f32 v78,v78,v228\nM v[148:151],a[20:23],a[148:151],v[148:151]\nv_add_f32 v79,v79,v228\n" \
"v_add_f32 v84,v84,v228\nM v[148:151],a[16:19],a[144:147],v[148:151]\nv_add_f32 v85,v85,v228\nv_cndmask_b32 v92,v92,v252,s[82:83]\ns_waitcnt lgkmcnt(4)\nM v[160:163],a[32:35],a[128:131],v[224:227]\nv_exp_f32 v76,v76\nv_cndmask_b32 v93,v93,v252,s[84:85]\n" \
"M v[160:163],a[36:39],a[132:135],v[160:163]\nv_exp_f32 v77,v77\nM v[160:163],a[40:43],a[136:139],v[160:163]\nv_exp_f32 v78,v78\nv_cndmask_b32 v94,v94,v252,s[86:87]\nM v[160:163],a[44:47],a[140:143],v[160:163]\nv_exp_f32 v79,v79\nM v[164:167],a[44:47],a[156:159],v[228:231]\n" \
"v_cndmask_b32 v95,v95,v252,s[88:89]\nv_add_f32 v86,v86,v228\nv_add_f32 v87,v87,v228\nM v[164:167],a[40:43],a[152:155],v[164:167]\nv_exp_f32 v84,v84\nv_cvt_pk_bf16_f32 v68,v68,v69\nM v[164:167],a[36:39],a[148:151],v[164:167]\nv_exp_f32 v85,v85\n" \
"M v[164:167],a[32:35],a[144:147],v[164:167]\nv_exp_f32 v86,v86\nv_add_f32 v92,v92,v228\nM v[176:179],a[48:51],a[128:131],v[224:227]\nv_exp_f32 v87,v87\nv_add_f32 v93,v93,v228\nM v[176:179],a[52:55],a[132:135],v[176:179]\nv_add_f32 v94,v94,v228\n" \
"v_add_f32 v95,v95,v228\nM v[176:179],a[56:59],a[136:139],v[176:179]\nbuffer_load_dword v248,v251,s[52:55],s80 offen sc1\nv_cvt_pk_bf16_f32 v69,v70,v71\nM v[176:179],a[60:63],a[140:143],v[176:179]\nv_exp_f32 v92,v92\nv_cvt_pk_bf16_f32 v70,v76,v77\nM v[180:183],a[60:63],a[156:159],v[228:231]\n" \
"v_exp_f32 v93,v93\nM v[180:183],a[56:59],a[152:155],v[180:183]\nv_exp_f32 v94,v94\nv_cvt_pk_bf16_f32 v71,v78,v79\nM v[180:183],a[52:55],a[148:151],v[180:183]\nv_exp_f32 v95,v95\nM v[180:183],a[48:51],a[144:147],v[180:183]\nv_cvt_pk_bf16_f32 v84,v84,v85\n" \
"v_cvt_pk_bf16_f32 v85,v86,v87\nv_cvt_pk_bf16_f32 v86,v92,v93\nM v[0:3],a[64:67],v[64:67],0\nv_cvt_pk_bf16_f32 v87,v94,v95\nds_read_b128 a[0:3],v240 offset:49152\nds_read_b128 a[4:7],v240 offset:50176\nM v[0:3],a[96:99],v[80:83],v[0:3]\nds_read_b128 a[8:11],v240 offset:51200\n" \
"v_max3_f32 v237,v140,v141,v142\nM v[8:11],a[68:71],v[64:67],0\nds_read_b128 a[12:15],v240 offset:52224\nv_exp_f32 v128,v128\nM v[8:11],a[100:103],v[80:83],v[8:11]\nds_read_b128 a[16:19],v240 offset:53248\nv_exp_f32 v129,v129\nM v[16:19],a[72:75],v[64:67],0\n" \
"v_exp_f32 v130,v130\nM v[16:19],a[104:107],v[80:83],v[16:19]\nv_exp_f32 v131,v131\nds_read_b128 a[20:23],v240 offset:54272\nM v[24:27],a[76:79],v[64:67],0\nv_cvt_pk_bf16_f32 v192,v128,v129\nv_cvt_pk_bf16_f32 v193,v130,v131\nds_read_b128 a[24:27],v240 offset:55296\n" \
"M v[24:27],a[108:111],v[80:83],v[24:27]\nds_read_b128 a[28:31],v240 offset:56320\nv_max3_f32 v237,v237,v143,v156\nM v[32:35],a[80:83],v[64:67],0\nv_exp_f32 v132,v132\nv_max3_f32 v233,v136,v137,v138\nM v[32:35],a[112:115],v[80:83],v[32:35]\nv_exp_f32 v133,v133\n" \
"M v[40:43],a[84:87],v[64:67],0\nv_exp_f32 v134,v134\nv_cvt_pk_bf16_f32 v196,v132,v133\nM v[40:43],a[116:119],v[80:83],v[40:43]\nv_exp_f32 v135,v135\ns_waitcnt lgkmcnt(4)\nM v[48:51],a[88:91],v[64:67],0\nv_cvt_pk_bf16_f32 v197,v134,v135\n" \
"v_max3_f32 v237,v237,v157,v158\nM v[48:51],a[120:123],v[80:83],v[48:51]\nv_exp_f32 v144,v144\nv_max_f32 v233,v233,v139\nM v[56:59],a[92:95],v[64:67],0\nv_exp_f32 v145,v145\nv_max_f32 v237,v237,v159\nM v[56:59],a[124:127],v[80:83],v[56:59]\n" \
"v_exp_f32 v146,v146\nM a[192:195],a[208:211],v[64:67],0\nv_exp_f32 v147,v147\nv_cvt_pk_bf16_f32 v194,v144,v145\nM a[192:195],a[208:211],v[80:83],a[192:195]\nv_cvt_pk_bf16_f32 v195,v146,v147\nv_cndmask_b32 v233,v232,v233,s[90:91]\nM v[4:7],a[64:67],v[68:71],0\n" \
"v_cndmask_b32 v237,v236,v237,s[90:91]\nv_exp_f32 v148,v148\nM v[4:7],a[96:99],v[84:87],v[4:7]\nv_permlane16_swap_b32 v233,v237\nv_max_f32 v233,v233,v237\nv_mov_b32 v237,v233\nM v[12:15],a[68:71],v[68:71],0\nv_exp_f32 v149,v149\n" \
"M v[12:15],a[100:103],v[84:87],v[12:15]\nv_exp_f32 v150,v150\nv_permlane32_swap_b32 v233,v237\nM v[20:23],a[72:75],v[68:71],0\nv_exp_f32 v151,v151\nv_cvt_pk_bf16_f32 v198,v148,v149\nM v[20:23],a[104:107],v[84:87],v[20:23]\nv_cvt_pk_bf16_f32 v199,v150,v151\n" \
"v_max_f32 v233,v233,v237\nM v[28:31],a[76:79],v[68:71],0\nv_mov_b32 v237,v233\nv_cndmask_b32 v136,v136,v252,s[82:83]\nv_cndmask_b32 v137,v137,v252,s[84:85]\nM v[28:31],a[108:111],v[84:87],v[28:31]\nv_permlane16_swap_b32 v233,v237\nv_cndmask_b32 v138,v138,v252,s[86:87]\n" \
"v_cndmask_b32 v139,v139,v252,s[88:89]\nM v[36:39],a[80:83],v[68:71],0\nv_sub_f32 v232,0,v233\nv_sub_f32 v234,0,v233\nM v[36:39],a[112:115],v[84:87],v[36:39]\nv_sub_f32 v235,0,v233\nv_add_f32 v136,v136,v232\nv_add_f32 v137,v137,v232\n" \
"M v[44:47],a[84:87],v[68:71],0\nv_add_f32 v138,v138,v232\nv_add_f32 v139,v139,v232\nM v[44:47],a[116:119],v[84:87],v[44:47]\nv_sub_f32 v233,0,v233\nv_exp_f32 v136,v136\nM v[52:55],a[88:91],v[68:71],0\nv_exp_f32 v137,v137\n" \
"v_sub_f32 v236,0,v237\nM v[52:55],a[120:123],v[84:87],v[52:55]\nv_exp_f32 v138,v138\nM v[60:63],a[92:95],v[68:71],0\nv_exp_f32 v139,v139\nv_cvt_pk_bf16_f32 v200,v136,v137\nM v[60:63],a[124:127],v[84:87],v[60:63]\nv_cvt_pk_bf16_f32 v201,v138,v139\n" \
"v_sub_f32 v238,0,v237\nv_sub_f32 v239,0,v237\nM a[196:199],a[208:211],v[68:71],0\nv_add_f32 v140,v140,v236\nv_add_f32 v141,v141,v236\nM a[196:199],a[208:211],v[84:87],a[196:199]\nv_add_f32 v142,v142,v236\nv_add_f32 v143,v143,v236\n" \
"v_sub_f32 v237,0,v237\nM v[128:131],a[0:3],a[128:131],v[224:227]\nv_exp_f32 v140,v140\nds_read_b128 a[32:35],v240 offset:57344\nM v[128:131],a[4:7],a[132:135],v[128:131]\nv_exp_f32 v141,v141\nM v[128:131],a[8:11],a[136:139],v[128:131]\nv_exp_f32 v142,v142\n" \
"v_cvt_pk_bf16_f32 v204,v140,v141\nM v[128:131],a[12:15],a[140:143],v[128:131]\nv_exp_f32 v143,v143\nM v[132:135],a[12:15],a[156:159],v[228:231]\nv_cvt_pk_bf16_f32 v205,v142,v143\nds_read_b128 a[36:39],v240 offset:58368\nds_read_b128 a[40:43],v240 offset:59392\nM v[132:135],a[8:11],a[152:155],v[132:135]\n" \
"ds_read_b128 a[44:47],v240 offset:60416\nv_cndmask_b32 v156,v156,v252,s[82:83]\nv_cndmask_b32 v157,v157,v252,s[84:85]\nM v[132:135],a[4:7],a[148:151],v[132:135]\nv_cndmask_b32 v158,v158,v252,s[86:87]\nv_cndmask_b32 v159,v159,v252,s[88:89]\nM v[132:135],a[0:3],a[144:147],v[132:135]\nv_add_f32 v156,v156,v236\n" \
"v_add_f32 v157,v157,v236\ns_waitcnt lgkmcnt(4)\nM v[144:147],a[16:19],a[128:131],v[224:227]\ns_waitcnt vmcnt(1)\ns_barrier\nv_add_f32 v158,v158,v236\nM v[144:147],a[20:23],a[132:135],v[144:147]\nv_add_f32 v159,v159,v236\n" \
"ds_read_b128 a[48:51],v240 offset:61440\nM v[144:147],a[24:27],a[136:139],v[144:147]\nv_exp_f32 v156,v156\nds_read_b128 a[52:55],v240 offset:62464\nM v[144:147],a[28:31],a[140:143],v[144:147]\nv_exp_f32 v157,v157\nM v[148:151],a[28:31],a[156:159],v[228:231]\nv_exp_f32 v158,v158\n" \
"v_cvt_pk_bf16_f32 v206,v156,v157\nM v[148:151],a[24:27],a[152:155],v[148:151]\nv_exp_f32 v159,v159\nds_read_b128 a[56:59],v240 offset:63488\nM v[148:151],a[20:23],a[148:151],v[148:151]\nv_cvt_pk_bf16_f32 v207,v158,v159\nds_read_b128 a[60:63],v240 offset:64512\nM v[148:151],a[16:19],a[144:147],v[148:151]\n" \
"v_exp_f32 v160,v160\nds_read_b128 a[64:67],v241 offset:32768\nM v[136:139],a[0:3],a[160:163],v[232:235]\nv_exp_f32 v161,v161\nds_read_b128 a[96:99],v241 offset:40960\nM v[136:139],a[4:7],a[164:167],v[136:139]\nv_exp_f32 v162,v162\nM v[136:139],a[8:11],a[168:171],v[136:139]\n" \
"v_exp_f32 v163,v163\nv_cvt_pk_bf16_f32 v208,v160,v161\nM v[136:139],a[12:15],a[172:175],v[136:139]\nv_cvt_pk_bf16_f32 v209,v162,v163\nv_exp_f32 v164,v164\nM v[140:143],a[12:15],a[188:191],v[236:239]\nv_exp_f32 v165,v165\nM v[140:143],a[8:11],a[184:187],v[140:143]\n" \
"v_exp_f32 v166,v166\nv_cvt_pk_bf16_f32 v212,v164,v165\nM v[140:143],a[4:7],a[180:183],v[140:143]\nv_exp_f32 v167,v167\nM v[140:143],a[0:3],a[176:179],v[140:143]\nv_cvt_pk_bf16_f32 v213,v166,v167\nv_exp_f32 v176,v176\nM v[152:155],a[16:19],a[160:163],v[232:235]\n" \
"v_exp_f32 v177,v177\nds_read_b128 a[68:71],v241 offset:33792\nM v[152:155],a[20:23],a[164:167],v[152:155]\nv_exp_f32 v178,v178\nM v[152:155],a[24:27],a[168:171],v[152:155]\nv_exp_f32 v179,v179\nv_cvt_pk_bf16_f32 v210,v176,v177\nM v[152:155],a[28:31],a[172:175],v[152:155]\n" \
"v_cvt_pk_bf16_f32 v211,v178,v179\nv_exp_f32 v180,v180\nM v[156:159],a[28:31],a[188:191],v[236:239]\nv_exp_f32 v181,v181\nM v[156:159],a[24:27],a[184:187],v[156:159]\nv_exp_f32 v182,v182\nv_cvt_pk_bf16_f32 v214,v180,v181\nM v[156:159],a[20:23],a[180:183],v[156:159]\n" \
"v_exp_f32 v183,v183\nds_read_b128 a[100:103],v241 offset:41984\nM v[156:159],a[16:19],a[176:179],v[156:159]\nv_cvt_pk_bf16_f32 v215,v182,v183\ns_waitcnt lgkmcnt(2)\nM v[160:163],a[32:35],a[128:131],v[224:227]\nds_read_b128 a[72:75],v241 offset:34816\nds_read_b128 a[104:107],v241 offset:43008\n" \
"s_waitcnt vmcnt(0)\nv_cmp_eq_u32 vcc,0,v248\nM v[160:163],a[36:39],a[132:135],v[160:163]\nds_read_b128 a[76:79],v241 offset:35840\ns_cbranch_vccnz .Lps3\n.Lpb3:\nM v[160:163],a[40:43],a[136:139],v[160:163]\nds_read_b128 a[108:111],v241 offset:44032\n" \
"v_exp_f32 v128,v128\nM v[160:163],a[44:47],a[140:143],v[160:163]\nds_read_b128 a[80:83],v241 offset:36864\nv_exp_f32 v129,v129\nM v[164:167],a[44:47],a[156:159],v[228:231]\nds_read_b128 a[112:115],v241 offset:45056\ns_add_u32 m0,s64,65536\nM v[164:167],a[40:43],a[152:155],v[164:167]\n" \
"ds_read_b128 a[84:87],v241 offset:37888\nbuffer_load_dwordx4 v240,s[48:51],s36 offen offset:0 sc1 lds\nM v[164:167],a[36:39],a[148:151],v[164:167]\nds_read_b128 a[116:119],v241 offset:46080\nv_exp_f32 v130,v130\nM v[164:167],a[32:35],a[144:147],v[164:167]\nv_exp_f32 v131,v131\nM v[176:179],a[48:51],a[128:131],v[224:227]\n" \
"ds_read_b128 a[88:91],v241 offset:38912\nv_exp_f32 v132,v132\nM v[176:179],a[52:55],a[132:135],v[176:179]\nds_read_b128 a[120:123],v241 offset:47104\nds_read_b128 a[92:95],v241 offset:39936\nM v[176:179],a[56:59],a[136:139],v[176:179]\nds_read_b128 a[124:127],v241 offset:48128\nbuffer_load_dwordx4 v240,s[48:51],s36 offen offset:1024 sc1 lds\n" \
"M v[176:179],a[60:63],a[140:143],v[176:179]\nds_read_b128 a[0:3],v240 offset:0\nv_exp_f32 v133,v133\nM v[180:183],a[60:63],a[156:159],v[228:231]\nv_exp_f32 v134,v134\nM v[180:183],a[56:59],a[152:155],v[180:183]\nds_read_b128 a[4:7],v240 offset:1024\nv_exp_f32 v135,v135\n" \
"M v[180:183],a[52:55],a[148:151],v[180:183]\nds_read_b128 a[8:11],v240 offset:2048\nbuffer_load_dwordx4 v240,s[48:51],s36 offen offset:2048 sc1 lds\nM v[180:183],a[48:51],a[144:147],v[180:183]\nds_read_b128 a[12:15],v240 offset:3072\nds_read_b128 a[16:19],v240 offset:4096\nM v[168:171],a[32:35],a[160:163],v[232:235]\nds_read_b128 a[20:23],v240 offset:5120\n" \
"v_exp_f32 v144,v144\nM v[168:171],a[36:39],a[164:167],v[168:171]\nds_read_b128 a[24:27],v240 offset:6144\nv_exp_f32 v145,v145\nM v[168:171],a[40:43],a[168:171],v[168:171]\nbuffer_load_dwordx4 v240,s[48:51],s36 offen offset:3072 sc1 lds\nM v[168:171],a[44:47],a[172:175],v[168:171]\nds_read_b128 a[28:31],v240 offset:7168\n" \
"v_exp_f32 v146,v146\nM v[172:175],a[44:47],a[188:191],v[236:239]\nv_exp_f32 v147,v147\nM v[172:175],a[40:43],a[184:187],v[172:175]\ns_add_u32 m0,s64,16384\nv_exp_f32 v148,v148\nM v[172:175],a[36:39],a[180:183],v[172:175]\nbuffer_load_dwordx4 v240,s[44:47],s65 offen offset:0 sc1 lds\n" \
"M v[172:175],a[32:35],a[176:179],v[172:175]\nds_read_b128 a[32:35],v240 offset:8192\nds_read_b128 a[36:39],v240 offset:9216\nM v[184:187],a[48:51],a[160:163],v[232:235]\nds_read_b128 a[40:43],v240 offset:10240\nv_exp_f32 v149,v149\nM v[184:187],a[52:55],a[164:167],v[184:187]\nds_read_b128 a[44:47],v240 offset:11264\n" \
"v_exp_f32 v150,v150\nM v[184:187],a[56:59],a[168:171],v[184:187]\nbuffer_load_dwordx4 v240,s[44:47],s65 offen offset:1024 sc1 lds\nM v[184:187],a[60:63],a[172:175],v[184:187]\nv_exp_f32 v151,v151\nM v[188:191],a[60:63],a[188:191],v[236:239]\nv_exp_f32 v136,v136\nM v[188:191],a[56:59],a[184:187],v[188:191]\n" \
"v_exp_f32 v137,v137\nM v[188:191],a[52:55],a[180:183],v[188:191]\nbuffer_load_dwordx4 v240,s[44:47],s65 offen offset:2048 sc1 lds\nM v[188:191],a[48:51],a[176:179],v[188:191]\nds_read_b128 a[48:51],v240 offset:12288\nds_read_b128 a[52:55],v240 offset:13312\nM v[0:3],a[64:67],v[192:195],v[0:3]\nds_read_b128 a[56:59],v240 offset:14336\n" \
"v_exp_f32 v138,v138\nM v[0:3],a[96:99],v[208:211],v[0:3]\nv_exp_f32 v139,v139\ns_waitcnt lgkmcnt(4)\nM v[8:11],a[68:71],v[192:195],v[8:11]\nbuffer_load_dwordx4 v240,s[44:47],s65 offen offset:3072 sc1 lds\nM v[8:11],a[100:103],v[208:211],v[8:11]\ns_add_u32 m0,s64,81920\n" \
"v_exp_f32 v152,v152\nM v[16:19],a[72:75],v[192:195],v[16:19]\nds_read_b128 a[60:63],v240 offset:15360\nv_exp_f32 v153,v153\nM v[16:19],a[104:107],v[208:211],v[16:19]\nv_exp_f32 v154,v154\nM v[24:27],a[76:79],v[192:195],v[24:27]\nbuffer_load_dwordx4 v240,s[48:51],s65 offen offset:0 sc1 lds\n" \
"M v[24:27],a[108:111],v[208:211],v[24:27]\nv_exp_f32 v155,v155\nM v[32:35],a[80:83],v[192:195],v[32:35]\ns_barrier\nv_exp_f32 v160,v160\nM v[32:35],a[112:115],v[208:211],v[32:35]\nv_exp_f32 v161,v161\nM v[40:43],a[84:87],v[192:195],v[40:43]\n" \
"buffer_load_dwordx4 v240,s[48:51],s65 offen offset:1024 sc1 lds\nM v[40:43],a[116:119],v[208:211],v[40:43]\nv_exp_f32 v162,v162\nM v[48:51],a[88:91],v[192:195],v[48:51]\nv_exp_f32 v163,v163\nM v[48:51],a[120:123],v[208:211],v[48:51]\nv_exp_f32 v176,v176\nM v[56:59],a[92:95],v[192:195],v[56:59]\n" \
"buffer_load_dwordx4 v240,s[48:51],s65 offen offset:2048 sc1 lds\nM v[56:59],a[124:127],v[208:211],v[56:59]\nv_exp_f32 v177,v177\nM a[192:195],a[208:211],v[192:195],a[192:195]\nv_exp_f32 v178,v178\nM a[192:195],a[208:211],v[208:211],a[192:195]\nv_exp_f32 v179,v179\nv_cvt_pk_bf16_f32 v192,v128,v129\n" \
"M v[4:7],a[64:67],v[196:199],v[4:7]\nbuffer_load_dwordx4 v240,s[48:51],s65 offen offset:3072 sc1 lds\nM v[4:7],a[96:99],v[212:215],v[4:7]\ns_add_u32 m0,s64,32768\nv_exp_f32 v140,v140\nM v[12:15],a[68:71],v[196:199],v[12:15]\nv_exp_f32 v141,v141\nv_cvt_pk_bf16_f32 v193,v130,v131\n" \
"M v[12:15],a[100:103],v[212:215],v[12:15]\nv_exp_f32 v142,v142\nM v[20:23],a[72:75],v[196:199],v[20:23]\nbuffer_load_dwordx4 v240,s[44:47],s66 offen offset:0 sc1 lds\nv_cvt_pk_bf16_f32 v194,v144,v145\nM v[20:23],a[104:107],v[212:215],v[20:23]\nv_exp_f32 v143,v143\nv_cvt_pk_bf16_f32 v195,v146,v147\n" \
"M v[28:31],a[76:79],v[196:199],v[28:31]\nv_exp_f32 v156,v156\nM v[28:31],a[108:111],v[212:215],v[28:31]\nv_exp_f32 v157,v157\nv_cvt_pk_bf16_f32 v208,v160,v161\nM v[36:39],a[80:83],v[196:199],v[36:39]\nbuffer_load_dwordx4 v240,s[44:47],s66 offen offset:1024 sc1 lds\nM v[36:39],a[112:115],v[212:215],v[36:39]\n" \
"v_exp_f32 v158,v158\nv_cvt_pk_bf16_f32 v209,v162,v163\nM v[44:47],a[84:87],v[196:199],v[44:47]\nv_exp_f32 v159,v159\nv_cvt_pk_bf16_f32 v210,v176,v177\nM v[44:47],a[116:119],v[212:215],v[44:47]\nv_exp_f32 v164,v164\nM v[52:55],a[88:91],v[196:199],v[52:55]\n" \
"buffer_load_dwordx4 v240,s[44:47],s66 offen offset:2048 sc1 lds\nv_cvt_pk_bf16_f32 v211,v178,v179\nM v[52:55],a[120:123],v[212:215],v[52:55]\nv_exp_f32 v165,v165\nM v[60:63],a[92:95],v[196:199],v[60:63]\nv_exp_f32 v166,v166\nM v[60:63],a[124:127],v[212:215],v[60:63]\nv_exp_f32 v167,v167\n" \
"M a[196:199],a[208:211],v[196:199],a[196:199]\nbuffer_load_dwordx4 v240,s[44:47],s66 offen offset:3072 sc1 lds\ns_add_u32 m0,s64,98304\nM a[196:199],a[208:211],v[212:215],a[196:199]\nv_exp_f32 v180,v180\nM v[64:67],a[64:67],v[200:203],0\nv_exp_f32 v181,v181\nv_cvt_pk_bf16_f32 v196,v132,v133\n" \
"M v[72:75],a[68:71],v[200:203],0\nv_exp_f32 v182,v182\nM v[80:83],a[72:75],v[200:203],0\nv_exp_f32 v183,v183\nv_cvt_pk_bf16_f32 v197,v134,v135\nM v[88:91],a[76:79],v[200:203],0\nv_exp_f32 v168,v168\nv_cvt_pk_bf16_f32 v198,v148,v149\n" \
"M v[96:99],a[80:83],v[200:203],0\nv_exp_f32 v169,v169\nM v[104:107],a[84:87],v[200:203],0\nv_exp_f32 v170,v170\nv_cvt_pk_bf16_f32 v199,v150,v151\nM v[112:115],a[88:91],v[200:203],0\nv_exp_f32 v171,v171\nM v[120:123],a[92:95],v[200:203],0\n" \
"buffer_load_dwordx4 v240,s[48:51],s66 offen offset:0 sc1 lds\nM a[200:203],a[208:211],v[200:203],0\nv_cvt_pk_bf16_f32 v200,v136,v137\nv_cvt_pk_bf16_f32 v201,v138,v139\nv_cvt_pk_bf16_f32 v202,v152,v153\nM v[68:71],a[64:67],v[204:207],0\nv_cvt_pk_bf16_f32 v203,v154,v155\nM v[76:79],a[68:71],v[204:207],0\n" \
"M v[84:87],a[72:75],v[204:207],0\nbuffer_load_dwordx4 v240,s[48:51],s66 offen offset:1024 sc1 lds\nM v[92:95],a[76:79],v[204:207],0\nM v[100:103],a[80:83],v[204:207],0\nM v[108:111],a[84:87],v[204:207],0\nM v[116:119],a[88:91],v[204:207],0\nbuffer_load_dwordx4 v240,s[48:51],s66 offen offset:2048 sc1 lds\nM v[124:127],a[92:95],v[204:207],0\n" \
"buffer_load_dwordx4 v240,s[48:51],s66 offen offset:3072 sc1 lds\nM a[204:207],a[208:211],v[204:207],0\ns_cmp_lt_u32 s62,2\ns_cbranch_scc1 .Lx01\ns_branch .Ldual\n.Lp23:\ns_cmp_eq_u32 s7,2\ns_cbranch_scc0 .Lpro3\n" \
".Lpro2:\ns_barrier\nM v[64:67],a[0:3],a[128:131],0\nds_read_b128 a[64:67],v241 offset:0\nds_read_b128 a[96:99],v241 offset:8192\nM v[64:67],a[4:7],a[132:135],v[64:67]\nds_read_b128 a[68:71],v241 offset:1024\ns_mov_b32 m0,s64\n" \
"v_mov_b32 v250,139264\nM v[64:67],a[8:11],a[136:139],v[64:67]\nds_read_b128 a[100:103],v241 offset:9216\nbuffer_load_dwordx4 v240,s[44:47],s36 offen offset:0 sc1 lds\nM v[64:67],a[12:15],a[140:143],v[64:67]\nds_read_b128 a[72:75],v241 offset:2048\nv_mov_b32 v184,0xff800000\nv_mov_b32 v185,0xff800000\n" \
"M v[68:71],a[12:15],a[156:159],0\nds_read_b128 a[12:15],v240 offset:19456\nv_mov_b32 v186,0xff800000\nv_mov_b32 v187,0xff800000\nM v[68:71],a[8:11],a[152:155],v[68:71]\nds_read_b128 a[8:11],v240 offset:18432\nv_max3_f32 v225,v64,v65,v66\nM v[68:71],a[4:7],a[148:151],v[68:71]\n" \
"ds_read_b128 a[4:7],v240 offset:17408\nbuffer_load_dwordx4 v240,s[44:47],s36 offen offset:1024 sc1 lds\nM v[68:71],a[0:3],a[144:147],v[68:71]\nds_read_b128 a[0:3],v240 offset:16384\nM v[72:75],a[16:19],a[128:131],0\nds_read_b128 a[104:107],v241 offset:10240\nM v[72:75],a[20:23],a[132:135],v[72:75]\nds_read_b128 a[76:79],v241 offset:3072\n" \
"M v[72:75],a[24:27],a[136:139],v[72:75]\nds_read_b128 a[108:111],v241 offset:11264\nbuffer_load_dwordx4 v240,s[44:47],s36 offen offset:2048 sc1 lds\nM v[72:75],a[28:31],a[140:143],v[72:75]\nv_max3_f32 v229,v68,v69,v70\nds_write_b128 v250,a[244:247]\nM v[76:79],a[28:31],a[156:159],0\nds_read_b128 a[28:31],v240 offset:23552\n" \
"M v[76:79],a[24:27],a[152:155],v[76:79]\nds_read_b128 a[24:27],v240 offset:22528\nM v[76:79],a[20:23],a[148:151],v[76:79]\nds_read_b128 a[20:23],v240 offset:21504\nv_max3_f32 v225,v225,v67,v72\nv_max3_f32 v225,v225,v73,v74\nM v[76:79],a[16:19],a[144:147],v[76:79]\nv_max_f32 v225,v225,v75\n" \
"v_cndmask_b32 v225,v224,v225,s[90:91]\nds_read_b128 a[16:19],v240 offset:20480\nM v[80:83],a[32:35],a[128:131],0\nds_read_b128 a[80:83],v241 offset:4096\nbuffer_load_dwordx4 v240,s[44:47],s36 offen offset:3072 sc1 lds\nM v[80:83],a[36:39],a[132:135],v[80:83]\nds_read_b128 a[112:115],v241 offset:12288\nv_max3_f32 v229,v229,v71,v76\n" \
"v_max3_f32 v229,v229,v77,v78\nM v[80:83],a[40:43],a[136:139],v[80:83]\nv_max_f32 v229,v229,v79\nv_cndmask_b32 v229,v228,v229,s[90:91]\nds_read_b128 a[84:87],v241 offset:5120\nM v[80:83],a[44:47],a[140:143],v[80:83]\nv_permlane16_swap_b32 v225,v229\nv_max_f32 v225,v225,v229\n" \
"v_mov_b32 v229,v225\nM v[84:87],a[44:47],a[156:159],0\nds_read_b128 a[116:119],v241 offset:13312\nv_permlane32_swap_b32 v225,v229\nv_max_f32 v225,v225,v229\nM v[84:87],a[40:43],a[152:155],v[84:87]\nv_mov_b32 v229,v225\nds_read_b128 a[88:91],v241 offset:6144\n" \
"ds_read_b128 a[120:123],v241 offset:14336\nM v[84:87],a[36:39],a[148:151],v[84:87]\nv_permlane16_swap_b32 v225,v229\nv_sub_f32 v224,0,v225\nv_sub_f32 v226,0,v225\nM v[84:87],a[32:35],a[144:147],v[84:87]\nv_sub_f32 v227,0,v225\nv_sub_f32 v225,0,v225\n" \
"v_sub_f32 v228,0,v229\nM v[88:91],a[48:51],a[128:131],0\nv_sub_f32 v230,0,v229\nv_sub_f32 v231,0,v229\nM v[88:91],a[52:55],a[132:135],v[88:91]\nv_sub_f32 v229,0,v229\nv_add_f32 v64,v64,v224\nv_add_f32 v65,v65,v224\n" \
"M v[88:91],a[56:59],a[136:139],v[88:91]\nv_add_f32 v66,v66,v224\nv_add_f32 v67,v67,v224\nv_add_f32 v72,v72,v224\nM v[88:91],a[60:63],a[140:143],v[88:91]\nv_exp_f32 v64,v64\nv_add_f32 v73,v73,v224\nM v[92:95],a[60:63],a[156:159],0\n" \
"v_exp_f32 v65,v65\nv_add_f32 v74,v74,v224\nM v[92:95],a[56:59],a[152:155],v[92:95]\nv_exp_f32 v66,v66\nv_add_f32 v75,v75,v224\nM v[92:95],a[52:55],a[148:151],v[92:95]\nv_exp_f32 v67,v67\nv_add_f32 v80,v80,v224\n" \
"M v[92:95],a[48:51],a[144:147],v[92:95]\nv_add_f32 v81,v81,v224\nv_add_f32 v82,v82,v224\ns_waitcnt lgkmcnt(0)\nM v[96:99],a[0:3],a[128:131],v[224:227]\nv_exp_f32 v72,v72\nv_add_f32 v83,v83,v224\nM v[96:99],a[4:7],a[132:135],v[96:99]\n" \
"v_exp_f32 v73,v73\nv_cvt_pk_bf16_f32 v64,v64,v65\nM v[96:99],a[8:11],a[136:139],v[96:99]\nv_exp_f32 v74,v74\nv_add_f32 v88,v88,v224\nM v[96:99],a[12:15],a[140:143],v[96:99]\nv_exp_f32 v75,v75\nv_add_f32 v89,v89,v224\n" \
"M v[100:103],a[12:15],a[156:159],v[228:231]\nv_exp_f32 v80,v80\nv_add_f32 v90,v90,v224\nM v[100:103],a[8:11],a[152:155],v[100:103]\nv_exp_f32 v81,v81\nM v[100:103],a[4:7],a[148:151],v[100:103]\nv_exp_f32 v82,v82\nv_add_f32 v91,v91,v224\n" \
"M v[100:103],a[0:3],a[144:147],v[100:103]\nv_exp_f32 v83,v83\nv_cvt_pk_bf16_f32 v65,v66,v67\nM v[108:111],a[28:31],a[156:159],v[228:231]\nv_exp_f32 v88,v88\nv_cvt_pk_bf16_f32 v66,v72,v73\nM v[108:111],a[24:27],a[152:155],v[108:111]\nv_exp_f32 v89,v89\n" \
"v_cvt_pk_bf16_f32 v67,v74,v75\nM v[108:111],a[20:23],a[148:151],v[108:111]\nv_exp_f32 v90,v90\nv_exp_f32 v91,v91\nv_cvt_pk_bf16_f32 v80,v80,v81\nM v[108:111],a[16:19],a[144:147],v[108:111]\nv_cvt_pk_bf16_f32 v81,v82,v83\nv_cvt_pk_bf16_f32 v82,v88,v89\n" \
"v_cvt_pk_bf16_f32 v83,v90,v91\nM v[0:3],a[64:67],v[64:67],0\nds_read_b128 a[92:95],v241 offset:7168\nds_read_b128 a[124:127],v241 offset:15360\nv_add_f32 v68,v68,v228\nM v[0:3],a[96:99],v[80:83],v[0:3]\nv_add_f32 v69,v69,v228\nv_add_f32 v70,v70,v228\n" \
"v_add_f32 v71,v71,v228\nM v[8:11],a[68:71],v[64:67],0\nv_exp_f32 v68,v68\nv_add_f32 v76,v76,v228\nM v[8:11],a[100:103],v[80:83],v[8:11]\nv_exp_f32 v69,v69\nv_add_f32 v77,v77,v228\nM v[16:19],a[72:75],v[64:67],0\n" \
"v_exp_f32 v70,v70\nv_add_f32 v78,v78,v228\nM v[16:19],a[104:107],v[80:83],v[16:19]\nv_exp_f32 v71,v71\nM v[24:27],a[76:79],v[64:67],0\nv_add_f32 v79,v79,v228\nv_add_f32 v84,v84,v228\nv_add_f32 v85,v85,v228\n" \
"M v[24:27],a[108:111],v[80:83],v[24:27]\nv_exp_f32 v76,v76\nv_add_f32 v86,v86,v228\nM v[32:35],a[80:83],v[64:67],0\nv_exp_f32 v77,v77\nv_add_f32 v87,v87,v228\nM v[32:35],a[112:115],v[80:83],v[32:35]\nv_exp_f32 v78,v78\n" \
"v_cvt_pk_bf16_f32 v68,v68,v69\nM v[40:43],a[84:87],v[64:67],0\nv_exp_f32 v79,v79\nv_add_f32 v92,v92,v228\nM v[40:43],a[116:119],v[80:83],v[40:43]\nv_exp_f32 v84,v84\nv_add_f32 v93,v93,v228\nM v[48:51],a[88:91],v[64:67],0\n" \
"v_exp_f32 v85,v85\nv_add_f32 v94,v94,v228\nM v[48:51],a[120:123],v[80:83],v[48:51]\nv_exp_f32 v86,v86\ns_waitcnt lgkmcnt(0)\nM v[56:59],a[92:95],v[64:67],0\nv_exp_f32 v87,v87\nv_add_f32 v95,v95,v228\n" \
"M v[56:59],a[124:127],v[80:83],v[56:59]\nv_cvt_pk_bf16_f32 v69,v70,v71\nv_exp_f32 v92,v92\nM a[192:195],a[208:211],v[64:67],0\nv_exp_f32 v93,v93\nv_exp_f32 v94,v94\nv_exp_f32 v95,v95\nv_cvt_pk_bf16_f32 v70,v76,v77\n" \
"v_cvt_pk_bf16_f32 v71,v78,v79\nv_cvt_pk_bf16_f32 v84,v84,v85\nM a[192:195],a[208:211],v[80:83],a[192:195]\nv_cvt_pk_bf16_f32 v85,v86,v87\nv_cvt_pk_bf16_f32 v86,v92,v93\nv_cvt_pk_bf16_f32 v87,v94,v95\nM v[4:7],a[64:67],v[68:71],0\nds_read_b128 a[0:3],v240 offset:32768\n" \
"ds_read_b128 a[4:7],v240 offset:33792\nM v[4:7],a[96:99],v[84:87],v[4:7]\nds_read_b128 a[8:11],v240 offset:34816\nv_cndmask_b32 v96,v96,v252,s[82:83]\nv_cndmask_b32 v97,v97,v252,s[84:85]\nM v[12:15],a[68:71],v[68:71],0\nds_read_b128 a[12:15],v240 offset:35840\nv_cndmask_b32 v98,v98,v252,s[86:87]\n" \
"v_cndmask_b32 v99,v99,v252,s[88:89]\nM v[12:15],a[100:103],v[84:87],v[12:15]\nds_read_b128 a[16:19],v240 offset:36864\nv_exp_f32 v96,v96\nM v[20:23],a[72:75],v[68:71],0\nds_read_b128 a[20:23],v240 offset:37888\nv_exp_f32 v97,v97\nM v[20:23],a[104:107],v[84:87],v[20:23]\n" \
"ds_read_b128 a[24:27],v240 offset:38912\nv_exp_f32 v98,v98\nM v[28:31],a[76:79],v[68:71],0\nbuffer_load_dword v248,v251,s[52:55],s80 offen sc1\nds_read_b128 a[28:31],v240 offset:39936\nM v[28:31],a[108:111],v[84:87],v[28:31]\nds_read_b128 a[32:35],v240 offset:40960\nv_exp_f32 v99,v99\n" \
"M v[36:39],a[80:83],v[68:71],0\nds_read_b128 a[36:39],v240 offset:41984\nv_cvt_pk_bf16_f32 v96,v96,v97\nv_cvt_pk_bf16_f32 v97,v98,v99\nM v[36:39],a[112:115],v[84:87],v[36:39]\nds_read_b128 a[40:43],v240 offset:43008\nv_mov_b32 v98,0\nv_mov_b32 v99,0\n" \
"M v[44:47],a[84:87],v[68:71],0\nds_read_b128 a[44:47],v240 offset:44032\nv_exp_f32 v100,v100\nM v[44:47],a[116:119],v[84:87],v[44:47]\nds_read_b128 a[48:51],v240 offset:45056\nv_exp_f32 v101,v101\nM v[52:55],a[88:91],v[68:71],0\nds_read_b128 a[52:55],v240 offset:46080\n" \
"v_cndmask_b32 v108,v108,v252,s[82:83]\nM v[52:55],a[120:123],v[84:87],v[52:55]\nds_read_b128 a[56:59],v240 offset:47104\nv_cndmask_b32 v109,v109,v252,s[84:85]\nv_cndmask_b32 v110,v110,v252,s[86:87]\nM v[60:63],a[92:95],v[68:71],0\nds_read_b128 a[60:63],v240 offset:48128\nv_cndmask_b32 v111,v111,v252,s[88:89]\n" \
"v_cvt_pk_bf16_f32 v100,v100,v101\nM v[60:63],a[124:127],v[84:87],v[60:63]\nds_read_b128 a[64:67],v241 offset:16384\nv_exp_f32 v102,v102\nM a[196:199],a[208:211],v[68:71],0\nds_read_b128 a[68:71],v241 offset:17408\nv_exp_f32 v103,v103\nM a[196:199],a[208:211],v[84:87],a[196:199]\n" \
"ds_read_b128 a[72:75],v241 offset:18432\nv_cvt_pk_bf16_f32 v101,v102,v103\ns_waitcnt lgkmcnt(8)\nM v[128:131],a[0:3],a[128:131],v[224:227]\nds_read_b128 a[76:79],v241 offset:19456\nv_exp_f32 v108,v108\nM v[128:131],a[4:7],a[132:135],v[128:131]\nds_read_b128 a[80:83],v241 offset:20480\n" \
"v_exp_f32 v109,v109\nM v[128:131],a[8:11],a[136:139],v[128:131]\nds_read_b128 a[84:87],v241 offset:21504\nv_exp_f32 v110,v110\nM v[128:131],a[12:15],a[140:143],v[128:131]\nds_read_b128 a[88:91],v241 offset:22528\nv_exp_f32 v111,v111\nM v[132:135],a[12:15],a[156:159],v[228:231]\n" \
"ds_read_b128 a[92:95],v241 offset:23552\nv_cvt_pk_bf16_f32 v102,v108,v109\nv_cvt_pk_bf16_f32 v103,v110,v111\nM v[132:135],a[8:11],a[152:155],v[132:135]\nds_read_b128 a[8:11],v240 offset:51200\nv_exp_f32 v128,v128\nM v[132:135],a[4:7],a[148:151],v[132:135]\nds_read_b128 a[4:7],v240 offset:50176\n" \
"v_exp_f32 v129,v129\nM v[132:135],a[0:3],a[144:147],v[132:135]\nds_read_b128 a[0:3],v240 offset:49152\nv_cvt_pk_bf16_f32 v192,v128,v129\nM v[144:147],a[16:19],a[128:131],v[224:227]\nds_read_b128 a[12:15],v240 offset:52224\nv_exp_f32 v130,v130\nM v[144:147],a[20:23],a[132:135],v[144:147]\n" \
"v_exp_f32 v131,v131\nds_read_b128 a[96:99],v241 offset:40960\nM v[144:147],a[24:27],a[136:139],v[144:147]\nv_cvt_pk_bf16_f32 v193,v130,v131\nv_exp_f32 v132,v132\nM v[144:147],a[28:31],a[140:143],v[144:147]\nv_exp_f32 v133,v133\nds_read_b128 v[64:67],v250\n" \
"M v[148:151],a[28:31],a[156:159],v[228:231]\nds_read_b128 a[28:31],v240 offset:56320\nv_exp_f32 v134,v134\nM v[148:151],a[24:27],a[152:155],v[148:151]\nds_read_b128 a[24:27],v240 offset:55296\nv_exp_f32 v135,v135\nM v[148:151],a[20:23],a[148:151],v[148:151]\nds_read_b128 a[20:23],v240 offset:54272\n" \
"v_cvt_pk_bf16_f32 v196,v132,v133\nv_cvt_pk_bf16_f32 v197,v134,v135\nM v[148:151],a[16:19],a[144:147],v[148:151]\nds_read_b128 a[16:19],v240 offset:53248\nv_exp_f32 v144,v144\nM v[160:163],a[32:35],a[128:131],v[224:227]\nv_exp_f32 v145,v145\nds_read_b128 v[68:71],v250\n" \
"M v[160:163],a[36:39],a[132:135],v[160:163]\nv_exp_f32 v146,v146\nv_cvt_pk_bf16_f32 v194,v144,v145\nM v[160:163],a[40:43],a[136:139],v[160:163]\nv_exp_f32 v147,v147\ns_waitcnt lgkmcnt(6)\nM v[160:163],a[44:47],a[140:143],v[160:163]\nv_cvt_pk_bf16_f32 v195,v146,v147\n" \
"ds_read_b128 v[72:75],v250\nM v[164:167],a[44:47],a[156:159],v[228:231]\nds_read_b128 a[44:47],v240 offset:60416\nv_exp_f32 v148,v148\nM v[164:167],a[40:43],a[152:155],v[164:167]\nds_read_b128 a[40:43],v240 offset:59392\nv_exp_f32 v149,v149\nM v[164:167],a[36:39],a[148:151],v[164:167]\n" \
"ds_read_b128 a[36:39],v240 offset:58368\nv_exp_f32 v150,v150\nM v[164:167],a[32:35],a[144:147],v[164:167]\nds_read_b128 a[32:35],v240 offset:57344\nv_exp_f32 v151,v151\nM v[176:179],a[48:51],a[128:131],v[224:227]\nv_cvt_pk_bf16_f32 v198,v148,v149\nv_cvt_pk_bf16_f32 v199,v150,v151\n" \
"ds_read_b128 v[76:79],v250\nM v[176:179],a[52:55],a[132:135],v[176:179]\nv_exp_f32 v160,v160\nds_read_b128 v[80:83],v250\nM v[176:179],a[56:59],a[136:139],v[176:179]\nv_exp_f32 v161,v161\nds_read_b128 v[84:87],v250\nM v[176:179],a[60:63],a[140:143],v[176:179]\n" \
"v_exp_f32 v162,v162\nv_cvt_pk_bf16_f32 v208,v160,v161\nM v[180:183],a[60:63],a[156:159],v[228:231]\nv_exp_f32 v163,v163\nds_read_b128 a[60:63],v240 offset:64512\nM v[180:183],a[56:59],a[152:155],v[180:183]\nv_cvt_pk_bf16_f32 v209,v162,v163\nds_read_b128 a[56:59],v240 offset:63488\n" \
"ds_read_b128 v[88:91],v250\nM v[180:183],a[52:55],a[148:151],v[180:183]\nds_read_b128 a[52:55],v240 offset:62464\nv_exp_f32 v164,v164\nM v[180:183],a[48:51],a[144:147],v[180:183]\nds_read_b128 a[48:51],v240 offset:61440\nv_exp_f32 v165,v165\nM v[0:3],a[64:67],v[96:99],v[0:3]\n" \
"v_exp_f32 v166,v166\nM v[8:11],a[68:71],v[96:99],v[8:11]\nv_exp_f32 v167,v167\nv_cvt_pk_bf16_f32 v212,v164,v165\nM v[16:19],a[72:75],v[96:99],v[16:19]\nv_cvt_pk_bf16_f32 v213,v166,v167\nv_exp_f32 v176,v176\nM v[24:27],a[76:79],v[96:99],v[24:27]\n" \
"v_exp_f32 v177,v177\nds_read_b128 v[92:95],v250\nM v[32:35],a[80:83],v[96:99],v[32:35]\nv_exp_f32 v178,v178\nv_cvt_pk_bf16_f32 v210,v176,v177\nM v[40:43],a[84:87],v[96:99],v[40:43]\nv_exp_f32 v179,v179\nds_read_b128 v[104:107],v250\n" \
"M v[48:51],a[88:91],v[96:99],v[48:51]\ns_waitcnt vmcnt(0)\ns_barrier\nv_cvt_pk_bf16_f32 v211,v178,v179\nM v[56:59],a[92:95],v[96:99],v[56:59]\nv_exp_f32 v180,v180\nds_read_b128 v[108:111],v250\nM a[192:195],a[208:211],v[96:99],a[192:195]\n" \
"v_exp_f32 v181,v181\nds_read_b128 v[96:99],v250\nM v[4:7],a[64:67],v[100:103],v[4:7]\nv_exp_f32 v182,v182\nv_cvt_pk_bf16_f32 v214,v180,v181\nM v[12:15],a[68:71],v[100:103],v[12:15]\nv_exp_f32 v183,v183\nds_read_b128 a[64:67],v241 offset:32768\n" \
"M v[20:23],a[72:75],v[100:103],v[20:23]\nv_cvt_pk_bf16_f32 v215,v182,v183\nds_read_b128 a[68:71],v241 offset:33792\nds_read_b128 v[112:115],v250\nM v[28:31],a[76:79],v[100:103],v[28:31]\nds_read_b128 v[116:119],v250\nv_cmp_eq_u32 vcc,0,v248\nM v[36:39],a[80:83],v[100:103],v[36:39]\n" \
"ds_read_b128 v[120:123],v250\ns_cbranch_vccnz .Lps4\n.Lpb4:\ns_add_u32 m0,s64,65536\nM v[44:47],a[84:87],v[100:103],v[44:47]\nds_read_b128 v[124:127],v250\nbuffer_load_dwordx4 v240,s[48:51],s36 offen offset:0 sc1 lds\nM v[52:55],a[88:91],v[100:103],v[52:55]\n" \
"ds_read_b128 a[200:203],v250\nM v[60:63],a[92:95],v[100:103],v[60:63]\nds_read_b128 a[204:207],v250\nM a[196:199],a[208:211],v[100:103],a[196:199]\nds_read_b128 v[100:103],v250\nv_add_u32 v250,0xc000,v241\nM v[136:139],a[0:3],a[160:163],0\nds_read_b128 a[100:103],v241 offset:41984\n" \
"buffer_load_dwordx4 v240,s[48:51],s36 offen offset:1024 sc1 lds\nM v[136:139],a[4:7],a[164:167],v[136:139]\nds_read_b128 a[72:75],v241 offset:34816\nM v[136:139],a[8:11],a[168:171],v[136:139]\nds_read_b128 a[104:107],v241 offset:43008\nM v[136:139],a[12:15],a[172:175],v[136:139]\nds_read_b128 a[76:79],v241 offset:35840\nM v[140:143],a[12:15],a[188:191],0\n" \
"ds_read_b128 a[108:111],v241 offset:44032\nbuffer_load_dwordx4 v240,s[48:51],s36 offen offset:2048 sc1 lds\nM v[140:143],a[8:11],a[184:187],v[140:143]\nds_read_b128 a[80:83],v241 offset:36864\nM v[140:143],a[4:7],a[180:183],v[140:143]\nds_read_b128 a[112:115],v241 offset:45056\nv_max3_f32 v233,v136,v137,v138\nM v[140:143],a[0:3],a[176:179],v[140:143]\n" \
"ds_read_b128 a[84:87],v241 offset:37888\ns_waitcnt lgkmcnt(11)\nM v[152:155],a[16:19],a[160:163],0\nds_read_b128 a[116:119],v241 offset:46080\nbuffer_load_dwordx4 v240,s[48:51],s36 offen offset:3072 sc1 lds\nM v[152:155],a[20:23],a[164:167],v[152:155]\nds_read_b128 a[88:91],v241 offset:38912\ns_add_u32 m0,s64,16384\n" \
"v_max3_f32 v237,v140,v141,v142\nM v[152:155],a[24:27],a[168:171],v[152:155]\nds_read_b128 a[120:123],v241 offset:47104\nM v[152:155],a[28:31],a[172:175],v[152:155]\nds_read_b128 a[92:95],v241 offset:39936\nM v[156:159],a[28:31],a[188:191],0\nds_read_b128 a[124:127],v241 offset:48128\nbuffer_load_dwordx4 v240,s[44:47],s65 offen offset:0 sc1 lds\n" \
"M v[156:159],a[24:27],a[184:187],v[156:159]\nM v[156:159],a[20:23],a[180:183],v[156:159]\nM v[156:159],a[16:19],a[176:179],v[156:159]\nM v[128:131],a[0:3],a[128:131],v[224:227]\nv_max3_f32 v233,v233,v139,v152\nv_max3_f32 v233,v233,v153,v154\nv_max_f32 v233,v233,v155\nM v[128:131],a[4:7],a[132:135],v[128:131]\n" \
"v_cndmask_b32 v233,v232,v233,s[90:91]\nbuffer_load_dwordx4 v240,s[44:47],s65 offen offset:1024 sc1 lds\nM v[128:131],a[8:11],a[136:139],v[128:131]\nv_max3_f32 v237,v237,v143,v156\nv_max3_f32 v237,v237,v157,v158\nv_max_f32 v237,v237,v159\nM v[128:131],a[12:15],a[140:143],v[128:131]\nv_cndmask_b32 v237,v236,v237,s[90:91]\n" \
"M v[132:135],a[12:15],a[156:159],v[228:231]\nM v[132:135],a[8:11],a[152:155],v[132:135]\nv_permlane16_swap_b32 v233,v237\nv_max_f32 v233,v233,v237\nv_mov_b32 v237,v233\nM v[132:135],a[4:7],a[148:151],v[132:135]\nbuffer_load_dwordx4 v240,s[44:47],s65 offen offset:2048 sc1 lds\nv_permlane32_swap_b32 v233,v237\n" \
"M v[132:135],a[0:3],a[144:147],v[132:135]\nv_max_f32 v233,v233,v237\nv_mov_b32 v237,v233\nds_read_b128 a[0:3],v240 offset:0\nM v[144:147],a[16:19],a[128:131],v[224:227]\nv_permlane16_swap_b32 v233,v237\nv_sub_f32 v232,0,v233\nv_sub_f32 v234,0,v233\n" \
"M v[144:147],a[20:23],a[132:135],v[144:147]\nv_sub_f32 v235,0,v233\nv_sub_f32 v233,0,v233\nv_sub_f32 v236,0,v237\nM v[144:147],a[24:27],a[136:139],v[144:147]\nv_sub_f32 v238,0,v237\nv_sub_f32 v239,0,v237\nv_sub_f32 v237,0,v237\n" \
"M v[144:147],a[28:31],a[140:143],v[144:147]\nds_read_b128 a[4:7],v240 offset:1024\nds_read_b128 a[8:11],v240 offset:2048\nv_add_f32 v136,v136,v232\nM v[148:151],a[28:31],a[156:159],v[228:231]\nds_read_b128 a[12:15],v240 offset:3072\nbuffer_load_dwordx4 v240,s[44:47],s65 offen offset:3072 sc1 lds\nM v[148:151],a[24:27],a[152:155],v[148:151]\n" \
"s_add_u32 m0,s64,81920\nv_add_f32 v137,v137,v232\nv_add_f32 v138,v138,v232\nM v[148:151],a[20:23],a[148:151],v[148:151]\nv_add_f32 v139,v139,v232\nv_add_f32 v152,v152,v232\nv_add_f32 v153,v153,v232\nM v[148:151],a[16:19],a[144:147],v[148:151]\n" \
"ds_read_b128 a[16:19],v240 offset:4096\nds_read_b128 a[20:23],v240 offset:5120\nv_add_f32 v154,v154,v232\nM v[160:163],a[32:35],a[128:131],v[224:227]\nds_read_b128 a[24:27],v240 offset:6144\nv_add_f32 v155,v155,v232\nM v[160:163],a[36:39],a[132:135],v[160:163]\nds_read_b128 a[28:31],v240 offset:7168\n" \
"buffer_load_dwordx4 v240,s[48:51],s65 offen offset:0 sc1 lds\nM v[160:163],a[40:43],a[136:139],v[160:163]\nv_exp_f32 v136,v136\nv_add_f32 v140,v140,v236\nM v[160:163],a[44:47],a[140:143],v[160:163]\nv_exp_f32 v137,v137\nv_add_f32 v141,v141,v236\nM v[164:167],a[44:47],a[156:159],v[228:231]\n" \
"v_exp_f32 v138,v138\nv_add_f32 v142,v142,v236\nM v[164:167],a[40:43],a[152:155],v[164:167]\nbuffer_load_dwordx4 v240,s[48:51],s65 offen offset:1024 sc1 lds\nv_add_f32 v143,v143,v236\nM v[164:167],a[36:39],a[148:151],v[164:167]\nv_exp_f32 v139,v139\nv_add_f32 v156,v156,v236\n" \
"M v[164:167],a[32:35],a[144:147],v[164:167]\nv_exp_f32 v152,v152\nv_add_f32 v157,v157,v236\nM v[176:179],a[48:51],a[128:131],v[224:227]\nv_exp_f32 v153,v153\nv_add_f32 v158,v158,v236\nM v[176:179],a[52:55],a[132:135],v[176:179]\nbuffer_load_dwordx4 v240,s[48:51],s65 offen offset:2048 sc1 lds\n" \
"v_add_f32 v159,v159,v236\nM v[176:179],a[56:59],a[136:139],v[176:179]\nv_exp_f32 v154,v154\nv_cvt_pk_bf16_f32 v200,v136,v137\nM v[176:179],a[60:63],a[140:143],v[176:179]\nv_exp_f32 v155,v155\nv_cvt_pk_bf16_f32 v201,v138,v139\nM v[180:183],a[60:63],a[156:159],v[228:231]\n" \
"v_exp_f32 v128,v128\nM v[180:183],a[56:59],a[152:155],v[180:183]\nbuffer_load_dwordx4 v240,s[48:51],s65 offen offset:3072 sc1 lds\ns_add_u32 m0,s64,32768\nM v[180:183],a[52:55],a[148:151],v[180:183]\nv_exp_f32 v129,v129\nv_cvt_pk_bf16_f32 v202,v152,v153\nM v[180:183],a[48:51],a[144:147],v[180:183]\n" \
"v_exp_f32 v130,v130\nv_cvt_pk_bf16_f32 v203,v154,v155\nM v[168:171],a[32:35],a[160:163],v[232:235]\nv_exp_f32 v131,v131\nM v[168:171],a[36:39],a[164:167],v[168:171]\nbuffer_load_dwordx4 v240,s[44:47],s66 offen offset:0 sc1 lds\nM v[168:171],a[40:43],a[168:171],v[168:171]\nv_exp_f32 v132,v132\n" \
"M v[168:171],a[44:47],a[172:175],v[168:171]\nv_exp_f32 v133,v133\nM v[172:175],a[44:47],a[188:191],v[236:239]\nv_exp_f32 v134,v134\nM v[172:175],a[40:43],a[184:187],v[172:175]\nbuffer_load_dwordx4 v240,s[44:47],s66 offen offset:1024 sc1 lds\nM v[172:175],a[36:39],a[180:183],v[172:175]\ns_waitcnt lgkmcnt(0)\n" \
"s_barrier\nv_cndmask_b32 v168,v168,v252,s[82:83]\nM v[172:175],a[32:35],a[176:179],v[172:175]\nds_read_b128 a[32:35],v240 offset:8192\nds_read_b128 a[36:39],v240 offset:9216\nv_cndmask_b32 v169,v169,v252,s[84:85]\nM v[188:191],a[60:63],a[188:191],v[236:239]\nds_read_b128 a[40:43],v240 offset:10240\n" \
"v_exp_f32 v135,v135\nM v[188:191],a[56:59],a[184:187],v[188:191]\nds_read_b128 a[44:47],v240 offset:11264\nv_cndmask_b32 v170,v170,v252,s[86:87]\nM v[188:191],a[52:55],a[180:183],v[188:191]\nbuffer_load_dwordx4 v240,s[44:47],s66 offen offset:2048 sc1 lds\nv_cndmask_b32 v171,v171,v252,s[88:89]\nM v[188:191],a[48:51],a[176:179],v[188:191]\n" \
"ds_read_b128 a[48:51],v240 offset:12288\nv_exp_f32 v144,v144\nM v[0:3],a[64:67],v[192:195],v[0:3]\nds_read_b128 a[52:55],v240 offset:13312\nv_exp_f32 v145,v145\nM v[0:3],a[96:99],v[208:211],v[0:3]\nds_read_b128 a[56:59],v240 offset:14336\nv_exp_f32 v146,v146\n" \
"M v[8:11],a[68:71],v[192:195],v[8:11]\nds_read_b128 a[60:63],v240 offset:15360\nbuffer_load_dwordx4 v240,s[44:47],s66 offen offset:3072 sc1 lds\nM v[8:11],a[100:103],v[208:211],v[8:11]\nv_exp_f32 v147,v147\ns_add_u32 m0,s64,98304\nM v[16:19],a[72:75],v[192:195],v[16:19]\nv_exp_f32 v148,v148\n" \
"v_cndmask_b32 v188,v188,v252,s[82:83]\nM v[16:19],a[104:107],v[208:211],v[16:19]\nv_exp_f32 v149,v149\nv_cndmask_b32 v189,v189,v252,s[84:85]\nM v[24:27],a[76:79],v[192:195],v[24:27]\nv_exp_f32 v150,v150\nv_cndmask_b32 v190,v190,v252,s[86:87]\nM v[24:27],a[108:111],v[208:211],v[24:27]\n" \
"v_exp_f32 v151,v151\nv_cndmask_b32 v191,v191,v252,s[88:89]\nM v[32:35],a[80:83],v[192:195],v[32:35]\nv_exp_f32 v160,v160\nM v[32:35],a[112:115],v[208:211],v[32:35]\nv_exp_f32 v161,v161\nM v[40:43],a[84:87],v[192:195],v[40:43]\nv_exp_f32 v162,v162\n" \
"M v[40:43],a[116:119],v[208:211],v[40:43]\nv_exp_f32 v163,v163\nM v[48:51],a[88:91],v[192:195],v[48:51]\nv_exp_f32 v176,v176\nM v[48:51],a[120:123],v[208:211],v[48:51]\nv_exp_f32 v177,v177\nM v[56:59],a[92:95],v[192:195],v[56:59]\nv_exp_f32 v178,v178\n" \
"M v[56:59],a[124:127],v[208:211],v[56:59]\nv_exp_f32 v179,v179\nM a[192:195],a[208:211],v[192:195],a[192:195]\nv_exp_f32 v140,v140\nv_cvt_pk_bf16_f32 v192,v128,v129\nM a[192:195],a[208:211],v[208:211],a[192:195]\nv_exp_f32 v141,v141\nv_cvt_pk_bf16_f32 v193,v130,v131\n" \
"M v[4:7],a[64:67],v[196:199],v[4:7]\nv_exp_f32 v142,v142\nv_cvt_pk_bf16_f32 v194,v144,v145\nM v[4:7],a[96:99],v[212:215],v[4:7]\nv_exp_f32 v143,v143\nv_cvt_pk_bf16_f32 v195,v146,v147\nM v[12:15],a[68:71],v[196:199],v[12:15]\nv_exp_f32 v156,v156\n" \
"v_cvt_pk_bf16_f32 v208,v160,v161\nM v[12:15],a[100:103],v[212:215],v[12:15]\nv_exp_f32 v157,v157\nv_cvt_pk_bf16_f32 v209,v162,v163\nM v[20:23],a[72:75],v[196:199],v[20:23]\nv_exp_f32 v158,v158\nM v[20:23],a[104:107],v[212:215],v[20:23]\nv_exp_f32 v159,v159\n" \
"v_cvt_pk_bf16_f32 v210,v176,v177\nM v[28:31],a[76:79],v[196:199],v[28:31]\nv_exp_f32 v164,v164\nv_cvt_pk_bf16_f32 v211,v178,v179\nM v[28:31],a[108:111],v[212:215],v[28:31]\nv_exp_f32 v165,v165\nM v[36:39],a[80:83],v[196:199],v[36:39]\nv_exp_f32 v166,v166\n" \
"M v[36:39],a[112:115],v[212:215],v[36:39]\nv_exp_f32 v167,v167\nM v[44:47],a[84:87],v[196:199],v[44:47]\nv_exp_f32 v180,v180\nM v[44:47],a[116:119],v[212:215],v[44:47]\nv_exp_f32 v181,v181\nM v[52:55],a[88:91],v[196:199],v[52:55]\nv_exp_f32 v182,v182\n" \
"M v[52:55],a[120:123],v[212:215],v[52:55]\nv_exp_f32 v183,v183\nM v[60:63],a[92:95],v[196:199],v[60:63]\nv_exp_f32 v168,v168\nM v[60:63],a[124:127],v[212:215],v[60:63]\nv_exp_f32 v169,v169\nM a[196:199],a[208:211],v[196:199],a[196:199]\nv_exp_f32 v170,v170\n" \
"v_exp_f32 v171,v171\nv_cvt_pk_bf16_f32 v196,v132,v133\nv_cvt_pk_bf16_f32 v197,v134,v135\nv_cvt_pk_bf16_f32 v198,v148,v149\nv_cvt_pk_bf16_f32 v199,v150,v151\nbuffer_load_dwordx4 v240,s[48:51],s66 offen offset:0 sc1 lds\nbuffer_load_dwordx4 v240,s[48:51],s66 offen offset:1024 sc1 lds\nbuffer_load_dwordx4 v240,s[48:51],s66 offen offset:2048 sc1 lds\n" \
"buffer_load_dwordx4 v240,s[48:51],s66 offen offset:3072 sc1 lds\nM a[196:199],a[208:211],v[212:215],a[196:199]\ns_cmp_lt_u32 s62,2\ns_cbranch_scc1 .Lx01\ns_branch .Ldual\n.Lpro3:\ns_barrier\nM v[64:67],a[0:3],a[128:131],0\n" \
"ds_read_b128 a[64:67],v241 offset:0\nds_read_b128 a[96:99],v241 offset:8192\nM v[64:67],a[4:7],a[132:135],v[64:67]\nds_read_b128 a[68:71],v241 offset:1024\ns_mov_b32 m0,s64\nv_mov_b32 v250,143360\nM v[64:67],a[8:11],a[136:139],v[64:67]\nds_read_b128 a[100:103],v241 offset:9216\n" \
"buffer_load_dwordx4 v240,s[44:47],s36 offen offset:0 sc1 lds\nM v[64:67],a[12:15],a[140:143],v[64:67]\nds_read_b128 a[72:75],v241 offset:2048\nv_mov_b32 v168,0\nv_mov_b32 v169,0\nM v[68:71],a[12:15],a[156:159],0\nds_read_b128 a[12:15],v240 offset:19456\nv_mov_b32 v170,0\n" \
"v_mov_b32 v171,0\nM v[68:71],a[8:11],a[152:155],v[68:71]\nds_read_b128 a[8:11],v240 offset:18432\nv_max3_f32 v225,v64,v65,v66\nv_mov_b32 v172,0xff800000\nM v[68:71],a[4:7],a[148:151],v[68:71]\nds_read_b128 a[4:7],v240 offset:17408\nbuffer_load_dwordx4 v240,s[44:47],s36 offen offset:1024 sc1 lds\n" \
"M v[68:71],a[0:3],a[144:147],v[68:71]\nds_read_b128 a[0:3],v240 offset:16384\nv_mov_b32 v173,0xff800000\nv_mov_b32 v174,0xff800000\nM v[72:75],a[16:19],a[128:131],0\nds_read_b128 a[104:107],v241 offset:10240\nv_mov_b32 v175,0xff800000\nv_mov_b32 v184,0xff800000\n" \
"M v[72:75],a[20:23],a[132:135],v[72:75]\nv_max3_f32 v229,v68,v69,v70\nds_read_b128 a[76:79],v241 offset:3072\nv_mov_b32 v185,0xff800000\nM v[72:75],a[24:27],a[136:139],v[72:75]\nds_read_b128 a[108:111],v241 offset:11264\nv_mov_b32 v186,0xff800000\nM v[72:75],a[28:31],a[140:143],v[72:75]\n" \
"ds_read_b128 a[80:83],v241 offset:4096\nbuffer_load_dwordx4 v240,s[44:47],s36 offen offset:2048 sc1 lds\nM v[76:79],a[28:31],a[156:159],0\nds_read_b128 a[28:31],v240 offset:23552\nv_mov_b32 v187,0xff800000\nv_mov_b32 v188,0xff800000\nM v[76:79],a[24:27],a[152:155],v[76:79]\nds_read_b128 a[24:27],v240 offset:22528\n" \
"v_max3_f32 v225,v225,v67,v72\nv_max3_f32 v225,v225,v73,v74\nM v[76:79],a[20:23],a[148:151],v[76:79]\nv_max_f32 v225,v225,v75\nv_cndmask_b32 v225,v224,v225,s[90:91]\nds_read_b128 a[20:23],v240 offset:21504\nM v[76:79],a[16:19],a[144:147],v[76:79]\nds_read_b128 a[16:19],v240 offset:20480\n" \
"buffer_load_dwordx4 v240,s[44:47],s36 offen offset:3072 sc1 lds\nM v[80:83],a[32:35],a[128:131],0\nds_read_b128 a[112:115],v241 offset:12288\nv_mov_b32 v189,0xff800000\nv_mov_b32 v190,0xff800000\nM v[80:83],a[36:39],a[132:135],v[80:83]\nds_read_b128 a[84:87],v241 offset:5120\nv_max3_f32 v229,v229,v71,v76\n" \
"v_max3_f32 v229,v229,v77,v78\nM v[80:83],a[40:43],a[136:139],v[80:83]\nv_max_f32 v229,v229,v79\nv_cndmask_b32 v229,v228,v229,s[90:91]\nds_read_b128 a[116:119],v241 offset:13312\nM v[80:83],a[44:47],a[140:143],v[80:83]\nv_permlane16_swap_b32 v225,v229\nv_max_f32 v225,v225,v229\n" \
"v_mov_b32 v229,v225\nM v[84:87],a[44:47],a[156:159],0\nds_read_b128 a[44:47],v240 offset:27648\nv_permlane32_swap_b32 v225,v229\nv_max_f32 v225,v225,v229\nM v[84:87],a[40:43],a[152:155],v[84:87]\nv_mov_b32 v229,v225\nds_read_b128 a[40:43],v240 offset:26624\n" \
"M v[84:87],a[36:39],a[148:151],v[84:87]\nv_permlane16_swap_b32 v225,v229\nv_sub_f32 v224,0,v225\nv_sub_f32 v226,0,v225\nM v[84:87],a[32:35],a[144:147],v[84:87]\nv_sub_f32 v227,0,v225\nv_sub_f32 v225,0,v225\nv_sub_f32 v228,0,v229\n" \
"M v[88:91],a[48:51],a[128:131],0\nv_sub_f32 v230,0,v229\nv_sub_f32 v231,0,v229\nv_sub_f32 v229,0,v229\nM v[88:91],a[52:55],a[132:135],v[88:91]\nds_read_b128 a[32:35],v240 offset:24576\nds_read_b128 a[36:39],v240 offset:25600\nv_add_f32 v64,v64,v224\n" \
"M v[88:91],a[56:59],a[136:139],v[88:91]\nv_add_f32 v65,v65,v224\nv_add_f32 v66,v66,v224\nv_add_f32 v67,v67,v224\nM v[88:91],a[60:63],a[140:143],v[88:91]\nv_exp_f32 v64,v64\nv_add_f32 v72,v72,v224\nM v[92:95],a[60:63],a[156:159],0\n" \
"ds_read_b128 a[60:63],v240 offset:31744\nv_exp_f32 v65,v65\nM v[92:95],a[56:59],a[152:155],v[92:95]\nds_read_b128 a[56:59],v240 offset:30720\nv_exp_f32 v66,v66\nM v[92:95],a[52:55],a[148:151],v[92:95]\nds_read_b128 a[52:55],v240 offset:29696\nv_exp_f32 v67,v67\n" \
"M v[92:95],a[48:51],a[144:147],v[92:95]\nds_read_b128 a[48:51],v240 offset:28672\nv_add_f32 v73,v73,v224\ns_waitcnt lgkmcnt(6)\nM v[96:99],a[0:3],a[128:131],v[224:227]\nv_add_f32 v74,v74,v224\nv_add_f32 v75,v75,v224\nM v[96:99],a[4:7],a[132:135],v[96:99]\n" \
"v_add_f32 v80,v80,v224\nv_add_f32 v81,v81,v224\nv_add_f32 v82,v82,v224\nM v[96:99],a[8:11],a[136:139],v[96:99]\nv_exp_f32 v72,v72\nv_add_f32 v83,v83,v224\nM v[96:99],a[12:15],a[140:143],v[96:99]\nv_exp_f32 v73,v73\n" \
"v_cvt_pk_bf16_f32 v64,v64,v65\nM v[100:103],a[12:15],a[156:159],v[228:231]\nv_exp_f32 v74,v74\nv_add_f32 v88,v88,v224\nM v[100:103],a[8:11],a[152:155],v[100:103]\nv_exp_f32 v75,v75\nv_add_f32 v89,v89,v224\nM v[100:103],a[4:7],a[148:151],v[100:103]\n" \
"v_exp_f32 v80,v80\nv_add_f32 v90,v90,v224\nM v[100:103],a[0:3],a[144:147],v[100:103]\nv_exp_f32 v81,v81\nv_add_f32 v91,v91,v224\nM v[104:107],a[16:19],a[128:131],v[224:227]\nv_exp_f32 v82,v82\nv_cvt_pk_bf16_f32 v65,v66,v67\n" \
"M v[104:107],a[20:23],a[132:135],v[104:107]\nv_exp_f32 v83,v83\nv_cvt_pk_bf16_f32 v66,v72,v73\nM v[104:107],a[24:27],a[136:139],v[104:107]\nv_exp_f32 v88,v88\nv_cvt_pk_bf16_f32 v67,v74,v75\nM v[104:107],a[28:31],a[140:143],v[104:107]\nv_exp_f32 v89,v89\n" \
"M v[108:111],a[28:31],a[156:159],v[228:231]\nv_exp_f32 v90,v90\nv_cvt_pk_bf16_f32 v80,v80,v81\nM v[108:111],a[24:27],a[152:155],v[108:111]\nv_exp_f32 v91,v91\nv_cvt_pk_bf16_f32 v81,v82,v83\nM v[108:111],a[20:23],a[148:151],v[108:111]\nv_cvt_pk_bf16_f32 v82,v88,v89\n" \
"v_cvt_pk_bf16_f32 v83,v90,v91\nds_read_b128 a[88:91],v241 offset:6144\nM v[108:111],a[16:19],a[144:147],v[108:111]\nds_read_b128 a[120:123],v241 offset:14336\nds_read_b128 a[92:95],v241 offset:7168\ns_waitcnt lgkmcnt(3)\nM v[112:115],a[32:35],a[128:131],v[224:227]\nds_read_b128 a[124:127],v241 offset:15360\n" \
"v_add_f32 v68,v68,v228\nv_add_f32 v69,v69,v228\nM v[112:115],a[36:39],a[132:135],v[112:115]\nv_add_f32 v70,v70,v228\nv_add_f32 v71,v71,v228\nv_add_f32 v76,v76,v228\nM v[112:115],a[40:43],a[136:139],v[112:115]\nv_exp_f32 v68,v68\n" \
"v_add_f32 v77,v77,v228\nM v[112:115],a[44:47],a[140:143],v[112:115]\nv_exp_f32 v69,v69\nv_add_f32 v78,v78,v228\nM v[116:119],a[44:47],a[156:159],v[228:231]\nv_exp_f32 v70,v70\nv_add_f32 v79,v79,v228\nM v[116:119],a[40:43],a[152:155],v[116:119]\n" \
"v_exp_f32 v71,v71\nv_add_f32 v84,v84,v228\nM v[116:119],a[36:39],a[148:151],v[116:119]\nv_add_f32 v85,v85,v228\nv_add_f32 v86,v86,v228\nM v[116:119],a[32:35],a[144:147],v[116:119]\nv_exp_f32 v76,v76\nv_add_f32 v87,v87,v228\n" \
"M v[124:127],a[60:63],a[156:159],v[228:231]\nv_exp_f32 v77,v77\nv_cvt_pk_bf16_f32 v68,v68,v69\nM v[124:127],a[56:59],a[152:155],v[124:127]\nv_exp_f32 v78,v78\nv_add_f32 v92,v92,v228\nM v[124:127],a[52:55],a[148:151],v[124:127]\nv_exp_f32 v79,v79\n" \
"v_add_f32 v93,v93,v228\nM v[124:127],a[48:51],a[144:147],v[124:127]\nv_exp_f32 v84,v84\nv_add_f32 v94,v94,v228\nM v[0:3],a[64:67],v[64:67],0\nv_exp_f32 v85,v85\nv_add_f32 v95,v95,v228\nM v[0:3],a[96:99],v[80:83],v[0:3]\n" \
"v_exp_f32 v86,v86\nv_cvt_pk_bf16_f32 v69,v70,v71\nM v[8:11],a[68:71],v[64:67],0\nv_exp_f32 v87,v87\nv_cvt_pk_bf16_f32 v70,v76,v77\nM v[8:11],a[100:103],v[80:83],v[8:11]\nv_exp_f32 v92,v92\nv_cvt_pk_bf16_f32 v71,v78,v79\n" \
"M v[16:19],a[72:75],v[64:67],0\nv_exp_f32 v93,v93\nv_cvt_pk_bf16_f32 v84,v84,v85\nM v[16:19],a[104:107],v[80:83],v[16:19]\nv_exp_f32 v94,v94\nM v[24:27],a[76:79],v[64:67],0\nv_exp_f32 v95,v95\nv_cvt_pk_bf16_f32 v85,v86,v87\n" \
"M v[24:27],a[108:111],v[80:83],v[24:27]\nv_cvt_pk_bf16_f32 v86,v92,v93\nv_cvt_pk_bf16_f32 v87,v94,v95\nds_read_b128 a[0:3],v240 offset:32768\nM v[32:35],a[80:83],v[64:67],0\nds_read_b128 a[4:7],v240 offset:33792\nds_read_b128 a[8:11],v240 offset:34816\nv_cndmask_b32 v112,v112,v252,s[82:83]\n" \
"M v[32:35],a[112:115],v[80:83],v[32:35]\nds_read_b128 a[12:15],v240 offset:35840\nv_exp_f32 v96,v96\nM v[40:43],a[84:87],v[64:67],0\nds_read_b128 a[16:19],v240 offset:36864\nv_exp_f32 v97,v97\nM v[40:43],a[116:119],v[80:83],v[40:43]\nds_read_b128 a[20:23],v240 offset:37888\n" \
"v_cndmask_b32 v113,v113,v252,s[84:85]\ns_waitcnt lgkmcnt(6)\nM v[48:51],a[88:91],v[64:67],0\nds_read_b128 a[24:27],v240 offset:38912\nv_exp_f32 v98,v98\nM v[48:51],a[120:123],v[80:83],v[48:51]\nds_read_b128 a[28:31],v240 offset:39936\nv_exp_f32 v99,v99\n" \
"M v[56:59],a[92:95],v[64:67],0\nbuffer_load_dword v248,v251,s[52:55],s80 offen sc1\nds_read_b128 a[32:35],v240 offset:40960\nM v[56:59],a[124:127],v[80:83],v[56:59]\nds_read_b128 a[36:39],v240 offset:41984\nv_exp_f32 v104,v104\nM a[192:195],a[208:211],v[64:67],0\nds_read_b128 a[40:43],v240 offset:43008\n" \
"v_cndmask_b32 v114,v114,v252,s[86:87]\nM a[192:195],a[208:211],v[80:83],a[192:195]\nds_read_b128 a[44:47],v240 offset:44032\nv_exp_f32 v105,v105\nM v[4:7],a[64:67],v[68:71],0\nds_read_b128 a[48:51],v240 offset:45056\nv_exp_f32 v106,v106\nM v[4:7],a[96:99],v[84:87],v[4:7]\n" \
"ds_read_b128 a[52:55],v240 offset:46080\nv_exp_f32 v107,v107\nM v[12:15],a[68:71],v[68:71],0\nds_read_b128 a[56:59],v240 offset:47104\nv_cndmask_b32 v115,v115,v252,s[88:89]\nv_cvt_pk_bf16_f32 v96,v96,v97\nM v[12:15],a[100:103],v[84:87],v[12:15]\nds_read_b128 a[60:63],v240 offset:48128\n" \
"v_exp_f32 v112,v112\nM v[20:23],a[72:75],v[68:71],0\nds_read_b128 a[64:67],v241 offset:16384\nv_exp_f32 v113,v113\nM v[20:23],a[104:107],v[84:87],v[20:23]\nds_read_b128 a[96:99],v241 offset:24576\nv_exp_f32 v114,v114\nM v[28:31],a[76:79],v[68:71],0\n" \
"ds_read_b128 a[68:71],v241 offset:17408\nv_exp_f32 v115,v115\nM v[28:31],a[108:111],v[84:87],v[28:31]\nds_read_b128 a[100:103],v241 offset:25600\nv_cvt_pk_bf16_f32 v97,v98,v99\nv_cvt_pk_bf16_f32 v98,v104,v105\nM v[36:39],a[80:83],v[68:71],0\nds_read_b128 a[72:75],v241 offset:18432\n" \
"v_cvt_pk_bf16_f32 v99,v106,v107\nM v[36:39],a[112:115],v[84:87],v[36:39]\nds_read_b128 a[104:107],v241 offset:26624\nv_cvt_pk_bf16_f32 v112,v112,v113\nv_cvt_pk_bf16_f32 v113,v114,v115\nM v[44:47],a[84:87],v[68:71],0\nds_read_b128 a[76:79],v241 offset:19456\nv_mov_b32 v114,0\n" \
"v_mov_b32 v115,0\nM v[44:47],a[116:119],v[84:87],v[44:47]\nds_read_b128 a[108:111],v241 offset:27648\nv_exp_f32 v100,v100\nM v[52:55],a[88:91],v[68:71],0\nds_read_b128 a[80:83],v241 offset:20480\nv_exp_f32 v101,v101\nM v[52:55],a[120:123],v[84:87],v[52:55]\n" \
"ds_read_b128 a[112:115],v241 offset:28672\nv_exp_f32 v102,v102\nM v[60:63],a[92:95],v[68:71],0\nds_read_b128 a[84:87],v241 offset:21504\nv_exp_f32 v103,v103\nM v[60:63],a[124:127],v[84:87],v[60:63]\nds_read_b128 a[116:119],v241 offset:29696\nv_exp_f32 v108,v108\n" \
"M a[196:199],a[208:211],v[68:71],0\nds_read_b128 a[88:91],v241 offset:22528\nv_exp_f32 v109,v109\nM a[196:199],a[208:211],v[84:87],a[196:199]\nds_read_b128 a[120:123],v241 offset:30720\nv_cvt_pk_bf16_f32 v100,v100,v101\ns_waitcnt lgkmcnt(8)\nM v[128:131],a[0:3],a[128:131],v[224:227]\n" \
"ds_read_b128 a[92:95],v241 offset:23552\nv_exp_f32 v110,v110\nM v[128:131],a[4:7],a[132:135],v[128:131]\nds_read_b128 a[124:127],v241 offset:31744\nv_cndmask_b32 v124,v124,v252,s[82:83]\nM v[128:131],a[8:11],a[136:139],v[128:131]\nv_exp_f32 v111,v111\nv_cndmask_b32 v125,v125,v252,s[84:85]\n" \
"M v[128:131],a[12:15],a[140:143],v[128:131]\nv_exp_f32 v116,v116\nv_cndmask_b32 v126,v126,v252,s[86:87]\nM v[132:135],a[12:15],a[156:159],v[228:231]\nv_exp_f32 v117,v117\nv_cndmask_b32 v127,v127,v252,s[88:89]\nM v[132:135],a[8:11],a[152:155],v[132:135]\nv_exp_f32 v118,v118\n" \
"v_cvt_pk_bf16_f32 v101,v102,v103\nM v[132:135],a[4:7],a[148:151],v[132:135]\nv_exp_f32 v119,v119\nv_cvt_pk_bf16_f32 v102,v108,v109\nM v[132:135],a[0:3],a[144:147],v[132:135]\nv_exp_f32 v124,v124\nv_cvt_pk_bf16_f32 v103,v110,v111\nM v[144:147],a[16:19],a[128:131],v[224:227]\n" \
"v_exp_f32 v125,v125\nv_cvt_pk_bf16_f32 v116,v116,v117\nM v[144:147],a[20:23],a[132:135],v[144:147]\nv_exp_f32 v126,v126\nv_cvt_pk_bf16_f32 v117,v118,v119\nM v[144:147],a[24:27],a[136:139],v[144:147]\nv_exp_f32 v127,v127\nv_cvt_pk_bf16_f32 v118,v124,v125\n" \
"M v[144:147],a[28:31],a[140:143],v[144:147]\nv_cvt_pk_bf16_f32 v119,v126,v127\nds_read_b128 a[0:3],v240 offset:49152\nds_read_b128 a[4:7],v240 offset:50176\nM v[148:151],a[28:31],a[156:159],v[228:231]\nds_read_b128 a[8:11],v240 offset:51200\nv_mov_b32 v191,0xff800000\nM v[148:151],a[24:27],a[152:155],v[148:151]\n" \
"ds_read_b128 a[12:15],v240 offset:52224\nv_exp_f32 v128,v128\nM v[148:151],a[20:23],a[148:151],v[148:151]\nds_read_b128 a[28:31],v240 offset:56320\nv_exp_f32 v129,v129\nM v[148:151],a[16:19],a[144:147],v[148:151]\nds_read_b128 a[24:27],v240 offset:55296\nv_exp_f32 v130,v130\n" \
"M v[160:163],a[32:35],a[128:131],v[224:227]\nds_read_b128 a[20:23],v240 offset:54272\nv_exp_f32 v131,v131\nM v[160:163],a[36:39],a[132:135],v[160:163]\nds_read_b128 a[16:19],v240 offset:53248\nv_cvt_pk_bf16_f32 v192,v128,v129\nv_cvt_pk_bf16_f32 v193,v130,v131\nM v[160:163],a[40:43],a[136:139],v[160:163]\n" \
"v_exp_f32 v132,v132\nv_mov_b32 v202,0\nM v[160:163],a[44:47],a[140:143],v[160:163]\nv_exp_f32 v133,v133\nv_mov_b32 v203,0\nM v[164:167],a[44:47],a[156:159],v[228:231]\nv_exp_f32 v134,v134\nv_cvt_pk_bf16_f32 v196,v132,v133\n" \
"M v[164:167],a[40:43],a[152:155],v[164:167]\nv_exp_f32 v135,v135\nds_read_b128 a[40:43],v240 offset:59392\nM v[164:167],a[36:39],a[148:151],v[164:167]\nv_cvt_pk_bf16_f32 v197,v134,v135\nv_exp_f32 v144,v144\nM v[164:167],a[32:35],a[144:147],v[164:167]\nds_read_b128 a[32:35],v240 offset:57344\n" \
"ds_read_b128 a[36:39],v240 offset:58368\nM v[176:179],a[48:51],a[128:131],v[224:227]\nv_exp_f32 v145,v145\nds_read_b128 a[44:47],v240 offset:60416\nM v[176:179],a[52:55],a[132:135],v[176:179]\nv_exp_f32 v146,v146\nv_cvt_pk_bf16_f32 v194,v144,v145\nM v[176:179],a[56:59],a[136:139],v[176:179]\n" \
"v_exp_f32 v147,v147\ns_waitcnt vmcnt(0)\nv_cmp_eq_u32 vcc,0,v248\nM v[176:179],a[60:63],a[140:143],v[176:179]\nv_cvt_pk_bf16_f32 v195,v146,v147\nv_exp_f32 v148,v148\nM v[180:183],a[60:63],a[156:159],v[228:231]\ns_waitcnt lgkmcnt(4)\n" \
"s_barrier\nds_read_b128 a[60:63],v240 offset:64512\nM v[180:183],a[56:59],a[152:155],v[180:183]\nv_exp_f32 v149,v149\nds_read_b128 a[56:59],v240 offset:63488\nM v[180:183],a[52:55],a[148:151],v[180:183]\nv_exp_f32 v150,v150\nv_cvt_pk_bf16_f32 v198,v148,v149\n" \
"M v[180:183],a[48:51],a[144:147],v[180:183]\nv_exp_f32 v151,v151\nds_read_b128 a[48:51],v240 offset:61440\nM v[0:3],a[64:67],v[96:99],v[0:3]\nv_cvt_pk_bf16_f32 v199,v150,v151\nv_exp_f32 v160,v160\nM v[0:3],a[96:99],v[112:115],v[0:3]\nv_exp_f32 v161,v161\n" \
"ds_read_b128 a[52:55],v240 offset:62464\nM v[8:11],a[68:71],v[96:99],v[8:11]\nv_exp_f32 v162,v162\nM v[8:11],a[100:103],v[112:115],v[8:11]\nv_exp_f32 v163,v163\nv_cvt_pk_bf16_f32 v208,v160,v161\nM v[16:19],a[72:75],v[96:99],v[16:19]\nv_cvt_pk_bf16_f32 v209,v162,v163\n" \
"ds_write_b128 v250,a[244:247]\nM v[16:19],a[104:107],v[112:115],v[16:19]\nv_exp_f32 v164,v164\nds_read_b128 v[64:67],v250\nM v[24:27],a[76:79],v[96:99],v[24:27]\nv_exp_f32 v165,v165\nds_read_b128 v[68:71],v250\nM v[24:27],a[108:111],v[112:115],v[24:27]\n" \
"v_exp_f32 v166,v166\nds_read_b128 v[72:75],v250\nM v[32:35],a[80:83],v[96:99],v[32:35]\nv_exp_f32 v167,v167\nds_read_b128 v[76:79],v250\nM v[32:35],a[112:115],v[112:115],v[32:35]\nds_read_b128 v[80:83],v250\nds_read_b128 v[84:87],v250\n" \
"v_cvt_pk_bf16_f32 v212,v164,v165\nM v[40:43],a[84:87],v[96:99],v[40:43]\nds_read_b128 v[88:91],v250\nv_cvt_pk_bf16_f32 v213,v166,v167\ns_cbranch_vccnz .Lps5\n.Lpb5:\nM v[40:43],a[116:119],v[112:115],v[40:43]\nds_read_b128 v[92:95],v250\n" \
"v_exp_f32 v176,v176\nM v[48:51],a[88:91],v[96:99],v[48:51]\nds_read_b128 v[104:107],v250\nv_exp_f32 v177,v177\nM v[48:51],a[120:123],v[112:115],v[48:51]\nds_read_b128 v[108:111],v250\nv_cvt_pk_bf16_f32 v210,v176,v177\nM v[56:59],a[92:95],v[96:99],v[56:59]\n" \
"ds_read_b128 v[120:123],v250\nv_exp_f32 v178,v178\nM v[56:59],a[124:127],v[112:115],v[56:59]\nds_read_b128 v[124:127],v250\nv_exp_f32 v179,v179\nM a[192:195],a[208:211],v[96:99],a[192:195]\nds_read_b128 v[96:99],v250\nv_cvt_pk_bf16_f32 v211,v178,v179\n" \
"s_add_u32 m0,s64,65536\nM a[192:195],a[208:211],v[112:115],a[192:195]\nds_read_b128 v[112:115],v250\nv_exp_f32 v180,v180\nM v[4:7],a[64:67],v[100:103],v[4:7]\nds_read_b128 a[200:203],v250\nv_exp_f32 v181,v181\nM v[4:7],a[96:99],v[116:119],v[4:7]\n" \
"ds_read_b128 a[204:207],v250\nv_exp_f32 v182,v182\nM v[12:15],a[68:71],v[100:103],v[12:15]\nds_read_b128 a[64:67],v241 offset:32768\nv_exp_f32 v183,v183\nM v[12:15],a[100:103],v[116:119],v[12:15]\nds_read_b128 a[96:99],v241 offset:40960\nv_cvt_pk_bf16_f32 v214,v180,v181\n" \
"v_cvt_pk_bf16_f32 v215,v182,v183\nM v[20:23],a[72:75],v[100:103],v[20:23]\nds_read_b128 a[68:71],v241 offset:33792\nbuffer_load_dwordx4 v240,s[48:51],s36 offen offset:0 sc1 lds\nM v[20:23],a[104:107],v[116:119],v[20:23]\nds_read_b128 a[100:103],v241 offset:41984\nM v[28:31],a[76:79],v[100:103],v[28:31]\nds_read_b128 a[72:75],v241 offset:34816\n" \
"M v[28:31],a[108:111],v[116:119],v[28:31]\nds_read_b128 a[104:107],v241 offset:43008\nM v[36:39],a[80:83],v[100:103],v[36:39]\nds_read_b128 a[76:79],v241 offset:35840\nbuffer_load_dwordx4 v240,s[48:51],s36 offen offset:1024 sc1 lds\nM v[36:39],a[112:115],v[116:119],v[36:39]\nds_read_b128 a[108:111],v241 offset:44032\nM v[44:47],a[84:87],v[100:103],v[44:47]\n" \
"ds_read_b128 a[80:83],v241 offset:36864\nM v[44:47],a[116:119],v[116:119],v[44:47]\nds_read_b128 a[112:115],v241 offset:45056\nM v[52:55],a[88:91],v[100:103],v[52:55]\nds_read_b128 a[84:87],v241 offset:37888\nbuffer_load_dwordx4 v240,s[48:51],s36 offen offset:2048 sc1 lds\nM v[52:55],a[120:123],v[116:119],v[52:55]\nds_read_b128 a[116:119],v241 offset:46080\n" \
"M v[60:63],a[92:95],v[100:103],v[60:63]\nds_read_b128 a[88:91],v241 offset:38912\nM v[60:63],a[124:127],v[116:119],v[60:63]\nds_read_b128 a[120:123],v241 offset:47104\nM a[196:199],a[208:211],v[100:103],a[196:199]\nds_read_b128 v[100:103],v250\nbuffer_load_dwordx4 v240,s[48:51],s36 offen offset:3072 sc1 lds\nM a[196:199],a[208:211],v[116:119],a[196:199]\n" \
"ds_read_b128 v[116:119],v250\ns_add_u32 m0,s64,16384\nM v[136:139],a[0:3],a[160:163],0\nds_read_b128 a[92:95],v241 offset:39936\nv_add_u32 v250,0xc000,v241\nM v[136:139],a[4:7],a[164:167],v[136:139]\nds_read_b128 a[124:127],v241 offset:48128\nM v[136:139],a[8:11],a[168:171],v[136:139]\n" \
"buffer_load_dwordx4 v240,s[44:47],s65 offen offset:0 sc1 lds\nM v[136:139],a[12:15],a[172:175],v[136:139]\nM v[140:143],a[12:15],a[188:191],0\nM v[140:143],a[8:11],a[184:187],v[140:143]\nM v[140:143],a[4:7],a[180:183],v[140:143]\nbuffer_load_dwordx4 v240,s[44:47],s65 offen offset:1024 sc1 lds\nM v[140:143],a[0:3],a[176:179],v[140:143]\nM v[156:159],a[28:31],a[188:191],0\n" \
"M v[156:159],a[24:27],a[184:187],v[156:159]\nM v[156:159],a[20:23],a[180:183],v[156:159]\nv_max3_f32 v233,v136,v137,v138\nv_max_f32 v233,v233,v139\nv_cndmask_b32 v233,v232,v233,s[90:91]\nM v[156:159],a[16:19],a[176:179],v[156:159]\nbuffer_load_dwordx4 v240,s[44:47],s65 offen offset:2048 sc1 lds\nv_max3_f32 v237,v140,v141,v142\n" \
"M v[128:131],a[0:3],a[128:131],v[224:227]\nv_cndmask_b32 v136,v136,v252,s[82:83]\nv_cndmask_b32 v137,v137,v252,s[84:85]\nv_cndmask_b32 v138,v138,v252,s[86:87]\nM v[128:131],a[4:7],a[132:135],v[128:131]\nv_cndmask_b32 v139,v139,v252,s[88:89]\nv_max3_f32 v237,v237,v143,v156\nv_max3_f32 v237,v237,v157,v158\n" \
"M v[128:131],a[8:11],a[136:139],v[128:131]\nv_max_f32 v237,v237,v159\nv_cndmask_b32 v237,v236,v237,s[90:91]\nv_cndmask_b32 v156,v156,v252,s[82:83]\nM v[128:131],a[12:15],a[140:143],v[128:131]\nv_permlane16_swap_b32 v233,v237\nv_max_f32 v233,v233,v237\nv_mov_b32 v237,v233\n" \
"M v[132:135],a[12:15],a[156:159],v[228:231]\nbuffer_load_dwordx4 v240,s[44:47],s65 offen offset:3072 sc1 lds\nv_permlane32_swap_b32 v233,v237\nM v[132:135],a[8:11],a[152:155],v[132:135]\nv_max_f32 v233,v233,v237\ns_add_u32 m0,s64,81920\nv_mov_b32 v237,v233\nM v[132:135],a[4:7],a[148:151],v[132:135]\n" \
"v_cndmask_b32 v157,v157,v252,s[84:85]\nv_permlane16_swap_b32 v233,v237\nv_sub_f32 v232,0,v233\nM v[132:135],a[0:3],a[144:147],v[132:135]\nds_read_b128 a[0:3],v240 offset:0\nds_read_b128 a[4:7],v240 offset:1024\nv_add_f32 v136,v136,v232\nM v[144:147],a[16:19],a[128:131],v[224:227]\n" \
"ds_read_b128 a[8:11],v240 offset:2048\nv_add_f32 v137,v137,v232\nM v[144:147],a[20:23],a[132:135],v[144:147]\nds_read_b128 a[12:15],v240 offset:3072\nbuffer_load_dwordx4 v240,s[48:51],s65 offen offset:0 sc1 lds\nM v[144:147],a[24:27],a[136:139],v[144:147]\nv_add_f32 v138,v138,v232\nv_add_f32 v139,v139,v232\n" \
"v_cndmask_b32 v158,v158,v252,s[86:87]\nM v[144:147],a[28:31],a[140:143],v[144:147]\nv_cndmask_b32 v159,v159,v252,s[88:89]\nv_sub_f32 v236,0,v237\nv_sub_f32 v234,0,v233\nM v[148:151],a[28:31],a[156:159],v[228:231]\nv_exp_f32 v136,v136\nv_sub_f32 v235,0,v233\n" \
"M v[148:151],a[24:27],a[152:155],v[148:151]\nbuffer_load_dwordx4 v240,s[48:51],s65 offen offset:1024 sc1 lds\nv_sub_f32 v238,0,v237\nM v[148:151],a[20:23],a[148:151],v[148:151]\nv_exp_f32 v137,v137\nv_sub_f32 v239,0,v237\nM v[148:151],a[16:19],a[144:147],v[148:151]\nds_read_b128 a[16:19],v240 offset:4096\n" \
"ds_read_b128 a[20:23],v240 offset:5120\ns_waitcnt lgkmcnt(5)\nM v[160:163],a[32:35],a[128:131],v[224:227]\nds_read_b128 a[24:27],v240 offset:6144\nv_exp_f32 v138,v138\nM v[160:163],a[36:39],a[132:135],v[160:163]\nds_read_b128 a[28:31],v240 offset:7168\nbuffer_load_dwordx4 v240,s[48:51],s65 offen offset:2048 sc1 lds\n" \
"M v[160:163],a[40:43],a[136:139],v[160:163]\nv_exp_f32 v139,v139\nv_add_f32 v140,v140,v236\nM v[160:163],a[44:47],a[140:143],v[160:163]\nv_exp_f32 v128,v128\nM v[164:167],a[44:47],a[156:159],v[228:231]\ns_barrier\nv_exp_f32 v129,v129\n" \
"M v[164:167],a[40:43],a[152:155],v[164:167]\nbuffer_load_dwordx4 v240,s[48:51],s65 offen offset:3072 sc1 lds\ns_add_u32 m0,s64,32768\nM v[164:167],a[36:39],a[148:151],v[164:167]\nv_exp_f32 v130,v130\nv_add_f32 v141,v141,v236\nM v[164:167],a[32:35],a[144:147],v[164:167]\nds_read_b128 a[32:35],v240 offset:8192\n" \
"ds_read_b128 a[36:39],v240 offset:9216\nv_add_f32 v142,v142,v236\nM v[176:179],a[48:51],a[128:131],v[224:227]\nds_read_b128 a[40:43],v240 offset:10240\nv_exp_f32 v131,v131\nM v[176:179],a[52:55],a[132:135],v[176:179]\nds_read_b128 a[44:47],v240 offset:11264\nbuffer_load_dwordx4 v240,s[44:47],s66 offen offset:0 sc1 lds\n" \
"M v[176:179],a[56:59],a[136:139],v[176:179]\nv_exp_f32 v132,v132\nv_add_f32 v143,v143,v236\nM v[176:179],a[60:63],a[140:143],v[176:179]\nv_exp_f32 v133,v133\nv_add_f32 v156,v156,v236\nM v[180:183],a[60:63],a[156:159],v[228:231]\nv_exp_f32 v134,v134\n" \
"v_add_f32 v157,v157,v236\nM v[180:183],a[56:59],a[152:155],v[180:183]\nbuffer_load_dwordx4 v240,s[44:47],s66 offen offset:1024 sc1 lds\nv_add_f32 v158,v158,v236\nM v[180:183],a[52:55],a[148:151],v[180:183]\nv_exp_f32 v135,v135\nM v[180:183],a[48:51],a[144:147],v[180:183]\nds_read_b128 a[48:51],v240 offset:12288\n" \
"ds_read_b128 a[52:55],v240 offset:13312\nv_add_f32 v159,v159,v236\nM v[0:3],a[64:67],v[192:195],v[0:3]\nds_read_b128 a[56:59],v240 offset:14336\nv_exp_f32 v144,v144\nM v[0:3],a[96:99],v[208:211],v[0:3]\nds_read_b128 a[60:63],v240 offset:15360\nbuffer_load_dwordx4 v240,s[44:47],s66 offen offset:2048 sc1 lds\n" \
"M v[8:11],a[68:71],v[192:195],v[8:11]\nv_exp_f32 v145,v145\nv_sub_f32 v233,0,v233\nM v[8:11],a[100:103],v[208:211],v[8:11]\nv_exp_f32 v146,v146\nv_sub_f32 v237,0,v237\nM v[16:19],a[72:75],v[192:195],v[16:19]\nv_exp_f32 v147,v147\n" \
"v_cvt_pk_bf16_f32 v200,v136,v137\nM v[16:19],a[104:107],v[208:211],v[16:19]\nbuffer_load_dwordx4 v240,s[44:47],s66 offen offset:3072 sc1 lds\ns_add_u32 m0,s64,98304\nM v[24:27],a[76:79],v[192:195],v[24:27]\nv_exp_f32 v148,v148\nv_cvt_pk_bf16_f32 v201,v138,v139\nM v[24:27],a[108:111],v[208:211],v[24:27]\n" \
"v_exp_f32 v149,v149\nM v[32:35],a[80:83],v[192:195],v[32:35]\nv_exp_f32 v150,v150\nM v[32:35],a[112:115],v[208:211],v[32:35]\nv_exp_f32 v151,v151\nM v[40:43],a[84:87],v[192:195],v[40:43]\nv_exp_f32 v160,v160\nM v[40:43],a[116:119],v[208:211],v[40:43]\n" \
"v_exp_f32 v161,v161\nM v[48:51],a[88:91],v[192:195],v[48:51]\nv_exp_f32 v162,v162\nM v[48:51],a[120:123],v[208:211],v[48:51]\nv_exp_f32 v163,v163\nM v[56:59],a[92:95],v[192:195],v[56:59]\nv_exp_f32 v176,v176\nM v[56:59],a[124:127],v[208:211],v[56:59]\n" \
"v_exp_f32 v177,v177\nM a[192:195],a[208:211],v[192:195],a[192:195]\nv_exp_f32 v178,v178\nv_cvt_pk_bf16_f32 v192,v128,v129\nM a[192:195],a[208:211],v[208:211],a[192:195]\nv_exp_f32 v179,v179\nv_cvt_pk_bf16_f32 v193,v130,v131\nM v[4:7],a[64:67],v[196:199],v[4:7]\n" \
"v_exp_f32 v140,v140\nv_cvt_pk_bf16_f32 v194,v144,v145\nM v[4:7],a[96:99],v[212:215],v[4:7]\nv_exp_f32 v141,v141\nv_cvt_pk_bf16_f32 v195,v146,v147\nM v[12:15],a[68:71],v[196:199],v[12:15]\nv_exp_f32 v142,v142\nM v[12:15],a[100:103],v[212:215],v[12:15]\n" \
"v_exp_f32 v143,v143\nv_cvt_pk_bf16_f32 v208,v160,v161\nM v[20:23],a[72:75],v[196:199],v[20:23]\nv_exp_f32 v156,v156\nv_cvt_pk_bf16_f32 v209,v162,v163\nM v[20:23],a[104:107],v[212:215],v[20:23]\nv_exp_f32 v157,v157\nv_cvt_pk_bf16_f32 v210,v176,v177\n" \
"M v[28:31],a[76:79],v[196:199],v[28:31]\nv_exp_f32 v158,v158\nv_cvt_pk_bf16_f32 v211,v178,v179\nM v[28:31],a[108:111],v[212:215],v[28:31]\nv_exp_f32 v159,v159\nM v[36:39],a[80:83],v[196:199],v[36:39]\nv_exp_f32 v164,v164\nM v[36:39],a[112:115],v[212:215],v[36:39]\n" \
"v_exp_f32 v165,v165\nM v[44:47],a[84:87],v[196:199],v[44:47]\nv_exp_f32 v166,v166\nM v[44:47],a[116:119],v[212:215],v[44:47]\nv_exp_f32 v167,v167\nM v[52:55],a[88:91],v[196:199],v[52:55]\nv_exp_f32 v180,v180\nM v[52:55],a[120:123],v[212:215],v[52:55]\n" \
"v_exp_f32 v181,v181\nM v[60:63],a[92:95],v[196:199],v[60:63]\nv_exp_f32 v182,v182\nM v[60:63],a[124:127],v[212:215],v[60:63]\nv_exp_f32 v183,v183\nM a[196:199],a[208:211],v[196:199],a[196:199]\nv_cvt_pk_bf16_f32 v196,v132,v133\nv_cvt_pk_bf16_f32 v197,v134,v135\n" \
"v_cvt_pk_bf16_f32 v198,v148,v149\nv_cvt_pk_bf16_f32 v199,v150,v151\nbuffer_load_dwordx4 v240,s[48:51],s66 offen offset:0 sc1 lds\nbuffer_load_dwordx4 v240,s[48:51],s66 offen offset:1024 sc1 lds\nbuffer_load_dwordx4 v240,s[48:51],s66 offen offset:2048 sc1 lds\nbuffer_load_dwordx4 v240,s[48:51],s66 offen offset:3072 sc1 lds\nM a[196:199],a[208:211],v[212:215],a[196:199]\ns_cmp_lt_u32 s62,2\n" \
"s_cbranch_scc1 .Lx01\n.Ldual:\ns_waitcnt lgkmcnt(4)\nM v[128:131],a[0:3],a[128:131],v[224:227]\ns_add_u32 m0,s94,s64\nv_cvt_pk_bf16_f32 v204,v140,v141\nM v[128:131],a[4:7],a[132:135],v[128:131]\nds_read_b128 a[64:67],v250 offset:0\n" \
"ds_read_b128 a[96:99],v250 offset:8192\nM v[128:131],a[8:11],a[136:139],v[128:131]\nv_cvt_pk_bf16_f32 v205,v142,v143\nv_cvt_pk_bf16_f32 v206,v156,v157\nM v[128:131],a[12:15],a[140:143],v[128:131]\nv_cvt_pk_bf16_f32 v207,v158,v159\nv_cvt_pk_bf16_f32 v212,v164,v165\nM v[132:135],a[12:15],a[156:159],v[228:231]\n" \
"buffer_load_dwordx4 v240,s[44:47],s95 offen offset:0 sc1 lds\nM v[132:135],a[8:11],a[152:155],v[132:135]\nv_cvt_pk_bf16_f32 v213,v166,v167\nv_cvt_pk_bf16_f32 v216,v168,v169\nM v[132:135],a[4:7],a[148:151],v[132:135]\nv_cvt_pk_bf16_f32 v217,v170,v171\nv_cvt_pk_bf16_f32 v214,v180,v181\nM v[132:135],a[0:3],a[144:147],v[132:135]\n" \
"ds_read_b128 a[68:71],v250 offset:1024\nds_read_b128 a[100:103],v250 offset:9216\nM v[136:139],a[0:3],a[160:163],v[232:235]\nv_exp_f32 v172,v172\nM v[136:139],a[4:7],a[164:167],v[136:139]\nv_exp_f32 v173,v173\nM v[136:139],a[8:11],a[168:171],v[136:139]\nv_exp_f32 v174,v174\n" \
"M v[136:139],a[12:15],a[172:175],v[136:139]\nv_exp_f32 v175,v175\nM v[140:143],a[12:15],a[188:191],v[236:239]\nv_cvt_pk_bf16_f32 v220,v172,v173\nv_cvt_pk_bf16_f32 v221,v174,v175\nM v[140:143],a[8:11],a[184:187],v[140:143]\nds_read_b128 a[72:75],v250 offset:2048\nds_read_b128 a[104:107],v250 offset:10240\n" \
"M v[140:143],a[4:7],a[180:183],v[140:143]\nv_cvt_pk_bf16_f32 v215,v182,v183\ns_add_u32 s94,s94,0x4000\nM v[140:143],a[0:3],a[176:179],v[140:143]\nv_exp_f32 v184,v184\nM v[144:147],a[16:19],a[128:131],v[224:227]\nv_exp_f32 v185,v185\nM v[144:147],a[20:23],a[132:135],v[144:147]\n" \
"v_exp_f32 v186,v186\nM v[144:147],a[24:27],a[136:139],v[144:147]\nv_exp_f32 v187,v187\nM v[144:147],a[28:31],a[140:143],v[144:147]\nds_read_b128 a[76:79],v250 offset:3072\nds_read_b128 a[108:111],v250 offset:11264\nM v[148:151],a[28:31],a[156:159],v[228:231]\nv_cvt_pk_bf16_f32 v218,v184,v185\n" \
"v_cvt_pk_bf16_f32 v219,v186,v187\nM v[148:151],a[24:27],a[152:155],v[148:151]\nv_exp_f32 v188,v188\nM v[148:151],a[20:23],a[148:151],v[148:151]\nbuffer_load_dwordx4 v240,s[44:47],s95 offen offset:1024 sc1 lds\nM v[148:151],a[16:19],a[144:147],v[148:151]\nv_exp_f32 v189,v189\nM v[152:155],a[16:19],a[160:163],v[232:235]\n" \
"v_exp_f32 v190,v190\nM v[152:155],a[20:23],a[164:167],v[152:155]\nds_read_b128 a[80:83],v250 offset:4096\nds_read_b128 a[112:115],v250 offset:12288\nM v[152:155],a[24:27],a[168:171],v[152:155]\nv_exp_f32 v191,v191\nM v[152:155],a[28:31],a[172:175],v[152:155]\nv_cvt_pk_bf16_f32 v222,v188,v189\n" \
"v_cvt_pk_bf16_f32 v223,v190,v191\nM v[156:159],a[28:31],a[188:191],v[236:239]\nv_exp_f32 v128,v128\nM v[156:159],a[24:27],a[184:187],v[156:159]\nv_exp_f32 v129,v129\nM v[156:159],a[20:23],a[180:183],v[156:159]\nv_exp_f32 v130,v130\nM v[156:159],a[16:19],a[176:179],v[156:159]\n" \
"ds_read_b128 a[84:87],v250 offset:5120\nds_read_b128 a[116:119],v250 offset:13312\nM v[160:163],a[32:35],a[128:131],v[224:227]\nv_exp_f32 v131,v131\nM v[160:163],a[36:39],a[132:135],v[160:163]\nv_exp_f32 v144,v144\nM v[160:163],a[40:43],a[136:139],v[160:163]\nv_exp_f32 v145,v145\n" \
"M v[160:163],a[44:47],a[140:143],v[160:163]\nv_exp_f32 v146,v146\nM v[164:167],a[44:47],a[156:159],v[228:231]\nv_exp_f32 v147,v147\nM v[164:167],a[40:43],a[152:155],v[164:167]\nds_read_b128 a[88:91],v250 offset:6144\nds_read_b128 a[120:123],v250 offset:14336\nM v[164:167],a[36:39],a[148:151],v[164:167]\n" \
"v_exp_f32 v132,v132\nM v[164:167],a[32:35],a[144:147],v[164:167]\nv_exp_f32 v133,v133\nM v[168:171],a[32:35],a[160:163],v[232:235]\nbuffer_load_dwordx4 v240,s[44:47],s95 offen offset:2048 sc1 lds\nM v[168:171],a[36:39],a[164:167],v[168:171]\nv_exp_f32 v134,v134\nM v[168:171],a[40:43],a[168:171],v[168:171]\n" \
"v_exp_f32 v135,v135\nM v[168:171],a[44:47],a[172:175],v[168:171]\nds_read_b128 a[92:95],v250 offset:7168\nds_read_b128 a[124:127],v250 offset:15360\nM v[172:175],a[44:47],a[188:191],v[236:239]\nv_xor_b32 v250,0x18000,v250\ns_and_b32 s94,s94,0xffff\nM v[172:175],a[40:43],a[184:187],v[172:175]\n" \
"v_exp_f32 v148,v148\nM v[172:175],a[36:39],a[180:183],v[172:175]\nv_exp_f32 v149,v149\nM v[172:175],a[32:35],a[176:179],v[172:175]\ns_waitcnt lgkmcnt(4)\nM v[176:179],a[48:51],a[128:131],v[224:227]\nv_exp_f32 v150,v150\nM v[176:179],a[52:55],a[132:135],v[176:179]\n" \
"v_exp_f32 v151,v151\nM v[176:179],a[56:59],a[136:139],v[176:179]\nv_exp_f32 v136,v136\nM v[176:179],a[60:63],a[140:143],v[176:179]\nv_exp_f32 v137,v137\nM v[180:183],a[60:63],a[156:159],v[228:231]\nv_exp_f32 v138,v138\nM v[180:183],a[56:59],a[152:155],v[180:183]\n" \
"v_exp_f32 v139,v139\nM v[180:183],a[52:55],a[148:151],v[180:183]\nv_exp_f32 v152,v152\nM v[180:183],a[48:51],a[144:147],v[180:183]\nv_exp_f32 v153,v153\nM v[184:187],a[48:51],a[160:163],v[232:235]\nv_exp_f32 v154,v154\nM v[184:187],a[52:55],a[164:167],v[184:187]\n" \
"v_exp_f32 v155,v155\nM v[184:187],a[56:59],a[168:171],v[184:187]\nbuffer_load_dwordx4 v240,s[44:47],s95 offen offset:3072 sc1 lds\nM v[184:187],a[60:63],a[172:175],v[184:187]\nv_exp_f32 v160,v160\nM v[188:191],a[60:63],a[188:191],v[236:239]\nv_exp_f32 v161,v161\nM v[188:191],a[56:59],a[184:187],v[188:191]\n" \
"v_exp_f32 v162,v162\nM v[188:191],a[52:55],a[180:183],v[188:191]\nv_exp_f32 v163,v163\nM v[188:191],a[48:51],a[176:179],v[188:191]\nv_exp_f32 v176,v176\nM v[0:3],a[64:67],v[192:195],v[0:3]\ns_waitcnt vmcnt(16) lgkmcnt(0)\ns_barrier\n" \
"M v[0:3],a[96:99],v[208:211],v[0:3]\nds_read_b128 a[0:3],v250 offset:0\nds_read_b128 a[4:7],v250 offset:1024\nM v[8:11],a[68:71],v[192:195],v[8:11]\nv_exp_f32 v177,v177\nM v[8:11],a[100:103],v[208:211],v[8:11]\nv_exp_f32 v178,v178\nM v[16:19],a[72:75],v[192:195],v[16:19]\n" \
"s_add_u32 m0,m0,0x10000\nM v[16:19],a[104:107],v[208:211],v[16:19]\nbuffer_load_dwordx4 v240,s[48:51],s95 offen offset:0 sc1 lds\nM v[24:27],a[76:79],v[192:195],v[24:27]\nv_exp_f32 v179,v179\nM v[24:27],a[108:111],v[208:211],v[24:27]\nv_exp_f32 v140,v140\nM v[32:35],a[80:83],v[192:195],v[32:35]\n" \
"v_exp_f32 v141,v141\nM v[32:35],a[112:115],v[208:211],v[32:35]\nds_read_b128 a[8:11],v250 offset:2048\nds_read_b128 a[12:15],v250 offset:3072\nM v[40:43],a[84:87],v[192:195],v[40:43]\nv_exp_f32 v142,v142\nM v[40:43],a[116:119],v[208:211],v[40:43]\nv_exp_f32 v143,v143\n" \
"M v[48:51],a[88:91],v[192:195],v[48:51]\nv_exp_f32 v156,v156\nM v[48:51],a[120:123],v[208:211],v[48:51]\nv_exp_f32 v157,v157\nM v[56:59],a[92:95],v[192:195],v[56:59]\nv_exp_f32 v158,v158\nM v[56:59],a[124:127],v[208:211],v[56:59]\nv_exp_f32 v159,v159\n" \
"M a[192:195],a[208:211],v[192:195],a[192:195]\nv_exp_f32 v164,v164\nM a[192:195],a[208:211],v[208:211],a[192:195]\nds_read_b128 a[16:19],v250 offset:4096\nds_read_b128 a[20:23],v250 offset:5120\nM v[4:7],a[64:67],v[196:199],v[4:7]\nv_exp_f32 v165,v165\nM v[4:7],a[96:99],v[212:215],v[4:7]\n" \
"v_exp_f32 v166,v166\nM v[12:15],a[68:71],v[196:199],v[12:15]\nv_exp_f32 v167,v167\nM v[12:15],a[100:103],v[212:215],v[12:15]\nbuffer_load_dwordx4 v240,s[48:51],s95 offen offset:1024 sc1 lds\nM v[20:23],a[72:75],v[196:199],v[20:23]\nv_exp_f32 v180,v180\nM v[20:23],a[104:107],v[212:215],v[20:23]\n" \
"v_exp_f32 v181,v181\nM v[28:31],a[76:79],v[196:199],v[28:31]\nv_exp_f32 v182,v182\nM v[28:31],a[108:111],v[212:215],v[28:31]\nds_read_b128 a[24:27],v250 offset:6144\nds_read_b128 a[28:31],v250 offset:7168\nM v[36:39],a[80:83],v[196:199],v[36:39]\nv_exp_f32 v183,v183\n" \
"M v[36:39],a[112:115],v[212:215],v[36:39]\nv_exp_f32 v168,v168\nM v[44:47],a[84:87],v[196:199],v[44:47]\nv_exp_f32 v169,v169\nM v[44:47],a[116:119],v[212:215],v[44:47]\nv_exp_f32 v170,v170\nM v[52:55],a[88:91],v[196:199],v[52:55]\nv_exp_f32 v171,v171\n" \
"M v[52:55],a[120:123],v[212:215],v[52:55]\nv_cvt_pk_bf16_f32 v192,v128,v129\nv_cvt_pk_bf16_f32 v193,v130,v131\nM v[60:63],a[92:95],v[196:199],v[60:63]\nv_cvt_pk_bf16_f32 v194,v144,v145\nv_cvt_pk_bf16_f32 v195,v146,v147\nM v[60:63],a[124:127],v[212:215],v[60:63]\nds_read_b128 a[32:35],v250 offset:8192\n" \
"ds_read_b128 a[36:39],v250 offset:9216\nM a[196:199],a[208:211],v[196:199],a[196:199]\nv_cvt_pk_bf16_f32 v208,v160,v161\nv_cvt_pk_bf16_f32 v209,v162,v163\nM a[196:199],a[208:211],v[212:215],a[196:199]\nv_cvt_pk_bf16_f32 v210,v176,v177\nv_cvt_pk_bf16_f32 v211,v178,v179\nM v[64:67],a[64:67],v[200:203],v[64:67]\n" \
"v_cvt_pk_bf16_f32 v196,v132,v133\nv_cvt_pk_bf16_f32 v197,v134,v135\nM v[64:67],a[96:99],v[216:219],v[64:67]\nbuffer_load_dwordx4 v240,s[48:51],s95 offen offset:2048 sc1 lds\nM v[72:75],a[68:71],v[200:203],v[72:75]\nv_cvt_pk_bf16_f32 v198,v148,v149\nv_cvt_pk_bf16_f32 v199,v150,v151\nM v[72:75],a[100:103],v[216:219],v[72:75]\n" \
"M v[80:83],a[72:75],v[200:203],v[80:83]\nM v[80:83],a[104:107],v[216:219],v[80:83]\nds_read_b128 a[40:43],v250 offset:10240\nds_read_b128 a[44:47],v250 offset:11264\nM v[88:91],a[76:79],v[200:203],v[88:91]\nM v[88:91],a[108:111],v[216:219],v[88:91]\nM v[96:99],a[80:83],v[200:203],v[96:99]\nM v[96:99],a[112:115],v[216:219],v[96:99]\n" \
"M v[104:107],a[84:87],v[200:203],v[104:107]\nM v[104:107],a[116:119],v[216:219],v[104:107]\nM v[112:115],a[88:91],v[200:203],v[112:115]\nM v[112:115],a[120:123],v[216:219],v[112:115]\nds_read_b128 a[48:51],v250 offset:12288\nds_read_b128 a[52:55],v250 offset:13312\nM v[120:123],a[92:95],v[200:203],v[120:123]\nM v[120:123],a[124:127],v[216:219],v[120:123]\n" \
"M a[200:203],a[208:211],v[200:203],a[200:203]\nv_cvt_pk_bf16_f32 v200,v136,v137\nv_cvt_pk_bf16_f32 v201,v138,v139\nM a[200:203],a[208:211],v[216:219],a[200:203]\nbuffer_load_dwordx4 v240,s[48:51],s95 offen offset:3072 sc1 lds\nM v[68:71],a[64:67],v[204:207],v[68:71]\nv_cvt_pk_bf16_f32 v202,v152,v153\nv_cvt_pk_bf16_f32 v203,v154,v155\n" \
"M v[68:71],a[96:99],v[220:223],v[68:71]\ns_cmp_eq_u32 s95,s96\ns_cselect_b32 s72,0xc000,s98\ns_add_u32 s95,s95,s72\nM v[76:79],a[68:71],v[204:207],v[76:79]\nM v[76:79],a[100:103],v[220:223],v[76:79]\nds_read_b128 a[56:59],v250 offset:14336\nds_read_b128 a[60:63],v250 offset:15360\n" \
"M v[84:87],a[72:75],v[204:207],v[84:87]\nv_add_u32 v250,s94,v241\nM v[84:87],a[104:107],v[220:223],v[84:87]\nM v[92:95],a[76:79],v[204:207],v[92:95]\nM v[92:95],a[108:111],v[220:223],v[92:95]\nM v[100:103],a[80:83],v[204:207],v[100:103]\nM v[100:103],a[112:115],v[220:223],v[100:103]\nM v[108:111],a[84:87],v[204:207],v[108:111]\n" \
"M v[108:111],a[116:119],v[220:223],v[108:111]\nM v[116:119],a[88:91],v[204:207],v[116:119]\nbuffer_load_dword v247,v254,s[56:59],s1 offen\nM v[116:119],a[120:123],v[220:223],v[116:119]\ns_add_u32 s1,s1,s0\nM v[124:127],a[92:95],v[204:207],v[124:127]\nM v[124:127],a[124:127],v[220:223],v[124:127]\nM a[204:207],a[208:211],v[204:207],a[204:207]\n" \
"M a[204:207],a[208:211],v[220:223],a[204:207]\ns_cmp_lg_u32 s95,s97\ns_cbranch_scc1 .Ldual\n.Ldf:\ns_waitcnt lgkmcnt(4)\nM v[128:131],a[0:3],a[128:131],v[224:227]\ns_add_u32 m0,s94,s64\nv_cvt_pk_bf16_f32 v204,v140,v141\n" \
"M v[128:131],a[4:7],a[132:135],v[128:131]\nds_read_b128 a[64:67],v250 offset:0\nds_read_b128 a[96:99],v250 offset:8192\nM v[128:131],a[8:11],a[136:139],v[128:131]\nv_cvt_pk_bf16_f32 v205,v142,v143\nv_lshrrev_b32 v248,2,v249\nM v[128:131],a[12:15],a[140:143],v[128:131]\nv_mul_u32_u24 v248,3584,v248\n" \
"v_and_b32 v251,3,v249\nM v[132:135],a[12:15],a[156:159],v[228:231]\nbuffer_load_dwordx4 v240,s[44:47],s95 offen offset:0 sc1 lds\nM v[132:135],a[8:11],a[152:155],v[132:135]\ns_mul_i32 s70,s7,458752\nv_lshl_add_u32 v248,v251,3,v248\nM v[132:135],a[4:7],a[148:151],v[132:135]\ns_add_u32 s70,s70,s2\n" \
"v_add_u32 v248,s70,v248\nM v[132:135],a[0:3],a[144:147],v[132:135]\nds_read_b128 a[68:71],v250 offset:1024\nds_read_b128 a[100:103],v250 offset:9216\nM v[136:139],a[0:3],a[160:163],v[232:235]\nv_and_b32 v248,s3,v248\nv_cvt_pk_bf16_f32 v206,v156,v157\nM v[136:139],a[4:7],a[164:167],v[136:139]\n" \
"v_cvt_pk_bf16_f32 v207,v158,v159\nv_cvt_pk_bf16_f32 v212,v164,v165\nM v[136:139],a[8:11],a[168:171],v[136:139]\nv_cvt_pk_bf16_f32 v213,v166,v167\nv_cvt_pk_bf16_f32 v216,v168,v169\nM v[136:139],a[12:15],a[172:175],v[136:139]\nv_cvt_pk_bf16_f32 v217,v170,v171\ns_and_b32 s71,s3,57344\n" \
"M v[140:143],a[12:15],a[188:191],v[236:239]\nv_exp_f32 v172,v172\nM v[140:143],a[8:11],a[184:187],v[140:143]\nds_read_b128 a[72:75],v250 offset:2048\nds_read_b128 a[104:107],v250 offset:10240\nM v[140:143],a[4:7],a[180:183],v[140:143]\nglobal_load_dwordx2 a[212:213],v248,s[22:23]\nM v[140:143],a[0:3],a[176:179],v[140:143]\n" \
"v_exp_f32 v173,v173\nM v[144:147],a[16:19],a[128:131],v[224:227]\nv_exp_f32 v174,v174\nM v[144:147],a[20:23],a[132:135],v[144:147]\nv_exp_f32 v175,v175\nM v[144:147],a[24:27],a[136:139],v[144:147]\nv_cvt_pk_bf16_f32 v220,v172,v173\nv_cvt_pk_bf16_f32 v221,v174,v175\n" \
"M v[144:147],a[28:31],a[140:143],v[144:147]\nds_read_b128 a[76:79],v250 offset:3072\nds_read_b128 a[108:111],v250 offset:11264\nM v[148:151],a[28:31],a[156:159],v[228:231]\nv_cvt_pk_bf16_f32 v214,v180,v181\nv_cvt_pk_bf16_f32 v215,v182,v183\nM v[148:151],a[24:27],a[152:155],v[148:151]\nv_exp_f32 v184,v184\n" \
"M v[148:151],a[20:23],a[148:151],v[148:151]\nbuffer_load_dwordx4 v240,s[44:47],s95 offen offset:1024 sc1 lds\nM v[148:151],a[16:19],a[144:147],v[148:151]\nv_exp_f32 v185,v185\nM v[152:155],a[16:19],a[160:163],v[232:235]\nv_exp_f32 v186,v186\nM v[152:155],a[20:23],a[164:167],v[152:155]\nds_read_b128 a[80:83],v250 offset:4096\n" \
"ds_read_b128 a[112:115],v250 offset:12288\nM v[152:155],a[24:27],a[168:171],v[152:155]\nv_exp_f32 v187,v187\nM v[152:155],a[28:31],a[172:175],v[152:155]\nv_cvt_pk_bf16_f32 v218,v184,v185\nv_cvt_pk_bf16_f32 v219,v186,v187\nM v[156:159],a[28:31],a[188:191],v[236:239]\nv_exp_f32 v188,v188\n" \
"M v[156:159],a[24:27],a[184:187],v[156:159]\nv_exp_f32 v189,v189\nM v[156:159],a[20:23],a[180:183],v[156:159]\nv_exp_f32 v190,v190\nM v[156:159],a[16:19],a[176:179],v[156:159]\nds_read_b128 a[84:87],v250 offset:5120\nds_read_b128 a[116:119],v250 offset:13312\nM v[160:163],a[32:35],a[128:131],v[224:227]\n" \
"global_load_dwordx2 a[228:229],v248,s[24:25]\nM v[160:163],a[36:39],a[132:135],v[160:163]\nv_add_u32 v248,s71,v248\nv_cvt_pk_bf16_f32 v222,v188,v189\nM v[160:163],a[40:43],a[136:139],v[160:163]\nv_exp_f32 v191,v191\nM v[160:163],a[44:47],a[140:143],v[160:163]\nv_cvt_pk_bf16_f32 v223,v190,v191\n" \
"s_add_u32 s94,s94,0x4000\nM v[164:167],a[44:47],a[156:159],v[228:231]\nv_exp_f32 v128,v128\nM v[164:167],a[40:43],a[152:155],v[164:167]\nds_read_b128 a[88:91],v250 offset:6144\nds_read_b128 a[120:123],v250 offset:14336\nM v[164:167],a[36:39],a[148:151],v[164:167]\nv_exp_f32 v129,v129\n" \
"M v[164:167],a[32:35],a[144:147],v[164:167]\nv_exp_f32 v130,v130\nM v[168:171],a[32:35],a[160:163],v[232:235]\nbuffer_load_dwordx4 v240,s[44:47],s95 offen offset:2048 sc1 lds\nM v[168:171],a[36:39],a[164:167],v[168:171]\nv_exp_f32 v131,v131\nM v[168:171],a[40:43],a[168:171],v[168:171]\nv_exp_f32 v144,v144\n" \
"M v[168:171],a[44:47],a[172:175],v[168:171]\nds_read_b128 a[92:95],v250 offset:7168\nds_read_b128 a[124:127],v250 offset:15360\nM v[172:175],a[44:47],a[188:191],v[236:239]\nv_xor_b32 v250,0x18000,v250\ns_and_b32 s94,s94,0xffff\nM v[172:175],a[40:43],a[184:187],v[172:175]\nv_exp_f32 v145,v145\n" \
"M v[172:175],a[36:39],a[180:183],v[172:175]\nv_exp_f32 v146,v146\nM v[172:175],a[32:35],a[176:179],v[172:175]\ns_waitcnt lgkmcnt(4)\nM v[176:179],a[48:51],a[128:131],v[224:227]\nv_exp_f32 v147,v147\nM v[176:179],a[52:55],a[132:135],v[176:179]\nv_exp_f32 v132,v132\n" \
"M v[176:179],a[56:59],a[136:139],v[176:179]\nglobal_load_dwordx2 a[214:215],v248,s[22:23]\nM v[176:179],a[60:63],a[140:143],v[176:179]\nv_exp_f32 v133,v133\nM v[180:183],a[60:63],a[156:159],v[228:231]\nv_exp_f32 v134,v134\nM v[180:183],a[56:59],a[152:155],v[180:183]\nv_exp_f32 v135,v135\n" \
"M v[180:183],a[52:55],a[148:151],v[180:183]\nv_exp_f32 v148,v148\nM v[180:183],a[48:51],a[144:147],v[180:183]\nv_exp_f32 v149,v149\nM v[184:187],a[48:51],a[160:163],v[232:235]\nv_exp_f32 v150,v150\nM v[184:187],a[52:55],a[164:167],v[184:187]\nv_exp_f32 v151,v151\n" \
"M v[184:187],a[56:59],a[168:171],v[184:187]\nbuffer_load_dwordx4 v240,s[44:47],s95 offen offset:3072 sc1 lds\nM v[184:187],a[60:63],a[172:175],v[184:187]\nv_exp_f32 v136,v136\nM v[188:191],a[60:63],a[188:191],v[236:239]\nv_exp_f32 v137,v137\nM v[188:191],a[56:59],a[184:187],v[188:191]\nv_exp_f32 v138,v138\n" \
"M v[188:191],a[52:55],a[180:183],v[188:191]\nglobal_load_dwordx2 a[230:231],v248,s[24:25]\nM v[188:191],a[48:51],a[176:179],v[188:191]\ns_add_u32 m0,m0,0x10000\nv_add_u32 v248,s71,v248\nM v[0:3],a[64:67],v[192:195],v[0:3]\ns_waitcnt vmcnt(16) lgkmcnt(0)\ns_barrier\n" \
"M v[0:3],a[96:99],v[208:211],v[0:3]\nds_read_b128 a[0:3],v250 offset:0\nds_read_b128 a[4:7],v250 offset:1024\nM v[8:11],a[68:71],v[192:195],v[8:11]\nv_exp_f32 v139,v139\nM v[8:11],a[100:103],v[208:211],v[8:11]\nv_exp_f32 v152,v152\nM v[16:19],a[72:75],v[192:195],v[16:19]\n" \
"v_exp_f32 v153,v153\nM v[16:19],a[104:107],v[208:211],v[16:19]\nbuffer_load_dwordx4 v240,s[48:51],s95 offen offset:0 sc1 lds\nM v[24:27],a[76:79],v[192:195],v[24:27]\nv_exp_f32 v154,v154\nM v[24:27],a[108:111],v[208:211],v[24:27]\nv_exp_f32 v155,v155\nM v[32:35],a[80:83],v[192:195],v[32:35]\n" \
"v_exp_f32 v160,v160\nM v[32:35],a[112:115],v[208:211],v[32:35]\nds_read_b128 a[8:11],v250 offset:2048\nds_read_b128 a[12:15],v250 offset:3072\nM v[40:43],a[84:87],v[192:195],v[40:43]\nv_exp_f32 v161,v161\nM v[40:43],a[116:119],v[208:211],v[40:43]\nv_exp_f32 v162,v162\n" \
"M v[48:51],a[88:91],v[192:195],v[48:51]\nv_exp_f32 v163,v163\nM v[48:51],a[120:123],v[208:211],v[48:51]\nglobal_load_dwordx2 a[216:217],v248,s[22:23]\nM v[56:59],a[92:95],v[192:195],v[56:59]\nv_exp_f32 v176,v176\nM v[56:59],a[124:127],v[208:211],v[56:59]\nv_exp_f32 v177,v177\n" \
"M a[192:195],a[208:211],v[192:195],a[192:195]\nv_exp_f32 v178,v178\nM a[192:195],a[208:211],v[208:211],a[192:195]\nds_read_b128 a[16:19],v250 offset:4096\nds_read_b128 a[20:23],v250 offset:5120\nM v[4:7],a[64:67],v[196:199],v[4:7]\nv_exp_f32 v179,v179\nM v[4:7],a[96:99],v[212:215],v[4:7]\n" \
"v_exp_f32 v140,v140\nM v[12:15],a[68:71],v[196:199],v[12:15]\nv_exp_f32 v141,v141\nM v[12:15],a[100:103],v[212:215],v[12:15]\nbuffer_load_dwordx4 v240,s[48:51],s95 offen offset:1024 sc1 lds\nM v[20:23],a[72:75],v[196:199],v[20:23]\nv_exp_f32 v142,v142\nM v[20:23],a[104:107],v[212:215],v[20:23]\n" \
"v_exp_f32 v143,v143\nM v[28:31],a[76:79],v[196:199],v[28:31]\nv_exp_f32 v156,v156\nM v[28:31],a[108:111],v[212:215],v[28:31]\nds_read_b128 a[24:27],v250 offset:6144\nds_read_b128 a[28:31],v250 offset:7168\nM v[36:39],a[80:83],v[196:199],v[36:39]\nv_exp_f32 v157,v157\n" \
"M v[36:39],a[112:115],v[212:215],v[36:39]\nv_exp_f32 v158,v158\nM v[44:47],a[84:87],v[196:199],v[44:47]\nv_exp_f32 v159,v159\nM v[44:47],a[116:119],v[212:215],v[44:47]\nglobal_load_dwordx2 a[232:233],v248,s[24:25]\nM v[52:55],a[88:91],v[196:199],v[52:55]\nv_add_u32 v248,s71,v248\n" \
"v_cvt_pk_bf16_f32 v192,v128,v129\nM v[52:55],a[120:123],v[212:215],v[52:55]\nv_exp_f32 v164,v164\nM v[60:63],a[92:95],v[196:199],v[60:63]\nv_exp_f32 v165,v165\nM v[60:63],a[124:127],v[212:215],v[60:63]\nds_read_b128 a[32:35],v250 offset:8192\nds_read_b128 a[36:39],v250 offset:9216\n" \
"M a[196:199],a[208:211],v[196:199],a[196:199]\nv_exp_f32 v166,v166\nM a[196:199],a[208:211],v[212:215],a[196:199]\nv_exp_f32 v167,v167\nM v[64:67],a[64:67],v[200:203],v[64:67]\nv_exp_f32 v180,v180\nM v[64:67],a[96:99],v[216:219],v[64:67]\nbuffer_load_dwordx4 v240,s[48:51],s95 offen offset:2048 sc1 lds\n" \
"M v[72:75],a[68:71],v[200:203],v[72:75]\nv_exp_f32 v181,v181\nM v[72:75],a[100:103],v[216:219],v[72:75]\nv_exp_f32 v182,v182\nM v[80:83],a[72:75],v[200:203],v[80:83]\nv_exp_f32 v183,v183\nM v[80:83],a[104:107],v[216:219],v[80:83]\nds_read_b128 a[40:43],v250 offset:10240\n" \
"ds_read_b128 a[44:47],v250 offset:11264\nM v[88:91],a[76:79],v[200:203],v[88:91]\nv_exp_f32 v168,v168\nM v[88:91],a[108:111],v[216:219],v[88:91]\nv_exp_f32 v169,v169\nM v[96:99],a[80:83],v[200:203],v[96:99]\nv_exp_f32 v170,v170\nM v[96:99],a[112:115],v[216:219],v[96:99]\n" \
"global_load_dwordx2 a[218:219],v248,s[22:23]\nM v[104:107],a[84:87],v[200:203],v[104:107]\nv_exp_f32 v171,v171\nM v[104:107],a[116:119],v[216:219],v[104:107]\nv_cvt_pk_bf16_f32 v193,v130,v131\nv_cvt_pk_bf16_f32 v194,v144,v145\nM v[112:115],a[88:91],v[200:203],v[112:115]\nv_cvt_pk_bf16_f32 v195,v146,v147\n" \
"v_cvt_pk_bf16_f32 v208,v160,v161\nM v[112:115],a[120:123],v[216:219],v[112:115]\nds_read_b128 a[48:51],v250 offset:12288\nds_read_b128 a[52:55],v250 offset:13312\nM v[120:123],a[92:95],v[200:203],v[120:123]\nv_cvt_pk_bf16_f32 v209,v162,v163\nv_cvt_pk_bf16_f32 v210,v176,v177\nM v[120:123],a[124:127],v[216:219],v[120:123]\n" \
"v_cvt_pk_bf16_f32 v211,v178,v179\nv_cvt_pk_bf16_f32 v196,v132,v133\nM a[200:203],a[208:211],v[200:203],a[200:203]\nv_cvt_pk_bf16_f32 v197,v134,v135\nv_cvt_pk_bf16_f32 v198,v148,v149\nM a[200:203],a[208:211],v[216:219],a[200:203]\nbuffer_load_dwordx4 v240,s[48:51],s95 offen offset:3072 sc1 lds\nM v[68:71],a[64:67],v[204:207],v[68:71]\n" \
"v_cvt_pk_bf16_f32 v199,v150,v151\nv_cvt_pk_bf16_f32 v200,v136,v137\nM v[68:71],a[96:99],v[220:223],v[68:71]\nv_cvt_pk_bf16_f32 v201,v138,v139\nv_cvt_pk_bf16_f32 v202,v152,v153\nM v[76:79],a[68:71],v[204:207],v[76:79]\nv_cvt_pk_bf16_f32 v203,v154,v155\ns_add_u32 s95,s95,0x4000\n" \
"M v[76:79],a[100:103],v[220:223],v[76:79]\nds_read_b128 a[56:59],v250 offset:14336\nds_read_b128 a[60:63],v250 offset:15360\nM v[84:87],a[72:75],v[204:207],v[84:87]\nv_add_u32 v250,s94,v241\nM v[84:87],a[104:107],v[220:223],v[84:87]\nM v[92:95],a[76:79],v[204:207],v[92:95]\nM v[92:95],a[108:111],v[220:223],v[92:95]\n" \
"global_load_dwordx2 a[234:235],v248,s[24:25]\nM v[100:103],a[80:83],v[204:207],v[100:103]\nv_add_u32 v248,s71,v248\nM v[100:103],a[112:115],v[220:223],v[100:103]\nM v[108:111],a[84:87],v[204:207],v[108:111]\nM v[108:111],a[116:119],v[220:223],v[108:111]\nM v[116:119],a[88:91],v[204:207],v[116:119]\nM v[116:119],a[120:123],v[220:223],v[116:119]\n" \
"M v[124:127],a[92:95],v[204:207],v[124:127]\nM v[124:127],a[124:127],v[220:223],v[124:127]\nM a[204:207],a[208:211],v[204:207],a[204:207]\nM a[204:207],a[208:211],v[220:223],a[204:207]\ns_waitcnt lgkmcnt(4)\nM v[128:131],a[0:3],a[128:131],v[224:227]\ns_add_u32 m0,s94,s64\nv_cvt_pk_bf16_f32 v204,v140,v141\n" \
"M v[128:131],a[4:7],a[132:135],v[128:131]\nds_read_b128 a[64:67],v250 offset:0\nds_read_b128 a[96:99],v250 offset:8192\nM v[128:131],a[8:11],a[136:139],v[128:131]\nv_cvt_pk_bf16_f32 v205,v142,v143\nv_cvt_pk_bf16_f32 v206,v156,v157\nM v[128:131],a[12:15],a[140:143],v[128:131]\nv_cvt_pk_bf16_f32 v207,v158,v159\n" \
"v_cvt_pk_bf16_f32 v212,v164,v165\nM v[132:135],a[12:15],a[156:159],v[228:231]\nbuffer_load_dwordx4 v240,s[44:47],s95 offen offset:0 sc1 lds\nM v[132:135],a[8:11],a[152:155],v[132:135]\nv_cvt_pk_bf16_f32 v213,v166,v167\nv_cvt_pk_bf16_f32 v216,v168,v169\nM v[132:135],a[4:7],a[148:151],v[132:135]\nv_cvt_pk_bf16_f32 v217,v170,v171\n" \
"v_cvt_pk_bf16_f32 v214,v180,v181\nM v[132:135],a[0:3],a[144:147],v[132:135]\nds_read_b128 a[68:71],v250 offset:1024\nds_read_b128 a[100:103],v250 offset:9216\nM v[136:139],a[0:3],a[160:163],v[232:235]\nv_exp_f32 v172,v172\nM v[136:139],a[4:7],a[164:167],v[136:139]\nv_exp_f32 v173,v173\n" \
"M v[136:139],a[8:11],a[168:171],v[136:139]\nv_exp_f32 v174,v174\nM v[136:139],a[12:15],a[172:175],v[136:139]\nv_exp_f32 v175,v175\nM v[140:143],a[12:15],a[188:191],v[236:239]\nv_cvt_pk_bf16_f32 v220,v172,v173\nv_cvt_pk_bf16_f32 v221,v174,v175\nM v[140:143],a[8:11],a[184:187],v[140:143]\n" \
"ds_read_b128 a[72:75],v250 offset:2048\nds_read_b128 a[104:107],v250 offset:10240\nM v[140:143],a[4:7],a[180:183],v[140:143]\nglobal_load_dwordx2 a[220:221],v248,s[22:23]\nM v[140:143],a[0:3],a[176:179],v[140:143]\nv_cvt_pk_bf16_f32 v215,v182,v183\ns_add_u32 s94,s94,0x4000\nM v[144:147],a[16:19],a[128:131],v[224:227]\n" \
"v_exp_f32 v184,v184\nM v[144:147],a[20:23],a[132:135],v[144:147]\nv_exp_f32 v185,v185\nM v[144:147],a[24:27],a[136:139],v[144:147]\nv_exp_f32 v186,v186\nM v[144:147],a[28:31],a[140:143],v[144:147]\nds_read_b128 a[76:79],v250 offset:3072\nds_read_b128 a[108:111],v250 offset:11264\n" \
"M v[148:151],a[28:31],a[156:159],v[228:231]\nv_exp_f32 v187,v187\nM v[148:151],a[24:27],a[152:155],v[148:151]\nv_cvt_pk_bf16_f32 v218,v184,v185\nv_cvt_pk_bf16_f32 v219,v186,v187\nM v[148:151],a[20:23],a[148:151],v[148:151]\nbuffer_load_dwordx4 v240,s[44:47],s95 offen offset:1024 sc1 lds\nM v[148:151],a[16:19],a[144:147],v[148:151]\n" \
"v_exp_f32 v188,v188\nM v[152:155],a[16:19],a[160:163],v[232:235]\nv_exp_f32 v189,v189\nM v[152:155],a[20:23],a[164:167],v[152:155]\nds_read_b128 a[80:83],v250 offset:4096\nds_read_b128 a[112:115],v250 offset:12288\nM v[152:155],a[24:27],a[168:171],v[152:155]\nv_exp_f32 v190,v190\n" \
"M v[152:155],a[28:31],a[172:175],v[152:155]\nv_exp_f32 v191,v191\nM v[156:159],a[28:31],a[188:191],v[236:239]\nv_cvt_pk_bf16_f32 v222,v188,v189\nv_cvt_pk_bf16_f32 v223,v190,v191\nM v[156:159],a[24:27],a[184:187],v[156:159]\nv_exp_f32 v128,v128\nM v[156:159],a[20:23],a[180:183],v[156:159]\n" \
"v_exp_f32 v129,v129\nM v[156:159],a[16:19],a[176:179],v[156:159]\nds_read_b128 a[84:87],v250 offset:5120\nds_read_b128 a[116:119],v250 offset:13312\nM v[160:163],a[32:35],a[128:131],v[224:227]\nglobal_load_dwordx2 a[236:237],v248,s[24:25]\nM v[160:163],a[36:39],a[132:135],v[160:163]\nv_add_u32 v248,s71,v248\n" \
"s_and_b32 s94,s94,0xffff\nM v[160:163],a[40:43],a[136:139],v[160:163]\nv_exp_f32 v130,v130\nM v[160:163],a[44:47],a[140:143],v[160:163]\nv_exp_f32 v131,v131\nM v[164:167],a[44:47],a[156:159],v[228:231]\nv_exp_f32 v144,v144\nM v[164:167],a[40:43],a[152:155],v[164:167]\n" \
"ds_read_b128 a[88:91],v250 offset:6144\nds_read_b128 a[120:123],v250 offset:14336\nM v[164:167],a[36:39],a[148:151],v[164:167]\nv_exp_f32 v145,v145\nM v[164:167],a[32:35],a[144:147],v[164:167]\nv_exp_f32 v146,v146\nM v[168:171],a[32:35],a[160:163],v[232:235]\nbuffer_load_dwordx4 v240,s[44:47],s95 offen offset:2048 sc1 lds\n" \
"M v[168:171],a[36:39],a[164:167],v[168:171]\nv_exp_f32 v147,v147\nM v[168:171],a[40:43],a[168:171],v[168:171]\nv_exp_f32 v132,v132\nM v[168:171],a[44:47],a[172:175],v[168:171]\nds_read_b128 a[92:95],v250 offset:7168\nds_read_b128 a[124:127],v250 offset:15360\nM v[172:175],a[44:47],a[188:191],v[236:239]\n" \
"v_exp_f32 v133,v133\nM v[172:175],a[40:43],a[184:187],v[172:175]\nv_exp_f32 v134,v134\nM v[172:175],a[36:39],a[180:183],v[172:175]\nv_exp_f32 v135,v135\nM v[172:175],a[32:35],a[176:179],v[172:175]\nv_xor_b32 v250,0x18000,v250\ns_waitcnt lgkmcnt(3)\n" \
"M v[176:179],a[48:51],a[128:131],v[224:227]\nv_exp_f32 v148,v148\nM v[176:179],a[52:55],a[132:135],v[176:179]\nv_exp_f32 v149,v149\nM v[176:179],a[56:59],a[136:139],v[176:179]\nglobal_load_dwordx2 a[222:223],v248,s[22:23]\nM v[176:179],a[60:63],a[140:143],v[176:179]\nv_exp_f32 v150,v150\n" \
"M v[180:183],a[60:63],a[156:159],v[228:231]\nv_exp_f32 v151,v151\nM v[180:183],a[56:59],a[152:155],v[180:183]\nv_exp_f32 v136,v136\nM v[180:183],a[52:55],a[148:151],v[180:183]\nv_exp_f32 v137,v137\nM v[180:183],a[48:51],a[144:147],v[180:183]\nv_exp_f32 v138,v138\n" \
"M v[184:187],a[48:51],a[160:163],v[232:235]\nv_exp_f32 v139,v139\nM v[184:187],a[52:55],a[164:167],v[184:187]\nv_exp_f32 v152,v152\nM v[184:187],a[56:59],a[168:171],v[184:187]\nbuffer_load_dwordx4 v240,s[44:47],s95 offen offset:3072 sc1 lds\nM v[184:187],a[60:63],a[172:175],v[184:187]\nv_exp_f32 v153,v153\n" \
"M v[188:191],a[60:63],a[188:191],v[236:239]\nv_exp_f32 v154,v154\nM v[188:191],a[56:59],a[184:187],v[188:191]\nv_exp_f32 v155,v155\nM v[188:191],a[52:55],a[180:183],v[188:191]\nglobal_load_dwordx2 a[238:239],v248,s[24:25]\nM v[188:191],a[48:51],a[176:179],v[188:191]\ns_add_u32 m0,m0,0x10000\n" \
"v_add_u32 v248,s71,v248\nM v[0:3],a[64:67],v[192:195],v[0:3]\ns_waitcnt vmcnt(16) lgkmcnt(0)\ns_barrier\nM v[0:3],a[96:99],v[208:211],v[0:3]\nds_read_b128 a[0:3],v250 offset:0\nds_read_b128 a[4:7],v250 offset:1024\nM v[8:11],a[68:71],v[192:195],v[8:11]\n" \
"v_exp_f32 v160,v160\nM v[8:11],a[100:103],v[208:211],v[8:11]\nv_exp_f32 v161,v161\nM v[16:19],a[72:75],v[192:195],v[16:19]\nv_exp_f32 v162,v162\nM v[16:19],a[104:107],v[208:211],v[16:19]\nbuffer_load_dwordx4 v240,s[48:51],s95 offen offset:0 sc1 lds\nM v[24:27],a[76:79],v[192:195],v[24:27]\n" \
"v_exp_f32 v163,v163\nM v[24:27],a[108:111],v[208:211],v[24:27]\nv_exp_f32 v176,v176\nM v[32:35],a[80:83],v[192:195],v[32:35]\nv_exp_f32 v177,v177\nM v[32:35],a[112:115],v[208:211],v[32:35]\nds_read_b128 a[8:11],v250 offset:2048\nds_read_b128 a[12:15],v250 offset:3072\n" \
"M v[40:43],a[84:87],v[192:195],v[40:43]\nv_exp_f32 v178,v178\nM v[40:43],a[116:119],v[208:211],v[40:43]\nv_exp_f32 v179,v179\nM v[48:51],a[88:91],v[192:195],v[48:51]\nv_exp_f32 v140,v140\nM v[48:51],a[120:123],v[208:211],v[48:51]\nglobal_load_dwordx2 a[224:225],v248,s[22:23]\n" \
"M v[56:59],a[92:95],v[192:195],v[56:59]\nv_exp_f32 v141,v141\nM v[56:59],a[124:127],v[208:211],v[56:59]\nv_exp_f32 v142,v142\nM a[192:195],a[208:211],v[192:195],a[192:195]\nv_exp_f32 v143,v143\nM a[192:195],a[208:211],v[208:211],a[192:195]\nds_read_b128 a[16:19],v250 offset:4096\n" \
"ds_read_b128 a[20:23],v250 offset:5120\nM v[4:7],a[64:67],v[196:199],v[4:7]\nv_exp_f32 v156,v156\nM v[4:7],a[96:99],v[212:215],v[4:7]\nv_exp_f32 v157,v157\nM v[12:15],a[68:71],v[196:199],v[12:15]\nv_exp_f32 v158,v158\nM v[12:15],a[100:103],v[212:215],v[12:15]\n" \
"buffer_load_dwordx4 v240,s[48:51],s95 offen offset:1024 sc1 lds\nM v[20:23],a[72:75],v[196:199],v[20:23]\nv_exp_f32 v159,v159\nM v[20:23],a[104:107],v[212:215],v[20:23]\nv_exp_f32 v164,v164\nM v[28:31],a[76:79],v[196:199],v[28:31]\nv_exp_f32 v165,v165\nM v[28:31],a[108:111],v[212:215],v[28:31]\n" \
"ds_read_b128 a[24:27],v250 offset:6144\nds_read_b128 a[28:31],v250 offset:7168\nM v[36:39],a[80:83],v[196:199],v[36:39]\nv_exp_f32 v166,v166\nM v[36:39],a[112:115],v[212:215],v[36:39]\nv_exp_f32 v167,v167\nM v[44:47],a[84:87],v[196:199],v[44:47]\nv_exp_f32 v180,v180\n" \
"M v[44:47],a[116:119],v[212:215],v[44:47]\nglobal_load_dwordx2 a[240:241],v248,s[24:25]\nM v[52:55],a[88:91],v[196:199],v[52:55]\nv_add_u32 v248,s71,v248\nv_cvt_pk_bf16_f32 v192,v128,v129\nM v[52:55],a[120:123],v[212:215],v[52:55]\nv_exp_f32 v181,v181\nM v[60:63],a[92:95],v[196:199],v[60:63]\n" \
"v_exp_f32 v182,v182\nM v[60:63],a[124:127],v[212:215],v[60:63]\nds_read_b128 a[32:35],v250 offset:8192\nds_read_b128 a[36:39],v250 offset:9216\nM a[196:199],a[208:211],v[196:199],a[196:199]\nv_exp_f32 v183,v183\nM a[196:199],a[208:211],v[212:215],a[196:199]\nv_exp_f32 v168,v168\n" \
"M v[64:67],a[64:67],v[200:203],v[64:67]\nv_exp_f32 v169,v169\nM v[64:67],a[96:99],v[216:219],v[64:67]\nbuffer_load_dwordx4 v240,s[48:51],s95 offen offset:2048 sc1 lds\nM v[72:75],a[68:71],v[200:203],v[72:75]\nv_exp_f32 v170,v170\nM v[72:75],a[100:103],v[216:219],v[72:75]\nv_exp_f32 v171,v171\n" \
"M v[80:83],a[72:75],v[200:203],v[80:83]\nv_cvt_pk_bf16_f32 v193,v130,v131\nv_cvt_pk_bf16_f32 v194,v144,v145\nM v[80:83],a[104:107],v[216:219],v[80:83]\nds_read_b128 a[40:43],v250 offset:10240\nds_read_b128 a[44:47],v250 offset:11264\nM v[88:91],a[76:79],v[200:203],v[88:91]\nv_cvt_pk_bf16_f32 v195,v146,v147\n" \
"v_cvt_pk_bf16_f32 v208,v160,v161\nM v[88:91],a[108:111],v[216:219],v[88:91]\nv_cvt_pk_bf16_f32 v209,v162,v163\nv_cvt_pk_bf16_f32 v210,v176,v177\nM v[96:99],a[80:83],v[200:203],v[96:99]\nv_cvt_pk_bf16_f32 v211,v178,v179\nv_cvt_pk_bf16_f32 v196,v132,v133\nM v[96:99],a[112:115],v[216:219],v[96:99]\n" \
"global_load_dwordx2 a[226:227],v248,s[22:23]\nM v[104:107],a[84:87],v[200:203],v[104:107]\nv_cvt_pk_bf16_f32 v197,v134,v135\nv_cvt_pk_bf16_f32 v198,v148,v149\nM v[104:107],a[116:119],v[216:219],v[104:107]\nv_cvt_pk_bf16_f32 v199,v150,v151\nM v[112:115],a[88:91],v[200:203],v[112:115]\nM v[112:115],a[120:123],v[216:219],v[112:115]\n" \
"ds_read_b128 a[48:51],v250 offset:12288\nds_read_b128 a[52:55],v250 offset:13312\nM v[120:123],a[92:95],v[200:203],v[120:123]\nM v[120:123],a[124:127],v[216:219],v[120:123]\nM a[200:203],a[208:211],v[200:203],a[200:203]\nv_cvt_pk_bf16_f32 v200,v136,v137\nv_cvt_pk_bf16_f32 v201,v138,v139\nM a[200:203],a[208:211],v[216:219],a[200:203]\n" \
"buffer_load_dwordx4 v240,s[48:51],s95 offen offset:3072 sc1 lds\nM v[68:71],a[64:67],v[204:207],v[68:71]\nv_cvt_pk_bf16_f32 v202,v152,v153\nv_cvt_pk_bf16_f32 v203,v154,v155\nM v[68:71],a[96:99],v[220:223],v[68:71]\ns_add_u32 s95,s95,0x4000\nM v[76:79],a[68:71],v[204:207],v[76:79]\nM v[76:79],a[100:103],v[220:223],v[76:79]\n" \
"ds_read_b128 a[56:59],v250 offset:14336\nds_read_b128 a[60:63],v250 offset:15360\nM v[84:87],a[72:75],v[204:207],v[84:87]\nv_add_u32 v250,s94,v241\nM v[84:87],a[104:107],v[220:223],v[84:87]\nM v[92:95],a[76:79],v[204:207],v[92:95]\nM v[92:95],a[108:111],v[220:223],v[92:95]\nglobal_load_dwordx2 a[242:243],v248,s[24:25]\n" \
"M v[100:103],a[80:83],v[204:207],v[100:103]\nv_add_u32 v248,s71,v248\nM v[100:103],a[112:115],v[220:223],v[100:103]\nM v[108:111],a[84:87],v[204:207],v[108:111]\nM v[108:111],a[116:119],v[220:223],v[108:111]\nM v[116:119],a[88:91],v[204:207],v[116:119]\nM v[116:119],a[120:123],v[220:223],v[116:119]\nM v[124:127],a[92:95],v[204:207],v[124:127]\n" \
"M v[124:127],a[124:127],v[220:223],v[124:127]\nM a[204:207],a[208:211],v[204:207],a[204:207]\nM a[204:207],a[208:211],v[220:223],a[204:207]\ns_cmp_eq_u32 s62,7\ns_cbranch_scc0 .Ltrans\ns_waitcnt lgkmcnt(4)\nM v[136:139],a[0:3],a[160:163],v[232:235]\ns_add_u32 m0,s94,s64\n" \
"v_cvt_pk_bf16_f32 v204,v140,v141\nM v[136:139],a[4:7],a[164:167],v[136:139]\nds_read_b128 a[64:67],v250 offset:0\nds_read_b128 a[96:99],v250 offset:8192\nM v[136:139],a[8:11],a[168:171],v[136:139]\nbuffer_load_dwordx4 v240,s[44:47],s95 offen offset:0 sc1 lds\nM v[136:139],a[12:15],a[172:175],v[136:139]\nv_cvt_pk_bf16_f32 v205,v142,v143\n" \
"v_cvt_pk_bf16_f32 v206,v156,v157\nM v[140:143],a[12:15],a[188:191],v[236:239]\nds_read_b128 a[68:71],v250 offset:1024\nds_read_b128 a[100:103],v250 offset:9216\nM v[140:143],a[8:11],a[184:187],v[140:143]\nbuffer_store_dwordx4 a[248:251],v253,s[52:55],s33 offen offset:0 sc1\nM v[140:143],a[4:7],a[180:183],v[140:143]\nv_cvt_pk_bf16_f32 v207,v158,v159\n" \
"v_cvt_pk_bf16_f32 v216,v168,v169\nM v[140:143],a[0:3],a[176:179],v[140:143]\nds_read_b128 a[72:75],v250 offset:2048\nds_read_b128 a[104:107],v250 offset:10240\nM v[152:155],a[16:19],a[160:163],v[232:235]\nv_cvt_pk_bf16_f32 v217,v170,v171\ns_add_u32 s71,s33,16384\nM v[152:155],a[20:23],a[164:167],v[152:155]\n" \
"v_exp_f32 v172,v172\nM v[152:155],a[24:27],a[168:171],v[152:155]\nds_read_b128 a[76:79],v250 offset:3072\nds_read_b128 a[108:111],v250 offset:11264\nM v[152:155],a[28:31],a[172:175],v[152:155]\nbuffer_load_dwordx4 v240,s[44:47],s95 offen offset:1024 sc1 lds\nM v[156:159],a[28:31],a[188:191],v[236:239]\nv_exp_f32 v173,v173\n" \
"M v[156:159],a[24:27],a[184:187],v[156:159]\nds_read_b128 a[80:83],v250 offset:4096\nds_read_b128 a[112:115],v250 offset:12288\nM v[156:159],a[20:23],a[180:183],v[156:159]\nbuffer_store_dwordx4 a[248:251],v253,s[52:55],s33 offen offset:1024 sc1\nM v[156:159],a[16:19],a[176:179],v[156:159]\nv_exp_f32 v174,v174\nM v[168:171],a[32:35],a[160:163],v[232:235]\n" \
"ds_read_b128 a[84:87],v250 offset:5120\nds_read_b128 a[116:119],v250 offset:13312\nM v[168:171],a[36:39],a[164:167],v[168:171]\nv_exp_f32 v175,v175\nM v[168:171],a[40:43],a[168:171],v[168:171]\nv_cvt_pk_bf16_f32 v220,v172,v173\nv_cvt_pk_bf16_f32 v221,v174,v175\nM v[168:171],a[44:47],a[172:175],v[168:171]\n" \
"ds_read_b128 a[88:91],v250 offset:6144\nds_read_b128 a[120:123],v250 offset:14336\nM v[172:175],a[44:47],a[188:191],v[236:239]\nbuffer_load_dwordx4 v240,s[44:47],s95 offen offset:2048 sc1 lds\nM v[172:175],a[40:43],a[184:187],v[172:175]\nv_exp_f32 v184,v184\nM v[172:175],a[36:39],a[180:183],v[172:175]\nv_exp_f32 v185,v185\n" \
"v_exp_f32 v186,v186\nv_exp_f32 v187,v187\nds_read_b128 a[92:95],v250 offset:7168\nds_read_b128 a[124:127],v250 offset:15360\nM v[172:175],a[32:35],a[176:179],v[172:175]\nbuffer_store_dwordx4 a[248:251],v253,s[52:55],s33 offen offset:2048 sc1\nv_cvt_pk_bf16_f32 v218,v184,v185\nv_cvt_pk_bf16_f32 v219,v186,v187\n" \
"s_waitcnt lgkmcnt(6)\nM v[184:187],a[48:51],a[160:163],v[232:235]\nv_exp_f32 v188,v188\nM v[184:187],a[52:55],a[164:167],v[184:187]\nv_exp_f32 v189,v189\nM v[184:187],a[56:59],a[168:171],v[184:187]\nv_exp_f32 v190,v190\nv_exp_f32 v191,v191\n" \
"M v[184:187],a[60:63],a[172:175],v[184:187]\nv_cvt_pk_bf16_f32 v222,v188,v189\nv_cvt_pk_bf16_f32 v223,v190,v191\nM v[188:191],a[60:63],a[188:191],v[236:239]\nv_xor_b32 v250,0x18000,v250\ns_add_u32 s70,s78,16384\nM v[188:191],a[56:59],a[184:187],v[188:191]\nbuffer_load_dwordx4 v240,s[44:47],s95 offen offset:3072 sc1 lds\n" \
"M v[188:191],a[52:55],a[180:183],v[188:191]\ns_add_u32 m0,m0,0x10000\nv_cvt_pk_bf16_f32 v212,v164,v165\nM v[188:191],a[48:51],a[176:179],v[188:191]\nv_cvt_pk_bf16_f32 v213,v166,v167\nv_cvt_pk_bf16_f32 v214,v180,v181\nM v[64:67],a[64:67],v[200:203],v[64:67]\nbuffer_store_dwordx4 a[248:251],v253,s[52:55],s33 offen offset:3072 sc1\n" \
"M v[64:67],a[96:99],v[216:219],v[64:67]\nv_cvt_pk_bf16_f32 v215,v182,v183\ns_add_u32 s94,s94,0x4000\nM v[72:75],a[68:71],v[200:203],v[72:75]\ns_waitcnt vmcnt(16) lgkmcnt(0)\ns_barrier\nM v[72:75],a[100:103],v[216:219],v[72:75]\nds_read_b128 a[0:3],v250 offset:0\n" \
"ds_read_b128 a[4:7],v250 offset:1024\nM v[80:83],a[72:75],v[200:203],v[80:83]\nv_exp_f32 v136,v136\nM v[80:83],a[104:107],v[216:219],v[80:83]\nbuffer_store_dwordx4 a[248:251],v253,s[52:55],s71 offen offset:0 sc1\nM v[88:91],a[76:79],v[200:203],v[88:91]\nv_exp_f32 v137,v137\nM v[88:91],a[108:111],v[216:219],v[88:91]\n" \
"buffer_load_dwordx4 v240,s[48:51],s95 offen offset:0 sc1 lds\nM v[96:99],a[80:83],v[200:203],v[96:99]\nv_exp_f32 v138,v138\nM v[96:99],a[112:115],v[216:219],v[96:99]\nbuffer_store_dwordx4 a[248:251],v253,s[52:55],s71 offen offset:1024 sc1\nM v[104:107],a[84:87],v[200:203],v[104:107]\nv_exp_f32 v139,v139\nM v[104:107],a[116:119],v[216:219],v[104:107]\n" \
"ds_read_b128 a[8:11],v250 offset:2048\nds_read_b128 a[12:15],v250 offset:3072\nM v[112:115],a[88:91],v[200:203],v[112:115]\nv_exp_f32 v152,v152\nM v[112:115],a[120:123],v[216:219],v[112:115]\nbuffer_store_dwordx4 a[248:251],v253,s[52:55],s71 offen offset:2048 sc1\nM v[120:123],a[92:95],v[200:203],v[120:123]\nv_exp_f32 v153,v153\n" \
"M v[120:123],a[124:127],v[216:219],v[120:123]\nv_exp_f32 v154,v154\nM a[200:203],a[208:211],v[200:203],a[200:203]\nv_exp_f32 v155,v155\nM a[200:203],a[208:211],v[216:219],a[200:203]\nbuffer_store_dwordx4 a[248:251],v253,s[52:55],s71 offen offset:3072 sc1\nM v[68:71],a[64:67],v[204:207],v[68:71]\nv_exp_f32 v168,v168\n" \
"M v[68:71],a[96:99],v[220:223],v[68:71]\nds_read_b128 a[16:19],v250 offset:4096\nds_read_b128 a[20:23],v250 offset:5120\nM v[76:79],a[68:71],v[204:207],v[76:79]\nv_exp_f32 v169,v169\nM v[76:79],a[100:103],v[220:223],v[76:79]\nv_exp_f32 v170,v170\nM v[84:87],a[72:75],v[204:207],v[84:87]\n" \
"v_exp_f32 v171,v171\nM v[84:87],a[104:107],v[220:223],v[84:87]\nbuffer_load_dwordx4 v240,s[48:51],s95 offen offset:1024 sc1 lds\nM v[92:95],a[76:79],v[204:207],v[92:95]\nv_exp_f32 v184,v184\nM v[92:95],a[108:111],v[220:223],v[92:95]\nbuffer_store_dwordx4 a[248:251],v253,s[52:55],s78 offen offset:0 sc1\nM v[100:103],a[80:83],v[204:207],v[100:103]\n" \
"v_exp_f32 v185,v185\nM v[100:103],a[112:115],v[220:223],v[100:103]\nds_read_b128 a[24:27],v250 offset:6144\nds_read_b128 a[28:31],v250 offset:7168\nM v[108:111],a[84:87],v[204:207],v[108:111]\nv_exp_f32 v186,v186\nM v[108:111],a[116:119],v[220:223],v[108:111]\nbuffer_store_dwordx4 a[248:251],v253,s[52:55],s78 offen offset:1024 sc1\n" \
"M v[116:119],a[88:91],v[204:207],v[116:119]\nv_exp_f32 v187,v187\nM v[116:119],a[120:123],v[220:223],v[116:119]\ns_and_b32 s94,s94,0xffff\nv_cvt_pk_bf16_f32 v200,v136,v137\nM v[124:127],a[92:95],v[204:207],v[124:127]\nv_exp_f32 v140,v140\nM v[124:127],a[124:127],v[220:223],v[124:127]\n" \
"buffer_store_dwordx4 a[248:251],v253,s[52:55],s78 offen offset:2048 sc1\nM a[204:207],a[208:211],v[204:207],a[204:207]\nv_exp_f32 v141,v141\nM a[204:207],a[208:211],v[220:223],a[204:207]\nds_read_b128 a[32:35],v250 offset:8192\nds_read_b128 a[36:39],v250 offset:9216\nM v[0:3],a[64:67],v[192:195],v[0:3]\nv_exp_f32 v142,v142\n" \
"M v[0:3],a[96:99],v[208:211],v[0:3]\nv_exp_f32 v143,v143\nM v[8:11],a[68:71],v[192:195],v[8:11]\nv_exp_f32 v156,v156\nM v[8:11],a[100:103],v[208:211],v[8:11]\nbuffer_load_dwordx4 v240,s[48:51],s95 offen offset:2048 sc1 lds\nM v[16:19],a[72:75],v[192:195],v[16:19]\nv_exp_f32 v157,v157\n" \
"M v[16:19],a[104:107],v[208:211],v[16:19]\nbuffer_store_dwordx4 a[248:251],v253,s[52:55],s78 offen offset:3072 sc1\nM v[24:27],a[76:79],v[192:195],v[24:27]\nv_exp_f32 v158,v158\nM v[24:27],a[108:111],v[208:211],v[24:27]\nds_read_b128 a[40:43],v250 offset:10240\nds_read_b128 a[44:47],v250 offset:11264\nM v[32:35],a[80:83],v[192:195],v[32:35]\n" \
"v_exp_f32 v159,v159\nM v[32:35],a[112:115],v[208:211],v[32:35]\nbuffer_store_dwordx4 a[248:251],v253,s[52:55],s70 offen offset:0 sc1\nM v[40:43],a[84:87],v[192:195],v[40:43]\nv_cvt_pk_bf16_f32 v201,v138,v139\nv_cvt_pk_bf16_f32 v202,v152,v153\nM v[40:43],a[116:119],v[208:211],v[40:43]\nv_cvt_pk_bf16_f32 v203,v154,v155\n" \
"v_cvt_pk_bf16_f32 v216,v168,v169\nM v[48:51],a[88:91],v[192:195],v[48:51]\nv_cvt_pk_bf16_f32 v217,v170,v171\nv_cvt_pk_bf16_f32 v218,v184,v185\nM v[48:51],a[120:123],v[208:211],v[48:51]\nbuffer_store_dwordx4 a[248:251],v253,s[52:55],s70 offen offset:1024 sc1\nM v[56:59],a[92:95],v[192:195],v[56:59]\nv_cvt_pk_bf16_f32 v219,v186,v187\n" \
"M v[56:59],a[124:127],v[208:211],v[56:59]\nds_read_b128 a[48:51],v250 offset:12288\nds_read_b128 a[52:55],v250 offset:13312\nM a[192:195],a[208:211],v[192:195],a[192:195]\nM a[192:195],a[208:211],v[208:211],a[192:195]\nM v[4:7],a[64:67],v[196:199],v[4:7]\nM v[4:7],a[96:99],v[212:215],v[4:7]\nbuffer_load_dwordx4 v240,s[48:51],s95 offen offset:3072 sc1 lds\n" \
"M v[12:15],a[68:71],v[196:199],v[12:15]\ns_add_u32 s95,s95,0xffff4000\nM v[12:15],a[100:103],v[212:215],v[12:15]\nbuffer_store_dwordx4 a[248:251],v253,s[52:55],s70 offen offset:2048 sc1\nM v[20:23],a[72:75],v[196:199],v[20:23]\nM v[20:23],a[104:107],v[212:215],v[20:23]\nds_read_b128 a[56:59],v250 offset:14336\nds_read_b128 a[60:63],v250 offset:15360\n" \
"M v[28:31],a[76:79],v[196:199],v[28:31]\nv_add_u32 v250,s94,v241\nM v[28:31],a[108:111],v[212:215],v[28:31]\nbuffer_store_dwordx4 a[248:251],v253,s[52:55],s70 offen offset:3072 sc1\nM v[36:39],a[80:83],v[196:199],v[36:39]\nM v[36:39],a[112:115],v[212:215],v[36:39]\nM v[44:47],a[84:87],v[196:199],v[44:47]\nM v[44:47],a[116:119],v[212:215],v[44:47]\n" \
"M v[52:55],a[88:91],v[196:199],v[52:55]\nM v[52:55],a[120:123],v[212:215],v[52:55]\nM v[60:63],a[92:95],v[196:199],v[60:63]\nM v[60:63],a[124:127],v[212:215],v[60:63]\nM a[196:199],a[208:211],v[196:199],a[196:199]\nM a[196:199],a[208:211],v[212:215],a[196:199]\n.Lsl:\ns_waitcnt lgkmcnt(4)\n" \
"M v[136:139],a[0:3],a[160:163],v[232:235]\ns_add_u32 m0,s94,s64\nv_cvt_pk_bf16_f32 v204,v140,v141\nM v[136:139],a[4:7],a[164:167],v[136:139]\nds_read_b128 a[64:67],v250 offset:0\nds_read_b128 a[96:99],v250 offset:8192\nM v[136:139],a[8:11],a[168:171],v[136:139]\nbuffer_load_dwordx4 v240,s[44:47],s95 offen offset:0 sc1 lds\n" \
"M v[136:139],a[12:15],a[172:175],v[136:139]\nv_cvt_pk_bf16_f32 v205,v142,v143\nv_cvt_pk_bf16_f32 v206,v156,v157\nM v[140:143],a[12:15],a[188:191],v[236:239]\nds_read_b128 a[68:71],v250 offset:1024\nds_read_b128 a[100:103],v250 offset:9216\nM v[140:143],a[8:11],a[184:187],v[140:143]\nv_cvt_pk_bf16_f32 v207,v158,v159\n" \
"s_add_u32 s94,s94,0x4000\nM v[140:143],a[4:7],a[180:183],v[140:143]\nv_exp_f32 v172,v172\nM v[140:143],a[0:3],a[176:179],v[140:143]\nds_read_b128 a[72:75],v250 offset:2048\nds_read_b128 a[104:107],v250 offset:10240\nM v[152:155],a[16:19],a[160:163],v[232:235]\nv_exp_f32 v173,v173\n" \
"M v[152:155],a[20:23],a[164:167],v[152:155]\nv_exp_f32 v174,v174\nM v[152:155],a[24:27],a[168:171],v[152:155]\nds_read_b128 a[76:79],v250 offset:3072\nds_read_b128 a[108:111],v250 offset:11264\nM v[152:155],a[28:31],a[172:175],v[152:155]\nbuffer_load_dwordx4 v240,s[44:47],s95 offen offset:1024 sc1 lds\nM v[156:159],a[28:31],a[188:191],v[236:239]\n" \
"v_exp_f32 v175,v175\nM v[156:159],a[24:27],a[184:187],v[156:159]\nds_read_b128 a[80:83],v250 offset:4096\nds_read_b128 a[112:115],v250 offset:12288\nM v[156:159],a[20:23],a[180:183],v[156:159]\nv_cvt_pk_bf16_f32 v220,v172,v173\nv_cvt_pk_bf16_f32 v221,v174,v175\nM v[156:159],a[16:19],a[176:179],v[156:159]\n" \
"v_exp_f32 v188,v188\nM v[168:171],a[32:35],a[160:163],v[232:235]\nds_read_b128 a[84:87],v250 offset:5120\nds_read_b128 a[116:119],v250 offset:13312\nM v[168:171],a[36:39],a[164:167],v[168:171]\nv_exp_f32 v189,v189\nM v[168:171],a[40:43],a[168:171],v[168:171]\nv_exp_f32 v190,v190\n" \
"M v[168:171],a[44:47],a[172:175],v[168:171]\nds_read_b128 a[88:91],v250 offset:6144\nds_read_b128 a[120:123],v250 offset:14336\nM v[172:175],a[44:47],a[188:191],v[236:239]\nbuffer_load_dwordx4 v240,s[44:47],s95 offen offset:2048 sc1 lds\nM v[172:175],a[40:43],a[184:187],v[172:175]\nv_exp_f32 v191,v191\nM v[172:175],a[36:39],a[180:183],v[172:175]\n" \
"ds_read_b128 a[92:95],v250 offset:7168\nds_read_b128 a[124:127],v250 offset:15360\nM v[172:175],a[32:35],a[176:179],v[172:175]\nv_cvt_pk_bf16_f32 v222,v188,v189\ns_waitcnt lgkmcnt(7)\nM v[184:187],a[48:51],a[160:163],v[232:235]\nv_cvt_pk_bf16_f32 v223,v190,v191\nv_xor_b32 v250,0x18000,v250\n" \
"M v[184:187],a[52:55],a[164:167],v[184:187]\nv_exp_f32 v136,v136\nM v[184:187],a[56:59],a[168:171],v[184:187]\nv_exp_f32 v137,v137\nM v[184:187],a[60:63],a[172:175],v[184:187]\nv_exp_f32 v138,v138\nM v[188:191],a[60:63],a[188:191],v[236:239]\nv_exp_f32 v139,v139\n" \
"M v[188:191],a[56:59],a[184:187],v[188:191]\nbuffer_load_dwordx4 v240,s[44:47],s95 offen offset:3072 sc1 lds\nM v[188:191],a[52:55],a[180:183],v[188:191]\ns_add_u32 m0,m0,0x10000\ns_and_b32 s94,s94,0xffff\nM v[188:191],a[48:51],a[176:179],v[188:191]\nv_exp_f32 v152,v152\nM v[64:67],a[64:67],v[200:203],v[64:67]\n" \
"v_exp_f32 v153,v153\nM v[64:67],a[96:99],v[216:219],v[64:67]\nv_exp_f32 v154,v154\nM v[72:75],a[68:71],v[200:203],v[72:75]\ns_waitcnt vmcnt(16) lgkmcnt(0)\ns_barrier\nM v[72:75],a[100:103],v[216:219],v[72:75]\nds_read_b128 a[0:3],v250 offset:0\n" \
"ds_read_b128 a[4:7],v250 offset:1024\nM v[80:83],a[72:75],v[200:203],v[80:83]\nv_exp_f32 v155,v155\nM v[80:83],a[104:107],v[216:219],v[80:83]\nbuffer_load_dwordx4 v240,s[48:51],s95 offen offset:0 sc1 lds\nM v[88:91],a[76:79],v[200:203],v[88:91]\nv_exp_f32 v168,v168\nM v[88:91],a[108:111],v[216:219],v[88:91]\n" \
"ds_read_b128 a[8:11],v250 offset:2048\nds_read_b128 a[12:15],v250 offset:3072\nM v[96:99],a[80:83],v[200:203],v[96:99]\nv_exp_f32 v169,v169\nM v[96:99],a[112:115],v[216:219],v[96:99]\nv_exp_f32 v170,v170\nM v[104:107],a[84:87],v[200:203],v[104:107]\nv_exp_f32 v171,v171\n" \
"M v[104:107],a[116:119],v[216:219],v[104:107]\nds_read_b128 a[16:19],v250 offset:4096\nds_read_b128 a[20:23],v250 offset:5120\nM v[112:115],a[88:91],v[200:203],v[112:115]\nv_exp_f32 v184,v184\nM v[112:115],a[120:123],v[216:219],v[112:115]\nbuffer_load_dwordx4 v240,s[48:51],s95 offen offset:1024 sc1 lds\nM v[120:123],a[92:95],v[200:203],v[120:123]\n" \
"v_exp_f32 v185,v185\nM v[120:123],a[124:127],v[216:219],v[120:123]\nds_read_b128 a[24:27],v250 offset:6144\nds_read_b128 a[28:31],v250 offset:7168\nM a[200:203],a[208:211],v[200:203],a[200:203]\nv_exp_f32 v186,v186\nM a[200:203],a[208:211],v[216:219],a[200:203]\nv_exp_f32 v187,v187\n" \
"M v[68:71],a[64:67],v[204:207],v[68:71]\nv_exp_f32 v140,v140\nM v[68:71],a[96:99],v[220:223],v[68:71]\nds_read_b128 a[32:35],v250 offset:8192\nds_read_b128 a[36:39],v250 offset:9216\nM v[76:79],a[68:71],v[204:207],v[76:79]\nv_exp_f32 v141,v141\nM v[76:79],a[100:103],v[220:223],v[76:79]\n" \
"buffer_load_dwordx4 v240,s[48:51],s95 offen offset:2048 sc1 lds\nM v[84:87],a[72:75],v[204:207],v[84:87]\nv_exp_f32 v142,v142\nM v[84:87],a[104:107],v[220:223],v[84:87]\nds_read_b128 a[40:43],v250 offset:10240\nds_read_b128 a[44:47],v250 offset:11264\nM v[92:95],a[76:79],v[204:207],v[92:95]\nv_exp_f32 v143,v143\n" \
"M v[92:95],a[108:111],v[220:223],v[92:95]\nv_exp_f32 v156,v156\nM v[100:103],a[80:83],v[204:207],v[100:103]\nv_exp_f32 v157,v157\nM v[100:103],a[112:115],v[220:223],v[100:103]\nds_read_b128 a[48:51],v250 offset:12288\nds_read_b128 a[52:55],v250 offset:13312\nM v[108:111],a[84:87],v[204:207],v[108:111]\n" \
"v_exp_f32 v158,v158\nM v[108:111],a[116:119],v[220:223],v[108:111]\nbuffer_load_dwordx4 v240,s[48:51],s95 offen offset:3072 sc1 lds\nM v[116:119],a[88:91],v[204:207],v[116:119]\nv_exp_f32 v159,v159\nM v[116:119],a[120:123],v[220:223],v[116:119]\nds_read_b128 a[56:59],v250 offset:14336\nds_read_b128 a[60:63],v250 offset:15360\n" \
"M v[124:127],a[92:95],v[204:207],v[124:127]\nv_cvt_pk_bf16_f32 v200,v136,v137\nv_cvt_pk_bf16_f32 v201,v138,v139\nM v[124:127],a[124:127],v[220:223],v[124:127]\nv_cvt_pk_bf16_f32 v202,v152,v153\nv_cvt_pk_bf16_f32 v203,v154,v155\nM a[204:207],a[208:211],v[204:207],a[204:207]\nv_cvt_pk_bf16_f32 v216,v168,v169\n" \
"v_cvt_pk_bf16_f32 v217,v170,v171\nv_cvt_pk_bf16_f32 v218,v184,v185\nv_cvt_pk_bf16_f32 v219,v186,v187\nv_add_u32 v250,s94,v241\ns_add_u32 s95,s95,0x4000\nM a[204:207],a[208:211],v[220:223],a[204:207]\ns_cmp_lg_u32 s95,s96\ns_cbranch_scc1 .Lsl\n" \
"s_waitcnt lgkmcnt(4)\nM v[128:131],a[0:3],a[128:131],v[224:227]\ns_add_u32 m0,s94,s64\nv_cvt_pk_bf16_f32 v204,v140,v141\nM v[128:131],a[4:7],a[132:135],v[128:131]\nds_read_b128 a[64:67],v250 offset:0\nds_read_b128 a[96:99],v250 offset:8192\nM v[128:131],a[8:11],a[136:139],v[128:131]\n" \
"buffer_load_dwordx4 v240,s[44:47],s95 offen offset:0 sc1 lds\nM v[128:131],a[12:15],a[140:143],v[128:131]\nv_exp_f32 v188,v188\nM v[132:135],a[12:15],a[156:159],v[228:231]\nds_read_b128 a[68:71],v250 offset:1024\nds_read_b128 a[100:103],v250 offset:9216\nM v[132:135],a[8:11],a[152:155],v[132:135]\nv_exp_f32 v189,v189\n" \
"M v[132:135],a[4:7],a[148:151],v[132:135]\nv_exp_f32 v190,v190\nM v[132:135],a[0:3],a[144:147],v[132:135]\nds_read_b128 a[72:75],v250 offset:2048\nds_read_b128 a[104:107],v250 offset:10240\nM v[144:147],a[16:19],a[128:131],v[224:227]\nv_exp_f32 v191,v191\nM v[144:147],a[20:23],a[132:135],v[144:147]\n" \
"v_exp_f32 v172,v172\nM v[144:147],a[24:27],a[136:139],v[144:147]\nds_read_b128 a[76:79],v250 offset:3072\nds_read_b128 a[108:111],v250 offset:11264\nM v[144:147],a[28:31],a[140:143],v[144:147]\nbuffer_load_dwordx4 v240,s[44:47],s95 offen offset:1024 sc1 lds\nM v[148:151],a[28:31],a[156:159],v[228:231]\nv_exp_f32 v173,v173\n" \
"M v[148:151],a[24:27],a[152:155],v[148:151]\nds_read_b128 a[80:83],v250 offset:4096\nds_read_b128 a[112:115],v250 offset:12288\nM v[148:151],a[20:23],a[148:151],v[148:151]\nv_exp_f32 v174,v174\nM v[148:151],a[16:19],a[144:147],v[148:151]\nv_exp_f32 v175,v175\nM v[160:163],a[32:35],a[128:131],v[224:227]\n" \
"ds_read_b128 a[84:87],v250 offset:5120\nds_read_b128 a[116:119],v250 offset:13312\nM v[160:163],a[36:39],a[132:135],v[160:163]\nv_cvt_pk_bf16_f32 v205,v142,v143\nv_cvt_pk_bf16_f32 v206,v156,v157\nM v[160:163],a[40:43],a[136:139],v[160:163]\nv_cvt_pk_bf16_f32 v207,v158,v159\nv_cvt_pk_bf16_f32 v220,v172,v173\n" \
"M v[160:163],a[44:47],a[140:143],v[160:163]\nds_read_b128 a[88:91],v250 offset:6144\nds_read_b128 a[120:123],v250 offset:14336\nM v[164:167],a[44:47],a[156:159],v[228:231]\nbuffer_load_dwordx4 v240,s[44:47],s95 offen offset:2048 sc1 lds\nM v[164:167],a[40:43],a[152:155],v[164:167]\nv_cvt_pk_bf16_f32 v221,v174,v175\nv_cvt_pk_bf16_f32 v222,v188,v189\n" \
"M v[164:167],a[36:39],a[148:151],v[164:167]\nds_read_b128 a[92:95],v250 offset:7168\nds_read_b128 a[124:127],v250 offset:15360\nM v[164:167],a[32:35],a[144:147],v[164:167]\nv_xor_b32 v250,0x18000,v250\ns_waitcnt lgkmcnt(7)\nM v[176:179],a[48:51],a[128:131],v[224:227]\nv_cvt_pk_bf16_f32 v223,v190,v191\n" \
"s_add_u32 s94,s94,0x4000\nM v[176:179],a[52:55],a[132:135],v[176:179]\nv_exp_f32 v128,v128\nM v[176:179],a[56:59],a[136:139],v[176:179]\nv_exp_f32 v129,v129\nM v[176:179],a[60:63],a[140:143],v[176:179]\nv_exp_f32 v130,v130\nM v[180:183],a[60:63],a[156:159],v[228:231]\n" \
"v_exp_f32 v131,v131\nM v[180:183],a[56:59],a[152:155],v[180:183]\nbuffer_load_dwordx4 v240,s[44:47],s95 offen offset:3072 sc1 lds\nM v[180:183],a[52:55],a[148:151],v[180:183]\ns_add_u32 m0,m0,0x10000\ns_and_b32 s94,s94,0xffff\nM v[180:183],a[48:51],a[144:147],v[180:183]\nv_exp_f32 v144,v144\n" \
"M v[64:67],a[64:67],v[200:203],v[64:67]\nv_exp_f32 v145,v145\nM v[64:67],a[96:99],v[216:219],v[64:67]\nv_exp_f32 v146,v146\nM v[72:75],a[68:71],v[200:203],v[72:75]\ns_waitcnt vmcnt(16) lgkmcnt(0)\ns_barrier\nM v[72:75],a[100:103],v[216:219],v[72:75]\n" \
"ds_read_b128 a[0:3],v250 offset:0\nds_read_b128 a[4:7],v250 offset:1024\nM v[80:83],a[72:75],v[200:203],v[80:83]\nv_exp_f32 v147,v147\nM v[80:83],a[104:107],v[216:219],v[80:83]\nbuffer_load_dwordx4 v240,s[48:51],s95 offen offset:0 sc1 lds\nM v[88:91],a[76:79],v[200:203],v[88:91]\nv_exp_f32 v160,v160\n" \
"M v[88:91],a[108:111],v[216:219],v[88:91]\nds_read_b128 a[8:11],v250 offset:2048\nds_read_b128 a[12:15],v250 offset:3072\nM v[96:99],a[80:83],v[200:203],v[96:99]\nv_exp_f32 v161,v161\nM v[96:99],a[112:115],v[216:219],v[96:99]\nv_exp_f32 v162,v162\nM v[104:107],a[84:87],v[200:203],v[104:107]\n" \
"v_exp_f32 v163,v163\nM v[104:107],a[116:119],v[216:219],v[104:107]\nds_read_b128 a[16:19],v250 offset:4096\nds_read_b128 a[20:23],v250 offset:5120\nM v[112:115],a[88:91],v[200:203],v[112:115]\nv_exp_f32 v176,v176\nM v[112:115],a[120:123],v[216:219],v[112:115]\nbuffer_load_dwordx4 v240,s[48:51],s95 offen offset:1024 sc1 lds\n" \
"M v[120:123],a[92:95],v[200:203],v[120:123]\nv_exp_f32 v177,v177\nM v[120:123],a[124:127],v[216:219],v[120:123]\nds_read_b128 a[24:27],v250 offset:6144\nds_read_b128 a[28:31],v250 offset:7168\nM a[200:203],a[208:211],v[200:203],a[200:203]\nv_exp_f32 v178,v178\nM a[200:203],a[208:211],v[216:219],a[200:203]\n" \
"v_exp_f32 v179,v179\nM v[68:71],a[64:67],v[204:207],v[68:71]\nv_exp_f32 v132,v132\nM v[68:71],a[96:99],v[220:223],v[68:71]\nds_read_b128 a[32:35],v250 offset:8192\nds_read_b128 a[36:39],v250 offset:9216\nM v[76:79],a[68:71],v[204:207],v[76:79]\nv_exp_f32 v133,v133\n" \
"M v[76:79],a[100:103],v[220:223],v[76:79]\nbuffer_load_dwordx4 v240,s[48:51],s95 offen offset:2048 sc1 lds\nM v[84:87],a[72:75],v[204:207],v[84:87]\nv_exp_f32 v134,v134\nM v[84:87],a[104:107],v[220:223],v[84:87]\nds_read_b128 a[40:43],v250 offset:10240\nds_read_b128 a[44:47],v250 offset:11264\nM v[92:95],a[76:79],v[204:207],v[92:95]\n" \
"v_exp_f32 v135,v135\nM v[92:95],a[108:111],v[220:223],v[92:95]\nv_exp_f32 v148,v148\nM v[100:103],a[80:83],v[204:207],v[100:103]\nv_exp_f32 v149,v149\nM v[100:103],a[112:115],v[220:223],v[100:103]\nds_read_b128 a[48:51],v250 offset:12288\nds_read_b128 a[52:55],v250 offset:13312\n" \
"M v[108:111],a[84:87],v[204:207],v[108:111]\nv_exp_f32 v150,v150\nM v[108:111],a[116:119],v[220:223],v[108:111]\nbuffer_load_dwordx4 v240,s[48:51],s95 offen offset:3072 sc1 lds\nM v[116:119],a[88:91],v[204:207],v[116:119]\nv_exp_f32 v151,v151\nM v[116:119],a[120:123],v[220:223],v[116:119]\nds_read_b128 a[56:59],v250 offset:14336\n" \
"ds_read_b128 a[60:63],v250 offset:15360\nM v[124:127],a[92:95],v[204:207],v[124:127]\nv_cvt_pk_bf16_f32 v192,v128,v129\nv_cvt_pk_bf16_f32 v193,v130,v131\nM v[124:127],a[124:127],v[220:223],v[124:127]\nv_cvt_pk_bf16_f32 v194,v144,v145\nv_cvt_pk_bf16_f32 v195,v146,v147\nM a[204:207],a[208:211],v[204:207],a[204:207]\n" \
"v_cvt_pk_bf16_f32 v208,v160,v161\nv_cvt_pk_bf16_f32 v209,v162,v163\nv_cvt_pk_bf16_f32 v210,v176,v177\nv_cvt_pk_bf16_f32 v211,v178,v179\nv_add_u32 v250,s94,v241\ns_add_u32 s95,s95,0x4000\nM a[204:207],a[208:211],v[220:223],a[204:207]\ns_branch .Lse\n" \
".Ltrans:\ns_waitcnt lgkmcnt(4)\nM v[128:131],a[0:3],a[128:131],v[224:227]\ns_add_u32 m0,s94,s64\nv_cvt_pk_bf16_f32 v212,v164,v165\nM v[128:131],a[4:7],a[132:135],v[128:131]\nds_read_b128 a[64:67],v250 offset:0\nds_read_b128 a[96:99],v250 offset:8192\n" \
"M v[128:131],a[8:11],a[136:139],v[128:131]\nbuffer_load_dwordx4 v240,s[44:47],s95 offen offset:0 sc1 lds\nM v[128:131],a[12:15],a[140:143],v[128:131]\nv_cvt_pk_bf16_f32 v213,v166,v167\nv_cvt_pk_bf16_f32 v214,v180,v181\nM v[132:135],a[12:15],a[156:159],v[228:231]\nds_read_b128 a[68:71],v250 offset:1024\nds_read_b128 a[100:103],v250 offset:9216\n" \
"M v[132:135],a[8:11],a[152:155],v[132:135]\nbuffer_store_dwordx4 a[248:251],v253,s[52:55],s33 offen offset:0 sc1\nM v[132:135],a[4:7],a[148:151],v[132:135]\nv_cvt_pk_bf16_f32 v215,v182,v183\ns_add_u32 s71,s33,16384\nM v[132:135],a[0:3],a[144:147],v[132:135]\nds_read_b128 a[72:75],v250 offset:2048\nds_read_b128 a[104:107],v250 offset:10240\n" \
"M v[144:147],a[16:19],a[128:131],v[224:227]\nv_exp_f32 v184,v184\nM v[144:147],a[20:23],a[132:135],v[144:147]\nv_exp_f32 v185,v185\nM v[144:147],a[24:27],a[136:139],v[144:147]\nds_read_b128 a[76:79],v250 offset:3072\nds_read_b128 a[108:111],v250 offset:11264\nM v[144:147],a[28:31],a[140:143],v[144:147]\n" \
"buffer_load_dwordx4 v240,s[44:47],s95 offen offset:1024 sc1 lds\nM v[148:151],a[28:31],a[156:159],v[228:231]\nv_exp_f32 v186,v186\nM v[148:151],a[24:27],a[152:155],v[148:151]\nds_read_b128 a[80:83],v250 offset:4096\nds_read_b128 a[112:115],v250 offset:12288\nM v[148:151],a[20:23],a[148:151],v[148:151]\nbuffer_store_dwordx4 a[248:251],v253,s[52:55],s33 offen offset:1024 sc1\n" \
"M v[148:151],a[16:19],a[144:147],v[148:151]\nv_exp_f32 v187,v187\nM v[160:163],a[32:35],a[128:131],v[224:227]\nds_read_b128 a[84:87],v250 offset:5120\nds_read_b128 a[116:119],v250 offset:13312\nM v[160:163],a[36:39],a[132:135],v[160:163]\nv_cvt_pk_bf16_f32 v216,v168,v169\nv_cvt_pk_bf16_f32 v217,v170,v171\n" \
"M v[160:163],a[40:43],a[136:139],v[160:163]\nv_cvt_pk_bf16_f32 v218,v184,v185\nv_cvt_pk_bf16_f32 v219,v186,v187\nM v[160:163],a[44:47],a[140:143],v[160:163]\nds_read_b128 a[88:91],v250 offset:6144\nds_read_b128 a[120:123],v250 offset:14336\nM v[164:167],a[44:47],a[156:159],v[228:231]\nbuffer_load_dwordx4 v240,s[44:47],s95 offen offset:2048 sc1 lds\n" \
"M v[164:167],a[40:43],a[152:155],v[164:167]\ns_add_u32 s70,s78,16384\nv_cvt_pk_bf16_f32 v204,v140,v141\nM v[164:167],a[36:39],a[148:151],v[164:167]\nds_read_b128 a[92:95],v250 offset:7168\nds_read_b128 a[124:127],v250 offset:15360\nM v[164:167],a[32:35],a[144:147],v[164:167]\nbuffer_store_dwordx4 a[248:251],v253,s[52:55],s33 offen offset:2048 sc1\n" \
"s_waitcnt lgkmcnt(6)\nM v[176:179],a[48:51],a[128:131],v[224:227]\nv_xor_b32 v250,0x18000,v250\nv_cvt_pk_bf16_f32 v205,v142,v143\nM v[176:179],a[52:55],a[132:135],v[176:179]\nv_exp_f32 v172,v172\nM v[176:179],a[56:59],a[136:139],v[176:179]\nv_exp_f32 v173,v173\n" \
"M v[176:179],a[60:63],a[140:143],v[176:179]\nv_exp_f32 v174,v174\nM v[180:183],a[60:63],a[156:159],v[228:231]\nv_exp_f32 v175,v175\nM v[180:183],a[56:59],a[152:155],v[180:183]\nbuffer_load_dwordx4 v240,s[44:47],s95 offen offset:3072 sc1 lds\nM v[180:183],a[52:55],a[148:151],v[180:183]\ns_add_u32 m0,m0,0x10000\n" \
"v_cvt_pk_bf16_f32 v206,v156,v157\nM v[180:183],a[48:51],a[144:147],v[180:183]\nv_exp_f32 v188,v188\nM v[0:3],a[64:67],v[192:195],v[0:3]\nbuffer_store_dwordx4 a[248:251],v253,s[52:55],s33 offen offset:3072 sc1\nM v[0:3],a[96:99],v[208:211],v[0:3]\nv_exp_f32 v189,v189\nM v[8:11],a[68:71],v[192:195],v[8:11]\n" \
"s_waitcnt vmcnt(16) lgkmcnt(0)\ns_barrier\nM v[8:11],a[100:103],v[208:211],v[8:11]\nds_read_b128 a[0:3],v250 offset:0\nds_read_b128 a[4:7],v250 offset:1024\nM v[16:19],a[72:75],v[192:195],v[16:19]\nv_exp_f32 v190,v190\nM v[16:19],a[104:107],v[208:211],v[16:19]\n" \
"buffer_store_dwordx4 a[248:251],v253,s[52:55],s71 offen offset:0 sc1\nM v[24:27],a[76:79],v[192:195],v[24:27]\nv_exp_f32 v191,v191\nM v[24:27],a[108:111],v[208:211],v[24:27]\nbuffer_load_dwordx4 v240,s[48:51],s95 offen offset:0 sc1 lds\nM v[32:35],a[80:83],v[192:195],v[32:35]\nv_cvt_pk_bf16_f32 v207,v158,v159\nv_cvt_pk_bf16_f32 v220,v172,v173\n" \
"M v[32:35],a[112:115],v[208:211],v[32:35]\nbuffer_store_dwordx4 a[248:251],v253,s[52:55],s71 offen offset:1024 sc1\nM v[40:43],a[84:87],v[192:195],v[40:43]\nv_cvt_pk_bf16_f32 v221,v174,v175\nv_cvt_pk_bf16_f32 v222,v188,v189\nM v[40:43],a[116:119],v[208:211],v[40:43]\nds_read_b128 a[8:11],v250 offset:2048\nds_read_b128 a[12:15],v250 offset:3072\n" \
"M v[48:51],a[88:91],v[192:195],v[48:51]\nv_cvt_pk_bf16_f32 v223,v190,v191\ns_add_u32 s94,s94,0x4000\nM v[48:51],a[120:123],v[208:211],v[48:51]\nbuffer_store_dwordx4 a[248:251],v253,s[52:55],s71 offen offset:2048 sc1\nM v[56:59],a[92:95],v[192:195],v[56:59]\nv_exp_f32 v128,v128\nM v[56:59],a[124:127],v[208:211],v[56:59]\n" \
"v_exp_f32 v129,v129\nM a[192:195],a[208:211],v[192:195],a[192:195]\nv_exp_f32 v130,v130\nM a[192:195],a[208:211],v[208:211],a[192:195]\nbuffer_store_dwordx4 a[248:251],v253,s[52:55],s71 offen offset:3072 sc1\nM v[4:7],a[64:67],v[196:199],v[4:7]\nv_exp_f32 v131,v131\nM v[4:7],a[96:99],v[212:215],v[4:7]\n" \
"ds_read_b128 a[16:19],v250 offset:4096\nds_read_b128 a[20:23],v250 offset:5120\nM v[12:15],a[68:71],v[196:199],v[12:15]\nv_exp_f32 v144,v144\nM v[12:15],a[100:103],v[212:215],v[12:15]\nv_exp_f32 v145,v145\nM v[20:23],a[72:75],v[196:199],v[20:23]\nv_exp_f32 v146,v146\n" \
"M v[20:23],a[104:107],v[212:215],v[20:23]\nbuffer_load_dwordx4 v240,s[48:51],s95 offen offset:1024 sc1 lds\nM v[28:31],a[76:79],v[196:199],v[28:31]\nv_exp_f32 v147,v147\nM v[28:31],a[108:111],v[212:215],v[28:31]\nbuffer_store_dwordx4 a[248:251],v253,s[52:55],s78 offen offset:0 sc1\nM v[36:39],a[80:83],v[196:199],v[36:39]\nv_exp_f32 v160,v160\n" \
"M v[36:39],a[112:115],v[212:215],v[36:39]\nds_read_b128 a[24:27],v250 offset:6144\nds_read_b128 a[28:31],v250 offset:7168\nM v[44:47],a[84:87],v[196:199],v[44:47]\nv_exp_f32 v161,v161\nM v[44:47],a[116:119],v[212:215],v[44:47]\nbuffer_store_dwordx4 a[248:251],v253,s[52:55],s78 offen offset:1024 sc1\nM v[52:55],a[88:91],v[196:199],v[52:55]\n" \
"v_exp_f32 v162,v162\nM v[52:55],a[120:123],v[212:215],v[52:55]\nv_exp_f32 v163,v163\nM v[60:63],a[92:95],v[196:199],v[60:63]\nv_exp_f32 v176,v176\nM v[60:63],a[124:127],v[212:215],v[60:63]\nbuffer_store_dwordx4 a[248:251],v253,s[52:55],s78 offen offset:2048 sc1\nM a[196:199],a[208:211],v[196:199],a[196:199]\n" \
"v_exp_f32 v177,v177\nM a[196:199],a[208:211],v[212:215],a[196:199]\nds_read_b128 a[32:35],v250 offset:8192\nds_read_b128 a[36:39],v250 offset:9216\nM v[64:67],a[64:67],v[200:203],v[64:67]\nv_exp_f32 v178,v178\nM v[64:67],a[96:99],v[216:219],v[64:67]\nv_exp_f32 v179,v179\n" \
"M v[72:75],a[68:71],v[200:203],v[72:75]\ns_and_b32 s94,s94,0xffff\nv_cvt_pk_bf16_f32 v192,v128,v129\nM v[72:75],a[100:103],v[216:219],v[72:75]\nbuffer_load_dwordx4 v240,s[48:51],s95 offen offset:2048 sc1 lds\nM v[80:83],a[72:75],v[200:203],v[80:83]\nv_exp_f32 v132,v132\nM v[80:83],a[104:107],v[216:219],v[80:83]\n" \
"buffer_store_dwordx4 a[248:251],v253,s[52:55],s78 offen offset:3072 sc1\nM v[88:91],a[76:79],v[200:203],v[88:91]\nv_exp_f32 v133,v133\nM v[88:91],a[108:111],v[216:219],v[88:91]\nds_read_b128 a[40:43],v250 offset:10240\nds_read_b128 a[44:47],v250 offset:11264\nM v[96:99],a[80:83],v[200:203],v[96:99]\nv_exp_f32 v134,v134\n" \
"M v[96:99],a[112:115],v[216:219],v[96:99]\nbuffer_store_dwordx4 a[248:251],v253,s[52:55],s70 offen offset:0 sc1\nM v[104:107],a[84:87],v[200:203],v[104:107]\nv_exp_f32 v135,v135\nM v[104:107],a[116:119],v[216:219],v[104:107]\nv_exp_f32 v148,v148\nM v[112:115],a[88:91],v[200:203],v[112:115]\nv_exp_f32 v149,v149\n" \
"M v[112:115],a[120:123],v[216:219],v[112:115]\nbuffer_store_dwordx4 a[248:251],v253,s[52:55],s70 offen offset:1024 sc1\nM v[120:123],a[92:95],v[200:203],v[120:123]\nv_exp_f32 v150,v150\nM v[120:123],a[124:127],v[216:219],v[120:123]\nds_read_b128 a[48:51],v250 offset:12288\nds_read_b128 a[52:55],v250 offset:13312\nM a[200:203],a[208:211],v[200:203],a[200:203]\n" \
"v_exp_f32 v151,v151\nM a[200:203],a[208:211],v[216:219],a[200:203]\nv_cvt_pk_bf16_f32 v193,v130,v131\nv_cvt_pk_bf16_f32 v194,v144,v145\nM v[68:71],a[64:67],v[204:207],v[68:71]\nv_cvt_pk_bf16_f32 v195,v146,v147\nv_cvt_pk_bf16_f32 v208,v160,v161\nM v[68:71],a[96:99],v[220:223],v[68:71]\n" \
"buffer_load_dwordx4 v240,s[48:51],s95 offen offset:3072 sc1 lds\nM v[76:79],a[68:71],v[204:207],v[76:79]\nv_cvt_pk_bf16_f32 v209,v162,v163\nv_cvt_pk_bf16_f32 v210,v176,v177\nM v[76:79],a[100:103],v[220:223],v[76:79]\nbuffer_store_dwordx4 a[248:251],v253,s[52:55],s70 offen offset:2048 sc1\nM v[84:87],a[72:75],v[204:207],v[84:87]\nv_cvt_pk_bf16_f32 v211,v178,v179\n" \
"s_add_u32 s95,s95,0x4000\nM v[84:87],a[104:107],v[220:223],v[84:87]\nds_read_b128 a[56:59],v250 offset:14336\nds_read_b128 a[60:63],v250 offset:15360\nM v[92:95],a[76:79],v[204:207],v[92:95]\nv_add_u32 v250,s94,v241\nM v[92:95],a[108:111],v[220:223],v[92:95]\nbuffer_store_dwordx4 a[248:251],v253,s[52:55],s70 offen offset:3072 sc1\n" \
"M v[100:103],a[80:83],v[204:207],v[100:103]\nM v[100:103],a[112:115],v[220:223],v[100:103]\nM v[108:111],a[84:87],v[204:207],v[108:111]\nM v[108:111],a[116:119],v[220:223],v[108:111]\nM v[116:119],a[88:91],v[204:207],v[116:119]\nM v[116:119],a[120:123],v[220:223],v[116:119]\nM v[124:127],a[92:95],v[204:207],v[124:127]\nM v[124:127],a[124:127],v[220:223],v[124:127]\n" \
"M a[204:207],a[208:211],v[204:207],a[204:207]\nM a[204:207],a[208:211],v[220:223],a[204:207]\n.Lse:\ns_waitcnt lgkmcnt(4)\nM v[128:131],a[0:3],a[128:131],v[224:227]\ns_add_u32 m0,s94,s64\nv_accvgpr_read_b32 v232,a200\nv_cvt_pk_bf16_f32 v196,v132,v133\n" \
"v_cvt_pk_bf16_f32 v197,v134,v135\nM v[128:131],a[4:7],a[132:135],v[128:131]\nds_read_b128 a[64:67],v250 offset:0\nds_read_b128 a[96:99],v250 offset:8192\nv_accvgpr_read_b32 v236,a204\nv_add_f32 v233,v232,v236\nM v[128:131],a[8:11],a[136:139],v[128:131]\nbuffer_load_dwordx4 v240,s[44:47],s95 offen offset:0 sc1 lds\n" \
"v_rcp_f32 v232,v232\nM v[128:131],a[12:15],a[140:143],v[128:131]\nv_rcp_f32 v236,v236\nv_add_f32 v233,v233,v232\nv_add_f32 v233,v233,v236\nM v[132:135],a[12:15],a[156:159],v[228:231]\nds_read_b128 a[68:71],v250 offset:1024\nds_read_b128 a[100:103],v250 offset:9216\n" \
"v_cmp_ngt_f32 vcc,s28,v233\nv_mul_f32 v64,v64,v232\nM v[132:135],a[8:11],a[152:155],v[132:135]\ns_and_b64 vcc,vcc,s[90:91]\ns_cselect_b32 s39,0x8000000,s16\ns_cselect_b32 s16,s16,0x8000000\ns_cselect_b32 s71,2,1\ns_addc_u32 s99,s99,0\n" \
"M v[132:135],a[4:7],a[148:151],v[132:135]\nv_mul_f32 v65,v65,v232\ns_add_u32 s37,s12,s67\nv_mul_f32 v66,v66,v232\nv_mul_f32 v67,v67,v232\nM v[132:135],a[0:3],a[144:147],v[132:135]\nds_read_b128 a[72:75],v250 offset:2048\nds_read_b128 a[104:107],v250 offset:10240\n" \
"v_cvt_pk_bf16_f32 v64,v64,v65\ns_add_u32 s37,s37,1048576\nM v[144:147],a[16:19],a[128:131],v[224:227]\nv_mul_f32 v72,v72,v232\nv_mul_f32 v73,v73,v232\nv_mul_f32 v74,v74,v232\nv_mul_f32 v75,v75,v232\nv_cvt_pk_bf16_f32 v65,v66,v67\n" \
"M v[144:147],a[20:23],a[132:135],v[144:147]\ns_or_b32 s37,s37,s39\nv_cvt_pk_bf16_f32 v66,v72,v73\nv_cvt_pk_bf16_f32 v67,v74,v75\nbuffer_store_dwordx4 v[64:67],v240,s[52:55],s37 offen offset:0 sc1\nM v[144:147],a[24:27],a[136:139],v[144:147]\nds_read_b128 a[76:79],v250 offset:3072\nds_read_b128 a[108:111],v250 offset:11264\n" \
"v_cvt_pk_bf16_f32 v198,v148,v149\nv_cvt_pk_bf16_f32 v199,v150,v151\nv_mul_f32 v68,v68,v236\nM v[144:147],a[28:31],a[140:143],v[144:147]\nbuffer_load_dwordx4 v240,s[44:47],s95 offen offset:1024 sc1 lds\nv_mul_f32 v69,v69,v236\nv_mul_f32 v70,v70,v236\nM v[148:151],a[28:31],a[156:159],v[228:231]\n" \
"v_mul_f32 v71,v71,v236\nv_cvt_pk_bf16_f32 v68,v68,v69\nv_mul_f32 v76,v76,v236\nv_mul_f32 v77,v77,v236\nM v[148:151],a[24:27],a[152:155],v[148:151]\nds_read_b128 a[80:83],v250 offset:4096\nds_read_b128 a[112:115],v250 offset:12288\nv_mul_f32 v78,v78,v236\n" \
"v_mul_f32 v79,v79,v236\nM v[148:151],a[20:23],a[148:151],v[148:151]\nv_cvt_pk_bf16_f32 v69,v70,v71\nv_cvt_pk_bf16_f32 v70,v76,v77\nv_cvt_pk_bf16_f32 v71,v78,v79\nv_mul_f32 v80,v80,v232\nM v[148:151],a[16:19],a[144:147],v[148:151]\nv_exp_f32 v164,v164\n" \
"v_exp_f32 v165,v165\nM v[160:163],a[32:35],a[128:131],v[224:227]\nds_read_b128 a[84:87],v250 offset:5120\nds_read_b128 a[116:119],v250 offset:13312\nv_exp_f32 v166,v166\nv_cvt_pk_bf16_f32 v212,v164,v165\nM v[160:163],a[36:39],a[132:135],v[160:163]\nbuffer_store_dwordx4 v[68:71],v240,s[52:55],s37 offen offset:1024 sc1\n" \
"v_exp_f32 v167,v167\nM v[160:163],a[40:43],a[136:139],v[160:163]\nv_cvt_pk_bf16_f32 v213,v166,v167\nv_mul_f32 v81,v81,v232\nv_mul_f32 v82,v82,v232\nv_mul_f32 v83,v83,v232\nM v[160:163],a[44:47],a[140:143],v[160:163]\nds_read_b128 a[88:91],v250 offset:6144\n" \
"ds_read_b128 a[120:123],v250 offset:14336\nv_cvt_pk_bf16_f32 v80,v80,v81\nv_mul_f32 v88,v88,v232\nM v[164:167],a[44:47],a[156:159],v[228:231]\nbuffer_load_dwordx4 v240,s[44:47],s95 offen offset:2048 sc1 lds\nv_mul_f32 v89,v89,v232\nv_mul_f32 v90,v90,v232\nM v[164:167],a[40:43],a[152:155],v[164:167]\n" \
"v_mul_f32 v91,v91,v232\nv_cvt_pk_bf16_f32 v81,v82,v83\nv_cvt_pk_bf16_f32 v82,v88,v89\nv_cvt_pk_bf16_f32 v83,v90,v91\nv_mul_f32 v84,v84,v236\nM v[164:167],a[36:39],a[148:151],v[164:167]\nds_read_b128 a[92:95],v250 offset:7168\nds_read_b128 a[124:127],v250 offset:15360\n" \
"v_exp_f32 v180,v180\nM v[164:167],a[32:35],a[144:147],v[164:167]\nv_exp_f32 v181,v181\nv_mul_f32 v85,v85,v236\ns_waitcnt lgkmcnt(4)\nM v[176:179],a[48:51],a[128:131],v[224:227]\nv_exp_f32 v182,v182\nv_exp_f32 v183,v183\n" \
"M v[176:179],a[52:55],a[132:135],v[176:179]\nbuffer_store_dwordx4 v[80:83],v240,s[52:55],s37 offen offset:2048 sc1\nv_cvt_pk_bf16_f32 v214,v180,v181\nv_cvt_pk_bf16_f32 v215,v182,v183\nM v[176:179],a[56:59],a[136:139],v[176:179]\nv_mul_f32 v86,v86,v236\nv_mul_f32 v87,v87,v236\nv_cvt_pk_bf16_f32 v84,v84,v85\n" \
"v_mul_f32 v92,v92,v236\nM v[176:179],a[60:63],a[140:143],v[176:179]\nv_mul_f32 v93,v93,v236\nv_mul_f32 v94,v94,v236\nv_mul_f32 v95,v95,v236\nv_cvt_pk_bf16_f32 v85,v86,v87\nv_cvt_pk_bf16_f32 v86,v92,v93\nM v[180:183],a[60:63],a[156:159],v[228:231]\n" \
"v_cvt_pk_bf16_f32 v87,v94,v95\nv_xor_b32 v250,0x18000,v250\nv_mul_f32 v96,v96,v232\nv_mul_f32 v97,v97,v232\nM v[180:183],a[56:59],a[152:155],v[180:183]\nbuffer_load_dwordx4 v240,s[44:47],s95 offen offset:3072 sc1 lds\ns_add_u32 m0,m0,0x10000\nv_mul_f32 v98,v98,v232\n" \
"M v[180:183],a[52:55],a[148:151],v[180:183]\nv_mul_f32 v99,v99,v232\nv_cvt_pk_bf16_f32 v96,v96,v97\nv_mul_f32 v104,v104,v232\nv_mul_f32 v105,v105,v232\nM v[180:183],a[48:51],a[144:147],v[180:183]\nv_mul_f32 v106,v106,v232\nv_mul_f32 v107,v107,v232\n" \
"v_cvt_pk_bf16_f32 v97,v98,v99\ns_add_u32 s38,s37,4096\nM v[0:3],a[64:67],v[192:195],v[0:3]\nv_cvt_pk_bf16_f32 v98,v104,v105\nv_cvt_pk_bf16_f32 v99,v106,v107\nv_mul_f32 v100,v100,v236\nv_mul_f32 v101,v101,v236\nv_mul_f32 v102,v102,v236\n" \
"M v[0:3],a[96:99],v[208:211],v[0:3]\nbuffer_store_dwordx4 v[84:87],v240,s[52:55],s37 offen offset:3072 sc1\nv_mul_f32 v103,v103,v236\nv_cvt_pk_bf16_f32 v100,v100,v101\nM v[8:11],a[68:71],v[192:195],v[8:11]\ns_waitcnt vmcnt(16) lgkmcnt(0)\ns_barrier\nv_mul_f32 v108,v108,v236\n" \
"v_mul_f32 v109,v109,v236\nM v[8:11],a[100:103],v[208:211],v[8:11]\nds_read_b128 a[0:3],v250 offset:0\nds_read_b128 a[4:7],v250 offset:1024\nv_mul_f32 v110,v110,v236\nv_mul_f32 v111,v111,v236\nM v[16:19],a[72:75],v[192:195],v[16:19]\nv_cvt_pk_bf16_f32 v101,v102,v103\n" \
"v_cvt_pk_bf16_f32 v102,v108,v109\nv_cvt_pk_bf16_f32 v103,v110,v111\nv_mul_f32 v112,v112,v232\nM v[16:19],a[104:107],v[208:211],v[16:19]\nbuffer_load_dwordx4 v240,s[48:51],s95 offen offset:0 sc1 lds\nv_mul_f32 v113,v113,v232\nv_mul_f32 v114,v114,v232\nv_mul_f32 v115,v115,v232\n" \
"M v[24:27],a[76:79],v[192:195],v[24:27]\nv_cvt_pk_bf16_f32 v112,v112,v113\nv_mul_f32 v120,v120,v232\nv_mul_f32 v121,v121,v232\nv_mul_f32 v122,v122,v232\nM v[24:27],a[108:111],v[208:211],v[24:27]\nds_read_b128 a[8:11],v250 offset:2048\nds_read_b128 a[12:15],v250 offset:3072\n" \
"v_mul_f32 v123,v123,v232\nv_cvt_pk_bf16_f32 v113,v114,v115\nM v[32:35],a[80:83],v[192:195],v[32:35]\nv_cvt_pk_bf16_f32 v114,v120,v121\nv_cvt_pk_bf16_f32 v115,v122,v123\nv_mul_f32 v116,v116,v236\nv_mul_f32 v117,v117,v236\nM v[32:35],a[112:115],v[208:211],v[32:35]\n" \
"buffer_store_dwordx4 v[96:99],v240,s[52:55],s38 offen offset:0 sc1\nv_mul_f32 v118,v118,v236\nv_mul_f32 v119,v119,v236\nM v[40:43],a[84:87],v[192:195],v[40:43]\nv_cvt_pk_bf16_f32 v116,v116,v117\nv_mul_f32 v124,v124,v236\nv_mul_f32 v125,v125,v236\nv_mul_f32 v126,v126,v236\n" \
"M v[40:43],a[116:119],v[208:211],v[40:43]\nds_read_b128 a[16:19],v250 offset:4096\nds_read_b128 a[20:23],v250 offset:5120\nv_mul_f32 v127,v127,v236\nv_cvt_pk_bf16_f32 v117,v118,v119\nv_cvt_pk_bf16_f32 v118,v124,v125\nM v[48:51],a[88:91],v[192:195],v[48:51]\nv_cvt_pk_bf16_f32 v119,v126,v127\n" \
"v_exp_f32 v128,v128\ns_add_u32 s94,s94,0x4000\nM v[48:51],a[120:123],v[208:211],v[48:51]\nbuffer_load_dwordx4 v240,s[48:51],s95 offen offset:1024 sc1 lds\nv_exp_f32 v129,v129\nM v[56:59],a[92:95],v[192:195],v[56:59]\nv_exp_f32 v130,v130\nv_exp_f32 v131,v131\n" \
"M v[56:59],a[124:127],v[208:211],v[56:59]\nds_read_b128 a[24:27],v250 offset:6144\nds_read_b128 a[28:31],v250 offset:7168\nv_exp_f32 v144,v144\nM a[192:195],a[208:211],v[192:195],a[192:195]\nv_exp_f32 v145,v145\nv_exp_f32 v146,v146\ns_lshl2_add_u32 s77,s61,s79\n" \
"M a[192:195],a[208:211],v[208:211],a[192:195]\nbuffer_store_dwordx4 v[100:103],v240,s[52:55],s38 offen offset:1024 sc1\nv_exp_f32 v147,v147\nM v[4:7],a[64:67],v[196:199],v[4:7]\nv_exp_f32 v160,v160\nv_exp_f32 v161,v161\nM v[4:7],a[96:99],v[212:215],v[4:7]\nds_read_b128 a[32:35],v250 offset:8192\n" \
"ds_read_b128 a[36:39],v250 offset:9216\nv_exp_f32 v162,v162\nM v[12:15],a[68:71],v[196:199],v[12:15]\nv_exp_f32 v163,v163\nv_exp_f32 v176,v176\nM v[12:15],a[100:103],v[212:215],v[12:15]\nbuffer_load_dwordx4 v240,s[48:51],s95 offen offset:2048 sc1 lds\nv_exp_f32 v177,v177\n" \
"M v[20:23],a[72:75],v[196:199],v[20:23]\nv_exp_f32 v178,v178\nv_exp_f32 v179,v179\ns_and_b32 s94,s94,0xffff\nM v[20:23],a[104:107],v[212:215],v[20:23]\nds_read_b128 a[40:43],v250 offset:10240\nds_read_b128 a[44:47],v250 offset:11264\ns_add_u32 s77,s77,4096\n" \
"v_mov_b32 v233,s71\nM v[28:31],a[76:79],v[196:199],v[28:31]\nv_exp_f32 v132,v132\nv_exp_f32 v133,v133\nM v[28:31],a[108:111],v[212:215],v[28:31]\nbuffer_store_dwordx4 v[112:115],v240,s[52:55],s38 offen offset:2048 sc1\nv_exp_f32 v134,v134\nM v[36:39],a[80:83],v[196:199],v[36:39]\n" \
"v_exp_f32 v135,v135\nv_exp_f32 v148,v148\nM v[36:39],a[112:115],v[212:215],v[36:39]\nds_read_b128 a[48:51],v250 offset:12288\nds_read_b128 a[52:55],v250 offset:13312\nv_exp_f32 v149,v149\nv_cvt_pk_bf16_f32 v192,v128,v129\nM v[44:47],a[84:87],v[196:199],v[44:47]\n" \
"v_exp_f32 v150,v150\nv_exp_f32 v151,v151\nM v[44:47],a[116:119],v[212:215],v[44:47]\nbuffer_load_dwordx4 v240,s[48:51],s95 offen offset:3072 sc1 lds\nv_cvt_pk_bf16_f32 v193,v130,v131\nv_cvt_pk_bf16_f32 v194,v144,v145\nM v[52:55],a[88:91],v[196:199],v[52:55]\nv_cvt_pk_bf16_f32 v195,v146,v147\n" \
"v_cvt_pk_bf16_f32 v208,v160,v161\nv_cvt_pk_bf16_f32 v209,v162,v163\nv_cvt_pk_bf16_f32 v210,v176,v177\nM v[52:55],a[120:123],v[212:215],v[52:55]\nds_read_b128 a[56:59],v250 offset:14336\nds_read_b128 a[60:63],v250 offset:15360\nv_cvt_pk_bf16_f32 v211,v178,v179\nv_add_u32 v250,s94,v241\n" \
"M v[60:63],a[92:95],v[196:199],v[60:63]\ns_add_u32 s95,s95,0x4000\nM v[60:63],a[124:127],v[212:215],v[60:63]\nbuffer_store_dwordx4 v[116:119],v240,s[52:55],s38 offen offset:3072 sc1\nM a[196:199],a[208:211],v[196:199],a[196:199]\nM a[196:199],a[208:211],v[212:215],a[196:199]\ns_cmp_ge_u32 s95,s15\ns_cbranch_scc1 .Lsx\n" \
".Lsingle:\ns_waitcnt lgkmcnt(4)\nM v[128:131],a[0:3],a[128:131],v[224:227]\ns_xor_b32 m0,m0,0x14000\nv_cvt_pk_bf16_f32 v196,v132,v133\nM v[128:131],a[4:7],a[132:135],v[128:131]\nds_read_b128 a[64:67],v250 offset:0\nds_read_b128 a[96:99],v250 offset:8192\n" \
"M v[128:131],a[8:11],a[136:139],v[128:131]\nbuffer_load_dwordx4 v240,s[44:47],s95 offen offset:0 sc1 lds\nM v[128:131],a[12:15],a[140:143],v[128:131]\nv_cvt_pk_bf16_f32 v197,v134,v135\nv_cvt_pk_bf16_f32 v198,v148,v149\nM v[132:135],a[12:15],a[156:159],v[228:231]\nds_read_b128 a[68:71],v250 offset:1024\nds_read_b128 a[100:103],v250 offset:9216\n" \
"M v[132:135],a[8:11],a[152:155],v[132:135]\nv_exp_f32 v164,v164\nM v[132:135],a[4:7],a[148:151],v[132:135]\nv_exp_f32 v165,v165\nM v[132:135],a[0:3],a[144:147],v[132:135]\nds_read_b128 a[72:75],v250 offset:2048\nds_read_b128 a[104:107],v250 offset:10240\nM v[144:147],a[16:19],a[128:131],v[224:227]\n" \
"v_cvt_pk_bf16_f32 v199,v150,v151\nv_cvt_pk_bf16_f32 v212,v164,v165\nM v[144:147],a[20:23],a[132:135],v[144:147]\nv_exp_f32 v166,v166\nM v[144:147],a[24:27],a[136:139],v[144:147]\nds_read_b128 a[76:79],v250 offset:3072\nds_read_b128 a[108:111],v250 offset:11264\nM v[144:147],a[28:31],a[140:143],v[144:147]\n" \
"buffer_load_dwordx4 v240,s[44:47],s95 offen offset:1024 sc1 lds\nM v[148:151],a[28:31],a[156:159],v[228:231]\nv_exp_f32 v167,v167\nM v[148:151],a[24:27],a[152:155],v[148:151]\nds_read_b128 a[80:83],v250 offset:4096\nds_read_b128 a[112:115],v250 offset:12288\nM v[148:151],a[20:23],a[148:151],v[148:151]\nv_exp_f32 v180,v180\n" \
"M v[148:151],a[16:19],a[144:147],v[148:151]\nv_exp_f32 v181,v181\nM v[160:163],a[32:35],a[128:131],v[224:227]\nds_read_b128 a[84:87],v250 offset:5120\nds_read_b128 a[116:119],v250 offset:13312\nM v[160:163],a[36:39],a[132:135],v[160:163]\nv_cvt_pk_bf16_f32 v213,v166,v167\nv_cvt_pk_bf16_f32 v214,v180,v181\n" \
"M v[160:163],a[40:43],a[136:139],v[160:163]\nv_exp_f32 v182,v182\nM v[160:163],a[44:47],a[140:143],v[160:163]\nds_read_b128 a[88:91],v250 offset:6144\nds_read_b128 a[120:123],v250 offset:14336\nM v[164:167],a[44:47],a[156:159],v[228:231]\nbuffer_load_dwordx4 v240,s[44:47],s95 offen offset:2048 sc1 lds\nM v[164:167],a[40:43],a[152:155],v[164:167]\n" \
"v_exp_f32 v183,v183\nM v[164:167],a[36:39],a[148:151],v[164:167]\nds_read_b128 a[92:95],v250 offset:7168\nds_read_b128 a[124:127],v250 offset:15360\nM v[164:167],a[32:35],a[144:147],v[164:167]\nv_cvt_pk_bf16_f32 v215,v182,v183\ns_waitcnt lgkmcnt(7)\nM v[176:179],a[48:51],a[128:131],v[224:227]\n" \
"v_exp_f32 v128,v128\nM v[176:179],a[52:55],a[132:135],v[176:179]\nv_exp_f32 v129,v129\nM v[176:179],a[56:59],a[136:139],v[176:179]\nv_exp_f32 v130,v130\nM v[176:179],a[60:63],a[140:143],v[176:179]\nv_exp_f32 v131,v131\nM v[180:183],a[60:63],a[156:159],v[228:231]\n" \
"v_exp_f32 v132,v132\nM v[180:183],a[56:59],a[152:155],v[180:183]\nbuffer_load_dwordx4 v240,s[44:47],s95 offen offset:3072 sc1 lds\nM v[180:183],a[52:55],a[148:151],v[180:183]\nv_xor_b32 v250,0x18000,v250\ns_xor_b32 m0,m0,0x10000\nM v[180:183],a[48:51],a[144:147],v[180:183]\nv_exp_f32 v133,v133\n" \
"M v[0:3],a[64:67],v[192:195],v[0:3]\nv_exp_f32 v134,v134\nM v[0:3],a[96:99],v[208:211],v[0:3]\nv_exp_f32 v135,v135\nM v[8:11],a[68:71],v[192:195],v[8:11]\ns_waitcnt vmcnt(16) lgkmcnt(0)\ns_barrier\nM v[8:11],a[100:103],v[208:211],v[8:11]\n" \
"ds_read_b128 a[0:3],v250 offset:0\nds_read_b128 a[4:7],v250 offset:1024\nM v[16:19],a[72:75],v[192:195],v[16:19]\nv_exp_f32 v144,v144\nM v[16:19],a[104:107],v[208:211],v[16:19]\nbuffer_load_dwordx4 v240,s[48:51],s95 offen offset:0 sc1 lds\nM v[24:27],a[76:79],v[192:195],v[24:27]\nv_exp_f32 v145,v145\n" \
"M v[24:27],a[108:111],v[208:211],v[24:27]\nds_read_b128 a[8:11],v250 offset:2048\nds_read_b128 a[12:15],v250 offset:3072\nM v[32:35],a[80:83],v[192:195],v[32:35]\nv_exp_f32 v146,v146\nM v[32:35],a[112:115],v[208:211],v[32:35]\nv_exp_f32 v147,v147\nM v[40:43],a[84:87],v[192:195],v[40:43]\n" \
"v_exp_f32 v148,v148\nM v[40:43],a[116:119],v[208:211],v[40:43]\nds_read_b128 a[16:19],v250 offset:4096\nds_read_b128 a[20:23],v250 offset:5120\nM v[48:51],a[88:91],v[192:195],v[48:51]\nv_exp_f32 v149,v149\nM v[48:51],a[120:123],v[208:211],v[48:51]\nbuffer_load_dwordx4 v240,s[48:51],s95 offen offset:1024 sc1 lds\n" \
"M v[56:59],a[92:95],v[192:195],v[56:59]\nv_exp_f32 v150,v150\nM v[56:59],a[124:127],v[208:211],v[56:59]\nds_read_b128 a[24:27],v250 offset:6144\nds_read_b128 a[28:31],v250 offset:7168\nM a[192:195],a[208:211],v[192:195],a[192:195]\nv_cvt_pk_bf16_f32 v192,v128,v129\nv_cvt_pk_bf16_f32 v193,v130,v131\n" \
"M a[192:195],a[208:211],v[208:211],a[192:195]\nv_cvt_pk_bf16_f32 v194,v144,v145\nv_cvt_pk_bf16_f32 v195,v146,v147\nM v[4:7],a[64:67],v[196:199],v[4:7]\nv_exp_f32 v151,v151\nM v[4:7],a[96:99],v[212:215],v[4:7]\nds_read_b128 a[32:35],v250 offset:8192\nds_read_b128 a[36:39],v250 offset:9216\n" \
"M v[12:15],a[68:71],v[196:199],v[12:15]\nv_exp_f32 v160,v160\nM v[12:15],a[100:103],v[212:215],v[12:15]\nbuffer_load_dwordx4 v240,s[48:51],s95 offen offset:2048 sc1 lds\nM v[20:23],a[72:75],v[196:199],v[20:23]\nv_exp_f32 v161,v161\nM v[20:23],a[104:107],v[212:215],v[20:23]\nds_read_b128 a[40:43],v250 offset:10240\n" \
"ds_read_b128 a[44:47],v250 offset:11264\nM v[28:31],a[76:79],v[196:199],v[28:31]\nv_exp_f32 v162,v162\nM v[28:31],a[108:111],v[212:215],v[28:31]\nv_exp_f32 v163,v163\nM v[36:39],a[80:83],v[196:199],v[36:39]\nv_cvt_pk_bf16_f32 v208,v160,v161\nv_cvt_pk_bf16_f32 v209,v162,v163\n" \
"M v[36:39],a[112:115],v[212:215],v[36:39]\nds_read_b128 a[48:51],v250 offset:12288\nds_read_b128 a[52:55],v250 offset:13312\nM v[44:47],a[84:87],v[196:199],v[44:47]\nv_exp_f32 v164,v164\nM v[44:47],a[116:119],v[212:215],v[44:47]\nbuffer_load_dwordx4 v240,s[48:51],s95 offen offset:3072 sc1 lds\nM v[52:55],a[88:91],v[196:199],v[52:55]\n" \
"s_add_u32 s95,s95,0x4000\ns_xor_b32 m0,m0,0x1c000\nM v[52:55],a[120:123],v[212:215],v[52:55]\nds_read_b128 a[56:59],v250 offset:14336\nds_read_b128 a[60:63],v250 offset:15360\nM v[60:63],a[92:95],v[196:199],v[60:63]\nv_exp_f32 v165,v165\nM v[60:63],a[124:127],v[212:215],v[60:63]\n" \
"v_exp_f32 v166,v166\nM a[196:199],a[208:211],v[196:199],a[196:199]\nv_xor_b32 v250,0x14000,v250\nv_cvt_pk_bf16_f32 v196,v132,v133\nM a[196:199],a[208:211],v[212:215],a[196:199]\nv_cvt_pk_bf16_f32 v197,v134,v135\ns_waitcnt lgkmcnt(4)\nM v[128:131],a[0:3],a[128:131],v[224:227]\n" \
"v_cvt_pk_bf16_f32 v198,v148,v149\nv_cvt_pk_bf16_f32 v199,v150,v151\nM v[128:131],a[4:7],a[132:135],v[128:131]\nds_read_b128 a[64:67],v250 offset:0\nds_read_b128 a[96:99],v250 offset:8192\nM v[128:131],a[8:11],a[136:139],v[128:131]\nbuffer_load_dwordx4 v240,s[44:47],s95 offen offset:0 sc1 lds\nM v[128:131],a[12:15],a[140:143],v[128:131]\n" \
"v_exp_f32 v167,v167\nM v[132:135],a[12:15],a[156:159],v[228:231]\nds_read_b128 a[68:71],v250 offset:1024\nds_read_b128 a[100:103],v250 offset:9216\nM v[132:135],a[8:11],a[152:155],v[132:135]\nv_cvt_pk_bf16_f32 v212,v164,v165\nv_cvt_pk_bf16_f32 v213,v166,v167\nM v[132:135],a[4:7],a[148:151],v[132:135]\n" \
"v_exp_f32 v176,v176\nM v[132:135],a[0:3],a[144:147],v[132:135]\nds_read_b128 a[72:75],v250 offset:2048\nds_read_b128 a[104:107],v250 offset:10240\nM v[144:147],a[16:19],a[128:131],v[224:227]\nv_exp_f32 v177,v177\nM v[144:147],a[20:23],a[132:135],v[144:147]\nv_exp_f32 v178,v178\n" \
"M v[144:147],a[24:27],a[136:139],v[144:147]\nds_read_b128 a[76:79],v250 offset:3072\nds_read_b128 a[108:111],v250 offset:11264\nM v[144:147],a[28:31],a[140:143],v[144:147]\nbuffer_load_dwordx4 v240,s[44:47],s95 offen offset:1024 sc1 lds\nM v[148:151],a[28:31],a[156:159],v[228:231]\nv_exp_f32 v179,v179\nM v[148:151],a[24:27],a[152:155],v[148:151]\n" \
"ds_read_b128 a[80:83],v250 offset:4096\nds_read_b128 a[112:115],v250 offset:12288\nM v[148:151],a[20:23],a[148:151],v[148:151]\nv_cvt_pk_bf16_f32 v210,v176,v177\nv_cvt_pk_bf16_f32 v211,v178,v179\nM v[148:151],a[16:19],a[144:147],v[148:151]\nv_exp_f32 v180,v180\nM v[160:163],a[32:35],a[128:131],v[224:227]\n" \
"ds_read_b128 a[84:87],v250 offset:5120\nds_read_b128 a[116:119],v250 offset:13312\nM v[160:163],a[36:39],a[132:135],v[160:163]\nv_exp_f32 v181,v181\nM v[160:163],a[40:43],a[136:139],v[160:163]\nv_exp_f32 v182,v182\nM v[160:163],a[44:47],a[140:143],v[160:163]\nds_read_b128 a[88:91],v250 offset:6144\n" \
"ds_read_b128 a[120:123],v250 offset:14336\nM v[164:167],a[44:47],a[156:159],v[228:231]\nbuffer_load_dwordx4 v240,s[44:47],s95 offen offset:2048 sc1 lds\nM v[164:167],a[40:43],a[152:155],v[164:167]\nv_exp_f32 v183,v183\nM v[164:167],a[36:39],a[148:151],v[164:167]\nds_read_b128 a[92:95],v250 offset:7168\nds_read_b128 a[124:127],v250 offset:15360\n" \
"M v[164:167],a[32:35],a[144:147],v[164:167]\nv_cvt_pk_bf16_f32 v214,v180,v181\ns_waitcnt lgkmcnt(7)\nM v[176:179],a[48:51],a[128:131],v[224:227]\nv_cvt_pk_bf16_f32 v215,v182,v183\nv_xor_b32 v250,0x18000,v250\nM v[176:179],a[52:55],a[132:135],v[176:179]\nv_exp_f32 v128,v128\n" \
"M v[176:179],a[56:59],a[136:139],v[176:179]\nv_exp_f32 v129,v129\nM v[176:179],a[60:63],a[140:143],v[176:179]\nv_exp_f32 v130,v130\nM v[180:183],a[60:63],a[156:159],v[228:231]\nv_exp_f32 v131,v131\nM v[180:183],a[56:59],a[152:155],v[180:183]\nbuffer_load_dwordx4 v240,s[44:47],s95 offen offset:3072 sc1 lds\n" \
"M v[180:183],a[52:55],a[148:151],v[180:183]\nv_exp_f32 v144,v144\nM v[180:183],a[48:51],a[144:147],v[180:183]\nv_exp_f32 v145,v145\nM v[0:3],a[64:67],v[192:195],v[0:3]\nv_exp_f32 v146,v146\nM v[0:3],a[96:99],v[208:211],v[0:3]\nv_exp_f32 v147,v147\n" \
"M v[8:11],a[68:71],v[192:195],v[8:11]\ns_waitcnt vmcnt(16) lgkmcnt(0)\ns_barrier\nM v[8:11],a[100:103],v[208:211],v[8:11]\nds_read_b128 a[0:3],v250 offset:0\nds_read_b128 a[4:7],v250 offset:1024\nM v[16:19],a[72:75],v[192:195],v[16:19]\ns_xor_b32 m0,m0,0x10000\n" \
"M v[16:19],a[104:107],v[208:211],v[16:19]\nbuffer_load_dwordx4 v240,s[48:51],s95 offen offset:0 sc1 lds\nM v[24:27],a[76:79],v[192:195],v[24:27]\nv_exp_f32 v160,v160\nM v[24:27],a[108:111],v[208:211],v[24:27]\nds_read_b128 a[8:11],v250 offset:2048\nds_read_b128 a[12:15],v250 offset:3072\nM v[32:35],a[80:83],v[192:195],v[32:35]\n" \
"v_exp_f32 v161,v161\nM v[32:35],a[112:115],v[208:211],v[32:35]\nv_exp_f32 v162,v162\nM v[40:43],a[84:87],v[192:195],v[40:43]\nv_exp_f32 v163,v163\nM v[40:43],a[116:119],v[208:211],v[40:43]\nds_read_b128 a[16:19],v250 offset:4096\nds_read_b128 a[20:23],v250 offset:5120\n" \
"M v[48:51],a[88:91],v[192:195],v[48:51]\nv_exp_f32 v176,v176\nM v[48:51],a[120:123],v[208:211],v[48:51]\nbuffer_load_dwordx4 v240,s[48:51],s95 offen offset:1024 sc1 lds\nM v[56:59],a[92:95],v[192:195],v[56:59]\nv_exp_f32 v177,v177\nM v[56:59],a[124:127],v[208:211],v[56:59]\nds_read_b128 a[24:27],v250 offset:6144\n" \
"ds_read_b128 a[28:31],v250 offset:7168\nM a[192:195],a[208:211],v[192:195],a[192:195]\nv_exp_f32 v178,v178\nM a[192:195],a[208:211],v[208:211],a[192:195]\nv_exp_f32 v179,v179\nM v[4:7],a[64:67],v[196:199],v[4:7]\nv_exp_f32 v132,v132\nM v[4:7],a[96:99],v[212:215],v[4:7]\n" \
"ds_read_b128 a[32:35],v250 offset:8192\nds_read_b128 a[36:39],v250 offset:9216\nM v[12:15],a[68:71],v[196:199],v[12:15]\nv_exp_f32 v133,v133\nM v[12:15],a[100:103],v[212:215],v[12:15]\nbuffer_load_dwordx4 v240,s[48:51],s95 offen offset:2048 sc1 lds\nM v[20:23],a[72:75],v[196:199],v[20:23]\nv_exp_f32 v134,v134\n" \
"M v[20:23],a[104:107],v[212:215],v[20:23]\nds_read_b128 a[40:43],v250 offset:10240\nds_read_b128 a[44:47],v250 offset:11264\nM v[28:31],a[76:79],v[196:199],v[28:31]\nv_exp_f32 v135,v135\nM v[28:31],a[108:111],v[212:215],v[28:31]\nv_exp_f32 v148,v148\nM v[36:39],a[80:83],v[196:199],v[36:39]\n" \
"v_exp_f32 v149,v149\nM v[36:39],a[112:115],v[212:215],v[36:39]\nds_read_b128 a[48:51],v250 offset:12288\nds_read_b128 a[52:55],v250 offset:13312\nM v[44:47],a[84:87],v[196:199],v[44:47]\nv_exp_f32 v150,v150\nM v[44:47],a[116:119],v[212:215],v[44:47]\nbuffer_load_dwordx4 v240,s[48:51],s95 offen offset:3072 sc1 lds\n" \
"M v[52:55],a[88:91],v[196:199],v[52:55]\nv_exp_f32 v151,v151\nM v[52:55],a[120:123],v[212:215],v[52:55]\nds_read_b128 a[56:59],v250 offset:14336\nds_read_b128 a[60:63],v250 offset:15360\nM v[60:63],a[92:95],v[196:199],v[60:63]\nv_cvt_pk_bf16_f32 v192,v128,v129\nv_cvt_pk_bf16_f32 v193,v130,v131\n" \
"M v[60:63],a[124:127],v[212:215],v[60:63]\nv_cvt_pk_bf16_f32 v194,v144,v145\nv_cvt_pk_bf16_f32 v195,v146,v147\nM a[196:199],a[208:211],v[196:199],a[196:199]\nv_cvt_pk_bf16_f32 v208,v160,v161\nv_cvt_pk_bf16_f32 v209,v162,v163\nv_cvt_pk_bf16_f32 v210,v176,v177\nv_cvt_pk_bf16_f32 v211,v178,v179\n" \
"v_xor_b32 v250,0x1c000,v250\ns_add_u32 s95,s95,0x4000\nM a[196:199],a[208:211],v[212:215],a[196:199]\ns_cmp_lg_u32 s95,s15\ns_cbranch_scc1 .Lsingle\ns_and_b32 s94,m0,0x8000\ns_or_b32 s94,s94,0x4000\n.Lsx:\n" \
".Lsw:\ns_waitcnt lgkmcnt(4)\nM v[128:131],a[0:3],a[128:131],v[224:227]\ns_lshl4_add_u32 s69,s12,s68\ns_add_u32 m0,s64,131072\nM v[128:131],a[4:7],a[132:135],v[128:131]\nds_read_b128 a[64:67],v250 offset:0\nds_read_b128 a[96:99],v250 offset:8192\n" \
"v_cvt_pk_bf16_f32 v196,v132,v133\nM v[128:131],a[8:11],a[136:139],v[128:131]\nbuffer_load_dwordx4 v243,s[40:43],s69 offen offset:0 lds\nM v[128:131],a[12:15],a[140:143],v[128:131]\nv_cvt_pk_bf16_f32 v197,v134,v135\nv_cvt_pk_bf16_f32 v198,v148,v149\nv_cvt_pk_bf16_f32 v199,v150,v151\nM v[132:135],a[12:15],a[156:159],v[228:231]\n" \
"ds_read_b128 a[68:71],v250 offset:1024\nds_read_b128 a[100:103],v250 offset:9216\nM v[132:135],a[8:11],a[152:155],v[132:135]\nbuffer_load_dwordx4 v244,s[40:43],s69 offen offset:1024 lds\ns_add_u32 s94,s94,0x4000\nM v[132:135],a[4:7],a[148:151],v[132:135]\nv_exp_f32 v164,v164\nM v[132:135],a[0:3],a[144:147],v[132:135]\n" \
"ds_read_b128 a[72:75],v250 offset:2048\nds_read_b128 a[104:107],v250 offset:10240\ns_and_b32 s94,s94,0xffff\nM v[144:147],a[16:19],a[128:131],v[224:227]\nbuffer_load_dwordx4 v245,s[40:43],s69 offen offset:2048 lds\nM v[144:147],a[20:23],a[132:135],v[144:147]\nv_exp_f32 v165,v165\ns_add_u32 s95,s95,0x4000\n" \
"M v[144:147],a[24:27],a[136:139],v[144:147]\nds_read_b128 a[76:79],v250 offset:3072\nds_read_b128 a[108:111],v250 offset:11264\nM v[144:147],a[28:31],a[140:143],v[144:147]\nbuffer_load_dwordx4 v246,s[40:43],s69 offen offset:3072 lds\ns_lshl4_add_u32 s69,s12,s68\nM v[148:151],a[28:31],a[156:159],v[228:231]\ns_add_u32 s69,s69,0x10000\n" \
"s_add_u32 m0,s64,147456\nM v[148:151],a[24:27],a[152:155],v[148:151]\nds_read_b128 a[80:83],v250 offset:4096\nds_read_b128 a[112:115],v250 offset:12288\nv_cvt_pk_bf16_f32 v212,v164,v165\nM v[148:151],a[20:23],a[148:151],v[148:151]\nv_exp_f32 v166,v166\nM v[148:151],a[16:19],a[144:147],v[148:151]\n" \
"v_exp_f32 v167,v167\nM v[160:163],a[32:35],a[128:131],v[224:227]\nds_read_b128 a[84:87],v250 offset:5120\nds_read_b128 a[116:119],v250 offset:13312\nv_cvt_pk_bf16_f32 v213,v166,v167\nM v[160:163],a[36:39],a[132:135],v[160:163]\nbuffer_load_dwordx4 v243,s[40:43],s69 offen offset:0 lds\nM v[160:163],a[40:43],a[136:139],v[160:163]\n" \
"v_exp_f32 v180,v180\nM v[160:163],a[44:47],a[140:143],v[160:163]\nds_read_b128 a[88:91],v250 offset:6144\nds_read_b128 a[120:123],v250 offset:14336\nM v[164:167],a[44:47],a[156:159],v[228:231]\nv_exp_f32 v181,v181\nM v[164:167],a[40:43],a[152:155],v[164:167]\nbuffer_load_dwordx4 v244,s[40:43],s69 offen offset:1024 lds\n" \
"M v[164:167],a[36:39],a[148:151],v[164:167]\nds_read_b128 a[92:95],v250 offset:7168\nds_read_b128 a[124:127],v250 offset:15360\nv_cvt_pk_bf16_f32 v214,v180,v181\nM v[164:167],a[32:35],a[144:147],v[164:167]\nv_xor_b32 v250,0x18000,v250\ns_waitcnt lgkmcnt(6)\nM v[176:179],a[48:51],a[128:131],v[224:227]\n" \
"s_waitcnt vmcnt(6)\nbuffer_store_dword v233,off,s[52:55],s77 sc1\nM v[176:179],a[52:55],a[132:135],v[176:179]\nbuffer_load_dwordx4 v245,s[40:43],s69 offen offset:2048 lds\nM v[176:179],a[56:59],a[136:139],v[176:179]\nv_exp_f32 v182,v182\nv_exp_f32 v183,v183\nM v[176:179],a[60:63],a[140:143],v[176:179]\n" \
"v_cvt_pk_bf16_f32 v215,v182,v183\nM v[180:183],a[60:63],a[156:159],v[228:231]\nv_exp_f32 v128,v128\nM v[180:183],a[56:59],a[152:155],v[180:183]\nbuffer_load_dwordx4 v246,s[40:43],s69 offen offset:3072 lds\nM v[180:183],a[52:55],a[148:151],v[180:183]\nv_exp_f32 v129,v129\nM v[180:183],a[48:51],a[144:147],v[180:183]\n" \
"s_lshl4_add_u32 s69,s10,s68\nv_exp_f32 v130,v130\nM v[0:3],a[64:67],v[192:195],v[0:3]\nv_exp_f32 v131,v131\nM v[0:3],a[96:99],v[208:211],v[0:3]\ns_add_u32 m0,s64,0\nv_exp_f32 v144,v144\nM v[8:11],a[68:71],v[192:195],v[8:11]\n" \
"s_waitcnt vmcnt(16) lgkmcnt(0)\ns_barrier\nM v[8:11],a[100:103],v[208:211],v[8:11]\nds_read_b128 a[0:3],v250 offset:0\nds_read_b128 a[4:7],v250 offset:1024\nM v[16:19],a[72:75],v[192:195],v[16:19]\nv_exp_f32 v145,v145\nM v[16:19],a[104:107],v[208:211],v[16:19]\n" \
"buffer_load_dwordx4 v243,s[40:43],s69 offen offset:0 lds\nM v[24:27],a[76:79],v[192:195],v[24:27]\nv_exp_f32 v146,v146\nM v[24:27],a[108:111],v[208:211],v[24:27]\nds_read_b128 a[8:11],v250 offset:2048\nds_read_b128 a[12:15],v250 offset:3072\nM v[32:35],a[80:83],v[192:195],v[32:35]\nv_exp_f32 v147,v147\n" \
"M v[32:35],a[112:115],v[208:211],v[32:35]\nbuffer_load_dwordx4 v244,s[40:43],s69 offen offset:1024 lds\nM v[40:43],a[84:87],v[192:195],v[40:43]\nv_exp_f32 v160,v160\nM v[40:43],a[116:119],v[208:211],v[40:43]\nds_read_b128 a[16:19],v250 offset:4096\nds_read_b128 a[20:23],v250 offset:5120\nM v[48:51],a[88:91],v[192:195],v[48:51]\n" \
"v_exp_f32 v161,v161\nM v[48:51],a[120:123],v[208:211],v[48:51]\nbuffer_load_dwordx4 v245,s[40:43],s69 offen offset:2048 lds\nM v[56:59],a[92:95],v[192:195],v[56:59]\nv_exp_f32 v162,v162\nM v[56:59],a[124:127],v[208:211],v[56:59]\nds_read_b128 a[24:27],v250 offset:6144\nds_read_b128 a[28:31],v250 offset:7168\n" \
"M a[192:195],a[208:211],v[192:195],a[192:195]\nv_exp_f32 v163,v163\nv_cvt_pk_bf16_f32 v192,v128,v129\nM a[192:195],a[208:211],v[208:211],a[192:195]\nbuffer_load_dwordx4 v246,s[40:43],s69 offen offset:3072 lds\nM v[4:7],a[64:67],v[196:199],v[4:7]\ns_lshl4_add_u32 s69,s10,s68\ns_add_u32 s69,s69,0x10000\n" \
"s_add_u32 m0,s64,32768\nM v[4:7],a[96:99],v[212:215],v[4:7]\nds_read_b128 a[32:35],v250 offset:8192\nds_read_b128 a[36:39],v250 offset:9216\nM v[12:15],a[68:71],v[196:199],v[12:15]\nv_exp_f32 v176,v176\nv_cvt_pk_bf16_f32 v193,v130,v131\nM v[12:15],a[100:103],v[212:215],v[12:15]\n" \
"buffer_load_dwordx4 v243,s[40:43],s69 offen offset:0 lds\nM v[20:23],a[72:75],v[196:199],v[20:23]\nv_exp_f32 v177,v177\nv_cvt_pk_bf16_f32 v194,v144,v145\nM v[20:23],a[104:107],v[212:215],v[20:23]\nds_read_b128 a[40:43],v250 offset:10240\nds_read_b128 a[44:47],v250 offset:11264\nM v[28:31],a[76:79],v[196:199],v[28:31]\n" \
"v_exp_f32 v178,v178\nv_cvt_pk_bf16_f32 v195,v146,v147\nM v[28:31],a[108:111],v[212:215],v[28:31]\nbuffer_load_dwordx4 v244,s[40:43],s69 offen offset:1024 lds\nM v[36:39],a[80:83],v[196:199],v[36:39]\nv_exp_f32 v179,v179\nv_cvt_pk_bf16_f32 v208,v160,v161\nM v[36:39],a[112:115],v[212:215],v[36:39]\n" \
"ds_read_b128 a[48:51],v250 offset:12288\nds_read_b128 a[52:55],v250 offset:13312\nM v[44:47],a[84:87],v[196:199],v[44:47]\nv_exp_f32 v132,v132\nv_cvt_pk_bf16_f32 v209,v162,v163\nM v[44:47],a[116:119],v[212:215],v[44:47]\nbuffer_load_dwordx4 v245,s[40:43],s69 offen offset:2048 lds\nv_cvt_pk_bf16_f32 v210,v176,v177\n" \
"M v[52:55],a[88:91],v[196:199],v[52:55]\nv_exp_f32 v133,v133\nM v[52:55],a[120:123],v[212:215],v[52:55]\nds_read_b128 a[56:59],v250 offset:14336\nds_read_b128 a[60:63],v250 offset:15360\nv_cvt_pk_bf16_f32 v211,v178,v179\nM v[60:63],a[92:95],v[196:199],v[60:63]\nv_exp_f32 v134,v134\n" \
"M v[60:63],a[124:127],v[212:215],v[60:63]\nbuffer_load_dwordx4 v246,s[40:43],s69 offen offset:3072 lds\nv_add_u32 v250,s94,v241\nM a[196:199],a[208:211],v[196:199],a[196:199]\nv_exp_f32 v135,v135\nM a[196:199],a[208:211],v[212:215],a[196:199]\nv_exp_f32 v148,v148\nv_exp_f32 v149,v149\n" \
"v_exp_f32 v150,v150\nv_exp_f32 v151,v151\n.Lod:\ns_waitcnt lgkmcnt(2)\nM v[128:131],a[0:3],a[128:131],v[224:227]\nv_cvt_pk_bf16_f32 v196,v132,v133\nv_cvt_pk_bf16_f32 v197,v134,v135\nM v[128:131],a[4:7],a[132:135],v[128:131]\n" \
"ds_read_b128 a[64:67],v250 offset:0\nds_read_b128 a[96:99],v250 offset:8192\nM v[128:131],a[8:11],a[136:139],v[128:131]\ns_lshl2_add_u32 s39,s62,s7\ns_lshl_b32 s39,s39,5\nM v[128:131],a[12:15],a[140:143],v[128:131]\ns_add_u32 s39,s39,s63\nv_cvt_pk_bf16_f32 v198,v148,v149\n" \
"v_cvt_pk_bf16_f32 v199,v150,v151\nM v[132:135],a[12:15],a[156:159],v[228:231]\nds_read_b128 a[68:71],v250 offset:1024\nds_read_b128 a[100:103],v250 offset:9216\nM v[132:135],a[8:11],a[152:155],v[132:135]\ns_lshl2_add_u32 s39,s39,73728\nv_and_b32 v251,3,v249\nM v[132:135],a[4:7],a[148:151],v[132:135]\n" \
"s_waitcnt vmcnt(16)\nbuffer_store_dword v252,off,s[52:55],s39 sc1\nM v[132:135],a[0:3],a[144:147],v[132:135]\nds_read_b128 a[72:75],v250 offset:2048\nds_read_b128 a[104:107],v250 offset:10240\nv_lshlrev_b32 v251,2,v251\nM v[144:147],a[16:19],a[128:131],v[224:227]\nv_exp_f32 v164,v164\n" \
"M v[144:147],a[20:23],a[132:135],v[144:147]\nv_exp_f32 v165,v165\nM v[144:147],a[24:27],a[136:139],v[144:147]\nds_read_b128 a[76:79],v250 offset:3072\nds_read_b128 a[108:111],v250 offset:11264\nM v[144:147],a[28:31],a[140:143],v[144:147]\nv_exp_f32 v166,v166\nv_cvt_pk_bf16_f32 v212,v164,v165\n" \
"M v[148:151],a[28:31],a[156:159],v[228:231]\nv_exp_f32 v167,v167\nM v[148:151],a[24:27],a[152:155],v[148:151]\nds_read_b128 a[80:83],v250 offset:4096\nds_read_b128 a[112:115],v250 offset:12288\nM v[148:151],a[20:23],a[148:151],v[148:151]\nv_cvt_pk_bf16_f32 v213,v166,v167\ns_add_u32 s80,s79,4096\n" \
"M v[148:151],a[16:19],a[144:147],v[148:151]\nv_exp_f32 v180,v180\ns_add_u32 s94,s94,0x4000\nM v[160:163],a[32:35],a[128:131],v[224:227]\nds_read_b128 a[84:87],v250 offset:5120\nds_read_b128 a[116:119],v250 offset:13312\nM v[160:163],a[36:39],a[132:135],v[160:163]\nv_exp_f32 v181,v181\n" \
"M v[160:163],a[40:43],a[136:139],v[160:163]\nv_exp_f32 v182,v182\nM v[160:163],a[44:47],a[140:143],v[160:163]\nds_read_b128 a[88:91],v250 offset:6144\nds_read_b128 a[120:123],v250 offset:14336\nv_cvt_pk_bf16_f32 v214,v180,v181\nM v[164:167],a[44:47],a[156:159],v[228:231]\nbuffer_load_dword v248,v251,s[52:55],s80 offen sc1\n" \
"M v[164:167],a[40:43],a[152:155],v[164:167]\nv_exp_f32 v183,v183\nM v[164:167],a[36:39],a[148:151],v[164:167]\nds_read_b128 a[92:95],v250 offset:7168\nds_read_b128 a[124:127],v250 offset:15360\nM v[164:167],a[32:35],a[144:147],v[164:167]\nv_cvt_pk_bf16_f32 v215,v182,v183\ns_and_b32 s94,s94,0xffff\n" \
"v_add_u32 v250,s94,v241\nM v[176:179],a[48:51],a[128:131],v[224:227]\nv_exp_f32 v128,v128\nM v[176:179],a[52:55],a[132:135],v[176:179]\ns_add_u32 s65,s10,s67\ns_waitcnt lgkmcnt(4)\nM v[176:179],a[56:59],a[136:139],v[176:179]\nv_exp_f32 v129,v129\n" \
"M v[176:179],a[60:63],a[140:143],v[176:179]\nv_exp_f32 v130,v130\ns_add_u32 s65,s65,1048576\nM v[180:183],a[60:63],a[156:159],v[228:231]\nv_exp_f32 v131,v131\nM v[180:183],a[56:59],a[152:155],v[180:183]\nv_exp_f32 v144,v144\nM v[180:183],a[52:55],a[148:151],v[180:183]\n" \
"v_exp_f32 v145,v145\nM v[180:183],a[48:51],a[144:147],v[180:183]\nv_exp_f32 v146,v146\nv_add_u32 v226,131072,v242\nM v[0:3],a[64:67],v[192:195],v[0:3]\nv_exp_f32 v147,v147\nM v[0:3],a[96:99],v[208:211],v[0:3]\nv_exp_f32 v160,v160\n" \
"M v[8:11],a[68:71],v[192:195],v[8:11]\ns_waitcnt lgkmcnt(0)\ns_barrier\nM v[8:11],a[100:103],v[208:211],v[8:11]\ns_lshl4_add_u32 s69,s11,s68\ns_add_u32 m0,s64,16384\nv_xor_b32 v227,32,v226\nM v[16:19],a[72:75],v[192:195],v[16:19]\n" \
"v_exp_f32 v161,v161\nM v[16:19],a[104:107],v[208:211],v[16:19]\nv_exp_f32 v162,v162\nM v[24:27],a[76:79],v[192:195],v[24:27]\nv_exp_f32 v163,v163\nM v[24:27],a[108:111],v[208:211],v[24:27]\nv_exp_f32 v176,v176\ns_add_u32 s37,s12,s67\n" \
"M v[32:35],a[80:83],v[192:195],v[32:35]\nbuffer_load_dwordx4 v243,s[40:43],s69 offen offset:0 lds\nM v[32:35],a[112:115],v[208:211],v[32:35]\nv_exp_f32 v177,v177\nM v[40:43],a[84:87],v[192:195],v[40:43]\nv_exp_f32 v178,v178\nM v[40:43],a[116:119],v[208:211],v[40:43]\nv_exp_f32 v179,v179\n" \
"s_add_u32 s38,s37,4096\nM v[48:51],a[88:91],v[192:195],v[48:51]\nv_exp_f32 v132,v132\nM v[48:51],a[120:123],v[208:211],v[48:51]\nv_exp_f32 v133,v133\nM v[56:59],a[92:95],v[192:195],v[56:59]\nv_exp_f32 v134,v134\nM v[56:59],a[124:127],v[208:211],v[56:59]\n" \
"v_exp_f32 v135,v135\ns_lshl2_add_u32 s76,s61,s79\nM a[192:195],a[208:211],v[192:195],a[192:195]\nbuffer_load_dwordx4 v244,s[40:43],s69 offen offset:1024 lds\nM a[192:195],a[208:211],v[208:211],a[192:195]\nv_cvt_pk_bf16_f32 v192,v128,v129\nv_cvt_pk_bf16_f32 v193,v130,v131\nM v[4:7],a[64:67],v[196:199],v[4:7]\n" \
"ds_read_b128 a[64:67],v250 offset:0\nv_cvt_pk_bf16_f32 v194,v144,v145\nM v[4:7],a[96:99],v[212:215],v[4:7]\nds_read_b128 a[96:99],v250 offset:8192\nv_cvt_pk_bf16_f32 v195,v146,v147\nv_cvt_pk_bf16_f32 v208,v160,v161\nM v[12:15],a[68:71],v[196:199],v[12:15]\nds_read_b128 a[68:71],v250 offset:1024\n" \
"v_cvt_pk_bf16_f32 v209,v162,v163\nM v[12:15],a[100:103],v[212:215],v[12:15]\nds_read_b128 a[100:103],v250 offset:9216\nv_cvt_pk_bf16_f32 v210,v176,v177\nM v[20:23],a[72:75],v[196:199],v[20:23]\nds_read_b128 a[72:75],v250 offset:2048\nv_cvt_pk_bf16_f32 v211,v178,v179\nM v[20:23],a[104:107],v[212:215],v[20:23]\n" \
"ds_read_b128 a[104:107],v250 offset:10240\nv_exp_f32 v148,v148\nM v[28:31],a[76:79],v[196:199],v[28:31]\nbuffer_load_dwordx4 v245,s[40:43],s69 offen offset:2048 lds\nM v[28:31],a[108:111],v[212:215],v[28:31]\nds_read_b128 a[76:79],v250 offset:3072\nds_read_b128 a[108:111],v250 offset:11264\nM v[36:39],a[80:83],v[196:199],v[36:39]\n" \
"ds_read_b128 a[80:83],v250 offset:4096\nv_lshlrev_b32 v238,1,v240\nM v[36:39],a[112:115],v[212:215],v[36:39]\nds_read_b128 a[112:115],v250 offset:12288\nv_exp_f32 v149,v149\nM v[44:47],a[84:87],v[196:199],v[44:47]\nds_read_b128 a[84:87],v250 offset:5120\nv_and_b32 v254,32,v249\n" \
"M v[44:47],a[116:119],v[212:215],v[44:47]\nds_read_b128 a[116:119],v250 offset:13312\ns_lshr_b32 s70,s60,1\nM v[52:55],a[88:91],v[196:199],v[52:55]\nds_read_b128 a[88:91],v250 offset:6144\nv_lshlrev_b32 v254,3,v254\nM v[52:55],a[120:123],v[212:215],v[52:55]\nds_read_b128 a[120:123],v250 offset:14336\n" \
"v_exp_f32 v150,v150\nM v[60:63],a[92:95],v[196:199],v[60:63]\nbuffer_load_dwordx4 v246,s[40:43],s69 offen offset:3072 lds\nM v[60:63],a[124:127],v[212:215],v[60:63]\ns_waitcnt vmcnt(4)\nv_and_b32 v248,s17,v248\nM a[196:199],a[208:211],v[196:199],a[196:199]\nv_cmp_eq_u32 vcc,0,v248\n" \
"ds_read_b128 a[92:95],v250 offset:7168\nM a[196:199],a[208:211],v[212:215],a[196:199]\ns_cbranch_vccnz .Lps6\n.Lpb6:\ns_lshl4_add_u32 s69,s11,s68\ns_waitcnt lgkmcnt(4)\nM v[0:3],a[64:67],v[192:195],v[0:3]\nbuffer_load_dwordx4 v[72:75],v240,s[52:55],s65 offen offset:0 sc1\n" \
"M v[0:3],a[96:99],v[208:211],v[0:3]\ns_add_u32 s69,s69,0x10000\ns_add_u32 m0,s64,49152\nM v[8:11],a[68:71],v[192:195],v[8:11]\nds_read_b128 a[124:127],v250 offset:15360\nv_cvt_pk_bf16_f32 v196,v132,v133\nM v[8:11],a[100:103],v[208:211],v[8:11]\nv_exp_f32 v151,v151\n" \
"v_cvt_pk_bf16_f32 v197,v134,v135\nM v[16:19],a[72:75],v[192:195],v[16:19]\nbuffer_load_dwordx4 v243,s[40:43],s69 offen offset:0 lds\nM v[16:19],a[104:107],v[208:211],v[16:19]\nv_exp_f32 v164,v164\nM v[24:27],a[76:79],v[192:195],v[24:27]\nv_exp_f32 v165,v165\nM v[24:27],a[108:111],v[208:211],v[24:27]\n" \
"v_exp_f32 v166,v166\nv_cvt_pk_bf16_f32 v198,v148,v149\nM v[32:35],a[80:83],v[192:195],v[32:35]\nbuffer_load_dwordx4 v[76:79],v240,s[52:55],s65 offen offset:1024 sc1\nM v[32:35],a[112:115],v[208:211],v[32:35]\nv_exp_f32 v167,v167\nM v[40:43],a[84:87],v[192:195],v[40:43]\nv_cvt_pk_bf16_f32 v199,v150,v151\n" \
"s_waitcnt lgkmcnt(0)\nM v[40:43],a[116:119],v[208:211],v[40:43]\nv_exp_f32 v180,v180\nv_cvt_pk_bf16_f32 v212,v164,v165\nM v[48:51],a[88:91],v[192:195],v[48:51]\nbuffer_load_dwordx4 v244,s[40:43],s69 offen offset:1024 lds\nM v[48:51],a[120:123],v[208:211],v[48:51]\nv_exp_f32 v181,v181\n" \
"M v[56:59],a[92:95],v[192:195],v[56:59]\nv_exp_f32 v182,v182\nM v[56:59],a[124:127],v[208:211],v[56:59]\nv_exp_f32 v183,v183\nv_cvt_pk_bf16_f32 v213,v166,v167\nM a[192:195],a[208:211],v[192:195],a[192:195]\nbuffer_load_dwordx4 v[88:91],v240,s[52:55],s65 offen offset:2048 sc1\nM a[192:195],a[208:211],v[208:211],a[192:195]\n" \
"v_cvt_pk_bf16_f32 v214,v180,v181\nv_cvt_pk_bf16_f32 v215,v182,v183\nM v[4:7],a[64:67],v[196:199],v[4:7]\ns_barrier\nds_read_b64_tr_b16 v[200:201],v226 offset:0\nM v[4:7],a[96:99],v[212:215],v[4:7]\nds_read_b64_tr_b16 v[202:203],v226 offset:4096\nds_read_b64_tr_b16 v[204:205],v226 offset:8192\n" \
"ds_read_b64_tr_b16 v[206:207],v226 offset:12288\nM v[12:15],a[68:71],v[196:199],v[12:15]\nbuffer_load_dwordx4 v245,s[40:43],s69 offen offset:2048 lds\nM v[12:15],a[100:103],v[212:215],v[12:15]\nds_read_b64_tr_b16 v[216:217],v227 offset:0\nds_read_b64_tr_b16 v[218:219],v227 offset:4096\nM v[20:23],a[72:75],v[196:199],v[20:23]\nds_read_b64_tr_b16 v[220:221],v227 offset:8192\n" \
"ds_read_b64_tr_b16 v[222:223],v227 offset:12288\nM v[20:23],a[104:107],v[212:215],v[20:23]\nds_read_b64_tr_b16 v[192:193],v226 offset:512\nds_read_b64_tr_b16 v[194:195],v226 offset:4608\nds_read_b64_tr_b16 v[208:209],v226 offset:8704\nM v[28:31],a[76:79],v[196:199],v[28:31]\nbuffer_load_dwordx4 v[92:95],v240,s[52:55],s65 offen offset:3072 sc1\nM v[28:31],a[108:111],v[212:215],v[28:31]\n" \
"s_add_u32 s65,s65,4096\nds_read_b64_tr_b16 v[210:211],v226 offset:12800\nM v[36:39],a[80:83],v[196:199],v[36:39]\nv_accvgpr_read_b32 v224,a192\nv_cmp_eq_u32 vcc,2,v248\nM v[36:39],a[112:115],v[212:215],v[36:39]\ns_cmp_lg_u64 vcc,0\ns_addc_u32 s99,s99,0\n" \
"v_xor_b32 v250,0x8000,v250\nM v[44:47],a[84:87],v[196:199],v[44:47]\nbuffer_load_dwordx4 v246,s[40:43],s69 offen offset:3072 lds\nM v[44:47],a[116:119],v[212:215],v[44:47]\nv_add_u32 v229,s64,v250\ns_lshl_b32 s70,s70,7\nM v[52:55],a[88:91],v[196:199],v[52:55]\nv_lshl_add_u32 v254,v249,2,v254\n" \
"v_xor_b32 v254,s70,v254\nM v[52:55],a[120:123],v[212:215],v[52:55]\ns_mov_b32 s97,4096\ns_lshl_b32 s80,s62,9\ns_add_u32 s80,s80,73728\nM v[60:63],a[92:95],v[196:199],v[60:63]\nbuffer_load_dwordx4 v[104:107],v240,s[52:55],s65 offen offset:0 sc1\nM v[60:63],a[124:127],v[212:215],v[60:63]\n" \
"v_and_b32 v236,15,v249\ns_lshl2_add_u32 s74,s63,s7\nM a[196:199],a[208:211],v[196:199],a[196:199]\nds_read_b64_tr_b16 v[196:197],v227 offset:512\nds_read_b64_tr_b16 v[198:199],v227 offset:4608\nM a[196:199],a[208:211],v[212:215],a[196:199]\nds_read_b64_tr_b16 v[212:213],v227 offset:8704\nds_read_b64_tr_b16 v[214:215],v227 offset:12800\n" \
"s_waitcnt lgkmcnt(4)\nM v[128:131],v[64:67],v[200:203],0\ns_lshl_b32 s72,s62,7\nv_lshrrev_b32 v235,4,v249\nv_lshlrev_b32 v236,1,v236\ns_add_u32 s95,s74,s72\nM v[128:131],v[80:83],v[204:207],v[128:131]\nv_accvgpr_read_b32 v228,a196\n" \
"v_add_f32 v225,v224,v228\nv_rcp_f32 v224,v224\nv_lshl_or_b32 v235,v235,8,v236\nM v[132:135],v[68:71],v[200:203],0\nv_mul_f32 v0,v0,v224\nv_mul_f32 v1,v1,v224\nv_mul_f32 v2,v2,v224\nv_mul_f32 v3,v3,v224\n" \
"M v[132:135],v[84:87],v[204:207],v[132:135]\nv_cvt_pk_bf16_f32 v0,v0,v1\nv_mul_f32 v8,v8,v224\nv_mul_f32 v9,v9,v224\nv_mul_f32 v10,v10,v224\nv_mul_f32 v11,v11,v224\nM v[136:139],v[64:67],v[216:219],0\nbuffer_load_dwordx4 v[108:111],v240,s[52:55],s65 offen offset:1024 sc1\n" \
"v_cvt_pk_bf16_f32 v1,v2,v3\nds_read_b64_tr_b16 v[200:201],v226 offset:1024\nM v[136:139],v[80:83],v[220:223],v[136:139]\nds_read_b64_tr_b16 v[202:203],v226 offset:5120\nv_cvt_pk_bf16_f32 v2,v8,v9\nv_cvt_pk_bf16_f32 v3,v10,v11\nds_read_b64_tr_b16 v[204:205],v226 offset:9216\nds_read_b64_tr_b16 v[206:207],v226 offset:13312\n" \
"M v[140:143],v[68:71],v[216:219],0\nbuffer_store_dwordx4 v[0:3],v240,s[52:55],s37 offen offset:0 sc1\nv_rcp_f32 v228,v228\nM v[140:143],v[84:87],v[220:223],v[140:143]\nv_mul_f32 v4,v4,v228\nv_mul_f32 v5,v5,v228\nds_read_b64_tr_b16 v[216:217],v227 offset:1024\nds_read_b64_tr_b16 v[218:219],v227 offset:5120\n" \
"v_mul_f32 v6,v6,v228\nM v[144:147],v[64:67],v[192:195],0\nv_mul_f32 v7,v7,v228\nv_cvt_pk_bf16_f32 v4,v4,v5\nv_mul_f32 v12,v12,v228\nv_mul_f32 v13,v13,v228\nM v[144:147],v[80:83],v[208:211],v[144:147]\nv_mul_f32 v14,v14,v228\n" \
"v_mul_f32 v15,v15,v228\nv_cvt_pk_bf16_f32 v5,v6,v7\nds_read_b64_tr_b16 v[220:221],v227 offset:9216\nds_read_b64_tr_b16 v[222:223],v227 offset:13312\nM v[148:151],v[68:71],v[192:195],0\nv_cvt_pk_bf16_f32 v6,v12,v13\nv_cvt_pk_bf16_f32 v7,v14,v15\nds_read_b64_tr_b16 v[192:193],v226 offset:1536\n" \
"ds_read_b64_tr_b16 v[194:195],v226 offset:5632\nM v[148:151],v[84:87],v[208:211],v[148:151]\nbuffer_store_dwordx4 v[4:7],v240,s[52:55],s37 offen offset:1024 sc1\nv_mul_f32 v16,v16,v224\nv_mul_f32 v17,v17,v224\ns_waitcnt lgkmcnt(6)\nM v[152:155],v[64:67],v[196:199],0\nbuffer_load_dwordx4 v[120:123],v240,s[52:55],s65 offen offset:2048 sc1\n" \
"ds_read_b64_tr_b16 v[208:209],v226 offset:9728\nds_read_b64_tr_b16 v[210:211],v226 offset:13824\nM v[152:155],v[80:83],v[212:215],v[152:155]\nv_mul_f32 v18,v18,v224\nv_mul_f32 v19,v19,v224\nv_cvt_pk_bf16_f32 v16,v16,v17\nv_mul_f32 v24,v24,v224\nv_mul_f32 v25,v25,v224\n" \
"M v[156:159],v[68:71],v[196:199],0\nv_mul_f32 v26,v26,v224\nv_mul_f32 v27,v27,v224\nv_cvt_pk_bf16_f32 v17,v18,v19\nv_cvt_pk_bf16_f32 v18,v24,v25\nM v[156:159],v[84:87],v[212:215],v[156:159]\nv_cvt_pk_bf16_f32 v19,v26,v27\nds_read_b64_tr_b16 v[196:197],v227 offset:1536\n" \
"ds_read_b64_tr_b16 v[198:199],v227 offset:5632\nds_read_b64_tr_b16 v[212:213],v227 offset:9728\nv_mul_f32 v20,v20,v228\nM v[160:163],v[64:67],v[200:203],0\nbuffer_store_dwordx4 v[16:19],v240,s[52:55],s37 offen offset:2048 sc1\nds_read_b64_tr_b16 v[214:215],v227 offset:13824\nv_mul_f32 v21,v21,v228\nM v[160:163],v[80:83],v[204:207],v[160:163]\n" \
"v_mul_f32 v22,v22,v228\nv_mul_f32 v23,v23,v228\nv_cvt_pk_bf16_f32 v20,v20,v21\nv_mul_f32 v28,v28,v228\nv_mul_f32 v29,v29,v228\nM v[164:167],v[68:71],v[200:203],0\nv_mul_f32 v30,v30,v228\nv_mul_f32 v31,v31,v228\n" \
"v_cvt_pk_bf16_f32 v21,v22,v23\nv_cvt_pk_bf16_f32 v22,v28,v29\nM v[164:167],v[84:87],v[204:207],v[164:167]\nv_cvt_pk_bf16_f32 v23,v30,v31\nds_read_b64_tr_b16 v[200:201],v226 offset:16384\nds_read_b64_tr_b16 v[202:203],v226 offset:20480\nds_read_b64_tr_b16 v[204:205],v226 offset:24576\ns_waitcnt lgkmcnt(6)\n" \
"M v[168:171],v[64:67],v[216:219],0\nbuffer_load_dwordx4 v[124:127],v240,s[52:55],s65 offen offset:3072 sc1\nds_read_b64_tr_b16 v[206:207],v226 offset:28672\nv_mul_f32 v32,v32,v224\nM v[168:171],v[80:83],v[220:223],v[168:171]\nbuffer_store_dwordx4 v[20:23],v240,s[52:55],s37 offen offset:3072 sc1\nv_mul_f32 v33,v33,v224\nv_mul_f32 v34,v34,v224\n" \
"v_mul_f32 v35,v35,v224\nM v[172:175],v[68:71],v[216:219],0\nv_cvt_pk_bf16_f32 v32,v32,v33\nv_mul_f32 v40,v40,v224\nv_mul_f32 v41,v41,v224\nv_mul_f32 v42,v42,v224\nM v[172:175],v[84:87],v[220:223],v[172:175]\nv_mul_f32 v43,v43,v224\n" \
"v_cvt_pk_bf16_f32 v33,v34,v35\nds_read_b64_tr_b16 v[216:217],v227 offset:16384\nds_read_b64_tr_b16 v[218:219],v227 offset:20480\nv_cvt_pk_bf16_f32 v34,v40,v41\nM v[176:179],v[64:67],v[192:195],0\nv_cvt_pk_bf16_f32 v35,v42,v43\nds_read_b64_tr_b16 v[220:221],v227 offset:24576\nds_read_b64_tr_b16 v[222:223],v227 offset:28672\n" \
"s_add_u32 s65,s11,s67\nM v[176:179],v[80:83],v[208:211],v[176:179]\ns_add_u32 s65,s65,1048576\nv_mul_f32 v36,v36,v228\nv_mul_f32 v37,v37,v228\nv_mul_f32 v38,v38,v228\nv_mul_f32 v39,v39,v228\nM v[180:183],v[68:71],v[192:195],0\n" \
"buffer_store_dwordx4 v[32:35],v240,s[52:55],s38 offen offset:0 sc1\nds_read_b64_tr_b16 v[192:193],v226 offset:16896\nds_read_b64_tr_b16 v[194:195],v226 offset:20992\nM v[180:183],v[84:87],v[208:211],v[180:183]\nv_cvt_pk_bf16_f32 v36,v36,v37\nv_mul_f32 v44,v44,v228\nv_mul_f32 v45,v45,v228\nv_mul_f32 v46,v46,v228\n" \
"s_waitcnt lgkmcnt(6)\nM v[184:187],v[64:67],v[196:199],0\nbuffer_load_dwordx4 a[0:3],v240,s[52:55],s65 offen offset:0 sc1\nv_mul_f32 v47,v47,v228\nv_cvt_pk_bf16_f32 v37,v38,v39\nM v[184:187],v[80:83],v[212:215],v[184:187]\nds_read_b64_tr_b16 v[208:209],v226 offset:25088\nds_read_b64_tr_b16 v[210:211],v226 offset:29184\n" \
"v_cvt_pk_bf16_f32 v38,v44,v45\nv_cvt_pk_bf16_f32 v39,v46,v47\nv_add_f32 v225,v225,v224\nM v[188:191],v[68:71],v[196:199],0\nds_read_b64_tr_b16 v[196:197],v227 offset:16896\nds_read_b64_tr_b16 v[198:199],v227 offset:20992\nv_add_f32 v225,v225,v228\nv_cmp_ngt_f32 vcc,s28,v225\n" \
"M v[188:191],v[84:87],v[212:215],v[188:191]\nbuffer_store_dwordx4 v[36:39],v240,s[52:55],s38 offen offset:1024 sc1\nv_mul_f32 v48,v48,v224\nv_mul_f32 v49,v49,v224\nds_read_b64_tr_b16 v[212:213],v227 offset:25088\nM v[128:131],v[96:99],v[200:203],v[128:131]\nds_read_b64_tr_b16 v[214:215],v227 offset:29184\ns_and_b64 vcc,vcc,s[90:91]\n" \
"s_addc_u32 s99,s99,0\nv_mul_f32 v50,v50,v224\nM v[128:131],v[112:115],v[204:207],v[128:131]\nv_mul_f32 v51,v51,v224\nv_cvt_pk_bf16_f32 v48,v48,v49\nv_mul_f32 v56,v56,v224\nv_mul_f32 v57,v57,v224\nv_mul_f32 v58,v58,v224\n" \
"M v[132:135],v[100:103],v[200:203],v[132:135]\nv_mul_f32 v59,v59,v224\nv_cvt_pk_bf16_f32 v49,v50,v51\nv_cvt_pk_bf16_f32 v50,v56,v57\nv_cvt_pk_bf16_f32 v51,v58,v59\nM v[132:135],v[116:119],v[204:207],v[132:135]\nv_mov_b32 v230,s99\nds_write_b32 v229,v230\n" \
"ds_read_b64_tr_b16 v[200:201],v226 offset:17408\nds_read_b64_tr_b16 v[202:203],v226 offset:21504\ns_waitcnt lgkmcnt(5)\nM v[136:139],v[96:99],v[216:219],v[136:139]\nbuffer_store_dwordx4 v[48:51],v240,s[52:55],s38 offen offset:2048 sc1\nbuffer_load_dwordx4 a[4:7],v240,s[52:55],s65 offen offset:1024 sc1\nM v[136:139],v[112:115],v[220:223],v[136:139]\nds_read_b64_tr_b16 v[204:205],v226 offset:25600\n" \
"ds_read_b64_tr_b16 v[206:207],v226 offset:29696\nv_mul_f32 v52,v52,v228\nv_mul_f32 v53,v53,v228\nv_mul_f32 v54,v54,v228\nM v[140:143],v[100:103],v[216:219],v[140:143]\nv_mul_f32 v55,v55,v228\nv_cvt_pk_bf16_f32 v52,v52,v53\nv_mul_f32 v60,v60,v228\n" \
"v_mul_f32 v61,v61,v228\nM v[140:143],v[116:119],v[220:223],v[140:143]\nv_mul_f32 v62,v62,v228\nv_mul_f32 v63,v63,v228\nv_cvt_pk_bf16_f32 v53,v54,v55\nv_cvt_pk_bf16_f32 v54,v60,v61\nv_cvt_pk_bf16_f32 v55,v62,v63\nM v[144:147],v[96:99],v[192:195],v[144:147]\n" \
"ds_read_b64_tr_b16 v[216:217],v227 offset:17408\nds_read_b64_tr_b16 v[218:219],v227 offset:21504\nds_read_b64_tr_b16 v[220:221],v227 offset:25600\ns_lshl4_add_u32 s69,s14,s68\nM v[144:147],v[112:115],v[208:211],v[144:147]\nbuffer_store_dwordx4 v[52:55],v240,s[52:55],s38 offen offset:3072 sc1\nds_read_b64_tr_b16 v[222:223],v227 offset:29696\ns_add_u32 m0,s64,65536\n" \
"v_lshl_add_u32 v229,v251,10,v250\nM v[148:151],v[100:103],v[192:195],v[148:151]\nds_read_b64_tr_b16 v[192:193],v226 offset:17920\nds_read_b64_tr_b16 v[194:195],v226 offset:22016\ns_lshl_b32 s95,s95,10\nv_add_u32 v236,s95,v235\nM v[148:151],v[116:119],v[208:211],v[148:151]\nds_read_b64_tr_b16 v[208:209],v226 offset:26112\n" \
"ds_read_b64_tr_b16 v[210:211],v226 offset:30208\nv_add_u32 v226,0,v242\ns_sub_u32 s74,s74,s72\ns_add_u32 s74,s74,1920\nM v[152:155],v[96:99],v[196:199],v[152:155]\nbuffer_load_dwordx4 a[8:11],v240,s[52:55],s65 offen offset:2048 sc1\ns_lshl_b32 s74,s74,10\ns_waitcnt lgkmcnt(7)\n" \
"M v[152:155],v[112:115],v[212:215],v[152:155]\nv_add_u32 v237,s74,v235\ns_lshl_b32 s72,s7,9\nv_lshl_add_u32 v239,v249,3,s72\ns_add_u32 s38,s33,16384\nv_bfe_u32 v228,v249,2,2\nM v[156:159],v[100:103],v[196:199],v[156:159]\ns_barrier\n" \
"buffer_load_dwordx4 v243,s[40:43],s69 offen offset:0 lds\nds_read_b64_tr_b16 v[196:197],v227 offset:17920\nM v[156:159],v[116:119],v[212:215],v[156:159]\nds_read_b64_tr_b16 v[198:199],v227 offset:22016\nds_read_b32 v230,v229\nds_read_b64_tr_b16 v[212:213],v227 offset:26112\nv_lshrrev_b32 v229,4,v249\ns_add_u32 s95,s95,54525952\n" \
"M v[160:163],v[96:99],v[200:203],v[160:163]\nds_read_b64_tr_b16 v[214:215],v227 offset:30208\nv_xor_b32 v227,32,v226\ns_add_u32 s74,s74,54525952\nM v[160:163],v[112:115],v[204:207],v[160:163]\nM v[164:167],v[100:103],v[200:203],v[164:167]\nds_read_b64_tr_b16 v[200:201],v226 offset:0\nds_read_b64_tr_b16 v[202:203],v226 offset:4096\n" \
"M v[164:167],v[116:119],v[204:207],v[164:167]\nds_read_b64_tr_b16 v[204:205],v226 offset:8192\ns_waitcnt lgkmcnt(8)\nM v[168:171],v[96:99],v[216:219],v[168:171]\nbuffer_load_dwordx4 a[12:15],v240,s[52:55],s65 offen offset:3072 sc1\nM v[168:171],v[112:115],v[220:223],v[168:171]\nds_read_b64_tr_b16 v[206:207],v226 offset:12288\ns_add_u32 s65,s65,4096\n" \
"M v[172:175],v[100:103],v[216:219],v[172:175]\nbuffer_load_dwordx4 v244,s[40:43],s69 offen offset:1024 lds\nM v[172:175],v[116:119],v[220:223],v[172:175]\nds_read_b64_tr_b16 v[216:217],v227 offset:0\nds_read_b64_tr_b16 v[218:219],v227 offset:4096\nM v[176:179],v[96:99],v[192:195],v[176:179]\nds_read_b64_tr_b16 v[220:221],v227 offset:8192\nds_read_b64_tr_b16 v[222:223],v227 offset:12288\n" \
"M v[176:179],v[112:115],v[208:211],v[176:179]\ns_waitcnt lgkmcnt(8)\nv_cmp_ne_u32 vcc,0,v230\nM v[180:183],v[100:103],v[192:195],v[180:183]\ns_cbranch_vccnz .Lredo\nds_read_b64_tr_b16 v[192:193],v226 offset:512\nM v[180:183],v[116:119],v[208:211],v[180:183]\nds_read_b64_tr_b16 v[194:195],v226 offset:4608\n" \
"ds_read_b64_tr_b16 v[208:209],v226 offset:8704\nM v[184:187],v[96:99],v[196:199],v[184:187]\nbuffer_load_dwordx4 a[16:19],v240,s[52:55],s65 offen offset:0 sc1\nM v[184:187],v[112:115],v[212:215],v[184:187]\nds_read_b64_tr_b16 v[210:211],v226 offset:12800\ns_lshl_b32 s70,s2,9\nM v[188:191],v[100:103],v[196:199],v[188:191]\nbuffer_load_dwordx4 v245,s[40:43],s69 offen offset:2048 lds\n" \
"M v[188:191],v[116:119],v[212:215],v[188:191]\nds_read_b64_tr_b16 v[196:197],v227 offset:512\ns_waitcnt vmcnt(27) lgkmcnt(6)\nM v[128:131],v[72:75],v[200:203],v[128:131]\nds_read_b64_tr_b16 v[198:199],v227 offset:4608\ns_waitcnt vmcnt(23)\nM v[128:131],v[88:91],v[204:207],v[128:131]\nds_read_b64_tr_b16 v[212:213],v227 offset:8704\n" \
"ds_read_b64_tr_b16 v[214:215],v227 offset:12800\nM v[132:135],v[76:79],v[200:203],v[132:135]\nds_read_b64_tr_b16 v[200:201],v226 offset:1024\ns_waitcnt vmcnt(21)\nM v[132:135],v[92:95],v[204:207],v[132:135]\nds_read_b64_tr_b16 v[202:203],v226 offset:5120\nds_read_b64_tr_b16 v[204:205],v226 offset:9216\nM v[136:139],v[72:75],v[216:219],v[136:139]\n" \
"buffer_load_dwordx4 a[20:23],v240,s[52:55],s65 offen offset:1024 sc1\ns_waitcnt lgkmcnt(7)\nM v[136:139],v[88:91],v[220:223],v[136:139]\nds_read_b64_tr_b16 v[206:207],v226 offset:13312\ns_lshl_b32 s71,s7,8\nM v[140:143],v[76:79],v[216:219],v[140:143]\nbuffer_load_dwordx4 v246,s[40:43],s69 offen offset:3072 lds\nM v[140:143],v[92:95],v[220:223],v[140:143]\n" \
"ds_read_b64_tr_b16 v[216:217],v227 offset:1024\nds_read_b64_tr_b16 v[218:219],v227 offset:5120\nM v[144:147],v[72:75],v[192:195],v[144:147]\nds_read_b64_tr_b16 v[220:221],v227 offset:9216\nds_read_b64_tr_b16 v[222:223],v227 offset:13312\nM v[144:147],v[88:91],v[208:211],v[144:147]\ns_lshl4_add_u32 s69,s14,s68\ns_add_u32 s69,s69,0x10000\n" \
"M v[148:151],v[76:79],v[192:195],v[148:151]\nds_read_b64_tr_b16 v[192:193],v226 offset:1536\nds_read_b64_tr_b16 v[194:195],v226 offset:5632\nM v[148:151],v[92:95],v[208:211],v[148:151]\ns_add_u32 m0,s64,98304\ns_waitcnt lgkmcnt(7)\nM v[152:155],v[72:75],v[196:199],v[152:155]\nbuffer_load_dwordx4 a[24:27],v240,s[52:55],s65 offen offset:2048 sc1\n" \
"M v[152:155],v[88:91],v[212:215],v[152:155]\nds_read_b64_tr_b16 v[208:209],v226 offset:9728\nds_read_b64_tr_b16 v[210:211],v226 offset:13824\nM v[156:159],v[76:79],v[196:199],v[156:159]\nbuffer_load_dwordx4 v243,s[40:43],s69 offen offset:0 lds\nM v[156:159],v[92:95],v[212:215],v[156:159]\nds_read_b64_tr_b16 v[196:197],v227 offset:1536\nds_read_b64_tr_b16 v[198:199],v227 offset:5632\n" \
"M v[160:163],v[72:75],v[200:203],v[160:163]\nds_read_b64_tr_b16 v[212:213],v227 offset:9728\ns_waitcnt lgkmcnt(7)\nM v[160:163],v[88:91],v[204:207],v[160:163]\nds_read_b64_tr_b16 v[214:215],v227 offset:13824\nv_lshl_add_u32 v241,v249,2,s71\nM v[164:167],v[76:79],v[200:203],v[164:167]\nds_read_b64_tr_b16 v[200:201],v226 offset:32768\n" \
"ds_read_b64_tr_b16 v[202:203],v226 offset:36864\nM v[164:167],v[92:95],v[204:207],v[164:167]\nds_read_b64_tr_b16 v[204:205],v226 offset:40960\nds_read_b64_tr_b16 v[206:207],v226 offset:45056\nM v[168:171],v[72:75],v[216:219],v[168:171]\nbuffer_load_dwordx4 a[28:31],v240,s[52:55],s65 offen offset:3072 sc1\nM v[168:171],v[88:91],v[220:223],v[168:171]\ns_add_u32 s65,s14,s67\n" \
"s_add_u32 s65,s65,1048576\nM v[172:175],v[76:79],v[216:219],v[172:175]\nbuffer_load_dwordx4 v244,s[40:43],s69 offen offset:1024 lds\nM v[172:175],v[92:95],v[220:223],v[172:175]\nds_read_b64_tr_b16 v[216:217],v227 offset:32768\ns_waitcnt lgkmcnt(6)\nM v[176:179],v[72:75],v[192:195],v[176:179]\nds_read_b64_tr_b16 v[218:219],v227 offset:36864\n" \
"ds_read_b64_tr_b16 v[220:221],v227 offset:40960\nM v[176:179],v[88:91],v[208:211],v[176:179]\nds_read_b64_tr_b16 v[222:223],v227 offset:45056\ns_add_u32 s70,s70,4096\nM v[180:183],v[76:79],v[192:195],v[180:183]\nds_read_b64_tr_b16 v[192:193],v226 offset:33280\nds_read_b64_tr_b16 v[194:195],v226 offset:37376\nM v[180:183],v[92:95],v[208:211],v[180:183]\n" \
"ds_read_b64_tr_b16 v[208:209],v226 offset:41472\nds_read_b64_tr_b16 v[210:211],v226 offset:45568\nM v[184:187],v[72:75],v[196:199],v[184:187]\nbuffer_load_dwordx4 a[32:35],v240,s[52:55],s65 offen offset:0 sc1\ns_waitcnt lgkmcnt(8)\nM v[184:187],v[88:91],v[212:215],v[184:187]\nv_add_u32 v230,s70,v241\nv_add_u32 v231,8192,v230\n" \
"M v[188:191],v[76:79],v[196:199],v[188:191]\nbuffer_load_dwordx4 v245,s[40:43],s69 offen offset:2048 lds\nM v[188:191],v[92:95],v[212:215],v[188:191]\nds_read_b64_tr_b16 v[196:197],v227 offset:33280\ns_waitcnt vmcnt(26)\nM v[128:131],v[104:107],v[200:203],v[128:131]\nds_read_b64_tr_b16 v[198:199],v227 offset:37376\ns_waitcnt vmcnt(23)\n" \
"M v[128:131],v[120:123],v[204:207],v[128:131]\nds_read_b64_tr_b16 v[212:213],v227 offset:41472\nds_read_b64_tr_b16 v[214:215],v227 offset:45568\nM v[132:135],v[108:111],v[200:203],v[132:135]\nds_read_b64_tr_b16 v[200:201],v226 offset:33792\ns_waitcnt vmcnt(21)\nM v[132:135],v[124:127],v[204:207],v[132:135]\nds_read_b64_tr_b16 v[202:203],v226 offset:37888\n" \
"s_waitcnt lgkmcnt(6)\nM v[136:139],v[104:107],v[216:219],v[136:139]\nbuffer_load_dwordx4 a[36:39],v240,s[52:55],s65 offen offset:1024 sc1\nM v[136:139],v[120:123],v[220:223],v[136:139]\nds_read_b64_tr_b16 v[204:205],v226 offset:41984\nds_read_b64_tr_b16 v[206:207],v226 offset:46080\nM v[140:143],v[108:111],v[216:219],v[140:143]\nbuffer_load_dwordx4 v246,s[40:43],s69 offen offset:3072 lds\n" \
"M v[140:143],v[124:127],v[220:223],v[140:143]\nds_read_b64_tr_b16 v[216:217],v227 offset:33792\nds_read_b64_tr_b16 v[218:219],v227 offset:37888\nM v[144:147],v[104:107],v[192:195],v[144:147]\nds_read_b64_tr_b16 v[220:221],v227 offset:41984\nds_read_b64_tr_b16 v[222:223],v227 offset:46080\nM v[144:147],v[120:123],v[208:211],v[144:147]\nv_mov_b32 v250,83968\n" \
"v_accvgpr_write_b32 a194,0\nM v[148:151],v[108:111],v[192:195],v[148:151]\nds_read_b64_tr_b16 v[192:193],v226 offset:34304\nds_read_b64_tr_b16 v[194:195],v226 offset:38400\nM v[148:151],v[124:127],v[208:211],v[148:151]\nds_read_b64_tr_b16 v[208:209],v226 offset:42496\ns_waitcnt lgkmcnt(9)\nM v[152:155],v[104:107],v[196:199],v[152:155]\n" \
"buffer_load_dwordx4 a[40:43],v240,s[52:55],s65 offen offset:2048 sc1\nM v[152:155],v[120:123],v[212:215],v[152:155]\nds_read_b64_tr_b16 v[210:211],v226 offset:46592\nv_accvgpr_write_b32 a195,0\nM v[156:159],v[108:111],v[196:199],v[156:159]\ns_waitcnt vmcnt(16)\nbuffer_store_dword v252,off,s[52:55],s76 sc1\ns_barrier\n" \
"M v[156:159],v[124:127],v[212:215],v[156:159]\nds_read_b64_tr_b16 v[196:197],v227 offset:34304\nds_read_b64_tr_b16 v[198:199],v227 offset:38400\nM v[160:163],v[104:107],v[200:203],v[160:163]\nds_read_b64_tr_b16 v[212:213],v227 offset:42496\ns_waitcnt lgkmcnt(7)\nM v[160:163],v[120:123],v[204:207],v[160:163]\nds_read_b64_tr_b16 v[214:215],v227 offset:46592\n" \
"v_accvgpr_write_b32 a196,0\nM v[164:167],v[108:111],v[200:203],v[164:167]\nds_read_b64_tr_b16 v[200:201],v226 offset:16384\nds_read_b64_tr_b16 v[202:203],v226 offset:20480\nM v[164:167],v[124:127],v[204:207],v[164:167]\nds_read_b64_tr_b16 v[204:205],v226 offset:24576\nds_read_b64_tr_b16 v[206:207],v226 offset:28672\nM v[168:171],v[104:107],v[216:219],v[168:171]\n" \
"buffer_load_dwordx4 a[44:47],v240,s[52:55],s65 offen offset:3072 sc1\nM v[168:171],v[120:123],v[220:223],v[168:171]\ns_add_u32 s65,s65,4096\nv_accvgpr_write_b32 a197,0\nM v[172:175],v[108:111],v[216:219],v[172:175]\nds_read_b64_tr_b16 v[216:217],v227 offset:16384\nds_read_b64_tr_b16 v[218:219],v227 offset:20480\nM v[172:175],v[124:127],v[220:223],v[172:175]\n" \
"ds_read_b64_tr_b16 v[220:221],v227 offset:24576\ns_waitcnt lgkmcnt(8)\nM v[176:179],v[104:107],v[192:195],v[176:179]\nds_read_b64_tr_b16 v[222:223],v227 offset:28672\ns_load_dwordx2 s[44:45],s[4:5],0x68\nM v[176:179],v[120:123],v[208:211],v[176:179]\nds_write_b128 v250,a[244:247]\nM v[180:183],v[108:111],v[192:195],v[180:183]\n" \
"ds_read_b64_tr_b16 v[192:193],v226 offset:16896\nds_read_b64_tr_b16 v[194:195],v226 offset:20992\nM v[180:183],v[124:127],v[208:211],v[180:183]\nds_read_b64_tr_b16 v[208:209],v226 offset:25088\nds_read_b64_tr_b16 v[210:211],v226 offset:29184\nM v[184:187],v[104:107],v[196:199],v[184:187]\nbuffer_load_dwordx4 a[48:51],v240,s[52:55],s65 offen offset:0 sc1\ns_waitcnt lgkmcnt(9)\n" \
"M v[184:187],v[120:123],v[212:215],v[184:187]\ns_mov_b32 s46,2097152\nM v[188:191],v[108:111],v[196:199],v[188:191]\nds_read_b64_tr_b16 v[196:197],v227 offset:16896\nds_read_b64_tr_b16 v[198:199],v227 offset:20992\nM v[188:191],v[124:127],v[212:215],v[188:191]\nds_read_b64_tr_b16 v[212:213],v227 offset:25088\nds_read_b64_tr_b16 v[214:215],v227 offset:29184\n" \
"M v[128:131],a[0:3],v[200:203],v[128:131]\nM v[128:131],a[8:11],v[204:207],v[128:131]\nM v[132:135],a[4:7],v[200:203],v[132:135]\nbuffer_load_dwordx4 a[160:163],v238,s[52:55],0 offen offset:0 sc1\ns_waitcnt vmcnt(18)\nM v[132:135],a[12:15],v[204:207],v[132:135]\nds_read_b64_tr_b16 v[200:201],v226 offset:17408\ns_waitcnt lgkmcnt(5)\n" \
"M v[136:139],a[0:3],v[216:219],v[136:139]\nbuffer_load_dwordx4 a[52:55],v240,s[52:55],s65 offen offset:1024 sc1\nM v[136:139],a[8:11],v[220:223],v[136:139]\nds_read_b64_tr_b16 v[202:203],v226 offset:21504\nds_read_b64_tr_b16 v[204:205],v226 offset:25600\nM v[140:143],a[4:7],v[216:219],v[140:143]\nds_read_b64_tr_b16 v[206:207],v226 offset:29696\nds_read_b64_tr_b16 v[216:217],v227 offset:17408\n" \
"M v[140:143],a[12:15],v[220:223],v[140:143]\nglobal_load_dword a200,v230,s[26:27] offset:-4096\nM v[144:147],a[0:3],v[192:195],v[144:147]\nbuffer_load_dwordx4 a[164:167],v238,s[52:55],0 offen offset:16 sc1\nM v[144:147],a[8:11],v[208:211],v[144:147]\nds_read_b64_tr_b16 v[218:219],v227 offset:21504\nglobal_load_dword a201,v230,s[26:27] offset:-3072\nM v[148:151],a[4:7],v[192:195],v[148:151]\n" \
"ds_read_b64_tr_b16 v[220:221],v227 offset:25600\nds_read_b64_tr_b16 v[222:223],v227 offset:29696\nM v[148:151],a[12:15],v[208:211],v[148:151]\nglobal_load_dword a202,v230,s[26:27] offset:-2048\ns_waitcnt lgkmcnt(7)\nM v[152:155],a[0:3],v[196:199],v[152:155]\nbuffer_load_dwordx4 a[56:59],v240,s[52:55],s65 offen offset:2048 sc1\nM v[152:155],a[8:11],v[212:215],v[152:155]\n" \
"ds_read_b64_tr_b16 v[192:193],v226 offset:17920\nds_read_b64_tr_b16 v[194:195],v226 offset:22016\nglobal_load_dword a203,v230,s[26:27] offset:-1024\nM v[156:159],a[4:7],v[196:199],v[156:159]\nds_read_b64_tr_b16 v[208:209],v226 offset:26112\nds_read_b64_tr_b16 v[210:211],v226 offset:30208\nbuffer_load_dwordx4 a[168:171],v238,s[52:55],0 offen offset:2048 sc1\nM v[156:159],a[12:15],v[212:215],v[156:159]\n" \
"global_load_dword a204,v230,s[26:27] offset:0\ns_waitcnt lgkmcnt(6)\nM v[160:163],a[0:3],v[200:203],v[160:163]\nds_read_b64_tr_b16 v[196:197],v227 offset:17920\nds_read_b64_tr_b16 v[198:199],v227 offset:22016\nM v[160:163],a[8:11],v[204:207],v[160:163]\nglobal_load_dword a205,v230,s[26:27] offset:1024\nM v[164:167],a[4:7],v[200:203],v[164:167]\n" \
"ds_read_b64_tr_b16 v[212:213],v227 offset:26112\nds_read_b64_tr_b16 v[214:215],v227 offset:30208\nM v[164:167],a[12:15],v[204:207],v[164:167]\nglobal_load_dword a206,v230,s[26:27] offset:2048\nM v[168:171],a[0:3],v[216:219],v[168:171]\nbuffer_load_dwordx4 a[60:63],v240,s[52:55],s65 offen offset:3072 sc1\nbuffer_load_dwordx4 a[172:175],v238,s[52:55],0 offen offset:2064 sc1\ns_waitcnt lgkmcnt(4)\n" \
"M v[168:171],a[8:11],v[220:223],v[168:171]\nds_read_b64_tr_b16 v[200:201],v226 offset:49152\nds_read_b64_tr_b16 v[202:203],v226 offset:53248\nglobal_load_dword a207,v230,s[26:27] offset:3072\nM v[172:175],a[4:7],v[216:219],v[172:175]\nds_read_b64_tr_b16 v[204:205],v226 offset:57344\nds_read_b64_tr_b16 v[206:207],v226 offset:61440\nM v[172:175],a[12:15],v[220:223],v[172:175]\n" \
"global_load_dword a208,v231,s[26:27] offset:-4096\nM v[176:179],a[0:3],v[192:195],v[176:179]\nds_read_b64_tr_b16 v[216:217],v227 offset:49152\nds_read_b64_tr_b16 v[218:219],v227 offset:53248\nM v[176:179],a[8:11],v[208:211],v[176:179]\nglobal_load_dword a209,v231,s[26:27] offset:-3072\nM v[180:183],a[4:7],v[192:195],v[180:183]\nds_read_b64_tr_b16 v[220:221],v227 offset:57344\n" \
"ds_read_b64_tr_b16 v[222:223],v227 offset:61440\nbuffer_load_dwordx4 a[176:179],v238,s[52:55],s97 offen offset:0 sc1\nM v[180:183],a[12:15],v[208:211],v[180:183]\nglobal_load_dword a210,v231,s[26:27] offset:-2048\ns_waitcnt lgkmcnt(6)\nM v[184:187],a[0:3],v[196:199],v[184:187]\nds_read_b64_tr_b16 v[192:193],v226 offset:49664\nds_read_b64_tr_b16 v[194:195],v226 offset:53760\n" \
"M v[184:187],a[8:11],v[212:215],v[184:187]\nglobal_load_dword a211,v231,s[26:27] offset:-1024\nM v[188:191],a[4:7],v[196:199],v[188:191]\nds_read_b64_tr_b16 v[208:209],v226 offset:57856\nds_read_b64_tr_b16 v[210:211],v226 offset:61952\nM v[188:191],a[12:15],v[212:215],v[188:191]\nglobal_load_dword a252,v231,s[26:27] offset:0\ns_waitcnt vmcnt(36)\n" \
"M v[128:131],a[16:19],v[200:203],v[128:131]\nbuffer_load_dwordx4 a[180:183],v238,s[52:55],s97 offen offset:16 sc1\ns_waitcnt vmcnt(33) lgkmcnt(4)\nM v[128:131],a[24:27],v[204:207],v[128:131]\nds_read_b64_tr_b16 v[196:197],v227 offset:49664\nds_read_b64_tr_b16 v[198:199],v227 offset:53760\nglobal_load_dword a253,v231,s[26:27] offset:1024\nM v[132:135],a[20:23],v[200:203],v[132:135]\n" \
"ds_read_b64_tr_b16 v[212:213],v227 offset:57856\nds_read_b64_tr_b16 v[214:215],v227 offset:61952\ns_waitcnt vmcnt(32)\nM v[132:135],a[28:31],v[204:207],v[132:135]\nglobal_load_dword a254,v231,s[26:27] offset:2048\nM v[136:139],a[16:19],v[216:219],v[136:139]\nds_read_b64_tr_b16 v[200:201],v226 offset:50176\nds_read_b64_tr_b16 v[202:203],v226 offset:54272\n" \
"M v[136:139],a[24:27],v[220:223],v[136:139]\nglobal_load_dword a255,v231,s[26:27] offset:3072\nM v[140:143],a[20:23],v[216:219],v[140:143]\nds_read_b64_tr_b16 v[204:205],v226 offset:58368\nds_read_b64_tr_b16 v[206:207],v226 offset:62464\nbuffer_load_dwordx4 a[184:187],v238,s[52:55],s97 offen offset:2048 sc1\nM v[140:143],a[28:31],v[220:223],v[140:143]\nds_read_b64_tr_b16 v[216:217],v227 offset:50176\n" \
"s_waitcnt lgkmcnt(7)\nM v[144:147],a[16:19],v[192:195],v[144:147]\nbuffer_load_dword v248,v254,s[52:55],s80 offen sc1\nM v[144:147],a[24:27],v[208:211],v[144:147]\nds_read_b64_tr_b16 v[218:219],v227 offset:54272\nds_read_b64_tr_b16 v[220:221],v227 offset:58368\nM v[148:151],a[20:23],v[192:195],v[148:151]\nds_read_b64_tr_b16 v[222:223],v227 offset:62464\n" \
"ds_read_b64_tr_b16 v[192:193],v226 offset:50688\nM v[148:151],a[28:31],v[208:211],v[148:151]\nglobal_load_ushort v64,v236,s[8:9] offset:0\nM v[152:155],a[16:19],v[196:199],v[152:155]\nbuffer_load_dwordx4 a[188:191],v238,s[52:55],s97 offen offset:2064 sc1\ns_waitcnt lgkmcnt(7)\nM v[152:155],a[24:27],v[212:215],v[152:155]\nds_read_b64_tr_b16 v[194:195],v226 offset:54784\n" \
"global_load_ushort v80,v237,s[8:9] offset:0\nM v[156:159],a[20:23],v[196:199],v[156:159]\ns_waitcnt vmcnt(33)\ns_barrier\nds_read_b64_tr_b16 v[208:209],v226 offset:58880\nds_read_b64_tr_b16 v[210:211],v226 offset:62976\nM v[156:159],a[28:31],v[212:215],v[156:159]\nglobal_load_ushort v96,v235,s[20:21] offset:0\n" \
"M v[160:163],a[16:19],v[200:203],v[160:163]\nds_read_b64_tr_b16 v[196:197],v227 offset:50688\ns_waitcnt lgkmcnt(5)\nM v[160:163],a[24:27],v[204:207],v[160:163]\nds_read_b64_tr_b16 v[198:199],v227 offset:54784\nglobal_load_ushort v65,v236,s[8:9] offset:32\nM v[164:167],a[20:23],v[200:203],v[164:167]\nds_read_b64_tr_b16 v[212:213],v227 offset:58880\n" \
"ds_read_b64_tr_b16 v[214:215],v227 offset:62976\nM v[164:167],a[28:31],v[204:207],v[164:167]\nglobal_load_ushort v81,v237,s[8:9] offset:32\nM v[168:171],a[16:19],v[216:219],v[168:171]\nv_add_u32 v226,65536,v242\nds_read_b64_tr_b16 v[200:201],v226 offset:0\nM v[168:171],a[24:27],v[220:223],v[168:171]\nds_read_b64_tr_b16 v[202:203],v226 offset:4096\n" \
"global_load_ushort v97,v235,s[20:21] offset:32\nM v[172:175],a[20:23],v[216:219],v[172:175]\nds_read_b64_tr_b16 v[204:205],v226 offset:8192\nds_read_b64_tr_b16 v[206:207],v226 offset:12288\nM v[172:175],a[28:31],v[220:223],v[172:175]\nglobal_load_ushort v66,v236,s[8:9] offset:64\ns_waitcnt lgkmcnt(7)\nM v[176:179],a[16:19],v[192:195],v[176:179]\n" \
"v_xor_b32 v227,32,v226\nds_read_b64_tr_b16 v[216:217],v227 offset:0\nM v[176:179],a[24:27],v[208:211],v[176:179]\nds_read_b64_tr_b16 v[218:219],v227 offset:4096\nglobal_load_ushort v82,v237,s[8:9] offset:64\nM v[180:183],a[20:23],v[192:195],v[180:183]\nds_read_b64_tr_b16 v[220:221],v227 offset:8192\nds_read_b64_tr_b16 v[222:223],v227 offset:12288\n" \
"M v[180:183],a[28:31],v[208:211],v[180:183]\nglobal_load_ushort v98,v235,s[20:21] offset:64\ns_waitcnt lgkmcnt(6)\nM v[184:187],a[16:19],v[196:199],v[184:187]\nds_read_b64_tr_b16 v[192:193],v226 offset:512\nds_read_b64_tr_b16 v[194:195],v226 offset:4608\nM v[184:187],a[24:27],v[212:215],v[184:187]\nglobal_load_ushort v67,v236,s[8:9] offset:96\n" \
"M v[188:191],a[20:23],v[196:199],v[188:191]\nds_read_b64_tr_b16 v[208:209],v226 offset:8704\nds_read_b64_tr_b16 v[210:211],v226 offset:12800\nM v[188:191],a[28:31],v[212:215],v[188:191]\nglobal_load_ushort v83,v237,s[8:9] offset:96\nM v[128:131],a[32:35],v[200:203],v[128:131]\nds_read_b64_tr_b16 v[196:197],v227 offset:512\ns_waitcnt lgkmcnt(7)\n" \
"M v[128:131],a[40:43],v[204:207],v[128:131]\nds_read_b64_tr_b16 v[198:199],v227 offset:4608\nglobal_load_ushort v99,v235,s[20:21] offset:96\nM v[132:135],a[36:39],v[200:203],v[132:135]\nds_read_b64_tr_b16 v[212:213],v227 offset:8704\nds_read_b64_tr_b16 v[214:215],v227 offset:12800\ns_waitcnt vmcnt(32)\nM v[132:135],a[44:47],v[204:207],v[132:135]\n" \
"global_load_ushort v68,v236,s[8:9] offset:128\nM v[136:139],a[32:35],v[216:219],v[136:139]\nds_read_b64_tr_b16 v[200:201],v226 offset:1024\ns_waitcnt lgkmcnt(7)\nM v[136:139],a[40:43],v[220:223],v[136:139]\nds_read_b64_tr_b16 v[202:203],v226 offset:5120\nglobal_load_ushort v84,v237,s[8:9] offset:128\nM v[140:143],a[36:39],v[216:219],v[140:143]\n" \
"ds_read_b64_tr_b16 v[204:205],v226 offset:9216\nds_read_b64_tr_b16 v[206:207],v226 offset:13312\nM v[140:143],a[44:47],v[220:223],v[140:143]\nglobal_load_ushort v100,v235,s[20:21] offset:128\nM v[144:147],a[32:35],v[192:195],v[144:147]\nds_read_b64_tr_b16 v[216:217],v227 offset:1024\ns_waitcnt lgkmcnt(7)\nM v[144:147],a[40:43],v[208:211],v[144:147]\n" \
"ds_read_b64_tr_b16 v[218:219],v227 offset:5120\nglobal_load_ushort v69,v236,s[8:9] offset:160\nM v[148:151],a[36:39],v[192:195],v[148:151]\nds_read_b64_tr_b16 v[220:221],v227 offset:9216\nds_read_b64_tr_b16 v[222:223],v227 offset:13312\nM v[148:151],a[44:47],v[208:211],v[148:151]\nglobal_load_ushort v85,v237,s[8:9] offset:160\nM v[152:155],a[32:35],v[196:199],v[152:155]\n" \
"ds_read_b64_tr_b16 v[192:193],v226 offset:1536\ns_waitcnt lgkmcnt(7)\nM v[152:155],a[40:43],v[212:215],v[152:155]\nds_read_b64_tr_b16 v[194:195],v226 offset:5632\nglobal_load_ushort v101,v235,s[20:21] offset:160\nM v[156:159],a[36:39],v[196:199],v[156:159]\nds_read_b64_tr_b16 v[208:209],v226 offset:9728\nds_read_b64_tr_b16 v[210:211],v226 offset:13824\n" \
"M v[156:159],a[44:47],v[212:215],v[156:159]\nglobal_load_ushort v70,v236,s[8:9] offset:192\nM v[160:163],a[32:35],v[200:203],v[160:163]\nds_read_b64_tr_b16 v[196:197],v227 offset:1536\ns_waitcnt lgkmcnt(7)\nM v[160:163],a[40:43],v[204:207],v[160:163]\nds_read_b64_tr_b16 v[198:199],v227 offset:5632\nglobal_load_ushort v86,v237,s[8:9] offset:192\n" \
"M v[164:167],a[36:39],v[200:203],v[164:167]\nds_read_b64_tr_b16 v[212:213],v227 offset:9728\nds_read_b64_tr_b16 v[214:215],v227 offset:13824\nM v[164:167],a[44:47],v[204:207],v[164:167]\ns_waitcnt vmcnt(21)\nv_cmp_eq_u32 vcc,0,v248\nglobal_load_ushort v102,v235,s[20:21] offset:192\nM v[168:171],a[32:35],v[216:219],v[168:171]\n" \
"s_cbranch_vccnz .Lps7\n.Lpb7:\nbuffer_load_dword v248,v251,s[52:55],s79 offen sc1\ns_waitcnt lgkmcnt(4)\nM v[168:171],a[40:43],v[220:223],v[168:171]\nds_read_b64_tr_b16 v[200:201],v226 offset:32768\nds_read_b64_tr_b16 v[202:203],v226 offset:36864\nglobal_load_ushort v71,v236,s[8:9] offset:224\n" \
"M v[172:175],a[36:39],v[216:219],v[172:175]\nds_read_b64_tr_b16 v[204:205],v226 offset:40960\nds_read_b64_tr_b16 v[206:207],v226 offset:45056\nM v[172:175],a[44:47],v[220:223],v[172:175]\nglobal_load_ushort v87,v237,s[8:9] offset:224\nM v[176:179],a[32:35],v[192:195],v[176:179]\nds_read_b64_tr_b16 v[216:217],v227 offset:32768\nds_read_b64_tr_b16 v[218:219],v227 offset:36864\n" \
"M v[176:179],a[40:43],v[208:211],v[176:179]\nglobal_load_ushort v103,v235,s[20:21] offset:224\nM v[180:183],a[36:39],v[192:195],v[180:183]\nds_read_b64_tr_b16 v[220:221],v227 offset:40960\nds_read_b64_tr_b16 v[222:223],v227 offset:45056\nM v[180:183],a[44:47],v[208:211],v[180:183]\nbuffer_load_dwordx2 v[112:113],v239,s[52:55],0 offen offset:0 sc1\ns_waitcnt lgkmcnt(8)\n" \
"M v[184:187],a[32:35],v[196:199],v[184:187]\nds_read_b64_tr_b16 v[192:193],v226 offset:33280\nds_read_b64_tr_b16 v[194:195],v226 offset:37376\nM v[184:187],a[40:43],v[212:215],v[184:187]\nbuffer_load_dwordx2 v[114:115],v239,s[52:55],0 offen offset:2048 sc1\nM v[188:191],a[36:39],v[196:199],v[188:191]\nds_read_b64_tr_b16 v[208:209],v226 offset:41472\nds_read_b64_tr_b16 v[210:211],v226 offset:45568\n" \
"M v[188:191],a[44:47],v[212:215],v[188:191]\nbuffer_load_dwordx2 v[116:117],v239,s[52:55],s97 offen offset:0 sc1\ns_waitcnt lgkmcnt(6)\nM v[128:131],a[48:51],v[200:203],v[128:131]\nds_read_b64_tr_b16 v[196:197],v227 offset:33280\nds_read_b64_tr_b16 v[198:199],v227 offset:37376\nM v[128:131],a[56:59],v[204:207],v[128:131]\nbuffer_load_dwordx2 v[118:119],v239,s[52:55],s97 offen offset:2048 sc1\n" \
"M v[132:135],a[52:55],v[200:203],v[132:135]\nds_read_b64_tr_b16 v[212:213],v227 offset:41472\nds_read_b64_tr_b16 v[214:215],v227 offset:45568\nM v[132:135],a[60:63],v[204:207],v[132:135]\nds_read_b64_tr_b16 v[200:201],v226 offset:33792\nds_read_b64_tr_b16 v[202:203],v226 offset:37888\nM v[136:139],a[48:51],v[216:219],v[136:139]\nds_read_b64_tr_b16 v[204:205],v226 offset:41984\n" \
"s_waitcnt lgkmcnt(9)\nM v[136:139],a[56:59],v[220:223],v[136:139]\nds_read_b64_tr_b16 v[206:207],v226 offset:46080\ns_add_u32 s65,s10,s67\nM v[140:143],a[52:55],v[216:219],v[140:143]\nds_read_b64_tr_b16 v[216:217],v227 offset:33792\nds_read_b64_tr_b16 v[218:219],v227 offset:37888\nM v[140:143],a[60:63],v[220:223],v[140:143]\n" \
"ds_read_b64_tr_b16 v[220:221],v227 offset:41984\nds_read_b64_tr_b16 v[222:223],v227 offset:46080\nM v[144:147],a[48:51],v[192:195],v[144:147]\nv_cvt_pk_bf16_f32 v128,v128,v136\ns_waitcnt lgkmcnt(10)\nM v[144:147],a[56:59],v[208:211],v[144:147]\nv_cvt_pk_bf16_f32 v129,v129,v137\nv_cvt_pk_bf16_f32 v130,v130,v138\n" \
"M v[148:151],a[52:55],v[192:195],v[148:151]\nds_read_b64_tr_b16 v[192:193],v226 offset:34304\nds_read_b64_tr_b16 v[194:195],v226 offset:38400\nM v[148:151],a[60:63],v[208:211],v[148:151]\nds_read_b64_tr_b16 v[208:209],v226 offset:42496\nds_read_b64_tr_b16 v[210:211],v226 offset:46592\nM v[152:155],a[48:51],v[196:199],v[152:155]\nv_add_u32 v226,131072,v242\n" \
"s_waitcnt lgkmcnt(8)\nM v[152:155],a[56:59],v[212:215],v[152:155]\nv_cvt_pk_bf16_f32 v131,v131,v139\nv_min_u32 v136,-2,v128\nM v[156:159],a[52:55],v[196:199],v[156:159]\nds_read_b64_tr_b16 v[196:197],v227 offset:34304\nds_read_b64_tr_b16 v[198:199],v227 offset:38400\nM v[156:159],a[60:63],v[212:215],v[156:159]\n" \
"ds_read_b64_tr_b16 v[212:213],v227 offset:42496\nds_read_b64_tr_b16 v[214:215],v227 offset:46592\nM v[160:163],a[48:51],v[200:203],v[160:163]\nv_xor_b32 v227,32,v226\nv_cvt_pk_bf16_f32 v144,v144,v152\nM v[160:163],a[56:59],v[204:207],v[160:163]\nv_min_u32 v137,-2,v144\nv_cvt_pk_bf16_f32 v145,v145,v153\n" \
"M v[164:167],a[52:55],v[200:203],v[164:167]\nds_read_b64_tr_b16 v[200:201],v226 offset:0\nds_read_b64_tr_b16 v[202:203],v226 offset:4096\nM v[164:167],a[60:63],v[204:207],v[164:167]\nds_read_b64_tr_b16 v[204:205],v226 offset:8192\ns_waitcnt lgkmcnt(7)\nM v[168:171],a[48:51],v[216:219],v[168:171]\nds_read_b64_tr_b16 v[206:207],v226 offset:12288\n" \
"v_cvt_pk_bf16_f32 v146,v146,v154\nM v[168:171],a[56:59],v[220:223],v[168:171]\nv_cvt_pk_bf16_f32 v147,v147,v155\nv_min_u32 v152,-2,v129\nM v[172:175],a[52:55],v[216:219],v[172:175]\ns_waitcnt vmcnt(7)\nv_cmp_eq_u32 vcc,0,v248\nM v[172:175],a[60:63],v[220:223],v[172:175]\n" \
"s_cbranch_vccnz .Lps8\n.Lpb8:\nds_read_b64_tr_b16 v[216:217],v227 offset:0\nM v[176:179],a[48:51],v[192:195],v[176:179]\nbuffer_load_dwordx4 a[64:67],v240,s[52:55],s65 offen offset:0 sc1\nM v[176:179],a[56:59],v[208:211],v[176:179]\nds_read_b64_tr_b16 v[218:219],v227 offset:4096\nds_read_b64_tr_b16 v[220:221],v227 offset:8192\n" \
"M v[180:183],a[52:55],v[192:195],v[180:183]\nds_read_b64_tr_b16 v[222:223],v227 offset:12288\nds_read_b64_tr_b16 v[192:193],v226 offset:512\nM v[180:183],a[60:63],v[208:211],v[180:183]\nds_read_b64_tr_b16 v[194:195],v226 offset:4608\ns_waitcnt lgkmcnt(6)\nM v[184:187],a[48:51],v[196:199],v[184:187]\nbuffer_load_dwordx4 a[68:71],v240,s[52:55],s65 offen offset:1024 sc1\n" \
"M v[184:187],a[56:59],v[212:215],v[184:187]\nds_read_b64_tr_b16 v[208:209],v226 offset:8704\nds_read_b64_tr_b16 v[210:211],v226 offset:12800\nM v[188:191],a[52:55],v[196:199],v[188:191]\nds_read_b64_tr_b16 v[196:197],v227 offset:512\nds_read_b64_tr_b16 v[198:199],v227 offset:4608\nM v[188:191],a[60:63],v[212:215],v[188:191]\nds_read_b64_tr_b16 v[212:213],v227 offset:8704\n" \
"ds_read_b64_tr_b16 v[214:215],v227 offset:12800\nM v[8:11],v[0:3],v[200:203],0\nbuffer_load_dwordx4 a[72:75],v240,s[52:55],s65 offen offset:2048 sc1\nM v[8:11],v[16:19],v[204:207],v[8:11]\nv_cvt_pk_bf16_f32 v160,v160,v168\nv_cvt_pk_bf16_f32 v176,v176,v184\nM v[12:15],v[4:7],v[200:203],0\nds_read_b64_tr_b16 v[200:201],v226 offset:1024\n" \
"ds_read_b64_tr_b16 v[202:203],v226 offset:5120\nM v[12:15],v[20:23],v[204:207],v[12:15]\nds_read_b64_tr_b16 v[204:205],v226 offset:9216\ns_waitcnt lgkmcnt(9)\nM v[24:27],v[0:3],v[216:219],0\nbuffer_load_dwordx4 a[76:79],v240,s[52:55],s65 offen offset:3072 sc1\nM v[24:27],v[16:19],v[220:223],v[24:27]\nds_read_b64_tr_b16 v[206:207],v226 offset:13312\n" \
"s_add_u32 s65,s65,4096\nM v[28:31],v[4:7],v[216:219],0\nds_read_b64_tr_b16 v[216:217],v227 offset:1024\nds_read_b64_tr_b16 v[218:219],v227 offset:5120\nM v[28:31],v[20:23],v[220:223],v[28:31]\nds_read_b64_tr_b16 v[220:221],v227 offset:9216\nds_read_b64_tr_b16 v[222:223],v227 offset:13312\nM v[40:43],v[0:3],v[192:195],0\n" \
"buffer_load_dwordx4 a[80:83],v240,s[52:55],s65 offen offset:0 sc1\ns_waitcnt lgkmcnt(8)\nM v[40:43],v[16:19],v[208:211],v[40:43]\nv_min_u32 v138,-2,v160\nv_min_u32 v139,-2,v176\nM v[44:47],v[4:7],v[192:195],0\nds_read_b64_tr_b16 v[192:193],v226 offset:1536\nds_read_b64_tr_b16 v[194:195],v226 offset:5632\n" \
"M v[44:47],v[20:23],v[208:211],v[44:47]\nds_read_b64_tr_b16 v[208:209],v226 offset:9728\nds_read_b64_tr_b16 v[210:211],v226 offset:13824\nM v[56:59],v[0:3],v[196:199],0\nbuffer_load_dwordx4 a[84:87],v240,s[52:55],s65 offen offset:1024 sc1\nM v[56:59],v[16:19],v[212:215],v[56:59]\nv_cvt_pk_bf16_f32 v161,v161,v169\nv_cvt_pk_bf16_f32 v177,v177,v185\n" \
"M v[60:63],v[4:7],v[196:199],0\nbuffer_store_dwordx4 v[136:139],v253,s[52:55],s33 offen offset:0 sc1\nM v[60:63],v[20:23],v[212:215],v[60:63]\nds_read_b64_tr_b16 v[196:197],v227 offset:1536\ns_waitcnt lgkmcnt(5)\nM v[72:75],v[0:3],v[200:203],0\nbuffer_load_dwordx4 a[88:91],v240,s[52:55],s65 offen offset:2048 sc1\nM v[72:75],v[16:19],v[204:207],v[72:75]\n" \
"ds_read_b64_tr_b16 v[198:199],v227 offset:5632\nds_read_b64_tr_b16 v[212:213],v227 offset:9728\nM v[76:79],v[4:7],v[200:203],0\nds_read_b64_tr_b16 v[214:215],v227 offset:13824\nds_read_b64_tr_b16 v[200:201],v226 offset:16384\nM v[76:79],v[20:23],v[204:207],v[76:79]\nds_read_b64_tr_b16 v[202:203],v226 offset:20480\nds_read_b64_tr_b16 v[204:205],v226 offset:24576\n" \
"M v[88:91],v[0:3],v[216:219],0\nbuffer_load_dwordx4 a[92:95],v240,s[52:55],s65 offen offset:3072 sc1\nM v[88:91],v[16:19],v[220:223],v[88:91]\nds_read_b64_tr_b16 v[206:207],v226 offset:28672\ns_add_u32 s65,s11,s67\nM v[92:95],v[4:7],v[216:219],0\nds_read_b64_tr_b16 v[216:217],v227 offset:16384\nds_read_b64_tr_b16 v[218:219],v227 offset:20480\n" \
"M v[92:95],v[20:23],v[220:223],v[92:95]\nds_read_b64_tr_b16 v[220:221],v227 offset:24576\ns_waitcnt lgkmcnt(10)\nM v[104:107],v[0:3],v[192:195],0\nbuffer_load_dwordx4 a[96:99],v240,s[52:55],s65 offen offset:0 sc1\nM v[104:107],v[16:19],v[208:211],v[104:107]\nds_read_b64_tr_b16 v[222:223],v227 offset:28672\nv_min_u32 v153,-2,v145\n" \
"M v[108:111],v[4:7],v[192:195],0\nds_read_b64_tr_b16 v[192:193],v226 offset:16896\nds_read_b64_tr_b16 v[194:195],v226 offset:20992\nM v[108:111],v[20:23],v[208:211],v[108:111]\nv_min_u32 v154,-2,v161\ns_waitcnt lgkmcnt(7)\nM v[120:123],v[0:3],v[196:199],0\nbuffer_load_dwordx4 a[100:103],v240,s[52:55],s65 offen offset:1024 sc1\n" \
"M v[120:123],v[16:19],v[212:215],v[120:123]\nv_min_u32 v155,-2,v177\nds_read_b64_tr_b16 v[208:209],v226 offset:25088\nM v[124:127],v[4:7],v[196:199],0\nds_read_b64_tr_b16 v[210:211],v226 offset:29184\nbuffer_store_dwordx4 v[152:155],v253,s[52:55],s33 offen offset:1024 sc1\nM v[124:127],v[20:23],v[212:215],v[124:127]\nds_read_b64_tr_b16 v[196:197],v227 offset:16896\n" \
"ds_read_b64_tr_b16 v[198:199],v227 offset:20992\nM v[8:11],v[32:35],v[200:203],v[8:11]\nbuffer_load_dwordx4 a[104:107],v240,s[52:55],s65 offen offset:2048 sc1\ns_waitcnt lgkmcnt(6)\nM v[8:11],v[48:51],v[204:207],v[8:11]\nds_read_b64_tr_b16 v[212:213],v227 offset:25088\nds_read_b64_tr_b16 v[214:215],v227 offset:29184\nM v[12:15],v[36:39],v[200:203],v[12:15]\n" \
"ds_read_b64_tr_b16 v[200:201],v226 offset:17408\nds_read_b64_tr_b16 v[202:203],v226 offset:21504\nM v[12:15],v[52:55],v[204:207],v[12:15]\nds_read_b64_tr_b16 v[204:205],v226 offset:25600\nds_read_b64_tr_b16 v[206:207],v226 offset:29696\nM v[24:27],v[32:35],v[216:219],v[24:27]\nbuffer_load_dwordx4 a[108:111],v240,s[52:55],s65 offen offset:3072 sc1\nM v[24:27],v[48:51],v[220:223],v[24:27]\n" \
"s_add_u32 s65,s65,4096\nv_cvt_pk_bf16_f32 v162,v162,v170\nM v[28:31],v[36:39],v[216:219],v[28:31]\nds_read_b64_tr_b16 v[216:217],v227 offset:17408\nds_read_b64_tr_b16 v[218:219],v227 offset:21504\nM v[28:31],v[52:55],v[220:223],v[28:31]\nds_read_b64_tr_b16 v[220:221],v227 offset:25600\ns_waitcnt lgkmcnt(9)\n" \
"M v[40:43],v[32:35],v[192:195],v[40:43]\nbuffer_load_dwordx4 a[112:115],v240,s[52:55],s65 offen offset:0 sc1\nM v[40:43],v[48:51],v[208:211],v[40:43]\nds_read_b64_tr_b16 v[222:223],v227 offset:29696\nv_cvt_pk_bf16_f32 v163,v163,v171\nM v[44:47],v[36:39],v[192:195],v[44:47]\nds_read_b64_tr_b16 v[192:193],v226 offset:17920\nds_read_b64_tr_b16 v[194:195],v226 offset:22016\n" \
"M v[44:47],v[52:55],v[208:211],v[44:47]\nv_cvt_pk_bf16_f32 v178,v178,v186\nv_min_u32 v168,-2,v130\nM v[56:59],v[32:35],v[196:199],v[56:59]\nbuffer_load_dwordx4 a[116:119],v240,s[52:55],s65 offen offset:1024 sc1\ns_waitcnt lgkmcnt(6)\nM v[56:59],v[48:51],v[212:215],v[56:59]\nv_min_u32 v169,-2,v146\n" \
"v_min_u32 v170,-2,v162\nM v[60:63],v[36:39],v[196:199],v[60:63]\nv_min_u32 v171,-2,v178\nds_read_b64_tr_b16 v[208:209],v226 offset:26112\nds_read_b64_tr_b16 v[210:211],v226 offset:30208\nbuffer_store_dwordx4 v[168:171],v253,s[52:55],s33 offen offset:2048 sc1\nM v[60:63],v[52:55],v[212:215],v[60:63]\nds_read_b64_tr_b16 v[196:197],v227 offset:17920\n" \
"ds_read_b64_tr_b16 v[198:199],v227 offset:22016\nM v[72:75],v[32:35],v[200:203],v[72:75]\nbuffer_load_dwordx4 a[120:123],v240,s[52:55],s65 offen offset:2048 sc1\nM v[72:75],v[48:51],v[204:207],v[72:75]\nds_read_b64_tr_b16 v[212:213],v227 offset:26112\nds_read_b64_tr_b16 v[214:215],v227 offset:30208\nM v[76:79],v[36:39],v[200:203],v[76:79]\nv_add_u32 v226,0,v242\n" \
"ds_read_b64_tr_b16 v[200:201],v226 offset:0\nM v[76:79],v[52:55],v[204:207],v[76:79]\nds_read_b64_tr_b16 v[202:203],v226 offset:4096\ns_waitcnt lgkmcnt(8)\nM v[88:91],v[32:35],v[216:219],v[88:91]\nbuffer_load_dwordx4 a[124:127],v240,s[52:55],s65 offen offset:3072 sc1\nM v[88:91],v[48:51],v[220:223],v[88:91]\nds_read_b64_tr_b16 v[204:205],v226 offset:8192\n" \
"ds_read_b64_tr_b16 v[206:207],v226 offset:12288\nM v[92:95],v[36:39],v[216:219],v[92:95]\ns_add_u32 s65,s14,s67\nv_xor_b32 v227,32,v226\nM v[92:95],v[52:55],v[220:223],v[92:95]\nds_read_b64_tr_b16 v[216:217],v227 offset:0\nds_read_b64_tr_b16 v[218:219],v227 offset:4096\nM v[104:107],v[32:35],v[192:195],v[104:107]\n" \
"buffer_load_dwordx4 a[128:131],v240,s[52:55],s65 offen offset:0 sc1\ns_waitcnt lgkmcnt(6)\nM v[104:107],v[48:51],v[208:211],v[104:107]\nds_read_b64_tr_b16 v[220:221],v227 offset:8192\nds_read_b64_tr_b16 v[222:223],v227 offset:12288\nM v[108:111],v[36:39],v[192:195],v[108:111]\nv_cvt_pk_bf16_f32 v179,v179,v187\nds_read_b64_tr_b16 v[192:193],v226 offset:512\n" \
"M v[108:111],v[52:55],v[208:211],v[108:111]\nds_read_b64_tr_b16 v[194:195],v226 offset:4608\nv_min_u32 v184,-2,v131\nM v[120:123],v[32:35],v[196:199],v[120:123]\nbuffer_load_dwordx4 a[132:135],v240,s[52:55],s65 offen offset:1024 sc1\nM v[120:123],v[48:51],v[212:215],v[120:123]\nv_min_u32 v185,-2,v147\nv_min_u32 v186,-2,v163\n" \
"M v[124:127],v[36:39],v[196:199],v[124:127]\nv_min_u32 v187,-2,v179\nbuffer_store_dwordx4 v[184:187],v253,s[52:55],s33 offen offset:3072 sc1\nds_read_b64_tr_b16 v[208:209],v226 offset:8704\nds_read_b64_tr_b16 v[210:211],v226 offset:12800\nM v[124:127],v[52:55],v[212:215],v[124:127]\nds_read_b64_tr_b16 v[196:197],v227 offset:512\ns_waitcnt vmcnt(18) lgkmcnt(7)\n" \
"M v[8:11],a[64:67],v[200:203],v[8:11]\nbuffer_load_dwordx4 a[136:139],v240,s[52:55],s65 offen offset:2048 sc1\nM v[8:11],a[72:75],v[204:207],v[8:11]\nds_read_b64_tr_b16 v[198:199],v227 offset:4608\nds_read_b64_tr_b16 v[212:213],v227 offset:8704\nM v[12:15],a[68:71],v[200:203],v[12:15]\nds_read_b64_tr_b16 v[214:215],v227 offset:12800\nds_read_b64_tr_b16 v[200:201],v226 offset:1024\n" \
"M v[12:15],a[76:79],v[204:207],v[12:15]\nds_read_b64_tr_b16 v[202:203],v226 offset:5120\nds_read_b64_tr_b16 v[204:205],v226 offset:9216\nM v[24:27],a[64:67],v[216:219],v[24:27]\nbuffer_load_dwordx4 a[140:143],v240,s[52:55],s65 offen offset:3072 sc1\ns_waitcnt lgkmcnt(9)\nM v[24:27],a[72:75],v[220:223],v[24:27]\nds_read_b64_tr_b16 v[206:207],v226 offset:13312\n" \
"s_add_u32 s65,s65,4096\nM v[28:31],a[68:71],v[216:219],v[28:31]\nds_read_b64_tr_b16 v[216:217],v227 offset:1024\nds_read_b64_tr_b16 v[218:219],v227 offset:5120\nM v[28:31],a[76:79],v[220:223],v[28:31]\nds_read_b64_tr_b16 v[220:221],v227 offset:9216\nds_read_b64_tr_b16 v[222:223],v227 offset:13312\nM v[40:43],a[64:67],v[192:195],v[40:43]\n" \
"buffer_load_dwordx4 a[144:147],v240,s[52:55],s65 offen offset:0 sc1\ns_waitcnt lgkmcnt(9)\nM v[40:43],a[72:75],v[208:211],v[40:43]\nv_cvt_pk_bf16_f32 v132,v132,v140\nv_cvt_pk_bf16_f32 v133,v133,v141\nM v[44:47],a[68:71],v[192:195],v[44:47]\nv_cvt_pk_bf16_f32 v134,v134,v142\nv_cvt_pk_bf16_f32 v135,v135,v143\n" \
"M v[44:47],a[76:79],v[208:211],v[44:47]\nv_cvt_pk_bf16_f32 v148,v148,v156\nv_cvt_pk_bf16_f32 v164,v164,v172\nM v[56:59],a[64:67],v[196:199],v[56:59]\nbuffer_load_dwordx4 a[148:151],v240,s[52:55],s65 offen offset:1024 sc1\ns_waitcnt lgkmcnt(4)\nM v[56:59],a[72:75],v[212:215],v[56:59]\nv_cvt_pk_bf16_f32 v180,v180,v188\n" \
"ds_read_b64_tr_b16 v[192:193],v226 offset:1536\nds_read_b64_tr_b16 v[194:195],v226 offset:5632\nM v[60:63],a[68:71],v[196:199],v[60:63]\nv_min_u32 v140,-2,v132\nv_min_u32 v141,-2,v148\nv_min_u32 v142,-2,v164\nv_min_u32 v143,-2,v180\nbuffer_store_dwordx4 v[140:143],v253,s[52:55],s38 offen offset:0 sc1\n" \
"ds_read_b64_tr_b16 v[208:209],v226 offset:9728\nds_read_b64_tr_b16 v[210:211],v226 offset:13824\nM v[60:63],a[76:79],v[212:215],v[60:63]\nds_read_b64_tr_b16 v[196:197],v227 offset:1536\nds_read_b64_tr_b16 v[198:199],v227 offset:5632\nM v[72:75],a[64:67],v[200:203],v[72:75]\nbuffer_load_dwordx4 a[152:155],v240,s[52:55],s65 offen offset:2048 sc1\nM v[72:75],a[72:75],v[204:207],v[72:75]\n" \
"ds_read_b64_tr_b16 v[212:213],v227 offset:9728\nds_read_b64_tr_b16 v[214:215],v227 offset:13824\nM v[76:79],a[68:71],v[200:203],v[76:79]\nds_read_b64_tr_b16 v[200:201],v226 offset:32768\nds_read_b64_tr_b16 v[202:203],v226 offset:36864\nM v[76:79],a[76:79],v[204:207],v[76:79]\nds_read_b64_tr_b16 v[204:205],v226 offset:40960\ns_waitcnt lgkmcnt(9)\n" \
"M v[88:91],a[64:67],v[216:219],v[88:91]\nbuffer_load_dwordx4 a[156:159],v240,s[52:55],s65 offen offset:3072 sc1\nM v[88:91],a[72:75],v[220:223],v[88:91]\nds_read_b64_tr_b16 v[206:207],v226 offset:45056\nv_cvt_pk_bf16_f32 v149,v149,v157\nM v[92:95],a[68:71],v[216:219],v[92:95]\nds_read_b64_tr_b16 v[216:217],v227 offset:32768\nds_read_b64_tr_b16 v[218:219],v227 offset:36864\n" \
"M v[92:95],a[76:79],v[220:223],v[92:95]\nds_read_b64_tr_b16 v[220:221],v227 offset:40960\nds_read_b64_tr_b16 v[222:223],v227 offset:45056\nM v[104:107],a[64:67],v[192:195],v[104:107]\nv_cvt_pk_bf16_f32 v150,v150,v158\ns_waitcnt lgkmcnt(9)\nM v[104:107],a[72:75],v[208:211],v[104:107]\nv_cvt_pk_bf16_f32 v151,v151,v159\n" \
"v_cvt_pk_bf16_f32 v165,v165,v173\nM v[108:111],a[68:71],v[192:195],v[108:111]\nv_cvt_pk_bf16_f32 v181,v181,v189\nds_read_b64_tr_b16 v[192:193],v226 offset:33280\nM v[108:111],a[76:79],v[208:211],v[108:111]\nds_read_b64_tr_b16 v[194:195],v226 offset:37376\nv_min_u32 v156,-2,v133\nM v[120:123],a[64:67],v[196:199],v[120:123]\n" \
"v_min_u32 v157,-2,v149\ns_waitcnt lgkmcnt(6)\nM v[120:123],a[72:75],v[212:215],v[120:123]\nv_min_u32 v158,-2,v165\nv_min_u32 v159,-2,v181\nM v[124:127],a[68:71],v[196:199],v[124:127]\nbuffer_store_dwordx4 v[156:159],v253,s[52:55],s38 offen offset:1024 sc1\nds_read_b64_tr_b16 v[208:209],v226 offset:41472\n" \
"ds_read_b64_tr_b16 v[210:211],v226 offset:45568\nM v[124:127],a[76:79],v[212:215],v[124:127]\nds_read_b64_tr_b16 v[196:197],v227 offset:33280\ns_waitcnt vmcnt(24)\nM v[8:11],a[80:83],v[200:203],v[8:11]\nds_read_b64_tr_b16 v[198:199],v227 offset:37376\ns_waitcnt vmcnt(19)\nM v[8:11],a[88:91],v[204:207],v[8:11]\n" \
"ds_read_b64_tr_b16 v[212:213],v227 offset:41472\nds_read_b64_tr_b16 v[214:215],v227 offset:45568\nM v[12:15],a[84:87],v[200:203],v[12:15]\nds_read_b64_tr_b16 v[200:201],v226 offset:33792\nds_read_b64_tr_b16 v[202:203],v226 offset:37888\nM v[12:15],a[92:95],v[204:207],v[12:15]\nds_read_b64_tr_b16 v[204:205],v226 offset:41984\ns_waitcnt lgkmcnt(9)\n" \
"M v[24:27],a[80:83],v[216:219],v[24:27]\nds_read_b64_tr_b16 v[206:207],v226 offset:46080\nv_cvt_pk_bf16_f32 v166,v166,v174\nM v[24:27],a[88:91],v[220:223],v[24:27]\nv_cvt_pk_bf16_f32 v167,v167,v175\nv_cvt_pk_bf16_f32 v182,v182,v190\nM v[28:31],a[84:87],v[216:219],v[28:31]\nds_read_b64_tr_b16 v[216:217],v227 offset:33792\n" \
"ds_read_b64_tr_b16 v[218:219],v227 offset:37888\nM v[28:31],a[92:95],v[220:223],v[28:31]\nds_read_b64_tr_b16 v[220:221],v227 offset:41984\nds_read_b64_tr_b16 v[222:223],v227 offset:46080\nM v[40:43],a[80:83],v[192:195],v[40:43]\nv_min_u32 v172,-2,v134\ns_waitcnt lgkmcnt(9)\nM v[40:43],a[88:91],v[208:211],v[40:43]\n" \
"v_min_u32 v173,-2,v150\nv_min_u32 v174,-2,v166\nM v[44:47],a[84:87],v[192:195],v[44:47]\nds_read_b64_tr_b16 v[192:193],v226 offset:34304\nds_read_b64_tr_b16 v[194:195],v226 offset:38400\nM v[44:47],a[92:95],v[208:211],v[44:47]\nv_min_u32 v175,-2,v182\nds_read_b64_tr_b16 v[208:209],v226 offset:42496\n" \
"M v[56:59],a[80:83],v[196:199],v[56:59]\nds_read_b64_tr_b16 v[210:211],v226 offset:46592\ns_waitcnt lgkmcnt(8)\nM v[56:59],a[88:91],v[212:215],v[56:59]\nv_cvt_pk_bf16_f32 v183,v183,v191\nv_min_u32 v188,-2,v135\nM v[60:63],a[84:87],v[196:199],v[60:63]\nbuffer_store_dwordx4 v[172:175],v253,s[52:55],s38 offen offset:2048 sc1\n" \
"M v[60:63],a[92:95],v[212:215],v[60:63]\nds_read_b64_tr_b16 v[196:197],v227 offset:34304\nds_read_b64_tr_b16 v[198:199],v227 offset:38400\nM v[72:75],a[80:83],v[200:203],v[72:75]\nds_read_b64_tr_b16 v[212:213],v227 offset:42496\nds_read_b64_tr_b16 v[214:215],v227 offset:46592\nM v[72:75],a[88:91],v[204:207],v[72:75]\nv_min_u32 v189,-2,v151\n" \
"v_min_u32 v190,-2,v167\nM v[76:79],a[84:87],v[200:203],v[76:79]\nds_read_b64_tr_b16 v[200:201],v226 offset:16384\nds_read_b64_tr_b16 v[202:203],v226 offset:20480\nM v[76:79],a[92:95],v[204:207],v[76:79]\nds_read_b64_tr_b16 v[204:205],v226 offset:24576\ns_waitcnt lgkmcnt(7)\nM v[88:91],a[80:83],v[216:219],v[88:91]\n" \
"ds_read_b64_tr_b16 v[206:207],v226 offset:28672\nv_min_u32 v191,-2,v183\nM v[88:91],a[88:91],v[220:223],v[88:91]\nv_cmp_eq_u32 vcc,v228,v229\nv_and_b32 v228,1,v249\nM v[92:95],a[84:87],v[216:219],v[92:95]\nds_read_b64_tr_b16 v[216:217],v227 offset:16384\nds_read_b64_tr_b16 v[218:219],v227 offset:20480\n" \
"M v[92:95],a[92:95],v[220:223],v[92:95]\nds_read_b64_tr_b16 v[220:221],v227 offset:24576\nds_read_b64_tr_b16 v[222:223],v227 offset:28672\nM v[104:107],a[80:83],v[192:195],v[104:107]\nv_lshlrev_b32 v228,4,v228\nv_mov_b32 v229,0x3f80\nM v[104:107],a[88:91],v[208:211],v[104:107]\nv_lshlrev_b32 v228,v228,v229\n" \
"v_cndmask_b32 v228,0,v228,vcc\nM v[108:111],a[84:87],v[192:195],v[108:111]\nds_read_b64_tr_b16 v[192:193],v226 offset:16896\nds_read_b64_tr_b16 v[194:195],v226 offset:20992\nM v[108:111],a[92:95],v[208:211],v[108:111]\nds_read_b64_tr_b16 v[208:209],v226 offset:25088\ns_waitcnt lgkmcnt(7)\nM v[120:123],a[80:83],v[196:199],v[120:123]\n" \
"ds_read_b64_tr_b16 v[210:211],v226 offset:29184\nv_and_b32 v229,2,v249\nM v[120:123],a[88:91],v[212:215],v[120:123]\nv_cmp_eq_u32 vcc,0,v229\nv_and_b32 v244,15,v249\nM v[124:127],a[84:87],v[196:199],v[124:127]\nbuffer_store_dwordx4 v[188:191],v253,s[52:55],s38 offen offset:3072 sc1\nM v[124:127],a[92:95],v[212:215],v[124:127]\n" \
"ds_read_b64_tr_b16 v[196:197],v227 offset:16896\nds_read_b64_tr_b16 v[198:199],v227 offset:20992\nM v[8:11],a[96:99],v[200:203],v[8:11]\nds_read_b64_tr_b16 v[212:213],v227 offset:25088\ns_waitcnt vmcnt(16)\nM v[8:11],a[104:107],v[204:207],v[8:11]\nds_read_b64_tr_b16 v[214:215],v227 offset:29184\nv_cndmask_b32 v229,0,v228,vcc\n" \
"M v[12:15],a[100:103],v[200:203],v[12:15]\nds_read_b64_tr_b16 v[200:201],v226 offset:17408\nds_read_b64_tr_b16 v[202:203],v226 offset:21504\nM v[12:15],a[108:111],v[204:207],v[12:15]\nds_read_b64_tr_b16 v[204:205],v226 offset:25600\ns_waitcnt lgkmcnt(7)\nM v[24:27],a[96:99],v[216:219],v[24:27]\nds_read_b64_tr_b16 v[206:207],v226 offset:29696\n" \
"v_and_b32 v245,0x300,v240\nM v[24:27],a[104:107],v[220:223],v[24:27]\nv_lshlrev_b32 v246,11,v249\nv_cmp_lt_u32 vcc,7,v249\nM v[28:31],a[100:103],v[216:219],v[28:31]\nds_read_b64_tr_b16 v[216:217],v227 offset:17408\nds_read_b64_tr_b16 v[218:219],v227 offset:21504\nM v[28:31],a[108:111],v[220:223],v[28:31]\n" \
"ds_read_b64_tr_b16 v[220:221],v227 offset:25600\nds_read_b64_tr_b16 v[222:223],v227 offset:29696\nM v[40:43],a[96:99],v[192:195],v[40:43]\nv_mov_b32 v230,0x40000000\nv_lshl_or_b32 v244,v244,1,v245\nM v[40:43],a[104:107],v[208:211],v[40:43]\ns_add_u32 s70,s64,114688\nv_cndmask_b32 v246,v246,v230,vcc\n" \
"M v[44:47],a[100:103],v[192:195],v[44:47]\nds_read_b64_tr_b16 v[192:193],v226 offset:17920\nds_read_b64_tr_b16 v[194:195],v226 offset:22016\nM v[44:47],a[108:111],v[208:211],v[44:47]\nds_read_b64_tr_b16 v[208:209],v226 offset:26112\ns_waitcnt lgkmcnt(7)\nM v[56:59],a[96:99],v[196:199],v[56:59]\nds_read_b64_tr_b16 v[210:211],v226 offset:30208\n" \
"v_add_u32 v244,s70,v244\nM v[56:59],a[104:107],v[212:215],v[56:59]\nv_add_u32 v245,s70,v240\ns_lshl_b32 s80,s60,11\nM v[60:63],a[100:103],v[196:199],v[60:63]\nds_read_b64_tr_b16 v[196:197],v227 offset:17920\nds_read_b64_tr_b16 v[198:199],v227 offset:22016\nM v[60:63],a[108:111],v[212:215],v[60:63]\n" \
"ds_read_b64_tr_b16 v[212:213],v227 offset:26112\nds_read_b64_tr_b16 v[214:215],v227 offset:30208\nM v[72:75],a[96:99],v[200:203],v[72:75]\nv_and_b32 v230,3,v249\nv_lshrrev_b32 v231,2,v249\nM v[72:75],a[104:107],v[204:207],v[72:75]\nv_accvgpr_read_b32 v32,a176\nv_accvgpr_read_b32 v33,a177\n" \
"M v[76:79],a[100:103],v[200:203],v[76:79]\nds_read_b64_tr_b16 v[200:201],v226 offset:49152\nds_read_b64_tr_b16 v[202:203],v226 offset:53248\nM v[76:79],a[108:111],v[204:207],v[76:79]\nds_read_b64_tr_b16 v[204:205],v226 offset:57344\ns_waitcnt lgkmcnt(7)\nM v[88:91],a[96:99],v[216:219],v[88:91]\nds_read_b64_tr_b16 v[206:207],v226 offset:61440\n" \
"v_accvgpr_read_b32 v34,a178\nM v[88:91],a[104:107],v[220:223],v[88:91]\nv_accvgpr_read_b32 v35,a179\nv_accvgpr_read_b32 v36,a180\nM v[92:95],a[100:103],v[216:219],v[92:95]\nds_read_b64_tr_b16 v[216:217],v227 offset:49152\nds_read_b64_tr_b16 v[218:219],v227 offset:53248\nM v[92:95],a[108:111],v[220:223],v[92:95]\n" \
"ds_read_b64_tr_b16 v[220:221],v227 offset:57344\nds_read_b64_tr_b16 v[222:223],v227 offset:61440\nM v[104:107],a[96:99],v[192:195],v[104:107]\nv_accvgpr_read_b32 v37,a181\nv_accvgpr_read_b32 v38,a182\nM v[104:107],a[104:107],v[208:211],v[104:107]\nv_accvgpr_read_b32 v39,a183\ns_lshl3_add_u32 s37,s6,s75\n" \
"M v[108:111],a[100:103],v[192:195],v[108:111]\nds_read_b64_tr_b16 v[192:193],v226 offset:49664\nds_read_b64_tr_b16 v[194:195],v226 offset:53760\nM v[108:111],a[108:111],v[208:211],v[108:111]\nds_read_b64_tr_b16 v[208:209],v226 offset:57856\ns_waitcnt lgkmcnt(7)\nM v[120:123],a[96:99],v[196:199],v[120:123]\nds_read_b64_tr_b16 v[210:211],v226 offset:61952\n" \
"s_add_u32 s80,s80,557056\nM v[120:123],a[104:107],v[212:215],v[120:123]\nv_lshl_add_u32 v231,v230,5,v231\ns_lshl_b32 s70,s7,8\nM v[124:127],a[100:103],v[196:199],v[124:127]\nds_read_b64_tr_b16 v[196:197],v227 offset:49664\nds_read_b64_tr_b16 v[198:199],v227 offset:53760\nM v[124:127],a[108:111],v[212:215],v[124:127]\n" \
"ds_read_b64_tr_b16 v[212:213],v227 offset:57856\nds_read_b64_tr_b16 v[214:215],v227 offset:61952\nM v[8:11],a[112:115],v[200:203],v[8:11]\nv_add_f32 v32,1.0,v32\ns_waitcnt vmcnt(11)\nM v[8:11],a[120:123],v[204:207],v[8:11]\nv_add_f32 v33,1.0,v33\nv_add_f32 v34,1.0,v34\n" \
"M v[12:15],a[116:119],v[200:203],v[12:15]\nds_read_b64_tr_b16 v[200:201],v226 offset:50176\nds_read_b64_tr_b16 v[202:203],v226 offset:54272\nM v[12:15],a[124:127],v[204:207],v[12:15]\nds_read_b64_tr_b16 v[204:205],v226 offset:58368\ns_waitcnt lgkmcnt(7)\nM v[24:27],a[112:115],v[216:219],v[24:27]\nds_read_b64_tr_b16 v[206:207],v226 offset:62464\n" \
"v_add_f32 v35,1.0,v35\nM v[24:27],a[120:123],v[220:223],v[24:27]\nv_add_f32 v36,1.0,v36\nv_add_f32 v37,1.0,v37\nM v[28:31],a[116:119],v[216:219],v[28:31]\nds_read_b64_tr_b16 v[216:217],v227 offset:50176\nds_read_b64_tr_b16 v[218:219],v227 offset:54272\nM v[28:31],a[124:127],v[220:223],v[28:31]\n" \
"ds_read_b64_tr_b16 v[220:221],v227 offset:58368\nds_read_b64_tr_b16 v[222:223],v227 offset:62464\nM v[40:43],a[112:115],v[192:195],v[40:43]\nv_add_f32 v38,1.0,v38\nv_add_f32 v39,1.0,v39\nM v[40:43],a[120:123],v[208:211],v[40:43]\nv_add_f32 v116,1.0,v116\nv_add_f32 v117,1.0,v117\n" \
"M v[44:47],a[116:119],v[192:195],v[44:47]\nds_read_b64_tr_b16 v[192:193],v226 offset:50688\nds_read_b64_tr_b16 v[194:195],v226 offset:54784\nM v[44:47],a[124:127],v[208:211],v[44:47]\nds_read_b64_tr_b16 v[208:209],v226 offset:58880\ns_waitcnt lgkmcnt(7)\nM v[56:59],a[112:115],v[196:199],v[56:59]\nds_read_b64_tr_b16 v[210:211],v226 offset:62976\n" \
"v_add_u32 v226,65536,v242\nM v[56:59],a[120:123],v[212:215],v[56:59]\ns_add_u32 s37,s37,557056\nv_lshlrev_b32 v251,5,v249\nM v[60:63],a[116:119],v[196:199],v[60:63]\nds_read_b64_tr_b16 v[196:197],v227 offset:50688\nds_read_b64_tr_b16 v[198:199],v227 offset:54784\nM v[60:63],a[124:127],v[212:215],v[60:63]\n" \
"ds_read_b64_tr_b16 v[212:213],v227 offset:58880\nds_read_b64_tr_b16 v[214:215],v227 offset:62976\nM v[72:75],a[112:115],v[200:203],v[72:75]\nv_xor_b32 v227,32,v226\ns_or_b32 s80,s80,s75\nM v[72:75],a[120:123],v[204:207],v[72:75]\nv_lshl_add_u32 v248,v231,1,s70\nv_accvgpr_read_b32 v0,a160\n" \
"M v[76:79],a[116:119],v[200:203],v[76:79]\nds_read_b64_tr_b16 v[200:201],v226 offset:0\nds_read_b64_tr_b16 v[202:203],v226 offset:4096\nM v[76:79],a[124:127],v[204:207],v[76:79]\nds_read_b64_tr_b16 v[204:205],v226 offset:8192\ns_waitcnt lgkmcnt(7)\nM v[88:91],a[112:115],v[216:219],v[88:91]\nds_read_b64_tr_b16 v[206:207],v226 offset:12288\n" \
"v_accvgpr_read_b32 v1,a161\nM v[88:91],a[120:123],v[220:223],v[88:91]\nv_accvgpr_read_b32 v2,a162\nv_accvgpr_read_b32 v3,a163\nM v[92:95],a[116:119],v[216:219],v[92:95]\nds_read_b64_tr_b16 v[216:217],v227 offset:0\nds_read_b64_tr_b16 v[218:219],v227 offset:4096\nM v[92:95],a[124:127],v[220:223],v[92:95]\n" \
"ds_read_b64_tr_b16 v[220:221],v227 offset:8192\nds_read_b64_tr_b16 v[222:223],v227 offset:12288\nM v[104:107],a[112:115],v[192:195],v[104:107]\nv_accvgpr_read_b32 v4,a164\nv_accvgpr_read_b32 v5,a165\nM v[104:107],a[120:123],v[208:211],v[104:107]\nv_accvgpr_read_b32 v6,a166\nv_accvgpr_read_b32 v7,a167\n" \
"M v[108:111],a[116:119],v[192:195],v[108:111]\nds_read_b64_tr_b16 v[192:193],v226 offset:512\nds_read_b64_tr_b16 v[194:195],v226 offset:4608\nM v[108:111],a[124:127],v[208:211],v[108:111]\nds_read_b64_tr_b16 v[208:209],v226 offset:8704\ns_waitcnt lgkmcnt(7)\nM v[120:123],a[112:115],v[196:199],v[120:123]\nds_read_b64_tr_b16 v[210:211],v226 offset:12800\n" \
"v_accvgpr_read_b32 v16,a168\nM v[120:123],a[120:123],v[212:215],v[120:123]\nv_accvgpr_read_b32 v17,a169\nv_accvgpr_read_b32 v18,a170\nM v[124:127],a[116:119],v[196:199],v[124:127]\nds_read_b64_tr_b16 v[196:197],v227 offset:512\nds_read_b64_tr_b16 v[198:199],v227 offset:4608\nM v[124:127],a[124:127],v[212:215],v[124:127]\n" \
"ds_read_b64_tr_b16 v[212:213],v227 offset:8704\nds_read_b64_tr_b16 v[214:215],v227 offset:12800\nM v[8:11],a[128:131],v[200:203],v[8:11]\nv_accvgpr_read_b32 v19,a171\ns_waitcnt vmcnt(6)\nM v[8:11],a[136:139],v[204:207],v[8:11]\nv_accvgpr_read_b32 v20,a172\nv_accvgpr_read_b32 v21,a173\n" \
"M v[12:15],a[132:135],v[200:203],v[12:15]\nds_read_b64_tr_b16 v[200:201],v226 offset:1024\nds_read_b64_tr_b16 v[202:203],v226 offset:5120\nM v[12:15],a[140:143],v[204:207],v[12:15]\nds_read_b64_tr_b16 v[204:205],v226 offset:9216\ns_waitcnt lgkmcnt(7)\nM v[24:27],a[128:131],v[216:219],v[24:27]\nds_read_b64_tr_b16 v[206:207],v226 offset:13312\n" \
"v_accvgpr_read_b32 v22,a174\nM v[24:27],a[136:139],v[220:223],v[24:27]\nv_accvgpr_read_b32 v23,a175\nv_accvgpr_read_b32 v48,a184\nM v[28:31],a[132:135],v[216:219],v[28:31]\nds_read_b64_tr_b16 v[216:217],v227 offset:1024\nds_read_b64_tr_b16 v[218:219],v227 offset:5120\nM v[28:31],a[140:143],v[220:223],v[28:31]\n" \
"ds_read_b64_tr_b16 v[220:221],v227 offset:9216\nds_read_b64_tr_b16 v[222:223],v227 offset:13312\nM v[40:43],a[128:131],v[192:195],v[40:43]\nv_accvgpr_read_b32 v49,a185\nv_accvgpr_read_b32 v50,a186\nM v[40:43],a[136:139],v[208:211],v[40:43]\nv_accvgpr_read_b32 v51,a187\nv_accvgpr_read_b32 v52,a188\n" \
"M v[44:47],a[132:135],v[192:195],v[44:47]\nds_read_b64_tr_b16 v[192:193],v226 offset:1536\nds_read_b64_tr_b16 v[194:195],v226 offset:5632\nM v[44:47],a[140:143],v[208:211],v[44:47]\nds_read_b64_tr_b16 v[208:209],v226 offset:9728\ns_waitcnt lgkmcnt(7)\nM v[56:59],a[128:131],v[196:199],v[56:59]\nds_read_b64_tr_b16 v[210:211],v226 offset:13824\n" \
"v_accvgpr_read_b32 v53,a189\nM v[56:59],a[136:139],v[212:215],v[56:59]\nv_accvgpr_read_b32 v54,a190\nv_accvgpr_read_b32 v55,a191\nM v[60:63],a[132:135],v[196:199],v[60:63]\nds_read_b64_tr_b16 v[196:197],v227 offset:1536\nds_read_b64_tr_b16 v[198:199],v227 offset:5632\nM v[60:63],a[140:143],v[212:215],v[60:63]\n" \
"ds_read_b64_tr_b16 v[212:213],v227 offset:9728\nds_read_b64_tr_b16 v[214:215],v227 offset:13824\nM v[72:75],a[128:131],v[200:203],v[72:75]\nv_lshlrev_b32 v64,16,v64\nv_lshlrev_b32 v65,16,v65\nM v[72:75],a[136:139],v[204:207],v[72:75]\nv_lshlrev_b32 v66,16,v66\nv_lshlrev_b32 v67,16,v67\n" \
"M v[76:79],a[132:135],v[200:203],v[76:79]\nds_read_b64_tr_b16 v[200:201],v226 offset:32768\nds_read_b64_tr_b16 v[202:203],v226 offset:36864\nM v[76:79],a[140:143],v[204:207],v[76:79]\nds_read_b64_tr_b16 v[204:205],v226 offset:40960\ns_waitcnt lgkmcnt(7)\nM v[88:91],a[128:131],v[216:219],v[88:91]\nds_read_b64_tr_b16 v[206:207],v226 offset:45056\n" \
"v_lshlrev_b32 v68,16,v68\nM v[88:91],a[136:139],v[220:223],v[88:91]\nv_lshlrev_b32 v69,16,v69\nv_lshlrev_b32 v70,16,v70\nM v[92:95],a[132:135],v[216:219],v[92:95]\nds_read_b64_tr_b16 v[216:217],v227 offset:32768\nds_read_b64_tr_b16 v[218:219],v227 offset:36864\nM v[92:95],a[140:143],v[220:223],v[92:95]\n" \
"ds_read_b64_tr_b16 v[220:221],v227 offset:40960\nds_read_b64_tr_b16 v[222:223],v227 offset:45056\nM v[104:107],a[128:131],v[192:195],v[104:107]\nv_lshlrev_b32 v71,16,v71\nv_lshlrev_b32 v80,16,v80\nM v[104:107],a[136:139],v[208:211],v[104:107]\nv_lshlrev_b32 v81,16,v81\nv_lshlrev_b32 v82,16,v82\n" \
"M v[108:111],a[132:135],v[192:195],v[108:111]\nds_read_b64_tr_b16 v[192:193],v226 offset:33280\nds_read_b64_tr_b16 v[194:195],v226 offset:37376\nM v[108:111],a[140:143],v[208:211],v[108:111]\nds_read_b64_tr_b16 v[208:209],v226 offset:41472\ns_waitcnt lgkmcnt(7)\nM v[120:123],a[128:131],v[196:199],v[120:123]\nds_read_b64_tr_b16 v[210:211],v226 offset:45568\n" \
"v_lshlrev_b32 v83,16,v83\nM v[120:123],a[136:139],v[212:215],v[120:123]\nv_lshlrev_b32 v84,16,v84\nv_lshlrev_b32 v85,16,v85\nM v[124:127],a[132:135],v[196:199],v[124:127]\nds_read_b64_tr_b16 v[196:197],v227 offset:33280\nds_read_b64_tr_b16 v[198:199],v227 offset:37376\nM v[124:127],a[140:143],v[212:215],v[124:127]\n" \
"ds_read_b64_tr_b16 v[212:213],v227 offset:41472\nds_read_b64_tr_b16 v[214:215],v227 offset:45568\nM v[8:11],a[144:147],v[200:203],v[8:11]\nv_lshlrev_b32 v86,16,v86\ns_waitcnt vmcnt(3)\nM v[8:11],a[152:155],v[204:207],v[8:11]\nv_lshlrev_b32 v87,16,v87\nv_lshlrev_b32 v96,16,v96\n" \
"M v[12:15],a[148:151],v[200:203],v[12:15]\nds_read_b64_tr_b16 v[200:201],v226 offset:33792\nds_read_b64_tr_b16 v[202:203],v226 offset:37888\nM v[12:15],a[156:159],v[204:207],v[12:15]\nds_read_b64_tr_b16 v[204:205],v226 offset:41984\ns_waitcnt lgkmcnt(7)\nM v[24:27],a[144:147],v[216:219],v[24:27]\nds_read_b64_tr_b16 v[206:207],v226 offset:46080\n" \
"v_lshlrev_b32 v97,16,v97\nM v[24:27],a[152:155],v[220:223],v[24:27]\nv_lshlrev_b32 v98,16,v98\nv_lshlrev_b32 v99,16,v99\nM v[28:31],a[148:151],v[216:219],v[28:31]\nv_lshlrev_b32 v100,16,v100\nv_lshlrev_b32 v101,16,v101\nM v[28:31],a[156:159],v[220:223],v[28:31]\n" \
"v_lshlrev_b32 v102,16,v102\nv_lshlrev_b32 v103,16,v103\nM v[40:43],a[144:147],v[192:195],v[40:43]\nv_cvt_pk_bf16_f32 v32,0,v32\nv_cvt_pk_bf16_f32 v33,0,v33\nv_cvt_pk_bf16_f32 v34,0,v34\nv_cvt_pk_bf16_f32 v35,0,v35\nv_cvt_pk_bf16_f32 v36,0,v36\n" \
"v_cvt_pk_bf16_f32 v37,0,v37\nv_cvt_pk_bf16_f32 v38,0,v38\nv_cvt_pk_bf16_f32 v39,0,v39\nv_cvt_pk_bf16_f32 v116,0,v116\nv_cvt_pk_bf16_f32 v117,0,v117\nM v[40:43],a[152:155],v[208:211],v[40:43]\nds_read_b64_tr_b16 v[216:217],v227 offset:33792\nds_read_b64_tr_b16 v[218:219],v227 offset:37888\n" \
"M v[44:47],a[148:151],v[192:195],v[44:47]\nds_read_b64_tr_b16 v[220:221],v227 offset:41984\nds_read_b64_tr_b16 v[222:223],v227 offset:46080\nM v[44:47],a[156:159],v[208:211],v[44:47]\nds_read_b64_tr_b16 v[192:193],v226 offset:34304\ns_waitcnt lgkmcnt(5)\nM v[56:59],a[144:147],v[196:199],v[56:59]\nds_read_b64_tr_b16 v[194:195],v226 offset:38400\n" \
"ds_read_b64_tr_b16 v[208:209],v226 offset:42496\nM v[56:59],a[152:155],v[212:215],v[56:59]\nds_read_b64_tr_b16 v[210:211],v226 offset:46592\nv_cvt_pk_bf16_f32 v8,v8,v24\nM v[60:63],a[148:151],v[196:199],v[60:63]\nds_read_b64_tr_b16 v[196:197],v227 offset:34304\nds_read_b64_tr_b16 v[198:199],v227 offset:38400\nM v[60:63],a[156:159],v[212:215],v[60:63]\n" \
"ds_read_b64_tr_b16 v[212:213],v227 offset:42496\nds_read_b64_tr_b16 v[214:215],v227 offset:46592\nM v[72:75],a[144:147],v[200:203],v[72:75]\nv_cvt_pk_bf16_f32 v9,v9,v25\nv_cvt_pk_bf16_f32 v10,v10,v26\nM v[72:75],a[152:155],v[204:207],v[72:75]\nv_cvt_pk_bf16_f32 v11,v11,v27\nv_cvt_pk_bf16_f32 v40,v40,v56\n" \
"M v[76:79],a[148:151],v[200:203],v[76:79]\nv_cvt_pk_bf16_f32 v41,v41,v57\nv_cvt_pk_bf16_f32 v42,v42,v58\nM v[76:79],a[156:159],v[204:207],v[76:79]\nv_cvt_pk_bf16_f32 v43,v43,v59\ns_waitcnt lgkmcnt(6)\nM v[88:91],a[144:147],v[216:219],v[88:91]\nv_cvt_pk_bf16_f32 v12,v12,v28\n" \
"v_cvt_pk_bf16_f32 v13,v13,v29\nM v[88:91],a[152:155],v[220:223],v[88:91]\nv_cvt_pk_bf16_f32 v14,v14,v30\nv_cvt_pk_bf16_f32 v15,v15,v31\nM v[92:95],a[148:151],v[216:219],v[92:95]\nv_cvt_pk_bf16_f32 v44,v44,v60\nv_cvt_pk_bf16_f32 v45,v45,v61\nM v[92:95],a[156:159],v[220:223],v[92:95]\n" \
"v_cvt_pk_bf16_f32 v46,v46,v62\nv_cvt_pk_bf16_f32 v47,v47,v63\nM v[104:107],a[144:147],v[192:195],v[104:107]\nv_cvt_pk_bf16_f32 v72,v72,v88\ns_waitcnt lgkmcnt(0)\nM v[104:107],a[152:155],v[208:211],v[104:107]\nv_cvt_pk_bf16_f32 v73,v73,v89\nv_cvt_pk_bf16_f32 v74,v74,v90\n" \
"M v[108:111],a[148:151],v[192:195],v[108:111]\nv_cvt_pk_bf16_f32 v75,v75,v91\nv_cvt_pk_bf16_f32 v76,v76,v92\nM v[108:111],a[156:159],v[208:211],v[108:111]\nv_cvt_pk_bf16_f32 v77,v77,v93\nv_cvt_pk_bf16_f32 v78,v78,v94\nM v[120:123],a[144:147],v[196:199],v[120:123]\nv_cvt_pk_bf16_f32 v79,v79,v95\n" \
"v_xor_b32 v228,v228,v229\nM v[120:123],a[152:155],v[212:215],v[120:123]\ns_add_u32 s38,s78,16384\nv_min_u32 v24,-2,v8\nM v[124:127],a[148:151],v[196:199],v[124:127]\nv_min_u32 v25,-2,v40\nv_min_u32 v26,-2,v72\nM v[124:127],a[156:159],v[212:215],v[124:127]\n" \
"v_min_u32 v56,-2,v9\nv_min_u32 v57,-2,v41\nv_cvt_pk_bf16_f32 v104,v104,v120\nv_cvt_pk_bf16_f32 v105,v105,v121\nv_cvt_pk_bf16_f32 v106,v106,v122\nv_cvt_pk_bf16_f32 v107,v107,v123\nv_min_u32 v27,-2,v104\nv_min_u32 v58,-2,v73\n" \
"v_cvt_pk_bf16_f32 v108,v108,v124\nv_cvt_pk_bf16_f32 v109,v109,v125\nv_cvt_pk_bf16_f32 v110,v110,v126\nv_cvt_pk_bf16_f32 v111,v111,v127\nv_min_u32 v59,-2,v105\nv_min_u32 v88,-2,v10\nv_min_u32 v89,-2,v42\nv_min_u32 v90,-2,v74\n" \
"v_min_u32 v91,-2,v106\nv_min_u32 v120,-2,v11\nv_min_u32 v121,-2,v43\nv_min_u32 v122,-2,v75\nv_min_u32 v123,-2,v107\nv_min_u32 v28,-2,v12\nv_min_u32 v29,-2,v44\nv_min_u32 v30,-2,v76\n" \
"v_min_u32 v31,-2,v108\nv_min_u32 v60,-2,v13\nv_min_u32 v61,-2,v45\nv_min_u32 v62,-2,v77\nv_min_u32 v63,-2,v109\nv_min_u32 v92,-2,v14\nv_min_u32 v93,-2,v46\nv_min_u32 v94,-2,v78\n" \
"v_min_u32 v95,-2,v110\nv_min_u32 v124,-2,v15\nv_min_u32 v125,-2,v47\nv_min_u32 v126,-2,v79\nv_min_u32 v127,-2,v111\nv_accvgpr_write_b32 a192,v229\nv_accvgpr_write_b32 a193,v228\nv_accvgpr_write_b32 a198,v229\n" \
"v_accvgpr_write_b32 a199,v228\nbuffer_store_dwordx4 v[24:27],v253,s[52:55],s78 offen offset:0 sc1\nbuffer_store_dwordx4 v[56:59],v253,s[52:55],s78 offen offset:1024 sc1\nbuffer_store_dwordx4 v[88:91],v253,s[52:55],s78 offen offset:2048 sc1\nbuffer_store_dwordx4 v[120:123],v253,s[52:55],s78 offen offset:3072 sc1\nbuffer_store_dwordx4 v[28:31],v253,s[52:55],s38 offen offset:0 sc1\nbuffer_store_dwordx4 v[60:63],v253,s[52:55],s38 offen offset:1024 sc1\nbuffer_store_dwordx4 v[92:95],v253,s[52:55],s38 offen offset:2048 sc1\n" \
"buffer_store_dwordx4 v[124:127],v253,s[52:55],s38 offen offset:3072 sc1\n.Ltail:\ns_add_u32 s72,s95,0\nbuffer_load_dwordx4 v[160:163],v240,s[52:55],s72 offen sc1\ns_add_u32 s72,s95,2097152\nbuffer_load_dwordx4 v[164:167],v240,s[52:55],s72 offen sc1\ns_add_u32 s72,s95,4194304\nbuffer_load_dwordx4 v[168:171],v240,s[52:55],s72 offen sc1\n" \
"s_add_u32 s72,s95,6291456\nbuffer_load_dwordx4 v[172:175],v240,s[52:55],s72 offen sc1\ns_add_u32 s72,s95,8388608\nbuffer_load_dwordx4 v[176:179],v240,s[52:55],s72 offen sc1\ns_add_u32 s72,s95,10485760\nbuffer_load_dwordx4 v[180:183],v240,s[52:55],s72 offen sc1\ns_add_u32 s72,s95,12582912\nbuffer_load_dwordx4 v[184:187],v240,s[52:55],s72 offen sc1\n" \
"s_add_u32 s72,s95,14680064\nbuffer_load_dwordx4 v[188:191],v240,s[52:55],s72 offen sc1\n.Ltail2:\ns_add_u32 s72,s74,0\nbuffer_load_dwordx4 v[128:131],v240,s[52:55],s72 offen sc1\ns_add_u32 s72,s74,2097152\nbuffer_load_dwordx4 v[132:135],v240,s[52:55],s72 offen sc1\ns_add_u32 s72,s74,4194304\n" \
"buffer_load_dwordx4 v[136:139],v240,s[52:55],s72 offen sc1\ns_add_u32 s72,s74,6291456\nbuffer_load_dwordx4 v[140:143],v240,s[52:55],s72 offen sc1\ns_add_u32 s72,s74,8388608\nbuffer_load_dwordx4 v[144:147],v240,s[52:55],s72 offen sc1\ns_add_u32 s72,s74,10485760\nbuffer_load_dwordx4 v[148:151],v240,s[52:55],s72 offen sc1\ns_add_u32 s72,s74,12582912\n" \
"buffer_load_dwordx4 v[152:155],v240,s[52:55],s72 offen sc1\ns_add_u32 s72,s74,14680064\nbuffer_load_dwordx4 v[156:159],v240,s[52:55],s72 offen sc1\ns_cmp_eq_u32 s3,0\ns_cbranch_scc1 .Lnp\ns_mov_b32 s70,0x05040100\ns_mov_b32 s71,0x07060302\nv_accvgpr_read_b32 v228,a212\n" \
"v_accvgpr_read_b32 v229,a213\nv_accvgpr_read_b32 v230,a214\nv_accvgpr_read_b32 v231,a215\nv_perm_b32 v192,v230,v228,s70\nv_perm_b32 v193,v230,v228,s71\nv_perm_b32 v194,v231,v229,s70\nv_perm_b32 v195,v231,v229,s71\nv_accvgpr_read_b32 v228,a216\n" \
"v_accvgpr_read_b32 v229,a217\nv_accvgpr_read_b32 v230,a218\nv_accvgpr_read_b32 v231,a219\nv_perm_b32 v196,v230,v228,s70\nv_perm_b32 v197,v230,v228,s71\nv_perm_b32 v198,v231,v229,s70\nv_perm_b32 v199,v231,v229,s71\nv_accvgpr_read_b32 v228,a220\n" \
"v_accvgpr_read_b32 v229,a221\nv_accvgpr_read_b32 v230,a222\nv_accvgpr_read_b32 v231,a223\nv_perm_b32 v200,v230,v228,s70\nv_perm_b32 v201,v230,v228,s71\nv_perm_b32 v202,v231,v229,s70\nv_perm_b32 v203,v231,v229,s71\nv_accvgpr_read_b32 v228,a224\n" \
"v_accvgpr_read_b32 v229,a225\nv_accvgpr_read_b32 v230,a226\nv_accvgpr_read_b32 v231,a227\nv_perm_b32 v204,v230,v228,s70\nv_perm_b32 v205,v230,v228,s71\nv_perm_b32 v206,v231,v229,s70\nv_perm_b32 v207,v231,v229,s71\nv_accvgpr_read_b32 v228,a228\n" \
"v_accvgpr_read_b32 v229,a229\nv_accvgpr_read_b32 v230,a230\nv_accvgpr_read_b32 v231,a231\nv_perm_b32 v208,v230,v228,s70\nv_perm_b32 v209,v230,v228,s71\nv_perm_b32 v210,v231,v229,s70\nv_perm_b32 v211,v231,v229,s71\nv_accvgpr_read_b32 v228,a232\n" \
"v_accvgpr_read_b32 v229,a233\nv_accvgpr_read_b32 v230,a234\nv_accvgpr_read_b32 v231,a235\nv_perm_b32 v212,v230,v228,s70\nv_perm_b32 v213,v230,v228,s71\nv_perm_b32 v214,v231,v229,s70\nv_perm_b32 v215,v231,v229,s71\nv_accvgpr_read_b32 v228,a236\n" \
"v_accvgpr_read_b32 v229,a237\nv_accvgpr_read_b32 v230,a238\nv_accvgpr_read_b32 v231,a239\nv_perm_b32 v216,v230,v228,s70\nv_perm_b32 v217,v230,v228,s71\nv_perm_b32 v218,v231,v229,s70\nv_perm_b32 v219,v231,v229,s71\nv_accvgpr_read_b32 v228,a240\n" \
"v_accvgpr_read_b32 v229,a241\nv_accvgpr_read_b32 v230,a242\nv_accvgpr_read_b32 v231,a243\nv_perm_b32 v220,v230,v228,s70\nv_perm_b32 v221,v230,v228,s71\nv_perm_b32 v222,v231,v229,s70\nv_perm_b32 v223,v231,v229,s71\nv_and_b32 v228,3,v249\n" \
"v_lshrrev_b32 v229,4,v249\nv_and_b32 v230,1,v229\nv_lshrrev_b32 v229,1,v229\nv_lshlrev_b32 v230,5,v230\nv_lshl_or_b32 v230,v228,6,v230\nv_lshl_or_b32 v230,v229,8,v230\ns_lshl2_add_u32 s70,s7,84032\nv_add_u32 v252,s70,v230\n" \
"v_and_b32 v230,0xf0,v240\nv_add_u32 v253,84032,v230\n.Lnp:\n.Lrow1:\ns_waitcnt vmcnt(15)\nM v[72:75],a[192:195],v[160:163],0\nM v[76:79],a[196:199],v[160:163],0\ns_waitcnt vmcnt(14)\n" \
"M v[72:75],a[192:195],v[164:167],v[72:75]\nM v[76:79],a[196:199],v[164:167],v[76:79]\ns_waitcnt vmcnt(13)\nM v[72:75],a[192:195],v[168:171],v[72:75]\nM v[76:79],a[196:199],v[168:171],v[76:79]\ns_waitcnt vmcnt(12)\nM v[72:75],a[192:195],v[172:175],v[72:75]\nM v[76:79],a[196:199],v[172:175],v[76:79]\n" \
"s_waitcnt vmcnt(11)\nM v[72:75],a[192:195],v[176:179],v[72:75]\nM v[76:79],a[196:199],v[176:179],v[76:79]\ns_waitcnt vmcnt(10)\nM v[72:75],a[192:195],v[180:183],v[72:75]\nM v[76:79],a[196:199],v[180:183],v[76:79]\ns_waitcnt vmcnt(9)\nM v[72:75],a[192:195],v[184:187],v[72:75]\n" \
"M v[76:79],a[196:199],v[184:187],v[76:79]\ns_waitcnt vmcnt(8)\nM v[72:75],a[192:195],v[188:191],v[72:75]\nM v[76:79],a[196:199],v[188:191],v[76:79]\ns_sub_u32 s38,56622112,s95\ns_lshr_b32 s38,s38,5\ns_nop 4\nv_cvt_pk_bf16_f32 v72,0,v72\n" \
"v_cvt_pk_bf16_f32 v73,0,v73\nv_cvt_pk_bf16_f32 v74,0,v74\nv_cvt_pk_bf16_f32 v75,0,v75\nv_cvt_pk_bf16_f32 v76,0,v76\nv_cvt_pk_bf16_f32 v77,0,v77\nv_cvt_pk_bf16_f32 v78,0,v78\nv_cvt_pk_bf16_f32 v79,0,v79\nv_pk_mul_f32 v[72:73],v[72:73],v[0:1]\n" \
"v_pk_mul_f32 v[74:75],v[74:75],v[2:3]\nv_pk_mul_f32 v[76:77],v[76:77],v[4:5]\nv_pk_mul_f32 v[78:79],v[78:79],v[6:7]\nv_cvt_pk_bf16_f32 v72,0,v72\nv_cvt_pk_bf16_f32 v73,0,v73\nv_cvt_pk_bf16_f32 v74,0,v74\nv_cvt_pk_bf16_f32 v75,0,v75\nv_cvt_pk_bf16_f32 v76,0,v76\n" \
"v_cvt_pk_bf16_f32 v77,0,v77\nv_cvt_pk_bf16_f32 v78,0,v78\nv_cvt_pk_bf16_f32 v79,0,v79\nv_pk_add_f32 v[72:73],v[72:73],v[64:65]\nv_pk_add_f32 v[74:75],v[74:75],v[66:67]\nv_pk_add_f32 v[76:77],v[76:77],v[68:69]\nv_pk_add_f32 v[78:79],v[78:79],v[70:71]\nv_cvt_pk_bf16_f32 v72,0,v72\n" \
"v_cvt_pk_bf16_f32 v73,0,v73\nv_cvt_pk_bf16_f32 v74,0,v74\nv_cvt_pk_bf16_f32 v75,0,v75\nv_cvt_pk_bf16_f32 v76,0,v76\nv_cvt_pk_bf16_f32 v77,0,v77\nv_cvt_pk_bf16_f32 v78,0,v78\nv_cvt_pk_bf16_f32 v79,0,v79\nv_readfirstlane_b32 s70,v72\n" \
"s_nop 1\nv_subrev_f32 v104,s70,v72\nv_subrev_f32 v105,s70,v73\nv_subrev_f32 v106,s70,v74\nv_subrev_f32 v107,s70,v75\nv_subrev_f32 v108,s70,v76\nv_subrev_f32 v109,s70,v77\nv_subrev_f32 v110,s70,v78\n" \
"v_subrev_f32 v111,s70,v79\nv_pk_mul_f32 v[124:125],v[104:105],v[104:105]\nv_pk_add_f32 v[120:121],v[104:105],v[106:107]\nv_pk_fma_f32 v[124:125],v[106:107],v[106:107],v[124:125]\nv_pk_fma_f32 v[124:125],v[108:109],v[108:109],v[124:125]\nv_pk_fma_f32 v[124:125],v[110:111],v[110:111],v[124:125]\nv_pk_add_f32 v[108:109],v[108:109],v[110:111]\nv_pk_add_f32 v[120:121],v[120:121],v[108:109]\n" \
"v_add_f32 v120,v120,v121\nv_add_f32 v121,v124,v125\ns_nop 1\nv_permlane16_swap_b32 v120,v121\nv_add_f32 v120,v120,v121\nv_mov_b32 v121,v120\ns_nop 1\nv_permlane32_swap_b32 v120,v121\n" \
"v_add_f32 v120,v120,v121\ns_nop 1\nv_add_f32_dpp v120,v120,v120 quad_perm:[1,0,3,2] row_mask:0xf bank_mask:0xf\ns_nop 1\nv_add_f32_dpp v120,v120,v120 quad_perm:[2,3,0,1] row_mask:0xf bank_mask:0xf\ns_nop 1\nv_add_f32_dpp v120,v120,v120 row_half_mirror row_mask:0xf bank_mask:0xf\ns_nop 1\n" \
"v_add_f32_dpp v120,v120,v120 row_mirror row_mask:0xf bank_mask:0xf\nv_mul_f32 v120,0x3b000000,v120\nv_mov_b32 v121,v120\nv_cmp_u_f32 vcc,v120,v120\ns_nop 0\nv_permlane16_swap_b32 v120,v121\ns_cbranch_vccnz .Lrs1\n.Lrb1:\n" \
"v_fma_f32 v121,-v120,v120,v121\nv_add_f32 v122,s70,v120\nv_max_f32 v121,0,v121\nv_cvt_pk_bf16_f32 v122,0,v122\nv_cvt_pk_bf16_f32 v121,0,v121\nv_add_f32 v121,0x3727c5ac,v121\nv_cvt_pk_bf16_f32 v121,0,v121\nv_sqrt_f32 v121,v121\n" \
"v_pk_add_f32 v[88:89],v[72:73],v[122:123] op_sel_hi:[1,0] neg_lo:[0,1] neg_hi:[0,1]\nv_cvt_pk_bf16_f32 v121,0,v121\nv_rcp_f32 v123,v121\nv_cvt_pk_bf16_f32 v88,0,v88\nv_pk_add_f32 v[90:91],v[74:75],v[122:123] op_sel_hi:[1,0] neg_lo:[0,1] neg_hi:[0,1]\nv_pk_add_f32 v[92:93],v[76:77],v[122:123] op_sel_hi:[1,0] neg_lo:[0,1] neg_hi:[0,1]\nv_pk_add_f32 v[94:95],v[78:79],v[122:123] op_sel_hi:[1,0] neg_lo:[0,1] neg_hi:[0,1]\nv_cvt_pk_bf16_f32 v89,0,v89\n" \
"v_pk_mul_f32 v[88:89],v[88:89],v[122:123] op_sel:[0,1]\nv_cvt_pk_bf16_f32 v88,0,v88\nv_cvt_pk_bf16_f32 v89,0,v89\nv_pk_mul_f32 v[88:89],v[88:89],v[32:33]\nv_cvt_pk_bf16_f32 v88,0,v88\nv_cvt_pk_bf16_f32 v89,0,v89\nv_pk_add_f32 v[88:89],v[88:89],v[16:17]\nv_cvt_pk_bf16_f32 v88,0,v88\n" \
"v_cvt_pk_bf16_f32 v89,0,v89\nv_pk_mul_f32 v[124:125],v[88:89],v[96:97]\nv_cvt_pk_bf16_f32 v90,0,v90\nv_cvt_pk_bf16_f32 v91,0,v91\nv_pk_mul_f32 v[90:91],v[90:91],v[122:123] op_sel:[0,1]\nv_cvt_pk_bf16_f32 v90,0,v90\nv_cvt_pk_bf16_f32 v91,0,v91\nv_pk_mul_f32 v[90:91],v[90:91],v[34:35]\n" \
"v_cvt_pk_bf16_f32 v90,0,v90\nv_cvt_pk_bf16_f32 v91,0,v91\nv_pk_add_f32 v[90:91],v[90:91],v[18:19]\nv_cvt_pk_bf16_f32 v90,0,v90\nv_cvt_pk_bf16_f32 v91,0,v91\nv_pk_fma_f32 v[124:125],v[90:91],v[98:99],v[124:125]\nv_cvt_pk_bf16_f32 v92,0,v92\nv_cvt_pk_bf16_f32 v93,0,v93\n" \
"v_pk_mul_f32 v[92:93],v[92:93],v[122:123] op_sel:[0,1]\nv_cvt_pk_bf16_f32 v92,0,v92\nv_cvt_pk_bf16_f32 v93,0,v93\nv_pk_mul_f32 v[92:93],v[92:93],v[36:37]\nv_cvt_pk_bf16_f32 v92,0,v92\nv_cvt_pk_bf16_f32 v93,0,v93\nv_pk_add_f32 v[92:93],v[92:93],v[20:21]\nv_cvt_pk_bf16_f32 v92,0,v92\n" \
"v_cvt_pk_bf16_f32 v93,0,v93\nv_pk_fma_f32 v[124:125],v[92:93],v[100:101],v[124:125]\nv_cvt_pk_bf16_f32 v94,0,v94\nv_cvt_pk_bf16_f32 v95,0,v95\nv_pk_mul_f32 v[94:95],v[94:95],v[122:123] op_sel:[0,1]\nv_cvt_pk_bf16_f32 v94,0,v94\nv_cvt_pk_bf16_f32 v95,0,v95\nv_pk_mul_f32 v[94:95],v[94:95],v[38:39]\n" \
"v_cvt_pk_bf16_f32 v94,0,v94\nv_cvt_pk_bf16_f32 v95,0,v95\nv_pk_add_f32 v[94:95],v[94:95],v[22:23]\nv_cvt_pk_bf16_f32 v94,0,v94\nv_cvt_pk_bf16_f32 v95,0,v95\nv_pk_fma_f32 v[124:125],v[94:95],v[102:103],v[124:125]\nv_add_f32 v124,v124,v125\nv_mov_b32 v125,v124\n" \
"v_lshrrev_b32 v104,16,v121\nv_or_b32 v104,v104,v122\nv_permlane16_swap_b32 v124,v125\nv_add_f32 v124,v124,v125\nv_mov_b32 v125,v124\ns_nop 1\nv_permlane32_swap_b32 v124,v125\nv_add_f32 v124,v124,v125\n" \
"s_nop 1\nv_add_f32_dpp v124,v124,v124 quad_perm:[1,0,3,2] row_mask:0xf bank_mask:0xf\ns_nop 1\nv_add_f32_dpp v124,v124,v124 quad_perm:[2,3,0,1] row_mask:0xf bank_mask:0xf\ns_nop 1\nv_add_f32_dpp v124,v124,v124 row_half_mirror row_mask:0xf bank_mask:0xf\ns_nop 1\nv_add_f32_dpp v124,v124,v124 row_mirror row_mask:0xf bank_mask:0xf\n" \
"v_cvt_pk_bf16_f32 v124,0,v124\ns_nop 0\nv_readfirstlane_b32 s77,v124\ns_nop 0\ns_ashr_i32 s71,s77,31\ns_and_b32 s72,s77,0x7fffffff\ns_or_b32 s71,s71,0x80000000\ns_xor_b32 s77,s77,s71\n" \
"s_and_b32 s77,s77,0xffff0000\ns_cmp_gt_u32 s72,0x7f800000\ns_cselect_b32 s77,0,s77\ns_or_b32 s77,s77,s38\nv_mov_b32 v105,s77\nds_max_u64 v250,v[104:105]\n.Lrow0:\ns_waitcnt vmcnt(7)\n" \
"M v[8:11],a[192:195],v[128:131],0\nM v[12:15],a[196:199],v[128:131],0\ns_waitcnt vmcnt(6)\nM v[8:11],a[192:195],v[132:135],v[8:11]\nM v[12:15],a[196:199],v[132:135],v[12:15]\ns_waitcnt vmcnt(5)\nM v[8:11],a[192:195],v[136:139],v[8:11]\nM v[12:15],a[196:199],v[136:139],v[12:15]\n" \
"s_waitcnt vmcnt(4)\nM v[8:11],a[192:195],v[140:143],v[8:11]\nM v[12:15],a[196:199],v[140:143],v[12:15]\ns_waitcnt vmcnt(3)\nM v[8:11],a[192:195],v[144:147],v[8:11]\nM v[12:15],a[196:199],v[144:147],v[12:15]\ns_waitcnt vmcnt(2)\nM v[8:11],a[192:195],v[148:151],v[8:11]\n" \
"M v[12:15],a[196:199],v[148:151],v[12:15]\ns_waitcnt vmcnt(1)\nM v[8:11],a[192:195],v[152:155],v[8:11]\nM v[12:15],a[196:199],v[152:155],v[12:15]\ns_waitcnt vmcnt(0)\nM v[8:11],a[192:195],v[156:159],v[8:11]\nM v[12:15],a[196:199],v[156:159],v[12:15]\ns_sub_u32 s39,56622112,s74\n" \
"s_lshr_b32 s39,s39,5\ns_nop 4\nv_cvt_pk_bf16_f32 v8,0,v8\nv_cvt_pk_bf16_f32 v9,0,v9\nv_cvt_pk_bf16_f32 v10,0,v10\nv_cvt_pk_bf16_f32 v11,0,v11\nv_cvt_pk_bf16_f32 v12,0,v12\nv_cvt_pk_bf16_f32 v13,0,v13\n" \
"v_cvt_pk_bf16_f32 v14,0,v14\nv_cvt_pk_bf16_f32 v15,0,v15\nv_pk_mul_f32 v[8:9],v[8:9],v[0:1]\nv_pk_mul_f32 v[10:11],v[10:11],v[2:3]\nv_pk_mul_f32 v[12:13],v[12:13],v[4:5]\nv_pk_mul_f32 v[14:15],v[14:15],v[6:7]\nv_cvt_pk_bf16_f32 v8,0,v8\nv_cvt_pk_bf16_f32 v9,0,v9\n" \
"v_cvt_pk_bf16_f32 v10,0,v10\nv_cvt_pk_bf16_f32 v11,0,v11\nv_cvt_pk_bf16_f32 v12,0,v12\nv_cvt_pk_bf16_f32 v13,0,v13\nv_cvt_pk_bf16_f32 v14,0,v14\nv_cvt_pk_bf16_f32 v15,0,v15\nv_pk_add_f32 v[8:9],v[8:9],v[80:81]\nv_pk_add_f32 v[10:11],v[10:11],v[82:83]\n" \
"v_pk_add_f32 v[12:13],v[12:13],v[84:85]\nv_pk_add_f32 v[14:15],v[14:15],v[86:87]\nv_cvt_pk_bf16_f32 v8,0,v8\nv_cvt_pk_bf16_f32 v9,0,v9\nv_cvt_pk_bf16_f32 v10,0,v10\nv_cvt_pk_bf16_f32 v11,0,v11\nv_cvt_pk_bf16_f32 v12,0,v12\nv_cvt_pk_bf16_f32 v13,0,v13\n" \
"v_cvt_pk_bf16_f32 v14,0,v14\nv_cvt_pk_bf16_f32 v15,0,v15\nv_readfirstlane_b32 s70,v8\ns_nop 1\nv_subrev_f32 v40,s70,v8\nv_subrev_f32 v41,s70,v9\nv_subrev_f32 v42,s70,v10\nv_subrev_f32 v43,s70,v11\n" \
"v_subrev_f32 v44,s70,v12\nv_subrev_f32 v45,s70,v13\nv_subrev_f32 v46,s70,v14\nv_subrev_f32 v47,s70,v15\nv_pk_mul_f32 v[60:61],v[40:41],v[40:41]\nv_pk_add_f32 v[56:57],v[40:41],v[42:43]\nv_pk_fma_f32 v[60:61],v[42:43],v[42:43],v[60:61]\nv_pk_fma_f32 v[60:61],v[44:45],v[44:45],v[60:61]\n" \
"v_pk_fma_f32 v[60:61],v[46:47],v[46:47],v[60:61]\nv_pk_add_f32 v[44:45],v[44:45],v[46:47]\nv_pk_add_f32 v[56:57],v[56:57],v[44:45]\nv_add_f32 v56,v56,v57\nv_add_f32 v57,v60,v61\ns_nop 1\nv_permlane16_swap_b32 v56,v57\nv_add_f32 v56,v56,v57\n" \
"v_mov_b32 v57,v56\ns_nop 1\nv_permlane32_swap_b32 v56,v57\nv_add_f32 v56,v56,v57\ns_nop 1\nv_add_f32_dpp v56,v56,v56 quad_perm:[1,0,3,2] row_mask:0xf bank_mask:0xf\ns_nop 1\nv_add_f32_dpp v56,v56,v56 quad_perm:[2,3,0,1] row_mask:0xf bank_mask:0xf\n" \
"s_nop 1\nv_add_f32_dpp v56,v56,v56 row_half_mirror row_mask:0xf bank_mask:0xf\ns_nop 1\nv_add_f32_dpp v56,v56,v56 row_mirror row_mask:0xf bank_mask:0xf\nv_mul_f32 v56,0x3b000000,v56\nv_mov_b32 v57,v56\nv_cmp_u_f32 vcc,v56,v56\ns_nop 0\n" \
"v_permlane16_swap_b32 v56,v57\ns_cbranch_vccnz .Lrs0\n.Lrb0:\nbuffer_load_dwordx4 v[232:235],v251,s[52:55],s80 offen sc1\nbuffer_load_dwordx4 v[236:239],v251,s[52:55],s80 offen offset:16 sc1\nv_fma_f32 v57,-v56,v56,v57\nv_add_f32 v58,s70,v56\nv_max_f32 v57,0,v57\n" \
"v_cvt_pk_bf16_f32 v58,0,v58\nv_cvt_pk_bf16_f32 v57,0,v57\nv_add_f32 v57,0x3727c5ac,v57\nv_cvt_pk_bf16_f32 v57,0,v57\nv_sqrt_f32 v57,v57\nv_pk_add_f32 v[24:25],v[8:9],v[58:59] op_sel_hi:[1,0] neg_lo:[0,1] neg_hi:[0,1]\nv_cvt_pk_bf16_f32 v57,0,v57\nv_rcp_f32 v59,v57\n" \
"v_cvt_pk_bf16_f32 v24,0,v24\nv_pk_add_f32 v[26:27],v[10:11],v[58:59] op_sel_hi:[1,0] neg_lo:[0,1] neg_hi:[0,1]\nv_pk_add_f32 v[28:29],v[12:13],v[58:59] op_sel_hi:[1,0] neg_lo:[0,1] neg_hi:[0,1]\nv_pk_add_f32 v[30:31],v[14:15],v[58:59] op_sel_hi:[1,0] neg_lo:[0,1] neg_hi:[0,1]\nv_cvt_pk_bf16_f32 v25,0,v25\nv_pk_mul_f32 v[24:25],v[24:25],v[58:59] op_sel:[0,1]\nv_cvt_pk_bf16_f32 v24,0,v24\nv_cvt_pk_bf16_f32 v25,0,v25\n" \
"v_pk_mul_f32 v[24:25],v[24:25],v[32:33]\nv_cvt_pk_bf16_f32 v24,0,v24\nv_cvt_pk_bf16_f32 v25,0,v25\nv_pk_add_f32 v[24:25],v[24:25],v[16:17]\nv_cvt_pk_bf16_f32 v24,0,v24\nv_cvt_pk_bf16_f32 v25,0,v25\nv_pk_mul_f32 v[60:61],v[24:25],v[96:97]\nv_cvt_pk_bf16_f32 v26,0,v26\n" \
"v_cvt_pk_bf16_f32 v27,0,v27\nv_pk_mul_f32 v[26:27],v[26:27],v[58:59] op_sel:[0,1]\nv_cvt_pk_bf16_f32 v26,0,v26\nv_cvt_pk_bf16_f32 v27,0,v27\nv_pk_mul_f32 v[26:27],v[26:27],v[34:35]\nv_cvt_pk_bf16_f32 v26,0,v26\nv_cvt_pk_bf16_f32 v27,0,v27\nv_pk_add_f32 v[26:27],v[26:27],v[18:19]\n" \
"v_cvt_pk_bf16_f32 v26,0,v26\nv_cvt_pk_bf16_f32 v27,0,v27\nv_pk_fma_f32 v[60:61],v[26:27],v[98:99],v[60:61]\nv_cvt_pk_bf16_f32 v28,0,v28\nv_cvt_pk_bf16_f32 v29,0,v29\nv_pk_mul_f32 v[28:29],v[28:29],v[58:59] op_sel:[0,1]\nv_cvt_pk_bf16_f32 v28,0,v28\nv_cvt_pk_bf16_f32 v29,0,v29\n" \
"v_pk_mul_f32 v[28:29],v[28:29],v[36:37]\nv_cvt_pk_bf16_f32 v28,0,v28\nv_cvt_pk_bf16_f32 v29,0,v29\nv_pk_add_f32 v[28:29],v[28:29],v[20:21]\nv_cvt_pk_bf16_f32 v28,0,v28\nv_cvt_pk_bf16_f32 v29,0,v29\nv_pk_fma_f32 v[60:61],v[28:29],v[100:101],v[60:61]\nv_cvt_pk_bf16_f32 v30,0,v30\n" \
"v_cvt_pk_bf16_f32 v31,0,v31\nv_pk_mul_f32 v[30:31],v[30:31],v[58:59] op_sel:[0,1]\nv_cvt_pk_bf16_f32 v30,0,v30\nv_cvt_pk_bf16_f32 v31,0,v31\nv_pk_mul_f32 v[30:31],v[30:31],v[38:39]\nv_cvt_pk_bf16_f32 v30,0,v30\nv_cvt_pk_bf16_f32 v31,0,v31\nv_pk_add_f32 v[30:31],v[30:31],v[22:23]\n" \
"v_cvt_pk_bf16_f32 v30,0,v30\nv_cvt_pk_bf16_f32 v31,0,v31\nv_pk_fma_f32 v[60:61],v[30:31],v[102:103],v[60:61]\nv_add_f32 v60,v60,v61\nv_mov_b32 v61,v60\nv_lshrrev_b32 v40,16,v57\nv_or_b32 v40,v40,v58\nv_permlane16_swap_b32 v60,v61\n" \
"v_add_f32 v60,v60,v61\nv_mov_b32 v61,v60\ns_nop 1\nv_permlane32_swap_b32 v60,v61\nv_add_f32 v60,v60,v61\ns_nop 1\nv_add_f32_dpp v60,v60,v60 quad_perm:[1,0,3,2] row_mask:0xf bank_mask:0xf\ns_nop 1\n" \
"v_add_f32_dpp v60,v60,v60 quad_perm:[2,3,0,1] row_mask:0xf bank_mask:0xf\ns_nop 1\nv_add_f32_dpp v60,v60,v60 row_half_mirror row_mask:0xf bank_mask:0xf\ns_nop 1\nv_add_f32_dpp v60,v60,v60 row_mirror row_mask:0xf bank_mask:0xf\nv_cvt_pk_bf16_f32 v60,0,v60\ns_nop 0\nv_readfirstlane_b32 s76,v60\n" \
"s_nop 0\ns_ashr_i32 s71,s76,31\ns_and_b32 s72,s76,0x7fffffff\ns_or_b32 s71,s71,0x80000000\ns_xor_b32 s76,s76,s71\ns_and_b32 s76,s76,0xffff0000\ns_cmp_gt_u32 s72,0x7f800000\ns_cselect_b32 s76,0,s76\n" \
"s_or_b32 s76,s76,s39\nv_mov_b32 v41,s76\nds_max_u64 v250,v[40:41]\ns_waitcnt lgkmcnt(0)\ns_barrier\nds_read_b64 v[224:225],v250\ns_waitcnt lgkmcnt(0)\nbuffer_store_dwordx2 v[224:225],v246,s[52:55],s37 offen sc1\n" \
"v_readfirstlane_b32 s97,v225\ns_sub_u32 s95,s95,54525952\ns_sub_u32 s74,s74,54525952\ns_mov_b32 s94,0\ns_cmp_eq_u32 s97,s77\ns_cselect_b32 s65,s95,0x40000000\ns_cselect_b32 s95,0x40000000,s95\ns_cmp_eq_u32 s97,s76\n" \
"s_cselect_b32 s66,s74,0x40000000\ns_cselect_b32 s74,0x40000000,s74\ns_lshr_b32 s70,s6,7\ns_or_b32 s38,s7,s70\ns_cmp_lg_u32 s3,0\ns_waitcnt vmcnt(1)\ns_cbranch_scc1 .Lkp\nv_pk_mul_f32 v[88:89],v[88:89],v[48:49]\n" \
"v_pk_mul_f32 v[90:91],v[90:91],v[50:51]\nv_pk_mul_f32 v[92:93],v[92:93],v[52:53]\nv_pk_mul_f32 v[94:95],v[94:95],v[54:55]\nv_cvt_pk_bf16_f32 v88,0,v88\nv_cvt_pk_bf16_f32 v89,0,v89\nv_cvt_pk_bf16_f32 v90,0,v90\nv_cvt_pk_bf16_f32 v91,0,v91\nv_cvt_pk_bf16_f32 v92,0,v92\n" \
"v_cvt_pk_bf16_f32 v93,0,v93\nv_cvt_pk_bf16_f32 v94,0,v94\nv_cvt_pk_bf16_f32 v95,0,v95\nv_pk_add_f32 v[88:89],v[88:89],v[72:73]\nv_pk_add_f32 v[90:91],v[90:91],v[74:75]\nv_pk_add_f32 v[92:93],v[92:93],v[76:77]\nv_pk_add_f32 v[94:95],v[94:95],v[78:79]\nv_cvt_pk_bf16_f32 v88,0,v88\n" \
"ds_write_b16_d16_hi v244,v88 offset:0\nv_cvt_pk_bf16_f32 v89,0,v89\nds_write_b16_d16_hi v244,v89 offset:32\nv_cvt_pk_bf16_f32 v90,0,v90\nds_write_b16_d16_hi v244,v90 offset:64\nv_cvt_pk_bf16_f32 v91,0,v91\nds_write_b16_d16_hi v244,v91 offset:96\nv_cvt_pk_bf16_f32 v92,0,v92\n" \
"ds_write_b16_d16_hi v244,v92 offset:128\nv_cvt_pk_bf16_f32 v93,0,v93\nds_write_b16_d16_hi v244,v93 offset:160\nv_cvt_pk_bf16_f32 v94,0,v94\nds_write_b16_d16_hi v244,v94 offset:192\nv_cvt_pk_bf16_f32 v95,0,v95\nds_write_b16_d16_hi v244,v95 offset:224\nds_read_b128 v[64:67],v245 offset:0\n" \
"v_pk_mul_f32 v[24:25],v[24:25],v[48:49]\nv_pk_mul_f32 v[26:27],v[26:27],v[50:51]\nv_pk_mul_f32 v[28:29],v[28:29],v[52:53]\nv_pk_mul_f32 v[30:31],v[30:31],v[54:55]\nv_cvt_pk_bf16_f32 v24,0,v24\nv_cvt_pk_bf16_f32 v25,0,v25\nv_cvt_pk_bf16_f32 v26,0,v26\nv_cvt_pk_bf16_f32 v27,0,v27\n" \
"v_cvt_pk_bf16_f32 v28,0,v28\nv_cvt_pk_bf16_f32 v29,0,v29\nv_cvt_pk_bf16_f32 v30,0,v30\nv_cvt_pk_bf16_f32 v31,0,v31\nv_pk_add_f32 v[24:25],v[24:25],v[8:9]\nv_pk_add_f32 v[26:27],v[26:27],v[10:11]\nv_pk_add_f32 v[28:29],v[28:29],v[12:13]\nv_pk_add_f32 v[30:31],v[30:31],v[14:15]\n" \
"v_cvt_pk_bf16_f32 v24,0,v24\nds_write_b16_d16_hi v244,v24 offset:1024\nv_cvt_pk_bf16_f32 v25,0,v25\nds_write_b16_d16_hi v244,v25 offset:1056\nv_cvt_pk_bf16_f32 v26,0,v26\nds_write_b16_d16_hi v244,v26 offset:1088\nv_cvt_pk_bf16_f32 v27,0,v27\nds_write_b16_d16_hi v244,v27 offset:1120\n" \
"v_cvt_pk_bf16_f32 v28,0,v28\nds_write_b16_d16_hi v244,v28 offset:1152\nv_cvt_pk_bf16_f32 v29,0,v29\nds_write_b16_d16_hi v244,v29 offset:1184\nv_cvt_pk_bf16_f32 v30,0,v30\nds_write_b16_d16_hi v244,v30 offset:1216\nv_cvt_pk_bf16_f32 v31,0,v31\nds_write_b16_d16_hi v244,v31 offset:1248\n" \
"ds_read_b128 v[68:71],v245 offset:1024\ns_waitcnt lgkmcnt(0)\nbuffer_store_dwordx4 v[64:67],v240,s[44:47],s95 offen sc1\nbuffer_store_dwordx4 v[68:71],v240,s[44:47],s74 offen sc1\ns_cmp_eq_u32 s7,0\ns_cselect_b32 s70,1,0\ns_xor_b32 s71,s65,s66\ns_or_b32 s70,s70,s71\n" \
"s_cbranch_scc0 .Lfin\ns_lshr_b32 s70,s6,3\ns_and_b32 s70,s70,15\ns_lshl4_add_u32 s98,s60,s70\nv_lshrrev_b32 v242,4,v249\ns_lshl_b32 s71,s98,2\nv_add_u32 v242,s71,v242\nv_and_b32 v244,15,v242\n" \
"v_bfe_u32 v245,v242,4,3\nv_lshl_add_u32 v244,v244,3,v245\nv_and_b32 v245,0x180,v242\nv_add_lshl_u32 v243,v244,v245,1\nv_lshlrev_b32 v244,1,v243\ns_mov_b32 s70,4096\nv_lshlrev_b32 v242,1,v242\nbuffer_load_dword v62,v244,s[52:55],0 offen offset:0 sc1\n" \
"buffer_load_dword v63,v244,s[52:55],0 offen offset:2048 sc1\nbuffer_load_dword v126,v244,s[52:55],s70 offen offset:0 sc1\nbuffer_load_dword v127,v244,s[52:55],s70 offen offset:2048 sc1\n.Lkp:\ns_cmp_lg_u32 s7,0\ns_cbranch_scc1 .Lkl\nv_max3_u32 v228,v233,v235,v237\nv_min3_u32 v229,v233,v235,v237\n" \
"v_max_u32 v228,v228,v239\nv_min_u32 v229,v229,v239\nv_cmp_lt_u32 vcc,s94,v228\nv_cmp_eq_u32 s[90:91],0,v229\ns_cbranch_vccnz .Lnew\ns_cmp_eq_u64 s[90:91],0\ns_cbranch_scc1 .Lv0\ns_sub_u32 s73,s73,1\n" \
"s_cselect_b32 s73,0,s73\ns_cbranch_scc1 .Lv0\ns_sleep 3\nbuffer_load_dwordx4 v[232:235],v251,s[52:55],s80 offen sc1\nbuffer_load_dwordx4 v[236:239],v251,s[52:55],s80 offen offset:16 sc1\ns_waitcnt vmcnt(0)\ns_branch .Lkp\n.Lv0:\n" \
"v_mov_b32 v230,s96\ns_or_b32 s72,s94,2\nv_mov_b32 v231,s72\nds_write_b64 v250,v[230:231] offset:8\ns_branch .Lver\n.Lkl:\nds_read_b64 v[230:231],v250 offset:8\ns_mov_b32 s91,0\n" \
"s_waitcnt lgkmcnt(0)\nv_readfirstlane_b32 s72,v231\nv_readfirstlane_b32 s96,v230\ns_and_b32 s90,s72,2\ns_xor_b32 s72,s72,s90\ns_xor_b32 s90,s90,2\ns_cmp_lg_u32 s72,s94\ns_mov_b32 s94,s72\n" \
"s_cbranch_scc1 .Lsp\ns_cmp_eq_u32 s90,0\ns_cbranch_scc1 .Lver\ns_sub_u32 s73,s73,1\ns_cselect_b32 s73,0,s73\ns_cbranch_scc1 .Lver\ns_sleep 1\n.Lyield:\n" \
"s_branch .Lkl\n.Lnew:\nv_mov_b32 v230,v228\nv_cmp_eq_u32 vcc,v228,v233\ns_cmp_eq_u64 s[90:91],0\ns_cselect_b32 s71,2,0\nv_max_u32_dpp v230,v230,v230 quad_perm:[1,0,3,2] row_mask:0xf bank_mask:0xf\nv_cndmask_b32 v229,v238,v232,vcc\n" \
"v_cmp_eq_u32 vcc,v228,v235\nv_max_u32_dpp v230,v230,v230 quad_perm:[2,3,0,1] row_mask:0xf bank_mask:0xf\ns_nop 1\nv_max_u32_dpp v230,v230,v230 row_half_mirror row_mask:0xf bank_mask:0xf\nv_cndmask_b32 v229,v229,v234,vcc\nv_cmp_eq_u32 vcc,v228,v237\nv_max_u32_dpp v230,v230,v230 row_mirror row_mask:0xf bank_mask:0xf\nv_mov_b32 v231,v230\n" \
"v_cndmask_b32 v229,v229,v236,vcc\ns_nop 0\nv_permlane16_swap_b32 v230,v231\nv_max_u32 v230,v230,v231\nv_mov_b32 v231,v230\ns_nop 1\nv_permlane32_swap_b32 v230,v231\nv_max_u32 v230,v230,v231\n" \
"s_nop 0\nv_readfirstlane_b32 s94,v230\ns_nop 1\nv_cmp_eq_u32 vcc,s94,v228\ns_nop 0\ns_ff1_i32_b64 s72,vcc\nv_readlane_b32 s96,v229,s72\ns_nop 1\n" \
"v_mov_b32 v230,s96\ns_or_b32 s72,s94,s71\nv_mov_b32 v231,s72\nds_write_b64 v250,v[230:231] offset:8\n.Lsp:\ns_not_b32 s15,s94\ns_bfe_u32 s15,s15,0xb0005\ns_lshl_b32 s15,s15,10\n" \
"s_cmp_eq_u32 s3,0\ns_cbranch_scc1 .Lkp\ns_add_u32 s72,s15,54525952\nbuffer_load_dword v129,v241,s[52:55],s72 offen sc1\ns_add_u32 s72,s15,56623104\nbuffer_load_dword v131,v241,s[52:55],s72 offen sc1\ns_add_u32 s72,s15,58720256\nbuffer_load_dword v133,v241,s[52:55],s72 offen sc1\n" \
"s_add_u32 s72,s15,60817408\nbuffer_load_dword v135,v241,s[52:55],s72 offen sc1\ns_add_u32 s72,s15,62914560\nbuffer_load_dword v137,v241,s[52:55],s72 offen sc1\ns_add_u32 s72,s15,65011712\nbuffer_load_dword v139,v241,s[52:55],s72 offen sc1\ns_add_u32 s72,s15,67108864\nbuffer_load_dword v141,v241,s[52:55],s72 offen sc1\n" \
"s_add_u32 s72,s15,69206016\nbuffer_load_dword v143,v241,s[52:55],s72 offen sc1\nv_add_u32 v146,s15,v248\nglobal_load_ushort v144,v146,s[8:9]\nglobal_load_ushort v145,v146,s[8:9] offset:32\nbuffer_load_dwordx4 v[232:235],v251,s[52:55],s80 offen sc1\nbuffer_load_dwordx4 v[236:239],v251,s[52:55],s80 offen offset:16 sc1\nv_accvgpr_read_b32 v161,a200\n" \
"v_lshlrev_b32 v160,16,v161\nv_and_b32 v161,0xffff0000,v161\nv_accvgpr_read_b32 v163,a201\nv_lshlrev_b32 v162,16,v163\nv_and_b32 v163,0xffff0000,v163\nv_accvgpr_read_b32 v165,a202\nv_lshlrev_b32 v164,16,v165\nv_and_b32 v165,0xffff0000,v165\n" \
"v_accvgpr_read_b32 v167,a203\nv_lshlrev_b32 v166,16,v167\nv_and_b32 v167,0xffff0000,v167\nv_accvgpr_read_b32 v169,a204\nv_lshlrev_b32 v168,16,v169\nv_and_b32 v169,0xffff0000,v169\nv_accvgpr_read_b32 v171,a205\nv_lshlrev_b32 v170,16,v171\n" \
"v_and_b32 v171,0xffff0000,v171\nv_accvgpr_read_b32 v173,a206\nv_lshlrev_b32 v172,16,v173\nv_and_b32 v173,0xffff0000,v173\nv_accvgpr_read_b32 v175,a207\nv_lshlrev_b32 v174,16,v175\nv_and_b32 v175,0xffff0000,v175\nv_accvgpr_read_b32 v177,a208\n" \
"v_lshlrev_b32 v176,16,v177\nv_and_b32 v177,0xffff0000,v177\nv_accvgpr_read_b32 v179,a209\nv_lshlrev_b32 v178,16,v179\nv_and_b32 v179,0xffff0000,v179\nv_accvgpr_read_b32 v181,a210\nv_lshlrev_b32 v180,16,v181\nv_and_b32 v181,0xffff0000,v181\n" \
"v_accvgpr_read_b32 v183,a211\nv_lshlrev_b32 v182,16,v183\nv_and_b32 v183,0xffff0000,v183\nv_accvgpr_read_b32 v185,a252\nv_lshlrev_b32 v184,16,v185\nv_and_b32 v185,0xffff0000,v185\nv_accvgpr_read_b32 v187,a253\nv_lshlrev_b32 v186,16,v187\n" \
"v_and_b32 v187,0xffff0000,v187\nv_accvgpr_read_b32 v189,a254\nv_lshlrev_b32 v188,16,v189\nv_and_b32 v189,0xffff0000,v189\nv_accvgpr_read_b32 v191,a255\nv_lshlrev_b32 v190,16,v191\nv_and_b32 v191,0xffff0000,v191\ns_and_b32 s82,s96,0xffff0000\n" \
"s_lshl_b32 s83,s96,16\nv_rcp_f32 v148,s83\ns_waitcnt vmcnt(11)\nv_lshlrev_b32 v128,16,v129\nv_and_b32 v129,0xffff0000,v129\ns_waitcnt vmcnt(10)\nv_lshlrev_b32 v130,16,v131\nv_and_b32 v131,0xffff0000,v131\n" \
"v_pk_add_f32 v[128:129],v[128:129],v[130:131]\ns_waitcnt vmcnt(9)\nv_lshlrev_b32 v132,16,v133\nv_and_b32 v133,0xffff0000,v133\ns_waitcnt vmcnt(8)\nv_lshlrev_b32 v134,16,v135\nv_and_b32 v135,0xffff0000,v135\nv_pk_add_f32 v[132:133],v[132:133],v[134:135]\n" \
"v_pk_add_f32 v[128:129],v[128:129],v[132:133]\ns_waitcnt vmcnt(7)\nv_lshlrev_b32 v136,16,v137\nv_and_b32 v137,0xffff0000,v137\ns_waitcnt vmcnt(6)\nv_lshlrev_b32 v138,16,v139\nv_and_b32 v139,0xffff0000,v139\nv_pk_add_f32 v[136:137],v[136:137],v[138:139]\n" \
"s_waitcnt vmcnt(5)\nv_lshlrev_b32 v140,16,v141\nv_and_b32 v141,0xffff0000,v141\ns_waitcnt vmcnt(4)\nv_lshlrev_b32 v142,16,v143\nv_and_b32 v143,0xffff0000,v143\nv_pk_add_f32 v[140:141],v[140:141],v[142:143]\nv_pk_add_f32 v[136:137],v[136:137],v[140:141]\n" \
"v_pk_add_f32 v[128:129],v[128:129],v[136:137]\nv_cvt_pk_bf16_f32 v128,0,v128\nv_cvt_pk_bf16_f32 v129,0,v129\nv_pk_mul_f32 v[128:129],v[128:129],v[112:113]\nv_cvt_pk_bf16_f32 v128,0,v128\nv_cvt_pk_bf16_f32 v129,0,v129\ns_waitcnt vmcnt(3)\nv_lshlrev_b32 v144,16,v144\n" \
"s_waitcnt vmcnt(2)\nv_lshlrev_b32 v145,16,v145\nv_pk_add_f32 v[128:129],v[128:129],v[144:145]\nv_cvt_pk_bf16_f32 v128,0,v128\nv_cvt_pk_bf16_f32 v129,0,v129\nv_pk_add_f32 v[128:129],v[128:129],s[82:83] op_sel_hi:[1,0] neg_lo:[0,1] neg_hi:[0,1]\nv_cvt_pk_bf16_f32 v128,0,v128\nv_cvt_pk_bf16_f32 v129,0,v129\n" \
"v_pk_mul_f32 v[128:129],v[128:129],v[148:149] op_sel_hi:[1,0]\nv_cvt_pk_bf16_f32 v128,0,v128\nv_cvt_pk_bf16_f32 v129,0,v129\nv_pk_mul_f32 v[128:129],v[128:129],v[116:117]\nv_cvt_pk_bf16_f32 v128,0,v128\nv_cvt_pk_bf16_f32 v129,0,v129\nv_pk_add_f32 v[128:129],v[128:129],v[114:115]\nv_cvt_pk_bf16_f32 v128,v128,v129\n" \
"s_nop 1\nv_mov_b32_dpp v132,v128 quad_perm:[0,0,0,0] row_mask:0xf bank_mask:0xf\nv_mov_b32_dpp v133,v128 quad_perm:[1,1,1,1] row_mask:0xf bank_mask:0xf\nv_mov_b32_dpp v134,v128 quad_perm:[2,2,2,2] row_mask:0xf bank_mask:0xf\nv_mov_b32_dpp v135,v128 quad_perm:[3,3,3,3] row_mask:0xf bank_mask:0xf\nv_dot2_f32_bf16 v136,v192,v132,0\nv_dot2_f32_bf16 v137,v193,v132,0\nv_dot2_f32_bf16 v138,v194,v132,0\n" \
"v_dot2_f32_bf16 v139,v195,v132,0\nv_dot2_f32_bf16 v140,v208,v132,0\nv_dot2_f32_bf16 v141,v209,v132,0\nv_dot2_f32_bf16 v142,v210,v132,0\nv_dot2_f32_bf16 v143,v211,v132,0\nv_dot2_f32_bf16 v136,v196,v133,v136\nv_dot2_f32_bf16 v137,v197,v133,v137\nv_dot2_f32_bf16 v138,v198,v133,v138\n" \
"v_dot2_f32_bf16 v139,v199,v133,v139\nv_dot2_f32_bf16 v140,v212,v133,v140\nv_dot2_f32_bf16 v141,v213,v133,v141\nv_dot2_f32_bf16 v142,v214,v133,v142\nv_dot2_f32_bf16 v143,v215,v133,v143\nv_dot2_f32_bf16 v136,v200,v134,v136\nv_dot2_f32_bf16 v137,v201,v134,v137\nv_dot2_f32_bf16 v138,v202,v134,v138\n" \
"v_dot2_f32_bf16 v139,v203,v134,v139\nv_dot2_f32_bf16 v140,v216,v134,v140\nv_dot2_f32_bf16 v141,v217,v134,v141\nv_dot2_f32_bf16 v142,v218,v134,v142\nv_dot2_f32_bf16 v143,v219,v134,v143\nv_dot2_f32_bf16 v136,v204,v135,v136\nv_dot2_f32_bf16 v137,v205,v135,v137\nv_dot2_f32_bf16 v138,v206,v135,v138\n" \
"v_dot2_f32_bf16 v139,v207,v135,v139\nv_dot2_f32_bf16 v140,v220,v135,v140\nv_dot2_f32_bf16 v141,v221,v135,v141\nv_dot2_f32_bf16 v142,v222,v135,v142\nv_dot2_f32_bf16 v143,v223,v135,v143\nv_permlane32_swap_b32 v136,v140\nv_add_f32 v136,v136,v140\nv_permlane32_swap_b32 v137,v141\n" \
"v_add_f32 v137,v137,v141\nv_permlane32_swap_b32 v138,v142\nv_add_f32 v138,v138,v142\nv_permlane32_swap_b32 v139,v143\nv_add_f32 v139,v139,v143\nv_permlane16_swap_b32 v136,v138\nv_add_f32 v136,v136,v138\nv_permlane16_swap_b32 v137,v139\n" \
"v_add_f32 v137,v137,v139\nv_add_f32_dpp v136,v136,v136 row_ror:4 row_mask:0xf bank_mask:0xf\ns_nop 1\nv_add_f32_dpp v136,v136,v136 row_ror:8 row_mask:0xf bank_mask:0xf\nv_add_f32_dpp v137,v137,v137 row_ror:4 row_mask:0xf bank_mask:0xf\ns_nop 1\nv_add_f32_dpp v137,v137,v137 row_ror:8 row_mask:0xf bank_mask:0xf\ns_cmp_eq_u64 s[90:91],0\n" \
"s_cbranch_scc1 .Lver\ns_waitcnt vmcnt(0)\ns_branch .Lkp\n.Lver:\ns_cmp_eq_u32 s3,0\ns_cbranch_scc1 .Lnf\nds_write_b32 v252,v136\nds_write_b32 v252,v137 offset:16\n" \
"s_lshl_b32 s70,s2,6\ns_add_u32 s70,s70,98304\nv_lshlrev_b32 v241,1,v241\ns_waitcnt lgkmcnt(0)\ns_barrier\nds_read_b128 v[128:131],v253\nds_read_b128 v[132:135],v253 offset:256\ns_waitcnt lgkmcnt(1)\n" \
"v_pk_add_f32 v[128:129],v[128:129],v[130:131]\nv_add_f32 v128,v128,v129\nv_cvt_pk_bf16_f32 v128,0,v128\nv_mul_f32 v129,0xbfb8aa3b,v128\nv_exp_f32 v129,v129\ns_waitcnt lgkmcnt(0)\nv_pk_add_f32 v[132:133],v[132:133],v[134:135]\nv_add_f32 v129,1.0,v129\n" \
"v_rcp_f32 v129,v129\nv_add_f32 v132,v132,v133\nv_cvt_pk_bf16_f32 v132,0,v132\nv_mul_f32 v128,v128,v129\nv_cvt_pk_bf16_f32 v128,0,v128\nv_mul_f32 v128,v128,v132\nv_cvt_pk_bf16_f32 v128,0,v128\ns_nop 0\n" \
"v_readlane_b32 s8,v128,0\nv_readlane_b32 s9,v128,1\nv_readlane_b32 s10,v128,2\nv_readlane_b32 s11,v128,3\nv_readlane_b32 s12,v128,4\nv_readlane_b32 s13,v128,5\nv_readlane_b32 s14,v128,6\nv_readlane_b32 s15,v128,7\n" \
"v_readlane_b32 s16,v128,8\nv_readlane_b32 s17,v128,9\nv_readlane_b32 s18,v128,10\nv_readlane_b32 s19,v128,11\nv_readlane_b32 s20,v128,12\nv_readlane_b32 s21,v128,13\nv_readlane_b32 s22,v128,14\nv_readlane_b32 s23,v128,15\n" \
"v_pk_mul_f32 v[136:137],v[160:161],s[8:9] op_sel_hi:[1,0]\nv_pk_mul_f32 v[138:139],v[162:163],s[8:9] op_sel:[0,1]\nv_pk_fma_f32 v[136:137],v[164:165],s[10:11],v[136:137] op_sel_hi:[1,0,1]\nv_pk_fma_f32 v[138:139],v[166:167],s[10:11],v[138:139] op_sel:[0,1,0]\nv_pk_fma_f32 v[136:137],v[168:169],s[12:13],v[136:137] op_sel_hi:[1,0,1]\nv_pk_fma_f32 v[138:139],v[170:171],s[12:13],v[138:139] op_sel:[0,1,0]\nv_pk_fma_f32 v[136:137],v[172:173],s[14:15],v[136:137] op_sel_hi:[1,0,1]\nv_pk_fma_f32 v[138:139],v[174:175],s[14:15],v[138:139] op_sel:[0,1,0]\n" \
"v_pk_fma_f32 v[136:137],v[176:177],s[16:17],v[136:137] op_sel_hi:[1,0,1]\nv_pk_fma_f32 v[138:139],v[178:179],s[16:17],v[138:139] op_sel:[0,1,0]\nv_pk_fma_f32 v[136:137],v[180:181],s[18:19],v[136:137] op_sel_hi:[1,0,1]\nv_pk_fma_f32 v[138:139],v[182:183],s[18:19],v[138:139] op_sel:[0,1,0]\nv_pk_fma_f32 v[136:137],v[184:185],s[20:21],v[136:137] op_sel_hi:[1,0,1]\nv_pk_fma_f32 v[138:139],v[186:187],s[20:21],v[138:139] op_sel:[0,1,0]\nv_pk_fma_f32 v[136:137],v[188:189],s[22:23],v[136:137] op_sel_hi:[1,0,1]\nv_pk_fma_f32 v[138:139],v[190:191],s[22:23],v[138:139] op_sel:[0,1,0]\n" \
"v_pk_add_f32 v[136:137],v[136:137],v[138:139]\nv_min_u32 v136,-2,v136\nv_min_u32 v137,-2,v137\nbuffer_store_dwordx2 v[136:137],v241,s[52:55],s70 offen sc1\nv_pk_mul_f32 v[88:89],v[88:89],v[48:49]\nv_pk_mul_f32 v[90:91],v[90:91],v[50:51]\nv_pk_mul_f32 v[92:93],v[92:93],v[52:53]\nv_pk_mul_f32 v[94:95],v[94:95],v[54:55]\n" \
"v_cvt_pk_bf16_f32 v88,0,v88\nv_cvt_pk_bf16_f32 v89,0,v89\nv_cvt_pk_bf16_f32 v90,0,v90\nv_cvt_pk_bf16_f32 v91,0,v91\nv_cvt_pk_bf16_f32 v92,0,v92\nv_cvt_pk_bf16_f32 v93,0,v93\nv_cvt_pk_bf16_f32 v94,0,v94\nv_cvt_pk_bf16_f32 v95,0,v95\n" \
"v_pk_add_f32 v[88:89],v[88:89],v[72:73]\nv_pk_add_f32 v[90:91],v[90:91],v[74:75]\nv_pk_add_f32 v[92:93],v[92:93],v[76:77]\nv_pk_add_f32 v[94:95],v[94:95],v[78:79]\nv_cvt_pk_bf16_f32 v88,0,v88\nds_write_b16_d16_hi v244,v88 offset:0\nv_cvt_pk_bf16_f32 v89,0,v89\nds_write_b16_d16_hi v244,v89 offset:32\n" \
"v_cvt_pk_bf16_f32 v90,0,v90\nds_write_b16_d16_hi v244,v90 offset:64\nv_cvt_pk_bf16_f32 v91,0,v91\nds_write_b16_d16_hi v244,v91 offset:96\nv_cvt_pk_bf16_f32 v92,0,v92\nds_write_b16_d16_hi v244,v92 offset:128\nv_cvt_pk_bf16_f32 v93,0,v93\nds_write_b16_d16_hi v244,v93 offset:160\n" \
"v_cvt_pk_bf16_f32 v94,0,v94\nds_write_b16_d16_hi v244,v94 offset:192\nv_cvt_pk_bf16_f32 v95,0,v95\nds_write_b16_d16_hi v244,v95 offset:224\nds_read_b128 v[64:67],v245 offset:0\nv_pk_mul_f32 v[24:25],v[24:25],v[48:49]\nv_pk_mul_f32 v[26:27],v[26:27],v[50:51]\nv_pk_mul_f32 v[28:29],v[28:29],v[52:53]\n" \
"v_pk_mul_f32 v[30:31],v[30:31],v[54:55]\nv_cvt_pk_bf16_f32 v24,0,v24\nv_cvt_pk_bf16_f32 v25,0,v25\nv_cvt_pk_bf16_f32 v26,0,v26\nv_cvt_pk_bf16_f32 v27,0,v27\nv_cvt_pk_bf16_f32 v28,0,v28\nv_cvt_pk_bf16_f32 v29,0,v29\nv_cvt_pk_bf16_f32 v30,0,v30\n" \
"v_cvt_pk_bf16_f32 v31,0,v31\nv_pk_add_f32 v[24:25],v[24:25],v[8:9]\nv_pk_add_f32 v[26:27],v[26:27],v[10:11]\nv_pk_add_f32 v[28:29],v[28:29],v[12:13]\nv_pk_add_f32 v[30:31],v[30:31],v[14:15]\nv_cvt_pk_bf16_f32 v24,0,v24\nds_write_b16_d16_hi v244,v24 offset:1024\nv_cvt_pk_bf16_f32 v25,0,v25\n" \
"ds_write_b16_d16_hi v244,v25 offset:1056\nv_cvt_pk_bf16_f32 v26,0,v26\nds_write_b16_d16_hi v244,v26 offset:1088\nv_cvt_pk_bf16_f32 v27,0,v27\nds_write_b16_d16_hi v244,v27 offset:1120\nv_cvt_pk_bf16_f32 v28,0,v28\nds_write_b16_d16_hi v244,v28 offset:1152\nv_cvt_pk_bf16_f32 v29,0,v29\n" \
"ds_write_b16_d16_hi v244,v29 offset:1184\nv_cvt_pk_bf16_f32 v30,0,v30\nds_write_b16_d16_hi v244,v30 offset:1216\nv_cvt_pk_bf16_f32 v31,0,v31\nds_write_b16_d16_hi v244,v31 offset:1248\nds_read_b128 v[68:71],v245 offset:1024\ns_waitcnt lgkmcnt(0)\nbuffer_store_dwordx4 v[64:67],v240,s[44:47],s95 offen sc1\n" \
"buffer_store_dwordx4 v[68:71],v240,s[44:47],s74 offen sc1\n.Lnf:\ns_cmp_eq_u32 s94,s97\ns_cbranch_scc1 .Lwin\nbuffer_store_dwordx4 v[64:67],v240,s[44:47],s65 offen sc1\nbuffer_store_dwordx4 v[68:71],v240,s[44:47],s66 offen sc1\n.Lwin:\ns_cmp_lg_u32 s38,0\n" \
"s_cbranch_scc1 .Lfin\ns_add_u32 s72,s15,54525952\nbuffer_load_ushort v128,v243,s[52:55],s72 offen sc1\ns_add_u32 s72,s15,56623104\nbuffer_load_ushort v129,v243,s[52:55],s72 offen sc1\ns_add_u32 s72,s15,58720256\nbuffer_load_ushort v130,v243,s[52:55],s72 offen sc1\ns_add_u32 s72,s15,60817408\n" \
"buffer_load_ushort v131,v243,s[52:55],s72 offen sc1\ns_add_u32 s72,s15,62914560\nbuffer_load_ushort v132,v243,s[52:55],s72 offen sc1\ns_add_u32 s72,s15,65011712\nbuffer_load_ushort v133,v243,s[52:55],s72 offen sc1\ns_add_u32 s72,s15,67108864\nbuffer_load_ushort v134,v243,s[52:55],s72 offen sc1\ns_add_u32 s72,s15,69206016\n" \
"buffer_load_ushort v135,v243,s[52:55],s72 offen sc1\nv_add_u32 v242,s15,v242\nglobal_load_ushort v144,v242,s[8:9]\nv_min_u32 v168,55,v249\nv_lshlrev_b32 v168,11,v168\ns_lshl4_add_u32 s70,s98,98304\ns_add_u32 s71,s70,114688\ns_and_b32 s82,s96,0xffff0000\n" \
"s_lshl_b32 s83,s96,16\nv_rcp_f32 v148,s83\ns_ashr_i32 s37,s94,31\ns_not_b32 s37,s37\ns_or_b32 s37,s37,0x80000000\ns_xor_b32 s37,s37,s94\ns_and_b32 s37,s37,0xffff0000\nv_mov_b32 v149,0xbfb8aa3b\n" \
"v_mul_f32 v149,s37,v149\nv_exp_f32 v149,v149\nv_and_b32 v150,15,v249\nv_add_f32 v149,1.0,v149\nv_rcp_f32 v149,v149\nv_cmp_eq_u32 vcc,0,v150\nv_cvt_pk_bf16_f32 v149,0,v149\nv_mov_b32 v150,0x40000000\n" \
"v_cndmask_b32 v150,v150,v242,vcc\nv_cmp_gt_u32 s[90:91],56,v249\ns_waitcnt vmcnt(8)\nv_lshlrev_b32 v128,16,v128\ns_waitcnt vmcnt(7)\nv_lshlrev_b32 v129,16,v129\nv_add_f32 v128,v128,v129\ns_waitcnt vmcnt(6)\n" \
"v_lshlrev_b32 v130,16,v130\ns_waitcnt vmcnt(5)\nv_lshlrev_b32 v131,16,v131\nv_add_f32 v130,v130,v131\nv_add_f32 v128,v128,v130\ns_waitcnt vmcnt(4)\nv_lshlrev_b32 v132,16,v132\ns_waitcnt vmcnt(3)\n" \
"v_lshlrev_b32 v133,16,v133\nv_add_f32 v132,v132,v133\ns_waitcnt vmcnt(2)\nv_lshlrev_b32 v134,16,v134\ns_waitcnt vmcnt(1)\nv_lshlrev_b32 v135,16,v135\nv_add_f32 v134,v134,v135\nv_add_f32 v132,v132,v134\n" \
"v_add_f32 v128,v128,v132\nv_cvt_pk_bf16_f32 v128,0,v128\nv_mul_f32 v128,v128,v62\nv_cvt_pk_bf16_f32 v128,0,v128\ns_waitcnt vmcnt(0)\nv_add_f32 v126,1.0,v126\nv_cvt_pk_bf16_f32 v126,0,v126\nv_lshlrev_b32 v144,16,v144\n" \
"v_add_f32 v128,v128,v144\nv_cvt_pk_bf16_f32 v128,0,v128\nv_subrev_f32 v129,s82,v128\nv_cvt_pk_bf16_f32 v129,0,v129\nv_mul_f32 v129,v129,v148\nv_cvt_pk_bf16_f32 v129,0,v129\nv_mul_f32 v129,v129,v126\nv_cvt_pk_bf16_f32 v129,0,v129\n" \
"v_add_f32 v129,v129,v63\nv_cvt_pk_bf16_f32 v129,0,v129\n.Lp2:\nbuffer_load_dwordx4 v[160:163],v168,s[52:55],s70 offen sc1\nbuffer_load_dwordx4 v[164:167],v168,s[52:55],s71 offen sc1\ns_waitcnt vmcnt(0)\nv_max3_u32 v228,v160,v161,v162\nv_max3_u32 v229,v163,v164,v165\n" \
"v_max3_u32 v228,v228,v166,v167\nv_max_u32 v228,v228,v229\nv_pk_add_f32 v[160:161],v[160:161],v[164:165]\nv_cmp_eq_u32 vcc,-1,v228\nv_pk_add_f32 v[162:163],v[162:163],v[166:167]\ns_sub_u32 s73,s73,1\ns_cselect_b32 s73,0,s73\ns_cselect_b64 vcc,0,vcc\n" \
"s_cmp_lg_u64 vcc,0\ns_cselect_b32 s72,0x8000000,0\nv_cndmask_b32 v160,0,v160,s[90:91]\nv_cndmask_b32 v161,0,v161,s[90:91]\nv_cndmask_b32 v162,0,v162,s[90:91]\nv_cndmask_b32 v163,0,v163,s[90:91]\ns_nop 0\nv_permlane32_swap_b32 v160,v162\n" \
"v_permlane32_swap_b32 v161,v163\nv_pk_add_f32 v[160:161],v[160:161],v[162:163]\ns_nop 1\nv_permlane16_swap_b32 v160,v161\nv_add_f32 v160,v160,v161\ns_nop 1\nv_add_f32_dpp v160,v160,v160 quad_perm:[1,0,3,2] row_mask:0xf bank_mask:0xf\ns_nop 1\n" \
"v_add_f32_dpp v160,v160,v160 quad_perm:[2,3,0,1] row_mask:0xf bank_mask:0xf\ns_nop 1\nv_add_f32_dpp v160,v160,v160 row_half_mirror row_mask:0xf bank_mask:0xf\ns_nop 1\nv_add_f32_dpp v160,v160,v160 row_mirror row_mask:0xf bank_mask:0xf\nv_cvt_pk_bf16_f32 v160,0,v160\nv_mul_f32 v160,v160,v149\nv_cvt_pk_bf16_f32 v160,0,v160\n" \
"v_add_f32 v160,v160,v129\nv_cvt_pk_bf16_f32 v160,0,v160\nv_mul_f32 v160,v160,v127\nv_cvt_pk_bf16_f32 v160,0,v160\nv_add_f32 v160,v160,v128\nv_cvt_pk_bf16_f32 v160,0,v160\nbuffer_store_short_d16_hi v160,v150,s[44:47],s72 offen sc1\n.Lp2s:\n" \
"s_cbranch_scc1 .Lp2\ns_branch .Lfin\n.Lhs:\ns_sub_u32 s73,s73,1\ns_cselect_b32 s73,0,s73\ns_cbranch_scc1 .Lhb\ns_sleep 2\nbuffer_load_dwordx2 v[166:167],v165,s[52:55],s80 offen sc1\n" \
"s_waitcnt vmcnt(0)\nv_xor_b32 v166,v166,v230\nv_xor_b32 v167,v167,v231\nv_or_b32 v166,v166,v167\nv_cmp_ne_u32 vcc,0,v166\ns_nop 0\ns_cbranch_vccnz .Lhs\ns_branch .Lhb\n" \
".Lps0:\ns_sub_u32 s73,s73,1\ns_cselect_b32 s73,0,s73\ns_cbranch_scc1 .Lpb0\ns_sleep 1\nbuffer_load_dword v248,v228,s[52:55],s80 offen sc1\ns_waitcnt vmcnt(0)\nv_cmp_eq_u32 vcc,0,v248\n" \
"s_nop 0\ns_cbranch_vccnz .Lps0\ns_branch .Lpb0\n.Lps1:\ns_sub_u32 s73,s73,1\ns_cselect_b32 s73,0,s73\ns_cbranch_scc1 .Lpb1\ns_sleep 1\n" \
"buffer_load_dword v248,v228,s[52:55],s80 offen sc1\ns_waitcnt vmcnt(0)\nv_cmp_eq_u32 vcc,0,v248\ns_nop 0\ns_cbranch_vccnz .Lps1\ns_branch .Lpb1\n.Lps2:\ns_sub_u32 s73,s73,1\n" \
"s_cselect_b32 s73,0,s73\ns_cbranch_scc1 .Lpb2\ns_sleep 1\nbuffer_load_dword v248,v251,s[52:55],s80 offen sc1\ns_waitcnt vmcnt(0)\nv_cmp_eq_u32 vcc,0,v248\ns_nop 0\ns_cbranch_vccnz .Lps2\n" \
"s_branch .Lpb2\n.Lps3:\ns_sub_u32 s73,s73,1\ns_cselect_b32 s73,0,s73\ns_cbranch_scc1 .Lpb3\ns_sleep 1\nbuffer_load_dword v248,v251,s[52:55],s80 offen sc1\ns_waitcnt vmcnt(0)\n" \
"v_cmp_eq_u32 vcc,0,v248\ns_nop 0\ns_cbranch_vccnz .Lps3\ns_branch .Lpb3\n.Lps4:\ns_sub_u32 s73,s73,1\ns_cselect_b32 s73,0,s73\ns_cbranch_scc1 .Lpb4\n" \
"s_sleep 1\nbuffer_load_dword v248,v251,s[52:55],s80 offen sc1\ns_waitcnt vmcnt(0)\nv_cmp_eq_u32 vcc,0,v248\ns_nop 0\ns_cbranch_vccnz .Lps4\ns_branch .Lpb4\n.Lps5:\n" \
"s_sub_u32 s73,s73,1\ns_cselect_b32 s73,0,s73\ns_cbranch_scc1 .Lpb5\ns_sleep 1\nbuffer_load_dword v248,v251,s[52:55],s80 offen sc1\ns_waitcnt vmcnt(0)\nv_cmp_eq_u32 vcc,0,v248\ns_nop 0\n" \
"s_cbranch_vccnz .Lps5\ns_branch .Lpb5\n.Lx01:\ns_cmp_eq_u32 s62,0\ns_cbranch_scc1 .Ltrans\ns_branch .Ldf\n.Lps6:\ns_sub_u32 s73,s73,1\n" \
"s_cselect_b32 s73,0,s73\ns_cbranch_scc1 .Lpb6\ns_sleep 1\nbuffer_load_dword v248,v251,s[52:55],s80 offen sc1\ns_waitcnt vmcnt(0)\nv_and_b32 v248,s17,v248\nv_cmp_eq_u32 vcc,0,v248\ns_nop 0\n" \
"s_cbranch_vccnz .Lps6\ns_branch .Lpb6\n.Lps7:\ns_sub_u32 s73,s73,1\ns_cselect_b32 s73,0,s73\ns_cbranch_scc1 .Lpb7\ns_sleep 1\nbuffer_load_dword v248,v254,s[52:55],s80 offen sc1\n" \
"s_waitcnt vmcnt(0)\nv_cmp_eq_u32 vcc,0,v248\ns_nop 0\ns_cbranch_vccnz .Lps7\ns_branch .Lpb7\n.Lps8:\ns_sub_u32 s73,s73,1\ns_cselect_b32 s73,0,s73\n" \
"s_cbranch_scc1 .Lpb8\ns_sleep 1\nbuffer_load_dword v248,v251,s[52:55],s79 offen sc1\ns_waitcnt vmcnt(0)\nv_cmp_eq_u32 vcc,0,v248\ns_nop 0\ns_cbranch_vccnz .Lps8\ns_branch .Lpb8\n" \
".Lrs1:\ns_mov_b32 s71,0\n.Lrl1:\ns_mov_b32 s97,0\nv_max3_u32 v124,v160,v161,v162\nv_max3_u32 v124,v124,v163,v163\nv_cmp_eq_u32 vcc,-1,v124\ns_add_u32 s72,s95,0\n" \
"s_cbranch_vccz .Lrz10\ns_mov_b64 exec,vcc\ns_mov_b32 s97,1\nbuffer_load_dwordx4 v[160:163],v240,s[52:55],s72 offen sc1\ns_mov_b64 exec,-1\n.Lrz10:\nv_max3_u32 v124,v164,v165,v166\nv_max3_u32 v124,v124,v167,v167\n" \
"v_cmp_eq_u32 vcc,-1,v124\ns_add_u32 s72,s95,2097152\ns_cbranch_vccz .Lrz11\ns_mov_b64 exec,vcc\ns_mov_b32 s97,1\nbuffer_load_dwordx4 v[164:167],v240,s[52:55],s72 offen sc1\ns_mov_b64 exec,-1\n.Lrz11:\n" \
"v_max3_u32 v124,v168,v169,v170\nv_max3_u32 v124,v124,v171,v171\nv_cmp_eq_u32 vcc,-1,v124\ns_add_u32 s72,s95,4194304\ns_cbranch_vccz .Lrz12\ns_mov_b64 exec,vcc\ns_mov_b32 s97,1\nbuffer_load_dwordx4 v[168:171],v240,s[52:55],s72 offen sc1\n" \
"s_mov_b64 exec,-1\n.Lrz12:\nv_max3_u32 v124,v172,v173,v174\nv_max3_u32 v124,v124,v175,v175\nv_cmp_eq_u32 vcc,-1,v124\ns_add_u32 s72,s95,6291456\ns_cbranch_vccz .Lrz13\ns_mov_b64 exec,vcc\n" \
"s_mov_b32 s97,1\nbuffer_load_dwordx4 v[172:175],v240,s[52:55],s72 offen sc1\ns_mov_b64 exec,-1\n.Lrz13:\nv_max3_u32 v124,v176,v177,v178\nv_max3_u32 v124,v124,v179,v179\nv_cmp_eq_u32 vcc,-1,v124\ns_add_u32 s72,s95,8388608\n" \
"s_cbranch_vccz .Lrz14\ns_mov_b64 exec,vcc\ns_mov_b32 s97,1\nbuffer_load_dwordx4 v[176:179],v240,s[52:55],s72 offen sc1\ns_mov_b64 exec,-1\n.Lrz14:\nv_max3_u32 v124,v180,v181,v182\nv_max3_u32 v124,v124,v183,v183\n" \
"v_cmp_eq_u32 vcc,-1,v124\ns_add_u32 s72,s95,10485760\ns_cbranch_vccz .Lrz15\ns_mov_b64 exec,vcc\ns_mov_b32 s97,1\nbuffer_load_dwordx4 v[180:183],v240,s[52:55],s72 offen sc1\ns_mov_b64 exec,-1\n.Lrz15:\n" \
"v_max3_u32 v124,v184,v185,v186\nv_max3_u32 v124,v124,v187,v187\nv_cmp_eq_u32 vcc,-1,v124\ns_add_u32 s72,s95,12582912\ns_cbranch_vccz .Lrz16\ns_mov_b64 exec,vcc\ns_mov_b32 s97,1\nbuffer_load_dwordx4 v[184:187],v240,s[52:55],s72 offen sc1\n" \
"s_mov_b64 exec,-1\n.Lrz16:\nv_max3_u32 v124,v188,v189,v190\nv_max3_u32 v124,v124,v191,v191\nv_cmp_eq_u32 vcc,-1,v124\ns_add_u32 s72,s95,14680064\ns_cbranch_vccz .Lrz17\ns_mov_b64 exec,vcc\n" \
"s_mov_b32 s97,1\nbuffer_load_dwordx4 v[188:191],v240,s[52:55],s72 offen sc1\ns_mov_b64 exec,-1\n.Lrz17:\ns_cmp_lg_u32 s97,0\ns_cbranch_scc1 .Lrm1\ns_cmp_eq_u32 s71,0\ns_cbranch_scc1 .Lrb1\n" \
"s_branch .Lrow1\n.Lrm1:\ns_mov_b32 s71,1\ns_waitcnt vmcnt(0)\ns_sub_u32 s73,s73,1\ns_cselect_b32 s73,0,s73\ns_cbranch_scc1 .Lrb1\ns_sleep 2\n" \
"s_branch .Lrl1\n.Lrs0:\ns_mov_b32 s71,0\n.Lrl0:\ns_mov_b32 s97,0\nv_max3_u32 v60,v128,v129,v130\nv_max3_u32 v60,v60,v131,v131\nv_cmp_eq_u32 vcc,-1,v60\n" \
"s_add_u32 s72,s74,0\ns_cbranch_vccz .Lrz00\ns_mov_b64 exec,vcc\ns_mov_b32 s97,1\nbuffer_load_dwordx4 v[128:131],v240,s[52:55],s72 offen sc1\ns_mov_b64 exec,-1\n.Lrz00:\nv_max3_u32 v60,v132,v133,v134\n" \
"v_max3_u32 v60,v60,v135,v135\nv_cmp_eq_u32 vcc,-1,v60\ns_add_u32 s72,s74,2097152\ns_cbranch_vccz .Lrz01\ns_mov_b64 exec,vcc\ns_mov_b32 s97,1\nbuffer_load_dwordx4 v[132:135],v240,s[52:55],s72 offen sc1\ns_mov_b64 exec,-1\n" \
".Lrz01:\nv_max3_u32 v60,v136,v137,v138\nv_max3_u32 v60,v60,v139,v139\nv_cmp_eq_u32 vcc,-1,v60\ns_add_u32 s72,s74,4194304\ns_cbranch_vccz .Lrz02\ns_mov_b64 exec,vcc\ns_mov_b32 s97,1\n" \
"buffer_load_dwordx4 v[136:139],v240,s[52:55],s72 offen sc1\ns_mov_b64 exec,-1\n.Lrz02:\nv_max3_u32 v60,v140,v141,v142\nv_max3_u32 v60,v60,v143,v143\nv_cmp_eq_u32 vcc,-1,v60\ns_add_u32 s72,s74,6291456\ns_cbranch_vccz .Lrz03\n" \
"s_mov_b64 exec,vcc\ns_mov_b32 s97,1\nbuffer_load_dwordx4 v[140:143],v240,s[52:55],s72 offen sc1\ns_mov_b64 exec,-1\n.Lrz03:\nv_max3_u32 v60,v144,v145,v146\nv_max3_u32 v60,v60,v147,v147\nv_cmp_eq_u32 vcc,-1,v60\n" \
"s_add_u32 s72,s74,8388608\ns_cbranch_vccz .Lrz04\ns_mov_b64 exec,vcc\ns_mov_b32 s97,1\nbuffer_load_dwordx4 v[144:147],v240,s[52:55],s72 offen sc1\ns_mov_b64 exec,-1\n.Lrz04:\nv_max3_u32 v60,v148,v149,v150\n" \
"v_max3_u32 v60,v60,v151,v151\nv_cmp_eq_u32 vcc,-1,v60\ns_add_u32 s72,s74,10485760\ns_cbranch_vccz .Lrz05\ns_mov_b64 exec,vcc\ns_mov_b32 s97,1\nbuffer_load_dwordx4 v[148:151],v240,s[52:55],s72 offen sc1\ns_mov_b64 exec,-1\n" \
".Lrz05:\nv_max3_u32 v60,v152,v153,v154\nv_max3_u32 v60,v60,v155,v155\nv_cmp_eq_u32 vcc,-1,v60\ns_add_u32 s72,s74,12582912\ns_cbranch_vccz .Lrz06\ns_mov_b64 exec,vcc\ns_mov_b32 s97,1\n" \
"buffer_load_dwordx4 v[152:155],v240,s[52:55],s72 offen sc1\ns_mov_b64 exec,-1\n.Lrz06:\nv_max3_u32 v60,v156,v157,v158\nv_max3_u32 v60,v60,v159,v159\nv_cmp_eq_u32 vcc,-1,v60\ns_add_u32 s72,s74,14680064\ns_cbranch_vccz .Lrz07\n" \
"s_mov_b64 exec,vcc\ns_mov_b32 s97,1\nbuffer_load_dwordx4 v[156:159],v240,s[52:55],s72 offen sc1\ns_mov_b64 exec,-1\n.Lrz07:\ns_cmp_lg_u32 s97,0\ns_cbranch_scc1 .Lrm0\ns_cmp_eq_u32 s71,0\n" \
"s_cbranch_scc1 .Lrb0\ns_branch .Lrow0\n.Lrm0:\ns_mov_b32 s71,1\ns_waitcnt vmcnt(0)\ns_sub_u32 s73,s73,1\ns_cselect_b32 s73,0,s73\ns_cbranch_scc1 .Lrb0\n" \
"s_sleep 2\ns_branch .Lrl0\n.Lredo:\ns_waitcnt vmcnt(0) lgkmcnt(0)\ns_barrier\ns_mov_b64 s[90:91],0\ns_mov_b32 s99,0\ns_mov_b32 s17,1\n" \
"s_lshl_b32 s70,s63,19\ns_add_u32 s37,s70,3145728\ns_lshl_b32 s71,s62,15\ns_add_u32 s72,s37,s64\ns_add_u32 s72,s72,491520\ns_sub_u32 s72,s72,s71\ns_add_u32 m0,s64,0\ns_nop 0\n" \
"buffer_load_dwordx4 v240,s[52:55],s72 offen offset:0 sc1 lds\nbuffer_load_dwordx4 v240,s[52:55],s72 offen offset:1024 sc1 lds\nbuffer_load_dwordx4 v240,s[52:55],s72 offen offset:2048 sc1 lds\nbuffer_load_dwordx4 v240,s[52:55],s72 offen offset:3072 sc1 lds\ns_add_u32 m0,s64,65536\ns_add_u32 s72,s72,16777216\nbuffer_load_dwordx4 v240,s[52:55],s72 offen offset:0 sc1 lds\nbuffer_load_dwordx4 v240,s[52:55],s72 offen offset:1024 sc1 lds\n" \
"buffer_load_dwordx4 v240,s[52:55],s72 offen offset:2048 sc1 lds\nbuffer_load_dwordx4 v240,s[52:55],s72 offen offset:3072 sc1 lds\ns_add_u32 s72,s37,s64\ns_add_u32 s72,s72,507904\ns_sub_u32 s72,s72,s71\ns_add_u32 m0,s64,16384\ns_nop 0\nbuffer_load_dwordx4 v240,s[52:55],s72 offen offset:0 sc1 lds\n" \
"buffer_load_dwordx4 v240,s[52:55],s72 offen offset:1024 sc1 lds\nbuffer_load_dwordx4 v240,s[52:55],s72 offen offset:2048 sc1 lds\nbuffer_load_dwordx4 v240,s[52:55],s72 offen offset:3072 sc1 lds\ns_add_u32 m0,s64,81920\ns_add_u32 s72,s72,16777216\nbuffer_load_dwordx4 v240,s[52:55],s72 offen offset:0 sc1 lds\nbuffer_load_dwordx4 v240,s[52:55],s72 offen offset:1024 sc1 lds\nbuffer_load_dwordx4 v240,s[52:55],s72 offen offset:2048 sc1 lds\n" \
"buffer_load_dwordx4 v240,s[52:55],s72 offen offset:3072 sc1 lds\ns_add_u32 s72,s37,s64\ns_add_u32 s72,s72,16384\ns_add_u32 s72,s72,s71\ns_add_u32 m0,s64,32768\ns_nop 0\nbuffer_load_dwordx4 v240,s[52:55],s72 offen offset:0 sc1 lds\nbuffer_load_dwordx4 v240,s[52:55],s72 offen offset:1024 sc1 lds\n" \
"buffer_load_dwordx4 v240,s[52:55],s72 offen offset:2048 sc1 lds\nbuffer_load_dwordx4 v240,s[52:55],s72 offen offset:3072 sc1 lds\ns_add_u32 m0,s64,98304\ns_add_u32 s72,s72,16777216\nbuffer_load_dwordx4 v240,s[52:55],s72 offen offset:0 sc1 lds\nbuffer_load_dwordx4 v240,s[52:55],s72 offen offset:1024 sc1 lds\nbuffer_load_dwordx4 v240,s[52:55],s72 offen offset:2048 sc1 lds\nbuffer_load_dwordx4 v240,s[52:55],s72 offen offset:3072 sc1 lds\n" \
"s_add_u32 s72,s37,s64\ns_add_u32 s72,s72,0\ns_add_u32 s72,s72,s71\ns_add_u32 m0,s64,49152\ns_nop 0\nbuffer_load_dwordx4 v240,s[52:55],s72 offen offset:0 sc1 lds\nbuffer_load_dwordx4 v240,s[52:55],s72 offen offset:1024 sc1 lds\nbuffer_load_dwordx4 v240,s[52:55],s72 offen offset:2048 sc1 lds\n" \
"buffer_load_dwordx4 v240,s[52:55],s72 offen offset:3072 sc1 lds\ns_add_u32 m0,s64,114688\ns_add_u32 s72,s72,16777216\nbuffer_load_dwordx4 v240,s[52:55],s72 offen offset:0 sc1 lds\nbuffer_load_dwordx4 v240,s[52:55],s72 offen offset:1024 sc1 lds\nbuffer_load_dwordx4 v240,s[52:55],s72 offen offset:2048 sc1 lds\nbuffer_load_dwordx4 v240,s[52:55],s72 offen offset:3072 sc1 lds\nv_and_b32 v251,15,v249\n" \
"v_lshrrev_b32 v250,4,v249\nv_lshlrev_b32 v250,2,v250\nv_sub_u32 v251,v251,v250\ns_lshl_b32 s70,s62,7\ns_lshl_b32 s72,s7,5\ns_sub_u32 s38,s72,s70\ns_add_u32 s38,s38,1920\ns_sub_u32 s39,s70,s72\n" \
"s_add_u32 s39,s39,96\nv_add_u32 v225,s38,v251\nv_add_u32 v225,0,v225\nv_mov_b32 v224,0xff800000\nv_add_u32 v229,s38,v251\nv_add_u32 v229,16,v229\nv_mov_b32 v228,0xff800000\nv_add_u32 v233,s39,v251\n" \
"v_add_u32 v233,0,v233\nv_mov_b32 v232,0xff800000\nv_add_u32 v237,s39,v251\nv_add_u32 v237,16,v237\nv_mov_b32 v236,0xff800000\ns_sub_u32 s38,524288,s71\ns_add_u32 s38,s38,s37\n.Lrl:\n" \
"buffer_load_dwordx4 a[0:3],v240,s[52:55],s37 offen offset:0 sc1\nbuffer_load_dwordx4 a[4:7],v240,s[52:55],s37 offen offset:1024 sc1\nbuffer_load_dwordx4 a[8:11],v240,s[52:55],s37 offen offset:2048 sc1\nbuffer_load_dwordx4 a[12:15],v240,s[52:55],s37 offen offset:3072 sc1\ns_add_u32 s37,s37,4096\nbuffer_load_dwordx4 a[16:19],v240,s[52:55],s37 offen offset:0 sc1\nbuffer_load_dwordx4 a[20:23],v240,s[52:55],s37 offen offset:1024 sc1\nbuffer_load_dwordx4 a[24:27],v240,s[52:55],s37 offen offset:2048 sc1\n" \
"buffer_load_dwordx4 a[28:31],v240,s[52:55],s37 offen offset:3072 sc1\ns_add_u32 s37,s37,4096\nbuffer_load_dwordx4 a[32:35],v240,s[52:55],s37 offen offset:0 sc1\nbuffer_load_dwordx4 a[36:39],v240,s[52:55],s37 offen offset:1024 sc1\nbuffer_load_dwordx4 a[40:43],v240,s[52:55],s37 offen offset:2048 sc1\nbuffer_load_dwordx4 a[44:47],v240,s[52:55],s37 offen offset:3072 sc1\ns_add_u32 s37,s37,4096\nbuffer_load_dwordx4 a[48:51],v240,s[52:55],s37 offen offset:0 sc1\n" \
"buffer_load_dwordx4 a[52:55],v240,s[52:55],s37 offen offset:1024 sc1\nbuffer_load_dwordx4 a[56:59],v240,s[52:55],s37 offen offset:2048 sc1\nbuffer_load_dwordx4 a[60:63],v240,s[52:55],s37 offen offset:3072 sc1\ns_add_u32 s37,s37,4096\ns_waitcnt vmcnt(15)\nM v[128:131],a[0:3],a[128:131],0\ns_waitcnt vmcnt(14)\nM v[128:131],a[4:7],a[132:135],v[128:131]\n" \
"s_waitcnt vmcnt(13)\nM v[128:131],a[8:11],a[136:139],v[128:131]\ns_waitcnt vmcnt(12)\nM v[128:131],a[12:15],a[140:143],v[128:131]\nM v[132:135],a[0:3],a[144:147],0\nM v[132:135],a[4:7],a[148:151],v[132:135]\nM v[132:135],a[8:11],a[152:155],v[132:135]\nM v[132:135],a[12:15],a[156:159],v[132:135]\n" \
"M v[136:139],a[0:3],a[160:163],0\nM v[136:139],a[4:7],a[164:167],v[136:139]\nM v[136:139],a[8:11],a[168:171],v[136:139]\nM v[136:139],a[12:15],a[172:175],v[136:139]\nM v[140:143],a[0:3],a[176:179],0\nM v[140:143],a[4:7],a[180:183],v[140:143]\nM v[140:143],a[8:11],a[184:187],v[140:143]\nM v[140:143],a[12:15],a[188:191],v[140:143]\n" \
"s_waitcnt vmcnt(11)\nM v[144:147],a[16:19],a[128:131],0\ns_waitcnt vmcnt(10)\nM v[144:147],a[20:23],a[132:135],v[144:147]\ns_waitcnt vmcnt(9)\nM v[144:147],a[24:27],a[136:139],v[144:147]\ns_waitcnt vmcnt(8)\nM v[144:147],a[28:31],a[140:143],v[144:147]\n" \
"M v[148:151],a[16:19],a[144:147],0\nM v[148:151],a[20:23],a[148:151],v[148:151]\nM v[148:151],a[24:27],a[152:155],v[148:151]\nM v[148:151],a[28:31],a[156:159],v[148:151]\nM v[152:155],a[16:19],a[160:163],0\nM v[152:155],a[20:23],a[164:167],v[152:155]\nM v[152:155],a[24:27],a[168:171],v[152:155]\nM v[152:155],a[28:31],a[172:175],v[152:155]\n" \
"M v[156:159],a[16:19],a[176:179],0\nM v[156:159],a[20:23],a[180:183],v[156:159]\nM v[156:159],a[24:27],a[184:187],v[156:159]\nM v[156:159],a[28:31],a[188:191],v[156:159]\ns_waitcnt vmcnt(7)\nM v[160:163],a[32:35],a[128:131],0\ns_waitcnt vmcnt(6)\nM v[160:163],a[36:39],a[132:135],v[160:163]\n" \
"s_waitcnt vmcnt(5)\nM v[160:163],a[40:43],a[136:139],v[160:163]\ns_waitcnt vmcnt(4)\nM v[160:163],a[44:47],a[140:143],v[160:163]\nM v[164:167],a[32:35],a[144:147],0\nM v[164:167],a[36:39],a[148:151],v[164:167]\nM v[164:167],a[40:43],a[152:155],v[164:167]\nM v[164:167],a[44:47],a[156:159],v[164:167]\n" \
"M v[168:171],a[32:35],a[160:163],0\nM v[168:171],a[36:39],a[164:167],v[168:171]\nM v[168:171],a[40:43],a[168:171],v[168:171]\nM v[168:171],a[44:47],a[172:175],v[168:171]\nM v[172:175],a[32:35],a[176:179],0\nM v[172:175],a[36:39],a[180:183],v[172:175]\nM v[172:175],a[40:43],a[184:187],v[172:175]\nM v[172:175],a[44:47],a[188:191],v[172:175]\n" \
"s_waitcnt vmcnt(3)\nM v[176:179],a[48:51],a[128:131],0\ns_waitcnt vmcnt(2)\nM v[176:179],a[52:55],a[132:135],v[176:179]\ns_waitcnt vmcnt(1)\nM v[176:179],a[56:59],a[136:139],v[176:179]\ns_waitcnt vmcnt(0)\nM v[176:179],a[60:63],a[140:143],v[176:179]\n" \
"M v[180:183],a[48:51],a[144:147],0\nM v[180:183],a[52:55],a[148:151],v[180:183]\nM v[180:183],a[56:59],a[152:155],v[180:183]\nM v[180:183],a[60:63],a[156:159],v[180:183]\nM v[184:187],a[48:51],a[160:163],0\nM v[184:187],a[52:55],a[164:167],v[184:187]\nM v[184:187],a[56:59],a[168:171],v[184:187]\nM v[184:187],a[60:63],a[172:175],v[184:187]\n" \
"M v[188:191],a[48:51],a[176:179],0\nM v[188:191],a[52:55],a[180:183],v[188:191]\nM v[188:191],a[56:59],a[184:187],v[188:191]\nM v[188:191],a[60:63],a[188:191],v[188:191]\nv_cmp_le_i32 vcc,0,v225\ns_nop 1\nv_cndmask_b32 v128,v252,v128,vcc\nv_max_f32 v224,v224,v128\n" \
"v_cmp_le_i32 vcc,1,v225\ns_nop 1\nv_cndmask_b32 v129,v252,v129,vcc\nv_max_f32 v224,v224,v129\nv_cmp_le_i32 vcc,2,v225\ns_nop 1\nv_cndmask_b32 v130,v252,v130,vcc\nv_max_f32 v224,v224,v130\n" \
"v_cmp_le_i32 vcc,3,v225\ns_nop 1\nv_cndmask_b32 v131,v252,v131,vcc\nv_max_f32 v224,v224,v131\nv_cmp_le_i32 vcc,0,v229\ns_nop 1\nv_cndmask_b32 v132,v252,v132,vcc\nv_max_f32 v228,v228,v132\n" \
"v_cmp_le_i32 vcc,1,v229\ns_nop 1\nv_cndmask_b32 v133,v252,v133,vcc\nv_max_f32 v228,v228,v133\nv_cmp_le_i32 vcc,2,v229\ns_nop 1\nv_cndmask_b32 v134,v252,v134,vcc\nv_max_f32 v228,v228,v134\n" \
"v_cmp_le_i32 vcc,3,v229\ns_nop 1\nv_cndmask_b32 v135,v252,v135,vcc\nv_max_f32 v228,v228,v135\nv_cmp_le_i32 vcc,0,v233\ns_nop 1\nv_cndmask_b32 v136,v252,v136,vcc\nv_max_f32 v232,v232,v136\n" \
"v_cmp_le_i32 vcc,1,v233\ns_nop 1\nv_cndmask_b32 v137,v252,v137,vcc\nv_max_f32 v232,v232,v137\nv_cmp_le_i32 vcc,2,v233\ns_nop 1\nv_cndmask_b32 v138,v252,v138,vcc\nv_max_f32 v232,v232,v138\n" \
"v_cmp_le_i32 vcc,3,v233\ns_nop 1\nv_cndmask_b32 v139,v252,v139,vcc\nv_max_f32 v232,v232,v139\nv_cmp_le_i32 vcc,0,v237\ns_nop 1\nv_cndmask_b32 v140,v252,v140,vcc\nv_max_f32 v236,v236,v140\n" \
"v_cmp_le_i32 vcc,1,v237\ns_nop 1\nv_cndmask_b32 v141,v252,v141,vcc\nv_max_f32 v236,v236,v141\nv_cmp_le_i32 vcc,2,v237\ns_nop 1\nv_cndmask_b32 v142,v252,v142,vcc\nv_max_f32 v236,v236,v142\n" \
"v_cmp_le_i32 vcc,3,v237\ns_nop 1\nv_cndmask_b32 v143,v252,v143,vcc\nv_max_f32 v236,v236,v143\nv_cmp_le_i32 vcc,16,v225\ns_nop 1\nv_cndmask_b32 v144,v252,v144,vcc\nv_max_f32 v224,v224,v144\n" \
"v_cmp_le_i32 vcc,17,v225\ns_nop 1\nv_cndmask_b32 v145,v252,v145,vcc\nv_max_f32 v224,v224,v145\nv_cmp_le_i32 vcc,18,v225\ns_nop 1\nv_cndmask_b32 v146,v252,v146,vcc\nv_max_f32 v224,v224,v146\n" \
"v_cmp_le_i32 vcc,19,v225\ns_nop 1\nv_cndmask_b32 v147,v252,v147,vcc\nv_max_f32 v224,v224,v147\nv_cmp_le_i32 vcc,16,v229\ns_nop 1\nv_cndmask_b32 v148,v252,v148,vcc\nv_max_f32 v228,v228,v148\n" \
"v_cmp_le_i32 vcc,17,v229\ns_nop 1\nv_cndmask_b32 v149,v252,v149,vcc\nv_max_f32 v228,v228,v149\nv_cmp_le_i32 vcc,18,v229\ns_nop 1\nv_cndmask_b32 v150,v252,v150,vcc\nv_max_f32 v228,v228,v150\n" \
"v_cmp_le_i32 vcc,19,v229\ns_nop 1\nv_cndmask_b32 v151,v252,v151,vcc\nv_max_f32 v228,v228,v151\nv_cmp_le_i32 vcc,16,v233\ns_nop 1\nv_cndmask_b32 v152,v252,v152,vcc\nv_max_f32 v232,v232,v152\n" \
"v_cmp_le_i32 vcc,17,v233\ns_nop 1\nv_cndmask_b32 v153,v252,v153,vcc\nv_max_f32 v232,v232,v153\nv_cmp_le_i32 vcc,18,v233\ns_nop 1\nv_cndmask_b32 v154,v252,v154,vcc\nv_max_f32 v232,v232,v154\n" \
"v_cmp_le_i32 vcc,19,v233\ns_nop 1\nv_cndmask_b32 v155,v252,v155,vcc\nv_max_f32 v232,v232,v155\nv_cmp_le_i32 vcc,16,v237\ns_nop 1\nv_cndmask_b32 v156,v252,v156,vcc\nv_max_f32 v236,v236,v156\n" \
"v_cmp_le_i32 vcc,17,v237\ns_nop 1\nv_cndmask_b32 v157,v252,v157,vcc\nv_max_f32 v236,v236,v157\nv_cmp_le_i32 vcc,18,v237\ns_nop 1\nv_cndmask_b32 v158,v252,v158,vcc\nv_max_f32 v236,v236,v158\n" \
"v_cmp_le_i32 vcc,19,v237\ns_nop 1\nv_cndmask_b32 v159,v252,v159,vcc\nv_max_f32 v236,v236,v159\nv_cmp_le_i32 vcc,32,v225\ns_nop 1\nv_cndmask_b32 v160,v252,v160,vcc\nv_max_f32 v224,v224,v160\n" \
"v_cmp_le_i32 vcc,33,v225\ns_nop 1\nv_cndmask_b32 v161,v252,v161,vcc\nv_max_f32 v224,v224,v161\nv_cmp_le_i32 vcc,34,v225\ns_nop 1\nv_cndmask_b32 v162,v252,v162,vcc\nv_max_f32 v224,v224,v162\n" \
"v_cmp_le_i32 vcc,35,v225\ns_nop 1\nv_cndmask_b32 v163,v252,v163,vcc\nv_max_f32 v224,v224,v163\nv_cmp_le_i32 vcc,32,v229\ns_nop 1\nv_cndmask_b32 v164,v252,v164,vcc\nv_max_f32 v228,v228,v164\n" \
"v_cmp_le_i32 vcc,33,v229\ns_nop 1\nv_cndmask_b32 v165,v252,v165,vcc\nv_max_f32 v228,v228,v165\nv_cmp_le_i32 vcc,34,v229\ns_nop 1\nv_cndmask_b32 v166,v252,v166,vcc\nv_max_f32 v228,v228,v166\n" \
"v_cmp_le_i32 vcc,35,v229\ns_nop 1\nv_cndmask_b32 v167,v252,v167,vcc\nv_max_f32 v228,v228,v167\nv_cmp_le_i32 vcc,32,v233\ns_nop 1\nv_cndmask_b32 v168,v252,v168,vcc\nv_max_f32 v232,v232,v168\n" \
"v_cmp_le_i32 vcc,33,v233\ns_nop 1\nv_cndmask_b32 v169,v252,v169,vcc\nv_max_f32 v232,v232,v169\nv_cmp_le_i32 vcc,34,v233\ns_nop 1\nv_cndmask_b32 v170,v252,v170,vcc\nv_max_f32 v232,v232,v170\n" \
"v_cmp_le_i32 vcc,35,v233\ns_nop 1\nv_cndmask_b32 v171,v252,v171,vcc\nv_max_f32 v232,v232,v171\nv_cmp_le_i32 vcc,32,v237\ns_nop 1\nv_cndmask_b32 v172,v252,v172,vcc\nv_max_f32 v236,v236,v172\n" \
"v_cmp_le_i32 vcc,33,v237\ns_nop 1\nv_cndmask_b32 v173,v252,v173,vcc\nv_max_f32 v236,v236,v173\nv_cmp_le_i32 vcc,34,v237\ns_nop 1\nv_cndmask_b32 v174,v252,v174,vcc\nv_max_f32 v236,v236,v174\n" \
"v_cmp_le_i32 vcc,35,v237\ns_nop 1\nv_cndmask_b32 v175,v252,v175,vcc\nv_max_f32 v236,v236,v175\nv_cmp_le_i32 vcc,48,v225\ns_nop 1\nv_cndmask_b32 v176,v252,v176,vcc\nv_max_f32 v224,v224,v176\n" \
"v_cmp_le_i32 vcc,49,v225\ns_nop 1\nv_cndmask_b32 v177,v252,v177,vcc\nv_max_f32 v224,v224,v177\nv_cmp_le_i32 vcc,50,v225\ns_nop 1\nv_cndmask_b32 v178,v252,v178,vcc\nv_max_f32 v224,v224,v178\n" \
"v_cmp_le_i32 vcc,51,v225\ns_nop 1\nv_cndmask_b32 v179,v252,v179,vcc\nv_max_f32 v224,v224,v179\nv_cmp_le_i32 vcc,48,v229\ns_nop 1\nv_cndmask_b32 v180,v252,v180,vcc\nv_max_f32 v228,v228,v180\n" \
"v_cmp_le_i32 vcc,49,v229\ns_nop 1\nv_cndmask_b32 v181,v252,v181,vcc\nv_max_f32 v228,v228,v181\nv_cmp_le_i32 vcc,50,v229\ns_nop 1\nv_cndmask_b32 v182,v252,v182,vcc\nv_max_f32 v228,v228,v182\n" \
"v_cmp_le_i32 vcc,51,v229\ns_nop 1\nv_cndmask_b32 v183,v252,v183,vcc\nv_max_f32 v228,v228,v183\nv_cmp_le_i32 vcc,48,v233\ns_nop 1\nv_cndmask_b32 v184,v252,v184,vcc\nv_max_f32 v232,v232,v184\n" \
"v_cmp_le_i32 vcc,49,v233\ns_nop 1\nv_cndmask_b32 v185,v252,v185,vcc\nv_max_f32 v232,v232,v185\nv_cmp_le_i32 vcc,50,v233\ns_nop 1\nv_cndmask_b32 v186,v252,v186,vcc\nv_max_f32 v232,v232,v186\n" \
"v_cmp_le_i32 vcc,51,v233\ns_nop 1\nv_cndmask_b32 v187,v252,v187,vcc\nv_max_f32 v232,v232,v187\nv_cmp_le_i32 vcc,48,v237\ns_nop 1\nv_cndmask_b32 v188,v252,v188,vcc\nv_max_f32 v236,v236,v188\n" \
"v_cmp_le_i32 vcc,49,v237\ns_nop 1\nv_cndmask_b32 v189,v252,v189,vcc\nv_max_f32 v236,v236,v189\nv_cmp_le_i32 vcc,50,v237\ns_nop 1\nv_cndmask_b32 v190,v252,v190,vcc\nv_max_f32 v236,v236,v190\n" \
"v_cmp_le_i32 vcc,51,v237\ns_nop 1\nv_cndmask_b32 v191,v252,v191,vcc\nv_max_f32 v236,v236,v191\nv_subrev_u32 v225,64,v225\nv_subrev_u32 v229,64,v229\nv_subrev_u32 v233,64,v233\nv_subrev_u32 v237,64,v237\n" \
"s_cmp_lt_u32 s37,s38\ns_cbranch_scc1 .Lrl\ns_waitcnt vmcnt(0)\ns_barrier\nds_read_b128 a[0:3],v240 offset:0\nds_read_b128 a[4:7],v240 offset:1024\nds_read_b128 a[8:11],v240 offset:2048\nds_read_b128 a[12:15],v240 offset:3072\n" \
"ds_read_b128 a[16:19],v240 offset:4096\nds_read_b128 a[20:23],v240 offset:5120\nds_read_b128 a[24:27],v240 offset:6144\nds_read_b128 a[28:31],v240 offset:7168\nds_read_b128 a[32:35],v240 offset:8192\nds_read_b128 a[36:39],v240 offset:9216\nds_read_b128 a[40:43],v240 offset:10240\nds_read_b128 a[44:47],v240 offset:11264\n" \
"ds_read_b128 a[48:51],v240 offset:12288\nds_read_b128 a[52:55],v240 offset:13312\nds_read_b128 a[56:59],v240 offset:14336\nds_read_b128 a[60:63],v240 offset:15360\ns_cmp_eq_u32 s62,0\ns_cselect_b32 s36,0x8000,0\ns_add_u32 s36,s36,s64\ns_cmp_eq_u32 s36,s96\n" \
"s_cselect_b32 s72,0xc000,s98\ns_add_u32 s65,s36,s72\ns_cmp_eq_u32 s65,s96\ns_cselect_b32 s72,0xc000,s98\ns_add_u32 s66,s65,s72\ns_cmp_eq_u32 s66,s96\ns_cselect_b32 s72,0xc000,s98\ns_add_u32 s95,s66,s72\n" \
"s_add_u32 s97,s96,65536\ns_add_u32 s15,s64,s46\ns_cmp_gt_i32 s62,-1\ns_cselect_b32 s72,s98,0xfffe4000\ns_add_u32 s15,s15,s72\ns_mov_b32 s94,0xc000\ns_cmp_eq_u32 s62,7\ns_cselect_b32 s72,98304,0\n" \
"s_sub_u32 s97,s97,s72\nv_and_b32 v251,31,v249\nv_lshlrev_b32 v251,2,v251\ns_lshl_b32 s80,s63,7\ns_add_u32 s80,s80,86016\nv_add_u32 v250,0xc000,v241\ns_waitcnt lgkmcnt(0)\ns_branch .Lattn\n" \
".Lfin:\n" \
""
constexpr size_t WS_DBG = (size_t)51 << 20;
constexpr unsigned LDS_BYTES = 163840;
struct Args {
    const u16 *x, *ckv, *w_q, *w_uk, *w_uv, *w_o, *w_r, *wg, *wu, *wd, *cvec, *w_mod;
    char* ws; u16* out;
};
typedef const __attribute__((address_space(4))) Args* kargs_t;
#define STAMPD(i) do {} while (0)
#define C8(p, a) p #a "0", p #a "1", p #a "2", p #a "3", p #a "4", p #a "5", p #a "6", p #a "7", p #a "8", p #a "9"
#define C100(p, a) C8(p, a##0), C8(p, a##1), C8(p, a##2), C8(p, a##3), C8(p, a##4), C8(p, a##5), C8(p, a##6), C8(p, a##7), C8(p, a##8), C8(p, a##9)
#define C10_99(p) C8(p, 1), C8(p, 2), C8(p, 3), C8(p, 4), C8(p, 5), C8(p, 6), C8(p, 7), C8(p, 8), C8(p, 9)
#define C200_249(p) C8(p, 20), C8(p, 21), C8(p, 22), C8(p, 23), C8(p, 24)
#define BODY_CLOB "memory", "vcc", "scc", "s0", "s1", "s2", "s3", "s8", "s9", C8("s", 1), C8("s", 2), "s30", "s31", "s33", "s34", "s35", "s36", "s37", "s38", "s39", C8("s", 4), C8("s", 5), C8("s", 6), C8("s", 7), C8("s", 8), C8("s", 9), \
    C8("v", ), C10_99("v"), C100("v", 1), C200_249("v"), "v250", "v251", "v252", "v253", "v254", C8("a", ), C10_99("a"), C100("a", 1), C200_249("a"), "a250", "a251", "a252", "a253", "a254", "a255"

__global__ __launch_bounds__(256, 1) void k_all(const Args a_) {
    kargs_t ap = (kargs_t)__builtin_amdgcn_kernarg_segment_ptr(); unsigned bid = blockIdx.x, wid = rfl(threadIdx.x >> 6), sv_;
    asm volatile(BODY_ASM : "+{s[4:5]}"(ap), "+{s6}"(bid), "+{s7}"(wid), "={v255}"(sv_) :: BODY_CLOB);
    asm volatile(".Lcode_e:");
}
extern "C" void kernel_launch(void* const* d_in, const int* in_sizes, int n_in, void* d_out, int out_size, void* d_ws, size_t ws_size, hipStream_t stream) {
    Args a;
    a.x = (const u16*)d_in[0]; a.ckv = (const u16*)d_in[1]; a.w_q = (const u16*)d_in[2]; a.w_uk = (const u16*)d_in[3]; a.w_uv = (const u16*)d_in[4]; a.w_o = (const u16*)d_in[5];
    a.w_r = (const u16*)d_in[7]; a.wg = (const u16*)d_in[8]; a.wu = (const u16*)d_in[9]; a.wd = (const u16*)d_in[10]; a.cvec = (const u16*)d_in[11]; a.w_mod = (const u16*)d_in[12];
    a.ws = (char*)d_ws;
    a.out = (u16*)d_out;
    (void)hipFuncSetAttribute((const void*)k_all, hipFuncAttributeMaxDynamicSharedMemorySize, LDS_BYTES);
    k_all<<<256, 256, LDS_BYTES, stream>>>(a);
}
```

```cpp
#include <hip/hip_runtime.h>
#include <hip/hip_bf16.h>
#include <stdint.h>
typedef unsigned short u16;
#define DI __device__ __forceinline__
DI unsigned lane_id() { unsigned l; asm volatile("v_mbcnt_lo_u32_b32 %0, -1, 0\n\tv_mbcnt_hi_u32_b32 %0, -1, %0" : "=v"(l)); return l; }
DI unsigned rfl(unsigned x) { return __builtin_amdgcn_readfirstlane(x); }
#define BODY_ASM ".macro M a,b,c,d\nv_mfma_f32_16x16x32_bf16 \\a,\\b,\\c,\\d\n.endm\n" \
".Lcode_b:\ns_load_dwordx16 s[8:23],s[4:5],0x0\ns_load_dwordx8 s[24:31],s[4:5],0x40\ns_load_dwordx2 s[34:35],s[4:5],0x60\nv_mbcnt_lo_u32_b32 v249,-1,0\nv_mbcnt_hi_u32_b32 v249,-1,v249\ns_bfe_u32 s61,s6,0x20003\ns_lshl_b32 s64,s7,12\n" \
"s_and_b32 s60,s6,7\ns_lshr_b32 s62,s6,5\ns_lshl2_add_u32 s63,s60,s61\nv_lshlrev_b32 v240,4,v249\nv_lshrrev_b32 v0,3,v249\nv_and_b32 v2,7,v249\nv_bfe_u32 v6,v249,2,3\nv_lshrrev_b32 v7,5,v249\n" \
"v_and_b32 v8,48,v240\nv_lshlrev_b32 v6,13,v6\nv_lshl_or_b32 v7,v7,6,v8\nv_or_b32 v6,0x1000,v6\nv_or_b32 v243,v6,v7\nv_add_u32 v244,0xfffffc80,v243\nv_xor_b32 v6,32,v243\nv_add_u32 v245,63488,v6\n" \
"v_add_u32 v246,62592,v6\ns_lshl_b32 s68,s7,17\ns_lshl_b32 s70,s63,8\ns_add_u32 s68,s68,s70\ns_mov_b32 s42,4198400\ns_mov_b32 s43,0x20000\nv_lshrrev_b32 v6,4,v249\nv_xor_b32 v7,v6,v2\n" \
"v_lshlrev_b32 v7,4,v7\nv_lshl_or_b32 v224,v0,10,v7\nv_xor_b32 v7,64,v224\nv_add_u32 v225,7168,v7\nv_add_u32 v226,14336,v224\nv_add_u32 v227,21504,v7\ns_lshl_b32 s70,s62,1\ns_sub_u32 s71,s7,s70\n" \
"s_add_u32 s71,s71,30\ns_sub_u32 s72,s70,s7\ns_add_u32 s72,s72,3\ns_cmp_lt_u32 s7,2\ns_cselect_b32 s74,s71,s72\ns_lshl_b32 s66,s74,16\ns_mul_i32 s65,s7,24576\ns_mov_b32 s38,2097152\n" \
"s_mov_b32 s39,0x20000\ns_xor_b32 s94,s61,s62\ns_and_b32 s94,s94,1\ns_lshl_b32 s99,s94,19\ns_or_b32 s99,s99,s68\ns_waitcnt lgkmcnt(0)\ns_sub_u32 s40,s14,0x1000\ns_subb_u32 s41,s15,0\n" \
"s_mov_b64 s[36:37],s[10:11]\ns_add_u32 m0,s64,147456\ns_add_u32 s69,s68,0\nbuffer_load_dwordx4 v243,s[40:43],s69 offen offset:0 lds\nbuffer_load_dwordx4 v244,s[40:43],s69 offen offset:1024 lds\nbuffer_load_dwordx4 v245,s[40:43],s69 offen offset:2048 lds\nbuffer_load_dwordx4 v246,s[40:43],s69 offen offset:3072 lds\ns_add_u32 m0,s65,0\n" \
"s_xor_b32 s67,s66,0\nbuffer_load_dwordx4 v224,s[36:39],s67 offen offset:0 lds\nbuffer_load_dwordx4 v225,s[36:39],s67 offen offset:1024 lds\nbuffer_load_dwordx4 v226,s[36:39],s67 offen offset:2048 lds\nbuffer_load_dwordx4 v227,s[36:39],s67 offen offset:3072 lds\ns_add_u32 m0,s65,4096\ns_xor_b32 s67,s66,32768\nbuffer_load_dwordx4 v224,s[36:39],s67 offen offset:0 lds\n" \
"buffer_load_dwordx4 v225,s[36:39],s67 offen offset:1024 lds\nbuffer_load_dwordx4 v226,s[36:39],s67 offen offset:2048 lds\nbuffer_load_dwordx4 v227,s[36:39],s67 offen offset:3072 lds\ns_cmp_lt_u32 s6,192\ns_cselect_b32 s70,s6,0\ns_mul_i32 s71,s70,171\ns_lshr_b32 s71,s71,13\ns_mul_i32 s72,s71,48\n" \
"s_sub_u32 s70,s70,s72\ns_lshl2_add_u32 s72,s71,s7\ns_lshl_b32 s72,s72,5\nv_lshl_add_u32 v1,v0,2,s72\nv_mul_u32_u24 v3,6144,v1\nv_lshl_add_u32 v3,v2,4,v3\ns_lshl_b32 s72,s70,7\ns_add_u32 s72,s72,3072\n" \
"v_add_u32 v3,s72,v3\nv_add_u32 v4,12288,v3\nv_lshlrev_b32 v1,1,v1\nglobal_load_dwordx2 v[168:169],v1,s[28:29]\nglobal_load_dwordx4 v[152:155],v3,s[30:31] offset:-3072\nglobal_load_dwordx4 v[156:159],v3,s[30:31] offset:3072\nglobal_load_dwordx4 v[160:163],v4,s[30:31] offset:-3072\nglobal_load_dwordx4 v[164:167],v4,s[30:31] offset:3072\n" \
"s_mul_i32 s71,s71,12288\ns_lshl_b32 s70,s70,8\ns_add_u32 s79,s70,s71\ns_add_u32 s79,s79,786432\ns_cmp_eq_u32 s7,0\ns_cselect_b32 s75,0,0x8000000\ns_cmp_lt_u32 s6,192\ns_cselect_b32 s70,s75,0x8000000\n" \
"s_or_b32 s79,s79,s70\ns_mov_b64 s[52:53],s[34:35]\ns_mov_b32 s54,71303168\ns_mov_b32 s55,0x20000\ns_mov_b32 s70,94208\nbuffer_load_dwordx2 v[230:231],off,s[52:55],s70 sc1\ns_getpc_b64 s[56:57]\n.Lpc0:\n" \
"s_and_b32 s57,s57,0xffff\ns_mov_b32 s58,.Lcode_e-.Lpc0\ns_mov_b32 s59,0x20000\ns_lshr_b32 s70,s6,3\nv_and_b32 v251,1,v249\ns_lshl2_add_u32 s78,s70,s7\nv_lshlrev_b32 v251,14,v251\ns_lshl_b32 s78,s78,7\n" \
"buffer_load_dword v247,v251,s[56:59],s78 offen\nv_and_b32 v34,15,v249\nv_lshrrev_b32 v35,1,v34\nv_xor_b32 v35,v35,v6\nv_lshlrev_b32 v35,4,v35\nv_lshl_or_b32 v35,v34,7,v35\nv_add_u32 v228,s65,v35\nv_xor_b32 v229,64,v228\n" \
"v_bfe_u32 v34,v249,2,2\nv_and_b32 v35,3,v249\nv_lshlrev_b32 v35,3,v35\nv_lshl_or_b32 v35,v34,6,v35\nv_lshl_or_b32 v35,v6,11,v35\nv_and_b32 v34,16,v249\nv_lshl_or_b32 v35,v34,1,v35\nv_add_u32 v241,98304,v35\n" \
"v_xor_b32 v242,32,v241\ns_mov_b32 s73,0x400000\nv_mov_b32 v252,0xff800000\ns_mov_b32 s82,0x05040100\ns_mov_b32 s83,0x07060302\nv_lshlrev_b32 v190,5,v2\nv_lshl_or_b32 v190,v6,3,v190\ns_lshl_b32 s72,s7,8\n" \
"s_add_u32 s72,s72,114688\nv_add_u32 v190,s72,v190\ns_lshl_b32 s72,s7,3\ns_add_u32 s72,s72,115712\nv_lshl_add_u32 v191,v2,5,s72\nv_lshlrev_b32 v251,5,v2\nv_add_u32 v251,115712,v251\nv_lshlrev_b32 v250,2,v249\n" \
"v_add_u32 v250,114688,v250\ns_lshl_b32 s70,s62,7\ns_xor_b32 s71,s63,12\ns_lshl3_add_u32 s71,s71,s70\ns_lshl_b32 s72,s63,3\ns_sub_u32 s72,s72,s70\ns_add_u32 s72,s72,1792\ns_cmp_lt_u32 s63,16\n" \
"s_cselect_b32 s71,s71,s72\ns_lshl_b32 s71,s71,10\ns_lshl_b32 s72,s7,7\ns_add_u32 s71,s71,s72\nv_lshlrev_b32 v5,10,v2\nv_lshl_add_u32 v5,v0,4,v5\nv_add_u32 v5,s71,v5\ns_lshl_b32 s70,s6,11\n" \
"s_cmp_lt_u32 s6,112\ns_cselect_b32 s70,s70,0x8000000\ns_lshl_b32 s71,s7,9\ns_add_u32 s70,s70,s71\ns_add_u32 s80,s70,98304\nv_mov_b32 v28,-1\nv_mov_b32 v29,-1\nv_lshlrev_b32 v30,3,v249\n" \
"s_lshl_b32 s70,s6,7\ns_cmp_lt_u32 s6,224\ns_cselect_b32 s71,65536,0x40000000\ns_cmp_lt_u32 s6,128\ns_cselect_b32 s72,556928,0x40000000\nv_mov_b32 v31,s71\nv_mov_b32 v32,s72\nv_cmp_lt_u32 vcc,31,v249\n" \
"s_or_b32 s70,s70,s75\nv_mov_b32 v33,0\nv_cndmask_b32 v31,v31,v32,vcc\nv_lshl_add_u32 v31,v249,2,v31\ns_add_u32 m0,s64,98304\ns_add_u32 s69,s68,524288\nbuffer_load_dwordx4 v243,s[40:43],s69 offen offset:0 lds\nbuffer_load_dwordx4 v244,s[40:43],s69 offen offset:1024 lds\n" \
"buffer_load_dwordx4 v245,s[40:43],s69 offen offset:2048 lds\nbuffer_load_dwordx4 v246,s[40:43],s69 offen offset:3072 lds\ns_add_u32 m0,s65,8192\ns_xor_b32 s67,s66,128\nbuffer_load_dwordx4 v224,s[36:39],s67 offen offset:0 lds\nbuffer_load_dwordx4 v225,s[36:39],s67 offen offset:1024 lds\nbuffer_load_dwordx4 v226,s[36:39],s67 offen offset:2048 lds\nbuffer_load_dwordx4 v227,s[36:39],s67 offen offset:3072 lds\n" \
"s_add_u32 m0,s65,12288\ns_xor_b32 s67,s66,32896\nbuffer_load_dwordx4 v224,s[36:39],s67 offen offset:0 lds\nbuffer_load_dwordx4 v225,s[36:39],s67 offen offset:1024 lds\nbuffer_load_dwordx4 v226,s[36:39],s67 offen offset:2048 lds\nbuffer_load_dwordx4 v227,s[36:39],s67 offen offset:3072 lds\nglobal_load_dwordx4 v[128:131],v5,s[8:9]\nglobal_load_dwordx4 v[132:135],v5,s[8:9] offset:512\n" \
"buffer_store_dwordx2 v[28:29],v30,s[52:55],s80 offen sc1\nbuffer_store_dword v33,v31,s[52:55],s70 offen sc1\ns_waitcnt vmcnt(23)\ns_barrier\ns_add_u32 m0,s64,131072\ns_add_u32 s69,s68,1048576\nds_read_b128 a[0:3],v228 offset:0\nds_read_b128 a[4:7],v228 offset:2048\n" \
"ds_read_b128 a[8:11],v228 offset:4096\nds_read_b128 a[12:15],v228 offset:6144\nds_read_b128 a[16:19],v229 offset:0\nds_read_b128 a[20:23],v229 offset:2048\nds_read_b128 a[24:27],v229 offset:4096\nds_read_b128 a[28:31],v229 offset:6144\nds_read_b64_tr_b16 v[192:193],v241 offset:49152\nds_read_b64_tr_b16 v[194:195],v241 offset:49408\n" \
"ds_read_b64_tr_b16 v[196:197],v241 offset:57344\nds_read_b64_tr_b16 v[198:199],v241 offset:57600\nds_read_b64_tr_b16 v[200:201],v242 offset:49152\nds_read_b64_tr_b16 v[202:203],v242 offset:49408\nds_read_b64_tr_b16 v[204:205],v242 offset:57344\nds_read_b64_tr_b16 v[206:207],v242 offset:57600\nds_read_b64_tr_b16 v[208:209],v241 offset:49664\nds_read_b64_tr_b16 v[210:211],v241 offset:49920\n" \
"ds_read_b64_tr_b16 v[212:213],v241 offset:57856\nds_read_b64_tr_b16 v[214:215],v241 offset:58112\nds_read_b64_tr_b16 v[216:217],v242 offset:49664\nds_read_b64_tr_b16 v[218:219],v242 offset:49920\nds_read_b64_tr_b16 v[220:221],v242 offset:57856\nds_read_b64_tr_b16 v[222:223],v242 offset:58112\ns_waitcnt vmcnt(22)\nv_lshlrev_b32 v170,16,v168\n" \
"v_and_b32 v171,0xffff0000,v168\nv_lshlrev_b32 v172,16,v169\nv_and_b32 v173,0xffff0000,v169\nv_mul_f32 v184,0xbfb8aa3b,v170\nv_mul_f32 v185,0xbfb8aa3b,v171\nv_mul_f32 v186,0xbfb8aa3b,v172\nv_mul_f32 v187,0xbfb8aa3b,v173\nv_exp_f32 v184,v184\n" \
"v_exp_f32 v185,v185\nv_exp_f32 v186,v186\nv_exp_f32 v187,v187\nv_add_f32 v184,1.0,v184\nv_add_f32 v185,1.0,v185\nv_add_f32 v186,1.0,v186\nv_add_f32 v187,1.0,v187\nv_rcp_f32 v184,v184\n" \
"v_rcp_f32 v185,v185\nv_rcp_f32 v186,v186\nv_rcp_f32 v187,v187\nv_mul_f32 v170,v170,v184\nv_mul_f32 v171,v171,v185\nv_mul_f32 v172,v172,v186\nv_mul_f32 v173,v173,v187\nv_cvt_pk_bf16_f32 v170,v170,v171\n" \
"v_cvt_pk_bf16_f32 v171,v172,v173\ns_waitcnt lgkmcnt(5)\nM v[0:3],v[192:195],a[0:3],0\nbuffer_load_dwordx4 v243,s[40:43],s69 offen offset:0 lds\nM v[0:3],v[196:199],a[16:19],v[0:3]\nv_bfe_u32 v253,v249,1,2\ns_lshl2_add_u32 s76,s62,s7\nv_and_b32 v248,1,v249\n" \
"v_lshlrev_b32 v253,13,v253\ns_mul_i32 s76,s76,32768\ns_lshl_b32 s77,s63,8\nM v[4:7],v[192:195],a[4:7],0\nv_lshl_or_b32 v253,v248,7,v253\ns_add_u32 s76,s76,s77\nv_and_b32 v254,7,v249\nv_add_u32 v253,s76,v253\n" \
"M v[4:7],v[196:199],a[20:23],v[4:7]\nv_and_b32 v234,7,v249\ns_lshl3_add_u32 s72,s6,s75\nM v[64:67],v[192:195],a[8:11],0\nv_lshlrev_b32 v234,11,v234\ns_add_u32 s72,s72,573440\nM v[64:67],v[196:199],a[24:27],v[64:67]\nbuffer_load_dwordx4 v244,s[40:43],s69 offen offset:1024 lds\n" \
"M v[68:71],v[192:195],a[12:15],0\ns_waitcnt vmcnt(22)\nv_perm_b32 v182,v156,v152,s82\nv_perm_b32 v183,v156,v152,s83\nv_perm_b32 v184,v157,v153,s82\nv_perm_b32 v185,v157,v153,s83\nv_perm_b32 v186,v158,v154,s82\nv_perm_b32 v187,v158,v154,s83\n" \
"v_perm_b32 v188,v159,v155,s82\nv_perm_b32 v189,v159,v155,s83\nv_dot2_f32_bf16 v174,v182,v170,0\nv_dot2_f32_bf16 v175,v183,v170,0\nv_dot2_f32_bf16 v176,v184,v170,0\nv_dot2_f32_bf16 v177,v185,v170,0\nv_dot2_f32_bf16 v178,v186,v170,0\nv_dot2_f32_bf16 v179,v187,v170,0\n" \
"v_dot2_f32_bf16 v180,v188,v170,0\nv_dot2_f32_bf16 v181,v189,v170,0\ns_waitcnt vmcnt(20)\nv_perm_b32 v182,v164,v160,s82\nv_perm_b32 v183,v164,v160,s83\nv_perm_b32 v184,v165,v161,s82\nv_perm_b32 v185,v165,v161,s83\nv_perm_b32 v186,v166,v162,s82\n" \
"v_perm_b32 v187,v166,v162,s83\nv_perm_b32 v188,v167,v163,s82\nv_perm_b32 v189,v167,v163,s83\nv_dot2_f32_bf16 v174,v182,v171,v174\nv_dot2_f32_bf16 v175,v183,v171,v175\nv_dot2_f32_bf16 v176,v184,v171,v176\nv_dot2_f32_bf16 v177,v185,v171,v177\nv_dot2_f32_bf16 v178,v186,v171,v178\n" \
"v_dot2_f32_bf16 v179,v187,v171,v179\nv_dot2_f32_bf16 v180,v188,v171,v180\nv_dot2_f32_bf16 v181,v189,v171,v181\nv_permlane32_swap_b32 v174,v178\nv_permlane32_swap_b32 v175,v179\nv_permlane32_swap_b32 v176,v180\nv_permlane32_swap_b32 v177,v181\nv_add_f32 v174,v174,v178\n" \
"v_add_f32 v175,v175,v179\nv_add_f32 v176,v176,v180\nv_add_f32 v177,v177,v181\nds_read_b64_tr_b16 v[192:193],v241 offset:50176\nv_permlane16_swap_b32 v174,v176\nv_permlane16_swap_b32 v175,v177\nv_add_f32 v174,v174,v176\nv_add_f32 v175,v175,v177\n" \
"ds_read_b64_tr_b16 v[194:195],v241 offset:50432\nv_add_f32_dpp v174,v174,v174 row_ror:8 row_mask:0xf bank_mask:0xf\nv_add_f32_dpp v175,v175,v175 row_ror:8 row_mask:0xf bank_mask:0xf\nds_write_b64 v190,v[174:175]\nM v[68:71],v[196:199],a[28:31],v[68:71]\nds_read_b64_tr_b16 v[196:197],v241 offset:58368\nds_read_b64_tr_b16 v[198:199],v241 offset:58624\nM v[8:11],v[200:203],a[0:3],0\n" \
"s_waitcnt vmcnt(18)\nv_readfirstlane_b32 s92,v230\nv_readfirstlane_b32 s93,v231\nM v[8:11],v[204:207],a[16:19],v[8:11]\ns_add_u32 s70,s92,1\ns_addc_u32 s71,s93,0\nM v[12:15],v[200:203],a[4:7],0\nbuffer_load_dwordx4 v245,s[40:43],s69 offen offset:2048 lds\n" \
"M v[12:15],v[204:207],a[20:23],v[12:15]\ns_add_u32 s92,s92,0x7f4a7c15\ns_addc_u32 s93,s93,0x9e3779b9\nM v[72:75],v[200:203],a[8:11],0\nv_mov_b32 v230,s92\nv_mov_b32 v231,s93\nM v[72:75],v[204:207],a[24:27],v[72:75]\ns_lshl2_add_u32 s80,s60,s7\n" \
"s_lshl_b32 s80,s80,9\nM v[76:79],v[200:203],a[12:15],0\nds_read_b64_tr_b16 v[200:201],v242 offset:50176\nds_read_b64_tr_b16 v[202:203],v242 offset:50432\nM v[76:79],v[204:207],a[28:31],v[76:79]\nbuffer_load_dwordx4 v246,s[40:43],s69 offen offset:3072 lds\nds_read_b64_tr_b16 v[204:205],v242 offset:58368\nds_read_b64_tr_b16 v[206:207],v242 offset:58624\n" \
"M v[16:19],v[208:211],a[0:3],0\ns_add_u32 s80,s80,573440\ns_waitcnt lgkmcnt(6)\nM v[16:19],v[212:215],a[16:19],v[16:19]\ns_add_u32 m0,s65,16384\ns_xor_b32 s67,s66,256\nM v[20:23],v[208:211],a[4:7],0\nv_mov_b32 v232,s70\n" \
"v_mov_b32 v233,s71\nM v[20:23],v[212:215],a[20:23],v[20:23]\ns_lshr_b32 s70,s64,2\ns_add_u32 s71,s70,786432\nM v[80:83],v[208:211],a[8:11],0\nbuffer_load_dwordx4 v224,s[36:39],s67 offen offset:0 lds\nM v[80:83],v[212:215],a[24:27],v[80:83]\nv_lshlrev_b32 v254,14,v254\n" \
"s_add_u32 s78,s78,32768\nM v[84:87],v[208:211],a[12:15],0\nds_read_b64_tr_b16 v[208:209],v241 offset:50688\nds_read_b64_tr_b16 v[210:211],v241 offset:50944\nM v[84:87],v[212:215],a[28:31],v[84:87]\nds_read_b64_tr_b16 v[212:213],v241 offset:58880\nds_read_b64_tr_b16 v[214:215],v241 offset:59136\nM v[24:27],v[216:219],a[0:3],0\n" \
"s_mov_b32 s98,0x4000\ns_mov_b32 s0,28672\nM v[24:27],v[220:223],a[16:19],v[24:27]\nbuffer_load_dwordx4 v225,s[36:39],s67 offen offset:1024 lds\nM v[28:31],v[216:219],a[4:7],0\ns_add_u32 s10,s61,1\ns_add_u32 s11,s61,2\nM v[28:31],v[220:223],a[20:23],v[28:31]\n" \
"s_add_u32 s14,s61,3\ns_and_b32 s10,s10,3\nM v[88:91],v[216:219],a[8:11],0\ns_and_b32 s11,s11,3\ns_and_b32 s14,s14,3\nM v[88:91],v[220:223],a[24:27],v[88:91]\ns_mov_b32 s47,0x20000\ns_mov_b32 s51,0x20000\n" \
"M v[92:95],v[216:219],a[12:15],0\nbuffer_load_dwordx4 v226,s[36:39],s67 offen offset:2048 lds\nds_read_b64_tr_b16 v[216:217],v242 offset:50688\nds_read_b64_tr_b16 v[218:219],v242 offset:50944\nM v[92:95],v[220:223],a[28:31],v[92:95]\nds_read_b64_tr_b16 v[220:221],v242 offset:58880\nds_read_b64_tr_b16 v[222:223],v242 offset:59136\nM v[32:35],v[192:195],a[0:3],0\n" \
"s_lshl_b32 s10,s10,13\ns_waitcnt lgkmcnt(6)\nM v[32:35],v[196:199],a[16:19],v[32:35]\ns_lshl_b32 s11,s11,13\ns_lshl_b32 s14,s14,13\nM v[36:39],v[192:195],a[4:7],0\ns_mov_b32 s82,0xf0000\ns_mov_b32 s83,0xfff00ff\n" \
"M v[36:39],v[196:199],a[20:23],v[36:39]\nbuffer_load_dwordx4 v227,s[36:39],s67 offen offset:3072 lds\nM v[96:99],v[192:195],a[8:11],0\ns_mov_b32 s84,0x1f0001\ns_mov_b32 s85,0x1fff01ff\nM v[96:99],v[196:199],a[24:27],v[96:99]\ns_add_u32 m0,s65,20480\ns_xor_b32 s67,s66,33024\n" \
"M v[100:103],v[192:195],a[12:15],0\ns_mov_b32 s86,0x3f0003\ns_mov_b32 s87,0x3fff03ff\nM v[100:103],v[196:199],a[28:31],v[100:103]\ns_mov_b32 s88,0x7f0007\ns_mov_b32 s89,0x7fff07ff\nM v[40:43],v[200:203],a[0:3],0\nbuffer_load_dwordx4 v224,s[36:39],s67 offen offset:0 lds\n" \
"s_waitcnt vmcnt(11)\nv_lshlrev_b32 v136,16,v128\nv_and_b32 v137,0xffff0000,v128\nv_lshlrev_b32 v138,16,v129\nv_and_b32 v139,0xffff0000,v129\nv_lshlrev_b32 v140,16,v130\nv_and_b32 v141,0xffff0000,v130\nv_lshlrev_b32 v142,16,v131\n" \
"v_and_b32 v143,0xffff0000,v131\nv_lshlrev_b32 v144,16,v132\nv_and_b32 v145,0xffff0000,v132\nv_lshlrev_b32 v146,16,v133\nv_and_b32 v147,0xffff0000,v133\nv_lshlrev_b32 v148,16,v134\nv_and_b32 v149,0xffff0000,v134\nv_lshlrev_b32 v150,16,v135\n" \
"v_and_b32 v151,0xffff0000,v135\nv_add_f32 v168,v136,v140\nv_add_f32 v128,v144,v148\nv_add_f32 v169,v137,v141\nv_add_f32 v129,v145,v149\nv_add_f32 v170,v138,v142\nv_add_f32 v130,v146,v150\nv_add_f32 v171,v139,v143\n" \
"v_add_f32 v131,v147,v151\nv_add_f32 v168,v168,v128\nv_add_f32 v169,v169,v129\nv_add_f32 v170,v170,v130\nv_add_f32 v171,v171,v131\nv_add_f32 v168,v168,v169\nv_add_f32 v170,v170,v171\nv_add_f32 v168,v168,v170\n" \
"M v[40:43],v[204:207],a[16:19],v[40:43]\ns_mov_b64 s[90:91],-1\nv_add_f32_dpp v168,v168,v168 row_ror:8 row_mask:0xf bank_mask:0xf\nv_mov_b32 v170,v168\nM v[44:47],v[200:203],a[4:7],0\ns_mov_b32 s28,0x5f800000\nv_permlane16_swap_b32 v168,v170\nv_add_f32 v168,v168,v170\n" \
"v_mov_b32 v170,v168\ns_nop 1\nv_permlane32_swap_b32 v168,v170\nv_add_f32 v168,v168,v170\nv_mul_f32 v168,0x3c000000,v168\nv_sub_f32 v152,v136,v168\nv_sub_f32 v153,v137,v168\nv_sub_f32 v154,v138,v168\n" \
"v_sub_f32 v155,v139,v168\nv_sub_f32 v156,v140,v168\nv_sub_f32 v157,v141,v168\nv_sub_f32 v158,v142,v168\nv_sub_f32 v159,v143,v168\nv_mul_f32 v128,v152,v152\nv_mul_f32 v129,v153,v153\nv_mul_f32 v130,v154,v154\n" \
"v_mul_f32 v131,v155,v155\nv_sub_f32 v160,v144,v168\nv_sub_f32 v161,v145,v168\nv_sub_f32 v162,v146,v168\nv_sub_f32 v163,v147,v168\nv_fmac_f32 v128,v156,v156\nv_fmac_f32 v129,v157,v157\nv_fmac_f32 v130,v158,v158\n" \
"v_fmac_f32 v131,v159,v159\nv_sub_f32 v164,v148,v168\nv_sub_f32 v165,v149,v168\nv_sub_f32 v166,v150,v168\nv_sub_f32 v167,v151,v168\nv_fmac_f32 v128,v160,v160\nv_fmac_f32 v129,v161,v161\nv_fmac_f32 v130,v162,v162\n" \
"v_fmac_f32 v131,v163,v163\nv_fmac_f32 v128,v164,v164\nv_fmac_f32 v129,v165,v165\nv_fmac_f32 v130,v166,v166\nv_fmac_f32 v131,v167,v167\nv_add_f32 v128,v128,v129\nv_add_f32 v130,v130,v131\nv_add_f32 v169,v128,v130\n" \
"s_nop 1\nv_add_f32_dpp v169,v169,v169 row_ror:8 row_mask:0xf bank_mask:0xf\nv_mov_b32 v170,v169\ns_nop 1\nv_permlane16_swap_b32 v169,v170\nv_add_f32 v169,v169,v170\nv_mov_b32 v170,v169\ns_nop 1\n" \
"v_permlane32_swap_b32 v169,v170\nv_add_f32 v169,v169,v170\nds_write_b64 v191,v[168:169]\nM v[44:47],v[204:207],a[20:23],v[44:47]\nv_and_b32 v169,3,v249\nv_mul_u32_u24 v169,12288,v169\nM v[104:107],v[200:203],a[8:11],0\nv_and_b32 v170,28,v249\n" \
"v_add_u32 v169,v169,v170\nM v[104:107],v[204:207],a[24:27],v[104:107]\nbuffer_load_dwordx4 v225,s[36:39],s67 offen offset:1024 lds\nM v[108:111],v[200:203],a[12:15],0\nv_lshlrev_b32 v170,3,v170\nM v[108:111],v[204:207],a[28:31],v[108:111]\nM v[48:51],v[208:211],a[0:3],0\ns_waitcnt lgkmcnt(1)\n" \
"M v[48:51],v[212:215],a[16:19],v[48:51]\nM v[52:55],v[208:211],a[4:7],0\nbuffer_load_dwordx4 v226,s[36:39],s67 offen offset:2048 lds\nM v[52:55],v[212:215],a[20:23],v[52:55]\nM v[112:115],v[208:211],a[8:11],0\ns_barrier\nds_read_b128 a[32:35],v228 offset:8192\nds_read_b128 a[36:39],v228 offset:10240\n" \
"ds_read_b128 a[40:43],v228 offset:12288\nds_read_b128 a[44:47],v228 offset:14336\nds_read_b128 a[48:51],v229 offset:8192\nds_read_b128 a[52:55],v229 offset:10240\nds_read_b128 a[56:59],v229 offset:12288\nds_read_b128 a[60:63],v229 offset:14336\nds_read_b32 v184,v250 offset:0\nds_read_b32 v185,v250 offset:256\n" \
"ds_read_b32 v186,v250 offset:512\nds_read_b32 v187,v250 offset:768\ns_waitcnt lgkmcnt(2)\nv_add_f32 v184,v184,v185\ns_waitcnt lgkmcnt(0)\nv_add_f32 v186,v186,v187\nv_lshlrev_b32 v185,2,v249\nv_add_f32 v184,v184,v186\n" \
"buffer_store_dword v184,v185,s[52:55],s79 offen sc1\nds_read_b64_tr_b16 v[192:193],v241 offset:0\nds_read_b64_tr_b16 v[194:195],v241 offset:256\nds_read_b64_tr_b16 v[196:197],v241 offset:8192\nds_read_b64_tr_b16 v[198:199],v241 offset:8448\nds_read_b64_tr_b16 v[200:201],v242 offset:0\nds_read_b64_tr_b16 v[202:203],v242 offset:256\nds_read_b64_tr_b16 v[204:205],v242 offset:8192\n" \
"ds_read_b64_tr_b16 v[206:207],v242 offset:8448\nM v[112:115],v[212:215],a[24:27],v[112:115]\nv_bfe_u32 v250,v249,3,2\nM v[116:119],v[208:211],a[12:15],0\nds_read_b64_tr_b16 v[208:209],v241 offset:512\nds_read_b64_tr_b16 v[210:211],v241 offset:768\nM v[116:119],v[212:215],a[28:31],v[116:119]\nbuffer_load_dwordx4 v227,s[36:39],s67 offen offset:3072 lds\n" \
"ds_read_b64_tr_b16 v[212:213],v241 offset:8704\nds_read_b64_tr_b16 v[214:215],v241 offset:8960\nM v[56:59],v[216:219],a[0:3],0\nM v[56:59],v[220:223],a[16:19],v[56:59]\ns_add_u32 m0,s65,0\ns_xor_b32 s67,s66,384\nM v[60:63],v[216:219],a[4:7],0\nM v[60:63],v[220:223],a[20:23],v[60:63]\n" \
"M v[120:123],v[216:219],a[8:11],0\nbuffer_load_dwordx4 v224,s[36:39],s67 offen offset:0 lds\nM v[120:123],v[220:223],a[24:27],v[120:123]\nM v[124:127],v[216:219],a[12:15],0\nds_read_b64_tr_b16 v[216:217],v242 offset:512\nds_read_b64_tr_b16 v[218:219],v242 offset:768\nM v[124:127],v[220:223],a[28:31],v[124:127]\nds_read_b64_tr_b16 v[220:221],v242 offset:8704\n" \
"ds_read_b64_tr_b16 v[222:223],v242 offset:8960\ns_waitcnt lgkmcnt(11)\nM v[0:3],v[192:195],a[32:35],v[0:3]\nM v[0:3],v[196:199],a[48:51],v[0:3]\nbuffer_load_dwordx4 v225,s[36:39],s67 offen offset:1024 lds\nM v[4:7],v[192:195],a[36:39],v[4:7]\nM v[4:7],v[196:199],a[52:55],v[4:7]\nM v[64:67],v[192:195],a[40:43],v[64:67]\n" \
"M v[64:67],v[196:199],a[56:59],v[64:67]\nM v[68:71],v[192:195],a[44:47],v[68:71]\nbuffer_load_dwordx4 v226,s[36:39],s67 offen offset:2048 lds\nds_read_b64_tr_b16 v[192:193],v241 offset:1024\nds_read_b64_tr_b16 v[194:195],v241 offset:1280\nM v[68:71],v[196:199],a[60:63],v[68:71]\nds_read_b64_tr_b16 v[196:197],v241 offset:9216\nds_read_b64_tr_b16 v[198:199],v241 offset:9472\n" \
"s_waitcnt lgkmcnt(8)\nM v[8:11],v[200:203],a[32:35],v[8:11]\nM v[8:11],v[204:207],a[48:51],v[8:11]\nM v[12:15],v[200:203],a[36:39],v[12:15]\nM v[12:15],v[204:207],a[52:55],v[12:15]\nbuffer_load_dwordx4 v227,s[36:39],s67 offen offset:3072 lds\nM v[72:75],v[200:203],a[40:43],v[72:75]\nM v[72:75],v[204:207],a[56:59],v[72:75]\n" \
"s_add_u32 m0,s65,4096\ns_xor_b32 s67,s66,33152\nM v[76:79],v[200:203],a[44:47],v[76:79]\nds_read_b64_tr_b16 v[200:201],v242 offset:1024\nds_read_b64_tr_b16 v[202:203],v242 offset:1280\nM v[76:79],v[204:207],a[60:63],v[76:79]\nds_read_b64_tr_b16 v[204:205],v242 offset:9216\nds_read_b64_tr_b16 v[206:207],v242 offset:9472\n" \
"M v[16:19],v[208:211],a[32:35],v[16:19]\nbuffer_load_dwordx4 v224,s[36:39],s67 offen offset:0 lds\nM v[16:19],v[212:215],a[48:51],v[16:19]\nM v[20:23],v[208:211],a[36:39],v[20:23]\nM v[20:23],v[212:215],a[52:55],v[20:23]\nM v[80:83],v[208:211],a[40:43],v[80:83]\nM v[80:83],v[212:215],a[56:59],v[80:83]\nbuffer_load_dwordx4 v225,s[36:39],s67 offen offset:1024 lds\n" \
"M v[84:87],v[208:211],a[44:47],v[84:87]\nds_read_b64_tr_b16 v[208:209],v241 offset:1536\nds_read_b64_tr_b16 v[210:211],v241 offset:1792\nM v[84:87],v[212:215],a[60:63],v[84:87]\nds_read_b64_tr_b16 v[212:213],v241 offset:9728\nds_read_b64_tr_b16 v[214:215],v241 offset:9984\ns_waitcnt lgkmcnt(8)\nM v[24:27],v[216:219],a[32:35],v[24:27]\n" \
"M v[24:27],v[220:223],a[48:51],v[24:27]\nM v[28:31],v[216:219],a[36:39],v[28:31]\nbuffer_load_dwordx4 v226,s[36:39],s67 offen offset:2048 lds\nM v[28:31],v[220:223],a[52:55],v[28:31]\nM v[88:91],v[216:219],a[40:43],v[88:91]\nM v[88:91],v[220:223],a[56:59],v[88:91]\nM v[92:95],v[216:219],a[44:47],v[92:95]\nds_read_b64_tr_b16 v[216:217],v242 offset:1536\n" \
"ds_read_b64_tr_b16 v[218:219],v242 offset:1792\nM v[92:95],v[220:223],a[60:63],v[92:95]\nbuffer_load_dwordx4 v227,s[36:39],s67 offen offset:3072 lds\nds_read_b64_tr_b16 v[220:221],v242 offset:9728\nds_read_b64_tr_b16 v[222:223],v242 offset:9984\nM v[32:35],v[192:195],a[32:35],v[32:35]\nM v[32:35],v[196:199],a[48:51],v[32:35]\ns_add_u32 m0,s64,147456\n" \
"s_add_u32 s69,s68,1572864\nM v[36:39],v[192:195],a[36:39],v[36:39]\nM v[36:39],v[196:199],a[52:55],v[36:39]\nM v[96:99],v[192:195],a[40:43],v[96:99]\nbuffer_load_dwordx4 v243,s[40:43],s69 offen offset:0 lds\nM v[96:99],v[196:199],a[56:59],v[96:99]\nM v[100:103],v[192:195],a[44:47],v[100:103]\nM v[100:103],v[196:199],a[60:63],v[100:103]\n" \
"s_waitcnt lgkmcnt(4)\nM v[40:43],v[200:203],a[32:35],v[40:43]\nM v[40:43],v[204:207],a[48:51],v[40:43]\nbuffer_load_dwordx4 v244,s[40:43],s69 offen offset:1024 lds\nM v[44:47],v[200:203],a[36:39],v[44:47]\nM v[44:47],v[204:207],a[52:55],v[44:47]\nM v[104:107],v[200:203],a[40:43],v[104:107]\nM v[104:107],v[204:207],a[56:59],v[104:107]\n" \
"M v[108:111],v[200:203],a[44:47],v[108:111]\nbuffer_load_dwordx4 v245,s[40:43],s69 offen offset:2048 lds\nM v[108:111],v[204:207],a[60:63],v[108:111]\nM v[48:51],v[208:211],a[32:35],v[48:51]\nM v[48:51],v[212:215],a[48:51],v[48:51]\nM v[52:55],v[208:211],a[36:39],v[52:55]\nM v[52:55],v[212:215],a[52:55],v[52:55]\nbuffer_load_dwordx4 v246,s[40:43],s69 offen offset:3072 lds\n" \
"M v[112:115],v[208:211],a[40:43],v[112:115]\ns_waitcnt vmcnt(12) lgkmcnt(0)\ns_barrier\nds_read_b128 a[64:67],v228 offset:16384\nds_read_b128 a[68:71],v228 offset:18432\nds_read_b128 a[72:75],v228 offset:20480\nds_read_b128 a[76:79],v228 offset:22528\nds_read_b128 a[80:83],v229 offset:16384\n" \
"ds_read_b128 a[84:87],v229 offset:18432\nds_read_b128 a[88:91],v229 offset:20480\nds_read_b128 a[92:95],v229 offset:22528\nM v[112:115],v[212:215],a[56:59],v[112:115]\ns_add_u32 m0,s65,8192\ns_xor_b32 s67,s66,512\nds_read_b64_tr_b16 v[192:193],v241 offset:32768\nds_read_b64_tr_b16 v[194:195],v241 offset:33024\n" \
"M v[116:119],v[208:211],a[44:47],v[116:119]\nbuffer_store_dwordx2 v[230:231],v234,s[52:55],s72 offen sc1\nds_read_b64_tr_b16 v[196:197],v241 offset:40960\nds_read_b64_tr_b16 v[198:199],v241 offset:41216\nM v[116:119],v[212:215],a[60:63],v[116:119]\ns_cmp_eq_u32 s6,0\ns_cselect_b32 s72,s75,0x8000000\nM v[56:59],v[216:219],a[32:35],v[56:59]\n" \
"buffer_load_dwordx4 v224,s[36:39],s67 offen offset:0 lds\nM v[56:59],v[220:223],a[48:51],v[56:59]\ns_add_u32 s72,s72,94208\nM v[60:63],v[216:219],a[36:39],v[60:63]\nM v[60:63],v[220:223],a[52:55],v[60:63]\nds_read_b64_tr_b16 v[200:201],v242 offset:32768\nds_read_b64_tr_b16 v[202:203],v242 offset:33024\nds_read_b64_tr_b16 v[204:205],v242 offset:40960\n" \
"ds_read_b64_tr_b16 v[206:207],v242 offset:41216\nds_read_b64_tr_b16 v[208:209],v241 offset:33280\nds_read_b64_tr_b16 v[210:211],v241 offset:33536\nds_read_b64_tr_b16 v[212:213],v241 offset:41472\nds_read_b64_tr_b16 v[214:215],v241 offset:41728\nM v[120:123],v[216:219],a[40:43],v[120:123]\nM v[120:123],v[220:223],a[56:59],v[120:123]\nbuffer_load_dwordx4 v225,s[36:39],s67 offen offset:1024 lds\n" \
"M v[124:127],v[216:219],a[44:47],v[124:127]\nds_read_b64_tr_b16 v[216:217],v242 offset:33280\nds_read_b64_tr_b16 v[218:219],v242 offset:33536\nM v[124:127],v[220:223],a[60:63],v[124:127]\nds_read_b64_tr_b16 v[220:221],v242 offset:41472\nds_read_b64_tr_b16 v[222:223],v242 offset:41728\ns_waitcnt lgkmcnt(14)\nM v[0:3],v[192:195],a[64:67],v[0:3]\n" \
"s_waitcnt lgkmcnt(12)\nM v[0:3],v[196:199],a[80:83],v[0:3]\nM v[4:7],v[192:195],a[68:71],v[4:7]\nbuffer_load_dwordx4 v226,s[36:39],s67 offen offset:2048 lds\nM v[4:7],v[196:199],a[84:87],v[4:7]\nds_read_b128 v[156:159],v251\nM v[64:67],v[192:195],a[72:75],v[64:67]\nds_read_b128 v[160:163],v251 offset:16\n" \
"v_lshrrev_b32 v251,3,v249\nM v[64:67],v[196:199],a[88:91],v[64:67]\nM v[68:71],v[192:195],a[76:79],v[68:71]\nds_read_b64_tr_b16 v[192:193],v241 offset:33792\nds_read_b64_tr_b16 v[194:195],v241 offset:34048\nM v[68:71],v[196:199],a[92:95],v[68:71]\nbuffer_load_dwordx4 v227,s[36:39],s67 offen offset:3072 lds\nds_read_b64_tr_b16 v[196:197],v241 offset:41984\n" \
"ds_read_b64_tr_b16 v[198:199],v241 offset:42240\ns_waitcnt lgkmcnt(10)\nM v[8:11],v[200:203],a[64:67],v[8:11]\nM v[8:11],v[204:207],a[80:83],v[8:11]\ns_add_u32 m0,s65,12288\ns_xor_b32 s67,s66,33280\nM v[12:15],v[200:203],a[68:71],v[12:15]\nM v[12:15],v[204:207],a[84:87],v[12:15]\n" \
"M v[72:75],v[200:203],a[72:75],v[72:75]\nbuffer_load_dwordx4 v224,s[36:39],s67 offen offset:0 lds\nM v[72:75],v[204:207],a[88:91],v[72:75]\ns_waitcnt lgkmcnt(5)\nv_add_f32 v164,v156,v158\nM v[76:79],v[200:203],a[76:79],v[76:79]\nds_read_b64_tr_b16 v[200:201],v242 offset:33792\nds_read_b64_tr_b16 v[202:203],v242 offset:34048\n" \
"M v[76:79],v[204:207],a[92:95],v[76:79]\nds_read_b64_tr_b16 v[204:205],v242 offset:41984\nds_read_b64_tr_b16 v[206:207],v242 offset:42240\nM v[16:19],v[208:211],a[64:67],v[16:19]\ns_waitcnt lgkmcnt(8)\nv_add_f32 v165,v160,v162\nM v[16:19],v[212:215],a[80:83],v[16:19]\nbuffer_load_dwordx4 v225,s[36:39],s67 offen offset:1024 lds\n" \
"M v[20:23],v[208:211],a[68:71],v[20:23]\nv_add_f32 v164,v164,v165\nv_lshlrev_b32 v165,3,v249\nM v[20:23],v[212:215],a[84:87],v[20:23]\nv_mul_f32 v164,0x3e800000,v164\nv_sub_f32 v156,v156,v164\nM v[80:83],v[208:211],a[72:75],v[80:83]\nv_sub_f32 v158,v158,v164\n" \
"v_mul_f32 v156,v156,v156\nM v[80:83],v[212:215],a[88:91],v[80:83]\nv_sub_f32 v160,v160,v164\nv_fmac_f32 v156,v158,v158\nM v[84:87],v[208:211],a[76:79],v[84:87]\nbuffer_load_dwordx4 v226,s[36:39],s67 offen offset:2048 lds\nds_read_b64_tr_b16 v[208:209],v241 offset:34304\nds_read_b64_tr_b16 v[210:211],v241 offset:34560\n" \
"M v[84:87],v[212:215],a[92:95],v[84:87]\nds_read_b64_tr_b16 v[212:213],v241 offset:42496\nds_read_b64_tr_b16 v[214:215],v241 offset:42752\nM v[24:27],v[216:219],a[64:67],v[24:27]\nv_add_f32 v157,v157,v159\nv_add_f32 v161,v161,v163\nM v[24:27],v[220:223],a[80:83],v[24:27]\nv_sub_f32 v162,v162,v164\n" \
"v_fmac_f32 v156,v160,v160\nM v[28:31],v[216:219],a[68:71],v[28:31]\nglobal_load_dword v247,v253,s[16:17]\nM v[28:31],v[220:223],a[84:87],v[28:31]\nbuffer_load_dwordx4 v227,s[36:39],s67 offen offset:3072 lds\nM v[88:91],v[216:219],a[72:75],v[88:91]\nv_add_u32 v253,0x100000,v253\nv_add_f32 v157,v157,v161\n" \
"M v[88:91],v[220:223],a[88:91],v[88:91]\ns_add_u32 m0,s64,98304\ns_add_u32 s69,s68,2097152\nM v[92:95],v[216:219],a[76:79],v[92:95]\nds_read_b64_tr_b16 v[216:217],v242 offset:34304\nds_read_b64_tr_b16 v[218:219],v242 offset:34560\nM v[92:95],v[220:223],a[92:95],v[92:95]\nds_read_b64_tr_b16 v[220:221],v242 offset:42496\n" \
"ds_read_b64_tr_b16 v[222:223],v242 offset:42752\ns_waitcnt lgkmcnt(8)\nM v[32:35],v[192:195],a[64:67],v[32:35]\nbuffer_load_dwordx4 v243,s[40:43],s69 offen offset:0 lds\nM v[32:35],v[196:199],a[80:83],v[32:35]\nv_fmac_f32 v156,v162,v162\nv_fmac_f32 v157,0x43000000,v156\nM v[36:39],v[192:195],a[68:71],v[36:39]\n" \
"v_mul_f32 v157,0x3b000000,v157\nv_cvt_pk_bf16_f32 v157,0,v157\nM v[36:39],v[196:199],a[84:87],v[36:39]\nv_add_f32 v157,0x3727c5ac,v157\nv_cvt_pk_bf16_f32 v157,0,v157\nM v[96:99],v[192:195],a[72:75],v[96:99]\nv_sqrt_f32 v157,v157\nM v[96:99],v[196:199],a[88:91],v[96:99]\n" \
"buffer_load_dwordx4 v244,s[40:43],s69 offen offset:1024 lds\nM v[100:103],v[192:195],a[76:79],v[100:103]\nv_cvt_pk_bf16_f32 v164,0,v164\nv_cvt_pk_bf16_f32 v157,0,v157\nM v[100:103],v[196:199],a[92:95],v[100:103]\nv_rcp_f32 v157,v157\nM v[40:43],v[200:203],a[64:67],v[40:43]\nv_sub_f32 v136,v136,v164\n" \
"v_sub_f32 v137,v137,v164\nM v[40:43],v[204:207],a[80:83],v[40:43]\nv_sub_f32 v138,v138,v164\nv_sub_f32 v139,v139,v164\nM v[44:47],v[200:203],a[68:71],v[44:47]\nbuffer_load_dwordx4 v245,s[40:43],s69 offen offset:2048 lds\nM v[44:47],v[204:207],a[84:87],v[44:47]\nv_sub_f32 v140,v140,v164\n" \
"v_sub_f32 v141,v141,v164\nM v[104:107],v[200:203],a[72:75],v[104:107]\nv_sub_f32 v142,v142,v164\nv_sub_f32 v143,v143,v164\nM v[104:107],v[204:207],a[88:91],v[104:107]\nv_sub_f32 v144,v144,v164\nv_sub_f32 v145,v145,v164\nM v[108:111],v[200:203],a[76:79],v[108:111]\n" \
"v_sub_f32 v146,v146,v164\nv_sub_f32 v147,v147,v164\nM v[108:111],v[204:207],a[92:95],v[108:111]\nbuffer_load_dwordx4 v246,s[40:43],s69 offen offset:3072 lds\ns_waitcnt lgkmcnt(0)\nM v[48:51],v[208:211],a[64:67],v[48:51]\nv_sub_f32 v148,v148,v164\nv_sub_f32 v149,v149,v164\n" \
"M v[48:51],v[212:215],a[80:83],v[48:51]\ns_add_u32 m0,s65,16384\ns_xor_b32 s67,s66,640\nM v[52:55],v[208:211],a[68:71],v[52:55]\nv_sub_f32 v150,v150,v164\nv_sub_f32 v151,v151,v164\nM v[52:55],v[212:215],a[84:87],v[52:55]\nv_cvt_pk_bf16_f32 v136,0,v136\n" \
"v_cvt_pk_bf16_f32 v137,0,v137\nM v[112:115],v[208:211],a[72:75],v[112:115]\nbuffer_load_dwordx4 v224,s[36:39],s67 offen offset:0 lds\ns_waitcnt vmcnt(15)\ns_barrier\nds_read_b128 a[96:99],v228 offset:0\nds_read_b128 a[100:103],v228 offset:2048\nds_read_b128 a[104:107],v228 offset:4096\n" \
"ds_read_b128 a[108:111],v228 offset:6144\nds_read_b128 a[112:115],v229 offset:0\nds_read_b128 a[116:119],v229 offset:2048\nds_read_b128 a[120:123],v229 offset:4096\nds_read_b128 a[124:127],v229 offset:6144\nM v[112:115],v[212:215],a[88:91],v[112:115]\nds_read_b64_tr_b16 v[192:193],v241 offset:49152\nds_read_b64_tr_b16 v[194:195],v241 offset:49408\n" \
"M v[116:119],v[208:211],a[76:79],v[116:119]\nds_read_b64_tr_b16 v[196:197],v241 offset:57344\nds_read_b64_tr_b16 v[198:199],v241 offset:57600\nM v[116:119],v[212:215],a[92:95],v[116:119]\nv_cvt_pk_bf16_f32 v138,0,v138\nv_cvt_pk_bf16_f32 v139,0,v139\nM v[56:59],v[216:219],a[64:67],v[56:59]\nv_cvt_pk_bf16_f32 v140,0,v140\n" \
"v_cvt_pk_bf16_f32 v141,0,v141\nM v[56:59],v[220:223],a[80:83],v[56:59]\nbuffer_load_dwordx4 v225,s[36:39],s67 offen offset:1024 lds\nM v[60:63],v[216:219],a[68:71],v[60:63]\nv_cvt_pk_bf16_f32 v142,0,v142\nv_cvt_pk_bf16_f32 v143,0,v143\nM v[60:63],v[220:223],a[84:87],v[60:63]\nv_cvt_pk_bf16_f32 v144,0,v144\n" \
"v_cvt_pk_bf16_f32 v145,0,v145\nM v[120:123],v[216:219],a[72:75],v[120:123]\nv_cvt_pk_bf16_f32 v146,0,v146\nv_cvt_pk_bf16_f32 v147,0,v147\nM v[120:123],v[220:223],a[88:91],v[120:123]\nds_read_b64_tr_b16 v[200:201],v242 offset:49152\nds_read_b64_tr_b16 v[202:203],v242 offset:49408\nM v[124:127],v[216:219],a[76:79],v[124:127]\n" \
"buffer_load_dwordx4 v226,s[36:39],s67 offen offset:2048 lds\nds_read_b64_tr_b16 v[204:205],v242 offset:57344\nds_read_b64_tr_b16 v[206:207],v242 offset:57600\nM v[124:127],v[220:223],a[92:95],v[124:127]\nds_read_b64_tr_b16 v[208:209],v241 offset:49664\ns_waitcnt lgkmcnt(7)\nM v[0:3],v[192:195],a[96:99],v[0:3]\nds_read_b64_tr_b16 v[210:211],v241 offset:49920\n" \
"s_waitcnt lgkmcnt(6)\nM v[0:3],v[196:199],a[112:115],v[0:3]\nds_read_b64_tr_b16 v[212:213],v241 offset:57856\nds_read_b64_tr_b16 v[214:215],v241 offset:58112\nM v[4:7],v[192:195],a[100:103],v[4:7]\nglobal_load_dword v247,v253,s[16:17]\nM v[4:7],v[196:199],a[116:119],v[4:7]\nbuffer_load_dwordx4 v227,s[36:39],s67 offen offset:3072 lds\n" \
"M v[64:67],v[192:195],a[104:107],v[64:67]\nds_read_b64_tr_b16 v[216:217],v242 offset:49664\nds_read_b64_tr_b16 v[218:219],v242 offset:49920\nM v[64:67],v[196:199],a[120:123],v[64:67]\ns_add_u32 m0,s65,20480\ns_xor_b32 s67,s66,33408\nM v[68:71],v[192:195],a[108:111],v[68:71]\nds_read_b64_tr_b16 v[220:221],v242 offset:57856\n" \
"ds_read_b64_tr_b16 v[222:223],v242 offset:58112\nM v[68:71],v[196:199],a[124:127],v[68:71]\nds_read_b64_tr_b16 v[192:193],v241 offset:50176\ns_waitcnt lgkmcnt(11)\nM v[8:11],v[200:203],a[96:99],v[8:11]\nbuffer_load_dwordx4 v224,s[36:39],s67 offen offset:0 lds\ns_waitcnt lgkmcnt(9)\nM v[8:11],v[204:207],a[112:115],v[8:11]\n" \
"ds_read_b64_tr_b16 v[194:195],v241 offset:50432\nds_read_b64_tr_b16 v[196:197],v241 offset:58368\nM v[12:15],v[200:203],a[100:103],v[12:15]\nds_read_b64_tr_b16 v[198:199],v241 offset:58624\nv_add_u32 v253,0x100000,v253\nM v[12:15],v[204:207],a[116:119],v[12:15]\nv_cvt_pk_bf16_f32 v148,0,v148\nv_cvt_pk_bf16_f32 v149,0,v149\n" \
"M v[72:75],v[200:203],a[104:107],v[72:75]\nv_cvt_pk_bf16_f32 v150,0,v150\nv_cvt_pk_bf16_f32 v151,0,v151\nM v[72:75],v[204:207],a[120:123],v[72:75]\nbuffer_load_dwordx4 v225,s[36:39],s67 offen offset:1024 lds\nM v[76:79],v[200:203],a[108:111],v[76:79]\nds_read_b64_tr_b16 v[200:201],v242 offset:50176\nds_read_b64_tr_b16 v[202:203],v242 offset:50432\n" \
"M v[76:79],v[204:207],a[124:127],v[76:79]\nds_read_b64_tr_b16 v[204:205],v242 offset:58368\ns_waitcnt lgkmcnt(9)\nM v[16:19],v[208:211],a[96:99],v[16:19]\nds_read_b64_tr_b16 v[206:207],v242 offset:58624\nv_mul_f32 v136,v136,v157\nM v[16:19],v[212:215],a[112:115],v[16:19]\nv_mul_f32 v137,v137,v157\n" \
"v_mul_f32 v138,v138,v157\nM v[20:23],v[208:211],a[100:103],v[20:23]\nbuffer_load_dwordx4 v226,s[36:39],s67 offen offset:2048 lds\nM v[20:23],v[212:215],a[116:119],v[20:23]\nv_mul_f32 v139,v139,v157\nv_mul_f32 v140,v140,v157\nM v[80:83],v[208:211],a[104:107],v[80:83]\nv_mul_f32 v141,v141,v157\n" \
"v_mul_f32 v142,v142,v157\nM v[80:83],v[212:215],a[120:123],v[80:83]\nv_mul_f32 v143,v143,v157\nv_mul_f32 v144,v144,v157\nM v[84:87],v[208:211],a[108:111],v[84:87]\nds_read_b64_tr_b16 v[208:209],v241 offset:50688\nds_read_b64_tr_b16 v[210:211],v241 offset:50944\nM v[84:87],v[212:215],a[124:127],v[84:87]\n" \
"buffer_load_dwordx4 v227,s[36:39],s67 offen offset:3072 lds\nM v[24:27],v[216:219],a[96:99],v[24:27]\nds_read_b64_tr_b16 v[212:213],v241 offset:58880\ns_waitcnt lgkmcnt(6)\nM v[24:27],v[220:223],a[112:115],v[24:27]\ns_add_u32 m0,s64,114688\ns_add_u32 s69,s68,2621440\nM v[28:31],v[216:219],a[100:103],v[28:31]\n" \
"ds_read_b64_tr_b16 v[214:215],v241 offset:59136\nv_mul_f32 v145,v145,v157\nM v[28:31],v[220:223],a[116:119],v[28:31]\nv_mul_f32 v146,v146,v157\nv_mul_f32 v147,v147,v157\nM v[88:91],v[216:219],a[104:107],v[88:91]\nbuffer_load_dwordx4 v243,s[40:43],s69 offen offset:0 lds\nM v[88:91],v[220:223],a[120:123],v[88:91]\n" \
"v_mul_f32 v148,v148,v157\nv_mul_f32 v149,v149,v157\nM v[92:95],v[216:219],a[108:111],v[92:95]\nds_read_b64_tr_b16 v[216:217],v242 offset:50688\nds_read_b64_tr_b16 v[218:219],v242 offset:50944\nM v[92:95],v[220:223],a[124:127],v[92:95]\nds_read_b64_tr_b16 v[220:221],v242 offset:58880\nds_read_b64_tr_b16 v[222:223],v242 offset:59136\n" \
"M v[32:35],v[192:195],a[96:99],v[32:35]\nbuffer_load_dwordx2 v[166:167],v165,s[52:55],s80 offen sc1\nM v[32:35],v[196:199],a[112:115],v[32:35]\nbuffer_load_dwordx4 v244,s[40:43],s69 offen offset:1024 lds\nM v[36:39],v[192:195],a[100:103],v[36:39]\nv_mul_f32 v150,v150,v157\nv_mul_f32 v151,v151,v157\nM v[36:39],v[196:199],a[116:119],v[36:39]\n" \
"v_cvt_pk_bf16_f32 v144,0,v144\nv_cvt_pk_bf16_f32 v145,0,v145\nM v[96:99],v[192:195],a[104:107],v[96:99]\nv_cvt_pk_bf16_f32 v136,0,v136\nv_cvt_pk_bf16_f32 v137,0,v137\nM v[96:99],v[196:199],a[120:123],v[96:99]\nv_cvt_pk_bf16_f32 v138,0,v138\nv_cvt_pk_bf16_f32 v139,0,v139\n" \
"M v[100:103],v[192:195],a[108:111],v[100:103]\nbuffer_load_dwordx4 v245,s[40:43],s69 offen offset:2048 lds\nM v[100:103],v[196:199],a[124:127],v[100:103]\nv_cvt_pk_bf16_f32 v140,0,v140\ns_waitcnt lgkmcnt(4)\nM v[40:43],v[200:203],a[96:99],v[40:43]\nv_cvt_pk_bf16_f32 v141,0,v141\nv_cvt_pk_bf16_f32 v142,0,v142\n" \
"M v[40:43],v[204:207],a[112:115],v[40:43]\nv_cvt_pk_bf16_f32 v143,0,v143\nv_cvt_pk_bf16_f32 v146,0,v146\nM v[44:47],v[200:203],a[100:103],v[44:47]\nglobal_load_dword v247,v253,s[16:17]\nM v[44:47],v[204:207],a[116:119],v[44:47]\nbuffer_load_dwordx4 v246,s[40:43],s69 offen offset:3072 lds\nM v[104:107],v[200:203],a[104:107],v[104:107]\n" \
"v_add_u32 v253,0x100000,v253\nv_cvt_pk_bf16_f32 v147,0,v147\nM v[104:107],v[204:207],a[120:123],v[104:107]\ns_add_u32 m0,s65,0\ns_xor_b32 s67,s66,768\nM v[108:111],v[200:203],a[108:111],v[108:111]\nv_cvt_pk_bf16_f32 v148,0,v148\nv_cvt_pk_bf16_f32 v149,0,v149\n" \
"M v[108:111],v[204:207],a[124:127],v[108:111]\nv_cvt_pk_bf16_f32 v150,0,v150\nv_cvt_pk_bf16_f32 v151,0,v151\nM v[48:51],v[208:211],a[96:99],v[48:51]\nbuffer_load_dwordx4 v224,s[36:39],s67 offen offset:0 lds\nM v[48:51],v[212:215],a[112:115],v[48:51]\nM v[52:55],v[208:211],a[100:103],v[52:55]\nM v[52:55],v[212:215],a[116:119],v[52:55]\n" \
"M v[112:115],v[208:211],a[104:107],v[112:115]\ns_waitcnt vmcnt(16) lgkmcnt(0)\ns_barrier\nM v[112:115],v[212:215],a[120:123],v[112:115]\nbuffer_load_dwordx4 v225,s[36:39],s67 offen offset:1024 lds\nds_read_b128 a[128:131],v228 offset:8192\nds_read_b64_tr_b16 v[192:193],v241 offset:0\nds_read_b64_tr_b16 v[194:195],v241 offset:256\n" \
"M v[116:119],v[208:211],a[108:111],v[116:119]\nds_read_b128 a[144:147],v229 offset:8192\nds_read_b64_tr_b16 v[196:197],v241 offset:8192\nds_read_b64_tr_b16 v[198:199],v241 offset:8448\nM v[116:119],v[212:215],a[124:127],v[116:119]\nds_read_b128 a[132:135],v228 offset:10240\nM v[56:59],v[216:219],a[96:99],v[56:59]\nds_read_b128 a[148:151],v229 offset:10240\n" \
"M v[56:59],v[220:223],a[112:115],v[56:59]\nds_read_b128 a[136:139],v228 offset:12288\nM v[60:63],v[216:219],a[100:103],v[60:63]\nbuffer_load_dwordx4 v226,s[36:39],s67 offen offset:2048 lds\nds_read_b128 a[152:155],v229 offset:12288\nM v[60:63],v[220:223],a[116:119],v[60:63]\nds_read_b128 a[140:143],v228 offset:14336\nM v[120:123],v[216:219],a[104:107],v[120:123]\n" \
"ds_read_b128 a[156:159],v229 offset:14336\nM v[120:123],v[220:223],a[120:123],v[120:123]\nds_read_b64_tr_b16 v[200:201],v242 offset:0\nds_read_b64_tr_b16 v[202:203],v242 offset:256\nM v[124:127],v[216:219],a[108:111],v[124:127]\nds_read_b64_tr_b16 v[204:205],v242 offset:8192\nds_read_b64_tr_b16 v[206:207],v242 offset:8448\nM v[124:127],v[220:223],a[124:127],v[124:127]\n" \
"buffer_load_dwordx4 v227,s[36:39],s67 offen offset:3072 lds\ns_waitcnt lgkmcnt(13)\nM v[0:3],v[192:195],a[128:131],v[0:3]\nds_read_b64_tr_b16 v[208:209],v241 offset:512\ns_waitcnt lgkmcnt(11)\nM v[0:3],v[196:199],a[144:147],v[0:3]\ns_add_u32 m0,s65,4096\ns_xor_b32 s67,s66,33536\n" \
"s_waitcnt lgkmcnt(10)\nM v[4:7],v[192:195],a[132:135],v[4:7]\nds_read_b64_tr_b16 v[210:211],v241 offset:768\ns_waitcnt lgkmcnt(10)\nM v[4:7],v[196:199],a[148:151],v[4:7]\nds_read_b64_tr_b16 v[212:213],v241 offset:8704\ns_waitcnt lgkmcnt(10)\nM v[64:67],v[192:195],a[136:139],v[64:67]\n" \
"buffer_load_dwordx4 v224,s[36:39],s67 offen offset:0 lds\ns_waitcnt lgkmcnt(9)\nM v[64:67],v[196:199],a[152:155],v[64:67]\nds_read_b64_tr_b16 v[214:215],v241 offset:8960\ns_waitcnt lgkmcnt(9)\nM v[68:71],v[192:195],a[140:143],v[68:71]\nds_read_b64_tr_b16 v[216:217],v242 offset:512\ns_waitcnt lgkmcnt(9)\n" \
"M v[68:71],v[196:199],a[156:159],v[68:71]\nds_read_b64_tr_b16 v[218:219],v242 offset:768\ns_waitcnt lgkmcnt(8)\nM v[8:11],v[200:203],a[128:131],v[8:11]\nds_read_b64_tr_b16 v[220:221],v242 offset:8704\ns_waitcnt lgkmcnt(7)\nM v[8:11],v[204:207],a[144:147],v[8:11]\nbuffer_load_dwordx4 v225,s[36:39],s67 offen offset:1024 lds\n" \
"M v[12:15],v[200:203],a[132:135],v[12:15]\nds_read_b64_tr_b16 v[222:223],v242 offset:8960\nds_read_b64_tr_b16 v[192:193],v241 offset:1024\nM v[12:15],v[204:207],a[148:151],v[12:15]\nds_read_b64_tr_b16 v[194:195],v241 offset:1280\nds_read_b64_tr_b16 v[196:197],v241 offset:9216\nM v[72:75],v[200:203],a[136:139],v[72:75]\nds_read_b64_tr_b16 v[198:199],v241 offset:9472\n" \
"M v[72:75],v[204:207],a[152:155],v[72:75]\nM v[76:79],v[200:203],a[140:143],v[76:79]\nbuffer_load_dwordx4 v226,s[36:39],s67 offen offset:2048 lds\nM v[76:79],v[204:207],a[156:159],v[76:79]\nds_read_b64_tr_b16 v[200:201],v242 offset:1024\ns_waitcnt lgkmcnt(10)\nM v[16:19],v[208:211],a[128:131],v[16:19]\nds_read_b64_tr_b16 v[202:203],v242 offset:1280\n" \
"s_waitcnt lgkmcnt(10)\nM v[16:19],v[212:215],a[144:147],v[16:19]\nds_read_b64_tr_b16 v[204:205],v242 offset:9216\nds_read_b64_tr_b16 v[206:207],v242 offset:9472\nM v[20:23],v[208:211],a[132:135],v[20:23]\nglobal_load_dword v247,v253,s[16:17]\nM v[20:23],v[212:215],a[148:151],v[20:23]\nbuffer_load_dwordx4 v227,s[36:39],s67 offen offset:3072 lds\n" \
"M v[80:83],v[208:211],a[136:139],v[80:83]\nv_add_u32 v253,0xffd00000,v253\nM v[80:83],v[212:215],a[152:155],v[80:83]\ns_add_u32 m0,s64,131072\ns_add_u32 s69,s68,3145728\nM v[84:87],v[208:211],a[140:143],v[84:87]\nds_read_b64_tr_b16 v[208:209],v241 offset:1536\nds_read_b64_tr_b16 v[210:211],v241 offset:1792\n" \
"M v[84:87],v[212:215],a[156:159],v[84:87]\nds_read_b64_tr_b16 v[212:213],v241 offset:9728\ns_waitcnt lgkmcnt(7)\nM v[24:27],v[216:219],a[128:131],v[24:27]\nbuffer_load_dwordx4 v243,s[40:43],s69 offen offset:0 lds\nM v[24:27],v[220:223],a[144:147],v[24:27]\nds_read_b64_tr_b16 v[214:215],v241 offset:9984\nM v[28:31],v[216:219],a[132:135],v[28:31]\n" \
"M v[28:31],v[220:223],a[148:151],v[28:31]\nM v[88:91],v[216:219],a[136:139],v[88:91]\nM v[88:91],v[220:223],a[152:155],v[88:91]\nbuffer_load_dwordx4 v244,s[40:43],s69 offen offset:1024 lds\nM v[92:95],v[216:219],a[140:143],v[92:95]\nds_read_b64_tr_b16 v[216:217],v242 offset:1536\nds_read_b64_tr_b16 v[218:219],v242 offset:1792\nM v[92:95],v[220:223],a[156:159],v[92:95]\n" \
"ds_read_b64_tr_b16 v[220:221],v242 offset:9728\nds_read_b64_tr_b16 v[222:223],v242 offset:9984\nM v[32:35],v[192:195],a[128:131],v[32:35]\nM v[32:35],v[196:199],a[144:147],v[32:35]\nM v[36:39],v[192:195],a[132:135],v[36:39]\nbuffer_load_dwordx4 v245,s[40:43],s69 offen offset:2048 lds\nM v[36:39],v[196:199],a[148:151],v[36:39]\nM v[96:99],v[192:195],a[136:139],v[96:99]\n" \
"s_waitcnt vmcnt(16)\nv_xor_b32 v166,v166,v230\nM v[96:99],v[196:199],a[152:155],v[96:99]\nv_xor_b32 v167,v167,v231\nv_or_b32 v166,v166,v167\nM v[100:103],v[192:195],a[140:143],v[100:103]\nv_cmp_ne_u32 vcc,0,v166\nM v[100:103],v[196:199],a[156:159],v[100:103]\n" \
"buffer_load_dwordx4 v246,s[40:43],s69 offen offset:3072 lds\ns_waitcnt lgkmcnt(4)\nM v[40:43],v[200:203],a[128:131],v[40:43]\ns_cbranch_vccnz .Lhs\n.Lhb:\nv_bfe_u32 v231,v249,2,3\nM v[40:43],v[204:207],a[144:147],v[40:43]\ns_add_u32 m0,s65,8192\n" \
"s_xor_b32 s67,s66,896\nM v[44:47],v[200:203],a[132:135],v[44:47]\nv_mul_u32_u24 v231,3584,v231\nv_and_b32 v230,31,v249\nM v[44:47],v[204:207],a[148:151],v[44:47]\nv_lshlrev_b32 v230,7,v230\nM v[104:107],v[200:203],a[136:139],v[104:107]\nbuffer_load_dwordx4 v224,s[36:39],s67 offen offset:0 lds\n" \
"M v[104:107],v[204:207],a[152:155],v[104:107]\nM v[108:111],v[200:203],a[140:143],v[108:111]\nM v[108:111],v[204:207],a[156:159],v[108:111]\nM v[48:51],v[208:211],a[128:131],v[48:51]\nM v[48:51],v[212:215],a[144:147],v[48:51]\nbuffer_load_dwordx4 v225,s[36:39],s67 offen offset:1024 lds\nM v[52:55],v[208:211],a[132:135],v[52:55]\nM v[52:55],v[212:215],a[148:151],v[52:55]\n" \
"M v[112:115],v[208:211],a[136:139],v[112:115]\ns_waitcnt vmcnt(15) lgkmcnt(0)\ns_barrier\nM v[112:115],v[212:215],a[152:155],v[112:115]\nds_read_b128 a[160:163],v228 offset:16384\nds_read_b64_tr_b16 v[192:193],v241 offset:16384\nds_read_b64_tr_b16 v[194:195],v241 offset:16640\nM v[116:119],v[208:211],a[140:143],v[116:119]\n" \
"ds_read_b128 a[176:179],v229 offset:16384\nds_read_b64_tr_b16 v[196:197],v241 offset:24576\nds_read_b64_tr_b16 v[198:199],v241 offset:24832\nM v[116:119],v[212:215],a[156:159],v[116:119]\nds_read_b128 a[164:167],v228 offset:18432\nM v[56:59],v[216:219],a[128:131],v[56:59]\nbuffer_load_dwordx4 v[152:155],v240,s[52:55],s71 offen sc1\nds_read_b128 a[180:183],v229 offset:18432\n" \
"M v[56:59],v[220:223],a[144:147],v[56:59]\nbuffer_load_dwordx4 v226,s[36:39],s67 offen offset:2048 lds\ns_add_u32 s71,s70,798720\nds_read_b128 a[168:171],v228 offset:20480\nbuffer_load_dwordx4 v[156:159],v240,s[52:55],s71 offen sc1\nM v[60:63],v[216:219],a[132:135],v[60:63]\nds_read_b128 a[184:187],v229 offset:20480\ns_add_u32 s71,s70,811008\n" \
"M v[60:63],v[220:223],a[148:151],v[60:63]\nbuffer_load_dwordx4 v227,s[36:39],s67 offen offset:3072 lds\nbuffer_load_dwordx4 v[160:163],v240,s[52:55],s71 offen sc1\nds_read_b128 a[172:175],v228 offset:22528\nM v[120:123],v[216:219],a[136:139],v[120:123]\ns_add_u32 s71,s70,823296\nds_read_b128 a[188:191],v229 offset:22528\nbuffer_load_dwordx4 v[164:167],v240,s[52:55],s71 offen sc1\n" \
"M v[120:123],v[220:223],a[152:155],v[120:123]\ns_add_u32 m0,s65,12288\ns_xor_b32 s67,s66,33664\nds_read_b64_tr_b16 v[200:201],v242 offset:16384\nds_read_b64_tr_b16 v[202:203],v242 offset:16640\nM v[124:127],v[216:219],a[140:143],v[124:127]\nds_read_b64_tr_b16 v[204:205],v242 offset:24576\nds_read_b64_tr_b16 v[206:207],v242 offset:24832\n" \
"M v[124:127],v[220:223],a[156:159],v[124:127]\ns_lshl_b32 s71,s6,5\ns_waitcnt lgkmcnt(9)\nM v[0:3],v[192:195],a[160:163],v[0:3]\nds_read_b64_tr_b16 v[208:209],v241 offset:16896\ns_add_u32 s71,s71,790528\nM v[0:3],v[196:199],a[176:179],v[0:3]\nds_read_b64_tr_b16 v[210:211],v241 offset:17152\n" \
"ds_read_b64_tr_b16 v[212:213],v241 offset:25088\nM v[4:7],v[192:195],a[164:167],v[4:7]\nds_read_b64_tr_b16 v[214:215],v241 offset:25344\ns_waitcnt lgkmcnt(11)\nM v[4:7],v[196:199],a[180:183],v[4:7]\nds_read_b64_tr_b16 v[216:217],v242 offset:16896\nds_read_b64_tr_b16 v[218:219],v242 offset:17152\nM v[64:67],v[192:195],a[168:171],v[64:67]\n" \
"ds_read_b64_tr_b16 v[220:221],v242 offset:25088\ns_waitcnt lgkmcnt(13)\nM v[64:67],v[196:199],a[184:187],v[64:67]\nds_read_b64_tr_b16 v[222:223],v242 offset:25344\ns_waitcnt lgkmcnt(13)\nM v[68:71],v[192:195],a[172:175],v[68:71]\nds_read_b64_tr_b16 v[192:193],v241 offset:17408\ns_waitcnt lgkmcnt(13)\n" \
"M v[68:71],v[196:199],a[188:191],v[68:71]\nds_read_b64_tr_b16 v[194:195],v241 offset:17664\ns_waitcnt lgkmcnt(12)\nM v[8:11],v[200:203],a[160:163],v[8:11]\nds_read_b64_tr_b16 v[196:197],v241 offset:25600\ns_waitcnt lgkmcnt(11)\nM v[8:11],v[204:207],a[176:179],v[8:11]\ns_cmp_lt_u32 s7,2\n" \
"s_cselect_b32 s70,0,1.0\nM v[12:15],v[200:203],a[164:167],v[12:15]\nbuffer_load_dword v247,v254,s[56:59],s78 offen\nM v[12:15],v[204:207],a[180:183],v[12:15]\nds_read_b64_tr_b16 v[198:199],v241 offset:25856\nv_and_b32 v254,3,v249\nM v[72:75],v[200:203],a[168:171],v[72:75]\ns_cmp_eq_u32 s62,5\n" \
"s_cselect_b64 s[56:57],s[22:23],s[24:25]\nM v[72:75],v[204:207],a[184:187],v[72:75]\nv_lshl_add_u32 v231,v254,7,v231\nM v[76:79],v[200:203],a[172:175],v[76:79]\nglobal_load_dword v247,v253,s[12:13]\nM v[76:79],v[204:207],a[188:191],v[76:79]\nds_read_b64_tr_b16 v[200:201],v242 offset:17408\ns_waitcnt lgkmcnt(6)\n" \
"M v[16:19],v[208:211],a[160:163],v[16:19]\nds_read_b64_tr_b16 v[202:203],v242 offset:17664\nds_read_b64_tr_b16 v[204:205],v242 offset:25600\nM v[16:19],v[212:215],a[176:179],v[16:19]\nds_read_b64_tr_b16 v[206:207],v242 offset:25856\ns_add_u32 s69,s68,3670016\nM v[20:23],v[208:211],a[164:167],v[20:23]\nbuffer_load_dwordx4 v224,s[36:39],s67 offen offset:0 lds\n" \
"M v[20:23],v[212:215],a[180:183],v[20:23]\nv_add_u32 v253,0x100000,v253\nM v[80:83],v[208:211],a[168:171],v[80:83]\nM v[80:83],v[212:215],a[184:187],v[80:83]\nM v[84:87],v[208:211],a[172:175],v[84:87]\nbuffer_load_dwordx4 v225,s[36:39],s67 offen offset:1024 lds\nM v[84:87],v[212:215],a[188:191],v[84:87]\nds_read_b64_tr_b16 v[208:209],v241 offset:17920\n" \
"ds_read_b64_tr_b16 v[210:211],v241 offset:18176\nM v[24:27],v[216:219],a[160:163],v[24:27]\nds_read_b64_tr_b16 v[212:213],v241 offset:26112\ns_waitcnt lgkmcnt(7)\nM v[24:27],v[220:223],a[176:179],v[24:27]\nds_read_b64_tr_b16 v[214:215],v241 offset:26368\nM v[28:31],v[216:219],a[164:167],v[28:31]\nbuffer_load_dwordx4 v226,s[36:39],s67 offen offset:2048 lds\n" \
"M v[28:31],v[220:223],a[180:183],v[28:31]\nM v[88:91],v[216:219],a[168:171],v[88:91]\nM v[88:91],v[220:223],a[184:187],v[88:91]\nM v[92:95],v[216:219],a[172:175],v[92:95]\nbuffer_load_dwordx4 v227,s[36:39],s67 offen offset:3072 lds\nM v[92:95],v[220:223],a[188:191],v[92:95]\ns_add_u32 m0,s64,147456\nds_read_b64_tr_b16 v[216:217],v242 offset:17920\n" \
"M v[32:35],v[192:195],a[160:163],v[32:35]\nds_read_b64_tr_b16 v[218:219],v242 offset:18176\nds_read_b64_tr_b16 v[220:221],v242 offset:26112\nM v[32:35],v[196:199],a[176:179],v[32:35]\ns_cmp_eq_u32 s62,0\ns_cselect_b32 s36,0x8000,0\nM v[36:39],v[192:195],a[164:167],v[36:39]\nds_read_b64_tr_b16 v[222:223],v242 offset:26368\n" \
"s_add_u32 s36,s36,s64\nM v[36:39],v[196:199],a[180:183],v[36:39]\nbuffer_load_dwordx4 v243,s[40:43],s69 offen offset:0 lds\nM v[96:99],v[192:195],a[168:171],v[96:99]\nM v[96:99],v[196:199],a[184:187],v[96:99]\nM v[100:103],v[192:195],a[172:175],v[100:103]\nM v[100:103],v[196:199],a[188:191],v[100:103]\ns_waitcnt lgkmcnt(4)\n" \
"M v[40:43],v[200:203],a[160:163],v[40:43]\nbuffer_load_dwordx4 v244,s[40:43],s69 offen offset:1024 lds\nM v[40:43],v[204:207],a[176:179],v[40:43]\nM v[44:47],v[200:203],a[164:167],v[44:47]\nM v[44:47],v[204:207],a[180:183],v[44:47]\nM v[104:107],v[200:203],a[168:171],v[104:107]\nbuffer_load_dwordx4 v245,s[40:43],s69 offen offset:2048 lds\nM v[104:107],v[204:207],a[184:187],v[104:107]\n" \
"M v[108:111],v[200:203],a[172:175],v[108:111]\nM v[108:111],v[204:207],a[188:191],v[108:111]\nM v[48:51],v[208:211],a[160:163],v[48:51]\nbuffer_load_dwordx4 v246,s[40:43],s69 offen offset:3072 lds\nM v[48:51],v[212:215],a[176:179],v[48:51]\ns_sub_u32 s40,s16,0x1000\ns_subb_u32 s41,s17,0\nM v[52:55],v[208:211],a[164:167],v[52:55]\n" \
"s_add_u32 m0,s64,98304\ns_add_u32 s69,s68,0\nM v[52:55],v[212:215],a[180:183],v[52:55]\ns_mov_b32 s16,0\ns_mov_b32 s17,3\nM v[112:115],v[208:211],a[168:171],v[112:115]\ns_waitcnt vmcnt(18) lgkmcnt(0)\ns_barrier\n" \
"M v[112:115],v[212:215],a[184:187],v[112:115]\nds_read_b128 a[192:195],v228 offset:0\nds_read_b64_tr_b16 v[192:193],v241 offset:32768\nds_read_b64_tr_b16 v[194:195],v241 offset:33024\nM v[116:119],v[208:211],a[172:175],v[116:119]\nglobal_load_dword v247,v253,s[12:13]\nds_read_b128 a[208:211],v229 offset:0\nds_read_b64_tr_b16 v[196:197],v241 offset:40960\n" \
"ds_read_b64_tr_b16 v[198:199],v241 offset:41216\nM v[116:119],v[212:215],a[188:191],v[116:119]\nds_read_b128 a[196:199],v228 offset:2048\nv_add_u32 v253,0x100000,v253\nM v[56:59],v[216:219],a[160:163],v[56:59]\nds_read_b128 a[212:215],v229 offset:2048\nM v[56:59],v[220:223],a[176:179],v[56:59]\nds_read_b128 a[200:203],v228 offset:4096\n" \
"M v[60:63],v[216:219],a[164:167],v[60:63]\nds_read_b128 a[216:219],v229 offset:4096\nM v[60:63],v[220:223],a[180:183],v[60:63]\nds_read_b128 a[204:207],v228 offset:6144\nM v[120:123],v[216:219],a[168:171],v[120:123]\nds_read_b128 a[220:223],v229 offset:6144\nM v[120:123],v[220:223],a[184:187],v[120:123]\nds_read_b64_tr_b16 v[200:201],v242 offset:32768\n" \
"ds_read_b64_tr_b16 v[202:203],v242 offset:33024\nM v[124:127],v[216:219],a[172:175],v[124:127]\nds_read_b64_tr_b16 v[204:205],v242 offset:40960\nds_read_b64_tr_b16 v[206:207],v242 offset:41216\nM v[124:127],v[220:223],a[188:191],v[124:127]\ns_waitcnt vmcnt(14)\nv_add_f32 v152,v152,v156\ns_waitcnt lgkmcnt(13)\n" \
"M v[0:3],v[192:195],a[192:195],v[0:3]\nds_read_b64_tr_b16 v[208:209],v241 offset:33280\ns_waitcnt lgkmcnt(11)\nM v[0:3],v[196:199],a[208:211],v[0:3]\nds_read_b64_tr_b16 v[210:211],v241 offset:33536\ns_waitcnt lgkmcnt(11)\nM v[4:7],v[192:195],a[196:199],v[4:7]\nds_read_b64_tr_b16 v[212:213],v241 offset:41472\n" \
"s_waitcnt lgkmcnt(11)\nM v[4:7],v[196:199],a[212:215],v[4:7]\nds_read_b64_tr_b16 v[214:215],v241 offset:41728\ns_waitcnt lgkmcnt(11)\nM v[64:67],v[192:195],a[200:203],v[64:67]\nds_read_b64_tr_b16 v[216:217],v242 offset:33280\ns_waitcnt lgkmcnt(11)\nM v[64:67],v[196:199],a[216:219],v[64:67]\n" \
"ds_read_b64_tr_b16 v[218:219],v242 offset:33536\ns_waitcnt lgkmcnt(11)\nM v[68:71],v[192:195],a[204:207],v[68:71]\nds_read_b64_tr_b16 v[220:221],v242 offset:41472\ns_waitcnt lgkmcnt(11)\nM v[68:71],v[196:199],a[220:223],v[68:71]\nds_read_b64_tr_b16 v[222:223],v242 offset:41728\ns_waitcnt lgkmcnt(10)\n" \
"M v[8:11],v[200:203],a[192:195],v[8:11]\nds_read_b64_tr_b16 v[192:193],v241 offset:33792\ns_waitcnt lgkmcnt(9)\nM v[8:11],v[204:207],a[208:211],v[8:11]\nds_read_b64_tr_b16 v[194:195],v241 offset:34048\nds_read_b64_tr_b16 v[196:197],v241 offset:41984\nM v[12:15],v[200:203],a[196:199],v[12:15]\nds_read_b64_tr_b16 v[198:199],v241 offset:42240\n" \
"s_waitcnt vmcnt(11)\nv_add_f32 v160,v160,v164\nM v[12:15],v[204:207],a[212:215],v[12:15]\nv_add_f32 v153,v153,v157\nv_add_f32 v161,v161,v165\nM v[72:75],v[200:203],a[200:203],v[72:75]\nv_add_f32 v154,v154,v158\nv_add_f32 v162,v162,v166\n" \
"M v[72:75],v[204:207],a[216:219],v[72:75]\nv_add_f32 v155,v155,v159\nv_add_f32 v163,v163,v167\nM v[76:79],v[200:203],a[204:207],v[76:79]\nds_read_b64_tr_b16 v[200:201],v242 offset:33792\nds_read_b64_tr_b16 v[202:203],v242 offset:34048\nM v[76:79],v[204:207],a[220:223],v[76:79]\nds_read_b64_tr_b16 v[204:205],v242 offset:41984\n" \
"s_waitcnt lgkmcnt(9)\nM v[16:19],v[208:211],a[192:195],v[16:19]\nds_read_b64_tr_b16 v[206:207],v242 offset:42240\nv_add_f32 v152,v152,v160\nM v[16:19],v[212:215],a[208:211],v[16:19]\nbuffer_load_dword v168,v169,s[52:55],s71 offen sc1\nM v[20:23],v[208:211],a[196:199],v[20:23]\nv_add_f32 v153,v153,v161\n" \
"v_add_f32 v154,v154,v162\nM v[20:23],v[212:215],a[212:215],v[20:23]\nv_add_f32 v155,v155,v163\nv_cvt_pk_bf16_f32 v152,0,v152\nM v[80:83],v[208:211],a[200:203],v[80:83]\nv_cvt_pk_bf16_f32 v153,0,v153\nv_cvt_pk_bf16_f32 v154,0,v154\nM v[80:83],v[212:215],a[216:219],v[80:83]\n" \
"v_cvt_pk_bf16_f32 v155,0,v155\nv_add_f32 v152,s70,v152\nM v[84:87],v[208:211],a[204:207],v[84:87]\nds_read_b64_tr_b16 v[208:209],v241 offset:34304\nds_read_b64_tr_b16 v[210:211],v241 offset:34560\nM v[84:87],v[212:215],a[220:223],v[84:87]\nds_read_b64_tr_b16 v[212:213],v241 offset:42496\nds_read_b64_tr_b16 v[214:215],v241 offset:42752\n" \
"M v[24:27],v[216:219],a[192:195],v[24:27]\nv_add_f32 v153,s70,v153\ns_waitcnt lgkmcnt(7)\nM v[24:27],v[220:223],a[208:211],v[24:27]\nv_add_f32 v154,s70,v154\nv_add_f32 v155,s70,v155\nM v[28:31],v[216:219],a[196:199],v[28:31]\nglobal_load_dword v247,v253,s[12:13]\n" \
"M v[28:31],v[220:223],a[212:215],v[28:31]\ns_lshr_b32 s71,s64,2\nv_cvt_pk_bf16_f32 v152,0,v152\nM v[88:91],v[216:219],a[200:203],v[88:91]\nv_cvt_pk_bf16_f32 v153,0,v153\nv_cvt_pk_bf16_f32 v154,0,v154\nM v[88:91],v[220:223],a[216:219],v[88:91]\nv_cvt_pk_bf16_f32 v155,0,v155\n" \
"v_add_u32 v171,s71,v240\nM v[92:95],v[216:219],a[204:207],v[92:95]\nds_write_b128 v171,v[152:155] offset:16384\nM v[92:95],v[220:223],a[220:223],v[92:95]\nds_read_b64_tr_b16 v[216:217],v242 offset:34304\nds_read_b64_tr_b16 v[218:219],v242 offset:34560\nM v[32:35],v[192:195],a[192:195],v[32:35]\nds_read_b64_tr_b16 v[220:221],v242 offset:42496\n" \
"ds_read_b64_tr_b16 v[222:223],v242 offset:42752\nM v[32:35],v[196:199],a[208:211],v[32:35]\nv_add_u32 v253,0x100000,v253\ns_lshl_b32 s70,s7,8\nM v[36:39],v[192:195],a[196:199],v[36:39]\nbuffer_load_dwordx4 v243,s[40:43],s69 offen offset:0 lds\nM v[36:39],v[196:199],a[212:215],v[36:39]\nv_lshl_add_u32 v251,v251,5,s70\n" \
"s_lshl_b32 s71,s6,5\nM v[96:99],v[192:195],a[200:203],v[96:99]\ns_and_b32 s71,s71,0xfffffe00\ns_xor_b32 s70,s63,16\nM v[96:99],v[196:199],a[216:219],v[96:99]\ns_lshr_b32 s70,s70,2\ns_lshl3_add_u32 s70,s62,s70\nM v[100:103],v[192:195],a[204:207],v[100:103]\n" \
"buffer_load_dwordx4 v244,s[40:43],s69 offen offset:1024 lds\nM v[100:103],v[196:199],a[220:223],v[100:103]\ns_lshl_b32 s70,s70,15\ns_waitcnt lgkmcnt(5)\nM v[40:43],v[200:203],a[192:195],v[40:43]\ns_add_u32 s70,s70,s64\nM v[40:43],v[204:207],a[208:211],v[40:43]\nM v[44:47],v[200:203],a[196:199],v[44:47]\n" \
"buffer_load_dwordx4 v245,s[40:43],s69 offen offset:2048 lds\nM v[44:47],v[204:207],a[212:215],v[44:47]\nM v[104:107],v[200:203],a[200:203],v[104:107]\nM v[104:107],v[204:207],a[216:219],v[104:107]\nM v[108:111],v[200:203],a[204:207],v[108:111]\nbuffer_load_dwordx4 v246,s[40:43],s69 offen offset:3072 lds\nM v[108:111],v[204:207],a[220:223],v[108:111]\ns_add_u32 m0,s64,114688\n" \
"s_add_u32 s69,s68,524288\nM v[48:51],v[208:211],a[192:195],v[48:51]\nM v[48:51],v[212:215],a[208:211],v[48:51]\nM v[52:55],v[208:211],a[196:199],v[52:55]\nbuffer_store_dwordx2 v[232:233],off,s[52:55],s72 sc1\nM v[52:55],v[212:215],a[212:215],v[52:55]\ns_and_b32 s72,s6,1\ns_lshl_b32 s72,s72,8\n" \
"M v[112:115],v[208:211],a[200:203],v[112:115]\ns_waitcnt vmcnt(8) lgkmcnt(0)\ns_barrier\nM v[112:115],v[212:215],a[216:219],v[112:115]\nds_read_b128 a[224:227],v228 offset:8192\nds_read_b64_tr_b16 v[192:193],v241 offset:49152\nds_read_b64_tr_b16 v[194:195],v241 offset:49408\nM v[116:119],v[208:211],a[204:207],v[116:119]\n" \
"ds_read_b128 a[240:243],v229 offset:8192\nds_read_b64_tr_b16 v[196:197],v241 offset:57344\nds_read_b64_tr_b16 v[198:199],v241 offset:57600\nM v[116:119],v[212:215],a[220:223],v[116:119]\nds_read_b128 a[228:231],v228 offset:10240\ns_add_u32 s71,s71,s72\nM v[56:59],v[216:219],a[192:195],v[56:59]\nds_read_b128 a[244:247],v229 offset:10240\n" \
"s_and_b32 s72,s6,14\nM v[56:59],v[220:223],a[208:211],v[56:59]\nds_read_b128 a[232:235],v228 offset:12288\ns_lshl1_add_u32 s71,s72,s71\nM v[60:63],v[216:219],a[196:199],v[60:63]\nds_read_b128 a[248:251],v229 offset:12288\ns_or_b32 s71,s71,s75\nM v[60:63],v[220:223],a[212:215],v[60:63]\n" \
"ds_read_b128 a[236:239],v228 offset:14336\ns_mov_b32 s72,0x3e0293ee\nM v[120:123],v[216:219],a[200:203],v[120:123]\nds_read_b128 a[252:255],v229 offset:14336\nv_and_b32 v228,15,v249\nM v[120:123],v[220:223],a[216:219],v[120:123]\nds_read_b64_tr_b16 v[200:201],v242 offset:49152\nds_read_b64_tr_b16 v[202:203],v242 offset:49408\n" \
"M v[124:127],v[216:219],a[204:207],v[124:127]\nds_read_b64_tr_b16 v[204:205],v242 offset:57344\nds_read_b64_tr_b16 v[206:207],v242 offset:57600\nM v[124:127],v[220:223],a[220:223],v[124:127]\nv_and_b32 v229,16,v249\ns_waitcnt lgkmcnt(13)\nM v[0:3],v[192:195],a[224:227],v[0:3]\nds_read_b64_tr_b16 v[208:209],v241 offset:49664\n" \
"s_waitcnt lgkmcnt(11)\nM v[0:3],v[196:199],a[240:243],v[0:3]\nds_read_b64_tr_b16 v[210:211],v241 offset:49920\ns_waitcnt lgkmcnt(11)\nM v[4:7],v[192:195],a[228:231],v[4:7]\nds_read_b64_tr_b16 v[212:213],v241 offset:57856\ns_waitcnt lgkmcnt(11)\nM v[4:7],v[196:199],a[244:247],v[4:7]\n" \
"ds_read_b64_tr_b16 v[214:215],v241 offset:58112\ns_waitcnt lgkmcnt(11)\nM v[64:67],v[192:195],a[232:235],v[64:67]\nds_read_b64_tr_b16 v[216:217],v242 offset:49664\ns_waitcnt lgkmcnt(11)\nM v[64:67],v[196:199],a[248:251],v[64:67]\nds_read_b64_tr_b16 v[218:219],v242 offset:49920\ns_waitcnt lgkmcnt(11)\n" \
"M v[68:71],v[192:195],a[236:239],v[68:71]\nglobal_load_dword v247,v253,s[12:13]\ns_waitcnt lgkmcnt(10)\nM v[68:71],v[196:199],a[252:255],v[68:71]\nds_read_b64_tr_b16 v[220:221],v242 offset:57856\ns_waitcnt lgkmcnt(9)\nM v[8:11],v[200:203],a[224:227],v[8:11]\nds_read_b64_tr_b16 v[222:223],v242 offset:58112\n" \
"s_waitcnt lgkmcnt(8)\nM v[8:11],v[204:207],a[240:243],v[8:11]\nds_read_b64_tr_b16 v[192:193],v241 offset:50176\nds_read_b64_tr_b16 v[194:195],v241 offset:50432\nM v[12:15],v[200:203],a[228:231],v[12:15]\nds_read_b64_tr_b16 v[196:197],v241 offset:58368\nds_read_b64_tr_b16 v[198:199],v241 offset:58624\nM v[12:15],v[204:207],a[244:247],v[12:15]\n" \
"ds_read_b128 v[176:179],v251 offset:19456\nds_read_b128 v[160:163],v251 offset:17408\nM v[72:75],v[200:203],a[232:235],v[72:75]\nds_read_b128 v[172:175],v251 offset:18448\nv_cvt_pk_bf16_f32 v128,v0,v1\nM v[72:75],v[204:207],a[248:251],v[72:75]\nds_read_b128 v[152:155],v251 offset:16384\nv_cvt_pk_bf16_f32 v129,v2,v3\n" \
"M v[76:79],v[200:203],a[236:239],v[76:79]\nds_read_b64_tr_b16 v[200:201],v242 offset:50176\nv_cvt_pk_bf16_f32 v130,v8,v9\nM v[76:79],v[204:207],a[252:255],v[76:79]\nds_read_b64_tr_b16 v[202:203],v242 offset:50432\ns_waitcnt lgkmcnt(12)\nM v[16:19],v[208:211],a[224:227],v[16:19]\nds_read_b64_tr_b16 v[204:205],v242 offset:58368\n" \
"ds_read_b64_tr_b16 v[206:207],v242 offset:58624\nM v[16:19],v[212:215],a[240:243],v[16:19]\nds_read_b128 v[156:159],v251 offset:16400\nds_read_b128 v[180:183],v251 offset:19472\nM v[20:23],v[208:211],a[228:231],v[20:23]\nds_read_b128 v[164:167],v251 offset:17424\nv_cvt_pk_bf16_f32 v131,v10,v11\nM v[20:23],v[212:215],a[244:247],v[20:23]\n" \
"buffer_load_dwordx4 v243,s[40:43],s69 offen offset:0 lds\nM v[80:83],v[208:211],a[232:235],v[80:83]\nv_cvt_pk_bf16_f32 v132,v16,v17\nv_cvt_pk_bf16_f32 v133,v18,v19\nM v[80:83],v[212:215],a[248:251],v[80:83]\ns_waitcnt lgkmcnt(10)\nv_mul_f32 v144,v144,v176\nM v[84:87],v[208:211],a[236:239],v[84:87]\n" \
"ds_read_b64_tr_b16 v[208:209],v241 offset:50688\nds_read_b64_tr_b16 v[210:211],v241 offset:50944\nM v[84:87],v[212:215],a[252:255],v[84:87]\nds_read_b64_tr_b16 v[212:213],v241 offset:58880\nds_read_b64_tr_b16 v[214:215],v241 offset:59136\nM v[24:27],v[216:219],a[224:227],v[24:27]\ns_waitcnt vmcnt(8)\nv_add_f32_dpp v168,v168,v168 quad_perm:[1,0,3,2] row_mask:0xf bank_mask:0xf\n" \
"M v[24:27],v[220:223],a[240:243],v[24:27]\nv_mul_f32 v145,v145,v177\nv_add_f32_dpp v168,v168,v168 quad_perm:[2,3,0,1] row_mask:0xf bank_mask:0xf\nM v[28:31],v[216:219],a[228:231],v[28:31]\nv_cvt_pk_bf16_f32 v168,0,v168\nv_cvt_pk_bf16_f32 v144,0,v144\nM v[28:31],v[220:223],a[244:247],v[28:31]\nbuffer_store_dword v168,v170,s[52:55],s71 offen sc1\n" \
"M v[88:91],v[216:219],a[232:235],v[88:91]\nds_read_b128 v[168:171],v251 offset:18432\nv_cvt_pk_bf16_f32 v145,0,v145\nM v[88:91],v[220:223],a[248:251],v[88:91]\ns_waitcnt lgkmcnt(5)\nv_add_f32 v144,v144,v160\nM v[92:95],v[216:219],a[236:239],v[92:95]\nds_read_b64_tr_b16 v[216:217],v242 offset:50688\n" \
"ds_read_b64_tr_b16 v[218:219],v242 offset:50944\nM v[92:95],v[220:223],a[252:255],v[92:95]\nds_read_b64_tr_b16 v[220:221],v242 offset:58880\nds_read_b64_tr_b16 v[222:223],v242 offset:59136\nM v[32:35],v[192:195],a[224:227],v[32:35]\nv_add_f32 v145,v145,v161\nv_cvt_pk_bf16_f32 v144,0,v144\nM v[32:35],v[196:199],a[240:243],v[32:35]\n" \
"v_cvt_pk_bf16_f32 v145,0,v145\nv_mul_f32 v144,s72,v144\nM v[36:39],v[192:195],a[228:231],v[36:39]\nv_mul_f32 v145,s72,v145\nv_cvt_pk_bf16_f32 v236,v144,v145\nM v[36:39],v[196:199],a[244:247],v[36:39]\nv_cvt_pk_bf16_f32 v144,v4,v5\nv_cvt_pk_bf16_f32 v145,v6,v7\n" \
"M v[96:99],v[192:195],a[232:235],v[96:99]\nv_cvt_pk_bf16_f32 v160,v64,v65\nv_cvt_pk_bf16_f32 v161,v66,v67\nM v[96:99],v[196:199],a[248:251],v[96:99]\ns_and_b32 s71,s63,2\nv_mul_f32 v140,v140,v172\nM v[100:103],v[192:195],a[236:239],v[100:103]\ns_waitcnt lgkmcnt(4)\n" \
"v_mul_f32 v136,v136,v168\nM v[100:103],v[196:199],a[252:255],v[100:103]\nv_mul_f32 v137,v137,v169\nv_mul_f32 v138,v138,v170\nM v[40:43],v[200:203],a[224:227],v[40:43]\nv_mul_f32 v139,v139,v171\nv_mul_f32 v141,v141,v173\nM v[40:43],v[204:207],a[240:243],v[40:43]\n" \
"v_mul_f32 v142,v142,v174\nv_mul_f32 v143,v143,v175\nM v[44:47],v[200:203],a[228:231],v[44:47]\ns_lshl_b32 s71,s71,9\nv_cvt_pk_bf16_f32 v176,v68,v69\nM v[44:47],v[204:207],a[244:247],v[44:47]\nv_cvt_pk_bf16_f32 v177,v70,v71\nv_cvt_pk_bf16_f32 v136,0,v136\n" \
"M v[104:107],v[200:203],a[232:235],v[104:107]\nv_cvt_pk_bf16_f32 v137,0,v137\nv_cvt_pk_bf16_f32 v138,0,v138\nM v[104:107],v[204:207],a[248:251],v[104:107]\nv_cvt_pk_bf16_f32 v139,0,v139\nv_cvt_pk_bf16_f32 v140,0,v140\nM v[108:111],v[200:203],a[236:239],v[108:111]\nv_cvt_pk_bf16_f32 v141,0,v141\n" \
"v_cvt_pk_bf16_f32 v142,0,v142\nM v[108:111],v[204:207],a[252:255],v[108:111]\nv_cvt_pk_bf16_f32 v143,0,v143\nv_lshrrev_b32 v251,5,v249\nM v[48:51],v[208:211],a[224:227],v[48:51]\ns_add_u32 s70,s70,s71\nv_add_f32 v136,v136,v152\nM v[48:51],v[212:215],a[240:243],v[48:51]\n" \
"v_add_f32 v137,v137,v153\nv_add_f32 v138,v138,v154\nM v[52:55],v[208:211],a[228:231],v[52:55]\nv_add_f32 v139,v139,v155\nv_add_f32 v140,v140,v156\nM v[52:55],v[212:215],a[244:247],v[52:55]\nv_add_f32 v141,v141,v157\nv_add_f32 v142,v142,v158\n" \
"M v[112:115],v[208:211],a[232:235],v[112:115]\ns_waitcnt vmcnt(4) lgkmcnt(0)\ns_barrier\nM v[112:115],v[212:215],a[248:251],v[112:115]\nds_read_b64_tr_b16 v[192:193],v241 offset:0\nds_read_b64_tr_b16 v[194:195],v241 offset:256\nM v[116:119],v[208:211],a[236:239],v[116:119]\nds_read_b64_tr_b16 v[196:197],v241 offset:8192\n" \
"ds_read_b64_tr_b16 v[198:199],v241 offset:8448\nM v[116:119],v[212:215],a[252:255],v[116:119]\nds_read_b64_tr_b16 v[200:201],v242 offset:0\nds_read_b64_tr_b16 v[202:203],v242 offset:256\nM v[56:59],v[216:219],a[224:227],v[56:59]\nds_read_b64_tr_b16 v[204:205],v242 offset:8192\nds_read_b64_tr_b16 v[206:207],v242 offset:8448\nM v[56:59],v[220:223],a[240:243],v[56:59]\n" \
"ds_read_b64_tr_b16 v[208:209],v241 offset:512\nds_read_b64_tr_b16 v[210:211],v241 offset:768\nM v[60:63],v[216:219],a[228:231],v[60:63]\nv_add_f32 v143,v143,v159\nv_mul_f32 v146,v146,v178\nM v[60:63],v[220:223],a[244:247],v[60:63]\nv_mul_f32 v147,v147,v179\nv_lshlrev_b32 v251,11,v251\n" \
"M v[120:123],v[216:219],a[232:235],v[120:123]\ns_and_b32 s71,s63,1\nds_read_b64_tr_b16 v[212:213],v241 offset:8704\nM v[120:123],v[220:223],a[248:251],v[120:123]\nds_read_b64_tr_b16 v[214:215],v241 offset:8960\nv_cvt_pk_bf16_f32 v136,0,v136\nM v[124:127],v[216:219],a[236:239],v[124:127]\nv_cvt_pk_bf16_f32 v137,0,v137\n" \
"v_cvt_pk_bf16_f32 v138,0,v138\nM v[124:127],v[220:223],a[252:255],v[124:127]\nv_cvt_pk_bf16_f32 v139,0,v139\ns_waitcnt lgkmcnt(10)\nM v[0:3],a[0:3],v[192:195],0\nv_cvt_pk_bf16_f32 v140,0,v140\ns_waitcnt lgkmcnt(8)\nM v[0:3],a[16:19],v[196:199],v[0:3]\n" \
"v_cvt_pk_bf16_f32 v141,0,v141\nv_cvt_pk_bf16_f32 v142,0,v142\nM v[4:7],a[4:7],v[192:195],0\nv_cvt_pk_bf16_f32 v143,0,v143\nv_cvt_pk_bf16_f32 v146,0,v146\nM v[4:7],a[20:23],v[196:199],v[4:7]\nv_cvt_pk_bf16_f32 v147,0,v147\nv_lshl_or_b32 v251,v250,8,v251\n" \
"M v[64:67],a[8:11],v[192:195],0\ns_lshl_b32 s71,s71,7\nv_mul_f32 v136,s72,v136\nM v[64:67],a[24:27],v[196:199],v[64:67]\nv_mul_f32 v137,s72,v137\nv_mul_f32 v138,s72,v138\nM v[68:71],a[12:15],v[192:195],0\nv_mul_f32 v139,s72,v139\n" \
"v_mul_f32 v140,s72,v140\nM v[68:71],a[28:31],v[196:199],v[68:71]\nv_mul_f32 v141,s72,v141\nv_mul_f32 v142,s72,v142\nv_mul_f32 v143,s72,v143\nv_add_f32 v146,v146,v162\nv_add_f32 v147,v147,v163\nv_and_b32 v250,0x70,v240\n" \
"s_add_u32 s70,s70,s71\ns_waitcnt lgkmcnt(0)\nM v[8:11],a[0:3],v[200:203],0\nv_cvt_pk_bf16_f32 v232,v136,v137\nv_cvt_pk_bf16_f32 v233,v138,v139\nv_cvt_pk_bf16_f32 v234,v140,v141\nv_cvt_pk_bf16_f32 v235,v142,v143\nv_cvt_pk_bf16_f32 v146,0,v146\n" \
"v_cvt_pk_bf16_f32 v147,0,v147\nv_or_b32 v251,v251,v250\ns_add_u32 s70,s70,1048576\nv_mul_f32 v146,s72,v146\nv_mul_f32 v147,s72,v147\nbuffer_store_dwordx4 v[232:235],v251,s[52:55],s70 offen sc1\nM v[8:11],a[16:19],v[204:207],v[8:11]\nv_cvt_pk_bf16_f32 v237,v146,v147\n" \
"v_mul_f32 v148,v148,v180\nv_mul_f32 v149,v149,v181\nv_mul_f32 v150,v150,v182\nv_mul_f32 v151,v151,v183\nv_cvt_pk_bf16_f32 v146,v12,v13\nv_cvt_pk_bf16_f32 v147,v14,v15\nM v[12:15],a[4:7],v[200:203],0\nv_cvt_pk_bf16_f32 v148,0,v148\n" \
"v_cvt_pk_bf16_f32 v149,0,v149\nv_cvt_pk_bf16_f32 v150,0,v150\nv_cvt_pk_bf16_f32 v151,0,v151\nv_add_f32 v148,v148,v164\nv_add_f32 v149,v149,v165\nv_add_f32 v150,v150,v166\nv_add_f32 v151,v151,v167\nM v[12:15],a[20:23],v[204:207],v[12:15]\n" \
"v_cvt_pk_bf16_f32 v148,0,v148\nv_cvt_pk_bf16_f32 v149,0,v149\nv_cvt_pk_bf16_f32 v150,0,v150\nv_cvt_pk_bf16_f32 v151,0,v151\nv_mul_f32 v148,s72,v148\nv_mul_f32 v149,s72,v149\nv_mul_f32 v150,s72,v150\nv_mul_f32 v151,s72,v151\n" \
"v_cvt_pk_bf16_f32 v162,v72,v73\nv_cvt_pk_bf16_f32 v163,v74,v75\nM v[72:75],a[8:11],v[200:203],0\nv_cvt_pk_bf16_f32 v238,v148,v149\nv_cvt_pk_bf16_f32 v239,v150,v151\ns_add_u32 s71,s70,16384\nbuffer_load_dwordx4 v244,s[40:43],s69 offen offset:1024 lds\nbuffer_load_dwordx4 v245,s[40:43],s69 offen offset:2048 lds\n" \
"buffer_load_dwordx4 v246,s[40:43],s69 offen offset:3072 lds\nbuffer_store_dwordx4 v[236:239],v251,s[52:55],s71 offen sc1\nM v[72:75],a[24:27],v[204:207],v[72:75]\nv_cvt_pk_bf16_f32 v178,v76,v77\nv_cvt_pk_bf16_f32 v179,v78,v79\nds_read_b64_tr_b16 v[216:217],v242 offset:512\nds_read_b64_tr_b16 v[218:219],v242 offset:768\nM v[76:79],a[12:15],v[200:203],0\n" \
"ds_read_b64_tr_b16 v[220:221],v242 offset:8704\nds_read_b64_tr_b16 v[222:223],v242 offset:8960\nM v[76:79],a[28:31],v[204:207],v[76:79]\nv_cvt_pk_bf16_f32 v148,v20,v21\nv_cvt_pk_bf16_f32 v149,v22,v23\nM v[16:19],a[0:3],v[208:211],0\nv_cvt_pk_bf16_f32 v164,v80,v81\nv_cvt_pk_bf16_f32 v165,v82,v83\n" \
"M v[16:19],a[16:19],v[212:215],v[16:19]\nv_cvt_pk_bf16_f32 v180,v84,v85\nv_cvt_pk_bf16_f32 v181,v86,v87\nM v[20:23],a[4:7],v[208:211],0\nds_read_b64_tr_b16 v[192:193],v241 offset:1024\nds_read_b64_tr_b16 v[194:195],v241 offset:1280\nM v[20:23],a[20:23],v[212:215],v[20:23]\nds_read_b64_tr_b16 v[196:197],v241 offset:9216\n" \
"ds_read_b64_tr_b16 v[198:199],v241 offset:9472\nM v[80:83],a[8:11],v[208:211],0\nv_cvt_pk_bf16_f32 v134,v24,v25\nv_cvt_pk_bf16_f32 v135,v26,v27\nM v[80:83],a[24:27],v[212:215],v[80:83]\nv_cvt_pk_bf16_f32 v150,v28,v29\nv_cvt_pk_bf16_f32 v151,v30,v31\nM v[84:87],a[12:15],v[208:211],0\n" \
"v_cvt_pk_bf16_f32 v166,v88,v89\nv_cvt_pk_bf16_f32 v167,v90,v91\nM v[84:87],a[28:31],v[212:215],v[84:87]\nv_cvt_pk_bf16_f32 v182,v92,v93\ns_waitcnt lgkmcnt(6)\nM v[24:27],a[0:3],v[216:219],0\nv_cvt_pk_bf16_f32 v183,v94,v95\ns_waitcnt lgkmcnt(4)\n" \
"M v[24:27],a[16:19],v[220:223],v[24:27]\nds_read_b64_tr_b16 v[200:201],v242 offset:1024\nds_read_b64_tr_b16 v[202:203],v242 offset:1280\nM v[28:31],a[4:7],v[216:219],0\nds_read_b64_tr_b16 v[204:205],v242 offset:9216\nds_read_b64_tr_b16 v[206:207],v242 offset:9472\nM v[28:31],a[20:23],v[220:223],v[28:31]\nv_cvt_pk_bf16_f32 v136,v32,v33\n" \
"v_cvt_pk_bf16_f32 v137,v34,v35\nM v[88:91],a[8:11],v[216:219],0\nv_cvt_pk_bf16_f32 v152,v36,v37\nv_cvt_pk_bf16_f32 v153,v38,v39\nM v[88:91],a[24:27],v[220:223],v[88:91]\nv_cvt_pk_bf16_f32 v168,v96,v97\nv_cvt_pk_bf16_f32 v169,v98,v99\nM v[92:95],a[12:15],v[216:219],0\n" \
"s_add_u32 m0,s64,131072\ns_add_u32 s69,s68,1048576\nM v[92:95],a[28:31],v[220:223],v[92:95]\nv_cvt_pk_bf16_f32 v184,v100,v101\ns_waitcnt lgkmcnt(4)\nM v[32:35],a[0:3],v[192:195],0\nv_cvt_pk_bf16_f32 v185,v102,v103\nds_read_b64_tr_b16 v[208:209],v241 offset:1536\n" \
"M v[32:35],a[16:19],v[196:199],v[32:35]\nds_read_b64_tr_b16 v[210:211],v241 offset:1792\nds_read_b64_tr_b16 v[212:213],v241 offset:9728\nM v[36:39],a[4:7],v[192:195],0\nds_read_b64_tr_b16 v[214:215],v241 offset:9984\nv_cvt_pk_bf16_f32 v138,v40,v41\nM v[36:39],a[20:23],v[196:199],v[36:39]\nv_cvt_pk_bf16_f32 v139,v42,v43\n" \
"v_cvt_pk_bf16_f32 v154,v44,v45\nM v[96:99],a[8:11],v[192:195],0\nv_cvt_pk_bf16_f32 v155,v46,v47\nds_read_b64_tr_b16 v[216:217],v242 offset:1536\nM v[96:99],a[24:27],v[196:199],v[96:99]\nds_read_b64_tr_b16 v[218:219],v242 offset:1792\nds_read_b64_tr_b16 v[220:221],v242 offset:9728\nM v[100:103],a[12:15],v[192:195],0\n" \
"ds_read_b64_tr_b16 v[222:223],v242 offset:9984\nv_cvt_pk_bf16_f32 v170,v104,v105\nM v[100:103],a[28:31],v[196:199],v[100:103]\nv_cvt_pk_bf16_f32 v171,v106,v107\ns_waitcnt lgkmcnt(8)\nM v[40:43],a[0:3],v[200:203],0\nv_cvt_pk_bf16_f32 v186,v108,v109\nv_cvt_pk_bf16_f32 v187,v110,v111\n" \
"M v[40:43],a[16:19],v[204:207],v[40:43]\nv_cvt_pk_bf16_f32 v140,v48,v49\nv_cvt_pk_bf16_f32 v141,v50,v51\nM v[44:47],a[4:7],v[200:203],0\nv_cvt_pk_bf16_f32 v156,v52,v53\nv_cvt_pk_bf16_f32 v157,v54,v55\nM v[44:47],a[20:23],v[204:207],v[44:47]\nv_cvt_pk_bf16_f32 v172,v112,v113\n" \
"v_cvt_pk_bf16_f32 v173,v114,v115\nM v[104:107],a[8:11],v[200:203],0\nv_cvt_pk_bf16_f32 v188,v116,v117\nv_cvt_pk_bf16_f32 v189,v118,v119\nM v[104:107],a[24:27],v[204:207],v[104:107]\nv_cvt_pk_bf16_f32 v142,v56,v57\nv_cvt_pk_bf16_f32 v143,v58,v59\nM v[108:111],a[12:15],v[200:203],0\n" \
"v_cvt_pk_bf16_f32 v158,v60,v61\nv_cvt_pk_bf16_f32 v159,v62,v63\nM v[108:111],a[28:31],v[204:207],v[108:111]\nv_cvt_pk_bf16_f32 v174,v120,v121\ns_waitcnt lgkmcnt(2)\nM v[48:51],a[0:3],v[208:211],0\nv_cvt_pk_bf16_f32 v175,v122,v123\nv_cvt_pk_bf16_f32 v190,v124,v125\n" \
"M v[48:51],a[16:19],v[212:215],v[48:51]\nv_cvt_pk_bf16_f32 v191,v126,v127\ns_lshl_b32 s70,s62,5\nM v[52:55],a[4:7],v[208:211],0\ns_add_u32 s70,s70,s63\ns_lshl2_add_u32 s70,s70,s7\nM v[52:55],a[20:23],v[212:215],v[52:55]\ns_lshl2_add_u32 s79,s70,90112\n" \
"s_lshl_b32 s70,s60,4\nM v[112:115],a[8:11],v[208:211],0\ns_waitcnt vmcnt(1) lgkmcnt(0)\ns_barrier\nM v[112:115],a[24:27],v[212:215],v[112:115]\nds_read_b64_tr_b16 v[192:193],v241 offset:16384\nds_read_b64_tr_b16 v[194:195],v241 offset:16640\nM v[116:119],a[12:15],v[208:211],0\n" \
"ds_read_b64_tr_b16 v[196:197],v241 offset:24576\nds_read_b64_tr_b16 v[198:199],v241 offset:24832\nM v[116:119],a[28:31],v[212:215],v[116:119]\nds_read_b64_tr_b16 v[200:201],v242 offset:16384\nds_read_b64_tr_b16 v[202:203],v242 offset:16640\nM v[56:59],a[0:3],v[216:219],0\nds_read_b64_tr_b16 v[204:205],v242 offset:24576\nds_read_b64_tr_b16 v[206:207],v242 offset:24832\n" \
"M v[56:59],a[16:19],v[220:223],v[56:59]\nds_read_b64_tr_b16 v[208:209],v241 offset:16896\nds_read_b64_tr_b16 v[210:211],v241 offset:17152\nM v[60:63],a[4:7],v[216:219],0\nds_read_b64_tr_b16 v[212:213],v241 offset:25088\nds_read_b64_tr_b16 v[214:215],v241 offset:25344\nM v[60:63],a[20:23],v[220:223],v[60:63]\nbuffer_load_dwordx4 v243,s[40:43],s69 offen offset:0 lds\n" \
"M v[120:123],a[8:11],v[216:219],0\ns_sub_u32 s70,s70,s62\ns_add_u32 s70,s70,15\nM v[120:123],a[24:27],v[220:223],v[120:123]\ns_lshl2_add_u32 s70,s70,s7\ns_lshl_b32 s70,s70,15\nM v[124:127],a[12:15],v[216:219],0\nds_read_b64_tr_b16 v[216:217],v242 offset:16896\n" \
"ds_read_b64_tr_b16 v[218:219],v242 offset:17152\nM v[124:127],a[28:31],v[220:223],v[124:127]\ns_lshl_b32 s71,s61,8\ns_waitcnt lgkmcnt(12)\nM v[0:3],a[32:35],v[192:195],v[0:3]\nds_read_b64_tr_b16 v[220:221],v242 offset:25088\ns_waitcnt lgkmcnt(11)\nM v[0:3],a[48:51],v[196:199],v[0:3]\n" \
"buffer_load_dwordx4 v244,s[40:43],s69 offen offset:1024 lds\nM v[4:7],a[36:39],v[192:195],v[4:7]\nds_read_b64_tr_b16 v[222:223],v242 offset:25344\ns_add_u32 s70,s70,s71\nM v[4:7],a[52:55],v[196:199],v[4:7]\ns_add_u32 s78,s70,54525952\ns_lshl3_add_u32 s70,s62,s7\nM v[64:67],a[40:43],v[192:195],v[64:67]\n" \
"s_lshl_b32 s76,s70,15\ns_lshl_b32 s80,s70,6\nM v[64:67],a[56:59],v[196:199],v[64:67]\nbuffer_load_dwordx4 v245,s[40:43],s69 offen offset:2048 lds\nM v[68:71],a[44:47],v[192:195],v[68:71]\ns_add_u32 s76,s76,1048576\ns_add_u32 s77,s76,131072\nM v[68:71],a[60:63],v[196:199],v[68:71]\n" \
"s_lshl_b32 s70,s94,12\ns_waitcnt lgkmcnt(4)\nM v[8:11],a[32:35],v[200:203],v[8:11]\ns_or_b32 s76,s76,s70\ns_or_b32 s77,s77,s70\nM v[8:11],a[48:51],v[204:207],v[8:11]\nbuffer_load_dwordx4 v246,s[40:43],s69 offen offset:3072 lds\nM v[12:15],a[36:39],v[200:203],v[12:15]\n" \
"s_lshl_b32 s70,s63,5\ns_add_u32 s70,s70,s74\nM v[12:15],a[52:55],v[204:207],v[12:15]\ns_lshl_b32 s74,s70,14\ns_add_u32 s74,s74,3145728\nM v[72:75],a[40:43],v[200:203],v[72:75]\nds_read_b64_tr_b16 v[192:193],v241 offset:17408\nds_read_b64_tr_b16 v[194:195],v241 offset:17664\n" \
"M v[72:75],a[56:59],v[204:207],v[72:75]\nds_read_b64_tr_b16 v[196:197],v241 offset:25600\nds_read_b64_tr_b16 v[198:199],v241 offset:25856\nM v[76:79],a[44:47],v[200:203],v[76:79]\nds_read_b64_tr_b16 v[200:201],v242 offset:17408\nds_read_b64_tr_b16 v[202:203],v242 offset:17664\nM v[76:79],a[60:63],v[204:207],v[76:79]\nds_read_b64_tr_b16 v[204:205],v242 offset:25600\n" \
"ds_read_b64_tr_b16 v[206:207],v242 offset:25856\nM v[16:19],a[32:35],v[208:211],v[16:19]\nbuffer_store_dwordx4 v[128:131],v240,s[52:55],s74 offen offset:0 sc1\nM v[16:19],a[48:51],v[212:215],v[16:19]\ns_add_u32 m0,s64,147456\ns_add_u32 s69,s68,1572864\nM v[20:23],a[36:39],v[208:211],v[20:23]\nv_lshlrev_b32 v228,2,v228\n" \
"v_lshl_or_b32 v228,v229,4,v228\nM v[20:23],a[52:55],v[212:215],v[20:23]\ns_add_u32 s80,s80,90112\ns_lshl2_add_u32 s29,s70,81920\nM v[80:83],a[40:43],v[208:211],v[80:83]\nbuffer_load_dwordx4 v243,s[40:43],s69 offen offset:0 lds\nM v[80:83],a[56:59],v[212:215],v[80:83]\ns_lshl_b32 s70,s7,14\n" \
"v_add_u32 v250,s70,v240\nM v[84:87],a[44:47],v[208:211],v[84:87]\nds_read_b64_tr_b16 v[208:209],v241 offset:17920\nds_read_b64_tr_b16 v[210:211],v241 offset:18176\nM v[84:87],a[60:63],v[212:215],v[84:87]\nds_read_b64_tr_b16 v[212:213],v241 offset:26112\ns_waitcnt lgkmcnt(8)\nM v[24:27],a[32:35],v[216:219],v[24:27]\n" \
"ds_read_b64_tr_b16 v[214:215],v241 offset:26368\ns_lshl_b32 s70,s63,19\nM v[24:27],a[48:51],v[220:223],v[24:27]\nbuffer_load_dwordx4 v244,s[40:43],s69 offen offset:1024 lds\nM v[28:31],a[36:39],v[216:219],v[28:31]\nds_write_b64 v250,v[128:129] offset:0\ns_add_u32 s72,s78,16384\nM v[28:31],a[52:55],v[220:223],v[28:31]\n" \
"ds_write_b64 v250,v[130:131] offset:8\ns_mul_i32 s71,s60,14\nM v[88:91],a[40:43],v[216:219],v[88:91]\nds_write_b64 v250,v[132:133] offset:1024\nv_and_b32 v229,15,v249\nM v[88:91],a[56:59],v[220:223],v[88:91]\nbuffer_load_dwordx4 v245,s[40:43],s69 offen offset:2048 lds\nM v[92:95],a[44:47],v[216:219],v[92:95]\n" \
"ds_read_b64_tr_b16 v[216:217],v242 offset:17920\nds_read_b64_tr_b16 v[218:219],v242 offset:18176\nM v[92:95],a[60:63],v[220:223],v[92:95]\nds_read_b64_tr_b16 v[220:221],v242 offset:26112\nds_read_b64_tr_b16 v[222:223],v242 offset:26368\nM v[32:35],a[32:35],v[192:195],v[32:35]\nv_lshlrev_b32 v229,7,v229\ns_waitcnt lgkmcnt(10)\n" \
"M v[32:35],a[48:51],v[196:199],v[32:35]\nbuffer_load_dwordx4 v246,s[40:43],s69 offen offset:3072 lds\nM v[36:39],a[36:39],v[192:195],v[36:39]\ns_add_u32 m0,s64,98304\ns_add_u32 s69,s68,2097152\nM v[36:39],a[52:55],v[196:199],v[36:39]\nds_write_b64 v250,v[134:135] offset:1032\nv_add_u32 v253,0xffd00000,v253\n" \
"M v[96:99],a[40:43],v[192:195],v[96:99]\ns_add_u32 s44,s34,s70\ns_addc_u32 s45,s35,0\nM v[96:99],a[56:59],v[196:199],v[96:99]\nbuffer_store_dwordx4 v[132:135],v240,s[52:55],s74 offen offset:1024 sc1\nM v[100:103],a[44:47],v[192:195],v[100:103]\ns_lshl_b32 s70,s62,15\ns_add_u32 s96,s70,s64\n" \
"M v[100:103],a[60:63],v[196:199],v[100:103]\ns_sub_u32 s96,s96,16384\ns_sub_u32 s46,491520,s70\nM v[40:43],a[32:35],v[200:203],v[40:43]\ns_cmp_eq_u32 s36,s96\ns_cselect_b32 s72,0xc000,s98\ns_add_u32 s65,s36,s72\nM v[40:43],a[48:51],v[204:207],v[40:43]\n" \
"buffer_load_dwordx4 v243,s[40:43],s69 offen offset:0 lds\nM v[44:47],a[36:39],v[200:203],v[44:47]\ns_cmp_eq_u32 s65,s96\ns_cselect_b32 s72,0xc000,s98\ns_add_u32 s66,s65,s72\nM v[44:47],a[52:55],v[204:207],v[44:47]\ns_lshl2_add_u32 s70,s62,0\ns_sub_u32 s70,s70,s7\n" \
"M v[104:107],a[40:43],v[200:203],v[104:107]\ns_lshl_b32 s70,s70,16\ns_add_u32 s33,s78,s70\nM v[104:107],a[56:59],v[204:207],v[104:107]\nbuffer_load_dwordx4 v244,s[40:43],s69 offen offset:1024 lds\nM v[108:111],a[44:47],v[200:203],v[108:111]\nds_write_b64 v250,v[136:137] offset:2048\ns_lshl3_add_u32 s70,s62,s7\n" \
"M v[108:111],a[60:63],v[204:207],v[108:111]\ns_cmp_eq_u32 s66,s96\ns_cselect_b32 s72,0xc000,s98\ns_add_u32 s95,s66,s72\ns_waitcnt lgkmcnt(1)\nM v[48:51],a[32:35],v[208:211],v[48:51]\ns_cmp_eq_u32 s62,7\ns_cselect_b32 s46,229376,s46\n" \
"M v[48:51],a[48:51],v[212:215],v[48:51]\ns_waitcnt vmcnt(12)\nbuffer_store_dword v252,off,s[52:55],s79 sc1\nM v[52:55],a[36:39],v[208:211],v[52:55]\nbuffer_load_dwordx4 v245,s[40:43],s69 offen offset:2048 lds\nM v[52:55],a[52:55],v[212:215],v[52:55]\ns_lshl3_add_u32 s70,s70,s60\ns_lshl_b32 s67,s70,15\n" \
"M v[112:115],a[40:43],v[208:211],v[112:115]\ns_waitcnt vmcnt(10)\ns_barrier\nM v[112:115],a[56:59],v[212:215],v[112:115]\nds_read_b64_tr_b16 v[192:193],v241 offset:32768\nds_read_b64_tr_b16 v[194:195],v241 offset:33024\nM v[116:119],a[44:47],v[208:211],v[116:119]\nds_read_b64_tr_b16 v[196:197],v241 offset:40960\n" \
"ds_read_b64_tr_b16 v[198:199],v241 offset:41216\nM v[116:119],a[60:63],v[212:215],v[116:119]\nbuffer_store_dwordx4 v[136:139],v240,s[52:55],s74 offen offset:2048 sc1\nM v[56:59],a[32:35],v[216:219],v[56:59]\nds_read_b64_tr_b16 v[200:201],v242 offset:32768\nds_read_b64_tr_b16 v[202:203],v242 offset:33024\nM v[56:59],a[48:51],v[220:223],v[56:59]\nds_read_b64_tr_b16 v[204:205],v242 offset:40960\n" \
"ds_read_b64_tr_b16 v[206:207],v242 offset:41216\nM v[60:63],a[36:39],v[216:219],v[60:63]\nds_read_b64_tr_b16 v[208:209],v241 offset:33280\nds_read_b64_tr_b16 v[210:211],v241 offset:33536\nM v[60:63],a[52:55],v[220:223],v[60:63]\nds_read_b64_tr_b16 v[212:213],v241 offset:41472\nds_read_b64_tr_b16 v[214:215],v241 offset:41728\nM v[120:123],a[40:43],v[216:219],v[120:123]\n" \
"buffer_load_dwordx4 v246,s[40:43],s69 offen offset:3072 lds\nM v[120:123],a[56:59],v[220:223],v[120:123]\ns_add_u32 m0,s64,114688\ns_add_u32 s69,s68,2621440\nM v[124:127],a[44:47],v[216:219],v[124:127]\nds_read_b64_tr_b16 v[216:217],v242 offset:33280\nds_read_b64_tr_b16 v[218:219],v242 offset:33536\nM v[124:127],a[60:63],v[220:223],v[124:127]\n" \
"ds_read_b64_tr_b16 v[220:221],v242 offset:41472\ns_waitcnt lgkmcnt(13)\nM v[0:3],a[64:67],v[192:195],v[0:3]\nds_read_b64_tr_b16 v[222:223],v242 offset:41728\ns_waitcnt lgkmcnt(12)\nM v[0:3],a[80:83],v[196:199],v[0:3]\nbuffer_load_dwordx4 v243,s[40:43],s69 offen offset:0 lds\nM v[4:7],a[68:71],v[192:195],v[4:7]\n" \
"ds_write_b64 v250,v[138:139] offset:2056\ns_add_u32 s15,s64,s46\nM v[4:7],a[84:87],v[196:199],v[4:7]\ns_cmp_gt_i32 s62,-1\ns_cselect_b32 s72,s98,0xfffe4000\nM v[64:67],a[72:75],v[192:195],v[64:67]\ns_lshl3_add_u32 s70,s62,s60\ns_add_u32 s15,s15,s72\n" \
"M v[64:67],a[88:91],v[196:199],v[64:67]\nbuffer_load_dwordx4 v244,s[40:43],s69 offen offset:1024 lds\nM v[68:71],a[76:79],v[192:195],v[68:71]\nds_read_b64_tr_b16 v[192:193],v241 offset:33792\nds_read_b64_tr_b16 v[194:195],v241 offset:34048\nM v[68:71],a[92:95],v[196:199],v[68:71]\nds_read_b64_tr_b16 v[196:197],v241 offset:41984\ns_waitcnt lgkmcnt(8)\n" \
"M v[8:11],a[64:67],v[200:203],v[8:11]\nds_read_b64_tr_b16 v[198:199],v241 offset:42240\ns_lshl2_add_u32 s70,s70,s7\nM v[8:11],a[80:83],v[204:207],v[8:11]\nbuffer_store_dwordx4 v[140:143],v240,s[52:55],s74 offen offset:3072 sc1\nM v[12:15],a[68:71],v[200:203],v[12:15]\ns_add_u32 s74,s74,4096\ns_lshl4_add_u32 s79,s70,65536\n" \
"M v[12:15],a[84:87],v[204:207],v[12:15]\nds_write_b64 v250,v[140:141] offset:3072\ns_add_u32 s97,s96,65536\nM v[72:75],a[72:75],v[200:203],v[72:75]\ns_cmp_eq_u32 s62,7\ns_cselect_b32 s72,98304,0\nM v[72:75],a[88:91],v[204:207],v[72:75]\nbuffer_load_dwordx4 v245,s[40:43],s69 offen offset:2048 lds\n" \
"M v[76:79],a[76:79],v[200:203],v[76:79]\nds_read_b64_tr_b16 v[200:201],v242 offset:33792\nds_read_b64_tr_b16 v[202:203],v242 offset:34048\nM v[76:79],a[92:95],v[204:207],v[76:79]\nds_read_b64_tr_b16 v[204:205],v242 offset:41984\nds_read_b64_tr_b16 v[206:207],v242 offset:42240\nM v[16:19],a[64:67],v[208:211],v[16:19]\ns_lshr_b32 s70,s6,3\n" \
"s_sub_u32 s97,s97,s72\nM v[16:19],a[80:83],v[212:215],v[16:19]\nbuffer_load_dwordx4 v246,s[40:43],s69 offen offset:3072 lds\nM v[20:23],a[68:71],v[208:211],v[20:23]\nds_write_b64 v250,v[142:143] offset:3080\ns_sub_u32 s70,s70,16\nM v[20:23],a[84:87],v[212:215],v[20:23]\ns_add_u32 s2,s70,s71\n" \
"s_lshl_b32 s72,s60,1\nM v[80:83],a[72:75],v[208:211],v[80:83]\ns_mul_i32 s71,s60,448\ns_add_u32 s72,s72,s62\nM v[80:83],a[88:91],v[212:215],v[80:83]\ns_and_b32 s71,s71,0xffffff80\ns_sub_u32 s72,s72,6\nM v[84:87],a[76:79],v[208:211],v[84:87]\n" \
"ds_read_b64_tr_b16 v[208:209],v241 offset:34304\nds_read_b64_tr_b16 v[210:211],v241 offset:34560\nM v[84:87],a[92:95],v[212:215],v[84:87]\nds_read_b64_tr_b16 v[212:213],v241 offset:42496\ns_waitcnt lgkmcnt(8)\nM v[24:27],a[64:67],v[216:219],v[24:27]\nbuffer_store_dwordx4 v[144:147],v240,s[52:55],s74 offen offset:0 sc1\nM v[24:27],a[80:83],v[220:223],v[24:27]\n" \
"ds_read_b64_tr_b16 v[214:215],v241 offset:42752\ns_sub_u32 s58,1835008,s71\nM v[28:31],a[68:71],v[216:219],v[28:31]\ns_lshl_b32 s72,s72,18\ns_lshl_b32 s2,s2,5\nM v[28:31],a[84:87],v[220:223],v[28:31]\ns_cmp_ge_u32 s62,6\ns_cselect_b64 s[56:57],s[18:19],s[56:57]\n" \
"s_cselect_b32 s71,s72,s71\ns_cselect_b32 s58,0x40000,s58\ns_cselect_b32 s0,2048,s0\nM v[88:91],a[72:75],v[216:219],v[88:91]\ns_mul_i32 s72,s60,229376\nv_lshrrev_b32 v253,4,v249\nM v[88:91],a[88:91],v[220:223],v[88:91]\ns_cmp_eq_u32 s62,3\n" \
"s_cselect_b64 s[56:57],s[26:27],s[56:57]\ns_cselect_b32 s71,s72,s71\ns_cselect_b32 s58,229376,s58\ns_cselect_b32 s0,4096,s0\nM v[92:95],a[76:79],v[216:219],v[92:95]\nds_read_b64_tr_b16 v[216:217],v242 offset:34304\nds_read_b64_tr_b16 v[218:219],v242 offset:34560\nM v[92:95],a[92:95],v[220:223],v[92:95]\n" \
"ds_read_b64_tr_b16 v[220:221],v242 offset:42496\nds_read_b64_tr_b16 v[222:223],v242 offset:42752\nM v[32:35],a[64:67],v[192:195],v[32:35]\nds_write_b64 v250,v[144:145] offset:4096\nv_and_b32 v251,0xf0,v240\nM v[32:35],a[80:83],v[196:199],v[32:35]\ns_cmp_lt_u32 s70,14\ns_cselect_b32 s2,s2,0\n" \
"s_cselect_b32 s3,-1,0\nM v[36:39],a[68:71],v[192:195],v[36:39]\ns_lshl2_add_u32 s70,s61,s7\ns_mul_i32 s1,s70,s0\nM v[36:39],a[84:87],v[196:199],v[36:39]\ns_add_u32 s48,s44,19922944\ns_addc_u32 s49,s45,0\nM v[96:99],a[72:75],v[192:195],v[96:99]\n" \
"v_lshl_or_b32 v253,v253,12,v251\ns_lshl_b32 s70,s61,8\nM v[96:99],a[88:91],v[196:199],v[96:99]\ns_add_u32 s44,s44,3145728\ns_addc_u32 s45,s45,0\nM v[100:103],a[76:79],v[192:195],v[100:103]\ns_mov_b32 s50,s46\ns_mov_b32 s94,0xc000\n" \
"M v[100:103],a[92:95],v[196:199],v[100:103]\ns_sub_u32 s33,s33,1867776\ns_waitcnt lgkmcnt(5)\nM v[40:43],a[64:67],v[200:203],v[40:43]\ns_add_u32 s67,s67,36700160\ns_lshl_b32 s0,s0,4\nM v[40:43],a[80:83],v[204:207],v[40:43]\ns_cmp_lt_u32 s62,3\n" \
"s_cselect_b32 s58,0,s58\nM v[44:47],a[68:71],v[200:203],v[44:47]\nbuffer_store_dwordx4 v[148:151],v240,s[52:55],s74 offen offset:1024 sc1\nM v[44:47],a[84:87],v[204:207],v[44:47]\nds_write_b64 v250,v[146:147] offset:4104\nM v[104:107],a[72:75],v[200:203],v[104:107]\ns_add_u32 s56,s56,s71\ns_addc_u32 s57,s57,0\n" \
"M v[104:107],a[88:91],v[204:207],v[104:107]\nM v[108:111],a[76:79],v[200:203],v[108:111]\nM v[108:111],a[92:95],v[204:207],v[108:111]\nM v[48:51],a[64:67],v[208:211],v[48:51]\nM v[48:51],a[80:83],v[212:215],v[48:51]\nM v[52:55],a[68:71],v[208:211],v[52:55]\ns_add_u32 m0,s64,131072\ns_add_u32 s69,s68,3145728\n" \
"M v[52:55],a[84:87],v[212:215],v[52:55]\nM v[112:115],a[72:75],v[208:211],v[112:115]\ns_waitcnt vmcnt(14) lgkmcnt(1)\ns_barrier\nM v[112:115],a[88:91],v[212:215],v[112:115]\nds_read_b64_tr_b16 v[192:193],v241 offset:49152\nds_read_b64_tr_b16 v[194:195],v241 offset:49408\nM v[116:119],a[76:79],v[208:211],v[116:119]\n" \
"ds_read_b64_tr_b16 v[196:197],v241 offset:57344\nds_read_b64_tr_b16 v[198:199],v241 offset:57600\nM v[116:119],a[92:95],v[212:215],v[116:119]\nds_read_b64_tr_b16 v[200:201],v242 offset:49152\nds_read_b64_tr_b16 v[202:203],v242 offset:49408\nM v[56:59],a[64:67],v[216:219],v[56:59]\nds_read_b64_tr_b16 v[204:205],v242 offset:57344\nds_read_b64_tr_b16 v[206:207],v242 offset:57600\n" \
"M v[56:59],a[80:83],v[220:223],v[56:59]\nds_read_b64_tr_b16 v[208:209],v241 offset:49664\nds_read_b64_tr_b16 v[210:211],v241 offset:49920\nM v[60:63],a[68:71],v[216:219],v[60:63]\nds_read_b64_tr_b16 v[212:213],v241 offset:57856\nds_read_b64_tr_b16 v[214:215],v241 offset:58112\nM v[60:63],a[84:87],v[220:223],v[60:63]\nbuffer_load_dwordx4 v243,s[40:43],s69 offen offset:0 lds\n" \
"M v[120:123],a[72:75],v[216:219],v[120:123]\nds_write_b64 v250,v[148:149] offset:5120\nM v[120:123],a[88:91],v[220:223],v[120:123]\nM v[124:127],a[76:79],v[216:219],v[124:127]\nds_read_b64_tr_b16 v[216:217],v242 offset:49664\nds_read_b64_tr_b16 v[218:219],v242 offset:49920\nM v[124:127],a[92:95],v[220:223],v[124:127]\nds_read_b64_tr_b16 v[220:221],v242 offset:57856\n" \
"s_waitcnt lgkmcnt(14)\nM v[0:3],a[96:99],v[192:195],v[0:3]\nbuffer_store_dwordx4 v[152:155],v240,s[52:55],s74 offen offset:2048 sc1\ns_waitcnt lgkmcnt(12)\nM v[0:3],a[112:115],v[196:199],v[0:3]\nds_read_b64_tr_b16 v[222:223],v242 offset:58112\nM v[4:7],a[100:103],v[192:195],v[4:7]\nds_write_b64 v250,v[150:151] offset:5128\n" \
"M v[4:7],a[116:119],v[196:199],v[4:7]\nM v[64:67],a[104:107],v[192:195],v[64:67]\nbuffer_load_dwordx4 v244,s[40:43],s69 offen offset:1024 lds\nM v[64:67],a[120:123],v[196:199],v[64:67]\nM v[68:71],a[108:111],v[192:195],v[68:71]\nds_read_b64_tr_b16 v[192:193],v241 offset:50176\nds_read_b64_tr_b16 v[194:195],v241 offset:50432\nM v[68:71],a[124:127],v[196:199],v[68:71]\n" \
"ds_read_b64_tr_b16 v[196:197],v241 offset:58368\ns_waitcnt lgkmcnt(9)\nM v[8:11],a[96:99],v[200:203],v[8:11]\nbuffer_load_dwordx4 v245,s[40:43],s69 offen offset:2048 lds\nM v[8:11],a[112:115],v[204:207],v[8:11]\nds_read_b64_tr_b16 v[198:199],v241 offset:58624\nM v[12:15],a[100:103],v[200:203],v[12:15]\nM v[12:15],a[116:119],v[204:207],v[12:15]\n" \
"M v[72:75],a[104:107],v[200:203],v[72:75]\nbuffer_load_dwordx4 v246,s[40:43],s69 offen offset:3072 lds\nM v[72:75],a[120:123],v[204:207],v[72:75]\nM v[76:79],a[108:111],v[200:203],v[76:79]\nds_read_b64_tr_b16 v[200:201],v242 offset:50176\nds_read_b64_tr_b16 v[202:203],v242 offset:50432\nM v[76:79],a[124:127],v[204:207],v[76:79]\nds_read_b64_tr_b16 v[204:205],v242 offset:58368\n" \
"ds_read_b64_tr_b16 v[206:207],v242 offset:58624\nM v[16:19],a[96:99],v[208:211],v[16:19]\nbuffer_store_dwordx4 v[156:159],v240,s[52:55],s74 offen offset:3072 sc1\nM v[16:19],a[112:115],v[212:215],v[16:19]\nds_write_b64 v250,v[152:153] offset:6144\ns_add_u32 s74,s74,4096\nM v[20:23],a[100:103],v[208:211],v[20:23]\nM v[20:23],a[116:119],v[212:215],v[20:23]\n" \
"M v[80:83],a[104:107],v[208:211],v[80:83]\nM v[80:83],a[120:123],v[212:215],v[80:83]\nM v[84:87],a[108:111],v[208:211],v[84:87]\nds_read_b64_tr_b16 v[208:209],v241 offset:50688\nds_read_b64_tr_b16 v[210:211],v241 offset:50944\nM v[84:87],a[124:127],v[212:215],v[84:87]\nds_read_b64_tr_b16 v[212:213],v241 offset:58880\ns_waitcnt lgkmcnt(8)\n" \
"M v[24:27],a[96:99],v[216:219],v[24:27]\nds_read_b64_tr_b16 v[214:215],v241 offset:59136\nM v[24:27],a[112:115],v[220:223],v[24:27]\nM v[28:31],a[100:103],v[216:219],v[28:31]\nds_write_b64 v250,v[154:155] offset:6152\nM v[28:31],a[116:119],v[220:223],v[28:31]\nM v[88:91],a[104:107],v[216:219],v[88:91]\nM v[88:91],a[120:123],v[220:223],v[88:91]\n" \
"M v[92:95],a[108:111],v[216:219],v[92:95]\nds_read_b64_tr_b16 v[216:217],v242 offset:50688\nds_read_b64_tr_b16 v[218:219],v242 offset:50944\nM v[92:95],a[124:127],v[220:223],v[92:95]\nds_read_b64_tr_b16 v[220:221],v242 offset:58880\nds_read_b64_tr_b16 v[222:223],v242 offset:59136\nM v[32:35],a[96:99],v[192:195],v[32:35]\nbuffer_store_dwordx4 v[160:163],v240,s[52:55],s74 offen offset:0 sc1\n" \
"M v[32:35],a[112:115],v[196:199],v[32:35]\nM v[36:39],a[100:103],v[192:195],v[36:39]\nM v[36:39],a[116:119],v[196:199],v[36:39]\nM v[96:99],a[104:107],v[192:195],v[96:99]\nM v[96:99],a[120:123],v[196:199],v[96:99]\nM v[100:103],a[108:111],v[192:195],v[100:103]\nds_write_b64 v250,v[156:157] offset:7168\nM v[100:103],a[124:127],v[196:199],v[100:103]\n" \
"s_waitcnt lgkmcnt(7)\nM v[40:43],a[96:99],v[200:203],v[40:43]\nM v[40:43],a[112:115],v[204:207],v[40:43]\nM v[44:47],a[100:103],v[200:203],v[44:47]\nM v[44:47],a[116:119],v[204:207],v[44:47]\nM v[104:107],a[104:107],v[200:203],v[104:107]\nM v[104:107],a[120:123],v[204:207],v[104:107]\nM v[108:111],a[108:111],v[200:203],v[108:111]\n" \
"M v[108:111],a[124:127],v[204:207],v[108:111]\nM v[48:51],a[96:99],v[208:211],v[48:51]\ns_waitcnt lgkmcnt(3)\nM v[48:51],a[112:115],v[212:215],v[48:51]\nM v[52:55],a[100:103],v[208:211],v[52:55]\nbuffer_store_dwordx4 v[164:167],v240,s[52:55],s74 offen offset:1024 sc1\nM v[52:55],a[116:119],v[212:215],v[52:55]\ns_add_u32 m0,s64,147456\n" \
"s_add_u32 s69,s68,3670016\nM v[112:115],a[104:107],v[208:211],v[112:115]\ns_waitcnt vmcnt(15) lgkmcnt(1)\ns_barrier\nM v[112:115],a[120:123],v[212:215],v[112:115]\nds_read_b64_tr_b16 v[192:193],v241 offset:0\nds_read_b64_tr_b16 v[194:195],v241 offset:256\nM v[116:119],a[108:111],v[208:211],v[116:119]\n" \
"ds_read_b64_tr_b16 v[196:197],v241 offset:8192\nds_read_b64_tr_b16 v[198:199],v241 offset:8448\nM v[116:119],a[124:127],v[212:215],v[116:119]\nds_read_b64_tr_b16 v[200:201],v242 offset:0\nds_read_b64_tr_b16 v[202:203],v242 offset:256\nM v[56:59],a[96:99],v[216:219],v[56:59]\nds_read_b64_tr_b16 v[204:205],v242 offset:8192\nds_read_b64_tr_b16 v[206:207],v242 offset:8448\n" \
"M v[56:59],a[112:115],v[220:223],v[56:59]\nds_read_b64_tr_b16 v[208:209],v241 offset:512\nds_read_b64_tr_b16 v[210:211],v241 offset:768\nM v[60:63],a[100:103],v[216:219],v[60:63]\nds_read_b64_tr_b16 v[212:213],v241 offset:8704\nds_read_b64_tr_b16 v[214:215],v241 offset:8960\nM v[60:63],a[116:119],v[220:223],v[60:63]\nbuffer_load_dwordx4 v243,s[40:43],s69 offen offset:0 lds\n" \
"M v[120:123],a[104:107],v[216:219],v[120:123]\nds_write_b64 v250,v[158:159] offset:7176\ns_lshl_b32 s68,s60,19\nM v[120:123],a[120:123],v[220:223],v[120:123]\ns_lshl2_add_u32 s68,s64,s68\ns_add_u32 s68,s68,s70\nM v[124:127],a[108:111],v[216:219],v[124:127]\nds_read_b64_tr_b16 v[216:217],v242 offset:512\n" \
"ds_read_b64_tr_b16 v[218:219],v242 offset:768\nM v[124:127],a[124:127],v[220:223],v[124:127]\nds_read_b64_tr_b16 v[220:221],v242 offset:8704\ns_waitcnt lgkmcnt(14)\nM v[0:3],a[128:131],v[192:195],v[0:3]\nds_read_b64_tr_b16 v[222:223],v242 offset:8960\ns_waitcnt lgkmcnt(13)\nM v[0:3],a[144:147],v[196:199],v[0:3]\n" \
"buffer_load_dwordx4 v244,s[40:43],s69 offen offset:1024 lds\nM v[4:7],a[132:135],v[192:195],v[4:7]\nds_write_b64 v250,v[160:161] offset:8192\nM v[4:7],a[148:151],v[196:199],v[4:7]\nM v[64:67],a[136:139],v[192:195],v[64:67]\nM v[64:67],a[152:155],v[196:199],v[64:67]\nbuffer_store_dwordx4 v[168:171],v240,s[52:55],s74 offen offset:2048 sc1\nM v[68:71],a[140:143],v[192:195],v[68:71]\n" \
"ds_read_b64_tr_b16 v[192:193],v241 offset:1024\nds_read_b64_tr_b16 v[194:195],v241 offset:1280\nM v[68:71],a[156:159],v[196:199],v[68:71]\nds_read_b64_tr_b16 v[196:197],v241 offset:9216\ns_waitcnt lgkmcnt(9)\nM v[8:11],a[128:131],v[200:203],v[8:11]\nds_read_b64_tr_b16 v[198:199],v241 offset:9472\nM v[8:11],a[144:147],v[204:207],v[8:11]\n" \
"buffer_load_dwordx4 v245,s[40:43],s69 offen offset:2048 lds\nM v[12:15],a[132:135],v[200:203],v[12:15]\nds_write_b64 v250,v[162:163] offset:8200\nM v[12:15],a[148:151],v[204:207],v[12:15]\nM v[72:75],a[136:139],v[200:203],v[72:75]\nM v[72:75],a[152:155],v[204:207],v[72:75]\nbuffer_load_dwordx4 v246,s[40:43],s69 offen offset:3072 lds\nM v[76:79],a[140:143],v[200:203],v[76:79]\n" \
"ds_read_b64_tr_b16 v[200:201],v242 offset:1024\nds_read_b64_tr_b16 v[202:203],v242 offset:1280\nM v[76:79],a[156:159],v[204:207],v[76:79]\nds_read_b64_tr_b16 v[204:205],v242 offset:9216\nds_read_b64_tr_b16 v[206:207],v242 offset:9472\nM v[16:19],a[128:131],v[208:211],v[16:19]\nM v[16:19],a[144:147],v[212:215],v[16:19]\nM v[20:23],a[132:135],v[208:211],v[20:23]\n" \
"M v[20:23],a[148:151],v[212:215],v[20:23]\nM v[80:83],a[136:139],v[208:211],v[80:83]\nM v[80:83],a[152:155],v[212:215],v[80:83]\nM v[84:87],a[140:143],v[208:211],v[84:87]\nbuffer_load_dword v248,v228,s[52:55],s80 offen sc1\nM v[84:87],a[156:159],v[212:215],v[84:87]\nds_read_b64_tr_b16 v[208:209],v241 offset:1536\ns_waitcnt lgkmcnt(6)\n" \
"M v[24:27],a[128:131],v[216:219],v[24:27]\nbuffer_store_dwordx4 v[172:175],v240,s[52:55],s74 offen offset:3072 sc1\nM v[24:27],a[144:147],v[220:223],v[24:27]\nds_read_b64_tr_b16 v[210:211],v241 offset:1792\nds_read_b64_tr_b16 v[212:213],v241 offset:9728\nM v[28:31],a[132:135],v[216:219],v[28:31]\nds_read_b64_tr_b16 v[214:215],v241 offset:9984\ns_add_u32 s74,s74,4096\n" \
"M v[28:31],a[148:151],v[220:223],v[28:31]\nds_write_b64 v250,v[164:165] offset:9216\nM v[88:91],a[136:139],v[216:219],v[88:91]\nM v[88:91],a[152:155],v[220:223],v[88:91]\nM v[92:95],a[140:143],v[216:219],v[92:95]\nds_read_b64_tr_b16 v[216:217],v242 offset:1536\nds_read_b64_tr_b16 v[218:219],v242 offset:1792\nM v[92:95],a[156:159],v[220:223],v[92:95]\n" \
"ds_read_b64_tr_b16 v[220:221],v242 offset:9728\nds_read_b64_tr_b16 v[222:223],v242 offset:9984\nM v[32:35],a[128:131],v[192:195],v[32:35]\nM v[32:35],a[144:147],v[196:199],v[32:35]\nM v[36:39],a[132:135],v[192:195],v[36:39]\nds_write_b64 v250,v[166:167] offset:9224\nM v[36:39],a[148:151],v[196:199],v[36:39]\nM v[96:99],a[136:139],v[192:195],v[96:99]\n" \
"M v[96:99],a[152:155],v[196:199],v[96:99]\nM v[100:103],a[140:143],v[192:195],v[100:103]\nM v[100:103],a[156:159],v[196:199],v[100:103]\ns_waitcnt lgkmcnt(9)\nM v[40:43],a[128:131],v[200:203],v[40:43]\nbuffer_store_dwordx4 v[176:179],v240,s[52:55],s74 offen offset:0 sc1\nM v[40:43],a[144:147],v[204:207],v[40:43]\nM v[44:47],a[132:135],v[200:203],v[44:47]\n" \
"M v[44:47],a[148:151],v[204:207],v[44:47]\nM v[104:107],a[136:139],v[200:203],v[104:107]\nM v[104:107],a[152:155],v[204:207],v[104:107]\nM v[108:111],a[140:143],v[200:203],v[108:111]\nds_write_b64 v250,v[168:169] offset:10240\nM v[108:111],a[156:159],v[204:207],v[108:111]\ns_waitcnt lgkmcnt(4)\nM v[48:51],a[128:131],v[208:211],v[48:51]\n" \
"M v[48:51],a[144:147],v[212:215],v[48:51]\nM v[52:55],a[132:135],v[208:211],v[52:55]\ns_sub_u32 s40,s12,0x1000\ns_subb_u32 s41,s13,0\nM v[52:55],a[148:151],v[212:215],v[52:55]\ns_add_u32 m0,s64,98304\ns_xor_b32 s69,s99,0\nM v[112:115],a[136:139],v[208:211],v[112:115]\n" \
"s_waitcnt vmcnt(18) lgkmcnt(1)\ns_barrier\nM v[112:115],a[152:155],v[212:215],v[112:115]\nds_read_b64_tr_b16 v[192:193],v241 offset:16384\nds_read_b64_tr_b16 v[194:195],v241 offset:16640\nM v[116:119],a[140:143],v[208:211],v[116:119]\nds_read_b64_tr_b16 v[196:197],v241 offset:24576\nds_read_b64_tr_b16 v[198:199],v241 offset:24832\n" \
"M v[116:119],a[156:159],v[212:215],v[116:119]\nds_read_b64_tr_b16 v[200:201],v242 offset:16384\nds_read_b64_tr_b16 v[202:203],v242 offset:16640\nM v[56:59],a[128:131],v[216:219],v[56:59]\nds_read_b64_tr_b16 v[204:205],v242 offset:24576\nds_read_b64_tr_b16 v[206:207],v242 offset:24832\nM v[56:59],a[144:147],v[220:223],v[56:59]\nds_read_b64_tr_b16 v[208:209],v241 offset:16896\n" \
"ds_read_b64_tr_b16 v[210:211],v241 offset:17152\nM v[60:63],a[132:135],v[216:219],v[60:63]\nbuffer_store_dwordx4 v[180:183],v240,s[52:55],s74 offen offset:1024 sc1\nM v[60:63],a[148:151],v[220:223],v[60:63]\nds_read_b64_tr_b16 v[212:213],v241 offset:25088\nds_read_b64_tr_b16 v[214:215],v241 offset:25344\nM v[120:123],a[136:139],v[216:219],v[120:123]\nds_write_b64 v250,v[170:171] offset:10248\n" \
"s_lshl_b32 s12,s61,13\nM v[120:123],a[152:155],v[220:223],v[120:123]\nM v[124:127],a[140:143],v[216:219],v[124:127]\nds_read_b64_tr_b16 v[216:217],v242 offset:16896\nds_read_b64_tr_b16 v[218:219],v242 offset:17152\nM v[124:127],a[156:159],v[220:223],v[124:127]\nds_read_b64_tr_b16 v[220:221],v242 offset:25088\ns_waitcnt lgkmcnt(14)\n" \
"M v[0:3],a[160:163],v[192:195],v[0:3]\nds_read_b64_tr_b16 v[222:223],v242 offset:25344\ns_waitcnt lgkmcnt(13)\nM v[0:3],a[176:179],v[196:199],v[0:3]\nbuffer_load_dwordx4 v243,s[40:43],s69 offen offset:0 lds\nM v[4:7],a[164:167],v[192:195],v[4:7]\nM v[4:7],a[180:183],v[196:199],v[4:7]\nM v[64:67],a[168:171],v[192:195],v[64:67]\n" \
"M v[64:67],a[184:187],v[196:199],v[64:67]\nbuffer_load_dwordx4 v244,s[40:43],s69 offen offset:1024 lds\nM v[68:71],a[172:175],v[192:195],v[68:71]\nds_read_b64_tr_b16 v[192:193],v241 offset:17408\nds_read_b64_tr_b16 v[194:195],v241 offset:17664\nM v[68:71],a[188:191],v[196:199],v[68:71]\nds_read_b64_tr_b16 v[196:197],v241 offset:25600\ns_waitcnt lgkmcnt(9)\n" \
"M v[8:11],a[160:163],v[200:203],v[8:11]\nds_write_b64 v250,v[172:173] offset:11264\nds_read_b64_tr_b16 v[198:199],v241 offset:25856\nM v[8:11],a[176:179],v[204:207],v[8:11]\nbuffer_load_dwordx4 v245,s[40:43],s69 offen offset:2048 lds\nM v[12:15],a[164:167],v[200:203],v[12:15]\nM v[12:15],a[180:183],v[204:207],v[12:15]\nM v[72:75],a[168:171],v[200:203],v[72:75]\n" \
"M v[72:75],a[184:187],v[204:207],v[72:75]\nbuffer_store_dwordx4 v[184:187],v240,s[52:55],s74 offen offset:2048 sc1\nM v[76:79],a[172:175],v[200:203],v[76:79]\ns_waitcnt vmcnt(7)\nv_cmp_eq_u32 vcc,0,v248\nM v[76:79],a[188:191],v[204:207],v[76:79]\ns_cbranch_vccnz .Lps0\n.Lpb0:\n" \
"ds_read_b64_tr_b16 v[200:201],v242 offset:17408\nM v[16:19],a[160:163],v[208:211],v[16:19]\nds_read_b64_tr_b16 v[202:203],v242 offset:17664\ns_waitcnt lgkmcnt(7)\nM v[16:19],a[176:179],v[212:215],v[16:19]\nds_read_b64_tr_b16 v[204:205],v242 offset:25600\nds_read_b64_tr_b16 v[206:207],v242 offset:25856\nM v[20:23],a[164:167],v[208:211],v[20:23]\n" \
"buffer_load_dwordx4 a[0:3],v240,s[52:55],s76 offen offset:0 sc1\nM v[20:23],a[180:183],v[212:215],v[20:23]\nds_write_b64 v250,v[174:175] offset:11272\nv_and_b32 v228,31,v249\nM v[80:83],a[168:171],v[208:211],v[80:83]\ns_lshl_b32 s80,s63,7\nv_lshlrev_b32 v228,2,v228\nM v[80:83],a[184:187],v[212:215],v[80:83]\n" \
"s_add_u32 s80,s80,81920\nv_mov_b32 v251,v228\nM v[84:87],a[172:175],v[208:211],v[84:87]\nds_read_b64_tr_b16 v[208:209],v241 offset:17920\nds_read_b64_tr_b16 v[210:211],v241 offset:18176\nM v[84:87],a[188:191],v[212:215],v[84:87]\nds_read_b64_tr_b16 v[212:213],v241 offset:26112\nds_read_b64_tr_b16 v[214:215],v241 offset:26368\n" \
"M v[24:27],a[160:163],v[216:219],v[24:27]\nbuffer_load_dwordx4 a[16:19],v240,s[52:55],s76 offen offset:2048 sc1\nM v[24:27],a[176:179],v[220:223],v[24:27]\nM v[28:31],a[164:167],v[216:219],v[28:31]\nM v[28:31],a[180:183],v[220:223],v[28:31]\nM v[88:91],a[168:171],v[216:219],v[88:91]\nbuffer_load_dwordx4 a[4:7],v240,s[52:55],s76 offen offset:1024 sc1\nM v[88:91],a[184:187],v[220:223],v[88:91]\n" \
"M v[92:95],a[172:175],v[216:219],v[92:95]\nds_read_b64_tr_b16 v[216:217],v242 offset:17920\nds_read_b64_tr_b16 v[218:219],v242 offset:18176\nM v[92:95],a[188:191],v[220:223],v[92:95]\nds_read_b64_tr_b16 v[220:221],v242 offset:26112\ns_waitcnt lgkmcnt(7)\nM v[32:35],a[160:163],v[192:195],v[32:35]\nbuffer_store_dwordx4 v[188:191],v240,s[52:55],s74 offen offset:3072 sc1\n" \
"M v[32:35],a[176:179],v[196:199],v[32:35]\nds_read_b64_tr_b16 v[222:223],v242 offset:26368\ns_add_u32 s74,s74,16764928\nM v[36:39],a[164:167],v[192:195],v[36:39]\nds_write_b64 v250,v[176:177] offset:12288\nM v[36:39],a[180:183],v[196:199],v[36:39]\nM v[96:99],a[168:171],v[192:195],v[96:99]\nbuffer_load_dwordx4 a[20:23],v240,s[52:55],s76 offen offset:3072 sc1\n" \
"M v[96:99],a[184:187],v[196:199],v[96:99]\ns_xor_b32 s76,s76,4096\nM v[100:103],a[172:175],v[192:195],v[100:103]\nM v[100:103],a[188:191],v[196:199],v[100:103]\nM v[40:43],a[160:163],v[200:203],v[40:43]\nbuffer_load_dwordx4 a[8:11],v240,s[52:55],s77 offen offset:0 sc1\nM v[40:43],a[176:179],v[204:207],v[40:43]\nM v[44:47],a[164:167],v[200:203],v[44:47]\n" \
"ds_write_b64 v250,v[178:179] offset:12296\nM v[44:47],a[180:183],v[204:207],v[44:47]\nM v[104:107],a[168:171],v[200:203],v[104:107]\nbuffer_load_dwordx4 a[24:27],v240,s[52:55],s77 offen offset:2048 sc1\nM v[104:107],a[184:187],v[204:207],v[104:107]\nM v[108:111],a[172:175],v[200:203],v[108:111]\nM v[108:111],a[188:191],v[204:207],v[108:111]\ns_waitcnt lgkmcnt(3)\n" \
"M v[48:51],a[160:163],v[208:211],v[48:51]\nbuffer_load_dwordx4 a[12:15],v240,s[52:55],s77 offen offset:1024 sc1\nM v[48:51],a[176:179],v[212:215],v[48:51]\nM v[52:55],a[164:167],v[208:211],v[52:55]\nM v[52:55],a[180:183],v[212:215],v[52:55]\nM v[112:115],a[168:171],v[208:211],v[112:115]\ns_waitcnt lgkmcnt(1)\ns_barrier\n" \
"M v[112:115],a[184:187],v[212:215],v[112:115]\nds_read_b64_tr_b16 v[192:193],v241 offset:32768\nds_read_b64_tr_b16 v[194:195],v241 offset:33024\nM v[116:119],a[172:175],v[208:211],v[116:119]\nds_read_b64_tr_b16 v[196:197],v241 offset:40960\nds_read_b64_tr_b16 v[198:199],v241 offset:41216\nM v[116:119],a[188:191],v[212:215],v[116:119]\nds_read_b64_tr_b16 v[200:201],v242 offset:32768\n" \
"ds_read_b64_tr_b16 v[202:203],v242 offset:33024\nM v[56:59],a[160:163],v[216:219],v[56:59]\nds_read_b64_tr_b16 v[204:205],v242 offset:40960\nds_read_b64_tr_b16 v[206:207],v242 offset:41216\nM v[56:59],a[176:179],v[220:223],v[56:59]\nds_read_b64_tr_b16 v[208:209],v241 offset:33280\nds_read_b64_tr_b16 v[210:211],v241 offset:33536\nM v[60:63],a[164:167],v[216:219],v[60:63]\n" \
"ds_read_b64_tr_b16 v[212:213],v241 offset:41472\nds_read_b64_tr_b16 v[214:215],v241 offset:41728\nM v[60:63],a[180:183],v[220:223],v[60:63]\nbuffer_load_dwordx4 a[28:31],v240,s[52:55],s77 offen offset:3072 sc1\nM v[120:123],a[168:171],v[216:219],v[120:123]\nds_write_b64 v250,v[180:181] offset:13312\ns_xor_b32 s77,s77,4096\nM v[120:123],a[184:187],v[220:223],v[120:123]\n" \
"M v[124:127],a[172:175],v[216:219],v[124:127]\nds_read_b64_tr_b16 v[216:217],v242 offset:33280\nds_read_b64_tr_b16 v[218:219],v242 offset:33536\nM v[124:127],a[188:191],v[220:223],v[124:127]\nds_read_b64_tr_b16 v[220:221],v242 offset:41472\ns_waitcnt lgkmcnt(14)\nM v[0:3],a[192:195],v[192:195],v[0:3]\nds_read_b64_tr_b16 v[222:223],v242 offset:41728\n" \
"s_waitcnt lgkmcnt(13)\nM v[0:3],a[208:211],v[196:199],v[0:3]\nbuffer_load_dwordx4 v246,s[40:43],s69 offen offset:3072 lds\nM v[4:7],a[196:199],v[192:195],v[4:7]\ns_add_u32 m0,s64,114688\ns_xor_b32 s69,s99,524288\nM v[4:7],a[212:215],v[196:199],v[4:7]\nds_write_b64 v250,v[182:183] offset:13320\n" \
"M v[64:67],a[200:203],v[192:195],v[64:67]\nM v[64:67],a[216:219],v[196:199],v[64:67]\nbuffer_load_dwordx4 a[32:35],v240,s[52:55],s76 offen offset:0 sc1\nM v[68:71],a[204:207],v[192:195],v[68:71]\nds_read_b64_tr_b16 v[192:193],v241 offset:33792\nds_read_b64_tr_b16 v[194:195],v241 offset:34048\nM v[68:71],a[220:223],v[196:199],v[68:71]\nds_read_b64_tr_b16 v[196:197],v241 offset:41984\n" \
"s_waitcnt lgkmcnt(9)\nM v[8:11],a[192:195],v[200:203],v[8:11]\nds_read_b64_tr_b16 v[198:199],v241 offset:42240\nM v[8:11],a[208:211],v[204:207],v[8:11]\nbuffer_load_dwordx4 a[48:51],v240,s[52:55],s76 offen offset:2048 sc1\nM v[12:15],a[196:199],v[200:203],v[12:15]\nM v[12:15],a[212:215],v[204:207],v[12:15]\nM v[72:75],a[200:203],v[200:203],v[72:75]\n" \
"M v[72:75],a[216:219],v[204:207],v[72:75]\nbuffer_load_dwordx4 a[36:39],v240,s[52:55],s76 offen offset:1024 sc1\nM v[76:79],a[204:207],v[200:203],v[76:79]\nds_read_b64_tr_b16 v[200:201],v242 offset:33792\nds_read_b64_tr_b16 v[202:203],v242 offset:34048\nM v[76:79],a[220:223],v[204:207],v[76:79]\nds_read_b64_tr_b16 v[204:205],v242 offset:41984\nds_read_b64_tr_b16 v[206:207],v242 offset:42240\n" \
"M v[16:19],a[192:195],v[208:211],v[16:19]\nds_write_b64 v250,v[184:185] offset:14336\nM v[16:19],a[208:211],v[212:215],v[16:19]\nbuffer_load_dwordx4 a[52:55],v240,s[52:55],s76 offen offset:3072 sc1\nM v[20:23],a[196:199],v[208:211],v[20:23]\ns_xor_b32 s76,s76,12288\nM v[20:23],a[212:215],v[212:215],v[20:23]\nM v[80:83],a[200:203],v[208:211],v[80:83]\n" \
"M v[80:83],a[216:219],v[212:215],v[80:83]\nbuffer_load_dwordx4 a[40:43],v240,s[52:55],s77 offen offset:0 sc1\nM v[84:87],a[204:207],v[208:211],v[84:87]\nds_read_b64_tr_b16 v[208:209],v241 offset:34304\nds_read_b64_tr_b16 v[210:211],v241 offset:34560\nM v[84:87],a[220:223],v[212:215],v[84:87]\nds_read_b64_tr_b16 v[212:213],v241 offset:42496\ns_waitcnt lgkmcnt(8)\n" \
"M v[24:27],a[192:195],v[216:219],v[24:27]\nds_read_b64_tr_b16 v[214:215],v241 offset:42752\nM v[24:27],a[208:211],v[220:223],v[24:27]\nbuffer_load_dwordx4 a[56:59],v240,s[52:55],s77 offen offset:2048 sc1\nM v[28:31],a[196:199],v[216:219],v[28:31]\nds_write_b64 v250,v[186:187] offset:14344\nM v[28:31],a[212:215],v[220:223],v[28:31]\nM v[88:91],a[200:203],v[216:219],v[88:91]\n" \
"M v[88:91],a[216:219],v[220:223],v[88:91]\nbuffer_load_dwordx4 a[44:47],v240,s[52:55],s77 offen offset:1024 sc1\nM v[92:95],a[204:207],v[216:219],v[92:95]\nds_read_b64_tr_b16 v[216:217],v242 offset:34304\nds_read_b64_tr_b16 v[218:219],v242 offset:34560\nM v[92:95],a[220:223],v[220:223],v[92:95]\nds_read_b64_tr_b16 v[220:221],v242 offset:42496\nds_read_b64_tr_b16 v[222:223],v242 offset:42752\n" \
"M v[32:35],a[192:195],v[192:195],v[32:35]\nM v[32:35],a[208:211],v[196:199],v[32:35]\nbuffer_load_dwordx4 a[60:63],v240,s[52:55],s77 offen offset:3072 sc1\nM v[36:39],a[196:199],v[192:195],v[36:39]\ns_xor_b32 s77,s77,12288\nM v[36:39],a[212:215],v[196:199],v[36:39]\nM v[96:99],a[200:203],v[192:195],v[96:99]\nM v[96:99],a[216:219],v[196:199],v[96:99]\n" \
"buffer_load_dwordx4 v243,s[40:43],s69 offen offset:0 lds\nM v[100:103],a[204:207],v[192:195],v[100:103]\nds_write_b64 v250,v[188:189] offset:15360\nM v[100:103],a[220:223],v[196:199],v[100:103]\ns_waitcnt lgkmcnt(6)\nM v[40:43],a[192:195],v[200:203],v[40:43]\nM v[40:43],a[208:211],v[204:207],v[40:43]\nbuffer_load_dwordx4 v244,s[40:43],s69 offen offset:1024 lds\n" \
"M v[44:47],a[196:199],v[200:203],v[44:47]\nM v[44:47],a[212:215],v[204:207],v[44:47]\nM v[104:107],a[200:203],v[200:203],v[104:107]\nM v[104:107],a[216:219],v[204:207],v[104:107]\nbuffer_load_dwordx4 v245,s[40:43],s69 offen offset:2048 lds\nM v[108:111],a[204:207],v[200:203],v[108:111]\nM v[108:111],a[220:223],v[204:207],v[108:111]\nM v[48:51],a[192:195],v[208:211],v[48:51]\n" \
"M v[48:51],a[208:211],v[212:215],v[48:51]\nbuffer_load_dwordx4 v246,s[40:43],s69 offen offset:3072 lds\nM v[52:55],a[196:199],v[208:211],v[52:55]\ns_add_u32 m0,s64,131072\ns_xor_b32 s69,s99,1048576\nM v[52:55],a[212:215],v[212:215],v[52:55]\nds_write_b64 v250,v[190:191] offset:15368\nv_add_u32 v250,65536,v250\n" \
"M v[112:115],a[200:203],v[208:211],v[112:115]\ns_waitcnt lgkmcnt(2)\ns_barrier\nM v[112:115],a[216:219],v[212:215],v[112:115]\nds_read_b64_tr_b16 v[192:193],v241 offset:49152\nds_read_b64_tr_b16 v[194:195],v241 offset:49408\nM v[116:119],a[204:207],v[208:211],v[116:119]\nds_read_b64_tr_b16 v[196:197],v241 offset:57344\n" \
"ds_read_b64_tr_b16 v[198:199],v241 offset:57600\nM v[116:119],a[220:223],v[212:215],v[116:119]\nds_read_b64_tr_b16 v[200:201],v242 offset:49152\nds_read_b64_tr_b16 v[202:203],v242 offset:49408\nM v[56:59],a[192:195],v[216:219],v[56:59]\nds_read_b64_tr_b16 v[204:205],v242 offset:57344\nds_read_b64_tr_b16 v[206:207],v242 offset:57600\nM v[56:59],a[208:211],v[220:223],v[56:59]\n" \
"ds_read_b64_tr_b16 v[208:209],v241 offset:49664\nds_read_b64_tr_b16 v[210:211],v241 offset:49920\nM v[60:63],a[196:199],v[216:219],v[60:63]\nds_read_b64_tr_b16 v[212:213],v241 offset:57856\nds_read_b64_tr_b16 v[214:215],v241 offset:58112\nM v[60:63],a[212:215],v[220:223],v[60:63]\nbuffer_load_dwordx4 a[64:67],v240,s[52:55],s76 offen offset:0 sc1\nM v[120:123],a[200:203],v[216:219],v[120:123]\n" \
"M v[120:123],a[216:219],v[220:223],v[120:123]\nM v[124:127],a[204:207],v[216:219],v[124:127]\nds_read_b64_tr_b16 v[216:217],v242 offset:49664\nds_read_b64_tr_b16 v[218:219],v242 offset:49920\nM v[124:127],a[220:223],v[220:223],v[124:127]\nds_read_b64_tr_b16 v[220:221],v242 offset:57856\ns_waitcnt lgkmcnt(13)\nM v[0:3],a[224:227],v[192:195],v[0:3]\n" \
"ds_read_b64_tr_b16 v[222:223],v242 offset:58112\ns_waitcnt lgkmcnt(12)\nM v[0:3],a[240:243],v[196:199],v[0:3]\nbuffer_load_dwordx4 a[68:71],v240,s[52:55],s76 offen offset:1024 sc1\nM v[4:7],a[228:231],v[192:195],v[4:7]\nM v[4:7],a[244:247],v[196:199],v[4:7]\nM v[64:67],a[232:235],v[192:195],v[64:67]\nM v[64:67],a[248:251],v[196:199],v[64:67]\n" \
"buffer_load_dwordx4 a[80:83],v240,s[52:55],s76 offen offset:2048 sc1\nM v[68:71],a[236:239],v[192:195],v[68:71]\nds_read_b64_tr_b16 v[192:193],v241 offset:50176\nds_read_b64_tr_b16 v[194:195],v241 offset:50432\nM v[68:71],a[252:255],v[196:199],v[68:71]\nds_read_b64_tr_b16 v[196:197],v241 offset:58368\ns_waitcnt lgkmcnt(7)\nM v[8:11],a[224:227],v[200:203],v[8:11]\n" \
"ds_read_b64_tr_b16 v[198:199],v241 offset:58624\nv_cvt_pk_bf16_f32 v128,v0,v1\nM v[8:11],a[240:243],v[204:207],v[8:11]\nv_cvt_pk_bf16_f32 v129,v2,v3\nv_cvt_pk_bf16_f32 v130,v4,v5\nM v[12:15],a[228:231],v[200:203],v[12:15]\nv_cvt_pk_bf16_f32 v131,v6,v7\nv_cvt_pk_bf16_f32 v160,v64,v65\n" \
"M v[12:15],a[244:247],v[204:207],v[12:15]\nv_cvt_pk_bf16_f32 v161,v66,v67\nv_cvt_pk_bf16_f32 v162,v68,v69\nM v[72:75],a[232:235],v[200:203],v[72:75]\nv_cvt_pk_bf16_f32 v163,v70,v71\nv_cvt_pk_bf16_f32 v132,v8,v9\nM v[72:75],a[248:251],v[204:207],v[72:75]\nbuffer_load_dwordx4 a[84:87],v240,s[52:55],s76 offen offset:3072 sc1\n" \
"M v[76:79],a[236:239],v[200:203],v[76:79]\nds_read_b64_tr_b16 v[200:201],v242 offset:50176\nds_read_b64_tr_b16 v[202:203],v242 offset:50432\nM v[76:79],a[252:255],v[204:207],v[76:79]\nds_read_b64_tr_b16 v[204:205],v242 offset:58368\nds_read_b64_tr_b16 v[206:207],v242 offset:58624\nM v[16:19],a[224:227],v[208:211],v[16:19]\nv_cvt_pk_bf16_f32 v133,v10,v11\n" \
"v_cvt_pk_bf16_f32 v134,v12,v13\nM v[16:19],a[240:243],v[212:215],v[16:19]\nv_cvt_pk_bf16_f32 v135,v14,v15\nv_cvt_pk_bf16_f32 v164,v72,v73\nM v[20:23],a[228:231],v[208:211],v[20:23]\nv_cvt_pk_bf16_f32 v165,v74,v75\nv_cvt_pk_bf16_f32 v166,v76,v77\nM v[20:23],a[244:247],v[212:215],v[20:23]\n" \
"v_cvt_pk_bf16_f32 v167,v78,v79\ns_xor_b32 s76,s76,4096\nM v[80:83],a[232:235],v[208:211],v[80:83]\nv_cvt_pk_bf16_f32 v136,v16,v17\nv_cvt_pk_bf16_f32 v137,v18,v19\nM v[80:83],a[248:251],v[212:215],v[80:83]\nbuffer_load_dwordx4 a[72:75],v240,s[52:55],s77 offen offset:0 sc1\nM v[84:87],a[236:239],v[208:211],v[84:87]\n" \
"ds_read_b64_tr_b16 v[208:209],v241 offset:50688\nds_read_b64_tr_b16 v[210:211],v241 offset:50944\nM v[84:87],a[252:255],v[212:215],v[84:87]\nds_read_b64_tr_b16 v[212:213],v241 offset:58880\ns_waitcnt lgkmcnt(7)\nM v[24:27],a[224:227],v[216:219],v[24:27]\nds_read_b64_tr_b16 v[214:215],v241 offset:59136\nv_cvt_pk_bf16_f32 v138,v20,v21\n" \
"M v[24:27],a[240:243],v[220:223],v[24:27]\nv_cvt_pk_bf16_f32 v139,v22,v23\nv_cvt_pk_bf16_f32 v168,v80,v81\nM v[28:31],a[228:231],v[216:219],v[28:31]\nv_cvt_pk_bf16_f32 v169,v82,v83\nv_cvt_pk_bf16_f32 v170,v84,v85\nM v[28:31],a[244:247],v[220:223],v[28:31]\nbuffer_load_dwordx4 a[88:91],v240,s[52:55],s77 offen offset:2048 sc1\n" \
"M v[88:91],a[232:235],v[216:219],v[88:91]\nv_cvt_pk_bf16_f32 v171,v86,v87\nv_cvt_pk_bf16_f32 v140,v24,v25\nM v[88:91],a[248:251],v[220:223],v[88:91]\nv_cvt_pk_bf16_f32 v141,v26,v27\nM v[92:95],a[236:239],v[216:219],v[92:95]\nds_read_b64_tr_b16 v[216:217],v242 offset:50688\nds_read_b64_tr_b16 v[218:219],v242 offset:50944\n" \
"M v[92:95],a[252:255],v[220:223],v[92:95]\nds_read_b64_tr_b16 v[220:221],v242 offset:58880\nds_read_b64_tr_b16 v[222:223],v242 offset:59136\nM v[32:35],a[224:227],v[192:195],v[32:35]\nv_cvt_pk_bf16_f32 v142,v28,v29\nv_cvt_pk_bf16_f32 v143,v30,v31\nM v[32:35],a[240:243],v[196:199],v[32:35]\nv_cvt_pk_bf16_f32 v172,v88,v89\n" \
"v_cvt_pk_bf16_f32 v173,v90,v91\nM v[36:39],a[228:231],v[192:195],v[36:39]\nv_cvt_pk_bf16_f32 v174,v92,v93\nv_cvt_pk_bf16_f32 v175,v94,v95\nM v[36:39],a[244:247],v[196:199],v[36:39]\nbuffer_load_dwordx4 a[76:79],v240,s[52:55],s77 offen offset:1024 sc1\nM v[96:99],a[232:235],v[192:195],v[96:99]\nv_cvt_pk_bf16_f32 v144,v32,v33\n" \
"v_cvt_pk_bf16_f32 v145,v34,v35\nM v[96:99],a[248:251],v[196:199],v[96:99]\nM v[100:103],a[236:239],v[192:195],v[100:103]\nM v[100:103],a[252:255],v[196:199],v[100:103]\ns_waitcnt vmcnt(20)\nbuffer_store_dword v252,off,s[52:55],s29 sc1\ns_waitcnt lgkmcnt(4)\nM v[40:43],a[224:227],v[200:203],v[40:43]\n" \
"v_cvt_pk_bf16_f32 v146,v36,v37\nv_cvt_pk_bf16_f32 v147,v38,v39\nM v[40:43],a[240:243],v[204:207],v[40:43]\nv_cvt_pk_bf16_f32 v176,v96,v97\nv_cvt_pk_bf16_f32 v177,v98,v99\nM v[44:47],a[228:231],v[200:203],v[44:47]\nv_cvt_pk_bf16_f32 v178,v100,v101\nv_cvt_pk_bf16_f32 v179,v102,v103\n" \
"M v[44:47],a[244:247],v[204:207],v[44:47]\nbuffer_load_dwordx4 a[92:95],v240,s[52:55],s77 offen offset:3072 sc1\nM v[104:107],a[232:235],v[200:203],v[104:107]\nv_cvt_pk_bf16_f32 v148,v40,v41\nv_cvt_pk_bf16_f32 v149,v42,v43\nM v[104:107],a[248:251],v[204:207],v[104:107]\ns_xor_b32 s77,s77,4096\ns_add_u32 s29,s29,4096\n" \
"M v[108:111],a[236:239],v[200:203],v[108:111]\nv_cvt_pk_bf16_f32 v150,v44,v45\nv_cvt_pk_bf16_f32 v151,v46,v47\nM v[108:111],a[252:255],v[204:207],v[108:111]\nbuffer_load_dwordx4 v243,s[40:43],s69 offen offset:0 lds\nM v[48:51],a[224:227],v[208:211],v[48:51]\nv_cvt_pk_bf16_f32 v180,v104,v105\nv_cvt_pk_bf16_f32 v181,v106,v107\n" \
"M v[48:51],a[240:243],v[212:215],v[48:51]\nM v[52:55],a[228:231],v[208:211],v[52:55]\nM v[52:55],a[244:247],v[212:215],v[52:55]\nbuffer_load_dwordx4 v244,s[40:43],s69 offen offset:1024 lds\nM v[112:115],a[232:235],v[208:211],v[112:115]\ns_waitcnt vmcnt(15) lgkmcnt(0)\ns_barrier\nM v[112:115],a[248:251],v[212:215],v[112:115]\n" \
"ds_read_b64_tr_b16 v[192:193],v241 offset:0\nds_read_b64_tr_b16 v[194:195],v241 offset:256\nM v[116:119],a[236:239],v[208:211],v[116:119]\nds_read_b64_tr_b16 v[196:197],v241 offset:8192\nds_read_b64_tr_b16 v[198:199],v241 offset:8448\nM v[116:119],a[252:255],v[212:215],v[116:119]\nds_read_b64_tr_b16 v[200:201],v242 offset:0\nds_read_b64_tr_b16 v[202:203],v242 offset:256\n" \
"M v[56:59],a[224:227],v[216:219],v[56:59]\nds_read_b64_tr_b16 v[204:205],v242 offset:8192\nds_read_b64_tr_b16 v[206:207],v242 offset:8448\nM v[56:59],a[240:243],v[220:223],v[56:59]\nds_read_b64_tr_b16 v[208:209],v241 offset:512\nds_read_b64_tr_b16 v[210:211],v241 offset:768\nM v[60:63],a[228:231],v[216:219],v[60:63]\nds_read_b64_tr_b16 v[212:213],v241 offset:8704\n" \
"ds_read_b64_tr_b16 v[214:215],v241 offset:8960\nM v[60:63],a[244:247],v[220:223],v[60:63]\nv_cvt_pk_bf16_f32 v182,v108,v109\nv_cvt_pk_bf16_f32 v183,v110,v111\nM v[120:123],a[232:235],v[216:219],v[120:123]\nv_cvt_pk_bf16_f32 v152,v48,v49\nv_cvt_pk_bf16_f32 v153,v50,v51\nM v[120:123],a[248:251],v[220:223],v[120:123]\n" \
"v_cvt_pk_bf16_f32 v154,v52,v53\nv_cvt_pk_bf16_f32 v155,v54,v55\nM v[124:127],a[236:239],v[216:219],v[124:127]\nds_read_b64_tr_b16 v[216:217],v242 offset:512\nds_read_b64_tr_b16 v[218:219],v242 offset:768\nM v[124:127],a[252:255],v[220:223],v[124:127]\nds_read_b64_tr_b16 v[220:221],v242 offset:8704\ns_waitcnt lgkmcnt(13)\n" \
"M v[0:3],v[192:195],a[0:3],0\nds_read_b64_tr_b16 v[222:223],v242 offset:8960\ns_waitcnt lgkmcnt(12)\nM v[0:3],v[196:199],a[16:19],v[0:3]\nv_cvt_pk_bf16_f32 v184,v112,v113\nv_cvt_pk_bf16_f32 v185,v114,v115\nM v[4:7],v[192:195],a[4:7],0\nv_cvt_pk_bf16_f32 v186,v116,v117\n" \
"v_cvt_pk_bf16_f32 v187,v118,v119\nM v[4:7],v[196:199],a[20:23],v[4:7]\nv_cvt_pk_bf16_f32 v156,v56,v57\nv_cvt_pk_bf16_f32 v157,v58,v59\nM v[64:67],v[192:195],a[8:11],0\nv_cvt_pk_bf16_f32 v158,v60,v61\nv_cvt_pk_bf16_f32 v159,v62,v63\nM v[64:67],v[196:199],a[24:27],v[64:67]\n" \
"v_cvt_pk_bf16_f32 v188,v120,v121\nv_cvt_pk_bf16_f32 v189,v122,v123\nM v[68:71],v[192:195],a[12:15],0\nds_read_b64_tr_b16 v[192:193],v241 offset:1024\nds_read_b64_tr_b16 v[194:195],v241 offset:1280\nM v[68:71],v[196:199],a[28:31],v[68:71]\nds_read_b64_tr_b16 v[196:197],v241 offset:9216\ns_waitcnt lgkmcnt(7)\n" \
"M v[8:11],v[200:203],a[0:3],0\nds_read_b64_tr_b16 v[198:199],v241 offset:9472\nv_cvt_pk_bf16_f32 v190,v124,v125\nM v[8:11],v[204:207],a[16:19],v[8:11]\nbuffer_load_dwordx4 v245,s[40:43],s69 offen offset:2048 lds\nM v[12:15],v[200:203],a[4:7],0\nv_cvt_pk_bf16_f32 v191,v126,v127\nM v[12:15],v[204:207],a[20:23],v[12:15]\n" \
"M v[72:75],v[200:203],a[8:11],0\nM v[72:75],v[204:207],a[24:27],v[72:75]\nbuffer_load_dwordx4 v246,s[40:43],s69 offen offset:3072 lds\nM v[76:79],v[200:203],a[12:15],0\nds_read_b64_tr_b16 v[200:201],v242 offset:1024\nds_read_b64_tr_b16 v[202:203],v242 offset:1280\nM v[76:79],v[204:207],a[28:31],v[76:79]\nds_read_b64_tr_b16 v[204:205],v242 offset:9216\n" \
"ds_read_b64_tr_b16 v[206:207],v242 offset:9472\nM v[16:19],v[208:211],a[0:3],0\ns_add_u32 m0,s64,147456\ns_xor_b32 s69,s99,1572864\nM v[16:19],v[212:215],a[16:19],v[16:19]\nbuffer_load_dwordx4 a[96:99],v240,s[52:55],s76 offen offset:0 sc1\nM v[20:23],v[208:211],a[4:7],0\nM v[20:23],v[212:215],a[20:23],v[20:23]\n" \
"M v[80:83],v[208:211],a[8:11],0\nM v[80:83],v[212:215],a[24:27],v[80:83]\nbuffer_store_dwordx4 v[128:131],v240,s[52:55],s74 offen offset:0 sc1\nM v[84:87],v[208:211],a[12:15],0\nds_read_b64_tr_b16 v[208:209],v241 offset:1536\nds_read_b64_tr_b16 v[210:211],v241 offset:1792\nM v[84:87],v[212:215],a[28:31],v[84:87]\nds_read_b64_tr_b16 v[212:213],v241 offset:9728\n" \
"s_waitcnt lgkmcnt(7)\nM v[24:27],v[216:219],a[0:3],0\nds_read_b64_tr_b16 v[214:215],v241 offset:9984\nM v[24:27],v[220:223],a[16:19],v[24:27]\nbuffer_load_dwordx4 a[112:115],v240,s[52:55],s76 offen offset:2048 sc1\nM v[28:31],v[216:219],a[4:7],0\nM v[28:31],v[220:223],a[20:23],v[28:31]\nM v[88:91],v[216:219],a[8:11],0\n" \
"M v[88:91],v[220:223],a[24:27],v[88:91]\nbuffer_load_dwordx4 a[100:103],v240,s[52:55],s76 offen offset:1024 sc1\nM v[92:95],v[216:219],a[12:15],0\nds_read_b64_tr_b16 v[216:217],v242 offset:1536\nds_read_b64_tr_b16 v[218:219],v242 offset:1792\nM v[92:95],v[220:223],a[28:31],v[92:95]\nds_read_b64_tr_b16 v[220:221],v242 offset:9728\nds_read_b64_tr_b16 v[222:223],v242 offset:9984\n" \
"M v[32:35],v[192:195],a[0:3],0\nM v[32:35],v[196:199],a[16:19],v[32:35]\nbuffer_load_dwordx4 a[116:119],v240,s[52:55],s76 offen offset:3072 sc1\nM v[36:39],v[192:195],a[4:7],0\ns_xor_b32 s76,s76,28672\nM v[36:39],v[196:199],a[20:23],v[36:39]\nM v[96:99],v[192:195],a[8:11],0\nM v[96:99],v[196:199],a[24:27],v[96:99]\n" \
"buffer_load_dwordx4 a[104:107],v240,s[52:55],s77 offen offset:0 sc1\nM v[100:103],v[192:195],a[12:15],0\nM v[100:103],v[196:199],a[28:31],v[100:103]\ns_waitcnt lgkmcnt(4)\nM v[40:43],v[200:203],a[0:3],0\nM v[40:43],v[204:207],a[16:19],v[40:43]\nbuffer_load_dwordx4 a[120:123],v240,s[52:55],s77 offen offset:2048 sc1\nM v[44:47],v[200:203],a[4:7],0\n" \
"M v[44:47],v[204:207],a[20:23],v[44:47]\nM v[104:107],v[200:203],a[8:11],0\nM v[104:107],v[204:207],a[24:27],v[104:107]\nbuffer_load_dwordx4 a[108:111],v240,s[52:55],s77 offen offset:1024 sc1\nM v[108:111],v[200:203],a[12:15],0\nM v[108:111],v[204:207],a[28:31],v[108:111]\nM v[48:51],v[208:211],a[0:3],0\nM v[48:51],v[212:215],a[16:19],v[48:51]\n" \
"buffer_load_dwordx4 a[124:127],v240,s[52:55],s77 offen offset:3072 sc1\nM v[52:55],v[208:211],a[4:7],0\ns_xor_b32 s77,s77,28672\nM v[52:55],v[212:215],a[20:23],v[52:55]\nM v[112:115],v[208:211],a[8:11],0\ns_waitcnt vmcnt(15) lgkmcnt(0)\ns_barrier\nM v[112:115],v[212:215],a[24:27],v[112:115]\n" \
"ds_read_b64_tr_b16 v[192:193],v241 offset:16384\nds_read_b64_tr_b16 v[194:195],v241 offset:16640\nM v[116:119],v[208:211],a[12:15],0\nds_read_b64_tr_b16 v[196:197],v241 offset:24576\nds_read_b64_tr_b16 v[198:199],v241 offset:24832\nbuffer_store_dwordx4 v[132:135],v240,s[52:55],s74 offen offset:1024 sc1\nM v[116:119],v[212:215],a[28:31],v[116:119]\nds_read_b64_tr_b16 v[200:201],v242 offset:16384\n" \
"ds_read_b64_tr_b16 v[202:203],v242 offset:16640\nM v[56:59],v[216:219],a[0:3],0\nds_read_b64_tr_b16 v[204:205],v242 offset:24576\nds_read_b64_tr_b16 v[206:207],v242 offset:24832\nM v[56:59],v[220:223],a[16:19],v[56:59]\nds_read_b64_tr_b16 v[208:209],v241 offset:16896\nds_read_b64_tr_b16 v[210:211],v241 offset:17152\nM v[60:63],v[216:219],a[4:7],0\n" \
"ds_read_b64_tr_b16 v[212:213],v241 offset:25088\nds_read_b64_tr_b16 v[214:215],v241 offset:25344\nM v[60:63],v[220:223],a[20:23],v[60:63]\nbuffer_load_dwordx4 v243,s[40:43],s69 offen offset:0 lds\nM v[120:123],v[216:219],a[8:11],0\nM v[120:123],v[220:223],a[24:27],v[120:123]\nM v[124:127],v[216:219],a[12:15],0\nds_read_b64_tr_b16 v[216:217],v242 offset:16896\n" \
"ds_read_b64_tr_b16 v[218:219],v242 offset:17152\nM v[124:127],v[220:223],a[28:31],v[124:127]\nds_read_b64_tr_b16 v[220:221],v242 offset:25088\ns_waitcnt lgkmcnt(13)\nM v[0:3],v[192:195],a[32:35],v[0:3]\nds_read_b64_tr_b16 v[222:223],v242 offset:25344\ns_waitcnt lgkmcnt(12)\nM v[0:3],v[196:199],a[48:51],v[0:3]\n" \
"buffer_load_dwordx4 v244,s[40:43],s69 offen offset:1024 lds\nM v[4:7],v[192:195],a[36:39],v[4:7]\nM v[4:7],v[196:199],a[52:55],v[4:7]\nM v[64:67],v[192:195],a[40:43],v[64:67]\nM v[64:67],v[196:199],a[56:59],v[64:67]\nbuffer_load_dwordx4 v245,s[40:43],s69 offen offset:2048 lds\nM v[68:71],v[192:195],a[44:47],v[68:71]\nds_read_b64_tr_b16 v[192:193],v241 offset:17408\n" \
"ds_read_b64_tr_b16 v[194:195],v241 offset:17664\nM v[68:71],v[196:199],a[60:63],v[68:71]\nds_read_b64_tr_b16 v[196:197],v241 offset:25600\ns_waitcnt lgkmcnt(7)\nM v[8:11],v[200:203],a[32:35],v[8:11]\nds_read_b64_tr_b16 v[198:199],v241 offset:25856\nM v[8:11],v[204:207],a[48:51],v[8:11]\nbuffer_load_dwordx4 v246,s[40:43],s69 offen offset:3072 lds\n" \
"M v[12:15],v[200:203],a[36:39],v[12:15]\ns_add_u32 m0,s64,98304\ns_xor_b32 s69,s99,2097152\nM v[12:15],v[204:207],a[52:55],v[12:15]\nM v[72:75],v[200:203],a[40:43],v[72:75]\nM v[72:75],v[204:207],a[56:59],v[72:75]\nbuffer_load_dwordx4 a[128:131],v240,s[52:55],s76 offen offset:0 sc1\nM v[76:79],v[200:203],a[44:47],v[76:79]\n" \
"ds_read_b64_tr_b16 v[200:201],v242 offset:17408\nds_read_b64_tr_b16 v[202:203],v242 offset:17664\nM v[76:79],v[204:207],a[60:63],v[76:79]\nds_read_b64_tr_b16 v[204:205],v242 offset:25600\nds_read_b64_tr_b16 v[206:207],v242 offset:25856\nM v[16:19],v[208:211],a[32:35],v[16:19]\nM v[16:19],v[212:215],a[48:51],v[16:19]\nbuffer_store_dwordx4 v[136:139],v240,s[52:55],s74 offen offset:2048 sc1\n" \
"M v[20:23],v[208:211],a[36:39],v[20:23]\nM v[20:23],v[212:215],a[52:55],v[20:23]\nM v[80:83],v[208:211],a[40:43],v[80:83]\nM v[80:83],v[212:215],a[56:59],v[80:83]\nbuffer_load_dwordx4 a[144:147],v240,s[52:55],s76 offen offset:2048 sc1\nM v[84:87],v[208:211],a[44:47],v[84:87]\nds_read_b64_tr_b16 v[208:209],v241 offset:17920\nds_read_b64_tr_b16 v[210:211],v241 offset:18176\n" \
"M v[84:87],v[212:215],a[60:63],v[84:87]\nds_read_b64_tr_b16 v[212:213],v241 offset:26112\ns_waitcnt lgkmcnt(7)\nM v[24:27],v[216:219],a[32:35],v[24:27]\nds_read_b64_tr_b16 v[214:215],v241 offset:26368\nM v[24:27],v[220:223],a[48:51],v[24:27]\nbuffer_load_dwordx4 a[132:135],v240,s[52:55],s76 offen offset:1024 sc1\nM v[28:31],v[216:219],a[36:39],v[28:31]\n" \
"M v[28:31],v[220:223],a[52:55],v[28:31]\nM v[88:91],v[216:219],a[40:43],v[88:91]\nM v[88:91],v[220:223],a[56:59],v[88:91]\nbuffer_load_dwordx4 a[148:151],v240,s[52:55],s76 offen offset:3072 sc1\nM v[92:95],v[216:219],a[44:47],v[92:95]\nds_read_b64_tr_b16 v[216:217],v242 offset:17920\nds_read_b64_tr_b16 v[218:219],v242 offset:18176\nM v[92:95],v[220:223],a[60:63],v[92:95]\n" \
"ds_read_b64_tr_b16 v[220:221],v242 offset:26112\nds_read_b64_tr_b16 v[222:223],v242 offset:26368\nM v[32:35],v[192:195],a[32:35],v[32:35]\ns_xor_b32 s76,s76,4096\nM v[32:35],v[196:199],a[48:51],v[32:35]\nbuffer_load_dwordx4 a[136:139],v240,s[52:55],s77 offen offset:0 sc1\nM v[36:39],v[192:195],a[36:39],v[36:39]\nM v[36:39],v[196:199],a[52:55],v[36:39]\n" \
"M v[96:99],v[192:195],a[40:43],v[96:99]\nM v[96:99],v[196:199],a[56:59],v[96:99]\nbuffer_load_dwordx4 a[152:155],v240,s[52:55],s77 offen offset:2048 sc1\nM v[100:103],v[192:195],a[44:47],v[100:103]\nM v[100:103],v[196:199],a[60:63],v[100:103]\ns_waitcnt lgkmcnt(4)\nM v[40:43],v[200:203],a[32:35],v[40:43]\nM v[40:43],v[204:207],a[48:51],v[40:43]\n" \
"buffer_load_dwordx4 a[140:143],v240,s[52:55],s77 offen offset:1024 sc1\nM v[44:47],v[200:203],a[36:39],v[44:47]\nM v[44:47],v[204:207],a[52:55],v[44:47]\nM v[104:107],v[200:203],a[40:43],v[104:107]\nM v[104:107],v[204:207],a[56:59],v[104:107]\nbuffer_load_dwordx4 a[156:159],v240,s[52:55],s77 offen offset:3072 sc1\nM v[108:111],v[200:203],a[44:47],v[108:111]\ns_xor_b32 s77,s77,4096\n" \
"M v[108:111],v[204:207],a[60:63],v[108:111]\nM v[48:51],v[208:211],a[32:35],v[48:51]\nM v[48:51],v[212:215],a[48:51],v[48:51]\nbuffer_store_dwordx4 v[140:143],v240,s[52:55],s74 offen offset:3072 sc1\nM v[52:55],v[208:211],a[36:39],v[52:55]\ns_add_u32 s74,s74,4096\nM v[52:55],v[212:215],a[52:55],v[52:55]\nM v[112:115],v[208:211],a[40:43],v[112:115]\n" \
"s_waitcnt vmcnt(23) lgkmcnt(0)\ns_barrier\nM v[112:115],v[212:215],a[56:59],v[112:115]\nds_read_b64_tr_b16 v[192:193],v241 offset:32768\nds_read_b64_tr_b16 v[194:195],v241 offset:33024\nM v[116:119],v[208:211],a[44:47],v[116:119]\nds_read_b64_tr_b16 v[196:197],v241 offset:40960\nds_read_b64_tr_b16 v[198:199],v241 offset:41216\n" \
"M v[116:119],v[212:215],a[60:63],v[116:119]\nds_read_b64_tr_b16 v[200:201],v242 offset:32768\nds_read_b64_tr_b16 v[202:203],v242 offset:33024\nM v[56:59],v[216:219],a[32:35],v[56:59]\nds_read_b64_tr_b16 v[204:205],v242 offset:40960\nds_read_b64_tr_b16 v[206:207],v242 offset:41216\nM v[56:59],v[220:223],a[48:51],v[56:59]\nds_read_b64_tr_b16 v[208:209],v241 offset:33280\n" \
"ds_read_b64_tr_b16 v[210:211],v241 offset:33536\nM v[60:63],v[216:219],a[36:39],v[60:63]\nds_read_b64_tr_b16 v[212:213],v241 offset:41472\nds_read_b64_tr_b16 v[214:215],v241 offset:41728\nM v[60:63],v[220:223],a[52:55],v[60:63]\nbuffer_load_dwordx4 v243,s[40:43],s69 offen offset:0 lds\nM v[120:123],v[216:219],a[40:43],v[120:123]\nM v[120:123],v[220:223],a[56:59],v[120:123]\n" \
"M v[124:127],v[216:219],a[44:47],v[124:127]\nds_read_b64_tr_b16 v[216:217],v242 offset:33280\nds_read_b64_tr_b16 v[218:219],v242 offset:33536\nM v[124:127],v[220:223],a[60:63],v[124:127]\nds_read_b64_tr_b16 v[220:221],v242 offset:41472\ns_waitcnt lgkmcnt(13)\nM v[0:3],v[192:195],a[64:67],v[0:3]\nds_read_b64_tr_b16 v[222:223],v242 offset:41728\n" \
"s_waitcnt lgkmcnt(12)\nM v[0:3],v[196:199],a[80:83],v[0:3]\nbuffer_load_dwordx4 v244,s[40:43],s69 offen offset:1024 lds\nM v[4:7],v[192:195],a[68:71],v[4:7]\nM v[4:7],v[196:199],a[84:87],v[4:7]\nM v[64:67],v[192:195],a[72:75],v[64:67]\nM v[64:67],v[196:199],a[88:91],v[64:67]\nbuffer_load_dwordx4 v245,s[40:43],s69 offen offset:2048 lds\n" \
"M v[68:71],v[192:195],a[76:79],v[68:71]\nds_read_b64_tr_b16 v[192:193],v241 offset:33792\nds_read_b64_tr_b16 v[194:195],v241 offset:34048\nM v[68:71],v[196:199],a[92:95],v[68:71]\nds_read_b64_tr_b16 v[196:197],v241 offset:41984\ns_waitcnt lgkmcnt(7)\nM v[8:11],v[200:203],a[64:67],v[8:11]\nds_read_b64_tr_b16 v[198:199],v241 offset:42240\n" \
"M v[8:11],v[204:207],a[80:83],v[8:11]\nbuffer_load_dwordx4 v246,s[40:43],s69 offen offset:3072 lds\nM v[12:15],v[200:203],a[68:71],v[12:15]\ns_add_u32 m0,s64,114688\ns_xor_b32 s69,s99,2621440\nM v[12:15],v[204:207],a[84:87],v[12:15]\nM v[72:75],v[200:203],a[72:75],v[72:75]\nM v[72:75],v[204:207],a[88:91],v[72:75]\n" \
"buffer_store_dwordx4 v[144:147],v240,s[52:55],s74 offen offset:0 sc1\nM v[76:79],v[200:203],a[76:79],v[76:79]\nds_read_b64_tr_b16 v[200:201],v242 offset:33792\nds_read_b64_tr_b16 v[202:203],v242 offset:34048\nM v[76:79],v[204:207],a[92:95],v[76:79]\nds_read_b64_tr_b16 v[204:205],v242 offset:41984\nds_read_b64_tr_b16 v[206:207],v242 offset:42240\nM v[16:19],v[208:211],a[64:67],v[16:19]\n" \
"M v[16:19],v[212:215],a[80:83],v[16:19]\nbuffer_load_dwordx4 a[160:163],v240,s[52:55],s76 offen offset:0 sc1\nM v[20:23],v[208:211],a[68:71],v[20:23]\nM v[20:23],v[212:215],a[84:87],v[20:23]\nM v[80:83],v[208:211],a[72:75],v[80:83]\nM v[80:83],v[212:215],a[88:91],v[80:83]\nbuffer_load_dwordx4 a[176:179],v240,s[52:55],s76 offen offset:2048 sc1\nM v[84:87],v[208:211],a[76:79],v[84:87]\n" \
"ds_read_b64_tr_b16 v[208:209],v241 offset:34304\nds_read_b64_tr_b16 v[210:211],v241 offset:34560\nM v[84:87],v[212:215],a[92:95],v[84:87]\nds_read_b64_tr_b16 v[212:213],v241 offset:42496\ns_waitcnt lgkmcnt(7)\nM v[24:27],v[216:219],a[64:67],v[24:27]\nds_read_b64_tr_b16 v[214:215],v241 offset:42752\nM v[24:27],v[220:223],a[80:83],v[24:27]\n" \
"buffer_load_dwordx4 a[164:167],v240,s[52:55],s76 offen offset:1024 sc1\nM v[28:31],v[216:219],a[68:71],v[28:31]\nM v[28:31],v[220:223],a[84:87],v[28:31]\nM v[88:91],v[216:219],a[72:75],v[88:91]\nM v[88:91],v[220:223],a[88:91],v[88:91]\nbuffer_load_dwordx4 a[180:183],v240,s[52:55],s76 offen offset:3072 sc1\nM v[92:95],v[216:219],a[76:79],v[92:95]\nds_read_b64_tr_b16 v[216:217],v242 offset:34304\n" \
"ds_read_b64_tr_b16 v[218:219],v242 offset:34560\nM v[92:95],v[220:223],a[92:95],v[92:95]\nds_read_b64_tr_b16 v[220:221],v242 offset:42496\nds_read_b64_tr_b16 v[222:223],v242 offset:42752\nM v[32:35],v[192:195],a[64:67],v[32:35]\ns_xor_b32 s76,s76,12288\nM v[32:35],v[196:199],a[80:83],v[32:35]\nbuffer_load_dwordx4 a[168:171],v240,s[52:55],s77 offen offset:0 sc1\n" \
"M v[36:39],v[192:195],a[68:71],v[36:39]\nM v[36:39],v[196:199],a[84:87],v[36:39]\nM v[96:99],v[192:195],a[72:75],v[96:99]\nM v[96:99],v[196:199],a[88:91],v[96:99]\nbuffer_load_dwordx4 a[184:187],v240,s[52:55],s77 offen offset:2048 sc1\nM v[100:103],v[192:195],a[76:79],v[100:103]\nM v[100:103],v[196:199],a[92:95],v[100:103]\ns_waitcnt lgkmcnt(4)\n" \
"M v[40:43],v[200:203],a[64:67],v[40:43]\nM v[40:43],v[204:207],a[80:83],v[40:43]\nbuffer_load_dwordx4 a[172:175],v240,s[52:55],s77 offen offset:1024 sc1\nM v[44:47],v[200:203],a[68:71],v[44:47]\nM v[44:47],v[204:207],a[84:87],v[44:47]\nM v[104:107],v[200:203],a[72:75],v[104:107]\nM v[104:107],v[204:207],a[88:91],v[104:107]\nbuffer_store_dwordx4 v[148:151],v240,s[52:55],s74 offen offset:1024 sc1\n" \
"M v[108:111],v[200:203],a[76:79],v[108:111]\nM v[108:111],v[204:207],a[92:95],v[108:111]\nM v[48:51],v[208:211],a[64:67],v[48:51]\nM v[48:51],v[212:215],a[80:83],v[48:51]\nbuffer_load_dwordx4 a[188:191],v240,s[52:55],s77 offen offset:3072 sc1\nM v[52:55],v[208:211],a[68:71],v[52:55]\ns_xor_b32 s77,s77,12288\nM v[52:55],v[212:215],a[84:87],v[52:55]\n" \
"M v[112:115],v[208:211],a[72:75],v[112:115]\ns_waitcnt vmcnt(23) lgkmcnt(0)\ns_barrier\nM v[112:115],v[212:215],a[88:91],v[112:115]\nds_read_b64_tr_b16 v[192:193],v241 offset:49152\nds_read_b64_tr_b16 v[194:195],v241 offset:49408\nM v[116:119],v[208:211],a[76:79],v[116:119]\nds_read_b64_tr_b16 v[196:197],v241 offset:57344\n" \
"ds_read_b64_tr_b16 v[198:199],v241 offset:57600\nM v[116:119],v[212:215],a[92:95],v[116:119]\nds_read_b64_tr_b16 v[200:201],v242 offset:49152\nds_read_b64_tr_b16 v[202:203],v242 offset:49408\nM v[56:59],v[216:219],a[64:67],v[56:59]\nds_read_b64_tr_b16 v[204:205],v242 offset:57344\nds_read_b64_tr_b16 v[206:207],v242 offset:57600\nM v[56:59],v[220:223],a[80:83],v[56:59]\n" \
"ds_read_b64_tr_b16 v[208:209],v241 offset:49664\nds_read_b64_tr_b16 v[210:211],v241 offset:49920\nM v[60:63],v[216:219],a[68:71],v[60:63]\nds_read_b64_tr_b16 v[212:213],v241 offset:57856\nds_read_b64_tr_b16 v[214:215],v241 offset:58112\nM v[60:63],v[220:223],a[84:87],v[60:63]\nbuffer_load_dwordx4 v243,s[40:43],s69 offen offset:0 lds\nM v[120:123],v[216:219],a[72:75],v[120:123]\n" \
"M v[120:123],v[220:223],a[88:91],v[120:123]\nM v[124:127],v[216:219],a[76:79],v[124:127]\nds_read_b64_tr_b16 v[216:217],v242 offset:49664\nds_read_b64_tr_b16 v[218:219],v242 offset:49920\nM v[124:127],v[220:223],a[92:95],v[124:127]\nds_read_b64_tr_b16 v[220:221],v242 offset:57856\ns_waitcnt lgkmcnt(13)\nM v[0:3],v[192:195],a[96:99],v[0:3]\n" \
"ds_read_b64_tr_b16 v[222:223],v242 offset:58112\ns_waitcnt lgkmcnt(12)\nM v[0:3],v[196:199],a[112:115],v[0:3]\nbuffer_load_dwordx4 v244,s[40:43],s69 offen offset:1024 lds\nM v[4:7],v[192:195],a[100:103],v[4:7]\nM v[4:7],v[196:199],a[116:119],v[4:7]\nM v[64:67],v[192:195],a[104:107],v[64:67]\nM v[64:67],v[196:199],a[120:123],v[64:67]\n" \
"buffer_load_dwordx4 v245,s[40:43],s69 offen offset:2048 lds\nM v[68:71],v[192:195],a[108:111],v[68:71]\nds_read_b64_tr_b16 v[192:193],v241 offset:50176\nds_read_b64_tr_b16 v[194:195],v241 offset:50432\nM v[68:71],v[196:199],a[124:127],v[68:71]\nds_read_b64_tr_b16 v[196:197],v241 offset:58368\ns_waitcnt lgkmcnt(7)\nM v[8:11],v[200:203],a[96:99],v[8:11]\n" \
"ds_read_b64_tr_b16 v[198:199],v241 offset:58624\nM v[8:11],v[204:207],a[112:115],v[8:11]\nbuffer_store_dwordx4 v[152:155],v240,s[52:55],s74 offen offset:2048 sc1\nM v[12:15],v[200:203],a[100:103],v[12:15]\nM v[12:15],v[204:207],a[116:119],v[12:15]\nM v[72:75],v[200:203],a[104:107],v[72:75]\nM v[72:75],v[204:207],a[120:123],v[72:75]\nbuffer_load_dwordx4 v246,s[40:43],s69 offen offset:3072 lds\n" \
"M v[76:79],v[200:203],a[108:111],v[76:79]\nds_read_b64_tr_b16 v[200:201],v242 offset:50176\nds_read_b64_tr_b16 v[202:203],v242 offset:50432\nM v[76:79],v[204:207],a[124:127],v[76:79]\nds_read_b64_tr_b16 v[204:205],v242 offset:58368\nds_read_b64_tr_b16 v[206:207],v242 offset:58624\nM v[16:19],v[208:211],a[96:99],v[16:19]\ns_add_u32 m0,s64,131072\n" \
"s_xor_b32 s69,s99,3145728\nM v[16:19],v[212:215],a[112:115],v[16:19]\nbuffer_load_dwordx4 a[192:195],v240,s[52:55],s76 offen offset:0 sc1\nM v[20:23],v[208:211],a[100:103],v[20:23]\nM v[20:23],v[212:215],a[116:119],v[20:23]\nM v[80:83],v[208:211],a[104:107],v[80:83]\nM v[80:83],v[212:215],a[120:123],v[80:83]\nbuffer_load_dwordx4 a[208:211],v240,s[52:55],s76 offen offset:2048 sc1\n" \
"M v[84:87],v[208:211],a[108:111],v[84:87]\nds_read_b64_tr_b16 v[208:209],v241 offset:50688\nds_read_b64_tr_b16 v[210:211],v241 offset:50944\nM v[84:87],v[212:215],a[124:127],v[84:87]\nds_read_b64_tr_b16 v[212:213],v241 offset:58880\ns_waitcnt lgkmcnt(7)\nM v[24:27],v[216:219],a[96:99],v[24:27]\nds_read_b64_tr_b16 v[214:215],v241 offset:59136\n" \
"M v[24:27],v[220:223],a[112:115],v[24:27]\nbuffer_load_dwordx4 a[196:199],v240,s[52:55],s76 offen offset:1024 sc1\nM v[28:31],v[216:219],a[100:103],v[28:31]\nM v[28:31],v[220:223],a[116:119],v[28:31]\nM v[88:91],v[216:219],a[104:107],v[88:91]\nM v[88:91],v[220:223],a[120:123],v[88:91]\nbuffer_load_dwordx4 a[212:215],v240,s[52:55],s76 offen offset:3072 sc1\nM v[92:95],v[216:219],a[108:111],v[92:95]\n" \
"ds_read_b64_tr_b16 v[216:217],v242 offset:50688\nds_read_b64_tr_b16 v[218:219],v242 offset:50944\nM v[92:95],v[220:223],a[124:127],v[92:95]\nds_read_b64_tr_b16 v[220:221],v242 offset:58880\nds_read_b64_tr_b16 v[222:223],v242 offset:59136\nM v[32:35],v[192:195],a[96:99],v[32:35]\ns_xor_b32 s76,s76,4096\nM v[32:35],v[196:199],a[112:115],v[32:35]\n" \
"buffer_load_dwordx4 a[200:203],v240,s[52:55],s77 offen offset:0 sc1\nM v[36:39],v[192:195],a[100:103],v[36:39]\nM v[36:39],v[196:199],a[116:119],v[36:39]\nM v[96:99],v[192:195],a[104:107],v[96:99]\nM v[96:99],v[196:199],a[120:123],v[96:99]\nbuffer_load_dwordx4 a[216:219],v240,s[52:55],s77 offen offset:2048 sc1\nM v[100:103],v[192:195],a[108:111],v[100:103]\nM v[100:103],v[196:199],a[124:127],v[100:103]\n" \
"s_waitcnt lgkmcnt(4)\nM v[40:43],v[200:203],a[96:99],v[40:43]\nM v[40:43],v[204:207],a[112:115],v[40:43]\nbuffer_store_dwordx4 v[156:159],v240,s[52:55],s74 offen offset:3072 sc1\nM v[44:47],v[200:203],a[100:103],v[44:47]\ns_add_u32 s74,s74,4096\nM v[44:47],v[204:207],a[116:119],v[44:47]\nM v[104:107],v[200:203],a[104:107],v[104:107]\n" \
"M v[104:107],v[204:207],a[120:123],v[104:107]\nbuffer_load_dwordx4 a[204:207],v240,s[52:55],s77 offen offset:1024 sc1\nM v[108:111],v[200:203],a[108:111],v[108:111]\nM v[108:111],v[204:207],a[124:127],v[108:111]\nM v[48:51],v[208:211],a[96:99],v[48:51]\nM v[48:51],v[212:215],a[112:115],v[48:51]\nbuffer_load_dwordx4 a[220:223],v240,s[52:55],s77 offen offset:3072 sc1\nM v[52:55],v[208:211],a[100:103],v[52:55]\n" \
"s_xor_b32 s77,s77,4096\nM v[52:55],v[212:215],a[116:119],v[52:55]\nM v[112:115],v[208:211],a[104:107],v[112:115]\ns_waitcnt vmcnt(24) lgkmcnt(0)\ns_barrier\nM v[112:115],v[212:215],a[120:123],v[112:115]\nds_read_b64_tr_b16 v[192:193],v241 offset:0\nds_read_b64_tr_b16 v[194:195],v241 offset:256\n" \
"M v[116:119],v[208:211],a[108:111],v[116:119]\nds_read_b64_tr_b16 v[196:197],v241 offset:8192\nds_read_b64_tr_b16 v[198:199],v241 offset:8448\nM v[116:119],v[212:215],a[124:127],v[116:119]\nds_read_b64_tr_b16 v[200:201],v242 offset:0\nds_read_b64_tr_b16 v[202:203],v242 offset:256\nM v[56:59],v[216:219],a[96:99],v[56:59]\nds_read_b64_tr_b16 v[204:205],v242 offset:8192\n" \
"ds_read_b64_tr_b16 v[206:207],v242 offset:8448\nM v[56:59],v[220:223],a[112:115],v[56:59]\nds_read_b64_tr_b16 v[208:209],v241 offset:512\nds_read_b64_tr_b16 v[210:211],v241 offset:768\nM v[60:63],v[216:219],a[100:103],v[60:63]\nds_read_b64_tr_b16 v[212:213],v241 offset:8704\nds_read_b64_tr_b16 v[214:215],v241 offset:8960\nM v[60:63],v[220:223],a[116:119],v[60:63]\n" \
"buffer_load_dwordx4 v243,s[40:43],s69 offen offset:0 lds\nM v[120:123],v[216:219],a[104:107],v[120:123]\nM v[120:123],v[220:223],a[120:123],v[120:123]\nM v[124:127],v[216:219],a[108:111],v[124:127]\nds_read_b64_tr_b16 v[216:217],v242 offset:512\nds_read_b64_tr_b16 v[218:219],v242 offset:768\nM v[124:127],v[220:223],a[124:127],v[124:127]\nds_read_b64_tr_b16 v[220:221],v242 offset:8704\n" \
"s_waitcnt lgkmcnt(13)\nM v[0:3],v[192:195],a[128:131],v[0:3]\nds_read_b64_tr_b16 v[222:223],v242 offset:8960\ns_waitcnt lgkmcnt(12)\nM v[0:3],v[196:199],a[144:147],v[0:3]\nbuffer_load_dwordx4 v244,s[40:43],s69 offen offset:1024 lds\nM v[4:7],v[192:195],a[132:135],v[4:7]\nM v[4:7],v[196:199],a[148:151],v[4:7]\n" \
"M v[64:67],v[192:195],a[136:139],v[64:67]\nM v[64:67],v[196:199],a[152:155],v[64:67]\nbuffer_store_dwordx4 v[160:163],v240,s[52:55],s74 offen offset:0 sc1\nM v[68:71],v[192:195],a[140:143],v[68:71]\nds_read_b64_tr_b16 v[192:193],v241 offset:1024\nds_read_b64_tr_b16 v[194:195],v241 offset:1280\nM v[68:71],v[196:199],a[156:159],v[68:71]\nds_read_b64_tr_b16 v[196:197],v241 offset:9216\n" \
"s_waitcnt lgkmcnt(7)\nM v[8:11],v[200:203],a[128:131],v[8:11]\nds_read_b64_tr_b16 v[198:199],v241 offset:9472\nM v[8:11],v[204:207],a[144:147],v[8:11]\nbuffer_load_dwordx4 v245,s[40:43],s69 offen offset:2048 lds\nM v[12:15],v[200:203],a[132:135],v[12:15]\nM v[12:15],v[204:207],a[148:151],v[12:15]\nM v[72:75],v[200:203],a[136:139],v[72:75]\n" \
"M v[72:75],v[204:207],a[152:155],v[72:75]\nbuffer_load_dwordx4 v246,s[40:43],s69 offen offset:3072 lds\nM v[76:79],v[200:203],a[140:143],v[76:79]\nds_read_b64_tr_b16 v[200:201],v242 offset:1024\nds_read_b64_tr_b16 v[202:203],v242 offset:1280\nM v[76:79],v[204:207],a[156:159],v[76:79]\nds_read_b64_tr_b16 v[204:205],v242 offset:9216\nds_read_b64_tr_b16 v[206:207],v242 offset:9472\n" \
"M v[16:19],v[208:211],a[128:131],v[16:19]\ns_add_u32 m0,s64,147456\ns_xor_b32 s69,s99,3670016\nM v[16:19],v[212:215],a[144:147],v[16:19]\nbuffer_load_dwordx4 a[224:227],v240,s[52:55],s76 offen offset:0 sc1\nM v[20:23],v[208:211],a[132:135],v[20:23]\ns_mov_b32 s99,0\nM v[20:23],v[212:215],a[148:151],v[20:23]\n" \
"M v[80:83],v[208:211],a[136:139],v[80:83]\nds_read_b128 a[0:3],v240 offset:0\nM v[80:83],v[212:215],a[152:155],v[80:83]\nbuffer_load_dword v248,v228,s[52:55],s80 offen sc1\nM v[84:87],v[208:211],a[140:143],v[84:87]\nds_read_b64_tr_b16 v[208:209],v241 offset:1536\nds_read_b64_tr_b16 v[210:211],v241 offset:1792\nM v[84:87],v[212:215],a[156:159],v[84:87]\n" \
"ds_read_b64_tr_b16 v[212:213],v241 offset:9728\ns_waitcnt lgkmcnt(8)\nM v[24:27],v[216:219],a[128:131],v[24:27]\nbuffer_load_dwordx4 a[240:243],v240,s[52:55],s76 offen offset:2048 sc1\nM v[24:27],v[220:223],a[144:147],v[24:27]\nds_read_b64_tr_b16 v[214:215],v241 offset:9984\nM v[28:31],v[216:219],a[132:135],v[28:31]\nds_read_b128 a[4:7],v240 offset:1024\n" \
"M v[28:31],v[220:223],a[148:151],v[28:31]\nM v[88:91],v[216:219],a[136:139],v[88:91]\nbuffer_load_dwordx4 a[228:231],v240,s[52:55],s76 offen offset:1024 sc1\nM v[88:91],v[220:223],a[152:155],v[88:91]\nM v[92:95],v[216:219],a[140:143],v[92:95]\nds_read_b64_tr_b16 v[216:217],v242 offset:1536\nds_read_b64_tr_b16 v[218:219],v242 offset:1792\nM v[92:95],v[220:223],a[156:159],v[92:95]\n" \
"ds_read_b64_tr_b16 v[220:221],v242 offset:9728\nds_read_b64_tr_b16 v[222:223],v242 offset:9984\nM v[32:35],v[192:195],a[128:131],v[32:35]\nbuffer_load_dwordx4 a[244:247],v240,s[52:55],s76 offen offset:3072 sc1\nM v[32:35],v[196:199],a[144:147],v[32:35]\nds_read_b128 a[8:11],v240 offset:2048\nM v[36:39],v[192:195],a[132:135],v[36:39]\nM v[36:39],v[196:199],a[148:151],v[36:39]\n" \
"M v[96:99],v[192:195],a[136:139],v[96:99]\nbuffer_store_dwordx4 v[164:167],v240,s[52:55],s74 offen offset:1024 sc1\nM v[96:99],v[196:199],a[152:155],v[96:99]\nM v[100:103],v[192:195],a[140:143],v[100:103]\nds_read_b128 a[12:15],v240 offset:3072\nM v[100:103],v[196:199],a[156:159],v[100:103]\ns_waitcnt lgkmcnt(7)\nM v[40:43],v[200:203],a[128:131],v[40:43]\n" \
"buffer_load_dwordx4 a[232:235],v240,s[52:55],s77 offen offset:0 sc1\nM v[40:43],v[204:207],a[144:147],v[40:43]\nM v[44:47],v[200:203],a[132:135],v[44:47]\nM v[44:47],v[204:207],a[148:151],v[44:47]\nM v[104:107],v[200:203],a[136:139],v[104:107]\nbuffer_load_dwordx4 a[248:251],v240,s[52:55],s77 offen offset:2048 sc1\nM v[104:107],v[204:207],a[152:155],v[104:107]\nds_read_b128 a[16:19],v240 offset:4096\n" \
"M v[108:111],v[200:203],a[140:143],v[108:111]\nM v[108:111],v[204:207],a[156:159],v[108:111]\nM v[48:51],v[208:211],a[128:131],v[48:51]\nbuffer_load_dwordx4 a[236:239],v240,s[52:55],s77 offen offset:1024 sc1\nM v[48:51],v[212:215],a[144:147],v[48:51]\nM v[52:55],v[208:211],a[132:135],v[52:55]\nds_read_b128 a[20:23],v240 offset:5120\nM v[52:55],v[212:215],a[148:151],v[52:55]\n" \
"M v[112:115],v[208:211],a[136:139],v[112:115]\ns_waitcnt vmcnt(17) lgkmcnt(3)\ns_barrier\nM v[112:115],v[212:215],a[152:155],v[112:115]\nds_read_b64_tr_b16 v[192:193],v241 offset:16384\nds_read_b64_tr_b16 v[194:195],v241 offset:16640\nM v[116:119],v[208:211],a[140:143],v[116:119]\nds_read_b64_tr_b16 v[196:197],v241 offset:24576\n" \
"ds_read_b64_tr_b16 v[198:199],v241 offset:24832\nM v[116:119],v[212:215],a[156:159],v[116:119]\nds_read_b64_tr_b16 v[200:201],v242 offset:16384\nds_read_b64_tr_b16 v[202:203],v242 offset:16640\nM v[56:59],v[216:219],a[128:131],v[56:59]\nds_read_b64_tr_b16 v[204:205],v242 offset:24576\nds_read_b64_tr_b16 v[206:207],v242 offset:24832\nM v[56:59],v[220:223],a[144:147],v[56:59]\n" \
"ds_read_b64_tr_b16 v[208:209],v241 offset:16896\nds_read_b64_tr_b16 v[210:211],v241 offset:17152\nM v[60:63],v[216:219],a[132:135],v[60:63]\nds_read_b64_tr_b16 v[212:213],v241 offset:25088\nds_read_b64_tr_b16 v[214:215],v241 offset:25344\nM v[60:63],v[220:223],a[148:151],v[60:63]\nbuffer_load_dwordx4 a[252:255],v240,s[52:55],s77 offen offset:3072 sc1\nM v[120:123],v[216:219],a[136:139],v[120:123]\n" \
"ds_read_b128 a[24:27],v240 offset:6144\nM v[120:123],v[220:223],a[152:155],v[120:123]\nM v[124:127],v[216:219],a[140:143],v[124:127]\nds_read_b64_tr_b16 v[216:217],v242 offset:16896\nds_read_b64_tr_b16 v[218:219],v242 offset:17152\nM v[124:127],v[220:223],a[156:159],v[124:127]\nds_read_b64_tr_b16 v[220:221],v242 offset:25088\ns_waitcnt lgkmcnt(14)\n" \
"M v[0:3],v[192:195],a[160:163],v[0:3]\nds_read_b64_tr_b16 v[222:223],v242 offset:25344\ns_waitcnt lgkmcnt(13)\nM v[0:3],v[196:199],a[176:179],v[0:3]\nbuffer_store_dwordx4 v[168:171],v240,s[52:55],s74 offen offset:2048 sc1\nM v[4:7],v[192:195],a[164:167],v[4:7]\nds_read_b128 a[28:31],v240 offset:7168\nM v[4:7],v[196:199],a[180:183],v[4:7]\n" \
"M v[64:67],v[192:195],a[168:171],v[64:67]\nds_read_b128 a[32:35],v240 offset:8192\nM v[64:67],v[196:199],a[184:187],v[64:67]\nbuffer_load_dwordx4 v243,s[40:43],s69 offen offset:0 lds\nM v[68:71],v[192:195],a[172:175],v[68:71]\nds_read_b64_tr_b16 v[192:193],v241 offset:17408\nds_read_b64_tr_b16 v[194:195],v241 offset:17664\nM v[68:71],v[196:199],a[188:191],v[68:71]\n" \
"ds_read_b64_tr_b16 v[196:197],v241 offset:25600\ns_waitcnt lgkmcnt(10)\nM v[8:11],v[200:203],a[160:163],v[8:11]\nds_read_b64_tr_b16 v[198:199],v241 offset:25856\nM v[8:11],v[204:207],a[176:179],v[8:11]\nbuffer_load_dwordx4 v244,s[40:43],s69 offen offset:1024 lds\nM v[12:15],v[200:203],a[164:167],v[12:15]\nds_read_b128 a[36:39],v240 offset:9216\n" \
"M v[12:15],v[204:207],a[180:183],v[12:15]\nM v[72:75],v[200:203],a[168:171],v[72:75]\nM v[72:75],v[204:207],a[184:187],v[72:75]\nbuffer_load_dwordx4 v245,s[40:43],s69 offen offset:2048 lds\nM v[76:79],v[200:203],a[172:175],v[76:79]\nds_read_b64_tr_b16 v[200:201],v242 offset:17408\nds_read_b64_tr_b16 v[202:203],v242 offset:17664\nM v[76:79],v[204:207],a[188:191],v[76:79]\n" \
"ds_read_b64_tr_b16 v[204:205],v242 offset:25600\nds_read_b64_tr_b16 v[206:207],v242 offset:25856\nM v[16:19],v[208:211],a[160:163],v[16:19]\nds_read_b128 a[40:43],v240 offset:10240\nM v[16:19],v[212:215],a[176:179],v[16:19]\nbuffer_load_dwordx4 v246,s[40:43],s69 offen offset:3072 lds\nM v[20:23],v[208:211],a[164:167],v[20:23]\ns_sub_u32 s40,s18,0x1000\n" \
"s_subb_u32 s41,s19,0\nM v[20:23],v[212:215],a[180:183],v[20:23]\ns_waitcnt vmcnt(13)\nv_cmp_eq_u32 vcc,0,v248\nM v[80:83],v[208:211],a[168:171],v[80:83]\ns_cbranch_vccnz .Lps1\n.Lpb1:\ns_add_u32 s80,s80,4096\n" \
"M v[80:83],v[212:215],a[184:187],v[80:83]\ns_cmp_ge_u32 s62,6\ns_cselect_b64 vcc,-1,0\nM v[84:87],v[208:211],a[172:175],v[84:87]\nds_read_b64_tr_b16 v[208:209],v241 offset:17920\nds_read_b64_tr_b16 v[210:211],v241 offset:18176\nM v[84:87],v[212:215],a[188:191],v[84:87]\nds_read_b64_tr_b16 v[212:213],v241 offset:26112\n" \
"s_waitcnt lgkmcnt(8)\nM v[24:27],v[216:219],a[160:163],v[24:27]\nds_read_b64_tr_b16 v[214:215],v241 offset:26368\nv_cndmask_b32 v254,v231,v229,vcc\nM v[24:27],v[220:223],a[176:179],v[24:27]\ns_cmp_eq_u32 s62,3\ns_cselect_b64 vcc,-1,0\nM v[28:31],v[216:219],a[164:167],v[28:31]\n" \
"v_cndmask_b32 v254,v254,v230,vcc\nv_bfe_u32 v230,v249,2,3\nM v[28:31],v[220:223],a[180:183],v[28:31]\nv_and_b32 v231,0x70,v243\nv_lshl_or_b32 v230,v230,10,v231\nM v[88:91],v[216:219],a[168:171],v[88:91]\nv_and_b32 v231,16,v249\nv_lshlrev_b32 v231,1,v231\n" \
"M v[88:91],v[220:223],a[184:187],v[88:91]\nv_xor_b32 v230,v230,v231\nv_add_u32 v243,4096,v230\nM v[92:95],v[216:219],a[172:175],v[92:95]\nbuffer_store_dwordx4 v[172:175],v240,s[52:55],s74 offen offset:3072 sc1\nM v[92:95],v[220:223],a[188:191],v[92:95]\nds_read_b64_tr_b16 v[216:217],v242 offset:17920\nds_read_b64_tr_b16 v[218:219],v242 offset:18176\n" \
"M v[32:35],v[192:195],a[160:163],v[32:35]\nds_read_b64_tr_b16 v[220:221],v242 offset:26112\nds_read_b64_tr_b16 v[222:223],v242 offset:26368\nM v[32:35],v[196:199],a[176:179],v[32:35]\ns_add_u32 s74,s74,4096\nv_add_u32 v244,3200,v230\nM v[36:39],v[192:195],a[164:167],v[36:39]\nv_add_u32 v245,10240,v230\n" \
"v_add_u32 v246,9344,v230\nM v[36:39],v[196:199],a[180:183],v[36:39]\nv_and_b32 v229,3,v249\nv_cmp_eq_u32 vcc,0,v229\nM v[96:99],v[192:195],a[168:171],v[96:99]\nv_mov_b32 v230,0x3f803f80\nv_cndmask_b32 v229,0,v230,vcc\nM v[96:99],v[196:199],a[184:187],v[96:99]\n" \
"v_and_b32 v230,0xff,v241\nv_and_b32 v231,16,v249\nM v[100:103],v[192:195],a[172:175],v[100:103]\nv_lshl_or_b32 v230,v231,4,v230\nv_and_b32 v231,32,v249\nM v[100:103],v[196:199],a[188:191],v[100:103]\nds_read_b128 a[44:47],v240 offset:11264\ns_waitcnt lgkmcnt(5)\n" \
"M v[40:43],v[200:203],a[160:163],v[40:43]\nds_read_b128 a[48:51],v240 offset:12288\nds_read_b128 a[52:55],v240 offset:13312\nM v[40:43],v[204:207],a[176:179],v[40:43]\nds_read_b128 a[56:59],v240 offset:14336\nM v[44:47],v[200:203],a[164:167],v[44:47]\nM v[44:47],v[204:207],a[180:183],v[44:47]\nM v[104:107],v[200:203],a[168:171],v[104:107]\n" \
"M v[104:107],v[204:207],a[184:187],v[104:107]\nM v[108:111],v[200:203],a[172:175],v[108:111]\nds_read_b128 a[60:63],v240 offset:15360\nM v[108:111],v[204:207],a[188:191],v[108:111]\nM v[48:51],v[208:211],a[160:163],v[48:51]\nM v[48:51],v[212:215],a[176:179],v[48:51]\nM v[52:55],v[208:211],a[164:167],v[52:55]\nM v[52:55],v[212:215],a[180:183],v[52:55]\n" \
"M v[112:115],v[208:211],a[168:171],v[112:115]\ns_waitcnt lgkmcnt(5)\ns_barrier\nM v[112:115],v[212:215],a[184:187],v[112:115]\nds_read_b64_tr_b16 v[192:193],v241 offset:32768\nds_read_b64_tr_b16 v[194:195],v241 offset:33024\nM v[116:119],v[208:211],a[172:175],v[116:119]\nds_read_b64_tr_b16 v[196:197],v241 offset:40960\n" \
"ds_read_b64_tr_b16 v[198:199],v241 offset:41216\nM v[116:119],v[212:215],a[188:191],v[116:119]\nds_read_b64_tr_b16 v[200:201],v242 offset:32768\nds_read_b64_tr_b16 v[202:203],v242 offset:33024\nM v[56:59],v[216:219],a[160:163],v[56:59]\nds_read_b64_tr_b16 v[204:205],v242 offset:40960\nds_read_b64_tr_b16 v[206:207],v242 offset:41216\nM v[56:59],v[220:223],a[176:179],v[56:59]\n" \
"ds_read_b64_tr_b16 v[208:209],v241 offset:33280\nds_read_b64_tr_b16 v[210:211],v241 offset:33536\nM v[60:63],v[216:219],a[164:167],v[60:63]\nds_read_b64_tr_b16 v[212:213],v241 offset:41472\nds_read_b64_tr_b16 v[214:215],v241 offset:41728\nM v[60:63],v[220:223],a[180:183],v[60:63]\nds_write_b64 v250,v[128:129] offset:0\nM v[120:123],v[216:219],a[168:171],v[120:123]\n" \
"buffer_store_dwordx4 v[176:179],v240,s[52:55],s74 offen offset:0 sc1\nM v[120:123],v[220:223],a[184:187],v[120:123]\nds_write_b64 v250,v[130:131] offset:8\nM v[124:127],v[216:219],a[172:175],v[124:127]\nds_read_b64_tr_b16 v[216:217],v242 offset:33280\nds_read_b64_tr_b16 v[218:219],v242 offset:33536\nM v[124:127],v[220:223],a[188:191],v[124:127]\nds_read_b64_tr_b16 v[220:221],v242 offset:41472\n" \
"s_waitcnt lgkmcnt(15)\nM v[0:3],v[192:195],a[192:195],v[0:3]\nds_read_b64_tr_b16 v[222:223],v242 offset:41728\ns_waitcnt lgkmcnt(14)\nM v[0:3],v[196:199],a[208:211],v[0:3]\nds_write_b64 v250,v[132:133] offset:1024\nM v[4:7],v[192:195],a[196:199],v[4:7]\nds_write_b64 v250,v[134:135] offset:1032\n" \
"M v[4:7],v[196:199],a[212:215],v[4:7]\nds_write_b64 v250,v[136:137] offset:2048\nM v[64:67],v[192:195],a[200:203],v[64:67]\nM v[64:67],v[196:199],a[216:219],v[64:67]\nds_write_b64 v250,v[138:139] offset:2056\nM v[68:71],v[192:195],a[204:207],v[68:71]\nds_read_b64_tr_b16 v[192:193],v241 offset:33792\nds_read_b64_tr_b16 v[194:195],v241 offset:34048\n" \
"M v[68:71],v[196:199],a[220:223],v[68:71]\nds_read_b64_tr_b16 v[196:197],v241 offset:41984\ns_waitcnt lgkmcnt(12)\nM v[8:11],v[200:203],a[192:195],v[8:11]\nds_write_b64 v250,v[140:141] offset:3072\nds_read_b64_tr_b16 v[198:199],v241 offset:42240\nM v[8:11],v[204:207],a[208:211],v[8:11]\nM v[12:15],v[200:203],a[196:199],v[12:15]\n" \
"M v[12:15],v[204:207],a[212:215],v[12:15]\nds_write_b64 v250,v[142:143] offset:3080\nM v[72:75],v[200:203],a[200:203],v[72:75]\nM v[72:75],v[204:207],a[216:219],v[72:75]\nM v[76:79],v[200:203],a[204:207],v[76:79]\nds_read_b64_tr_b16 v[200:201],v242 offset:33792\nds_read_b64_tr_b16 v[202:203],v242 offset:34048\nM v[76:79],v[204:207],a[220:223],v[76:79]\n" \
"ds_read_b64_tr_b16 v[204:205],v242 offset:41984\nds_read_b64_tr_b16 v[206:207],v242 offset:42240\nM v[16:19],v[208:211],a[192:195],v[16:19]\nds_write_b64 v250,v[144:145] offset:4096\nM v[16:19],v[212:215],a[208:211],v[16:19]\nds_write_b64 v250,v[146:147] offset:4104\nM v[20:23],v[208:211],a[196:199],v[20:23]\nM v[20:23],v[212:215],a[212:215],v[20:23]\n" \
"M v[80:83],v[208:211],a[200:203],v[80:83]\nds_write_b64 v250,v[148:149] offset:5120\nM v[80:83],v[212:215],a[216:219],v[80:83]\nM v[84:87],v[208:211],a[204:207],v[84:87]\nds_read_b64_tr_b16 v[208:209],v241 offset:34304\nds_read_b64_tr_b16 v[210:211],v241 offset:34560\nM v[84:87],v[212:215],a[220:223],v[84:87]\nds_read_b64_tr_b16 v[212:213],v241 offset:42496\n" \
"s_waitcnt lgkmcnt(11)\nM v[24:27],v[216:219],a[192:195],v[24:27]\nds_write_b64 v250,v[150:151] offset:5128\nds_read_b64_tr_b16 v[214:215],v241 offset:42752\nM v[24:27],v[220:223],a[208:211],v[24:27]\nM v[28:31],v[216:219],a[196:199],v[28:31]\nbuffer_store_dwordx4 v[180:183],v240,s[52:55],s74 offen offset:1024 sc1\nM v[28:31],v[220:223],a[212:215],v[28:31]\n" \
"ds_write_b64 v250,v[152:153] offset:6144\nM v[88:91],v[216:219],a[200:203],v[88:91]\nM v[88:91],v[220:223],a[216:219],v[88:91]\nds_write_b64 v250,v[154:155] offset:6152\nM v[92:95],v[216:219],a[204:207],v[92:95]\nds_read_b64_tr_b16 v[216:217],v242 offset:34304\nds_read_b64_tr_b16 v[218:219],v242 offset:34560\nM v[92:95],v[220:223],a[220:223],v[92:95]\n" \
"ds_read_b64_tr_b16 v[220:221],v242 offset:42496\nds_read_b64_tr_b16 v[222:223],v242 offset:42752\nM v[32:35],v[192:195],a[192:195],v[32:35]\nds_write_b64 v250,v[156:157] offset:7168\nM v[32:35],v[196:199],a[208:211],v[32:35]\nM v[36:39],v[192:195],a[196:199],v[36:39]\nM v[36:39],v[196:199],a[212:215],v[36:39]\nds_write_b64 v250,v[158:159] offset:7176\n" \
"M v[96:99],v[192:195],a[200:203],v[96:99]\nM v[96:99],v[196:199],a[216:219],v[96:99]\nM v[100:103],v[192:195],a[204:207],v[100:103]\nds_write_b64 v250,v[160:161] offset:8192\nM v[100:103],v[196:199],a[220:223],v[100:103]\ns_waitcnt lgkmcnt(9)\nM v[40:43],v[200:203],a[192:195],v[40:43]\nM v[40:43],v[204:207],a[208:211],v[40:43]\n" \
"ds_write_b64 v250,v[162:163] offset:8200\nM v[44:47],v[200:203],a[196:199],v[44:47]\nM v[44:47],v[204:207],a[212:215],v[44:47]\nM v[104:107],v[200:203],a[200:203],v[104:107]\nds_write_b64 v250,v[164:165] offset:9216\nM v[104:107],v[204:207],a[216:219],v[104:107]\nM v[108:111],v[200:203],a[204:207],v[108:111]\nM v[108:111],v[204:207],a[220:223],v[108:111]\n" \
"ds_write_b64 v250,v[166:167] offset:9224\nM v[48:51],v[208:211],a[192:195],v[48:51]\nM v[48:51],v[212:215],a[208:211],v[48:51]\nM v[52:55],v[208:211],a[196:199],v[52:55]\nds_write_b64 v250,v[168:169] offset:10240\nM v[52:55],v[212:215],a[212:215],v[52:55]\nM v[112:115],v[208:211],a[200:203],v[112:115]\ns_waitcnt vmcnt(3) lgkmcnt(5)\n" \
"s_barrier\nM v[112:115],v[212:215],a[216:219],v[112:115]\nds_read_b64_tr_b16 v[192:193],v241 offset:49152\nds_read_b64_tr_b16 v[194:195],v241 offset:49408\nM v[116:119],v[208:211],a[204:207],v[116:119]\nds_read_b64_tr_b16 v[196:197],v241 offset:57344\nds_read_b64_tr_b16 v[198:199],v241 offset:57600\nM v[116:119],v[212:215],a[220:223],v[116:119]\n" \
"ds_read_b64_tr_b16 v[200:201],v242 offset:49152\nds_read_b64_tr_b16 v[202:203],v242 offset:49408\nM v[56:59],v[216:219],a[192:195],v[56:59]\nbuffer_store_dwordx4 v[184:187],v240,s[52:55],s74 offen offset:2048 sc1\nM v[56:59],v[220:223],a[208:211],v[56:59]\nds_read_b64_tr_b16 v[204:205],v242 offset:57344\nds_read_b64_tr_b16 v[206:207],v242 offset:57600\nM v[60:63],v[216:219],a[196:199],v[60:63]\n" \
"ds_read_b64_tr_b16 v[208:209],v241 offset:49664\nds_read_b64_tr_b16 v[210:211],v241 offset:49920\nM v[60:63],v[220:223],a[212:215],v[60:63]\nds_read_b64_tr_b16 v[212:213],v241 offset:57856\nds_read_b64_tr_b16 v[214:215],v241 offset:58112\nM v[120:123],v[216:219],a[200:203],v[120:123]\nds_write_b64 v250,v[170:171] offset:10248\nv_accvgpr_write_b32 a208,v229\n" \
"M v[120:123],v[220:223],a[216:219],v[120:123]\nds_write_b64 v250,v[172:173] offset:11264\nv_accvgpr_write_b32 a209,v229\nM v[124:127],v[216:219],a[204:207],v[124:127]\nds_read_b64_tr_b16 v[216:217],v242 offset:49664\nds_read_b64_tr_b16 v[218:219],v242 offset:49920\nM v[124:127],v[220:223],a[220:223],v[124:127]\nds_read_b64_tr_b16 v[220:221],v242 offset:57856\n" \
"s_waitcnt lgkmcnt(15)\nM v[0:3],v[192:195],a[224:227],v[0:3]\nds_read_b64_tr_b16 v[222:223],v242 offset:58112\ns_waitcnt lgkmcnt(14)\nM v[0:3],v[196:199],a[240:243],v[0:3]\nds_write_b64 v250,v[174:175] offset:11272\nv_accvgpr_write_b32 a210,v229\nM v[4:7],v[192:195],a[228:231],v[4:7]\n" \
"ds_write_b64 v250,v[176:177] offset:12288\nv_accvgpr_write_b32 a211,v229\nM v[4:7],v[196:199],a[244:247],v[4:7]\nds_write_b64 v250,v[178:179] offset:12296\nM v[64:67],v[192:195],a[232:235],v[64:67]\nv_cvt_pk_bf16_f32 v0,v0,v1\nv_cvt_pk_bf16_f32 v1,v2,v3\nM v[64:67],v[196:199],a[248:251],v[64:67]\n" \
"ds_write_b64 v250,v[180:181] offset:13312\nv_accvgpr_write_b32 a128,v0\nM v[68:71],v[192:195],a[236:239],v[68:71]\nds_read_b64_tr_b16 v[192:193],v241 offset:50176\nds_read_b64_tr_b16 v[194:195],v241 offset:50432\nM v[68:71],v[196:199],a[252:255],v[68:71]\nds_read_b64_tr_b16 v[196:197],v241 offset:58368\ns_waitcnt lgkmcnt(12)\n" \
"M v[8:11],v[200:203],a[224:227],v[8:11]\nds_read_b64_tr_b16 v[198:199],v241 offset:58624\nv_cvt_pk_bf16_f32 v4,v4,v5\nM v[8:11],v[204:207],a[240:243],v[8:11]\nv_cvt_pk_bf16_f32 v64,v64,v65\nv_cvt_pk_bf16_f32 v5,v6,v7\nM v[12:15],v[200:203],a[228:231],v[12:15]\nv_cvt_pk_bf16_f32 v68,v68,v69\n" \
"v_cvt_pk_bf16_f32 v65,v66,v67\nM v[12:15],v[204:207],a[244:247],v[12:15]\nds_write_b64 v250,v[182:183] offset:13320\nv_cvt_pk_bf16_f32 v69,v70,v71\nM v[72:75],v[200:203],a[232:235],v[72:75]\nds_write_b64 v250,v[184:185] offset:14336\nv_cvt_pk_bf16_f32 v2,v8,v9\nM v[72:75],v[204:207],a[248:251],v[72:75]\n" \
"ds_write_b64 v250,v[186:187] offset:14344\nv_cvt_pk_bf16_f32 v3,v10,v11\nM v[76:79],v[200:203],a[236:239],v[76:79]\nds_read_b64_tr_b16 v[200:201],v242 offset:50176\nds_read_b64_tr_b16 v[202:203],v242 offset:50432\nM v[76:79],v[204:207],a[252:255],v[76:79]\nds_read_b64_tr_b16 v[204:205],v242 offset:58368\nds_read_b64_tr_b16 v[206:207],v242 offset:58624\n" \
"M v[16:19],v[208:211],a[224:227],v[16:19]\nds_write_b64 v250,v[188:189] offset:15360\nv_cvt_pk_bf16_f32 v6,v12,v13\nM v[16:19],v[212:215],a[240:243],v[16:19]\nv_cvt_pk_bf16_f32 v7,v14,v15\nv_cvt_pk_bf16_f32 v66,v72,v73\nM v[20:23],v[208:211],a[228:231],v[20:23]\nv_cvt_pk_bf16_f32 v67,v74,v75\n" \
"v_cvt_pk_bf16_f32 v70,v76,v77\nM v[20:23],v[212:215],a[244:247],v[20:23]\nds_write_b64 v250,v[190:191] offset:15368\nv_cvt_pk_bf16_f32 v71,v78,v79\nM v[80:83],v[208:211],a[232:235],v[80:83]\nv_cvt_pk_bf16_f32 v16,v16,v17\nv_cvt_pk_bf16_f32 v17,v18,v19\nM v[80:83],v[212:215],a[248:251],v[80:83]\n" \
"v_add_u32 v250,114688,v240\nv_accvgpr_write_b32 a129,v1\nM v[84:87],v[208:211],a[236:239],v[84:87]\nbuffer_store_dwordx4 v[188:191],v240,s[52:55],s74 offen offset:3072 sc1\nM v[84:87],v[212:215],a[252:255],v[84:87]\nds_read_b64_tr_b16 v[208:209],v241 offset:50688\ns_waitcnt lgkmcnt(7)\nM v[24:27],v[216:219],a[224:227],v[24:27]\n" \
"ds_read_b64_tr_b16 v[210:211],v241 offset:50944\nds_read_b64_tr_b16 v[212:213],v241 offset:58880\nM v[24:27],v[220:223],a[240:243],v[24:27]\nds_read_b64_tr_b16 v[214:215],v241 offset:59136\nv_cvt_pk_bf16_f32 v20,v20,v21\nM v[28:31],v[216:219],a[228:231],v[28:31]\nv_cvt_pk_bf16_f32 v80,v80,v81\nv_cvt_pk_bf16_f32 v84,v84,v85\n" \
"M v[28:31],v[220:223],a[244:247],v[28:31]\nv_cvt_pk_bf16_f32 v21,v22,v23\nv_cvt_pk_bf16_f32 v81,v82,v83\nM v[88:91],v[216:219],a[232:235],v[88:91]\nv_cvt_pk_bf16_f32 v85,v86,v87\nv_cvt_pk_bf16_f32 v18,v24,v25\nM v[88:91],v[220:223],a[248:251],v[88:91]\nv_cvt_pk_bf16_f32 v19,v26,v27\n" \
"v_add_u32 v241,65536,v240\nM v[92:95],v[216:219],a[236:239],v[92:95]\nds_read_b64_tr_b16 v[216:217],v242 offset:50688\nds_read_b64_tr_b16 v[218:219],v242 offset:50944\nM v[92:95],v[220:223],a[252:255],v[92:95]\nds_read_b64_tr_b16 v[220:221],v242 offset:58880\nds_read_b64_tr_b16 v[222:223],v242 offset:59136\nM v[32:35],v[192:195],a[224:227],v[32:35]\n" \
"v_cvt_pk_bf16_f32 v22,v28,v29\nv_cvt_pk_bf16_f32 v23,v30,v31\nM v[32:35],v[196:199],a[240:243],v[32:35]\nv_cvt_pk_bf16_f32 v82,v88,v89\nv_cvt_pk_bf16_f32 v83,v90,v91\nM v[36:39],v[192:195],a[228:231],v[36:39]\nv_cvt_pk_bf16_f32 v86,v92,v93\nv_cvt_pk_bf16_f32 v87,v94,v95\n" \
"M v[36:39],v[196:199],a[244:247],v[36:39]\nv_lshl_or_b32 v242,v231,6,v230\nv_accvgpr_write_b32 a130,v2\nM v[96:99],v[192:195],a[232:235],v[96:99]\nv_cvt_pk_bf16_f32 v32,v32,v33\nv_cvt_pk_bf16_f32 v33,v34,v35\nM v[96:99],v[196:199],a[248:251],v[96:99]\nv_accvgpr_write_b32 a131,v3\n" \
"v_accvgpr_write_b32 a144,v4\nM v[100:103],v[192:195],a[236:239],v[100:103]\nv_cvt_pk_bf16_f32 v36,v36,v37\nv_cvt_pk_bf16_f32 v37,v38,v39\nM v[100:103],v[196:199],a[252:255],v[100:103]\nv_accvgpr_write_b32 a145,v5\ns_waitcnt lgkmcnt(4)\nM v[40:43],v[200:203],a[224:227],v[40:43]\n" \
"v_cvt_pk_bf16_f32 v96,v96,v97\nv_cvt_pk_bf16_f32 v97,v98,v99\nM v[40:43],v[204:207],a[240:243],v[40:43]\nv_accvgpr_write_b32 a146,v6\nv_accvgpr_write_b32 a147,v7\nM v[44:47],v[200:203],a[228:231],v[44:47]\nv_cvt_pk_bf16_f32 v100,v100,v101\nv_cvt_pk_bf16_f32 v101,v102,v103\n" \
"M v[44:47],v[204:207],a[244:247],v[44:47]\nv_accvgpr_write_b32 a160,v64\nv_accvgpr_write_b32 a161,v65\nM v[104:107],v[200:203],a[232:235],v[104:107]\nv_cvt_pk_bf16_f32 v34,v40,v41\nv_cvt_pk_bf16_f32 v35,v42,v43\nM v[104:107],v[204:207],a[248:251],v[104:107]\nv_accvgpr_write_b32 a162,v66\n" \
"v_accvgpr_write_b32 a163,v67\nM v[108:111],v[200:203],a[236:239],v[108:111]\nv_cvt_pk_bf16_f32 v38,v44,v45\nv_cvt_pk_bf16_f32 v39,v46,v47\nM v[108:111],v[204:207],a[252:255],v[108:111]\nv_accvgpr_write_b32 a176,v68\nv_accvgpr_write_b32 a177,v69\nM v[48:51],v[208:211],a[224:227],v[48:51]\n" \
"v_cvt_pk_bf16_f32 v98,v104,v105\nv_cvt_pk_bf16_f32 v99,v106,v107\nM v[48:51],v[212:215],a[240:243],v[48:51]\nv_accvgpr_write_b32 a178,v70\nv_accvgpr_write_b32 a179,v71\nM v[52:55],v[208:211],a[228:231],v[52:55]\nv_cvt_pk_bf16_f32 v102,v108,v109\nv_cvt_pk_bf16_f32 v103,v110,v111\n" \
"M v[52:55],v[212:215],a[244:247],v[52:55]\nv_accvgpr_write_b32 a132,v16\nv_accvgpr_write_b32 a133,v17\nM v[112:115],v[208:211],a[232:235],v[112:115]\nv_cvt_pk_bf16_f32 v48,v48,v49\nv_cvt_pk_bf16_f32 v49,v50,v51\nM v[112:115],v[212:215],a[248:251],v[112:115]\nv_accvgpr_write_b32 a134,v18\n" \
"v_accvgpr_write_b32 a135,v19\nM v[116:119],v[208:211],a[236:239],v[116:119]\nv_cvt_pk_bf16_f32 v52,v52,v53\nv_cvt_pk_bf16_f32 v53,v54,v55\nM v[116:119],v[212:215],a[252:255],v[116:119]\nv_accvgpr_write_b32 a148,v20\ns_waitcnt lgkmcnt(0)\nM v[56:59],v[216:219],a[224:227],v[56:59]\n" \
"v_cvt_pk_bf16_f32 v112,v112,v113\nv_cvt_pk_bf16_f32 v113,v114,v115\nM v[56:59],v[220:223],a[240:243],v[56:59]\nv_accvgpr_write_b32 a149,v21\nv_accvgpr_write_b32 a150,v22\nM v[60:63],v[216:219],a[228:231],v[60:63]\nv_cvt_pk_bf16_f32 v116,v116,v117\nv_cvt_pk_bf16_f32 v117,v118,v119\n" \
"M v[60:63],v[220:223],a[244:247],v[60:63]\nv_accvgpr_write_b32 a244,0\nv_accvgpr_write_b32 a245,0\nM v[120:123],v[216:219],a[232:235],v[120:123]\nv_cvt_pk_bf16_f32 v50,v56,v57\nv_cvt_pk_bf16_f32 v51,v58,v59\nM v[120:123],v[220:223],a[248:251],v[120:123]\nv_accvgpr_write_b32 a248,-1\n" \
"v_accvgpr_write_b32 a249,-1\nM v[124:127],v[216:219],a[236:239],v[124:127]\nv_cvt_pk_bf16_f32 v54,v60,v61\nv_cvt_pk_bf16_f32 v55,v62,v63\nM v[124:127],v[220:223],a[252:255],v[124:127]\ns_waitcnt vmcnt(0)\nbuffer_store_dword v252,off,s[52:55],s29 sc1\nv_cvt_pk_bf16_f32 v114,v120,v121\n" \
"v_cvt_pk_bf16_f32 v115,v122,v123\nv_accvgpr_write_b32 a246,0\nv_accvgpr_write_b32 a250,-1\nv_accvgpr_write_b32 a247,0\nv_accvgpr_write_b32 a251,-1\nv_cvt_pk_bf16_f32 v118,v124,v125\nv_cvt_pk_bf16_f32 v119,v126,v127\nv_accvgpr_write_b32 a151,v23\n" \
"v_accvgpr_write_b32 a164,v80\nv_accvgpr_write_b32 a165,v81\nv_accvgpr_write_b32 a166,v82\nv_accvgpr_write_b32 a167,v83\nv_accvgpr_write_b32 a180,v84\nv_accvgpr_write_b32 a181,v85\nv_accvgpr_write_b32 a182,v86\nv_accvgpr_write_b32 a183,v87\n" \
"v_accvgpr_write_b32 a136,v32\nv_accvgpr_write_b32 a137,v33\nv_accvgpr_write_b32 a138,v34\nv_accvgpr_write_b32 a139,v35\nv_accvgpr_write_b32 a152,v36\nv_accvgpr_write_b32 a153,v37\nv_accvgpr_write_b32 a154,v38\nv_accvgpr_write_b32 a155,v39\n" \
"v_accvgpr_write_b32 a168,v96\nv_accvgpr_write_b32 a169,v97\nv_accvgpr_write_b32 a170,v98\nv_accvgpr_write_b32 a171,v99\nv_accvgpr_write_b32 a184,v100\nv_accvgpr_write_b32 a185,v101\nv_accvgpr_write_b32 a186,v102\nv_accvgpr_write_b32 a187,v103\n" \
"v_accvgpr_write_b32 a140,v48\nv_accvgpr_write_b32 a141,v49\nv_accvgpr_write_b32 a142,v50\nv_accvgpr_write_b32 a143,v51\nv_accvgpr_write_b32 a156,v52\nv_accvgpr_write_b32 a157,v53\nv_accvgpr_write_b32 a158,v54\nv_accvgpr_write_b32 a159,v55\n" \
"v_accvgpr_write_b32 a172,v112\nv_accvgpr_write_b32 a173,v113\nv_accvgpr_write_b32 a174,v114\nv_accvgpr_write_b32 a175,v115\nv_accvgpr_write_b32 a188,v116\nv_accvgpr_write_b32 a189,v117\nv_accvgpr_write_b32 a190,v118\nv_accvgpr_write_b32 a191,v119\n" \
"s_waitcnt lgkmcnt(0)\n.Lattn:\ns_cmp_lt_u32 s7,2\ns_cbranch_scc0 .Lp23\ns_cmp_eq_u32 s7,0\ns_cbranch_scc0 .Lpro1\n.Lpro0:\ns_barrier\n" \
"M v[64:67],a[0:3],a[128:131],0\nds_read_b128 a[32:35],v240 offset:40960\nds_read_b128 a[36:39],v240 offset:41984\nM v[64:67],a[4:7],a[132:135],v[64:67]\nds_read_b128 a[40:43],v240 offset:43008\ns_mov_b32 m0,s64\nv_mov_b32 v218,0\nM v[64:67],a[8:11],a[136:139],v[64:67]\n" \
"ds_read_b128 a[44:47],v240 offset:44032\nv_mov_b32 v219,0\nM v[64:67],a[12:15],a[140:143],v[64:67]\nds_read_b128 a[48:51],v240 offset:45056\nbuffer_load_dwordx4 v240,s[44:47],s36 offen offset:0 sc1 lds\nM v[68:71],a[12:15],a[156:159],0\nds_read_b128 a[12:15],v240 offset:35840\nM v[68:71],a[8:11],a[152:155],v[68:71]\n" \
"ds_read_b128 a[8:11],v240 offset:34816\nM v[68:71],a[4:7],a[148:151],v[68:71]\nds_read_b128 a[4:7],v240 offset:33792\nv_max3_f32 v225,v64,v65,v66\nM v[68:71],a[0:3],a[144:147],v[68:71]\nds_read_b128 a[0:3],v240 offset:32768\nv_max_f32 v225,v225,v67\nv_cndmask_b32 v225,v224,v225,s[90:91]\n" \
"M v[76:79],a[28:31],a[156:159],0\nds_read_b128 a[28:31],v240 offset:39936\nv_cndmask_b32 v64,v64,v252,s[82:83]\nM v[76:79],a[24:27],a[152:155],v[76:79]\nds_read_b128 a[24:27],v240 offset:38912\nv_max3_f32 v229,v68,v69,v70\nv_cndmask_b32 v65,v65,v252,s[84:85]\nM v[76:79],a[20:23],a[148:151],v[76:79]\n" \
"ds_read_b128 a[20:23],v240 offset:37888\nv_cndmask_b32 v66,v66,v252,s[86:87]\nM v[76:79],a[16:19],a[144:147],v[76:79]\nds_read_b128 a[16:19],v240 offset:36864\nv_cndmask_b32 v67,v67,v252,s[88:89]\ns_waitcnt lgkmcnt(4)\nM v[136:139],a[0:3],a[160:163],0\nbuffer_load_dwordx4 v240,s[44:47],s36 offen offset:1024 sc1 lds\n" \
"M v[136:139],a[4:7],a[164:167],v[136:139]\nds_read_b128 a[52:55],v240 offset:46080\nds_read_b128 a[56:59],v240 offset:47104\nv_max3_f32 v229,v229,v71,v76\nM v[136:139],a[8:11],a[168:171],v[136:139]\nv_max3_f32 v229,v229,v77,v78\nv_max_f32 v229,v229,v79\nv_cndmask_b32 v229,v228,v229,s[90:91]\n" \
"M v[136:139],a[12:15],a[172:175],v[136:139]\nds_read_b128 a[60:63],v240 offset:48128\nv_permlane16_swap_b32 v225,v229\nM v[140:143],a[12:15],a[188:191],0\nv_max_f32 v225,v225,v229\nv_mov_b32 v229,v225\nds_read_b128 a[64:67],v241 offset:0\nM v[140:143],a[8:11],a[184:187],v[140:143]\n" \
"v_permlane32_swap_b32 v225,v229\nv_max_f32 v225,v225,v229\nM v[140:143],a[4:7],a[180:183],v[140:143]\nv_mov_b32 v229,v225\nv_max3_f32 v233,v136,v137,v138\nds_read_b128 a[68:71],v241 offset:1024\nM v[140:143],a[0:3],a[176:179],v[140:143]\nv_permlane16_swap_b32 v225,v229\n" \
"s_waitcnt lgkmcnt(5)\nM v[152:155],a[16:19],a[160:163],0\nv_sub_f32 v224,0,v225\nv_sub_f32 v226,0,v225\nv_sub_f32 v227,0,v225\nM v[152:155],a[20:23],a[164:167],v[152:155]\nv_sub_f32 v225,0,v225\nv_sub_f32 v228,0,v229\n" \
"M v[152:155],a[24:27],a[168:171],v[152:155]\nv_sub_f32 v230,0,v229\nv_sub_f32 v231,0,v229\nv_sub_f32 v229,0,v229\nM v[152:155],a[28:31],a[172:175],v[152:155]\nv_max3_f32 v237,v140,v141,v142\nds_read_b128 a[72:75],v241 offset:2048\nM v[156:159],a[28:31],a[188:191],0\n" \
"buffer_load_dwordx4 v240,s[44:47],s36 offen offset:2048 sc1 lds\nds_read_b128 a[76:79],v241 offset:3072\nM v[156:159],a[24:27],a[184:187],v[156:159]\nds_read_b128 a[80:83],v241 offset:4096\nds_read_b128 a[84:87],v241 offset:5120\nM v[156:159],a[20:23],a[180:183],v[156:159]\nv_max3_f32 v233,v233,v139,v152\nv_max3_f32 v233,v233,v153,v154\n" \
"v_max_f32 v233,v233,v155\nM v[156:159],a[16:19],a[176:179],v[156:159]\nv_cndmask_b32 v233,v232,v233,s[90:91]\nds_read_b128 a[88:91],v241 offset:6144\nv_add_f32 v64,v64,v224\nM v[128:131],a[0:3],a[128:131],v[224:227]\nbuffer_load_dwordx4 v240,s[44:47],s36 offen offset:3072 sc1 lds\nM v[128:131],a[4:7],a[132:135],v[128:131]\n" \
"v_add_f32 v65,v65,v224\nv_add_f32 v66,v66,v224\nv_max3_f32 v237,v237,v143,v156\nM v[128:131],a[8:11],a[136:139],v[128:131]\nv_max3_f32 v237,v237,v157,v158\nv_max_f32 v237,v237,v159\nM v[128:131],a[12:15],a[140:143],v[128:131]\nv_cndmask_b32 v237,v236,v237,s[90:91]\n" \
"v_add_f32 v67,v67,v224\nds_read_b128 a[92:95],v241 offset:7168\nM v[132:135],a[12:15],a[156:159],v[228:231]\nv_permlane16_swap_b32 v233,v237\nv_max_f32 v233,v233,v237\nM v[132:135],a[8:11],a[152:155],v[132:135]\nv_mov_b32 v237,v233\nv_exp_f32 v64,v64\n" \
"M v[132:135],a[4:7],a[148:151],v[132:135]\nv_permlane32_swap_b32 v233,v237\nv_max_f32 v233,v233,v237\nM v[132:135],a[0:3],a[144:147],v[132:135]\nv_mov_b32 v237,v233\nv_exp_f32 v65,v65\nM v[144:147],a[16:19],a[128:131],v[224:227]\nv_permlane16_swap_b32 v233,v237\n" \
"v_sub_f32 v232,0,v233\nM v[144:147],a[20:23],a[132:135],v[144:147]\nv_sub_f32 v234,0,v233\nv_sub_f32 v235,0,v233\nv_sub_f32 v233,0,v233\nM v[144:147],a[24:27],a[136:139],v[144:147]\nv_sub_f32 v236,0,v237\nv_sub_f32 v238,0,v237\n" \
"M v[144:147],a[28:31],a[140:143],v[144:147]\nv_sub_f32 v239,0,v237\nv_sub_f32 v237,0,v237\nv_cvt_pk_bf16_f32 v64,v64,v65\nM v[148:151],a[28:31],a[156:159],v[228:231]\nv_exp_f32 v66,v66\nv_add_f32 v68,v68,v228\nM v[148:151],a[24:27],a[152:155],v[148:151]\n" \
"v_exp_f32 v67,v67\nM v[148:151],a[20:23],a[148:151],v[148:151]\nv_cvt_pk_bf16_f32 v65,v66,v67\nv_mov_b32 v66,0\nv_mov_b32 v67,0\nM v[148:151],a[16:19],a[144:147],v[148:151]\nv_add_f32 v69,v69,v228\nv_cndmask_b32 v76,v76,v252,s[82:83]\n" \
"M v[160:163],a[32:35],a[128:131],v[224:227]\nv_cndmask_b32 v77,v77,v252,s[84:85]\nv_cndmask_b32 v78,v78,v252,s[86:87]\nv_cndmask_b32 v79,v79,v252,s[88:89]\nM v[160:163],a[36:39],a[132:135],v[160:163]\nv_add_f32 v70,v70,v228\nv_add_f32 v71,v71,v228\nM v[160:163],a[40:43],a[136:139],v[160:163]\n" \
"v_exp_f32 v68,v68\nv_add_f32 v76,v76,v228\nM v[160:163],a[44:47],a[140:143],v[160:163]\nv_exp_f32 v69,v69\nM v[164:167],a[44:47],a[156:159],v[228:231]\nv_exp_f32 v70,v70\nv_add_f32 v77,v77,v228\nM v[164:167],a[40:43],a[152:155],v[164:167]\n" \
"v_exp_f32 v71,v71\nM v[164:167],a[36:39],a[148:151],v[164:167]\nv_add_f32 v78,v78,v228\nv_add_f32 v79,v79,v228\nv_cvt_pk_bf16_f32 v68,v68,v69\nM v[164:167],a[32:35],a[144:147],v[164:167]\nv_exp_f32 v76,v76\nM v[176:179],a[48:51],a[128:131],v[224:227]\n" \
"v_exp_f32 v77,v77\ns_waitcnt lgkmcnt(0)\nM v[176:179],a[52:55],a[132:135],v[176:179]\nv_exp_f32 v78,v78\nv_cvt_pk_bf16_f32 v69,v70,v71\nM v[176:179],a[56:59],a[136:139],v[176:179]\nv_exp_f32 v79,v79\nM v[176:179],a[60:63],a[140:143],v[176:179]\n" \
"v_cvt_pk_bf16_f32 v70,v76,v77\nv_cvt_pk_bf16_f32 v71,v78,v79\nds_read_b128 a[0:3],v240 offset:49152\nM v[180:183],a[60:63],a[156:159],v[228:231]\nds_read_b128 a[4:7],v240 offset:50176\nds_read_b128 a[8:11],v240 offset:51200\nM v[180:183],a[56:59],a[152:155],v[180:183]\nds_read_b128 a[12:15],v240 offset:52224\n" \
"v_exp_f32 v128,v128\nM v[180:183],a[52:55],a[148:151],v[180:183]\nds_read_b128 a[16:19],v240 offset:53248\nv_add_f32 v136,v136,v232\nM v[180:183],a[48:51],a[144:147],v[180:183]\nv_exp_f32 v129,v129\nds_read_b128 a[20:23],v240 offset:54272\nM v[168:171],a[32:35],a[160:163],v[232:235]\n" \
"v_exp_f32 v130,v130\nM v[168:171],a[36:39],a[164:167],v[168:171]\nv_exp_f32 v131,v131\nv_cvt_pk_bf16_f32 v192,v128,v129\nM v[168:171],a[40:43],a[168:171],v[168:171]\nv_cvt_pk_bf16_f32 v193,v130,v131\nds_read_b128 a[24:27],v240 offset:55296\nM v[168:171],a[44:47],a[172:175],v[168:171]\n" \
"ds_read_b128 a[28:31],v240 offset:56320\nv_exp_f32 v132,v132\nM v[172:175],a[44:47],a[188:191],v[236:239]\nv_exp_f32 v133,v133\nM v[172:175],a[40:43],a[184:187],v[172:175]\nv_exp_f32 v134,v134\nv_cvt_pk_bf16_f32 v196,v132,v133\nM v[172:175],a[36:39],a[180:183],v[172:175]\n" \
"buffer_load_dword v248,v251,s[52:55],s80 offen sc1\nv_add_f32 v137,v137,v232\nM v[172:175],a[32:35],a[176:179],v[172:175]\nv_exp_f32 v135,v135\nM v[188:191],a[60:63],a[188:191],v[236:239]\nv_cvt_pk_bf16_f32 v197,v134,v135\nv_exp_f32 v144,v144\nM v[188:191],a[56:59],a[184:187],v[188:191]\n" \
"v_exp_f32 v145,v145\nM v[188:191],a[52:55],a[180:183],v[188:191]\nv_exp_f32 v146,v146\nv_cvt_pk_bf16_f32 v194,v144,v145\nM v[188:191],a[48:51],a[176:179],v[188:191]\nv_exp_f32 v147,v147\nM v[0:3],a[64:67],v[64:67],0\nv_cvt_pk_bf16_f32 v195,v146,v147\n" \
"v_exp_f32 v148,v148\nM v[8:11],a[68:71],v[64:67],0\nv_exp_f32 v149,v149\nM v[16:19],a[72:75],v[64:67],0\nv_exp_f32 v150,v150\nv_cvt_pk_bf16_f32 v198,v148,v149\nM v[24:27],a[76:79],v[64:67],0\nv_exp_f32 v151,v151\n" \
"M v[32:35],a[80:83],v[64:67],0\nv_cvt_pk_bf16_f32 v199,v150,v151\nv_add_f32 v138,v138,v232\nv_add_f32 v139,v139,v232\nM v[40:43],a[84:87],v[64:67],0\nv_exp_f32 v136,v136\nM v[48:51],a[88:91],v[64:67],0\nv_exp_f32 v137,v137\n" \
"v_add_f32 v140,v140,v236\nM v[56:59],a[92:95],v[64:67],0\nv_exp_f32 v138,v138\nv_cvt_pk_bf16_f32 v200,v136,v137\nM a[192:195],a[208:211],v[64:67],0\nv_exp_f32 v139,v139\nM v[4:7],a[64:67],v[68:71],0\nv_cvt_pk_bf16_f32 v201,v138,v139\n" \
"v_add_f32 v141,v141,v236\nv_add_f32 v142,v142,v236\nM v[12:15],a[68:71],v[68:71],0\nv_add_f32 v143,v143,v236\nds_read_b128 a[32:35],v240 offset:57344\nM v[20:23],a[72:75],v[68:71],0\nv_exp_f32 v140,v140\nv_add_f32 v152,v152,v232\n" \
"M v[28:31],a[76:79],v[68:71],0\nv_exp_f32 v141,v141\nM v[36:39],a[80:83],v[68:71],0\nv_exp_f32 v142,v142\nv_cvt_pk_bf16_f32 v204,v140,v141\nM v[44:47],a[84:87],v[68:71],0\nv_exp_f32 v143,v143\nM v[52:55],a[88:91],v[68:71],0\n" \
"v_cvt_pk_bf16_f32 v205,v142,v143\nv_add_f32 v153,v153,v232\nv_add_f32 v154,v154,v232\nM v[60:63],a[92:95],v[68:71],0\nv_add_f32 v155,v155,v232\nds_read_b128 a[36:39],v240 offset:58368\nM a[196:199],a[208:211],v[68:71],0\nv_exp_f32 v152,v152\n" \
"s_waitcnt lgkmcnt(2)\nM v[128:131],a[0:3],a[128:131],v[224:227]\nv_exp_f32 v153,v153\nM v[128:131],a[4:7],a[132:135],v[128:131]\nv_exp_f32 v154,v154\nv_cvt_pk_bf16_f32 v202,v152,v153\nM v[128:131],a[8:11],a[136:139],v[128:131]\nv_exp_f32 v155,v155\n" \
"M v[128:131],a[12:15],a[140:143],v[128:131]\nv_cvt_pk_bf16_f32 v203,v154,v155\nds_read_b128 a[40:43],v240 offset:59392\nds_read_b128 a[44:47],v240 offset:60416\nM v[132:135],a[12:15],a[156:159],v[228:231]\nv_add_f32 v156,v156,v236\nv_add_f32 v157,v157,v236\nv_add_f32 v158,v158,v236\n" \
"M v[132:135],a[8:11],a[152:155],v[132:135]\nv_add_f32 v159,v159,v236\nds_read_b128 a[48:51],v240 offset:61440\nM v[132:135],a[4:7],a[148:151],v[132:135]\nv_exp_f32 v156,v156\nds_read_b128 a[52:55],v240 offset:62464\nM v[132:135],a[0:3],a[144:147],v[132:135]\nv_exp_f32 v157,v157\n" \
"M v[144:147],a[16:19],a[128:131],v[224:227]\nv_exp_f32 v158,v158\nv_cvt_pk_bf16_f32 v206,v156,v157\nM v[144:147],a[20:23],a[132:135],v[144:147]\nv_exp_f32 v159,v159\nM v[144:147],a[24:27],a[136:139],v[144:147]\nv_cvt_pk_bf16_f32 v207,v158,v159\nv_exp_f32 v160,v160\n" \
"M v[144:147],a[28:31],a[140:143],v[144:147]\nv_exp_f32 v161,v161\nM v[148:151],a[28:31],a[156:159],v[228:231]\nv_exp_f32 v162,v162\nv_cvt_pk_bf16_f32 v208,v160,v161\nM v[148:151],a[24:27],a[152:155],v[148:151]\nv_exp_f32 v163,v163\nM v[148:151],a[20:23],a[148:151],v[148:151]\n" \
"v_cvt_pk_bf16_f32 v209,v162,v163\nds_read_b128 a[56:59],v240 offset:63488\nds_read_b128 a[60:63],v240 offset:64512\nM v[148:151],a[16:19],a[144:147],v[148:151]\nv_exp_f32 v164,v164\nM v[136:139],a[0:3],a[160:163],v[232:235]\nv_exp_f32 v165,v165\nv_cndmask_b32 v168,v168,v252,s[82:83]\n" \
"M v[136:139],a[4:7],a[164:167],v[136:139]\nv_exp_f32 v166,v166\nv_cvt_pk_bf16_f32 v212,v164,v165\nM v[136:139],a[8:11],a[168:171],v[136:139]\nv_exp_f32 v167,v167\nM v[136:139],a[12:15],a[172:175],v[136:139]\nv_cvt_pk_bf16_f32 v213,v166,v167\nv_exp_f32 v176,v176\n" \
"M v[140:143],a[12:15],a[188:191],v[236:239]\nv_exp_f32 v177,v177\nM v[140:143],a[8:11],a[184:187],v[140:143]\nv_exp_f32 v178,v178\nv_cvt_pk_bf16_f32 v210,v176,v177\nM v[140:143],a[4:7],a[180:183],v[140:143]\nv_exp_f32 v179,v179\nM v[140:143],a[0:3],a[176:179],v[140:143]\n" \
"v_cvt_pk_bf16_f32 v211,v178,v179\nv_exp_f32 v180,v180\nM v[152:155],a[16:19],a[160:163],v[232:235]\nv_exp_f32 v181,v181\nM v[152:155],a[20:23],a[164:167],v[152:155]\nv_exp_f32 v182,v182\nv_cvt_pk_bf16_f32 v214,v180,v181\nM v[152:155],a[24:27],a[168:171],v[152:155]\n" \
"s_waitcnt vmcnt(1)\ns_barrier\nM v[152:155],a[28:31],a[172:175],v[152:155]\nv_exp_f32 v183,v183\nv_cndmask_b32 v169,v169,v252,s[84:85]\nM v[156:159],a[28:31],a[188:191],v[236:239]\nv_cvt_pk_bf16_f32 v215,v182,v183\nv_cndmask_b32 v170,v170,v252,s[86:87]\n" \
"M v[156:159],a[24:27],a[184:187],v[156:159]\nv_cndmask_b32 v171,v171,v252,s[88:89]\nv_exp_f32 v168,v168\nM v[156:159],a[20:23],a[180:183],v[156:159]\nv_exp_f32 v169,v169\nds_read_b128 a[64:67],v241 offset:32768\nM v[156:159],a[16:19],a[176:179],v[156:159]\nv_cvt_pk_bf16_f32 v216,v168,v169\n" \
"s_waitcnt lgkmcnt(1)\nM v[160:163],a[32:35],a[128:131],v[224:227]\nv_exp_f32 v170,v170\nds_read_b128 a[96:99],v241 offset:40960\nM v[160:163],a[36:39],a[132:135],v[160:163]\nv_exp_f32 v171,v171\nM v[160:163],a[40:43],a[136:139],v[160:163]\nv_cvt_pk_bf16_f32 v217,v170,v171\n" \
"v_exp_f32 v172,v172\nM v[160:163],a[44:47],a[140:143],v[160:163]\nv_exp_f32 v173,v173\nM v[164:167],a[44:47],a[156:159],v[228:231]\nv_exp_f32 v174,v174\nv_cvt_pk_bf16_f32 v220,v172,v173\nM v[164:167],a[40:43],a[152:155],v[164:167]\nv_exp_f32 v175,v175\n" \
"M v[164:167],a[36:39],a[148:151],v[164:167]\nv_cvt_pk_bf16_f32 v221,v174,v175\nds_read_b128 a[68:71],v241 offset:33792\nds_read_b128 a[100:103],v241 offset:41984\nM v[164:167],a[32:35],a[144:147],v[164:167]\nds_read_b128 a[72:75],v241 offset:34816\nv_cndmask_b32 v188,v188,v252,s[82:83]\nM v[176:179],a[48:51],a[128:131],v[224:227]\n" \
"v_cndmask_b32 v189,v189,v252,s[84:85]\nv_cndmask_b32 v190,v190,v252,s[86:87]\nv_cndmask_b32 v191,v191,v252,s[88:89]\nM v[176:179],a[52:55],a[132:135],v[176:179]\nv_exp_f32 v188,v188\nM v[176:179],a[56:59],a[136:139],v[176:179]\nv_exp_f32 v189,v189\nds_read_b128 a[104:107],v241 offset:43008\n" \
"M v[176:179],a[60:63],a[140:143],v[176:179]\nv_exp_f32 v190,v190\nv_cvt_pk_bf16_f32 v222,v188,v189\nM v[180:183],a[60:63],a[156:159],v[228:231]\nv_exp_f32 v191,v191\nM v[180:183],a[56:59],a[152:155],v[180:183]\nv_cvt_pk_bf16_f32 v223,v190,v191\nds_read_b128 a[76:79],v241 offset:35840\n" \
"ds_read_b128 a[108:111],v241 offset:44032\nM v[180:183],a[52:55],a[148:151],v[180:183]\nds_read_b128 a[80:83],v241 offset:36864\ns_waitcnt vmcnt(0)\nv_cmp_eq_u32 vcc,0,v248\nM v[180:183],a[48:51],a[144:147],v[180:183]\nds_read_b128 a[112:115],v241 offset:45056\ns_cbranch_vccnz .Lps2\n" \
".Lpb2:\ns_add_u32 m0,s64,65536\nM v[168:171],a[32:35],a[160:163],v[232:235]\nbuffer_load_dwordx4 v240,s[48:51],s36 offen offset:0 sc1 lds\nM v[168:171],a[36:39],a[164:167],v[168:171]\nds_read_b128 a[84:87],v241 offset:37888\nv_exp_f32 v128,v128\nM v[168:171],a[40:43],a[168:171],v[168:171]\n" \
"ds_read_b128 a[116:119],v241 offset:46080\nds_read_b128 a[88:91],v241 offset:38912\nM v[168:171],a[44:47],a[172:175],v[168:171]\nds_read_b128 a[120:123],v241 offset:47104\nv_exp_f32 v129,v129\nM v[172:175],a[44:47],a[188:191],v[236:239]\nbuffer_load_dwordx4 v240,s[48:51],s36 offen offset:1024 sc1 lds\nM v[172:175],a[40:43],a[184:187],v[172:175]\n" \
"ds_read_b128 a[92:95],v241 offset:39936\nv_exp_f32 v130,v130\nM v[172:175],a[36:39],a[180:183],v[172:175]\nds_read_b128 a[124:127],v241 offset:48128\nds_read_b128 a[0:3],v240 offset:0\nM v[172:175],a[32:35],a[176:179],v[172:175]\nds_read_b128 a[4:7],v240 offset:1024\nv_exp_f32 v131,v131\n" \
"M v[184:187],a[48:51],a[160:163],v[232:235]\nds_read_b128 a[8:11],v240 offset:2048\nbuffer_load_dwordx4 v240,s[48:51],s36 offen offset:2048 sc1 lds\nM v[184:187],a[52:55],a[164:167],v[184:187]\nv_exp_f32 v132,v132\nM v[184:187],a[56:59],a[168:171],v[184:187]\nds_read_b128 a[12:15],v240 offset:3072\nv_exp_f32 v133,v133\n" \
"M v[184:187],a[60:63],a[172:175],v[184:187]\nds_read_b128 a[16:19],v240 offset:4096\nds_read_b128 a[20:23],v240 offset:5120\nM v[188:191],a[60:63],a[188:191],v[236:239]\nds_read_b128 a[24:27],v240 offset:6144\nbuffer_load_dwordx4 v240,s[48:51],s36 offen offset:3072 sc1 lds\nM v[188:191],a[56:59],a[184:187],v[188:191]\nds_read_b128 a[28:31],v240 offset:7168\n" \
"s_add_u32 m0,s64,16384\nM v[188:191],a[52:55],a[180:183],v[188:191]\nds_read_b128 a[32:35],v240 offset:8192\nv_exp_f32 v134,v134\nM v[188:191],a[48:51],a[176:179],v[188:191]\nds_read_b128 a[36:39],v240 offset:9216\ns_waitcnt lgkmcnt(9)\nM v[0:3],a[64:67],v[192:195],v[0:3]\n" \
"ds_read_b128 a[40:43],v240 offset:10240\nbuffer_load_dwordx4 v240,s[44:47],s65 offen offset:0 sc1 lds\nM v[0:3],a[96:99],v[208:211],v[0:3]\nv_exp_f32 v135,v135\nM v[8:11],a[68:71],v[192:195],v[8:11]\nds_read_b128 a[44:47],v240 offset:11264\nv_exp_f32 v144,v144\nM v[8:11],a[100:103],v[208:211],v[8:11]\n" \
"ds_read_b128 a[48:51],v240 offset:12288\nds_read_b128 a[52:55],v240 offset:13312\nM v[16:19],a[72:75],v[192:195],v[16:19]\nbuffer_load_dwordx4 v240,s[44:47],s65 offen offset:1024 sc1 lds\nds_read_b128 a[56:59],v240 offset:14336\nM v[16:19],a[104:107],v[208:211],v[16:19]\nds_read_b128 a[60:63],v240 offset:15360\nv_exp_f32 v145,v145\n" \
"M v[24:27],a[76:79],v[192:195],v[24:27]\nv_exp_f32 v146,v146\nM v[24:27],a[108:111],v[208:211],v[24:27]\nv_exp_f32 v147,v147\nM v[32:35],a[80:83],v[192:195],v[32:35]\nbuffer_load_dwordx4 v240,s[44:47],s65 offen offset:2048 sc1 lds\nM v[32:35],a[112:115],v[208:211],v[32:35]\nv_exp_f32 v148,v148\n" \
"M v[40:43],a[84:87],v[192:195],v[40:43]\nv_exp_f32 v149,v149\nM v[40:43],a[116:119],v[208:211],v[40:43]\nv_exp_f32 v150,v150\nM v[48:51],a[88:91],v[192:195],v[48:51]\nbuffer_load_dwordx4 v240,s[44:47],s65 offen offset:3072 sc1 lds\nM v[48:51],a[120:123],v[208:211],v[48:51]\ns_add_u32 m0,s64,81920\n" \
"v_exp_f32 v151,v151\nM v[56:59],a[92:95],v[192:195],v[56:59]\nv_exp_f32 v136,v136\nM v[56:59],a[124:127],v[208:211],v[56:59]\nv_exp_f32 v137,v137\nM a[192:195],a[208:211],v[192:195],a[192:195]\nbuffer_load_dwordx4 v240,s[48:51],s65 offen offset:0 sc1 lds\nM a[192:195],a[208:211],v[208:211],a[192:195]\n" \
"v_exp_f32 v138,v138\nv_cvt_pk_bf16_f32 v192,v128,v129\nM v[4:7],a[64:67],v[196:199],v[4:7]\nv_exp_f32 v139,v139\nM v[4:7],a[96:99],v[212:215],v[4:7]\nv_exp_f32 v152,v152\nv_cvt_pk_bf16_f32 v193,v130,v131\nM v[12:15],a[68:71],v[196:199],v[12:15]\n" \
"buffer_load_dwordx4 v240,s[48:51],s65 offen offset:1024 sc1 lds\nv_cvt_pk_bf16_f32 v194,v144,v145\nM v[12:15],a[100:103],v[212:215],v[12:15]\nv_exp_f32 v153,v153\nM v[20:23],a[72:75],v[196:199],v[20:23]\nv_exp_f32 v154,v154\nv_cvt_pk_bf16_f32 v195,v146,v147\nM v[20:23],a[104:107],v[212:215],v[20:23]\n" \
"v_exp_f32 v155,v155\nM v[28:31],a[76:79],v[196:199],v[28:31]\nbuffer_load_dwordx4 v240,s[48:51],s65 offen offset:2048 sc1 lds\nM v[28:31],a[108:111],v[212:215],v[28:31]\nv_exp_f32 v160,v160\nM v[36:39],a[80:83],v[196:199],v[36:39]\ns_barrier\nv_exp_f32 v161,v161\n" \
"M v[36:39],a[112:115],v[212:215],v[36:39]\nv_exp_f32 v162,v162\nM v[44:47],a[84:87],v[196:199],v[44:47]\nbuffer_load_dwordx4 v240,s[48:51],s65 offen offset:3072 sc1 lds\ns_add_u32 m0,s64,32768\nM v[44:47],a[116:119],v[212:215],v[44:47]\nv_exp_f32 v163,v163\nM v[52:55],a[88:91],v[196:199],v[52:55]\n" \
"v_exp_f32 v176,v176\nv_cvt_pk_bf16_f32 v208,v160,v161\nM v[52:55],a[120:123],v[212:215],v[52:55]\nv_exp_f32 v177,v177\nM v[60:63],a[92:95],v[196:199],v[60:63]\nbuffer_load_dwordx4 v240,s[44:47],s66 offen offset:0 sc1 lds\nv_cvt_pk_bf16_f32 v209,v162,v163\nM v[60:63],a[124:127],v[212:215],v[60:63]\n" \
"v_exp_f32 v178,v178\nv_cvt_pk_bf16_f32 v210,v176,v177\nM a[196:199],a[208:211],v[196:199],a[196:199]\nv_exp_f32 v179,v179\nM a[196:199],a[208:211],v[212:215],a[196:199]\nv_exp_f32 v140,v140\nv_cvt_pk_bf16_f32 v196,v132,v133\nM v[64:67],a[64:67],v[200:203],0\n" \
"buffer_load_dwordx4 v240,s[44:47],s66 offen offset:1024 sc1 lds\nM v[64:67],a[96:99],v[216:219],v[64:67]\nv_exp_f32 v141,v141\nv_cvt_pk_bf16_f32 v197,v134,v135\nM v[72:75],a[68:71],v[200:203],0\nv_exp_f32 v142,v142\nM v[72:75],a[100:103],v[216:219],v[72:75]\nv_exp_f32 v143,v143\n" \
"v_cvt_pk_bf16_f32 v198,v148,v149\nM v[80:83],a[72:75],v[200:203],0\nbuffer_load_dwordx4 v240,s[44:47],s66 offen offset:2048 sc1 lds\nM v[80:83],a[104:107],v[216:219],v[80:83]\nv_exp_f32 v156,v156\nv_cvt_pk_bf16_f32 v199,v150,v151\nM v[88:91],a[76:79],v[200:203],0\nv_exp_f32 v157,v157\n" \
"M v[88:91],a[108:111],v[216:219],v[88:91]\nv_exp_f32 v158,v158\nv_cvt_pk_bf16_f32 v211,v178,v179\nM v[96:99],a[80:83],v[200:203],0\nbuffer_load_dwordx4 v240,s[44:47],s66 offen offset:3072 sc1 lds\nM v[96:99],a[112:115],v[216:219],v[96:99]\ns_add_u32 m0,s64,98304\nv_exp_f32 v159,v159\n" \
"M v[104:107],a[84:87],v[200:203],0\nv_exp_f32 v164,v164\nM v[104:107],a[116:119],v[216:219],v[104:107]\nv_exp_f32 v165,v165\nM v[112:115],a[88:91],v[200:203],0\nv_exp_f32 v166,v166\nM v[112:115],a[120:123],v[216:219],v[112:115]\nv_exp_f32 v167,v167\n" \
"M v[120:123],a[92:95],v[200:203],0\nv_exp_f32 v180,v180\nM v[120:123],a[124:127],v[216:219],v[120:123]\nv_exp_f32 v181,v181\nM a[200:203],a[208:211],v[200:203],0\nv_exp_f32 v182,v182\nv_cvt_pk_bf16_f32 v200,v136,v137\nM a[200:203],a[208:211],v[216:219],a[200:203]\n" \
"v_exp_f32 v183,v183\nM v[68:71],a[64:67],v[204:207],0\nv_exp_f32 v168,v168\nv_cvt_pk_bf16_f32 v201,v138,v139\nM v[68:71],a[96:99],v[220:223],v[68:71]\nv_exp_f32 v169,v169\nM v[76:79],a[68:71],v[204:207],0\nv_exp_f32 v170,v170\n" \
"v_cvt_pk_bf16_f32 v202,v152,v153\nM v[76:79],a[100:103],v[220:223],v[76:79]\nv_exp_f32 v171,v171\nM v[84:87],a[72:75],v[204:207],0\nv_cvt_pk_bf16_f32 v203,v154,v155\nbuffer_load_dwordx4 v240,s[48:51],s66 offen offset:0 sc1 lds\nM v[84:87],a[104:107],v[220:223],v[84:87]\nM v[92:95],a[76:79],v[204:207],0\n" \
"M v[92:95],a[108:111],v[220:223],v[92:95]\nM v[100:103],a[80:83],v[204:207],0\nbuffer_load_dwordx4 v240,s[48:51],s66 offen offset:1024 sc1 lds\nM v[100:103],a[112:115],v[220:223],v[100:103]\nM v[108:111],a[84:87],v[204:207],0\nM v[108:111],a[116:119],v[220:223],v[108:111]\nM v[116:119],a[88:91],v[204:207],0\nbuffer_load_dwordx4 v240,s[48:51],s66 offen offset:2048 sc1 lds\n" \
"M v[116:119],a[120:123],v[220:223],v[116:119]\nM v[124:127],a[92:95],v[204:207],0\nM v[124:127],a[124:127],v[220:223],v[124:127]\nM a[204:207],a[208:211],v[204:207],0\nbuffer_load_dwordx4 v240,s[48:51],s66 offen offset:3072 sc1 lds\nM a[204:207],a[208:211],v[220:223],a[204:207]\ns_cmp_lt_u32 s62,2\ns_cbranch_scc1 .Lx01\n" \
"s_branch .Ldual\n.Lpro1:\ns_barrier\nM v[64:67],a[0:3],a[128:131],0\nds_read_b128 a[64:67],v241 offset:0\nds_read_b128 a[96:99],v241 offset:8192\nM v[64:67],a[4:7],a[132:135],v[64:67]\nds_read_b128 a[68:71],v241 offset:1024\n" \
"s_mov_b32 m0,s64\nv_mov_b32 v202,0\nM v[64:67],a[8:11],a[136:139],v[64:67]\nbuffer_load_dwordx4 v240,s[44:47],s36 offen offset:0 sc1 lds\nM v[64:67],a[12:15],a[140:143],v[64:67]\nds_read_b128 a[100:103],v241 offset:9216\nds_read_b128 a[72:75],v241 offset:2048\nv_mov_b32 v203,0\n" \
"M v[68:71],a[12:15],a[156:159],0\nds_read_b128 a[12:15],v240 offset:35840\nM v[68:71],a[8:11],a[152:155],v[68:71]\nds_read_b128 a[8:11],v240 offset:34816\nM v[68:71],a[4:7],a[148:151],v[68:71]\nds_read_b128 a[4:7],v240 offset:33792\nbuffer_load_dwordx4 v240,s[44:47],s36 offen offset:1024 sc1 lds\nM v[68:71],a[0:3],a[144:147],v[68:71]\n" \
"ds_read_b128 a[0:3],v240 offset:32768\nv_max3_f32 v225,v64,v65,v66\nM v[72:75],a[16:19],a[128:131],0\nds_read_b128 a[104:107],v241 offset:10240\nM v[72:75],a[20:23],a[132:135],v[72:75]\nds_read_b128 a[76:79],v241 offset:3072\nM v[72:75],a[24:27],a[136:139],v[72:75]\nbuffer_load_dwordx4 v240,s[44:47],s36 offen offset:2048 sc1 lds\n" \
"M v[72:75],a[28:31],a[140:143],v[72:75]\nv_max3_f32 v229,v68,v69,v70\nds_read_b128 a[108:111],v241 offset:11264\nds_read_b128 a[80:83],v241 offset:4096\nM v[76:79],a[28:31],a[156:159],0\nds_read_b128 a[28:31],v240 offset:39936\nM v[76:79],a[24:27],a[152:155],v[76:79]\nds_read_b128 a[24:27],v240 offset:38912\n" \
"M v[76:79],a[20:23],a[148:151],v[76:79]\nds_read_b128 a[20:23],v240 offset:37888\nv_max3_f32 v225,v225,v67,v72\nv_max3_f32 v225,v225,v73,v74\nM v[76:79],a[16:19],a[144:147],v[76:79]\nds_read_b128 a[16:19],v240 offset:36864\nv_max_f32 v225,v225,v75\nv_cndmask_b32 v225,v224,v225,s[90:91]\n" \
"M v[80:83],a[32:35],a[128:131],0\nbuffer_load_dwordx4 v240,s[44:47],s36 offen offset:3072 sc1 lds\nM v[80:83],a[36:39],a[132:135],v[80:83]\nds_read_b128 a[112:115],v241 offset:12288\nds_read_b128 a[84:87],v241 offset:5120\nv_max3_f32 v229,v229,v71,v76\nM v[80:83],a[40:43],a[136:139],v[80:83]\nv_max3_f32 v229,v229,v77,v78\n" \
"v_max_f32 v229,v229,v79\nv_cndmask_b32 v229,v228,v229,s[90:91]\nM v[80:83],a[44:47],a[140:143],v[80:83]\nds_read_b128 a[116:119],v241 offset:13312\nv_permlane16_swap_b32 v225,v229\nM v[84:87],a[44:47],a[156:159],0\nv_max_f32 v225,v225,v229\nv_mov_b32 v229,v225\n" \
"ds_read_b128 a[44:47],v240 offset:44032\nM v[84:87],a[40:43],a[152:155],v[84:87]\nv_permlane32_swap_b32 v225,v229\nv_max_f32 v225,v225,v229\nM v[84:87],a[36:39],a[148:151],v[84:87]\nv_mov_b32 v229,v225\nds_read_b128 a[36:39],v240 offset:41984\nds_read_b128 a[40:43],v240 offset:43008\n" \
"M v[84:87],a[32:35],a[144:147],v[84:87]\nv_permlane16_swap_b32 v225,v229\nv_sub_f32 v224,0,v225\nv_sub_f32 v226,0,v225\nM v[92:95],a[60:63],a[156:159],0\nv_sub_f32 v227,0,v225\nv_sub_f32 v225,0,v225\nM v[92:95],a[56:59],a[152:155],v[92:95]\n" \
"v_sub_f32 v228,0,v229\nv_sub_f32 v230,0,v229\nv_sub_f32 v231,0,v229\nM v[92:95],a[52:55],a[148:151],v[92:95]\nv_sub_f32 v229,0,v229\nds_read_b128 a[32:35],v240 offset:40960\nds_read_b128 a[52:55],v240 offset:46080\nM v[92:95],a[48:51],a[144:147],v[92:95]\n" \
"ds_read_b128 a[48:51],v240 offset:45056\ns_waitcnt lgkmcnt(7)\nM v[136:139],a[0:3],a[160:163],0\nds_read_b128 a[56:59],v240 offset:47104\nv_add_f32 v64,v64,v224\nv_add_f32 v65,v65,v224\nM v[136:139],a[4:7],a[164:167],v[136:139]\nds_read_b128 a[60:63],v240 offset:48128\n" \
"v_add_f32 v66,v66,v224\nM v[136:139],a[8:11],a[168:171],v[136:139]\nv_add_f32 v67,v67,v224\nv_exp_f32 v64,v64\nM v[136:139],a[12:15],a[172:175],v[136:139]\nv_exp_f32 v65,v65\nv_cndmask_b32 v80,v80,v252,s[82:83]\nM v[140:143],a[12:15],a[188:191],0\n" \
"v_cndmask_b32 v81,v81,v252,s[84:85]\nv_add_f32 v72,v72,v224\nM v[140:143],a[8:11],a[184:187],v[140:143]\nv_exp_f32 v66,v66\nv_add_f32 v73,v73,v224\nM v[140:143],a[4:7],a[180:183],v[140:143]\nv_exp_f32 v67,v67\nv_add_f32 v74,v74,v224\n" \
"M v[140:143],a[0:3],a[176:179],v[140:143]\nv_add_f32 v75,v75,v224\nv_cndmask_b32 v82,v82,v252,s[86:87]\nM v[156:159],a[28:31],a[188:191],0\nv_cndmask_b32 v83,v83,v252,s[88:89]\nv_add_f32 v80,v80,v224\nv_add_f32 v81,v81,v224\nM v[156:159],a[24:27],a[184:187],v[156:159]\n" \
"v_exp_f32 v72,v72\nv_add_f32 v82,v82,v224\nM v[156:159],a[20:23],a[180:183],v[156:159]\nv_exp_f32 v73,v73\nM v[156:159],a[16:19],a[176:179],v[156:159]\nv_exp_f32 v74,v74\nv_add_f32 v83,v83,v224\nM v[128:131],a[0:3],a[128:131],v[224:227]\n" \
"v_exp_f32 v75,v75\nM v[128:131],a[4:7],a[132:135],v[128:131]\nv_exp_f32 v80,v80\nv_cvt_pk_bf16_f32 v64,v64,v65\nM v[128:131],a[8:11],a[136:139],v[128:131]\nv_exp_f32 v81,v81\nv_cvt_pk_bf16_f32 v65,v66,v67\nM v[128:131],a[12:15],a[140:143],v[128:131]\n" \
"v_exp_f32 v82,v82\nM v[132:135],a[12:15],a[156:159],v[228:231]\nv_exp_f32 v83,v83\nv_cvt_pk_bf16_f32 v66,v72,v73\nM v[132:135],a[8:11],a[152:155],v[132:135]\nv_cvt_pk_bf16_f32 v67,v74,v75\nv_cvt_pk_bf16_f32 v80,v80,v81\nv_cvt_pk_bf16_f32 v81,v82,v83\n" \
"M v[132:135],a[4:7],a[148:151],v[132:135]\nv_mov_b32 v82,0\nv_mov_b32 v83,0\nM v[132:135],a[0:3],a[144:147],v[132:135]\nds_read_b128 a[88:91],v241 offset:6144\nds_read_b128 a[120:123],v241 offset:14336\nv_add_f32 v68,v68,v228\nM v[144:147],a[16:19],a[128:131],v[224:227]\n" \
"ds_read_b128 a[92:95],v241 offset:7168\nv_add_f32 v69,v69,v228\nv_add_f32 v70,v70,v228\nM v[144:147],a[20:23],a[132:135],v[144:147]\nds_read_b128 a[124:127],v241 offset:15360\nv_add_f32 v71,v71,v228\nM v[144:147],a[24:27],a[136:139],v[144:147]\nv_exp_f32 v68,v68\n" \
"v_add_f32 v76,v76,v228\nM v[144:147],a[28:31],a[140:143],v[144:147]\nv_exp_f32 v69,v69\nM v[148:151],a[28:31],a[156:159],v[228:231]\nv_exp_f32 v70,v70\nv_add_f32 v77,v77,v228\nM v[148:151],a[24:27],a[152:155],v[148:151]\nv_exp_f32 v71,v71\n" \
"v_add_f32 v78,v78,v228\nM v[148:151],a[20:23],a[148:151],v[148:151]\nv_add_f32 v79,v79,v228\nv_add_f32 v84,v84,v228\nM v[148:151],a[16:19],a[144:147],v[148:151]\nv_add_f32 v85,v85,v228\nv_cndmask_b32 v92,v92,v252,s[82:83]\ns_waitcnt lgkmcnt(4)\n" \
"M v[160:163],a[32:35],a[128:131],v[224:227]\nv_exp_f32 v76,v76\nv_cndmask_b32 v93,v93,v252,s[84:85]\nM v[160:163],a[36:39],a[132:135],v[160:163]\nv_exp_f32 v77,v77\nM v[160:163],a[40:43],a[136:139],v[160:163]\nv_exp_f32 v78,v78\nv_cndmask_b32 v94,v94,v252,s[86:87]\n" \
"M v[160:163],a[44:47],a[140:143],v[160:163]\nv_exp_f32 v79,v79\nM v[164:167],a[44:47],a[156:159],v[228:231]\nv_cndmask_b32 v95,v95,v252,s[88:89]\nv_add_f32 v86,v86,v228\nv_add_f32 v87,v87,v228\nM v[164:167],a[40:43],a[152:155],v[164:167]\nv_exp_f32 v84,v84\n" \
"v_cvt_pk_bf16_f32 v68,v68,v69\nM v[164:167],a[36:39],a[148:151],v[164:167]\nv_exp_f32 v85,v85\nM v[164:167],a[32:35],a[144:147],v[164:167]\nv_exp_f32 v86,v86\nv_add_f32 v92,v92,v228\nM v[176:179],a[48:51],a[128:131],v[224:227]\nv_exp_f32 v87,v87\n" \
"v_add_f32 v93,v93,v228\nM v[176:179],a[52:55],a[132:135],v[176:179]\nv_add_f32 v94,v94,v228\nv_add_f32 v95,v95,v228\nM v[176:179],a[56:59],a[136:139],v[176:179]\nbuffer_load_dword v248,v251,s[52:55],s80 offen sc1\nv_cvt_pk_bf16_f32 v69,v70,v71\nM v[176:179],a[60:63],a[140:143],v[176:179]\n" \
"v_exp_f32 v92,v92\nv_cvt_pk_bf16_f32 v70,v76,v77\nM v[180:183],a[60:63],a[156:159],v[228:231]\nv_exp_f32 v93,v93\nM v[180:183],a[56:59],a[152:155],v[180:183]\nv_exp_f32 v94,v94\nv_cvt_pk_bf16_f32 v71,v78,v79\nM v[180:183],a[52:55],a[148:151],v[180:183]\n" \
"v_exp_f32 v95,v95\nM v[180:183],a[48:51],a[144:147],v[180:183]\nv_cvt_pk_bf16_f32 v84,v84,v85\nv_cvt_pk_bf16_f32 v85,v86,v87\nv_cvt_pk_bf16_f32 v86,v92,v93\nM v[0:3],a[64:67],v[64:67],0\nv_cvt_pk_bf16_f32 v87,v94,v95\nds_read_b128 a[0:3],v240 offset:49152\n" \
"ds_read_b128 a[4:7],v240 offset:50176\nM v[0:3],a[96:99],v[80:83],v[0:3]\nds_read_b128 a[8:11],v240 offset:51200\nv_max3_f32 v237,v140,v141,v142\nM v[8:11],a[68:71],v[64:67],0\nds_read_b128 a[12:15],v240 offset:52224\nv_exp_f32 v128,v128\nM v[8:11],a[100:103],v[80:83],v[8:11]\n" \
"ds_read_b128 a[16:19],v240 offset:53248\nv_exp_f32 v129,v129\nM v[16:19],a[72:75],v[64:67],0\nv_exp_f32 v130,v130\nM v[16:19],a[104:107],v[80:83],v[16:19]\nv_exp_f32 v131,v131\nds_read_b128 a[20:23],v240 offset:54272\nM v[24:27],a[76:79],v[64:67],0\n" \
"v_cvt_pk_bf16_f32 v192,v128,v129\nv_cvt_pk_bf16_f32 v193,v130,v131\nds_read_b128 a[24:27],v240 offset:55296\nM v[24:27],a[108:111],v[80:83],v[24:27]\nds_read_b128 a[28:31],v240 offset:56320\nv_max3_f32 v237,v237,v143,v156\nM v[32:35],a[80:83],v[64:67],0\nv_exp_f32 v132,v132\n" \
"v_max3_f32 v233,v136,v137,v138\nM v[32:35],a[112:115],v[80:83],v[32:35]\nv_exp_f32 v133,v133\nM v[40:43],a[84:87],v[64:67],0\nv_exp_f32 v134,v134\nv_cvt_pk_bf16_f32 v196,v132,v133\nM v[40:43],a[116:119],v[80:83],v[40:43]\nv_exp_f32 v135,v135\n" \
"s_waitcnt lgkmcnt(4)\nM v[48:51],a[88:91],v[64:67],0\nv_cvt_pk_bf16_f32 v197,v134,v135\nv_max3_f32 v237,v237,v157,v158\nM v[48:51],a[120:123],v[80:83],v[48:51]\nv_exp_f32 v144,v144\nv_max_f32 v233,v233,v139\nM v[56:59],a[92:95],v[64:67],0\n" \
"v_exp_f32 v145,v145\nv_max_f32 v237,v237,v159\nM v[56:59],a[124:127],v[80:83],v[56:59]\nv_exp_f32 v146,v146\nM a[192:195],a[208:211],v[64:67],0\nv_exp_f32 v147,v147\nv_cvt_pk_bf16_f32 v194,v144,v145\nM a[192:195],a[208:211],v[80:83],a[192:195]\n" \
"v_cvt_pk_bf16_f32 v195,v146,v147\nv_cndmask_b32 v233,v232,v233,s[90:91]\nM v[4:7],a[64:67],v[68:71],0\nv_cndmask_b32 v237,v236,v237,s[90:91]\nv_exp_f32 v148,v148\nM v[4:7],a[96:99],v[84:87],v[4:7]\nv_permlane16_swap_b32 v233,v237\nv_max_f32 v233,v233,v237\n" \
"v_mov_b32 v237,v233\nM v[12:15],a[68:71],v[68:71],0\nv_exp_f32 v149,v149\nM v[12:15],a[100:103],v[84:87],v[12:15]\nv_exp_f32 v150,v150\nv_permlane32_swap_b32 v233,v237\nM v[20:23],a[72:75],v[68:71],0\nv_exp_f32 v151,v151\n" \
"v_cvt_pk_bf16_f32 v198,v148,v149\nM v[20:23],a[104:107],v[84:87],v[20:23]\nv_cvt_pk_bf16_f32 v199,v150,v151\nv_max_f32 v233,v233,v237\nM v[28:31],a[76:79],v[68:71],0\nv_mov_b32 v237,v233\nv_cndmask_b32 v136,v136,v252,s[82:83]\nv_cndmask_b32 v137,v137,v252,s[84:85]\n" \
"M v[28:31],a[108:111],v[84:87],v[28:31]\nv_permlane16_swap_b32 v233,v237\nv_cndmask_b32 v138,v138,v252,s[86:87]\nv_cndmask_b32 v139,v139,v252,s[88:89]\nM v[36:39],a[80:83],v[68:71],0\nv_sub_f32 v232,0,v233\nv_sub_f32 v234,0,v233\nM v[36:39],a[112:115],v[84:87],v[36:39]\n" \
"v_sub_f32 v235,0,v233\nv_add_f32 v136,v136,v232\nv_add_f32 v137,v137,v232\nM v[44:47],a[84:87],v[68:71],0\nv_add_f32 v138,v138,v232\nv_add_f32 v139,v139,v232\nM v[44:47],a[116:119],v[84:87],v[44:47]\nv_sub_f32 v233,0,v233\n" \
"v_exp_f32 v136,v136\nM v[52:55],a[88:91],v[68:71],0\nv_exp_f32 v137,v137\nv_sub_f32 v236,0,v237\nM v[52:55],a[120:123],v[84:87],v[52:55]\nv_exp_f32 v138,v138\nM v[60:63],a[92:95],v[68:71],0\nv_exp_f32 v139,v139\n" \
"v_cvt_pk_bf16_f32 v200,v136,v137\nM v[60:63],a[124:127],v[84:87],v[60:63]\nv_cvt_pk_bf16_f32 v201,v138,v139\nv_sub_f32 v238,0,v237\nv_sub_f32 v239,0,v237\nM a[196:199],a[208:211],v[68:71],0\nv_add_f32 v140,v140,v236\nv_add_f32 v141,v141,v236\n" \
"M a[196:199],a[208:211],v[84:87],a[196:199]\nv_add_f32 v142,v142,v236\nv_add_f32 v143,v143,v236\nv_sub_f32 v237,0,v237\nM v[128:131],a[0:3],a[128:131],v[224:227]\nv_exp_f32 v140,v140\nds_read_b128 a[32:35],v240 offset:57344\nM v[128:131],a[4:7],a[132:135],v[128:131]\n" \
"v_exp_f32 v141,v141\nM v[128:131],a[8:11],a[136:139],v[128:131]\nv_exp_f32 v142,v142\nv_cvt_pk_bf16_f32 v204,v140,v141\nM v[128:131],a[12:15],a[140:143],v[128:131]\nv_exp_f32 v143,v143\nM v[132:135],a[12:15],a[156:159],v[228:231]\nv_cvt_pk_bf16_f32 v205,v142,v143\n" \
"ds_read_b128 a[36:39],v240 offset:58368\nds_read_b128 a[40:43],v240 offset:59392\nM v[132:135],a[8:11],a[152:155],v[132:135]\nds_read_b128 a[44:47],v240 offset:60416\nv_cndmask_b32 v156,v156,v252,s[82:83]\nv_cndmask_b32 v157,v157,v252,s[84:85]\nM v[132:135],a[4:7],a[148:151],v[132:135]\nv_cndmask_b32 v158,v158,v252,s[86:87]\n" \
"v_cndmask_b32 v159,v159,v252,s[88:89]\nM v[132:135],a[0:3],a[144:147],v[132:135]\nv_add_f32 v156,v156,v236\nv_add_f32 v157,v157,v236\ns_waitcnt lgkmcnt(4)\nM v[144:147],a[16:19],a[128:131],v[224:227]\ns_waitcnt vmcnt(1)\ns_barrier\n" \
"v_add_f32 v158,v158,v236\nM v[144:147],a[20:23],a[132:135],v[144:147]\nv_add_f32 v159,v159,v236\nds_read_b128 a[48:51],v240 offset:61440\nM v[144:147],a[24:27],a[136:139],v[144:147]\nv_exp_f32 v156,v156\nds_read_b128 a[52:55],v240 offset:62464\nM v[144:147],a[28:31],a[140:143],v[144:147]\n" \
"v_exp_f32 v157,v157\nM v[148:151],a[28:31],a[156:159],v[228:231]\nv_exp_f32 v158,v158\nv_cvt_pk_bf16_f32 v206,v156,v157\nM v[148:151],a[24:27],a[152:155],v[148:151]\nv_exp_f32 v159,v159\nds_read_b128 a[56:59],v240 offset:63488\nM v[148:151],a[20:23],a[148:151],v[148:151]\n" \
"v_cvt_pk_bf16_f32 v207,v158,v159\nds_read_b128 a[60:63],v240 offset:64512\nM v[148:151],a[16:19],a[144:147],v[148:151]\nv_exp_f32 v160,v160\nds_read_b128 a[64:67],v241 offset:32768\nM v[136:139],a[0:3],a[160:163],v[232:235]\nv_exp_f32 v161,v161\nds_read_b128 a[96:99],v241 offset:40960\n" \
"M v[136:139],a[4:7],a[164:167],v[136:139]\nv_exp_f32 v162,v162\nM v[136:139],a[8:11],a[168:171],v[136:139]\nv_exp_f32 v163,v163\nv_cvt_pk_bf16_f32 v208,v160,v161\nM v[136:139],a[12:15],a[172:175],v[136:139]\nv_cvt_pk_bf16_f32 v209,v162,v163\nv_exp_f32 v164,v164\n" \
"M v[140:143],a[12:15],a[188:191],v[236:239]\nv_exp_f32 v165,v165\nM v[140:143],a[8:11],a[184:187],v[140:143]\nv_exp_f32 v166,v166\nv_cvt_pk_bf16_f32 v212,v164,v165\nM v[140:143],a[4:7],a[180:183],v[140:143]\nv_exp_f32 v167,v167\nM v[140:143],a[0:3],a[176:179],v[140:143]\n" \
"v_cvt_pk_bf16_f32 v213,v166,v167\nv_exp_f32 v176,v176\nM v[152:155],a[16:19],a[160:163],v[232:235]\nv_exp_f32 v177,v177\nds_read_b128 a[68:71],v241 offset:33792\nM v[152:155],a[20:23],a[164:167],v[152:155]\nv_exp_f32 v178,v178\nM v[152:155],a[24:27],a[168:171],v[152:155]\n" \
"v_exp_f32 v179,v179\nv_cvt_pk_bf16_f32 v210,v176,v177\nM v[152:155],a[28:31],a[172:175],v[152:155]\nv_cvt_pk_bf16_f32 v211,v178,v179\nv_exp_f32 v180,v180\nM v[156:159],a[28:31],a[188:191],v[236:239]\nv_exp_f32 v181,v181\nM v[156:159],a[24:27],a[184:187],v[156:159]\n" \
"v_exp_f32 v182,v182\nv_cvt_pk_bf16_f32 v214,v180,v181\nM v[156:159],a[20:23],a[180:183],v[156:159]\nv_exp_f32 v183,v183\nds_read_b128 a[100:103],v241 offset:41984\nM v[156:159],a[16:19],a[176:179],v[156:159]\nv_cvt_pk_bf16_f32 v215,v182,v183\ns_waitcnt lgkmcnt(2)\n" \
"M v[160:163],a[32:35],a[128:131],v[224:227]\nds_read_b128 a[72:75],v241 offset:34816\nds_read_b128 a[104:107],v241 offset:43008\ns_waitcnt vmcnt(0)\nv_cmp_eq_u32 vcc,0,v248\nM v[160:163],a[36:39],a[132:135],v[160:163]\nds_read_b128 a[76:79],v241 offset:35840\ns_cbranch_vccnz .Lps3\n" \
".Lpb3:\nM v[160:163],a[40:43],a[136:139],v[160:163]\nds_read_b128 a[108:111],v241 offset:44032\nv_exp_f32 v128,v128\nM v[160:163],a[44:47],a[140:143],v[160:163]\nds_read_b128 a[80:83],v241 offset:36864\nv_exp_f32 v129,v129\nM v[164:167],a[44:47],a[156:159],v[228:231]\n" \
"ds_read_b128 a[112:115],v241 offset:45056\ns_add_u32 m0,s64,65536\nM v[164:167],a[40:43],a[152:155],v[164:167]\nds_read_b128 a[84:87],v241 offset:37888\nbuffer_load_dwordx4 v240,s[48:51],s36 offen offset:0 sc1 lds\nM v[164:167],a[36:39],a[148:151],v[164:167]\nds_read_b128 a[116:119],v241 offset:46080\nv_exp_f32 v130,v130\n" \
"M v[164:167],a[32:35],a[144:147],v[164:167]\nv_exp_f32 v131,v131\nM v[176:179],a[48:51],a[128:131],v[224:227]\nds_read_b128 a[88:91],v241 offset:38912\nv_exp_f32 v132,v132\nM v[176:179],a[52:55],a[132:135],v[176:179]\nds_read_b128 a[120:123],v241 offset:47104\nds_read_b128 a[92:95],v241 offset:39936\n" \
"M v[176:179],a[56:59],a[136:139],v[176:179]\nds_read_b128 a[124:127],v241 offset:48128\nbuffer_load_dwordx4 v240,s[48:51],s36 offen offset:1024 sc1 lds\nM v[176:179],a[60:63],a[140:143],v[176:179]\nds_read_b128 a[0:3],v240 offset:0\nv_exp_f32 v133,v133\nM v[180:183],a[60:63],a[156:159],v[228:231]\nv_exp_f32 v134,v134\n" \
"M v[180:183],a[56:59],a[152:155],v[180:183]\nds_read_b128 a[4:7],v240 offset:1024\nv_exp_f32 v135,v135\nM v[180:183],a[52:55],a[148:151],v[180:183]\nds_read_b128 a[8:11],v240 offset:2048\nbuffer_load_dwordx4 v240,s[48:51],s36 offen offset:2048 sc1 lds\nM v[180:183],a[48:51],a[144:147],v[180:183]\nds_read_b128 a[12:15],v240 offset:3072\n" \
"ds_read_b128 a[16:19],v240 offset:4096\nM v[168:171],a[32:35],a[160:163],v[232:235]\nds_read_b128 a[20:23],v240 offset:5120\nv_exp_f32 v144,v144\nM v[168:171],a[36:39],a[164:167],v[168:171]\nds_read_b128 a[24:27],v240 offset:6144\nv_exp_f32 v145,v145\nM v[168:171],a[40:43],a[168:171],v[168:171]\n" \
"buffer_load_dwordx4 v240,s[48:51],s36 offen offset:3072 sc1 lds\nM v[168:171],a[44:47],a[172:175],v[168:171]\nds_read_b128 a[28:31],v240 offset:7168\nv_exp_f32 v146,v146\nM v[172:175],a[44:47],a[188:191],v[236:239]\nv_exp_f32 v147,v147\nM v[172:175],a[40:43],a[184:187],v[172:175]\ns_add_u32 m0,s64,16384\n" \
"v_exp_f32 v148,v148\nM v[172:175],a[36:39],a[180:183],v[172:175]\nbuffer_load_dwordx4 v240,s[44:47],s65 offen offset:0 sc1 lds\nM v[172:175],a[32:35],a[176:179],v[172:175]\nds_read_b128 a[32:35],v240 offset:8192\nds_read_b128 a[36:39],v240 offset:9216\nM v[184:187],a[48:51],a[160:163],v[232:235]\nds_read_b128 a[40:43],v240 offset:10240\n" \
"v_exp_f32 v149,v149\nM v[184:187],a[52:55],a[164:167],v[184:187]\nds_read_b128 a[44:47],v240 offset:11264\nv_exp_f32 v150,v150\nM v[184:187],a[56:59],a[168:171],v[184:187]\nbuffer_load_dwordx4 v240,s[44:47],s65 offen offset:1024 sc1 lds\nM v[184:187],a[60:63],a[172:175],v[184:187]\nv_exp_f32 v151,v151\n" \
"M v[188:191],a[60:63],a[188:191],v[236:239]\nv_exp_f32 v136,v136\nM v[188:191],a[56:59],a[184:187],v[188:191]\nv_exp_f32 v137,v137\nM v[188:191],a[52:55],a[180:183],v[188:191]\nbuffer_load_dwordx4 v240,s[44:47],s65 offen offset:2048 sc1 lds\nM v[188:191],a[48:51],a[176:179],v[188:191]\nds_read_b128 a[48:51],v240 offset:12288\n" \
"ds_read_b128 a[52:55],v240 offset:13312\nM v[0:3],a[64:67],v[192:195],v[0:3]\nds_read_b128 a[56:59],v240 offset:14336\nv_exp_f32 v138,v138\nM v[0:3],a[96:99],v[208:211],v[0:3]\nv_exp_f32 v139,v139\ns_waitcnt lgkmcnt(4)\nM v[8:11],a[68:71],v[192:195],v[8:11]\n" \
"buffer_load_dwordx4 v240,s[44:47],s65 offen offset:3072 sc1 lds\nM v[8:11],a[100:103],v[208:211],v[8:11]\ns_add_u32 m0,s64,81920\nv_exp_f32 v152,v152\nM v[16:19],a[72:75],v[192:195],v[16:19]\nds_read_b128 a[60:63],v240 offset:15360\nv_exp_f32 v153,v153\nM v[16:19],a[104:107],v[208:211],v[16:19]\n" \
"v_exp_f32 v154,v154\nM v[24:27],a[76:79],v[192:195],v[24:27]\nbuffer_load_dwordx4 v240,s[48:51],s65 offen offset:0 sc1 lds\nM v[24:27],a[108:111],v[208:211],v[24:27]\nv_exp_f32 v155,v155\nM v[32:35],a[80:83],v[192:195],v[32:35]\ns_barrier\nv_exp_f32 v160,v160\n" \
"M v[32:35],a[112:115],v[208:211],v[32:35]\nv_exp_f32 v161,v161\nM v[40:43],a[84:87],v[192:195],v[40:43]\nbuffer_load_dwordx4 v240,s[48:51],s65 offen offset:1024 sc1 lds\nM v[40:43],a[116:119],v[208:211],v[40:43]\nv_exp_f32 v162,v162\nM v[48:51],a[88:91],v[192:195],v[48:51]\nv_exp_f32 v163,v163\n" \
"M v[48:51],a[120:123],v[208:211],v[48:51]\nv_exp_f32 v176,v176\nM v[56:59],a[92:95],v[192:195],v[56:59]\nbuffer_load_dwordx4 v240,s[48:51],s65 offen offset:2048 sc1 lds\nM v[56:59],a[124:127],v[208:211],v[56:59]\nv_exp_f32 v177,v177\nM a[192:195],a[208:211],v[192:195],a[192:195]\nv_exp_f32 v178,v178\n" \
"M a[192:195],a[208:211],v[208:211],a[192:195]\nv_exp_f32 v179,v179\nv_cvt_pk_bf16_f32 v192,v128,v129\nM v[4:7],a[64:67],v[196:199],v[4:7]\nbuffer_load_dwordx4 v240,s[48:51],s65 offen offset:3072 sc1 lds\nM v[4:7],a[96:99],v[212:215],v[4:7]\ns_add_u32 m0,s64,32768\nv_exp_f32 v140,v140\n" \
"M v[12:15],a[68:71],v[196:199],v[12:15]\nv_exp_f32 v141,v141\nv_cvt_pk_bf16_f32 v193,v130,v131\nM v[12:15],a[100:103],v[212:215],v[12:15]\nv_exp_f32 v142,v142\nM v[20:23],a[72:75],v[196:199],v[20:23]\nbuffer_load_dwordx4 v240,s[44:47],s66 offen offset:0 sc1 lds\nv_cvt_pk_bf16_f32 v194,v144,v145\n" \
"M v[20:23],a[104:107],v[212:215],v[20:23]\nv_exp_f32 v143,v143\nv_cvt_pk_bf16_f32 v195,v146,v147\nM v[28:31],a[76:79],v[196:199],v[28:31]\nv_exp_f32 v156,v156\nM v[28:31],a[108:111],v[212:215],v[28:31]\nv_exp_f32 v157,v157\nv_cvt_pk_bf16_f32 v208,v160,v161\n" \
"M v[36:39],a[80:83],v[196:199],v[36:39]\nbuffer_load_dwordx4 v240,s[44:47],s66 offen offset:1024 sc1 lds\nM v[36:39],a[112:115],v[212:215],v[36:39]\nv_exp_f32 v158,v158\nv_cvt_pk_bf16_f32 v209,v162,v163\nM v[44:47],a[84:87],v[196:199],v[44:47]\nv_exp_f32 v159,v159\nv_cvt_pk_bf16_f32 v210,v176,v177\n" \
"M v[44:47],a[116:119],v[212:215],v[44:47]\nv_exp_f32 v164,v164\nM v[52:55],a[88:91],v[196:199],v[52:55]\nbuffer_load_dwordx4 v240,s[44:47],s66 offen offset:2048 sc1 lds\nv_cvt_pk_bf16_f32 v211,v178,v179\nM v[52:55],a[120:123],v[212:215],v[52:55]\nv_exp_f32 v165,v165\nM v[60:63],a[92:95],v[196:199],v[60:63]\n" \
"v_exp_f32 v166,v166\nM v[60:63],a[124:127],v[212:215],v[60:63]\nv_exp_f32 v167,v167\nM a[196:199],a[208:211],v[196:199],a[196:199]\nbuffer_load_dwordx4 v240,s[44:47],s66 offen offset:3072 sc1 lds\ns_add_u32 m0,s64,98304\nM a[196:199],a[208:211],v[212:215],a[196:199]\nv_exp_f32 v180,v180\n" \
"M v[64:67],a[64:67],v[200:203],0\nv_exp_f32 v181,v181\nv_cvt_pk_bf16_f32 v196,v132,v133\nM v[72:75],a[68:71],v[200:203],0\nv_exp_f32 v182,v182\nM v[80:83],a[72:75],v[200:203],0\nv_exp_f32 v183,v183\nv_cvt_pk_bf16_f32 v197,v134,v135\n" \
"M v[88:91],a[76:79],v[200:203],0\nv_exp_f32 v168,v168\nv_cvt_pk_bf16_f32 v198,v148,v149\nM v[96:99],a[80:83],v[200:203],0\nv_exp_f32 v169,v169\nM v[104:107],a[84:87],v[200:203],0\nv_exp_f32 v170,v170\nv_cvt_pk_bf16_f32 v199,v150,v151\n" \
"M v[112:115],a[88:91],v[200:203],0\nv_exp_f32 v171,v171\nM v[120:123],a[92:95],v[200:203],0\nbuffer_load_dwordx4 v240,s[48:51],s66 offen offset:0 sc1 lds\nM a[200:203],a[208:211],v[200:203],0\nv_cvt_pk_bf16_f32 v200,v136,v137\nv_cvt_pk_bf16_f32 v201,v138,v139\nv_cvt_pk_bf16_f32 v202,v152,v153\n" \
"M v[68:71],a[64:67],v[204:207],0\nv_cvt_pk_bf16_f32 v203,v154,v155\nM v[76:79],a[68:71],v[204:207],0\nM v[84:87],a[72:75],v[204:207],0\nbuffer_load_dwordx4 v240,s[48:51],s66 offen offset:1024 sc1 lds\nM v[92:95],a[76:79],v[204:207],0\nM v[100:103],a[80:83],v[204:207],0\nM v[108:111],a[84:87],v[204:207],0\n" \
"M v[116:119],a[88:91],v[204:207],0\nbuffer_load_dwordx4 v240,s[48:51],s66 offen offset:2048 sc1 lds\nM v[124:127],a[92:95],v[204:207],0\nbuffer_load_dwordx4 v240,s[48:51],s66 offen offset:3072 sc1 lds\nM a[204:207],a[208:211],v[204:207],0\ns_cmp_lt_u32 s62,2\ns_cbranch_scc1 .Lx01\ns_branch .Ldual\n" \
".Lp23:\ns_cmp_eq_u32 s7,2\ns_cbranch_scc0 .Lpro3\n.Lpro2:\ns_barrier\nM v[64:67],a[0:3],a[128:131],0\nds_read_b128 a[64:67],v241 offset:0\nds_read_b128 a[96:99],v241 offset:8192\n" \
"M v[64:67],a[4:7],a[132:135],v[64:67]\nds_read_b128 a[68:71],v241 offset:1024\ns_mov_b32 m0,s64\nv_mov_b32 v250,139264\nM v[64:67],a[8:11],a[136:139],v[64:67]\nds_read_b128 a[100:103],v241 offset:9216\nbuffer_load_dwordx4 v240,s[44:47],s36 offen offset:0 sc1 lds\nM v[64:67],a[12:15],a[140:143],v[64:67]\n" \
"ds_read_b128 a[72:75],v241 offset:2048\nv_mov_b32 v184,0xff800000\nv_mov_b32 v185,0xff800000\nM v[68:71],a[12:15],a[156:159],0\nds_read_b128 a[12:15],v240 offset:19456\nv_mov_b32 v186,0xff800000\nv_mov_b32 v187,0xff800000\nM v[68:71],a[8:11],a[152:155],v[68:71]\n" \
"ds_read_b128 a[8:11],v240 offset:18432\nv_max3_f32 v225,v64,v65,v66\nM v[68:71],a[4:7],a[148:151],v[68:71]\nds_read_b128 a[4:7],v240 offset:17408\nbuffer_load_dwordx4 v240,s[44:47],s36 offen offset:1024 sc1 lds\nM v[68:71],a[0:3],a[144:147],v[68:71]\nds_read_b128 a[0:3],v240 offset:16384\nM v[72:75],a[16:19],a[128:131],0\n" \
"ds_read_b128 a[104:107],v241 offset:10240\nM v[72:75],a[20:23],a[132:135],v[72:75]\nds_read_b128 a[76:79],v241 offset:3072\nM v[72:75],a[24:27],a[136:139],v[72:75]\nds_read_b128 a[108:111],v241 offset:11264\nbuffer_load_dwordx4 v240,s[44:47],s36 offen offset:2048 sc1 lds\nM v[72:75],a[28:31],a[140:143],v[72:75]\nv_max3_f32 v229,v68,v69,v70\n" \
"ds_write_b128 v250,a[244:247]\nM v[76:79],a[28:31],a[156:159],0\nds_read_b128 a[28:31],v240 offset:23552\nM v[76:79],a[24:27],a[152:155],v[76:79]\nds_read_b128 a[24:27],v240 offset:22528\nM v[76:79],a[20:23],a[148:151],v[76:79]\nds_read_b128 a[20:23],v240 offset:21504\nv_max3_f32 v225,v225,v67,v72\n" \
"v_max3_f32 v225,v225,v73,v74\nM v[76:79],a[16:19],a[144:147],v[76:79]\nv_max_f32 v225,v225,v75\nv_cndmask_b32 v225,v224,v225,s[90:91]\nds_read_b128 a[16:19],v240 offset:20480\nM v[80:83],a[32:35],a[128:131],0\nds_read_b128 a[80:83],v241 offset:4096\nbuffer_load_dwordx4 v240,s[44:47],s36 offen offset:3072 sc1 lds\n" \
"M v[80:83],a[36:39],a[132:135],v[80:83]\nds_read_b128 a[112:115],v241 offset:12288\nv_max3_f32 v229,v229,v71,v76\nv_max3_f32 v229,v229,v77,v78\nM v[80:83],a[40:43],a[136:139],v[80:83]\nv_max_f32 v229,v229,v79\nv_cndmask_b32 v229,v228,v229,s[90:91]\nds_read_b128 a[84:87],v241 offset:5120\n" \
"M v[80:83],a[44:47],a[140:143],v[80:83]\nv_permlane16_swap_b32 v225,v229\nv_max_f32 v225,v225,v229\nv_mov_b32 v229,v225\nM v[84:87],a[44:47],a[156:159],0\nds_read_b128 a[116:119],v241 offset:13312\nv_permlane32_swap_b32 v225,v229\nv_max_f32 v225,v225,v229\n" \
"M v[84:87],a[40:43],a[152:155],v[84:87]\nv_mov_b32 v229,v225\nds_read_b128 a[88:91],v241 offset:6144\nds_read_b128 a[120:123],v241 offset:14336\nM v[84:87],a[36:39],a[148:151],v[84:87]\nv_permlane16_swap_b32 v225,v229\nv_sub_f32 v224,0,v225\nv_sub_f32 v226,0,v225\n" \
"M v[84:87],a[32:35],a[144:147],v[84:87]\nv_sub_f32 v227,0,v225\nv_sub_f32 v225,0,v225\nv_sub_f32 v228,0,v229\nM v[88:91],a[48:51],a[128:131],0\nv_sub_f32 v230,0,v229\nv_sub_f32 v231,0,v229\nM v[88:91],a[52:55],a[132:135],v[88:91]\n" \
"v_sub_f32 v229,0,v229\nv_add_f32 v64,v64,v224\nv_add_f32 v65,v65,v224\nM v[88:91],a[56:59],a[136:139],v[88:91]\nv_add_f32 v66,v66,v224\nv_add_f32 v67,v67,v224\nv_add_f32 v72,v72,v224\nM v[88:91],a[60:63],a[140:143],v[88:91]\n" \
"v_exp_f32 v64,v64\nv_add_f32 v73,v73,v224\nM v[92:95],a[60:63],a[156:159],0\nv_exp_f32 v65,v65\nv_add_f32 v74,v74,v224\nM v[92:95],a[56:59],a[152:155],v[92:95]\nv_exp_f32 v66,v66\nv_add_f32 v75,v75,v224\n" \
"M v[92:95],a[52:55],a[148:151],v[92:95]\nv_exp_f32 v67,v67\nv_add_f32 v80,v80,v224\nM v[92:95],a[48:51],a[144:147],v[92:95]\nv_add_f32 v81,v81,v224\nv_add_f32 v82,v82,v224\ns_waitcnt lgkmcnt(0)\nM v[96:99],a[0:3],a[128:131],v[224:227]\n" \
"v_exp_f32 v72,v72\nv_add_f32 v83,v83,v224\nM v[96:99],a[4:7],a[132:135],v[96:99]\nv_exp_f32 v73,v73\nv_cvt_pk_bf16_f32 v64,v64,v65\nM v[96:99],a[8:11],a[136:139],v[96:99]\nv_exp_f32 v74,v74\nv_add_f32 v88,v88,v224\n" \
"M v[96:99],a[12:15],a[140:143],v[96:99]\nv_exp_f32 v75,v75\nv_add_f32 v89,v89,v224\nM v[100:103],a[12:15],a[156:159],v[228:231]\nv_exp_f32 v80,v80\nv_add_f32 v90,v90,v224\nM v[100:103],a[8:11],a[152:155],v[100:103]\nv_exp_f32 v81,v81\n" \
"M v[100:103],a[4:7],a[148:151],v[100:103]\nv_exp_f32 v82,v82\nv_add_f32 v91,v91,v224\nM v[100:103],a[0:3],a[144:147],v[100:103]\nv_exp_f32 v83,v83\nv_cvt_pk_bf16_f32 v65,v66,v67\nM v[108:111],a[28:31],a[156:159],v[228:231]\nv_exp_f32 v88,v88\n" \
"v_cvt_pk_bf16_f32 v66,v72,v73\nM v[108:111],a[24:27],a[152:155],v[108:111]\nv_exp_f32 v89,v89\nv_cvt_pk_bf16_f32 v67,v74,v75\nM v[108:111],a[20:23],a[148:151],v[108:111]\nv_exp_f32 v90,v90\nv_exp_f32 v91,v91\nv_cvt_pk_bf16_f32 v80,v80,v81\n" \
"M v[108:111],a[16:19],a[144:147],v[108:111]\nv_cvt_pk_bf16_f32 v81,v82,v83\nv_cvt_pk_bf16_f32 v82,v88,v89\nv_cvt_pk_bf16_f32 v83,v90,v91\nM v[0:3],a[64:67],v[64:67],0\nds_read_b128 a[92:95],v241 offset:7168\nds_read_b128 a[124:127],v241 offset:15360\nv_add_f32 v68,v68,v228\n" \
"M v[0:3],a[96:99],v[80:83],v[0:3]\nv_add_f32 v69,v69,v228\nv_add_f32 v70,v70,v228\nv_add_f32 v71,v71,v228\nM v[8:11],a[68:71],v[64:67],0\nv_exp_f32 v68,v68\nv_add_f32 v76,v76,v228\nM v[8:11],a[100:103],v[80:83],v[8:11]\n" \
"v_exp_f32 v69,v69\nv_add_f32 v77,v77,v228\nM v[16:19],a[72:75],v[64:67],0\nv_exp_f32 v70,v70\nv_add_f32 v78,v78,v228\nM v[16:19],a[104:107],v[80:83],v[16:19]\nv_exp_f32 v71,v71\nM v[24:27],a[76:79],v[64:67],0\n" \
"v_add_f32 v79,v79,v228\nv_add_f32 v84,v84,v228\nv_add_f32 v85,v85,v228\nM v[24:27],a[108:111],v[80:83],v[24:27]\nv_exp_f32 v76,v76\nv_add_f32 v86,v86,v228\nM v[32:35],a[80:83],v[64:67],0\nv_exp_f32 v77,v77\n" \
"v_add_f32 v87,v87,v228\nM v[32:35],a[112:115],v[80:83],v[32:35]\nv_exp_f32 v78,v78\nv_cvt_pk_bf16_f32 v68,v68,v69\nM v[40:43],a[84:87],v[64:67],0\nv_exp_f32 v79,v79\nv_add_f32 v92,v92,v228\nM v[40:43],a[116:119],v[80:83],v[40:43]\n" \
"v_exp_f32 v84,v84\nv_add_f32 v93,v93,v228\nM v[48:51],a[88:91],v[64:67],0\nv_exp_f32 v85,v85\nv_add_f32 v94,v94,v228\nM v[48:51],a[120:123],v[80:83],v[48:51]\nv_exp_f32 v86,v86\ns_waitcnt lgkmcnt(0)\n" \
"M v[56:59],a[92:95],v[64:67],0\nv_exp_f32 v87,v87\nv_add_f32 v95,v95,v228\nM v[56:59],a[124:127],v[80:83],v[56:59]\nv_cvt_pk_bf16_f32 v69,v70,v71\nv_exp_f32 v92,v92\nM a[192:195],a[208:211],v[64:67],0\nv_exp_f32 v93,v93\n" \
"v_exp_f32 v94,v94\nv_exp_f32 v95,v95\nv_cvt_pk_bf16_f32 v70,v76,v77\nv_cvt_pk_bf16_f32 v71,v78,v79\nv_cvt_pk_bf16_f32 v84,v84,v85\nM a[192:195],a[208:211],v[80:83],a[192:195]\nv_cvt_pk_bf16_f32 v85,v86,v87\nv_cvt_pk_bf16_f32 v86,v92,v93\n" \
"v_cvt_pk_bf16_f32 v87,v94,v95\nM v[4:7],a[64:67],v[68:71],0\nds_read_b128 a[0:3],v240 offset:32768\nds_read_b128 a[4:7],v240 offset:33792\nM v[4:7],a[96:99],v[84:87],v[4:7]\nds_read_b128 a[8:11],v240 offset:34816\nv_cndmask_b32 v96,v96,v252,s[82:83]\nv_cndmask_b32 v97,v97,v252,s[84:85]\n" \
"M v[12:15],a[68:71],v[68:71],0\nds_read_b128 a[12:15],v240 offset:35840\nv_cndmask_b32 v98,v98,v252,s[86:87]\nv_cndmask_b32 v99,v99,v252,s[88:89]\nM v[12:15],a[100:103],v[84:87],v[12:15]\nds_read_b128 a[16:19],v240 offset:36864\nv_exp_f32 v96,v96\nM v[20:23],a[72:75],v[68:71],0\n" \
"ds_read_b128 a[20:23],v240 offset:37888\nv_exp_f32 v97,v97\nM v[20:23],a[104:107],v[84:87],v[20:23]\nds_read_b128 a[24:27],v240 offset:38912\nv_exp_f32 v98,v98\nM v[28:31],a[76:79],v[68:71],0\nbuffer_load_dword v248,v251,s[52:55],s80 offen sc1\nds_read_b128 a[28:31],v240 offset:39936\n" \
"M v[28:31],a[108:111],v[84:87],v[28:31]\nds_read_b128 a[32:35],v240 offset:40960\nv_exp_f32 v99,v99\nM v[36:39],a[80:83],v[68:71],0\nds_read_b128 a[36:39],v240 offset:41984\nv_cvt_pk_bf16_f32 v96,v96,v97\nv_cvt_pk_bf16_f32 v97,v98,v99\nM v[36:39],a[112:115],v[84:87],v[36:39]\n" \
"ds_read_b128 a[40:43],v240 offset:43008\nv_mov_b32 v98,0\nv_mov_b32 v99,0\nM v[44:47],a[84:87],v[68:71],0\nds_read_b128 a[44:47],v240 offset:44032\nv_exp_f32 v100,v100\nM v[44:47],a[116:119],v[84:87],v[44:47]\nds_read_b128 a[48:51],v240 offset:45056\n" \
"v_exp_f32 v101,v101\nM v[52:55],a[88:91],v[68:71],0\nds_read_b128 a[52:55],v240 offset:46080\nv_cndmask_b32 v108,v108,v252,s[82:83]\nM v[52:55],a[120:123],v[84:87],v[52:55]\nds_read_b128 a[56:59],v240 offset:47104\nv_cndmask_b32 v109,v109,v252,s[84:85]\nv_cndmask_b32 v110,v110,v252,s[86:87]\n" \
"M v[60:63],a[92:95],v[68:71],0\nds_read_b128 a[60:63],v240 offset:48128\nv_cndmask_b32 v111,v111,v252,s[88:89]\nv_cvt_pk_bf16_f32 v100,v100,v101\nM v[60:63],a[124:127],v[84:87],v[60:63]\nds_read_b128 a[64:67],v241 offset:16384\nv_exp_f32 v102,v102\nM a[196:199],a[208:211],v[68:71],0\n" \
"ds_read_b128 a[68:71],v241 offset:17408\nv_exp_f32 v103,v103\nM a[196:199],a[208:211],v[84:87],a[196:199]\nds_read_b128 a[72:75],v241 offset:18432\nv_cvt_pk_bf16_f32 v101,v102,v103\ns_waitcnt lgkmcnt(8)\nM v[128:131],a[0:3],a[128:131],v[224:227]\nds_read_b128 a[76:79],v241 offset:19456\n" \
"v_exp_f32 v108,v108\nM v[128:131],a[4:7],a[132:135],v[128:131]\nds_read_b128 a[80:83],v241 offset:20480\nv_exp_f32 v109,v109\nM v[128:131],a[8:11],a[136:139],v[128:131]\nds_read_b128 a[84:87],v241 offset:21504\nv_exp_f32 v110,v110\nM v[128:131],a[12:15],a[140:143],v[128:131]\n" \
"ds_read_b128 a[88:91],v241 offset:22528\nv_exp_f32 v111,v111\nM v[132:135],a[12:15],a[156:159],v[228:231]\nds_read_b128 a[92:95],v241 offset:23552\nv_cvt_pk_bf16_f32 v102,v108,v109\nv_cvt_pk_bf16_f32 v103,v110,v111\nM v[132:135],a[8:11],a[152:155],v[132:135]\nds_read_b128 a[8:11],v240 offset:51200\n" \
"v_exp_f32 v128,v128\nM v[132:135],a[4:7],a[148:151],v[132:135]\nds_read_b128 a[4:7],v240 offset:50176\nv_exp_f32 v129,v129\nM v[132:135],a[0:3],a[144:147],v[132:135]\nds_read_b128 a[0:3],v240 offset:49152\nv_cvt_pk_bf16_f32 v192,v128,v129\nM v[144:147],a[16:19],a[128:131],v[224:227]\n" \
"ds_read_b128 a[12:15],v240 offset:52224\nv_exp_f32 v130,v130\nM v[144:147],a[20:23],a[132:135],v[144:147]\nv_exp_f32 v131,v131\nds_read_b128 a[96:99],v241 offset:40960\nM v[144:147],a[24:27],a[136:139],v[144:147]\nv_cvt_pk_bf16_f32 v193,v130,v131\nv_exp_f32 v132,v132\n" \
"M v[144:147],a[28:31],a[140:143],v[144:147]\nv_exp_f32 v133,v133\nds_read_b128 v[64:67],v250\nM v[148:151],a[28:31],a[156:159],v[228:231]\nds_read_b128 a[28:31],v240 offset:56320\nv_exp_f32 v134,v134\nM v[148:151],a[24:27],a[152:155],v[148:151]\nds_read_b128 a[24:27],v240 offset:55296\n" \
"v_exp_f32 v135,v135\nM v[148:151],a[20:23],a[148:151],v[148:151]\nds_read_b128 a[20:23],v240 offset:54272\nv_cvt_pk_bf16_f32 v196,v132,v133\nv_cvt_pk_bf16_f32 v197,v134,v135\nM v[148:151],a[16:19],a[144:147],v[148:151]\nds_read_b128 a[16:19],v240 offset:53248\nv_exp_f32 v144,v144\n" \
"M v[160:163],a[32:35],a[128:131],v[224:227]\nv_exp_f32 v145,v145\nds_read_b128 v[68:71],v250\nM v[160:163],a[36:39],a[132:135],v[160:163]\nv_exp_f32 v146,v146\nv_cvt_pk_bf16_f32 v194,v144,v145\nM v[160:163],a[40:43],a[136:139],v[160:163]\nv_exp_f32 v147,v147\n" \
"s_waitcnt lgkmcnt(6)\nM v[160:163],a[44:47],a[140:143],v[160:163]\nv_cvt_pk_bf16_f32 v195,v146,v147\nds_read_b128 v[72:75],v250\nM v[164:167],a[44:47],a[156:159],v[228:231]\nds_read_b128 a[44:47],v240 offset:60416\nv_exp_f32 v148,v148\nM v[164:167],a[40:43],a[152:155],v[164:167]\n" \
"ds_read_b128 a[40:43],v240 offset:59392\nv_exp_f32 v149,v149\nM v[164:167],a[36:39],a[148:151],v[164:167]\nds_read_b128 a[36:39],v240 offset:58368\nv_exp_f32 v150,v150\nM v[164:167],a[32:35],a[144:147],v[164:167]\nds_read_b128 a[32:35],v240 offset:57344\nv_exp_f32 v151,v151\n" \
"M v[176:179],a[48:51],a[128:131],v[224:227]\nv_cvt_pk_bf16_f32 v198,v148,v149\nv_cvt_pk_bf16_f32 v199,v150,v151\nds_read_b128 v[76:79],v250\nM v[176:179],a[52:55],a[132:135],v[176:179]\nv_exp_f32 v160,v160\nds_read_b128 v[80:83],v250\nM v[176:179],a[56:59],a[136:139],v[176:179]\n" \
"v_exp_f32 v161,v161\nds_read_b128 v[84:87],v250\nM v[176:179],a[60:63],a[140:143],v[176:179]\nv_exp_f32 v162,v162\nv_cvt_pk_bf16_f32 v208,v160,v161\nM v[180:183],a[60:63],a[156:159],v[228:231]\nv_exp_f32 v163,v163\nds_read_b128 a[60:63],v240 offset:64512\n" \
"M v[180:183],a[56:59],a[152:155],v[180:183]\nv_cvt_pk_bf16_f32 v209,v162,v163\nds_read_b128 a[56:59],v240 offset:63488\nds_read_b128 v[88:91],v250\nM v[180:183],a[52:55],a[148:151],v[180:183]\nds_read_b128 a[52:55],v240 offset:62464\nv_exp_f32 v164,v164\nM v[180:183],a[48:51],a[144:147],v[180:183]\n" \
"ds_read_b128 a[48:51],v240 offset:61440\nv_exp_f32 v165,v165\nM v[0:3],a[64:67],v[96:99],v[0:3]\nv_exp_f32 v166,v166\nM v[8:11],a[68:71],v[96:99],v[8:11]\nv_exp_f32 v167,v167\nv_cvt_pk_bf16_f32 v212,v164,v165\nM v[16:19],a[72:75],v[96:99],v[16:19]\n" \
"v_cvt_pk_bf16_f32 v213,v166,v167\nv_exp_f32 v176,v176\nM v[24:27],a[76:79],v[96:99],v[24:27]\nv_exp_f32 v177,v177\nds_read_b128 v[92:95],v250\nM v[32:35],a[80:83],v[96:99],v[32:35]\nv_exp_f32 v178,v178\nv_cvt_pk_bf16_f32 v210,v176,v177\n" \
"M v[40:43],a[84:87],v[96:99],v[40:43]\nv_exp_f32 v179,v179\nds_read_b128 v[104:107],v250\nM v[48:51],a[88:91],v[96:99],v[48:51]\ns_waitcnt vmcnt(0)\ns_barrier\nv_cvt_pk_bf16_f32 v211,v178,v179\nM v[56:59],a[92:95],v[96:99],v[56:59]\n" \
"v_exp_f32 v180,v180\nds_read_b128 v[108:111],v250\nM a[192:195],a[208:211],v[96:99],a[192:195]\nv_exp_f32 v181,v181\nds_read_b128 v[96:99],v250\nM v[4:7],a[64:67],v[100:103],v[4:7]\nv_exp_f32 v182,v182\nv_cvt_pk_bf16_f32 v214,v180,v181\n" \
"M v[12:15],a[68:71],v[100:103],v[12:15]\nv_exp_f32 v183,v183\nds_read_b128 a[64:67],v241 offset:32768\nM v[20:23],a[72:75],v[100:103],v[20:23]\nv_cvt_pk_bf16_f32 v215,v182,v183\nds_read_b128 a[68:71],v241 offset:33792\nds_read_b128 v[112:115],v250\nM v[28:31],a[76:79],v[100:103],v[28:31]\n" \
"ds_read_b128 v[116:119],v250\nv_cmp_eq_u32 vcc,0,v248\nM v[36:39],a[80:83],v[100:103],v[36:39]\nds_read_b128 v[120:123],v250\ns_cbranch_vccnz .Lps4\n.Lpb4:\ns_add_u32 m0,s64,65536\nM v[44:47],a[84:87],v[100:103],v[44:47]\n" \
"ds_read_b128 v[124:127],v250\nbuffer_load_dwordx4 v240,s[48:51],s36 offen offset:0 sc1 lds\nM v[52:55],a[88:91],v[100:103],v[52:55]\nds_read_b128 a[200:203],v250\nM v[60:63],a[92:95],v[100:103],v[60:63]\nds_read_b128 a[204:207],v250\nM a[196:199],a[208:211],v[100:103],a[196:199]\nds_read_b128 v[100:103],v250\n" \
"v_add_u32 v250,0xc000,v241\nM v[136:139],a[0:3],a[160:163],0\nds_read_b128 a[100:103],v241 offset:41984\nbuffer_load_dwordx4 v240,s[48:51],s36 offen offset:1024 sc1 lds\nM v[136:139],a[4:7],a[164:167],v[136:139]\nds_read_b128 a[72:75],v241 offset:34816\nM v[136:139],a[8:11],a[168:171],v[136:139]\nds_read_b128 a[104:107],v241 offset:43008\n" \
"M v[136:139],a[12:15],a[172:175],v[136:139]\nds_read_b128 a[76:79],v241 offset:35840\nM v[140:143],a[12:15],a[188:191],0\nds_read_b128 a[108:111],v241 offset:44032\nbuffer_load_dwordx4 v240,s[48:51],s36 offen offset:2048 sc1 lds\nM v[140:143],a[8:11],a[184:187],v[140:143]\nds_read_b128 a[80:83],v241 offset:36864\nM v[140:143],a[4:7],a[180:183],v[140:143]\n" \
"ds_read_b128 a[112:115],v241 offset:45056\nv_max3_f32 v233,v136,v137,v138\nM v[140:143],a[0:3],a[176:179],v[140:143]\nds_read_b128 a[84:87],v241 offset:37888\ns_waitcnt lgkmcnt(11)\nM v[152:155],a[16:19],a[160:163],0\nds_read_b128 a[116:119],v241 offset:46080\nbuffer_load_dwordx4 v240,s[48:51],s36 offen offset:3072 sc1 lds\n" \
"M v[152:155],a[20:23],a[164:167],v[152:155]\nds_read_b128 a[88:91],v241 offset:38912\ns_add_u32 m0,s64,16384\nv_max3_f32 v237,v140,v141,v142\nM v[152:155],a[24:27],a[168:171],v[152:155]\nds_read_b128 a[120:123],v241 offset:47104\nM v[152:155],a[28:31],a[172:175],v[152:155]\nds_read_b128 a[92:95],v241 offset:39936\n" \
"M v[156:159],a[28:31],a[188:191],0\nds_read_b128 a[124:127],v241 offset:48128\nbuffer_load_dwordx4 v240,s[44:47],s65 offen offset:0 sc1 lds\nM v[156:159],a[24:27],a[184:187],v[156:159]\nM v[156:159],a[20:23],a[180:183],v[156:159]\nM v[156:159],a[16:19],a[176:179],v[156:159]\nM v[128:131],a[0:3],a[128:131],v[224:227]\nv_max3_f32 v233,v233,v139,v152\n" \
"v_max3_f32 v233,v233,v153,v154\nv_max_f32 v233,v233,v155\nM v[128:131],a[4:7],a[132:135],v[128:131]\nv_cndmask_b32 v233,v232,v233,s[90:91]\nbuffer_load_dwordx4 v240,s[44:47],s65 offen offset:1024 sc1 lds\nM v[128:131],a[8:11],a[136:139],v[128:131]\nv_max3_f32 v237,v237,v143,v156\nv_max3_f32 v237,v237,v157,v158\n" \
"v_max_f32 v237,v237,v159\nM v[128:131],a[12:15],a[140:143],v[128:131]\nv_cndmask_b32 v237,v236,v237,s[90:91]\nM v[132:135],a[12:15],a[156:159],v[228:231]\nM v[132:135],a[8:11],a[152:155],v[132:135]\nv_permlane16_swap_b32 v233,v237\nv_max_f32 v233,v233,v237\nv_mov_b32 v237,v233\n" \
"M v[132:135],a[4:7],a[148:151],v[132:135]\nbuffer_load_dwordx4 v240,s[44:47],s65 offen offset:2048 sc1 lds\nv_permlane32_swap_b32 v233,v237\nM v[132:135],a[0:3],a[144:147],v[132:135]\nv_max_f32 v233,v233,v237\nv_mov_b32 v237,v233\nds_read_b128 a[0:3],v240 offset:0\nM v[144:147],a[16:19],a[128:131],v[224:227]\n" \
"v_permlane16_swap_b32 v233,v237\nv_sub_f32 v232,0,v233\nv_sub_f32 v234,0,v233\nM v[144:147],a[20:23],a[132:135],v[144:147]\nv_sub_f32 v235,0,v233\nv_sub_f32 v233,0,v233\nv_sub_f32 v236,0,v237\nM v[144:147],a[24:27],a[136:139],v[144:147]\n" \
"v_sub_f32 v238,0,v237\nv_sub_f32 v239,0,v237\nv_sub_f32 v237,0,v237\nM v[144:147],a[28:31],a[140:143],v[144:147]\nds_read_b128 a[4:7],v240 offset:1024\nds_read_b128 a[8:11],v240 offset:2048\nv_add_f32 v136,v136,v232\nM v[148:151],a[28:31],a[156:159],v[228:231]\n" \
"ds_read_b128 a[12:15],v240 offset:3072\nbuffer_load_dwordx4 v240,s[44:47],s65 offen offset:3072 sc1 lds\nM v[148:151],a[24:27],a[152:155],v[148:151]\ns_add_u32 m0,s64,81920\nv_add_f32 v137,v137,v232\nv_add_f32 v138,v138,v232\nM v[148:151],a[20:23],a[148:151],v[148:151]\nv_add_f32 v139,v139,v232\n" \
"v_add_f32 v152,v152,v232\nv_add_f32 v153,v153,v232\nM v[148:151],a[16:19],a[144:147],v[148:151]\nds_read_b128 a[16:19],v240 offset:4096\nds_read_b128 a[20:23],v240 offset:5120\nv_add_f32 v154,v154,v232\nM v[160:163],a[32:35],a[128:131],v[224:227]\nds_read_b128 a[24:27],v240 offset:6144\n" \
"v_add_f32 v155,v155,v232\nM v[160:163],a[36:39],a[132:135],v[160:163]\nds_read_b128 a[28:31],v240 offset:7168\nbuffer_load_dwordx4 v240,s[48:51],s65 offen offset:0 sc1 lds\nM v[160:163],a[40:43],a[136:139],v[160:163]\nv_exp_f32 v136,v136\nv_add_f32 v140,v140,v236\nM v[160:163],a[44:47],a[140:143],v[160:163]\n" \
"v_exp_f32 v137,v137\nv_add_f32 v141,v141,v236\nM v[164:167],a[44:47],a[156:159],v[228:231]\nv_exp_f32 v138,v138\nv_add_f32 v142,v142,v236\nM v[164:167],a[40:43],a[152:155],v[164:167]\nbuffer_load_dwordx4 v240,s[48:51],s65 offen offset:1024 sc1 lds\nv_add_f32 v143,v143,v236\n" \
"M v[164:167],a[36:39],a[148:151],v[164:167]\nv_exp_f32 v139,v139\nv_add_f32 v156,v156,v236\nM v[164:167],a[32:35],a[144:147],v[164:167]\nv_exp_f32 v152,v152\nv_add_f32 v157,v157,v236\nM v[176:179],a[48:51],a[128:131],v[224:227]\nv_exp_f32 v153,v153\n" \
"v_add_f32 v158,v158,v236\nM v[176:179],a[52:55],a[132:135],v[176:179]\nbuffer_load_dwordx4 v240,s[48:51],s65 offen offset:2048 sc1 lds\nv_add_f32 v159,v159,v236\nM v[176:179],a[56:59],a[136:139],v[176:179]\nv_exp_f32 v154,v154\nv_cvt_pk_bf16_f32 v200,v136,v137\nM v[176:179],a[60:63],a[140:143],v[176:179]\n" \
"v_exp_f32 v155,v155\nv_cvt_pk_bf16_f32 v201,v138,v139\nM v[180:183],a[60:63],a[156:159],v[228:231]\nv_exp_f32 v128,v128\nM v[180:183],a[56:59],a[152:155],v[180:183]\nbuffer_load_dwordx4 v240,s[48:51],s65 offen offset:3072 sc1 lds\ns_add_u32 m0,s64,32768\nM v[180:183],a[52:55],a[148:151],v[180:183]\n" \
"v_exp_f32 v129,v129\nv_cvt_pk_bf16_f32 v202,v152,v153\nM v[180:183],a[48:51],a[144:147],v[180:183]\nv_exp_f32 v130,v130\nv_cvt_pk_bf16_f32 v203,v154,v155\nM v[168:171],a[32:35],a[160:163],v[232:235]\nv_exp_f32 v131,v131\nM v[168:171],a[36:39],a[164:167],v[168:171]\n" \
"buffer_load_dwordx4 v240,s[44:47],s66 offen offset:0 sc1 lds\nM v[168:171],a[40:43],a[168:171],v[168:171]\nv_exp_f32 v132,v132\nM v[168:171],a[44:47],a[172:175],v[168:171]\nv_exp_f32 v133,v133\nM v[172:175],a[44:47],a[188:191],v[236:239]\nv_exp_f32 v134,v134\nM v[172:175],a[40:43],a[184:187],v[172:175]\n" \
"buffer_load_dwordx4 v240,s[44:47],s66 offen offset:1024 sc1 lds\nM v[172:175],a[36:39],a[180:183],v[172:175]\ns_waitcnt lgkmcnt(0)\ns_barrier\nv_cndmask_b32 v168,v168,v252,s[82:83]\nM v[172:175],a[32:35],a[176:179],v[172:175]\nds_read_b128 a[32:35],v240 offset:8192\nds_read_b128 a[36:39],v240 offset:9216\n" \
"v_cndmask_b32 v169,v169,v252,s[84:85]\nM v[188:191],a[60:63],a[188:191],v[236:239]\nds_read_b128 a[40:43],v240 offset:10240\nv_exp_f32 v135,v135\nM v[188:191],a[56:59],a[184:187],v[188:191]\nds_read_b128 a[44:47],v240 offset:11264\nv_cndmask_b32 v170,v170,v252,s[86:87]\nM v[188:191],a[52:55],a[180:183],v[188:191]\n" \
"buffer_load_dwordx4 v240,s[44:47],s66 offen offset:2048 sc1 lds\nv_cndmask_b32 v171,v171,v252,s[88:89]\nM v[188:191],a[48:51],a[176:179],v[188:191]\nds_read_b128 a[48:51],v240 offset:12288\nv_exp_f32 v144,v144\nM v[0:3],a[64:67],v[192:195],v[0:3]\nds_read_b128 a[52:55],v240 offset:13312\nv_exp_f32 v145,v145\n" \
"M v[0:3],a[96:99],v[208:211],v[0:3]\nds_read_b128 a[56:59],v240 offset:14336\nv_exp_f32 v146,v146\nM v[8:11],a[68:71],v[192:195],v[8:11]\nds_read_b128 a[60:63],v240 offset:15360\nbuffer_load_dwordx4 v240,s[44:47],s66 offen offset:3072 sc1 lds\nM v[8:11],a[100:103],v[208:211],v[8:11]\nv_exp_f32 v147,v147\n" \
"s_add_u32 m0,s64,98304\nM v[16:19],a[72:75],v[192:195],v[16:19]\nv_exp_f32 v148,v148\nv_cndmask_b32 v188,v188,v252,s[82:83]\nM v[16:19],a[104:107],v[208:211],v[16:19]\nv_exp_f32 v149,v149\nv_cndmask_b32 v189,v189,v252,s[84:85]\nM v[24:27],a[76:79],v[192:195],v[24:27]\n" \
"v_exp_f32 v150,v150\nv_cndmask_b32 v190,v190,v252,s[86:87]\nM v[24:27],a[108:111],v[208:211],v[24:27]\nv_exp_f32 v151,v151\nv_cndmask_b32 v191,v191,v252,s[88:89]\nM v[32:35],a[80:83],v[192:195],v[32:35]\nv_exp_f32 v160,v160\nM v[32:35],a[112:115],v[208:211],v[32:35]\n" \
"v_exp_f32 v161,v161\nM v[40:43],a[84:87],v[192:195],v[40:43]\nv_exp_f32 v162,v162\nM v[40:43],a[116:119],v[208:211],v[40:43]\nv_exp_f32 v163,v163\nM v[48:51],a[88:91],v[192:195],v[48:51]\nv_exp_f32 v176,v176\nM v[48:51],a[120:123],v[208:211],v[48:51]\n" \
"v_exp_f32 v177,v177\nM v[56:59],a[92:95],v[192:195],v[56:59]\nv_exp_f32 v178,v178\nM v[56:59],a[124:127],v[208:211],v[56:59]\nv_exp_f32 v179,v179\nM a[192:195],a[208:211],v[192:195],a[192:195]\nv_exp_f32 v140,v140\nv_cvt_pk_bf16_f32 v192,v128,v129\n" \
"M a[192:195],a[208:211],v[208:211],a[192:195]\nv_exp_f32 v141,v141\nv_cvt_pk_bf16_f32 v193,v130,v131\nM v[4:7],a[64:67],v[196:199],v[4:7]\nv_exp_f32 v142,v142\nv_cvt_pk_bf16_f32 v194,v144,v145\nM v[4:7],a[96:99],v[212:215],v[4:7]\nv_exp_f32 v143,v143\n" \
"v_cvt_pk_bf16_f32 v195,v146,v147\nM v[12:15],a[68:71],v[196:199],v[12:15]\nv_exp_f32 v156,v156\nv_cvt_pk_bf16_f32 v208,v160,v161\nM v[12:15],a[100:103],v[212:215],v[12:15]\nv_exp_f32 v157,v157\nv_cvt_pk_bf16_f32 v209,v162,v163\nM v[20:23],a[72:75],v[196:199],v[20:23]\n" \
"v_exp_f32 v158,v158\nM v[20:23],a[104:107],v[212:215],v[20:23]\nv_exp_f32 v159,v159\nv_cvt_pk_bf16_f32 v210,v176,v177\nM v[28:31],a[76:79],v[196:199],v[28:31]\nv_exp_f32 v164,v164\nv_cvt_pk_bf16_f32 v211,v178,v179\nM v[28:31],a[108:111],v[212:215],v[28:31]\n" \
"v_exp_f32 v165,v165\nM v[36:39],a[80:83],v[196:199],v[36:39]\nv_exp_f32 v166,v166\nM v[36:39],a[112:115],v[212:215],v[36:39]\nv_exp_f32 v167,v167\nM v[44:47],a[84:87],v[196:199],v[44:47]\nv_exp_f32 v180,v180\nM v[44:47],a[116:119],v[212:215],v[44:47]\n" \
"v_exp_f32 v181,v181\nM v[52:55],a[88:91],v[196:199],v[52:55]\nv_exp_f32 v182,v182\nM v[52:55],a[120:123],v[212:215],v[52:55]\nv_exp_f32 v183,v183\nM v[60:63],a[92:95],v[196:199],v[60:63]\nv_exp_f32 v168,v168\nM v[60:63],a[124:127],v[212:215],v[60:63]\n" \
"v_exp_f32 v169,v169\nM a[196:199],a[208:211],v[196:199],a[196:199]\nv_exp_f32 v170,v170\nv_exp_f32 v171,v171\nv_cvt_pk_bf16_f32 v196,v132,v133\nv_cvt_pk_bf16_f32 v197,v134,v135\nv_cvt_pk_bf16_f32 v198,v148,v149\nv_cvt_pk_bf16_f32 v199,v150,v151\n" \
"buffer_load_dwordx4 v240,s[48:51],s66 offen offset:0 sc1 lds\nbuffer_load_dwordx4 v240,s[48:51],s66 offen offset:1024 sc1 lds\nbuffer_load_dwordx4 v240,s[48:51],s66 offen offset:2048 sc1 lds\nbuffer_load_dwordx4 v240,s[48:51],s66 offen offset:3072 sc1 lds\nM a[196:199],a[208:211],v[212:215],a[196:199]\ns_cmp_lt_u32 s62,2\ns_cbranch_scc1 .Lx01\ns_branch .Ldual\n" \
".Lpro3:\ns_barrier\nM v[64:67],a[0:3],a[128:131],0\nds_read_b128 a[64:67],v241 offset:0\nds_read_b128 a[96:99],v241 offset:8192\nM v[64:67],a[4:7],a[132:135],v[64:67]\nds_read_b128 a[68:71],v241 offset:1024\ns_mov_b32 m0,s64\n" \
"v_mov_b32 v250,143360\nM v[64:67],a[8:11],a[136:139],v[64:67]\nds_read_b128 a[100:103],v241 offset:9216\nbuffer_load_dwordx4 v240,s[44:47],s36 offen offset:0 sc1 lds\nM v[64:67],a[12:15],a[140:143],v[64:67]\nds_read_b128 a[72:75],v241 offset:2048\nv_mov_b32 v168,0\nv_mov_b32 v169,0\n" \
"M v[68:71],a[12:15],a[156:159],0\nds_read_b128 a[12:15],v240 offset:19456\nv_mov_b32 v170,0\nv_mov_b32 v171,0\nM v[68:71],a[8:11],a[152:155],v[68:71]\nds_read_b128 a[8:11],v240 offset:18432\nv_max3_f32 v225,v64,v65,v66\nv_mov_b32 v172,0xff800000\n" \
"M v[68:71],a[4:7],a[148:151],v[68:71]\nds_read_b128 a[4:7],v240 offset:17408\nbuffer_load_dwordx4 v240,s[44:47],s36 offen offset:1024 sc1 lds\nM v[68:71],a[0:3],a[144:147],v[68:71]\nds_read_b128 a[0:3],v240 offset:16384\nv_mov_b32 v173,0xff800000\nv_mov_b32 v174,0xff800000\nM v[72:75],a[16:19],a[128:131],0\n" \
"ds_read_b128 a[104:107],v241 offset:10240\nv_mov_b32 v175,0xff800000\nv_mov_b32 v184,0xff800000\nM v[72:75],a[20:23],a[132:135],v[72:75]\nv_max3_f32 v229,v68,v69,v70\nds_read_b128 a[76:79],v241 offset:3072\nv_mov_b32 v185,0xff800000\nM v[72:75],a[24:27],a[136:139],v[72:75]\n" \
"ds_read_b128 a[108:111],v241 offset:11264\nv_mov_b32 v186,0xff800000\nM v[72:75],a[28:31],a[140:143],v[72:75]\nds_read_b128 a[80:83],v241 offset:4096\nbuffer_load_dwordx4 v240,s[44:47],s36 offen offset:2048 sc1 lds\nM v[76:79],a[28:31],a[156:159],0\nds_read_b128 a[28:31],v240 offset:23552\nv_mov_b32 v187,0xff800000\n" \
"v_mov_b32 v188,0xff800000\nM v[76:79],a[24:27],a[152:155],v[76:79]\nds_read_b128 a[24:27],v240 offset:22528\nv_max3_f32 v225,v225,v67,v72\nv_max3_f32 v225,v225,v73,v74\nM v[76:79],a[20:23],a[148:151],v[76:79]\nv_max_f32 v225,v225,v75\nv_cndmask_b32 v225,v224,v225,s[90:91]\n" \
"ds_read_b128 a[20:23],v240 offset:21504\nM v[76:79],a[16:19],a[144:147],v[76:79]\nds_read_b128 a[16:19],v240 offset:20480\nbuffer_load_dwordx4 v240,s[44:47],s36 offen offset:3072 sc1 lds\nM v[80:83],a[32:35],a[128:131],0\nds_read_b128 a[112:115],v241 offset:12288\nv_mov_b32 v189,0xff800000\nv_mov_b32 v190,0xff800000\n" \
"M v[80:83],a[36:39],a[132:135],v[80:83]\nds_read_b128 a[84:87],v241 offset:5120\nv_max3_f32 v229,v229,v71,v76\nv_max3_f32 v229,v229,v77,v78\nM v[80:83],a[40:43],a[136:139],v[80:83]\nv_max_f32 v229,v229,v79\nv_cndmask_b32 v229,v228,v229,s[90:91]\nds_read_b128 a[116:119],v241 offset:13312\n" \
"M v[80:83],a[44:47],a[140:143],v[80:83]\nv_permlane16_swap_b32 v225,v229\nv_max_f32 v225,v225,v229\nv_mov_b32 v229,v225\nM v[84:87],a[44:47],a[156:159],0\nds_read_b128 a[44:47],v240 offset:27648\nv_permlane32_swap_b32 v225,v229\nv_max_f32 v225,v225,v229\n" \
"M v[84:87],a[40:43],a[152:155],v[84:87]\nv_mov_b32 v229,v225\nds_read_b128 a[40:43],v240 offset:26624\nM v[84:87],a[36:39],a[148:151],v[84:87]\nv_permlane16_swap_b32 v225,v229\nv_sub_f32 v224,0,v225\nv_sub_f32 v226,0,v225\nM v[84:87],a[32:35],a[144:147],v[84:87]\n" \
"v_sub_f32 v227,0,v225\nv_sub_f32 v225,0,v225\nv_sub_f32 v228,0,v229\nM v[88:91],a[48:51],a[128:131],0\nv_sub_f32 v230,0,v229\nv_sub_f32 v231,0,v229\nv_sub_f32 v229,0,v229\nM v[88:91],a[52:55],a[132:135],v[88:91]\n" \
"ds_read_b128 a[32:35],v240 offset:24576\nds_read_b128 a[36:39],v240 offset:25600\nv_add_f32 v64,v64,v224\nM v[88:91],a[56:59],a[136:139],v[88:91]\nv_add_f32 v65,v65,v224\nv_add_f32 v66,v66,v224\nv_add_f32 v67,v67,v224\nM v[88:91],a[60:63],a[140:143],v[88:91]\n" \
"v_exp_f32 v64,v64\nv_add_f32 v72,v72,v224\nM v[92:95],a[60:63],a[156:159],0\nds_read_b128 a[60:63],v240 offset:31744\nv_exp_f32 v65,v65\nM v[92:95],a[56:59],a[152:155],v[92:95]\nds_read_b128 a[56:59],v240 offset:30720\nv_exp_f32 v66,v66\n" \
"M v[92:95],a[52:55],a[148:151],v[92:95]\nds_read_b128 a[52:55],v240 offset:29696\nv_exp_f32 v67,v67\nM v[92:95],a[48:51],a[144:147],v[92:95]\nds_read_b128 a[48:51],v240 offset:28672\nv_add_f32 v73,v73,v224\ns_waitcnt lgkmcnt(6)\nM v[96:99],a[0:3],a[128:131],v[224:227]\n" \
"v_add_f32 v74,v74,v224\nv_add_f32 v75,v75,v224\nM v[96:99],a[4:7],a[132:135],v[96:99]\nv_add_f32 v80,v80,v224\nv_add_f32 v81,v81,v224\nv_add_f32 v82,v82,v224\nM v[96:99],a[8:11],a[136:139],v[96:99]\nv_exp_f32 v72,v72\n" \
"v_add_f32 v83,v83,v224\nM v[96:99],a[12:15],a[140:143],v[96:99]\nv_exp_f32 v73,v73\nv_cvt_pk_bf16_f32 v64,v64,v65\nM v[100:103],a[12:15],a[156:159],v[228:231]\nv_exp_f32 v74,v74\nv_add_f32 v88,v88,v224\nM v[100:103],a[8:11],a[152:155],v[100:103]\n" \
"v_exp_f32 v75,v75\nv_add_f32 v89,v89,v224\nM v[100:103],a[4:7],a[148:151],v[100:103]\nv_exp_f32 v80,v80\nv_add_f32 v90,v90,v224\nM v[100:103],a[0:3],a[144:147],v[100:103]\nv_exp_f32 v81,v81\nv_add_f32 v91,v91,v224\n" \
"M v[104:107],a[16:19],a[128:131],v[224:227]\nv_exp_f32 v82,v82\nv_cvt_pk_bf16_f32 v65,v66,v67\nM v[104:107],a[20:23],a[132:135],v[104:107]\nv_exp_f32 v83,v83\nv_cvt_pk_bf16_f32 v66,v72,v73\nM v[104:107],a[24:27],a[136:139],v[104:107]\nv_exp_f32 v88,v88\n" \
"v_cvt_pk_bf16_f32 v67,v74,v75\nM v[104:107],a[28:31],a[140:143],v[104:107]\nv_exp_f32 v89,v89\nM v[108:111],a[28:31],a[156:159],v[228:231]\nv_exp_f32 v90,v90\nv_cvt_pk_bf16_f32 v80,v80,v81\nM v[108:111],a[24:27],a[152:155],v[108:111]\nv_exp_f32 v91,v91\n" \
"v_cvt_pk_bf16_f32 v81,v82,v83\nM v[108:111],a[20:23],a[148:151],v[108:111]\nv_cvt_pk_bf16_f32 v82,v88,v89\nv_cvt_pk_bf16_f32 v83,v90,v91\nds_read_b128 a[88:91],v241 offset:6144\nM v[108:111],a[16:19],a[144:147],v[108:111]\nds_read_b128 a[120:123],v241 offset:14336\nds_read_b128 a[92:95],v241 offset:7168\n" \
"s_waitcnt lgkmcnt(3)\nM v[112:115],a[32:35],a[128:131],v[224:227]\nds_read_b128 a[124:127],v241 offset:15360\nv_add_f32 v68,v68,v228\nv_add_f32 v69,v69,v228\nM v[112:115],a[36:39],a[132:135],v[112:115]\nv_add_f32 v70,v70,v228\nv_add_f32 v71,v71,v228\n" \
"v_add_f32 v76,v76,v228\nM v[112:115],a[40:43],a[136:139],v[112:115]\nv_exp_f32 v68,v68\nv_add_f32 v77,v77,v228\nM v[112:115],a[44:47],a[140:143],v[112:115]\nv_exp_f32 v69,v69\nv_add_f32 v78,v78,v228\nM v[116:119],a[44:47],a[156:159],v[228:231]\n" \
"v_exp_f32 v70,v70\nv_add_f32 v79,v79,v228\nM v[116:119],a[40:43],a[152:155],v[116:119]\nv_exp_f32 v71,v71\nv_add_f32 v84,v84,v228\nM v[116:119],a[36:39],a[148:151],v[116:119]\nv_add_f32 v85,v85,v228\nv_add_f32 v86,v86,v228\n" \
"M v[116:119],a[32:35],a[144:147],v[116:119]\nv_exp_f32 v76,v76\nv_add_f32 v87,v87,v228\nM v[124:127],a[60:63],a[156:159],v[228:231]\nv_exp_f32 v77,v77\nv_cvt_pk_bf16_f32 v68,v68,v69\nM v[124:127],a[56:59],a[152:155],v[124:127]\nv_exp_f32 v78,v78\n" \
"v_add_f32 v92,v92,v228\nM v[124:127],a[52:55],a[148:151],v[124:127]\nv_exp_f32 v79,v79\nv_add_f32 v93,v93,v228\nM v[124:127],a[48:51],a[144:147],v[124:127]\nv_exp_f32 v84,v84\nv_add_f32 v94,v94,v228\nM v[0:3],a[64:67],v[64:67],0\n" \
"v_exp_f32 v85,v85\nv_add_f32 v95,v95,v228\nM v[0:3],a[96:99],v[80:83],v[0:3]\nv_exp_f32 v86,v86\nv_cvt_pk_bf16_f32 v69,v70,v71\nM v[8:11],a[68:71],v[64:67],0\nv_exp_f32 v87,v87\nv_cvt_pk_bf16_f32 v70,v76,v77\n" \
"M v[8:11],a[100:103],v[80:83],v[8:11]\nv_exp_f32 v92,v92\nv_cvt_pk_bf16_f32 v71,v78,v79\nM v[16:19],a[72:75],v[64:67],0\nv_exp_f32 v93,v93\nv_cvt_pk_bf16_f32 v84,v84,v85\nM v[16:19],a[104:107],v[80:83],v[16:19]\nv_exp_f32 v94,v94\n" \
"M v[24:27],a[76:79],v[64:67],0\nv_exp_f32 v95,v95\nv_cvt_pk_bf16_f32 v85,v86,v87\nM v[24:27],a[108:111],v[80:83],v[24:27]\nv_cvt_pk_bf16_f32 v86,v92,v93\nv_cvt_pk_bf16_f32 v87,v94,v95\nds_read_b128 a[0:3],v240 offset:32768\nM v[32:35],a[80:83],v[64:67],0\n" \
"ds_read_b128 a[4:7],v240 offset:33792\nds_read_b128 a[8:11],v240 offset:34816\nv_cndmask_b32 v112,v112,v252,s[82:83]\nM v[32:35],a[112:115],v[80:83],v[32:35]\nds_read_b128 a[12:15],v240 offset:35840\nv_exp_f32 v96,v96\nM v[40:43],a[84:87],v[64:67],0\nds_read_b128 a[16:19],v240 offset:36864\n" \
"v_exp_f32 v97,v97\nM v[40:43],a[116:119],v[80:83],v[40:43]\nds_read_b128 a[20:23],v240 offset:37888\nv_cndmask_b32 v113,v113,v252,s[84:85]\ns_waitcnt lgkmcnt(6)\nM v[48:51],a[88:91],v[64:67],0\nds_read_b128 a[24:27],v240 offset:38912\nv_exp_f32 v98,v98\n" \
"M v[48:51],a[120:123],v[80:83],v[48:51]\nds_read_b128 a[28:31],v240 offset:39936\nv_exp_f32 v99,v99\nM v[56:59],a[92:95],v[64:67],0\nbuffer_load_dword v248,v251,s[52:55],s80 offen sc1\nds_read_b128 a[32:35],v240 offset:40960\nM v[56:59],a[124:127],v[80:83],v[56:59]\nds_read_b128 a[36:39],v240 offset:41984\n" \
"v_exp_f32 v104,v104\nM a[192:195],a[208:211],v[64:67],0\nds_read_b128 a[40:43],v240 offset:43008\nv_cndmask_b32 v114,v114,v252,s[86:87]\nM a[192:195],a[208:211],v[80:83],a[192:195]\nds_read_b128 a[44:47],v240 offset:44032\nv_exp_f32 v105,v105\nM v[4:7],a[64:67],v[68:71],0\n" \
"ds_read_b128 a[48:51],v240 offset:45056\nv_exp_f32 v106,v106\nM v[4:7],a[96:99],v[84:87],v[4:7]\nds_read_b128 a[52:55],v240 offset:46080\nv_exp_f32 v107,v107\nM v[12:15],a[68:71],v[68:71],0\nds_read_b128 a[56:59],v240 offset:47104\nv_cndmask_b32 v115,v115,v252,s[88:89]\n" \
"v_cvt_pk_bf16_f32 v96,v96,v97\nM v[12:15],a[100:103],v[84:87],v[12:15]\nds_read_b128 a[60:63],v240 offset:48128\nv_exp_f32 v112,v112\nM v[20:23],a[72:75],v[68:71],0\nds_read_b128 a[64:67],v241 offset:16384\nv_exp_f32 v113,v113\nM v[20:23],a[104:107],v[84:87],v[20:23]\n" \
"ds_read_b128 a[96:99],v241 offset:24576\nv_exp_f32 v114,v114\nM v[28:31],a[76:79],v[68:71],0\nds_read_b128 a[68:71],v241 offset:17408\nv_exp_f32 v115,v115\nM v[28:31],a[108:111],v[84:87],v[28:31]\nds_read_b128 a[100:103],v241 offset:25600\nv_cvt_pk_bf16_f32 v97,v98,v99\n" \
"v_cvt_pk_bf16_f32 v98,v104,v105\nM v[36:39],a[80:83],v[68:71],0\nds_read_b128 a[72:75],v241 offset:18432\nv_cvt_pk_bf16_f32 v99,v106,v107\nM v[36:39],a[112:115],v[84:87],v[36:39]\nds_read_b128 a[104:107],v241 offset:26624\nv_cvt_pk_bf16_f32 v112,v112,v113\nv_cvt_pk_bf16_f32 v113,v114,v115\n" \
"M v[44:47],a[84:87],v[68:71],0\nds_read_b128 a[76:79],v241 offset:19456\nv_mov_b32 v114,0\nv_mov_b32 v115,0\nM v[44:47],a[116:119],v[84:87],v[44:47]\nds_read_b128 a[108:111],v241 offset:27648\nv_exp_f32 v100,v100\nM v[52:55],a[88:91],v[68:71],0\n" \
"ds_read_b128 a[80:83],v241 offset:20480\nv_exp_f32 v101,v101\nM v[52:55],a[120:123],v[84:87],v[52:55]\nds_read_b128 a[112:115],v241 offset:28672\nv_exp_f32 v102,v102\nM v[60:63],a[92:95],v[68:71],0\nds_read_b128 a[84:87],v241 offset:21504\nv_exp_f32 v103,v103\n" \
"M v[60:63],a[124:127],v[84:87],v[60:63]\nds_read_b128 a[116:119],v241 offset:29696\nv_exp_f32 v108,v108\nM a[196:199],a[208:211],v[68:71],0\nds_read_b128 a[88:91],v241 offset:22528\nv_exp_f32 v109,v109\nM a[196:199],a[208:211],v[84:87],a[196:199]\nds_read_b128 a[120:123],v241 offset:30720\n" \
"v_cvt_pk_bf16_f32 v100,v100,v101\ns_waitcnt lgkmcnt(8)\nM v[128:131],a[0:3],a[128:131],v[224:227]\nds_read_b128 a[92:95],v241 offset:23552\nv_exp_f32 v110,v110\nM v[128:131],a[4:7],a[132:135],v[128:131]\nds_read_b128 a[124:127],v241 offset:31744\nv_cndmask_b32 v124,v124,v252,s[82:83]\n" \
"M v[128:131],a[8:11],a[136:139],v[128:131]\nv_exp_f32 v111,v111\nv_cndmask_b32 v125,v125,v252,s[84:85]\nM v[128:131],a[12:15],a[140:143],v[128:131]\nv_exp_f32 v116,v116\nv_cndmask_b32 v126,v126,v252,s[86:87]\nM v[132:135],a[12:15],a[156:159],v[228:231]\nv_exp_f32 v117,v117\n" \
"v_cndmask_b32 v127,v127,v252,s[88:89]\nM v[132:135],a[8:11],a[152:155],v[132:135]\nv_exp_f32 v118,v118\nv_cvt_pk_bf16_f32 v101,v102,v103\nM v[132:135],a[4:7],a[148:151],v[132:135]\nv_exp_f32 v119,v119\nv_cvt_pk_bf16_f32 v102,v108,v109\nM v[132:135],a[0:3],a[144:147],v[132:135]\n" \
"v_exp_f32 v124,v124\nv_cvt_pk_bf16_f32 v103,v110,v111\nM v[144:147],a[16:19],a[128:131],v[224:227]\nv_exp_f32 v125,v125\nv_cvt_pk_bf16_f32 v116,v116,v117\nM v[144:147],a[20:23],a[132:135],v[144:147]\nv_exp_f32 v126,v126\nv_cvt_pk_bf16_f32 v117,v118,v119\n" \
"M v[144:147],a[24:27],a[136:139],v[144:147]\nv_exp_f32 v127,v127\nv_cvt_pk_bf16_f32 v118,v124,v125\nM v[144:147],a[28:31],a[140:143],v[144:147]\nv_cvt_pk_bf16_f32 v119,v126,v127\nds_read_b128 a[0:3],v240 offset:49152\nds_read_b128 a[4:7],v240 offset:50176\nM v[148:151],a[28:31],a[156:159],v[228:231]\n" \
"ds_read_b128 a[8:11],v240 offset:51200\nv_mov_b32 v191,0xff800000\nM v[148:151],a[24:27],a[152:155],v[148:151]\nds_read_b128 a[12:15],v240 offset:52224\nv_exp_f32 v128,v128\nM v[148:151],a[20:23],a[148:151],v[148:151]\nds_read_b128 a[28:31],v240 offset:56320\nv_exp_f32 v129,v129\n" \
"M v[148:151],a[16:19],a[144:147],v[148:151]\nds_read_b128 a[24:27],v240 offset:55296\nv_exp_f32 v130,v130\nM v[160:163],a[32:35],a[128:131],v[224:227]\nds_read_b128 a[20:23],v240 offset:54272\nv_exp_f32 v131,v131\nM v[160:163],a[36:39],a[132:135],v[160:163]\nds_read_b128 a[16:19],v240 offset:53248\n" \
"v_cvt_pk_bf16_f32 v192,v128,v129\nv_cvt_pk_bf16_f32 v193,v130,v131\nM v[160:163],a[40:43],a[136:139],v[160:163]\nv_exp_f32 v132,v132\nv_mov_b32 v202,0\nM v[160:163],a[44:47],a[140:143],v[160:163]\nv_exp_f32 v133,v133\nv_mov_b32 v203,0\n" \
"M v[164:167],a[44:47],a[156:159],v[228:231]\nv_exp_f32 v134,v134\nv_cvt_pk_bf16_f32 v196,v132,v133\nM v[164:167],a[40:43],a[152:155],v[164:167]\nv_exp_f32 v135,v135\nds_read_b128 a[40:43],v240 offset:59392\nM v[164:167],a[36:39],a[148:151],v[164:167]\nv_cvt_pk_bf16_f32 v197,v134,v135\n" \
"v_exp_f32 v144,v144\nM v[164:167],a[32:35],a[144:147],v[164:167]\nds_read_b128 a[32:35],v240 offset:57344\nds_read_b128 a[36:39],v240 offset:58368\nM v[176:179],a[48:51],a[128:131],v[224:227]\nv_exp_f32 v145,v145\nds_read_b128 a[44:47],v240 offset:60416\nM v[176:179],a[52:55],a[132:135],v[176:179]\n" \
"v_exp_f32 v146,v146\nv_cvt_pk_bf16_f32 v194,v144,v145\nM v[176:179],a[56:59],a[136:139],v[176:179]\nv_exp_f32 v147,v147\ns_waitcnt vmcnt(0)\nv_cmp_eq_u32 vcc,0,v248\nM v[176:179],a[60:63],a[140:143],v[176:179]\nv_cvt_pk_bf16_f32 v195,v146,v147\n" \
"v_exp_f32 v148,v148\nM v[180:183],a[60:63],a[156:159],v[228:231]\ns_waitcnt lgkmcnt(4)\ns_barrier\nds_read_b128 a[60:63],v240 offset:64512\nM v[180:183],a[56:59],a[152:155],v[180:183]\nv_exp_f32 v149,v149\nds_read_b128 a[56:59],v240 offset:63488\n" \
"M v[180:183],a[52:55],a[148:151],v[180:183]\nv_exp_f32 v150,v150\nv_cvt_pk_bf16_f32 v198,v148,v149\nM v[180:183],a[48:51],a[144:147],v[180:183]\nv_exp_f32 v151,v151\nds_read_b128 a[48:51],v240 offset:61440\nM v[0:3],a[64:67],v[96:99],v[0:3]\nv_cvt_pk_bf16_f32 v199,v150,v151\n" \
"v_exp_f32 v160,v160\nM v[0:3],a[96:99],v[112:115],v[0:3]\nv_exp_f32 v161,v161\nds_read_b128 a[52:55],v240 offset:62464\nM v[8:11],a[68:71],v[96:99],v[8:11]\nv_exp_f32 v162,v162\nM v[8:11],a[100:103],v[112:115],v[8:11]\nv_exp_f32 v163,v163\n" \
"v_cvt_pk_bf16_f32 v208,v160,v161\nM v[16:19],a[72:75],v[96:99],v[16:19]\nv_cvt_pk_bf16_f32 v209,v162,v163\nds_write_b128 v250,a[244:247]\nM v[16:19],a[104:107],v[112:115],v[16:19]\nv_exp_f32 v164,v164\nds_read_b128 v[64:67],v250\nM v[24:27],a[76:79],v[96:99],v[24:27]\n" \
"v_exp_f32 v165,v165\nds_read_b128 v[68:71],v250\nM v[24:27],a[108:111],v[112:115],v[24:27]\nv_exp_f32 v166,v166\nds_read_b128 v[72:75],v250\nM v[32:35],a[80:83],v[96:99],v[32:35]\nv_exp_f32 v167,v167\nds_read_b128 v[76:79],v250\n" \
"M v[32:35],a[112:115],v[112:115],v[32:35]\nds_read_b128 v[80:83],v250\nds_read_b128 v[84:87],v250\nv_cvt_pk_bf16_f32 v212,v164,v165\nM v[40:43],a[84:87],v[96:99],v[40:43]\nds_read_b128 v[88:91],v250\nv_cvt_pk_bf16_f32 v213,v166,v167\ns_cbranch_vccnz .Lps5\n" \
".Lpb5:\nM v[40:43],a[116:119],v[112:115],v[40:43]\nds_read_b128 v[92:95],v250\nv_exp_f32 v176,v176\nM v[48:51],a[88:91],v[96:99],v[48:51]\nds_read_b128 v[104:107],v250\nv_exp_f32 v177,v177\nM v[48:51],a[120:123],v[112:115],v[48:51]\n" \
"ds_read_b128 v[108:111],v250\nv_cvt_pk_bf16_f32 v210,v176,v177\nM v[56:59],a[92:95],v[96:99],v[56:59]\nds_read_b128 v[120:123],v250\nv_exp_f32 v178,v178\nM v[56:59],a[124:127],v[112:115],v[56:59]\nds_read_b128 v[124:127],v250\nv_exp_f32 v179,v179\n" \
"M a[192:195],a[208:211],v[96:99],a[192:195]\nds_read_b128 v[96:99],v250\nv_cvt_pk_bf16_f32 v211,v178,v179\ns_add_u32 m0,s64,65536\nM a[192:195],a[208:211],v[112:115],a[192:195]\nds_read_b128 v[112:115],v250\nv_exp_f32 v180,v180\nM v[4:7],a[64:67],v[100:103],v[4:7]\n" \
"ds_read_b128 a[200:203],v250\nv_exp_f32 v181,v181\nM v[4:7],a[96:99],v[116:119],v[4:7]\nds_read_b128 a[204:207],v250\nv_exp_f32 v182,v182\nM v[12:15],a[68:71],v[100:103],v[12:15]\nds_read_b128 a[64:67],v241 offset:32768\nv_exp_f32 v183,v183\n" \
"M v[12:15],a[100:103],v[116:119],v[12:15]\nds_read_b128 a[96:99],v241 offset:40960\nv_cvt_pk_bf16_f32 v214,v180,v181\nv_cvt_pk_bf16_f32 v215,v182,v183\nM v[20:23],a[72:75],v[100:103],v[20:23]\nds_read_b128 a[68:71],v241 offset:33792\nbuffer_load_dwordx4 v240,s[48:51],s36 offen offset:0 sc1 lds\nM v[20:23],a[104:107],v[116:119],v[20:23]\n" \
"ds_read_b128 a[100:103],v241 offset:41984\nM v[28:31],a[76:79],v[100:103],v[28:31]\nds_read_b128 a[72:75],v241 offset:34816\nM v[28:31],a[108:111],v[116:119],v[28:31]\nds_read_b128 a[104:107],v241 offset:43008\nM v[36:39],a[80:83],v[100:103],v[36:39]\nds_read_b128 a[76:79],v241 offset:35840\nbuffer_load_dwordx4 v240,s[48:51],s36 offen offset:1024 sc1 lds\n" \
"M v[36:39],a[112:115],v[116:119],v[36:39]\nds_read_b128 a[108:111],v241 offset:44032\nM v[44:47],a[84:87],v[100:103],v[44:47]\nds_read_b128 a[80:83],v241 offset:36864\nM v[44:47],a[116:119],v[116:119],v[44:47]\nds_read_b128 a[112:115],v241 offset:45056\nM v[52:55],a[88:91],v[100:103],v[52:55]\nds_read_b128 a[84:87],v241 offset:37888\n" \
"buffer_load_dwordx4 v240,s[48:51],s36 offen offset:2048 sc1 lds\nM v[52:55],a[120:123],v[116:119],v[52:55]\nds_read_b128 a[116:119],v241 offset:46080\nM v[60:63],a[92:95],v[100:103],v[60:63]\nds_read_b128 a[88:91],v241 offset:38912\nM v[60:63],a[124:127],v[116:119],v[60:63]\nds_read_b128 a[120:123],v241 offset:47104\nM a[196:199],a[208:211],v[100:103],a[196:199]\n" \
"ds_read_b128 v[100:103],v250\nbuffer_load_dwordx4 v240,s[48:51],s36 offen offset:3072 sc1 lds\nM a[196:199],a[208:211],v[116:119],a[196:199]\nds_read_b128 v[116:119],v250\ns_add_u32 m0,s64,16384\nM v[136:139],a[0:3],a[160:163],0\nds_read_b128 a[92:95],v241 offset:39936\nv_add_u32 v250,0xc000,v241\n" \
"M v[136:139],a[4:7],a[164:167],v[136:139]\nds_read_b128 a[124:127],v241 offset:48128\nM v[136:139],a[8:11],a[168:171],v[136:139]\nbuffer_load_dwordx4 v240,s[44:47],s65 offen offset:0 sc1 lds\nM v[136:139],a[12:15],a[172:175],v[136:139]\nM v[140:143],a[12:15],a[188:191],0\nM v[140:143],a[8:11],a[184:187],v[140:143]\nM v[140:143],a[4:7],a[180:183],v[140:143]\n" \
"buffer_load_dwordx4 v240,s[44:47],s65 offen offset:1024 sc1 lds\nM v[140:143],a[0:3],a[176:179],v[140:143]\nM v[156:159],a[28:31],a[188:191],0\nM v[156:159],a[24:27],a[184:187],v[156:159]\nM v[156:159],a[20:23],a[180:183],v[156:159]\nv_max3_f32 v233,v136,v137,v138\nv_max_f32 v233,v233,v139\nv_cndmask_b32 v233,v232,v233,s[90:91]\n" \
"M v[156:159],a[16:19],a[176:179],v[156:159]\nbuffer_load_dwordx4 v240,s[44:47],s65 offen offset:2048 sc1 lds\nv_max3_f32 v237,v140,v141,v142\nM v[128:131],a[0:3],a[128:131],v[224:227]\nv_cndmask_b32 v136,v136,v252,s[82:83]\nv_cndmask_b32 v137,v137,v252,s[84:85]\nv_cndmask_b32 v138,v138,v252,s[86:87]\nM v[128:131],a[4:7],a[132:135],v[128:131]\n" \
"v_cndmask_b32 v139,v139,v252,s[88:89]\nv_max3_f32 v237,v237,v143,v156\nv_max3_f32 v237,v237,v157,v158\nM v[128:131],a[8:11],a[136:139],v[128:131]\nv_max_f32 v237,v237,v159\nv_cndmask_b32 v237,v236,v237,s[90:91]\nv_cndmask_b32 v156,v156,v252,s[82:83]\nM v[128:131],a[12:15],a[140:143],v[128:131]\n" \
"v_permlane16_swap_b32 v233,v237\nv_max_f32 v233,v233,v237\nv_mov_b32 v237,v233\nM v[132:135],a[12:15],a[156:159],v[228:231]\nbuffer_load_dwordx4 v240,s[44:47],s65 offen offset:3072 sc1 lds\nv_permlane32_swap_b32 v233,v237\nM v[132:135],a[8:11],a[152:155],v[132:135]\nv_max_f32 v233,v233,v237\n" \
"s_add_u32 m0,s64,81920\nv_mov_b32 v237,v233\nM v[132:135],a[4:7],a[148:151],v[132:135]\nv_cndmask_b32 v157,v157,v252,s[84:85]\nv_permlane16_swap_b32 v233,v237\nv_sub_f32 v232,0,v233\nM v[132:135],a[0:3],a[144:147],v[132:135]\nds_read_b128 a[0:3],v240 offset:0\n" \
"ds_read_b128 a[4:7],v240 offset:1024\nv_add_f32 v136,v136,v232\nM v[144:147],a[16:19],a[128:131],v[224:227]\nds_read_b128 a[8:11],v240 offset:2048\nv_add_f32 v137,v137,v232\nM v[144:147],a[20:23],a[132:135],v[144:147]\nds_read_b128 a[12:15],v240 offset:3072\nbuffer_load_dwordx4 v240,s[48:51],s65 offen offset:0 sc1 lds\n" \
"M v[144:147],a[24:27],a[136:139],v[144:147]\nv_add_f32 v138,v138,v232\nv_add_f32 v139,v139,v232\nv_cndmask_b32 v158,v158,v252,s[86:87]\nM v[144:147],a[28:31],a[140:143],v[144:147]\nv_cndmask_b32 v159,v159,v252,s[88:89]\nv_sub_f32 v236,0,v237\nv_sub_f32 v234,0,v233\n" \
"M v[148:151],a[28:31],a[156:159],v[228:231]\nv_exp_f32 v136,v136\nv_sub_f32 v235,0,v233\nM v[148:151],a[24:27],a[152:155],v[148:151]\nbuffer_load_dwordx4 v240,s[48:51],s65 offen offset:1024 sc1 lds\nv_sub_f32 v238,0,v237\nM v[148:151],a[20:23],a[148:151],v[148:151]\nv_exp_f32 v137,v137\n" \
"v_sub_f32 v239,0,v237\nM v[148:151],a[16:19],a[144:147],v[148:151]\nds_read_b128 a[16:19],v240 offset:4096\nds_read_b128 a[20:23],v240 offset:5120\ns_waitcnt lgkmcnt(5)\nM v[160:163],a[32:35],a[128:131],v[224:227]\nds_read_b128 a[24:27],v240 offset:6144\nv_exp_f32 v138,v138\n" \
"M v[160:163],a[36:39],a[132:135],v[160:163]\nds_read_b128 a[28:31],v240 offset:7168\nbuffer_load_dwordx4 v240,s[48:51],s65 offen offset:2048 sc1 lds\nM v[160:163],a[40:43],a[136:139],v[160:163]\nv_exp_f32 v139,v139\nv_add_f32 v140,v140,v236\nM v[160:163],a[44:47],a[140:143],v[160:163]\nv_exp_f32 v128,v128\n" \
"M v[164:167],a[44:47],a[156:159],v[228:231]\ns_barrier\nv_exp_f32 v129,v129\nM v[164:167],a[40:43],a[152:155],v[164:167]\nbuffer_load_dwordx4 v240,s[48:51],s65 offen offset:3072 sc1 lds\ns_add_u32 m0,s64,32768\nM v[164:167],a[36:39],a[148:151],v[164:167]\nv_exp_f32 v130,v130\n" \
"v_add_f32 v141,v141,v236\nM v[164:167],a[32:35],a[144:147],v[164:167]\nds_read_b128 a[32:35],v240 offset:8192\nds_read_b128 a[36:39],v240 offset:9216\nv_add_f32 v142,v142,v236\nM v[176:179],a[48:51],a[128:131],v[224:227]\nds_read_b128 a[40:43],v240 offset:10240\nv_exp_f32 v131,v131\n" \
"M v[176:179],a[52:55],a[132:135],v[176:179]\nds_read_b128 a[44:47],v240 offset:11264\nbuffer_load_dwordx4 v240,s[44:47],s66 offen offset:0 sc1 lds\nM v[176:179],a[56:59],a[136:139],v[176:179]\nv_exp_f32 v132,v132\nv_add_f32 v143,v143,v236\nM v[176:179],a[60:63],a[140:143],v[176:179]\nv_exp_f32 v133,v133\n" \
"v_add_f32 v156,v156,v236\nM v[180:183],a[60:63],a[156:159],v[228:231]\nv_exp_f32 v134,v134\nv_add_f32 v157,v157,v236\nM v[180:183],a[56:59],a[152:155],v[180:183]\nbuffer_load_dwordx4 v240,s[44:47],s66 offen offset:1024 sc1 lds\nv_add_f32 v158,v158,v236\nM v[180:183],a[52:55],a[148:151],v[180:183]\n" \
"v_exp_f32 v135,v135\nM v[180:183],a[48:51],a[144:147],v[180:183]\nds_read_b128 a[48:51],v240 offset:12288\nds_read_b128 a[52:55],v240 offset:13312\nv_add_f32 v159,v159,v236\nM v[0:3],a[64:67],v[192:195],v[0:3]\nds_read_b128 a[56:59],v240 offset:14336\nv_exp_f32 v144,v144\n" \
"M v[0:3],a[96:99],v[208:211],v[0:3]\nds_read_b128 a[60:63],v240 offset:15360\nbuffer_load_dwordx4 v240,s[44:47],s66 offen offset:2048 sc1 lds\nM v[8:11],a[68:71],v[192:195],v[8:11]\nv_exp_f32 v145,v145\nv_sub_f32 v233,0,v233\nM v[8:11],a[100:103],v[208:211],v[8:11]\nv_exp_f32 v146,v146\n" \
"v_sub_f32 v237,0,v237\nM v[16:19],a[72:75],v[192:195],v[16:19]\nv_exp_f32 v147,v147\nv_cvt_pk_bf16_f32 v200,v136,v137\nM v[16:19],a[104:107],v[208:211],v[16:19]\nbuffer_load_dwordx4 v240,s[44:47],s66 offen offset:3072 sc1 lds\ns_add_u32 m0,s64,98304\nM v[24:27],a[76:79],v[192:195],v[24:27]\n" \
"v_exp_f32 v148,v148\nv_cvt_pk_bf16_f32 v201,v138,v139\nM v[24:27],a[108:111],v[208:211],v[24:27]\nv_exp_f32 v149,v149\nM v[32:35],a[80:83],v[192:195],v[32:35]\nv_exp_f32 v150,v150\nM v[32:35],a[112:115],v[208:211],v[32:35]\nv_exp_f32 v151,v151\n" \
"M v[40:43],a[84:87],v[192:195],v[40:43]\nv_exp_f32 v160,v160\nM v[40:43],a[116:119],v[208:211],v[40:43]\nv_exp_f32 v161,v161\nM v[48:51],a[88:91],v[192:195],v[48:51]\nv_exp_f32 v162,v162\nM v[48:51],a[120:123],v[208:211],v[48:51]\nv_exp_f32 v163,v163\n" \
"M v[56:59],a[92:95],v[192:195],v[56:59]\nv_exp_f32 v176,v176\nM v[56:59],a[124:127],v[208:211],v[56:59]\nv_exp_f32 v177,v177\nM a[192:195],a[208:211],v[192:195],a[192:195]\nv_exp_f32 v178,v178\nv_cvt_pk_bf16_f32 v192,v128,v129\nM a[192:195],a[208:211],v[208:211],a[192:195]\n" \
"v_exp_f32 v179,v179\nv_cvt_pk_bf16_f32 v193,v130,v131\nM v[4:7],a[64:67],v[196:199],v[4:7]\nv_exp_f32 v140,v140\nv_cvt_pk_bf16_f32 v194,v144,v145\nM v[4:7],a[96:99],v[212:215],v[4:7]\nv_exp_f32 v141,v141\nv_cvt_pk_bf16_f32 v195,v146,v147\n" \
"M v[12:15],a[68:71],v[196:199],v[12:15]\nv_exp_f32 v142,v142\nM v[12:15],a[100:103],v[212:215],v[12:15]\nv_exp_f32 v143,v143\nv_cvt_pk_bf16_f32 v208,v160,v161\nM v[20:23],a[72:75],v[196:199],v[20:23]\nv_exp_f32 v156,v156\nv_cvt_pk_bf16_f32 v209,v162,v163\n" \
"M v[20:23],a[104:107],v[212:215],v[20:23]\nv_exp_f32 v157,v157\nv_cvt_pk_bf16_f32 v210,v176,v177\nM v[28:31],a[76:79],v[196:199],v[28:31]\nv_exp_f32 v158,v158\nv_cvt_pk_bf16_f32 v211,v178,v179\nM v[28:31],a[108:111],v[212:215],v[28:31]\nv_exp_f32 v159,v159\n" \
"M v[36:39],a[80:83],v[196:199],v[36:39]\nv_exp_f32 v164,v164\nM v[36:39],a[112:115],v[212:215],v[36:39]\nv_exp_f32 v165,v165\nM v[44:47],a[84:87],v[196:199],v[44:47]\nv_exp_f32 v166,v166\nM v[44:47],a[116:119],v[212:215],v[44:47]\nv_exp_f32 v167,v167\n" \
"M v[52:55],a[88:91],v[196:199],v[52:55]\nv_exp_f32 v180,v180\nM v[52:55],a[120:123],v[212:215],v[52:55]\nv_exp_f32 v181,v181\nM v[60:63],a[92:95],v[196:199],v[60:63]\nv_exp_f32 v182,v182\nM v[60:63],a[124:127],v[212:215],v[60:63]\nv_exp_f32 v183,v183\n" \
"M a[196:199],a[208:211],v[196:199],a[196:199]\nv_cvt_pk_bf16_f32 v196,v132,v133\nv_cvt_pk_bf16_f32 v197,v134,v135\nv_cvt_pk_bf16_f32 v198,v148,v149\nv_cvt_pk_bf16_f32 v199,v150,v151\nbuffer_load_dwordx4 v240,s[48:51],s66 offen offset:0 sc1 lds\nbuffer_load_dwordx4 v240,s[48:51],s66 offen offset:1024 sc1 lds\nbuffer_load_dwordx4 v240,s[48:51],s66 offen offset:2048 sc1 lds\n" \
"buffer_load_dwordx4 v240,s[48:51],s66 offen offset:3072 sc1 lds\nM a[196:199],a[208:211],v[212:215],a[196:199]\ns_cmp_lt_u32 s62,2\ns_cbranch_scc1 .Lx01\n.Ldual:\ns_waitcnt lgkmcnt(4)\nM v[128:131],a[0:3],a[128:131],v[224:227]\ns_add_u32 m0,s94,s64\n" \
"v_cvt_pk_bf16_f32 v204,v140,v141\nM v[128:131],a[4:7],a[132:135],v[128:131]\nds_read_b128 a[64:67],v250 offset:0\nds_read_b128 a[96:99],v250 offset:8192\nM v[128:131],a[8:11],a[136:139],v[128:131]\nv_cvt_pk_bf16_f32 v205,v142,v143\nv_cvt_pk_bf16_f32 v206,v156,v157\nM v[128:131],a[12:15],a[140:143],v[128:131]\n" \
"v_cvt_pk_bf16_f32 v207,v158,v159\nv_cvt_pk_bf16_f32 v212,v164,v165\nM v[132:135],a[12:15],a[156:159],v[228:231]\nbuffer_load_dwordx4 v240,s[44:47],s95 offen offset:0 sc1 lds\nM v[132:135],a[8:11],a[152:155],v[132:135]\nv_cvt_pk_bf16_f32 v213,v166,v167\nv_cvt_pk_bf16_f32 v216,v168,v169\nM v[132:135],a[4:7],a[148:151],v[132:135]\n" \
"v_cvt_pk_bf16_f32 v217,v170,v171\nv_cvt_pk_bf16_f32 v214,v180,v181\nM v[132:135],a[0:3],a[144:147],v[132:135]\nds_read_b128 a[68:71],v250 offset:1024\nds_read_b128 a[100:103],v250 offset:9216\nM v[136:139],a[0:3],a[160:163],v[232:235]\nv_exp_f32 v172,v172\nM v[136:139],a[4:7],a[164:167],v[136:139]\n" \
"v_exp_f32 v173,v173\nM v[136:139],a[8:11],a[168:171],v[136:139]\nv_exp_f32 v174,v174\nM v[136:139],a[12:15],a[172:175],v[136:139]\nv_exp_f32 v175,v175\nM v[140:143],a[12:15],a[188:191],v[236:239]\nv_cvt_pk_bf16_f32 v220,v172,v173\nv_cvt_pk_bf16_f32 v221,v174,v175\n" \
"M v[140:143],a[8:11],a[184:187],v[140:143]\nds_read_b128 a[72:75],v250 offset:2048\nds_read_b128 a[104:107],v250 offset:10240\nM v[140:143],a[4:7],a[180:183],v[140:143]\nv_cvt_pk_bf16_f32 v215,v182,v183\ns_add_u32 s94,s94,0x4000\nM v[140:143],a[0:3],a[176:179],v[140:143]\nv_exp_f32 v184,v184\n" \
"M v[144:147],a[16:19],a[128:131],v[224:227]\nv_exp_f32 v185,v185\nM v[144:147],a[20:23],a[132:135],v[144:147]\nv_exp_f32 v186,v186\nM v[144:147],a[24:27],a[136:139],v[144:147]\nv_exp_f32 v187,v187\nM v[144:147],a[28:31],a[140:143],v[144:147]\nds_read_b128 a[76:79],v250 offset:3072\n" \
"ds_read_b128 a[108:111],v250 offset:11264\nM v[148:151],a[28:31],a[156:159],v[228:231]\nv_cvt_pk_bf16_f32 v218,v184,v185\nv_cvt_pk_bf16_f32 v219,v186,v187\nM v[148:151],a[24:27],a[152:155],v[148:151]\nv_exp_f32 v188,v188\nM v[148:151],a[20:23],a[148:151],v[148:151]\nbuffer_load_dwordx4 v240,s[44:47],s95 offen offset:1024 sc1 lds\n" \
"M v[148:151],a[16:19],a[144:147],v[148:151]\nv_exp_f32 v189,v189\nM v[152:155],a[16:19],a[160:163],v[232:235]\nv_exp_f32 v190,v190\nM v[152:155],a[20:23],a[164:167],v[152:155]\nds_read_b128 a[80:83],v250 offset:4096\nds_read_b128 a[112:115],v250 offset:12288\nM v[152:155],a[24:27],a[168:171],v[152:155]\n" \
"v_exp_f32 v191,v191\nM v[152:155],a[28:31],a[172:175],v[152:155]\nv_cvt_pk_bf16_f32 v222,v188,v189\nv_cvt_pk_bf16_f32 v223,v190,v191\nM v[156:159],a[28:31],a[188:191],v[236:239]\nv_exp_f32 v128,v128\nM v[156:159],a[24:27],a[184:187],v[156:159]\nv_exp_f32 v129,v129\n" \
"M v[156:159],a[20:23],a[180:183],v[156:159]\nv_exp_f32 v130,v130\nM v[156:159],a[16:19],a[176:179],v[156:159]\nds_read_b128 a[84:87],v250 offset:5120\nds_read_b128 a[116:119],v250 offset:13312\nM v[160:163],a[32:35],a[128:131],v[224:227]\nv_exp_f32 v131,v131\nM v[160:163],a[36:39],a[132:135],v[160:163]\n" \
"v_exp_f32 v144,v144\nM v[160:163],a[40:43],a[136:139],v[160:163]\nv_exp_f32 v145,v145\nM v[160:163],a[44:47],a[140:143],v[160:163]\nv_exp_f32 v146,v146\nM v[164:167],a[44:47],a[156:159],v[228:231]\nv_exp_f32 v147,v147\nM v[164:167],a[40:43],a[152:155],v[164:167]\n" \
"ds_read_b128 a[88:91],v250 offset:6144\nds_read_b128 a[120:123],v250 offset:14336\nM v[164:167],a[36:39],a[148:151],v[164:167]\nv_exp_f32 v132,v132\nM v[164:167],a[32:35],a[144:147],v[164:167]\nv_exp_f32 v133,v133\nM v[168:171],a[32:35],a[160:163],v[232:235]\nbuffer_load_dwordx4 v240,s[44:47],s95 offen offset:2048 sc1 lds\n" \
"M v[168:171],a[36:39],a[164:167],v[168:171]\nv_exp_f32 v134,v134\nM v[168:171],a[40:43],a[168:171],v[168:171]\nv_exp_f32 v135,v135\nM v[168:171],a[44:47],a[172:175],v[168:171]\nds_read_b128 a[92:95],v250 offset:7168\nds_read_b128 a[124:127],v250 offset:15360\nM v[172:175],a[44:47],a[188:191],v[236:239]\n" \
"v_xor_b32 v250,0x18000,v250\ns_and_b32 s94,s94,0xffff\nM v[172:175],a[40:43],a[184:187],v[172:175]\nv_exp_f32 v148,v148\nM v[172:175],a[36:39],a[180:183],v[172:175]\nv_exp_f32 v149,v149\nM v[172:175],a[32:35],a[176:179],v[172:175]\ns_waitcnt lgkmcnt(4)\n" \
"M v[176:179],a[48:51],a[128:131],v[224:227]\nv_exp_f32 v150,v150\nM v[176:179],a[52:55],a[132:135],v[176:179]\nv_exp_f32 v151,v151\nM v[176:179],a[56:59],a[136:139],v[176:179]\nv_exp_f32 v136,v136\nM v[176:179],a[60:63],a[140:143],v[176:179]\nv_exp_f32 v137,v137\n" \
"M v[180:183],a[60:63],a[156:159],v[228:231]\nv_exp_f32 v138,v138\nM v[180:183],a[56:59],a[152:155],v[180:183]\nv_exp_f32 v139,v139\nM v[180:183],a[52:55],a[148:151],v[180:183]\nv_exp_f32 v152,v152\nM v[180:183],a[48:51],a[144:147],v[180:183]\nv_exp_f32 v153,v153\n" \
"M v[184:187],a[48:51],a[160:163],v[232:235]\nv_exp_f32 v154,v154\nM v[184:187],a[52:55],a[164:167],v[184:187]\nv_exp_f32 v155,v155\nM v[184:187],a[56:59],a[168:171],v[184:187]\nbuffer_load_dwordx4 v240,s[44:47],s95 offen offset:3072 sc1 lds\nM v[184:187],a[60:63],a[172:175],v[184:187]\nv_exp_f32 v160,v160\n" \
"M v[188:191],a[60:63],a[188:191],v[236:239]\nv_exp_f32 v161,v161\nM v[188:191],a[56:59],a[184:187],v[188:191]\nv_exp_f32 v162,v162\nM v[188:191],a[52:55],a[180:183],v[188:191]\nv_exp_f32 v163,v163\nM v[188:191],a[48:51],a[176:179],v[188:191]\nv_exp_f32 v176,v176\n" \
"M v[0:3],a[64:67],v[192:195],v[0:3]\ns_waitcnt vmcnt(16) lgkmcnt(0)\ns_barrier\nM v[0:3],a[96:99],v[208:211],v[0:3]\nds_read_b128 a[0:3],v250 offset:0\nds_read_b128 a[4:7],v250 offset:1024\nM v[8:11],a[68:71],v[192:195],v[8:11]\nv_exp_f32 v177,v177\n" \
"M v[8:11],a[100:103],v[208:211],v[8:11]\nv_exp_f32 v178,v178\nM v[16:19],a[72:75],v[192:195],v[16:19]\ns_add_u32 m0,m0,0x10000\nM v[16:19],a[104:107],v[208:211],v[16:19]\nbuffer_load_dwordx4 v240,s[48:51],s95 offen offset:0 sc1 lds\nM v[24:27],a[76:79],v[192:195],v[24:27]\nv_exp_f32 v179,v179\n" \
"M v[24:27],a[108:111],v[208:211],v[24:27]\nv_exp_f32 v140,v140\nM v[32:35],a[80:83],v[192:195],v[32:35]\nv_exp_f32 v141,v141\nM v[32:35],a[112:115],v[208:211],v[32:35]\nds_read_b128 a[8:11],v250 offset:2048\nds_read_b128 a[12:15],v250 offset:3072\nM v[40:43],a[84:87],v[192:195],v[40:43]\n" \
"v_exp_f32 v142,v142\nM v[40:43],a[116:119],v[208:211],v[40:43]\nv_exp_f32 v143,v143\nM v[48:51],a[88:91],v[192:195],v[48:51]\nv_exp_f32 v156,v156\nM v[48:51],a[120:123],v[208:211],v[48:51]\nv_exp_f32 v157,v157\nM v[56:59],a[92:95],v[192:195],v[56:59]\n" \
"v_exp_f32 v158,v158\nM v[56:59],a[124:127],v[208:211],v[56:59]\nv_exp_f32 v159,v159\nM a[192:195],a[208:211],v[192:195],a[192:195]\nv_exp_f32 v164,v164\nM a[192:195],a[208:211],v[208:211],a[192:195]\nds_read_b128 a[16:19],v250 offset:4096\nds_read_b128 a[20:23],v250 offset:5120\n" \
"M v[4:7],a[64:67],v[196:199],v[4:7]\nv_exp_f32 v165,v165\nM v[4:7],a[96:99],v[212:215],v[4:7]\nv_exp_f32 v166,v166\nM v[12:15],a[68:71],v[196:199],v[12:15]\nv_exp_f32 v167,v167\nM v[12:15],a[100:103],v[212:215],v[12:15]\nbuffer_load_dwordx4 v240,s[48:51],s95 offen offset:1024 sc1 lds\n" \
"M v[20:23],a[72:75],v[196:199],v[20:23]\nv_exp_f32 v180,v180\nM v[20:23],a[104:107],v[212:215],v[20:23]\nv_exp_f32 v181,v181\nM v[28:31],a[76:79],v[196:199],v[28:31]\nv_exp_f32 v182,v182\nM v[28:31],a[108:111],v[212:215],v[28:31]\nds_read_b128 a[24:27],v250 offset:6144\n" \
"ds_read_b128 a[28:31],v250 offset:7168\nM v[36:39],a[80:83],v[196:199],v[36:39]\nv_exp_f32 v183,v183\nM v[36:39],a[112:115],v[212:215],v[36:39]\nv_exp_f32 v168,v168\nM v[44:47],a[84:87],v[196:199],v[44:47]\nv_exp_f32 v169,v169\nM v[44:47],a[116:119],v[212:215],v[44:47]\n" \
"v_exp_f32 v170,v170\nM v[52:55],a[88:91],v[196:199],v[52:55]\nv_exp_f32 v171,v171\nM v[52:55],a[120:123],v[212:215],v[52:55]\nv_cvt_pk_bf16_f32 v192,v128,v129\nv_cvt_pk_bf16_f32 v193,v130,v131\nM v[60:63],a[92:95],v[196:199],v[60:63]\nv_cvt_pk_bf16_f32 v194,v144,v145\n" \
"v_cvt_pk_bf16_f32 v195,v146,v147\nM v[60:63],a[124:127],v[212:215],v[60:63]\nds_read_b128 a[32:35],v250 offset:8192\nds_read_b128 a[36:39],v250 offset:9216\nM a[196:199],a[208:211],v[196:199],a[196:199]\nv_cvt_pk_bf16_f32 v208,v160,v161\nv_cvt_pk_bf16_f32 v209,v162,v163\nM a[196:199],a[208:211],v[212:215],a[196:199]\n" \
"v_cvt_pk_bf16_f32 v210,v176,v177\nv_cvt_pk_bf16_f32 v211,v178,v179\nM v[64:67],a[64:67],v[200:203],v[64:67]\nv_cvt_pk_bf16_f32 v196,v132,v133\nv_cvt_pk_bf16_f32 v197,v134,v135\nM v[64:67],a[96:99],v[216:219],v[64:67]\nbuffer_load_dwordx4 v240,s[48:51],s95 offen offset:2048 sc1 lds\nM v[72:75],a[68:71],v[200:203],v[72:75]\n" \
"v_cvt_pk_bf16_f32 v198,v148,v149\nv_cvt_pk_bf16_f32 v199,v150,v151\nM v[72:75],a[100:103],v[216:219],v[72:75]\nM v[80:83],a[72:75],v[200:203],v[80:83]\nM v[80:83],a[104:107],v[216:219],v[80:83]\nds_read_b128 a[40:43],v250 offset:10240\nds_read_b128 a[44:47],v250 offset:11264\nM v[88:91],a[76:79],v[200:203],v[88:91]\n" \
"M v[88:91],a[108:111],v[216:219],v[88:91]\nM v[96:99],a[80:83],v[200:203],v[96:99]\nM v[96:99],a[112:115],v[216:219],v[96:99]\nM v[104:107],a[84:87],v[200:203],v[104:107]\nM v[104:107],a[116:119],v[216:219],v[104:107]\nM v[112:115],a[88:91],v[200:203],v[112:115]\nM v[112:115],a[120:123],v[216:219],v[112:115]\nds_read_b128 a[48:51],v250 offset:12288\n" \
"ds_read_b128 a[52:55],v250 offset:13312\nM v[120:123],a[92:95],v[200:203],v[120:123]\nM v[120:123],a[124:127],v[216:219],v[120:123]\nM a[200:203],a[208:211],v[200:203],a[200:203]\nv_cvt_pk_bf16_f32 v200,v136,v137\nv_cvt_pk_bf16_f32 v201,v138,v139\nM a[200:203],a[208:211],v[216:219],a[200:203]\nbuffer_load_dwordx4 v240,s[48:51],s95 offen offset:3072 sc1 lds\n" \
"M v[68:71],a[64:67],v[204:207],v[68:71]\nv_cvt_pk_bf16_f32 v202,v152,v153\nv_cvt_pk_bf16_f32 v203,v154,v155\nM v[68:71],a[96:99],v[220:223],v[68:71]\ns_cmp_eq_u32 s95,s96\ns_cselect_b32 s72,0xc000,s98\ns_add_u32 s95,s95,s72\nM v[76:79],a[68:71],v[204:207],v[76:79]\n" \
"M v[76:79],a[100:103],v[220:223],v[76:79]\nds_read_b128 a[56:59],v250 offset:14336\nds_read_b128 a[60:63],v250 offset:15360\nM v[84:87],a[72:75],v[204:207],v[84:87]\nv_add_u32 v250,s94,v241\nM v[84:87],a[104:107],v[220:223],v[84:87]\nM v[92:95],a[76:79],v[204:207],v[92:95]\nM v[92:95],a[108:111],v[220:223],v[92:95]\n" \
"M v[100:103],a[80:83],v[204:207],v[100:103]\nM v[100:103],a[112:115],v[220:223],v[100:103]\nM v[108:111],a[84:87],v[204:207],v[108:111]\nM v[108:111],a[116:119],v[220:223],v[108:111]\nM v[116:119],a[88:91],v[204:207],v[116:119]\nbuffer_load_dword v247,v254,s[56:59],s1 offen\nM v[116:119],a[120:123],v[220:223],v[116:119]\ns_add_u32 s1,s1,s0\n" \
"M v[124:127],a[92:95],v[204:207],v[124:127]\nM v[124:127],a[124:127],v[220:223],v[124:127]\nM a[204:207],a[208:211],v[204:207],a[204:207]\nM a[204:207],a[208:211],v[220:223],a[204:207]\ns_cmp_lg_u32 s95,s97\ns_cbranch_scc1 .Ldual\n.Ldf:\ns_waitcnt lgkmcnt(4)\n" \
"M v[128:131],a[0:3],a[128:131],v[224:227]\ns_add_u32 m0,s94,s64\nv_cvt_pk_bf16_f32 v204,v140,v141\nM v[128:131],a[4:7],a[132:135],v[128:131]\nds_read_b128 a[64:67],v250 offset:0\nds_read_b128 a[96:99],v250 offset:8192\nM v[128:131],a[8:11],a[136:139],v[128:131]\nv_cvt_pk_bf16_f32 v205,v142,v143\n" \
"v_lshrrev_b32 v248,2,v249\nM v[128:131],a[12:15],a[140:143],v[128:131]\nv_mul_u32_u24 v248,3584,v248\nv_and_b32 v251,3,v249\nM v[132:135],a[12:15],a[156:159],v[228:231]\nbuffer_load_dwordx4 v240,s[44:47],s95 offen offset:0 sc1 lds\nM v[132:135],a[8:11],a[152:155],v[132:135]\ns_mul_i32 s70,s7,458752\n" \
"v_lshl_add_u32 v248,v251,3,v248\nM v[132:135],a[4:7],a[148:151],v[132:135]\ns_add_u32 s70,s70,s2\nv_add_u32 v248,s70,v248\nM v[132:135],a[0:3],a[144:147],v[132:135]\nds_read_b128 a[68:71],v250 offset:1024\nds_read_b128 a[100:103],v250 offset:9216\nM v[136:139],a[0:3],a[160:163],v[232:235]\n" \
"v_and_b32 v248,s3,v248\nv_cvt_pk_bf16_f32 v206,v156,v157\nM v[136:139],a[4:7],a[164:167],v[136:139]\nv_cvt_pk_bf16_f32 v207,v158,v159\nv_cvt_pk_bf16_f32 v212,v164,v165\nM v[136:139],a[8:11],a[168:171],v[136:139]\nv_cvt_pk_bf16_f32 v213,v166,v167\nv_cvt_pk_bf16_f32 v216,v168,v169\n" \
"M v[136:139],a[12:15],a[172:175],v[136:139]\nv_cvt_pk_bf16_f32 v217,v170,v171\ns_and_b32 s71,s3,57344\nM v[140:143],a[12:15],a[188:191],v[236:239]\nv_exp_f32 v172,v172\nM v[140:143],a[8:11],a[184:187],v[140:143]\nds_read_b128 a[72:75],v250 offset:2048\nds_read_b128 a[104:107],v250 offset:10240\n" \
"M v[140:143],a[4:7],a[180:183],v[140:143]\nglobal_load_dwordx2 a[212:213],v248,s[22:23]\nM v[140:143],a[0:3],a[176:179],v[140:143]\nv_exp_f32 v173,v173\nM v[144:147],a[16:19],a[128:131],v[224:227]\nv_exp_f32 v174,v174\nM v[144:147],a[20:23],a[132:135],v[144:147]\nv_exp_f32 v175,v175\n" \
"M v[144:147],a[24:27],a[136:139],v[144:147]\nv_cvt_pk_bf16_f32 v220,v172,v173\nv_cvt_pk_bf16_f32 v221,v174,v175\nM v[144:147],a[28:31],a[140:143],v[144:147]\nds_read_b128 a[76:79],v250 offset:3072\nds_read_b128 a[108:111],v250 offset:11264\nM v[148:151],a[28:31],a[156:159],v[228:231]\nv_cvt_pk_bf16_f32 v214,v180,v181\n" \
"v_cvt_pk_bf16_f32 v215,v182,v183\nM v[148:151],a[24:27],a[152:155],v[148:151]\nv_exp_f32 v184,v184\nM v[148:151],a[20:23],a[148:151],v[148:151]\nbuffer_load_dwordx4 v240,s[44:47],s95 offen offset:1024 sc1 lds\nM v[148:151],a[16:19],a[144:147],v[148:151]\nv_exp_f32 v185,v185\nM v[152:155],a[16:19],a[160:163],v[232:235]\n" \
"v_exp_f32 v186,v186\nM v[152:155],a[20:23],a[164:167],v[152:155]\nds_read_b128 a[80:83],v250 offset:4096\nds_read_b128 a[112:115],v250 offset:12288\nM v[152:155],a[24:27],a[168:171],v[152:155]\nv_exp_f32 v187,v187\nM v[152:155],a[28:31],a[172:175],v[152:155]\nv_cvt_pk_bf16_f32 v218,v184,v185\n" \
"v_cvt_pk_bf16_f32 v219,v186,v187\nM v[156:159],a[28:31],a[188:191],v[236:239]\nv_exp_f32 v188,v188\nM v[156:159],a[24:27],a[184:187],v[156:159]\nv_exp_f32 v189,v189\nM v[156:159],a[20:23],a[180:183],v[156:159]\nv_exp_f32 v190,v190\nM v[156:159],a[16:19],a[176:179],v[156:159]\n" \
"ds_read_b128 a[84:87],v250 offset:5120\nds_read_b128 a[116:119],v250 offset:13312\nM v[160:163],a[32:35],a[128:131],v[224:227]\nglobal_load_dwordx2 a[228:229],v248,s[24:25]\nM v[160:163],a[36:39],a[132:135],v[160:163]\nv_add_u32 v248,s71,v248\nv_cvt_pk_bf16_f32 v222,v188,v189\nM v[160:163],a[40:43],a[136:139],v[160:163]\n" \
"v_exp_f32 v191,v191\nM v[160:163],a[44:47],a[140:143],v[160:163]\nv_cvt_pk_bf16_f32 v223,v190,v191\ns_add_u32 s94,s94,0x4000\nM v[164:167],a[44:47],a[156:159],v[228:231]\nv_exp_f32 v128,v128\nM v[164:167],a[40:43],a[152:155],v[164:167]\nds_read_b128 a[88:91],v250 offset:6144\n" \
"ds_read_b128 a[120:123],v250 offset:14336\nM v[164:167],a[36:39],a[148:151],v[164:167]\nv_exp_f32 v129,v129\nM v[164:167],a[32:35],a[144:147],v[164:167]\nv_exp_f32 v130,v130\nM v[168:171],a[32:35],a[160:163],v[232:235]\nbuffer_load_dwordx4 v240,s[44:47],s95 offen offset:2048 sc1 lds\nM v[168:171],a[36:39],a[164:167],v[168:171]\n" \
"v_exp_f32 v131,v131\nM v[168:171],a[40:43],a[168:171],v[168:171]\nv_exp_f32 v144,v144\nM v[168:171],a[44:47],a[172:175],v[168:171]\nds_read_b128 a[92:95],v250 offset:7168\nds_read_b128 a[124:127],v250 offset:15360\nM v[172:175],a[44:47],a[188:191],v[236:239]\nv_xor_b32 v250,0x18000,v250\n" \
"s_and_b32 s94,s94,0xffff\nM v[172:175],a[40:43],a[184:187],v[172:175]\nv_exp_f32 v145,v145\nM v[172:175],a[36:39],a[180:183],v[172:175]\nv_exp_f32 v146,v146\nM v[172:175],a[32:35],a[176:179],v[172:175]\ns_waitcnt lgkmcnt(4)\nM v[176:179],a[48:51],a[128:131],v[224:227]\n" \
"v_exp_f32 v147,v147\nM v[176:179],a[52:55],a[132:135],v[176:179]\nv_exp_f32 v132,v132\nM v[176:179],a[56:59],a[136:139],v[176:179]\nglobal_load_dwordx2 a[214:215],v248,s[22:23]\nM v[176:179],a[60:63],a[140:143],v[176:179]\nv_exp_f32 v133,v133\nM v[180:183],a[60:63],a[156:159],v[228:231]\n" \
"v_exp_f32 v134,v134\nM v[180:183],a[56:59],a[152:155],v[180:183]\nv_exp_f32 v135,v135\nM v[180:183],a[52:55],a[148:151],v[180:183]\nv_exp_f32 v148,v148\nM v[180:183],a[48:51],a[144:147],v[180:183]\nv_exp_f32 v149,v149\nM v[184:187],a[48:51],a[160:163],v[232:235]\n" \
"v_exp_f32 v150,v150\nM v[184:187],a[52:55],a[164:167],v[184:187]\nv_exp_f32 v151,v151\nM v[184:187],a[56:59],a[168:171],v[184:187]\nbuffer_load_dwordx4 v240,s[44:47],s95 offen offset:3072 sc1 lds\nM v[184:187],a[60:63],a[172:175],v[184:187]\nv_exp_f32 v136,v136\nM v[188:191],a[60:63],a[188:191],v[236:239]\n" \
"v_exp_f32 v137,v137\nM v[188:191],a[56:59],a[184:187],v[188:191]\nv_exp_f32 v138,v138\nM v[188:191],a[52:55],a[180:183],v[188:191]\nglobal_load_dwordx2 a[230:231],v248,s[24:25]\nM v[188:191],a[48:51],a[176:179],v[188:191]\ns_add_u32 m0,m0,0x10000\nv_add_u32 v248,s71,v248\n" \
"M v[0:3],a[64:67],v[192:195],v[0:3]\ns_waitcnt vmcnt(16) lgkmcnt(0)\ns_barrier\nM v[0:3],a[96:99],v[208:211],v[0:3]\nds_read_b128 a[0:3],v250 offset:0\nds_read_b128 a[4:7],v250 offset:1024\nM v[8:11],a[68:71],v[192:195],v[8:11]\nv_exp_f32 v139,v139\n" \
"M v[8:11],a[100:103],v[208:211],v[8:11]\nv_exp_f32 v152,v152\nM v[16:19],a[72:75],v[192:195],v[16:19]\nv_exp_f32 v153,v153\nM v[16:19],a[104:107],v[208:211],v[16:19]\nbuffer_load_dwordx4 v240,s[48:51],s95 offen offset:0 sc1 lds\nM v[24:27],a[76:79],v[192:195],v[24:27]\nv_exp_f32 v154,v154\n" \
"M v[24:27],a[108:111],v[208:211],v[24:27]\nv_exp_f32 v155,v155\nM v[32:35],a[80:83],v[192:195],v[32:35]\nv_exp_f32 v160,v160\nM v[32:35],a[112:115],v[208:211],v[32:35]\nds_read_b128 a[8:11],v250 offset:2048\nds_read_b128 a[12:15],v250 offset:3072\nM v[40:43],a[84:87],v[192:195],v[40:43]\n" \
"v_exp_f32 v161,v161\nM v[40:43],a[116:119],v[208:211],v[40:43]\nv_exp_f32 v162,v162\nM v[48:51],a[88:91],v[192:195],v[48:51]\nv_exp_f32 v163,v163\nM v[48:51],a[120:123],v[208:211],v[48:51]\nglobal_load_dwordx2 a[216:217],v248,s[22:23]\nM v[56:59],a[92:95],v[192:195],v[56:59]\n" \
"v_exp_f32 v176,v176\nM v[56:59],a[124:127],v[208:211],v[56:59]\nv_exp_f32 v177,v177\nM a[192:195],a[208:211],v[192:195],a[192:195]\nv_exp_f32 v178,v178\nM a[192:195],a[208:211],v[208:211],a[192:195]\nds_read_b128 a[16:19],v250 offset:4096\nds_read_b128 a[20:23],v250 offset:5120\n" \
"M v[4:7],a[64:67],v[196:199],v[4:7]\nv_exp_f32 v179,v179\nM v[4:7],a[96:99],v[212:215],v[4:7]\nv_exp_f32 v140,v140\nM v[12:15],a[68:71],v[196:199],v[12:15]\nv_exp_f32 v141,v141\nM v[12:15],a[100:103],v[212:215],v[12:15]\nbuffer_load_dwordx4 v240,s[48:51],s95 offen offset:1024 sc1 lds\n" \
"M v[20:23],a[72:75],v[196:199],v[20:23]\nv_exp_f32 v142,v142\nM v[20:23],a[104:107],v[212:215],v[20:23]\nv_exp_f32 v143,v143\nM v[28:31],a[76:79],v[196:199],v[28:31]\nv_exp_f32 v156,v156\nM v[28:31],a[108:111],v[212:215],v[28:31]\nds_read_b128 a[24:27],v250 offset:6144\n" \
"ds_read_b128 a[28:31],v250 offset:7168\nM v[36:39],a[80:83],v[196:199],v[36:39]\nv_exp_f32 v157,v157\nM v[36:39],a[112:115],v[212:215],v[36:39]\nv_exp_f32 v158,v158\nM v[44:47],a[84:87],v[196:199],v[44:47]\nv_exp_f32 v159,v159\nM v[44:47],a[116:119],v[212:215],v[44:47]\n" \
"global_load_dwordx2 a[232:233],v248,s[24:25]\nM v[52:55],a[88:91],v[196:199],v[52:55]\nv_add_u32 v248,s71,v248\nv_cvt_pk_bf16_f32 v192,v128,v129\nM v[52:55],a[120:123],v[212:215],v[52:55]\nv_exp_f32 v164,v164\nM v[60:63],a[92:95],v[196:199],v[60:63]\nv_exp_f32 v165,v165\n" \
"M v[60:63],a[124:127],v[212:215],v[60:63]\nds_read_b128 a[32:35],v250 offset:8192\nds_read_b128 a[36:39],v250 offset:9216\nM a[196:199],a[208:211],v[196:199],a[196:199]\nv_exp_f32 v166,v166\nM a[196:199],a[208:211],v[212:215],a[196:199]\nv_exp_f32 v167,v167\nM v[64:67],a[64:67],v[200:203],v[64:67]\n" \
"v_exp_f32 v180,v180\nM v[64:67],a[96:99],v[216:219],v[64:67]\nbuffer_load_dwordx4 v240,s[48:51],s95 offen offset:2048 sc1 lds\nM v[72:75],a[68:71],v[200:203],v[72:75]\nv_exp_f32 v181,v181\nM v[72:75],a[100:103],v[216:219],v[72:75]\nv_exp_f32 v182,v182\nM v[80:83],a[72:75],v[200:203],v[80:83]\n" \
"v_exp_f32 v183,v183\nM v[80:83],a[104:107],v[216:219],v[80:83]\nds_read_b128 a[40:43],v250 offset:10240\nds_read_b128 a[44:47],v250 offset:11264\nM v[88:91],a[76:79],v[200:203],v[88:91]\nv_exp_f32 v168,v168\nM v[88:91],a[108:111],v[216:219],v[88:91]\nv_exp_f32 v169,v169\n" \
"M v[96:99],a[80:83],v[200:203],v[96:99]\nv_exp_f32 v170,v170\nM v[96:99],a[112:115],v[216:219],v[96:99]\nglobal_load_dwordx2 a[218:219],v248,s[22:23]\nM v[104:107],a[84:87],v[200:203],v[104:107]\nv_exp_f32 v171,v171\nM v[104:107],a[116:119],v[216:219],v[104:107]\nv_cvt_pk_bf16_f32 v193,v130,v131\n" \
"v_cvt_pk_bf16_f32 v194,v144,v145\nM v[112:115],a[88:91],v[200:203],v[112:115]\nv_cvt_pk_bf16_f32 v195,v146,v147\nv_cvt_pk_bf16_f32 v208,v160,v161\nM v[112:115],a[120:123],v[216:219],v[112:115]\nds_read_b128 a[48:51],v250 offset:12288\nds_read_b128 a[52:55],v250 offset:13312\nM v[120:123],a[92:95],v[200:203],v[120:123]\n" \
"v_cvt_pk_bf16_f32 v209,v162,v163\nv_cvt_pk_bf16_f32 v210,v176,v177\nM v[120:123],a[124:127],v[216:219],v[120:123]\nv_cvt_pk_bf16_f32 v211,v178,v179\nv_cvt_pk_bf16_f32 v196,v132,v133\nM a[200:203],a[208:211],v[200:203],a[200:203]\nv_cvt_pk_bf16_f32 v197,v134,v135\nv_cvt_pk_bf16_f32 v198,v148,v149\n" \
"M a[200:203],a[208:211],v[216:219],a[200:203]\nbuffer_load_dwordx4 v240,s[48:51],s95 offen offset:3072 sc1 lds\nM v[68:71],a[64:67],v[204:207],v[68:71]\nv_cvt_pk_bf16_f32 v199,v150,v151\nv_cvt_pk_bf16_f32 v200,v136,v137\nM v[68:71],a[96:99],v[220:223],v[68:71]\nv_cvt_pk_bf16_f32 v201,v138,v139\nv_cvt_pk_bf16_f32 v202,v152,v153\n" \
"M v[76:79],a[68:71],v[204:207],v[76:79]\nv_cvt_pk_bf16_f32 v203,v154,v155\ns_add_u32 s95,s95,0x4000\nM v[76:79],a[100:103],v[220:223],v[76:79]\nds_read_b128 a[56:59],v250 offset:14336\nds_read_b128 a[60:63],v250 offset:15360\nM v[84:87],a[72:75],v[204:207],v[84:87]\nv_add_u32 v250,s94,v241\n" \
"M v[84:87],a[104:107],v[220:223],v[84:87]\nM v[92:95],a[76:79],v[204:207],v[92:95]\nM v[92:95],a[108:111],v[220:223],v[92:95]\nglobal_load_dwordx2 a[234:235],v248,s[24:25]\nM v[100:103],a[80:83],v[204:207],v[100:103]\nv_add_u32 v248,s71,v248\nM v[100:103],a[112:115],v[220:223],v[100:103]\nM v[108:111],a[84:87],v[204:207],v[108:111]\n" \
"M v[108:111],a[116:119],v[220:223],v[108:111]\nM v[116:119],a[88:91],v[204:207],v[116:119]\nM v[116:119],a[120:123],v[220:223],v[116:119]\nM v[124:127],a[92:95],v[204:207],v[124:127]\nM v[124:127],a[124:127],v[220:223],v[124:127]\nM a[204:207],a[208:211],v[204:207],a[204:207]\nM a[204:207],a[208:211],v[220:223],a[204:207]\ns_waitcnt lgkmcnt(4)\n" \
"M v[128:131],a[0:3],a[128:131],v[224:227]\ns_add_u32 m0,s94,s64\nv_cvt_pk_bf16_f32 v204,v140,v141\nM v[128:131],a[4:7],a[132:135],v[128:131]\nds_read_b128 a[64:67],v250 offset:0\nds_read_b128 a[96:99],v250 offset:8192\nM v[128:131],a[8:11],a[136:139],v[128:131]\nv_cvt_pk_bf16_f32 v205,v142,v143\n" \
"v_cvt_pk_bf16_f32 v206,v156,v157\nM v[128:131],a[12:15],a[140:143],v[128:131]\nv_cvt_pk_bf16_f32 v207,v158,v159\nv_cvt_pk_bf16_f32 v212,v164,v165\nM v[132:135],a[12:15],a[156:159],v[228:231]\nbuffer_load_dwordx4 v240,s[44:47],s95 offen offset:0 sc1 lds\nM v[132:135],a[8:11],a[152:155],v[132:135]\nv_cvt_pk_bf16_f32 v213,v166,v167\n" \
"v_cvt_pk_bf16_f32 v216,v168,v169\nM v[132:135],a[4:7],a[148:151],v[132:135]\nv_cvt_pk_bf16_f32 v217,v170,v171\nv_cvt_pk_bf16_f32 v214,v180,v181\nM v[132:135],a[0:3],a[144:147],v[132:135]\nds_read_b128 a[68:71],v250 offset:1024\nds_read_b128 a[100:103],v250 offset:9216\nM v[136:139],a[0:3],a[160:163],v[232:235]\n" \
"v_exp_f32 v172,v172\nM v[136:139],a[4:7],a[164:167],v[136:139]\nv_exp_f32 v173,v173\nM v[136:139],a[8:11],a[168:171],v[136:139]\nv_exp_f32 v174,v174\nM v[136:139],a[12:15],a[172:175],v[136:139]\nv_exp_f32 v175,v175\nM v[140:143],a[12:15],a[188:191],v[236:239]\n" \
"v_cvt_pk_bf16_f32 v220,v172,v173\nv_cvt_pk_bf16_f32 v221,v174,v175\nM v[140:143],a[8:11],a[184:187],v[140:143]\nds_read_b128 a[72:75],v250 offset:2048\nds_read_b128 a[104:107],v250 offset:10240\nM v[140:143],a[4:7],a[180:183],v[140:143]\nglobal_load_dwordx2 a[220:221],v248,s[22:23]\nM v[140:143],a[0:3],a[176:179],v[140:143]\n" \
"v_cvt_pk_bf16_f32 v215,v182,v183\ns_add_u32 s94,s94,0x4000\nM v[144:147],a[16:19],a[128:131],v[224:227]\nv_exp_f32 v184,v184\nM v[144:147],a[20:23],a[132:135],v[144:147]\nv_exp_f32 v185,v185\nM v[144:147],a[24:27],a[136:139],v[144:147]\nv_exp_f32 v186,v186\n" \
"M v[144:147],a[28:31],a[140:143],v[144:147]\nds_read_b128 a[76:79],v250 offset:3072\nds_read_b128 a[108:111],v250 offset:11264\nM v[148:151],a[28:31],a[156:159],v[228:231]\nv_exp_f32 v187,v187\nM v[148:151],a[24:27],a[152:155],v[148:151]\nv_cvt_pk_bf16_f32 v218,v184,v185\nv_cvt_pk_bf16_f32 v219,v186,v187\n" \
"M v[148:151],a[20:23],a[148:151],v[148:151]\nbuffer_load_dwordx4 v240,s[44:47],s95 offen offset:1024 sc1 lds\nM v[148:151],a[16:19],a[144:147],v[148:151]\nv_exp_f32 v188,v188\nM v[152:155],a[16:19],a[160:163],v[232:235]\nv_exp_f32 v189,v189\nM v[152:155],a[20:23],a[164:167],v[152:155]\nds_read_b128 a[80:83],v250 offset:4096\n" \
"ds_read_b128 a[112:115],v250 offset:12288\nM v[152:155],a[24:27],a[168:171],v[152:155]\nv_exp_f32 v190,v190\nM v[152:155],a[28:31],a[172:175],v[152:155]\nv_exp_f32 v191,v191\nM v[156:159],a[28:31],a[188:191],v[236:239]\nv_cvt_pk_bf16_f32 v222,v188,v189\nv_cvt_pk_bf16_f32 v223,v190,v191\n" \
"M v[156:159],a[24:27],a[184:187],v[156:159]\nv_exp_f32 v128,v128\nM v[156:159],a[20:23],a[180:183],v[156:159]\nv_exp_f32 v129,v129\nM v[156:159],a[16:19],a[176:179],v[156:159]\nds_read_b128 a[84:87],v250 offset:5120\nds_read_b128 a[116:119],v250 offset:13312\nM v[160:163],a[32:35],a[128:131],v[224:227]\n" \
"global_load_dwordx2 a[236:237],v248,s[24:25]\nM v[160:163],a[36:39],a[132:135],v[160:163]\nv_add_u32 v248,s71,v248\ns_and_b32 s94,s94,0xffff\nM v[160:163],a[40:43],a[136:139],v[160:163]\nv_exp_f32 v130,v130\nM v[160:163],a[44:47],a[140:143],v[160:163]\nv_exp_f32 v131,v131\n" \
"M v[164:167],a[44:47],a[156:159],v[228:231]\nv_exp_f32 v144,v144\nM v[164:167],a[40:43],a[152:155],v[164:167]\nds_read_b128 a[88:91],v250 offset:6144\nds_read_b128 a[120:123],v250 offset:14336\nM v[164:167],a[36:39],a[148:151],v[164:167]\nv_exp_f32 v145,v145\nM v[164:167],a[32:35],a[144:147],v[164:167]\n" \
"v_exp_f32 v146,v146\nM v[168:171],a[32:35],a[160:163],v[232:235]\nbuffer_load_dwordx4 v240,s[44:47],s95 offen offset:2048 sc1 lds\nM v[168:171],a[36:39],a[164:167],v[168:171]\nv_exp_f32 v147,v147\nM v[168:171],a[40:43],a[168:171],v[168:171]\nv_exp_f32 v132,v132\nM v[168:171],a[44:47],a[172:175],v[168:171]\n" \
"ds_read_b128 a[92:95],v250 offset:7168\nds_read_b128 a[124:127],v250 offset:15360\nM v[172:175],a[44:47],a[188:191],v[236:239]\nv_exp_f32 v133,v133\nM v[172:175],a[40:43],a[184:187],v[172:175]\nv_exp_f32 v134,v134\nM v[172:175],a[36:39],a[180:183],v[172:175]\nv_exp_f32 v135,v135\n" \
"M v[172:175],a[32:35],a[176:179],v[172:175]\nv_xor_b32 v250,0x18000,v250\ns_waitcnt lgkmcnt(3)\nM v[176:179],a[48:51],a[128:131],v[224:227]\nv_exp_f32 v148,v148\nM v[176:179],a[52:55],a[132:135],v[176:179]\nv_exp_f32 v149,v149\nM v[176:179],a[56:59],a[136:139],v[176:179]\n" \
"global_load_dwordx2 a[222:223],v248,s[22:23]\nM v[176:179],a[60:63],a[140:143],v[176:179]\nv_exp_f32 v150,v150\nM v[180:183],a[60:63],a[156:159],v[228:231]\nv_exp_f32 v151,v151\nM v[180:183],a[56:59],a[152:155],v[180:183]\nv_exp_f32 v136,v136\nM v[180:183],a[52:55],a[148:151],v[180:183]\n" \
"v_exp_f32 v137,v137\nM v[180:183],a[48:51],a[144:147],v[180:183]\nv_exp_f32 v138,v138\nM v[184:187],a[48:51],a[160:163],v[232:235]\nv_exp_f32 v139,v139\nM v[184:187],a[52:55],a[164:167],v[184:187]\nv_exp_f32 v152,v152\nM v[184:187],a[56:59],a[168:171],v[184:187]\n" \
"buffer_load_dwordx4 v240,s[44:47],s95 offen offset:3072 sc1 lds\nM v[184:187],a[60:63],a[172:175],v[184:187]\nv_exp_f32 v153,v153\nM v[188:191],a[60:63],a[188:191],v[236:239]\nv_exp_f32 v154,v154\nM v[188:191],a[56:59],a[184:187],v[188:191]\nv_exp_f32 v155,v155\nM v[188:191],a[52:55],a[180:183],v[188:191]\n" \
"global_load_dwordx2 a[238:239],v248,s[24:25]\nM v[188:191],a[48:51],a[176:179],v[188:191]\ns_add_u32 m0,m0,0x10000\nv_add_u32 v248,s71,v248\nM v[0:3],a[64:67],v[192:195],v[0:3]\ns_waitcnt vmcnt(16) lgkmcnt(0)\ns_barrier\nM v[0:3],a[96:99],v[208:211],v[0:3]\n" \
"ds_read_b128 a[0:3],v250 offset:0\nds_read_b128 a[4:7],v250 offset:1024\nM v[8:11],a[68:71],v[192:195],v[8:11]\nv_exp_f32 v160,v160\nM v[8:11],a[100:103],v[208:211],v[8:11]\nv_exp_f32 v161,v161\nM v[16:19],a[72:75],v[192:195],v[16:19]\nv_exp_f32 v162,v162\n" \
"M v[16:19],a[104:107],v[208:211],v[16:19]\nbuffer_load_dwordx4 v240,s[48:51],s95 offen offset:0 sc1 lds\nM v[24:27],a[76:79],v[192:195],v[24:27]\nv_exp_f32 v163,v163\nM v[24:27],a[108:111],v[208:211],v[24:27]\nv_exp_f32 v176,v176\nM v[32:35],a[80:83],v[192:195],v[32:35]\nv_exp_f32 v177,v177\n" \
"M v[32:35],a[112:115],v[208:211],v[32:35]\nds_read_b128 a[8:11],v250 offset:2048\nds_read_b128 a[12:15],v250 offset:3072\nM v[40:43],a[84:87],v[192:195],v[40:43]\nv_exp_f32 v178,v178\nM v[40:43],a[116:119],v[208:211],v[40:43]\nv_exp_f32 v179,v179\nM v[48:51],a[88:91],v[192:195],v[48:51]\n" \
"v_exp_f32 v140,v140\nM v[48:51],a[120:123],v[208:211],v[48:51]\nglobal_load_dwordx2 a[224:225],v248,s[22:23]\nM v[56:59],a[92:95],v[192:195],v[56:59]\nv_exp_f32 v141,v141\nM v[56:59],a[124:127],v[208:211],v[56:59]\nv_exp_f32 v142,v142\nM a[192:195],a[208:211],v[192:195],a[192:195]\n" \
"v_exp_f32 v143,v143\nM a[192:195],a[208:211],v[208:211],a[192:195]\nds_read_b128 a[16:19],v250 offset:4096\nds_read_b128 a[20:23],v250 offset:5120\nM v[4:7],a[64:67],v[196:199],v[4:7]\nv_exp_f32 v156,v156\nM v[4:7],a[96:99],v[212:215],v[4:7]\nv_exp_f32 v157,v157\n" \
"M v[12:15],a[68:71],v[196:199],v[12:15]\nv_exp_f32 v158,v158\nM v[12:15],a[100:103],v[212:215],v[12:15]\nbuffer_load_dwordx4 v240,s[48:51],s95 offen offset:1024 sc1 lds\nM v[20:23],a[72:75],v[196:199],v[20:23]\nv_exp_f32 v159,v159\nM v[20:23],a[104:107],v[212:215],v[20:23]\nv_exp_f32 v164,v164\n" \
"M v[28:31],a[76:79],v[196:199],v[28:31]\nv_exp_f32 v165,v165\nM v[28:31],a[108:111],v[212:215],v[28:31]\nds_read_b128 a[24:27],v250 offset:6144\nds_read_b128 a[28:31],v250 offset:7168\nM v[36:39],a[80:83],v[196:199],v[36:39]\nv_exp_f32 v166,v166\nM v[36:39],a[112:115],v[212:215],v[36:39]\n" \
"v_exp_f32 v167,v167\nM v[44:47],a[84:87],v[196:199],v[44:47]\nv_exp_f32 v180,v180\nM v[44:47],a[116:119],v[212:215],v[44:47]\nglobal_load_dwordx2 a[240:241],v248,s[24:25]\nM v[52:55],a[88:91],v[196:199],v[52:55]\nv_add_u32 v248,s71,v248\nv_cvt_pk_bf16_f32 v192,v128,v129\n" \
"M v[52:55],a[120:123],v[212:215],v[52:55]\nv_exp_f32 v181,v181\nM v[60:63],a[92:95],v[196:199],v[60:63]\nv_exp_f32 v182,v182\nM v[60:63],a[124:127],v[212:215],v[60:63]\nds_read_b128 a[32:35],v250 offset:8192\nds_read_b128 a[36:39],v250 offset:9216\nM a[196:199],a[208:211],v[196:199],a[196:199]\n" \
"v_exp_f32 v183,v183\nM a[196:199],a[208:211],v[212:215],a[196:199]\nv_exp_f32 v168,v168\nM v[64:67],a[64:67],v[200:203],v[64:67]\nv_exp_f32 v169,v169\nM v[64:67],a[96:99],v[216:219],v[64:67]\nbuffer_load_dwordx4 v240,s[48:51],s95 offen offset:2048 sc1 lds\nM v[72:75],a[68:71],v[200:203],v[72:75]\n" \
"v_exp_f32 v170,v170\nM v[72:75],a[100:103],v[216:219],v[72:75]\nv_exp_f32 v171,v171\nM v[80:83],a[72:75],v[200:203],v[80:83]\nv_cvt_pk_bf16_f32 v193,v130,v131\nv_cvt_pk_bf16_f32 v194,v144,v145\nM v[80:83],a[104:107],v[216:219],v[80:83]\nds_read_b128 a[40:43],v250 offset:10240\n" \
"ds_read_b128 a[44:47],v250 offset:11264\nM v[88:91],a[76:79],v[200:203],v[88:91]\nv_cvt_pk_bf16_f32 v195,v146,v147\nv_cvt_pk_bf16_f32 v208,v160,v161\nM v[88:91],a[108:111],v[216:219],v[88:91]\nv_cvt_pk_bf16_f32 v209,v162,v163\nv_cvt_pk_bf16_f32 v210,v176,v177\nM v[96:99],a[80:83],v[200:203],v[96:99]\n" \
"v_cvt_pk_bf16_f32 v211,v178,v179\nv_cvt_pk_bf16_f32 v196,v132,v133\nM v[96:99],a[112:115],v[216:219],v[96:99]\nglobal_load_dwordx2 a[226:227],v248,s[22:23]\nM v[104:107],a[84:87],v[200:203],v[104:107]\nv_cvt_pk_bf16_f32 v197,v134,v135\nv_cvt_pk_bf16_f32 v198,v148,v149\nM v[104:107],a[116:119],v[216:219],v[104:107]\n" \
"v_cvt_pk_bf16_f32 v199,v150,v151\nM v[112:115],a[88:91],v[200:203],v[112:115]\nM v[112:115],a[120:123],v[216:219],v[112:115]\nds_read_b128 a[48:51],v250 offset:12288\nds_read_b128 a[52:55],v250 offset:13312\nM v[120:123],a[92:95],v[200:203],v[120:123]\nM v[120:123],a[124:127],v[216:219],v[120:123]\nM a[200:203],a[208:211],v[200:203],a[200:203]\n" \
"v_cvt_pk_bf16_f32 v200,v136,v137\nv_cvt_pk_bf16_f32 v201,v138,v139\nM a[200:203],a[208:211],v[216:219],a[200:203]\nbuffer_load_dwordx4 v240,s[48:51],s95 offen offset:3072 sc1 lds\nM v[68:71],a[64:67],v[204:207],v[68:71]\nv_cvt_pk_bf16_f32 v202,v152,v153\nv_cvt_pk_bf16_f32 v203,v154,v155\nM v[68:71],a[96:99],v[220:223],v[68:71]\n" \
"s_add_u32 s95,s95,0x4000\nM v[76:79],a[68:71],v[204:207],v[76:79]\nM v[76:79],a[100:103],v[220:223],v[76:79]\nds_read_b128 a[56:59],v250 offset:14336\nds_read_b128 a[60:63],v250 offset:15360\nM v[84:87],a[72:75],v[204:207],v[84:87]\nv_add_u32 v250,s94,v241\nM v[84:87],a[104:107],v[220:223],v[84:87]\n" \
"M v[92:95],a[76:79],v[204:207],v[92:95]\nM v[92:95],a[108:111],v[220:223],v[92:95]\nglobal_load_dwordx2 a[242:243],v248,s[24:25]\nM v[100:103],a[80:83],v[204:207],v[100:103]\nv_add_u32 v248,s71,v248\nM v[100:103],a[112:115],v[220:223],v[100:103]\nM v[108:111],a[84:87],v[204:207],v[108:111]\nM v[108:111],a[116:119],v[220:223],v[108:111]\n" \
"M v[116:119],a[88:91],v[204:207],v[116:119]\nM v[116:119],a[120:123],v[220:223],v[116:119]\nM v[124:127],a[92:95],v[204:207],v[124:127]\nM v[124:127],a[124:127],v[220:223],v[124:127]\nM a[204:207],a[208:211],v[204:207],a[204:207]\nM a[204:207],a[208:211],v[220:223],a[204:207]\ns_cmp_eq_u32 s62,7\ns_cbranch_scc0 .Ltrans\n" \
"s_waitcnt lgkmcnt(4)\nM v[136:139],a[0:3],a[160:163],v[232:235]\ns_add_u32 m0,s94,s64\nv_cvt_pk_bf16_f32 v204,v140,v141\nM v[136:139],a[4:7],a[164:167],v[136:139]\nds_read_b128 a[64:67],v250 offset:0\nds_read_b128 a[96:99],v250 offset:8192\nM v[136:139],a[8:11],a[168:171],v[136:139]\n" \
"buffer_load_dwordx4 v240,s[44:47],s95 offen offset:0 sc1 lds\nM v[136:139],a[12:15],a[172:175],v[136:139]\nv_cvt_pk_bf16_f32 v205,v142,v143\nv_cvt_pk_bf16_f32 v206,v156,v157\nM v[140:143],a[12:15],a[188:191],v[236:239]\nds_read_b128 a[68:71],v250 offset:1024\nds_read_b128 a[100:103],v250 offset:9216\nM v[140:143],a[8:11],a[184:187],v[140:143]\n" \
"buffer_store_dwordx4 a[248:251],v253,s[52:55],s33 offen offset:0 sc1\nM v[140:143],a[4:7],a[180:183],v[140:143]\nv_cvt_pk_bf16_f32 v207,v158,v159\nv_cvt_pk_bf16_f32 v216,v168,v169\nM v[140:143],a[0:3],a[176:179],v[140:143]\nds_read_b128 a[72:75],v250 offset:2048\nds_read_b128 a[104:107],v250 offset:10240\nM v[152:155],a[16:19],a[160:163],v[232:235]\n" \
"v_cvt_pk_bf16_f32 v217,v170,v171\ns_add_u32 s71,s33,16384\nM v[152:155],a[20:23],a[164:167],v[152:155]\nv_exp_f32 v172,v172\nM v[152:155],a[24:27],a[168:171],v[152:155]\nds_read_b128 a[76:79],v250 offset:3072\nds_read_b128 a[108:111],v250 offset:11264\nM v[152:155],a[28:31],a[172:175],v[152:155]\n" \
"buffer_load_dwordx4 v240,s[44:47],s95 offen offset:1024 sc1 lds\nM v[156:159],a[28:31],a[188:191],v[236:239]\nv_exp_f32 v173,v173\nM v[156:159],a[24:27],a[184:187],v[156:159]\nds_read_b128 a[80:83],v250 offset:4096\nds_read_b128 a[112:115],v250 offset:12288\nM v[156:159],a[20:23],a[180:183],v[156:159]\nbuffer_store_dwordx4 a[248:251],v253,s[52:55],s33 offen offset:1024 sc1\n" \
"M v[156:159],a[16:19],a[176:179],v[156:159]\nv_exp_f32 v174,v174\nM v[168:171],a[32:35],a[160:163],v[232:235]\nds_read_b128 a[84:87],v250 offset:5120\nds_read_b128 a[116:119],v250 offset:13312\nM v[168:171],a[36:39],a[164:167],v[168:171]\nv_exp_f32 v175,v175\nM v[168:171],a[40:43],a[168:171],v[168:171]\n" \
"v_cvt_pk_bf16_f32 v220,v172,v173\nv_cvt_pk_bf16_f32 v221,v174,v175\nM v[168:171],a[44:47],a[172:175],v[168:171]\nds_read_b128 a[88:91],v250 offset:6144\nds_read_b128 a[120:123],v250 offset:14336\nM v[172:175],a[44:47],a[188:191],v[236:239]\nbuffer_load_dwordx4 v240,s[44:47],s95 offen offset:2048 sc1 lds\nM v[172:175],a[40:43],a[184:187],v[172:175]\n" \
"v_exp_f32 v184,v184\nM v[172:175],a[36:39],a[180:183],v[172:175]\nv_exp_f32 v185,v185\nv_exp_f32 v186,v186\nv_exp_f32 v187,v187\nds_read_b128 a[92:95],v250 offset:7168\nds_read_b128 a[124:127],v250 offset:15360\nM v[172:175],a[32:35],a[176:179],v[172:175]\n" \
"buffer_store_dwordx4 a[248:251],v253,s[52:55],s33 offen offset:2048 sc1\nv_cvt_pk_bf16_f32 v218,v184,v185\nv_cvt_pk_bf16_f32 v219,v186,v187\ns_waitcnt lgkmcnt(6)\nM v[184:187],a[48:51],a[160:163],v[232:235]\nv_exp_f32 v188,v188\nM v[184:187],a[52:55],a[164:167],v[184:187]\nv_exp_f32 v189,v189\n" \
"M v[184:187],a[56:59],a[168:171],v[184:187]\nv_exp_f32 v190,v190\nv_exp_f32 v191,v191\nM v[184:187],a[60:63],a[172:175],v[184:187]\nv_cvt_pk_bf16_f32 v222,v188,v189\nv_cvt_pk_bf16_f32 v223,v190,v191\nM v[188:191],a[60:63],a[188:191],v[236:239]\nv_xor_b32 v250,0x18000,v250\n" \
"s_add_u32 s70,s78,16384\nM v[188:191],a[56:59],a[184:187],v[188:191]\nbuffer_load_dwordx4 v240,s[44:47],s95 offen offset:3072 sc1 lds\nM v[188:191],a[52:55],a[180:183],v[188:191]\ns_add_u32 m0,m0,0x10000\nv_cvt_pk_bf16_f32 v212,v164,v165\nM v[188:191],a[48:51],a[176:179],v[188:191]\nv_cvt_pk_bf16_f32 v213,v166,v167\n" \
"v_cvt_pk_bf16_f32 v214,v180,v181\nM v[64:67],a[64:67],v[200:203],v[64:67]\nbuffer_store_dwordx4 a[248:251],v253,s[52:55],s33 offen offset:3072 sc1\nM v[64:67],a[96:99],v[216:219],v[64:67]\nv_cvt_pk_bf16_f32 v215,v182,v183\ns_add_u32 s94,s94,0x4000\nM v[72:75],a[68:71],v[200:203],v[72:75]\ns_waitcnt vmcnt(16) lgkmcnt(0)\n" \
"s_barrier\nM v[72:75],a[100:103],v[216:219],v[72:75]\nds_read_b128 a[0:3],v250 offset:0\nds_read_b128 a[4:7],v250 offset:1024\nM v[80:83],a[72:75],v[200:203],v[80:83]\nv_exp_f32 v136,v136\nM v[80:83],a[104:107],v[216:219],v[80:83]\nbuffer_store_dwordx4 a[248:251],v253,s[52:55],s71 offen offset:0 sc1\n" \
"M v[88:91],a[76:79],v[200:203],v[88:91]\nv_exp_f32 v137,v137\nM v[88:91],a[108:111],v[216:219],v[88:91]\nbuffer_load_dwordx4 v240,s[48:51],s95 offen offset:0 sc1 lds\nM v[96:99],a[80:83],v[200:203],v[96:99]\nv_exp_f32 v138,v138\nM v[96:99],a[112:115],v[216:219],v[96:99]\nbuffer_store_dwordx4 a[248:251],v253,s[52:55],s71 offen offset:1024 sc1\n" \
"M v[104:107],a[84:87],v[200:203],v[104:107]\nv_exp_f32 v139,v139\nM v[104:107],a[116:119],v[216:219],v[104:107]\nds_read_b128 a[8:11],v250 offset:2048\nds_read_b128 a[12:15],v250 offset:3072\nM v[112:115],a[88:91],v[200:203],v[112:115]\nv_exp_f32 v152,v152\nM v[112:115],a[120:123],v[216:219],v[112:115]\n" \
"buffer_store_dwordx4 a[248:251],v253,s[52:55],s71 offen offset:2048 sc1\nM v[120:123],a[92:95],v[200:203],v[120:123]\nv_exp_f32 v153,v153\nM v[120:123],a[124:127],v[216:219],v[120:123]\nv_exp_f32 v154,v154\nM a[200:203],a[208:211],v[200:203],a[200:203]\nv_exp_f32 v155,v155\nM a[200:203],a[208:211],v[216:219],a[200:203]\n" \
"buffer_store_dwordx4 a[248:251],v253,s[52:55],s71 offen offset:3072 sc1\nM v[68:71],a[64:67],v[204:207],v[68:71]\nv_exp_f32 v168,v168\nM v[68:71],a[96:99],v[220:223],v[68:71]\nds_read_b128 a[16:19],v250 offset:4096\nds_read_b128 a[20:23],v250 offset:5120\nM v[76:79],a[68:71],v[204:207],v[76:79]\nv_exp_f32 v169,v169\n" \
"M v[76:79],a[100:103],v[220:223],v[76:79]\nv_exp_f32 v170,v170\nM v[84:87],a[72:75],v[204:207],v[84:87]\nv_exp_f32 v171,v171\nM v[84:87],a[104:107],v[220:223],v[84:87]\nbuffer_load_dwordx4 v240,s[48:51],s95 offen offset:1024 sc1 lds\nM v[92:95],a[76:79],v[204:207],v[92:95]\nv_exp_f32 v184,v184\n" \
"M v[92:95],a[108:111],v[220:223],v[92:95]\nbuffer_store_dwordx4 a[248:251],v253,s[52:55],s78 offen offset:0 sc1\nM v[100:103],a[80:83],v[204:207],v[100:103]\nv_exp_f32 v185,v185\nM v[100:103],a[112:115],v[220:223],v[100:103]\nds_read_b128 a[24:27],v250 offset:6144\nds_read_b128 a[28:31],v250 offset:7168\nM v[108:111],a[84:87],v[204:207],v[108:111]\n" \
"v_exp_f32 v186,v186\nM v[108:111],a[116:119],v[220:223],v[108:111]\nbuffer_store_dwordx4 a[248:251],v253,s[52:55],s78 offen offset:1024 sc1\nM v[116:119],a[88:91],v[204:207],v[116:119]\nv_exp_f32 v187,v187\nM v[116:119],a[120:123],v[220:223],v[116:119]\ns_and_b32 s94,s94,0xffff\nv_cvt_pk_bf16_f32 v200,v136,v137\n" \
"M v[124:127],a[92:95],v[204:207],v[124:127]\nv_exp_f32 v140,v140\nM v[124:127],a[124:127],v[220:223],v[124:127]\nbuffer_store_dwordx4 a[248:251],v253,s[52:55],s78 offen offset:2048 sc1\nM a[204:207],a[208:211],v[204:207],a[204:207]\nv_exp_f32 v141,v141\nM a[204:207],a[208:211],v[220:223],a[204:207]\nds_read_b128 a[32:35],v250 offset:8192\n" \
"ds_read_b128 a[36:39],v250 offset:9216\nM v[0:3],a[64:67],v[192:195],v[0:3]\nv_exp_f32 v142,v142\nM v[0:3],a[96:99],v[208:211],v[0:3]\nv_exp_f32 v143,v143\nM v[8:11],a[68:71],v[192:195],v[8:11]\nv_exp_f32 v156,v156\nM v[8:11],a[100:103],v[208:211],v[8:11]\n" \
"buffer_load_dwordx4 v240,s[48:51],s95 offen offset:2048 sc1 lds\nM v[16:19],a[72:75],v[192:195],v[16:19]\nv_exp_f32 v157,v157\nM v[16:19],a[104:107],v[208:211],v[16:19]\nbuffer_store_dwordx4 a[248:251],v253,s[52:55],s78 offen offset:3072 sc1\nM v[24:27],a[76:79],v[192:195],v[24:27]\nv_exp_f32 v158,v158\nM v[24:27],a[108:111],v[208:211],v[24:27]\n" \
"ds_read_b128 a[40:43],v250 offset:10240\nds_read_b128 a[44:47],v250 offset:11264\nM v[32:35],a[80:83],v[192:195],v[32:35]\nv_exp_f32 v159,v159\nM v[32:35],a[112:115],v[208:211],v[32:35]\nbuffer_store_dwordx4 a[248:251],v253,s[52:55],s70 offen offset:0 sc1\nM v[40:43],a[84:87],v[192:195],v[40:43]\nv_cvt_pk_bf16_f32 v201,v138,v139\n" \
"v_cvt_pk_bf16_f32 v202,v152,v153\nM v[40:43],a[116:119],v[208:211],v[40:43]\nv_cvt_pk_bf16_f32 v203,v154,v155\nv_cvt_pk_bf16_f32 v216,v168,v169\nM v[48:51],a[88:91],v[192:195],v[48:51]\nv_cvt_pk_bf16_f32 v217,v170,v171\nv_cvt_pk_bf16_f32 v218,v184,v185\nM v[48:51],a[120:123],v[208:211],v[48:51]\n" \
"buffer_store_dwordx4 a[248:251],v253,s[52:55],s70 offen offset:1024 sc1\nM v[56:59],a[92:95],v[192:195],v[56:59]\nv_cvt_pk_bf16_f32 v219,v186,v187\nM v[56:59],a[124:127],v[208:211],v[56:59]\nds_read_b128 a[48:51],v250 offset:12288\nds_read_b128 a[52:55],v250 offset:13312\nM a[192:195],a[208:211],v[192:195],a[192:195]\nM a[192:195],a[208:211],v[208:211],a[192:195]\n" \
"M v[4:7],a[64:67],v[196:199],v[4:7]\nM v[4:7],a[96:99],v[212:215],v[4:7]\nbuffer_load_dwordx4 v240,s[48:51],s95 offen offset:3072 sc1 lds\nM v[12:15],a[68:71],v[196:199],v[12:15]\ns_add_u32 s95,s95,0xffff4000\nM v[12:15],a[100:103],v[212:215],v[12:15]\nbuffer_store_dwordx4 a[248:251],v253,s[52:55],s70 offen offset:2048 sc1\nM v[20:23],a[72:75],v[196:199],v[20:23]\n" \
"M v[20:23],a[104:107],v[212:215],v[20:23]\nds_read_b128 a[56:59],v250 offset:14336\nds_read_b128 a[60:63],v250 offset:15360\nM v[28:31],a[76:79],v[196:199],v[28:31]\nv_add_u32 v250,s94,v241\nM v[28:31],a[108:111],v[212:215],v[28:31]\nbuffer_store_dwordx4 a[248:251],v253,s[52:55],s70 offen offset:3072 sc1\nM v[36:39],a[80:83],v[196:199],v[36:39]\n" \
"M v[36:39],a[112:115],v[212:215],v[36:39]\nM v[44:47],a[84:87],v[196:199],v[44:47]\nM v[44:47],a[116:119],v[212:215],v[44:47]\nM v[52:55],a[88:91],v[196:199],v[52:55]\nM v[52:55],a[120:123],v[212:215],v[52:55]\nM v[60:63],a[92:95],v[196:199],v[60:63]\nM v[60:63],a[124:127],v[212:215],v[60:63]\nM a[196:199],a[208:211],v[196:199],a[196:199]\n" \
"M a[196:199],a[208:211],v[212:215],a[196:199]\n.Lsl:\ns_waitcnt lgkmcnt(4)\nM v[136:139],a[0:3],a[160:163],v[232:235]\ns_add_u32 m0,s94,s64\nv_cvt_pk_bf16_f32 v204,v140,v141\nM v[136:139],a[4:7],a[164:167],v[136:139]\nds_read_b128 a[64:67],v250 offset:0\n" \
"ds_read_b128 a[96:99],v250 offset:8192\nM v[136:139],a[8:11],a[168:171],v[136:139]\nbuffer_load_dwordx4 v240,s[44:47],s95 offen offset:0 sc1 lds\nM v[136:139],a[12:15],a[172:175],v[136:139]\nv_cvt_pk_bf16_f32 v205,v142,v143\nv_cvt_pk_bf16_f32 v206,v156,v157\nM v[140:143],a[12:15],a[188:191],v[236:239]\nds_read_b128 a[68:71],v250 offset:1024\n" \
"ds_read_b128 a[100:103],v250 offset:9216\nM v[140:143],a[8:11],a[184:187],v[140:143]\nv_cvt_pk_bf16_f32 v207,v158,v159\ns_add_u32 s94,s94,0x4000\nM v[140:143],a[4:7],a[180:183],v[140:143]\nv_exp_f32 v172,v172\nM v[140:143],a[0:3],a[176:179],v[140:143]\nds_read_b128 a[72:75],v250 offset:2048\n" \
"ds_read_b128 a[104:107],v250 offset:10240\nM v[152:155],a[16:19],a[160:163],v[232:235]\nv_exp_f32 v173,v173\nM v[152:155],a[20:23],a[164:167],v[152:155]\nv_exp_f32 v174,v174\nM v[152:155],a[24:27],a[168:171],v[152:155]\nds_read_b128 a[76:79],v250 offset:3072\nds_read_b128 a[108:111],v250 offset:11264\n" \
"M v[152:155],a[28:31],a[172:175],v[152:155]\nbuffer_load_dwordx4 v240,s[44:47],s95 offen offset:1024 sc1 lds\nM v[156:159],a[28:31],a[188:191],v[236:239]\nv_exp_f32 v175,v175\nM v[156:159],a[24:27],a[184:187],v[156:159]\nds_read_b128 a[80:83],v250 offset:4096\nds_read_b128 a[112:115],v250 offset:12288\nM v[156:159],a[20:23],a[180:183],v[156:159]\n" \
"v_cvt_pk_bf16_f32 v220,v172,v173\nv_cvt_pk_bf16_f32 v221,v174,v175\nM v[156:159],a[16:19],a[176:179],v[156:159]\nv_exp_f32 v188,v188\nM v[168:171],a[32:35],a[160:163],v[232:235]\nds_read_b128 a[84:87],v250 offset:5120\nds_read_b128 a[116:119],v250 offset:13312\nM v[168:171],a[36:39],a[164:167],v[168:171]\n" \
"v_exp_f32 v189,v189\nM v[168:171],a[40:43],a[168:171],v[168:171]\nv_exp_f32 v190,v190\nM v[168:171],a[44:47],a[172:175],v[168:171]\nds_read_b128 a[88:91],v250 offset:6144\nds_read_b128 a[120:123],v250 offset:14336\nM v[172:175],a[44:47],a[188:191],v[236:239]\nbuffer_load_dwordx4 v240,s[44:47],s95 offen offset:2048 sc1 lds\n" \
"M v[172:175],a[40:43],a[184:187],v[172:175]\nv_exp_f32 v191,v191\nM v[172:175],a[36:39],a[180:183],v[172:175]\nds_read_b128 a[92:95],v250 offset:7168\nds_read_b128 a[124:127],v250 offset:15360\nM v[172:175],a[32:35],a[176:179],v[172:175]\nv_cvt_pk_bf16_f32 v222,v188,v189\ns_waitcnt lgkmcnt(7)\n" \
"M v[184:187],a[48:51],a[160:163],v[232:235]\nv_cvt_pk_bf16_f32 v223,v190,v191\nv_xor_b32 v250,0x18000,v250\nM v[184:187],a[52:55],a[164:167],v[184:187]\nv_exp_f32 v136,v136\nM v[184:187],a[56:59],a[168:171],v[184:187]\nv_exp_f32 v137,v137\nM v[184:187],a[60:63],a[172:175],v[184:187]\n" \
"v_exp_f32 v138,v138\nM v[188:191],a[60:63],a[188:191],v[236:239]\nv_exp_f32 v139,v139\nM v[188:191],a[56:59],a[184:187],v[188:191]\nbuffer_load_dwordx4 v240,s[44:47],s95 offen offset:3072 sc1 lds\nM v[188:191],a[52:55],a[180:183],v[188:191]\ns_add_u32 m0,m0,0x10000\ns_and_b32 s94,s94,0xffff\n" \
"M v[188:191],a[48:51],a[176:179],v[188:191]\nv_exp_f32 v152,v152\nM v[64:67],a[64:67],v[200:203],v[64:67]\nv_exp_f32 v153,v153\nM v[64:67],a[96:99],v[216:219],v[64:67]\nv_exp_f32 v154,v154\nM v[72:75],a[68:71],v[200:203],v[72:75]\ns_waitcnt vmcnt(16) lgkmcnt(0)\n" \
"s_barrier\nM v[72:75],a[100:103],v[216:219],v[72:75]\nds_read_b128 a[0:3],v250 offset:0\nds_read_b128 a[4:7],v250 offset:1024\nM v[80:83],a[72:75],v[200:203],v[80:83]\nv_exp_f32 v155,v155\nM v[80:83],a[104:107],v[216:219],v[80:83]\nbuffer_load_dwordx4 v240,s[48:51],s95 offen offset:0 sc1 lds\n" \
"M v[88:91],a[76:79],v[200:203],v[88:91]\nv_exp_f32 v168,v168\nM v[88:91],a[108:111],v[216:219],v[88:91]\nds_read_b128 a[8:11],v250 offset:2048\nds_read_b128 a[12:15],v250 offset:3072\nM v[96:99],a[80:83],v[200:203],v[96:99]\nv_exp_f32 v169,v169\nM v[96:99],a[112:115],v[216:219],v[96:99]\n" \
"v_exp_f32 v170,v170\nM v[104:107],a[84:87],v[200:203],v[104:107]\nv_exp_f32 v171,v171\nM v[104:107],a[116:119],v[216:219],v[104:107]\nds_read_b128 a[16:19],v250 offset:4096\nds_read_b128 a[20:23],v250 offset:5120\nM v[112:115],a[88:91],v[200:203],v[112:115]\nv_exp_f32 v184,v184\n" \
"M v[112:115],a[120:123],v[216:219],v[112:115]\nbuffer_load_dwordx4 v240,s[48:51],s95 offen offset:1024 sc1 lds\nM v[120:123],a[92:95],v[200:203],v[120:123]\nv_exp_f32 v185,v185\nM v[120:123],a[124:127],v[216:219],v[120:123]\nds_read_b128 a[24:27],v250 offset:6144\nds_read_b128 a[28:31],v250 offset:7168\nM a[200:203],a[208:211],v[200:203],a[200:203]\n" \
"v_exp_f32 v186,v186\nM a[200:203],a[208:211],v[216:219],a[200:203]\nv_exp_f32 v187,v187\nM v[68:71],a[64:67],v[204:207],v[68:71]\nv_exp_f32 v140,v140\nM v[68:71],a[96:99],v[220:223],v[68:71]\nds_read_b128 a[32:35],v250 offset:8192\nds_read_b128 a[36:39],v250 offset:9216\n" \
"M v[76:79],a[68:71],v[204:207],v[76:79]\nv_exp_f32 v141,v141\nM v[76:79],a[100:103],v[220:223],v[76:79]\nbuffer_load_dwordx4 v240,s[48:51],s95 offen offset:2048 sc1 lds\nM v[84:87],a[72:75],v[204:207],v[84:87]\nv_exp_f32 v142,v142\nM v[84:87],a[104:107],v[220:223],v[84:87]\nds_read_b128 a[40:43],v250 offset:10240\n" \
"ds_read_b128 a[44:47],v250 offset:11264\nM v[92:95],a[76:79],v[204:207],v[92:95]\nv_exp_f32 v143,v143\nM v[92:95],a[108:111],v[220:223],v[92:95]\nv_exp_f32 v156,v156\nM v[100:103],a[80:83],v[204:207],v[100:103]\nv_exp_f32 v157,v157\nM v[100:103],a[112:115],v[220:223],v[100:103]\n" \
"ds_read_b128 a[48:51],v250 offset:12288\nds_read_b128 a[52:55],v250 offset:13312\nM v[108:111],a[84:87],v[204:207],v[108:111]\nv_exp_f32 v158,v158\nM v[108:111],a[116:119],v[220:223],v[108:111]\nbuffer_load_dwordx4 v240,s[48:51],s95 offen offset:3072 sc1 lds\nM v[116:119],a[88:91],v[204:207],v[116:119]\nv_exp_f32 v159,v159\n" \
"M v[116:119],a[120:123],v[220:223],v[116:119]\nds_read_b128 a[56:59],v250 offset:14336\nds_read_b128 a[60:63],v250 offset:15360\nM v[124:127],a[92:95],v[204:207],v[124:127]\nv_cvt_pk_bf16_f32 v200,v136,v137\nv_cvt_pk_bf16_f32 v201,v138,v139\nM v[124:127],a[124:127],v[220:223],v[124:127]\nv_cvt_pk_bf16_f32 v202,v152,v153\n" \
"v_cvt_pk_bf16_f32 v203,v154,v155\nM a[204:207],a[208:211],v[204:207],a[204:207]\nv_cvt_pk_bf16_f32 v216,v168,v169\nv_cvt_pk_bf16_f32 v217,v170,v171\nv_cvt_pk_bf16_f32 v218,v184,v185\nv_cvt_pk_bf16_f32 v219,v186,v187\nv_add_u32 v250,s94,v241\ns_add_u32 s95,s95,0x4000\n" \
"M a[204:207],a[208:211],v[220:223],a[204:207]\ns_cmp_lg_u32 s95,s96\ns_cbranch_scc1 .Lsl\ns_waitcnt lgkmcnt(4)\nM v[128:131],a[0:3],a[128:131],v[224:227]\ns_add_u32 m0,s94,s64\nv_cvt_pk_bf16_f32 v204,v140,v141\nM v[128:131],a[4:7],a[132:135],v[128:131]\n" \
"ds_read_b128 a[64:67],v250 offset:0\nds_read_b128 a[96:99],v250 offset:8192\nM v[128:131],a[8:11],a[136:139],v[128:131]\nbuffer_load_dwordx4 v240,s[44:47],s95 offen offset:0 sc1 lds\nM v[128:131],a[12:15],a[140:143],v[128:131]\nv_exp_f32 v188,v188\nM v[132:135],a[12:15],a[156:159],v[228:231]\nds_read_b128 a[68:71],v250 offset:1024\n" \
"ds_read_b128 a[100:103],v250 offset:9216\nM v[132:135],a[8:11],a[152:155],v[132:135]\nv_exp_f32 v189,v189\nM v[132:135],a[4:7],a[148:151],v[132:135]\nv_exp_f32 v190,v190\nM v[132:135],a[0:3],a[144:147],v[132:135]\nds_read_b128 a[72:75],v250 offset:2048\nds_read_b128 a[104:107],v250 offset:10240\n" \
"M v[144:147],a[16:19],a[128:131],v[224:227]\nv_exp_f32 v191,v191\nM v[144:147],a[20:23],a[132:135],v[144:147]\nv_exp_f32 v172,v172\nM v[144:147],a[24:27],a[136:139],v[144:147]\nds_read_b128 a[76:79],v250 offset:3072\nds_read_b128 a[108:111],v250 offset:11264\nM v[144:147],a[28:31],a[140:143],v[144:147]\n" \
"buffer_load_dwordx4 v240,s[44:47],s95 offen offset:1024 sc1 lds\nM v[148:151],a[28:31],a[156:159],v[228:231]\nv_exp_f32 v173,v173\nM v[148:151],a[24:27],a[152:155],v[148:151]\nds_read_b128 a[80:83],v250 offset:4096\nds_read_b128 a[112:115],v250 offset:12288\nM v[148:151],a[20:23],a[148:151],v[148:151]\nv_exp_f32 v174,v174\n" \
"M v[148:151],a[16:19],a[144:147],v[148:151]\nv_exp_f32 v175,v175\nM v[160:163],a[32:35],a[128:131],v[224:227]\nds_read_b128 a[84:87],v250 offset:5120\nds_read_b128 a[116:119],v250 offset:13312\nM v[160:163],a[36:39],a[132:135],v[160:163]\nv_cvt_pk_bf16_f32 v205,v142,v143\nv_cvt_pk_bf16_f32 v206,v156,v157\n" \
"M v[160:163],a[40:43],a[136:139],v[160:163]\nv_cvt_pk_bf16_f32 v207,v158,v159\nv_cvt_pk_bf16_f32 v220,v172,v173\nM v[160:163],a[44:47],a[140:143],v[160:163]\nds_read_b128 a[88:91],v250 offset:6144\nds_read_b128 a[120:123],v250 offset:14336\nM v[164:167],a[44:47],a[156:159],v[228:231]\nbuffer_load_dwordx4 v240,s[44:47],s95 offen offset:2048 sc1 lds\n" \
"M v[164:167],a[40:43],a[152:155],v[164:167]\nv_cvt_pk_bf16_f32 v221,v174,v175\nv_cvt_pk_bf16_f32 v222,v188,v189\nM v[164:167],a[36:39],a[148:151],v[164:167]\nds_read_b128 a[92:95],v250 offset:7168\nds_read_b128 a[124:127],v250 offset:15360\nM v[164:167],a[32:35],a[144:147],v[164:167]\nv_xor_b32 v250,0x18000,v250\n" \
"s_waitcnt lgkmcnt(7)\nM v[176:179],a[48:51],a[128:131],v[224:227]\nv_cvt_pk_bf16_f32 v223,v190,v191\ns_add_u32 s94,s94,0x4000\nM v[176:179],a[52:55],a[132:135],v[176:179]\nv_exp_f32 v128,v128\nM v[176:179],a[56:59],a[136:139],v[176:179]\nv_exp_f32 v129,v129\n" \
"M v[176:179],a[60:63],a[140:143],v[176:179]\nv_exp_f32 v130,v130\nM v[180:183],a[60:63],a[156:159],v[228:231]\nv_exp_f32 v131,v131\nM v[180:183],a[56:59],a[152:155],v[180:183]\nbuffer_load_dwordx4 v240,s[44:47],s95 offen offset:3072 sc1 lds\nM v[180:183],a[52:55],a[148:151],v[180:183]\ns_add_u32 m0,m0,0x10000\n" \
"s_and_b32 s94,s94,0xffff\nM v[180:183],a[48:51],a[144:147],v[180:183]\nv_exp_f32 v144,v144\nM v[64:67],a[64:67],v[200:203],v[64:67]\nv_exp_f32 v145,v145\nM v[64:67],a[96:99],v[216:219],v[64:67]\nv_exp_f32 v146,v146\nM v[72:75],a[68:71],v[200:203],v[72:75]\n" \
"s_waitcnt vmcnt(16) lgkmcnt(0)\ns_barrier\nM v[72:75],a[100:103],v[216:219],v[72:75]\nds_read_b128 a[0:3],v250 offset:0\nds_read_b128 a[4:7],v250 offset:1024\nM v[80:83],a[72:75],v[200:203],v[80:83]\nv_exp_f32 v147,v147\nM v[80:83],a[104:107],v[216:219],v[80:83]\n" \
"buffer_load_dwordx4 v240,s[48:51],s95 offen offset:0 sc1 lds\nM v[88:91],a[76:79],v[200:203],v[88:91]\nv_exp_f32 v160,v160\nM v[88:91],a[108:111],v[216:219],v[88:91]\nds_read_b128 a[8:11],v250 offset:2048\nds_read_b128 a[12:15],v250 offset:3072\nM v[96:99],a[80:83],v[200:203],v[96:99]\nv_exp_f32 v161,v161\n" \
"M v[96:99],a[112:115],v[216:219],v[96:99]\nv_exp_f32 v162,v162\nM v[104:107],a[84:87],v[200:203],v[104:107]\nv_exp_f32 v163,v163\nM v[104:107],a[116:119],v[216:219],v[104:107]\nds_read_b128 a[16:19],v250 offset:4096\nds_read_b128 a[20:23],v250 offset:5120\nM v[112:115],a[88:91],v[200:203],v[112:115]\n" \
"v_exp_f32 v176,v176\nM v[112:115],a[120:123],v[216:219],v[112:115]\nbuffer_load_dwordx4 v240,s[48:51],s95 offen offset:1024 sc1 lds\nM v[120:123],a[92:95],v[200:203],v[120:123]\nv_exp_f32 v177,v177\nM v[120:123],a[124:127],v[216:219],v[120:123]\nds_read_b128 a[24:27],v250 offset:6144\nds_read_b128 a[28:31],v250 offset:7168\n" \
"M a[200:203],a[208:211],v[200:203],a[200:203]\nv_exp_f32 v178,v178\nM a[200:203],a[208:211],v[216:219],a[200:203]\nv_exp_f32 v179,v179\nM v[68:71],a[64:67],v[204:207],v[68:71]\nv_exp_f32 v132,v132\nM v[68:71],a[96:99],v[220:223],v[68:71]\nds_read_b128 a[32:35],v250 offset:8192\n" \
"ds_read_b128 a[36:39],v250 offset:9216\nM v[76:79],a[68:71],v[204:207],v[76:79]\nv_exp_f32 v133,v133\nM v[76:79],a[100:103],v[220:223],v[76:79]\nbuffer_load_dwordx4 v240,s[48:51],s95 offen offset:2048 sc1 lds\nM v[84:87],a[72:75],v[204:207],v[84:87]\nv_exp_f32 v134,v134\nM v[84:87],a[104:107],v[220:223],v[84:87]\n" \
"ds_read_b128 a[40:43],v250 offset:10240\nds_read_b128 a[44:47],v250 offset:11264\nM v[92:95],a[76:79],v[204:207],v[92:95]\nv_exp_f32 v135,v135\nM v[92:95],a[108:111],v[220:223],v[92:95]\nv_exp_f32 v148,v148\nM v[100:103],a[80:83],v[204:207],v[100:103]\nv_exp_f32 v149,v149\n" \
"M v[100:103],a[112:115],v[220:223],v[100:103]\nds_read_b128 a[48:51],v250 offset:12288\nds_read_b128 a[52:55],v250 offset:13312\nM v[108:111],a[84:87],v[204:207],v[108:111]\nv_exp_f32 v150,v150\nM v[108:111],a[116:119],v[220:223],v[108:111]\nbuffer_load_dwordx4 v240,s[48:51],s95 offen offset:3072 sc1 lds\nM v[116:119],a[88:91],v[204:207],v[116:119]\n" \
"v_exp_f32 v151,v151\nM v[116:119],a[120:123],v[220:223],v[116:119]\nds_read_b128 a[56:59],v250 offset:14336\nds_read_b128 a[60:63],v250 offset:15360\nM v[124:127],a[92:95],v[204:207],v[124:127]\nv_cvt_pk_bf16_f32 v192,v128,v129\nv_cvt_pk_bf16_f32 v193,v130,v131\nM v[124:127],a[124:127],v[220:223],v[124:127]\n" \
"v_cvt_pk_bf16_f32 v194,v144,v145\nv_cvt_pk_bf16_f32 v195,v146,v147\nM a[204:207],a[208:211],v[204:207],a[204:207]\nv_cvt_pk_bf16_f32 v208,v160,v161\nv_cvt_pk_bf16_f32 v209,v162,v163\nv_cvt_pk_bf16_f32 v210,v176,v177\nv_cvt_pk_bf16_f32 v211,v178,v179\nv_add_u32 v250,s94,v241\n" \
"s_add_u32 s95,s95,0x4000\nM a[204:207],a[208:211],v[220:223],a[204:207]\ns_branch .Lse\n.Ltrans:\ns_waitcnt lgkmcnt(4)\nM v[128:131],a[0:3],a[128:131],v[224:227]\ns_add_u32 m0,s94,s64\nv_cvt_pk_bf16_f32 v212,v164,v165\n" \
"M v[128:131],a[4:7],a[132:135],v[128:131]\nds_read_b128 a[64:67],v250 offset:0\nds_read_b128 a[96:99],v250 offset:8192\nM v[128:131],a[8:11],a[136:139],v[128:131]\nbuffer_load_dwordx4 v240,s[44:47],s95 offen offset:0 sc1 lds\nM v[128:131],a[12:15],a[140:143],v[128:131]\nv_cvt_pk_bf16_f32 v213,v166,v167\nv_cvt_pk_bf16_f32 v214,v180,v181\n" \
"M v[132:135],a[12:15],a[156:159],v[228:231]\nds_read_b128 a[68:71],v250 offset:1024\nds_read_b128 a[100:103],v250 offset:9216\nM v[132:135],a[8:11],a[152:155],v[132:135]\nbuffer_store_dwordx4 a[248:251],v253,s[52:55],s33 offen offset:0 sc1\nM v[132:135],a[4:7],a[148:151],v[132:135]\nv_cvt_pk_bf16_f32 v215,v182,v183\ns_add_u32 s71,s33,16384\n" \
"M v[132:135],a[0:3],a[144:147],v[132:135]\nds_read_b128 a[72:75],v250 offset:2048\nds_read_b128 a[104:107],v250 offset:10240\nM v[144:147],a[16:19],a[128:131],v[224:227]\nv_exp_f32 v184,v184\nM v[144:147],a[20:23],a[132:135],v[144:147]\nv_exp_f32 v185,v185\nM v[144:147],a[24:27],a[136:139],v[144:147]\n" \
"ds_read_b128 a[76:79],v250 offset:3072\nds_read_b128 a[108:111],v250 offset:11264\nM v[144:147],a[28:31],a[140:143],v[144:147]\nbuffer_load_dwordx4 v240,s[44:47],s95 offen offset:1024 sc1 lds\nM v[148:151],a[28:31],a[156:159],v[228:231]\nv_exp_f32 v186,v186\nM v[148:151],a[24:27],a[152:155],v[148:151]\nds_read_b128 a[80:83],v250 offset:4096\n" \
"ds_read_b128 a[112:115],v250 offset:12288\nM v[148:151],a[20:23],a[148:151],v[148:151]\nbuffer_store_dwordx4 a[248:251],v253,s[52:55],s33 offen offset:1024 sc1\nM v[148:151],a[16:19],a[144:147],v[148:151]\nv_exp_f32 v187,v187\nM v[160:163],a[32:35],a[128:131],v[224:227]\nds_read_b128 a[84:87],v250 offset:5120\nds_read_b128 a[116:119],v250 offset:13312\n" \
"M v[160:163],a[36:39],a[132:135],v[160:163]\nv_cvt_pk_bf16_f32 v216,v168,v169\nv_cvt_pk_bf16_f32 v217,v170,v171\nM v[160:163],a[40:43],a[136:139],v[160:163]\nv_cvt_pk_bf16_f32 v218,v184,v185\nv_cvt_pk_bf16_f32 v219,v186,v187\nM v[160:163],a[44:47],a[140:143],v[160:163]\nds_read_b128 a[88:91],v250 offset:6144\n" \
"ds_read_b128 a[120:123],v250 offset:14336\nM v[164:167],a[44:47],a[156:159],v[228:231]\nbuffer_load_dwordx4 v240,s[44:47],s95 offen offset:2048 sc1 lds\nM v[164:167],a[40:43],a[152:155],v[164:167]\ns_add_u32 s70,s78,16384\nv_cvt_pk_bf16_f32 v204,v140,v141\nM v[164:167],a[36:39],a[148:151],v[164:167]\nds_read_b128 a[92:95],v250 offset:7168\n" \
"ds_read_b128 a[124:127],v250 offset:15360\nM v[164:167],a[32:35],a[144:147],v[164:167]\nbuffer_store_dwordx4 a[248:251],v253,s[52:55],s33 offen offset:2048 sc1\ns_waitcnt lgkmcnt(6)\nM v[176:179],a[48:51],a[128:131],v[224:227]\nv_xor_b32 v250,0x18000,v250\nv_cvt_pk_bf16_f32 v205,v142,v143\nM v[176:179],a[52:55],a[132:135],v[176:179]\n" \
"v_exp_f32 v172,v172\nM v[176:179],a[56:59],a[136:139],v[176:179]\nv_exp_f32 v173,v173\nM v[176:179],a[60:63],a[140:143],v[176:179]\nv_exp_f32 v174,v174\nM v[180:183],a[60:63],a[156:159],v[228:231]\nv_exp_f32 v175,v175\nM v[180:183],a[56:59],a[152:155],v[180:183]\n" \
"buffer_load_dwordx4 v240,s[44:47],s95 offen offset:3072 sc1 lds\nM v[180:183],a[52:55],a[148:151],v[180:183]\ns_add_u32 m0,m0,0x10000\nv_cvt_pk_bf16_f32 v206,v156,v157\nM v[180:183],a[48:51],a[144:147],v[180:183]\nv_exp_f32 v188,v188\nM v[0:3],a[64:67],v[192:195],v[0:3]\nbuffer_store_dwordx4 a[248:251],v253,s[52:55],s33 offen offset:3072 sc1\n" \
"M v[0:3],a[96:99],v[208:211],v[0:3]\nv_exp_f32 v189,v189\nM v[8:11],a[68:71],v[192:195],v[8:11]\ns_waitcnt vmcnt(16) lgkmcnt(0)\ns_barrier\nM v[8:11],a[100:103],v[208:211],v[8:11]\nds_read_b128 a[0:3],v250 offset:0\nds_read_b128 a[4:7],v250 offset:1024\n" \
"M v[16:19],a[72:75],v[192:195],v[16:19]\nv_exp_f32 v190,v190\nM v[16:19],a[104:107],v[208:211],v[16:19]\nbuffer_store_dwordx4 a[248:251],v253,s[52:55],s71 offen offset:0 sc1\nM v[24:27],a[76:79],v[192:195],v[24:27]\nv_exp_f32 v191,v191\nM v[24:27],a[108:111],v[208:211],v[24:27]\nbuffer_load_dwordx4 v240,s[48:51],s95 offen offset:0 sc1 lds\n" \
"M v[32:35],a[80:83],v[192:195],v[32:35]\nv_cvt_pk_bf16_f32 v207,v158,v159\nv_cvt_pk_bf16_f32 v220,v172,v173\nM v[32:35],a[112:115],v[208:211],v[32:35]\nbuffer_store_dwordx4 a[248:251],v253,s[52:55],s71 offen offset:1024 sc1\nM v[40:43],a[84:87],v[192:195],v[40:43]\nv_cvt_pk_bf16_f32 v221,v174,v175\nv_cvt_pk_bf16_f32 v222,v188,v189\n" \
"M v[40:43],a[116:119],v[208:211],v[40:43]\nds_read_b128 a[8:11],v250 offset:2048\nds_read_b128 a[12:15],v250 offset:3072\nM v[48:51],a[88:91],v[192:195],v[48:51]\nv_cvt_pk_bf16_f32 v223,v190,v191\ns_add_u32 s94,s94,0x4000\nM v[48:51],a[120:123],v[208:211],v[48:51]\nbuffer_store_dwordx4 a[248:251],v253,s[52:55],s71 offen offset:2048 sc1\n" \
"M v[56:59],a[92:95],v[192:195],v[56:59]\nv_exp_f32 v128,v128\nM v[56:59],a[124:127],v[208:211],v[56:59]\nv_exp_f32 v129,v129\nM a[192:195],a[208:211],v[192:195],a[192:195]\nv_exp_f32 v130,v130\nM a[192:195],a[208:211],v[208:211],a[192:195]\nbuffer_store_dwordx4 a[248:251],v253,s[52:55],s71 offen offset:3072 sc1\n" \
"M v[4:7],a[64:67],v[196:199],v[4:7]\nv_exp_f32 v131,v131\nM v[4:7],a[96:99],v[212:215],v[4:7]\nds_read_b128 a[16:19],v250 offset:4096\nds_read_b128 a[20:23],v250 offset:5120\nM v[12:15],a[68:71],v[196:199],v[12:15]\nv_exp_f32 v144,v144\nM v[12:15],a[100:103],v[212:215],v[12:15]\n" \
"v_exp_f32 v145,v145\nM v[20:23],a[72:75],v[196:199],v[20:23]\nv_exp_f32 v146,v146\nM v[20:23],a[104:107],v[212:215],v[20:23]\nbuffer_load_dwordx4 v240,s[48:51],s95 offen offset:1024 sc1 lds\nM v[28:31],a[76:79],v[196:199],v[28:31]\nv_exp_f32 v147,v147\nM v[28:31],a[108:111],v[212:215],v[28:31]\n" \
"buffer_store_dwordx4 a[248:251],v253,s[52:55],s78 offen offset:0 sc1\nM v[36:39],a[80:83],v[196:199],v[36:39]\nv_exp_f32 v160,v160\nM v[36:39],a[112:115],v[212:215],v[36:39]\nds_read_b128 a[24:27],v250 offset:6144\nds_read_b128 a[28:31],v250 offset:7168\nM v[44:47],a[84:87],v[196:199],v[44:47]\nv_exp_f32 v161,v161\n" \
"M v[44:47],a[116:119],v[212:215],v[44:47]\nbuffer_store_dwordx4 a[248:251],v253,s[52:55],s78 offen offset:1024 sc1\nM v[52:55],a[88:91],v[196:199],v[52:55]\nv_exp_f32 v162,v162\nM v[52:55],a[120:123],v[212:215],v[52:55]\nv_exp_f32 v163,v163\nM v[60:63],a[92:95],v[196:199],v[60:63]\nv_exp_f32 v176,v176\n" \
"M v[60:63],a[124:127],v[212:215],v[60:63]\nbuffer_store_dwordx4 a[248:251],v253,s[52:55],s78 offen offset:2048 sc1\nM a[196:199],a[208:211],v[196:199],a[196:199]\nv_exp_f32 v177,v177\nM a[196:199],a[208:211],v[212:215],a[196:199]\nds_read_b128 a[32:35],v250 offset:8192\nds_read_b128 a[36:39],v250 offset:9216\nM v[64:67],a[64:67],v[200:203],v[64:67]\n" \
"v_exp_f32 v178,v178\nM v[64:67],a[96:99],v[216:219],v[64:67]\nv_exp_f32 v179,v179\nM v[72:75],a[68:71],v[200:203],v[72:75]\ns_and_b32 s94,s94,0xffff\nv_cvt_pk_bf16_f32 v192,v128,v129\nM v[72:75],a[100:103],v[216:219],v[72:75]\nbuffer_load_dwordx4 v240,s[48:51],s95 offen offset:2048 sc1 lds\n" \
"M v[80:83],a[72:75],v[200:203],v[80:83]\nv_exp_f32 v132,v132\nM v[80:83],a[104:107],v[216:219],v[80:83]\nbuffer_store_dwordx4 a[248:251],v253,s[52:55],s78 offen offset:3072 sc1\nM v[88:91],a[76:79],v[200:203],v[88:91]\nv_exp_f32 v133,v133\nM v[88:91],a[108:111],v[216:219],v[88:91]\nds_read_b128 a[40:43],v250 offset:10240\n" \
"ds_read_b128 a[44:47],v250 offset:11264\nM v[96:99],a[80:83],v[200:203],v[96:99]\nv_exp_f32 v134,v134\nM v[96:99],a[112:115],v[216:219],v[96:99]\nbuffer_store_dwordx4 a[248:251],v253,s[52:55],s70 offen offset:0 sc1\nM v[104:107],a[84:87],v[200:203],v[104:107]\nv_exp_f32 v135,v135\nM v[104:107],a[116:119],v[216:219],v[104:107]\n" \
"v_exp_f32 v148,v148\nM v[112:115],a[88:91],v[200:203],v[112:115]\nv_exp_f32 v149,v149\nM v[112:115],a[120:123],v[216:219],v[112:115]\nbuffer_store_dwordx4 a[248:251],v253,s[52:55],s70 offen offset:1024 sc1\nM v[120:123],a[92:95],v[200:203],v[120:123]\nv_exp_f32 v150,v150\nM v[120:123],a[124:127],v[216:219],v[120:123]\n" \
"ds_read_b128 a[48:51],v250 offset:12288\nds_read_b128 a[52:55],v250 offset:13312\nM a[200:203],a[208:211],v[200:203],a[200:203]\nv_exp_f32 v151,v151\nM a[200:203],a[208:211],v[216:219],a[200:203]\nv_cvt_pk_bf16_f32 v193,v130,v131\nv_cvt_pk_bf16_f32 v194,v144,v145\nM v[68:71],a[64:67],v[204:207],v[68:71]\n" \
"v_cvt_pk_bf16_f32 v195,v146,v147\nv_cvt_pk_bf16_f32 v208,v160,v161\nM v[68:71],a[96:99],v[220:223],v[68:71]\nbuffer_load_dwordx4 v240,s[48:51],s95 offen offset:3072 sc1 lds\nM v[76:79],a[68:71],v[204:207],v[76:79]\nv_cvt_pk_bf16_f32 v209,v162,v163\nv_cvt_pk_bf16_f32 v210,v176,v177\nM v[76:79],a[100:103],v[220:223],v[76:79]\n" \
"buffer_store_dwordx4 a[248:251],v253,s[52:55],s70 offen offset:2048 sc1\nM v[84:87],a[72:75],v[204:207],v[84:87]\nv_cvt_pk_bf16_f32 v211,v178,v179\ns_add_u32 s95,s95,0x4000\nM v[84:87],a[104:107],v[220:223],v[84:87]\nds_read_b128 a[56:59],v250 offset:14336\nds_read_b128 a[60:63],v250 offset:15360\nM v[92:95],a[76:79],v[204:207],v[92:95]\n" \
"v_add_u32 v250,s94,v241\nM v[92:95],a[108:111],v[220:223],v[92:95]\nbuffer_store_dwordx4 a[248:251],v253,s[52:55],s70 offen offset:3072 sc1\nM v[100:103],a[80:83],v[204:207],v[100:103]\nM v[100:103],a[112:115],v[220:223],v[100:103]\nM v[108:111],a[84:87],v[204:207],v[108:111]\nM v[108:111],a[116:119],v[220:223],v[108:111]\nM v[116:119],a[88:91],v[204:207],v[116:119]\n" \
"M v[116:119],a[120:123],v[220:223],v[116:119]\nM v[124:127],a[92:95],v[204:207],v[124:127]\nM v[124:127],a[124:127],v[220:223],v[124:127]\nM a[204:207],a[208:211],v[204:207],a[204:207]\nM a[204:207],a[208:211],v[220:223],a[204:207]\n.Lse:\ns_waitcnt lgkmcnt(4)\nM v[128:131],a[0:3],a[128:131],v[224:227]\n" \
"s_add_u32 m0,s94,s64\nv_accvgpr_read_b32 v232,a200\nv_cvt_pk_bf16_f32 v196,v132,v133\nv_cvt_pk_bf16_f32 v197,v134,v135\nM v[128:131],a[4:7],a[132:135],v[128:131]\nds_read_b128 a[64:67],v250 offset:0\nds_read_b128 a[96:99],v250 offset:8192\nv_accvgpr_read_b32 v236,a204\n" \
"v_add_f32 v233,v232,v236\nM v[128:131],a[8:11],a[136:139],v[128:131]\nbuffer_load_dwordx4 v240,s[44:47],s95 offen offset:0 sc1 lds\nv_rcp_f32 v232,v232\nM v[128:131],a[12:15],a[140:143],v[128:131]\nv_rcp_f32 v236,v236\nv_add_f32 v233,v233,v232\nv_add_f32 v233,v233,v236\n" \
"M v[132:135],a[12:15],a[156:159],v[228:231]\nds_read_b128 a[68:71],v250 offset:1024\nds_read_b128 a[100:103],v250 offset:9216\nv_cmp_ngt_f32 vcc,s28,v233\nv_mul_f32 v64,v64,v232\nM v[132:135],a[8:11],a[152:155],v[132:135]\ns_and_b64 vcc,vcc,s[90:91]\ns_cselect_b32 s39,0x8000000,s16\n" \
"s_cselect_b32 s16,s16,0x8000000\ns_cselect_b32 s71,2,1\ns_addc_u32 s99,s99,0\nM v[132:135],a[4:7],a[148:151],v[132:135]\nv_mul_f32 v65,v65,v232\ns_add_u32 s37,s12,s67\nv_mul_f32 v66,v66,v232\nv_mul_f32 v67,v67,v232\n" \
"M v[132:135],a[0:3],a[144:147],v[132:135]\nds_read_b128 a[72:75],v250 offset:2048\nds_read_b128 a[104:107],v250 offset:10240\nv_cvt_pk_bf16_f32 v64,v64,v65\ns_add_u32 s37,s37,1048576\nM v[144:147],a[16:19],a[128:131],v[224:227]\nv_mul_f32 v72,v72,v232\nv_mul_f32 v73,v73,v232\n" \
"v_mul_f32 v74,v74,v232\nv_mul_f32 v75,v75,v232\nv_cvt_pk_bf16_f32 v65,v66,v67\nM v[144:147],a[20:23],a[132:135],v[144:147]\ns_or_b32 s37,s37,s39\nv_cvt_pk_bf16_f32 v66,v72,v73\nv_cvt_pk_bf16_f32 v67,v74,v75\nbuffer_store_dwordx4 v[64:67],v240,s[52:55],s37 offen offset:0 sc1\n" \
"M v[144:147],a[24:27],a[136:139],v[144:147]\nds_read_b128 a[76:79],v250 offset:3072\nds_read_b128 a[108:111],v250 offset:11264\nv_cvt_pk_bf16_f32 v198,v148,v149\nv_cvt_pk_bf16_f32 v199,v150,v151\nv_mul_f32 v68,v68,v236\nM v[144:147],a[28:31],a[140:143],v[144:147]\nbuffer_load_dwordx4 v240,s[44:47],s95 offen offset:1024 sc1 lds\n" \
"v_mul_f32 v69,v69,v236\nv_mul_f32 v70,v70,v236\nM v[148:151],a[28:31],a[156:159],v[228:231]\nv_mul_f32 v71,v71,v236\nv_cvt_pk_bf16_f32 v68,v68,v69\nv_mul_f32 v76,v76,v236\nv_mul_f32 v77,v77,v236\nM v[148:151],a[24:27],a[152:155],v[148:151]\n" \
"ds_read_b128 a[80:83],v250 offset:4096\nds_read_b128 a[112:115],v250 offset:12288\nv_mul_f32 v78,v78,v236\nv_mul_f32 v79,v79,v236\nM v[148:151],a[20:23],a[148:151],v[148:151]\nv_cvt_pk_bf16_f32 v69,v70,v71\nv_cvt_pk_bf16_f32 v70,v76,v77\nv_cvt_pk_bf16_f32 v71,v78,v79\n" \
"v_mul_f32 v80,v80,v232\nM v[148:151],a[16:19],a[144:147],v[148:151]\nv_exp_f32 v164,v164\nv_exp_f32 v165,v165\nM v[160:163],a[32:35],a[128:131],v[224:227]\nds_read_b128 a[84:87],v250 offset:5120\nds_read_b128 a[116:119],v250 offset:13312\nv_exp_f32 v166,v166\n" \
"v_cvt_pk_bf16_f32 v212,v164,v165\nM v[160:163],a[36:39],a[132:135],v[160:163]\nbuffer_store_dwordx4 v[68:71],v240,s[52:55],s37 offen offset:1024 sc1\nv_exp_f32 v167,v167\nM v[160:163],a[40:43],a[136:139],v[160:163]\nv_cvt_pk_bf16_f32 v213,v166,v167\nv_mul_f32 v81,v81,v232\nv_mul_f32 v82,v82,v232\n" \
"v_mul_f32 v83,v83,v232\nM v[160:163],a[44:47],a[140:143],v[160:163]\nds_read_b128 a[88:91],v250 offset:6144\nds_read_b128 a[120:123],v250 offset:14336\nv_cvt_pk_bf16_f32 v80,v80,v81\nv_mul_f32 v88,v88,v232\nM v[164:167],a[44:47],a[156:159],v[228:231]\nbuffer_load_dwordx4 v240,s[44:47],s95 offen offset:2048 sc1 lds\n" \
"v_mul_f32 v89,v89,v232\nv_mul_f32 v90,v90,v232\nM v[164:167],a[40:43],a[152:155],v[164:167]\nv_mul_f32 v91,v91,v232\nv_cvt_pk_bf16_f32 v81,v82,v83\nv_cvt_pk_bf16_f32 v82,v88,v89\nv_cvt_pk_bf16_f32 v83,v90,v91\nv_mul_f32 v84,v84,v236\n" \
"M v[164:167],a[36:39],a[148:151],v[164:167]\nds_read_b128 a[92:95],v250 offset:7168\nds_read_b128 a[124:127],v250 offset:15360\nv_exp_f32 v180,v180\nM v[164:167],a[32:35],a[144:147],v[164:167]\nv_exp_f32 v181,v181\nv_mul_f32 v85,v85,v236\ns_waitcnt lgkmcnt(4)\n" \
"M v[176:179],a[48:51],a[128:131],v[224:227]\nv_exp_f32 v182,v182\nv_exp_f32 v183,v183\nM v[176:179],a[52:55],a[132:135],v[176:179]\nbuffer_store_dwordx4 v[80:83],v240,s[52:55],s37 offen offset:2048 sc1\nv_cvt_pk_bf16_f32 v214,v180,v181\nv_cvt_pk_bf16_f32 v215,v182,v183\nM v[176:179],a[56:59],a[136:139],v[176:179]\n" \
"v_mul_f32 v86,v86,v236\nv_mul_f32 v87,v87,v236\nv_cvt_pk_bf16_f32 v84,v84,v85\nv_mul_f32 v92,v92,v236\nM v[176:179],a[60:63],a[140:143],v[176:179]\nv_mul_f32 v93,v93,v236\nv_mul_f32 v94,v94,v236\nv_mul_f32 v95,v95,v236\n" \
"v_cvt_pk_bf16_f32 v85,v86,v87\nv_cvt_pk_bf16_f32 v86,v92,v93\nM v[180:183],a[60:63],a[156:159],v[228:231]\nv_cvt_pk_bf16_f32 v87,v94,v95\nv_xor_b32 v250,0x18000,v250\nv_mul_f32 v96,v96,v232\nv_mul_f32 v97,v97,v232\nM v[180:183],a[56:59],a[152:155],v[180:183]\n" \
"buffer_load_dwordx4 v240,s[44:47],s95 offen offset:3072 sc1 lds\ns_add_u32 m0,m0,0x10000\nv_mul_f32 v98,v98,v232\nM v[180:183],a[52:55],a[148:151],v[180:183]\nv_mul_f32 v99,v99,v232\nv_cvt_pk_bf16_f32 v96,v96,v97\nv_mul_f32 v104,v104,v232\nv_mul_f32 v105,v105,v232\n" \
"M v[180:183],a[48:51],a[144:147],v[180:183]\nv_mul_f32 v106,v106,v232\nv_mul_f32 v107,v107,v232\nv_cvt_pk_bf16_f32 v97,v98,v99\ns_add_u32 s38,s37,4096\nM v[0:3],a[64:67],v[192:195],v[0:3]\nv_cvt_pk_bf16_f32 v98,v104,v105\nv_cvt_pk_bf16_f32 v99,v106,v107\n" \
"v_mul_f32 v100,v100,v236\nv_mul_f32 v101,v101,v236\nv_mul_f32 v102,v102,v236\nM v[0:3],a[96:99],v[208:211],v[0:3]\nbuffer_store_dwordx4 v[84:87],v240,s[52:55],s37 offen offset:3072 sc1\nv_mul_f32 v103,v103,v236\nv_cvt_pk_bf16_f32 v100,v100,v101\nM v[8:11],a[68:71],v[192:195],v[8:11]\n" \
"s_waitcnt vmcnt(16) lgkmcnt(0)\ns_barrier\nv_mul_f32 v108,v108,v236\nv_mul_f32 v109,v109,v236\nM v[8:11],a[100:103],v[208:211],v[8:11]\nds_read_b128 a[0:3],v250 offset:0\nds_read_b128 a[4:7],v250 offset:1024\nv_mul_f32 v110,v110,v236\n" \
"v_mul_f32 v111,v111,v236\nM v[16:19],a[72:75],v[192:195],v[16:19]\nv_cvt_pk_bf16_f32 v101,v102,v103\nv_cvt_pk_bf16_f32 v102,v108,v109\nv_cvt_pk_bf16_f32 v103,v110,v111\nv_mul_f32 v112,v112,v232\nM v[16:19],a[104:107],v[208:211],v[16:19]\nbuffer_load_dwordx4 v240,s[48:51],s95 offen offset:0 sc1 lds\n" \
"v_mul_f32 v113,v113,v232\nv_mul_f32 v114,v114,v232\nv_mul_f32 v115,v115,v232\nM v[24:27],a[76:79],v[192:195],v[24:27]\nv_cvt_pk_bf16_f32 v112,v112,v113\nv_mul_f32 v120,v120,v232\nv_mul_f32 v121,v121,v232\nv_mul_f32 v122,v122,v232\n" \
"M v[24:27],a[108:111],v[208:211],v[24:27]\nds_read_b128 a[8:11],v250 offset:2048\nds_read_b128 a[12:15],v250 offset:3072\nv_mul_f32 v123,v123,v232\nv_cvt_pk_bf16_f32 v113,v114,v115\nM v[32:35],a[80:83],v[192:195],v[32:35]\nv_cvt_pk_bf16_f32 v114,v120,v121\nv_cvt_pk_bf16_f32 v115,v122,v123\n" \
"v_mul_f32 v116,v116,v236\nv_mul_f32 v117,v117,v236\nM v[32:35],a[112:115],v[208:211],v[32:35]\nbuffer_store_dwordx4 v[96:99],v240,s[52:55],s38 offen offset:0 sc1\nv_mul_f32 v118,v118,v236\nv_mul_f32 v119,v119,v236\nM v[40:43],a[84:87],v[192:195],v[40:43]\nv_cvt_pk_bf16_f32 v116,v116,v117\n" \
"v_mul_f32 v124,v124,v236\nv_mul_f32 v125,v125,v236\nv_mul_f32 v126,v126,v236\nM v[40:43],a[116:119],v[208:211],v[40:43]\nds_read_b128 a[16:19],v250 offset:4096\nds_read_b128 a[20:23],v250 offset:5120\nv_mul_f32 v127,v127,v236\nv_cvt_pk_bf16_f32 v117,v118,v119\n" \
"v_cvt_pk_bf16_f32 v118,v124,v125\nM v[48:51],a[88:91],v[192:195],v[48:51]\nv_cvt_pk_bf16_f32 v119,v126,v127\nv_exp_f32 v128,v128\ns_add_u32 s94,s94,0x4000\nM v[48:51],a[120:123],v[208:211],v[48:51]\nbuffer_load_dwordx4 v240,s[48:51],s95 offen offset:1024 sc1 lds\nv_exp_f32 v129,v129\n" \
"M v[56:59],a[92:95],v[192:195],v[56:59]\nv_exp_f32 v130,v130\nv_exp_f32 v131,v131\nM v[56:59],a[124:127],v[208:211],v[56:59]\nds_read_b128 a[24:27],v250 offset:6144\nds_read_b128 a[28:31],v250 offset:7168\nv_exp_f32 v144,v144\nM a[192:195],a[208:211],v[192:195],a[192:195]\n" \
"v_exp_f32 v145,v145\nv_exp_f32 v146,v146\ns_lshl2_add_u32 s77,s61,s79\nM a[192:195],a[208:211],v[208:211],a[192:195]\nbuffer_store_dwordx4 v[100:103],v240,s[52:55],s38 offen offset:1024 sc1\nv_exp_f32 v147,v147\nM v[4:7],a[64:67],v[196:199],v[4:7]\nv_exp_f32 v160,v160\n" \
"v_exp_f32 v161,v161\nM v[4:7],a[96:99],v[212:215],v[4:7]\nds_read_b128 a[32:35],v250 offset:8192\nds_read_b128 a[36:39],v250 offset:9216\nv_exp_f32 v162,v162\nM v[12:15],a[68:71],v[196:199],v[12:15]\nv_exp_f32 v163,v163\nv_exp_f32 v176,v176\n" \
"M v[12:15],a[100:103],v[212:215],v[12:15]\nbuffer_load_dwordx4 v240,s[48:51],s95 offen offset:2048 sc1 lds\nv_exp_f32 v177,v177\nM v[20:23],a[72:75],v[196:199],v[20:23]\nv_exp_f32 v178,v178\nv_exp_f32 v179,v179\ns_and_b32 s94,s94,0xffff\nM v[20:23],a[104:107],v[212:215],v[20:23]\n" \
"ds_read_b128 a[40:43],v250 offset:10240\nds_read_b128 a[44:47],v250 offset:11264\ns_add_u32 s77,s77,4096\nv_mov_b32 v233,s71\nM v[28:31],a[76:79],v[196:199],v[28:31]\nv_exp_f32 v132,v132\nv_exp_f32 v133,v133\nM v[28:31],a[108:111],v[212:215],v[28:31]\n" \
"buffer_store_dwordx4 v[112:115],v240,s[52:55],s38 offen offset:2048 sc1\nv_exp_f32 v134,v134\nM v[36:39],a[80:83],v[196:199],v[36:39]\nv_exp_f32 v135,v135\nv_exp_f32 v148,v148\nM v[36:39],a[112:115],v[212:215],v[36:39]\nds_read_b128 a[48:51],v250 offset:12288\nds_read_b128 a[52:55],v250 offset:13312\n" \
"v_exp_f32 v149,v149\nv_cvt_pk_bf16_f32 v192,v128,v129\nM v[44:47],a[84:87],v[196:199],v[44:47]\nv_exp_f32 v150,v150\nv_exp_f32 v151,v151\nM v[44:47],a[116:119],v[212:215],v[44:47]\nbuffer_load_dwordx4 v240,s[48:51],s95 offen offset:3072 sc1 lds\nv_cvt_pk_bf16_f32 v193,v130,v131\n" \
"v_cvt_pk_bf16_f32 v194,v144,v145\nM v[52:55],a[88:91],v[196:199],v[52:55]\nv_cvt_pk_bf16_f32 v195,v146,v147\nv_cvt_pk_bf16_f32 v208,v160,v161\nv_cvt_pk_bf16_f32 v209,v162,v163\nv_cvt_pk_bf16_f32 v210,v176,v177\nM v[52:55],a[120:123],v[212:215],v[52:55]\nds_read_b128 a[56:59],v250 offset:14336\n" \
"ds_read_b128 a[60:63],v250 offset:15360\nv_cvt_pk_bf16_f32 v211,v178,v179\nv_add_u32 v250,s94,v241\nM v[60:63],a[92:95],v[196:199],v[60:63]\ns_add_u32 s95,s95,0x4000\nM v[60:63],a[124:127],v[212:215],v[60:63]\nbuffer_store_dwordx4 v[116:119],v240,s[52:55],s38 offen offset:3072 sc1\nM a[196:199],a[208:211],v[196:199],a[196:199]\n" \
"M a[196:199],a[208:211],v[212:215],a[196:199]\ns_cmp_ge_u32 s95,s15\ns_cbranch_scc1 .Lsx\n.Lsingle:\ns_waitcnt lgkmcnt(4)\nM v[128:131],a[0:3],a[128:131],v[224:227]\ns_xor_b32 m0,m0,0x14000\nv_cvt_pk_bf16_f32 v196,v132,v133\n" \
"M v[128:131],a[4:7],a[132:135],v[128:131]\nds_read_b128 a[64:67],v250 offset:0\nds_read_b128 a[96:99],v250 offset:8192\nM v[128:131],a[8:11],a[136:139],v[128:131]\nbuffer_load_dwordx4 v240,s[44:47],s95 offen offset:0 sc1 lds\nM v[128:131],a[12:15],a[140:143],v[128:131]\nv_cvt_pk_bf16_f32 v197,v134,v135\nv_cvt_pk_bf16_f32 v198,v148,v149\n" \
"M v[132:135],a[12:15],a[156:159],v[228:231]\nds_read_b128 a[68:71],v250 offset:1024\nds_read_b128 a[100:103],v250 offset:9216\nM v[132:135],a[8:11],a[152:155],v[132:135]\nv_exp_f32 v164,v164\nM v[132:135],a[4:7],a[148:151],v[132:135]\nv_exp_f32 v165,v165\nM v[132:135],a[0:3],a[144:147],v[132:135]\n" \
"ds_read_b128 a[72:75],v250 offset:2048\nds_read_b128 a[104:107],v250 offset:10240\nM v[144:147],a[16:19],a[128:131],v[224:227]\nv_cvt_pk_bf16_f32 v199,v150,v151\nv_cvt_pk_bf16_f32 v212,v164,v165\nM v[144:147],a[20:23],a[132:135],v[144:147]\nv_exp_f32 v166,v166\nM v[144:147],a[24:27],a[136:139],v[144:147]\n" \
"ds_read_b128 a[76:79],v250 offset:3072\nds_read_b128 a[108:111],v250 offset:11264\nM v[144:147],a[28:31],a[140:143],v[144:147]\nbuffer_load_dwordx4 v240,s[44:47],s95 offen offset:1024 sc1 lds\nM v[148:151],a[28:31],a[156:159],v[228:231]\nv_exp_f32 v167,v167\nM v[148:151],a[24:27],a[152:155],v[148:151]\nds_read_b128 a[80:83],v250 offset:4096\n" \
"ds_read_b128 a[112:115],v250 offset:12288\nM v[148:151],a[20:23],a[148:151],v[148:151]\nv_exp_f32 v180,v180\nM v[148:151],a[16:19],a[144:147],v[148:151]\nv_exp_f32 v181,v181\nM v[160:163],a[32:35],a[128:131],v[224:227]\nds_read_b128 a[84:87],v250 offset:5120\nds_read_b128 a[116:119],v250 offset:13312\n" \
"M v[160:163],a[36:39],a[132:135],v[160:163]\nv_cvt_pk_bf16_f32 v213,v166,v167\nv_cvt_pk_bf16_f32 v214,v180,v181\nM v[160:163],a[40:43],a[136:139],v[160:163]\nv_exp_f32 v182,v182\nM v[160:163],a[44:47],a[140:143],v[160:163]\nds_read_b128 a[88:91],v250 offset:6144\nds_read_b128 a[120:123],v250 offset:14336\n" \
"M v[164:167],a[44:47],a[156:159],v[228:231]\nbuffer_load_dwordx4 v240,s[44:47],s95 offen offset:2048 sc1 lds\nM v[164:167],a[40:43],a[152:155],v[164:167]\nv_exp_f32 v183,v183\nM v[164:167],a[36:39],a[148:151],v[164:167]\nds_read_b128 a[92:95],v250 offset:7168\nds_read_b128 a[124:127],v250 offset:15360\nM v[164:167],a[32:35],a[144:147],v[164:167]\n" \
"v_cvt_pk_bf16_f32 v215,v182,v183\ns_waitcnt lgkmcnt(7)\nM v[176:179],a[48:51],a[128:131],v[224:227]\nv_exp_f32 v128,v128\nM v[176:179],a[52:55],a[132:135],v[176:179]\nv_exp_f32 v129,v129\nM v[176:179],a[56:59],a[136:139],v[176:179]\nv_exp_f32 v130,v130\n" \
"M v[176:179],a[60:63],a[140:143],v[176:179]\nv_exp_f32 v131,v131\nM v[180:183],a[60:63],a[156:159],v[228:231]\nv_exp_f32 v132,v132\nM v[180:183],a[56:59],a[152:155],v[180:183]\nbuffer_load_dwordx4 v240,s[44:47],s95 offen offset:3072 sc1 lds\nM v[180:183],a[52:55],a[148:151],v[180:183]\nv_xor_b32 v250,0x18000,v250\n" \
"s_xor_b32 m0,m0,0x10000\nM v[180:183],a[48:51],a[144:147],v[180:183]\nv_exp_f32 v133,v133\nM v[0:3],a[64:67],v[192:195],v[0:3]\nv_exp_f32 v134,v134\nM v[0:3],a[96:99],v[208:211],v[0:3]\nv_exp_f32 v135,v135\nM v[8:11],a[68:71],v[192:195],v[8:11]\n" \
"s_waitcnt vmcnt(16) lgkmcnt(0)\ns_barrier\nM v[8:11],a[100:103],v[208:211],v[8:11]\nds_read_b128 a[0:3],v250 offset:0\nds_read_b128 a[4:7],v250 offset:1024\nM v[16:19],a[72:75],v[192:195],v[16:19]\nv_exp_f32 v144,v144\nM v[16:19],a[104:107],v[208:211],v[16:19]\n" \
"buffer_load_dwordx4 v240,s[48:51],s95 offen offset:0 sc1 lds\nM v[24:27],a[76:79],v[192:195],v[24:27]\nv_exp_f32 v145,v145\nM v[24:27],a[108:111],v[208:211],v[24:27]\nds_read_b128 a[8:11],v250 offset:2048\nds_read_b128 a[12:15],v250 offset:3072\nM v[32:35],a[80:83],v[192:195],v[32:35]\nv_exp_f32 v146,v146\n" \
"M v[32:35],a[112:115],v[208:211],v[32:35]\nv_exp_f32 v147,v147\nM v[40:43],a[84:87],v[192:195],v[40:43]\nv_exp_f32 v148,v148\nM v[40:43],a[116:119],v[208:211],v[40:43]\nds_read_b128 a[16:19],v250 offset:4096\nds_read_b128 a[20:23],v250 offset:5120\nM v[48:51],a[88:91],v[192:195],v[48:51]\n" \
"v_exp_f32 v149,v149\nM v[48:51],a[120:123],v[208:211],v[48:51]\nbuffer_load_dwordx4 v240,s[48:51],s95 offen offset:1024 sc1 lds\nM v[56:59],a[92:95],v[192:195],v[56:59]\nv_exp_f32 v150,v150\nM v[56:59],a[124:127],v[208:211],v[56:59]\nds_read_b128 a[24:27],v250 offset:6144\nds_read_b128 a[28:31],v250 offset:7168\n" \
"M a[192:195],a[208:211],v[192:195],a[192:195]\nv_cvt_pk_bf16_f32 v192,v128,v129\nv_cvt_pk_bf16_f32 v193,v130,v131\nM a[192:195],a[208:211],v[208:211],a[192:195]\nv_cvt_pk_bf16_f32 v194,v144,v145\nv_cvt_pk_bf16_f32 v195,v146,v147\nM v[4:7],a[64:67],v[196:199],v[4:7]\nv_exp_f32 v151,v151\n" \
"M v[4:7],a[96:99],v[212:215],v[4:7]\nds_read_b128 a[32:35],v250 offset:8192\nds_read_b128 a[36:39],v250 offset:9216\nM v[12:15],a[68:71],v[196:199],v[12:15]\nv_exp_f32 v160,v160\nM v[12:15],a[100:103],v[212:215],v[12:15]\nbuffer_load_dwordx4 v240,s[48:51],s95 offen offset:2048 sc1 lds\nM v[20:23],a[72:75],v[196:199],v[20:23]\n" \
"v_exp_f32 v161,v161\nM v[20:23],a[104:107],v[212:215],v[20:23]\nds_read_b128 a[40:43],v250 offset:10240\nds_read_b128 a[44:47],v250 offset:11264\nM v[28:31],a[76:79],v[196:199],v[28:31]\nv_exp_f32 v162,v162\nM v[28:31],a[108:111],v[212:215],v[28:31]\nv_exp_f32 v163,v163\n" \
"M v[36:39],a[80:83],v[196:199],v[36:39]\nv_cvt_pk_bf16_f32 v208,v160,v161\nv_cvt_pk_bf16_f32 v209,v162,v163\nM v[36:39],a[112:115],v[212:215],v[36:39]\nds_read_b128 a[48:51],v250 offset:12288\nds_read_b128 a[52:55],v250 offset:13312\nM v[44:47],a[84:87],v[196:199],v[44:47]\nv_exp_f32 v164,v164\n" \
"M v[44:47],a[116:119],v[212:215],v[44:47]\nbuffer_load_dwordx4 v240,s[48:51],s95 offen offset:3072 sc1 lds\nM v[52:55],a[88:91],v[196:199],v[52:55]\ns_add_u32 s95,s95,0x4000\ns_xor_b32 m0,m0,0x1c000\nM v[52:55],a[120:123],v[212:215],v[52:55]\nds_read_b128 a[56:59],v250 offset:14336\nds_read_b128 a[60:63],v250 offset:15360\n" \
"M v[60:63],a[92:95],v[196:199],v[60:63]\nv_exp_f32 v165,v165\nM v[60:63],a[124:127],v[212:215],v[60:63]\nv_exp_f32 v166,v166\nM a[196:199],a[208:211],v[196:199],a[196:199]\nv_xor_b32 v250,0x14000,v250\nv_cvt_pk_bf16_f32 v196,v132,v133\nM a[196:199],a[208:211],v[212:215],a[196:199]\n" \
"v_cvt_pk_bf16_f32 v197,v134,v135\ns_waitcnt lgkmcnt(4)\nM v[128:131],a[0:3],a[128:131],v[224:227]\nv_cvt_pk_bf16_f32 v198,v148,v149\nv_cvt_pk_bf16_f32 v199,v150,v151\nM v[128:131],a[4:7],a[132:135],v[128:131]\nds_read_b128 a[64:67],v250 offset:0\nds_read_b128 a[96:99],v250 offset:8192\n" \
"M v[128:131],a[8:11],a[136:139],v[128:131]\nbuffer_load_dwordx4 v240,s[44:47],s95 offen offset:0 sc1 lds\nM v[128:131],a[12:15],a[140:143],v[128:131]\nv_exp_f32 v167,v167\nM v[132:135],a[12:15],a[156:159],v[228:231]\nds_read_b128 a[68:71],v250 offset:1024\nds_read_b128 a[100:103],v250 offset:9216\nM v[132:135],a[8:11],a[152:155],v[132:135]\n" \
"v_cvt_pk_bf16_f32 v212,v164,v165\nv_cvt_pk_bf16_f32 v213,v166,v167\nM v[132:135],a[4:7],a[148:151],v[132:135]\nv_exp_f32 v176,v176\nM v[132:135],a[0:3],a[144:147],v[132:135]\nds_read_b128 a[72:75],v250 offset:2048\nds_read_b128 a[104:107],v250 offset:10240\nM v[144:147],a[16:19],a[128:131],v[224:227]\n" \
"v_exp_f32 v177,v177\nM v[144:147],a[20:23],a[132:135],v[144:147]\nv_exp_f32 v178,v178\nM v[144:147],a[24:27],a[136:139],v[144:147]\nds_read_b128 a[76:79],v250 offset:3072\nds_read_b128 a[108:111],v250 offset:11264\nM v[144:147],a[28:31],a[140:143],v[144:147]\nbuffer_load_dwordx4 v240,s[44:47],s95 offen offset:1024 sc1 lds\n" \
"M v[148:151],a[28:31],a[156:159],v[228:231]\nv_exp_f32 v179,v179\nM v[148:151],a[24:27],a[152:155],v[148:151]\nds_read_b128 a[80:83],v250 offset:4096\nds_read_b128 a[112:115],v250 offset:12288\nM v[148:151],a[20:23],a[148:151],v[148:151]\nv_cvt_pk_bf16_f32 v210,v176,v177\nv_cvt_pk_bf16_f32 v211,v178,v179\n" \
"M v[148:151],a[16:19],a[144:147],v[148:151]\nv_exp_f32 v180,v180\nM v[160:163],a[32:35],a[128:131],v[224:227]\nds_read_b128 a[84:87],v250 offset:5120\nds_read_b128 a[116:119],v250 offset:13312\nM v[160:163],a[36:39],a[132:135],v[160:163]\nv_exp_f32 v181,v181\nM v[160:163],a[40:43],a[136:139],v[160:163]\n" \
"v_exp_f32 v182,v182\nM v[160:163],a[44:47],a[140:143],v[160:163]\nds_read_b128 a[88:91],v250 offset:6144\nds_read_b128 a[120:123],v250 offset:14336\nM v[164:167],a[44:47],a[156:159],v[228:231]\nbuffer_load_dwordx4 v240,s[44:47],s95 offen offset:2048 sc1 lds\nM v[164:167],a[40:43],a[152:155],v[164:167]\nv_exp_f32 v183,v183\n" \
"M v[164:167],a[36:39],a[148:151],v[164:167]\nds_read_b128 a[92:95],v250 offset:7168\nds_read_b128 a[124:127],v250 offset:15360\nM v[164:167],a[32:35],a[144:147],v[164:167]\nv_cvt_pk_bf16_f32 v214,v180,v181\ns_waitcnt lgkmcnt(7)\nM v[176:179],a[48:51],a[128:131],v[224:227]\nv_cvt_pk_bf16_f32 v215,v182,v183\n" \
"v_xor_b32 v250,0x18000,v250\nM v[176:179],a[52:55],a[132:135],v[176:179]\nv_exp_f32 v128,v128\nM v[176:179],a[56:59],a[136:139],v[176:179]\nv_exp_f32 v129,v129\nM v[176:179],a[60:63],a[140:143],v[176:179]\nv_exp_f32 v130,v130\nM v[180:183],a[60:63],a[156:159],v[228:231]\n" \
"v_exp_f32 v131,v131\nM v[180:183],a[56:59],a[152:155],v[180:183]\nbuffer_load_dwordx4 v240,s[44:47],s95 offen offset:3072 sc1 lds\nM v[180:183],a[52:55],a[148:151],v[180:183]\nv_exp_f32 v144,v144\nM v[180:183],a[48:51],a[144:147],v[180:183]\nv_exp_f32 v145,v145\nM v[0:3],a[64:67],v[192:195],v[0:3]\n" \
"v_exp_f32 v146,v146\nM v[0:3],a[96:99],v[208:211],v[0:3]\nv_exp_f32 v147,v147\nM v[8:11],a[68:71],v[192:195],v[8:11]\ns_waitcnt vmcnt(16) lgkmcnt(0)\ns_barrier\nM v[8:11],a[100:103],v[208:211],v[8:11]\nds_read_b128 a[0:3],v250 offset:0\n" \
"ds_read_b128 a[4:7],v250 offset:1024\nM v[16:19],a[72:75],v[192:195],v[16:19]\ns_xor_b32 m0,m0,0x10000\nM v[16:19],a[104:107],v[208:211],v[16:19]\nbuffer_load_dwordx4 v240,s[48:51],s95 offen offset:0 sc1 lds\nM v[24:27],a[76:79],v[192:195],v[24:27]\nv_exp_f32 v160,v160\nM v[24:27],a[108:111],v[208:211],v[24:27]\n" \
"ds_read_b128 a[8:11],v250 offset:2048\nds_read_b128 a[12:15],v250 offset:3072\nM v[32:35],a[80:83],v[192:195],v[32:35]\nv_exp_f32 v161,v161\nM v[32:35],a[112:115],v[208:211],v[32:35]\nv_exp_f32 v162,v162\nM v[40:43],a[84:87],v[192:195],v[40:43]\nv_exp_f32 v163,v163\n" \
"M v[40:43],a[116:119],v[208:211],v[40:43]\nds_read_b128 a[16:19],v250 offset:4096\nds_read_b128 a[20:23],v250 offset:5120\nM v[48:51],a[88:91],v[192:195],v[48:51]\nv_exp_f32 v176,v176\nM v[48:51],a[120:123],v[208:211],v[48:51]\nbuffer_load_dwordx4 v240,s[48:51],s95 offen offset:1024 sc1 lds\nM v[56:59],a[92:95],v[192:195],v[56:59]\n" \
"v_exp_f32 v177,v177\nM v[56:59],a[124:127],v[208:211],v[56:59]\nds_read_b128 a[24:27],v250 offset:6144\nds_read_b128 a[28:31],v250 offset:7168\nM a[192:195],a[208:211],v[192:195],a[192:195]\nv_exp_f32 v178,v178\nM a[192:195],a[208:211],v[208:211],a[192:195]\nv_exp_f32 v179,v179\n" \
"M v[4:7],a[64:67],v[196:199],v[4:7]\nv_exp_f32 v132,v132\nM v[4:7],a[96:99],v[212:215],v[4:7]\nds_read_b128 a[32:35],v250 offset:8192\nds_read_b128 a[36:39],v250 offset:9216\nM v[12:15],a[68:71],v[196:199],v[12:15]\nv_exp_f32 v133,v133\nM v[12:15],a[100:103],v[212:215],v[12:15]\n" \
"buffer_load_dwordx4 v240,s[48:51],s95 offen offset:2048 sc1 lds\nM v[20:23],a[72:75],v[196:199],v[20:23]\nv_exp_f32 v134,v134\nM v[20:23],a[104:107],v[212:215],v[20:23]\nds_read_b128 a[40:43],v250 offset:10240\nds_read_b128 a[44:47],v250 offset:11264\nM v[28:31],a[76:79],v[196:199],v[28:31]\nv_exp_f32 v135,v135\n" \
"M v[28:31],a[108:111],v[212:215],v[28:31]\nv_exp_f32 v148,v148\nM v[36:39],a[80:83],v[196:199],v[36:39]\nv_exp_f32 v149,v149\nM v[36:39],a[112:115],v[212:215],v[36:39]\nds_read_b128 a[48:51],v250 offset:12288\nds_read_b128 a[52:55],v250 offset:13312\nM v[44:47],a[84:87],v[196:199],v[44:47]\n" \
"v_exp_f32 v150,v150\nM v[44:47],a[116:119],v[212:215],v[44:47]\nbuffer_load_dwordx4 v240,s[48:51],s95 offen offset:3072 sc1 lds\nM v[52:55],a[88:91],v[196:199],v[52:55]\nv_exp_f32 v151,v151\nM v[52:55],a[120:123],v[212:215],v[52:55]\nds_read_b128 a[56:59],v250 offset:14336\nds_read_b128 a[60:63],v250 offset:15360\n" \
"M v[60:63],a[92:95],v[196:199],v[60:63]\nv_cvt_pk_bf16_f32 v192,v128,v129\nv_cvt_pk_bf16_f32 v193,v130,v131\nM v[60:63],a[124:127],v[212:215],v[60:63]\nv_cvt_pk_bf16_f32 v194,v144,v145\nv_cvt_pk_bf16_f32 v195,v146,v147\nM a[196:199],a[208:211],v[196:199],a[196:199]\nv_cvt_pk_bf16_f32 v208,v160,v161\n" \
"v_cvt_pk_bf16_f32 v209,v162,v163\nv_cvt_pk_bf16_f32 v210,v176,v177\nv_cvt_pk_bf16_f32 v211,v178,v179\nv_xor_b32 v250,0x1c000,v250\ns_add_u32 s95,s95,0x4000\nM a[196:199],a[208:211],v[212:215],a[196:199]\ns_cmp_lg_u32 s95,s15\ns_cbranch_scc1 .Lsingle\n" \
"s_and_b32 s94,m0,0x8000\ns_or_b32 s94,s94,0x4000\n.Lsx:\n.Lsw:\ns_waitcnt lgkmcnt(4)\nM v[128:131],a[0:3],a[128:131],v[224:227]\ns_lshl4_add_u32 s69,s12,s68\ns_add_u32 m0,s64,131072\n" \
"M v[128:131],a[4:7],a[132:135],v[128:131]\nds_read_b128 a[64:67],v250 offset:0\nds_read_b128 a[96:99],v250 offset:8192\nv_cvt_pk_bf16_f32 v196,v132,v133\nM v[128:131],a[8:11],a[136:139],v[128:131]\nbuffer_load_dwordx4 v243,s[40:43],s69 offen offset:0 lds\nM v[128:131],a[12:15],a[140:143],v[128:131]\nv_cvt_pk_bf16_f32 v197,v134,v135\n" \
"v_cvt_pk_bf16_f32 v198,v148,v149\nv_cvt_pk_bf16_f32 v199,v150,v151\nM v[132:135],a[12:15],a[156:159],v[228:231]\nds_read_b128 a[68:71],v250 offset:1024\nds_read_b128 a[100:103],v250 offset:9216\nM v[132:135],a[8:11],a[152:155],v[132:135]\nbuffer_load_dwordx4 v244,s[40:43],s69 offen offset:1024 lds\ns_add_u32 s94,s94,0x4000\n" \
"M v[132:135],a[4:7],a[148:151],v[132:135]\nv_exp_f32 v164,v164\nM v[132:135],a[0:3],a[144:147],v[132:135]\nds_read_b128 a[72:75],v250 offset:2048\nds_read_b128 a[104:107],v250 offset:10240\ns_and_b32 s94,s94,0xffff\nM v[144:147],a[16:19],a[128:131],v[224:227]\nbuffer_load_dwordx4 v245,s[40:43],s69 offen offset:2048 lds\n" \
"M v[144:147],a[20:23],a[132:135],v[144:147]\nv_exp_f32 v165,v165\ns_add_u32 s95,s95,0x4000\nM v[144:147],a[24:27],a[136:139],v[144:147]\nds_read_b128 a[76:79],v250 offset:3072\nds_read_b128 a[108:111],v250 offset:11264\nM v[144:147],a[28:31],a[140:143],v[144:147]\nbuffer_load_dwordx4 v246,s[40:43],s69 offen offset:3072 lds\n" \
"s_lshl4_add_u32 s69,s12,s68\nM v[148:151],a[28:31],a[156:159],v[228:231]\ns_add_u32 s69,s69,0x10000\ns_add_u32 m0,s64,147456\nM v[148:151],a[24:27],a[152:155],v[148:151]\nds_read_b128 a[80:83],v250 offset:4096\nds_read_b128 a[112:115],v250 offset:12288\nv_cvt_pk_bf16_f32 v212,v164,v165\n" \
"M v[148:151],a[20:23],a[148:151],v[148:151]\nv_exp_f32 v166,v166\nM v[148:151],a[16:19],a[144:147],v[148:151]\nv_exp_f32 v167,v167\nM v[160:163],a[32:35],a[128:131],v[224:227]\nds_read_b128 a[84:87],v250 offset:5120\nds_read_b128 a[116:119],v250 offset:13312\nv_cvt_pk_bf16_f32 v213,v166,v167\n" \
"M v[160:163],a[36:39],a[132:135],v[160:163]\nbuffer_load_dwordx4 v243,s[40:43],s69 offen offset:0 lds\nM v[160:163],a[40:43],a[136:139],v[160:163]\nv_exp_f32 v180,v180\nM v[160:163],a[44:47],a[140:143],v[160:163]\nds_read_b128 a[88:91],v250 offset:6144\nds_read_b128 a[120:123],v250 offset:14336\nM v[164:167],a[44:47],a[156:159],v[228:231]\n" \
"v_exp_f32 v181,v181\nM v[164:167],a[40:43],a[152:155],v[164:167]\nbuffer_load_dwordx4 v244,s[40:43],s69 offen offset:1024 lds\nM v[164:167],a[36:39],a[148:151],v[164:167]\nds_read_b128 a[92:95],v250 offset:7168\nds_read_b128 a[124:127],v250 offset:15360\nv_cvt_pk_bf16_f32 v214,v180,v181\nM v[164:167],a[32:35],a[144:147],v[164:167]\n" \
"v_xor_b32 v250,0x18000,v250\ns_waitcnt lgkmcnt(6)\nM v[176:179],a[48:51],a[128:131],v[224:227]\ns_waitcnt vmcnt(6)\nbuffer_store_dword v233,off,s[52:55],s77 sc1\nM v[176:179],a[52:55],a[132:135],v[176:179]\nbuffer_load_dwordx4 v245,s[40:43],s69 offen offset:2048 lds\nM v[176:179],a[56:59],a[136:139],v[176:179]\n" \
"v_exp_f32 v182,v182\nv_exp_f32 v183,v183\nM v[176:179],a[60:63],a[140:143],v[176:179]\nv_cvt_pk_bf16_f32 v215,v182,v183\nM v[180:183],a[60:63],a[156:159],v[228:231]\nv_exp_f32 v128,v128\nM v[180:183],a[56:59],a[152:155],v[180:183]\nbuffer_load_dwordx4 v246,s[40:43],s69 offen offset:3072 lds\n" \
"M v[180:183],a[52:55],a[148:151],v[180:183]\nv_exp_f32 v129,v129\nM v[180:183],a[48:51],a[144:147],v[180:183]\ns_lshl4_add_u32 s69,s10,s68\nv_exp_f32 v130,v130\nM v[0:3],a[64:67],v[192:195],v[0:3]\nv_exp_f32 v131,v131\nM v[0:3],a[96:99],v[208:211],v[0:3]\n" \
"s_add_u32 m0,s64,0\nv_exp_f32 v144,v144\nM v[8:11],a[68:71],v[192:195],v[8:11]\ns_waitcnt vmcnt(16) lgkmcnt(0)\ns_barrier\nM v[8:11],a[100:103],v[208:211],v[8:11]\nds_read_b128 a[0:3],v250 offset:0\nds_read_b128 a[4:7],v250 offset:1024\n" \
"M v[16:19],a[72:75],v[192:195],v[16:19]\nv_exp_f32 v145,v145\nM v[16:19],a[104:107],v[208:211],v[16:19]\nbuffer_load_dwordx4 v243,s[40:43],s69 offen offset:0 lds\nM v[24:27],a[76:79],v[192:195],v[24:27]\nv_exp_f32 v146,v146\nM v[24:27],a[108:111],v[208:211],v[24:27]\nds_read_b128 a[8:11],v250 offset:2048\n" \
"ds_read_b128 a[12:15],v250 offset:3072\nM v[32:35],a[80:83],v[192:195],v[32:35]\nv_exp_f32 v147,v147\nM v[32:35],a[112:115],v[208:211],v[32:35]\nbuffer_load_dwordx4 v244,s[40:43],s69 offen offset:1024 lds\nM v[40:43],a[84:87],v[192:195],v[40:43]\nv_exp_f32 v160,v160\nM v[40:43],a[116:119],v[208:211],v[40:43]\n" \
"ds_read_b128 a[16:19],v250 offset:4096\nds_read_b128 a[20:23],v250 offset:5120\nM v[48:51],a[88:91],v[192:195],v[48:51]\nv_exp_f32 v161,v161\nM v[48:51],a[120:123],v[208:211],v[48:51]\nbuffer_load_dwordx4 v245,s[40:43],s69 offen offset:2048 lds\nM v[56:59],a[92:95],v[192:195],v[56:59]\nv_exp_f32 v162,v162\n" \
"M v[56:59],a[124:127],v[208:211],v[56:59]\nds_read_b128 a[24:27],v250 offset:6144\nds_read_b128 a[28:31],v250 offset:7168\nM a[192:195],a[208:211],v[192:195],a[192:195]\nv_exp_f32 v163,v163\nv_cvt_pk_bf16_f32 v192,v128,v129\nM a[192:195],a[208:211],v[208:211],a[192:195]\nbuffer_load_dwordx4 v246,s[40:43],s69 offen offset:3072 lds\n" \
"M v[4:7],a[64:67],v[196:199],v[4:7]\ns_lshl4_add_u32 s69,s10,s68\ns_add_u32 s69,s69,0x10000\ns_add_u32 m0,s64,32768\nM v[4:7],a[96:99],v[212:215],v[4:7]\nds_read_b128 a[32:35],v250 offset:8192\nds_read_b128 a[36:39],v250 offset:9216\nM v[12:15],a[68:71],v[196:199],v[12:15]\n" \
"v_exp_f32 v176,v176\nv_cvt_pk_bf16_f32 v193,v130,v131\nM v[12:15],a[100:103],v[212:215],v[12:15]\nbuffer_load_dwordx4 v243,s[40:43],s69 offen offset:0 lds\nM v[20:23],a[72:75],v[196:199],v[20:23]\nv_exp_f32 v177,v177\nv_cvt_pk_bf16_f32 v194,v144,v145\nM v[20:23],a[104:107],v[212:215],v[20:23]\n" \
"ds_read_b128 a[40:43],v250 offset:10240\nds_read_b128 a[44:47],v250 offset:11264\nM v[28:31],a[76:79],v[196:199],v[28:31]\nv_exp_f32 v178,v178\nv_cvt_pk_bf16_f32 v195,v146,v147\nM v[28:31],a[108:111],v[212:215],v[28:31]\nbuffer_load_dwordx4 v244,s[40:43],s69 offen offset:1024 lds\nM v[36:39],a[80:83],v[196:199],v[36:39]\n" \
"v_exp_f32 v179,v179\nv_cvt_pk_bf16_f32 v208,v160,v161\nM v[36:39],a[112:115],v[212:215],v[36:39]\nds_read_b128 a[48:51],v250 offset:12288\nds_read_b128 a[52:55],v250 offset:13312\nM v[44:47],a[84:87],v[196:199],v[44:47]\nv_exp_f32 v132,v132\nv_cvt_pk_bf16_f32 v209,v162,v163\n" \
"M v[44:47],a[116:119],v[212:215],v[44:47]\nbuffer_load_dwordx4 v245,s[40:43],s69 offen offset:2048 lds\nv_cvt_pk_bf16_f32 v210,v176,v177\nM v[52:55],a[88:91],v[196:199],v[52:55]\nv_exp_f32 v133,v133\nM v[52:55],a[120:123],v[212:215],v[52:55]\nds_read_b128 a[56:59],v250 offset:14336\nds_read_b128 a[60:63],v250 offset:15360\n" \
"v_cvt_pk_bf16_f32 v211,v178,v179\nM v[60:63],a[92:95],v[196:199],v[60:63]\nv_exp_f32 v134,v134\nM v[60:63],a[124:127],v[212:215],v[60:63]\nbuffer_load_dwordx4 v246,s[40:43],s69 offen offset:3072 lds\nv_add_u32 v250,s94,v241\nM a[196:199],a[208:211],v[196:199],a[196:199]\nv_exp_f32 v135,v135\n" \
"M a[196:199],a[208:211],v[212:215],a[196:199]\nv_exp_f32 v148,v148\nv_exp_f32 v149,v149\nv_exp_f32 v150,v150\nv_exp_f32 v151,v151\n.Lod:\ns_waitcnt lgkmcnt(2)\nM v[128:131],a[0:3],a[128:131],v[224:227]\n" \
"v_cvt_pk_bf16_f32 v196,v132,v133\nv_cvt_pk_bf16_f32 v197,v134,v135\nM v[128:131],a[4:7],a[132:135],v[128:131]\nds_read_b128 a[64:67],v250 offset:0\nds_read_b128 a[96:99],v250 offset:8192\nM v[128:131],a[8:11],a[136:139],v[128:131]\ns_lshl2_add_u32 s39,s62,s7\ns_lshl_b32 s39,s39,5\n" \
"M v[128:131],a[12:15],a[140:143],v[128:131]\ns_add_u32 s39,s39,s63\nv_cvt_pk_bf16_f32 v198,v148,v149\nv_cvt_pk_bf16_f32 v199,v150,v151\nM v[132:135],a[12:15],a[156:159],v[228:231]\nds_read_b128 a[68:71],v250 offset:1024\nds_read_b128 a[100:103],v250 offset:9216\nM v[132:135],a[8:11],a[152:155],v[132:135]\n" \
"s_lshl2_add_u32 s39,s39,73728\nv_and_b32 v251,3,v249\nM v[132:135],a[4:7],a[148:151],v[132:135]\ns_waitcnt vmcnt(16)\nbuffer_store_dword v252,off,s[52:55],s39 sc1\nM v[132:135],a[0:3],a[144:147],v[132:135]\nds_read_b128 a[72:75],v250 offset:2048\nds_read_b128 a[104:107],v250 offset:10240\n" \
"v_lshlrev_b32 v251,2,v251\nM v[144:147],a[16:19],a[128:131],v[224:227]\nv_exp_f32 v164,v164\nM v[144:147],a[20:23],a[132:135],v[144:147]\nv_exp_f32 v165,v165\nM v[144:147],a[24:27],a[136:139],v[144:147]\nds_read_b128 a[76:79],v250 offset:3072\nds_read_b128 a[108:111],v250 offset:11264\n" \
"M v[144:147],a[28:31],a[140:143],v[144:147]\nv_exp_f32 v166,v166\nv_cvt_pk_bf16_f32 v212,v164,v165\nM v[148:151],a[28:31],a[156:159],v[228:231]\nv_exp_f32 v167,v167\nM v[148:151],a[24:27],a[152:155],v[148:151]\nds_read_b128 a[80:83],v250 offset:4096\nds_read_b128 a[112:115],v250 offset:12288\n" \
"M v[148:151],a[20:23],a[148:151],v[148:151]\nv_cvt_pk_bf16_f32 v213,v166,v167\ns_add_u32 s80,s79,4096\nM v[148:151],a[16:19],a[144:147],v[148:151]\nv_exp_f32 v180,v180\ns_add_u32 s94,s94,0x4000\nM v[160:163],a[32:35],a[128:131],v[224:227]\nds_read_b128 a[84:87],v250 offset:5120\n" \
"ds_read_b128 a[116:119],v250 offset:13312\nM v[160:163],a[36:39],a[132:135],v[160:163]\nv_exp_f32 v181,v181\nM v[160:163],a[40:43],a[136:139],v[160:163]\nv_exp_f32 v182,v182\nM v[160:163],a[44:47],a[140:143],v[160:163]\nds_read_b128 a[88:91],v250 offset:6144\nds_read_b128 a[120:123],v250 offset:14336\n" \
"v_cvt_pk_bf16_f32 v214,v180,v181\nM v[164:167],a[44:47],a[156:159],v[228:231]\nbuffer_load_dword v248,v251,s[52:55],s80 offen sc1\nM v[164:167],a[40:43],a[152:155],v[164:167]\nv_exp_f32 v183,v183\nM v[164:167],a[36:39],a[148:151],v[164:167]\nds_read_b128 a[92:95],v250 offset:7168\nds_read_b128 a[124:127],v250 offset:15360\n" \
"M v[164:167],a[32:35],a[144:147],v[164:167]\nv_cvt_pk_bf16_f32 v215,v182,v183\ns_and_b32 s94,s94,0xffff\nv_add_u32 v250,s94,v241\nM v[176:179],a[48:51],a[128:131],v[224:227]\nv_exp_f32 v128,v128\nM v[176:179],a[52:55],a[132:135],v[176:179]\ns_add_u32 s65,s10,s67\n" \
"s_waitcnt lgkmcnt(4)\nM v[176:179],a[56:59],a[136:139],v[176:179]\nv_exp_f32 v129,v129\nM v[176:179],a[60:63],a[140:143],v[176:179]\nv_exp_f32 v130,v130\ns_add_u32 s65,s65,1048576\nM v[180:183],a[60:63],a[156:159],v[228:231]\nv_exp_f32 v131,v131\n" \
"M v[180:183],a[56:59],a[152:155],v[180:183]\nv_exp_f32 v144,v144\nM v[180:183],a[52:55],a[148:151],v[180:183]\nv_exp_f32 v145,v145\nM v[180:183],a[48:51],a[144:147],v[180:183]\nv_exp_f32 v146,v146\nv_add_u32 v226,131072,v242\nM v[0:3],a[64:67],v[192:195],v[0:3]\n" \
"v_exp_f32 v147,v147\nM v[0:3],a[96:99],v[208:211],v[0:3]\nv_exp_f32 v160,v160\nM v[8:11],a[68:71],v[192:195],v[8:11]\ns_waitcnt lgkmcnt(0)\ns_barrier\nM v[8:11],a[100:103],v[208:211],v[8:11]\ns_lshl4_add_u32 s69,s11,s68\n" \
"s_add_u32 m0,s64,16384\nv_xor_b32 v227,32,v226\nM v[16:19],a[72:75],v[192:195],v[16:19]\nv_exp_f32 v161,v161\nM v[16:19],a[104:107],v[208:211],v[16:19]\nv_exp_f32 v162,v162\nM v[24:27],a[76:79],v[192:195],v[24:27]\nv_exp_f32 v163,v163\n" \
"M v[24:27],a[108:111],v[208:211],v[24:27]\nv_exp_f32 v176,v176\ns_add_u32 s37,s12,s67\nM v[32:35],a[80:83],v[192:195],v[32:35]\nbuffer_load_dwordx4 v243,s[40:43],s69 offen offset:0 lds\nM v[32:35],a[112:115],v[208:211],v[32:35]\nv_exp_f32 v177,v177\nM v[40:43],a[84:87],v[192:195],v[40:43]\n" \
"v_exp_f32 v178,v178\nM v[40:43],a[116:119],v[208:211],v[40:43]\nv_exp_f32 v179,v179\ns_add_u32 s38,s37,4096\nM v[48:51],a[88:91],v[192:195],v[48:51]\nv_exp_f32 v132,v132\nM v[48:51],a[120:123],v[208:211],v[48:51]\nv_exp_f32 v133,v133\n" \
"M v[56:59],a[92:95],v[192:195],v[56:59]\nv_exp_f32 v134,v134\nM v[56:59],a[124:127],v[208:211],v[56:59]\nv_exp_f32 v135,v135\ns_lshl2_add_u32 s76,s61,s79\nM a[192:195],a[208:211],v[192:195],a[192:195]\nbuffer_load_dwordx4 v244,s[40:43],s69 offen offset:1024 lds\nM a[192:195],a[208:211],v[208:211],a[192:195]\n" \
"v_cvt_pk_bf16_f32 v192,v128,v129\nv_cvt_pk_bf16_f32 v193,v130,v131\nM v[4:7],a[64:67],v[196:199],v[4:7]\nds_read_b128 a[64:67],v250 offset:0\nv_cvt_pk_bf16_f32 v194,v144,v145\nM v[4:7],a[96:99],v[212:215],v[4:7]\nds_read_b128 a[96:99],v250 offset:8192\nv_cvt_pk_bf16_f32 v195,v146,v147\n" \
"v_cvt_pk_bf16_f32 v208,v160,v161\nM v[12:15],a[68:71],v[196:199],v[12:15]\nds_read_b128 a[68:71],v250 offset:1024\nv_cvt_pk_bf16_f32 v209,v162,v163\nM v[12:15],a[100:103],v[212:215],v[12:15]\nds_read_b128 a[100:103],v250 offset:9216\nv_cvt_pk_bf16_f32 v210,v176,v177\nM v[20:23],a[72:75],v[196:199],v[20:23]\n" \
"ds_read_b128 a[72:75],v250 offset:2048\nv_cvt_pk_bf16_f32 v211,v178,v179\nM v[20:23],a[104:107],v[212:215],v[20:23]\nds_read_b128 a[104:107],v250 offset:10240\nv_exp_f32 v148,v148\nM v[28:31],a[76:79],v[196:199],v[28:31]\nbuffer_load_dwordx4 v245,s[40:43],s69 offen offset:2048 lds\nM v[28:31],a[108:111],v[212:215],v[28:31]\n" \
"ds_read_b128 a[76:79],v250 offset:3072\nds_read_b128 a[108:111],v250 offset:11264\nM v[36:39],a[80:83],v[196:199],v[36:39]\nds_read_b128 a[80:83],v250 offset:4096\nv_lshlrev_b32 v238,1,v240\nM v[36:39],a[112:115],v[212:215],v[36:39]\nds_read_b128 a[112:115],v250 offset:12288\nv_exp_f32 v149,v149\n" \
"M v[44:47],a[84:87],v[196:199],v[44:47]\nds_read_b128 a[84:87],v250 offset:5120\nv_and_b32 v254,32,v249\nM v[44:47],a[116:119],v[212:215],v[44:47]\nds_read_b128 a[116:119],v250 offset:13312\ns_lshr_b32 s70,s60,1\nM v[52:55],a[88:91],v[196:199],v[52:55]\nds_read_b128 a[88:91],v250 offset:6144\n" \
"v_lshlrev_b32 v254,3,v254\nM v[52:55],a[120:123],v[212:215],v[52:55]\nds_read_b128 a[120:123],v250 offset:14336\nv_exp_f32 v150,v150\nM v[60:63],a[92:95],v[196:199],v[60:63]\nbuffer_load_dwordx4 v246,s[40:43],s69 offen offset:3072 lds\nM v[60:63],a[124:127],v[212:215],v[60:63]\ns_waitcnt vmcnt(4)\n" \
"v_and_b32 v248,s17,v248\nM a[196:199],a[208:211],v[196:199],a[196:199]\nv_cmp_eq_u32 vcc,0,v248\nds_read_b128 a[92:95],v250 offset:7168\nM a[196:199],a[208:211],v[212:215],a[196:199]\ns_cbranch_vccnz .Lps6\n.Lpb6:\ns_lshl4_add_u32 s69,s11,s68\n" \
"s_waitcnt lgkmcnt(4)\nM v[0:3],a[64:67],v[192:195],v[0:3]\nbuffer_load_dwordx4 v[72:75],v240,s[52:55],s65 offen offset:0 sc1\nM v[0:3],a[96:99],v[208:211],v[0:3]\ns_add_u32 s69,s69,0x10000\ns_add_u32 m0,s64,49152\nM v[8:11],a[68:71],v[192:195],v[8:11]\nds_read_b128 a[124:127],v250 offset:15360\n" \
"v_cvt_pk_bf16_f32 v196,v132,v133\nM v[8:11],a[100:103],v[208:211],v[8:11]\nv_exp_f32 v151,v151\nv_cvt_pk_bf16_f32 v197,v134,v135\nM v[16:19],a[72:75],v[192:195],v[16:19]\nbuffer_load_dwordx4 v243,s[40:43],s69 offen offset:0 lds\nM v[16:19],a[104:107],v[208:211],v[16:19]\nv_exp_f32 v164,v164\n" \
"M v[24:27],a[76:79],v[192:195],v[24:27]\nv_exp_f32 v165,v165\nM v[24:27],a[108:111],v[208:211],v[24:27]\nv_exp_f32 v166,v166\nv_cvt_pk_bf16_f32 v198,v148,v149\nM v[32:35],a[80:83],v[192:195],v[32:35]\nbuffer_load_dwordx4 v[76:79],v240,s[52:55],s65 offen offset:1024 sc1\nM v[32:35],a[112:115],v[208:211],v[32:35]\n" \
"v_exp_f32 v167,v167\nM v[40:43],a[84:87],v[192:195],v[40:43]\nv_cvt_pk_bf16_f32 v199,v150,v151\ns_waitcnt lgkmcnt(0)\nM v[40:43],a[116:119],v[208:211],v[40:43]\nv_exp_f32 v180,v180\nv_cvt_pk_bf16_f32 v212,v164,v165\nM v[48:51],a[88:91],v[192:195],v[48:51]\n" \
"buffer_load_dwordx4 v244,s[40:43],s69 offen offset:1024 lds\nM v[48:51],a[120:123],v[208:211],v[48:51]\nv_exp_f32 v181,v181\nM v[56:59],a[92:95],v[192:195],v[56:59]\nv_exp_f32 v182,v182\nM v[56:59],a[124:127],v[208:211],v[56:59]\nv_exp_f32 v183,v183\nv_cvt_pk_bf16_f32 v213,v166,v167\n" \
"M a[192:195],a[208:211],v[192:195],a[192:195]\nbuffer_load_dwordx4 v[88:91],v240,s[52:55],s65 offen offset:2048 sc1\nM a[192:195],a[208:211],v[208:211],a[192:195]\nv_cvt_pk_bf16_f32 v214,v180,v181\nv_cvt_pk_bf16_f32 v215,v182,v183\nM v[4:7],a[64:67],v[196:199],v[4:7]\ns_barrier\nds_read_b64_tr_b16 v[200:201],v226 offset:0\n" \
"M v[4:7],a[96:99],v[212:215],v[4:7]\nds_read_b64_tr_b16 v[202:203],v226 offset:4096\nds_read_b64_tr_b16 v[204:205],v226 offset:8192\nds_read_b64_tr_b16 v[206:207],v226 offset:12288\nM v[12:15],a[68:71],v[196:199],v[12:15]\nbuffer_load_dwordx4 v245,s[40:43],s69 offen offset:2048 lds\nM v[12:15],a[100:103],v[212:215],v[12:15]\nds_read_b64_tr_b16 v[216:217],v227 offset:0\n" \
"ds_read_b64_tr_b16 v[218:219],v227 offset:4096\nM v[20:23],a[72:75],v[196:199],v[20:23]\nds_read_b64_tr_b16 v[220:221],v227 offset:8192\nds_read_b64_tr_b16 v[222:223],v227 offset:12288\nM v[20:23],a[104:107],v[212:215],v[20:23]\nds_read_b64_tr_b16 v[192:193],v226 offset:512\nds_read_b64_tr_b16 v[194:195],v226 offset:4608\nds_read_b64_tr_b16 v[208:209],v226 offset:8704\n" \
"M v[28:31],a[76:79],v[196:199],v[28:31]\nbuffer_load_dwordx4 v[92:95],v240,s[52:55],s65 offen offset:3072 sc1\nM v[28:31],a[108:111],v[212:215],v[28:31]\ns_add_u32 s65,s65,4096\nds_read_b64_tr_b16 v[210:211],v226 offset:12800\nM v[36:39],a[80:83],v[196:199],v[36:39]\nv_accvgpr_read_b32 v224,a192\nv_cmp_eq_u32 vcc,2,v248\n" \
"M v[36:39],a[112:115],v[212:215],v[36:39]\ns_cmp_lg_u64 vcc,0\ns_addc_u32 s99,s99,0\nv_xor_b32 v250,0x8000,v250\nM v[44:47],a[84:87],v[196:199],v[44:47]\nbuffer_load_dwordx4 v246,s[40:43],s69 offen offset:3072 lds\nM v[44:47],a[116:119],v[212:215],v[44:47]\nv_add_u32 v229,s64,v250\n" \
"s_lshl_b32 s70,s70,7\nM v[52:55],a[88:91],v[196:199],v[52:55]\nv_lshl_add_u32 v254,v249,2,v254\nv_xor_b32 v254,s70,v254\nM v[52:55],a[120:123],v[212:215],v[52:55]\ns_mov_b32 s97,4096\ns_lshl_b32 s80,s62,9\ns_add_u32 s80,s80,73728\n" \
"M v[60:63],a[92:95],v[196:199],v[60:63]\nbuffer_load_dwordx4 v[104:107],v240,s[52:55],s65 offen offset:0 sc1\nM v[60:63],a[124:127],v[212:215],v[60:63]\nv_and_b32 v236,15,v249\ns_lshl2_add_u32 s74,s63,s7\nM a[196:199],a[208:211],v[196:199],a[196:199]\nds_read_b64_tr_b16 v[196:197],v227 offset:512\nds_read_b64_tr_b16 v[198:199],v227 offset:4608\n" \
"M a[196:199],a[208:211],v[212:215],a[196:199]\nds_read_b64_tr_b16 v[212:213],v227 offset:8704\nds_read_b64_tr_b16 v[214:215],v227 offset:12800\ns_waitcnt lgkmcnt(4)\nM v[128:131],v[64:67],v[200:203],0\ns_lshl_b32 s72,s62,7\nv_lshrrev_b32 v235,4,v249\nv_lshlrev_b32 v236,1,v236\n" \
"s_add_u32 s95,s74,s72\nM v[128:131],v[80:83],v[204:207],v[128:131]\nv_accvgpr_read_b32 v228,a196\nv_add_f32 v225,v224,v228\nv_rcp_f32 v224,v224\nv_lshl_or_b32 v235,v235,8,v236\nM v[132:135],v[68:71],v[200:203],0\nv_mul_f32 v0,v0,v224\n" \
"v_mul_f32 v1,v1,v224\nv_mul_f32 v2,v2,v224\nv_mul_f32 v3,v3,v224\nM v[132:135],v[84:87],v[204:207],v[132:135]\nv_cvt_pk_bf16_f32 v0,v0,v1\nv_mul_f32 v8,v8,v224\nv_mul_f32 v9,v9,v224\nv_mul_f32 v10,v10,v224\n" \
"v_mul_f32 v11,v11,v224\nM v[136:139],v[64:67],v[216:219],0\nbuffer_load_dwordx4 v[108:111],v240,s[52:55],s65 offen offset:1024 sc1\nv_cvt_pk_bf16_f32 v1,v2,v3\nds_read_b64_tr_b16 v[200:201],v226 offset:1024\nM v[136:139],v[80:83],v[220:223],v[136:139]\nds_read_b64_tr_b16 v[202:203],v226 offset:5120\nv_cvt_pk_bf16_f32 v2,v8,v9\n" \
"v_cvt_pk_bf16_f32 v3,v10,v11\nds_read_b64_tr_b16 v[204:205],v226 offset:9216\nds_read_b64_tr_b16 v[206:207],v226 offset:13312\nM v[140:143],v[68:71],v[216:219],0\nbuffer_store_dwordx4 v[0:3],v240,s[52:55],s37 offen offset:0 sc1\nv_rcp_f32 v228,v228\nM v[140:143],v[84:87],v[220:223],v[140:143]\nv_mul_f32 v4,v4,v228\n" \
"v_mul_f32 v5,v5,v228\nds_read_b64_tr_b16 v[216:217],v227 offset:1024\nds_read_b64_tr_b16 v[218:219],v227 offset:5120\nv_mul_f32 v6,v6,v228\nM v[144:147],v[64:67],v[192:195],0\nv_mul_f32 v7,v7,v228\nv_cvt_pk_bf16_f32 v4,v4,v5\nv_mul_f32 v12,v12,v228\n" \
"v_mul_f32 v13,v13,v228\nM v[144:147],v[80:83],v[208:211],v[144:147]\nv_mul_f32 v14,v14,v228\nv_mul_f32 v15,v15,v228\nv_cvt_pk_bf16_f32 v5,v6,v7\nds_read_b64_tr_b16 v[220:221],v227 offset:9216\nds_read_b64_tr_b16 v[222:223],v227 offset:13312\nM v[148:151],v[68:71],v[192:195],0\n" \
"v_cvt_pk_bf16_f32 v6,v12,v13\nv_cvt_pk_bf16_f32 v7,v14,v15\nds_read_b64_tr_b16 v[192:193],v226 offset:1536\nds_read_b64_tr_b16 v[194:195],v226 offset:5632\nM v[148:151],v[84:87],v[208:211],v[148:151]\nbuffer_store_dwordx4 v[4:7],v240,s[52:55],s37 offen offset:1024 sc1\nv_mul_f32 v16,v16,v224\nv_mul_f32 v17,v17,v224\n" \
"s_waitcnt lgkmcnt(6)\nM v[152:155],v[64:67],v[196:199],0\nbuffer_load_dwordx4 v[120:123],v240,s[52:55],s65 offen offset:2048 sc1\nds_read_b64_tr_b16 v[208:209],v226 offset:9728\nds_read_b64_tr_b16 v[210:211],v226 offset:13824\nM v[152:155],v[80:83],v[212:215],v[152:155]\nv_mul_f32 v18,v18,v224\nv_mul_f32 v19,v19,v224\n" \
"v_cvt_pk_bf16_f32 v16,v16,v17\nv_mul_f32 v24,v24,v224\nv_mul_f32 v25,v25,v224\nM v[156:159],v[68:71],v[196:199],0\nv_mul_f32 v26,v26,v224\nv_mul_f32 v27,v27,v224\nv_cvt_pk_bf16_f32 v17,v18,v19\nv_cvt_pk_bf16_f32 v18,v24,v25\n" \
"M v[156:159],v[84:87],v[212:215],v[156:159]\nv_cvt_pk_bf16_f32 v19,v26,v27\nds_read_b64_tr_b16 v[196:197],v227 offset:1536\nds_read_b64_tr_b16 v[198:199],v227 offset:5632\nds_read_b64_tr_b16 v[212:213],v227 offset:9728\nv_mul_f32 v20,v20,v228\nM v[160:163],v[64:67],v[200:203],0\nbuffer_store_dwordx4 v[16:19],v240,s[52:55],s37 offen offset:2048 sc1\n" \
"ds_read_b64_tr_b16 v[214:215],v227 offset:13824\nv_mul_f32 v21,v21,v228\nM v[160:163],v[80:83],v[204:207],v[160:163]\nv_mul_f32 v22,v22,v228\nv_mul_f32 v23,v23,v228\nv_cvt_pk_bf16_f32 v20,v20,v21\nv_mul_f32 v28,v28,v228\nv_mul_f32 v29,v29,v228\n" \
"M v[164:167],v[68:71],v[200:203],0\nv_mul_f32 v30,v30,v228\nv_mul_f32 v31,v31,v228\nv_cvt_pk_bf16_f32 v21,v22,v23\nv_cvt_pk_bf16_f32 v22,v28,v29\nM v[164:167],v[84:87],v[204:207],v[164:167]\nv_cvt_pk_bf16_f32 v23,v30,v31\nds_read_b64_tr_b16 v[200:201],v226 offset:16384\n" \
"ds_read_b64_tr_b16 v[202:203],v226 offset:20480\nds_read_b64_tr_b16 v[204:205],v226 offset:24576\ns_waitcnt lgkmcnt(6)\nM v[168:171],v[64:67],v[216:219],0\nbuffer_load_dwordx4 v[124:127],v240,s[52:55],s65 offen offset:3072 sc1\nds_read_b64_tr_b16 v[206:207],v226 offset:28672\nv_mul_f32 v32,v32,v224\nM v[168:171],v[80:83],v[220:223],v[168:171]\n" \
"buffer_store_dwordx4 v[20:23],v240,s[52:55],s37 offen offset:3072 sc1\nv_mul_f32 v33,v33,v224\nv_mul_f32 v34,v34,v224\nv_mul_f32 v35,v35,v224\nM v[172:175],v[68:71],v[216:219],0\nv_cvt_pk_bf16_f32 v32,v32,v33\nv_mul_f32 v40,v40,v224\nv_mul_f32 v41,v41,v224\n" \
"v_mul_f32 v42,v42,v224\nM v[172:175],v[84:87],v[220:223],v[172:175]\nv_mul_f32 v43,v43,v224\nv_cvt_pk_bf16_f32 v33,v34,v35\nds_read_b64_tr_b16 v[216:217],v227 offset:16384\nds_read_b64_tr_b16 v[218:219],v227 offset:20480\nv_cvt_pk_bf16_f32 v34,v40,v41\nM v[176:179],v[64:67],v[192:195],0\n" \
"v_cvt_pk_bf16_f32 v35,v42,v43\nds_read_b64_tr_b16 v[220:221],v227 offset:24576\nds_read_b64_tr_b16 v[222:223],v227 offset:28672\ns_add_u32 s65,s11,s67\nM v[176:179],v[80:83],v[208:211],v[176:179]\ns_add_u32 s65,s65,1048576\nv_mul_f32 v36,v36,v228\nv_mul_f32 v37,v37,v228\n" \
"v_mul_f32 v38,v38,v228\nv_mul_f32 v39,v39,v228\nM v[180:183],v[68:71],v[192:195],0\nbuffer_store_dwordx4 v[32:35],v240,s[52:55],s38 offen offset:0 sc1\nds_read_b64_tr_b16 v[192:193],v226 offset:16896\nds_read_b64_tr_b16 v[194:195],v226 offset:20992\nM v[180:183],v[84:87],v[208:211],v[180:183]\nv_cvt_pk_bf16_f32 v36,v36,v37\n" \
"v_mul_f32 v44,v44,v228\nv_mul_f32 v45,v45,v228\nv_mul_f32 v46,v46,v228\ns_waitcnt lgkmcnt(6)\nM v[184:187],v[64:67],v[196:199],0\nbuffer_load_dwordx4 a[0:3],v240,s[52:55],s65 offen offset:0 sc1\nv_mul_f32 v47,v47,v228\nv_cvt_pk_bf16_f32 v37,v38,v39\n" \
"M v[184:187],v[80:83],v[212:215],v[184:187]\nds_read_b64_tr_b16 v[208:209],v226 offset:25088\nds_read_b64_tr_b16 v[210:211],v226 offset:29184\nv_cvt_pk_bf16_f32 v38,v44,v45\nv_cvt_pk_bf16_f32 v39,v46,v47\nv_add_f32 v225,v225,v224\nM v[188:191],v[68:71],v[196:199],0\nds_read_b64_tr_b16 v[196:197],v227 offset:16896\n" \
"ds_read_b64_tr_b16 v[198:199],v227 offset:20992\nv_add_f32 v225,v225,v228\nv_cmp_ngt_f32 vcc,s28,v225\nM v[188:191],v[84:87],v[212:215],v[188:191]\nbuffer_store_dwordx4 v[36:39],v240,s[52:55],s38 offen offset:1024 sc1\nv_mul_f32 v48,v48,v224\nv_mul_f32 v49,v49,v224\nds_read_b64_tr_b16 v[212:213],v227 offset:25088\n" \
"M v[128:131],v[96:99],v[200:203],v[128:131]\nds_read_b64_tr_b16 v[214:215],v227 offset:29184\ns_and_b64 vcc,vcc,s[90:91]\ns_addc_u32 s99,s99,0\nv_mul_f32 v50,v50,v224\nM v[128:131],v[112:115],v[204:207],v[128:131]\nv_mul_f32 v51,v51,v224\nv_cvt_pk_bf16_f32 v48,v48,v49\n" \
"v_mul_f32 v56,v56,v224\nv_mul_f32 v57,v57,v224\nv_mul_f32 v58,v58,v224\nM v[132:135],v[100:103],v[200:203],v[132:135]\nv_mul_f32 v59,v59,v224\nv_cvt_pk_bf16_f32 v49,v50,v51\nv_cvt_pk_bf16_f32 v50,v56,v57\nv_cvt_pk_bf16_f32 v51,v58,v59\n" \
"M v[132:135],v[116:119],v[204:207],v[132:135]\nv_mov_b32 v230,s99\nds_write_b32 v229,v230\nds_read_b64_tr_b16 v[200:201],v226 offset:17408\nds_read_b64_tr_b16 v[202:203],v226 offset:21504\ns_waitcnt lgkmcnt(5)\nM v[136:139],v[96:99],v[216:219],v[136:139]\nbuffer_store_dwordx4 v[48:51],v240,s[52:55],s38 offen offset:2048 sc1\n" \
"buffer_load_dwordx4 a[4:7],v240,s[52:55],s65 offen offset:1024 sc1\nM v[136:139],v[112:115],v[220:223],v[136:139]\nds_read_b64_tr_b16 v[204:205],v226 offset:25600\nds_read_b64_tr_b16 v[206:207],v226 offset:29696\nv_mul_f32 v52,v52,v228\nv_mul_f32 v53,v53,v228\nv_mul_f32 v54,v54,v228\nM v[140:143],v[100:103],v[216:219],v[140:143]\n" \
"v_mul_f32 v55,v55,v228\nv_cvt_pk_bf16_f32 v52,v52,v53\nv_mul_f32 v60,v60,v228\nv_mul_f32 v61,v61,v228\nM v[140:143],v[116:119],v[220:223],v[140:143]\nv_mul_f32 v62,v62,v228\nv_mul_f32 v63,v63,v228\nv_cvt_pk_bf16_f32 v53,v54,v55\n" \
"v_cvt_pk_bf16_f32 v54,v60,v61\nv_cvt_pk_bf16_f32 v55,v62,v63\nM v[144:147],v[96:99],v[192:195],v[144:147]\nds_read_b64_tr_b16 v[216:217],v227 offset:17408\nds_read_b64_tr_b16 v[218:219],v227 offset:21504\nds_read_b64_tr_b16 v[220:221],v227 offset:25600\ns_lshl4_add_u32 s69,s14,s68\nM v[144:147],v[112:115],v[208:211],v[144:147]\n" \
"buffer_store_dwordx4 v[52:55],v240,s[52:55],s38 offen offset:3072 sc1\nds_read_b64_tr_b16 v[222:223],v227 offset:29696\ns_add_u32 m0,s64,65536\nv_lshl_add_u32 v229,v251,10,v250\nM v[148:151],v[100:103],v[192:195],v[148:151]\nds_read_b64_tr_b16 v[192:193],v226 offset:17920\nds_read_b64_tr_b16 v[194:195],v226 offset:22016\ns_lshl_b32 s95,s95,10\n" \
"v_add_u32 v236,s95,v235\nM v[148:151],v[116:119],v[208:211],v[148:151]\nds_read_b64_tr_b16 v[208:209],v226 offset:26112\nds_read_b64_tr_b16 v[210:211],v226 offset:30208\nv_add_u32 v226,0,v242\ns_sub_u32 s74,s74,s72\ns_add_u32 s74,s74,1920\nM v[152:155],v[96:99],v[196:199],v[152:155]\n" \
"buffer_load_dwordx4 a[8:11],v240,s[52:55],s65 offen offset:2048 sc1\ns_lshl_b32 s74,s74,10\ns_waitcnt lgkmcnt(7)\nM v[152:155],v[112:115],v[212:215],v[152:155]\nv_add_u32 v237,s74,v235\ns_lshl_b32 s72,s7,9\nv_lshl_add_u32 v239,v249,3,s72\ns_add_u32 s38,s33,16384\n" \
"v_bfe_u32 v228,v249,2,2\nM v[156:159],v[100:103],v[196:199],v[156:159]\ns_barrier\nbuffer_load_dwordx4 v243,s[40:43],s69 offen offset:0 lds\nds_read_b64_tr_b16 v[196:197],v227 offset:17920\nM v[156:159],v[116:119],v[212:215],v[156:159]\nds_read_b64_tr_b16 v[198:199],v227 offset:22016\nds_read_b32 v230,v229\n" \
"ds_read_b64_tr_b16 v[212:213],v227 offset:26112\nv_lshrrev_b32 v229,4,v249\ns_add_u32 s95,s95,54525952\nM v[160:163],v[96:99],v[200:203],v[160:163]\nds_read_b64_tr_b16 v[214:215],v227 offset:30208\nv_xor_b32 v227,32,v226\ns_add_u32 s74,s74,54525952\nM v[160:163],v[112:115],v[204:207],v[160:163]\n" \
"M v[164:167],v[100:103],v[200:203],v[164:167]\nds_read_b64_tr_b16 v[200:201],v226 offset:0\nds_read_b64_tr_b16 v[202:203],v226 offset:4096\nM v[164:167],v[116:119],v[204:207],v[164:167]\nds_read_b64_tr_b16 v[204:205],v226 offset:8192\ns_waitcnt lgkmcnt(8)\nM v[168:171],v[96:99],v[216:219],v[168:171]\nbuffer_load_dwordx4 a[12:15],v240,s[52:55],s65 offen offset:3072 sc1\n" \
"M v[168:171],v[112:115],v[220:223],v[168:171]\nds_read_b64_tr_b16 v[206:207],v226 offset:12288\ns_add_u32 s65,s65,4096\nM v[172:175],v[100:103],v[216:219],v[172:175]\nbuffer_load_dwordx4 v244,s[40:43],s69 offen offset:1024 lds\nM v[172:175],v[116:119],v[220:223],v[172:175]\nds_read_b64_tr_b16 v[216:217],v227 offset:0\nds_read_b64_tr_b16 v[218:219],v227 offset:4096\n" \
"M v[176:179],v[96:99],v[192:195],v[176:179]\nds_read_b64_tr_b16 v[220:221],v227 offset:8192\nds_read_b64_tr_b16 v[222:223],v227 offset:12288\nM v[176:179],v[112:115],v[208:211],v[176:179]\ns_waitcnt lgkmcnt(8)\nv_cmp_ne_u32 vcc,0,v230\nM v[180:183],v[100:103],v[192:195],v[180:183]\ns_cbranch_vccnz .Lredo\n" \
"ds_read_b64_tr_b16 v[192:193],v226 offset:512\nM v[180:183],v[116:119],v[208:211],v[180:183]\nds_read_b64_tr_b16 v[194:195],v226 offset:4608\nds_read_b64_tr_b16 v[208:209],v226 offset:8704\nM v[184:187],v[96:99],v[196:199],v[184:187]\nbuffer_load_dwordx4 a[16:19],v240,s[52:55],s65 offen offset:0 sc1\nM v[184:187],v[112:115],v[212:215],v[184:187]\nds_read_b64_tr_b16 v[210:211],v226 offset:12800\n" \
"s_lshl_b32 s70,s2,9\nM v[188:191],v[100:103],v[196:199],v[188:191]\nbuffer_load_dwordx4 v245,s[40:43],s69 offen offset:2048 lds\nM v[188:191],v[116:119],v[212:215],v[188:191]\nds_read_b64_tr_b16 v[196:197],v227 offset:512\ns_waitcnt vmcnt(27) lgkmcnt(6)\nM v[128:131],v[72:75],v[200:203],v[128:131]\nds_read_b64_tr_b16 v[198:199],v227 offset:4608\n" \
"s_waitcnt vmcnt(23)\nM v[128:131],v[88:91],v[204:207],v[128:131]\nds_read_b64_tr_b16 v[212:213],v227 offset:8704\nds_read_b64_tr_b16 v[214:215],v227 offset:12800\nM v[132:135],v[76:79],v[200:203],v[132:135]\nds_read_b64_tr_b16 v[200:201],v226 offset:1024\ns_waitcnt vmcnt(21)\nM v[132:135],v[92:95],v[204:207],v[132:135]\n" \
"ds_read_b64_tr_b16 v[202:203],v226 offset:5120\nds_read_b64_tr_b16 v[204:205],v226 offset:9216\nM v[136:139],v[72:75],v[216:219],v[136:139]\nbuffer_load_dwordx4 a[20:23],v240,s[52:55],s65 offen offset:1024 sc1\ns_waitcnt lgkmcnt(7)\nM v[136:139],v[88:91],v[220:223],v[136:139]\nds_read_b64_tr_b16 v[206:207],v226 offset:13312\ns_lshl_b32 s71,s7,8\n" \
"M v[140:143],v[76:79],v[216:219],v[140:143]\nbuffer_load_dwordx4 v246,s[40:43],s69 offen offset:3072 lds\nM v[140:143],v[92:95],v[220:223],v[140:143]\nds_read_b64_tr_b16 v[216:217],v227 offset:1024\nds_read_b64_tr_b16 v[218:219],v227 offset:5120\nM v[144:147],v[72:75],v[192:195],v[144:147]\nds_read_b64_tr_b16 v[220:221],v227 offset:9216\nds_read_b64_tr_b16 v[222:223],v227 offset:13312\n" \
"M v[144:147],v[88:91],v[208:211],v[144:147]\ns_lshl4_add_u32 s69,s14,s68\ns_add_u32 s69,s69,0x10000\nM v[148:151],v[76:79],v[192:195],v[148:151]\nds_read_b64_tr_b16 v[192:193],v226 offset:1536\nds_read_b64_tr_b16 v[194:195],v226 offset:5632\nM v[148:151],v[92:95],v[208:211],v[148:151]\ns_add_u32 m0,s64,98304\n" \
"s_waitcnt lgkmcnt(7)\nM v[152:155],v[72:75],v[196:199],v[152:155]\nbuffer_load_dwordx4 a[24:27],v240,s[52:55],s65 offen offset:2048 sc1\nM v[152:155],v[88:91],v[212:215],v[152:155]\nds_read_b64_tr_b16 v[208:209],v226 offset:9728\nds_read_b64_tr_b16 v[210:211],v226 offset:13824\nM v[156:159],v[76:79],v[196:199],v[156:159]\nbuffer_load_dwordx4 v243,s[40:43],s69 offen offset:0 lds\n" \
"M v[156:159],v[92:95],v[212:215],v[156:159]\nds_read_b64_tr_b16 v[196:197],v227 offset:1536\nds_read_b64_tr_b16 v[198:199],v227 offset:5632\nM v[160:163],v[72:75],v[200:203],v[160:163]\nds_read_b64_tr_b16 v[212:213],v227 offset:9728\ns_waitcnt lgkmcnt(7)\nM v[160:163],v[88:91],v[204:207],v[160:163]\nds_read_b64_tr_b16 v[214:215],v227 offset:13824\n" \
"v_lshl_add_u32 v241,v249,2,s71\nM v[164:167],v[76:79],v[200:203],v[164:167]\nds_read_b64_tr_b16 v[200:201],v226 offset:32768\nds_read_b64_tr_b16 v[202:203],v226 offset:36864\nM v[164:167],v[92:95],v[204:207],v[164:167]\nds_read_b64_tr_b16 v[204:205],v226 offset:40960\nds_read_b64_tr_b16 v[206:207],v226 offset:45056\nM v[168:171],v[72:75],v[216:219],v[168:171]\n" \
"buffer_load_dwordx4 a[28:31],v240,s[52:55],s65 offen offset:3072 sc1\nM v[168:171],v[88:91],v[220:223],v[168:171]\ns_add_u32 s65,s14,s67\ns_add_u32 s65,s65,1048576\nM v[172:175],v[76:79],v[216:219],v[172:175]\nbuffer_load_dwordx4 v244,s[40:43],s69 offen offset:1024 lds\nM v[172:175],v[92:95],v[220:223],v[172:175]\nds_read_b64_tr_b16 v[216:217],v227 offset:32768\n" \
"s_waitcnt lgkmcnt(6)\nM v[176:179],v[72:75],v[192:195],v[176:179]\nds_read_b64_tr_b16 v[218:219],v227 offset:36864\nds_read_b64_tr_b16 v[220:221],v227 offset:40960\nM v[176:179],v[88:91],v[208:211],v[176:179]\nds_read_b64_tr_b16 v[222:223],v227 offset:45056\ns_add_u32 s70,s70,4096\nM v[180:183],v[76:79],v[192:195],v[180:183]\n" \
"ds_read_b64_tr_b16 v[192:193],v226 offset:33280\nds_read_b64_tr_b16 v[194:195],v226 offset:37376\nM v[180:183],v[92:95],v[208:211],v[180:183]\nds_read_b64_tr_b16 v[208:209],v226 offset:41472\nds_read_b64_tr_b16 v[210:211],v226 offset:45568\nM v[184:187],v[72:75],v[196:199],v[184:187]\nbuffer_load_dwordx4 a[32:35],v240,s[52:55],s65 offen offset:0 sc1\ns_waitcnt lgkmcnt(8)\n" \
"M v[184:187],v[88:91],v[212:215],v[184:187]\nv_add_u32 v230,s70,v241\nv_add_u32 v231,8192,v230\nM v[188:191],v[76:79],v[196:199],v[188:191]\nbuffer_load_dwordx4 v245,s[40:43],s69 offen offset:2048 lds\nM v[188:191],v[92:95],v[212:215],v[188:191]\nds_read_b64_tr_b16 v[196:197],v227 offset:33280\ns_waitcnt vmcnt(26)\n" \
"M v[128:131],v[104:107],v[200:203],v[128:131]\nds_read_b64_tr_b16 v[198:199],v227 offset:37376\ns_waitcnt vmcnt(23)\nM v[128:131],v[120:123],v[204:207],v[128:131]\nds_read_b64_tr_b16 v[212:213],v227 offset:41472\nds_read_b64_tr_b16 v[214:215],v227 offset:45568\nM v[132:135],v[108:111],v[200:203],v[132:135]\nds_read_b64_tr_b16 v[200:201],v226 offset:33792\n" \
"s_waitcnt vmcnt(21)\nM v[132:135],v[124:127],v[204:207],v[132:135]\nds_read_b64_tr_b16 v[202:203],v226 offset:37888\ns_waitcnt lgkmcnt(6)\nM v[136:139],v[104:107],v[216:219],v[136:139]\nbuffer_load_dwordx4 a[36:39],v240,s[52:55],s65 offen offset:1024 sc1\nM v[136:139],v[120:123],v[220:223],v[136:139]\nds_read_b64_tr_b16 v[204:205],v226 offset:41984\n" \
"ds_read_b64_tr_b16 v[206:207],v226 offset:46080\nM v[140:143],v[108:111],v[216:219],v[140:143]\nbuffer_load_dwordx4 v246,s[40:43],s69 offen offset:3072 lds\nM v[140:143],v[124:127],v[220:223],v[140:143]\nds_read_b64_tr_b16 v[216:217],v227 offset:33792\nds_read_b64_tr_b16 v[218:219],v227 offset:37888\nM v[144:147],v[104:107],v[192:195],v[144:147]\nds_read_b64_tr_b16 v[220:221],v227 offset:41984\n" \
"ds_read_b64_tr_b16 v[222:223],v227 offset:46080\nM v[144:147],v[120:123],v[208:211],v[144:147]\nv_mov_b32 v250,83968\nv_accvgpr_write_b32 a194,0\nM v[148:151],v[108:111],v[192:195],v[148:151]\nds_read_b64_tr_b16 v[192:193],v226 offset:34304\nds_read_b64_tr_b16 v[194:195],v226 offset:38400\nM v[148:151],v[124:127],v[208:211],v[148:151]\n" \
"ds_read_b64_tr_b16 v[208:209],v226 offset:42496\ns_waitcnt lgkmcnt(9)\nM v[152:155],v[104:107],v[196:199],v[152:155]\nbuffer_load_dwordx4 a[40:43],v240,s[52:55],s65 offen offset:2048 sc1\nM v[152:155],v[120:123],v[212:215],v[152:155]\nds_read_b64_tr_b16 v[210:211],v226 offset:46592\nv_accvgpr_write_b32 a195,0\nM v[156:159],v[108:111],v[196:199],v[156:159]\n" \
"s_waitcnt vmcnt(16)\nbuffer_store_dword v252,off,s[52:55],s76 sc1\ns_barrier\nM v[156:159],v[124:127],v[212:215],v[156:159]\nds_read_b64_tr_b16 v[196:197],v227 offset:34304\nds_read_b64_tr_b16 v[198:199],v227 offset:38400\nM v[160:163],v[104:107],v[200:203],v[160:163]\nds_read_b64_tr_b16 v[212:213],v227 offset:42496\n" \
"s_waitcnt lgkmcnt(7)\nM v[160:163],v[120:123],v[204:207],v[160:163]\nds_read_b64_tr_b16 v[214:215],v227 offset:46592\nv_accvgpr_write_b32 a196,0\nM v[164:167],v[108:111],v[200:203],v[164:167]\nds_read_b64_tr_b16 v[200:201],v226 offset:16384\nds_read_b64_tr_b16 v[202:203],v226 offset:20480\nM v[164:167],v[124:127],v[204:207],v[164:167]\n" \
"ds_read_b64_tr_b16 v[204:205],v226 offset:24576\nds_read_b64_tr_b16 v[206:207],v226 offset:28672\nM v[168:171],v[104:107],v[216:219],v[168:171]\nbuffer_load_dwordx4 a[44:47],v240,s[52:55],s65 offen offset:3072 sc1\nM v[168:171],v[120:123],v[220:223],v[168:171]\ns_add_u32 s65,s65,4096\nv_accvgpr_write_b32 a197,0\nM v[172:175],v[108:111],v[216:219],v[172:175]\n" \
"ds_read_b64_tr_b16 v[216:217],v227 offset:16384\nds_read_b64_tr_b16 v[218:219],v227 offset:20480\nM v[172:175],v[124:127],v[220:223],v[172:175]\nds_read_b64_tr_b16 v[220:221],v227 offset:24576\ns_waitcnt lgkmcnt(8)\nM v[176:179],v[104:107],v[192:195],v[176:179]\nds_read_b64_tr_b16 v[222:223],v227 offset:28672\ns_load_dwordx2 s[44:45],s[4:5],0x68\n" \
"M v[176:179],v[120:123],v[208:211],v[176:179]\nds_write_b128 v250,a[244:247]\nM v[180:183],v[108:111],v[192:195],v[180:183]\nds_read_b64_tr_b16 v[192:193],v226 offset:16896\nds_read_b64_tr_b16 v[194:195],v226 offset:20992\nM v[180:183],v[124:127],v[208:211],v[180:183]\nds_read_b64_tr_b16 v[208:209],v226 offset:25088\nds_read_b64_tr_b16 v[210:211],v226 offset:29184\n" \
"M v[184:187],v[104:107],v[196:199],v[184:187]\nbuffer_load_dwordx4 a[48:51],v240,s[52:55],s65 offen offset:0 sc1\ns_waitcnt lgkmcnt(9)\nM v[184:187],v[120:123],v[212:215],v[184:187]\ns_mov_b32 s46,2097152\nM v[188:191],v[108:111],v[196:199],v[188:191]\nds_read_b64_tr_b16 v[196:197],v227 offset:16896\nds_read_b64_tr_b16 v[198:199],v227 offset:20992\n" \
"M v[188:191],v[124:127],v[212:215],v[188:191]\nds_read_b64_tr_b16 v[212:213],v227 offset:25088\nds_read_b64_tr_b16 v[214:215],v227 offset:29184\nM v[128:131],a[0:3],v[200:203],v[128:131]\nM v[128:131],a[8:11],v[204:207],v[128:131]\nM v[132:135],a[4:7],v[200:203],v[132:135]\nbuffer_load_dwordx4 a[160:163],v238,s[52:55],0 offen offset:0 sc1\ns_waitcnt vmcnt(18)\n" \
"M v[132:135],a[12:15],v[204:207],v[132:135]\nds_read_b64_tr_b16 v[200:201],v226 offset:17408\ns_waitcnt lgkmcnt(5)\nM v[136:139],a[0:3],v[216:219],v[136:139]\nbuffer_load_dwordx4 a[52:55],v240,s[52:55],s65 offen offset:1024 sc1\nM v[136:139],a[8:11],v[220:223],v[136:139]\nds_read_b64_tr_b16 v[202:203],v226 offset:21504\nds_read_b64_tr_b16 v[204:205],v226 offset:25600\n" \
"M v[140:143],a[4:7],v[216:219],v[140:143]\nds_read_b64_tr_b16 v[206:207],v226 offset:29696\nds_read_b64_tr_b16 v[216:217],v227 offset:17408\nM v[140:143],a[12:15],v[220:223],v[140:143]\nglobal_load_dword a200,v230,s[26:27] offset:-4096\nM v[144:147],a[0:3],v[192:195],v[144:147]\nbuffer_load_dwordx4 a[164:167],v238,s[52:55],0 offen offset:16 sc1\nM v[144:147],a[8:11],v[208:211],v[144:147]\n" \
"ds_read_b64_tr_b16 v[218:219],v227 offset:21504\nglobal_load_dword a201,v230,s[26:27] offset:-3072\nM v[148:151],a[4:7],v[192:195],v[148:151]\nds_read_b64_tr_b16 v[220:221],v227 offset:25600\nds_read_b64_tr_b16 v[222:223],v227 offset:29696\nM v[148:151],a[12:15],v[208:211],v[148:151]\nglobal_load_dword a202,v230,s[26:27] offset:-2048\ns_waitcnt lgkmcnt(7)\n" \
"M v[152:155],a[0:3],v[196:199],v[152:155]\nbuffer_load_dwordx4 a[56:59],v240,s[52:55],s65 offen offset:2048 sc1\nM v[152:155],a[8:11],v[212:215],v[152:155]\nds_read_b64_tr_b16 v[192:193],v226 offset:17920\nds_read_b64_tr_b16 v[194:195],v226 offset:22016\nglobal_load_dword a203,v230,s[26:27] offset:-1024\nM v[156:159],a[4:7],v[196:199],v[156:159]\nds_read_b64_tr_b16 v[208:209],v226 offset:26112\n" \
"ds_read_b64_tr_b16 v[210:211],v226 offset:30208\nbuffer_load_dwordx4 a[168:171],v238,s[52:55],0 offen offset:2048 sc1\nM v[156:159],a[12:15],v[212:215],v[156:159]\nglobal_load_dword a204,v230,s[26:27] offset:0\ns_waitcnt lgkmcnt(6)\nM v[160:163],a[0:3],v[200:203],v[160:163]\nds_read_b64_tr_b16 v[196:197],v227 offset:17920\nds_read_b64_tr_b16 v[198:199],v227 offset:22016\n" \
"M v[160:163],a[8:11],v[204:207],v[160:163]\nglobal_load_dword a205,v230,s[26:27] offset:1024\nM v[164:167],a[4:7],v[200:203],v[164:167]\nds_read_b64_tr_b16 v[212:213],v227 offset:26112\nds_read_b64_tr_b16 v[214:215],v227 offset:30208\nM v[164:167],a[12:15],v[204:207],v[164:167]\nglobal_load_dword a206,v230,s[26:27] offset:2048\nM v[168:171],a[0:3],v[216:219],v[168:171]\n" \
"buffer_load_dwordx4 a[60:63],v240,s[52:55],s65 offen offset:3072 sc1\nbuffer_load_dwordx4 a[172:175],v238,s[52:55],0 offen offset:2064 sc1\ns_waitcnt lgkmcnt(4)\nM v[168:171],a[8:11],v[220:223],v[168:171]\nds_read_b64_tr_b16 v[200:201],v226 offset:49152\nds_read_b64_tr_b16 v[202:203],v226 offset:53248\nglobal_load_dword a207,v230,s[26:27] offset:3072\nM v[172:175],a[4:7],v[216:219],v[172:175]\n" \
"ds_read_b64_tr_b16 v[204:205],v226 offset:57344\nds_read_b64_tr_b16 v[206:207],v226 offset:61440\nM v[172:175],a[12:15],v[220:223],v[172:175]\nglobal_load_dword a208,v231,s[26:27] offset:-4096\nM v[176:179],a[0:3],v[192:195],v[176:179]\nds_read_b64_tr_b16 v[216:217],v227 offset:49152\nds_read_b64_tr_b16 v[218:219],v227 offset:53248\nM v[176:179],a[8:11],v[208:211],v[176:179]\n" \
"global_load_dword a209,v231,s[26:27] offset:-3072\nM v[180:183],a[4:7],v[192:195],v[180:183]\nds_read_b64_tr_b16 v[220:221],v227 offset:57344\nds_read_b64_tr_b16 v[222:223],v227 offset:61440\nbuffer_load_dwordx4 a[176:179],v238,s[52:55],s97 offen offset:0 sc1\nM v[180:183],a[12:15],v[208:211],v[180:183]\nglobal_load_dword a210,v231,s[26:27] offset:-2048\ns_waitcnt lgkmcnt(6)\n" \
"M v[184:187],a[0:3],v[196:199],v[184:187]\nds_read_b64_tr_b16 v[192:193],v226 offset:49664\nds_read_b64_tr_b16 v[194:195],v226 offset:53760\nM v[184:187],a[8:11],v[212:215],v[184:187]\nglobal_load_dword a211,v231,s[26:27] offset:-1024\nM v[188:191],a[4:7],v[196:199],v[188:191]\nds_read_b64_tr_b16 v[208:209],v226 offset:57856\nds_read_b64_tr_b16 v[210:211],v226 offset:61952\n" \
"M v[188:191],a[12:15],v[212:215],v[188:191]\nglobal_load_dword a252,v231,s[26:27] offset:0\ns_waitcnt vmcnt(36)\nM v[128:131],a[16:19],v[200:203],v[128:131]\nbuffer_load_dwordx4 a[180:183],v238,s[52:55],s97 offen offset:16 sc1\ns_waitcnt vmcnt(33) lgkmcnt(4)\nM v[128:131],a[24:27],v[204:207],v[128:131]\nds_read_b64_tr_b16 v[196:197],v227 offset:49664\n" \
"ds_read_b64_tr_b16 v[198:199],v227 offset:53760\nglobal_load_dword a253,v231,s[26:27] offset:1024\nM v[132:135],a[20:23],v[200:203],v[132:135]\nds_read_b64_tr_b16 v[212:213],v227 offset:57856\nds_read_b64_tr_b16 v[214:215],v227 offset:61952\ns_waitcnt vmcnt(32)\nM v[132:135],a[28:31],v[204:207],v[132:135]\nglobal_load_dword a254,v231,s[26:27] offset:2048\n" \
"M v[136:139],a[16:19],v[216:219],v[136:139]\nds_read_b64_tr_b16 v[200:201],v226 offset:50176\nds_read_b64_tr_b16 v[202:203],v226 offset:54272\nM v[136:139],a[24:27],v[220:223],v[136:139]\nglobal_load_dword a255,v231,s[26:27] offset:3072\nM v[140:143],a[20:23],v[216:219],v[140:143]\nds_read_b64_tr_b16 v[204:205],v226 offset:58368\nds_read_b64_tr_b16 v[206:207],v226 offset:62464\n" \
"buffer_load_dwordx4 a[184:187],v238,s[52:55],s97 offen offset:2048 sc1\nM v[140:143],a[28:31],v[220:223],v[140:143]\nds_read_b64_tr_b16 v[216:217],v227 offset:50176\ns_waitcnt lgkmcnt(7)\nM v[144:147],a[16:19],v[192:195],v[144:147]\nbuffer_load_dword v248,v254,s[52:55],s80 offen sc1\nM v[144:147],a[24:27],v[208:211],v[144:147]\nds_read_b64_tr_b16 v[218:219],v227 offset:54272\n" \
"ds_read_b64_tr_b16 v[220:221],v227 offset:58368\nM v[148:151],a[20:23],v[192:195],v[148:151]\nds_read_b64_tr_b16 v[222:223],v227 offset:62464\nds_read_b64_tr_b16 v[192:193],v226 offset:50688\nM v[148:151],a[28:31],v[208:211],v[148:151]\nglobal_load_ushort v64,v236,s[8:9] offset:0\nM v[152:155],a[16:19],v[196:199],v[152:155]\nbuffer_load_dwordx4 a[188:191],v238,s[52:55],s97 offen offset:2064 sc1\n" \
"s_waitcnt lgkmcnt(7)\nM v[152:155],a[24:27],v[212:215],v[152:155]\nds_read_b64_tr_b16 v[194:195],v226 offset:54784\nglobal_load_ushort v80,v237,s[8:9] offset:0\nM v[156:159],a[20:23],v[196:199],v[156:159]\ns_waitcnt vmcnt(33)\ns_barrier\nds_read_b64_tr_b16 v[208:209],v226 offset:58880\n" \
"ds_read_b64_tr_b16 v[210:211],v226 offset:62976\nM v[156:159],a[28:31],v[212:215],v[156:159]\nglobal_load_ushort v96,v235,s[20:21] offset:0\nM v[160:163],a[16:19],v[200:203],v[160:163]\nds_read_b64_tr_b16 v[196:197],v227 offset:50688\ns_waitcnt lgkmcnt(5)\nM v[160:163],a[24:27],v[204:207],v[160:163]\nds_read_b64_tr_b16 v[198:199],v227 offset:54784\n" \
"global_load_ushort v65,v236,s[8:9] offset:32\nM v[164:167],a[20:23],v[200:203],v[164:167]\nds_read_b64_tr_b16 v[212:213],v227 offset:58880\nds_read_b64_tr_b16 v[214:215],v227 offset:62976\nM v[164:167],a[28:31],v[204:207],v[164:167]\nglobal_load_ushort v81,v237,s[8:9] offset:32\nM v[168:171],a[16:19],v[216:219],v[168:171]\nv_add_u32 v226,65536,v242\n" \
"ds_read_b64_tr_b16 v[200:201],v226 offset:0\nM v[168:171],a[24:27],v[220:223],v[168:171]\nds_read_b64_tr_b16 v[202:203],v226 offset:4096\nglobal_load_ushort v97,v235,s[20:21] offset:32\nM v[172:175],a[20:23],v[216:219],v[172:175]\nds_read_b64_tr_b16 v[204:205],v226 offset:8192\nds_read_b64_tr_b16 v[206:207],v226 offset:12288\nM v[172:175],a[28:31],v[220:223],v[172:175]\n" \
"global_load_ushort v66,v236,s[8:9] offset:64\ns_waitcnt lgkmcnt(7)\nM v[176:179],a[16:19],v[192:195],v[176:179]\nv_xor_b32 v227,32,v226\nds_read_b64_tr_b16 v[216:217],v227 offset:0\nM v[176:179],a[24:27],v[208:211],v[176:179]\nds_read_b64_tr_b16 v[218:219],v227 offset:4096\nglobal_load_ushort v82,v237,s[8:9] offset:64\n" \
"M v[180:183],a[20:23],v[192:195],v[180:183]\nds_read_b64_tr_b16 v[220:221],v227 offset:8192\nds_read_b64_tr_b16 v[222:223],v227 offset:12288\nM v[180:183],a[28:31],v[208:211],v[180:183]\nglobal_load_ushort v98,v235,s[20:21] offset:64\ns_waitcnt lgkmcnt(6)\nM v[184:187],a[16:19],v[196:199],v[184:187]\nds_read_b64_tr_b16 v[192:193],v226 offset:512\n" \
"ds_read_b64_tr_b16 v[194:195],v226 offset:4608\nM v[184:187],a[24:27],v[212:215],v[184:187]\nglobal_load_ushort v67,v236,s[8:9] offset:96\nM v[188:191],a[20:23],v[196:199],v[188:191]\nds_read_b64_tr_b16 v[208:209],v226 offset:8704\nds_read_b64_tr_b16 v[210:211],v226 offset:12800\nM v[188:191],a[28:31],v[212:215],v[188:191]\nglobal_load_ushort v83,v237,s[8:9] offset:96\n" \
"M v[128:131],a[32:35],v[200:203],v[128:131]\nds_read_b64_tr_b16 v[196:197],v227 offset:512\ns_waitcnt lgkmcnt(7)\nM v[128:131],a[40:43],v[204:207],v[128:131]\nds_read_b64_tr_b16 v[198:199],v227 offset:4608\nglobal_load_ushort v99,v235,s[20:21] offset:96\nM v[132:135],a[36:39],v[200:203],v[132:135]\nds_read_b64_tr_b16 v[212:213],v227 offset:8704\n" \
"ds_read_b64_tr_b16 v[214:215],v227 offset:12800\ns_waitcnt vmcnt(32)\nM v[132:135],a[44:47],v[204:207],v[132:135]\nglobal_load_ushort v68,v236,s[8:9] offset:128\nM v[136:139],a[32:35],v[216:219],v[136:139]\nds_read_b64_tr_b16 v[200:201],v226 offset:1024\ns_waitcnt lgkmcnt(7)\nM v[136:139],a[40:43],v[220:223],v[136:139]\n" \
"ds_read_b64_tr_b16 v[202:203],v226 offset:5120\nglobal_load_ushort v84,v237,s[8:9] offset:128\nM v[140:143],a[36:39],v[216:219],v[140:143]\nds_read_b64_tr_b16 v[204:205],v226 offset:9216\nds_read_b64_tr_b16 v[206:207],v226 offset:13312\nM v[140:143],a[44:47],v[220:223],v[140:143]\nglobal_load_ushort v100,v235,s[20:21] offset:128\nM v[144:147],a[32:35],v[192:195],v[144:147]\n" \
"ds_read_b64_tr_b16 v[216:217],v227 offset:1024\ns_waitcnt lgkmcnt(7)\nM v[144:147],a[40:43],v[208:211],v[144:147]\nds_read_b64_tr_b16 v[218:219],v227 offset:5120\nglobal_load_ushort v69,v236,s[8:9] offset:160\nM v[148:151],a[36:39],v[192:195],v[148:151]\nds_read_b64_tr_b16 v[220:221],v227 offset:9216\nds_read_b64_tr_b16 v[222:223],v227 offset:13312\n" \
"M v[148:151],a[44:47],v[208:211],v[148:151]\nglobal_load_ushort v85,v237,s[8:9] offset:160\nM v[152:155],a[32:35],v[196:199],v[152:155]\nds_read_b64_tr_b16 v[192:193],v226 offset:1536\ns_waitcnt lgkmcnt(7)\nM v[152:155],a[40:43],v[212:215],v[152:155]\nds_read_b64_tr_b16 v[194:195],v226 offset:5632\nglobal_load_ushort v101,v235,s[20:21] offset:160\n" \
"M v[156:159],a[36:39],v[196:199],v[156:159]\nds_read_b64_tr_b16 v[208:209],v226 offset:9728\nds_read_b64_tr_b16 v[210:211],v226 offset:13824\nM v[156:159],a[44:47],v[212:215],v[156:159]\nglobal_load_ushort v70,v236,s[8:9] offset:192\nM v[160:163],a[32:35],v[200:203],v[160:163]\nds_read_b64_tr_b16 v[196:197],v227 offset:1536\ns_waitcnt lgkmcnt(7)\n" \
"M v[160:163],a[40:43],v[204:207],v[160:163]\nds_read_b64_tr_b16 v[198:199],v227 offset:5632\nglobal_load_ushort v86,v237,s[8:9] offset:192\nM v[164:167],a[36:39],v[200:203],v[164:167]\nds_read_b64_tr_b16 v[212:213],v227 offset:9728\nds_read_b64_tr_b16 v[214:215],v227 offset:13824\nM v[164:167],a[44:47],v[204:207],v[164:167]\ns_waitcnt vmcnt(21)\n" \
"v_cmp_eq_u32 vcc,0,v248\nglobal_load_ushort v102,v235,s[20:21] offset:192\nM v[168:171],a[32:35],v[216:219],v[168:171]\ns_cbranch_vccnz .Lps7\n.Lpb7:\nbuffer_load_dword v248,v251,s[52:55],s79 offen sc1\ns_waitcnt lgkmcnt(4)\nM v[168:171],a[40:43],v[220:223],v[168:171]\n" \
"ds_read_b64_tr_b16 v[200:201],v226 offset:32768\nds_read_b64_tr_b16 v[202:203],v226 offset:36864\nglobal_load_ushort v71,v236,s[8:9] offset:224\nM v[172:175],a[36:39],v[216:219],v[172:175]\nds_read_b64_tr_b16 v[204:205],v226 offset:40960\nds_read_b64_tr_b16 v[206:207],v226 offset:45056\nM v[172:175],a[44:47],v[220:223],v[172:175]\nglobal_load_ushort v87,v237,s[8:9] offset:224\n" \
"M v[176:179],a[32:35],v[192:195],v[176:179]\nds_read_b64_tr_b16 v[216:217],v227 offset:32768\nds_read_b64_tr_b16 v[218:219],v227 offset:36864\nM v[176:179],a[40:43],v[208:211],v[176:179]\nglobal_load_ushort v103,v235,s[20:21] offset:224\nM v[180:183],a[36:39],v[192:195],v[180:183]\nds_read_b64_tr_b16 v[220:221],v227 offset:40960\nds_read_b64_tr_b16 v[222:223],v227 offset:45056\n" \
"M v[180:183],a[44:47],v[208:211],v[180:183]\nbuffer_load_dwordx2 v[112:113],v239,s[52:55],0 offen offset:0 sc1\ns_waitcnt lgkmcnt(8)\nM v[184:187],a[32:35],v[196:199],v[184:187]\nds_read_b64_tr_b16 v[192:193],v226 offset:33280\nds_read_b64_tr_b16 v[194:195],v226 offset:37376\nM v[184:187],a[40:43],v[212:215],v[184:187]\nbuffer_load_dwordx2 v[114:115],v239,s[52:55],0 offen offset:2048 sc1\n" \
"M v[188:191],a[36:39],v[196:199],v[188:191]\nds_read_b64_tr_b16 v[208:209],v226 offset:41472\nds_read_b64_tr_b16 v[210:211],v226 offset:45568\nM v[188:191],a[44:47],v[212:215],v[188:191]\nbuffer_load_dwordx2 v[116:117],v239,s[52:55],s97 offen offset:0 sc1\ns_waitcnt lgkmcnt(6)\nM v[128:131],a[48:51],v[200:203],v[128:131]\nds_read_b64_tr_b16 v[196:197],v227 offset:33280\n" \
"ds_read_b64_tr_b16 v[198:199],v227 offset:37376\nM v[128:131],a[56:59],v[204:207],v[128:131]\nbuffer_load_dwordx2 v[118:119],v239,s[52:55],s97 offen offset:2048 sc1\nM v[132:135],a[52:55],v[200:203],v[132:135]\nds_read_b64_tr_b16 v[212:213],v227 offset:41472\nds_read_b64_tr_b16 v[214:215],v227 offset:45568\nM v[132:135],a[60:63],v[204:207],v[132:135]\nds_read_b64_tr_b16 v[200:201],v226 offset:33792\n" \
"ds_read_b64_tr_b16 v[202:203],v226 offset:37888\nM v[136:139],a[48:51],v[216:219],v[136:139]\nds_read_b64_tr_b16 v[204:205],v226 offset:41984\ns_waitcnt lgkmcnt(9)\nM v[136:139],a[56:59],v[220:223],v[136:139]\nds_read_b64_tr_b16 v[206:207],v226 offset:46080\ns_add_u32 s65,s10,s67\nM v[140:143],a[52:55],v[216:219],v[140:143]\n" \
"ds_read_b64_tr_b16 v[216:217],v227 offset:33792\nds_read_b64_tr_b16 v[218:219],v227 offset:37888\nM v[140:143],a[60:63],v[220:223],v[140:143]\nds_read_b64_tr_b16 v[220:221],v227 offset:41984\nds_read_b64_tr_b16 v[222:223],v227 offset:46080\nM v[144:147],a[48:51],v[192:195],v[144:147]\nv_cvt_pk_bf16_f32 v128,v128,v136\ns_waitcnt lgkmcnt(10)\n" \
"M v[144:147],a[56:59],v[208:211],v[144:147]\nv_cvt_pk_bf16_f32 v129,v129,v137\nv_cvt_pk_bf16_f32 v130,v130,v138\nM v[148:151],a[52:55],v[192:195],v[148:151]\nds_read_b64_tr_b16 v[192:193],v226 offset:34304\nds_read_b64_tr_b16 v[194:195],v226 offset:38400\nM v[148:151],a[60:63],v[208:211],v[148:151]\nds_read_b64_tr_b16 v[208:209],v226 offset:42496\n" \
"ds_read_b64_tr_b16 v[210:211],v226 offset:46592\nM v[152:155],a[48:51],v[196:199],v[152:155]\nv_add_u32 v226,131072,v242\ns_waitcnt lgkmcnt(8)\nM v[152:155],a[56:59],v[212:215],v[152:155]\nv_cvt_pk_bf16_f32 v131,v131,v139\nv_min_u32 v136,-2,v128\nM v[156:159],a[52:55],v[196:199],v[156:159]\n" \
"ds_read_b64_tr_b16 v[196:197],v227 offset:34304\nds_read_b64_tr_b16 v[198:199],v227 offset:38400\nM v[156:159],a[60:63],v[212:215],v[156:159]\nds_read_b64_tr_b16 v[212:213],v227 offset:42496\nds_read_b64_tr_b16 v[214:215],v227 offset:46592\nM v[160:163],a[48:51],v[200:203],v[160:163]\nv_xor_b32 v227,32,v226\nv_cvt_pk_bf16_f32 v144,v144,v152\n" \
"M v[160:163],a[56:59],v[204:207],v[160:163]\nv_min_u32 v137,-2,v144\nv_cvt_pk_bf16_f32 v145,v145,v153\nM v[164:167],a[52:55],v[200:203],v[164:167]\nds_read_b64_tr_b16 v[200:201],v226 offset:0\nds_read_b64_tr_b16 v[202:203],v226 offset:4096\nM v[164:167],a[60:63],v[204:207],v[164:167]\nds_read_b64_tr_b16 v[204:205],v226 offset:8192\n" \
"s_waitcnt lgkmcnt(7)\nM v[168:171],a[48:51],v[216:219],v[168:171]\nds_read_b64_tr_b16 v[206:207],v226 offset:12288\nv_cvt_pk_bf16_f32 v146,v146,v154\nM v[168:171],a[56:59],v[220:223],v[168:171]\nv_cvt_pk_bf16_f32 v147,v147,v155\nv_min_u32 v152,-2,v129\nM v[172:175],a[52:55],v[216:219],v[172:175]\n" \
"s_waitcnt vmcnt(7)\nv_cmp_eq_u32 vcc,0,v248\nM v[172:175],a[60:63],v[220:223],v[172:175]\ns_cbranch_vccnz .Lps8\n.Lpb8:\nds_read_b64_tr_b16 v[216:217],v227 offset:0\nM v[176:179],a[48:51],v[192:195],v[176:179]\nbuffer_load_dwordx4 a[64:67],v240,s[52:55],s65 offen offset:0 sc1\n" \
"M v[176:179],a[56:59],v[208:211],v[176:179]\nds_read_b64_tr_b16 v[218:219],v227 offset:4096\nds_read_b64_tr_b16 v[220:221],v227 offset:8192\nM v[180:183],a[52:55],v[192:195],v[180:183]\nds_read_b64_tr_b16 v[222:223],v227 offset:12288\nds_read_b64_tr_b16 v[192:193],v226 offset:512\nM v[180:183],a[60:63],v[208:211],v[180:183]\nds_read_b64_tr_b16 v[194:195],v226 offset:4608\n" \
"s_waitcnt lgkmcnt(6)\nM v[184:187],a[48:51],v[196:199],v[184:187]\nbuffer_load_dwordx4 a[68:71],v240,s[52:55],s65 offen offset:1024 sc1\nM v[184:187],a[56:59],v[212:215],v[184:187]\nds_read_b64_tr_b16 v[208:209],v226 offset:8704\nds_read_b64_tr_b16 v[210:211],v226 offset:12800\nM v[188:191],a[52:55],v[196:199],v[188:191]\nds_read_b64_tr_b16 v[196:197],v227 offset:512\n" \
"ds_read_b64_tr_b16 v[198:199],v227 offset:4608\nM v[188:191],a[60:63],v[212:215],v[188:191]\nds_read_b64_tr_b16 v[212:213],v227 offset:8704\nds_read_b64_tr_b16 v[214:215],v227 offset:12800\nM v[8:11],v[0:3],v[200:203],0\nbuffer_load_dwordx4 a[72:75],v240,s[52:55],s65 offen offset:2048 sc1\nM v[8:11],v[16:19],v[204:207],v[8:11]\nv_cvt_pk_bf16_f32 v160,v160,v168\n" \
"v_cvt_pk_bf16_f32 v176,v176,v184\nM v[12:15],v[4:7],v[200:203],0\nds_read_b64_tr_b16 v[200:201],v226 offset:1024\nds_read_b64_tr_b16 v[202:203],v226 offset:5120\nM v[12:15],v[20:23],v[204:207],v[12:15]\nds_read_b64_tr_b16 v[204:205],v226 offset:9216\ns_waitcnt lgkmcnt(9)\nM v[24:27],v[0:3],v[216:219],0\n" \
"buffer_load_dwordx4 a[76:79],v240,s[52:55],s65 offen offset:3072 sc1\nM v[24:27],v[16:19],v[220:223],v[24:27]\nds_read_b64_tr_b16 v[206:207],v226 offset:13312\ns_add_u32 s65,s65,4096\nM v[28:31],v[4:7],v[216:219],0\nds_read_b64_tr_b16 v[216:217],v227 offset:1024\nds_read_b64_tr_b16 v[218:219],v227 offset:5120\nM v[28:31],v[20:23],v[220:223],v[28:31]\n" \
"ds_read_b64_tr_b16 v[220:221],v227 offset:9216\nds_read_b64_tr_b16 v[222:223],v227 offset:13312\nM v[40:43],v[0:3],v[192:195],0\nbuffer_load_dwordx4 a[80:83],v240,s[52:55],s65 offen offset:0 sc1\ns_waitcnt lgkmcnt(8)\nM v[40:43],v[16:19],v[208:211],v[40:43]\nv_min_u32 v138,-2,v160\nv_min_u32 v139,-2,v176\n" \
"M v[44:47],v[4:7],v[192:195],0\nds_read_b64_tr_b16 v[192:193],v226 offset:1536\nds_read_b64_tr_b16 v[194:195],v226 offset:5632\nM v[44:47],v[20:23],v[208:211],v[44:47]\nds_read_b64_tr_b16 v[208:209],v226 offset:9728\nds_read_b64_tr_b16 v[210:211],v226 offset:13824\nM v[56:59],v[0:3],v[196:199],0\nbuffer_load_dwordx4 a[84:87],v240,s[52:55],s65 offen offset:1024 sc1\n" \
"M v[56:59],v[16:19],v[212:215],v[56:59]\nv_cvt_pk_bf16_f32 v161,v161,v169\nv_cvt_pk_bf16_f32 v177,v177,v185\nM v[60:63],v[4:7],v[196:199],0\nbuffer_store_dwordx4 v[136:139],v253,s[52:55],s33 offen offset:0 sc1\nM v[60:63],v[20:23],v[212:215],v[60:63]\nds_read_b64_tr_b16 v[196:197],v227 offset:1536\ns_waitcnt lgkmcnt(5)\n" \
"M v[72:75],v[0:3],v[200:203],0\nbuffer_load_dwordx4 a[88:91],v240,s[52:55],s65 offen offset:2048 sc1\nM v[72:75],v[16:19],v[204:207],v[72:75]\nds_read_b64_tr_b16 v[198:199],v227 offset:5632\nds_read_b64_tr_b16 v[212:213],v227 offset:9728\nM v[76:79],v[4:7],v[200:203],0\nds_read_b64_tr_b16 v[214:215],v227 offset:13824\nds_read_b64_tr_b16 v[200:201],v226 offset:16384\n" \
"M v[76:79],v[20:23],v[204:207],v[76:79]\nds_read_b64_tr_b16 v[202:203],v226 offset:20480\nds_read_b64_tr_b16 v[204:205],v226 offset:24576\nM v[88:91],v[0:3],v[216:219],0\nbuffer_load_dwordx4 a[92:95],v240,s[52:55],s65 offen offset:3072 sc1\nM v[88:91],v[16:19],v[220:223],v[88:91]\nds_read_b64_tr_b16 v[206:207],v226 offset:28672\ns_add_u32 s65,s11,s67\n" \
"M v[92:95],v[4:7],v[216:219],0\nds_read_b64_tr_b16 v[216:217],v227 offset:16384\nds_read_b64_tr_b16 v[218:219],v227 offset:20480\nM v[92:95],v[20:23],v[220:223],v[92:95]\nds_read_b64_tr_b16 v[220:221],v227 offset:24576\ns_waitcnt lgkmcnt(10)\nM v[104:107],v[0:3],v[192:195],0\nbuffer_load_dwordx4 a[96:99],v240,s[52:55],s65 offen offset:0 sc1\n" \
"M v[104:107],v[16:19],v[208:211],v[104:107]\nds_read_b64_tr_b16 v[222:223],v227 offset:28672\nv_min_u32 v153,-2,v145\nM v[108:111],v[4:7],v[192:195],0\nds_read_b64_tr_b16 v[192:193],v226 offset:16896\nds_read_b64_tr_b16 v[194:195],v226 offset:20992\nM v[108:111],v[20:23],v[208:211],v[108:111]\nv_min_u32 v154,-2,v161\n" \
"s_waitcnt lgkmcnt(7)\nM v[120:123],v[0:3],v[196:199],0\nbuffer_load_dwordx4 a[100:103],v240,s[52:55],s65 offen offset:1024 sc1\nM v[120:123],v[16:19],v[212:215],v[120:123]\nv_min_u32 v155,-2,v177\nds_read_b64_tr_b16 v[208:209],v226 offset:25088\nM v[124:127],v[4:7],v[196:199],0\nds_read_b64_tr_b16 v[210:211],v226 offset:29184\n" \
"buffer_store_dwordx4 v[152:155],v253,s[52:55],s33 offen offset:1024 sc1\nM v[124:127],v[20:23],v[212:215],v[124:127]\nds_read_b64_tr_b16 v[196:197],v227 offset:16896\nds_read_b64_tr_b16 v[198:199],v227 offset:20992\nM v[8:11],v[32:35],v[200:203],v[8:11]\nbuffer_load_dwordx4 a[104:107],v240,s[52:55],s65 offen offset:2048 sc1\ns_waitcnt lgkmcnt(6)\nM v[8:11],v[48:51],v[204:207],v[8:11]\n" \
"ds_read_b64_tr_b16 v[212:213],v227 offset:25088\nds_read_b64_tr_b16 v[214:215],v227 offset:29184\nM v[12:15],v[36:39],v[200:203],v[12:15]\nds_read_b64_tr_b16 v[200:201],v226 offset:17408\nds_read_b64_tr_b16 v[202:203],v226 offset:21504\nM v[12:15],v[52:55],v[204:207],v[12:15]\nds_read_b64_tr_b16 v[204:205],v226 offset:25600\nds_read_b64_tr_b16 v[206:207],v226 offset:29696\n" \
"M v[24:27],v[32:35],v[216:219],v[24:27]\nbuffer_load_dwordx4 a[108:111],v240,s[52:55],s65 offen offset:3072 sc1\nM v[24:27],v[48:51],v[220:223],v[24:27]\ns_add_u32 s65,s65,4096\nv_cvt_pk_bf16_f32 v162,v162,v170\nM v[28:31],v[36:39],v[216:219],v[28:31]\nds_read_b64_tr_b16 v[216:217],v227 offset:17408\nds_read_b64_tr_b16 v[218:219],v227 offset:21504\n" \
"M v[28:31],v[52:55],v[220:223],v[28:31]\nds_read_b64_tr_b16 v[220:221],v227 offset:25600\ns_waitcnt lgkmcnt(9)\nM v[40:43],v[32:35],v[192:195],v[40:43]\nbuffer_load_dwordx4 a[112:115],v240,s[52:55],s65 offen offset:0 sc1\nM v[40:43],v[48:51],v[208:211],v[40:43]\nds_read_b64_tr_b16 v[222:223],v227 offset:29696\nv_cvt_pk_bf16_f32 v163,v163,v171\n" \
"M v[44:47],v[36:39],v[192:195],v[44:47]\nds_read_b64_tr_b16 v[192:193],v226 offset:17920\nds_read_b64_tr_b16 v[194:195],v226 offset:22016\nM v[44:47],v[52:55],v[208:211],v[44:47]\nv_cvt_pk_bf16_f32 v178,v178,v186\nv_min_u32 v168,-2,v130\nM v[56:59],v[32:35],v[196:199],v[56:59]\nbuffer_load_dwordx4 a[116:119],v240,s[52:55],s65 offen offset:1024 sc1\n" \
"s_waitcnt lgkmcnt(6)\nM v[56:59],v[48:51],v[212:215],v[56:59]\nv_min_u32 v169,-2,v146\nv_min_u32 v170,-2,v162\nM v[60:63],v[36:39],v[196:199],v[60:63]\nv_min_u32 v171,-2,v178\nds_read_b64_tr_b16 v[208:209],v226 offset:26112\nds_read_b64_tr_b16 v[210:211],v226 offset:30208\n" \
"buffer_store_dwordx4 v[168:171],v253,s[52:55],s33 offen offset:2048 sc1\nM v[60:63],v[52:55],v[212:215],v[60:63]\nds_read_b64_tr_b16 v[196:197],v227 offset:17920\nds_read_b64_tr_b16 v[198:199],v227 offset:22016\nM v[72:75],v[32:35],v[200:203],v[72:75]\nbuffer_load_dwordx4 a[120:123],v240,s[52:55],s65 offen offset:2048 sc1\nM v[72:75],v[48:51],v[204:207],v[72:75]\nds_read_b64_tr_b16 v[212:213],v227 offset:26112\n" \
"ds_read_b64_tr_b16 v[214:215],v227 offset:30208\nM v[76:79],v[36:39],v[200:203],v[76:79]\nv_add_u32 v226,0,v242\nds_read_b64_tr_b16 v[200:201],v226 offset:0\nM v[76:79],v[52:55],v[204:207],v[76:79]\nds_read_b64_tr_b16 v[202:203],v226 offset:4096\ns_waitcnt lgkmcnt(8)\nM v[88:91],v[32:35],v[216:219],v[88:91]\n" \
"buffer_load_dwordx4 a[124:127],v240,s[52:55],s65 offen offset:3072 sc1\nM v[88:91],v[48:51],v[220:223],v[88:91]\nds_read_b64_tr_b16 v[204:205],v226 offset:8192\nds_read_b64_tr_b16 v[206:207],v226 offset:12288\nM v[92:95],v[36:39],v[216:219],v[92:95]\ns_add_u32 s65,s14,s67\nv_xor_b32 v227,32,v226\nM v[92:95],v[52:55],v[220:223],v[92:95]\n" \
"ds_read_b64_tr_b16 v[216:217],v227 offset:0\nds_read_b64_tr_b16 v[218:219],v227 offset:4096\nM v[104:107],v[32:35],v[192:195],v[104:107]\nbuffer_load_dwordx4 a[128:131],v240,s[52:55],s65 offen offset:0 sc1\ns_waitcnt lgkmcnt(6)\nM v[104:107],v[48:51],v[208:211],v[104:107]\nds_read_b64_tr_b16 v[220:221],v227 offset:8192\nds_read_b64_tr_b16 v[222:223],v227 offset:12288\n" \
"M v[108:111],v[36:39],v[192:195],v[108:111]\nv_cvt_pk_bf16_f32 v179,v179,v187\nds_read_b64_tr_b16 v[192:193],v226 offset:512\nM v[108:111],v[52:55],v[208:211],v[108:111]\nds_read_b64_tr_b16 v[194:195],v226 offset:4608\nv_min_u32 v184,-2,v131\nM v[120:123],v[32:35],v[196:199],v[120:123]\nbuffer_load_dwordx4 a[132:135],v240,s[52:55],s65 offen offset:1024 sc1\n" \
"M v[120:123],v[48:51],v[212:215],v[120:123]\nv_min_u32 v185,-2,v147\nv_min_u32 v186,-2,v163\nM v[124:127],v[36:39],v[196:199],v[124:127]\nv_min_u32 v187,-2,v179\nbuffer_store_dwordx4 v[184:187],v253,s[52:55],s33 offen offset:3072 sc1\nds_read_b64_tr_b16 v[208:209],v226 offset:8704\nds_read_b64_tr_b16 v[210:211],v226 offset:12800\n" \
"M v[124:127],v[52:55],v[212:215],v[124:127]\nds_read_b64_tr_b16 v[196:197],v227 offset:512\ns_waitcnt vmcnt(18) lgkmcnt(7)\nM v[8:11],a[64:67],v[200:203],v[8:11]\nbuffer_load_dwordx4 a[136:139],v240,s[52:55],s65 offen offset:2048 sc1\nM v[8:11],a[72:75],v[204:207],v[8:11]\nds_read_b64_tr_b16 v[198:199],v227 offset:4608\nds_read_b64_tr_b16 v[212:213],v227 offset:8704\n" \
"M v[12:15],a[68:71],v[200:203],v[12:15]\nds_read_b64_tr_b16 v[214:215],v227 offset:12800\nds_read_b64_tr_b16 v[200:201],v226 offset:1024\nM v[12:15],a[76:79],v[204:207],v[12:15]\nds_read_b64_tr_b16 v[202:203],v226 offset:5120\nds_read_b64_tr_b16 v[204:205],v226 offset:9216\nM v[24:27],a[64:67],v[216:219],v[24:27]\nbuffer_load_dwordx4 a[140:143],v240,s[52:55],s65 offen offset:3072 sc1\n" \
"s_waitcnt lgkmcnt(9)\nM v[24:27],a[72:75],v[220:223],v[24:27]\nds_read_b64_tr_b16 v[206:207],v226 offset:13312\ns_add_u32 s65,s65,4096\nM v[28:31],a[68:71],v[216:219],v[28:31]\nds_read_b64_tr_b16 v[216:217],v227 offset:1024\nds_read_b64_tr_b16 v[218:219],v227 offset:5120\nM v[28:31],a[76:79],v[220:223],v[28:31]\n" \
"ds_read_b64_tr_b16 v[220:221],v227 offset:9216\nds_read_b64_tr_b16 v[222:223],v227 offset:13312\nM v[40:43],a[64:67],v[192:195],v[40:43]\nbuffer_load_dwordx4 a[144:147],v240,s[52:55],s65 offen offset:0 sc1\ns_waitcnt lgkmcnt(9)\nM v[40:43],a[72:75],v[208:211],v[40:43]\nv_cvt_pk_bf16_f32 v132,v132,v140\nv_cvt_pk_bf16_f32 v133,v133,v141\n" \
"M v[44:47],a[68:71],v[192:195],v[44:47]\nv_cvt_pk_bf16_f32 v134,v134,v142\nv_cvt_pk_bf16_f32 v135,v135,v143\nM v[44:47],a[76:79],v[208:211],v[44:47]\nv_cvt_pk_bf16_f32 v148,v148,v156\nv_cvt_pk_bf16_f32 v164,v164,v172\nM v[56:59],a[64:67],v[196:199],v[56:59]\nbuffer_load_dwordx4 a[148:151],v240,s[52:55],s65 offen offset:1024 sc1\n" \
"s_waitcnt lgkmcnt(4)\nM v[56:59],a[72:75],v[212:215],v[56:59]\nv_cvt_pk_bf16_f32 v180,v180,v188\nds_read_b64_tr_b16 v[192:193],v226 offset:1536\nds_read_b64_tr_b16 v[194:195],v226 offset:5632\nM v[60:63],a[68:71],v[196:199],v[60:63]\nv_min_u32 v140,-2,v132\nv_min_u32 v141,-2,v148\n" \
"v_min_u32 v142,-2,v164\nv_min_u32 v143,-2,v180\nbuffer_store_dwordx4 v[140:143],v253,s[52:55],s38 offen offset:0 sc1\nds_read_b64_tr_b16 v[208:209],v226 offset:9728\nds_read_b64_tr_b16 v[210:211],v226 offset:13824\nM v[60:63],a[76:79],v[212:215],v[60:63]\nds_read_b64_tr_b16 v[196:197],v227 offset:1536\nds_read_b64_tr_b16 v[198:199],v227 offset:5632\n" \
"M v[72:75],a[64:67],v[200:203],v[72:75]\nbuffer_load_dwordx4 a[152:155],v240,s[52:55],s65 offen offset:2048 sc1\nM v[72:75],a[72:75],v[204:207],v[72:75]\nds_read_b64_tr_b16 v[212:213],v227 offset:9728\nds_read_b64_tr_b16 v[214:215],v227 offset:13824\nM v[76:79],a[68:71],v[200:203],v[76:79]\nds_read_b64_tr_b16 v[200:201],v226 offset:32768\nds_read_b64_tr_b16 v[202:203],v226 offset:36864\n" \
"M v[76:79],a[76:79],v[204:207],v[76:79]\nds_read_b64_tr_b16 v[204:205],v226 offset:40960\ns_waitcnt lgkmcnt(9)\nM v[88:91],a[64:67],v[216:219],v[88:91]\nbuffer_load_dwordx4 a[156:159],v240,s[52:55],s65 offen offset:3072 sc1\nM v[88:91],a[72:75],v[220:223],v[88:91]\nds_read_b64_tr_b16 v[206:207],v226 offset:45056\nv_cvt_pk_bf16_f32 v149,v149,v157\n" \
"M v[92:95],a[68:71],v[216:219],v[92:95]\nds_read_b64_tr_b16 v[216:217],v227 offset:32768\nds_read_b64_tr_b16 v[218:219],v227 offset:36864\nM v[92:95],a[76:79],v[220:223],v[92:95]\nds_read_b64_tr_b16 v[220:221],v227 offset:40960\nds_read_b64_tr_b16 v[222:223],v227 offset:45056\nM v[104:107],a[64:67],v[192:195],v[104:107]\nv_cvt_pk_bf16_f32 v150,v150,v158\n" \
"s_waitcnt lgkmcnt(9)\nM v[104:107],a[72:75],v[208:211],v[104:107]\nv_cvt_pk_bf16_f32 v151,v151,v159\nv_cvt_pk_bf16_f32 v165,v165,v173\nM v[108:111],a[68:71],v[192:195],v[108:111]\nv_cvt_pk_bf16_f32 v181,v181,v189\nds_read_b64_tr_b16 v[192:193],v226 offset:33280\nM v[108:111],a[76:79],v[208:211],v[108:111]\n" \
"ds_read_b64_tr_b16 v[194:195],v226 offset:37376\nv_min_u32 v156,-2,v133\nM v[120:123],a[64:67],v[196:199],v[120:123]\nv_min_u32 v157,-2,v149\ns_waitcnt lgkmcnt(6)\nM v[120:123],a[72:75],v[212:215],v[120:123]\nv_min_u32 v158,-2,v165\nv_min_u32 v159,-2,v181\n" \
"M v[124:127],a[68:71],v[196:199],v[124:127]\nbuffer_store_dwordx4 v[156:159],v253,s[52:55],s38 offen offset:1024 sc1\nds_read_b64_tr_b16 v[208:209],v226 offset:41472\nds_read_b64_tr_b16 v[210:211],v226 offset:45568\nM v[124:127],a[76:79],v[212:215],v[124:127]\nds_read_b64_tr_b16 v[196:197],v227 offset:33280\ns_waitcnt vmcnt(24)\nM v[8:11],a[80:83],v[200:203],v[8:11]\n" \
"ds_read_b64_tr_b16 v[198:199],v227 offset:37376\ns_waitcnt vmcnt(19)\nM v[8:11],a[88:91],v[204:207],v[8:11]\nds_read_b64_tr_b16 v[212:213],v227 offset:41472\nds_read_b64_tr_b16 v[214:215],v227 offset:45568\nM v[12:15],a[84:87],v[200:203],v[12:15]\nds_read_b64_tr_b16 v[200:201],v226 offset:33792\nds_read_b64_tr_b16 v[202:203],v226 offset:37888\n" \
"M v[12:15],a[92:95],v[204:207],v[12:15]\nds_read_b64_tr_b16 v[204:205],v226 offset:41984\ns_waitcnt lgkmcnt(9)\nM v[24:27],a[80:83],v[216:219],v[24:27]\nds_read_b64_tr_b16 v[206:207],v226 offset:46080\nv_cvt_pk_bf16_f32 v166,v166,v174\nM v[24:27],a[88:91],v[220:223],v[24:27]\nv_cvt_pk_bf16_f32 v167,v167,v175\n" \
"v_cvt_pk_bf16_f32 v182,v182,v190\nM v[28:31],a[84:87],v[216:219],v[28:31]\nds_read_b64_tr_b16 v[216:217],v227 offset:33792\nds_read_b64_tr_b16 v[218:219],v227 offset:37888\nM v[28:31],a[92:95],v[220:223],v[28:31]\nds_read_b64_tr_b16 v[220:221],v227 offset:41984\nds_read_b64_tr_b16 v[222:223],v227 offset:46080\nM v[40:43],a[80:83],v[192:195],v[40:43]\n" \
"v_min_u32 v172,-2,v134\ns_waitcnt lgkmcnt(9)\nM v[40:43],a[88:91],v[208:211],v[40:43]\nv_min_u32 v173,-2,v150\nv_min_u32 v174,-2,v166\nM v[44:47],a[84:87],v[192:195],v[44:47]\nds_read_b64_tr_b16 v[192:193],v226 offset:34304\nds_read_b64_tr_b16 v[194:195],v226 offset:38400\n" \
"M v[44:47],a[92:95],v[208:211],v[44:47]\nv_min_u32 v175,-2,v182\nds_read_b64_tr_b16 v[208:209],v226 offset:42496\nM v[56:59],a[80:83],v[196:199],v[56:59]\nds_read_b64_tr_b16 v[210:211],v226 offset:46592\ns_waitcnt lgkmcnt(8)\nM v[56:59],a[88:91],v[212:215],v[56:59]\nv_cvt_pk_bf16_f32 v183,v183,v191\n" \
"v_min_u32 v188,-2,v135\nM v[60:63],a[84:87],v[196:199],v[60:63]\nbuffer_store_dwordx4 v[172:175],v253,s[52:55],s38 offen offset:2048 sc1\nM v[60:63],a[92:95],v[212:215],v[60:63]\nds_read_b64_tr_b16 v[196:197],v227 offset:34304\nds_read_b64_tr_b16 v[198:199],v227 offset:38400\nM v[72:75],a[80:83],v[200:203],v[72:75]\nds_read_b64_tr_b16 v[212:213],v227 offset:42496\n" \
"ds_read_b64_tr_b16 v[214:215],v227 offset:46592\nM v[72:75],a[88:91],v[204:207],v[72:75]\nv_min_u32 v189,-2,v151\nv_min_u32 v190,-2,v167\nM v[76:79],a[84:87],v[200:203],v[76:79]\nds_read_b64_tr_b16 v[200:201],v226 offset:16384\nds_read_b64_tr_b16 v[202:203],v226 offset:20480\nM v[76:79],a[92:95],v[204:207],v[76:79]\n" \
"ds_read_b64_tr_b16 v[204:205],v226 offset:24576\ns_waitcnt lgkmcnt(7)\nM v[88:91],a[80:83],v[216:219],v[88:91]\nds_read_b64_tr_b16 v[206:207],v226 offset:28672\nv_min_u32 v191,-2,v183\nM v[88:91],a[88:91],v[220:223],v[88:91]\nv_cmp_eq_u32 vcc,v228,v229\nv_and_b32 v228,1,v249\n" \
"M v[92:95],a[84:87],v[216:219],v[92:95]\nds_read_b64_tr_b16 v[216:217],v227 offset:16384\nds_read_b64_tr_b16 v[218:219],v227 offset:20480\nM v[92:95],a[92:95],v[220:223],v[92:95]\nds_read_b64_tr_b16 v[220:221],v227 offset:24576\nds_read_b64_tr_b16 v[222:223],v227 offset:28672\nM v[104:107],a[80:83],v[192:195],v[104:107]\nv_lshlrev_b32 v228,4,v228\n" \
"v_mov_b32 v229,0x3f80\nM v[104:107],a[88:91],v[208:211],v[104:107]\nv_lshlrev_b32 v228,v228,v229\nv_cndmask_b32 v228,0,v228,vcc\nM v[108:111],a[84:87],v[192:195],v[108:111]\nds_read_b64_tr_b16 v[192:193],v226 offset:16896\nds_read_b64_tr_b16 v[194:195],v226 offset:20992\nM v[108:111],a[92:95],v[208:211],v[108:111]\n" \
"ds_read_b64_tr_b16 v[208:209],v226 offset:25088\ns_waitcnt lgkmcnt(7)\nM v[120:123],a[80:83],v[196:199],v[120:123]\nds_read_b64_tr_b16 v[210:211],v226 offset:29184\nv_and_b32 v229,2,v249\nM v[120:123],a[88:91],v[212:215],v[120:123]\nv_cmp_eq_u32 vcc,0,v229\nv_and_b32 v244,15,v249\n" \
"M v[124:127],a[84:87],v[196:199],v[124:127]\nbuffer_store_dwordx4 v[188:191],v253,s[52:55],s38 offen offset:3072 sc1\nM v[124:127],a[92:95],v[212:215],v[124:127]\nds_read_b64_tr_b16 v[196:197],v227 offset:16896\nds_read_b64_tr_b16 v[198:199],v227 offset:20992\nM v[8:11],a[96:99],v[200:203],v[8:11]\nds_read_b64_tr_b16 v[212:213],v227 offset:25088\ns_waitcnt vmcnt(16)\n" \
"M v[8:11],a[104:107],v[204:207],v[8:11]\nds_read_b64_tr_b16 v[214:215],v227 offset:29184\nv_cndmask_b32 v229,0,v228,vcc\nM v[12:15],a[100:103],v[200:203],v[12:15]\nds_read_b64_tr_b16 v[200:201],v226 offset:17408\nds_read_b64_tr_b16 v[202:203],v226 offset:21504\nM v[12:15],a[108:111],v[204:207],v[12:15]\nds_read_b64_tr_b16 v[204:205],v226 offset:25600\n" \
"s_waitcnt lgkmcnt(7)\nM v[24:27],a[96:99],v[216:219],v[24:27]\nds_read_b64_tr_b16 v[206:207],v226 offset:29696\nv_and_b32 v245,0x300,v240\nM v[24:27],a[104:107],v[220:223],v[24:27]\nv_lshlrev_b32 v246,11,v249\nv_cmp_lt_u32 vcc,7,v249\nM v[28:31],a[100:103],v[216:219],v[28:31]\n" \
"ds_read_b64_tr_b16 v[216:217],v227 offset:17408\nds_read_b64_tr_b16 v[218:219],v227 offset:21504\nM v[28:31],a[108:111],v[220:223],v[28:31]\nds_read_b64_tr_b16 v[220:221],v227 offset:25600\nds_read_b64_tr_b16 v[222:223],v227 offset:29696\nM v[40:43],a[96:99],v[192:195],v[40:43]\nv_mov_b32 v230,0x40000000\nv_lshl_or_b32 v244,v244,1,v245\n" \
"M v[40:43],a[104:107],v[208:211],v[40:43]\ns_add_u32 s70,s64,114688\nv_cndmask_b32 v246,v246,v230,vcc\nM v[44:47],a[100:103],v[192:195],v[44:47]\nds_read_b64_tr_b16 v[192:193],v226 offset:17920\nds_read_b64_tr_b16 v[194:195],v226 offset:22016\nM v[44:47],a[108:111],v[208:211],v[44:47]\nds_read_b64_tr_b16 v[208:209],v226 offset:26112\n" \
"s_waitcnt lgkmcnt(7)\nM v[56:59],a[96:99],v[196:199],v[56:59]\nds_read_b64_tr_b16 v[210:211],v226 offset:30208\nv_add_u32 v244,s70,v244\nM v[56:59],a[104:107],v[212:215],v[56:59]\nv_add_u32 v245,s70,v240\ns_lshl_b32 s80,s60,11\nM v[60:63],a[100:103],v[196:199],v[60:63]\n" \
"ds_read_b64_tr_b16 v[196:197],v227 offset:17920\nds_read_b64_tr_b16 v[198:199],v227 offset:22016\nM v[60:63],a[108:111],v[212:215],v[60:63]\nds_read_b64_tr_b16 v[212:213],v227 offset:26112\nds_read_b64_tr_b16 v[214:215],v227 offset:30208\nM v[72:75],a[96:99],v[200:203],v[72:75]\nv_and_b32 v230,3,v249\nv_lshrrev_b32 v231,2,v249\n" \
"M v[72:75],a[104:107],v[204:207],v[72:75]\nv_accvgpr_read_b32 v32,a176\nv_accvgpr_read_b32 v33,a177\nM v[76:79],a[100:103],v[200:203],v[76:79]\nds_read_b64_tr_b16 v[200:201],v226 offset:49152\nds_read_b64_tr_b16 v[202:203],v226 offset:53248\nM v[76:79],a[108:111],v[204:207],v[76:79]\nds_read_b64_tr_b16 v[204:205],v226 offset:57344\n" \
"s_waitcnt lgkmcnt(7)\nM v[88:91],a[96:99],v[216:219],v[88:91]\nds_read_b64_tr_b16 v[206:207],v226 offset:61440\nv_accvgpr_read_b32 v34,a178\nM v[88:91],a[104:107],v[220:223],v[88:91]\nv_accvgpr_read_b32 v35,a179\nv_accvgpr_read_b32 v36,a180\nM v[92:95],a[100:103],v[216:219],v[92:95]\n" \
"ds_read_b64_tr_b16 v[216:217],v227 offset:49152\nds_read_b64_tr_b16 v[218:219],v227 offset:53248\nM v[92:95],a[108:111],v[220:223],v[92:95]\nds_read_b64_tr_b16 v[220:221],v227 offset:57344\nds_read_b64_tr_b16 v[222:223],v227 offset:61440\nM v[104:107],a[96:99],v[192:195],v[104:107]\nv_accvgpr_read_b32 v37,a181\nv_accvgpr_read_b32 v38,a182\n" \
"M v[104:107],a[104:107],v[208:211],v[104:107]\nv_accvgpr_read_b32 v39,a183\ns_lshl3_add_u32 s37,s6,s75\nM v[108:111],a[100:103],v[192:195],v[108:111]\nds_read_b64_tr_b16 v[192:193],v226 offset:49664\nds_read_b64_tr_b16 v[194:195],v226 offset:53760\nM v[108:111],a[108:111],v[208:211],v[108:111]\nds_read_b64_tr_b16 v[208:209],v226 offset:57856\n" \
"s_waitcnt lgkmcnt(7)\nM v[120:123],a[96:99],v[196:199],v[120:123]\nds_read_b64_tr_b16 v[210:211],v226 offset:61952\ns_add_u32 s80,s80,557056\nM v[120:123],a[104:107],v[212:215],v[120:123]\nv_lshl_add_u32 v231,v230,5,v231\ns_lshl_b32 s70,s7,8\nM v[124:127],a[100:103],v[196:199],v[124:127]\n" \
"ds_read_b64_tr_b16 v[196:197],v227 offset:49664\nds_read_b64_tr_b16 v[198:199],v227 offset:53760\nM v[124:127],a[108:111],v[212:215],v[124:127]\nds_read_b64_tr_b16 v[212:213],v227 offset:57856\nds_read_b64_tr_b16 v[214:215],v227 offset:61952\nM v[8:11],a[112:115],v[200:203],v[8:11]\nv_add_f32 v32,1.0,v32\ns_waitcnt vmcnt(11)\n" \
"M v[8:11],a[120:123],v[204:207],v[8:11]\nv_add_f32 v33,1.0,v33\nv_add_f32 v34,1.0,v34\nM v[12:15],a[116:119],v[200:203],v[12:15]\nds_read_b64_tr_b16 v[200:201],v226 offset:50176\nds_read_b64_tr_b16 v[202:203],v226 offset:54272\nM v[12:15],a[124:127],v[204:207],v[12:15]\nds_read_b64_tr_b16 v[204:205],v226 offset:58368\n" \
"s_waitcnt lgkmcnt(7)\nM v[24:27],a[112:115],v[216:219],v[24:27]\nds_read_b64_tr_b16 v[206:207],v226 offset:62464\nv_add_f32 v35,1.0,v35\nM v[24:27],a[120:123],v[220:223],v[24:27]\nv_add_f32 v36,1.0,v36\nv_add_f32 v37,1.0,v37\nM v[28:31],a[116:119],v[216:219],v[28:31]\n" \
"ds_read_b64_tr_b16 v[216:217],v227 offset:50176\nds_read_b64_tr_b16 v[218:219],v227 offset:54272\nM v[28:31],a[124:127],v[220:223],v[28:31]\nds_read_b64_tr_b16 v[220:221],v227 offset:58368\nds_read_b64_tr_b16 v[222:223],v227 offset:62464\nM v[40:43],a[112:115],v[192:195],v[40:43]\nv_add_f32 v38,1.0,v38\nv_add_f32 v39,1.0,v39\n" \
"M v[40:43],a[120:123],v[208:211],v[40:43]\nv_add_f32 v116,1.0,v116\nv_add_f32 v117,1.0,v117\nM v[44:47],a[116:119],v[192:195],v[44:47]\nds_read_b64_tr_b16 v[192:193],v226 offset:50688\nds_read_b64_tr_b16 v[194:195],v226 offset:54784\nM v[44:47],a[124:127],v[208:211],v[44:47]\nds_read_b64_tr_b16 v[208:209],v226 offset:58880\n" \
"s_waitcnt lgkmcnt(7)\nM v[56:59],a[112:115],v[196:199],v[56:59]\nds_read_b64_tr_b16 v[210:211],v226 offset:62976\nv_add_u32 v226,65536,v242\nM v[56:59],a[120:123],v[212:215],v[56:59]\ns_add_u32 s37,s37,557056\nv_lshlrev_b32 v251,5,v249\nM v[60:63],a[116:119],v[196:199],v[60:63]\n" \
"ds_read_b64_tr_b16 v[196:197],v227 offset:50688\nds_read_b64_tr_b16 v[198:199],v227 offset:54784\nM v[60:63],a[124:127],v[212:215],v[60:63]\nds_read_b64_tr_b16 v[212:213],v227 offset:58880\nds_read_b64_tr_b16 v[214:215],v227 offset:62976\nM v[72:75],a[112:115],v[200:203],v[72:75]\nv_xor_b32 v227,32,v226\ns_or_b32 s80,s80,s75\n" \
"M v[72:75],a[120:123],v[204:207],v[72:75]\nv_lshl_add_u32 v248,v231,1,s70\nv_accvgpr_read_b32 v0,a160\nM v[76:79],a[116:119],v[200:203],v[76:79]\nds_read_b64_tr_b16 v[200:201],v226 offset:0\nds_read_b64_tr_b16 v[202:203],v226 offset:4096\nM v[76:79],a[124:127],v[204:207],v[76:79]\nds_read_b64_tr_b16 v[204:205],v226 offset:8192\n" \
"s_waitcnt lgkmcnt(7)\nM v[88:91],a[112:115],v[216:219],v[88:91]\nds_read_b64_tr_b16 v[206:207],v226 offset:12288\nv_accvgpr_read_b32 v1,a161\nM v[88:91],a[120:123],v[220:223],v[88:91]\nv_accvgpr_read_b32 v2,a162\nv_accvgpr_read_b32 v3,a163\nM v[92:95],a[116:119],v[216:219],v[92:95]\n" \
"ds_read_b64_tr_b16 v[216:217],v227 offset:0\nds_read_b64_tr_b16 v[218:219],v227 offset:4096\nM v[92:95],a[124:127],v[220:223],v[92:95]\nds_read_b64_tr_b16 v[220:221],v227 offset:8192\nds_read_b64_tr_b16 v[222:223],v227 offset:12288\nM v[104:107],a[112:115],v[192:195],v[104:107]\nv_accvgpr_read_b32 v4,a164\nv_accvgpr_read_b32 v5,a165\n" \
"M v[104:107],a[120:123],v[208:211],v[104:107]\nv_accvgpr_read_b32 v6,a166\nv_accvgpr_read_b32 v7,a167\nM v[108:111],a[116:119],v[192:195],v[108:111]\nds_read_b64_tr_b16 v[192:193],v226 offset:512\nds_read_b64_tr_b16 v[194:195],v226 offset:4608\nM v[108:111],a[124:127],v[208:211],v[108:111]\nds_read_b64_tr_b16 v[208:209],v226 offset:8704\n" \
"s_waitcnt lgkmcnt(7)\nM v[120:123],a[112:115],v[196:199],v[120:123]\nds_read_b64_tr_b16 v[210:211],v226 offset:12800\nv_accvgpr_read_b32 v16,a168\nM v[120:123],a[120:123],v[212:215],v[120:123]\nv_accvgpr_read_b32 v17,a169\nv_accvgpr_read_b32 v18,a170\nM v[124:127],a[116:119],v[196:199],v[124:127]\n" \
"ds_read_b64_tr_b16 v[196:197],v227 offset:512\nds_read_b64_tr_b16 v[198:199],v227 offset:4608\nM v[124:127],a[124:127],v[212:215],v[124:127]\nds_read_b64_tr_b16 v[212:213],v227 offset:8704\nds_read_b64_tr_b16 v[214:215],v227 offset:12800\nM v[8:11],a[128:131],v[200:203],v[8:11]\nv_accvgpr_read_b32 v19,a171\ns_waitcnt vmcnt(6)\n" \
"M v[8:11],a[136:139],v[204:207],v[8:11]\nv_accvgpr_read_b32 v20,a172\nv_accvgpr_read_b32 v21,a173\nM v[12:15],a[132:135],v[200:203],v[12:15]\nds_read_b64_tr_b16 v[200:201],v226 offset:1024\nds_read_b64_tr_b16 v[202:203],v226 offset:5120\nM v[12:15],a[140:143],v[204:207],v[12:15]\nds_read_b64_tr_b16 v[204:205],v226 offset:9216\n" \
"s_waitcnt lgkmcnt(7)\nM v[24:27],a[128:131],v[216:219],v[24:27]\nds_read_b64_tr_b16 v[206:207],v226 offset:13312\nv_accvgpr_read_b32 v22,a174\nM v[24:27],a[136:139],v[220:223],v[24:27]\nv_accvgpr_read_b32 v23,a175\nv_accvgpr_read_b32 v48,a184\nM v[28:31],a[132:135],v[216:219],v[28:31]\n" \
"ds_read_b64_tr_b16 v[216:217],v227 offset:1024\nds_read_b64_tr_b16 v[218:219],v227 offset:5120\nM v[28:31],a[140:143],v[220:223],v[28:31]\nds_read_b64_tr_b16 v[220:221],v227 offset:9216\nds_read_b64_tr_b16 v[222:223],v227 offset:13312\nM v[40:43],a[128:131],v[192:195],v[40:43]\nv_accvgpr_read_b32 v49,a185\nv_accvgpr_read_b32 v50,a186\n" \
"M v[40:43],a[136:139],v[208:211],v[40:43]\nv_accvgpr_read_b32 v51,a187\nv_accvgpr_read_b32 v52,a188\nM v[44:47],a[132:135],v[192:195],v[44:47]\nds_read_b64_tr_b16 v[192:193],v226 offset:1536\nds_read_b64_tr_b16 v[194:195],v226 offset:5632\nM v[44:47],a[140:143],v[208:211],v[44:47]\nds_read_b64_tr_b16 v[208:209],v226 offset:9728\n" \
"s_waitcnt lgkmcnt(7)\nM v[56:59],a[128:131],v[196:199],v[56:59]\nds_read_b64_tr_b16 v[210:211],v226 offset:13824\nv_accvgpr_read_b32 v53,a189\nM v[56:59],a[136:139],v[212:215],v[56:59]\nv_accvgpr_read_b32 v54,a190\nv_accvgpr_read_b32 v55,a191\nM v[60:63],a[132:135],v[196:199],v[60:63]\n" \
"ds_read_b64_tr_b16 v[196:197],v227 offset:1536\nds_read_b64_tr_b16 v[198:199],v227 offset:5632\nM v[60:63],a[140:143],v[212:215],v[60:63]\nds_read_b64_tr_b16 v[212:213],v227 offset:9728\nds_read_b64_tr_b16 v[214:215],v227 offset:13824\nM v[72:75],a[128:131],v[200:203],v[72:75]\nv_lshlrev_b32 v64,16,v64\nv_lshlrev_b32 v65,16,v65\n" \
"M v[72:75],a[136:139],v[204:207],v[72:75]\nv_lshlrev_b32 v66,16,v66\nv_lshlrev_b32 v67,16,v67\nM v[76:79],a[132:135],v[200:203],v[76:79]\nds_read_b64_tr_b16 v[200:201],v226 offset:32768\nds_read_b64_tr_b16 v[202:203],v226 offset:36864\nM v[76:79],a[140:143],v[204:207],v[76:79]\nds_read_b64_tr_b16 v[204:205],v226 offset:40960\n" \
"s_waitcnt lgkmcnt(7)\nM v[88:91],a[128:131],v[216:219],v[88:91]\nds_read_b64_tr_b16 v[206:207],v226 offset:45056\nv_lshlrev_b32 v68,16,v68\nM v[88:91],a[136:139],v[220:223],v[88:91]\nv_lshlrev_b32 v69,16,v69\nv_lshlrev_b32 v70,16,v70\nM v[92:95],a[132:135],v[216:219],v[92:95]\n" \
"ds_read_b64_tr_b16 v[216:217],v227 offset:32768\nds_read_b64_tr_b16 v[218:219],v227 offset:36864\nM v[92:95],a[140:143],v[220:223],v[92:95]\nds_read_b64_tr_b16 v[220:221],v227 offset:40960\nds_read_b64_tr_b16 v[222:223],v227 offset:45056\nM v[104:107],a[128:131],v[192:195],v[104:107]\nv_lshlrev_b32 v71,16,v71\nv_lshlrev_b32 v80,16,v80\n" \
"M v[104:107],a[136:139],v[208:211],v[104:107]\nv_lshlrev_b32 v81,16,v81\nv_lshlrev_b32 v82,16,v82\nM v[108:111],a[132:135],v[192:195],v[108:111]\nds_read_b64_tr_b16 v[192:193],v226 offset:33280\nds_read_b64_tr_b16 v[194:195],v226 offset:37376\nM v[108:111],a[140:143],v[208:211],v[108:111]\nds_read_b64_tr_b16 v[208:209],v226 offset:41472\n" \
"s_waitcnt lgkmcnt(7)\nM v[120:123],a[128:131],v[196:199],v[120:123]\nds_read_b64_tr_b16 v[210:211],v226 offset:45568\nv_lshlrev_b32 v83,16,v83\nM v[120:123],a[136:139],v[212:215],v[120:123]\nv_lshlrev_b32 v84,16,v84\nv_lshlrev_b32 v85,16,v85\nM v[124:127],a[132:135],v[196:199],v[124:127]\n" \
"ds_read_b64_tr_b16 v[196:197],v227 offset:33280\nds_read_b64_tr_b16 v[198:199],v227 offset:37376\nM v[124:127],a[140:143],v[212:215],v[124:127]\nds_read_b64_tr_b16 v[212:213],v227 offset:41472\nds_read_b64_tr_b16 v[214:215],v227 offset:45568\nM v[8:11],a[144:147],v[200:203],v[8:11]\nv_lshlrev_b32 v86,16,v86\ns_waitcnt vmcnt(3)\n" \
"M v[8:11],a[152:155],v[204:207],v[8:11]\nv_lshlrev_b32 v87,16,v87\nv_lshlrev_b32 v96,16,v96\nM v[12:15],a[148:151],v[200:203],v[12:15]\nds_read_b64_tr_b16 v[200:201],v226 offset:33792\nds_read_b64_tr_b16 v[202:203],v226 offset:37888\nM v[12:15],a[156:159],v[204:207],v[12:15]\nds_read_b64_tr_b16 v[204:205],v226 offset:41984\n" \
"s_waitcnt lgkmcnt(7)\nM v[24:27],a[144:147],v[216:219],v[24:27]\nds_read_b64_tr_b16 v[206:207],v226 offset:46080\nv_lshlrev_b32 v97,16,v97\nM v[24:27],a[152:155],v[220:223],v[24:27]\nv_lshlrev_b32 v98,16,v98\nv_lshlrev_b32 v99,16,v99\nM v[28:31],a[148:151],v[216:219],v[28:31]\n" \
"v_lshlrev_b32 v100,16,v100\nv_lshlrev_b32 v101,16,v101\nM v[28:31],a[156:159],v[220:223],v[28:31]\nv_lshlrev_b32 v102,16,v102\nv_lshlrev_b32 v103,16,v103\nM v[40:43],a[144:147],v[192:195],v[40:43]\nv_cvt_pk_bf16_f32 v32,0,v32\nv_cvt_pk_bf16_f32 v33,0,v33\n" \
"v_cvt_pk_bf16_f32 v34,0,v34\nv_cvt_pk_bf16_f32 v35,0,v35\nv_cvt_pk_bf16_f32 v36,0,v36\nv_cvt_pk_bf16_f32 v37,0,v37\nv_cvt_pk_bf16_f32 v38,0,v38\nv_cvt_pk_bf16_f32 v39,0,v39\nv_cvt_pk_bf16_f32 v116,0,v116\nv_cvt_pk_bf16_f32 v117,0,v117\n" \
"M v[40:43],a[152:155],v[208:211],v[40:43]\nds_read_b64_tr_b16 v[216:217],v227 offset:33792\nds_read_b64_tr_b16 v[218:219],v227 offset:37888\nM v[44:47],a[148:151],v[192:195],v[44:47]\nds_read_b64_tr_b16 v[220:221],v227 offset:41984\nds_read_b64_tr_b16 v[222:223],v227 offset:46080\nM v[44:47],a[156:159],v[208:211],v[44:47]\nds_read_b64_tr_b16 v[192:193],v226 offset:34304\n" \
"s_waitcnt lgkmcnt(5)\nM v[56:59],a[144:147],v[196:199],v[56:59]\nds_read_b64_tr_b16 v[194:195],v226 offset:38400\nds_read_b64_tr_b16 v[208:209],v226 offset:42496\nM v[56:59],a[152:155],v[212:215],v[56:59]\nds_read_b64_tr_b16 v[210:211],v226 offset:46592\nv_cvt_pk_bf16_f32 v8,v8,v24\nM v[60:63],a[148:151],v[196:199],v[60:63]\n" \
"ds_read_b64_tr_b16 v[196:197],v227 offset:34304\nds_read_b64_tr_b16 v[198:199],v227 offset:38400\nM v[60:63],a[156:159],v[212:215],v[60:63]\nds_read_b64_tr_b16 v[212:213],v227 offset:42496\nds_read_b64_tr_b16 v[214:215],v227 offset:46592\nM v[72:75],a[144:147],v[200:203],v[72:75]\nv_cvt_pk_bf16_f32 v9,v9,v25\nv_cvt_pk_bf16_f32 v10,v10,v26\n" \
"M v[72:75],a[152:155],v[204:207],v[72:75]\nv_cvt_pk_bf16_f32 v11,v11,v27\nv_cvt_pk_bf16_f32 v40,v40,v56\nM v[76:79],a[148:151],v[200:203],v[76:79]\nv_cvt_pk_bf16_f32 v41,v41,v57\nv_cvt_pk_bf16_f32 v42,v42,v58\nM v[76:79],a[156:159],v[204:207],v[76:79]\nv_cvt_pk_bf16_f32 v43,v43,v59\n" \
"s_waitcnt lgkmcnt(6)\nM v[88:91],a[144:147],v[216:219],v[88:91]\nv_cvt_pk_bf16_f32 v12,v12,v28\nv_cvt_pk_bf16_f32 v13,v13,v29\nM v[88:91],a[152:155],v[220:223],v[88:91]\nv_cvt_pk_bf16_f32 v14,v14,v30\nv_cvt_pk_bf16_f32 v15,v15,v31\nM v[92:95],a[148:151],v[216:219],v[92:95]\n" \
"v_cvt_pk_bf16_f32 v44,v44,v60\nv_cvt_pk_bf16_f32 v45,v45,v61\nM v[92:95],a[156:159],v[220:223],v[92:95]\nv_cvt_pk_bf16_f32 v46,v46,v62\nv_cvt_pk_bf16_f32 v47,v47,v63\nM v[104:107],a[144:147],v[192:195],v[104:107]\nv_cvt_pk_bf16_f32 v72,v72,v88\ns_waitcnt lgkmcnt(0)\n" \
"M v[104:107],a[152:155],v[208:211],v[104:107]\nv_cvt_pk_bf16_f32 v73,v73,v89\nv_cvt_pk_bf16_f32 v74,v74,v90\nM v[108:111],a[148:151],v[192:195],v[108:111]\nv_cvt_pk_bf16_f32 v75,v75,v91\nv_cvt_pk_bf16_f32 v76,v76,v92\nM v[108:111],a[156:159],v[208:211],v[108:111]\nv_cvt_pk_bf16_f32 v77,v77,v93\n" \
"v_cvt_pk_bf16_f32 v78,v78,v94\nM v[120:123],a[144:147],v[196:199],v[120:123]\nv_cvt_pk_bf16_f32 v79,v79,v95\nv_xor_b32 v228,v228,v229\nM v[120:123],a[152:155],v[212:215],v[120:123]\ns_add_u32 s38,s78,16384\nv_min_u32 v24,-2,v8\nM v[124:127],a[148:151],v[196:199],v[124:127]\n" \
"v_min_u32 v25,-2,v40\nv_min_u32 v26,-2,v72\nM v[124:127],a[156:159],v[212:215],v[124:127]\nv_min_u32 v56,-2,v9\nv_min_u32 v57,-2,v41\nv_cvt_pk_bf16_f32 v104,v104,v120\nv_cvt_pk_bf16_f32 v105,v105,v121\nv_cvt_pk_bf16_f32 v106,v106,v122\n" \
"v_cvt_pk_bf16_f32 v107,v107,v123\nv_min_u32 v27,-2,v104\nv_min_u32 v58,-2,v73\nv_cvt_pk_bf16_f32 v108,v108,v124\nv_cvt_pk_bf16_f32 v109,v109,v125\nv_cvt_pk_bf16_f32 v110,v110,v126\nv_cvt_pk_bf16_f32 v111,v111,v127\nv_min_u32 v59,-2,v105\n" \
"v_min_u32 v88,-2,v10\nv_min_u32 v89,-2,v42\nv_min_u32 v90,-2,v74\nv_min_u32 v91,-2,v106\nv_min_u32 v120,-2,v11\nv_min_u32 v121,-2,v43\nv_min_u32 v122,-2,v75\nv_min_u32 v123,-2,v107\n" \
"v_min_u32 v28,-2,v12\nv_min_u32 v29,-2,v44\nv_min_u32 v30,-2,v76\nv_min_u32 v31,-2,v108\nv_min_u32 v60,-2,v13\nv_min_u32 v61,-2,v45\nv_min_u32 v62,-2,v77\nv_min_u32 v63,-2,v109\n" \
"v_min_u32 v92,-2,v14\nv_min_u32 v93,-2,v46\nv_min_u32 v94,-2,v78\nv_min_u32 v95,-2,v110\nv_min_u32 v124,-2,v15\nv_min_u32 v125,-2,v47\nv_min_u32 v126,-2,v79\nv_min_u32 v127,-2,v111\n" \
"v_accvgpr_write_b32 a192,v229\nv_accvgpr_write_b32 a193,v228\nv_accvgpr_write_b32 a198,v229\nv_accvgpr_write_b32 a199,v228\nbuffer_store_dwordx4 v[24:27],v253,s[52:55],s78 offen offset:0 sc1\nbuffer_store_dwordx4 v[56:59],v253,s[52:55],s78 offen offset:1024 sc1\nbuffer_store_dwordx4 v[88:91],v253,s[52:55],s78 offen offset:2048 sc1\nbuffer_store_dwordx4 v[120:123],v253,s[52:55],s78 offen offset:3072 sc1\n" \
"buffer_store_dwordx4 v[28:31],v253,s[52:55],s38 offen offset:0 sc1\nbuffer_store_dwordx4 v[60:63],v253,s[52:55],s38 offen offset:1024 sc1\nbuffer_store_dwordx4 v[92:95],v253,s[52:55],s38 offen offset:2048 sc1\nbuffer_store_dwordx4 v[124:127],v253,s[52:55],s38 offen offset:3072 sc1\n.Ltail:\ns_add_u32 s72,s95,0\nbuffer_load_dwordx4 v[160:163],v240,s[52:55],s72 offen sc1\ns_add_u32 s72,s95,2097152\n" \
"buffer_load_dwordx4 v[164:167],v240,s[52:55],s72 offen sc1\ns_add_u32 s72,s95,4194304\nbuffer_load_dwordx4 v[168:171],v240,s[52:55],s72 offen sc1\ns_add_u32 s72,s95,6291456\nbuffer_load_dwordx4 v[172:175],v240,s[52:55],s72 offen sc1\ns_add_u32 s72,s95,8388608\nbuffer_load_dwordx4 v[176:179],v240,s[52:55],s72 offen sc1\ns_add_u32 s72,s95,10485760\n" \
"buffer_load_dwordx4 v[180:183],v240,s[52:55],s72 offen sc1\ns_add_u32 s72,s95,12582912\nbuffer_load_dwordx4 v[184:187],v240,s[52:55],s72 offen sc1\ns_add_u32 s72,s95,14680064\nbuffer_load_dwordx4 v[188:191],v240,s[52:55],s72 offen sc1\n.Ltail2:\ns_add_u32 s72,s74,0\nbuffer_load_dwordx4 v[128:131],v240,s[52:55],s72 offen sc1\n" \
"s_add_u32 s72,s74,2097152\nbuffer_load_dwordx4 v[132:135],v240,s[52:55],s72 offen sc1\ns_add_u32 s72,s74,4194304\nbuffer_load_dwordx4 v[136:139],v240,s[52:55],s72 offen sc1\ns_add_u32 s72,s74,6291456\nbuffer_load_dwordx4 v[140:143],v240,s[52:55],s72 offen sc1\ns_add_u32 s72,s74,8388608\nbuffer_load_dwordx4 v[144:147],v240,s[52:55],s72 offen sc1\n" \
"s_add_u32 s72,s74,10485760\nbuffer_load_dwordx4 v[148:151],v240,s[52:55],s72 offen sc1\ns_add_u32 s72,s74,12582912\nbuffer_load_dwordx4 v[152:155],v240,s[52:55],s72 offen sc1\ns_add_u32 s72,s74,14680064\nbuffer_load_dwordx4 v[156:159],v240,s[52:55],s72 offen sc1\ns_cmp_eq_u32 s3,0\ns_cbranch_scc1 .Lnp\n" \
"s_mov_b32 s70,0x05040100\ns_mov_b32 s71,0x07060302\nv_accvgpr_read_b32 v228,a212\nv_accvgpr_read_b32 v229,a213\nv_accvgpr_read_b32 v230,a214\nv_accvgpr_read_b32 v231,a215\nv_perm_b32 v192,v230,v228,s70\nv_perm_b32 v193,v230,v228,s71\n" \
"v_perm_b32 v194,v231,v229,s70\nv_perm_b32 v195,v231,v229,s71\nv_accvgpr_read_b32 v228,a216\nv_accvgpr_read_b32 v229,a217\nv_accvgpr_read_b32 v230,a218\nv_accvgpr_read_b32 v231,a219\nv_perm_b32 v196,v230,v228,s70\nv_perm_b32 v197,v230,v228,s71\n" \
"v_perm_b32 v198,v231,v229,s70\nv_perm_b32 v199,v231,v229,s71\nv_accvgpr_read_b32 v228,a220\nv_accvgpr_read_b32 v229,a221\nv_accvgpr_read_b32 v230,a222\nv_accvgpr_read_b32 v231,a223\nv_perm_b32 v200,v230,v228,s70\nv_perm_b32 v201,v230,v228,s71\n" \
"v_perm_b32 v202,v231,v229,s70\nv_perm_b32 v203,v231,v229,s71\nv_accvgpr_read_b32 v228,a224\nv_accvgpr_read_b32 v229,a225\nv_accvgpr_read_b32 v230,a226\nv_accvgpr_read_b32 v231,a227\nv_perm_b32 v204,v230,v228,s70\nv_perm_b32 v205,v230,v228,s71\n" \
"v_perm_b32 v206,v231,v229,s70\nv_perm_b32 v207,v231,v229,s71\nv_accvgpr_read_b32 v228,a228\nv_accvgpr_read_b32 v229,a229\nv_accvgpr_read_b32 v230,a230\nv_accvgpr_read_b32 v231,a231\nv_perm_b32 v208,v230,v228,s70\nv_perm_b32 v209,v230,v228,s71\n" \
"v_perm_b32 v210,v231,v229,s70\nv_perm_b32 v211,v231,v229,s71\nv_accvgpr_read_b32 v228,a232\nv_accvgpr_read_b32 v229,a233\nv_accvgpr_read_b32 v230,a234\nv_accvgpr_read_b32 v231,a235\nv_perm_b32 v212,v230,v228,s70\nv_perm_b32 v213,v230,v228,s71\n" \
"v_perm_b32 v214,v231,v229,s70\nv_perm_b32 v215,v231,v229,s71\nv_accvgpr_read_b32 v228,a236\nv_accvgpr_read_b32 v229,a237\nv_accvgpr_read_b32 v230,a238\nv_accvgpr_read_b32 v231,a239\nv_perm_b32 v216,v230,v228,s70\nv_perm_b32 v217,v230,v228,s71\n" \
"v_perm_b32 v218,v231,v229,s70\nv_perm_b32 v219,v231,v229,s71\nv_accvgpr_read_b32 v228,a240\nv_accvgpr_read_b32 v229,a241\nv_accvgpr_read_b32 v230,a242\nv_accvgpr_read_b32 v231,a243\nv_perm_b32 v220,v230,v228,s70\nv_perm_b32 v221,v230,v228,s71\n" \
"v_perm_b32 v222,v231,v229,s70\nv_perm_b32 v223,v231,v229,s71\nv_and_b32 v228,3,v249\nv_lshrrev_b32 v229,4,v249\nv_and_b32 v230,1,v229\nv_lshrrev_b32 v229,1,v229\nv_lshlrev_b32 v230,5,v230\nv_lshl_or_b32 v230,v228,6,v230\n" \
"v_lshl_or_b32 v230,v229,8,v230\ns_lshl2_add_u32 s70,s7,84032\nv_add_u32 v252,s70,v230\nv_and_b32 v230,0xf0,v240\nv_add_u32 v253,84032,v230\n.Lnp:\n.Lrow1:\ns_waitcnt vmcnt(15)\n" \
"M v[72:75],a[192:195],v[160:163],0\nM v[76:79],a[196:199],v[160:163],0\ns_waitcnt vmcnt(14)\nM v[72:75],a[192:195],v[164:167],v[72:75]\nM v[76:79],a[196:199],v[164:167],v[76:79]\ns_waitcnt vmcnt(13)\nM v[72:75],a[192:195],v[168:171],v[72:75]\nM v[76:79],a[196:199],v[168:171],v[76:79]\n" \
"s_waitcnt vmcnt(12)\nM v[72:75],a[192:195],v[172:175],v[72:75]\nM v[76:79],a[196:199],v[172:175],v[76:79]\ns_waitcnt vmcnt(11)\nM v[72:75],a[192:195],v[176:179],v[72:75]\nM v[76:79],a[196:199],v[176:179],v[76:79]\ns_waitcnt vmcnt(10)\nM v[72:75],a[192:195],v[180:183],v[72:75]\n" \
"M v[76:79],a[196:199],v[180:183],v[76:79]\ns_waitcnt vmcnt(9)\nM v[72:75],a[192:195],v[184:187],v[72:75]\nM v[76:79],a[196:199],v[184:187],v[76:79]\ns_waitcnt vmcnt(8)\nM v[72:75],a[192:195],v[188:191],v[72:75]\nM v[76:79],a[196:199],v[188:191],v[76:79]\ns_sub_u32 s38,56622112,s95\n" \
"s_lshr_b32 s38,s38,5\ns_nop 4\nv_cvt_pk_bf16_f32 v72,0,v72\nv_cvt_pk_bf16_f32 v73,0,v73\nv_cvt_pk_bf16_f32 v74,0,v74\nv_cvt_pk_bf16_f32 v75,0,v75\nv_cvt_pk_bf16_f32 v76,0,v76\nv_cvt_pk_bf16_f32 v77,0,v77\n" \
"v_cvt_pk_bf16_f32 v78,0,v78\nv_cvt_pk_bf16_f32 v79,0,v79\nv_pk_mul_f32 v[72:73],v[72:73],v[0:1]\nv_pk_mul_f32 v[74:75],v[74:75],v[2:3]\nv_pk_mul_f32 v[76:77],v[76:77],v[4:5]\nv_pk_mul_f32 v[78:79],v[78:79],v[6:7]\nv_cvt_pk_bf16_f32 v72,0,v72\nv_cvt_pk_bf16_f32 v73,0,v73\n" \
"v_cvt_pk_bf16_f32 v74,0,v74\nv_cvt_pk_bf16_f32 v75,0,v75\nv_cvt_pk_bf16_f32 v76,0,v76\nv_cvt_pk_bf16_f32 v77,0,v77\nv_cvt_pk_bf16_f32 v78,0,v78\nv_cvt_pk_bf16_f32 v79,0,v79\nv_pk_add_f32 v[72:73],v[72:73],v[64:65]\nv_pk_add_f32 v[74:75],v[74:75],v[66:67]\n" \
"v_pk_add_f32 v[76:77],v[76:77],v[68:69]\nv_pk_add_f32 v[78:79],v[78:79],v[70:71]\nv_cvt_pk_bf16_f32 v72,0,v72\nv_cvt_pk_bf16_f32 v73,0,v73\nv_cvt_pk_bf16_f32 v74,0,v74\nv_cvt_pk_bf16_f32 v75,0,v75\nv_cvt_pk_bf16_f32 v76,0,v76\nv_cvt_pk_bf16_f32 v77,0,v77\n" \
"v_cvt_pk_bf16_f32 v78,0,v78\nv_cvt_pk_bf16_f32 v79,0,v79\nv_readfirstlane_b32 s70,v72\ns_nop 1\nv_subrev_f32 v104,s70,v72\nv_subrev_f32 v105,s70,v73\nv_subrev_f32 v106,s70,v74\nv_subrev_f32 v107,s70,v75\n" \
"v_subrev_f32 v108,s70,v76\nv_subrev_f32 v109,s70,v77\nv_subrev_f32 v110,s70,v78\nv_subrev_f32 v111,s70,v79\nv_pk_mul_f32 v[124:125],v[104:105],v[104:105]\nv_pk_add_f32 v[120:121],v[104:105],v[106:107]\nv_pk_fma_f32 v[124:125],v[106:107],v[106:107],v[124:125]\nv_pk_fma_f32 v[124:125],v[108:109],v[108:109],v[124:125]\n" \
"v_pk_fma_f32 v[124:125],v[110:111],v[110:111],v[124:125]\nv_pk_add_f32 v[108:109],v[108:109],v[110:111]\nv_pk_add_f32 v[120:121],v[120:121],v[108:109]\nv_add_f32 v120,v120,v121\nv_add_f32 v121,v124,v125\ns_nop 1\nv_permlane16_swap_b32 v120,v121\nv_add_f32 v120,v120,v121\n" \
"v_mov_b32 v121,v120\ns_nop 1\nv_permlane32_swap_b32 v120,v121\nv_add_f32 v120,v120,v121\ns_nop 1\nv_add_f32_dpp v120,v120,v120 quad_perm:[1,0,3,2] row_mask:0xf bank_mask:0xf\ns_nop 1\nv_add_f32_dpp v120,v120,v120 quad_perm:[2,3,0,1] row_mask:0xf bank_mask:0xf\n" \
"s_nop 1\nv_add_f32_dpp v120,v120,v120 row_half_mirror row_mask:0xf bank_mask:0xf\ns_nop 1\nv_add_f32_dpp v120,v120,v120 row_mirror row_mask:0xf bank_mask:0xf\nv_mul_f32 v120,0x3b000000,v120\nv_mov_b32 v121,v120\nv_cmp_u_f32 vcc,v120,v120\ns_nop 0\n" \
"v_permlane16_swap_b32 v120,v121\ns_cbranch_vccnz .Lrs1\n.Lrb1:\nv_fma_f32 v121,-v120,v120,v121\nv_add_f32 v122,s70,v120\nv_max_f32 v121,0,v121\nv_cvt_pk_bf16_f32 v122,0,v122\nv_cvt_pk_bf16_f32 v121,0,v121\n" \
"v_add_f32 v121,0x3727c5ac,v121\nv_cvt_pk_bf16_f32 v121,0,v121\nv_sqrt_f32 v121,v121\nv_pk_add_f32 v[88:89],v[72:73],v[122:123] op_sel_hi:[1,0] neg_lo:[0,1] neg_hi:[0,1]\nv_cvt_pk_bf16_f32 v121,0,v121\nv_rcp_f32 v123,v121\nv_cvt_pk_bf16_f32 v88,0,v88\nv_pk_add_f32 v[90:91],v[74:75],v[122:123] op_sel_hi:[1,0] neg_lo:[0,1] neg_hi:[0,1]\n" \
"v_pk_add_f32 v[92:93],v[76:77],v[122:123] op_sel_hi:[1,0] neg_lo:[0,1] neg_hi:[0,1]\nv_pk_add_f32 v[94:95],v[78:79],v[122:123] op_sel_hi:[1,0] neg_lo:[0,1] neg_hi:[0,1]\nv_cvt_pk_bf16_f32 v89,0,v89\nv_pk_mul_f32 v[88:89],v[88:89],v[122:123] op_sel:[0,1]\nv_cvt_pk_bf16_f32 v88,0,v88\nv_cvt_pk_bf16_f32 v89,0,v89\nv_pk_mul_f32 v[88:89],v[88:89],v[32:33]\nv_cvt_pk_bf16_f32 v88,0,v88\n" \
"v_cvt_pk_bf16_f32 v89,0,v89\nv_pk_add_f32 v[88:89],v[88:89],v[16:17]\nv_cvt_pk_bf16_f32 v88,0,v88\nv_cvt_pk_bf16_f32 v89,0,v89\nv_pk_mul_f32 v[124:125],v[88:89],v[96:97]\nv_cvt_pk_bf16_f32 v90,0,v90\nv_cvt_pk_bf16_f32 v91,0,v91\nv_pk_mul_f32 v[90:91],v[90:91],v[122:123] op_sel:[0,1]\n" \
"v_cvt_pk_bf16_f32 v90,0,v90\nv_cvt_pk_bf16_f32 v91,0,v91\nv_pk_mul_f32 v[90:91],v[90:91],v[34:35]\nv_cvt_pk_bf16_f32 v90,0,v90\nv_cvt_pk_bf16_f32 v91,0,v91\nv_pk_add_f32 v[90:91],v[90:91],v[18:19]\nv_cvt_pk_bf16_f32 v90,0,v90\nv_cvt_pk_bf16_f32 v91,0,v91\n" \
"v_pk_fma_f32 v[124:125],v[90:91],v[98:99],v[124:125]\nv_cvt_pk_bf16_f32 v92,0,v92\nv_cvt_pk_bf16_f32 v93,0,v93\nv_pk_mul_f32 v[92:93],v[92:93],v[122:123] op_sel:[0,1]\nv_cvt_pk_bf16_f32 v92,0,v92\nv_cvt_pk_bf16_f32 v93,0,v93\nv_pk_mul_f32 v[92:93],v[92:93],v[36:37]\nv_cvt_pk_bf16_f32 v92,0,v92\n" \
"v_cvt_pk_bf16_f32 v93,0,v93\nv_pk_add_f32 v[92:93],v[92:93],v[20:21]\nv_cvt_pk_bf16_f32 v92,0,v92\nv_cvt_pk_bf16_f32 v93,0,v93\nv_pk_fma_f32 v[124:125],v[92:93],v[100:101],v[124:125]\nv_cvt_pk_bf16_f32 v94,0,v94\nv_cvt_pk_bf16_f32 v95,0,v95\nv_pk_mul_f32 v[94:95],v[94:95],v[122:123] op_sel:[0,1]\n" \
"v_cvt_pk_bf16_f32 v94,0,v94\nv_cvt_pk_bf16_f32 v95,0,v95\nv_pk_mul_f32 v[94:95],v[94:95],v[38:39]\nv_cvt_pk_bf16_f32 v94,0,v94\nv_cvt_pk_bf16_f32 v95,0,v95\nv_pk_add_f32 v[94:95],v[94:95],v[22:23]\nv_cvt_pk_bf16_f32 v94,0,v94\nv_cvt_pk_bf16_f32 v95,0,v95\n" \
"v_pk_fma_f32 v[124:125],v[94:95],v[102:103],v[124:125]\nv_add_f32 v124,v124,v125\nv_mov_b32 v125,v124\nv_lshrrev_b32 v104,16,v121\nv_or_b32 v104,v104,v122\nv_permlane16_swap_b32 v124,v125\nv_add_f32 v124,v124,v125\nv_mov_b32 v125,v124\n" \
"s_nop 1\nv_permlane32_swap_b32 v124,v125\nv_add_f32 v124,v124,v125\ns_nop 1\nv_add_f32_dpp v124,v124,v124 quad_perm:[1,0,3,2] row_mask:0xf bank_mask:0xf\ns_nop 1\nv_add_f32_dpp v124,v124,v124 quad_perm:[2,3,0,1] row_mask:0xf bank_mask:0xf\ns_nop 1\n" \
"v_add_f32_dpp v124,v124,v124 row_half_mirror row_mask:0xf bank_mask:0xf\ns_nop 1\nv_add_f32_dpp v124,v124,v124 row_mirror row_mask:0xf bank_mask:0xf\nv_cvt_pk_bf16_f32 v124,0,v124\ns_nop 0\nv_readfirstlane_b32 s77,v124\ns_nop 0\ns_ashr_i32 s71,s77,31\n" \
"s_and_b32 s72,s77,0x7fffffff\ns_or_b32 s71,s71,0x80000000\ns_xor_b32 s77,s77,s71\ns_and_b32 s77,s77,0xffff0000\ns_cmp_gt_u32 s72,0x7f800000\ns_cselect_b32 s77,0,s77\ns_or_b32 s77,s77,s38\nv_mov_b32 v105,s77\n" \
"ds_max_u64 v250,v[104:105]\n.Lrow0:\ns_waitcnt vmcnt(7)\nM v[8:11],a[192:195],v[128:131],0\nM v[12:15],a[196:199],v[128:131],0\ns_waitcnt vmcnt(6)\nM v[8:11],a[192:195],v[132:135],v[8:11]\nM v[12:15],a[196:199],v[132:135],v[12:15]\n" \
"s_waitcnt vmcnt(5)\nM v[8:11],a[192:195],v[136:139],v[8:11]\nM v[12:15],a[196:199],v[136:139],v[12:15]\ns_waitcnt vmcnt(4)\nM v[8:11],a[192:195],v[140:143],v[8:11]\nM v[12:15],a[196:199],v[140:143],v[12:15]\ns_waitcnt vmcnt(3)\nM v[8:11],a[192:195],v[144:147],v[8:11]\n" \
"M v[12:15],a[196:199],v[144:147],v[12:15]\ns_waitcnt vmcnt(2)\nM v[8:11],a[192:195],v[148:151],v[8:11]\nM v[12:15],a[196:199],v[148:151],v[12:15]\ns_waitcnt vmcnt(1)\nM v[8:11],a[192:195],v[152:155],v[8:11]\nM v[12:15],a[196:199],v[152:155],v[12:15]\ns_waitcnt vmcnt(0)\n" \
"M v[8:11],a[192:195],v[156:159],v[8:11]\nM v[12:15],a[196:199],v[156:159],v[12:15]\ns_sub_u32 s39,56622112,s74\ns_lshr_b32 s39,s39,5\ns_nop 4\nv_cvt_pk_bf16_f32 v8,0,v8\nv_cvt_pk_bf16_f32 v9,0,v9\nv_cvt_pk_bf16_f32 v10,0,v10\n" \
"v_cvt_pk_bf16_f32 v11,0,v11\nv_cvt_pk_bf16_f32 v12,0,v12\nv_cvt_pk_bf16_f32 v13,0,v13\nv_cvt_pk_bf16_f32 v14,0,v14\nv_cvt_pk_bf16_f32 v15,0,v15\nv_pk_mul_f32 v[8:9],v[8:9],v[0:1]\nv_pk_mul_f32 v[10:11],v[10:11],v[2:3]\nv_pk_mul_f32 v[12:13],v[12:13],v[4:5]\n" \
"v_pk_mul_f32 v[14:15],v[14:15],v[6:7]\nv_cvt_pk_bf16_f32 v8,0,v8\nv_cvt_pk_bf16_f32 v9,0,v9\nv_cvt_pk_bf16_f32 v10,0,v10\nv_cvt_pk_bf16_f32 v11,0,v11\nv_cvt_pk_bf16_f32 v12,0,v12\nv_cvt_pk_bf16_f32 v13,0,v13\nv_cvt_pk_bf16_f32 v14,0,v14\n" \
"v_cvt_pk_bf16_f32 v15,0,v15\nv_pk_add_f32 v[8:9],v[8:9],v[80:81]\nv_pk_add_f32 v[10:11],v[10:11],v[82:83]\nv_pk_add_f32 v[12:13],v[12:13],v[84:85]\nv_pk_add_f32 v[14:15],v[14:15],v[86:87]\nv_cvt_pk_bf16_f32 v8,0,v8\nv_cvt_pk_bf16_f32 v9,0,v9\nv_cvt_pk_bf16_f32 v10,0,v10\n" \
"v_cvt_pk_bf16_f32 v11,0,v11\nv_cvt_pk_bf16_f32 v12,0,v12\nv_cvt_pk_bf16_f32 v13,0,v13\nv_cvt_pk_bf16_f32 v14,0,v14\nv_cvt_pk_bf16_f32 v15,0,v15\nv_readfirstlane_b32 s70,v8\ns_nop 1\nv_subrev_f32 v40,s70,v8\n" \
"v_subrev_f32 v41,s70,v9\nv_subrev_f32 v42,s70,v10\nv_subrev_f32 v43,s70,v11\nv_subrev_f32 v44,s70,v12\nv_subrev_f32 v45,s70,v13\nv_subrev_f32 v46,s70,v14\nv_subrev_f32 v47,s70,v15\nv_pk_mul_f32 v[60:61],v[40:41],v[40:41]\n" \
"v_pk_add_f32 v[56:57],v[40:41],v[42:43]\nv_pk_fma_f32 v[60:61],v[42:43],v[42:43],v[60:61]\nv_pk_fma_f32 v[60:61],v[44:45],v[44:45],v[60:61]\nv_pk_fma_f32 v[60:61],v[46:47],v[46:47],v[60:61]\nv_pk_add_f32 v[44:45],v[44:45],v[46:47]\nv_pk_add_f32 v[56:57],v[56:57],v[44:45]\nv_add_f32 v56,v56,v57\nv_add_f32 v57,v60,v61\n" \
"s_nop 1\nv_permlane16_swap_b32 v56,v57\nv_add_f32 v56,v56,v57\nv_mov_b32 v57,v56\ns_nop 1\nv_permlane32_swap_b32 v56,v57\nv_add_f32 v56,v56,v57\ns_nop 1\n" \
"v_add_f32_dpp v56,v56,v56 quad_perm:[1,0,3,2] row_mask:0xf bank_mask:0xf\ns_nop 1\nv_add_f32_dpp v56,v56,v56 quad_perm:[2,3,0,1] row_mask:0xf bank_mask:0xf\ns_nop 1\nv_add_f32_dpp v56,v56,v56 row_half_mirror row_mask:0xf bank_mask:0xf\ns_nop 1\nv_add_f32_dpp v56,v56,v56 row_mirror row_mask:0xf bank_mask:0xf\nv_mul_f32 v56,0x3b000000,v56\n" \
"v_mov_b32 v57,v56\nv_cmp_u_f32 vcc,v56,v56\ns_nop 0\nv_permlane16_swap_b32 v56,v57\ns_cbranch_vccnz .Lrs0\n.Lrb0:\nbuffer_load_dwordx4 v[232:235],v251,s[52:55],s80 offen sc1\nbuffer_load_dwordx4 v[236:239],v251,s[52:55],s80 offen offset:16 sc1\n" \
"v_fma_f32 v57,-v56,v56,v57\nv_add_f32 v58,s70,v56\nv_max_f32 v57,0,v57\nv_cvt_pk_bf16_f32 v58,0,v58\nv_cvt_pk_bf16_f32 v57,0,v57\nv_add_f32 v57,0x3727c5ac,v57\nv_cvt_pk_bf16_f32 v57,0,v57\nv_sqrt_f32 v57,v57\n" \
"v_pk_add_f32 v[24:25],v[8:9],v[58:59] op_sel_hi:[1,0] neg_lo:[0,1] neg_hi:[0,1]\nv_cvt_pk_bf16_f32 v57,0,v57\nv_rcp_f32 v59,v57\nv_cvt_pk_bf16_f32 v24,0,v24\nv_pk_add_f32 v[26:27],v[10:11],v[58:59] op_sel_hi:[1,0] neg_lo:[0,1] neg_hi:[0,1]\nv_pk_add_f32 v[28:29],v[12:13],v[58:59] op_sel_hi:[1,0] neg_lo:[0,1] neg_hi:[0,1]\nv_pk_add_f32 v[30:31],v[14:15],v[58:59] op_sel_hi:[1,0] neg_lo:[0,1] neg_hi:[0,1]\nv_cvt_pk_bf16_f32 v25,0,v25\n" \
"v_pk_mul_f32 v[24:25],v[24:25],v[58:59] op_sel:[0,1]\nv_cvt_pk_bf16_f32 v24,0,v24\nv_cvt_pk_bf16_f32 v25,0,v25\nv_pk_mul_f32 v[24:25],v[24:25],v[32:33]\nv_cvt_pk_bf16_f32 v24,0,v24\nv_cvt_pk_bf16_f32 v25,0,v25\nv_pk_add_f32 v[24:25],v[24:25],v[16:17]\nv_cvt_pk_bf16_f32 v24,0,v24\n" \
"v_cvt_pk_bf16_f32 v25,0,v25\nv_pk_mul_f32 v[60:61],v[24:25],v[96:97]\nv_cvt_pk_bf16_f32 v26,0,v26\nv_cvt_pk_bf16_f32 v27,0,v27\nv_pk_mul_f32 v[26:27],v[26:27],v[58:59] op_sel:[0,1]\nv_cvt_pk_bf16_f32 v26,0,v26\nv_cvt_pk_bf16_f32 v27,0,v27\nv_pk_mul_f32 v[26:27],v[26:27],v[34:35]\n" \
"v_cvt_pk_bf16_f32 v26,0,v26\nv_cvt_pk_bf16_f32 v27,0,v27\nv_pk_add_f32 v[26:27],v[26:27],v[18:19]\nv_cvt_pk_bf16_f32 v26,0,v26\nv_cvt_pk_bf16_f32 v27,0,v27\nv_pk_fma_f32 v[60:61],v[26:27],v[98:99],v[60:61]\nv_cvt_pk_bf16_f32 v28,0,v28\nv_cvt_pk_bf16_f32 v29,0,v29\n" \
"v_pk_mul_f32 v[28:29],v[28:29],v[58:59] op_sel:[0,1]\nv_cvt_pk_bf16_f32 v28,0,v28\nv_cvt_pk_bf16_f32 v29,0,v29\nv_pk_mul_f32 v[28:29],v[28:29],v[36:37]\nv_cvt_pk_bf16_f32 v28,0,v28\nv_cvt_pk_bf16_f32 v29,0,v29\nv_pk_add_f32 v[28:29],v[28:29],v[20:21]\nv_cvt_pk_bf16_f32 v28,0,v28\n" \
"v_cvt_pk_bf16_f32 v29,0,v29\nv_pk_fma_f32 v[60:61],v[28:29],v[100:101],v[60:61]\nv_cvt_pk_bf16_f32 v30,0,v30\nv_cvt_pk_bf16_f32 v31,0,v31\nv_pk_mul_f32 v[30:31],v[30:31],v[58:59] op_sel:[0,1]\nv_cvt_pk_bf16_f32 v30,0,v30\nv_cvt_pk_bf16_f32 v31,0,v31\nv_pk_mul_f32 v[30:31],v[30:31],v[38:39]\n" \
"v_cvt_pk_bf16_f32 v30,0,v30\nv_cvt_pk_bf16_f32 v31,0,v31\nv_pk_add_f32 v[30:31],v[30:31],v[22:23]\nv_cvt_pk_bf16_f32 v30,0,v30\nv_cvt_pk_bf16_f32 v31,0,v31\nv_pk_fma_f32 v[60:61],v[30:31],v[102:103],v[60:61]\nv_add_f32 v60,v60,v61\nv_mov_b32 v61,v60\n" \
"v_lshrrev_b32 v40,16,v57\nv_or_b32 v40,v40,v58\nv_permlane16_swap_b32 v60,v61\nv_add_f32 v60,v60,v61\nv_mov_b32 v61,v60\ns_nop 1\nv_permlane32_swap_b32 v60,v61\nv_add_f32 v60,v60,v61\n" \
"s_nop 1\nv_add_f32_dpp v60,v60,v60 quad_perm:[1,0,3,2] row_mask:0xf bank_mask:0xf\ns_nop 1\nv_add_f32_dpp v60,v60,v60 quad_perm:[2,3,0,1] row_mask:0xf bank_mask:0xf\ns_nop 1\nv_add_f32_dpp v60,v60,v60 row_half_mirror row_mask:0xf bank_mask:0xf\ns_nop 1\nv_add_f32_dpp v60,v60,v60 row_mirror row_mask:0xf bank_mask:0xf\n" \
"v_cvt_pk_bf16_f32 v60,0,v60\ns_nop 0\nv_readfirstlane_b32 s76,v60\ns_nop 0\ns_ashr_i32 s71,s76,31\ns_and_b32 s72,s76,0x7fffffff\ns_or_b32 s71,s71,0x80000000\ns_xor_b32 s76,s76,s71\n" \
"s_and_b32 s76,s76,0xffff0000\ns_cmp_gt_u32 s72,0x7f800000\ns_cselect_b32 s76,0,s76\ns_or_b32 s76,s76,s39\nv_mov_b32 v41,s76\nds_max_u64 v250,v[40:41]\ns_waitcnt lgkmcnt(0)\ns_barrier\n" \
"ds_read_b64 v[224:225],v250\ns_waitcnt lgkmcnt(0)\nbuffer_store_dwordx2 v[224:225],v246,s[52:55],s37 offen sc1\nv_readfirstlane_b32 s97,v225\ns_sub_u32 s95,s95,54525952\ns_sub_u32 s74,s74,54525952\ns_mov_b32 s94,0\ns_cmp_eq_u32 s97,s77\n" \
"s_cselect_b32 s65,s95,0x40000000\ns_cselect_b32 s95,0x40000000,s95\ns_cmp_eq_u32 s97,s76\ns_cselect_b32 s66,s74,0x40000000\ns_cselect_b32 s74,0x40000000,s74\ns_lshr_b32 s70,s6,7\ns_or_b32 s38,s7,s70\ns_cmp_lg_u32 s3,0\n" \
"s_waitcnt vmcnt(1)\ns_cbranch_scc1 .Lkp\nv_pk_mul_f32 v[88:89],v[88:89],v[48:49]\nv_pk_mul_f32 v[90:91],v[90:91],v[50:51]\nv_pk_mul_f32 v[92:93],v[92:93],v[52:53]\nv_pk_mul_f32 v[94:95],v[94:95],v[54:55]\nv_cvt_pk_bf16_f32 v88,0,v88\nv_cvt_pk_bf16_f32 v89,0,v89\n" \
"v_cvt_pk_bf16_f32 v90,0,v90\nv_cvt_pk_bf16_f32 v91,0,v91\nv_cvt_pk_bf16_f32 v92,0,v92\nv_cvt_pk_bf16_f32 v93,0,v93\nv_cvt_pk_bf16_f32 v94,0,v94\nv_cvt_pk_bf16_f32 v95,0,v95\nv_pk_add_f32 v[88:89],v[88:89],v[72:73]\nv_pk_add_f32 v[90:91],v[90:91],v[74:75]\n" \
"v_pk_add_f32 v[92:93],v[92:93],v[76:77]\nv_pk_add_f32 v[94:95],v[94:95],v[78:79]\nv_cvt_pk_bf16_f32 v88,0,v88\nds_write_b16_d16_hi v244,v88 offset:0\nv_cvt_pk_bf16_f32 v89,0,v89\nds_write_b16_d16_hi v244,v89 offset:32\nv_cvt_pk_bf16_f32 v90,0,v90\nds_write_b16_d16_hi v244,v90 offset:64\n" \
"v_cvt_pk_bf16_f32 v91,0,v91\nds_write_b16_d16_hi v244,v91 offset:96\nv_cvt_pk_bf16_f32 v92,0,v92\nds_write_b16_d16_hi v244,v92 offset:128\nv_cvt_pk_bf16_f32 v93,0,v93\nds_write_b16_d16_hi v244,v93 offset:160\nv_cvt_pk_bf16_f32 v94,0,v94\nds_write_b16_d16_hi v244,v94 offset:192\n" \
"v_cvt_pk_bf16_f32 v95,0,v95\nds_write_b16_d16_hi v244,v95 offset:224\nds_read_b128 v[64:67],v245 offset:0\nv_pk_mul_f32 v[24:25],v[24:25],v[48:49]\nv_pk_mul_f32 v[26:27],v[26:27],v[50:51]\nv_pk_mul_f32 v[28:29],v[28:29],v[52:53]\nv_pk_mul_f32 v[30:31],v[30:31],v[54:55]\nv_cvt_pk_bf16_f32 v24,0,v24\n" \
"v_cvt_pk_bf16_f32 v25,0,v25\nv_cvt_pk_bf16_f32 v26,0,v26\nv_cvt_pk_bf16_f32 v27,0,v27\nv_cvt_pk_bf16_f32 v28,0,v28\nv_cvt_pk_bf16_f32 v29,0,v29\nv_cvt_pk_bf16_f32 v30,0,v30\nv_cvt_pk_bf16_f32 v31,0,v31\nv_pk_add_f32 v[24:25],v[24:25],v[8:9]\n" \
"v_pk_add_f32 v[26:27],v[26:27],v[10:11]\nv_pk_add_f32 v[28:29],v[28:29],v[12:13]\nv_pk_add_f32 v[30:31],v[30:31],v[14:15]\nv_cvt_pk_bf16_f32 v24,0,v24\nds_write_b16_d16_hi v244,v24 offset:1024\nv_cvt_pk_bf16_f32 v25,0,v25\nds_write_b16_d16_hi v244,v25 offset:1056\nv_cvt_pk_bf16_f32 v26,0,v26\n" \
"ds_write_b16_d16_hi v244,v26 offset:1088\nv_cvt_pk_bf16_f32 v27,0,v27\nds_write_b16_d16_hi v244,v27 offset:1120\nv_cvt_pk_bf16_f32 v28,0,v28\nds_write_b16_d16_hi v244,v28 offset:1152\nv_cvt_pk_bf16_f32 v29,0,v29\nds_write_b16_d16_hi v244,v29 offset:1184\nv_cvt_pk_bf16_f32 v30,0,v30\n" \
"ds_write_b16_d16_hi v244,v30 offset:1216\nv_cvt_pk_bf16_f32 v31,0,v31\nds_write_b16_d16_hi v244,v31 offset:1248\nds_read_b128 v[68:71],v245 offset:1024\ns_waitcnt lgkmcnt(0)\nbuffer_store_dwordx4 v[64:67],v240,s[44:47],s95 offen sc1\nbuffer_store_dwordx4 v[68:71],v240,s[44:47],s74 offen sc1\ns_cmp_eq_u32 s7,0\n" \
"s_cselect_b32 s70,1,0\ns_xor_b32 s71,s65,s66\ns_or_b32 s70,s70,s71\ns_cbranch_scc0 .Lfin\ns_lshr_b32 s70,s6,3\ns_and_b32 s70,s70,15\ns_lshl4_add_u32 s98,s60,s70\nv_lshrrev_b32 v242,4,v249\n" \
"s_lshl_b32 s71,s98,2\nv_add_u32 v242,s71,v242\nv_and_b32 v244,15,v242\nv_bfe_u32 v245,v242,4,3\nv_lshl_add_u32 v244,v244,3,v245\nv_and_b32 v245,0x180,v242\nv_add_lshl_u32 v243,v244,v245,1\nv_lshlrev_b32 v244,1,v243\n" \
"s_mov_b32 s70,4096\nv_lshlrev_b32 v242,1,v242\nbuffer_load_dword v62,v244,s[52:55],0 offen offset:0 sc1\nbuffer_load_dword v63,v244,s[52:55],0 offen offset:2048 sc1\nbuffer_load_dword v126,v244,s[52:55],s70 offen offset:0 sc1\nbuffer_load_dword v127,v244,s[52:55],s70 offen offset:2048 sc1\n.Lkp:\ns_cmp_lg_u32 s7,0\n" \
"s_cbranch_scc1 .Lkl\nv_max3_u32 v228,v233,v235,v237\nv_min3_u32 v229,v233,v235,v237\nv_max_u32 v228,v228,v239\nv_min_u32 v229,v229,v239\nv_cmp_lt_u32 vcc,s94,v228\nv_cmp_eq_u32 s[90:91],0,v229\ns_cbranch_vccnz .Lnew\n" \
"s_cmp_eq_u64 s[90:91],0\ns_cbranch_scc1 .Lv0\ns_sub_u32 s73,s73,1\ns_cselect_b32 s73,0,s73\ns_cbranch_scc1 .Lv0\ns_sleep 3\nbuffer_load_dwordx4 v[232:235],v251,s[52:55],s80 offen sc1\nbuffer_load_dwordx4 v[236:239],v251,s[52:55],s80 offen offset:16 sc1\n" \
"s_waitcnt vmcnt(0)\ns_branch .Lkp\n.Lv0:\nv_mov_b32 v230,s96\ns_or_b32 s72,s94,2\nv_mov_b32 v231,s72\nds_write_b64 v250,v[230:231] offset:8\ns_branch .Lver\n" \
".Lkl:\nds_read_b64 v[230:231],v250 offset:8\ns_mov_b32 s91,0\ns_waitcnt lgkmcnt(0)\nv_readfirstlane_b32 s72,v231\nv_readfirstlane_b32 s96,v230\ns_and_b32 s90,s72,2\ns_xor_b32 s72,s72,s90\n" \
"s_xor_b32 s90,s90,2\ns_cmp_lg_u32 s72,s94\ns_mov_b32 s94,s72\ns_cbranch_scc1 .Lsp\ns_cmp_eq_u32 s90,0\ns_cbranch_scc1 .Lver\ns_sub_u32 s73,s73,1\ns_cselect_b32 s73,0,s73\n" \
"s_cbranch_scc1 .Lver\ns_sleep 1\n.Lyield:\ns_branch .Lkl\n.Lnew:\nv_mov_b32 v230,v228\nv_cmp_eq_u32 vcc,v228,v233\ns_cmp_eq_u64 s[90:91],0\n" \
"s_cselect_b32 s71,2,0\nv_max_u32_dpp v230,v230,v230 quad_perm:[1,0,3,2] row_mask:0xf bank_mask:0xf\nv_cndmask_b32 v229,v238,v232,vcc\nv_cmp_eq_u32 vcc,v228,v235\nv_max_u32_dpp v230,v230,v230 quad_perm:[2,3,0,1] row_mask:0xf bank_mask:0xf\ns_nop 1\nv_max_u32_dpp v230,v230,v230 row_half_mirror row_mask:0xf bank_mask:0xf\nv_cndmask_b32 v229,v229,v234,vcc\n" \
"v_cmp_eq_u32 vcc,v228,v237\nv_max_u32_dpp v230,v230,v230 row_mirror row_mask:0xf bank_mask:0xf\nv_mov_b32 v231,v230\nv_cndmask_b32 v229,v229,v236,vcc\ns_nop 0\nv_permlane16_swap_b32 v230,v231\nv_max_u32 v230,v230,v231\nv_mov_b32 v231,v230\n" \
"s_nop 1\nv_permlane32_swap_b32 v230,v231\nv_max_u32 v230,v230,v231\ns_nop 0\nv_readfirstlane_b32 s94,v230\ns_nop 1\nv_cmp_eq_u32 vcc,s94,v228\ns_nop 0\n" \
"s_ff1_i32_b64 s72,vcc\nv_readlane_b32 s96,v229,s72\ns_nop 1\nv_mov_b32 v230,s96\ns_or_b32 s72,s94,s71\nv_mov_b32 v231,s72\nds_write_b64 v250,v[230:231] offset:8\n.Lsp:\n" \
"s_not_b32 s15,s94\ns_bfe_u32 s15,s15,0xb0005\ns_lshl_b32 s15,s15,10\ns_cmp_eq_u32 s3,0\ns_cbranch_scc1 .Lkp\ns_add_u32 s72,s15,54525952\nbuffer_load_dword v129,v241,s[52:55],s72 offen sc1\ns_add_u32 s72,s15,56623104\n" \
"buffer_load_dword v131,v241,s[52:55],s72 offen sc1\ns_add_u32 s72,s15,58720256\nbuffer_load_dword v133,v241,s[52:55],s72 offen sc1\ns_add_u32 s72,s15,60817408\nbuffer_load_dword v135,v241,s[52:55],s72 offen sc1\ns_add_u32 s72,s15,62914560\nbuffer_load_dword v137,v241,s[52:55],s72 offen sc1\ns_add_u32 s72,s15,65011712\n" \
"buffer_load_dword v139,v241,s[52:55],s72 offen sc1\ns_add_u32 s72,s15,67108864\nbuffer_load_dword v141,v241,s[52:55],s72 offen sc1\ns_add_u32 s72,s15,69206016\nbuffer_load_dword v143,v241,s[52:55],s72 offen sc1\nv_add_u32 v146,s15,v248\nglobal_load_ushort v144,v146,s[8:9]\nglobal_load_ushort v145,v146,s[8:9] offset:32\n" \
"buffer_load_dwordx4 v[232:235],v251,s[52:55],s80 offen sc1\nbuffer_load_dwordx4 v[236:239],v251,s[52:55],s80 offen offset:16 sc1\nv_accvgpr_read_b32 v161,a200\nv_lshlrev_b32 v160,16,v161\nv_and_b32 v161,0xffff0000,v161\nv_accvgpr_read_b32 v163,a201\nv_lshlrev_b32 v162,16,v163\nv_and_b32 v163,0xffff0000,v163\n" \
"v_accvgpr_read_b32 v165,a202\nv_lshlrev_b32 v164,16,v165\nv_and_b32 v165,0xffff0000,v165\nv_accvgpr_read_b32 v167,a203\nv_lshlrev_b32 v166,16,v167\nv_and_b32 v167,0xffff0000,v167\nv_accvgpr_read_b32 v169,a204\nv_lshlrev_b32 v168,16,v169\n" \
"v_and_b32 v169,0xffff0000,v169\nv_accvgpr_read_b32 v171,a205\nv_lshlrev_b32 v170,16,v171\nv_and_b32 v171,0xffff0000,v171\nv_accvgpr_read_b32 v173,a206\nv_lshlrev_b32 v172,16,v173\nv_and_b32 v173,0xffff0000,v173\nv_accvgpr_read_b32 v175,a207\n" \
"v_lshlrev_b32 v174,16,v175\nv_and_b32 v175,0xffff0000,v175\nv_accvgpr_read_b32 v177,a208\nv_lshlrev_b32 v176,16,v177\nv_and_b32 v177,0xffff0000,v177\nv_accvgpr_read_b32 v179,a209\nv_lshlrev_b32 v178,16,v179\nv_and_b32 v179,0xffff0000,v179\n" \
"v_accvgpr_read_b32 v181,a210\nv_lshlrev_b32 v180,16,v181\nv_and_b32 v181,0xffff0000,v181\nv_accvgpr_read_b32 v183,a211\nv_lshlrev_b32 v182,16,v183\nv_and_b32 v183,0xffff0000,v183\nv_accvgpr_read_b32 v185,a252\nv_lshlrev_b32 v184,16,v185\n" \
"v_and_b32 v185,0xffff0000,v185\nv_accvgpr_read_b32 v187,a253\nv_lshlrev_b32 v186,16,v187\nv_and_b32 v187,0xffff0000,v187\nv_accvgpr_read_b32 v189,a254\nv_lshlrev_b32 v188,16,v189\nv_and_b32 v189,0xffff0000,v189\nv_accvgpr_read_b32 v191,a255\n" \
"v_lshlrev_b32 v190,16,v191\nv_and_b32 v191,0xffff0000,v191\ns_and_b32 s82,s96,0xffff0000\ns_lshl_b32 s83,s96,16\nv_rcp_f32 v148,s83\ns_waitcnt vmcnt(11)\nv_lshlrev_b32 v128,16,v129\nv_and_b32 v129,0xffff0000,v129\n" \
"s_waitcnt vmcnt(10)\nv_lshlrev_b32 v130,16,v131\nv_and_b32 v131,0xffff0000,v131\nv_pk_add_f32 v[128:129],v[128:129],v[130:131]\ns_waitcnt vmcnt(9)\nv_lshlrev_b32 v132,16,v133\nv_and_b32 v133,0xffff0000,v133\ns_waitcnt vmcnt(8)\n" \
"v_lshlrev_b32 v134,16,v135\nv_and_b32 v135,0xffff0000,v135\nv_pk_add_f32 v[132:133],v[132:133],v[134:135]\nv_pk_add_f32 v[128:129],v[128:129],v[132:133]\ns_waitcnt vmcnt(7)\nv_lshlrev_b32 v136,16,v137\nv_and_b32 v137,0xffff0000,v137\ns_waitcnt vmcnt(6)\n" \
"v_lshlrev_b32 v138,16,v139\nv_and_b32 v139,0xffff0000,v139\nv_pk_add_f32 v[136:137],v[136:137],v[138:139]\ns_waitcnt vmcnt(5)\nv_lshlrev_b32 v140,16,v141\nv_and_b32 v141,0xffff0000,v141\ns_waitcnt vmcnt(4)\nv_lshlrev_b32 v142,16,v143\n" \
"v_and_b32 v143,0xffff0000,v143\nv_pk_add_f32 v[140:141],v[140:141],v[142:143]\nv_pk_add_f32 v[136:137],v[136:137],v[140:141]\nv_pk_add_f32 v[128:129],v[128:129],v[136:137]\nv_cvt_pk_bf16_f32 v128,0,v128\nv_cvt_pk_bf16_f32 v129,0,v129\nv_pk_mul_f32 v[128:129],v[128:129],v[112:113]\nv_cvt_pk_bf16_f32 v128,0,v128\n" \
"v_cvt_pk_bf16_f32 v129,0,v129\ns_waitcnt vmcnt(3)\nv_lshlrev_b32 v144,16,v144\ns_waitcnt vmcnt(2)\nv_lshlrev_b32 v145,16,v145\nv_pk_add_f32 v[128:129],v[128:129],v[144:145]\nv_cvt_pk_bf16_f32 v128,0,v128\nv_cvt_pk_bf16_f32 v129,0,v129\n" \
"v_pk_add_f32 v[128:129],v[128:129],s[82:83] op_sel_hi:[1,0] neg_lo:[0,1] neg_hi:[0,1]\nv_cvt_pk_bf16_f32 v128,0,v128\nv_cvt_pk_bf16_f32 v129,0,v129\nv_pk_mul_f32 v[128:129],v[128:129],v[148:149] op_sel_hi:[1,0]\nv_cvt_pk_bf16_f32 v128,0,v128\nv_cvt_pk_bf16_f32 v129,0,v129\nv_pk_mul_f32 v[128:129],v[128:129],v[116:117]\nv_cvt_pk_bf16_f32 v128,0,v128\n" \
"v_cvt_pk_bf16_f32 v129,0,v129\nv_pk_add_f32 v[128:129],v[128:129],v[114:115]\nv_cvt_pk_bf16_f32 v128,v128,v129\ns_nop 1\nv_mov_b32_dpp v132,v128 quad_perm:[0,0,0,0] row_mask:0xf bank_mask:0xf\nv_mov_b32_dpp v133,v128 quad_perm:[1,1,1,1] row_mask:0xf bank_mask:0xf\nv_mov_b32_dpp v134,v128 quad_perm:[2,2,2,2] row_mask:0xf bank_mask:0xf\nv_mov_b32_dpp v135,v128 quad_perm:[3,3,3,3] row_mask:0xf bank_mask:0xf\n" \
"v_dot2_f32_bf16 v136,v192,v132,0\nv_dot2_f32_bf16 v137,v193,v132,0\nv_dot2_f32_bf16 v138,v194,v132,0\nv_dot2_f32_bf16 v139,v195,v132,0\nv_dot2_f32_bf16 v140,v208,v132,0\nv_dot2_f32_bf16 v141,v209,v132,0\nv_dot2_f32_bf16 v142,v210,v132,0\nv_dot2_f32_bf16 v143,v211,v132,0\n" \
"v_dot2_f32_bf16 v136,v196,v133,v136\nv_dot2_f32_bf16 v137,v197,v133,v137\nv_dot2_f32_bf16 v138,v198,v133,v138\nv_dot2_f32_bf16 v139,v199,v133,v139\nv_dot2_f32_bf16 v140,v212,v133,v140\nv_dot2_f32_bf16 v141,v213,v133,v141\nv_dot2_f32_bf16 v142,v214,v133,v142\nv_dot2_f32_bf16 v143,v215,v133,v143\n" \
"v_dot2_f32_bf16 v136,v200,v134,v136\nv_dot2_f32_bf16 v137,v201,v134,v137\nv_dot2_f32_bf16 v138,v202,v134,v138\nv_dot2_f32_bf16 v139,v203,v134,v139\nv_dot2_f32_bf16 v140,v216,v134,v140\nv_dot2_f32_bf16 v141,v217,v134,v141\nv_dot2_f32_bf16 v142,v218,v134,v142\nv_dot2_f32_bf16 v143,v219,v134,v143\n" \
"v_dot2_f32_bf16 v136,v204,v135,v136\nv_dot2_f32_bf16 v137,v205,v135,v137\nv_dot2_f32_bf16 v138,v206,v135,v138\nv_dot2_f32_bf16 v139,v207,v135,v139\nv_dot2_f32_bf16 v140,v220,v135,v140\nv_dot2_f32_bf16 v141,v221,v135,v141\nv_dot2_f32_bf16 v142,v222,v135,v142\nv_dot2_f32_bf16 v143,v223,v135,v143\n" \
"v_permlane32_swap_b32 v136,v140\nv_add_f32 v136,v136,v140\nv_permlane32_swap_b32 v137,v141\nv_add_f32 v137,v137,v141\nv_permlane32_swap_b32 v138,v142\nv_add_f32 v138,v138,v142\nv_permlane32_swap_b32 v139,v143\nv_add_f32 v139,v139,v143\n" \
"v_permlane16_swap_b32 v136,v138\nv_add_f32 v136,v136,v138\nv_permlane16_swap_b32 v137,v139\nv_add_f32 v137,v137,v139\nv_add_f32_dpp v136,v136,v136 row_ror:4 row_mask:0xf bank_mask:0xf\ns_nop 1\nv_add_f32_dpp v136,v136,v136 row_ror:8 row_mask:0xf bank_mask:0xf\nv_add_f32_dpp v137,v137,v137 row_ror:4 row_mask:0xf bank_mask:0xf\n" \
"s_nop 1\nv_add_f32_dpp v137,v137,v137 row_ror:8 row_mask:0xf bank_mask:0xf\ns_cmp_eq_u64 s[90:91],0\ns_cbranch_scc1 .Lver\ns_waitcnt vmcnt(0)\ns_branch .Lkp\n.Lver:\ns_cmp_eq_u32 s3,0\n" \
"s_cbranch_scc1 .Lnf\nds_write_b32 v252,v136\nds_write_b32 v252,v137 offset:16\ns_lshl_b32 s70,s2,6\ns_add_u32 s70,s70,98304\nv_lshlrev_b32 v241,1,v241\ns_waitcnt lgkmcnt(0)\ns_barrier\n" \
"ds_read_b128 v[128:131],v253\nds_read_b128 v[132:135],v253 offset:256\ns_waitcnt lgkmcnt(1)\nv_pk_add_f32 v[128:129],v[128:129],v[130:131]\nv_add_f32 v128,v128,v129\nv_cvt_pk_bf16_f32 v128,0,v128\nv_mul_f32 v129,0xbfb8aa3b,v128\nv_exp_f32 v129,v129\n" \
"s_waitcnt lgkmcnt(0)\nv_pk_add_f32 v[132:133],v[132:133],v[134:135]\nv_add_f32 v129,1.0,v129\nv_rcp_f32 v129,v129\nv_add_f32 v132,v132,v133\nv_cvt_pk_bf16_f32 v132,0,v132\nv_mul_f32 v128,v128,v129\nv_cvt_pk_bf16_f32 v128,0,v128\n" \
"v_mul_f32 v128,v128,v132\nv_cvt_pk_bf16_f32 v128,0,v128\ns_nop 0\nv_readlane_b32 s8,v128,0\nv_readlane_b32 s9,v128,1\nv_readlane_b32 s10,v128,2\nv_readlane_b32 s11,v128,3\nv_readlane_b32 s12,v128,4\n" \
"v_readlane_b32 s13,v128,5\nv_readlane_b32 s14,v128,6\nv_readlane_b32 s15,v128,7\nv_readlane_b32 s16,v128,8\nv_readlane_b32 s17,v128,9\nv_readlane_b32 s18,v128,10\nv_readlane_b32 s19,v128,11\nv_readlane_b32 s20,v128,12\n" \
"v_readlane_b32 s21,v128,13\nv_readlane_b32 s22,v128,14\nv_readlane_b32 s23,v128,15\nv_pk_mul_f32 v[136:137],v[160:161],s[8:9] op_sel_hi:[1,0]\nv_pk_mul_f32 v[138:139],v[162:163],s[8:9] op_sel:[0,1]\nv_pk_fma_f32 v[136:137],v[164:165],s[10:11],v[136:137] op_sel_hi:[1,0,1]\nv_pk_fma_f32 v[138:139],v[166:167],s[10:11],v[138:139] op_sel:[0,1,0]\nv_pk_fma_f32 v[136:137],v[168:169],s[12:13],v[136:137] op_sel_hi:[1,0,1]\n" \
"v_pk_fma_f32 v[138:139],v[170:171],s[12:13],v[138:139] op_sel:[0,1,0]\nv_pk_fma_f32 v[136:137],v[172:173],s[14:15],v[136:137] op_sel_hi:[1,0,1]\nv_pk_fma_f32 v[138:139],v[174:175],s[14:15],v[138:139] op_sel:[0,1,0]\nv_pk_fma_f32 v[136:137],v[176:177],s[16:17],v[136:137] op_sel_hi:[1,0,1]\nv_pk_fma_f32 v[138:139],v[178:179],s[16:17],v[138:139] op_sel:[0,1,0]\nv_pk_fma_f32 v[136:137],v[180:181],s[18:19],v[136:137] op_sel_hi:[1,0,1]\nv_pk_fma_f32 v[138:139],v[182:183],s[18:19],v[138:139] op_sel:[0,1,0]\nv_pk_fma_f32 v[136:137],v[184:185],s[20:21],v[136:137] op_sel_hi:[1,0,1]\n" \
"v_pk_fma_f32 v[138:139],v[186:187],s[20:21],v[138:139] op_sel:[0,1,0]\nv_pk_fma_f32 v[136:137],v[188:189],s[22:23],v[136:137] op_sel_hi:[1,0,1]\nv_pk_fma_f32 v[138:139],v[190:191],s[22:23],v[138:139] op_sel:[0,1,0]\nv_pk_add_f32 v[136:137],v[136:137],v[138:139]\nv_min_u32 v136,-2,v136\nv_min_u32 v137,-2,v137\nbuffer_store_dwordx2 v[136:137],v241,s[52:55],s70 offen sc1\nv_pk_mul_f32 v[88:89],v[88:89],v[48:49]\n" \
"v_pk_mul_f32 v[90:91],v[90:91],v[50:51]\nv_pk_mul_f32 v[92:93],v[92:93],v[52:53]\nv_pk_mul_f32 v[94:95],v[94:95],v[54:55]\nv_cvt_pk_bf16_f32 v88,0,v88\nv_cvt_pk_bf16_f32 v89,0,v89\nv_cvt_pk_bf16_f32 v90,0,v90\nv_cvt_pk_bf16_f32 v91,0,v91\nv_cvt_pk_bf16_f32 v92,0,v92\n" \
"v_cvt_pk_bf16_f32 v93,0,v93\nv_cvt_pk_bf16_f32 v94,0,v94\nv_cvt_pk_bf16_f32 v95,0,v95\nv_pk_add_f32 v[88:89],v[88:89],v[72:73]\nv_pk_add_f32 v[90:91],v[90:91],v[74:75]\nv_pk_add_f32 v[92:93],v[92:93],v[76:77]\nv_pk_add_f32 v[94:95],v[94:95],v[78:79]\nv_cvt_pk_bf16_f32 v88,0,v88\n" \
"ds_write_b16_d16_hi v244,v88 offset:0\nv_cvt_pk_bf16_f32 v89,0,v89\nds_write_b16_d16_hi v244,v89 offset:32\nv_cvt_pk_bf16_f32 v90,0,v90\nds_write_b16_d16_hi v244,v90 offset:64\nv_cvt_pk_bf16_f32 v91,0,v91\nds_write_b16_d16_hi v244,v91 offset:96\nv_cvt_pk_bf16_f32 v92,0,v92\n" \
"ds_write_b16_d16_hi v244,v92 offset:128\nv_cvt_pk_bf16_f32 v93,0,v93\nds_write_b16_d16_hi v244,v93 offset:160\nv_cvt_pk_bf16_f32 v94,0,v94\nds_write_b16_d16_hi v244,v94 offset:192\nv_cvt_pk_bf16_f32 v95,0,v95\nds_write_b16_d16_hi v244,v95 offset:224\nds_read_b128 v[64:67],v245 offset:0\n" \
"v_pk_mul_f32 v[24:25],v[24:25],v[48:49]\nv_pk_mul_f32 v[26:27],v[26:27],v[50:51]\nv_pk_mul_f32 v[28:29],v[28:29],v[52:53]\nv_pk_mul_f32 v[30:31],v[30:31],v[54:55]\nv_cvt_pk_bf16_f32 v24,0,v24\nv_cvt_pk_bf16_f32 v25,0,v25\nv_cvt_pk_bf16_f32 v26,0,v26\nv_cvt_pk_bf16_f32 v27,0,v27\n" \
"v_cvt_pk_bf16_f32 v28,0,v28\nv_cvt_pk_bf16_f32 v29,0,v29\nv_cvt_pk_bf16_f32 v30,0,v30\nv_cvt_pk_bf16_f32 v31,0,v31\nv_pk_add_f32 v[24:25],v[24:25],v[8:9]\nv_pk_add_f32 v[26:27],v[26:27],v[10:11]\nv_pk_add_f32 v[28:29],v[28:29],v[12:13]\nv_pk_add_f32 v[30:31],v[30:31],v[14:15]\n" \
"v_cvt_pk_bf16_f32 v24,0,v24\nds_write_b16_d16_hi v244,v24 offset:1024\nv_cvt_pk_bf16_f32 v25,0,v25\nds_write_b16_d16_hi v244,v25 offset:1056\nv_cvt_pk_bf16_f32 v26,0,v26\nds_write_b16_d16_hi v244,v26 offset:1088\nv_cvt_pk_bf16_f32 v27,0,v27\nds_write_b16_d16_hi v244,v27 offset:1120\n" \
"v_cvt_pk_bf16_f32 v28,0,v28\nds_write_b16_d16_hi v244,v28 offset:1152\nv_cvt_pk_bf16_f32 v29,0,v29\nds_write_b16_d16_hi v244,v29 offset:1184\nv_cvt_pk_bf16_f32 v30,0,v30\nds_write_b16_d16_hi v244,v30 offset:1216\nv_cvt_pk_bf16_f32 v31,0,v31\nds_write_b16_d16_hi v244,v31 offset:1248\n" \
"ds_read_b128 v[68:71],v245 offset:1024\ns_waitcnt lgkmcnt(0)\nbuffer_store_dwordx4 v[64:67],v240,s[44:47],s95 offen sc1\nbuffer_store_dwordx4 v[68:71],v240,s[44:47],s74 offen sc1\n.Lnf:\ns_cmp_eq_u32 s94,s97\ns_cbranch_scc1 .Lwin\nbuffer_store_dwordx4 v[64:67],v240,s[44:47],s65 offen sc1\n" \
"buffer_store_dwordx4 v[68:71],v240,s[44:47],s66 offen sc1\n.Lwin:\ns_cmp_lg_u32 s38,0\ns_cbranch_scc1 .Lfin\ns_add_u32 s72,s15,54525952\nbuffer_load_ushort v128,v243,s[52:55],s72 offen sc1\ns_add_u32 s72,s15,56623104\nbuffer_load_ushort v129,v243,s[52:55],s72 offen sc1\n" \
"s_add_u32 s72,s15,58720256\nbuffer_load_ushort v130,v243,s[52:55],s72 offen sc1\ns_add_u32 s72,s15,60817408\nbuffer_load_ushort v131,v243,s[52:55],s72 offen sc1\ns_add_u32 s72,s15,62914560\nbuffer_load_ushort v132,v243,s[52:55],s72 offen sc1\ns_add_u32 s72,s15,65011712\nbuffer_load_ushort v133,v243,s[52:55],s72 offen sc1\n" \
"s_add_u32 s72,s15,67108864\nbuffer_load_ushort v134,v243,s[52:55],s72 offen sc1\ns_add_u32 s72,s15,69206016\nbuffer_load_ushort v135,v243,s[52:55],s72 offen sc1\nv_add_u32 v242,s15,v242\nglobal_load_ushort v144,v242,s[8:9]\nv_min_u32 v168,55,v249\nv_lshlrev_b32 v168,11,v168\n" \
"s_lshl4_add_u32 s70,s98,98304\ns_add_u32 s71,s70,114688\ns_and_b32 s82,s96,0xffff0000\ns_lshl_b32 s83,s96,16\nv_rcp_f32 v148,s83\ns_ashr_i32 s37,s94,31\ns_not_b32 s37,s37\ns_or_b32 s37,s37,0x80000000\n" \
"s_xor_b32 s37,s37,s94\ns_and_b32 s37,s37,0xffff0000\nv_mov_b32 v149,0xbfb8aa3b\nv_mul_f32 v149,s37,v149\nv_exp_f32 v149,v149\nv_and_b32 v150,15,v249\nv_add_f32 v149,1.0,v149\nv_rcp_f32 v149,v149\n" \
"v_cmp_eq_u32 vcc,0,v150\nv_cvt_pk_bf16_f32 v149,0,v149\nv_mov_b32 v150,0x40000000\nv_cndmask_b32 v150,v150,v242,vcc\nv_cmp_gt_u32 s[90:91],56,v249\ns_waitcnt vmcnt(8)\nv_lshlrev_b32 v128,16,v128\ns_waitcnt vmcnt(7)\n" \
"v_lshlrev_b32 v129,16,v129\nv_add_f32 v128,v128,v129\ns_waitcnt vmcnt(6)\nv_lshlrev_b32 v130,16,v130\ns_waitcnt vmcnt(5)\nv_lshlrev_b32 v131,16,v131\nv_add_f32 v130,v130,v131\nv_add_f32 v128,v128,v130\n" \
"s_waitcnt vmcnt(4)\nv_lshlrev_b32 v132,16,v132\ns_waitcnt vmcnt(3)\nv_lshlrev_b32 v133,16,v133\nv_add_f32 v132,v132,v133\ns_waitcnt vmcnt(2)\nv_lshlrev_b32 v134,16,v134\ns_waitcnt vmcnt(1)\n" \
"v_lshlrev_b32 v135,16,v135\nv_add_f32 v134,v134,v135\nv_add_f32 v132,v132,v134\nv_add_f32 v128,v128,v132\nv_cvt_pk_bf16_f32 v128,0,v128\nv_mul_f32 v128,v128,v62\nv_cvt_pk_bf16_f32 v128,0,v128\ns_waitcnt vmcnt(0)\n" \
"v_add_f32 v126,1.0,v126\nv_cvt_pk_bf16_f32 v126,0,v126\nv_lshlrev_b32 v144,16,v144\nv_add_f32 v128,v128,v144\nv_cvt_pk_bf16_f32 v128,0,v128\nv_subrev_f32 v129,s82,v128\nv_cvt_pk_bf16_f32 v129,0,v129\nv_mul_f32 v129,v129,v148\n" \
"v_cvt_pk_bf16_f32 v129,0,v129\nv_mul_f32 v129,v129,v126\nv_cvt_pk_bf16_f32 v129,0,v129\nv_add_f32 v129,v129,v63\nv_cvt_pk_bf16_f32 v129,0,v129\n.Lp2:\nbuffer_load_dwordx4 v[160:163],v168,s[52:55],s70 offen sc1\nbuffer_load_dwordx4 v[164:167],v168,s[52:55],s71 offen sc1\n" \
"s_waitcnt vmcnt(0)\nv_max3_u32 v228,v160,v161,v162\nv_max3_u32 v229,v163,v164,v165\nv_max3_u32 v228,v228,v166,v167\nv_max_u32 v228,v228,v229\nv_pk_add_f32 v[160:161],v[160:161],v[164:165]\nv_cmp_eq_u32 vcc,-1,v228\nv_pk_add_f32 v[162:163],v[162:163],v[166:167]\n" \
"s_sub_u32 s73,s73,1\ns_cselect_b32 s73,0,s73\ns_cselect_b64 vcc,0,vcc\ns_cmp_lg_u64 vcc,0\ns_cselect_b32 s72,0x8000000,0\nv_cndmask_b32 v160,0,v160,s[90:91]\nv_cndmask_b32 v161,0,v161,s[90:91]\nv_cndmask_b32 v162,0,v162,s[90:91]\n" \
"v_cndmask_b32 v163,0,v163,s[90:91]\ns_nop 0\nv_permlane32_swap_b32 v160,v162\nv_permlane32_swap_b32 v161,v163\nv_pk_add_f32 v[160:161],v[160:161],v[162:163]\ns_nop 1\nv_permlane16_swap_b32 v160,v161\nv_add_f32 v160,v160,v161\n" \
"s_nop 1\nv_add_f32_dpp v160,v160,v160 quad_perm:[1,0,3,2] row_mask:0xf bank_mask:0xf\ns_nop 1\nv_add_f32_dpp v160,v160,v160 quad_perm:[2,3,0,1] row_mask:0xf bank_mask:0xf\ns_nop 1\nv_add_f32_dpp v160,v160,v160 row_half_mirror row_mask:0xf bank_mask:0xf\ns_nop 1\nv_add_f32_dpp v160,v160,v160 row_mirror row_mask:0xf bank_mask:0xf\n" \
"v_cvt_pk_bf16_f32 v160,0,v160\nv_mul_f32 v160,v160,v149\nv_cvt_pk_bf16_f32 v160,0,v160\nv_add_f32 v160,v160,v129\nv_cvt_pk_bf16_f32 v160,0,v160\nv_mul_f32 v160,v160,v127\nv_cvt_pk_bf16_f32 v160,0,v160\nv_add_f32 v160,v160,v128\n" \
"v_cvt_pk_bf16_f32 v160,0,v160\nbuffer_store_short_d16_hi v160,v150,s[44:47],s72 offen sc1\n.Lp2s:\ns_cbranch_scc1 .Lp2\ns_branch .Lfin\n.Lhs:\ns_sub_u32 s73,s73,1\ns_cselect_b32 s73,0,s73\n" \
"s_cbranch_scc1 .Lhb\ns_sleep 2\nbuffer_load_dwordx2 v[166:167],v165,s[52:55],s80 offen sc1\ns_waitcnt vmcnt(0)\nv_xor_b32 v166,v166,v230\nv_xor_b32 v167,v167,v231\nv_or_b32 v166,v166,v167\nv_cmp_ne_u32 vcc,0,v166\n" \
"s_nop 0\ns_cbranch_vccnz .Lhs\ns_branch .Lhb\n.Lps0:\ns_sub_u32 s73,s73,1\ns_cselect_b32 s73,0,s73\ns_cbranch_scc1 .Lpb0\ns_sleep 1\n" \
"buffer_load_dword v248,v228,s[52:55],s80 offen sc1\ns_waitcnt vmcnt(0)\nv_cmp_eq_u32 vcc,0,v248\ns_nop 0\ns_cbranch_vccnz .Lps0\ns_branch .Lpb0\n.Lps1:\ns_sub_u32 s73,s73,1\n" \
"s_cselect_b32 s73,0,s73\ns_cbranch_scc1 .Lpb1\ns_sleep 1\nbuffer_load_dword v248,v228,s[52:55],s80 offen sc1\ns_waitcnt vmcnt(0)\nv_cmp_eq_u32 vcc,0,v248\ns_nop 0\ns_cbranch_vccnz .Lps1\n" \
"s_branch .Lpb1\n.Lps2:\ns_sub_u32 s73,s73,1\ns_cselect_b32 s73,0,s73\ns_cbranch_scc1 .Lpb2\ns_sleep 1\nbuffer_load_dword v248,v251,s[52:55],s80 offen sc1\ns_waitcnt vmcnt(0)\n" \
"v_cmp_eq_u32 vcc,0,v248\ns_nop 0\ns_cbranch_vccnz .Lps2\ns_branch .Lpb2\n.Lps3:\ns_sub_u32 s73,s73,1\ns_cselect_b32 s73,0,s73\ns_cbranch_scc1 .Lpb3\n" \
"s_sleep 1\nbuffer_load_dword v248,v251,s[52:55],s80 offen sc1\ns_waitcnt vmcnt(0)\nv_cmp_eq_u32 vcc,0,v248\ns_nop 0\ns_cbranch_vccnz .Lps3\ns_branch .Lpb3\n.Lps4:\n" \
"s_sub_u32 s73,s73,1\ns_cselect_b32 s73,0,s73\ns_cbranch_scc1 .Lpb4\ns_sleep 1\nbuffer_load_dword v248,v251,s[52:55],s80 offen sc1\ns_waitcnt vmcnt(0)\nv_cmp_eq_u32 vcc,0,v248\ns_nop 0\n" \
"s_cbranch_vccnz .Lps4\ns_branch .Lpb4\n.Lps5:\ns_sub_u32 s73,s73,1\ns_cselect_b32 s73,0,s73\ns_cbranch_scc1 .Lpb5\ns_sleep 1\nbuffer_load_dword v248,v251,s[52:55],s80 offen sc1\n" \
"s_waitcnt vmcnt(0)\nv_cmp_eq_u32 vcc,0,v248\ns_nop 0\ns_cbranch_vccnz .Lps5\ns_branch .Lpb5\n.Lx01:\ns_cmp_eq_u32 s62,0\ns_cbranch_scc1 .Ltrans\n" \
"s_branch .Ldf\n.Lps6:\ns_sub_u32 s73,s73,1\ns_cselect_b32 s73,0,s73\ns_cbranch_scc1 .Lpb6\ns_sleep 1\nbuffer_load_dword v248,v251,s[52:55],s80 offen sc1\ns_waitcnt vmcnt(0)\n" \
"v_and_b32 v248,s17,v248\nv_cmp_eq_u32 vcc,0,v248\ns_nop 0\ns_cbranch_vccnz .Lps6\ns_branch .Lpb6\n.Lps7:\ns_sub_u32 s73,s73,1\ns_cselect_b32 s73,0,s73\n" \
"s_cbranch_scc1 .Lpb7\ns_sleep 1\nbuffer_load_dword v248,v254,s[52:55],s80 offen sc1\ns_waitcnt vmcnt(0)\nv_cmp_eq_u32 vcc,0,v248\ns_nop 0\ns_cbranch_vccnz .Lps7\ns_branch .Lpb7\n" \
".Lps8:\ns_sub_u32 s73,s73,1\ns_cselect_b32 s73,0,s73\ns_cbranch_scc1 .Lpb8\ns_sleep 1\nbuffer_load_dword v248,v251,s[52:55],s79 offen sc1\ns_waitcnt vmcnt(0)\nv_cmp_eq_u32 vcc,0,v248\n" \
"s_nop 0\ns_cbranch_vccnz .Lps8\ns_branch .Lpb8\n.Lrs1:\ns_mov_b32 s71,0\n.Lrl1:\ns_mov_b32 s97,0\nv_max3_u32 v124,v160,v161,v162\n" \
"v_max3_u32 v124,v124,v163,v163\nv_cmp_eq_u32 vcc,-1,v124\ns_add_u32 s72,s95,0\ns_cbranch_vccz .Lrz10\ns_mov_b64 exec,vcc\ns_mov_b32 s97,1\nbuffer_load_dwordx4 v[160:163],v240,s[52:55],s72 offen sc1\ns_mov_b64 exec,-1\n" \
".Lrz10:\nv_max3_u32 v124,v164,v165,v166\nv_max3_u32 v124,v124,v167,v167\nv_cmp_eq_u32 vcc,-1,v124\ns_add_u32 s72,s95,2097152\ns_cbranch_vccz .Lrz11\ns_mov_b64 exec,vcc\ns_mov_b32 s97,1\n" \
"buffer_load_dwordx4 v[164:167],v240,s[52:55],s72 offen sc1\ns_mov_b64 exec,-1\n.Lrz11:\nv_max3_u32 v124,v168,v169,v170\nv_max3_u32 v124,v124,v171,v171\nv_cmp_eq_u32 vcc,-1,v124\ns_add_u32 s72,s95,4194304\ns_cbranch_vccz .Lrz12\n" \
"s_mov_b64 exec,vcc\ns_mov_b32 s97,1\nbuffer_load_dwordx4 v[168:171],v240,s[52:55],s72 offen sc1\ns_mov_b64 exec,-1\n.Lrz12:\nv_max3_u32 v124,v172,v173,v174\nv_max3_u32 v124,v124,v175,v175\nv_cmp_eq_u32 vcc,-1,v124\n" \
"s_add_u32 s72,s95,6291456\ns_cbranch_vccz .Lrz13\ns_mov_b64 exec,vcc\ns_mov_b32 s97,1\nbuffer_load_dwordx4 v[172:175],v240,s[52:55],s72 offen sc1\ns_mov_b64 exec,-1\n.Lrz13:\nv_max3_u32 v124,v176,v177,v178\n" \
"v_max3_u32 v124,v124,v179,v179\nv_cmp_eq_u32 vcc,-1,v124\ns_add_u32 s72,s95,8388608\ns_cbranch_vccz .Lrz14\ns_mov_b64 exec,vcc\ns_mov_b32 s97,1\nbuffer_load_dwordx4 v[176:179],v240,s[52:55],s72 offen sc1\ns_mov_b64 exec,-1\n" \
".Lrz14:\nv_max3_u32 v124,v180,v181,v182\nv_max3_u32 v124,v124,v183,v183\nv_cmp_eq_u32 vcc,-1,v124\ns_add_u32 s72,s95,10485760\ns_cbranch_vccz .Lrz15\ns_mov_b64 exec,vcc\ns_mov_b32 s97,1\n" \
"buffer_load_dwordx4 v[180:183],v240,s[52:55],s72 offen sc1\ns_mov_b64 exec,-1\n.Lrz15:\nv_max3_u32 v124,v184,v185,v186\nv_max3_u32 v124,v124,v187,v187\nv_cmp_eq_u32 vcc,-1,v124\ns_add_u32 s72,s95,12582912\ns_cbranch_vccz .Lrz16\n" \
"s_mov_b64 exec,vcc\ns_mov_b32 s97,1\nbuffer_load_dwordx4 v[184:187],v240,s[52:55],s72 offen sc1\ns_mov_b64 exec,-1\n.Lrz16:\nv_max3_u32 v124,v188,v189,v190\nv_max3_u32 v124,v124,v191,v191\nv_cmp_eq_u32 vcc,-1,v124\n" \
"s_add_u32 s72,s95,14680064\ns_cbranch_vccz .Lrz17\ns_mov_b64 exec,vcc\ns_mov_b32 s97,1\nbuffer_load_dwordx4 v[188:191],v240,s[52:55],s72 offen sc1\ns_mov_b64 exec,-1\n.Lrz17:\ns_cmp_lg_u32 s97,0\n" \
"s_cbranch_scc1 .Lrm1\ns_cmp_eq_u32 s71,0\ns_cbranch_scc1 .Lrb1\ns_branch .Lrow1\n.Lrm1:\ns_mov_b32 s71,1\ns_waitcnt vmcnt(0)\ns_sub_u32 s73,s73,1\n" \
"s_cselect_b32 s73,0,s73\ns_cbranch_scc1 .Lrb1\ns_sleep 2\ns_branch .Lrl1\n.Lrs0:\ns_mov_b32 s71,0\n.Lrl0:\ns_mov_b32 s97,0\n" \
"v_max3_u32 v60,v128,v129,v130\nv_max3_u32 v60,v60,v131,v131\nv_cmp_eq_u32 vcc,-1,v60\ns_add_u32 s72,s74,0\ns_cbranch_vccz .Lrz00\ns_mov_b64 exec,vcc\ns_mov_b32 s97,1\nbuffer_load_dwordx4 v[128:131],v240,s[52:55],s72 offen sc1\n" \
"s_mov_b64 exec,-1\n.Lrz00:\nv_max3_u32 v60,v132,v133,v134\nv_max3_u32 v60,v60,v135,v135\nv_cmp_eq_u32 vcc,-1,v60\ns_add_u32 s72,s74,2097152\ns_cbranch_vccz .Lrz01\ns_mov_b64 exec,vcc\n" \
"s_mov_b32 s97,1\nbuffer_load_dwordx4 v[132:135],v240,s[52:55],s72 offen sc1\ns_mov_b64 exec,-1\n.Lrz01:\nv_max3_u32 v60,v136,v137,v138\nv_max3_u32 v60,v60,v139,v139\nv_cmp_eq_u32 vcc,-1,v60\ns_add_u32 s72,s74,4194304\n" \
"s_cbranch_vccz .Lrz02\ns_mov_b64 exec,vcc\ns_mov_b32 s97,1\nbuffer_load_dwordx4 v[136:139],v240,s[52:55],s72 offen sc1\ns_mov_b64 exec,-1\n.Lrz02:\nv_max3_u32 v60,v140,v141,v142\nv_max3_u32 v60,v60,v143,v143\n" \
"v_cmp_eq_u32 vcc,-1,v60\ns_add_u32 s72,s74,6291456\ns_cbranch_vccz .Lrz03\ns_mov_b64 exec,vcc\ns_mov_b32 s97,1\nbuffer_load_dwordx4 v[140:143],v240,s[52:55],s72 offen sc1\ns_mov_b64 exec,-1\n.Lrz03:\n" \
"v_max3_u32 v60,v144,v145,v146\nv_max3_u32 v60,v60,v147,v147\nv_cmp_eq_u32 vcc,-1,v60\ns_add_u32 s72,s74,8388608\ns_cbranch_vccz .Lrz04\ns_mov_b64 exec,vcc\ns_mov_b32 s97,1\nbuffer_load_dwordx4 v[144:147],v240,s[52:55],s72 offen sc1\n" \
"s_mov_b64 exec,-1\n.Lrz04:\nv_max3_u32 v60,v148,v149,v150\nv_max3_u32 v60,v60,v151,v151\nv_cmp_eq_u32 vcc,-1,v60\ns_add_u32 s72,s74,10485760\ns_cbranch_vccz .Lrz05\ns_mov_b64 exec,vcc\n" \
"s_mov_b32 s97,1\nbuffer_load_dwordx4 v[148:151],v240,s[52:55],s72 offen sc1\ns_mov_b64 exec,-1\n.Lrz05:\nv_max3_u32 v60,v152,v153,v154\nv_max3_u32 v60,v60,v155,v155\nv_cmp_eq_u32 vcc,-1,v60\ns_add_u32 s72,s74,12582912\n" \
"s_cbranch_vccz .Lrz06\ns_mov_b64 exec,vcc\ns_mov_b32 s97,1\nbuffer_load_dwordx4 v[152:155],v240,s[52:55],s72 offen sc1\ns_mov_b64 exec,-1\n.Lrz06:\nv_max3_u32 v60,v156,v157,v158\nv_max3_u32 v60,v60,v159,v159\n" \
"v_cmp_eq_u32 vcc,-1,v60\ns_add_u32 s72,s74,14680064\ns_cbranch_vccz .Lrz07\ns_mov_b64 exec,vcc\ns_mov_b32 s97,1\nbuffer_load_dwordx4 v[156:159],v240,s[52:55],s72 offen sc1\ns_mov_b64 exec,-1\n.Lrz07:\n" \
"s_cmp_lg_u32 s97,0\ns_cbranch_scc1 .Lrm0\ns_cmp_eq_u32 s71,0\ns_cbranch_scc1 .Lrb0\ns_branch .Lrow0\n.Lrm0:\ns_mov_b32 s71,1\ns_waitcnt vmcnt(0)\n" \
"s_sub_u32 s73,s73,1\ns_cselect_b32 s73,0,s73\ns_cbranch_scc1 .Lrb0\ns_sleep 2\ns_branch .Lrl0\n.Lredo:\ns_waitcnt vmcnt(0) lgkmcnt(0)\ns_barrier\n" \
"s_mov_b64 s[90:91],0\ns_mov_b32 s99,0\ns_mov_b32 s17,1\ns_lshl_b32 s70,s63,19\ns_add_u32 s37,s70,3145728\ns_lshl_b32 s71,s62,15\ns_add_u32 s72,s37,s64\ns_add_u32 s72,s72,491520\n" \
"s_sub_u32 s72,s72,s71\ns_add_u32 m0,s64,0\ns_nop 0\nbuffer_load_dwordx4 v240,s[52:55],s72 offen offset:0 sc1 lds\nbuffer_load_dwordx4 v240,s[52:55],s72 offen offset:1024 sc1 lds\nbuffer_load_dwordx4 v240,s[52:55],s72 offen offset:2048 sc1 lds\nbuffer_load_dwordx4 v240,s[52:55],s72 offen offset:3072 sc1 lds\ns_add_u32 m0,s64,65536\n" \
"s_add_u32 s72,s72,16777216\nbuffer_load_dwordx4 v240,s[52:55],s72 offen offset:0 sc1 lds\nbuffer_load_dwordx4 v240,s[52:55],s72 offen offset:1024 sc1 lds\nbuffer_load_dwordx4 v240,s[52:55],s72 offen offset:2048 sc1 lds\nbuffer_load_dwordx4 v240,s[52:55],s72 offen offset:3072 sc1 lds\ns_add_u32 s72,s37,s64\ns_add_u32 s72,s72,507904\ns_sub_u32 s72,s72,s71\n" \
"s_add_u32 m0,s64,16384\ns_nop 0\nbuffer_load_dwordx4 v240,s[52:55],s72 offen offset:0 sc1 lds\nbuffer_load_dwordx4 v240,s[52:55],s72 offen offset:1024 sc1 lds\nbuffer_load_dwordx4 v240,s[52:55],s72 offen offset:2048 sc1 lds\nbuffer_load_dwordx4 v240,s[52:55],s72 offen offset:3072 sc1 lds\ns_add_u32 m0,s64,81920\ns_add_u32 s72,s72,16777216\n" \
"buffer_load_dwordx4 v240,s[52:55],s72 offen offset:0 sc1 lds\nbuffer_load_dwordx4 v240,s[52:55],s72 offen offset:1024 sc1 lds\nbuffer_load_dwordx4 v240,s[52:55],s72 offen offset:2048 sc1 lds\nbuffer_load_dwordx4 v240,s[52:55],s72 offen offset:3072 sc1 lds\ns_add_u32 s72,s37,s64\ns_add_u32 s72,s72,16384\ns_add_u32 s72,s72,s71\ns_add_u32 m0,s64,32768\n" \
"s_nop 0\nbuffer_load_dwordx4 v240,s[52:55],s72 offen offset:0 sc1 lds\nbuffer_load_dwordx4 v240,s[52:55],s72 offen offset:1024 sc1 lds\nbuffer_load_dwordx4 v240,s[52:55],s72 offen offset:2048 sc1 lds\nbuffer_load_dwordx4 v240,s[52:55],s72 offen offset:3072 sc1 lds\ns_add_u32 m0,s64,98304\ns_add_u32 s72,s72,16777216\nbuffer_load_dwordx4 v240,s[52:55],s72 offen offset:0 sc1 lds\n" \
"buffer_load_dwordx4 v240,s[52:55],s72 offen offset:1024 sc1 lds\nbuffer_load_dwordx4 v240,s[52:55],s72 offen offset:2048 sc1 lds\nbuffer_load_dwordx4 v240,s[52:55],s72 offen offset:3072 sc1 lds\ns_add_u32 s72,s37,s64\ns_add_u32 s72,s72,0\ns_add_u32 s72,s72,s71\ns_add_u32 m0,s64,49152\ns_nop 0\n" \
"buffer_load_dwordx4 v240,s[52:55],s72 offen offset:0 sc1 lds\nbuffer_load_dwordx4 v240,s[52:55],s72 offen offset:1024 sc1 lds\nbuffer_load_dwordx4 v240,s[52:55],s72 offen offset:2048 sc1 lds\nbuffer_load_dwordx4 v240,s[52:55],s72 offen offset:3072 sc1 lds\ns_add_u32 m0,s64,114688\ns_add_u32 s72,s72,16777216\nbuffer_load_dwordx4 v240,s[52:55],s72 offen offset:0 sc1 lds\nbuffer_load_dwordx4 v240,s[52:55],s72 offen offset:1024 sc1 lds\n" \
"buffer_load_dwordx4 v240,s[52:55],s72 offen offset:2048 sc1 lds\nbuffer_load_dwordx4 v240,s[52:55],s72 offen offset:3072 sc1 lds\nv_and_b32 v251,15,v249\nv_lshrrev_b32 v250,4,v249\nv_lshlrev_b32 v250,2,v250\nv_sub_u32 v251,v251,v250\ns_lshl_b32 s70,s62,7\ns_lshl_b32 s72,s7,5\n" \
"s_sub_u32 s38,s72,s70\ns_add_u32 s38,s38,1920\ns_sub_u32 s39,s70,s72\ns_add_u32 s39,s39,96\nv_add_u32 v225,s38,v251\nv_add_u32 v225,0,v225\nv_mov_b32 v224,0xff800000\nv_add_u32 v229,s38,v251\n" \
"v_add_u32 v229,16,v229\nv_mov_b32 v228,0xff800000\nv_add_u32 v233,s39,v251\nv_add_u32 v233,0,v233\nv_mov_b32 v232,0xff800000\nv_add_u32 v237,s39,v251\nv_add_u32 v237,16,v237\nv_mov_b32 v236,0xff800000\n" \
"s_sub_u32 s38,524288,s71\ns_add_u32 s38,s38,s37\n.Lrl:\nbuffer_load_dwordx4 a[0:3],v240,s[52:55],s37 offen offset:0 sc1\nbuffer_load_dwordx4 a[4:7],v240,s[52:55],s37 offen offset:1024 sc1\nbuffer_load_dwordx4 a[8:11],v240,s[52:55],s37 offen offset:2048 sc1\nbuffer_load_dwordx4 a[12:15],v240,s[52:55],s37 offen offset:3072 sc1\ns_add_u32 s37,s37,4096\n" \
"buffer_load_dwordx4 a[16:19],v240,s[52:55],s37 offen offset:0 sc1\nbuffer_load_dwordx4 a[20:23],v240,s[52:55],s37 offen offset:1024 sc1\nbuffer_load_dwordx4 a[24:27],v240,s[52:55],s37 offen offset:2048 sc1\nbuffer_load_dwordx4 a[28:31],v240,s[52:55],s37 offen offset:3072 sc1\ns_add_u32 s37,s37,4096\nbuffer_load_dwordx4 a[32:35],v240,s[52:55],s37 offen offset:0 sc1\nbuffer_load_dwordx4 a[36:39],v240,s[52:55],s37 offen offset:1024 sc1\nbuffer_load_dwordx4 a[40:43],v240,s[52:55],s37 offen offset:2048 sc1\n" \
"buffer_load_dwordx4 a[44:47],v240,s[52:55],s37 offen offset:3072 sc1\ns_add_u32 s37,s37,4096\nbuffer_load_dwordx4 a[48:51],v240,s[52:55],s37 offen offset:0 sc1\nbuffer_load_dwordx4 a[52:55],v240,s[52:55],s37 offen offset:1024 sc1\nbuffer_load_dwordx4 a[56:59],v240,s[52:55],s37 offen offset:2048 sc1\nbuffer_load_dwordx4 a[60:63],v240,s[52:55],s37 offen offset:3072 sc1\ns_add_u32 s37,s37,4096\ns_waitcnt vmcnt(15)\n" \
"M v[128:131],a[0:3],a[128:131],0\ns_waitcnt vmcnt(14)\nM v[128:131],a[4:7],a[132:135],v[128:131]\ns_waitcnt vmcnt(13)\nM v[128:131],a[8:11],a[136:139],v[128:131]\ns_waitcnt vmcnt(12)\nM v[128:131],a[12:15],a[140:143],v[128:131]\nM v[132:135],a[0:3],a[144:147],0\n" \
"M v[132:135],a[4:7],a[148:151],v[132:135]\nM v[132:135],a[8:11],a[152:155],v[132:135]\nM v[132:135],a[12:15],a[156:159],v[132:135]\nM v[136:139],a[0:3],a[160:163],0\nM v[136:139],a[4:7],a[164:167],v[136:139]\nM v[136:139],a[8:11],a[168:171],v[136:139]\nM v[136:139],a[12:15],a[172:175],v[136:139]\nM v[140:143],a[0:3],a[176:179],0\n" \
"M v[140:143],a[4:7],a[180:183],v[140:143]\nM v[140:143],a[8:11],a[184:187],v[140:143]\nM v[140:143],a[12:15],a[188:191],v[140:143]\ns_waitcnt vmcnt(11)\nM v[144:147],a[16:19],a[128:131],0\ns_waitcnt vmcnt(10)\nM v[144:147],a[20:23],a[132:135],v[144:147]\ns_waitcnt vmcnt(9)\n" \
"M v[144:147],a[24:27],a[136:139],v[144:147]\ns_waitcnt vmcnt(8)\nM v[144:147],a[28:31],a[140:143],v[144:147]\nM v[148:151],a[16:19],a[144:147],0\nM v[148:151],a[20:23],a[148:151],v[148:151]\nM v[148:151],a[24:27],a[152:155],v[148:151]\nM v[148:151],a[28:31],a[156:159],v[148:151]\nM v[152:155],a[16:19],a[160:163],0\n" \
"M v[152:155],a[20:23],a[164:167],v[152:155]\nM v[152:155],a[24:27],a[168:171],v[152:155]\nM v[152:155],a[28:31],a[172:175],v[152:155]\nM v[156:159],a[16:19],a[176:179],0\nM v[156:159],a[20:23],a[180:183],v[156:159]\nM v[156:159],a[24:27],a[184:187],v[156:159]\nM v[156:159],a[28:31],a[188:191],v[156:159]\ns_waitcnt vmcnt(7)\n" \
"M v[160:163],a[32:35],a[128:131],0\ns_waitcnt vmcnt(6)\nM v[160:163],a[36:39],a[132:135],v[160:163]\ns_waitcnt vmcnt(5)\nM v[160:163],a[40:43],a[136:139],v[160:163]\ns_waitcnt vmcnt(4)\nM v[160:163],a[44:47],a[140:143],v[160:163]\nM v[164:167],a[32:35],a[144:147],0\n" \
"M v[164:167],a[36:39],a[148:151],v[164:167]\nM v[164:167],a[40:43],a[152:155],v[164:167]\nM v[164:167],a[44:47],a[156:159],v[164:167]\nM v[168:171],a[32:35],a[160:163],0\nM v[168:171],a[36:39],a[164:167],v[168:171]\nM v[168:171],a[40:43],a[168:171],v[168:171]\nM v[168:171],a[44:47],a[172:175],v[168:171]\nM v[172:175],a[32:35],a[176:179],0\n" \
"M v[172:175],a[36:39],a[180:183],v[172:175]\nM v[172:175],a[40:43],a[184:187],v[172:175]\nM v[172:175],a[44:47],a[188:191],v[172:175]\ns_waitcnt vmcnt(3)\nM v[176:179],a[48:51],a[128:131],0\ns_waitcnt vmcnt(2)\nM v[176:179],a[52:55],a[132:135],v[176:179]\ns_waitcnt vmcnt(1)\n" \
"M v[176:179],a[56:59],a[136:139],v[176:179]\ns_waitcnt vmcnt(0)\nM v[176:179],a[60:63],a[140:143],v[176:179]\nM v[180:183],a[48:51],a[144:147],0\nM v[180:183],a[52:55],a[148:151],v[180:183]\nM v[180:183],a[56:59],a[152:155],v[180:183]\nM v[180:183],a[60:63],a[156:159],v[180:183]\nM v[184:187],a[48:51],a[160:163],0\n" \
"M v[184:187],a[52:55],a[164:167],v[184:187]\nM v[184:187],a[56:59],a[168:171],v[184:187]\nM v[184:187],a[60:63],a[172:175],v[184:187]\nM v[188:191],a[48:51],a[176:179],0\nM v[188:191],a[52:55],a[180:183],v[188:191]\nM v[188:191],a[56:59],a[184:187],v[188:191]\nM v[188:191],a[60:63],a[188:191],v[188:191]\nv_cmp_le_i32 vcc,0,v225\n" \
"s_nop 1\nv_cndmask_b32 v128,v252,v128,vcc\nv_max_f32 v224,v224,v128\nv_cmp_le_i32 vcc,1,v225\ns_nop 1\nv_cndmask_b32 v129,v252,v129,vcc\nv_max_f32 v224,v224,v129\nv_cmp_le_i32 vcc,2,v225\n" \
"s_nop 1\nv_cndmask_b32 v130,v252,v130,vcc\nv_max_f32 v224,v224,v130\nv_cmp_le_i32 vcc,3,v225\ns_nop 1\nv_cndmask_b32 v131,v252,v131,vcc\nv_max_f32 v224,v224,v131\nv_cmp_le_i32 vcc,0,v229\n" \
"s_nop 1\nv_cndmask_b32 v132,v252,v132,vcc\nv_max_f32 v228,v228,v132\nv_cmp_le_i32 vcc,1,v229\ns_nop 1\nv_cndmask_b32 v133,v252,v133,vcc\nv_max_f32 v228,v228,v133\nv_cmp_le_i32 vcc,2,v229\n" \
"s_nop 1\nv_cndmask_b32 v134,v252,v134,vcc\nv_max_f32 v228,v228,v134\nv_cmp_le_i32 vcc,3,v229\ns_nop 1\nv_cndmask_b32 v135,v252,v135,vcc\nv_max_f32 v228,v228,v135\nv_cmp_le_i32 vcc,0,v233\n" \
"s_nop 1\nv_cndmask_b32 v136,v252,v136,vcc\nv_max_f32 v232,v232,v136\nv_cmp_le_i32 vcc,1,v233\ns_nop 1\nv_cndmask_b32 v137,v252,v137,vcc\nv_max_f32 v232,v232,v137\nv_cmp_le_i32 vcc,2,v233\n" \
"s_nop 1\nv_cndmask_b32 v138,v252,v138,vcc\nv_max_f32 v232,v232,v138\nv_cmp_le_i32 vcc,3,v233\ns_nop 1\nv_cndmask_b32 v139,v252,v139,vcc\nv_max_f32 v232,v232,v139\nv_cmp_le_i32 vcc,0,v237\n" \
"s_nop 1\nv_cndmask_b32 v140,v252,v140,vcc\nv_max_f32 v236,v236,v140\nv_cmp_le_i32 vcc,1,v237\ns_nop 1\nv_cndmask_b32 v141,v252,v141,vcc\nv_max_f32 v236,v236,v141\nv_cmp_le_i32 vcc,2,v237\n" \
"s_nop 1\nv_cndmask_b32 v142,v252,v142,vcc\nv_max_f32 v236,v236,v142\nv_cmp_le_i32 vcc,3,v237\ns_nop 1\nv_cndmask_b32 v143,v252,v143,vcc\nv_max_f32 v236,v236,v143\nv_cmp_le_i32 vcc,16,v225\n" \
"s_nop 1\nv_cndmask_b32 v144,v252,v144,vcc\nv_max_f32 v224,v224,v144\nv_cmp_le_i32 vcc,17,v225\ns_nop 1\nv_cndmask_b32 v145,v252,v145,vcc\nv_max_f32 v224,v224,v145\nv_cmp_le_i32 vcc,18,v225\n" \
"s_nop 1\nv_cndmask_b32 v146,v252,v146,vcc\nv_max_f32 v224,v224,v146\nv_cmp_le_i32 vcc,19,v225\ns_nop 1\nv_cndmask_b32 v147,v252,v147,vcc\nv_max_f32 v224,v224,v147\nv_cmp_le_i32 vcc,16,v229\n" \
"s_nop 1\nv_cndmask_b32 v148,v252,v148,vcc\nv_max_f32 v228,v228,v148\nv_cmp_le_i32 vcc,17,v229\ns_nop 1\nv_cndmask_b32 v149,v252,v149,vcc\nv_max_f32 v228,v228,v149\nv_cmp_le_i32 vcc,18,v229\n" \
"s_nop 1\nv_cndmask_b32 v150,v252,v150,vcc\nv_max_f32 v228,v228,v150\nv_cmp_le_i32 vcc,19,v229\ns_nop 1\nv_cndmask_b32 v151,v252,v151,vcc\nv_max_f32 v228,v228,v151\nv_cmp_le_i32 vcc,16,v233\n" \
"s_nop 1\nv_cndmask_b32 v152,v252,v152,vcc\nv_max_f32 v232,v232,v152\nv_cmp_le_i32 vcc,17,v233\ns_nop 1\nv_cndmask_b32 v153,v252,v153,vcc\nv_max_f32 v232,v232,v153\nv_cmp_le_i32 vcc,18,v233\n" \
"s_nop 1\nv_cndmask_b32 v154,v252,v154,vcc\nv_max_f32 v232,v232,v154\nv_cmp_le_i32 vcc,19,v233\ns_nop 1\nv_cndmask_b32 v155,v252,v155,vcc\nv_max_f32 v232,v232,v155\nv_cmp_le_i32 vcc,16,v237\n" \
"s_nop 1\nv_cndmask_b32 v156,v252,v156,vcc\nv_max_f32 v236,v236,v156\nv_cmp_le_i32 vcc,17,v237\ns_nop 1\nv_cndmask_b32 v157,v252,v157,vcc\nv_max_f32 v236,v236,v157\nv_cmp_le_i32 vcc,18,v237\n" \
"s_nop 1\nv_cndmask_b32 v158,v252,v158,vcc\nv_max_f32 v236,v236,v158\nv_cmp_le_i32 vcc,19,v237\ns_nop 1\nv_cndmask_b32 v159,v252,v159,vcc\nv_max_f32 v236,v236,v159\nv_cmp_le_i32 vcc,32,v225\n" \
"s_nop 1\nv_cndmask_b32 v160,v252,v160,vcc\nv_max_f32 v224,v224,v160\nv_cmp_le_i32 vcc,33,v225\ns_nop 1\nv_cndmask_b32 v161,v252,v161,vcc\nv_max_f32 v224,v224,v161\nv_cmp_le_i32 vcc,34,v225\n" \
"s_nop 1\nv_cndmask_b32 v162,v252,v162,vcc\nv_max_f32 v224,v224,v162\nv_cmp_le_i32 vcc,35,v225\ns_nop 1\nv_cndmask_b32 v163,v252,v163,vcc\nv_max_f32 v224,v224,v163\nv_cmp_le_i32 vcc,32,v229\n" \
"s_nop 1\nv_cndmask_b32 v164,v252,v164,vcc\nv_max_f32 v228,v228,v164\nv_cmp_le_i32 vcc,33,v229\ns_nop 1\nv_cndmask_b32 v165,v252,v165,vcc\nv_max_f32 v228,v228,v165\nv_cmp_le_i32 vcc,34,v229\n" \
"s_nop 1\nv_cndmask_b32 v166,v252,v166,vcc\nv_max_f32 v228,v228,v166\nv_cmp_le_i32 vcc,35,v229\ns_nop 1\nv_cndmask_b32 v167,v252,v167,vcc\nv_max_f32 v228,v228,v167\nv_cmp_le_i32 vcc,32,v233\n" \
"s_nop 1\nv_cndmask_b32 v168,v252,v168,vcc\nv_max_f32 v232,v232,v168\nv_cmp_le_i32 vcc,33,v233\ns_nop 1\nv_cndmask_b32 v169,v252,v169,vcc\nv_max_f32 v232,v232,v169\nv_cmp_le_i32 vcc,34,v233\n" \
"s_nop 1\nv_cndmask_b32 v170,v252,v170,vcc\nv_max_f32 v232,v232,v170\nv_cmp_le_i32 vcc,35,v233\ns_nop 1\nv_cndmask_b32 v171,v252,v171,vcc\nv_max_f32 v232,v232,v171\nv_cmp_le_i32 vcc,32,v237\n" \
"s_nop 1\nv_cndmask_b32 v172,v252,v172,vcc\nv_max_f32 v236,v236,v172\nv_cmp_le_i32 vcc,33,v237\ns_nop 1\nv_cndmask_b32 v173,v252,v173,vcc\nv_max_f32 v236,v236,v173\nv_cmp_le_i32 vcc,34,v237\n" \
"s_nop 1\nv_cndmask_b32 v174,v252,v174,vcc\nv_max_f32 v236,v236,v174\nv_cmp_le_i32 vcc,35,v237\ns_nop 1\nv_cndmask_b32 v175,v252,v175,vcc\nv_max_f32 v236,v236,v175\nv_cmp_le_i32 vcc,48,v225\n" \
"s_nop 1\nv_cndmask_b32 v176,v252,v176,vcc\nv_max_f32 v224,v224,v176\nv_cmp_le_i32 vcc,49,v225\ns_nop 1\nv_cndmask_b32 v177,v252,v177,vcc\nv_max_f32 v224,v224,v177\nv_cmp_le_i32 vcc,50,v225\n" \
"s_nop 1\nv_cndmask_b32 v178,v252,v178,vcc\nv_max_f32 v224,v224,v178\nv_cmp_le_i32 vcc,51,v225\ns_nop 1\nv_cndmask_b32 v179,v252,v179,vcc\nv_max_f32 v224,v224,v179\nv_cmp_le_i32 vcc,48,v229\n" \
"s_nop 1\nv_cndmask_b32 v180,v252,v180,vcc\nv_max_f32 v228,v228,v180\nv_cmp_le_i32 vcc,49,v229\ns_nop 1\nv_cndmask_b32 v181,v252,v181,vcc\nv_max_f32 v228,v228,v181\nv_cmp_le_i32 vcc,50,v229\n" \
"s_nop 1\nv_cndmask_b32 v182,v252,v182,vcc\nv_max_f32 v228,v228,v182\nv_cmp_le_i32 vcc,51,v229\ns_nop 1\nv_cndmask_b32 v183,v252,v183,vcc\nv_max_f32 v228,v228,v183\nv_cmp_le_i32 vcc,48,v233\n" \
"s_nop 1\nv_cndmask_b32 v184,v252,v184,vcc\nv_max_f32 v232,v232,v184\nv_cmp_le_i32 vcc,49,v233\ns_nop 1\nv_cndmask_b32 v185,v252,v185,vcc\nv_max_f32 v232,v232,v185\nv_cmp_le_i32 vcc,50,v233\n" \
"s_nop 1\nv_cndmask_b32 v186,v252,v186,vcc\nv_max_f32 v232,v232,v186\nv_cmp_le_i32 vcc,51,v233\ns_nop 1\nv_cndmask_b32 v187,v252,v187,vcc\nv_max_f32 v232,v232,v187\nv_cmp_le_i32 vcc,48,v237\n" \
"s_nop 1\nv_cndmask_b32 v188,v252,v188,vcc\nv_max_f32 v236,v236,v188\nv_cmp_le_i32 vcc,49,v237\ns_nop 1\nv_cndmask_b32 v189,v252,v189,vcc\nv_max_f32 v236,v236,v189\nv_cmp_le_i32 vcc,50,v237\n" \
"s_nop 1\nv_cndmask_b32 v190,v252,v190,vcc\nv_max_f32 v236,v236,v190\nv_cmp_le_i32 vcc,51,v237\ns_nop 1\nv_cndmask_b32 v191,v252,v191,vcc\nv_max_f32 v236,v236,v191\nv_subrev_u32 v225,64,v225\n" \
"v_subrev_u32 v229,64,v229\nv_subrev_u32 v233,64,v233\nv_subrev_u32 v237,64,v237\ns_cmp_lt_u32 s37,s38\ns_cbranch_scc1 .Lrl\ns_waitcnt vmcnt(0)\ns_barrier\nds_read_b128 a[0:3],v240 offset:0\n" \
"ds_read_b128 a[4:7],v240 offset:1024\nds_read_b128 a[8:11],v240 offset:2048\nds_read_b128 a[12:15],v240 offset:3072\nds_read_b128 a[16:19],v240 offset:4096\nds_read_b128 a[20:23],v240 offset:5120\nds_read_b128 a[24:27],v240 offset:6144\nds_read_b128 a[28:31],v240 offset:7168\nds_read_b128 a[32:35],v240 offset:8192\n" \
"ds_read_b128 a[36:39],v240 offset:9216\nds_read_b128 a[40:43],v240 offset:10240\nds_read_b128 a[44:47],v240 offset:11264\nds_read_b128 a[48:51],v240 offset:12288\nds_read_b128 a[52:55],v240 offset:13312\nds_read_b128 a[56:59],v240 offset:14336\nds_read_b128 a[60:63],v240 offset:15360\ns_cmp_eq_u32 s62,0\n" \
"s_cselect_b32 s36,0x8000,0\ns_add_u32 s36,s36,s64\ns_cmp_eq_u32 s36,s96\ns_cselect_b32 s72,0xc000,s98\ns_add_u32 s65,s36,s72\ns_cmp_eq_u32 s65,s96\ns_cselect_b32 s72,0xc000,s98\ns_add_u32 s66,s65,s72\n" \
"s_cmp_eq_u32 s66,s96\ns_cselect_b32 s72,0xc000,s98\ns_add_u32 s95,s66,s72\ns_add_u32 s97,s96,65536\ns_add_u32 s15,s64,s46\ns_cmp_gt_i32 s62,-1\ns_cselect_b32 s72,s98,0xfffe4000\ns_add_u32 s15,s15,s72\n" \
"s_mov_b32 s94,0xc000\ns_cmp_eq_u32 s62,7\ns_cselect_b32 s72,98304,0\ns_sub_u32 s97,s97,s72\nv_and_b32 v251,31,v249\nv_lshlrev_b32 v251,2,v251\ns_lshl_b32 s80,s63,7\ns_add_u32 s80,s80,86016\n" \
"v_add_u32 v250,0xc000,v241\ns_waitcnt lgkmcnt(0)\ns_branch .Lattn\n.Lfin:\n" \
""
constexpr size_t WS_DBG = (size_t)51 << 20;
constexpr unsigned LDS_BYTES = 163840;
struct Args {
    const u16 *x, *ckv, *w_q, *w_uk, *w_uv, *w_o, *w_r, *wg, *wu, *wd, *cvec, *w_mod;
    char* ws; u16* out;
};
typedef const __attribute__((address_space(4))) Args* kargs_t;
#define STAMPD(i) do {} while (0)
#define C8(p, a) p #a "0", p #a "1", p #a "2", p #a "3", p #a "4", p #a "5", p #a "6", p #a "7", p #a "8", p #a "9"
#define C100(p, a) C8(p, a##0), C8(p, a##1), C8(p, a##2), C8(p, a##3), C8(p, a##4), C8(p, a##5), C8(p, a##6), C8(p, a##7), C8(p, a##8), C8(p, a##9)
#define C10_99(p) C8(p, 1), C8(p, 2), C8(p, 3), C8(p, 4), C8(p, 5), C8(p, 6), C8(p, 7), C8(p, 8), C8(p, 9)
#define C200_249(p) C8(p, 20), C8(p, 21), C8(p, 22), C8(p, 23), C8(p, 24)
#define BODY_CLOB "memory", "vcc", "scc", "s0", "s1", "s2", "s3", "s8", "s9", C8("s", 1), C8("s", 2), "s30", "s31", "s33", "s34", "s35", "s36", "s37", "s38", "s39", C8("s", 4), C8("s", 5), C8("s", 6), C8("s", 7), C8("s", 8), C8("s", 9), \
    C8("v", ), C10_99("v"), C100("v", 1), C200_249("v"), "v250", "v251", "v252", "v253", "v254", C8("a", ), C10_99("a"), C100("a", 1), C200_249("a"), "a250", "a251", "a252", "a253", "a254", "a255"

__global__ __launch_bounds__(256, 1) void k_all(const Args a_) {
    kargs_t ap = (kargs_t)__builtin_amdgcn_kernarg_segment_ptr(); unsigned bid = blockIdx.x, wid = rfl(threadIdx.x >> 6), sv_;
    asm volatile(BODY_ASM : "+{s[4:5]}"(ap), "+{s6}"(bid), "+{s7}"(wid), "={v255}"(sv_) :: BODY_CLOB);
    asm volatile(".Lcode_e:");
}
extern "C" void kernel_launch(void* const* d_in, const int* in_sizes, int n_in, void* d_out, int out_size, void* d_ws, size_t ws_size, hipStream_t stream) {
    Args a;
    a.x = (const u16*)d_in[0]; a.ckv = (const u16*)d_in[1]; a.w_q = (const u16*)d_in[2]; a.w_uk = (const u16*)d_in[3]; a.w_uv = (const u16*)d_in[4]; a.w_o = (const u16*)d_in[5];
    a.w_r = (const u16*)d_in[7]; a.wg = (const u16*)d_in[8]; a.wu = (const u16*)d_in[9]; a.wd = (const u16*)d_in[10]; a.cvec = (const u16*)d_in[11]; a.w_mod = (const u16*)d_in[12];
    a.ws = (char*)d_ws;
    a.out = (u16*)d_out;
    (void)hipFuncSetAttribute((const void*)k_all, hipFuncAttributeMaxDynamicSharedMemorySize, LDS_BYTES);
    k_all<<<256, 256, LDS_BYTES, stream>>>(a);
}
```

```cpp
#include <hip/hip_runtime.h>
#include <hip/hip_bf16.h>
#include <stdint.h>
typedef unsigned short u16;
#define DI __device__ __forceinline__
DI unsigned lane_id() { unsigned l; asm volatile("v_mbcnt_lo_u32_b32 %0, -1, 0\n\tv_mbcnt_hi_u32_b32 %0, -1, %0" : "=v"(l)); return l; }
DI unsigned rfl(unsigned x) { return __builtin_amdgcn_readfirstlane(x); }
#define BODY_ASM ".macro M a,b,c,d\nv_mfma_f32_16x16x32_bf16 \\a,\\b,\\c,\\d\n.endm\n" \
".Lcode_b:\ns_load_dwordx16 s[8:23],s[4:5],0x0\ns_load_dwordx8 s[24:31],s[4:5],0x40\ns_load_dwordx2 s[34:35],s[4:5],0x60\nv_mbcnt_lo_u32_b32 v249,-1,0\nv_mbcnt_hi_u32_b32 v249,-1,v249\ns_bfe_u32 s61,s6,0x20003\ns_lshl_b32 s64,s7,12\n" \
"s_and_b32 s60,s6,7\ns_lshr_b32 s62,s6,5\ns_lshl2_add_u32 s63,s60,s61\nv_lshlrev_b32 v240,4,v249\nv_lshrrev_b32 v0,3,v249\nv_and_b32 v2,7,v249\nv_bfe_u32 v6,v249,2,3\nv_lshrrev_b32 v7,5,v249\n" \
"v_and_b32 v8,48,v240\nv_lshlrev_b32 v6,13,v6\nv_lshl_or_b32 v7,v7,6,v8\nv_or_b32 v6,0x1000,v6\nv_or_b32 v243,v6,v7\nv_add_u32 v244,0xfffffc80,v243\nv_xor_b32 v6,32,v243\nv_add_u32 v245,63488,v6\n" \
"v_add_u32 v246,62592,v6\ns_lshl_b32 s68,s7,17\ns_lshl_b32 s70,s63,8\ns_add_u32 s68,s68,s70\ns_mov_b32 s42,4198400\ns_mov_b32 s43,0x20000\nv_lshrrev_b32 v6,4,v249\nv_xor_b32 v7,v6,v2\n" \
"v_lshlrev_b32 v7,4,v7\nv_lshl_or_b32 v224,v0,10,v7\nv_xor_b32 v7,64,v224\nv_add_u32 v225,7168,v7\nv_add_u32 v226,14336,v224\nv_add_u32 v227,21504,v7\ns_lshl_b32 s70,s62,1\ns_sub_u32 s71,s7,s70\n" \
"s_add_u32 s71,s71,30\ns_sub_u32 s72,s70,s7\ns_add_u32 s72,s72,3\ns_cmp_lt_u32 s7,2\ns_cselect_b32 s74,s71,s72\ns_lshl_b32 s66,s74,16\ns_mul_i32 s65,s7,24576\ns_mov_b32 s38,2097152\n" \
"s_mov_b32 s39,0x20000\ns_xor_b32 s94,s61,s62\ns_and_b32 s94,s94,1\ns_lshl_b32 s99,s94,19\ns_or_b32 s99,s99,s68\ns_waitcnt lgkmcnt(0)\ns_sub_u32 s40,s14,0x1000\ns_subb_u32 s41,s15,0\n" \
"s_mov_b64 s[36:37],s[10:11]\ns_add_u32 m0,s64,147456\ns_add_u32 s69,s68,0\nbuffer_load_dwordx4 v243,s[40:43],s69 offen offset:0 lds\nbuffer_load_dwordx4 v244,s[40:43],s69 offen offset:1024 lds\nbuffer_load_dwordx4 v245,s[40:43],s69 offen offset:2048 lds\nbuffer_load_dwordx4 v246,s[40:43],s69 offen offset:3072 lds\ns_add_u32 m0,s65,0\n" \
"s_xor_b32 s67,s66,0\nbuffer_load_dwordx4 v224,s[36:39],s67 offen offset:0 lds\nbuffer_load_dwordx4 v225,s[36:39],s67 offen offset:1024 lds\nbuffer_load_dwordx4 v226,s[36:39],s67 offen offset:2048 lds\nbuffer_load_dwordx4 v227,s[36:39],s67 offen offset:3072 lds\ns_add_u32 m0,s65,4096\ns_xor_b32 s67,s66,32768\nbuffer_load_dwordx4 v224,s[36:39],s67 offen offset:0 lds\n" \
"buffer_load_dwordx4 v225,s[36:39],s67 offen offset:1024 lds\nbuffer_load_dwordx4 v226,s[36:39],s67 offen offset:2048 lds\nbuffer_load_dwordx4 v227,s[36:39],s67 offen offset:3072 lds\ns_cmp_lt_u32 s6,192\ns_cselect_b32 s70,s6,0\ns_mul_i32 s71,s70,171\ns_lshr_b32 s71,s71,13\ns_mul_i32 s72,s71,48\n" \
"s_sub_u32 s70,s70,s72\ns_lshl2_add_u32 s72,s71,s7\ns_lshl_b32 s72,s72,5\nv_lshl_add_u32 v1,v0,2,s72\nv_mul_u32_u24 v3,6144,v1\nv_lshl_add_u32 v3,v2,4,v3\ns_lshl_b32 s72,s70,7\ns_add_u32 s72,s72,3072\n" \
"v_add_u32 v3,s72,v3\nv_add_u32 v4,12288,v3\nv_lshlrev_b32 v1,1,v1\nglobal_load_dwordx2 v[168:169],v1,s[28:29]\nglobal_load_dwordx4 v[152:155],v3,s[30:31] offset:-3072\nglobal_load_dwordx4 v[156:159],v3,s[30:31] offset:3072\nglobal_load_dwordx4 v[160:163],v4,s[30:31] offset:-3072\nglobal_load_dwordx4 v[164:167],v4,s[30:31] offset:3072\n" \
"s_mul_i32 s71,s71,12288\ns_lshl_b32 s70,s70,8\ns_add_u32 s79,s70,s71\ns_add_u32 s79,s79,786432\ns_cmp_eq_u32 s7,0\ns_cselect_b32 s75,0,0x8000000\ns_cmp_lt_u32 s6,192\ns_cselect_b32 s70,s75,0x8000000\n" \
"s_or_b32 s79,s79,s70\ns_mov_b64 s[52:53],s[34:35]\ns_mov_b32 s54,71303168\ns_mov_b32 s55,0x20000\ns_mov_b32 s70,94208\nbuffer_load_dwordx2 v[230:231],off,s[52:55],s70 sc1\ns_getpc_b64 s[56:57]\n.Lpc0:\n" \
"s_and_b32 s57,s57,0xffff\ns_mov_b32 s58,.Lcode_e-.Lpc0\ns_mov_b32 s59,0x20000\ns_lshr_b32 s70,s6,3\nv_and_b32 v251,1,v249\ns_lshl2_add_u32 s78,s70,s7\nv_lshlrev_b32 v251,14,v251\ns_lshl_b32 s78,s78,7\n" \
"buffer_load_dword v247,v251,s[56:59],s78 offen\nv_and_b32 v34,15,v249\nv_lshrrev_b32 v35,1,v34\nv_xor_b32 v35,v35,v6\nv_lshlrev_b32 v35,4,v35\nv_lshl_or_b32 v35,v34,7,v35\nv_add_u32 v228,s65,v35\nv_xor_b32 v229,64,v228\n" \
"v_bfe_u32 v34,v249,2,2\nv_and_b32 v35,3,v249\nv_lshlrev_b32 v35,3,v35\nv_lshl_or_b32 v35,v34,6,v35\nv_lshl_or_b32 v35,v6,11,v35\nv_and_b32 v34,16,v249\nv_lshl_or_b32 v35,v34,1,v35\nv_add_u32 v241,98304,v35\n" \
"v_xor_b32 v242,32,v241\ns_mov_b32 s73,0x400000\nv_mov_b32 v252,0xff800000\ns_mov_b32 s82,0x05040100\ns_mov_b32 s83,0x07060302\nv_lshlrev_b32 v190,5,v2\nv_lshl_or_b32 v190,v6,3,v190\ns_lshl_b32 s72,s7,8\n" \
"s_add_u32 s72,s72,114688\nv_add_u32 v190,s72,v190\ns_lshl_b32 s72,s7,3\ns_add_u32 s72,s72,115712\nv_lshl_add_u32 v191,v2,5,s72\nv_lshlrev_b32 v251,5,v2\nv_add_u32 v251,115712,v251\nv_lshlrev_b32 v250,2,v249\n" \
"v_add_u32 v250,114688,v250\ns_lshl_b32 s70,s62,7\ns_xor_b32 s71,s63,12\ns_lshl3_add_u32 s71,s71,s70\ns_lshl_b32 s72,s63,3\ns_sub_u32 s72,s72,s70\ns_add_u32 s72,s72,1792\ns_cmp_lt_u32 s63,16\n" \
"s_cselect_b32 s71,s71,s72\ns_lshl_b32 s71,s71,10\ns_lshl_b32 s72,s7,7\ns_add_u32 s71,s71,s72\nv_lshlrev_b32 v5,10,v2\nv_lshl_add_u32 v5,v0,4,v5\nv_add_u32 v5,s71,v5\ns_lshl_b32 s70,s6,11\n" \
"s_cmp_lt_u32 s6,112\ns_cselect_b32 s70,s70,0x8000000\ns_lshl_b32 s71,s7,9\ns_add_u32 s70,s70,s71\ns_add_u32 s80,s70,98304\nv_mov_b32 v28,-1\nv_mov_b32 v29,-1\nv_lshlrev_b32 v30,3,v249\n" \
"s_lshl_b32 s70,s6,7\ns_cmp_lt_u32 s6,224\ns_cselect_b32 s71,65536,0x40000000\ns_cmp_lt_u32 s6,128\ns_cselect_b32 s72,556928,0x40000000\nv_mov_b32 v31,s71\nv_mov_b32 v32,s72\nv_cmp_lt_u32 vcc,31,v249\n" \
"s_or_b32 s70,s70,s75\nv_mov_b32 v33,0\nv_cndmask_b32 v31,v31,v32,vcc\nv_lshl_add_u32 v31,v249,2,v31\ns_add_u32 m0,s64,98304\ns_add_u32 s69,s68,524288\nbuffer_load_dwordx4 v243,s[40:43],s69 offen offset:0 lds\nbuffer_load_dwordx4 v244,s[40:43],s69 offen offset:1024 lds\n" \
"buffer_load_dwordx4 v245,s[40:43],s69 offen offset:2048 lds\nbuffer_load_dwordx4 v246,s[40:43],s69 offen offset:3072 lds\ns_add_u32 m0,s65,8192\ns_xor_b32 s67,s66,128\nbuffer_load_dwordx4 v224,s[36:39],s67 offen offset:0 lds\nbuffer_load_dwordx4 v225,s[36:39],s67 offen offset:1024 lds\nbuffer_load_dwordx4 v226,s[36:39],s67 offen offset:2048 lds\nbuffer_load_dwordx4 v227,s[36:39],s67 offen offset:3072 lds\n" \
"s_add_u32 m0,s65,12288\ns_xor_b32 s67,s66,32896\nbuffer_load_dwordx4 v224,s[36:39],s67 offen offset:0 lds\nbuffer_load_dwordx4 v225,s[36:39],s67 offen offset:1024 lds\nbuffer_load_dwordx4 v226,s[36:39],s67 offen offset:2048 lds\nbuffer_load_dwordx4 v227,s[36:39],s67 offen offset:3072 lds\nglobal_load_dwordx4 v[128:131],v5,s[8:9]\nglobal_load_dwordx4 v[132:135],v5,s[8:9] offset:512\n" \
"buffer_store_dwordx2 v[28:29],v30,s[52:55],s80 offen sc1\nbuffer_store_dword v33,v31,s[52:55],s70 offen sc1\ns_waitcnt vmcnt(23)\ns_barrier\ns_add_u32 m0,s64,131072\ns_add_u32 s69,s68,1048576\nds_read_b128 a[0:3],v228 offset:0\nds_read_b128 a[4:7],v228 offset:2048\n" \
"ds_read_b128 a[8:11],v228 offset:4096\nds_read_b128 a[12:15],v228 offset:6144\nds_read_b128 a[16:19],v229 offset:0\nds_read_b128 a[20:23],v229 offset:2048\nds_read_b128 a[24:27],v229 offset:4096\nds_read_b128 a[28:31],v229 offset:6144\nds_read_b64_tr_b16 v[192:193],v241 offset:49152\nds_read_b64_tr_b16 v[194:195],v241 offset:49408\n" \
"ds_read_b64_tr_b16 v[196:197],v241 offset:57344\nds_read_b64_tr_b16 v[198:199],v241 offset:57600\nds_read_b64_tr_b16 v[200:201],v242 offset:49152\nds_read_b64_tr_b16 v[202:203],v242 offset:49408\nds_read_b64_tr_b16 v[204:205],v242 offset:57344\nds_read_b64_tr_b16 v[206:207],v242 offset:57600\nds_read_b64_tr_b16 v[208:209],v241 offset:49664\nds_read_b64_tr_b16 v[210:211],v241 offset:49920\n" \
"ds_read_b64_tr_b16 v[212:213],v241 offset:57856\nds_read_b64_tr_b16 v[214:215],v241 offset:58112\nds_read_b64_tr_b16 v[216:217],v242 offset:49664\nds_read_b64_tr_b16 v[218:219],v242 offset:49920\nds_read_b64_tr_b16 v[220:221],v242 offset:57856\nds_read_b64_tr_b16 v[222:223],v242 offset:58112\ns_waitcnt vmcnt(22)\nv_lshlrev_b32 v170,16,v168\n" \
"v_and_b32 v171,0xffff0000,v168\nv_lshlrev_b32 v172,16,v169\nv_and_b32 v173,0xffff0000,v169\nv_mul_f32 v184,0xbfb8aa3b,v170\nv_mul_f32 v185,0xbfb8aa3b,v171\nv_mul_f32 v186,0xbfb8aa3b,v172\nv_mul_f32 v187,0xbfb8aa3b,v173\nv_exp_f32 v184,v184\n" \
"v_exp_f32 v185,v185\nv_exp_f32 v186,v186\nv_exp_f32 v187,v187\nv_add_f32 v184,1.0,v184\nv_add_f32 v185,1.0,v185\nv_add_f32 v186,1.0,v186\nv_add_f32 v187,1.0,v187\nv_rcp_f32 v184,v184\n" \
"v_rcp_f32 v185,v185\nv_rcp_f32 v186,v186\nv_rcp_f32 v187,v187\nv_mul_f32 v170,v170,v184\nv_mul_f32 v171,v171,v185\nv_mul_f32 v172,v172,v186\nv_mul_f32 v173,v173,v187\nv_cvt_pk_bf16_f32 v170,v170,v171\n" \
"v_cvt_pk_bf16_f32 v171,v172,v173\ns_waitcnt lgkmcnt(5)\nM v[0:3],v[192:195],a[0:3],0\nbuffer_load_dwordx4 v243,s[40:43],s69 offen offset:0 lds\nM v[0:3],v[196:199],a[16:19],v[0:3]\nv_bfe_u32 v253,v249,1,2\ns_lshl2_add_u32 s76,s62,s7\nv_and_b32 v248,1,v249\n" \
"v_lshlrev_b32 v253,13,v253\ns_mul_i32 s76,s76,32768\ns_lshl_b32 s77,s63,8\nM v[4:7],v[192:195],a[4:7],0\nv_lshl_or_b32 v253,v248,7,v253\ns_add_u32 s76,s76,s77\nv_and_b32 v254,7,v249\nv_add_u32 v253,s76,v253\n" \
"M v[4:7],v[196:199],a[20:23],v[4:7]\nv_and_b32 v234,7,v249\ns_lshl3_add_u32 s72,s6,s75\nM v[64:67],v[192:195],a[8:11],0\nv_lshlrev_b32 v234,11,v234\ns_add_u32 s72,s72,573440\nM v[64:67],v[196:199],a[24:27],v[64:67]\nbuffer_load_dwordx4 v244,s[40:43],s69 offen offset:1024 lds\n" \
"M v[68:71],v[192:195],a[12:15],0\ns_waitcnt vmcnt(22)\nv_perm_b32 v182,v156,v152,s82\nv_perm_b32 v183,v156,v152,s83\nv_perm_b32 v184,v157,v153,s82\nv_perm_b32 v185,v157,v153,s83\nv_perm_b32 v186,v158,v154,s82\nv_perm_b32 v187,v158,v154,s83\n" \
"v_perm_b32 v188,v159,v155,s82\nv_perm_b32 v189,v159,v155,s83\nv_dot2_f32_bf16 v174,v182,v170,0\nv_dot2_f32_bf16 v175,v183,v170,0\nv_dot2_f32_bf16 v176,v184,v170,0\nv_dot2_f32_bf16 v177,v185,v170,0\nv_dot2_f32_bf16 v178,v186,v170,0\nv_dot2_f32_bf16 v179,v187,v170,0\n" \
"v_dot2_f32_bf16 v180,v188,v170,0\nv_dot2_f32_bf16 v181,v189,v170,0\ns_waitcnt vmcnt(20)\nv_perm_b32 v182,v164,v160,s82\nv_perm_b32 v183,v164,v160,s83\nv_perm_b32 v184,v165,v161,s82\nv_perm_b32 v185,v165,v161,s83\nv_perm_b32 v186,v166,v162,s82\n" \
"v_perm_b32 v187,v166,v162,s83\nv_perm_b32 v188,v167,v163,s82\nv_perm_b32 v189,v167,v163,s83\nv_dot2_f32_bf16 v174,v182,v171,v174\nv_dot2_f32_bf16 v175,v183,v171,v175\nv_dot2_f32_bf16 v176,v184,v171,v176\nv_dot2_f32_bf16 v177,v185,v171,v177\nv_dot2_f32_bf16 v178,v186,v171,v178\n" \
"v_dot2_f32_bf16 v179,v187,v171,v179\nv_dot2_f32_bf16 v180,v188,v171,v180\nv_dot2_f32_bf16 v181,v189,v171,v181\nv_permlane32_swap_b32 v174,v178\nv_permlane32_swap_b32 v175,v179\nv_permlane32_swap_b32 v176,v180\nv_permlane32_swap_b32 v177,v181\nv_add_f32 v174,v174,v178\n" \
"v_add_f32 v175,v175,v179\nv_add_f32 v176,v176,v180\nv_add_f32 v177,v177,v181\nds_read_b64_tr_b16 v[192:193],v241 offset:50176\nv_permlane16_swap_b32 v174,v176\nv_permlane16_swap_b32 v175,v177\nv_add_f32 v174,v174,v176\nv_add_f32 v175,v175,v177\n" \
"ds_read_b64_tr_b16 v[194:195],v241 offset:50432\nv_add_f32_dpp v174,v174,v174 row_ror:8 row_mask:0xf bank_mask:0xf\nv_add_f32_dpp v175,v175,v175 row_ror:8 row_mask:0xf bank_mask:0xf\nds_write_b64 v190,v[174:175]\nM v[68:71],v[196:199],a[28:31],v[68:71]\nds_read_b64_tr_b16 v[196:197],v241 offset:58368\nds_read_b64_tr_b16 v[198:199],v241 offset:58624\nM v[8:11],v[200:203],a[0:3],0\n" \
"s_waitcnt vmcnt(18)\nv_readfirstlane_b32 s92,v230\nv_readfirstlane_b32 s93,v231\nM v[8:11],v[204:207],a[16:19],v[8:11]\ns_add_u32 s70,s92,1\ns_addc_u32 s71,s93,0\nM v[12:15],v[200:203],a[4:7],0\nbuffer_load_dwordx4 v245,s[40:43],s69 offen offset:2048 lds\n" \
"M v[12:15],v[204:207],a[20:23],v[12:15]\ns_add_u32 s92,s92,0x7f4a7c15\ns_addc_u32 s93,s93,0x9e3779b9\nM v[72:75],v[200:203],a[8:11],0\nv_mov_b32 v230,s92\nv_mov_b32 v231,s93\nM v[72:75],v[204:207],a[24:27],v[72:75]\ns_lshl2_add_u32 s80,s60,s7\n" \
"s_lshl_b32 s80,s80,9\nM v[76:79],v[200:203],a[12:15],0\nds_read_b64_tr_b16 v[200:201],v242 offset:50176\nds_read_b64_tr_b16 v[202:203],v242 offset:50432\nM v[76:79],v[204:207],a[28:31],v[76:79]\nbuffer_load_dwordx4 v246,s[40:43],s69 offen offset:3072 lds\nds_read_b64_tr_b16 v[204:205],v242 offset:58368\nds_read_b64_tr_b16 v[206:207],v242 offset:58624\n" \
"M v[16:19],v[208:211],a[0:3],0\ns_add_u32 s80,s80,573440\ns_waitcnt lgkmcnt(6)\nM v[16:19],v[212:215],a[16:19],v[16:19]\ns_add_u32 m0,s65,16384\ns_xor_b32 s67,s66,256\nM v[20:23],v[208:211],a[4:7],0\nv_mov_b32 v232,s70\n" \
"v_mov_b32 v233,s71\nM v[20:23],v[212:215],a[20:23],v[20:23]\ns_lshr_b32 s70,s64,2\ns_add_u32 s71,s70,786432\nM v[80:83],v[208:211],a[8:11],0\nbuffer_load_dwordx4 v224,s[36:39],s67 offen offset:0 lds\nM v[80:83],v[212:215],a[24:27],v[80:83]\nv_lshlrev_b32 v254,14,v254\n" \
"s_add_u32 s78,s78,32768\nM v[84:87],v[208:211],a[12:15],0\nds_read_b64_tr_b16 v[208:209],v241 offset:50688\nds_read_b64_tr_b16 v[210:211],v241 offset:50944\nM v[84:87],v[212:215],a[28:31],v[84:87]\nds_read_b64_tr_b16 v[212:213],v241 offset:58880\nds_read_b64_tr_b16 v[214:215],v241 offset:59136\nM v[24:27],v[216:219],a[0:3],0\n" \
"s_mov_b32 s98,0x4000\ns_mov_b32 s0,28672\nM v[24:27],v[220:223],a[16:19],v[24:27]\nbuffer_load_dwordx4 v225,s[36:39],s67 offen offset:1024 lds\nM v[28:31],v[216:219],a[4:7],0\ns_add_u32 s10,s61,1\ns_add_u32 s11,s61,2\nM v[28:31],v[220:223],a[20:23],v[28:31]\n" \
"s_add_u32 s14,s61,3\ns_and_b32 s10,s10,3\nM v[88:91],v[216:219],a[8:11],0\ns_and_b32 s11,s11,3\ns_and_b32 s14,s14,3\nM v[88:91],v[220:223],a[24:27],v[88:91]\ns_mov_b32 s47,0x20000\ns_mov_b32 s51,0x20000\n" \
"M v[92:95],v[216:219],a[12:15],0\nbuffer_load_dwordx4 v226,s[36:39],s67 offen offset:2048 lds\nds_read_b64_tr_b16 v[216:217],v242 offset:50688\nds_read_b64_tr_b16 v[218:219],v242 offset:50944\nM v[92:95],v[220:223],a[28:31],v[92:95]\nds_read_b64_tr_b16 v[220:221],v242 offset:58880\nds_read_b64_tr_b16 v[222:223],v242 offset:59136\nM v[32:35],v[192:195],a[0:3],0\n" \
"s_lshl_b32 s10,s10,13\ns_waitcnt lgkmcnt(6)\nM v[32:35],v[196:199],a[16:19],v[32:35]\ns_lshl_b32 s11,s11,13\ns_lshl_b32 s14,s14,13\nM v[36:39],v[192:195],a[4:7],0\ns_mov_b32 s82,0xf0000\ns_mov_b32 s83,0xfff00ff\n" \
"M v[36:39],v[196:199],a[20:23],v[36:39]\nbuffer_load_dwordx4 v227,s[36:39],s67 offen offset:3072 lds\nM v[96:99],v[192:195],a[8:11],0\ns_mov_b32 s84,0x1f0001\ns_mov_b32 s85,0x1fff01ff\nM v[96:99],v[196:199],a[24:27],v[96:99]\ns_add_u32 m0,s65,20480\ns_xor_b32 s67,s66,33024\n" \
"M v[100:103],v[192:195],a[12:15],0\ns_mov_b32 s86,0x3f0003\ns_mov_b32 s87,0x3fff03ff\nM v[100:103],v[196:199],a[28:31],v[100:103]\ns_mov_b32 s88,0x7f0007\ns_mov_b32 s89,0x7fff07ff\nM v[40:43],v[200:203],a[0:3],0\nbuffer_load_dwordx4 v224,s[36:39],s67 offen offset:0 lds\n" \
"s_waitcnt vmcnt(11)\nv_lshlrev_b32 v136,16,v128\nv_and_b32 v137,0xffff0000,v128\nv_lshlrev_b32 v138,16,v129\nv_and_b32 v139,0xffff0000,v129\nv_lshlrev_b32 v140,16,v130\nv_and_b32 v141,0xffff0000,v130\nv_lshlrev_b32 v142,16,v131\n" \
"v_and_b32 v143,0xffff0000,v131\nv_lshlrev_b32 v144,16,v132\nv_and_b32 v145,0xffff0000,v132\nv_lshlrev_b32 v146,16,v133\nv_and_b32 v147,0xffff0000,v133\nv_lshlrev_b32 v148,16,v134\nv_and_b32 v149,0xffff0000,v134\nv_lshlrev_b32 v150,16,v135\n" \
"v_and_b32 v151,0xffff0000,v135\nv_add_f32 v168,v136,v140\nv_add_f32 v128,v144,v148\nv_add_f32 v169,v137,v141\nv_add_f32 v129,v145,v149\nv_add_f32 v170,v138,v142\nv_add_f32 v130,v146,v150\nv_add_f32 v171,v139,v143\n" \
"v_add_f32 v131,v147,v151\nv_add_f32 v168,v168,v128\nv_add_f32 v169,v169,v129\nv_add_f32 v170,v170,v130\nv_add_f32 v171,v171,v131\nv_add_f32 v168,v168,v169\nv_add_f32 v170,v170,v171\nv_add_f32 v168,v168,v170\n" \
"M v[40:43],v[204:207],a[16:19],v[40:43]\ns_mov_b64 s[90:91],-1\nv_add_f32_dpp v168,v168,v168 row_ror:8 row_mask:0xf bank_mask:0xf\nv_mov_b32 v170,v168\nM v[44:47],v[200:203],a[4:7],0\ns_mov_b32 s28,0x5f800000\nv_permlane16_swap_b32 v168,v170\nv_add_f32 v168,v168,v170\n" \
"v_mov_b32 v170,v168\ns_nop 1\nv_permlane32_swap_b32 v168,v170\nv_add_f32 v168,v168,v170\nv_mul_f32 v168,0x3c000000,v168\nv_sub_f32 v152,v136,v168\nv_sub_f32 v153,v137,v168\nv_sub_f32 v154,v138,v168\n" \
"v_sub_f32 v155,v139,v168\nv_sub_f32 v156,v140,v168\nv_sub_f32 v157,v141,v168\nv_sub_f32 v158,v142,v168\nv_sub_f32 v159,v143,v168\nv_mul_f32 v128,v152,v152\nv_mul_f32 v129,v153,v153\nv_mul_f32 v130,v154,v154\n" \
"v_mul_f32 v131,v155,v155\nv_sub_f32 v160,v144,v168\nv_sub_f32 v161,v145,v168\nv_sub_f32 v162,v146,v168\nv_sub_f32 v163,v147,v168\nv_fmac_f32 v128,v156,v156\nv_fmac_f32 v129,v157,v157\nv_fmac_f32 v130,v158,v158\n" \
"v_fmac_f32 v131,v159,v159\nv_sub_f32 v164,v148,v168\nv_sub_f32 v165,v149,v168\nv_sub_f32 v166,v150,v168\nv_sub_f32 v167,v151,v168\nv_fmac_f32 v128,v160,v160\nv_fmac_f32 v129,v161,v161\nv_fmac_f32 v130,v162,v162\n" \
"v_fmac_f32 v131,v163,v163\nv_fmac_f32 v128,v164,v164\nv_fmac_f32 v129,v165,v165\nv_fmac_f32 v130,v166,v166\nv_fmac_f32 v131,v167,v167\nv_add_f32 v128,v128,v129\nv_add_f32 v130,v130,v131\nv_add_f32 v169,v128,v130\n" \
"s_nop 1\nv_add_f32_dpp v169,v169,v169 row_ror:8 row_mask:0xf bank_mask:0xf\nv_mov_b32 v170,v169\ns_nop 1\nv_permlane16_swap_b32 v169,v170\nv_add_f32 v169,v169,v170\nv_mov_b32 v170,v169\ns_nop 1\n" \
"v_permlane32_swap_b32 v169,v170\nv_add_f32 v169,v169,v170\nds_write_b64 v191,v[168:169]\nM v[44:47],v[204:207],a[20:23],v[44:47]\nv_and_b32 v169,3,v249\nv_mul_u32_u24 v169,12288,v169\nM v[104:107],v[200:203],a[8:11],0\nv_and_b32 v170,28,v249\n" \
"v_add_u32 v169,v169,v170\nM v[104:107],v[204:207],a[24:27],v[104:107]\nbuffer_load_dwordx4 v225,s[36:39],s67 offen offset:1024 lds\nM v[108:111],v[200:203],a[12:15],0\nv_lshlrev_b32 v170,3,v170\nM v[108:111],v[204:207],a[28:31],v[108:111]\nM v[48:51],v[208:211],a[0:3],0\ns_waitcnt lgkmcnt(1)\n" \
"M v[48:51],v[212:215],a[16:19],v[48:51]\nM v[52:55],v[208:211],a[4:7],0\nbuffer_load_dwordx4 v226,s[36:39],s67 offen offset:2048 lds\nM v[52:55],v[212:215],a[20:23],v[52:55]\nM v[112:115],v[208:211],a[8:11],0\ns_barrier\nds_read_b128 a[32:35],v228 offset:8192\nds_read_b128 a[36:39],v228 offset:10240\n" \
"ds_read_b128 a[40:43],v228 offset:12288\nds_read_b128 a[44:47],v228 offset:14336\nds_read_b128 a[48:51],v229 offset:8192\nds_read_b128 a[52:55],v229 offset:10240\nds_read_b128 a[56:59],v229 offset:12288\nds_read_b128 a[60:63],v229 offset:14336\nds_read_b32 v184,v250 offset:0\nds_read_b32 v185,v250 offset:256\n" \
"ds_read_b32 v186,v250 offset:512\nds_read_b32 v187,v250 offset:768\ns_waitcnt lgkmcnt(2)\nv_add_f32 v184,v184,v185\ns_waitcnt lgkmcnt(0)\nv_add_f32 v186,v186,v187\nv_lshlrev_b32 v185,2,v249\nv_add_f32 v184,v184,v186\n" \
"buffer_store_dword v184,v185,s[52:55],s79 offen sc1\nds_read_b64_tr_b16 v[192:193],v241 offset:0\nds_read_b64_tr_b16 v[194:195],v241 offset:256\nds_read_b64_tr_b16 v[196:197],v241 offset:8192\nds_read_b64_tr_b16 v[198:199],v241 offset:8448\nds_read_b64_tr_b16 v[200:201],v242 offset:0\nds_read_b64_tr_b16 v[202:203],v242 offset:256\nds_read_b64_tr_b16 v[204:205],v242 offset:8192\n" \
"ds_read_b64_tr_b16 v[206:207],v242 offset:8448\nM v[112:115],v[212:215],a[24:27],v[112:115]\nv_bfe_u32 v250,v249,3,2\nM v[116:119],v[208:211],a[12:15],0\nds_read_b64_tr_b16 v[208:209],v241 offset:512\nds_read_b64_tr_b16 v[210:211],v241 offset:768\nM v[116:119],v[212:215],a[28:31],v[116:119]\nbuffer_load_dwordx4 v227,s[36:39],s67 offen offset:3072 lds\n" \
"ds_read_b64_tr_b16 v[212:213],v241 offset:8704\nds_read_b64_tr_b16 v[214:215],v241 offset:8960\nM v[56:59],v[216:219],a[0:3],0\nM v[56:59],v[220:223],a[16:19],v[56:59]\ns_add_u32 m0,s65,0\ns_xor_b32 s67,s66,384\nM v[60:63],v[216:219],a[4:7],0\nM v[60:63],v[220:223],a[20:23],v[60:63]\n" \
"M v[120:123],v[216:219],a[8:11],0\nbuffer_load_dwordx4 v224,s[36:39],s67 offen offset:0 lds\nM v[120:123],v[220:223],a[24:27],v[120:123]\nM v[124:127],v[216:219],a[12:15],0\nds_read_b64_tr_b16 v[216:217],v242 offset:512\nds_read_b64_tr_b16 v[218:219],v242 offset:768\nM v[124:127],v[220:223],a[28:31],v[124:127]\nds_read_b64_tr_b16 v[220:221],v242 offset:8704\n" \
"ds_read_b64_tr_b16 v[222:223],v242 offset:8960\ns_waitcnt lgkmcnt(11)\nM v[0:3],v[192:195],a[32:35],v[0:3]\nM v[0:3],v[196:199],a[48:51],v[0:3]\nbuffer_load_dwordx4 v225,s[36:39],s67 offen offset:1024 lds\nM v[4:7],v[192:195],a[36:39],v[4:7]\nM v[4:7],v[196:199],a[52:55],v[4:7]\nM v[64:67],v[192:195],a[40:43],v[64:67]\n" \
"M v[64:67],v[196:199],a[56:59],v[64:67]\nM v[68:71],v[192:195],a[44:47],v[68:71]\nbuffer_load_dwordx4 v226,s[36:39],s67 offen offset:2048 lds\nds_read_b64_tr_b16 v[192:193],v241 offset:1024\nds_read_b64_tr_b16 v[194:195],v241 offset:1280\nM v[68:71],v[196:199],a[60:63],v[68:71]\nds_read_b64_tr_b16 v[196:197],v241 offset:9216\nds_read_b64_tr_b16 v[198:199],v241 offset:9472\n" \
"s_waitcnt lgkmcnt(8)\nM v[8:11],v[200:203],a[32:35],v[8:11]\nM v[8:11],v[204:207],a[48:51],v[8:11]\nM v[12:15],v[200:203],a[36:39],v[12:15]\nM v[12:15],v[204:207],a[52:55],v[12:15]\nbuffer_load_dwordx4 v227,s[36:39],s67 offen offset:3072 lds\nM v[72:75],v[200:203],a[40:43],v[72:75]\nM v[72:75],v[204:207],a[56:59],v[72:75]\n" \
"s_add_u32 m0,s65,4096\ns_xor_b32 s67,s66,33152\nM v[76:79],v[200:203],a[44:47],v[76:79]\nds_read_b64_tr_b16 v[200:201],v242 offset:1024\nds_read_b64_tr_b16 v[202:203],v242 offset:1280\nM v[76:79],v[204:207],a[60:63],v[76:79]\nds_read_b64_tr_b16 v[204:205],v242 offset:9216\nds_read_b64_tr_b16 v[206:207],v242 offset:9472\n" \
"M v[16:19],v[208:211],a[32:35],v[16:19]\nbuffer_load_dwordx4 v224,s[36:39],s67 offen offset:0 lds\nM v[16:19],v[212:215],a[48:51],v[16:19]\nM v[20:23],v[208:211],a[36:39],v[20:23]\nM v[20:23],v[212:215],a[52:55],v[20:23]\nM v[80:83],v[208:211],a[40:43],v[80:83]\nM v[80:83],v[212:215],a[56:59],v[80:83]\nbuffer_load_dwordx4 v225,s[36:39],s67 offen offset:1024 lds\n" \
"M v[84:87],v[208:211],a[44:47],v[84:87]\nds_read_b64_tr_b16 v[208:209],v241 offset:1536\nds_read_b64_tr_b16 v[210:211],v241 offset:1792\nM v[84:87],v[212:215],a[60:63],v[84:87]\nds_read_b64_tr_b16 v[212:213],v241 offset:9728\nds_read_b64_tr_b16 v[214:215],v241 offset:9984\ns_waitcnt lgkmcnt(8)\nM v[24:27],v[216:219],a[32:35],v[24:27]\n" \
"M v[24:27],v[220:223],a[48:51],v[24:27]\nM v[28:31],v[216:219],a[36:39],v[28:31]\nbuffer_load_dwordx4 v226,s[36:39],s67 offen offset:2048 lds\nM v[28:31],v[220:223],a[52:55],v[28:31]\nM v[88:91],v[216:219],a[40:43],v[88:91]\nM v[88:91],v[220:223],a[56:59],v[88:91]\nM v[92:95],v[216:219],a[44:47],v[92:95]\nds_read_b64_tr_b16 v[216:217],v242 offset:1536\n" \
"ds_read_b64_tr_b16 v[218:219],v242 offset:1792\nM v[92:95],v[220:223],a[60:63],v[92:95]\nbuffer_load_dwordx4 v227,s[36:39],s67 offen offset:3072 lds\nds_read_b64_tr_b16 v[220:221],v242 offset:9728\nds_read_b64_tr_b16 v[222:223],v242 offset:9984\nM v[32:35],v[192:195],a[32:35],v[32:35]\nM v[32:35],v[196:199],a[48:51],v[32:35]\ns_add_u32 m0,s64,147456\n" \
"s_add_u32 s69,s68,1572864\nM v[36:39],v[192:195],a[36:39],v[36:39]\nM v[36:39],v[196:199],a[52:55],v[36:39]\nM v[96:99],v[192:195],a[40:43],v[96:99]\nbuffer_load_dwordx4 v243,s[40:43],s69 offen offset:0 lds\nM v[96:99],v[196:199],a[56:59],v[96:99]\nM v[100:103],v[192:195],a[44:47],v[100:103]\nM v[100:103],v[196:199],a[60:63],v[100:103]\n" \
"s_waitcnt lgkmcnt(4)\nM v[40:43],v[200:203],a[32:35],v[40:43]\nM v[40:43],v[204:207],a[48:51],v[40:43]\nbuffer_load_dwordx4 v244,s[40:43],s69 offen offset:1024 lds\nM v[44:47],v[200:203],a[36:39],v[44:47]\nM v[44:47],v[204:207],a[52:55],v[44:47]\nM v[104:107],v[200:203],a[40:43],v[104:107]\nM v[104:107],v[204:207],a[56:59],v[104:107]\n" \
"M v[108:111],v[200:203],a[44:47],v[108:111]\nbuffer_load_dwordx4 v245,s[40:43],s69 offen offset:2048 lds\nM v[108:111],v[204:207],a[60:63],v[108:111]\nM v[48:51],v[208:211],a[32:35],v[48:51]\nM v[48:51],v[212:215],a[48:51],v[48:51]\nM v[52:55],v[208:211],a[36:39],v[52:55]\nM v[52:55],v[212:215],a[52:55],v[52:55]\nbuffer_load_dwordx4 v246,s[40:43],s69 offen offset:3072 lds\n" \
"M v[112:115],v[208:211],a[40:43],v[112:115]\ns_waitcnt vmcnt(12) lgkmcnt(0)\ns_barrier\nds_read_b128 a[64:67],v228 offset:16384\nds_read_b128 a[68:71],v228 offset:18432\nds_read_b128 a[72:75],v228 offset:20480\nds_read_b128 a[76:79],v228 offset:22528\nds_read_b128 a[80:83],v229 offset:16384\n" \
"ds_read_b128 a[84:87],v229 offset:18432\nds_read_b128 a[88:91],v229 offset:20480\nds_read_b128 a[92:95],v229 offset:22528\nM v[112:115],v[212:215],a[56:59],v[112:115]\ns_add_u32 m0,s65,8192\ns_xor_b32 s67,s66,512\nds_read_b64_tr_b16 v[192:193],v241 offset:32768\nds_read_b64_tr_b16 v[194:195],v241 offset:33024\n" \
"M v[116:119],v[208:211],a[44:47],v[116:119]\nbuffer_store_dwordx2 v[230:231],v234,s[52:55],s72 offen sc1\nds_read_b64_tr_b16 v[196:197],v241 offset:40960\nds_read_b64_tr_b16 v[198:199],v241 offset:41216\nM v[116:119],v[212:215],a[60:63],v[116:119]\ns_cmp_eq_u32 s6,0\ns_cselect_b32 s72,s75,0x8000000\nM v[56:59],v[216:219],a[32:35],v[56:59]\n" \
"buffer_load_dwordx4 v224,s[36:39],s67 offen offset:0 lds\nM v[56:59],v[220:223],a[48:51],v[56:59]\ns_add_u32 s72,s72,94208\nM v[60:63],v[216:219],a[36:39],v[60:63]\nM v[60:63],v[220:223],a[52:55],v[60:63]\nds_read_b64_tr_b16 v[200:201],v242 offset:32768\nds_read_b64_tr_b16 v[202:203],v242 offset:33024\nds_read_b64_tr_b16 v[204:205],v242 offset:40960\n" \
"ds_read_b64_tr_b16 v[206:207],v242 offset:41216\nds_read_b64_tr_b16 v[208:209],v241 offset:33280\nds_read_b64_tr_b16 v[210:211],v241 offset:33536\nds_read_b64_tr_b16 v[212:213],v241 offset:41472\nds_read_b64_tr_b16 v[214:215],v241 offset:41728\nM v[120:123],v[216:219],a[40:43],v[120:123]\nM v[120:123],v[220:223],a[56:59],v[120:123]\nbuffer_load_dwordx4 v225,s[36:39],s67 offen offset:1024 lds\n" \
"M v[124:127],v[216:219],a[44:47],v[124:127]\nds_read_b64_tr_b16 v[216:217],v242 offset:33280\nds_read_b64_tr_b16 v[218:219],v242 offset:33536\nM v[124:127],v[220:223],a[60:63],v[124:127]\nds_read_b64_tr_b16 v[220:221],v242 offset:41472\nds_read_b64_tr_b16 v[222:223],v242 offset:41728\ns_waitcnt lgkmcnt(14)\nM v[0:3],v[192:195],a[64:67],v[0:3]\n" \
"s_waitcnt lgkmcnt(12)\nM v[0:3],v[196:199],a[80:83],v[0:3]\nM v[4:7],v[192:195],a[68:71],v[4:7]\nbuffer_load_dwordx4 v226,s[36:39],s67 offen offset:2048 lds\nM v[4:7],v[196:199],a[84:87],v[4:7]\nds_read_b128 v[156:159],v251\nM v[64:67],v[192:195],a[72:75],v[64:67]\nds_read_b128 v[160:163],v251 offset:16\n" \
"v_lshrrev_b32 v251,3,v249\nM v[64:67],v[196:199],a[88:91],v[64:67]\nM v[68:71],v[192:195],a[76:79],v[68:71]\nds_read_b64_tr_b16 v[192:193],v241 offset:33792\nds_read_b64_tr_b16 v[194:195],v241 offset:34048\nM v[68:71],v[196:199],a[92:95],v[68:71]\nbuffer_load_dwordx4 v227,s[36:39],s67 offen offset:3072 lds\nds_read_b64_tr_b16 v[196:197],v241 offset:41984\n" \
"ds_read_b64_tr_b16 v[198:199],v241 offset:42240\ns_waitcnt lgkmcnt(10)\nM v[8:11],v[200:203],a[64:67],v[8:11]\nM v[8:11],v[204:207],a[80:83],v[8:11]\ns_add_u32 m0,s65,12288\ns_xor_b32 s67,s66,33280\nM v[12:15],v[200:203],a[68:71],v[12:15]\nM v[12:15],v[204:207],a[84:87],v[12:15]\n" \
"M v[72:75],v[200:203],a[72:75],v[72:75]\nbuffer_load_dwordx4 v224,s[36:39],s67 offen offset:0 lds\nM v[72:75],v[204:207],a[88:91],v[72:75]\ns_waitcnt lgkmcnt(5)\nv_add_f32 v164,v156,v158\nM v[76:79],v[200:203],a[76:79],v[76:79]\nds_read_b64_tr_b16 v[200:201],v242 offset:33792\nds_read_b64_tr_b16 v[202:203],v242 offset:34048\n" \
"M v[76:79],v[204:207],a[92:95],v[76:79]\nds_read_b64_tr_b16 v[204:205],v242 offset:41984\nds_read_b64_tr_b16 v[206:207],v242 offset:42240\nM v[16:19],v[208:211],a[64:67],v[16:19]\ns_waitcnt lgkmcnt(8)\nv_add_f32 v165,v160,v162\nM v[16:19],v[212:215],a[80:83],v[16:19]\nbuffer_load_dwordx4 v225,s[36:39],s67 offen offset:1024 lds\n" \
"M v[20:23],v[208:211],a[68:71],v[20:23]\nv_add_f32 v164,v164,v165\nv_lshlrev_b32 v165,3,v249\nM v[20:23],v[212:215],a[84:87],v[20:23]\nv_mul_f32 v164,0x3e800000,v164\nv_sub_f32 v156,v156,v164\nM v[80:83],v[208:211],a[72:75],v[80:83]\nv_sub_f32 v158,v158,v164\n" \
"v_mul_f32 v156,v156,v156\nM v[80:83],v[212:215],a[88:91],v[80:83]\nv_sub_f32 v160,v160,v164\nv_fmac_f32 v156,v158,v158\nM v[84:87],v[208:211],a[76:79],v[84:87]\nbuffer_load_dwordx4 v226,s[36:39],s67 offen offset:2048 lds\nds_read_b64_tr_b16 v[208:209],v241 offset:34304\nds_read_b64_tr_b16 v[210:211],v241 offset:34560\n" \
"M v[84:87],v[212:215],a[92:95],v[84:87]\nds_read_b64_tr_b16 v[212:213],v241 offset:42496\nds_read_b64_tr_b16 v[214:215],v241 offset:42752\nM v[24:27],v[216:219],a[64:67],v[24:27]\nv_add_f32 v157,v157,v159\nv_add_f32 v161,v161,v163\nM v[24:27],v[220:223],a[80:83],v[24:27]\nv_sub_f32 v162,v162,v164\n" \
"v_fmac_f32 v156,v160,v160\nM v[28:31],v[216:219],a[68:71],v[28:31]\nglobal_load_dword v247,v253,s[16:17]\nM v[28:31],v[220:223],a[84:87],v[28:31]\nbuffer_load_dwordx4 v227,s[36:39],s67 offen offset:3072 lds\nM v[88:91],v[216:219],a[72:75],v[88:91]\nv_add_u32 v253,0x100000,v253\nv_add_f32 v157,v157,v161\n" \
"M v[88:91],v[220:223],a[88:91],v[88:91]\ns_add_u32 m0,s64,98304\ns_add_u32 s69,s68,2097152\nM v[92:95],v[216:219],a[76:79],v[92:95]\nds_read_b64_tr_b16 v[216:217],v242 offset:34304\nds_read_b64_tr_b16 v[218:219],v242 offset:34560\nM v[92:95],v[220:223],a[92:95],v[92:95]\nds_read_b64_tr_b16 v[220:221],v242 offset:42496\n" \
"ds_read_b64_tr_b16 v[222:223],v242 offset:42752\ns_waitcnt lgkmcnt(8)\nM v[32:35],v[192:195],a[64:67],v[32:35]\nbuffer_load_dwordx4 v243,s[40:43],s69 offen offset:0 lds\nM v[32:35],v[196:199],a[80:83],v[32:35]\nv_fmac_f32 v156,v162,v162\nv_fmac_f32 v157,0x43000000,v156\nM v[36:39],v[192:195],a[68:71],v[36:39]\n" \
"v_mul_f32 v157,0x3b000000,v157\nv_cvt_pk_bf16_f32 v157,0,v157\nM v[36:39],v[196:199],a[84:87],v[36:39]\nv_add_f32 v157,0x3727c5ac,v157\nv_cvt_pk_bf16_f32 v157,0,v157\nM v[96:99],v[192:195],a[72:75],v[96:99]\nv_sqrt_f32 v157,v157\nM v[96:99],v[196:199],a[88:91],v[96:99]\n" \
"buffer_load_dwordx4 v244,s[40:43],s69 offen offset:1024 lds\nM v[100:103],v[192:195],a[76:79],v[100:103]\nv_cvt_pk_bf16_f32 v164,0,v164\nv_cvt_pk_bf16_f32 v157,0,v157\nM v[100:103],v[196:199],a[92:95],v[100:103]\nv_rcp_f32 v157,v157\nM v[40:43],v[200:203],a[64:67],v[40:43]\nv_sub_f32 v136,v136,v164\n" \
"v_sub_f32 v137,v137,v164\nM v[40:43],v[204:207],a[80:83],v[40:43]\nv_sub_f32 v138,v138,v164\nv_sub_f32 v139,v139,v164\nM v[44:47],v[200:203],a[68:71],v[44:47]\nbuffer_load_dwordx4 v245,s[40:43],s69 offen offset:2048 lds\nM v[44:47],v[204:207],a[84:87],v[44:47]\nv_sub_f32 v140,v140,v164\n" \
"v_sub_f32 v141,v141,v164\nM v[104:107],v[200:203],a[72:75],v[104:107]\nv_sub_f32 v142,v142,v164\nv_sub_f32 v143,v143,v164\nM v[104:107],v[204:207],a[88:91],v[104:107]\nv_sub_f32 v144,v144,v164\nv_sub_f32 v145,v145,v164\nM v[108:111],v[200:203],a[76:79],v[108:111]\n" \
"v_sub_f32 v146,v146,v164\nv_sub_f32 v147,v147,v164\nM v[108:111],v[204:207],a[92:95],v[108:111]\nbuffer_load_dwordx4 v246,s[40:43],s69 offen offset:3072 lds\ns_waitcnt lgkmcnt(0)\nM v[48:51],v[208:211],a[64:67],v[48:51]\nv_sub_f32 v148,v148,v164\nv_sub_f32 v149,v149,v164\n" \
"M v[48:51],v[212:215],a[80:83],v[48:51]\ns_add_u32 m0,s65,16384\ns_xor_b32 s67,s66,640\nM v[52:55],v[208:211],a[68:71],v[52:55]\nv_sub_f32 v150,v150,v164\nv_sub_f32 v151,v151,v164\nM v[52:55],v[212:215],a[84:87],v[52:55]\nv_cvt_pk_bf16_f32 v136,0,v136\n" \
"v_cvt_pk_bf16_f32 v137,0,v137\nM v[112:115],v[208:211],a[72:75],v[112:115]\nbuffer_load_dwordx4 v224,s[36:39],s67 offen offset:0 lds\ns_waitcnt vmcnt(15)\ns_barrier\nds_read_b128 a[96:99],v228 offset:0\nds_read_b128 a[100:103],v228 offset:2048\nds_read_b128 a[104:107],v228 offset:4096\n" \
"ds_read_b128 a[108:111],v228 offset:6144\nds_read_b128 a[112:115],v229 offset:0\nds_read_b128 a[116:119],v229 offset:2048\nds_read_b128 a[120:123],v229 offset:4096\nds_read_b128 a[124:127],v229 offset:6144\nM v[112:115],v[212:215],a[88:91],v[112:115]\nds_read_b64_tr_b16 v[192:193],v241 offset:49152\nds_read_b64_tr_b16 v[194:195],v241 offset:49408\n" \
"M v[116:119],v[208:211],a[76:79],v[116:119]\nds_read_b64_tr_b16 v[196:197],v241 offset:57344\nds_read_b64_tr_b16 v[198:199],v241 offset:57600\nM v[116:119],v[212:215],a[92:95],v[116:119]\nv_cvt_pk_bf16_f32 v138,0,v138\nv_cvt_pk_bf16_f32 v139,0,v139\nM v[56:59],v[216:219],a[64:67],v[56:59]\nv_cvt_pk_bf16_f32 v140,0,v140\n" \
"v_cvt_pk_bf16_f32 v141,0,v141\nM v[56:59],v[220:223],a[80:83],v[56:59]\nbuffer_load_dwordx4 v225,s[36:39],s67 offen offset:1024 lds\nM v[60:63],v[216:219],a[68:71],v[60:63]\nv_cvt_pk_bf16_f32 v142,0,v142\nv_cvt_pk_bf16_f32 v143,0,v143\nM v[60:63],v[220:223],a[84:87],v[60:63]\nv_cvt_pk_bf16_f32 v144,0,v144\n" \
"v_cvt_pk_bf16_f32 v145,0,v145\nM v[120:123],v[216:219],a[72:75],v[120:123]\nv_cvt_pk_bf16_f32 v146,0,v146\nv_cvt_pk_bf16_f32 v147,0,v147\nM v[120:123],v[220:223],a[88:91],v[120:123]\nds_read_b64_tr_b16 v[200:201],v242 offset:49152\nds_read_b64_tr_b16 v[202:203],v242 offset:49408\nM v[124:127],v[216:219],a[76:79],v[124:127]\n" \
"buffer_load_dwordx4 v226,s[36:39],s67 offen offset:2048 lds\nds_read_b64_tr_b16 v[204:205],v242 offset:57344\nds_read_b64_tr_b16 v[206:207],v242 offset:57600\nM v[124:127],v[220:223],a[92:95],v[124:127]\nds_read_b64_tr_b16 v[208:209],v241 offset:49664\ns_waitcnt lgkmcnt(7)\nM v[0:3],v[192:195],a[96:99],v[0:3]\nds_read_b64_tr_b16 v[210:211],v241 offset:49920\n" \
"s_waitcnt lgkmcnt(6)\nM v[0:3],v[196:199],a[112:115],v[0:3]\nds_read_b64_tr_b16 v[212:213],v241 offset:57856\nds_read_b64_tr_b16 v[214:215],v241 offset:58112\nM v[4:7],v[192:195],a[100:103],v[4:7]\nglobal_load_dword v247,v253,s[16:17]\nM v[4:7],v[196:199],a[116:119],v[4:7]\nbuffer_load_dwordx4 v227,s[36:39],s67 offen offset:3072 lds\n" \
"M v[64:67],v[192:195],a[104:107],v[64:67]\nds_read_b64_tr_b16 v[216:217],v242 offset:49664\nds_read_b64_tr_b16 v[218:219],v242 offset:49920\nM v[64:67],v[196:199],a[120:123],v[64:67]\ns_add_u32 m0,s65,20480\ns_xor_b32 s67,s66,33408\nM v[68:71],v[192:195],a[108:111],v[68:71]\nds_read_b64_tr_b16 v[220:221],v242 offset:57856\n" \
"ds_read_b64_tr_b16 v[222:223],v242 offset:58112\nM v[68:71],v[196:199],a[124:127],v[68:71]\nds_read_b64_tr_b16 v[192:193],v241 offset:50176\ns_waitcnt lgkmcnt(11)\nM v[8:11],v[200:203],a[96:99],v[8:11]\nbuffer_load_dwordx4 v224,s[36:39],s67 offen offset:0 lds\ns_waitcnt lgkmcnt(9)\nM v[8:11],v[204:207],a[112:115],v[8:11]\n" \
"ds_read_b64_tr_b16 v[194:195],v241 offset:50432\nds_read_b64_tr_b16 v[196:197],v241 offset:58368\nM v[12:15],v[200:203],a[100:103],v[12:15]\nds_read_b64_tr_b16 v[198:199],v241 offset:58624\nv_add_u32 v253,0x100000,v253\nM v[12:15],v[204:207],a[116:119],v[12:15]\nv_cvt_pk_bf16_f32 v148,0,v148\nv_cvt_pk_bf16_f32 v149,0,v149\n" \
"M v[72:75],v[200:203],a[104:107],v[72:75]\nv_cvt_pk_bf16_f32 v150,0,v150\nv_cvt_pk_bf16_f32 v151,0,v151\nM v[72:75],v[204:207],a[120:123],v[72:75]\nbuffer_load_dwordx4 v225,s[36:39],s67 offen offset:1024 lds\nM v[76:79],v[200:203],a[108:111],v[76:79]\nds_read_b64_tr_b16 v[200:201],v242 offset:50176\nds_read_b64_tr_b16 v[202:203],v242 offset:50432\n" \
"M v[76:79],v[204:207],a[124:127],v[76:79]\nds_read_b64_tr_b16 v[204:205],v242 offset:58368\ns_waitcnt lgkmcnt(9)\nM v[16:19],v[208:211],a[96:99],v[16:19]\nds_read_b64_tr_b16 v[206:207],v242 offset:58624\nv_mul_f32 v136,v136,v157\nM v[16:19],v[212:215],a[112:115],v[16:19]\nv_mul_f32 v137,v137,v157\n" \
"v_mul_f32 v138,v138,v157\nM v[20:23],v[208:211],a[100:103],v[20:23]\nbuffer_load_dwordx4 v226,s[36:39],s67 offen offset:2048 lds\nM v[20:23],v[212:215],a[116:119],v[20:23]\nv_mul_f32 v139,v139,v157\nv_mul_f32 v140,v140,v157\nM v[80:83],v[208:211],a[104:107],v[80:83]\nv_mul_f32 v141,v141,v157\n" \
"v_mul_f32 v142,v142,v157\nM v[80:83],v[212:215],a[120:123],v[80:83]\nv_mul_f32 v143,v143,v157\nv_mul_f32 v144,v144,v157\nM v[84:87],v[208:211],a[108:111],v[84:87]\nds_read_b64_tr_b16 v[208:209],v241 offset:50688\nds_read_b64_tr_b16 v[210:211],v241 offset:50944\nM v[84:87],v[212:215],a[124:127],v[84:87]\n" \
"buffer_load_dwordx4 v227,s[36:39],s67 offen offset:3072 lds\nM v[24:27],v[216:219],a[96:99],v[24:27]\nds_read_b64_tr_b16 v[212:213],v241 offset:58880\ns_waitcnt lgkmcnt(6)\nM v[24:27],v[220:223],a[112:115],v[24:27]\ns_add_u32 m0,s64,114688\ns_add_u32 s69,s68,2621440\nM v[28:31],v[216:219],a[100:103],v[28:31]\n" \
"ds_read_b64_tr_b16 v[214:215],v241 offset:59136\nv_mul_f32 v145,v145,v157\nM v[28:31],v[220:223],a[116:119],v[28:31]\nv_mul_f32 v146,v146,v157\nv_mul_f32 v147,v147,v157\nM v[88:91],v[216:219],a[104:107],v[88:91]\nbuffer_load_dwordx4 v243,s[40:43],s69 offen offset:0 lds\nM v[88:91],v[220:223],a[120:123],v[88:91]\n" \
"v_mul_f32 v148,v148,v157\nv_mul_f32 v149,v149,v157\nM v[92:95],v[216:219],a[108:111],v[92:95]\nds_read_b64_tr_b16 v[216:217],v242 offset:50688\nds_read_b64_tr_b16 v[218:219],v242 offset:50944\nM v[92:95],v[220:223],a[124:127],v[92:95]\nds_read_b64_tr_b16 v[220:221],v242 offset:58880\nds_read_b64_tr_b16 v[222:223],v242 offset:59136\n" \
"M v[32:35],v[192:195],a[96:99],v[32:35]\nbuffer_load_dwordx2 v[166:167],v165,s[52:55],s80 offen sc1\nM v[32:35],v[196:199],a[112:115],v[32:35]\nbuffer_load_dwordx4 v244,s[40:43],s69 offen offset:1024 lds\nM v[36:39],v[192:195],a[100:103],v[36:39]\nv_mul_f32 v150,v150,v157\nv_mul_f32 v151,v151,v157\nM v[36:39],v[196:199],a[116:119],v[36:39]\n" \
"v_cvt_pk_bf16_f32 v144,0,v144\nv_cvt_pk_bf16_f32 v145,0,v145\nM v[96:99],v[192:195],a[104:107],v[96:99]\nv_cvt_pk_bf16_f32 v136,0,v136\nv_cvt_pk_bf16_f32 v137,0,v137\nM v[96:99],v[196:199],a[120:123],v[96:99]\nv_cvt_pk_bf16_f32 v138,0,v138\nv_cvt_pk_bf16_f32 v139,0,v139\n" \
"M v[100:103],v[192:195],a[108:111],v[100:103]\nbuffer_load_dwordx4 v245,s[40:43],s69 offen offset:2048 lds\nM v[100:103],v[196:199],a[124:127],v[100:103]\nv_cvt_pk_bf16_f32 v140,0,v140\ns_waitcnt lgkmcnt(4)\nM v[40:43],v[200:203],a[96:99],v[40:43]\nv_cvt_pk_bf16_f32 v141,0,v141\nv_cvt_pk_bf16_f32 v142,0,v142\n" \
"M v[40:43],v[204:207],a[112:115],v[40:43]\nv_cvt_pk_bf16_f32 v143,0,v143\nv_cvt_pk_bf16_f32 v146,0,v146\nM v[44:47],v[200:203],a[100:103],v[44:47]\nglobal_load_dword v247,v253,s[16:17]\nM v[44:47],v[204:207],a[116:119],v[44:47]\nbuffer_load_dwordx4 v246,s[40:43],s69 offen offset:3072 lds\nM v[104:107],v[200:203],a[104:107],v[104:107]\n" \
"v_add_u32 v253,0x100000,v253\nv_cvt_pk_bf16_f32 v147,0,v147\nM v[104:107],v[204:207],a[120:123],v[104:107]\ns_add_u32 m0,s65,0\ns_xor_b32 s67,s66,768\nM v[108:111],v[200:203],a[108:111],v[108:111]\nv_cvt_pk_bf16_f32 v148,0,v148\nv_cvt_pk_bf16_f32 v149,0,v149\n" \
"M v[108:111],v[204:207],a[124:127],v[108:111]\nv_cvt_pk_bf16_f32 v150,0,v150\nv_cvt_pk_bf16_f32 v151,0,v151\nM v[48:51],v[208:211],a[96:99],v[48:51]\nbuffer_load_dwordx4 v224,s[36:39],s67 offen offset:0 lds\nM v[48:51],v[212:215],a[112:115],v[48:51]\nM v[52:55],v[208:211],a[100:103],v[52:55]\nM v[52:55],v[212:215],a[116:119],v[52:55]\n" \
"M v[112:115],v[208:211],a[104:107],v[112:115]\ns_waitcnt vmcnt(16) lgkmcnt(0)\ns_barrier\nM v[112:115],v[212:215],a[120:123],v[112:115]\nbuffer_load_dwordx4 v225,s[36:39],s67 offen offset:1024 lds\nds_read_b128 a[128:131],v228 offset:8192\nds_read_b64_tr_b16 v[192:193],v241 offset:0\nds_read_b64_tr_b16 v[194:195],v241 offset:256\n" \
"M v[116:119],v[208:211],a[108:111],v[116:119]\nds_read_b128 a[144:147],v229 offset:8192\nds_read_b64_tr_b16 v[196:197],v241 offset:8192\nds_read_b64_tr_b16 v[198:199],v241 offset:8448\nM v[116:119],v[212:215],a[124:127],v[116:119]\nds_read_b128 a[132:135],v228 offset:10240\nM v[56:59],v[216:219],a[96:99],v[56:59]\nds_read_b128 a[148:151],v229 offset:10240\n" \
"M v[56:59],v[220:223],a[112:115],v[56:59]\nds_read_b128 a[136:139],v228 offset:12288\nM v[60:63],v[216:219],a[100:103],v[60:63]\nbuffer_load_dwordx4 v226,s[36:39],s67 offen offset:2048 lds\nds_read_b128 a[152:155],v229 offset:12288\nM v[60:63],v[220:223],a[116:119],v[60:63]\nds_read_b128 a[140:143],v228 offset:14336\nM v[120:123],v[216:219],a[104:107],v[120:123]\n" \
"ds_read_b128 a[156:159],v229 offset:14336\nM v[120:123],v[220:223],a[120:123],v[120:123]\nds_read_b64_tr_b16 v[200:201],v242 offset:0\nds_read_b64_tr_b16 v[202:203],v242 offset:256\nM v[124:127],v[216:219],a[108:111],v[124:127]\nds_read_b64_tr_b16 v[204:205],v242 offset:8192\nds_read_b64_tr_b16 v[206:207],v242 offset:8448\nM v[124:127],v[220:223],a[124:127],v[124:127]\n" \
"buffer_load_dwordx4 v227,s[36:39],s67 offen offset:3072 lds\ns_waitcnt lgkmcnt(13)\nM v[0:3],v[192:195],a[128:131],v[0:3]\nds_read_b64_tr_b16 v[208:209],v241 offset:512\ns_waitcnt lgkmcnt(11)\nM v[0:3],v[196:199],a[144:147],v[0:3]\ns_add_u32 m0,s65,4096\ns_xor_b32 s67,s66,33536\n" \
"s_waitcnt lgkmcnt(10)\nM v[4:7],v[192:195],a[132:135],v[4:7]\nds_read_b64_tr_b16 v[210:211],v241 offset:768\ns_waitcnt lgkmcnt(10)\nM v[4:7],v[196:199],a[148:151],v[4:7]\nds_read_b64_tr_b16 v[212:213],v241 offset:8704\ns_waitcnt lgkmcnt(10)\nM v[64:67],v[192:195],a[136:139],v[64:67]\n" \
"buffer_load_dwordx4 v224,s[36:39],s67 offen offset:0 lds\ns_waitcnt lgkmcnt(9)\nM v[64:67],v[196:199],a[152:155],v[64:67]\nds_read_b64_tr_b16 v[214:215],v241 offset:8960\ns_waitcnt lgkmcnt(9)\nM v[68:71],v[192:195],a[140:143],v[68:71]\nds_read_b64_tr_b16 v[216:217],v242 offset:512\ns_waitcnt lgkmcnt(9)\n" \
"M v[68:71],v[196:199],a[156:159],v[68:71]\nds_read_b64_tr_b16 v[218:219],v242 offset:768\ns_waitcnt lgkmcnt(8)\nM v[8:11],v[200:203],a[128:131],v[8:11]\nds_read_b64_tr_b16 v[220:221],v242 offset:8704\ns_waitcnt lgkmcnt(7)\nM v[8:11],v[204:207],a[144:147],v[8:11]\nbuffer_load_dwordx4 v225,s[36:39],s67 offen offset:1024 lds\n" \
"M v[12:15],v[200:203],a[132:135],v[12:15]\nds_read_b64_tr_b16 v[222:223],v242 offset:8960\nds_read_b64_tr_b16 v[192:193],v241 offset:1024\nM v[12:15],v[204:207],a[148:151],v[12:15]\nds_read_b64_tr_b16 v[194:195],v241 offset:1280\nds_read_b64_tr_b16 v[196:197],v241 offset:9216\nM v[72:75],v[200:203],a[136:139],v[72:75]\nds_read_b64_tr_b16 v[198:199],v241 offset:9472\n" \
"M v[72:75],v[204:207],a[152:155],v[72:75]\nM v[76:79],v[200:203],a[140:143],v[76:79]\nbuffer_load_dwordx4 v226,s[36:39],s67 offen offset:2048 lds\nM v[76:79],v[204:207],a[156:159],v[76:79]\nds_read_b64_tr_b16 v[200:201],v242 offset:1024\ns_waitcnt lgkmcnt(10)\nM v[16:19],v[208:211],a[128:131],v[16:19]\nds_read_b64_tr_b16 v[202:203],v242 offset:1280\n" \
"s_waitcnt lgkmcnt(10)\nM v[16:19],v[212:215],a[144:147],v[16:19]\nds_read_b64_tr_b16 v[204:205],v242 offset:9216\nds_read_b64_tr_b16 v[206:207],v242 offset:9472\nM v[20:23],v[208:211],a[132:135],v[20:23]\nglobal_load_dword v247,v253,s[16:17]\nM v[20:23],v[212:215],a[148:151],v[20:23]\nbuffer_load_dwordx4 v227,s[36:39],s67 offen offset:3072 lds\n" \
"M v[80:83],v[208:211],a[136:139],v[80:83]\nv_add_u32 v253,0xffd00000,v253\nM v[80:83],v[212:215],a[152:155],v[80:83]\ns_add_u32 m0,s64,131072\ns_add_u32 s69,s68,3145728\nM v[84:87],v[208:211],a[140:143],v[84:87]\nds_read_b64_tr_b16 v[208:209],v241 offset:1536\nds_read_b64_tr_b16 v[210:211],v241 offset:1792\n" \
"M v[84:87],v[212:215],a[156:159],v[84:87]\nds_read_b64_tr_b16 v[212:213],v241 offset:9728\ns_waitcnt lgkmcnt(7)\nM v[24:27],v[216:219],a[128:131],v[24:27]\nbuffer_load_dwordx4 v243,s[40:43],s69 offen offset:0 lds\nM v[24:27],v[220:223],a[144:147],v[24:27]\nds_read_b64_tr_b16 v[214:215],v241 offset:9984\nM v[28:31],v[216:219],a[132:135],v[28:31]\n" \
"M v[28:31],v[220:223],a[148:151],v[28:31]\nM v[88:91],v[216:219],a[136:139],v[88:91]\nM v[88:91],v[220:223],a[152:155],v[88:91]\nbuffer_load_dwordx4 v244,s[40:43],s69 offen offset:1024 lds\nM v[92:95],v[216:219],a[140:143],v[92:95]\nds_read_b64_tr_b16 v[216:217],v242 offset:1536\nds_read_b64_tr_b16 v[218:219],v242 offset:1792\nM v[92:95],v[220:223],a[156:159],v[92:95]\n" \
"ds_read_b64_tr_b16 v[220:221],v242 offset:9728\nds_read_b64_tr_b16 v[222:223],v242 offset:9984\nM v[32:35],v[192:195],a[128:131],v[32:35]\nM v[32:35],v[196:199],a[144:147],v[32:35]\nM v[36:39],v[192:195],a[132:135],v[36:39]\nbuffer_load_dwordx4 v245,s[40:43],s69 offen offset:2048 lds\nM v[36:39],v[196:199],a[148:151],v[36:39]\nM v[96:99],v[192:195],a[136:139],v[96:99]\n" \
"s_waitcnt vmcnt(16)\nv_xor_b32 v166,v166,v230\nM v[96:99],v[196:199],a[152:155],v[96:99]\nv_xor_b32 v167,v167,v231\nv_or_b32 v166,v166,v167\nM v[100:103],v[192:195],a[140:143],v[100:103]\nv_cmp_ne_u32 vcc,0,v166\nM v[100:103],v[196:199],a[156:159],v[100:103]\n" \
"buffer_load_dwordx4 v246,s[40:43],s69 offen offset:3072 lds\ns_waitcnt lgkmcnt(4)\nM v[40:43],v[200:203],a[128:131],v[40:43]\ns_cbranch_vccnz .Lhs\n.Lhb:\nv_bfe_u32 v231,v249,2,3\nM v[40:43],v[204:207],a[144:147],v[40:43]\ns_add_u32 m0,s65,8192\n" \
"s_xor_b32 s67,s66,896\nM v[44:47],v[200:203],a[132:135],v[44:47]\nv_mul_u32_u24 v231,3584,v231\nv_and_b32 v230,31,v249\nM v[44:47],v[204:207],a[148:151],v[44:47]\nv_lshlrev_b32 v230,7,v230\nM v[104:107],v[200:203],a[136:139],v[104:107]\nbuffer_load_dwordx4 v224,s[36:39],s67 offen offset:0 lds\n" \
"M v[104:107],v[204:207],a[152:155],v[104:107]\nM v[108:111],v[200:203],a[140:143],v[108:111]\nM v[108:111],v[204:207],a[156:159],v[108:111]\nM v[48:51],v[208:211],a[128:131],v[48:51]\nM v[48:51],v[212:215],a[144:147],v[48:51]\nbuffer_load_dwordx4 v225,s[36:39],s67 offen offset:1024 lds\nM v[52:55],v[208:211],a[132:135],v[52:55]\nM v[52:55],v[212:215],a[148:151],v[52:55]\n" \
"M v[112:115],v[208:211],a[136:139],v[112:115]\ns_waitcnt vmcnt(15) lgkmcnt(0)\ns_barrier\nM v[112:115],v[212:215],a[152:155],v[112:115]\nds_read_b128 a[160:163],v228 offset:16384\nds_read_b64_tr_b16 v[192:193],v241 offset:16384\nds_read_b64_tr_b16 v[194:195],v241 offset:16640\nM v[116:119],v[208:211],a[140:143],v[116:119]\n" \
"ds_read_b128 a[176:179],v229 offset:16384\nds_read_b64_tr_b16 v[196:197],v241 offset:24576\nds_read_b64_tr_b16 v[198:199],v241 offset:24832\nM v[116:119],v[212:215],a[156:159],v[116:119]\nds_read_b128 a[164:167],v228 offset:18432\nM v[56:59],v[216:219],a[128:131],v[56:59]\nbuffer_load_dwordx4 v[152:155],v240,s[52:55],s71 offen sc1\nds_read_b128 a[180:183],v229 offset:18432\n" \
"M v[56:59],v[220:223],a[144:147],v[56:59]\nbuffer_load_dwordx4 v226,s[36:39],s67 offen offset:2048 lds\ns_add_u32 s71,s70,798720\nds_read_b128 a[168:171],v228 offset:20480\nbuffer_load_dwordx4 v[156:159],v240,s[52:55],s71 offen sc1\nM v[60:63],v[216:219],a[132:135],v[60:63]\nds_read_b128 a[184:187],v229 offset:20480\ns_add_u32 s71,s70,811008\n" \
"M v[60:63],v[220:223],a[148:151],v[60:63]\nbuffer_load_dwordx4 v227,s[36:39],s67 offen offset:3072 lds\nbuffer_load_dwordx4 v[160:163],v240,s[52:55],s71 offen sc1\nds_read_b128 a[172:175],v228 offset:22528\nM v[120:123],v[216:219],a[136:139],v[120:123]\ns_add_u32 s71,s70,823296\nds_read_b128 a[188:191],v229 offset:22528\nbuffer_load_dwordx4 v[164:167],v240,s[52:55],s71 offen sc1\n" \
"M v[120:123],v[220:223],a[152:155],v[120:123]\ns_add_u32 m0,s65,12288\ns_xor_b32 s67,s66,33664\nds_read_b64_tr_b16 v[200:201],v242 offset:16384\nds_read_b64_tr_b16 v[202:203],v242 offset:16640\nM v[124:127],v[216:219],a[140:143],v[124:127]\nds_read_b64_tr_b16 v[204:205],v242 offset:24576\nds_read_b64_tr_b16 v[206:207],v242 offset:24832\n" \
"M v[124:127],v[220:223],a[156:159],v[124:127]\ns_lshl_b32 s71,s6,5\ns_waitcnt lgkmcnt(9)\nM v[0:3],v[192:195],a[160:163],v[0:3]\nds_read_b64_tr_b16 v[208:209],v241 offset:16896\ns_add_u32 s71,s71,790528\nM v[0:3],v[196:199],a[176:179],v[0:3]\nds_read_b64_tr_b16 v[210:211],v241 offset:17152\n" \
"ds_read_b64_tr_b16 v[212:213],v241 offset:25088\nM v[4:7],v[192:195],a[164:167],v[4:7]\nds_read_b64_tr_b16 v[214:215],v241 offset:25344\ns_waitcnt lgkmcnt(11)\nM v[4:7],v[196:199],a[180:183],v[4:7]\nds_read_b64_tr_b16 v[216:217],v242 offset:16896\nds_read_b64_tr_b16 v[218:219],v242 offset:17152\nM v[64:67],v[192:195],a[168:171],v[64:67]\n" \
"ds_read_b64_tr_b16 v[220:221],v242 offset:25088\ns_waitcnt lgkmcnt(13)\nM v[64:67],v[196:199],a[184:187],v[64:67]\nds_read_b64_tr_b16 v[222:223],v242 offset:25344\ns_waitcnt lgkmcnt(13)\nM v[68:71],v[192:195],a[172:175],v[68:71]\nds_read_b64_tr_b16 v[192:193],v241 offset:17408\ns_waitcnt lgkmcnt(13)\n" \
"M v[68:71],v[196:199],a[188:191],v[68:71]\nds_read_b64_tr_b16 v[194:195],v241 offset:17664\ns_waitcnt lgkmcnt(12)\nM v[8:11],v[200:203],a[160:163],v[8:11]\nds_read_b64_tr_b16 v[196:197],v241 offset:25600\ns_waitcnt lgkmcnt(11)\nM v[8:11],v[204:207],a[176:179],v[8:11]\ns_cmp_lt_u32 s7,2\n" \
"s_cselect_b32 s70,0,1.0\nM v[12:15],v[200:203],a[164:167],v[12:15]\nbuffer_load_dword v247,v254,s[56:59],s78 offen\nM v[12:15],v[204:207],a[180:183],v[12:15]\nds_read_b64_tr_b16 v[198:199],v241 offset:25856\nv_and_b32 v254,3,v249\nM v[72:75],v[200:203],a[168:171],v[72:75]\ns_cmp_eq_u32 s62,5\n" \
"s_cselect_b64 s[56:57],s[22:23],s[24:25]\nM v[72:75],v[204:207],a[184:187],v[72:75]\nv_lshl_add_u32 v231,v254,7,v231\nM v[76:79],v[200:203],a[172:175],v[76:79]\nglobal_load_dword v247,v253,s[12:13]\nM v[76:79],v[204:207],a[188:191],v[76:79]\nds_read_b64_tr_b16 v[200:201],v242 offset:17408\ns_waitcnt lgkmcnt(6)\n" \
"M v[16:19],v[208:211],a[160:163],v[16:19]\nds_read_b64_tr_b16 v[202:203],v242 offset:17664\nds_read_b64_tr_b16 v[204:205],v242 offset:25600\nM v[16:19],v[212:215],a[176:179],v[16:19]\nds_read_b64_tr_b16 v[206:207],v242 offset:25856\ns_add_u32 s69,s68,3670016\nM v[20:23],v[208:211],a[164:167],v[20:23]\nbuffer_load_dwordx4 v224,s[36:39],s67 offen offset:0 lds\n" \
"M v[20:23],v[212:215],a[180:183],v[20:23]\nv_add_u32 v253,0x100000,v253\nM v[80:83],v[208:211],a[168:171],v[80:83]\nM v[80:83],v[212:215],a[184:187],v[80:83]\nM v[84:87],v[208:211],a[172:175],v[84:87]\nbuffer_load_dwordx4 v225,s[36:39],s67 offen offset:1024 lds\nM v[84:87],v[212:215],a[188:191],v[84:87]\nds_read_b64_tr_b16 v[208:209],v241 offset:17920\n" \
"ds_read_b64_tr_b16 v[210:211],v241 offset:18176\nM v[24:27],v[216:219],a[160:163],v[24:27]\nds_read_b64_tr_b16 v[212:213],v241 offset:26112\ns_waitcnt lgkmcnt(7)\nM v[24:27],v[220:223],a[176:179],v[24:27]\nds_read_b64_tr_b16 v[214:215],v241 offset:26368\nM v[28:31],v[216:219],a[164:167],v[28:31]\nbuffer_load_dwordx4 v226,s[36:39],s67 offen offset:2048 lds\n" \
"M v[28:31],v[220:223],a[180:183],v[28:31]\nM v[88:91],v[216:219],a[168:171],v[88:91]\nM v[88:91],v[220:223],a[184:187],v[88:91]\nM v[92:95],v[216:219],a[172:175],v[92:95]\nbuffer_load_dwordx4 v227,s[36:39],s67 offen offset:3072 lds\nM v[92:95],v[220:223],a[188:191],v[92:95]\ns_add_u32 m0,s64,147456\nds_read_b64_tr_b16 v[216:217],v242 offset:17920\n" \
"M v[32:35],v[192:195],a[160:163],v[32:35]\nds_read_b64_tr_b16 v[218:219],v242 offset:18176\nds_read_b64_tr_b16 v[220:221],v242 offset:26112\nM v[32:35],v[196:199],a[176:179],v[32:35]\ns_cmp_eq_u32 s62,0\ns_cselect_b32 s36,0x8000,0\nM v[36:39],v[192:195],a[164:167],v[36:39]\nds_read_b64_tr_b16 v[222:223],v242 offset:26368\n" \
"s_add_u32 s36,s36,s64\nM v[36:39],v[196:199],a[180:183],v[36:39]\nbuffer_load_dwordx4 v243,s[40:43],s69 offen offset:0 lds\nM v[96:99],v[192:195],a[168:171],v[96:99]\nM v[96:99],v[196:199],a[184:187],v[96:99]\nM v[100:103],v[192:195],a[172:175],v[100:103]\nM v[100:103],v[196:199],a[188:191],v[100:103]\ns_waitcnt lgkmcnt(4)\n" \
"M v[40:43],v[200:203],a[160:163],v[40:43]\nbuffer_load_dwordx4 v244,s[40:43],s69 offen offset:1024 lds\nM v[40:43],v[204:207],a[176:179],v[40:43]\nM v[44:47],v[200:203],a[164:167],v[44:47]\nM v[44:47],v[204:207],a[180:183],v[44:47]\nM v[104:107],v[200:203],a[168:171],v[104:107]\nbuffer_load_dwordx4 v245,s[40:43],s69 offen offset:2048 lds\nM v[104:107],v[204:207],a[184:187],v[104:107]\n" \
"M v[108:111],v[200:203],a[172:175],v[108:111]\nM v[108:111],v[204:207],a[188:191],v[108:111]\nM v[48:51],v[208:211],a[160:163],v[48:51]\nbuffer_load_dwordx4 v246,s[40:43],s69 offen offset:3072 lds\nM v[48:51],v[212:215],a[176:179],v[48:51]\ns_sub_u32 s40,s16,0x1000\ns_subb_u32 s41,s17,0\nM v[52:55],v[208:211],a[164:167],v[52:55]\n" \
"s_add_u32 m0,s64,98304\ns_add_u32 s69,s68,0\nM v[52:55],v[212:215],a[180:183],v[52:55]\ns_mov_b32 s16,0\ns_mov_b32 s17,3\nM v[112:115],v[208:211],a[168:171],v[112:115]\ns_waitcnt vmcnt(18) lgkmcnt(0)\ns_barrier\n" \
"M v[112:115],v[212:215],a[184:187],v[112:115]\nds_read_b128 a[192:195],v228 offset:0\nds_read_b64_tr_b16 v[192:193],v241 offset:32768\nds_read_b64_tr_b16 v[194:195],v241 offset:33024\nM v[116:119],v[208:211],a[172:175],v[116:119]\nglobal_load_dword v247,v253,s[12:13]\nds_read_b128 a[208:211],v229 offset:0\nds_read_b64_tr_b16 v[196:197],v241 offset:40960\n" \
"ds_read_b64_tr_b16 v[198:199],v241 offset:41216\nM v[116:119],v[212:215],a[188:191],v[116:119]\nds_read_b128 a[196:199],v228 offset:2048\nv_add_u32 v253,0x100000,v253\nM v[56:59],v[216:219],a[160:163],v[56:59]\nds_read_b128 a[212:215],v229 offset:2048\nM v[56:59],v[220:223],a[176:179],v[56:59]\nds_read_b128 a[200:203],v228 offset:4096\n" \
"M v[60:63],v[216:219],a[164:167],v[60:63]\nds_read_b128 a[216:219],v229 offset:4096\nM v[60:63],v[220:223],a[180:183],v[60:63]\nds_read_b128 a[204:207],v228 offset:6144\nM v[120:123],v[216:219],a[168:171],v[120:123]\nds_read_b128 a[220:223],v229 offset:6144\nM v[120:123],v[220:223],a[184:187],v[120:123]\nds_read_b64_tr_b16 v[200:201],v242 offset:32768\n" \
"ds_read_b64_tr_b16 v[202:203],v242 offset:33024\nM v[124:127],v[216:219],a[172:175],v[124:127]\nds_read_b64_tr_b16 v[204:205],v242 offset:40960\nds_read_b64_tr_b16 v[206:207],v242 offset:41216\nM v[124:127],v[220:223],a[188:191],v[124:127]\ns_waitcnt vmcnt(14)\nv_add_f32 v152,v152,v156\ns_waitcnt lgkmcnt(13)\n" \
"M v[0:3],v[192:195],a[192:195],v[0:3]\nds_read_b64_tr_b16 v[208:209],v241 offset:33280\ns_waitcnt lgkmcnt(11)\nM v[0:3],v[196:199],a[208:211],v[0:3]\nds_read_b64_tr_b16 v[210:211],v241 offset:33536\ns_waitcnt lgkmcnt(11)\nM v[4:7],v[192:195],a[196:199],v[4:7]\nds_read_b64_tr_b16 v[212:213],v241 offset:41472\n" \
"s_waitcnt lgkmcnt(11)\nM v[4:7],v[196:199],a[212:215],v[4:7]\nds_read_b64_tr_b16 v[214:215],v241 offset:41728\ns_waitcnt lgkmcnt(11)\nM v[64:67],v[192:195],a[200:203],v[64:67]\nds_read_b64_tr_b16 v[216:217],v242 offset:33280\ns_waitcnt lgkmcnt(11)\nM v[64:67],v[196:199],a[216:219],v[64:67]\n" \
"ds_read_b64_tr_b16 v[218:219],v242 offset:33536\ns_waitcnt lgkmcnt(11)\nM v[68:71],v[192:195],a[204:207],v[68:71]\nds_read_b64_tr_b16 v[220:221],v242 offset:41472\ns_waitcnt lgkmcnt(11)\nM v[68:71],v[196:199],a[220:223],v[68:71]\nds_read_b64_tr_b16 v[222:223],v242 offset:41728\ns_waitcnt lgkmcnt(10)\n" \
"M v[8:11],v[200:203],a[192:195],v[8:11]\nds_read_b64_tr_b16 v[192:193],v241 offset:33792\ns_waitcnt lgkmcnt(9)\nM v[8:11],v[204:207],a[208:211],v[8:11]\nds_read_b64_tr_b16 v[194:195],v241 offset:34048\nds_read_b64_tr_b16 v[196:197],v241 offset:41984\nM v[12:15],v[200:203],a[196:199],v[12:15]\nds_read_b64_tr_b16 v[198:199],v241 offset:42240\n" \
"s_waitcnt vmcnt(11)\nv_add_f32 v160,v160,v164\nM v[12:15],v[204:207],a[212:215],v[12:15]\nv_add_f32 v153,v153,v157\nv_add_f32 v161,v161,v165\nM v[72:75],v[200:203],a[200:203],v[72:75]\nv_add_f32 v154,v154,v158\nv_add_f32 v162,v162,v166\n" \
"M v[72:75],v[204:207],a[216:219],v[72:75]\nv_add_f32 v155,v155,v159\nv_add_f32 v163,v163,v167\nM v[76:79],v[200:203],a[204:207],v[76:79]\nds_read_b64_tr_b16 v[200:201],v242 offset:33792\nds_read_b64_tr_b16 v[202:203],v242 offset:34048\nM v[76:79],v[204:207],a[220:223],v[76:79]\nds_read_b64_tr_b16 v[204:205],v242 offset:41984\n" \
"s_waitcnt lgkmcnt(9)\nM v[16:19],v[208:211],a[192:195],v[16:19]\nds_read_b64_tr_b16 v[206:207],v242 offset:42240\nv_add_f32 v152,v152,v160\nM v[16:19],v[212:215],a[208:211],v[16:19]\nbuffer_load_dword v168,v169,s[52:55],s71 offen sc1\nM v[20:23],v[208:211],a[196:199],v[20:23]\nv_add_f32 v153,v153,v161\n" \
"v_add_f32 v154,v154,v162\nM v[20:23],v[212:215],a[212:215],v[20:23]\nv_add_f32 v155,v155,v163\nv_cvt_pk_bf16_f32 v152,0,v152\nM v[80:83],v[208:211],a[200:203],v[80:83]\nv_cvt_pk_bf16_f32 v153,0,v153\nv_cvt_pk_bf16_f32 v154,0,v154\nM v[80:83],v[212:215],a[216:219],v[80:83]\n" \
"v_cvt_pk_bf16_f32 v155,0,v155\nv_add_f32 v152,s70,v152\nM v[84:87],v[208:211],a[204:207],v[84:87]\nds_read_b64_tr_b16 v[208:209],v241 offset:34304\nds_read_b64_tr_b16 v[210:211],v241 offset:34560\nM v[84:87],v[212:215],a[220:223],v[84:87]\nds_read_b64_tr_b16 v[212:213],v241 offset:42496\nds_read_b64_tr_b16 v[214:215],v241 offset:42752\n" \
"M v[24:27],v[216:219],a[192:195],v[24:27]\nv_add_f32 v153,s70,v153\ns_waitcnt lgkmcnt(7)\nM v[24:27],v[220:223],a[208:211],v[24:27]\nv_add_f32 v154,s70,v154\nv_add_f32 v155,s70,v155\nM v[28:31],v[216:219],a[196:199],v[28:31]\nglobal_load_dword v247,v253,s[12:13]\n" \
"M v[28:31],v[220:223],a[212:215],v[28:31]\ns_lshr_b32 s71,s64,2\nv_cvt_pk_bf16_f32 v152,0,v152\nM v[88:91],v[216:219],a[200:203],v[88:91]\nv_cvt_pk_bf16_f32 v153,0,v153\nv_cvt_pk_bf16_f32 v154,0,v154\nM v[88:91],v[220:223],a[216:219],v[88:91]\nv_cvt_pk_bf16_f32 v155,0,v155\n" \
"v_add_u32 v171,s71,v240\nM v[92:95],v[216:219],a[204:207],v[92:95]\nds_write_b128 v171,v[152:155] offset:16384\nM v[92:95],v[220:223],a[220:223],v[92:95]\nds_read_b64_tr_b16 v[216:217],v242 offset:34304\nds_read_b64_tr_b16 v[218:219],v242 offset:34560\nM v[32:35],v[192:195],a[192:195],v[32:35]\nds_read_b64_tr_b16 v[220:221],v242 offset:42496\n" \
"ds_read_b64_tr_b16 v[222:223],v242 offset:42752\nM v[32:35],v[196:199],a[208:211],v[32:35]\nv_add_u32 v253,0x100000,v253\ns_lshl_b32 s70,s7,8\nM v[36:39],v[192:195],a[196:199],v[36:39]\nbuffer_load_dwordx4 v243,s[40:43],s69 offen offset:0 lds\nM v[36:39],v[196:199],a[212:215],v[36:39]\nv_lshl_add_u32 v251,v251,5,s70\n" \
"s_lshl_b32 s71,s6,5\nM v[96:99],v[192:195],a[200:203],v[96:99]\ns_and_b32 s71,s71,0xfffffe00\ns_xor_b32 s70,s63,16\nM v[96:99],v[196:199],a[216:219],v[96:99]\ns_lshr_b32 s70,s70,2\ns_lshl3_add_u32 s70,s62,s70\nM v[100:103],v[192:195],a[204:207],v[100:103]\n" \
"buffer_load_dwordx4 v244,s[40:43],s69 offen offset:1024 lds\nM v[100:103],v[196:199],a[220:223],v[100:103]\ns_lshl_b32 s70,s70,15\ns_waitcnt lgkmcnt(5)\nM v[40:43],v[200:203],a[192:195],v[40:43]\ns_add_u32 s70,s70,s64\nM v[40:43],v[204:207],a[208:211],v[40:43]\nM v[44:47],v[200:203],a[196:199],v[44:47]\n" \
"buffer_load_dwordx4 v245,s[40:43],s69 offen offset:2048 lds\nM v[44:47],v[204:207],a[212:215],v[44:47]\nM v[104:107],v[200:203],a[200:203],v[104:107]\nM v[104:107],v[204:207],a[216:219],v[104:107]\nM v[108:111],v[200:203],a[204:207],v[108:111]\nbuffer_load_dwordx4 v246,s[40:43],s69 offen offset:3072 lds\nM v[108:111],v[204:207],a[220:223],v[108:111]\ns_add_u32 m0,s64,114688\n" \
"s_add_u32 s69,s68,524288\nM v[48:51],v[208:211],a[192:195],v[48:51]\nM v[48:51],v[212:215],a[208:211],v[48:51]\nM v[52:55],v[208:211],a[196:199],v[52:55]\nbuffer_store_dwordx2 v[232:233],off,s[52:55],s72 sc1\nM v[52:55],v[212:215],a[212:215],v[52:55]\ns_and_b32 s72,s6,1\ns_lshl_b32 s72,s72,8\n" \
"M v[112:115],v[208:211],a[200:203],v[112:115]\ns_waitcnt vmcnt(8) lgkmcnt(0)\ns_barrier\nM v[112:115],v[212:215],a[216:219],v[112:115]\nds_read_b128 a[224:227],v228 offset:8192\nds_read_b64_tr_b16 v[192:193],v241 offset:49152\nds_read_b64_tr_b16 v[194:195],v241 offset:49408\nM v[116:119],v[208:211],a[204:207],v[116:119]\n" \
"ds_read_b128 a[240:243],v229 offset:8192\nds_read_b64_tr_b16 v[196:197],v241 offset:57344\nds_read_b64_tr_b16 v[198:199],v241 offset:57600\nM v[116:119],v[212:215],a[220:223],v[116:119]\nds_read_b128 a[228:231],v228 offset:10240\ns_add_u32 s71,s71,s72\nM v[56:59],v[216:219],a[192:195],v[56:59]\nds_read_b128 a[244:247],v229 offset:10240\n" \
"s_and_b32 s72,s6,14\nM v[56:59],v[220:223],a[208:211],v[56:59]\nds_read_b128 a[232:235],v228 offset:12288\ns_lshl1_add_u32 s71,s72,s71\nM v[60:63],v[216:219],a[196:199],v[60:63]\nds_read_b128 a[248:251],v229 offset:12288\ns_or_b32 s71,s71,s75\nM v[60:63],v[220:223],a[212:215],v[60:63]\n" \
"ds_read_b128 a[236:239],v228 offset:14336\ns_mov_b32 s72,0x3e0293ee\nM v[120:123],v[216:219],a[200:203],v[120:123]\nds_read_b128 a[252:255],v229 offset:14336\nv_and_b32 v228,15,v249\nM v[120:123],v[220:223],a[216:219],v[120:123]\nds_read_b64_tr_b16 v[200:201],v242 offset:49152\nds_read_b64_tr_b16 v[202:203],v242 offset:49408\n" \
"M v[124:127],v[216:219],a[204:207],v[124:127]\nds_read_b64_tr_b16 v[204:205],v242 offset:57344\nds_read_b64_tr_b16 v[206:207],v242 offset:57600\nM v[124:127],v[220:223],a[220:223],v[124:127]\nv_and_b32 v229,16,v249\ns_waitcnt lgkmcnt(13)\nM v[0:3],v[192:195],a[224:227],v[0:3]\nds_read_b64_tr_b16 v[208:209],v241 offset:49664\n" \
"s_waitcnt lgkmcnt(11)\nM v[0:3],v[196:199],a[240:243],v[0:3]\nds_read_b64_tr_b16 v[210:211],v241 offset:49920\ns_waitcnt lgkmcnt(11)\nM v[4:7],v[192:195],a[228:231],v[4:7]\nds_read_b64_tr_b16 v[212:213],v241 offset:57856\ns_waitcnt lgkmcnt(11)\nM v[4:7],v[196:199],a[244:247],v[4:7]\n" \
"ds_read_b64_tr_b16 v[214:215],v241 offset:58112\ns_waitcnt lgkmcnt(11)\nM v[64:67],v[192:195],a[232:235],v[64:67]\nds_read_b64_tr_b16 v[216:217],v242 offset:49664\ns_waitcnt lgkmcnt(11)\nM v[64:67],v[196:199],a[248:251],v[64:67]\nds_read_b64_tr_b16 v[218:219],v242 offset:49920\ns_waitcnt lgkmcnt(11)\n" \
"M v[68:71],v[192:195],a[236:239],v[68:71]\nglobal_load_dword v247,v253,s[12:13]\ns_waitcnt lgkmcnt(10)\nM v[68:71],v[196:199],a[252:255],v[68:71]\nds_read_b64_tr_b16 v[220:221],v242 offset:57856\ns_waitcnt lgkmcnt(9)\nM v[8:11],v[200:203],a[224:227],v[8:11]\nds_read_b64_tr_b16 v[222:223],v242 offset:58112\n" \
"s_waitcnt lgkmcnt(8)\nM v[8:11],v[204:207],a[240:243],v[8:11]\nds_read_b64_tr_b16 v[192:193],v241 offset:50176\nds_read_b64_tr_b16 v[194:195],v241 offset:50432\nM v[12:15],v[200:203],a[228:231],v[12:15]\nds_read_b64_tr_b16 v[196:197],v241 offset:58368\nds_read_b64_tr_b16 v[198:199],v241 offset:58624\nM v[12:15],v[204:207],a[244:247],v[12:15]\n" \
"ds_read_b128 v[176:179],v251 offset:19456\nds_read_b128 v[160:163],v251 offset:17408\nM v[72:75],v[200:203],a[232:235],v[72:75]\nds_read_b128 v[172:175],v251 offset:18448\nv_cvt_pk_bf16_f32 v128,v0,v1\nM v[72:75],v[204:207],a[248:251],v[72:75]\nds_read_b128 v[152:155],v251 offset:16384\nv_cvt_pk_bf16_f32 v129,v2,v3\n" \
"M v[76:79],v[200:203],a[236:239],v[76:79]\nds_read_b64_tr_b16 v[200:201],v242 offset:50176\nv_cvt_pk_bf16_f32 v130,v8,v9\nM v[76:79],v[204:207],a[252:255],v[76:79]\nds_read_b64_tr_b16 v[202:203],v242 offset:50432\ns_waitcnt lgkmcnt(12)\nM v[16:19],v[208:211],a[224:227],v[16:19]\nds_read_b64_tr_b16 v[204:205],v242 offset:58368\n" \
"ds_read_b64_tr_b16 v[206:207],v242 offset:58624\nM v[16:19],v[212:215],a[240:243],v[16:19]\nds_read_b128 v[156:159],v251 offset:16400\nds_read_b128 v[180:183],v251 offset:19472\nM v[20:23],v[208:211],a[228:231],v[20:23]\nds_read_b128 v[164:167],v251 offset:17424\nv_cvt_pk_bf16_f32 v131,v10,v11\nM v[20:23],v[212:215],a[244:247],v[20:23]\n" \
"buffer_load_dwordx4 v243,s[40:43],s69 offen offset:0 lds\nM v[80:83],v[208:211],a[232:235],v[80:83]\nv_cvt_pk_bf16_f32 v132,v16,v17\nv_cvt_pk_bf16_f32 v133,v18,v19\nM v[80:83],v[212:215],a[248:251],v[80:83]\ns_waitcnt lgkmcnt(10)\nv_mul_f32 v144,v144,v176\nM v[84:87],v[208:211],a[236:239],v[84:87]\n" \
"ds_read_b64_tr_b16 v[208:209],v241 offset:50688\nds_read_b64_tr_b16 v[210:211],v241 offset:50944\nM v[84:87],v[212:215],a[252:255],v[84:87]\nds_read_b64_tr_b16 v[212:213],v241 offset:58880\nds_read_b64_tr_b16 v[214:215],v241 offset:59136\nM v[24:27],v[216:219],a[224:227],v[24:27]\ns_waitcnt vmcnt(8)\nv_add_f32_dpp v168,v168,v168 quad_perm:[1,0,3,2] row_mask:0xf bank_mask:0xf\n" \
"M v[24:27],v[220:223],a[240:243],v[24:27]\nv_mul_f32 v145,v145,v177\nv_add_f32_dpp v168,v168,v168 quad_perm:[2,3,0,1] row_mask:0xf bank_mask:0xf\nM v[28:31],v[216:219],a[228:231],v[28:31]\nv_cvt_pk_bf16_f32 v168,0,v168\nv_cvt_pk_bf16_f32 v144,0,v144\nM v[28:31],v[220:223],a[244:247],v[28:31]\nbuffer_store_dword v168,v170,s[52:55],s71 offen sc1\n" \
"M v[88:91],v[216:219],a[232:235],v[88:91]\nds_read_b128 v[168:171],v251 offset:18432\nv_cvt_pk_bf16_f32 v145,0,v145\nM v[88:91],v[220:223],a[248:251],v[88:91]\ns_waitcnt lgkmcnt(5)\nv_add_f32 v144,v144,v160\nM v[92:95],v[216:219],a[236:239],v[92:95]\nds_read_b64_tr_b16 v[216:217],v242 offset:50688\n" \
"ds_read_b64_tr_b16 v[218:219],v242 offset:50944\nM v[92:95],v[220:223],a[252:255],v[92:95]\nds_read_b64_tr_b16 v[220:221],v242 offset:58880\nds_read_b64_tr_b16 v[222:223],v242 offset:59136\nM v[32:35],v[192:195],a[224:227],v[32:35]\nv_add_f32 v145,v145,v161\nv_cvt_pk_bf16_f32 v144,0,v144\nM v[32:35],v[196:199],a[240:243],v[32:35]\n" \
"v_cvt_pk_bf16_f32 v145,0,v145\nv_mul_f32 v144,s72,v144\nM v[36:39],v[192:195],a[228:231],v[36:39]\nv_mul_f32 v145,s72,v145\nv_cvt_pk_bf16_f32 v236,v144,v145\nM v[36:39],v[196:199],a[244:247],v[36:39]\nv_cvt_pk_bf16_f32 v144,v4,v5\nv_cvt_pk_bf16_f32 v145,v6,v7\n" \
"M v[96:99],v[192:195],a[232:235],v[96:99]\nv_cvt_pk_bf16_f32 v160,v64,v65\nv_cvt_pk_bf16_f32 v161,v66,v67\nM v[96:99],v[196:199],a[248:251],v[96:99]\ns_and_b32 s71,s63,2\nv_mul_f32 v140,v140,v172\nM v[100:103],v[192:195],a[236:239],v[100:103]\ns_waitcnt lgkmcnt(4)\n" \
"v_mul_f32 v136,v136,v168\nM v[100:103],v[196:199],a[252:255],v[100:103]\nv_mul_f32 v137,v137,v169\nv_mul_f32 v138,v138,v170\nM v[40:43],v[200:203],a[224:227],v[40:43]\nv_mul_f32 v139,v139,v171\nv_mul_f32 v141,v141,v173\nM v[40:43],v[204:207],a[240:243],v[40:43]\n" \
"v_mul_f32 v142,v142,v174\nv_mul_f32 v143,v143,v175\nM v[44:47],v[200:203],a[228:231],v[44:47]\ns_lshl_b32 s71,s71,9\nv_cvt_pk_bf16_f32 v176,v68,v69\nM v[44:47],v[204:207],a[244:247],v[44:47]\nv_cvt_pk_bf16_f32 v177,v70,v71\nv_cvt_pk_bf16_f32 v136,0,v136\n" \
"M v[104:107],v[200:203],a[232:235],v[104:107]\nv_cvt_pk_bf16_f32 v137,0,v137\nv_cvt_pk_bf16_f32 v138,0,v138\nM v[104:107],v[204:207],a[248:251],v[104:107]\nv_cvt_pk_bf16_f32 v139,0,v139\nv_cvt_pk_bf16_f32 v140,0,v140\nM v[108:111],v[200:203],a[236:239],v[108:111]\nv_cvt_pk_bf16_f32 v141,0,v141\n" \
"v_cvt_pk_bf16_f32 v142,0,v142\nM v[108:111],v[204:207],a[252:255],v[108:111]\nv_cvt_pk_bf16_f32 v143,0,v143\nv_lshrrev_b32 v251,5,v249\nM v[48:51],v[208:211],a[224:227],v[48:51]\ns_add_u32 s70,s70,s71\nv_add_f32 v136,v136,v152\nM v[48:51],v[212:215],a[240:243],v[48:51]\n" \
"v_add_f32 v137,v137,v153\nv_add_f32 v138,v138,v154\nM v[52:55],v[208:211],a[228:231],v[52:55]\nv_add_f32 v139,v139,v155\nv_add_f32 v140,v140,v156\nM v[52:55],v[212:215],a[244:247],v[52:55]\nv_add_f32 v141,v141,v157\nv_add_f32 v142,v142,v158\n" \
"M v[112:115],v[208:211],a[232:235],v[112:115]\ns_waitcnt vmcnt(4) lgkmcnt(0)\ns_barrier\nM v[112:115],v[212:215],a[248:251],v[112:115]\nds_read_b64_tr_b16 v[192:193],v241 offset:0\nds_read_b64_tr_b16 v[194:195],v241 offset:256\nM v[116:119],v[208:211],a[236:239],v[116:119]\nds_read_b64_tr_b16 v[196:197],v241 offset:8192\n" \
"ds_read_b64_tr_b16 v[198:199],v241 offset:8448\nM v[116:119],v[212:215],a[252:255],v[116:119]\nds_read_b64_tr_b16 v[200:201],v242 offset:0\nds_read_b64_tr_b16 v[202:203],v242 offset:256\nM v[56:59],v[216:219],a[224:227],v[56:59]\nds_read_b64_tr_b16 v[204:205],v242 offset:8192\nds_read_b64_tr_b16 v[206:207],v242 offset:8448\nM v[56:59],v[220:223],a[240:243],v[56:59]\n" \
"ds_read_b64_tr_b16 v[208:209],v241 offset:512\nds_read_b64_tr_b16 v[210:211],v241 offset:768\nM v[60:63],v[216:219],a[228:231],v[60:63]\nv_add_f32 v143,v143,v159\nv_mul_f32 v146,v146,v178\nM v[60:63],v[220:223],a[244:247],v[60:63]\nv_mul_f32 v147,v147,v179\nv_lshlrev_b32 v251,11,v251\n" \
"M v[120:123],v[216:219],a[232:235],v[120:123]\ns_and_b32 s71,s63,1\nds_read_b64_tr_b16 v[212:213],v241 offset:8704\nM v[120:123],v[220:223],a[248:251],v[120:123]\nds_read_b64_tr_b16 v[214:215],v241 offset:8960\nv_cvt_pk_bf16_f32 v136,0,v136\nM v[124:127],v[216:219],a[236:239],v[124:127]\nv_cvt_pk_bf16_f32 v137,0,v137\n" \
"v_cvt_pk_bf16_f32 v138,0,v138\nM v[124:127],v[220:223],a[252:255],v[124:127]\nv_cvt_pk_bf16_f32 v139,0,v139\ns_waitcnt lgkmcnt(10)\nM v[0:3],a[0:3],v[192:195],0\nv_cvt_pk_bf16_f32 v140,0,v140\ns_waitcnt lgkmcnt(8)\nM v[0:3],a[16:19],v[196:199],v[0:3]\n" \
"v_cvt_pk_bf16_f32 v141,0,v141\nv_cvt_pk_bf16_f32 v142,0,v142\nM v[4:7],a[4:7],v[192:195],0\nv_cvt_pk_bf16_f32 v143,0,v143\nv_cvt_pk_bf16_f32 v146,0,v146\nM v[4:7],a[20:23],v[196:199],v[4:7]\nv_cvt_pk_bf16_f32 v147,0,v147\nv_lshl_or_b32 v251,v250,8,v251\n" \
"M v[64:67],a[8:11],v[192:195],0\ns_lshl_b32 s71,s71,7\nv_mul_f32 v136,s72,v136\nM v[64:67],a[24:27],v[196:199],v[64:67]\nv_mul_f32 v137,s72,v137\nv_mul_f32 v138,s72,v138\nM v[68:71],a[12:15],v[192:195],0\nv_mul_f32 v139,s72,v139\n" \
"v_mul_f32 v140,s72,v140\nM v[68:71],a[28:31],v[196:199],v[68:71]\nv_mul_f32 v141,s72,v141\nv_mul_f32 v142,s72,v142\nv_mul_f32 v143,s72,v143\nv_add_f32 v146,v146,v162\nv_add_f32 v147,v147,v163\nv_and_b32 v250,0x70,v240\n" \
"s_add_u32 s70,s70,s71\ns_waitcnt lgkmcnt(0)\nM v[8:11],a[0:3],v[200:203],0\nv_cvt_pk_bf16_f32 v232,v136,v137\nv_cvt_pk_bf16_f32 v233,v138,v139\nv_cvt_pk_bf16_f32 v234,v140,v141\nv_cvt_pk_bf16_f32 v235,v142,v143\nv_cvt_pk_bf16_f32 v146,0,v146\n" \
"v_cvt_pk_bf16_f32 v147,0,v147\nv_or_b32 v251,v251,v250\ns_add_u32 s70,s70,1048576\nv_mul_f32 v146,s72,v146\nv_mul_f32 v147,s72,v147\nbuffer_store_dwordx4 v[232:235],v251,s[52:55],s70 offen sc1\nM v[8:11],a[16:19],v[204:207],v[8:11]\nv_cvt_pk_bf16_f32 v237,v146,v147\n" \
"v_mul_f32 v148,v148,v180\nv_mul_f32 v149,v149,v181\nv_mul_f32 v150,v150,v182\nv_mul_f32 v151,v151,v183\nv_cvt_pk_bf16_f32 v146,v12,v13\nv_cvt_pk_bf16_f32 v147,v14,v15\nM v[12:15],a[4:7],v[200:203],0\nv_cvt_pk_bf16_f32 v148,0,v148\n" \
"v_cvt_pk_bf16_f32 v149,0,v149\nv_cvt_pk_bf16_f32 v150,0,v150\nv_cvt_pk_bf16_f32 v151,0,v151\nv_add_f32 v148,v148,v164\nv_add_f32 v149,v149,v165\nv_add_f32 v150,v150,v166\nv_add_f32 v151,v151,v167\nM v[12:15],a[20:23],v[204:207],v[12:15]\n" \
"v_cvt_pk_bf16_f32 v148,0,v148\nv_cvt_pk_bf16_f32 v149,0,v149\nv_cvt_pk_bf16_f32 v150,0,v150\nv_cvt_pk_bf16_f32 v151,0,v151\nv_mul_f32 v148,s72,v148\nv_mul_f32 v149,s72,v149\nv_mul_f32 v150,s72,v150\nv_mul_f32 v151,s72,v151\n" \
"v_cvt_pk_bf16_f32 v162,v72,v73\nv_cvt_pk_bf16_f32 v163,v74,v75\nM v[72:75],a[8:11],v[200:203],0\nv_cvt_pk_bf16_f32 v238,v148,v149\nv_cvt_pk_bf16_f32 v239,v150,v151\ns_add_u32 s71,s70,16384\nbuffer_load_dwordx4 v244,s[40:43],s69 offen offset:1024 lds\nbuffer_load_dwordx4 v245,s[40:43],s69 offen offset:2048 lds\n" \
"buffer_load_dwordx4 v246,s[40:43],s69 offen offset:3072 lds\nbuffer_store_dwordx4 v[236:239],v251,s[52:55],s71 offen sc1\nM v[72:75],a[24:27],v[204:207],v[72:75]\nv_cvt_pk_bf16_f32 v178,v76,v77\nv_cvt_pk_bf16_f32 v179,v78,v79\nds_read_b64_tr_b16 v[216:217],v242 offset:512\nds_read_b64_tr_b16 v[218:219],v242 offset:768\nM v[76:79],a[12:15],v[200:203],0\n" \
"ds_read_b64_tr_b16 v[220:221],v242 offset:8704\nds_read_b64_tr_b16 v[222:223],v242 offset:8960\nM v[76:79],a[28:31],v[204:207],v[76:79]\nv_cvt_pk_bf16_f32 v148,v20,v21\nv_cvt_pk_bf16_f32 v149,v22,v23\nM v[16:19],a[0:3],v[208:211],0\nv_cvt_pk_bf16_f32 v164,v80,v81\nv_cvt_pk_bf16_f32 v165,v82,v83\n" \
"M v[16:19],a[16:19],v[212:215],v[16:19]\nv_cvt_pk_bf16_f32 v180,v84,v85\nv_cvt_pk_bf16_f32 v181,v86,v87\nM v[20:23],a[4:7],v[208:211],0\nds_read_b64_tr_b16 v[192:193],v241 offset:1024\nds_read_b64_tr_b16 v[194:195],v241 offset:1280\nM v[20:23],a[20:23],v[212:215],v[20:23]\nds_read_b64_tr_b16 v[196:197],v241 offset:9216\n" \
"ds_read_b64_tr_b16 v[198:199],v241 offset:9472\nM v[80:83],a[8:11],v[208:211],0\nv_cvt_pk_bf16_f32 v134,v24,v25\nv_cvt_pk_bf16_f32 v135,v26,v27\nM v[80:83],a[24:27],v[212:215],v[80:83]\nv_cvt_pk_bf16_f32 v150,v28,v29\nv_cvt_pk_bf16_f32 v151,v30,v31\nM v[84:87],a[12:15],v[208:211],0\n" \
"v_cvt_pk_bf16_f32 v166,v88,v89\nv_cvt_pk_bf16_f32 v167,v90,v91\nM v[84:87],a[28:31],v[212:215],v[84:87]\nv_cvt_pk_bf16_f32 v182,v92,v93\ns_waitcnt lgkmcnt(6)\nM v[24:27],a[0:3],v[216:219],0\nv_cvt_pk_bf16_f32 v183,v94,v95\ns_waitcnt lgkmcnt(4)\n" \
"M v[24:27],a[16:19],v[220:223],v[24:27]\nds_read_b64_tr_b16 v[200:201],v242 offset:1024\nds_read_b64_tr_b16 v[202:203],v242 offset:1280\nM v[28:31],a[4:7],v[216:219],0\nds_read_b64_tr_b16 v[204:205],v242 offset:9216\nds_read_b64_tr_b16 v[206:207],v242 offset:9472\nM v[28:31],a[20:23],v[220:223],v[28:31]\nv_cvt_pk_bf16_f32 v136,v32,v33\n" \
"v_cvt_pk_bf16_f32 v137,v34,v35\nM v[88:91],a[8:11],v[216:219],0\nv_cvt_pk_bf16_f32 v152,v36,v37\nv_cvt_pk_bf16_f32 v153,v38,v39\nM v[88:91],a[24:27],v[220:223],v[88:91]\nv_cvt_pk_bf16_f32 v168,v96,v97\nv_cvt_pk_bf16_f32 v169,v98,v99\nM v[92:95],a[12:15],v[216:219],0\n" \
"s_add_u32 m0,s64,131072\ns_add_u32 s69,s68,1048576\nM v[92:95],a[28:31],v[220:223],v[92:95]\nv_cvt_pk_bf16_f32 v184,v100,v101\ns_waitcnt lgkmcnt(4)\nM v[32:35],a[0:3],v[192:195],0\nv_cvt_pk_bf16_f32 v185,v102,v103\nds_read_b64_tr_b16 v[208:209],v241 offset:1536\n" \
"M v[32:35],a[16:19],v[196:199],v[32:35]\nds_read_b64_tr_b16 v[210:211],v241 offset:1792\nds_read_b64_tr_b16 v[212:213],v241 offset:9728\nM v[36:39],a[4:7],v[192:195],0\nds_read_b64_tr_b16 v[214:215],v241 offset:9984\nv_cvt_pk_bf16_f32 v138,v40,v41\nM v[36:39],a[20:23],v[196:199],v[36:39]\nv_cvt_pk_bf16_f32 v139,v42,v43\n" \
"v_cvt_pk_bf16_f32 v154,v44,v45\nM v[96:99],a[8:11],v[192:195],0\nv_cvt_pk_bf16_f32 v155,v46,v47\nds_read_b64_tr_b16 v[216:217],v242 offset:1536\nM v[96:99],a[24:27],v[196:199],v[96:99]\nds_read_b64_tr_b16 v[218:219],v242 offset:1792\nds_read_b64_tr_b16 v[220:221],v242 offset:9728\nM v[100:103],a[12:15],v[192:195],0\n" \
"ds_read_b64_tr_b16 v[222:223],v242 offset:9984\nv_cvt_pk_bf16_f32 v170,v104,v105\nM v[100:103],a[28:31],v[196:199],v[100:103]\nv_cvt_pk_bf16_f32 v171,v106,v107\ns_waitcnt lgkmcnt(8)\nM v[40:43],a[0:3],v[200:203],0\nv_cvt_pk_bf16_f32 v186,v108,v109\nv_cvt_pk_bf16_f32 v187,v110,v111\n" \
"M v[40:43],a[16:19],v[204:207],v[40:43]\nv_cvt_pk_bf16_f32 v140,v48,v49\nv_cvt_pk_bf16_f32 v141,v50,v51\nM v[44:47],a[4:7],v[200:203],0\nv_cvt_pk_bf16_f32 v156,v52,v53\nv_cvt_pk_bf16_f32 v157,v54,v55\nM v[44:47],a[20:23],v[204:207],v[44:47]\nv_cvt_pk_bf16_f32 v172,v112,v113\n" \
"v_cvt_pk_bf16_f32 v173,v114,v115\nM v[104:107],a[8:11],v[200:203],0\nv_cvt_pk_bf16_f32 v188,v116,v117\nv_cvt_pk_bf16_f32 v189,v118,v119\nM v[104:107],a[24:27],v[204:207],v[104:107]\nv_cvt_pk_bf16_f32 v142,v56,v57\nv_cvt_pk_bf16_f32 v143,v58,v59\nM v[108:111],a[12:15],v[200:203],0\n" \
"v_cvt_pk_bf16_f32 v158,v60,v61\nv_cvt_pk_bf16_f32 v159,v62,v63\nM v[108:111],a[28:31],v[204:207],v[108:111]\nv_cvt_pk_bf16_f32 v174,v120,v121\ns_waitcnt lgkmcnt(2)\nM v[48:51],a[0:3],v[208:211],0\nv_cvt_pk_bf16_f32 v175,v122,v123\nv_cvt_pk_bf16_f32 v190,v124,v125\n" \
"M v[48:51],a[16:19],v[212:215],v[48:51]\nv_cvt_pk_bf16_f32 v191,v126,v127\ns_lshl_b32 s70,s62,5\nM v[52:55],a[4:7],v[208:211],0\ns_add_u32 s70,s70,s63\ns_lshl2_add_u32 s70,s70,s7\nM v[52:55],a[20:23],v[212:215],v[52:55]\ns_lshl2_add_u32 s79,s70,90112\n" \
"s_lshl_b32 s70,s60,4\nM v[112:115],a[8:11],v[208:211],0\ns_waitcnt vmcnt(1) lgkmcnt(0)\ns_barrier\nM v[112:115],a[24:27],v[212:215],v[112:115]\nds_read_b64_tr_b16 v[192:193],v241 offset:16384\nds_read_b64_tr_b16 v[194:195],v241 offset:16640\nM v[116:119],a[12:15],v[208:211],0\n" \
"ds_read_b64_tr_b16 v[196:197],v241 offset:24576\nds_read_b64_tr_b16 v[198:199],v241 offset:24832\nM v[116:119],a[28:31],v[212:215],v[116:119]\nds_read_b64_tr_b16 v[200:201],v242 offset:16384\nds_read_b64_tr_b16 v[202:203],v242 offset:16640\nM v[56:59],a[0:3],v[216:219],0\nds_read_b64_tr_b16 v[204:205],v242 offset:24576\nds_read_b64_tr_b16 v[206:207],v242 offset:24832\n" \
"M v[56:59],a[16:19],v[220:223],v[56:59]\nds_read_b64_tr_b16 v[208:209],v241 offset:16896\nds_read_b64_tr_b16 v[210:211],v241 offset:17152\nM v[60:63],a[4:7],v[216:219],0\nds_read_b64_tr_b16 v[212:213],v241 offset:25088\nds_read_b64_tr_b16 v[214:215],v241 offset:25344\nM v[60:63],a[20:23],v[220:223],v[60:63]\nbuffer_load_dwordx4 v243,s[40:43],s69 offen offset:0 lds\n" \
"M v[120:123],a[8:11],v[216:219],0\ns_sub_u32 s70,s70,s62\ns_add_u32 s70,s70,15\nM v[120:123],a[24:27],v[220:223],v[120:123]\ns_lshl2_add_u32 s70,s70,s7\ns_lshl_b32 s70,s70,15\nM v[124:127],a[12:15],v[216:219],0\nds_read_b64_tr_b16 v[216:217],v242 offset:16896\n" \
"ds_read_b64_tr_b16 v[218:219],v242 offset:17152\nM v[124:127],a[28:31],v[220:223],v[124:127]\ns_lshl_b32 s71,s61,8\ns_waitcnt lgkmcnt(12)\nM v[0:3],a[32:35],v[192:195],v[0:3]\nds_read_b64_tr_b16 v[220:221],v242 offset:25088\ns_waitcnt lgkmcnt(11)\nM v[0:3],a[48:51],v[196:199],v[0:3]\n" \
"buffer_load_dwordx4 v244,s[40:43],s69 offen offset:1024 lds\nM v[4:7],a[36:39],v[192:195],v[4:7]\nds_read_b64_tr_b16 v[222:223],v242 offset:25344\ns_add_u32 s70,s70,s71\nM v[4:7],a[52:55],v[196:199],v[4:7]\ns_add_u32 s78,s70,54525952\ns_lshl3_add_u32 s70,s62,s7\nM v[64:67],a[40:43],v[192:195],v[64:67]\n" \
"s_lshl_b32 s76,s70,15\ns_lshl_b32 s80,s70,6\nM v[64:67],a[56:59],v[196:199],v[64:67]\nbuffer_load_dwordx4 v245,s[40:43],s69 offen offset:2048 lds\nM v[68:71],a[44:47],v[192:195],v[68:71]\ns_add_u32 s76,s76,1048576\ns_add_u32 s77,s76,131072\nM v[68:71],a[60:63],v[196:199],v[68:71]\n" \
"s_lshl_b32 s70,s94,12\ns_waitcnt lgkmcnt(4)\nM v[8:11],a[32:35],v[200:203],v[8:11]\ns_or_b32 s76,s76,s70\ns_or_b32 s77,s77,s70\nM v[8:11],a[48:51],v[204:207],v[8:11]\nbuffer_load_dwordx4 v246,s[40:43],s69 offen offset:3072 lds\nM v[12:15],a[36:39],v[200:203],v[12:15]\n" \
"s_lshl_b32 s70,s63,5\ns_add_u32 s70,s70,s74\nM v[12:15],a[52:55],v[204:207],v[12:15]\ns_lshl_b32 s74,s70,14\ns_add_u32 s74,s74,3145728\nM v[72:75],a[40:43],v[200:203],v[72:75]\nds_read_b64_tr_b16 v[192:193],v241 offset:17408\nds_read_b64_tr_b16 v[194:195],v241 offset:17664\n" \
"M v[72:75],a[56:59],v[204:207],v[72:75]\nds_read_b64_tr_b16 v[196:197],v241 offset:25600\nds_read_b64_tr_b16 v[198:199],v241 offset:25856\nM v[76:79],a[44:47],v[200:203],v[76:79]\nds_read_b64_tr_b16 v[200:201],v242 offset:17408\nds_read_b64_tr_b16 v[202:203],v242 offset:17664\nM v[76:79],a[60:63],v[204:207],v[76:79]\nds_read_b64_tr_b16 v[204:205],v242 offset:25600\n" \
"ds_read_b64_tr_b16 v[206:207],v242 offset:25856\nM v[16:19],a[32:35],v[208:211],v[16:19]\nbuffer_store_dwordx4 v[128:131],v240,s[52:55],s74 offen offset:0 sc1\nM v[16:19],a[48:51],v[212:215],v[16:19]\ns_add_u32 m0,s64,147456\ns_add_u32 s69,s68,1572864\nM v[20:23],a[36:39],v[208:211],v[20:23]\nv_lshlrev_b32 v228,2,v228\n" \
"v_lshl_or_b32 v228,v229,4,v228\nM v[20:23],a[52:55],v[212:215],v[20:23]\ns_add_u32 s80,s80,90112\ns_lshl2_add_u32 s29,s70,81920\nM v[80:83],a[40:43],v[208:211],v[80:83]\nbuffer_load_dwordx4 v243,s[40:43],s69 offen offset:0 lds\nM v[80:83],a[56:59],v[212:215],v[80:83]\ns_lshl_b32 s70,s7,14\n" \
"v_add_u32 v250,s70,v240\nM v[84:87],a[44:47],v[208:211],v[84:87]\nds_read_b64_tr_b16 v[208:209],v241 offset:17920\nds_read_b64_tr_b16 v[210:211],v241 offset:18176\nM v[84:87],a[60:63],v[212:215],v[84:87]\nds_read_b64_tr_b16 v[212:213],v241 offset:26112\ns_waitcnt lgkmcnt(8)\nM v[24:27],a[32:35],v[216:219],v[24:27]\n" \
"ds_read_b64_tr_b16 v[214:215],v241 offset:26368\ns_lshl_b32 s70,s63,19\nM v[24:27],a[48:51],v[220:223],v[24:27]\nbuffer_load_dwordx4 v244,s[40:43],s69 offen offset:1024 lds\nM v[28:31],a[36:39],v[216:219],v[28:31]\nds_write_b64 v250,v[128:129] offset:0\ns_add_u32 s72,s78,16384\nM v[28:31],a[52:55],v[220:223],v[28:31]\n" \
"ds_write_b64 v250,v[130:131] offset:8\ns_mul_i32 s71,s60,14\nM v[88:91],a[40:43],v[216:219],v[88:91]\nds_write_b64 v250,v[132:133] offset:1024\nv_and_b32 v229,15,v249\nM v[88:91],a[56:59],v[220:223],v[88:91]\nbuffer_load_dwordx4 v245,s[40:43],s69 offen offset:2048 lds\nM v[92:95],a[44:47],v[216:219],v[92:95]\n" \
"ds_read_b64_tr_b16 v[216:217],v242 offset:17920\nds_read_b64_tr_b16 v[218:219],v242 offset:18176\nM v[92:95],a[60:63],v[220:223],v[92:95]\nds_read_b64_tr_b16 v[220:221],v242 offset:26112\nds_read_b64_tr_b16 v[222:223],v242 offset:26368\nM v[32:35],a[32:35],v[192:195],v[32:35]\nv_lshlrev_b32 v229,7,v229\ns_waitcnt lgkmcnt(10)\n" \
"M v[32:35],a[48:51],v[196:199],v[32:35]\nbuffer_load_dwordx4 v246,s[40:43],s69 offen offset:3072 lds\nM v[36:39],a[36:39],v[192:195],v[36:39]\ns_add_u32 m0,s64,98304\ns_add_u32 s69,s68,2097152\nM v[36:39],a[52:55],v[196:199],v[36:39]\nds_write_b64 v250,v[134:135] offset:1032\nv_add_u32 v253,0xffd00000,v253\n" \
"M v[96:99],a[40:43],v[192:195],v[96:99]\ns_add_u32 s44,s34,s70\ns_addc_u32 s45,s35,0\nM v[96:99],a[56:59],v[196:199],v[96:99]\nbuffer_store_dwordx4 v[132:135],v240,s[52:55],s74 offen offset:1024 sc1\nM v[100:103],a[44:47],v[192:195],v[100:103]\ns_lshl_b32 s70,s62,15\ns_add_u32 s96,s70,s64\n" \
"M v[100:103],a[60:63],v[196:199],v[100:103]\ns_sub_u32 s96,s96,16384\ns_sub_u32 s46,491520,s70\nM v[40:43],a[32:35],v[200:203],v[40:43]\ns_cmp_eq_u32 s36,s96\ns_cselect_b32 s72,0xc000,s98\ns_add_u32 s65,s36,s72\nM v[40:43],a[48:51],v[204:207],v[40:43]\n" \
"buffer_load_dwordx4 v243,s[40:43],s69 offen offset:0 lds\nM v[44:47],a[36:39],v[200:203],v[44:47]\ns_cmp_eq_u32 s65,s96\ns_cselect_b32 s72,0xc000,s98\ns_add_u32 s66,s65,s72\nM v[44:47],a[52:55],v[204:207],v[44:47]\ns_lshl2_add_u32 s70,s62,0\ns_sub_u32 s70,s70,s7\n" \
"M v[104:107],a[40:43],v[200:203],v[104:107]\ns_lshl_b32 s70,s70,16\ns_add_u32 s33,s78,s70\nM v[104:107],a[56:59],v[204:207],v[104:107]\nbuffer_load_dwordx4 v244,s[40:43],s69 offen offset:1024 lds\nM v[108:111],a[44:47],v[200:203],v[108:111]\nds_write_b64 v250,v[136:137] offset:2048\ns_lshl3_add_u32 s70,s62,s7\n" \
"M v[108:111],a[60:63],v[204:207],v[108:111]\ns_cmp_eq_u32 s66,s96\ns_cselect_b32 s72,0xc000,s98\ns_add_u32 s95,s66,s72\ns_waitcnt lgkmcnt(1)\nM v[48:51],a[32:35],v[208:211],v[48:51]\ns_cmp_eq_u32 s62,7\ns_cselect_b32 s46,229376,s46\n" \
"M v[48:51],a[48:51],v[212:215],v[48:51]\ns_waitcnt vmcnt(12)\nbuffer_store_dword v252,off,s[52:55],s79 sc1\nM v[52:55],a[36:39],v[208:211],v[52:55]\nbuffer_load_dwordx4 v245,s[40:43],s69 offen offset:2048 lds\nM v[52:55],a[52:55],v[212:215],v[52:55]\ns_lshl3_add_u32 s70,s70,s60\ns_lshl_b32 s67,s70,15\n" \
"M v[112:115],a[40:43],v[208:211],v[112:115]\ns_waitcnt vmcnt(10)\ns_barrier\nM v[112:115],a[56:59],v[212:215],v[112:115]\nds_read_b64_tr_b16 v[192:193],v241 offset:32768\nds_read_b64_tr_b16 v[194:195],v241 offset:33024\nM v[116:119],a[44:47],v[208:211],v[116:119]\nds_read_b64_tr_b16 v[196:197],v241 offset:40960\n" \
"ds_read_b64_tr_b16 v[198:199],v241 offset:41216\nM v[116:119],a[60:63],v[212:215],v[116:119]\nbuffer_store_dwordx4 v[136:139],v240,s[52:55],s74 offen offset:2048 sc1\nM v[56:59],a[32:35],v[216:219],v[56:59]\nds_read_b64_tr_b16 v[200:201],v242 offset:32768\nds_read_b64_tr_b16 v[202:203],v242 offset:33024\nM v[56:59],a[48:51],v[220:223],v[56:59]\nds_read_b64_tr_b16 v[204:205],v242 offset:40960\n" \
"ds_read_b64_tr_b16 v[206:207],v242 offset:41216\nM v[60:63],a[36:39],v[216:219],v[60:63]\nds_read_b64_tr_b16 v[208:209],v241 offset:33280\nds_read_b64_tr_b16 v[210:211],v241 offset:33536\nM v[60:63],a[52:55],v[220:223],v[60:63]\nds_read_b64_tr_b16 v[212:213],v241 offset:41472\nds_read_b64_tr_b16 v[214:215],v241 offset:41728\nM v[120:123],a[40:43],v[216:219],v[120:123]\n" \
"buffer_load_dwordx4 v246,s[40:43],s69 offen offset:3072 lds\nM v[120:123],a[56:59],v[220:223],v[120:123]\ns_add_u32 m0,s64,114688\ns_add_u32 s69,s68,2621440\nM v[124:127],a[44:47],v[216:219],v[124:127]\nds_read_b64_tr_b16 v[216:217],v242 offset:33280\nds_read_b64_tr_b16 v[218:219],v242 offset:33536\nM v[124:127],a[60:63],v[220:223],v[124:127]\n" \
"ds_read_b64_tr_b16 v[220:221],v242 offset:41472\ns_waitcnt lgkmcnt(13)\nM v[0:3],a[64:67],v[192:195],v[0:3]\nds_read_b64_tr_b16 v[222:223],v242 offset:41728\ns_waitcnt lgkmcnt(12)\nM v[0:3],a[80:83],v[196:199],v[0:3]\nbuffer_load_dwordx4 v243,s[40:43],s69 offen offset:0 lds\nM v[4:7],a[68:71],v[192:195],v[4:7]\n" \
"ds_write_b64 v250,v[138:139] offset:2056\ns_add_u32 s15,s64,s46\nM v[4:7],a[84:87],v[196:199],v[4:7]\ns_cmp_gt_i32 s62,-1\ns_cselect_b32 s72,s98,0xfffe4000\nM v[64:67],a[72:75],v[192:195],v[64:67]\ns_lshl3_add_u32 s70,s62,s60\ns_add_u32 s15,s15,s72\n" \
"M v[64:67],a[88:91],v[196:199],v[64:67]\nbuffer_load_dwordx4 v244,s[40:43],s69 offen offset:1024 lds\nM v[68:71],a[76:79],v[192:195],v[68:71]\nds_read_b64_tr_b16 v[192:193],v241 offset:33792\nds_read_b64_tr_b16 v[194:195],v241 offset:34048\nM v[68:71],a[92:95],v[196:199],v[68:71]\nds_read_b64_tr_b16 v[196:197],v241 offset:41984\ns_waitcnt lgkmcnt(8)\n" \
"M v[8:11],a[64:67],v[200:203],v[8:11]\nds_read_b64_tr_b16 v[198:199],v241 offset:42240\ns_lshl2_add_u32 s70,s70,s7\nM v[8:11],a[80:83],v[204:207],v[8:11]\nbuffer_store_dwordx4 v[140:143],v240,s[52:55],s74 offen offset:3072 sc1\nM v[12:15],a[68:71],v[200:203],v[12:15]\ns_add_u32 s74,s74,4096\ns_lshl4_add_u32 s79,s70,65536\n" \
"M v[12:15],a[84:87],v[204:207],v[12:15]\nds_write_b64 v250,v[140:141] offset:3072\ns_add_u32 s97,s96,65536\nM v[72:75],a[72:75],v[200:203],v[72:75]\ns_cmp_eq_u32 s62,7\ns_cselect_b32 s72,98304,0\nM v[72:75],a[88:91],v[204:207],v[72:75]\nbuffer_load_dwordx4 v245,s[40:43],s69 offen offset:2048 lds\n" \
"M v[76:79],a[76:79],v[200:203],v[76:79]\nds_read_b64_tr_b16 v[200:201],v242 offset:33792\nds_read_b64_tr_b16 v[202:203],v242 offset:34048\nM v[76:79],a[92:95],v[204:207],v[76:79]\nds_read_b64_tr_b16 v[204:205],v242 offset:41984\nds_read_b64_tr_b16 v[206:207],v242 offset:42240\nM v[16:19],a[64:67],v[208:211],v[16:19]\ns_lshr_b32 s70,s6,3\n" \
"s_sub_u32 s97,s97,s72\nM v[16:19],a[80:83],v[212:215],v[16:19]\nbuffer_load_dwordx4 v246,s[40:43],s69 offen offset:3072 lds\nM v[20:23],a[68:71],v[208:211],v[20:23]\nds_write_b64 v250,v[142:143] offset:3080\ns_sub_u32 s70,s70,16\nM v[20:23],a[84:87],v[212:215],v[20:23]\ns_add_u32 s2,s70,s71\n" \
"s_lshl_b32 s72,s60,1\nM v[80:83],a[72:75],v[208:211],v[80:83]\ns_mul_i32 s71,s60,448\ns_add_u32 s72,s72,s62\nM v[80:83],a[88:91],v[212:215],v[80:83]\ns_and_b32 s71,s71,0xffffff80\ns_sub_u32 s72,s72,6\nM v[84:87],a[76:79],v[208:211],v[84:87]\n" \
"ds_read_b64_tr_b16 v[208:209],v241 offset:34304\nds_read_b64_tr_b16 v[210:211],v241 offset:34560\nM v[84:87],a[92:95],v[212:215],v[84:87]\nds_read_b64_tr_b16 v[212:213],v241 offset:42496\ns_waitcnt lgkmcnt(8)\nM v[24:27],a[64:67],v[216:219],v[24:27]\nbuffer_store_dwordx4 v[144:147],v240,s[52:55],s74 offen offset:0 sc1\nM v[24:27],a[80:83],v[220:223],v[24:27]\n" \
"ds_read_b64_tr_b16 v[214:215],v241 offset:42752\ns_sub_u32 s58,1835008,s71\nM v[28:31],a[68:71],v[216:219],v[28:31]\ns_lshl_b32 s72,s72,18\ns_lshl_b32 s2,s2,5\nM v[28:31],a[84:87],v[220:223],v[28:31]\ns_cmp_ge_u32 s62,6\ns_cselect_b64 s[56:57],s[18:19],s[56:57]\n" \
"s_cselect_b32 s71,s72,s71\ns_cselect_b32 s58,0x40000,s58\ns_cselect_b32 s0,2048,s0\nM v[88:91],a[72:75],v[216:219],v[88:91]\ns_mul_i32 s72,s60,229376\nv_lshrrev_b32 v253,4,v249\nM v[88:91],a[88:91],v[220:223],v[88:91]\ns_cmp_eq_u32 s62,3\n" \
"s_cselect_b64 s[56:57],s[26:27],s[56:57]\ns_cselect_b32 s71,s72,s71\ns_cselect_b32 s58,229376,s58\ns_cselect_b32 s0,4096,s0\nM v[92:95],a[76:79],v[216:219],v[92:95]\nds_read_b64_tr_b16 v[216:217],v242 offset:34304\nds_read_b64_tr_b16 v[218:219],v242 offset:34560\nM v[92:95],a[92:95],v[220:223],v[92:95]\n" \
"ds_read_b64_tr_b16 v[220:221],v242 offset:42496\nds_read_b64_tr_b16 v[222:223],v242 offset:42752\nM v[32:35],a[64:67],v[192:195],v[32:35]\nds_write_b64 v250,v[144:145] offset:4096\nv_and_b32 v251,0xf0,v240\nM v[32:35],a[80:83],v[196:199],v[32:35]\ns_cmp_lt_u32 s70,14\ns_cselect_b32 s2,s2,0\n" \
"s_cselect_b32 s3,-1,0\nM v[36:39],a[68:71],v[192:195],v[36:39]\ns_lshl2_add_u32 s70,s61,s7\ns_mul_i32 s1,s70,s0\nM v[36:39],a[84:87],v[196:199],v[36:39]\ns_add_u32 s48,s44,19922944\ns_addc_u32 s49,s45,0\nM v[96:99],a[72:75],v[192:195],v[96:99]\n" \
"v_lshl_or_b32 v253,v253,12,v251\ns_lshl_b32 s70,s61,8\nM v[96:99],a[88:91],v[196:199],v[96:99]\ns_add_u32 s44,s44,3145728\ns_addc_u32 s45,s45,0\nM v[100:103],a[76:79],v[192:195],v[100:103]\ns_mov_b32 s50,s46\ns_mov_b32 s94,0xc000\n" \
"M v[100:103],a[92:95],v[196:199],v[100:103]\ns_sub_u32 s33,s33,1867776\ns_waitcnt lgkmcnt(5)\nM v[40:43],a[64:67],v[200:203],v[40:43]\ns_add_u32 s67,s67,36700160\ns_lshl_b32 s0,s0,4\nM v[40:43],a[80:83],v[204:207],v[40:43]\ns_cmp_lt_u32 s62,3\n" \
"s_cselect_b32 s58,0,s58\nM v[44:47],a[68:71],v[200:203],v[44:47]\nbuffer_store_dwordx4 v[148:151],v240,s[52:55],s74 offen offset:1024 sc1\nM v[44:47],a[84:87],v[204:207],v[44:47]\nds_write_b64 v250,v[146:147] offset:4104\nM v[104:107],a[72:75],v[200:203],v[104:107]\ns_add_u32 s56,s56,s71\ns_addc_u32 s57,s57,0\n" \
"M v[104:107],a[88:91],v[204:207],v[104:107]\nM v[108:111],a[76:79],v[200:203],v[108:111]\nM v[108:111],a[92:95],v[204:207],v[108:111]\nM v[48:51],a[64:67],v[208:211],v[48:51]\nM v[48:51],a[80:83],v[212:215],v[48:51]\nM v[52:55],a[68:71],v[208:211],v[52:55]\ns_add_u32 m0,s64,131072\ns_add_u32 s69,s68,3145728\n" \
"M v[52:55],a[84:87],v[212:215],v[52:55]\nM v[112:115],a[72:75],v[208:211],v[112:115]\ns_waitcnt vmcnt(14) lgkmcnt(1)\ns_barrier\nM v[112:115],a[88:91],v[212:215],v[112:115]\nds_read_b64_tr_b16 v[192:193],v241 offset:49152\nds_read_b64_tr_b16 v[194:195],v241 offset:49408\nM v[116:119],a[76:79],v[208:211],v[116:119]\n" \
"ds_read_b64_tr_b16 v[196:197],v241 offset:57344\nds_read_b64_tr_b16 v[198:199],v241 offset:57600\nM v[116:119],a[92:95],v[212:215],v[116:119]\nds_read_b64_tr_b16 v[200:201],v242 offset:49152\nds_read_b64_tr_b16 v[202:203],v242 offset:49408\nM v[56:59],a[64:67],v[216:219],v[56:59]\nds_read_b64_tr_b16 v[204:205],v242 offset:57344\nds_read_b64_tr_b16 v[206:207],v242 offset:57600\n" \
"M v[56:59],a[80:83],v[220:223],v[56:59]\nds_read_b64_tr_b16 v[208:209],v241 offset:49664\nds_read_b64_tr_b16 v[210:211],v241 offset:49920\nM v[60:63],a[68:71],v[216:219],v[60:63]\nds_read_b64_tr_b16 v[212:213],v241 offset:57856\nds_read_b64_tr_b16 v[214:215],v241 offset:58112\nM v[60:63],a[84:87],v[220:223],v[60:63]\nbuffer_load_dwordx4 v243,s[40:43],s69 offen offset:0 lds\n" \
"M v[120:123],a[72:75],v[216:219],v[120:123]\nds_write_b64 v250,v[148:149] offset:5120\nM v[120:123],a[88:91],v[220:223],v[120:123]\nM v[124:127],a[76:79],v[216:219],v[124:127]\nds_read_b64_tr_b16 v[216:217],v242 offset:49664\nds_read_b64_tr_b16 v[218:219],v242 offset:49920\nM v[124:127],a[92:95],v[220:223],v[124:127]\nds_read_b64_tr_b16 v[220:221],v242 offset:57856\n" \
"s_waitcnt lgkmcnt(14)\nM v[0:3],a[96:99],v[192:195],v[0:3]\nbuffer_store_dwordx4 v[152:155],v240,s[52:55],s74 offen offset:2048 sc1\ns_waitcnt lgkmcnt(12)\nM v[0:3],a[112:115],v[196:199],v[0:3]\nds_read_b64_tr_b16 v[222:223],v242 offset:58112\nM v[4:7],a[100:103],v[192:195],v[4:7]\nds_write_b64 v250,v[150:151] offset:5128\n" \
"M v[4:7],a[116:119],v[196:199],v[4:7]\nM v[64:67],a[104:107],v[192:195],v[64:67]\nbuffer_load_dwordx4 v244,s[40:43],s69 offen offset:1024 lds\nM v[64:67],a[120:123],v[196:199],v[64:67]\nM v[68:71],a[108:111],v[192:195],v[68:71]\nds_read_b64_tr_b16 v[192:193],v241 offset:50176\nds_read_b64_tr_b16 v[194:195],v241 offset:50432\nM v[68:71],a[124:127],v[196:199],v[68:71]\n" \
"ds_read_b64_tr_b16 v[196:197],v241 offset:58368\ns_waitcnt lgkmcnt(9)\nM v[8:11],a[96:99],v[200:203],v[8:11]\nbuffer_load_dwordx4 v245,s[40:43],s69 offen offset:2048 lds\nM v[8:11],a[112:115],v[204:207],v[8:11]\nds_read_b64_tr_b16 v[198:199],v241 offset:58624\nM v[12:15],a[100:103],v[200:203],v[12:15]\nM v[12:15],a[116:119],v[204:207],v[12:15]\n" \
"M v[72:75],a[104:107],v[200:203],v[72:75]\nbuffer_load_dwordx4 v246,s[40:43],s69 offen offset:3072 lds\nM v[72:75],a[120:123],v[204:207],v[72:75]\nM v[76:79],a[108:111],v[200:203],v[76:79]\nds_read_b64_tr_b16 v[200:201],v242 offset:50176\nds_read_b64_tr_b16 v[202:203],v242 offset:50432\nM v[76:79],a[124:127],v[204:207],v[76:79]\nds_read_b64_tr_b16 v[204:205],v242 offset:58368\n" \
"ds_read_b64_tr_b16 v[206:207],v242 offset:58624\nM v[16:19],a[96:99],v[208:211],v[16:19]\nbuffer_store_dwordx4 v[156:159],v240,s[52:55],s74 offen offset:3072 sc1\nM v[16:19],a[112:115],v[212:215],v[16:19]\nds_write_b64 v250,v[152:153] offset:6144\ns_add_u32 s74,s74,4096\nM v[20:23],a[100:103],v[208:211],v[20:23]\nM v[20:23],a[116:119],v[212:215],v[20:23]\n" \
"M v[80:83],a[104:107],v[208:211],v[80:83]\nM v[80:83],a[120:123],v[212:215],v[80:83]\nM v[84:87],a[108:111],v[208:211],v[84:87]\nds_read_b64_tr_b16 v[208:209],v241 offset:50688\nds_read_b64_tr_b16 v[210:211],v241 offset:50944\nM v[84:87],a[124:127],v[212:215],v[84:87]\nds_read_b64_tr_b16 v[212:213],v241 offset:58880\ns_waitcnt lgkmcnt(8)\n" \
"M v[24:27],a[96:99],v[216:219],v[24:27]\nds_read_b64_tr_b16 v[214:215],v241 offset:59136\nM v[24:27],a[112:115],v[220:223],v[24:27]\nM v[28:31],a[100:103],v[216:219],v[28:31]\nds_write_b64 v250,v[154:155] offset:6152\nM v[28:31],a[116:119],v[220:223],v[28:31]\nM v[88:91],a[104:107],v[216:219],v[88:91]\nM v[88:91],a[120:123],v[220:223],v[88:91]\n" \
"M v[92:95],a[108:111],v[216:219],v[92:95]\nds_read_b64_tr_b16 v[216:217],v242 offset:50688\nds_read_b64_tr_b16 v[218:219],v242 offset:50944\nM v[92:95],a[124:127],v[220:223],v[92:95]\nds_read_b64_tr_b16 v[220:221],v242 offset:58880\nds_read_b64_tr_b16 v[222:223],v242 offset:59136\nM v[32:35],a[96:99],v[192:195],v[32:35]\nbuffer_store_dwordx4 v[160:163],v240,s[52:55],s74 offen offset:0 sc1\n" \
"M v[32:35],a[112:115],v[196:199],v[32:35]\nM v[36:39],a[100:103],v[192:195],v[36:39]\nM v[36:39],a[116:119],v[196:199],v[36:39]\nM v[96:99],a[104:107],v[192:195],v[96:99]\nM v[96:99],a[120:123],v[196:199],v[96:99]\nM v[100:103],a[108:111],v[192:195],v[100:103]\nds_write_b64 v250,v[156:157] offset:7168\nM v[100:103],a[124:127],v[196:199],v[100:103]\n" \
"s_waitcnt lgkmcnt(7)\nM v[40:43],a[96:99],v[200:203],v[40:43]\nM v[40:43],a[112:115],v[204:207],v[40:43]\nM v[44:47],a[100:103],v[200:203],v[44:47]\nM v[44:47],a[116:119],v[204:207],v[44:47]\nM v[104:107],a[104:107],v[200:203],v[104:107]\nM v[104:107],a[120:123],v[204:207],v[104:107]\nM v[108:111],a[108:111],v[200:203],v[108:111]\n" \
"M v[108:111],a[124:127],v[204:207],v[108:111]\nM v[48:51],a[96:99],v[208:211],v[48:51]\ns_waitcnt lgkmcnt(3)\nM v[48:51],a[112:115],v[212:215],v[48:51]\nM v[52:55],a[100:103],v[208:211],v[52:55]\nbuffer_store_dwordx4 v[164:167],v240,s[52:55],s74 offen offset:1024 sc1\nM v[52:55],a[116:119],v[212:215],v[52:55]\ns_add_u32 m0,s64,147456\n" \
"s_add_u32 s69,s68,3670016\nM v[112:115],a[104:107],v[208:211],v[112:115]\ns_waitcnt vmcnt(15) lgkmcnt(1)\ns_barrier\nM v[112:115],a[120:123],v[212:215],v[112:115]\nds_read_b64_tr_b16 v[192:193],v241 offset:0\nds_read_b64_tr_b16 v[194:195],v241 offset:256\nM v[116:119],a[108:111],v[208:211],v[116:119]\n" \
"ds_read_b64_tr_b16 v[196:197],v241 offset:8192\nds_read_b64_tr_b16 v[198:199],v241 offset:8448\nM v[116:119],a[124:127],v[212:215],v[116:119]\nds_read_b64_tr_b16 v[200:201],v242 offset:0\nds_read_b64_tr_b16 v[202:203],v242 offset:256\nM v[56:59],a[96:99],v[216:219],v[56:59]\nds_read_b64_tr_b16 v[204:205],v242 offset:8192\nds_read_b64_tr_b16 v[206:207],v242 offset:8448\n" \
"M v[56:59],a[112:115],v[220:223],v[56:59]\nds_read_b64_tr_b16 v[208:209],v241 offset:512\nds_read_b64_tr_b16 v[210:211],v241 offset:768\nM v[60:63],a[100:103],v[216:219],v[60:63]\nds_read_b64_tr_b16 v[212:213],v241 offset:8704\nds_read_b64_tr_b16 v[214:215],v241 offset:8960\nM v[60:63],a[116:119],v[220:223],v[60:63]\nbuffer_load_dwordx4 v243,s[40:43],s69 offen offset:0 lds\n" \
"M v[120:123],a[104:107],v[216:219],v[120:123]\nds_write_b64 v250,v[158:159] offset:7176\ns_lshl_b32 s68,s60,19\nM v[120:123],a[120:123],v[220:223],v[120:123]\ns_lshl2_add_u32 s68,s64,s68\ns_add_u32 s68,s68,s70\nM v[124:127],a[108:111],v[216:219],v[124:127]\nds_read_b64_tr_b16 v[216:217],v242 offset:512\n" \
"ds_read_b64_tr_b16 v[218:219],v242 offset:768\nM v[124:127],a[124:127],v[220:223],v[124:127]\nds_read_b64_tr_b16 v[220:221],v242 offset:8704\ns_waitcnt lgkmcnt(14)\nM v[0:3],a[128:131],v[192:195],v[0:3]\nds_read_b64_tr_b16 v[222:223],v242 offset:8960\ns_waitcnt lgkmcnt(13)\nM v[0:3],a[144:147],v[196:199],v[0:3]\n" \
"buffer_load_dwordx4 v244,s[40:43],s69 offen offset:1024 lds\nM v[4:7],a[132:135],v[192:195],v[4:7]\nds_write_b64 v250,v[160:161] offset:8192\nM v[4:7],a[148:151],v[196:199],v[4:7]\nM v[64:67],a[136:139],v[192:195],v[64:67]\nM v[64:67],a[152:155],v[196:199],v[64:67]\nbuffer_store_dwordx4 v[168:171],v240,s[52:55],s74 offen offset:2048 sc1\nM v[68:71],a[140:143],v[192:195],v[68:71]\n" \
"ds_read_b64_tr_b16 v[192:193],v241 offset:1024\nds_read_b64_tr_b16 v[194:195],v241 offset:1280\nM v[68:71],a[156:159],v[196:199],v[68:71]\nds_read_b64_tr_b16 v[196:197],v241 offset:9216\ns_waitcnt lgkmcnt(9)\nM v[8:11],a[128:131],v[200:203],v[8:11]\nds_read_b64_tr_b16 v[198:199],v241 offset:9472\nM v[8:11],a[144:147],v[204:207],v[8:11]\n" \
"buffer_load_dwordx4 v245,s[40:43],s69 offen offset:2048 lds\nM v[12:15],a[132:135],v[200:203],v[12:15]\nds_write_b64 v250,v[162:163] offset:8200\nM v[12:15],a[148:151],v[204:207],v[12:15]\nM v[72:75],a[136:139],v[200:203],v[72:75]\nM v[72:75],a[152:155],v[204:207],v[72:75]\nbuffer_load_dwordx4 v246,s[40:43],s69 offen offset:3072 lds\nM v[76:79],a[140:143],v[200:203],v[76:79]\n" \
"ds_read_b64_tr_b16 v[200:201],v242 offset:1024\nds_read_b64_tr_b16 v[202:203],v242 offset:1280\nM v[76:79],a[156:159],v[204:207],v[76:79]\nds_read_b64_tr_b16 v[204:205],v242 offset:9216\nds_read_b64_tr_b16 v[206:207],v242 offset:9472\nM v[16:19],a[128:131],v[208:211],v[16:19]\nM v[16:19],a[144:147],v[212:215],v[16:19]\nM v[20:23],a[132:135],v[208:211],v[20:23]\n" \
"M v[20:23],a[148:151],v[212:215],v[20:23]\nM v[80:83],a[136:139],v[208:211],v[80:83]\nM v[80:83],a[152:155],v[212:215],v[80:83]\nM v[84:87],a[140:143],v[208:211],v[84:87]\nbuffer_load_dword v248,v228,s[52:55],s80 offen sc1\nM v[84:87],a[156:159],v[212:215],v[84:87]\nds_read_b64_tr_b16 v[208:209],v241 offset:1536\ns_waitcnt lgkmcnt(6)\n" \
"M v[24:27],a[128:131],v[216:219],v[24:27]\nbuffer_store_dwordx4 v[172:175],v240,s[52:55],s74 offen offset:3072 sc1\nM v[24:27],a[144:147],v[220:223],v[24:27]\nds_read_b64_tr_b16 v[210:211],v241 offset:1792\nds_read_b64_tr_b16 v[212:213],v241 offset:9728\nM v[28:31],a[132:135],v[216:219],v[28:31]\nds_read_b64_tr_b16 v[214:215],v241 offset:9984\ns_add_u32 s74,s74,4096\n" \
"M v[28:31],a[148:151],v[220:223],v[28:31]\nds_write_b64 v250,v[164:165] offset:9216\nM v[88:91],a[136:139],v[216:219],v[88:91]\nM v[88:91],a[152:155],v[220:223],v[88:91]\nM v[92:95],a[140:143],v[216:219],v[92:95]\nds_read_b64_tr_b16 v[216:217],v242 offset:1536\nds_read_b64_tr_b16 v[218:219],v242 offset:1792\nM v[92:95],a[156:159],v[220:223],v[92:95]\n" \
"ds_read_b64_tr_b16 v[220:221],v242 offset:9728\nds_read_b64_tr_b16 v[222:223],v242 offset:9984\nM v[32:35],a[128:131],v[192:195],v[32:35]\nM v[32:35],a[144:147],v[196:199],v[32:35]\nM v[36:39],a[132:135],v[192:195],v[36:39]\nds_write_b64 v250,v[166:167] offset:9224\nM v[36:39],a[148:151],v[196:199],v[36:39]\nM v[96:99],a[136:139],v[192:195],v[96:99]\n" \
"M v[96:99],a[152:155],v[196:199],v[96:99]\nM v[100:103],a[140:143],v[192:195],v[100:103]\nM v[100:103],a[156:159],v[196:199],v[100:103]\ns_waitcnt lgkmcnt(9)\nM v[40:43],a[128:131],v[200:203],v[40:43]\nbuffer_store_dwordx4 v[176:179],v240,s[52:55],s74 offen offset:0 sc1\nM v[40:43],a[144:147],v[204:207],v[40:43]\nM v[44:47],a[132:135],v[200:203],v[44:47]\n" \
"M v[44:47],a[148:151],v[204:207],v[44:47]\nM v[104:107],a[136:139],v[200:203],v[104:107]\nM v[104:107],a[152:155],v[204:207],v[104:107]\nM v[108:111],a[140:143],v[200:203],v[108:111]\nds_write_b64 v250,v[168:169] offset:10240\nM v[108:111],a[156:159],v[204:207],v[108:111]\ns_waitcnt lgkmcnt(4)\nM v[48:51],a[128:131],v[208:211],v[48:51]\n" \
"M v[48:51],a[144:147],v[212:215],v[48:51]\nM v[52:55],a[132:135],v[208:211],v[52:55]\ns_sub_u32 s40,s12,0x1000\ns_subb_u32 s41,s13,0\nM v[52:55],a[148:151],v[212:215],v[52:55]\ns_add_u32 m0,s64,98304\ns_xor_b32 s69,s99,0\nM v[112:115],a[136:139],v[208:211],v[112:115]\n" \
"s_waitcnt vmcnt(18) lgkmcnt(1)\ns_barrier\nM v[112:115],a[152:155],v[212:215],v[112:115]\nds_read_b64_tr_b16 v[192:193],v241 offset:16384\nds_read_b64_tr_b16 v[194:195],v241 offset:16640\nM v[116:119],a[140:143],v[208:211],v[116:119]\nds_read_b64_tr_b16 v[196:197],v241 offset:24576\nds_read_b64_tr_b16 v[198:199],v241 offset:24832\n" \
"M v[116:119],a[156:159],v[212:215],v[116:119]\nds_read_b64_tr_b16 v[200:201],v242 offset:16384\nds_read_b64_tr_b16 v[202:203],v242 offset:16640\nM v[56:59],a[128:131],v[216:219],v[56:59]\nds_read_b64_tr_b16 v[204:205],v242 offset:24576\nds_read_b64_tr_b16 v[206:207],v242 offset:24832\nM v[56:59],a[144:147],v[220:223],v[56:59]\nds_read_b64_tr_b16 v[208:209],v241 offset:16896\n" \
"ds_read_b64_tr_b16 v[210:211],v241 offset:17152\nM v[60:63],a[132:135],v[216:219],v[60:63]\nbuffer_store_dwordx4 v[180:183],v240,s[52:55],s74 offen offset:1024 sc1\nM v[60:63],a[148:151],v[220:223],v[60:63]\nds_read_b64_tr_b16 v[212:213],v241 offset:25088\nds_read_b64_tr_b16 v[214:215],v241 offset:25344\nM v[120:123],a[136:139],v[216:219],v[120:123]\nds_write_b64 v250,v[170:171] offset:10248\n" \
"s_lshl_b32 s12,s61,13\nM v[120:123],a[152:155],v[220:223],v[120:123]\nM v[124:127],a[140:143],v[216:219],v[124:127]\nds_read_b64_tr_b16 v[216:217],v242 offset:16896\nds_read_b64_tr_b16 v[218:219],v242 offset:17152\nM v[124:127],a[156:159],v[220:223],v[124:127]\nds_read_b64_tr_b16 v[220:221],v242 offset:25088\ns_waitcnt lgkmcnt(14)\n" \
"M v[0:3],a[160:163],v[192:195],v[0:3]\nds_read_b64_tr_b16 v[222:223],v242 offset:25344\ns_waitcnt lgkmcnt(13)\nM v[0:3],a[176:179],v[196:199],v[0:3]\nbuffer_load_dwordx4 v243,s[40:43],s69 offen offset:0 lds\nM v[4:7],a[164:167],v[192:195],v[4:7]\nM v[4:7],a[180:183],v[196:199],v[4:7]\nM v[64:67],a[168:171],v[192:195],v[64:67]\n" \
"M v[64:67],a[184:187],v[196:199],v[64:67]\nbuffer_load_dwordx4 v244,s[40:43],s69 offen offset:1024 lds\nM v[68:71],a[172:175],v[192:195],v[68:71]\nds_read_b64_tr_b16 v[192:193],v241 offset:17408\nds_read_b64_tr_b16 v[194:195],v241 offset:17664\nM v[68:71],a[188:191],v[196:199],v[68:71]\nds_read_b64_tr_b16 v[196:197],v241 offset:25600\ns_waitcnt lgkmcnt(9)\n" \
"M v[8:11],a[160:163],v[200:203],v[8:11]\nds_write_b64 v250,v[172:173] offset:11264\nds_read_b64_tr_b16 v[198:199],v241 offset:25856\nM v[8:11],a[176:179],v[204:207],v[8:11]\nbuffer_load_dwordx4 v245,s[40:43],s69 offen offset:2048 lds\nM v[12:15],a[164:167],v[200:203],v[12:15]\nM v[12:15],a[180:183],v[204:207],v[12:15]\nM v[72:75],a[168:171],v[200:203],v[72:75]\n" \
"M v[72:75],a[184:187],v[204:207],v[72:75]\nbuffer_store_dwordx4 v[184:187],v240,s[52:55],s74 offen offset:2048 sc1\nM v[76:79],a[172:175],v[200:203],v[76:79]\ns_waitcnt vmcnt(7)\nv_cmp_eq_u32 vcc,0,v248\nM v[76:79],a[188:191],v[204:207],v[76:79]\ns_cbranch_vccnz .Lps0\n.Lpb0:\n" \
"ds_read_b64_tr_b16 v[200:201],v242 offset:17408\nM v[16:19],a[160:163],v[208:211],v[16:19]\nds_read_b64_tr_b16 v[202:203],v242 offset:17664\ns_waitcnt lgkmcnt(7)\nM v[16:19],a[176:179],v[212:215],v[16:19]\nds_read_b64_tr_b16 v[204:205],v242 offset:25600\nds_read_b64_tr_b16 v[206:207],v242 offset:25856\nM v[20:23],a[164:167],v[208:211],v[20:23]\n" \
"buffer_load_dwordx4 a[0:3],v240,s[52:55],s76 offen offset:0 sc1\nM v[20:23],a[180:183],v[212:215],v[20:23]\nds_write_b64 v250,v[174:175] offset:11272\nv_and_b32 v228,31,v249\nM v[80:83],a[168:171],v[208:211],v[80:83]\ns_lshl_b32 s80,s63,7\nv_lshlrev_b32 v228,2,v228\nM v[80:83],a[184:187],v[212:215],v[80:83]\n" \
"s_add_u32 s80,s80,81920\nv_mov_b32 v251,v228\nM v[84:87],a[172:175],v[208:211],v[84:87]\nds_read_b64_tr_b16 v[208:209],v241 offset:17920\nds_read_b64_tr_b16 v[210:211],v241 offset:18176\nM v[84:87],a[188:191],v[212:215],v[84:87]\nds_read_b64_tr_b16 v[212:213],v241 offset:26112\nds_read_b64_tr_b16 v[214:215],v241 offset:26368\n" \
"M v[24:27],a[160:163],v[216:219],v[24:27]\nbuffer_load_dwordx4 a[16:19],v240,s[52:55],s76 offen offset:2048 sc1\nM v[24:27],a[176:179],v[220:223],v[24:27]\nM v[28:31],a[164:167],v[216:219],v[28:31]\nM v[28:31],a[180:183],v[220:223],v[28:31]\nM v[88:91],a[168:171],v[216:219],v[88:91]\nbuffer_load_dwordx4 a[4:7],v240,s[52:55],s76 offen offset:1024 sc1\nM v[88:91],a[184:187],v[220:223],v[88:91]\n" \
"M v[92:95],a[172:175],v[216:219],v[92:95]\nds_read_b64_tr_b16 v[216:217],v242 offset:17920\nds_read_b64_tr_b16 v[218:219],v242 offset:18176\nM v[92:95],a[188:191],v[220:223],v[92:95]\nds_read_b64_tr_b16 v[220:221],v242 offset:26112\ns_waitcnt lgkmcnt(7)\nM v[32:35],a[160:163],v[192:195],v[32:35]\nbuffer_store_dwordx4 v[188:191],v240,s[52:55],s74 offen offset:3072 sc1\n" \
"M v[32:35],a[176:179],v[196:199],v[32:35]\nds_read_b64_tr_b16 v[222:223],v242 offset:26368\ns_add_u32 s74,s74,16764928\nM v[36:39],a[164:167],v[192:195],v[36:39]\nds_write_b64 v250,v[176:177] offset:12288\nM v[36:39],a[180:183],v[196:199],v[36:39]\nM v[96:99],a[168:171],v[192:195],v[96:99]\nbuffer_load_dwordx4 a[20:23],v240,s[52:55],s76 offen offset:3072 sc1\n" \
"M v[96:99],a[184:187],v[196:199],v[96:99]\ns_xor_b32 s76,s76,4096\nM v[100:103],a[172:175],v[192:195],v[100:103]\nM v[100:103],a[188:191],v[196:199],v[100:103]\nM v[40:43],a[160:163],v[200:203],v[40:43]\nbuffer_load_dwordx4 a[8:11],v240,s[52:55],s77 offen offset:0 sc1\nM v[40:43],a[176:179],v[204:207],v[40:43]\nM v[44:47],a[164:167],v[200:203],v[44:47]\n" \
"ds_write_b64 v250,v[178:179] offset:12296\nM v[44:47],a[180:183],v[204:207],v[44:47]\nM v[104:107],a[168:171],v[200:203],v[104:107]\nbuffer_load_dwordx4 a[24:27],v240,s[52:55],s77 offen offset:2048 sc1\nM v[104:107],a[184:187],v[204:207],v[104:107]\nM v[108:111],a[172:175],v[200:203],v[108:111]\nM v[108:111],a[188:191],v[204:207],v[108:111]\ns_waitcnt lgkmcnt(3)\n" \
"M v[48:51],a[160:163],v[208:211],v[48:51]\nbuffer_load_dwordx4 a[12:15],v240,s[52:55],s77 offen offset:1024 sc1\nM v[48:51],a[176:179],v[212:215],v[48:51]\nM v[52:55],a[164:167],v[208:211],v[52:55]\nM v[52:55],a[180:183],v[212:215],v[52:55]\nM v[112:115],a[168:171],v[208:211],v[112:115]\ns_waitcnt lgkmcnt(1)\ns_barrier\n" \
"M v[112:115],a[184:187],v[212:215],v[112:115]\nds_read_b64_tr_b16 v[192:193],v241 offset:32768\nds_read_b64_tr_b16 v[194:195],v241 offset:33024\nM v[116:119],a[172:175],v[208:211],v[116:119]\nds_read_b64_tr_b16 v[196:197],v241 offset:40960\nds_read_b64_tr_b16 v[198:199],v241 offset:41216\nM v[116:119],a[188:191],v[212:215],v[116:119]\nds_read_b64_tr_b16 v[200:201],v242 offset:32768\n" \
"ds_read_b64_tr_b16 v[202:203],v242 offset:33024\nM v[56:59],a[160:163],v[216:219],v[56:59]\nds_read_b64_tr_b16 v[204:205],v242 offset:40960\nds_read_b64_tr_b16 v[206:207],v242 offset:41216\nM v[56:59],a[176:179],v[220:223],v[56:59]\nds_read_b64_tr_b16 v[208:209],v241 offset:33280\nds_read_b64_tr_b16 v[210:211],v241 offset:33536\nM v[60:63],a[164:167],v[216:219],v[60:63]\n" \
"ds_read_b64_tr_b16 v[212:213],v241 offset:41472\nds_read_b64_tr_b16 v[214:215],v241 offset:41728\nM v[60:63],a[180:183],v[220:223],v[60:63]\nbuffer_load_dwordx4 a[28:31],v240,s[52:55],s77 offen offset:3072 sc1\nM v[120:123],a[168:171],v[216:219],v[120:123]\nds_write_b64 v250,v[180:181] offset:13312\ns_xor_b32 s77,s77,4096\nM v[120:123],a[184:187],v[220:223],v[120:123]\n" \
"M v[124:127],a[172:175],v[216:219],v[124:127]\nds_read_b64_tr_b16 v[216:217],v242 offset:33280\nds_read_b64_tr_b16 v[218:219],v242 offset:33536\nM v[124:127],a[188:191],v[220:223],v[124:127]\nds_read_b64_tr_b16 v[220:221],v242 offset:41472\ns_waitcnt lgkmcnt(14)\nM v[0:3],a[192:195],v[192:195],v[0:3]\nds_read_b64_tr_b16 v[222:223],v242 offset:41728\n" \
"s_waitcnt lgkmcnt(13)\nM v[0:3],a[208:211],v[196:199],v[0:3]\nbuffer_load_dwordx4 v246,s[40:43],s69 offen offset:3072 lds\nM v[4:7],a[196:199],v[192:195],v[4:7]\ns_add_u32 m0,s64,114688\ns_xor_b32 s69,s99,524288\nM v[4:7],a[212:215],v[196:199],v[4:7]\nds_write_b64 v250,v[182:183] offset:13320\n" \
"M v[64:67],a[200:203],v[192:195],v[64:67]\nM v[64:67],a[216:219],v[196:199],v[64:67]\nbuffer_load_dwordx4 a[32:35],v240,s[52:55],s76 offen offset:0 sc1\nM v[68:71],a[204:207],v[192:195],v[68:71]\nds_read_b64_tr_b16 v[192:193],v241 offset:33792\nds_read_b64_tr_b16 v[194:195],v241 offset:34048\nM v[68:71],a[220:223],v[196:199],v[68:71]\nds_read_b64_tr_b16 v[196:197],v241 offset:41984\n" \
"s_waitcnt lgkmcnt(9)\nM v[8:11],a[192:195],v[200:203],v[8:11]\nds_read_b64_tr_b16 v[198:199],v241 offset:42240\nM v[8:11],a[208:211],v[204:207],v[8:11]\nbuffer_load_dwordx4 a[48:51],v240,s[52:55],s76 offen offset:2048 sc1\nM v[12:15],a[196:199],v[200:203],v[12:15]\nM v[12:15],a[212:215],v[204:207],v[12:15]\nM v[72:75],a[200:203],v[200:203],v[72:75]\n" \
"M v[72:75],a[216:219],v[204:207],v[72:75]\nbuffer_load_dwordx4 a[36:39],v240,s[52:55],s76 offen offset:1024 sc1\nM v[76:79],a[204:207],v[200:203],v[76:79]\nds_read_b64_tr_b16 v[200:201],v242 offset:33792\nds_read_b64_tr_b16 v[202:203],v242 offset:34048\nM v[76:79],a[220:223],v[204:207],v[76:79]\nds_read_b64_tr_b16 v[204:205],v242 offset:41984\nds_read_b64_tr_b16 v[206:207],v242 offset:42240\n" \
"M v[16:19],a[192:195],v[208:211],v[16:19]\nds_write_b64 v250,v[184:185] offset:14336\nM v[16:19],a[208:211],v[212:215],v[16:19]\nbuffer_load_dwordx4 a[52:55],v240,s[52:55],s76 offen offset:3072 sc1\nM v[20:23],a[196:199],v[208:211],v[20:23]\ns_xor_b32 s76,s76,12288\nM v[20:23],a[212:215],v[212:215],v[20:23]\nM v[80:83],a[200:203],v[208:211],v[80:83]\n" \
"M v[80:83],a[216:219],v[212:215],v[80:83]\nbuffer_load_dwordx4 a[40:43],v240,s[52:55],s77 offen offset:0 sc1\nM v[84:87],a[204:207],v[208:211],v[84:87]\nds_read_b64_tr_b16 v[208:209],v241 offset:34304\nds_read_b64_tr_b16 v[210:211],v241 offset:34560\nM v[84:87],a[220:223],v[212:215],v[84:87]\nds_read_b64_tr_b16 v[212:213],v241 offset:42496\ns_waitcnt lgkmcnt(8)\n" \
"M v[24:27],a[192:195],v[216:219],v[24:27]\nds_read_b64_tr_b16 v[214:215],v241 offset:42752\nM v[24:27],a[208:211],v[220:223],v[24:27]\nbuffer_load_dwordx4 a[56:59],v240,s[52:55],s77 offen offset:2048 sc1\nM v[28:31],a[196:199],v[216:219],v[28:31]\nds_write_b64 v250,v[186:187] offset:14344\nM v[28:31],a[212:215],v[220:223],v[28:31]\nM v[88:91],a[200:203],v[216:219],v[88:91]\n" \
"M v[88:91],a[216:219],v[220:223],v[88:91]\nbuffer_load_dwordx4 a[44:47],v240,s[52:55],s77 offen offset:1024 sc1\nM v[92:95],a[204:207],v[216:219],v[92:95]\nds_read_b64_tr_b16 v[216:217],v242 offset:34304\nds_read_b64_tr_b16 v[218:219],v242 offset:34560\nM v[92:95],a[220:223],v[220:223],v[92:95]\nds_read_b64_tr_b16 v[220:221],v242 offset:42496\nds_read_b64_tr_b16 v[222:223],v242 offset:42752\n" \
"M v[32:35],a[192:195],v[192:195],v[32:35]\nM v[32:35],a[208:211],v[196:199],v[32:35]\nbuffer_load_dwordx4 a[60:63],v240,s[52:55],s77 offen offset:3072 sc1\nM v[36:39],a[196:199],v[192:195],v[36:39]\ns_xor_b32 s77,s77,12288\nM v[36:39],a[212:215],v[196:199],v[36:39]\nM v[96:99],a[200:203],v[192:195],v[96:99]\nM v[96:99],a[216:219],v[196:199],v[96:99]\n" \
"buffer_load_dwordx4 v243,s[40:43],s69 offen offset:0 lds\nM v[100:103],a[204:207],v[192:195],v[100:103]\nds_write_b64 v250,v[188:189] offset:15360\nM v[100:103],a[220:223],v[196:199],v[100:103]\ns_waitcnt lgkmcnt(6)\nM v[40:43],a[192:195],v[200:203],v[40:43]\nM v[40:43],a[208:211],v[204:207],v[40:43]\nbuffer_load_dwordx4 v244,s[40:43],s69 offen offset:1024 lds\n" \
"M v[44:47],a[196:199],v[200:203],v[44:47]\nM v[44:47],a[212:215],v[204:207],v[44:47]\nM v[104:107],a[200:203],v[200:203],v[104:107]\nM v[104:107],a[216:219],v[204:207],v[104:107]\nbuffer_load_dwordx4 v245,s[40:43],s69 offen offset:2048 lds\nM v[108:111],a[204:207],v[200:203],v[108:111]\nM v[108:111],a[220:223],v[204:207],v[108:111]\nM v[48:51],a[192:195],v[208:211],v[48:51]\n" \
"M v[48:51],a[208:211],v[212:215],v[48:51]\nbuffer_load_dwordx4 v246,s[40:43],s69 offen offset:3072 lds\nM v[52:55],a[196:199],v[208:211],v[52:55]\ns_add_u32 m0,s64,131072\ns_xor_b32 s69,s99,1048576\nM v[52:55],a[212:215],v[212:215],v[52:55]\nds_write_b64 v250,v[190:191] offset:15368\nv_add_u32 v250,65536,v250\n" \
"M v[112:115],a[200:203],v[208:211],v[112:115]\ns_waitcnt lgkmcnt(2)\ns_barrier\nM v[112:115],a[216:219],v[212:215],v[112:115]\nds_read_b64_tr_b16 v[192:193],v241 offset:49152\nds_read_b64_tr_b16 v[194:195],v241 offset:49408\nM v[116:119],a[204:207],v[208:211],v[116:119]\nds_read_b64_tr_b16 v[196:197],v241 offset:57344\n" \
"ds_read_b64_tr_b16 v[198:199],v241 offset:57600\nM v[116:119],a[220:223],v[212:215],v[116:119]\nds_read_b64_tr_b16 v[200:201],v242 offset:49152\nds_read_b64_tr_b16 v[202:203],v242 offset:49408\nM v[56:59],a[192:195],v[216:219],v[56:59]\nds_read_b64_tr_b16 v[204:205],v242 offset:57344\nds_read_b64_tr_b16 v[206:207],v242 offset:57600\nM v[56:59],a[208:211],v[220:223],v[56:59]\n" \
"ds_read_b64_tr_b16 v[208:209],v241 offset:49664\nds_read_b64_tr_b16 v[210:211],v241 offset:49920\nM v[60:63],a[196:199],v[216:219],v[60:63]\nds_read_b64_tr_b16 v[212:213],v241 offset:57856\nds_read_b64_tr_b16 v[214:215],v241 offset:58112\nM v[60:63],a[212:215],v[220:223],v[60:63]\nbuffer_load_dwordx4 a[64:67],v240,s[52:55],s76 offen offset:0 sc1\nM v[120:123],a[200:203],v[216:219],v[120:123]\n" \
"M v[120:123],a[216:219],v[220:223],v[120:123]\nM v[124:127],a[204:207],v[216:219],v[124:127]\nds_read_b64_tr_b16 v[216:217],v242 offset:49664\nds_read_b64_tr_b16 v[218:219],v242 offset:49920\nM v[124:127],a[220:223],v[220:223],v[124:127]\nds_read_b64_tr_b16 v[220:221],v242 offset:57856\ns_waitcnt lgkmcnt(13)\nM v[0:3],a[224:227],v[192:195],v[0:3]\n" \
"ds_read_b64_tr_b16 v[222:223],v242 offset:58112\ns_waitcnt lgkmcnt(12)\nM v[0:3],a[240:243],v[196:199],v[0:3]\nbuffer_load_dwordx4 a[68:71],v240,s[52:55],s76 offen offset:1024 sc1\nM v[4:7],a[228:231],v[192:195],v[4:7]\nM v[4:7],a[244:247],v[196:199],v[4:7]\nM v[64:67],a[232:235],v[192:195],v[64:67]\nM v[64:67],a[248:251],v[196:199],v[64:67]\n" \
"buffer_load_dwordx4 a[80:83],v240,s[52:55],s76 offen offset:2048 sc1\nM v[68:71],a[236:239],v[192:195],v[68:71]\nds_read_b64_tr_b16 v[192:193],v241 offset:50176\nds_read_b64_tr_b16 v[194:195],v241 offset:50432\nM v[68:71],a[252:255],v[196:199],v[68:71]\nds_read_b64_tr_b16 v[196:197],v241 offset:58368\ns_waitcnt lgkmcnt(7)\nM v[8:11],a[224:227],v[200:203],v[8:11]\n" \
"ds_read_b64_tr_b16 v[198:199],v241 offset:58624\nv_cvt_pk_bf16_f32 v128,v0,v1\nM v[8:11],a[240:243],v[204:207],v[8:11]\nv_cvt_pk_bf16_f32 v129,v2,v3\nv_cvt_pk_bf16_f32 v130,v4,v5\nM v[12:15],a[228:231],v[200:203],v[12:15]\nv_cvt_pk_bf16_f32 v131,v6,v7\nv_cvt_pk_bf16_f32 v160,v64,v65\n" \
"M v[12:15],a[244:247],v[204:207],v[12:15]\nv_cvt_pk_bf16_f32 v161,v66,v67\nv_cvt_pk_bf16_f32 v162,v68,v69\nM v[72:75],a[232:235],v[200:203],v[72:75]\nv_cvt_pk_bf16_f32 v163,v70,v71\nv_cvt_pk_bf16_f32 v132,v8,v9\nM v[72:75],a[248:251],v[204:207],v[72:75]\nbuffer_load_dwordx4 a[84:87],v240,s[52:55],s76 offen offset:3072 sc1\n" \
"M v[76:79],a[236:239],v[200:203],v[76:79]\nds_read_b64_tr_b16 v[200:201],v242 offset:50176\nds_read_b64_tr_b16 v[202:203],v242 offset:50432\nM v[76:79],a[252:255],v[204:207],v[76:79]\nds_read_b64_tr_b16 v[204:205],v242 offset:58368\nds_read_b64_tr_b16 v[206:207],v242 offset:58624\nM v[16:19],a[224:227],v[208:211],v[16:19]\nv_cvt_pk_bf16_f32 v133,v10,v11\n" \
"v_cvt_pk_bf16_f32 v134,v12,v13\nM v[16:19],a[240:243],v[212:215],v[16:19]\nv_cvt_pk_bf16_f32 v135,v14,v15\nv_cvt_pk_bf16_f32 v164,v72,v73\nM v[20:23],a[228:231],v[208:211],v[20:23]\nv_cvt_pk_bf16_f32 v165,v74,v75\nv_cvt_pk_bf16_f32 v166,v76,v77\nM v[20:23],a[244:247],v[212:215],v[20:23]\n" \
"v_cvt_pk_bf16_f32 v167,v78,v79\ns_xor_b32 s76,s76,4096\nM v[80:83],a[232:235],v[208:211],v[80:83]\nv_cvt_pk_bf16_f32 v136,v16,v17\nv_cvt_pk_bf16_f32 v137,v18,v19\nM v[80:83],a[248:251],v[212:215],v[80:83]\nbuffer_load_dwordx4 a[72:75],v240,s[52:55],s77 offen offset:0 sc1\nM v[84:87],a[236:239],v[208:211],v[84:87]\n" \
"ds_read_b64_tr_b16 v[208:209],v241 offset:50688\nds_read_b64_tr_b16 v[210:211],v241 offset:50944\nM v[84:87],a[252:255],v[212:215],v[84:87]\nds_read_b64_tr_b16 v[212:213],v241 offset:58880\ns_waitcnt lgkmcnt(7)\nM v[24:27],a[224:227],v[216:219],v[24:27]\nds_read_b64_tr_b16 v[214:215],v241 offset:59136\nv_cvt_pk_bf16_f32 v138,v20,v21\n" \
"M v[24:27],a[240:243],v[220:223],v[24:27]\nv_cvt_pk_bf16_f32 v139,v22,v23\nv_cvt_pk_bf16_f32 v168,v80,v81\nM v[28:31],a[228:231],v[216:219],v[28:31]\nv_cvt_pk_bf16_f32 v169,v82,v83\nv_cvt_pk_bf16_f32 v170,v84,v85\nM v[28:31],a[244:247],v[220:223],v[28:31]\nbuffer_load_dwordx4 a[88:91],v240,s[52:55],s77 offen offset:2048 sc1\n" \
"M v[88:91],a[232:235],v[216:219],v[88:91]\nv_cvt_pk_bf16_f32 v171,v86,v87\nv_cvt_pk_bf16_f32 v140,v24,v25\nM v[88:91],a[248:251],v[220:223],v[88:91]\nv_cvt_pk_bf16_f32 v141,v26,v27\nM v[92:95],a[236:239],v[216:219],v[92:95]\nds_read_b64_tr_b16 v[216:217],v242 offset:50688\nds_read_b64_tr_b16 v[218:219],v242 offset:50944\n" \
"M v[92:95],a[252:255],v[220:223],v[92:95]\nds_read_b64_tr_b16 v[220:221],v242 offset:58880\nds_read_b64_tr_b16 v[222:223],v242 offset:59136\nM v[32:35],a[224:227],v[192:195],v[32:35]\nv_cvt_pk_bf16_f32 v142,v28,v29\nv_cvt_pk_bf16_f32 v143,v30,v31\nM v[32:35],a[240:243],v[196:199],v[32:35]\nv_cvt_pk_bf16_f32 v172,v88,v89\n" \
"v_cvt_pk_bf16_f32 v173,v90,v91\nM v[36:39],a[228:231],v[192:195],v[36:39]\nv_cvt_pk_bf16_f32 v174,v92,v93\nv_cvt_pk_bf16_f32 v175,v94,v95\nM v[36:39],a[244:247],v[196:199],v[36:39]\nbuffer_load_dwordx4 a[76:79],v240,s[52:55],s77 offen offset:1024 sc1\nM v[96:99],a[232:235],v[192:195],v[96:99]\nv_cvt_pk_bf16_f32 v144,v32,v33\n" \
"v_cvt_pk_bf16_f32 v145,v34,v35\nM v[96:99],a[248:251],v[196:199],v[96:99]\nM v[100:103],a[236:239],v[192:195],v[100:103]\nM v[100:103],a[252:255],v[196:199],v[100:103]\ns_waitcnt vmcnt(20)\nbuffer_store_dword v252,off,s[52:55],s29 sc1\ns_waitcnt lgkmcnt(4)\nM v[40:43],a[224:227],v[200:203],v[40:43]\n" \
"v_cvt_pk_bf16_f32 v146,v36,v37\nv_cvt_pk_bf16_f32 v147,v38,v39\nM v[40:43],a[240:243],v[204:207],v[40:43]\nv_cvt_pk_bf16_f32 v176,v96,v97\nv_cvt_pk_bf16_f32 v177,v98,v99\nM v[44:47],a[228:231],v[200:203],v[44:47]\nv_cvt_pk_bf16_f32 v178,v100,v101\nv_cvt_pk_bf16_f32 v179,v102,v103\n" \
"M v[44:47],a[244:247],v[204:207],v[44:47]\nbuffer_load_dwordx4 a[92:95],v240,s[52:55],s77 offen offset:3072 sc1\nM v[104:107],a[232:235],v[200:203],v[104:107]\nv_cvt_pk_bf16_f32 v148,v40,v41\nv_cvt_pk_bf16_f32 v149,v42,v43\nM v[104:107],a[248:251],v[204:207],v[104:107]\ns_xor_b32 s77,s77,4096\ns_add_u32 s29,s29,4096\n" \
"M v[108:111],a[236:239],v[200:203],v[108:111]\nv_cvt_pk_bf16_f32 v150,v44,v45\nv_cvt_pk_bf16_f32 v151,v46,v47\nM v[108:111],a[252:255],v[204:207],v[108:111]\nbuffer_load_dwordx4 v243,s[40:43],s69 offen offset:0 lds\nM v[48:51],a[224:227],v[208:211],v[48:51]\nv_cvt_pk_bf16_f32 v180,v104,v105\nv_cvt_pk_bf16_f32 v181,v106,v107\n" \
"M v[48:51],a[240:243],v[212:215],v[48:51]\nM v[52:55],a[228:231],v[208:211],v[52:55]\nM v[52:55],a[244:247],v[212:215],v[52:55]\nbuffer_load_dwordx4 v244,s[40:43],s69 offen offset:1024 lds\nM v[112:115],a[232:235],v[208:211],v[112:115]\ns_waitcnt vmcnt(15) lgkmcnt(0)\ns_barrier\nM v[112:115],a[248:251],v[212:215],v[112:115]\n" \
"ds_read_b64_tr_b16 v[192:193],v241 offset:0\nds_read_b64_tr_b16 v[194:195],v241 offset:256\nM v[116:119],a[236:239],v[208:211],v[116:119]\nds_read_b64_tr_b16 v[196:197],v241 offset:8192\nds_read_b64_tr_b16 v[198:199],v241 offset:8448\nM v[116:119],a[252:255],v[212:215],v[116:119]\nds_read_b64_tr_b16 v[200:201],v242 offset:0\nds_read_b64_tr_b16 v[202:203],v242 offset:256\n" \
"M v[56:59],a[224:227],v[216:219],v[56:59]\nds_read_b64_tr_b16 v[204:205],v242 offset:8192\nds_read_b64_tr_b16 v[206:207],v242 offset:8448\nM v[56:59],a[240:243],v[220:223],v[56:59]\nds_read_b64_tr_b16 v[208:209],v241 offset:512\nds_read_b64_tr_b16 v[210:211],v241 offset:768\nM v[60:63],a[228:231],v[216:219],v[60:63]\nds_read_b64_tr_b16 v[212:213],v241 offset:8704\n" \
"ds_read_b64_tr_b16 v[214:215],v241 offset:8960\nM v[60:63],a[244:247],v[220:223],v[60:63]\nv_cvt_pk_bf16_f32 v182,v108,v109\nv_cvt_pk_bf16_f32 v183,v110,v111\nM v[120:123],a[232:235],v[216:219],v[120:123]\nv_cvt_pk_bf16_f32 v152,v48,v49\nv_cvt_pk_bf16_f32 v153,v50,v51\nM v[120:123],a[248:251],v[220:223],v[120:123]\n" \
"v_cvt_pk_bf16_f32 v154,v52,v53\nv_cvt_pk_bf16_f32 v155,v54,v55\nM v[124:127],a[236:239],v[216:219],v[124:127]\nds_read_b64_tr_b16 v[216:217],v242 offset:512\nds_read_b64_tr_b16 v[218:219],v242 offset:768\nM v[124:127],a[252:255],v[220:223],v[124:127]\nds_read_b64_tr_b16 v[220:221],v242 offset:8704\ns_waitcnt lgkmcnt(13)\n" \
"M v[0:3],v[192:195],a[0:3],0\nds_read_b64_tr_b16 v[222:223],v242 offset:8960\ns_waitcnt lgkmcnt(12)\nM v[0:3],v[196:199],a[16:19],v[0:3]\nv_cvt_pk_bf16_f32 v184,v112,v113\nv_cvt_pk_bf16_f32 v185,v114,v115\nM v[4:7],v[192:195],a[4:7],0\nv_cvt_pk_bf16_f32 v186,v116,v117\n" \
"v_cvt_pk_bf16_f32 v187,v118,v119\nM v[4:7],v[196:199],a[20:23],v[4:7]\nv_cvt_pk_bf16_f32 v156,v56,v57\nv_cvt_pk_bf16_f32 v157,v58,v59\nM v[64:67],v[192:195],a[8:11],0\nv_cvt_pk_bf16_f32 v158,v60,v61\nv_cvt_pk_bf16_f32 v159,v62,v63\nM v[64:67],v[196:199],a[24:27],v[64:67]\n" \
"v_cvt_pk_bf16_f32 v188,v120,v121\nv_cvt_pk_bf16_f32 v189,v122,v123\nM v[68:71],v[192:195],a[12:15],0\nds_read_b64_tr_b16 v[192:193],v241 offset:1024\nds_read_b64_tr_b16 v[194:195],v241 offset:1280\nM v[68:71],v[196:199],a[28:31],v[68:71]\nds_read_b64_tr_b16 v[196:197],v241 offset:9216\ns_waitcnt lgkmcnt(7)\n" \
"M v[8:11],v[200:203],a[0:3],0\nds_read_b64_tr_b16 v[198:199],v241 offset:9472\nv_cvt_pk_bf16_f32 v190,v124,v125\nM v[8:11],v[204:207],a[16:19],v[8:11]\nbuffer_load_dwordx4 v245,s[40:43],s69 offen offset:2048 lds\nM v[12:15],v[200:203],a[4:7],0\nv_cvt_pk_bf16_f32 v191,v126,v127\nM v[12:15],v[204:207],a[20:23],v[12:15]\n" \
"M v[72:75],v[200:203],a[8:11],0\nM v[72:75],v[204:207],a[24:27],v[72:75]\nbuffer_load_dwordx4 v246,s[40:43],s69 offen offset:3072 lds\nM v[76:79],v[200:203],a[12:15],0\nds_read_b64_tr_b16 v[200:201],v242 offset:1024\nds_read_b64_tr_b16 v[202:203],v242 offset:1280\nM v[76:79],v[204:207],a[28:31],v[76:79]\nds_read_b64_tr_b16 v[204:205],v242 offset:9216\n" \
"ds_read_b64_tr_b16 v[206:207],v242 offset:9472\nM v[16:19],v[208:211],a[0:3],0\ns_add_u32 m0,s64,147456\ns_xor_b32 s69,s99,1572864\nM v[16:19],v[212:215],a[16:19],v[16:19]\nbuffer_load_dwordx4 a[96:99],v240,s[52:55],s76 offen offset:0 sc1\nM v[20:23],v[208:211],a[4:7],0\nM v[20:23],v[212:215],a[20:23],v[20:23]\n" \
"M v[80:83],v[208:211],a[8:11],0\nM v[80:83],v[212:215],a[24:27],v[80:83]\nbuffer_store_dwordx4 v[128:131],v240,s[52:55],s74 offen offset:0 sc1\nM v[84:87],v[208:211],a[12:15],0\nds_read_b64_tr_b16 v[208:209],v241 offset:1536\nds_read_b64_tr_b16 v[210:211],v241 offset:1792\nM v[84:87],v[212:215],a[28:31],v[84:87]\nds_read_b64_tr_b16 v[212:213],v241 offset:9728\n" \
"s_waitcnt lgkmcnt(7)\nM v[24:27],v[216:219],a[0:3],0\nds_read_b64_tr_b16 v[214:215],v241 offset:9984\nM v[24:27],v[220:223],a[16:19],v[24:27]\nbuffer_load_dwordx4 a[112:115],v240,s[52:55],s76 offen offset:2048 sc1\nM v[28:31],v[216:219],a[4:7],0\nM v[28:31],v[220:223],a[20:23],v[28:31]\nM v[88:91],v[216:219],a[8:11],0\n" \
"M v[88:91],v[220:223],a[24:27],v[88:91]\nbuffer_load_dwordx4 a[100:103],v240,s[52:55],s76 offen offset:1024 sc1\nM v[92:95],v[216:219],a[12:15],0\nds_read_b64_tr_b16 v[216:217],v242 offset:1536\nds_read_b64_tr_b16 v[218:219],v242 offset:1792\nM v[92:95],v[220:223],a[28:31],v[92:95]\nds_read_b64_tr_b16 v[220:221],v242 offset:9728\nds_read_b64_tr_b16 v[222:223],v242 offset:9984\n" \
"M v[32:35],v[192:195],a[0:3],0\nM v[32:35],v[196:199],a[16:19],v[32:35]\nbuffer_load_dwordx4 a[116:119],v240,s[52:55],s76 offen offset:3072 sc1\nM v[36:39],v[192:195],a[4:7],0\ns_xor_b32 s76,s76,28672\nM v[36:39],v[196:199],a[20:23],v[36:39]\nM v[96:99],v[192:195],a[8:11],0\nM v[96:99],v[196:199],a[24:27],v[96:99]\n" \
"buffer_load_dwordx4 a[104:107],v240,s[52:55],s77 offen offset:0 sc1\nM v[100:103],v[192:195],a[12:15],0\nM v[100:103],v[196:199],a[28:31],v[100:103]\ns_waitcnt lgkmcnt(4)\nM v[40:43],v[200:203],a[0:3],0\nM v[40:43],v[204:207],a[16:19],v[40:43]\nbuffer_load_dwordx4 a[120:123],v240,s[52:55],s77 offen offset:2048 sc1\nM v[44:47],v[200:203],a[4:7],0\n" \
"M v[44:47],v[204:207],a[20:23],v[44:47]\nM v[104:107],v[200:203],a[8:11],0\nM v[104:107],v[204:207],a[24:27],v[104:107]\nbuffer_load_dwordx4 a[108:111],v240,s[52:55],s77 offen offset:1024 sc1\nM v[108:111],v[200:203],a[12:15],0\nM v[108:111],v[204:207],a[28:31],v[108:111]\nM v[48:51],v[208:211],a[0:3],0\nM v[48:51],v[212:215],a[16:19],v[48:51]\n" \
"buffer_load_dwordx4 a[124:127],v240,s[52:55],s77 offen offset:3072 sc1\nM v[52:55],v[208:211],a[4:7],0\ns_xor_b32 s77,s77,28672\nM v[52:55],v[212:215],a[20:23],v[52:55]\nM v[112:115],v[208:211],a[8:11],0\ns_waitcnt vmcnt(15) lgkmcnt(0)\ns_barrier\nM v[112:115],v[212:215],a[24:27],v[112:115]\n" \
"ds_read_b64_tr_b16 v[192:193],v241 offset:16384\nds_read_b64_tr_b16 v[194:195],v241 offset:16640\nM v[116:119],v[208:211],a[12:15],0\nds_read_b64_tr_b16 v[196:197],v241 offset:24576\nds_read_b64_tr_b16 v[198:199],v241 offset:24832\nbuffer_store_dwordx4 v[132:135],v240,s[52:55],s74 offen offset:1024 sc1\nM v[116:119],v[212:215],a[28:31],v[116:119]\nds_read_b64_tr_b16 v[200:201],v242 offset:16384\n" \
"ds_read_b64_tr_b16 v[202:203],v242 offset:16640\nM v[56:59],v[216:219],a[0:3],0\nds_read_b64_tr_b16 v[204:205],v242 offset:24576\nds_read_b64_tr_b16 v[206:207],v242 offset:24832\nM v[56:59],v[220:223],a[16:19],v[56:59]\nds_read_b64_tr_b16 v[208:209],v241 offset:16896\nds_read_b64_tr_b16 v[210:211],v241 offset:17152\nM v[60:63],v[216:219],a[4:7],0\n" \
"ds_read_b64_tr_b16 v[212:213],v241 offset:25088\nds_read_b64_tr_b16 v[214:215],v241 offset:25344\nM v[60:63],v[220:223],a[20:23],v[60:63]\nbuffer_load_dwordx4 v243,s[40:43],s69 offen offset:0 lds\nM v[120:123],v[216:219],a[8:11],0\nM v[120:123],v[220:223],a[24:27],v[120:123]\nM v[124:127],v[216:219],a[12:15],0\nds_read_b64_tr_b16 v[216:217],v242 offset:16896\n" \
"ds_read_b64_tr_b16 v[218:219],v242 offset:17152\nM v[124:127],v[220:223],a[28:31],v[124:127]\nds_read_b64_tr_b16 v[220:221],v242 offset:25088\ns_waitcnt lgkmcnt(13)\nM v[0:3],v[192:195],a[32:35],v[0:3]\nds_read_b64_tr_b16 v[222:223],v242 offset:25344\ns_waitcnt lgkmcnt(12)\nM v[0:3],v[196:199],a[48:51],v[0:3]\n" \
"buffer_load_dwordx4 v244,s[40:43],s69 offen offset:1024 lds\nM v[4:7],v[192:195],a[36:39],v[4:7]\nM v[4:7],v[196:199],a[52:55],v[4:7]\nM v[64:67],v[192:195],a[40:43],v[64:67]\nM v[64:67],v[196:199],a[56:59],v[64:67]\nbuffer_load_dwordx4 v245,s[40:43],s69 offen offset:2048 lds\nM v[68:71],v[192:195],a[44:47],v[68:71]\nds_read_b64_tr_b16 v[192:193],v241 offset:17408\n" \
"ds_read_b64_tr_b16 v[194:195],v241 offset:17664\nM v[68:71],v[196:199],a[60:63],v[68:71]\nds_read_b64_tr_b16 v[196:197],v241 offset:25600\ns_waitcnt lgkmcnt(7)\nM v[8:11],v[200:203],a[32:35],v[8:11]\nds_read_b64_tr_b16 v[198:199],v241 offset:25856\nM v[8:11],v[204:207],a[48:51],v[8:11]\nbuffer_load_dwordx4 v246,s[40:43],s69 offen offset:3072 lds\n" \
"M v[12:15],v[200:203],a[36:39],v[12:15]\ns_add_u32 m0,s64,98304\ns_xor_b32 s69,s99,2097152\nM v[12:15],v[204:207],a[52:55],v[12:15]\nM v[72:75],v[200:203],a[40:43],v[72:75]\nM v[72:75],v[204:207],a[56:59],v[72:75]\nbuffer_load_dwordx4 a[128:131],v240,s[52:55],s76 offen offset:0 sc1\nM v[76:79],v[200:203],a[44:47],v[76:79]\n" \
"ds_read_b64_tr_b16 v[200:201],v242 offset:17408\nds_read_b64_tr_b16 v[202:203],v242 offset:17664\nM v[76:79],v[204:207],a[60:63],v[76:79]\nds_read_b64_tr_b16 v[204:205],v242 offset:25600\nds_read_b64_tr_b16 v[206:207],v242 offset:25856\nM v[16:19],v[208:211],a[32:35],v[16:19]\nM v[16:19],v[212:215],a[48:51],v[16:19]\nbuffer_store_dwordx4 v[136:139],v240,s[52:55],s74 offen offset:2048 sc1\n" \
"M v[20:23],v[208:211],a[36:39],v[20:23]\nM v[20:23],v[212:215],a[52:55],v[20:23]\nM v[80:83],v[208:211],a[40:43],v[80:83]\nM v[80:83],v[212:215],a[56:59],v[80:83]\nbuffer_load_dwordx4 a[144:147],v240,s[52:55],s76 offen offset:2048 sc1\nM v[84:87],v[208:211],a[44:47],v[84:87]\nds_read_b64_tr_b16 v[208:209],v241 offset:17920\nds_read_b64_tr_b16 v[210:211],v241 offset:18176\n" \
"M v[84:87],v[212:215],a[60:63],v[84:87]\nds_read_b64_tr_b16 v[212:213],v241 offset:26112\ns_waitcnt lgkmcnt(7)\nM v[24:27],v[216:219],a[32:35],v[24:27]\nds_read_b64_tr_b16 v[214:215],v241 offset:26368\nM v[24:27],v[220:223],a[48:51],v[24:27]\nbuffer_load_dwordx4 a[132:135],v240,s[52:55],s76 offen offset:1024 sc1\nM v[28:31],v[216:219],a[36:39],v[28:31]\n" \
"M v[28:31],v[220:223],a[52:55],v[28:31]\nM v[88:91],v[216:219],a[40:43],v[88:91]\nM v[88:91],v[220:223],a[56:59],v[88:91]\nbuffer_load_dwordx4 a[148:151],v240,s[52:55],s76 offen offset:3072 sc1\nM v[92:95],v[216:219],a[44:47],v[92:95]\nds_read_b64_tr_b16 v[216:217],v242 offset:17920\nds_read_b64_tr_b16 v[218:219],v242 offset:18176\nM v[92:95],v[220:223],a[60:63],v[92:95]\n" \
"ds_read_b64_tr_b16 v[220:221],v242 offset:26112\nds_read_b64_tr_b16 v[222:223],v242 offset:26368\nM v[32:35],v[192:195],a[32:35],v[32:35]\ns_xor_b32 s76,s76,4096\nM v[32:35],v[196:199],a[48:51],v[32:35]\nbuffer_load_dwordx4 a[136:139],v240,s[52:55],s77 offen offset:0 sc1\nM v[36:39],v[192:195],a[36:39],v[36:39]\nM v[36:39],v[196:199],a[52:55],v[36:39]\n" \
"M v[96:99],v[192:195],a[40:43],v[96:99]\nM v[96:99],v[196:199],a[56:59],v[96:99]\nbuffer_load_dwordx4 a[152:155],v240,s[52:55],s77 offen offset:2048 sc1\nM v[100:103],v[192:195],a[44:47],v[100:103]\nM v[100:103],v[196:199],a[60:63],v[100:103]\ns_waitcnt lgkmcnt(4)\nM v[40:43],v[200:203],a[32:35],v[40:43]\nM v[40:43],v[204:207],a[48:51],v[40:43]\n" \
"buffer_load_dwordx4 a[140:143],v240,s[52:55],s77 offen offset:1024 sc1\nM v[44:47],v[200:203],a[36:39],v[44:47]\nM v[44:47],v[204:207],a[52:55],v[44:47]\nM v[104:107],v[200:203],a[40:43],v[104:107]\nM v[104:107],v[204:207],a[56:59],v[104:107]\nbuffer_load_dwordx4 a[156:159],v240,s[52:55],s77 offen offset:3072 sc1\nM v[108:111],v[200:203],a[44:47],v[108:111]\ns_xor_b32 s77,s77,4096\n" \
"M v[108:111],v[204:207],a[60:63],v[108:111]\nM v[48:51],v[208:211],a[32:35],v[48:51]\nM v[48:51],v[212:215],a[48:51],v[48:51]\nbuffer_store_dwordx4 v[140:143],v240,s[52:55],s74 offen offset:3072 sc1\nM v[52:55],v[208:211],a[36:39],v[52:55]\ns_add_u32 s74,s74,4096\nM v[52:55],v[212:215],a[52:55],v[52:55]\nM v[112:115],v[208:211],a[40:43],v[112:115]\n" \
"s_waitcnt vmcnt(23) lgkmcnt(0)\ns_barrier\nM v[112:115],v[212:215],a[56:59],v[112:115]\nds_read_b64_tr_b16 v[192:193],v241 offset:32768\nds_read_b64_tr_b16 v[194:195],v241 offset:33024\nM v[116:119],v[208:211],a[44:47],v[116:119]\nds_read_b64_tr_b16 v[196:197],v241 offset:40960\nds_read_b64_tr_b16 v[198:199],v241 offset:41216\n" \
"M v[116:119],v[212:215],a[60:63],v[116:119]\nds_read_b64_tr_b16 v[200:201],v242 offset:32768\nds_read_b64_tr_b16 v[202:203],v242 offset:33024\nM v[56:59],v[216:219],a[32:35],v[56:59]\nds_read_b64_tr_b16 v[204:205],v242 offset:40960\nds_read_b64_tr_b16 v[206:207],v242 offset:41216\nM v[56:59],v[220:223],a[48:51],v[56:59]\nds_read_b64_tr_b16 v[208:209],v241 offset:33280\n" \
"ds_read_b64_tr_b16 v[210:211],v241 offset:33536\nM v[60:63],v[216:219],a[36:39],v[60:63]\nds_read_b64_tr_b16 v[212:213],v241 offset:41472\nds_read_b64_tr_b16 v[214:215],v241 offset:41728\nM v[60:63],v[220:223],a[52:55],v[60:63]\nbuffer_load_dwordx4 v243,s[40:43],s69 offen offset:0 lds\nM v[120:123],v[216:219],a[40:43],v[120:123]\nM v[120:123],v[220:223],a[56:59],v[120:123]\n" \
"M v[124:127],v[216:219],a[44:47],v[124:127]\nds_read_b64_tr_b16 v[216:217],v242 offset:33280\nds_read_b64_tr_b16 v[218:219],v242 offset:33536\nM v[124:127],v[220:223],a[60:63],v[124:127]\nds_read_b64_tr_b16 v[220:221],v242 offset:41472\ns_waitcnt lgkmcnt(13)\nM v[0:3],v[192:195],a[64:67],v[0:3]\nds_read_b64_tr_b16 v[222:223],v242 offset:41728\n" \
"s_waitcnt lgkmcnt(12)\nM v[0:3],v[196:199],a[80:83],v[0:3]\nbuffer_load_dwordx4 v244,s[40:43],s69 offen offset:1024 lds\nM v[4:7],v[192:195],a[68:71],v[4:7]\nM v[4:7],v[196:199],a[84:87],v[4:7]\nM v[64:67],v[192:195],a[72:75],v[64:67]\nM v[64:67],v[196:199],a[88:91],v[64:67]\nbuffer_load_dwordx4 v245,s[40:43],s69 offen offset:2048 lds\n" \
"M v[68:71],v[192:195],a[76:79],v[68:71]\nds_read_b64_tr_b16 v[192:193],v241 offset:33792\nds_read_b64_tr_b16 v[194:195],v241 offset:34048\nM v[68:71],v[196:199],a[92:95],v[68:71]\nds_read_b64_tr_b16 v[196:197],v241 offset:41984\ns_waitcnt lgkmcnt(7)\nM v[8:11],v[200:203],a[64:67],v[8:11]\nds_read_b64_tr_b16 v[198:199],v241 offset:42240\n" \
"M v[8:11],v[204:207],a[80:83],v[8:11]\nbuffer_load_dwordx4 v246,s[40:43],s69 offen offset:3072 lds\nM v[12:15],v[200:203],a[68:71],v[12:15]\ns_add_u32 m0,s64,114688\ns_xor_b32 s69,s99,2621440\nM v[12:15],v[204:207],a[84:87],v[12:15]\nM v[72:75],v[200:203],a[72:75],v[72:75]\nM v[72:75],v[204:207],a[88:91],v[72:75]\n" \
"buffer_store_dwordx4 v[144:147],v240,s[52:55],s74 offen offset:0 sc1\nM v[76:79],v[200:203],a[76:79],v[76:79]\nds_read_b64_tr_b16 v[200:201],v242 offset:33792\nds_read_b64_tr_b16 v[202:203],v242 offset:34048\nM v[76:79],v[204:207],a[92:95],v[76:79]\nds_read_b64_tr_b16 v[204:205],v242 offset:41984\nds_read_b64_tr_b16 v[206:207],v242 offset:42240\nM v[16:19],v[208:211],a[64:67],v[16:19]\n" \
"M v[16:19],v[212:215],a[80:83],v[16:19]\nbuffer_load_dwordx4 a[160:163],v240,s[52:55],s76 offen offset:0 sc1\nM v[20:23],v[208:211],a[68:71],v[20:23]\nM v[20:23],v[212:215],a[84:87],v[20:23]\nM v[80:83],v[208:211],a[72:75],v[80:83]\nM v[80:83],v[212:215],a[88:91],v[80:83]\nbuffer_load_dwordx4 a[176:179],v240,s[52:55],s76 offen offset:2048 sc1\nM v[84:87],v[208:211],a[76:79],v[84:87]\n" \
"ds_read_b64_tr_b16 v[208:209],v241 offset:34304\nds_read_b64_tr_b16 v[210:211],v241 offset:34560\nM v[84:87],v[212:215],a[92:95],v[84:87]\nds_read_b64_tr_b16 v[212:213],v241 offset:42496\ns_waitcnt lgkmcnt(7)\nM v[24:27],v[216:219],a[64:67],v[24:27]\nds_read_b64_tr_b16 v[214:215],v241 offset:42752\nM v[24:27],v[220:223],a[80:83],v[24:27]\n" \
"buffer_load_dwordx4 a[164:167],v240,s[52:55],s76 offen offset:1024 sc1\nM v[28:31],v[216:219],a[68:71],v[28:31]\nM v[28:31],v[220:223],a[84:87],v[28:31]\nM v[88:91],v[216:219],a[72:75],v[88:91]\nM v[88:91],v[220:223],a[88:91],v[88:91]\nbuffer_load_dwordx4 a[180:183],v240,s[52:55],s76 offen offset:3072 sc1\nM v[92:95],v[216:219],a[76:79],v[92:95]\nds_read_b64_tr_b16 v[216:217],v242 offset:34304\n" \
"ds_read_b64_tr_b16 v[218:219],v242 offset:34560\nM v[92:95],v[220:223],a[92:95],v[92:95]\nds_read_b64_tr_b16 v[220:221],v242 offset:42496\nds_read_b64_tr_b16 v[222:223],v242 offset:42752\nM v[32:35],v[192:195],a[64:67],v[32:35]\ns_xor_b32 s76,s76,12288\nM v[32:35],v[196:199],a[80:83],v[32:35]\nbuffer_load_dwordx4 a[168:171],v240,s[52:55],s77 offen offset:0 sc1\n" \
"M v[36:39],v[192:195],a[68:71],v[36:39]\nM v[36:39],v[196:199],a[84:87],v[36:39]\nM v[96:99],v[192:195],a[72:75],v[96:99]\nM v[96:99],v[196:199],a[88:91],v[96:99]\nbuffer_load_dwordx4 a[184:187],v240,s[52:55],s77 offen offset:2048 sc1\nM v[100:103],v[192:195],a[76:79],v[100:103]\nM v[100:103],v[196:199],a[92:95],v[100:103]\ns_waitcnt lgkmcnt(4)\n" \
"M v[40:43],v[200:203],a[64:67],v[40:43]\nM v[40:43],v[204:207],a[80:83],v[40:43]\nbuffer_load_dwordx4 a[172:175],v240,s[52:55],s77 offen offset:1024 sc1\nM v[44:47],v[200:203],a[68:71],v[44:47]\nM v[44:47],v[204:207],a[84:87],v[44:47]\nM v[104:107],v[200:203],a[72:75],v[104:107]\nM v[104:107],v[204:207],a[88:91],v[104:107]\nbuffer_store_dwordx4 v[148:151],v240,s[52:55],s74 offen offset:1024 sc1\n" \
"M v[108:111],v[200:203],a[76:79],v[108:111]\nM v[108:111],v[204:207],a[92:95],v[108:111]\nM v[48:51],v[208:211],a[64:67],v[48:51]\nM v[48:51],v[212:215],a[80:83],v[48:51]\nbuffer_load_dwordx4 a[188:191],v240,s[52:55],s77 offen offset:3072 sc1\nM v[52:55],v[208:211],a[68:71],v[52:55]\ns_xor_b32 s77,s77,12288\nM v[52:55],v[212:215],a[84:87],v[52:55]\n" \
"M v[112:115],v[208:211],a[72:75],v[112:115]\ns_waitcnt vmcnt(23) lgkmcnt(0)\ns_barrier\nM v[112:115],v[212:215],a[88:91],v[112:115]\nds_read_b64_tr_b16 v[192:193],v241 offset:49152\nds_read_b64_tr_b16 v[194:195],v241 offset:49408\nM v[116:119],v[208:211],a[76:79],v[116:119]\nds_read_b64_tr_b16 v[196:197],v241 offset:57344\n" \
"ds_read_b64_tr_b16 v[198:199],v241 offset:57600\nM v[116:119],v[212:215],a[92:95],v[116:119]\nds_read_b64_tr_b16 v[200:201],v242 offset:49152\nds_read_b64_tr_b16 v[202:203],v242 offset:49408\nM v[56:59],v[216:219],a[64:67],v[56:59]\nds_read_b64_tr_b16 v[204:205],v242 offset:57344\nds_read_b64_tr_b16 v[206:207],v242 offset:57600\nM v[56:59],v[220:223],a[80:83],v[56:59]\n" \
"ds_read_b64_tr_b16 v[208:209],v241 offset:49664\nds_read_b64_tr_b16 v[210:211],v241 offset:49920\nM v[60:63],v[216:219],a[68:71],v[60:63]\nds_read_b64_tr_b16 v[212:213],v241 offset:57856\nds_read_b64_tr_b16 v[214:215],v241 offset:58112\nM v[60:63],v[220:223],a[84:87],v[60:63]\nbuffer_load_dwordx4 v243,s[40:43],s69 offen offset:0 lds\nM v[120:123],v[216:219],a[72:75],v[120:123]\n" \
"M v[120:123],v[220:223],a[88:91],v[120:123]\nM v[124:127],v[216:219],a[76:79],v[124:127]\nds_read_b64_tr_b16 v[216:217],v242 offset:49664\nds_read_b64_tr_b16 v[218:219],v242 offset:49920\nM v[124:127],v[220:223],a[92:95],v[124:127]\nds_read_b64_tr_b16 v[220:221],v242 offset:57856\ns_waitcnt lgkmcnt(13)\nM v[0:3],v[192:195],a[96:99],v[0:3]\n" \
"ds_read_b64_tr_b16 v[222:223],v242 offset:58112\ns_waitcnt lgkmcnt(12)\nM v[0:3],v[196:199],a[112:115],v[0:3]\nbuffer_load_dwordx4 v244,s[40:43],s69 offen offset:1024 lds\nM v[4:7],v[192:195],a[100:103],v[4:7]\nM v[4:7],v[196:199],a[116:119],v[4:7]\nM v[64:67],v[192:195],a[104:107],v[64:67]\nM v[64:67],v[196:199],a[120:123],v[64:67]\n" \
"buffer_load_dwordx4 v245,s[40:43],s69 offen offset:2048 lds\nM v[68:71],v[192:195],a[108:111],v[68:71]\nds_read_b64_tr_b16 v[192:193],v241 offset:50176\nds_read_b64_tr_b16 v[194:195],v241 offset:50432\nM v[68:71],v[196:199],a[124:127],v[68:71]\nds_read_b64_tr_b16 v[196:197],v241 offset:58368\ns_waitcnt lgkmcnt(7)\nM v[8:11],v[200:203],a[96:99],v[8:11]\n" \
"ds_read_b64_tr_b16 v[198:199],v241 offset:58624\nM v[8:11],v[204:207],a[112:115],v[8:11]\nbuffer_store_dwordx4 v[152:155],v240,s[52:55],s74 offen offset:2048 sc1\nM v[12:15],v[200:203],a[100:103],v[12:15]\nM v[12:15],v[204:207],a[116:119],v[12:15]\nM v[72:75],v[200:203],a[104:107],v[72:75]\nM v[72:75],v[204:207],a[120:123],v[72:75]\nbuffer_load_dwordx4 v246,s[40:43],s69 offen offset:3072 lds\n" \
"M v[76:79],v[200:203],a[108:111],v[76:79]\nds_read_b64_tr_b16 v[200:201],v242 offset:50176\nds_read_b64_tr_b16 v[202:203],v242 offset:50432\nM v[76:79],v[204:207],a[124:127],v[76:79]\nds_read_b64_tr_b16 v[204:205],v242 offset:58368\nds_read_b64_tr_b16 v[206:207],v242 offset:58624\nM v[16:19],v[208:211],a[96:99],v[16:19]\ns_add_u32 m0,s64,131072\n" \
"s_xor_b32 s69,s99,3145728\nM v[16:19],v[212:215],a[112:115],v[16:19]\nbuffer_load_dwordx4 a[192:195],v240,s[52:55],s76 offen offset:0 sc1\nM v[20:23],v[208:211],a[100:103],v[20:23]\nM v[20:23],v[212:215],a[116:119],v[20:23]\nM v[80:83],v[208:211],a[104:107],v[80:83]\nM v[80:83],v[212:215],a[120:123],v[80:83]\nbuffer_load_dwordx4 a[208:211],v240,s[52:55],s76 offen offset:2048 sc1\n" \
"M v[84:87],v[208:211],a[108:111],v[84:87]\nds_read_b64_tr_b16 v[208:209],v241 offset:50688\nds_read_b64_tr_b16 v[210:211],v241 offset:50944\nM v[84:87],v[212:215],a[124:127],v[84:87]\nds_read_b64_tr_b16 v[212:213],v241 offset:58880\ns_waitcnt lgkmcnt(7)\nM v[24:27],v[216:219],a[96:99],v[24:27]\nds_read_b64_tr_b16 v[214:215],v241 offset:59136\n" \
"M v[24:27],v[220:223],a[112:115],v[24:27]\nbuffer_load_dwordx4 a[196:199],v240,s[52:55],s76 offen offset:1024 sc1\nM v[28:31],v[216:219],a[100:103],v[28:31]\nM v[28:31],v[220:223],a[116:119],v[28:31]\nM v[88:91],v[216:219],a[104:107],v[88:91]\nM v[88:91],v[220:223],a[120:123],v[88:91]\nbuffer_load_dwordx4 a[212:215],v240,s[52:55],s76 offen offset:3072 sc1\nM v[92:95],v[216:219],a[108:111],v[92:95]\n" \
"ds_read_b64_tr_b16 v[216:217],v242 offset:50688\nds_read_b64_tr_b16 v[218:219],v242 offset:50944\nM v[92:95],v[220:223],a[124:127],v[92:95]\nds_read_b64_tr_b16 v[220:221],v242 offset:58880\nds_read_b64_tr_b16 v[222:223],v242 offset:59136\nM v[32:35],v[192:195],a[96:99],v[32:35]\ns_xor_b32 s76,s76,4096\nM v[32:35],v[196:199],a[112:115],v[32:35]\n" \
"buffer_load_dwordx4 a[200:203],v240,s[52:55],s77 offen offset:0 sc1\nM v[36:39],v[192:195],a[100:103],v[36:39]\nM v[36:39],v[196:199],a[116:119],v[36:39]\nM v[96:99],v[192:195],a[104:107],v[96:99]\nM v[96:99],v[196:199],a[120:123],v[96:99]\nbuffer_load_dwordx4 a[216:219],v240,s[52:55],s77 offen offset:2048 sc1\nM v[100:103],v[192:195],a[108:111],v[100:103]\nM v[100:103],v[196:199],a[124:127],v[100:103]\n" \
"s_waitcnt lgkmcnt(4)\nM v[40:43],v[200:203],a[96:99],v[40:43]\nM v[40:43],v[204:207],a[112:115],v[40:43]\nbuffer_store_dwordx4 v[156:159],v240,s[52:55],s74 offen offset:3072 sc1\nM v[44:47],v[200:203],a[100:103],v[44:47]\ns_add_u32 s74,s74,4096\nM v[44:47],v[204:207],a[116:119],v[44:47]\nM v[104:107],v[200:203],a[104:107],v[104:107]\n" \
"M v[104:107],v[204:207],a[120:123],v[104:107]\nbuffer_load_dwordx4 a[204:207],v240,s[52:55],s77 offen offset:1024 sc1\nM v[108:111],v[200:203],a[108:111],v[108:111]\nM v[108:111],v[204:207],a[124:127],v[108:111]\nM v[48:51],v[208:211],a[96:99],v[48:51]\nM v[48:51],v[212:215],a[112:115],v[48:51]\nbuffer_load_dwordx4 a[220:223],v240,s[52:55],s77 offen offset:3072 sc1\nM v[52:55],v[208:211],a[100:103],v[52:55]\n" \
"s_xor_b32 s77,s77,4096\nM v[52:55],v[212:215],a[116:119],v[52:55]\nM v[112:115],v[208:211],a[104:107],v[112:115]\ns_waitcnt vmcnt(24) lgkmcnt(0)\ns_barrier\nM v[112:115],v[212:215],a[120:123],v[112:115]\nds_read_b64_tr_b16 v[192:193],v241 offset:0\nds_read_b64_tr_b16 v[194:195],v241 offset:256\n" \
"M v[116:119],v[208:211],a[108:111],v[116:119]\nds_read_b64_tr_b16 v[196:197],v241 offset:8192\nds_read_b64_tr_b16 v[198:199],v241 offset:8448\nM v[116:119],v[212:215],a[124:127],v[116:119]\nds_read_b64_tr_b16 v[200:201],v242 offset:0\nds_read_b64_tr_b16 v[202:203],v242 offset:256\nM v[56:59],v[216:219],a[96:99],v[56:59]\nds_read_b64_tr_b16 v[204:205],v242 offset:8192\n" \
"ds_read_b64_tr_b16 v[206:207],v242 offset:8448\nM v[56:59],v[220:223],a[112:115],v[56:59]\nds_read_b64_tr_b16 v[208:209],v241 offset:512\nds_read_b64_tr_b16 v[210:211],v241 offset:768\nM v[60:63],v[216:219],a[100:103],v[60:63]\nds_read_b64_tr_b16 v[212:213],v241 offset:8704\nds_read_b64_tr_b16 v[214:215],v241 offset:8960\nM v[60:63],v[220:223],a[116:119],v[60:63]\n" \
"buffer_load_dwordx4 v243,s[40:43],s69 offen offset:0 lds\nM v[120:123],v[216:219],a[104:107],v[120:123]\nM v[120:123],v[220:223],a[120:123],v[120:123]\nM v[124:127],v[216:219],a[108:111],v[124:127]\nds_read_b64_tr_b16 v[216:217],v242 offset:512\nds_read_b64_tr_b16 v[218:219],v242 offset:768\nM v[124:127],v[220:223],a[124:127],v[124:127]\nds_read_b64_tr_b16 v[220:221],v242 offset:8704\n" \
"s_waitcnt lgkmcnt(13)\nM v[0:3],v[192:195],a[128:131],v[0:3]\nds_read_b64_tr_b16 v[222:223],v242 offset:8960\ns_waitcnt lgkmcnt(12)\nM v[0:3],v[196:199],a[144:147],v[0:3]\nbuffer_load_dwordx4 v244,s[40:43],s69 offen offset:1024 lds\nM v[4:7],v[192:195],a[132:135],v[4:7]\nM v[4:7],v[196:199],a[148:151],v[4:7]\n" \
"M v[64:67],v[192:195],a[136:139],v[64:67]\nM v[64:67],v[196:199],a[152:155],v[64:67]\nbuffer_store_dwordx4 v[160:163],v240,s[52:55],s74 offen offset:0 sc1\nM v[68:71],v[192:195],a[140:143],v[68:71]\nds_read_b64_tr_b16 v[192:193],v241 offset:1024\nds_read_b64_tr_b16 v[194:195],v241 offset:1280\nM v[68:71],v[196:199],a[156:159],v[68:71]\nds_read_b64_tr_b16 v[196:197],v241 offset:9216\n" \
"s_waitcnt lgkmcnt(7)\nM v[8:11],v[200:203],a[128:131],v[8:11]\nds_read_b64_tr_b16 v[198:199],v241 offset:9472\nM v[8:11],v[204:207],a[144:147],v[8:11]\nbuffer_load_dwordx4 v245,s[40:43],s69 offen offset:2048 lds\nM v[12:15],v[200:203],a[132:135],v[12:15]\nM v[12:15],v[204:207],a[148:151],v[12:15]\nM v[72:75],v[200:203],a[136:139],v[72:75]\n" \
"M v[72:75],v[204:207],a[152:155],v[72:75]\nbuffer_load_dwordx4 v246,s[40:43],s69 offen offset:3072 lds\nM v[76:79],v[200:203],a[140:143],v[76:79]\nds_read_b64_tr_b16 v[200:201],v242 offset:1024\nds_read_b64_tr_b16 v[202:203],v242 offset:1280\nM v[76:79],v[204:207],a[156:159],v[76:79]\nds_read_b64_tr_b16 v[204:205],v242 offset:9216\nds_read_b64_tr_b16 v[206:207],v242 offset:9472\n" \
"M v[16:19],v[208:211],a[128:131],v[16:19]\ns_add_u32 m0,s64,147456\ns_xor_b32 s69,s99,3670016\nM v[16:19],v[212:215],a[144:147],v[16:19]\nbuffer_load_dwordx4 a[224:227],v240,s[52:55],s76 offen offset:0 sc1\nM v[20:23],v[208:211],a[132:135],v[20:23]\ns_mov_b32 s99,0\nM v[20:23],v[212:215],a[148:151],v[20:23]\n" \
"M v[80:83],v[208:211],a[136:139],v[80:83]\nds_read_b128 a[0:3],v240 offset:0\nM v[80:83],v[212:215],a[152:155],v[80:83]\nbuffer_load_dword v248,v228,s[52:55],s80 offen sc1\nM v[84:87],v[208:211],a[140:143],v[84:87]\nds_read_b64_tr_b16 v[208:209],v241 offset:1536\nds_read_b64_tr_b16 v[210:211],v241 offset:1792\nM v[84:87],v[212:215],a[156:159],v[84:87]\n" \
"ds_read_b64_tr_b16 v[212:213],v241 offset:9728\ns_waitcnt lgkmcnt(8)\nM v[24:27],v[216:219],a[128:131],v[24:27]\nbuffer_load_dwordx4 a[240:243],v240,s[52:55],s76 offen offset:2048 sc1\nM v[24:27],v[220:223],a[144:147],v[24:27]\nds_read_b64_tr_b16 v[214:215],v241 offset:9984\nM v[28:31],v[216:219],a[132:135],v[28:31]\nds_read_b128 a[4:7],v240 offset:1024\n" \
"M v[28:31],v[220:223],a[148:151],v[28:31]\nM v[88:91],v[216:219],a[136:139],v[88:91]\nbuffer_load_dwordx4 a[228:231],v240,s[52:55],s76 offen offset:1024 sc1\nM v[88:91],v[220:223],a[152:155],v[88:91]\nM v[92:95],v[216:219],a[140:143],v[92:95]\nds_read_b64_tr_b16 v[216:217],v242 offset:1536\nds_read_b64_tr_b16 v[218:219],v242 offset:1792\nM v[92:95],v[220:223],a[156:159],v[92:95]\n" \
"ds_read_b64_tr_b16 v[220:221],v242 offset:9728\nds_read_b64_tr_b16 v[222:223],v242 offset:9984\nM v[32:35],v[192:195],a[128:131],v[32:35]\nbuffer_load_dwordx4 a[244:247],v240,s[52:55],s76 offen offset:3072 sc1\nM v[32:35],v[196:199],a[144:147],v[32:35]\nds_read_b128 a[8:11],v240 offset:2048\nM v[36:39],v[192:195],a[132:135],v[36:39]\nM v[36:39],v[196:199],a[148:151],v[36:39]\n" \
"M v[96:99],v[192:195],a[136:139],v[96:99]\nbuffer_store_dwordx4 v[164:167],v240,s[52:55],s74 offen offset:1024 sc1\nM v[96:99],v[196:199],a[152:155],v[96:99]\nM v[100:103],v[192:195],a[140:143],v[100:103]\nds_read_b128 a[12:15],v240 offset:3072\nM v[100:103],v[196:199],a[156:159],v[100:103]\ns_waitcnt lgkmcnt(7)\nM v[40:43],v[200:203],a[128:131],v[40:43]\n" \
"buffer_load_dwordx4 a[232:235],v240,s[52:55],s77 offen offset:0 sc1\nM v[40:43],v[204:207],a[144:147],v[40:43]\nM v[44:47],v[200:203],a[132:135],v[44:47]\nM v[44:47],v[204:207],a[148:151],v[44:47]\nM v[104:107],v[200:203],a[136:139],v[104:107]\nbuffer_load_dwordx4 a[248:251],v240,s[52:55],s77 offen offset:2048 sc1\nM v[104:107],v[204:207],a[152:155],v[104:107]\nds_read_b128 a[16:19],v240 offset:4096\n" \
"M v[108:111],v[200:203],a[140:143],v[108:111]\nM v[108:111],v[204:207],a[156:159],v[108:111]\nM v[48:51],v[208:211],a[128:131],v[48:51]\nbuffer_load_dwordx4 a[236:239],v240,s[52:55],s77 offen offset:1024 sc1\nM v[48:51],v[212:215],a[144:147],v[48:51]\nM v[52:55],v[208:211],a[132:135],v[52:55]\nds_read_b128 a[20:23],v240 offset:5120\nM v[52:55],v[212:215],a[148:151],v[52:55]\n" \
"M v[112:115],v[208:211],a[136:139],v[112:115]\ns_waitcnt vmcnt(17) lgkmcnt(3)\ns_barrier\nM v[112:115],v[212:215],a[152:155],v[112:115]\nds_read_b64_tr_b16 v[192:193],v241 offset:16384\nds_read_b64_tr_b16 v[194:195],v241 offset:16640\nM v[116:119],v[208:211],a[140:143],v[116:119]\nds_read_b64_tr_b16 v[196:197],v241 offset:24576\n" \
"ds_read_b64_tr_b16 v[198:199],v241 offset:24832\nM v[116:119],v[212:215],a[156:159],v[116:119]\nds_read_b64_tr_b16 v[200:201],v242 offset:16384\nds_read_b64_tr_b16 v[202:203],v242 offset:16640\nM v[56:59],v[216:219],a[128:131],v[56:59]\nds_read_b64_tr_b16 v[204:205],v242 offset:24576\nds_read_b64_tr_b16 v[206:207],v242 offset:24832\nM v[56:59],v[220:223],a[144:147],v[56:59]\n" \
"ds_read_b64_tr_b16 v[208:209],v241 offset:16896\nds_read_b64_tr_b16 v[210:211],v241 offset:17152\nM v[60:63],v[216:219],a[132:135],v[60:63]\nds_read_b64_tr_b16 v[212:213],v241 offset:25088\nds_read_b64_tr_b16 v[214:215],v241 offset:25344\nM v[60:63],v[220:223],a[148:151],v[60:63]\nbuffer_load_dwordx4 a[252:255],v240,s[52:55],s77 offen offset:3072 sc1\nM v[120:123],v[216:219],a[136:139],v[120:123]\n" \
"ds_read_b128 a[24:27],v240 offset:6144\nM v[120:123],v[220:223],a[152:155],v[120:123]\nM v[124:127],v[216:219],a[140:143],v[124:127]\nds_read_b64_tr_b16 v[216:217],v242 offset:16896\nds_read_b64_tr_b16 v[218:219],v242 offset:17152\nM v[124:127],v[220:223],a[156:159],v[124:127]\nds_read_b64_tr_b16 v[220:221],v242 offset:25088\ns_waitcnt lgkmcnt(14)\n" \
"M v[0:3],v[192:195],a[160:163],v[0:3]\nds_read_b64_tr_b16 v[222:223],v242 offset:25344\ns_waitcnt lgkmcnt(13)\nM v[0:3],v[196:199],a[176:179],v[0:3]\nbuffer_store_dwordx4 v[168:171],v240,s[52:55],s74 offen offset:2048 sc1\nM v[4:7],v[192:195],a[164:167],v[4:7]\nds_read_b128 a[28:31],v240 offset:7168\nM v[4:7],v[196:199],a[180:183],v[4:7]\n" \
"M v[64:67],v[192:195],a[168:171],v[64:67]\nds_read_b128 a[32:35],v240 offset:8192\nM v[64:67],v[196:199],a[184:187],v[64:67]\nbuffer_load_dwordx4 v243,s[40:43],s69 offen offset:0 lds\nM v[68:71],v[192:195],a[172:175],v[68:71]\nds_read_b64_tr_b16 v[192:193],v241 offset:17408\nds_read_b64_tr_b16 v[194:195],v241 offset:17664\nM v[68:71],v[196:199],a[188:191],v[68:71]\n" \
"ds_read_b64_tr_b16 v[196:197],v241 offset:25600\ns_waitcnt lgkmcnt(10)\nM v[8:11],v[200:203],a[160:163],v[8:11]\nds_read_b64_tr_b16 v[198:199],v241 offset:25856\nM v[8:11],v[204:207],a[176:179],v[8:11]\nbuffer_load_dwordx4 v244,s[40:43],s69 offen offset:1024 lds\nM v[12:15],v[200:203],a[164:167],v[12:15]\nds_read_b128 a[36:39],v240 offset:9216\n" \
"M v[12:15],v[204:207],a[180:183],v[12:15]\nM v[72:75],v[200:203],a[168:171],v[72:75]\nM v[72:75],v[204:207],a[184:187],v[72:75]\nbuffer_load_dwordx4 v245,s[40:43],s69 offen offset:2048 lds\nM v[76:79],v[200:203],a[172:175],v[76:79]\nds_read_b64_tr_b16 v[200:201],v242 offset:17408\nds_read_b64_tr_b16 v[202:203],v242 offset:17664\nM v[76:79],v[204:207],a[188:191],v[76:79]\n" \
"ds_read_b64_tr_b16 v[204:205],v242 offset:25600\nds_read_b64_tr_b16 v[206:207],v242 offset:25856\nM v[16:19],v[208:211],a[160:163],v[16:19]\nds_read_b128 a[40:43],v240 offset:10240\nM v[16:19],v[212:215],a[176:179],v[16:19]\nbuffer_load_dwordx4 v246,s[40:43],s69 offen offset:3072 lds\nM v[20:23],v[208:211],a[164:167],v[20:23]\ns_sub_u32 s40,s18,0x1000\n" \
"s_subb_u32 s41,s19,0\nM v[20:23],v[212:215],a[180:183],v[20:23]\ns_waitcnt vmcnt(13)\nv_cmp_eq_u32 vcc,0,v248\nM v[80:83],v[208:211],a[168:171],v[80:83]\ns_cbranch_vccnz .Lps1\n.Lpb1:\ns_add_u32 s80,s80,4096\n" \
"M v[80:83],v[212:215],a[184:187],v[80:83]\ns_cmp_ge_u32 s62,6\ns_cselect_b64 vcc,-1,0\nM v[84:87],v[208:211],a[172:175],v[84:87]\nds_read_b64_tr_b16 v[208:209],v241 offset:17920\nds_read_b64_tr_b16 v[210:211],v241 offset:18176\nM v[84:87],v[212:215],a[188:191],v[84:87]\nds_read_b64_tr_b16 v[212:213],v241 offset:26112\n" \
"s_waitcnt lgkmcnt(8)\nM v[24:27],v[216:219],a[160:163],v[24:27]\nds_read_b64_tr_b16 v[214:215],v241 offset:26368\nv_cndmask_b32 v254,v231,v229,vcc\nM v[24:27],v[220:223],a[176:179],v[24:27]\ns_cmp_eq_u32 s62,3\ns_cselect_b64 vcc,-1,0\nM v[28:31],v[216:219],a[164:167],v[28:31]\n" \
"v_cndmask_b32 v254,v254,v230,vcc\nv_bfe_u32 v230,v249,2,3\nM v[28:31],v[220:223],a[180:183],v[28:31]\nv_and_b32 v231,0x70,v243\nv_lshl_or_b32 v230,v230,10,v231\nM v[88:91],v[216:219],a[168:171],v[88:91]\nv_and_b32 v231,16,v249\nv_lshlrev_b32 v231,1,v231\n" \
"M v[88:91],v[220:223],a[184:187],v[88:91]\nv_xor_b32 v230,v230,v231\nv_add_u32 v243,4096,v230\nM v[92:95],v[216:219],a[172:175],v[92:95]\nbuffer_store_dwordx4 v[172:175],v240,s[52:55],s74 offen offset:3072 sc1\nM v[92:95],v[220:223],a[188:191],v[92:95]\nds_read_b64_tr_b16 v[216:217],v242 offset:17920\nds_read_b64_tr_b16 v[218:219],v242 offset:18176\n" \
"M v[32:35],v[192:195],a[160:163],v[32:35]\nds_read_b64_tr_b16 v[220:221],v242 offset:26112\nds_read_b64_tr_b16 v[222:223],v242 offset:26368\nM v[32:35],v[196:199],a[176:179],v[32:35]\ns_add_u32 s74,s74,4096\nv_add_u32 v244,3200,v230\nM v[36:39],v[192:195],a[164:167],v[36:39]\nv_add_u32 v245,10240,v230\n" \
"v_add_u32 v246,9344,v230\nM v[36:39],v[196:199],a[180:183],v[36:39]\nv_and_b32 v229,3,v249\nv_cmp_eq_u32 vcc,0,v229\nM v[96:99],v[192:195],a[168:171],v[96:99]\nv_mov_b32 v230,0x3f803f80\nv_cndmask_b32 v229,0,v230,vcc\nM v[96:99],v[196:199],a[184:187],v[96:99]\n" \
"v_and_b32 v230,0xff,v241\nv_and_b32 v231,16,v249\nM v[100:103],v[192:195],a[172:175],v[100:103]\nv_lshl_or_b32 v230,v231,4,v230\nv_and_b32 v231,32,v249\nM v[100:103],v[196:199],a[188:191],v[100:103]\nds_read_b128 a[44:47],v240 offset:11264\ns_waitcnt lgkmcnt(5)\n" \
"M v[40:43],v[200:203],a[160:163],v[40:43]\nds_read_b128 a[48:51],v240 offset:12288\nds_read_b128 a[52:55],v240 offset:13312\nM v[40:43],v[204:207],a[176:179],v[40:43]\nds_read_b128 a[56:59],v240 offset:14336\nM v[44:47],v[200:203],a[164:167],v[44:47]\nM v[44:47],v[204:207],a[180:183],v[44:47]\nM v[104:107],v[200:203],a[168:171],v[104:107]\n" \
"M v[104:107],v[204:207],a[184:187],v[104:107]\nM v[108:111],v[200:203],a[172:175],v[108:111]\nds_read_b128 a[60:63],v240 offset:15360\nM v[108:111],v[204:207],a[188:191],v[108:111]\nM v[48:51],v[208:211],a[160:163],v[48:51]\nM v[48:51],v[212:215],a[176:179],v[48:51]\nM v[52:55],v[208:211],a[164:167],v[52:55]\nM v[52:55],v[212:215],a[180:183],v[52:55]\n" \
"M v[112:115],v[208:211],a[168:171],v[112:115]\ns_waitcnt lgkmcnt(5)\ns_barrier\nM v[112:115],v[212:215],a[184:187],v[112:115]\nds_read_b64_tr_b16 v[192:193],v241 offset:32768\nds_read_b64_tr_b16 v[194:195],v241 offset:33024\nM v[116:119],v[208:211],a[172:175],v[116:119]\nds_read_b64_tr_b16 v[196:197],v241 offset:40960\n" \
"ds_read_b64_tr_b16 v[198:199],v241 offset:41216\nM v[116:119],v[212:215],a[188:191],v[116:119]\nds_read_b64_tr_b16 v[200:201],v242 offset:32768\nds_read_b64_tr_b16 v[202:203],v242 offset:33024\nM v[56:59],v[216:219],a[160:163],v[56:59]\nds_read_b64_tr_b16 v[204:205],v242 offset:40960\nds_read_b64_tr_b16 v[206:207],v242 offset:41216\nM v[56:59],v[220:223],a[176:179],v[56:59]\n" \
"ds_read_b64_tr_b16 v[208:209],v241 offset:33280\nds_read_b64_tr_b16 v[210:211],v241 offset:33536\nM v[60:63],v[216:219],a[164:167],v[60:63]\nds_read_b64_tr_b16 v[212:213],v241 offset:41472\nds_read_b64_tr_b16 v[214:215],v241 offset:41728\nM v[60:63],v[220:223],a[180:183],v[60:63]\nds_write_b64 v250,v[128:129] offset:0\nM v[120:123],v[216:219],a[168:171],v[120:123]\n" \
"buffer_store_dwordx4 v[176:179],v240,s[52:55],s74 offen offset:0 sc1\nM v[120:123],v[220:223],a[184:187],v[120:123]\nds_write_b64 v250,v[130:131] offset:8\nM v[124:127],v[216:219],a[172:175],v[124:127]\nds_read_b64_tr_b16 v[216:217],v242 offset:33280\nds_read_b64_tr_b16 v[218:219],v242 offset:33536\nM v[124:127],v[220:223],a[188:191],v[124:127]\nds_read_b64_tr_b16 v[220:221],v242 offset:41472\n" \
"s_waitcnt lgkmcnt(15)\nM v[0:3],v[192:195],a[192:195],v[0:3]\nds_read_b64_tr_b16 v[222:223],v242 offset:41728\ns_waitcnt lgkmcnt(14)\nM v[0:3],v[196:199],a[208:211],v[0:3]\nds_write_b64 v250,v[132:133] offset:1024\nM v[4:7],v[192:195],a[196:199],v[4:7]\nds_write_b64 v250,v[134:135] offset:1032\n" \
"M v[4:7],v[196:199],a[212:215],v[4:7]\nds_write_b64 v250,v[136:137] offset:2048\nM v[64:67],v[192:195],a[200:203],v[64:67]\nM v[64:67],v[196:199],a[216:219],v[64:67]\nds_write_b64 v250,v[138:139] offset:2056\nM v[68:71],v[192:195],a[204:207],v[68:71]\nds_read_b64_tr_b16 v[192:193],v241 offset:33792\nds_read_b64_tr_b16 v[194:195],v241 offset:34048\n" \
"M v[68:71],v[196:199],a[220:223],v[68:71]\nds_read_b64_tr_b16 v[196:197],v241 offset:41984\ns_waitcnt lgkmcnt(12)\nM v[8:11],v[200:203],a[192:195],v[8:11]\nds_write_b64 v250,v[140:141] offset:3072\nds_read_b64_tr_b16 v[198:199],v241 offset:42240\nM v[8:11],v[204:207],a[208:211],v[8:11]\nM v[12:15],v[200:203],a[196:199],v[12:15]\n" \
"M v[12:15],v[204:207],a[212:215],v[12:15]\nds_write_b64 v250,v[142:143] offset:3080\nM v[72:75],v[200:203],a[200:203],v[72:75]\nM v[72:75],v[204:207],a[216:219],v[72:75]\nM v[76:79],v[200:203],a[204:207],v[76:79]\nds_read_b64_tr_b16 v[200:201],v242 offset:33792\nds_read_b64_tr_b16 v[202:203],v242 offset:34048\nM v[76:79],v[204:207],a[220:223],v[76:79]\n" \
"ds_read_b64_tr_b16 v[204:205],v242 offset:41984\nds_read_b64_tr_b16 v[206:207],v242 offset:42240\nM v[16:19],v[208:211],a[192:195],v[16:19]\nds_write_b64 v250,v[144:145] offset:4096\nM v[16:19],v[212:215],a[208:211],v[16:19]\nds_write_b64 v250,v[146:147] offset:4104\nM v[20:23],v[208:211],a[196:199],v[20:23]\nM v[20:23],v[212:215],a[212:215],v[20:23]\n" \
"M v[80:83],v[208:211],a[200:203],v[80:83]\nds_write_b64 v250,v[148:149] offset:5120\nM v[80:83],v[212:215],a[216:219],v[80:83]\nM v[84:87],v[208:211],a[204:207],v[84:87]\nds_read_b64_tr_b16 v[208:209],v241 offset:34304\nds_read_b64_tr_b16 v[210:211],v241 offset:34560\nM v[84:87],v[212:215],a[220:223],v[84:87]\nds_read_b64_tr_b16 v[212:213],v241 offset:42496\n" \
"s_waitcnt lgkmcnt(11)\nM v[24:27],v[216:219],a[192:195],v[24:27]\nds_write_b64 v250,v[150:151] offset:5128\nds_read_b64_tr_b16 v[214:215],v241 offset:42752\nM v[24:27],v[220:223],a[208:211],v[24:27]\nM v[28:31],v[216:219],a[196:199],v[28:31]\nbuffer_store_dwordx4 v[180:183],v240,s[52:55],s74 offen offset:1024 sc1\nM v[28:31],v[220:223],a[212:215],v[28:31]\n" \
"ds_write_b64 v250,v[152:153] offset:6144\nM v[88:91],v[216:219],a[200:203],v[88:91]\nM v[88:91],v[220:223],a[216:219],v[88:91]\nds_write_b64 v250,v[154:155] offset:6152\nM v[92:95],v[216:219],a[204:207],v[92:95]\nds_read_b64_tr_b16 v[216:217],v242 offset:34304\nds_read_b64_tr_b16 v[218:219],v242 offset:34560\nM v[92:95],v[220:223],a[220:223],v[92:95]\n" \
"ds_read_b64_tr_b16 v[220:221],v242 offset:42496\nds_read_b64_tr_b16 v[222:223],v242 offset:42752\nM v[32:35],v[192:195],a[192:195],v[32:35]\nds_write_b64 v250,v[156:157] offset:7168\nM v[32:35],v[196:199],a[208:211],v[32:35]\nM v[36:39],v[192:195],a[196:199],v[36:39]\nM v[36:39],v[196:199],a[212:215],v[36:39]\nds_write_b64 v250,v[158:159] offset:7176\n" \
"M v[96:99],v[192:195],a[200:203],v[96:99]\nM v[96:99],v[196:199],a[216:219],v[96:99]\nM v[100:103],v[192:195],a[204:207],v[100:103]\nds_write_b64 v250,v[160:161] offset:8192\nM v[100:103],v[196:199],a[220:223],v[100:103]\ns_waitcnt lgkmcnt(9)\nM v[40:43],v[200:203],a[192:195],v[40:43]\nM v[40:43],v[204:207],a[208:211],v[40:43]\n" \
"ds_write_b64 v250,v[162:163] offset:8200\nM v[44:47],v[200:203],a[196:199],v[44:47]\nM v[44:47],v[204:207],a[212:215],v[44:47]\nM v[104:107],v[200:203],a[200:203],v[104:107]\nds_write_b64 v250,v[164:165] offset:9216\nM v[104:107],v[204:207],a[216:219],v[104:107]\nM v[108:111],v[200:203],a[204:207],v[108:111]\nM v[108:111],v[204:207],a[220:223],v[108:111]\n" \
"ds_write_b64 v250,v[166:167] offset:9224\nM v[48:51],v[208:211],a[192:195],v[48:51]\nM v[48:51],v[212:215],a[208:211],v[48:51]\nM v[52:55],v[208:211],a[196:199],v[52:55]\nds_write_b64 v250,v[168:169] offset:10240\nM v[52:55],v[212:215],a[212:215],v[52:55]\nM v[112:115],v[208:211],a[200:203],v[112:115]\ns_waitcnt vmcnt(3) lgkmcnt(5)\n" \
"s_barrier\nM v[112:115],v[212:215],a[216:219],v[112:115]\nds_read_b64_tr_b16 v[192:193],v241 offset:49152\nds_read_b64_tr_b16 v[194:195],v241 offset:49408\nM v[116:119],v[208:211],a[204:207],v[116:119]\nds_read_b64_tr_b16 v[196:197],v241 offset:57344\nds_read_b64_tr_b16 v[198:199],v241 offset:57600\nM v[116:119],v[212:215],a[220:223],v[116:119]\n" \
"ds_read_b64_tr_b16 v[200:201],v242 offset:49152\nds_read_b64_tr_b16 v[202:203],v242 offset:49408\nM v[56:59],v[216:219],a[192:195],v[56:59]\nbuffer_store_dwordx4 v[184:187],v240,s[52:55],s74 offen offset:2048 sc1\nM v[56:59],v[220:223],a[208:211],v[56:59]\nds_read_b64_tr_b16 v[204:205],v242 offset:57344\nds_read_b64_tr_b16 v[206:207],v242 offset:57600\nM v[60:63],v[216:219],a[196:199],v[60:63]\n" \
"ds_read_b64_tr_b16 v[208:209],v241 offset:49664\nds_read_b64_tr_b16 v[210:211],v241 offset:49920\nM v[60:63],v[220:223],a[212:215],v[60:63]\nds_read_b64_tr_b16 v[212:213],v241 offset:57856\nds_read_b64_tr_b16 v[214:215],v241 offset:58112\nM v[120:123],v[216:219],a[200:203],v[120:123]\nds_write_b64 v250,v[170:171] offset:10248\nv_accvgpr_write_b32 a208,v229\n" \
"M v[120:123],v[220:223],a[216:219],v[120:123]\nds_write_b64 v250,v[172:173] offset:11264\nv_accvgpr_write_b32 a209,v229\nM v[124:127],v[216:219],a[204:207],v[124:127]\nds_read_b64_tr_b16 v[216:217],v242 offset:49664\nds_read_b64_tr_b16 v[218:219],v242 offset:49920\nM v[124:127],v[220:223],a[220:223],v[124:127]\nds_read_b64_tr_b16 v[220:221],v242 offset:57856\n" \
"s_waitcnt lgkmcnt(15)\nM v[0:3],v[192:195],a[224:227],v[0:3]\nds_read_b64_tr_b16 v[222:223],v242 offset:58112\ns_waitcnt lgkmcnt(14)\nM v[0:3],v[196:199],a[240:243],v[0:3]\nds_write_b64 v250,v[174:175] offset:11272\nv_accvgpr_write_b32 a210,v229\nM v[4:7],v[192:195],a[228:231],v[4:7]\n" \
"ds_write_b64 v250,v[176:177] offset:12288\nv_accvgpr_write_b32 a211,v229\nM v[4:7],v[196:199],a[244:247],v[4:7]\nds_write_b64 v250,v[178:179] offset:12296\nM v[64:67],v[192:195],a[232:235],v[64:67]\nv_cvt_pk_bf16_f32 v0,v0,v1\nv_cvt_pk_bf16_f32 v1,v2,v3\nM v[64:67],v[196:199],a[248:251],v[64:67]\n" \
"ds_write_b64 v250,v[180:181] offset:13312\nv_accvgpr_write_b32 a128,v0\nM v[68:71],v[192:195],a[236:239],v[68:71]\nds_read_b64_tr_b16 v[192:193],v241 offset:50176\nds_read_b64_tr_b16 v[194:195],v241 offset:50432\nM v[68:71],v[196:199],a[252:255],v[68:71]\nds_read_b64_tr_b16 v[196:197],v241 offset:58368\ns_waitcnt lgkmcnt(12)\n" \
"M v[8:11],v[200:203],a[224:227],v[8:11]\nds_read_b64_tr_b16 v[198:199],v241 offset:58624\nv_cvt_pk_bf16_f32 v4,v4,v5\nM v[8:11],v[204:207],a[240:243],v[8:11]\nv_cvt_pk_bf16_f32 v64,v64,v65\nv_cvt_pk_bf16_f32 v5,v6,v7\nM v[12:15],v[200:203],a[228:231],v[12:15]\nv_cvt_pk_bf16_f32 v68,v68,v69\n" \
"v_cvt_pk_bf16_f32 v65,v66,v67\nM v[12:15],v[204:207],a[244:247],v[12:15]\nds_write_b64 v250,v[182:183] offset:13320\nv_cvt_pk_bf16_f32 v69,v70,v71\nM v[72:75],v[200:203],a[232:235],v[72:75]\nds_write_b64 v250,v[184:185] offset:14336\nv_cvt_pk_bf16_f32 v2,v8,v9\nM v[72:75],v[204:207],a[248:251],v[72:75]\n" \
"ds_write_b64 v250,v[186:187] offset:14344\nv_cvt_pk_bf16_f32 v3,v10,v11\nM v[76:79],v[200:203],a[236:239],v[76:79]\nds_read_b64_tr_b16 v[200:201],v242 offset:50176\nds_read_b64_tr_b16 v[202:203],v242 offset:50432\nM v[76:79],v[204:207],a[252:255],v[76:79]\nds_read_b64_tr_b16 v[204:205],v242 offset:58368\nds_read_b64_tr_b16 v[206:207],v242 offset:58624\n" \
"M v[16:19],v[208:211],a[224:227],v[16:19]\nds_write_b64 v250,v[188:189] offset:15360\nv_cvt_pk_bf16_f32 v6,v12,v13\nM v[16:19],v[212:215],a[240:243],v[16:19]\nv_cvt_pk_bf16_f32 v7,v14,v15\nv_cvt_pk_bf16_f32 v66,v72,v73\nM v[20:23],v[208:211],a[228:231],v[20:23]\nv_cvt_pk_bf16_f32 v67,v74,v75\n" \
"v_cvt_pk_bf16_f32 v70,v76,v77\nM v[20:23],v[212:215],a[244:247],v[20:23]\nds_write_b64 v250,v[190:191] offset:15368\nv_cvt_pk_bf16_f32 v71,v78,v79\nM v[80:83],v[208:211],a[232:235],v[80:83]\nv_cvt_pk_bf16_f32 v16,v16,v17\nv_cvt_pk_bf16_f32 v17,v18,v19\nM v[80:83],v[212:215],a[248:251],v[80:83]\n" \
"v_add_u32 v250,114688,v240\nv_accvgpr_write_b32 a129,v1\nM v[84:87],v[208:211],a[236:239],v[84:87]\nbuffer_store_dwordx4 v[188:191],v240,s[52:55],s74 offen offset:3072 sc1\nM v[84:87],v[212:215],a[252:255],v[84:87]\nds_read_b64_tr_b16 v[208:209],v241 offset:50688\ns_waitcnt lgkmcnt(7)\nM v[24:27],v[216:219],a[224:227],v[24:27]\n" \
"ds_read_b64_tr_b16 v[210:211],v241 offset:50944\nds_read_b64_tr_b16 v[212:213],v241 offset:58880\nM v[24:27],v[220:223],a[240:243],v[24:27]\nds_read_b64_tr_b16 v[214:215],v241 offset:59136\nv_cvt_pk_bf16_f32 v20,v20,v21\nM v[28:31],v[216:219],a[228:231],v[28:31]\nv_cvt_pk_bf16_f32 v80,v80,v81\nv_cvt_pk_bf16_f32 v84,v84,v85\n" \
"M v[28:31],v[220:223],a[244:247],v[28:31]\nv_cvt_pk_bf16_f32 v21,v22,v23\nv_cvt_pk_bf16_f32 v81,v82,v83\nM v[88:91],v[216:219],a[232:235],v[88:91]\nv_cvt_pk_bf16_f32 v85,v86,v87\nv_cvt_pk_bf16_f32 v18,v24,v25\nM v[88:91],v[220:223],a[248:251],v[88:91]\nv_cvt_pk_bf16_f32 v19,v26,v27\n" \
"v_add_u32 v241,65536,v240\nM v[92:95],v[216:219],a[236:239],v[92:95]\nds_read_b64_tr_b16 v[216:217],v242 offset:50688\nds_read_b64_tr_b16 v[218:219],v242 offset:50944\nM v[92:95],v[220:223],a[252:255],v[92:95]\nds_read_b64_tr_b16 v[220:221],v242 offset:58880\nds_read_b64_tr_b16 v[222:223],v242 offset:59136\nM v[32:35],v[192:195],a[224:227],v[32:35]\n" \
"v_cvt_pk_bf16_f32 v22,v28,v29\nv_cvt_pk_bf16_f32 v23,v30,v31\nM v[32:35],v[196:199],a[240:243],v[32:35]\nv_cvt_pk_bf16_f32 v82,v88,v89\nv_cvt_pk_bf16_f32 v83,v90,v91\nM v[36:39],v[192:195],a[228:231],v[36:39]\nv_cvt_pk_bf16_f32 v86,v92,v93\nv_cvt_pk_bf16_f32 v87,v94,v95\n" \
"M v[36:39],v[196:199],a[244:247],v[36:39]\nv_lshl_or_b32 v242,v231,6,v230\nv_accvgpr_write_b32 a130,v2\nM v[96:99],v[192:195],a[232:235],v[96:99]\nv_cvt_pk_bf16_f32 v32,v32,v33\nv_cvt_pk_bf16_f32 v33,v34,v35\nM v[96:99],v[196:199],a[248:251],v[96:99]\nv_accvgpr_write_b32 a131,v3\n" \
"v_accvgpr_write_b32 a144,v4\nM v[100:103],v[192:195],a[236:239],v[100:103]\nv_cvt_pk_bf16_f32 v36,v36,v37\nv_cvt_pk_bf16_f32 v37,v38,v39\nM v[100:103],v[196:199],a[252:255],v[100:103]\nv_accvgpr_write_b32 a145,v5\ns_waitcnt lgkmcnt(4)\nM v[40:43],v[200:203],a[224:227],v[40:43]\n" \
"v_cvt_pk_bf16_f32 v96,v96,v97\nv_cvt_pk_bf16_f32 v97,v98,v99\nM v[40:43],v[204:207],a[240:243],v[40:43]\nv_accvgpr_write_b32 a146,v6\nv_accvgpr_write_b32 a147,v7\nM v[44:47],v[200:203],a[228:231],v[44:47]\nv_cvt_pk_bf16_f32 v100,v100,v101\nv_cvt_pk_bf16_f32 v101,v102,v103\n" \
"M v[44:47],v[204:207],a[244:247],v[44:47]\nv_accvgpr_write_b32 a160,v64\nv_accvgpr_write_b32 a161,v65\nM v[104:107],v[200:203],a[232:235],v[104:107]\nv_cvt_pk_bf16_f32 v34,v40,v41\nv_cvt_pk_bf16_f32 v35,v42,v43\nM v[104:107],v[204:207],a[248:251],v[104:107]\nv_accvgpr_write_b32 a162,v66\n" \
"v_accvgpr_write_b32 a163,v67\nM v[108:111],v[200:203],a[236:239],v[108:111]\nv_cvt_pk_bf16_f32 v38,v44,v45\nv_cvt_pk_bf16_f32 v39,v46,v47\nM v[108:111],v[204:207],a[252:255],v[108:111]\nv_accvgpr_write_b32 a176,v68\nv_accvgpr_write_b32 a177,v69\nM v[48:51],v[208:211],a[224:227],v[48:51]\n" \
"v_cvt_pk_bf16_f32 v98,v104,v105\nv_cvt_pk_bf16_f32 v99,v106,v107\nM v[48:51],v[212:215],a[240:243],v[48:51]\nv_accvgpr_write_b32 a178,v70\nv_accvgpr_write_b32 a179,v71\nM v[52:55],v[208:211],a[228:231],v[52:55]\nv_cvt_pk_bf16_f32 v102,v108,v109\nv_cvt_pk_bf16_f32 v103,v110,v111\n" \
"M v[52:55],v[212:215],a[244:247],v[52:55]\nv_accvgpr_write_b32 a132,v16\nv_accvgpr_write_b32 a133,v17\nM v[112:115],v[208:211],a[232:235],v[112:115]\nv_cvt_pk_bf16_f32 v48,v48,v49\nv_cvt_pk_bf16_f32 v49,v50,v51\nM v[112:115],v[212:215],a[248:251],v[112:115]\nv_accvgpr_write_b32 a134,v18\n" \
"v_accvgpr_write_b32 a135,v19\nM v[116:119],v[208:211],a[236:239],v[116:119]\nv_cvt_pk_bf16_f32 v52,v52,v53\nv_cvt_pk_bf16_f32 v53,v54,v55\nM v[116:119],v[212:215],a[252:255],v[116:119]\nv_accvgpr_write_b32 a148,v20\ns_waitcnt lgkmcnt(0)\nM v[56:59],v[216:219],a[224:227],v[56:59]\n" \
"v_cvt_pk_bf16_f32 v112,v112,v113\nv_cvt_pk_bf16_f32 v113,v114,v115\nM v[56:59],v[220:223],a[240:243],v[56:59]\nv_accvgpr_write_b32 a149,v21\nv_accvgpr_write_b32 a150,v22\nM v[60:63],v[216:219],a[228:231],v[60:63]\nv_cvt_pk_bf16_f32 v116,v116,v117\nv_cvt_pk_bf16_f32 v117,v118,v119\n" \
"M v[60:63],v[220:223],a[244:247],v[60:63]\nv_accvgpr_write_b32 a244,0\nv_accvgpr_write_b32 a245,0\nM v[120:123],v[216:219],a[232:235],v[120:123]\nv_cvt_pk_bf16_f32 v50,v56,v57\nv_cvt_pk_bf16_f32 v51,v58,v59\nM v[120:123],v[220:223],a[248:251],v[120:123]\nv_accvgpr_write_b32 a248,-1\n" \
"v_accvgpr_write_b32 a249,-1\nM v[124:127],v[216:219],a[236:239],v[124:127]\nv_cvt_pk_bf16_f32 v54,v60,v61\nv_cvt_pk_bf16_f32 v55,v62,v63\nM v[124:127],v[220:223],a[252:255],v[124:127]\ns_waitcnt vmcnt(0)\nbuffer_store_dword v252,off,s[52:55],s29 sc1\nv_cvt_pk_bf16_f32 v114,v120,v121\n" \
"v_cvt_pk_bf16_f32 v115,v122,v123\nv_accvgpr_write_b32 a246,0\nv_accvgpr_write_b32 a250,-1\nv_accvgpr_write_b32 a247,0\nv_accvgpr_write_b32 a251,-1\nv_cvt_pk_bf16_f32 v118,v124,v125\nv_cvt_pk_bf16_f32 v119,v126,v127\nv_accvgpr_write_b32 a151,v23\n" \
"v_accvgpr_write_b32 a164,v80\nv_accvgpr_write_b32 a165,v81\nv_accvgpr_write_b32 a166,v82\nv_accvgpr_write_b32 a167,v83\nv_accvgpr_write_b32 a180,v84\nv_accvgpr_write_b32 a181,v85\nv_accvgpr_write_b32 a182,v86\nv_accvgpr_write_b32 a183,v87\n" \
"v_accvgpr_write_b32 a136,v32\nv_accvgpr_write_b32 a137,v33\nv_accvgpr_write_b32 a138,v34\nv_accvgpr_write_b32 a139,v35\nv_accvgpr_write_b32 a152,v36\nv_accvgpr_write_b32 a153,v37\nv_accvgpr_write_b32 a154,v38\nv_accvgpr_write_b32 a155,v39\n" \
"v_accvgpr_write_b32 a168,v96\nv_accvgpr_write_b32 a169,v97\nv_accvgpr_write_b32 a170,v98\nv_accvgpr_write_b32 a171,v99\nv_accvgpr_write_b32 a184,v100\nv_accvgpr_write_b32 a185,v101\nv_accvgpr_write_b32 a186,v102\nv_accvgpr_write_b32 a187,v103\n" \
"v_accvgpr_write_b32 a140,v48\nv_accvgpr_write_b32 a141,v49\nv_accvgpr_write_b32 a142,v50\nv_accvgpr_write_b32 a143,v51\nv_accvgpr_write_b32 a156,v52\nv_accvgpr_write_b32 a157,v53\nv_accvgpr_write_b32 a158,v54\nv_accvgpr_write_b32 a159,v55\n" \
"v_accvgpr_write_b32 a172,v112\nv_accvgpr_write_b32 a173,v113\nv_accvgpr_write_b32 a174,v114\nv_accvgpr_write_b32 a175,v115\nv_accvgpr_write_b32 a188,v116\nv_accvgpr_write_b32 a189,v117\nv_accvgpr_write_b32 a190,v118\nv_accvgpr_write_b32 a191,v119\n" \
"s_waitcnt lgkmcnt(0)\n.Lattn:\ns_cmp_lt_u32 s7,2\ns_cbranch_scc0 .Lp23\ns_cmp_eq_u32 s7,0\ns_cbranch_scc0 .Lpro1\n.Lpro0:\ns_barrier\n" \
"M v[64:67],a[0:3],a[128:131],0\nds_read_b128 a[32:35],v240 offset:40960\nds_read_b128 a[36:39],v240 offset:41984\nM v[64:67],a[4:7],a[132:135],v[64:67]\nds_read_b128 a[40:43],v240 offset:43008\ns_mov_b32 m0,s64\nv_mov_b32 v218,0\nM v[64:67],a[8:11],a[136:139],v[64:67]\n" \
"ds_read_b128 a[44:47],v240 offset:44032\nv_mov_b32 v219,0\nM v[64:67],a[12:15],a[140:143],v[64:67]\nds_read_b128 a[48:51],v240 offset:45056\nbuffer_load_dwordx4 v240,s[44:47],s36 offen offset:0 sc1 lds\nM v[68:71],a[12:15],a[156:159],0\nds_read_b128 a[12:15],v240 offset:35840\nM v[68:71],a[8:11],a[152:155],v[68:71]\n" \
"ds_read_b128 a[8:11],v240 offset:34816\nM v[68:71],a[4:7],a[148:151],v[68:71]\nds_read_b128 a[4:7],v240 offset:33792\nv_max3_f32 v225,v64,v65,v66\nM v[68:71],a[0:3],a[144:147],v[68:71]\nds_read_b128 a[0:3],v240 offset:32768\nv_max_f32 v225,v225,v67\nv_cndmask_b32 v225,v224,v225,s[90:91]\n" \
"M v[76:79],a[28:31],a[156:159],0\nds_read_b128 a[28:31],v240 offset:39936\nv_cndmask_b32 v64,v64,v252,s[82:83]\nM v[76:79],a[24:27],a[152:155],v[76:79]\nds_read_b128 a[24:27],v240 offset:38912\nv_max3_f32 v229,v68,v69,v70\nv_cndmask_b32 v65,v65,v252,s[84:85]\nM v[76:79],a[20:23],a[148:151],v[76:79]\n" \
"ds_read_b128 a[20:23],v240 offset:37888\nv_cndmask_b32 v66,v66,v252,s[86:87]\nM v[76:79],a[16:19],a[144:147],v[76:79]\nds_read_b128 a[16:19],v240 offset:36864\nv_cndmask_b32 v67,v67,v252,s[88:89]\ns_waitcnt lgkmcnt(4)\nM v[136:139],a[0:3],a[160:163],0\nbuffer_load_dwordx4 v240,s[44:47],s36 offen offset:1024 sc1 lds\n" \
"M v[136:139],a[4:7],a[164:167],v[136:139]\nds_read_b128 a[52:55],v240 offset:46080\nds_read_b128 a[56:59],v240 offset:47104\nv_max3_f32 v229,v229,v71,v76\nM v[136:139],a[8:11],a[168:171],v[136:139]\nv_max3_f32 v229,v229,v77,v78\nv_max_f32 v229,v229,v79\nv_cndmask_b32 v229,v228,v229,s[90:91]\n" \
"M v[136:139],a[12:15],a[172:175],v[136:139]\nds_read_b128 a[60:63],v240 offset:48128\nv_permlane16_swap_b32 v225,v229\nM v[140:143],a[12:15],a[188:191],0\nv_max_f32 v225,v225,v229\nv_mov_b32 v229,v225\nds_read_b128 a[64:67],v241 offset:0\nM v[140:143],a[8:11],a[184:187],v[140:143]\n" \
"v_permlane32_swap_b32 v225,v229\nv_max_f32 v225,v225,v229\nM v[140:143],a[4:7],a[180:183],v[140:143]\nv_mov_b32 v229,v225\nv_max3_f32 v233,v136,v137,v138\nds_read_b128 a[68:71],v241 offset:1024\nM v[140:143],a[0:3],a[176:179],v[140:143]\nv_permlane16_swap_b32 v225,v229\n" \
"s_waitcnt lgkmcnt(5)\nM v[152:155],a[16:19],a[160:163],0\nv_sub_f32 v224,0,v225\nv_sub_f32 v226,0,v225\nv_sub_f32 v227,0,v225\nM v[152:155],a[20:23],a[164:167],v[152:155]\nv_sub_f32 v225,0,v225\nv_sub_f32 v228,0,v229\n" \
"M v[152:155],a[24:27],a[168:171],v[152:155]\nv_sub_f32 v230,0,v229\nv_sub_f32 v231,0,v229\nv_sub_f32 v229,0,v229\nM v[152:155],a[28:31],a[172:175],v[152:155]\nv_max3_f32 v237,v140,v141,v142\nds_read_b128 a[72:75],v241 offset:2048\nM v[156:159],a[28:31],a[188:191],0\n" \
"buffer_load_dwordx4 v240,s[44:47],s36 offen offset:2048 sc1 lds\nds_read_b128 a[76:79],v241 offset:3072\nM v[156:159],a[24:27],a[184:187],v[156:159]\nds_read_b128 a[80:83],v241 offset:4096\nds_read_b128 a[84:87],v241 offset:5120\nM v[156:159],a[20:23],a[180:183],v[156:159]\nv_max3_f32 v233,v233,v139,v152\nv_max3_f32 v233,v233,v153,v154\n" \
"v_max_f32 v233,v233,v155\nM v[156:159],a[16:19],a[176:179],v[156:159]\nv_cndmask_b32 v233,v232,v233,s[90:91]\nds_read_b128 a[88:91],v241 offset:6144\nv_add_f32 v64,v64,v224\nM v[128:131],a[0:3],a[128:131],v[224:227]\nbuffer_load_dwordx4 v240,s[44:47],s36 offen offset:3072 sc1 lds\nM v[128:131],a[4:7],a[132:135],v[128:131]\n" \
"v_add_f32 v65,v65,v224\nv_add_f32 v66,v66,v224\nv_max3_f32 v237,v237,v143,v156\nM v[128:131],a[8:11],a[136:139],v[128:131]\nv_max3_f32 v237,v237,v157,v158\nv_max_f32 v237,v237,v159\nM v[128:131],a[12:15],a[140:143],v[128:131]\nv_cndmask_b32 v237,v236,v237,s[90:91]\n" \
"v_add_f32 v67,v67,v224\nds_read_b128 a[92:95],v241 offset:7168\nM v[132:135],a[12:15],a[156:159],v[228:231]\nv_permlane16_swap_b32 v233,v237\nv_max_f32 v233,v233,v237\nM v[132:135],a[8:11],a[152:155],v[132:135]\nv_mov_b32 v237,v233\nv_exp_f32 v64,v64\n" \
"M v[132:135],a[4:7],a[148:151],v[132:135]\nv_permlane32_swap_b32 v233,v237\nv_max_f32 v233,v233,v237\nM v[132:135],a[0:3],a[144:147],v[132:135]\nv_mov_b32 v237,v233\nv_exp_f32 v65,v65\nM v[144:147],a[16:19],a[128:131],v[224:227]\nv_permlane16_swap_b32 v233,v237\n" \
"v_sub_f32 v232,0,v233\nM v[144:147],a[20:23],a[132:135],v[144:147]\nv_sub_f32 v234,0,v233\nv_sub_f32 v235,0,v233\nv_sub_f32 v233,0,v233\nM v[144:147],a[24:27],a[136:139],v[144:147]\nv_sub_f32 v236,0,v237\nv_sub_f32 v238,0,v237\n" \
"M v[144:147],a[28:31],a[140:143],v[144:147]\nv_sub_f32 v239,0,v237\nv_sub_f32 v237,0,v237\nv_cvt_pk_bf16_f32 v64,v64,v65\nM v[148:151],a[28:31],a[156:159],v[228:231]\nv_exp_f32 v66,v66\nv_add_f32 v68,v68,v228\nM v[148:151],a[24:27],a[152:155],v[148:151]\n" \
"v_exp_f32 v67,v67\nM v[148:151],a[20:23],a[148:151],v[148:151]\nv_cvt_pk_bf16_f32 v65,v66,v67\nv_mov_b32 v66,0\nv_mov_b32 v67,0\nM v[148:151],a[16:19],a[144:147],v[148:151]\nv_add_f32 v69,v69,v228\nv_cndmask_b32 v76,v76,v252,s[82:83]\n" \
"M v[160:163],a[32:35],a[128:131],v[224:227]\nv_cndmask_b32 v77,v77,v252,s[84:85]\nv_cndmask_b32 v78,v78,v252,s[86:87]\nv_cndmask_b32 v79,v79,v252,s[88:89]\nM v[160:163],a[36:39],a[132:135],v[160:163]\nv_add_f32 v70,v70,v228\nv_add_f32 v71,v71,v228\nM v[160:163],a[40:43],a[136:139],v[160:163]\n" \
"v_exp_f32 v68,v68\nv_add_f32 v76,v76,v228\nM v[160:163],a[44:47],a[140:143],v[160:163]\nv_exp_f32 v69,v69\nM v[164:167],a[44:47],a[156:159],v[228:231]\nv_exp_f32 v70,v70\nv_add_f32 v77,v77,v228\nM v[164:167],a[40:43],a[152:155],v[164:167]\n" \
"v_exp_f32 v71,v71\nM v[164:167],a[36:39],a[148:151],v[164:167]\nv_add_f32 v78,v78,v228\nv_add_f32 v79,v79,v228\nv_cvt_pk_bf16_f32 v68,v68,v69\nM v[164:167],a[32:35],a[144:147],v[164:167]\nv_exp_f32 v76,v76\nM v[176:179],a[48:51],a[128:131],v[224:227]\n" \
"v_exp_f32 v77,v77\ns_waitcnt lgkmcnt(0)\nM v[176:179],a[52:55],a[132:135],v[176:179]\nv_exp_f32 v78,v78\nv_cvt_pk_bf16_f32 v69,v70,v71\nM v[176:179],a[56:59],a[136:139],v[176:179]\nv_exp_f32 v79,v79\nM v[176:179],a[60:63],a[140:143],v[176:179]\n" \
"v_cvt_pk_bf16_f32 v70,v76,v77\nv_cvt_pk_bf16_f32 v71,v78,v79\nds_read_b128 a[0:3],v240 offset:49152\nM v[180:183],a[60:63],a[156:159],v[228:231]\nds_read_b128 a[4:7],v240 offset:50176\nds_read_b128 a[8:11],v240 offset:51200\nM v[180:183],a[56:59],a[152:155],v[180:183]\nds_read_b128 a[12:15],v240 offset:52224\n" \
"v_exp_f32 v128,v128\nM v[180:183],a[52:55],a[148:151],v[180:183]\nds_read_b128 a[16:19],v240 offset:53248\nv_add_f32 v136,v136,v232\nM v[180:183],a[48:51],a[144:147],v[180:183]\nv_exp_f32 v129,v129\nds_read_b128 a[20:23],v240 offset:54272\nM v[168:171],a[32:35],a[160:163],v[232:235]\n" \
"v_exp_f32 v130,v130\nM v[168:171],a[36:39],a[164:167],v[168:171]\nv_exp_f32 v131,v131\nv_cvt_pk_bf16_f32 v192,v128,v129\nM v[168:171],a[40:43],a[168:171],v[168:171]\nv_cvt_pk_bf16_f32 v193,v130,v131\nds_read_b128 a[24:27],v240 offset:55296\nM v[168:171],a[44:47],a[172:175],v[168:171]\n" \
"ds_read_b128 a[28:31],v240 offset:56320\nv_exp_f32 v132,v132\nM v[172:175],a[44:47],a[188:191],v[236:239]\nv_exp_f32 v133,v133\nM v[172:175],a[40:43],a[184:187],v[172:175]\nv_exp_f32 v134,v134\nv_cvt_pk_bf16_f32 v196,v132,v133\nM v[172:175],a[36:39],a[180:183],v[172:175]\n" \
"buffer_load_dword v248,v251,s[52:55],s80 offen sc1\nv_add_f32 v137,v137,v232\nM v[172:175],a[32:35],a[176:179],v[172:175]\nv_exp_f32 v135,v135\nM v[188:191],a[60:63],a[188:191],v[236:239]\nv_cvt_pk_bf16_f32 v197,v134,v135\nv_exp_f32 v144,v144\nM v[188:191],a[56:59],a[184:187],v[188:191]\n" \
"v_exp_f32 v145,v145\nM v[188:191],a[52:55],a[180:183],v[188:191]\nv_exp_f32 v146,v146\nv_cvt_pk_bf16_f32 v194,v144,v145\nM v[188:191],a[48:51],a[176:179],v[188:191]\nv_exp_f32 v147,v147\nM v[0:3],a[64:67],v[64:67],0\nv_cvt_pk_bf16_f32 v195,v146,v147\n" \
"v_exp_f32 v148,v148\nM v[8:11],a[68:71],v[64:67],0\nv_exp_f32 v149,v149\nM v[16:19],a[72:75],v[64:67],0\nv_exp_f32 v150,v150\nv_cvt_pk_bf16_f32 v198,v148,v149\nM v[24:27],a[76:79],v[64:67],0\nv_exp_f32 v151,v151\n" \
"M v[32:35],a[80:83],v[64:67],0\nv_cvt_pk_bf16_f32 v199,v150,v151\nv_add_f32 v138,v138,v232\nv_add_f32 v139,v139,v232\nM v[40:43],a[84:87],v[64:67],0\nv_exp_f32 v136,v136\nM v[48:51],a[88:91],v[64:67],0\nv_exp_f32 v137,v137\n" \
"v_add_f32 v140,v140,v236\nM v[56:59],a[92:95],v[64:67],0\nv_exp_f32 v138,v138\nv_cvt_pk_bf16_f32 v200,v136,v137\nM a[192:195],a[208:211],v[64:67],0\nv_exp_f32 v139,v139\nM v[4:7],a[64:67],v[68:71],0\nv_cvt_pk_bf16_f32 v201,v138,v139\n" \
"v_add_f32 v141,v141,v236\nv_add_f32 v142,v142,v236\nM v[12:15],a[68:71],v[68:71],0\nv_add_f32 v143,v143,v236\nds_read_b128 a[32:35],v240 offset:57344\nM v[20:23],a[72:75],v[68:71],0\nv_exp_f32 v140,v140\nv_add_f32 v152,v152,v232\n" \
"M v[28:31],a[76:79],v[68:71],0\nv_exp_f32 v141,v141\nM v[36:39],a[80:83],v[68:71],0\nv_exp_f32 v142,v142\nv_cvt_pk_bf16_f32 v204,v140,v141\nM v[44:47],a[84:87],v[68:71],0\nv_exp_f32 v143,v143\nM v[52:55],a[88:91],v[68:71],0\n" \
"v_cvt_pk_bf16_f32 v205,v142,v143\nv_add_f32 v153,v153,v232\nv_add_f32 v154,v154,v232\nM v[60:63],a[92:95],v[68:71],0\nv_add_f32 v155,v155,v232\nds_read_b128 a[36:39],v240 offset:58368\nM a[196:199],a[208:211],v[68:71],0\nv_exp_f32 v152,v152\n" \
"s_waitcnt lgkmcnt(2)\nM v[128:131],a[0:3],a[128:131],v[224:227]\nv_exp_f32 v153,v153\nM v[128:131],a[4:7],a[132:135],v[128:131]\nv_exp_f32 v154,v154\nv_cvt_pk_bf16_f32 v202,v152,v153\nM v[128:131],a[8:11],a[136:139],v[128:131]\nv_exp_f32 v155,v155\n" \
"M v[128:131],a[12:15],a[140:143],v[128:131]\nv_cvt_pk_bf16_f32 v203,v154,v155\nds_read_b128 a[40:43],v240 offset:59392\nds_read_b128 a[44:47],v240 offset:60416\nM v[132:135],a[12:15],a[156:159],v[228:231]\nv_add_f32 v156,v156,v236\nv_add_f32 v157,v157,v236\nv_add_f32 v158,v158,v236\n" \
"M v[132:135],a[8:11],a[152:155],v[132:135]\nv_add_f32 v159,v159,v236\nds_read_b128 a[48:51],v240 offset:61440\nM v[132:135],a[4:7],a[148:151],v[132:135]\nv_exp_f32 v156,v156\nds_read_b128 a[52:55],v240 offset:62464\nM v[132:135],a[0:3],a[144:147],v[132:135]\nv_exp_f32 v157,v157\n" \
"M v[144:147],a[16:19],a[128:131],v[224:227]\nv_exp_f32 v158,v158\nv_cvt_pk_bf16_f32 v206,v156,v157\nM v[144:147],a[20:23],a[132:135],v[144:147]\nv_exp_f32 v159,v159\nM v[144:147],a[24:27],a[136:139],v[144:147]\nv_cvt_pk_bf16_f32 v207,v158,v159\nv_exp_f32 v160,v160\n" \
"M v[144:147],a[28:31],a[140:143],v[144:147]\nv_exp_f32 v161,v161\nM v[148:151],a[28:31],a[156:159],v[228:231]\nv_exp_f32 v162,v162\nv_cvt_pk_bf16_f32 v208,v160,v161\nM v[148:151],a[24:27],a[152:155],v[148:151]\nv_exp_f32 v163,v163\nM v[148:151],a[20:23],a[148:151],v[148:151]\n" \
"v_cvt_pk_bf16_f32 v209,v162,v163\nds_read_b128 a[56:59],v240 offset:63488\nds_read_b128 a[60:63],v240 offset:64512\nM v[148:151],a[16:19],a[144:147],v[148:151]\nv_exp_f32 v164,v164\nM v[136:139],a[0:3],a[160:163],v[232:235]\nv_exp_f32 v165,v165\nv_cndmask_b32 v168,v168,v252,s[82:83]\n" \
"M v[136:139],a[4:7],a[164:167],v[136:139]\nv_exp_f32 v166,v166\nv_cvt_pk_bf16_f32 v212,v164,v165\nM v[136:139],a[8:11],a[168:171],v[136:139]\nv_exp_f32 v167,v167\nM v[136:139],a[12:15],a[172:175],v[136:139]\nv_cvt_pk_bf16_f32 v213,v166,v167\nv_exp_f32 v176,v176\n" \
"M v[140:143],a[12:15],a[188:191],v[236:239]\nv_exp_f32 v177,v177\nM v[140:143],a[8:11],a[184:187],v[140:143]\nv_exp_f32 v178,v178\nv_cvt_pk_bf16_f32 v210,v176,v177\nM v[140:143],a[4:7],a[180:183],v[140:143]\nv_exp_f32 v179,v179\nM v[140:143],a[0:3],a[176:179],v[140:143]\n" \
"v_cvt_pk_bf16_f32 v211,v178,v179\nv_exp_f32 v180,v180\nM v[152:155],a[16:19],a[160:163],v[232:235]\nv_exp_f32 v181,v181\nM v[152:155],a[20:23],a[164:167],v[152:155]\nv_exp_f32 v182,v182\nv_cvt_pk_bf16_f32 v214,v180,v181\nM v[152:155],a[24:27],a[168:171],v[152:155]\n" \
"s_waitcnt vmcnt(1)\ns_barrier\nM v[152:155],a[28:31],a[172:175],v[152:155]\nv_exp_f32 v183,v183\nv_cndmask_b32 v169,v169,v252,s[84:85]\nM v[156:159],a[28:31],a[188:191],v[236:239]\nv_cvt_pk_bf16_f32 v215,v182,v183\nv_cndmask_b32 v170,v170,v252,s[86:87]\n" \
"M v[156:159],a[24:27],a[184:187],v[156:159]\nv_cndmask_b32 v171,v171,v252,s[88:89]\nv_exp_f32 v168,v168\nM v[156:159],a[20:23],a[180:183],v[156:159]\nv_exp_f32 v169,v169\nds_read_b128 a[64:67],v241 offset:32768\nM v[156:159],a[16:19],a[176:179],v[156:159]\nv_cvt_pk_bf16_f32 v216,v168,v169\n" \
"s_waitcnt lgkmcnt(1)\nM v[160:163],a[32:35],a[128:131],v[224:227]\nv_exp_f32 v170,v170\nds_read_b128 a[96:99],v241 offset:40960\nM v[160:163],a[36:39],a[132:135],v[160:163]\nv_exp_f32 v171,v171\nM v[160:163],a[40:43],a[136:139],v[160:163]\nv_cvt_pk_bf16_f32 v217,v170,v171\n" \
"v_exp_f32 v172,v172\nM v[160:163],a[44:47],a[140:143],v[160:163]\nv_exp_f32 v173,v173\nM v[164:167],a[44:47],a[156:159],v[228:231]\nv_exp_f32 v174,v174\nv_cvt_pk_bf16_f32 v220,v172,v173\nM v[164:167],a[40:43],a[152:155],v[164:167]\nv_exp_f32 v175,v175\n" \
"M v[164:167],a[36:39],a[148:151],v[164:167]\nv_cvt_pk_bf16_f32 v221,v174,v175\nds_read_b128 a[68:71],v241 offset:33792\nds_read_b128 a[100:103],v241 offset:41984\nM v[164:167],a[32:35],a[144:147],v[164:167]\nds_read_b128 a[72:75],v241 offset:34816\nv_cndmask_b32 v188,v188,v252,s[82:83]\nM v[176:179],a[48:51],a[128:131],v[224:227]\n" \
"v_cndmask_b32 v189,v189,v252,s[84:85]\nv_cndmask_b32 v190,v190,v252,s[86:87]\nv_cndmask_b32 v191,v191,v252,s[88:89]\nM v[176:179],a[52:55],a[132:135],v[176:179]\nv_exp_f32 v188,v188\nM v[176:179],a[56:59],a[136:139],v[176:179]\nv_exp_f32 v189,v189\nds_read_b128 a[104:107],v241 offset:43008\n" \
"M v[176:179],a[60:63],a[140:143],v[176:179]\nv_exp_f32 v190,v190\nv_cvt_pk_bf16_f32 v222,v188,v189\nM v[180:183],a[60:63],a[156:159],v[228:231]\nv_exp_f32 v191,v191\nM v[180:183],a[56:59],a[152:155],v[180:183]\nv_cvt_pk_bf16_f32 v223,v190,v191\nds_read_b128 a[76:79],v241 offset:35840\n" \
"ds_read_b128 a[108:111],v241 offset:44032\nM v[180:183],a[52:55],a[148:151],v[180:183]\nds_read_b128 a[80:83],v241 offset:36864\ns_waitcnt vmcnt(0)\nv_cmp_eq_u32 vcc,0,v248\nM v[180:183],a[48:51],a[144:147],v[180:183]\nds_read_b128 a[112:115],v241 offset:45056\ns_cbranch_vccnz .Lps2\n" \
".Lpb2:\ns_add_u32 m0,s64,65536\nM v[168:171],a[32:35],a[160:163],v[232:235]\nbuffer_load_dwordx4 v240,s[48:51],s36 offen offset:0 sc1 lds\nM v[168:171],a[36:39],a[164:167],v[168:171]\nds_read_b128 a[84:87],v241 offset:37888\nv_exp_f32 v128,v128\nM v[168:171],a[40:43],a[168:171],v[168:171]\n" \
"ds_read_b128 a[116:119],v241 offset:46080\nds_read_b128 a[88:91],v241 offset:38912\nM v[168:171],a[44:47],a[172:175],v[168:171]\nds_read_b128 a[120:123],v241 offset:47104\nv_exp_f32 v129,v129\nM v[172:175],a[44:47],a[188:191],v[236:239]\nbuffer_load_dwordx4 v240,s[48:51],s36 offen offset:1024 sc1 lds\nM v[172:175],a[40:43],a[184:187],v[172:175]\n" \
"ds_read_b128 a[92:95],v241 offset:39936\nv_exp_f32 v130,v130\nM v[172:175],a[36:39],a[180:183],v[172:175]\nds_read_b128 a[124:127],v241 offset:48128\nds_read_b128 a[0:3],v240 offset:0\nM v[172:175],a[32:35],a[176:179],v[172:175]\nds_read_b128 a[4:7],v240 offset:1024\nv_exp_f32 v131,v131\n" \
"M v[184:187],a[48:51],a[160:163],v[232:235]\nds_read_b128 a[8:11],v240 offset:2048\nbuffer_load_dwordx4 v240,s[48:51],s36 offen offset:2048 sc1 lds\nM v[184:187],a[52:55],a[164:167],v[184:187]\nv_exp_f32 v132,v132\nM v[184:187],a[56:59],a[168:171],v[184:187]\nds_read_b128 a[12:15],v240 offset:3072\nv_exp_f32 v133,v133\n" \
"M v[184:187],a[60:63],a[172:175],v[184:187]\nds_read_b128 a[16:19],v240 offset:4096\nds_read_b128 a[20:23],v240 offset:5120\nM v[188:191],a[60:63],a[188:191],v[236:239]\nds_read_b128 a[24:27],v240 offset:6144\nbuffer_load_dwordx4 v240,s[48:51],s36 offen offset:3072 sc1 lds\nM v[188:191],a[56:59],a[184:187],v[188:191]\nds_read_b128 a[28:31],v240 offset:7168\n" \
"s_add_u32 m0,s64,16384\nM v[188:191],a[52:55],a[180:183],v[188:191]\nds_read_b128 a[32:35],v240 offset:8192\nv_exp_f32 v134,v134\nM v[188:191],a[48:51],a[176:179],v[188:191]\nds_read_b128 a[36:39],v240 offset:9216\ns_waitcnt lgkmcnt(9)\nM v[0:3],a[64:67],v[192:195],v[0:3]\n" \
"ds_read_b128 a[40:43],v240 offset:10240\nbuffer_load_dwordx4 v240,s[44:47],s65 offen offset:0 sc1 lds\nM v[0:3],a[96:99],v[208:211],v[0:3]\nv_exp_f32 v135,v135\nM v[8:11],a[68:71],v[192:195],v[8:11]\nds_read_b128 a[44:47],v240 offset:11264\nv_exp_f32 v144,v144\nM v[8:11],a[100:103],v[208:211],v[8:11]\n" \
"ds_read_b128 a[48:51],v240 offset:12288\nds_read_b128 a[52:55],v240 offset:13312\nM v[16:19],a[72:75],v[192:195],v[16:19]\nbuffer_load_dwordx4 v240,s[44:47],s65 offen offset:1024 sc1 lds\nds_read_b128 a[56:59],v240 offset:14336\nM v[16:19],a[104:107],v[208:211],v[16:19]\nds_read_b128 a[60:63],v240 offset:15360\nv_exp_f32 v145,v145\n" \
"M v[24:27],a[76:79],v[192:195],v[24:27]\nv_exp_f32 v146,v146\nM v[24:27],a[108:111],v[208:211],v[24:27]\nv_exp_f32 v147,v147\nM v[32:35],a[80:83],v[192:195],v[32:35]\nbuffer_load_dwordx4 v240,s[44:47],s65 offen offset:2048 sc1 lds\nM v[32:35],a[112:115],v[208:211],v[32:35]\nv_exp_f32 v148,v148\n" \
"M v[40:43],a[84:87],v[192:195],v[40:43]\nv_exp_f32 v149,v149\nM v[40:43],a[116:119],v[208:211],v[40:43]\nv_exp_f32 v150,v150\nM v[48:51],a[88:91],v[192:195],v[48:51]\nbuffer_load_dwordx4 v240,s[44:47],s65 offen offset:3072 sc1 lds\nM v[48:51],a[120:123],v[208:211],v[48:51]\ns_add_u32 m0,s64,81920\n" \
"v_exp_f32 v151,v151\nM v[56:59],a[92:95],v[192:195],v[56:59]\nv_exp_f32 v136,v136\nM v[56:59],a[124:127],v[208:211],v[56:59]\nv_exp_f32 v137,v137\nM a[192:195],a[208:211],v[192:195],a[192:195]\nbuffer_load_dwordx4 v240,s[48:51],s65 offen offset:0 sc1 lds\nM a[192:195],a[208:211],v[208:211],a[192:195]\n" \
"v_exp_f32 v138,v138\nv_cvt_pk_bf16_f32 v192,v128,v129\nM v[4:7],a[64:67],v[196:199],v[4:7]\nv_exp_f32 v139,v139\nM v[4:7],a[96:99],v[212:215],v[4:7]\nv_exp_f32 v152,v152\nv_cvt_pk_bf16_f32 v193,v130,v131\nM v[12:15],a[68:71],v[196:199],v[12:15]\n" \
"buffer_load_dwordx4 v240,s[48:51],s65 offen offset:1024 sc1 lds\nv_cvt_pk_bf16_f32 v194,v144,v145\nM v[12:15],a[100:103],v[212:215],v[12:15]\nv_exp_f32 v153,v153\nM v[20:23],a[72:75],v[196:199],v[20:23]\nv_exp_f32 v154,v154\nv_cvt_pk_bf16_f32 v195,v146,v147\nM v[20:23],a[104:107],v[212:215],v[20:23]\n" \
"v_exp_f32 v155,v155\nM v[28:31],a[76:79],v[196:199],v[28:31]\nbuffer_load_dwordx4 v240,s[48:51],s65 offen offset:2048 sc1 lds\nM v[28:31],a[108:111],v[212:215],v[28:31]\nv_exp_f32 v160,v160\nM v[36:39],a[80:83],v[196:199],v[36:39]\ns_barrier\nv_exp_f32 v161,v161\n" \
"M v[36:39],a[112:115],v[212:215],v[36:39]\nv_exp_f32 v162,v162\nM v[44:47],a[84:87],v[196:199],v[44:47]\nbuffer_load_dwordx4 v240,s[48:51],s65 offen offset:3072 sc1 lds\ns_add_u32 m0,s64,32768\nM v[44:47],a[116:119],v[212:215],v[44:47]\nv_exp_f32 v163,v163\nM v[52:55],a[88:91],v[196:199],v[52:55]\n" \
"v_exp_f32 v176,v176\nv_cvt_pk_bf16_f32 v208,v160,v161\nM v[52:55],a[120:123],v[212:215],v[52:55]\nv_exp_f32 v177,v177\nM v[60:63],a[92:95],v[196:199],v[60:63]\nbuffer_load_dwordx4 v240,s[44:47],s66 offen offset:0 sc1 lds\nv_cvt_pk_bf16_f32 v209,v162,v163\nM v[60:63],a[124:127],v[212:215],v[60:63]\n" \
"v_exp_f32 v178,v178\nv_cvt_pk_bf16_f32 v210,v176,v177\nM a[196:199],a[208:211],v[196:199],a[196:199]\nv_exp_f32 v179,v179\nM a[196:199],a[208:211],v[212:215],a[196:199]\nv_exp_f32 v140,v140\nv_cvt_pk_bf16_f32 v196,v132,v133\nM v[64:67],a[64:67],v[200:203],0\n" \
"buffer_load_dwordx4 v240,s[44:47],s66 offen offset:1024 sc1 lds\nM v[64:67],a[96:99],v[216:219],v[64:67]\nv_exp_f32 v141,v141\nv_cvt_pk_bf16_f32 v197,v134,v135\nM v[72:75],a[68:71],v[200:203],0\nv_exp_f32 v142,v142\nM v[72:75],a[100:103],v[216:219],v[72:75]\nv_exp_f32 v143,v143\n" \
"v_cvt_pk_bf16_f32 v198,v148,v149\nM v[80:83],a[72:75],v[200:203],0\nbuffer_load_dwordx4 v240,s[44:47],s66 offen offset:2048 sc1 lds\nM v[80:83],a[104:107],v[216:219],v[80:83]\nv_exp_f32 v156,v156\nv_cvt_pk_bf16_f32 v199,v150,v151\nM v[88:91],a[76:79],v[200:203],0\nv_exp_f32 v157,v157\n" \
"M v[88:91],a[108:111],v[216:219],v[88:91]\nv_exp_f32 v158,v158\nv_cvt_pk_bf16_f32 v211,v178,v179\nM v[96:99],a[80:83],v[200:203],0\nbuffer_load_dwordx4 v240,s[44:47],s66 offen offset:3072 sc1 lds\nM v[96:99],a[112:115],v[216:219],v[96:99]\ns_add_u32 m0,s64,98304\nv_exp_f32 v159,v159\n" \
"M v[104:107],a[84:87],v[200:203],0\nv_exp_f32 v164,v164\nM v[104:107],a[116:119],v[216:219],v[104:107]\nv_exp_f32 v165,v165\nM v[112:115],a[88:91],v[200:203],0\nv_exp_f32 v166,v166\nM v[112:115],a[120:123],v[216:219],v[112:115]\nv_exp_f32 v167,v167\n" \
"M v[120:123],a[92:95],v[200:203],0\nv_exp_f32 v180,v180\nM v[120:123],a[124:127],v[216:219],v[120:123]\nv_exp_f32 v181,v181\nM a[200:203],a[208:211],v[200:203],0\nv_exp_f32 v182,v182\nv_cvt_pk_bf16_f32 v200,v136,v137\nM a[200:203],a[208:211],v[216:219],a[200:203]\n" \
"v_exp_f32 v183,v183\nM v[68:71],a[64:67],v[204:207],0\nv_exp_f32 v168,v168\nv_cvt_pk_bf16_f32 v201,v138,v139\nM v[68:71],a[96:99],v[220:223],v[68:71]\nv_exp_f32 v169,v169\nM v[76:79],a[68:71],v[204:207],0\nv_exp_f32 v170,v170\n" \
"v_cvt_pk_bf16_f32 v202,v152,v153\nM v[76:79],a[100:103],v[220:223],v[76:79]\nv_exp_f32 v171,v171\nM v[84:87],a[72:75],v[204:207],0\nv_cvt_pk_bf16_f32 v203,v154,v155\nbuffer_load_dwordx4 v240,s[48:51],s66 offen offset:0 sc1 lds\nM v[84:87],a[104:107],v[220:223],v[84:87]\nM v[92:95],a[76:79],v[204:207],0\n" \
"M v[92:95],a[108:111],v[220:223],v[92:95]\nM v[100:103],a[80:83],v[204:207],0\nbuffer_load_dwordx4 v240,s[48:51],s66 offen offset:1024 sc1 lds\nM v[100:103],a[112:115],v[220:223],v[100:103]\nM v[108:111],a[84:87],v[204:207],0\nM v[108:111],a[116:119],v[220:223],v[108:111]\nM v[116:119],a[88:91],v[204:207],0\nbuffer_load_dwordx4 v240,s[48:51],s66 offen offset:2048 sc1 lds\n" \
"M v[116:119],a[120:123],v[220:223],v[116:119]\nM v[124:127],a[92:95],v[204:207],0\nM v[124:127],a[124:127],v[220:223],v[124:127]\nM a[204:207],a[208:211],v[204:207],0\nbuffer_load_dwordx4 v240,s[48:51],s66 offen offset:3072 sc1 lds\nM a[204:207],a[208:211],v[220:223],a[204:207]\ns_cmp_lt_u32 s62,2\ns_cbranch_scc1 .Lx01\n" \
"s_branch .Ldual\n.Lpro1:\ns_barrier\nM v[64:67],a[0:3],a[128:131],0\nds_read_b128 a[64:67],v241 offset:0\nds_read_b128 a[96:99],v241 offset:8192\nM v[64:67],a[4:7],a[132:135],v[64:67]\nds_read_b128 a[68:71],v241 offset:1024\n" \
"s_mov_b32 m0,s64\nv_mov_b32 v202,0\nM v[64:67],a[8:11],a[136:139],v[64:67]\nbuffer_load_dwordx4 v240,s[44:47],s36 offen offset:0 sc1 lds\nM v[64:67],a[12:15],a[140:143],v[64:67]\nds_read_b128 a[100:103],v241 offset:9216\nds_read_b128 a[72:75],v241 offset:2048\nv_mov_b32 v203,0\n" \
"M v[68:71],a[12:15],a[156:159],0\nds_read_b128 a[12:15],v240 offset:35840\nM v[68:71],a[8:11],a[152:155],v[68:71]\nds_read_b128 a[8:11],v240 offset:34816\nM v[68:71],a[4:7],a[148:151],v[68:71]\nds_read_b128 a[4:7],v240 offset:33792\nbuffer_load_dwordx4 v240,s[44:47],s36 offen offset:1024 sc1 lds\nM v[68:71],a[0:3],a[144:147],v[68:71]\n" \
"ds_read_b128 a[0:3],v240 offset:32768\nv_max3_f32 v225,v64,v65,v66\nM v[72:75],a[16:19],a[128:131],0\nds_read_b128 a[104:107],v241 offset:10240\nM v[72:75],a[20:23],a[132:135],v[72:75]\nds_read_b128 a[76:79],v241 offset:3072\nM v[72:75],a[24:27],a[136:139],v[72:75]\nbuffer_load_dwordx4 v240,s[44:47],s36 offen offset:2048 sc1 lds\n" \
"M v[72:75],a[28:31],a[140:143],v[72:75]\nv_max3_f32 v229,v68,v69,v70\nds_read_b128 a[108:111],v241 offset:11264\nds_read_b128 a[80:83],v241 offset:4096\nM v[76:79],a[28:31],a[156:159],0\nds_read_b128 a[28:31],v240 offset:39936\nM v[76:79],a[24:27],a[152:155],v[76:79]\nds_read_b128 a[24:27],v240 offset:38912\n" \
"M v[76:79],a[20:23],a[148:151],v[76:79]\nds_read_b128 a[20:23],v240 offset:37888\nv_max3_f32 v225,v225,v67,v72\nv_max3_f32 v225,v225,v73,v74\nM v[76:79],a[16:19],a[144:147],v[76:79]\nds_read_b128 a[16:19],v240 offset:36864\nv_max_f32 v225,v225,v75\nv_cndmask_b32 v225,v224,v225,s[90:91]\n" \
"M v[80:83],a[32:35],a[128:131],0\nbuffer_load_dwordx4 v240,s[44:47],s36 offen offset:3072 sc1 lds\nM v[80:83],a[36:39],a[132:135],v[80:83]\nds_read_b128 a[112:115],v241 offset:12288\nds_read_b128 a[84:87],v241 offset:5120\nv_max3_f32 v229,v229,v71,v76\nM v[80:83],a[40:43],a[136:139],v[80:83]\nv_max3_f32 v229,v229,v77,v78\n" \
"v_max_f32 v229,v229,v79\nv_cndmask_b32 v229,v228,v229,s[90:91]\nM v[80:83],a[44:47],a[140:143],v[80:83]\nds_read_b128 a[116:119],v241 offset:13312\nv_permlane16_swap_b32 v225,v229\nM v[84:87],a[44:47],a[156:159],0\nv_max_f32 v225,v225,v229\nv_mov_b32 v229,v225\n" \
"ds_read_b128 a[44:47],v240 offset:44032\nM v[84:87],a[40:43],a[152:155],v[84:87]\nv_permlane32_swap_b32 v225,v229\nv_max_f32 v225,v225,v229\nM v[84:87],a[36:39],a[148:151],v[84:87]\nv_mov_b32 v229,v225\nds_read_b128 a[36:39],v240 offset:41984\nds_read_b128 a[40:43],v240 offset:43008\n" \
"M v[84:87],a[32:35],a[144:147],v[84:87]\nv_permlane16_swap_b32 v225,v229\nv_sub_f32 v224,0,v225\nv_sub_f32 v226,0,v225\nM v[92:95],a[60:63],a[156:159],0\nv_sub_f32 v227,0,v225\nv_sub_f32 v225,0,v225\nM v[92:95],a[56:59],a[152:155],v[92:95]\n" \
"v_sub_f32 v228,0,v229\nv_sub_f32 v230,0,v229\nv_sub_f32 v231,0,v229\nM v[92:95],a[52:55],a[148:151],v[92:95]\nv_sub_f32 v229,0,v229\nds_read_b128 a[32:35],v240 offset:40960\nds_read_b128 a[52:55],v240 offset:46080\nM v[92:95],a[48:51],a[144:147],v[92:95]\n" \
"ds_read_b128 a[48:51],v240 offset:45056\ns_waitcnt lgkmcnt(7)\nM v[136:139],a[0:3],a[160:163],0\nds_read_b128 a[56:59],v240 offset:47104\nv_add_f32 v64,v64,v224\nv_add_f32 v65,v65,v224\nM v[136:139],a[4:7],a[164:167],v[136:139]\nds_read_b128 a[60:63],v240 offset:48128\n" \
"v_add_f32 v66,v66,v224\nM v[136:139],a[8:11],a[168:171],v[136:139]\nv_add_f32 v67,v67,v224\nv_exp_f32 v64,v64\nM v[136:139],a[12:15],a[172:175],v[136:139]\nv_exp_f32 v65,v65\nv_cndmask_b32 v80,v80,v252,s[82:83]\nM v[140:143],a[12:15],a[188:191],0\n" \
"v_cndmask_b32 v81,v81,v252,s[84:85]\nv_add_f32 v72,v72,v224\nM v[140:143],a[8:11],a[184:187],v[140:143]\nv_exp_f32 v66,v66\nv_add_f32 v73,v73,v224\nM v[140:143],a[4:7],a[180:183],v[140:143]\nv_exp_f32 v67,v67\nv_add_f32 v74,v74,v224\n" \
"M v[140:143],a[0:3],a[176:179],v[140:143]\nv_add_f32 v75,v75,v224\nv_cndmask_b32 v82,v82,v252,s[86:87]\nM v[156:159],a[28:31],a[188:191],0\nv_cndmask_b32 v83,v83,v252,s[88:89]\nv_add_f32 v80,v80,v224\nv_add_f32 v81,v81,v224\nM v[156:159],a[24:27],a[184:187],v[156:159]\n" \
"v_exp_f32 v72,v72\nv_add_f32 v82,v82,v224\nM v[156:159],a[20:23],a[180:183],v[156:159]\nv_exp_f32 v73,v73\nM v[156:159],a[16:19],a[176:179],v[156:159]\nv_exp_f32 v74,v74\nv_add_f32 v83,v83,v224\nM v[128:131],a[0:3],a[128:131],v[224:227]\n" \
"v_exp_f32 v75,v75\nM v[128:131],a[4:7],a[132:135],v[128:131]\nv_exp_f32 v80,v80\nv_cvt_pk_bf16_f32 v64,v64,v65\nM v[128:131],a[8:11],a[136:139],v[128:131]\nv_exp_f32 v81,v81\nv_cvt_pk_bf16_f32 v65,v66,v67\nM v[128:131],a[12:15],a[140:143],v[128:131]\n" \
"v_exp_f32 v82,v82\nM v[132:135],a[12:15],a[156:159],v[228:231]\nv_exp_f32 v83,v83\nv_cvt_pk_bf16_f32 v66,v72,v73\nM v[132:135],a[8:11],a[152:155],v[132:135]\nv_cvt_pk_bf16_f32 v67,v74,v75\nv_cvt_pk_bf16_f32 v80,v80,v81\nv_cvt_pk_bf16_f32 v81,v82,v83\n" \
"M v[132:135],a[4:7],a[148:151],v[132:135]\nv_mov_b32 v82,0\nv_mov_b32 v83,0\nM v[132:135],a[0:3],a[144:147],v[132:135]\nds_read_b128 a[88:91],v241 offset:6144\nds_read_b128 a[120:123],v241 offset:14336\nv_add_f32 v68,v68,v228\nM v[144:147],a[16:19],a[128:131],v[224:227]\n" \
"ds_read_b128 a[92:95],v241 offset:7168\nv_add_f32 v69,v69,v228\nv_add_f32 v70,v70,v228\nM v[144:147],a[20:23],a[132:135],v[144:147]\nds_read_b128 a[124:127],v241 offset:15360\nv_add_f32 v71,v71,v228\nM v[144:147],a[24:27],a[136:139],v[144:147]\nv_exp_f32 v68,v68\n" \
"v_add_f32 v76,v76,v228\nM v[144:147],a[28:31],a[140:143],v[144:147]\nv_exp_f32 v69,v69\nM v[148:151],a[28:31],a[156:159],v[228:231]\nv_exp_f32 v70,v70\nv_add_f32 v77,v77,v228\nM v[148:151],a[24:27],a[152:155],v[148:151]\nv_exp_f32 v71,v71\n" \
"v_add_f32 v78,v78,v228\nM v[148:151],a[20:23],a[148:151],v[148:151]\nv_add_f32 v79,v79,v228\nv_add_f32 v84,v84,v228\nM v[148:151],a[16:19],a[144:147],v[148:151]\nv_add_f32 v85,v85,v228\nv_cndmask_b32 v92,v92,v252,s[82:83]\ns_waitcnt lgkmcnt(4)\n" \
"M v[160:163],a[32:35],a[128:131],v[224:227]\nv_exp_f32 v76,v76\nv_cndmask_b32 v93,v93,v252,s[84:85]\nM v[160:163],a[36:39],a[132:135],v[160:163]\nv_exp_f32 v77,v77\nM v[160:163],a[40:43],a[136:139],v[160:163]\nv_exp_f32 v78,v78\nv_cndmask_b32 v94,v94,v252,s[86:87]\n" \
"M v[160:163],a[44:47],a[140:143],v[160:163]\nv_exp_f32 v79,v79\nM v[164:167],a[44:47],a[156:159],v[228:231]\nv_cndmask_b32 v95,v95,v252,s[88:89]\nv_add_f32 v86,v86,v228\nv_add_f32 v87,v87,v228\nM v[164:167],a[40:43],a[152:155],v[164:167]\nv_exp_f32 v84,v84\n" \
"v_cvt_pk_bf16_f32 v68,v68,v69\nM v[164:167],a[36:39],a[148:151],v[164:167]\nv_exp_f32 v85,v85\nM v[164:167],a[32:35],a[144:147],v[164:167]\nv_exp_f32 v86,v86\nv_add_f32 v92,v92,v228\nM v[176:179],a[48:51],a[128:131],v[224:227]\nv_exp_f32 v87,v87\n" \
"v_add_f32 v93,v93,v228\nM v[176:179],a[52:55],a[132:135],v[176:179]\nv_add_f32 v94,v94,v228\nv_add_f32 v95,v95,v228\nM v[176:179],a[56:59],a[136:139],v[176:179]\nbuffer_load_dword v248,v251,s[52:55],s80 offen sc1\nv_cvt_pk_bf16_f32 v69,v70,v71\nM v[176:179],a[60:63],a[140:143],v[176:179]\n" \
"v_exp_f32 v92,v92\nv_cvt_pk_bf16_f32 v70,v76,v77\nM v[180:183],a[60:63],a[156:159],v[228:231]\nv_exp_f32 v93,v93\nM v[180:183],a[56:59],a[152:155],v[180:183]\nv_exp_f32 v94,v94\nv_cvt_pk_bf16_f32 v71,v78,v79\nM v[180:183],a[52:55],a[148:151],v[180:183]\n" \
"v_exp_f32 v95,v95\nM v[180:183],a[48:51],a[144:147],v[180:183]\nv_cvt_pk_bf16_f32 v84,v84,v85\nv_cvt_pk_bf16_f32 v85,v86,v87\nv_cvt_pk_bf16_f32 v86,v92,v93\nM v[0:3],a[64:67],v[64:67],0\nv_cvt_pk_bf16_f32 v87,v94,v95\nds_read_b128 a[0:3],v240 offset:49152\n" \
"ds_read_b128 a[4:7],v240 offset:50176\nM v[0:3],a[96:99],v[80:83],v[0:3]\nds_read_b128 a[8:11],v240 offset:51200\nv_max3_f32 v237,v140,v141,v142\nM v[8:11],a[68:71],v[64:67],0\nds_read_b128 a[12:15],v240 offset:52224\nv_exp_f32 v128,v128\nM v[8:11],a[100:103],v[80:83],v[8:11]\n" \
"ds_read_b128 a[16:19],v240 offset:53248\nv_exp_f32 v129,v129\nM v[16:19],a[72:75],v[64:67],0\nv_exp_f32 v130,v130\nM v[16:19],a[104:107],v[80:83],v[16:19]\nv_exp_f32 v131,v131\nds_read_b128 a[20:23],v240 offset:54272\nM v[24:27],a[76:79],v[64:67],0\n" \
"v_cvt_pk_bf16_f32 v192,v128,v129\nv_cvt_pk_bf16_f32 v193,v130,v131\nds_read_b128 a[24:27],v240 offset:55296\nM v[24:27],a[108:111],v[80:83],v[24:27]\nds_read_b128 a[28:31],v240 offset:56320\nv_max3_f32 v237,v237,v143,v156\nM v[32:35],a[80:83],v[64:67],0\nv_exp_f32 v132,v132\n" \
"v_max3_f32 v233,v136,v137,v138\nM v[32:35],a[112:115],v[80:83],v[32:35]\nv_exp_f32 v133,v133\nM v[40:43],a[84:87],v[64:67],0\nv_exp_f32 v134,v134\nv_cvt_pk_bf16_f32 v196,v132,v133\nM v[40:43],a[116:119],v[80:83],v[40:43]\nv_exp_f32 v135,v135\n" \
"s_waitcnt lgkmcnt(4)\nM v[48:51],a[88:91],v[64:67],0\nv_cvt_pk_bf16_f32 v197,v134,v135\nv_max3_f32 v237,v237,v157,v158\nM v[48:51],a[120:123],v[80:83],v[48:51]\nv_exp_f32 v144,v144\nv_max_f32 v233,v233,v139\nM v[56:59],a[92:95],v[64:67],0\n" \
"v_exp_f32 v145,v145\nv_max_f32 v237,v237,v159\nM v[56:59],a[124:127],v[80:83],v[56:59]\nv_exp_f32 v146,v146\nM a[192:195],a[208:211],v[64:67],0\nv_exp_f32 v147,v147\nv_cvt_pk_bf16_f32 v194,v144,v145\nM a[192:195],a[208:211],v[80:83],a[192:195]\n" \
"v_cvt_pk_bf16_f32 v195,v146,v147\nv_cndmask_b32 v233,v232,v233,s[90:91]\nM v[4:7],a[64:67],v[68:71],0\nv_cndmask_b32 v237,v236,v237,s[90:91]\nv_exp_f32 v148,v148\nM v[4:7],a[96:99],v[84:87],v[4:7]\nv_permlane16_swap_b32 v233,v237\nv_max_f32 v233,v233,v237\n" \
"v_mov_b32 v237,v233\nM v[12:15],a[68:71],v[68:71],0\nv_exp_f32 v149,v149\nM v[12:15],a[100:103],v[84:87],v[12:15]\nv_exp_f32 v150,v150\nv_permlane32_swap_b32 v233,v237\nM v[20:23],a[72:75],v[68:71],0\nv_exp_f32 v151,v151\n" \
"v_cvt_pk_bf16_f32 v198,v148,v149\nM v[20:23],a[104:107],v[84:87],v[20:23]\nv_cvt_pk_bf16_f32 v199,v150,v151\nv_max_f32 v233,v233,v237\nM v[28:31],a[76:79],v[68:71],0\nv_mov_b32 v237,v233\nv_cndmask_b32 v136,v136,v252,s[82:83]\nv_cndmask_b32 v137,v137,v252,s[84:85]\n" \
"M v[28:31],a[108:111],v[84:87],v[28:31]\nv_permlane16_swap_b32 v233,v237\nv_cndmask_b32 v138,v138,v252,s[86:87]\nv_cndmask_b32 v139,v139,v252,s[88:89]\nM v[36:39],a[80:83],v[68:71],0\nv_sub_f32 v232,0,v233\nv_sub_f32 v234,0,v233\nM v[36:39],a[112:115],v[84:87],v[36:39]\n" \
"v_sub_f32 v235,0,v233\nv_add_f32 v136,v136,v232\nv_add_f32 v137,v137,v232\nM v[44:47],a[84:87],v[68:71],0\nv_add_f32 v138,v138,v232\nv_add_f32 v139,v139,v232\nM v[44:47],a[116:119],v[84:87],v[44:47]\nv_sub_f32 v233,0,v233\n" \
"v_exp_f32 v136,v136\nM v[52:55],a[88:91],v[68:71],0\nv_exp_f32 v137,v137\nv_sub_f32 v236,0,v237\nM v[52:55],a[120:123],v[84:87],v[52:55]\nv_exp_f32 v138,v138\nM v[60:63],a[92:95],v[68:71],0\nv_exp_f32 v139,v139\n" \
"v_cvt_pk_bf16_f32 v200,v136,v137\nM v[60:63],a[124:127],v[84:87],v[60:63]\nv_cvt_pk_bf16_f32 v201,v138,v139\nv_sub_f32 v238,0,v237\nv_sub_f32 v239,0,v237\nM a[196:199],a[208:211],v[68:71],0\nv_add_f32 v140,v140,v236\nv_add_f32 v141,v141,v236\n" \
"M a[196:199],a[208:211],v[84:87],a[196:199]\nv_add_f32 v142,v142,v236\nv_add_f32 v143,v143,v236\nv_sub_f32 v237,0,v237\nM v[128:131],a[0:3],a[128:131],v[224:227]\nv_exp_f32 v140,v140\nds_read_b128 a[32:35],v240 offset:57344\nM v[128:131],a[4:7],a[132:135],v[128:131]\n" \
"v_exp_f32 v141,v141\nM v[128:131],a[8:11],a[136:139],v[128:131]\nv_exp_f32 v142,v142\nv_cvt_pk_bf16_f32 v204,v140,v141\nM v[128:131],a[12:15],a[140:143],v[128:131]\nv_exp_f32 v143,v143\nM v[132:135],a[12:15],a[156:159],v[228:231]\nv_cvt_pk_bf16_f32 v205,v142,v143\n" \
"ds_read_b128 a[36:39],v240 offset:58368\nds_read_b128 a[40:43],v240 offset:59392\nM v[132:135],a[8:11],a[152:155],v[132:135]\nds_read_b128 a[44:47],v240 offset:60416\nv_cndmask_b32 v156,v156,v252,s[82:83]\nv_cndmask_b32 v157,v157,v252,s[84:85]\nM v[132:135],a[4:7],a[148:151],v[132:135]\nv_cndmask_b32 v158,v158,v252,s[86:87]\n" \
"v_cndmask_b32 v159,v159,v252,s[88:89]\nM v[132:135],a[0:3],a[144:147],v[132:135]\nv_add_f32 v156,v156,v236\nv_add_f32 v157,v157,v236\ns_waitcnt lgkmcnt(4)\nM v[144:147],a[16:19],a[128:131],v[224:227]\ns_waitcnt vmcnt(1)\ns_barrier\n" \
"v_add_f32 v158,v158,v236\nM v[144:147],a[20:23],a[132:135],v[144:147]\nv_add_f32 v159,v159,v236\nds_read_b128 a[48:51],v240 offset:61440\nM v[144:147],a[24:27],a[136:139],v[144:147]\nv_exp_f32 v156,v156\nds_read_b128 a[52:55],v240 offset:62464\nM v[144:147],a[28:31],a[140:143],v[144:147]\n" \
"v_exp_f32 v157,v157\nM v[148:151],a[28:31],a[156:159],v[228:231]\nv_exp_f32 v158,v158\nv_cvt_pk_bf16_f32 v206,v156,v157\nM v[148:151],a[24:27],a[152:155],v[148:151]\nv_exp_f32 v159,v159\nds_read_b128 a[56:59],v240 offset:63488\nM v[148:151],a[20:23],a[148:151],v[148:151]\n" \
"v_cvt_pk_bf16_f32 v207,v158,v159\nds_read_b128 a[60:63],v240 offset:64512\nM v[148:151],a[16:19],a[144:147],v[148:151]\nv_exp_f32 v160,v160\nds_read_b128 a[64:67],v241 offset:32768\nM v[136:139],a[0:3],a[160:163],v[232:235]\nv_exp_f32 v161,v161\nds_read_b128 a[96:99],v241 offset:40960\n" \
"M v[136:139],a[4:7],a[164:167],v[136:139]\nv_exp_f32 v162,v162\nM v[136:139],a[8:11],a[168:171],v[136:139]\nv_exp_f32 v163,v163\nv_cvt_pk_bf16_f32 v208,v160,v161\nM v[136:139],a[12:15],a[172:175],v[136:139]\nv_cvt_pk_bf16_f32 v209,v162,v163\nv_exp_f32 v164,v164\n" \
"M v[140:143],a[12:15],a[188:191],v[236:239]\nv_exp_f32 v165,v165\nM v[140:143],a[8:11],a[184:187],v[140:143]\nv_exp_f32 v166,v166\nv_cvt_pk_bf16_f32 v212,v164,v165\nM v[140:143],a[4:7],a[180:183],v[140:143]\nv_exp_f32 v167,v167\nM v[140:143],a[0:3],a[176:179],v[140:143]\n" \
"v_cvt_pk_bf16_f32 v213,v166,v167\nv_exp_f32 v176,v176\nM v[152:155],a[16:19],a[160:163],v[232:235]\nv_exp_f32 v177,v177\nds_read_b128 a[68:71],v241 offset:33792\nM v[152:155],a[20:23],a[164:167],v[152:155]\nv_exp_f32 v178,v178\nM v[152:155],a[24:27],a[168:171],v[152:155]\n" \
"v_exp_f32 v179,v179\nv_cvt_pk_bf16_f32 v210,v176,v177\nM v[152:155],a[28:31],a[172:175],v[152:155]\nv_cvt_pk_bf16_f32 v211,v178,v179\nv_exp_f32 v180,v180\nM v[156:159],a[28:31],a[188:191],v[236:239]\nv_exp_f32 v181,v181\nM v[156:159],a[24:27],a[184:187],v[156:159]\n" \
"v_exp_f32 v182,v182\nv_cvt_pk_bf16_f32 v214,v180,v181\nM v[156:159],a[20:23],a[180:183],v[156:159]\nv_exp_f32 v183,v183\nds_read_b128 a[100:103],v241 offset:41984\nM v[156:159],a[16:19],a[176:179],v[156:159]\nv_cvt_pk_bf16_f32 v215,v182,v183\ns_waitcnt lgkmcnt(2)\n" \
"M v[160:163],a[32:35],a[128:131],v[224:227]\nds_read_b128 a[72:75],v241 offset:34816\nds_read_b128 a[104:107],v241 offset:43008\ns_waitcnt vmcnt(0)\nv_cmp_eq_u32 vcc,0,v248\nM v[160:163],a[36:39],a[132:135],v[160:163]\nds_read_b128 a[76:79],v241 offset:35840\ns_cbranch_vccnz .Lps3\n" \
".Lpb3:\nM v[160:163],a[40:43],a[136:139],v[160:163]\nds_read_b128 a[108:111],v241 offset:44032\nv_exp_f32 v128,v128\nM v[160:163],a[44:47],a[140:143],v[160:163]\nds_read_b128 a[80:83],v241 offset:36864\nv_exp_f32 v129,v129\nM v[164:167],a[44:47],a[156:159],v[228:231]\n" \
"ds_read_b128 a[112:115],v241 offset:45056\ns_add_u32 m0,s64,65536\nM v[164:167],a[40:43],a[152:155],v[164:167]\nds_read_b128 a[84:87],v241 offset:37888\nbuffer_load_dwordx4 v240,s[48:51],s36 offen offset:0 sc1 lds\nM v[164:167],a[36:39],a[148:151],v[164:167]\nds_read_b128 a[116:119],v241 offset:46080\nv_exp_f32 v130,v130\n" \
"M v[164:167],a[32:35],a[144:147],v[164:167]\nv_exp_f32 v131,v131\nM v[176:179],a[48:51],a[128:131],v[224:227]\nds_read_b128 a[88:91],v241 offset:38912\nv_exp_f32 v132,v132\nM v[176:179],a[52:55],a[132:135],v[176:179]\nds_read_b128 a[120:123],v241 offset:47104\nds_read_b128 a[92:95],v241 offset:39936\n" \
"M v[176:179],a[56:59],a[136:139],v[176:179]\nds_read_b128 a[124:127],v241 offset:48128\nbuffer_load_dwordx4 v240,s[48:51],s36 offen offset:1024 sc1 lds\nM v[176:179],a[60:63],a[140:143],v[176:179]\nds_read_b128 a[0:3],v240 offset:0\nv_exp_f32 v133,v133\nM v[180:183],a[60:63],a[156:159],v[228:231]\nv_exp_f32 v134,v134\n" \
"M v[180:183],a[56:59],a[152:155],v[180:183]\nds_read_b128 a[4:7],v240 offset:1024\nv_exp_f32 v135,v135\nM v[180:183],a[52:55],a[148:151],v[180:183]\nds_read_b128 a[8:11],v240 offset:2048\nbuffer_load_dwordx4 v240,s[48:51],s36 offen offset:2048 sc1 lds\nM v[180:183],a[48:51],a[144:147],v[180:183]\nds_read_b128 a[12:15],v240 offset:3072\n" \
"ds_read_b128 a[16:19],v240 offset:4096\nM v[168:171],a[32:35],a[160:163],v[232:235]\nds_read_b128 a[20:23],v240 offset:5120\nv_exp_f32 v144,v144\nM v[168:171],a[36:39],a[164:167],v[168:171]\nds_read_b128 a[24:27],v240 offset:6144\nv_exp_f32 v145,v145\nM v[168:171],a[40:43],a[168:171],v[168:171]\n" \
"buffer_load_dwordx4 v240,s[48:51],s36 offen offset:3072 sc1 lds\nM v[168:171],a[44:47],a[172:175],v[168:171]\nds_read_b128 a[28:31],v240 offset:7168\nv_exp_f32 v146,v146\nM v[172:175],a[44:47],a[188:191],v[236:239]\nv_exp_f32 v147,v147\nM v[172:175],a[40:43],a[184:187],v[172:175]\ns_add_u32 m0,s64,16384\n" \
"v_exp_f32 v148,v148\nM v[172:175],a[36:39],a[180:183],v[172:175]\nbuffer_load_dwordx4 v240,s[44:47],s65 offen offset:0 sc1 lds\nM v[172:175],a[32:35],a[176:179],v[172:175]\nds_read_b128 a[32:35],v240 offset:8192\nds_read_b128 a[36:39],v240 offset:9216\nM v[184:187],a[48:51],a[160:163],v[232:235]\nds_read_b128 a[40:43],v240 offset:10240\n" \
"v_exp_f32 v149,v149\nM v[184:187],a[52:55],a[164:167],v[184:187]\nds_read_b128 a[44:47],v240 offset:11264\nv_exp_f32 v150,v150\nM v[184:187],a[56:59],a[168:171],v[184:187]\nbuffer_load_dwordx4 v240,s[44:47],s65 offen offset:1024 sc1 lds\nM v[184:187],a[60:63],a[172:175],v[184:187]\nv_exp_f32 v151,v151\n" \
"M v[188:191],a[60:63],a[188:191],v[236:239]\nv_exp_f32 v136,v136\nM v[188:191],a[56:59],a[184:187],v[188:191]\nv_exp_f32 v137,v137\nM v[188:191],a[52:55],a[180:183],v[188:191]\nbuffer_load_dwordx4 v240,s[44:47],s65 offen offset:2048 sc1 lds\nM v[188:191],a[48:51],a[176:179],v[188:191]\nds_read_b128 a[48:51],v240 offset:12288\n" \
"ds_read_b128 a[52:55],v240 offset:13312\nM v[0:3],a[64:67],v[192:195],v[0:3]\nds_read_b128 a[56:59],v240 offset:14336\nv_exp_f32 v138,v138\nM v[0:3],a[96:99],v[208:211],v[0:3]\nv_exp_f32 v139,v139\ns_waitcnt lgkmcnt(4)\nM v[8:11],a[68:71],v[192:195],v[8:11]\n" \
"buffer_load_dwordx4 v240,s[44:47],s65 offen offset:3072 sc1 lds\nM v[8:11],a[100:103],v[208:211],v[8:11]\ns_add_u32 m0,s64,81920\nv_exp_f32 v152,v152\nM v[16:19],a[72:75],v[192:195],v[16:19]\nds_read_b128 a[60:63],v240 offset:15360\nv_exp_f32 v153,v153\nM v[16:19],a[104:107],v[208:211],v[16:19]\n" \
"v_exp_f32 v154,v154\nM v[24:27],a[76:79],v[192:195],v[24:27]\nbuffer_load_dwordx4 v240,s[48:51],s65 offen offset:0 sc1 lds\nM v[24:27],a[108:111],v[208:211],v[24:27]\nv_exp_f32 v155,v155\nM v[32:35],a[80:83],v[192:195],v[32:35]\ns_barrier\nv_exp_f32 v160,v160\n" \
"M v[32:35],a[112:115],v[208:211],v[32:35]\nv_exp_f32 v161,v161\nM v[40:43],a[84:87],v[192:195],v[40:43]\nbuffer_load_dwordx4 v240,s[48:51],s65 offen offset:1024 sc1 lds\nM v[40:43],a[116:119],v[208:211],v[40:43]\nv_exp_f32 v162,v162\nM v[48:51],a[88:91],v[192:195],v[48:51]\nv_exp_f32 v163,v163\n" \
"M v[48:51],a[120:123],v[208:211],v[48:51]\nv_exp_f32 v176,v176\nM v[56:59],a[92:95],v[192:195],v[56:59]\nbuffer_load_dwordx4 v240,s[48:51],s65 offen offset:2048 sc1 lds\nM v[56:59],a[124:127],v[208:211],v[56:59]\nv_exp_f32 v177,v177\nM a[192:195],a[208:211],v[192:195],a[192:195]\nv_exp_f32 v178,v178\n" \
"M a[192:195],a[208:211],v[208:211],a[192:195]\nv_exp_f32 v179,v179\nv_cvt_pk_bf16_f32 v192,v128,v129\nM v[4:7],a[64:67],v[196:199],v[4:7]\nbuffer_load_dwordx4 v240,s[48:51],s65 offen offset:3072 sc1 lds\nM v[4:7],a[96:99],v[212:215],v[4:7]\ns_add_u32 m0,s64,32768\nv_exp_f32 v140,v140\n" \
"M v[12:15],a[68:71],v[196:199],v[12:15]\nv_exp_f32 v141,v141\nv_cvt_pk_bf16_f32 v193,v130,v131\nM v[12:15],a[100:103],v[212:215],v[12:15]\nv_exp_f32 v142,v142\nM v[20:23],a[72:75],v[196:199],v[20:23]\nbuffer_load_dwordx4 v240,s[44:47],s66 offen offset:0 sc1 lds\nv_cvt_pk_bf16_f32 v194,v144,v145\n" \
"M v[20:23],a[104:107],v[212:215],v[20:23]\nv_exp_f32 v143,v143\nv_cvt_pk_bf16_f32 v195,v146,v147\nM v[28:31],a[76:79],v[196:199],v[28:31]\nv_exp_f32 v156,v156\nM v[28:31],a[108:111],v[212:215],v[28:31]\nv_exp_f32 v157,v157\nv_cvt_pk_bf16_f32 v208,v160,v161\n" \
"M v[36:39],a[80:83],v[196:199],v[36:39]\nbuffer_load_dwordx4 v240,s[44:47],s66 offen offset:1024 sc1 lds\nM v[36:39],a[112:115],v[212:215],v[36:39]\nv_exp_f32 v158,v158\nv_cvt_pk_bf16_f32 v209,v162,v163\nM v[44:47],a[84:87],v[196:199],v[44:47]\nv_exp_f32 v159,v159\nv_cvt_pk_bf16_f32 v210,v176,v177\n" \
"M v[44:47],a[116:119],v[212:215],v[44:47]\nv_exp_f32 v164,v164\nM v[52:55],a[88:91],v[196:199],v[52:55]\nbuffer_load_dwordx4 v240,s[44:47],s66 offen offset:2048 sc1 lds\nv_cvt_pk_bf16_f32 v211,v178,v179\nM v[52:55],a[120:123],v[212:215],v[52:55]\nv_exp_f32 v165,v165\nM v[60:63],a[92:95],v[196:199],v[60:63]\n" \
"v_exp_f32 v166,v166\nM v[60:63],a[124:127],v[212:215],v[60:63]\nv_exp_f32 v167,v167\nM a[196:199],a[208:211],v[196:199],a[196:199]\nbuffer_load_dwordx4 v240,s[44:47],s66 offen offset:3072 sc1 lds\ns_add_u32 m0,s64,98304\nM a[196:199],a[208:211],v[212:215],a[196:199]\nv_exp_f32 v180,v180\n" \
"M v[64:67],a[64:67],v[200:203],0\nv_exp_f32 v181,v181\nv_cvt_pk_bf16_f32 v196,v132,v133\nM v[72:75],a[68:71],v[200:203],0\nv_exp_f32 v182,v182\nM v[80:83],a[72:75],v[200:203],0\nv_exp_f32 v183,v183\nv_cvt_pk_bf16_f32 v197,v134,v135\n" \
"M v[88:91],a[76:79],v[200:203],0\nv_exp_f32 v168,v168\nv_cvt_pk_bf16_f32 v198,v148,v149\nM v[96:99],a[80:83],v[200:203],0\nv_exp_f32 v169,v169\nM v[104:107],a[84:87],v[200:203],0\nv_exp_f32 v170,v170\nv_cvt_pk_bf16_f32 v199,v150,v151\n" \
"M v[112:115],a[88:91],v[200:203],0\nv_exp_f32 v171,v171\nM v[120:123],a[92:95],v[200:203],0\nbuffer_load_dwordx4 v240,s[48:51],s66 offen offset:0 sc1 lds\nM a[200:203],a[208:211],v[200:203],0\nv_cvt_pk_bf16_f32 v200,v136,v137\nv_cvt_pk_bf16_f32 v201,v138,v139\nv_cvt_pk_bf16_f32 v202,v152,v153\n" \
"M v[68:71],a[64:67],v[204:207],0\nv_cvt_pk_bf16_f32 v203,v154,v155\nM v[76:79],a[68:71],v[204:207],0\nM v[84:87],a[72:75],v[204:207],0\nbuffer_load_dwordx4 v240,s[48:51],s66 offen offset:1024 sc1 lds\nM v[92:95],a[76:79],v[204:207],0\nM v[100:103],a[80:83],v[204:207],0\nM v[108:111],a[84:87],v[204:207],0\n" \
"M v[116:119],a[88:91],v[204:207],0\nbuffer_load_dwordx4 v240,s[48:51],s66 offen offset:2048 sc1 lds\nM v[124:127],a[92:95],v[204:207],0\nbuffer_load_dwordx4 v240,s[48:51],s66 offen offset:3072 sc1 lds\nM a[204:207],a[208:211],v[204:207],0\ns_cmp_lt_u32 s62,2\ns_cbranch_scc1 .Lx01\ns_branch .Ldual\n" \
".Lp23:\ns_cmp_eq_u32 s7,2\ns_cbranch_scc0 .Lpro3\n.Lpro2:\ns_barrier\nM v[64:67],a[0:3],a[128:131],0\nds_read_b128 a[64:67],v241 offset:0\nds_read_b128 a[96:99],v241 offset:8192\n" \
"M v[64:67],a[4:7],a[132:135],v[64:67]\nds_read_b128 a[68:71],v241 offset:1024\ns_mov_b32 m0,s64\nv_mov_b32 v250,139264\nM v[64:67],a[8:11],a[136:139],v[64:67]\nds_read_b128 a[100:103],v241 offset:9216\nbuffer_load_dwordx4 v240,s[44:47],s36 offen offset:0 sc1 lds\nM v[64:67],a[12:15],a[140:143],v[64:67]\n" \
"ds_read_b128 a[72:75],v241 offset:2048\nv_mov_b32 v184,0xff800000\nv_mov_b32 v185,0xff800000\nM v[68:71],a[12:15],a[156:159],0\nds_read_b128 a[12:15],v240 offset:19456\nv_mov_b32 v186,0xff800000\nv_mov_b32 v187,0xff800000\nM v[68:71],a[8:11],a[152:155],v[68:71]\n" \
"ds_read_b128 a[8:11],v240 offset:18432\nv_max3_f32 v225,v64,v65,v66\nM v[68:71],a[4:7],a[148:151],v[68:71]\nds_read_b128 a[4:7],v240 offset:17408\nbuffer_load_dwordx4 v240,s[44:47],s36 offen offset:1024 sc1 lds\nM v[68:71],a[0:3],a[144:147],v[68:71]\nds_read_b128 a[0:3],v240 offset:16384\nM v[72:75],a[16:19],a[128:131],0\n" \
"ds_read_b128 a[104:107],v241 offset:10240\nM v[72:75],a[20:23],a[132:135],v[72:75]\nds_read_b128 a[76:79],v241 offset:3072\nM v[72:75],a[24:27],a[136:139],v[72:75]\nds_read_b128 a[108:111],v241 offset:11264\nbuffer_load_dwordx4 v240,s[44:47],s36 offen offset:2048 sc1 lds\nM v[72:75],a[28:31],a[140:143],v[72:75]\nv_max3_f32 v229,v68,v69,v70\n" \
"ds_write_b128 v250,a[244:247]\nM v[76:79],a[28:31],a[156:159],0\nds_read_b128 a[28:31],v240 offset:23552\nM v[76:79],a[24:27],a[152:155],v[76:79]\nds_read_b128 a[24:27],v240 offset:22528\nM v[76:79],a[20:23],a[148:151],v[76:79]\nds_read_b128 a[20:23],v240 offset:21504\nv_max3_f32 v225,v225,v67,v72\n" \
"v_max3_f32 v225,v225,v73,v74\nM v[76:79],a[16:19],a[144:147],v[76:79]\nv_max_f32 v225,v225,v75\nv_cndmask_b32 v225,v224,v225,s[90:91]\nds_read_b128 a[16:19],v240 offset:20480\nM v[80:83],a[32:35],a[128:131],0\nds_read_b128 a[80:83],v241 offset:4096\nbuffer_load_dwordx4 v240,s[44:47],s36 offen offset:3072 sc1 lds\n" \
"M v[80:83],a[36:39],a[132:135],v[80:83]\nds_read_b128 a[112:115],v241 offset:12288\nv_max3_f32 v229,v229,v71,v76\nv_max3_f32 v229,v229,v77,v78\nM v[80:83],a[40:43],a[136:139],v[80:83]\nv_max_f32 v229,v229,v79\nv_cndmask_b32 v229,v228,v229,s[90:91]\nds_read_b128 a[84:87],v241 offset:5120\n" \
"M v[80:83],a[44:47],a[140:143],v[80:83]\nv_permlane16_swap_b32 v225,v229\nv_max_f32 v225,v225,v229\nv_mov_b32 v229,v225\nM v[84:87],a[44:47],a[156:159],0\nds_read_b128 a[116:119],v241 offset:13312\nv_permlane32_swap_b32 v225,v229\nv_max_f32 v225,v225,v229\n" \
"M v[84:87],a[40:43],a[152:155],v[84:87]\nv_mov_b32 v229,v225\nds_read_b128 a[88:91],v241 offset:6144\nds_read_b128 a[120:123],v241 offset:14336\nM v[84:87],a[36:39],a[148:151],v[84:87]\nv_permlane16_swap_b32 v225,v229\nv_sub_f32 v224,0,v225\nv_sub_f32 v226,0,v225\n" \
"M v[84:87],a[32:35],a[144:147],v[84:87]\nv_sub_f32 v227,0,v225\nv_sub_f32 v225,0,v225\nv_sub_f32 v228,0,v229\nM v[88:91],a[48:51],a[128:131],0\nv_sub_f32 v230,0,v229\nv_sub_f32 v231,0,v229\nM v[88:91],a[52:55],a[132:135],v[88:91]\n" \
"v_sub_f32 v229,0,v229\nv_add_f32 v64,v64,v224\nv_add_f32 v65,v65,v224\nM v[88:91],a[56:59],a[136:139],v[88:91]\nv_add_f32 v66,v66,v224\nv_add_f32 v67,v67,v224\nv_add_f32 v72,v72,v224\nM v[88:91],a[60:63],a[140:143],v[88:91]\n" \
"v_exp_f32 v64,v64\nv_add_f32 v73,v73,v224\nM v[92:95],a[60:63],a[156:159],0\nv_exp_f32 v65,v65\nv_add_f32 v74,v74,v224\nM v[92:95],a[56:59],a[152:155],v[92:95]\nv_exp_f32 v66,v66\nv_add_f32 v75,v75,v224\n" \
"M v[92:95],a[52:55],a[148:151],v[92:95]\nv_exp_f32 v67,v67\nv_add_f32 v80,v80,v224\nM v[92:95],a[48:51],a[144:147],v[92:95]\nv_add_f32 v81,v81,v224\nv_add_f32 v82,v82,v224\ns_waitcnt lgkmcnt(0)\nM v[96:99],a[0:3],a[128:131],v[224:227]\n" \
"v_exp_f32 v72,v72\nv_add_f32 v83,v83,v224\nM v[96:99],a[4:7],a[132:135],v[96:99]\nv_exp_f32 v73,v73\nv_cvt_pk_bf16_f32 v64,v64,v65\nM v[96:99],a[8:11],a[136:139],v[96:99]\nv_exp_f32 v74,v74\nv_add_f32 v88,v88,v224\n" \
"M v[96:99],a[12:15],a[140:143],v[96:99]\nv_exp_f32 v75,v75\nv_add_f32 v89,v89,v224\nM v[100:103],a[12:15],a[156:159],v[228:231]\nv_exp_f32 v80,v80\nv_add_f32 v90,v90,v224\nM v[100:103],a[8:11],a[152:155],v[100:103]\nv_exp_f32 v81,v81\n" \
"M v[100:103],a[4:7],a[148:151],v[100:103]\nv_exp_f32 v82,v82\nv_add_f32 v91,v91,v224\nM v[100:103],a[0:3],a[144:147],v[100:103]\nv_exp_f32 v83,v83\nv_cvt_pk_bf16_f32 v65,v66,v67\nM v[108:111],a[28:31],a[156:159],v[228:231]\nv_exp_f32 v88,v88\n" \
"v_cvt_pk_bf16_f32 v66,v72,v73\nM v[108:111],a[24:27],a[152:155],v[108:111]\nv_exp_f32 v89,v89\nv_cvt_pk_bf16_f32 v67,v74,v75\nM v[108:111],a[20:23],a[148:151],v[108:111]\nv_exp_f32 v90,v90\nv_exp_f32 v91,v91\nv_cvt_pk_bf16_f32 v80,v80,v81\n" \
"M v[108:111],a[16:19],a[144:147],v[108:111]\nv_cvt_pk_bf16_f32 v81,v82,v83\nv_cvt_pk_bf16_f32 v82,v88,v89\nv_cvt_pk_bf16_f32 v83,v90,v91\nM v[0:3],a[64:67],v[64:67],0\nds_read_b128 a[92:95],v241 offset:7168\nds_read_b128 a[124:127],v241 offset:15360\nv_add_f32 v68,v68,v228\n" \
"M v[0:3],a[96:99],v[80:83],v[0:3]\nv_add_f32 v69,v69,v228\nv_add_f32 v70,v70,v228\nv_add_f32 v71,v71,v228\nM v[8:11],a[68:71],v[64:67],0\nv_exp_f32 v68,v68\nv_add_f32 v76,v76,v228\nM v[8:11],a[100:103],v[80:83],v[8:11]\n" \
"v_exp_f32 v69,v69\nv_add_f32 v77,v77,v228\nM v[16:19],a[72:75],v[64:67],0\nv_exp_f32 v70,v70\nv_add_f32 v78,v78,v228\nM v[16:19],a[104:107],v[80:83],v[16:19]\nv_exp_f32 v71,v71\nM v[24:27],a[76:79],v[64:67],0\n" \
"v_add_f32 v79,v79,v228\nv_add_f32 v84,v84,v228\nv_add_f32 v85,v85,v228\nM v[24:27],a[108:111],v[80:83],v[24:27]\nv_exp_f32 v76,v76\nv_add_f32 v86,v86,v228\nM v[32:35],a[80:83],v[64:67],0\nv_exp_f32 v77,v77\n" \
"v_add_f32 v87,v87,v228\nM v[32:35],a[112:115],v[80:83],v[32:35]\nv_exp_f32 v78,v78\nv_cvt_pk_bf16_f32 v68,v68,v69\nM v[40:43],a[84:87],v[64:67],0\nv_exp_f32 v79,v79\nv_add_f32 v92,v92,v228\nM v[40:43],a[116:119],v[80:83],v[40:43]\n" \
"v_exp_f32 v84,v84\nv_add_f32 v93,v93,v228\nM v[48:51],a[88:91],v[64:67],0\nv_exp_f32 v85,v85\nv_add_f32 v94,v94,v228\nM v[48:51],a[120:123],v[80:83],v[48:51]\nv_exp_f32 v86,v86\ns_waitcnt lgkmcnt(0)\n" \
"M v[56:59],a[92:95],v[64:67],0\nv_exp_f32 v87,v87\nv_add_f32 v95,v95,v228\nM v[56:59],a[124:127],v[80:83],v[56:59]\nv_cvt_pk_bf16_f32 v69,v70,v71\nv_exp_f32 v92,v92\nM a[192:195],a[208:211],v[64:67],0\nv_exp_f32 v93,v93\n" \
"v_exp_f32 v94,v94\nv_exp_f32 v95,v95\nv_cvt_pk_bf16_f32 v70,v76,v77\nv_cvt_pk_bf16_f32 v71,v78,v79\nv_cvt_pk_bf16_f32 v84,v84,v85\nM a[192:195],a[208:211],v[80:83],a[192:195]\nv_cvt_pk_bf16_f32 v85,v86,v87\nv_cvt_pk_bf16_f32 v86,v92,v93\n" \
"v_cvt_pk_bf16_f32 v87,v94,v95\nM v[4:7],a[64:67],v[68:71],0\nds_read_b128 a[0:3],v240 offset:32768\nds_read_b128 a[4:7],v240 offset:33792\nM v[4:7],a[96:99],v[84:87],v[4:7]\nds_read_b128 a[8:11],v240 offset:34816\nv_cndmask_b32 v96,v96,v252,s[82:83]\nv_cndmask_b32 v97,v97,v252,s[84:85]\n" \
"M v[12:15],a[68:71],v[68:71],0\nds_read_b128 a[12:15],v240 offset:35840\nv_cndmask_b32 v98,v98,v252,s[86:87]\nv_cndmask_b32 v99,v99,v252,s[88:89]\nM v[12:15],a[100:103],v[84:87],v[12:15]\nds_read_b128 a[16:19],v240 offset:36864\nv_exp_f32 v96,v96\nM v[20:23],a[72:75],v[68:71],0\n" \
"ds_read_b128 a[20:23],v240 offset:37888\nv_exp_f32 v97,v97\nM v[20:23],a[104:107],v[84:87],v[20:23]\nds_read_b128 a[24:27],v240 offset:38912\nv_exp_f32 v98,v98\nM v[28:31],a[76:79],v[68:71],0\nbuffer_load_dword v248,v251,s[52:55],s80 offen sc1\nds_read_b128 a[28:31],v240 offset:39936\n" \
"M v[28:31],a[108:111],v[84:87],v[28:31]\nds_read_b128 a[32:35],v240 offset:40960\nv_exp_f32 v99,v99\nM v[36:39],a[80:83],v[68:71],0\nds_read_b128 a[36:39],v240 offset:41984\nv_cvt_pk_bf16_f32 v96,v96,v97\nv_cvt_pk_bf16_f32 v97,v98,v99\nM v[36:39],a[112:115],v[84:87],v[36:39]\n" \
"ds_read_b128 a[40:43],v240 offset:43008\nv_mov_b32 v98,0\nv_mov_b32 v99,0\nM v[44:47],a[84:87],v[68:71],0\nds_read_b128 a[44:47],v240 offset:44032\nv_exp_f32 v100,v100\nM v[44:47],a[116:119],v[84:87],v[44:47]\nds_read_b128 a[48:51],v240 offset:45056\n" \
"v_exp_f32 v101,v101\nM v[52:55],a[88:91],v[68:71],0\nds_read_b128 a[52:55],v240 offset:46080\nv_cndmask_b32 v108,v108,v252,s[82:83]\nM v[52:55],a[120:123],v[84:87],v[52:55]\nds_read_b128 a[56:59],v240 offset:47104\nv_cndmask_b32 v109,v109,v252,s[84:85]\nv_cndmask_b32 v110,v110,v252,s[86:87]\n" \
"M v[60:63],a[92:95],v[68:71],0\nds_read_b128 a[60:63],v240 offset:48128\nv_cndmask_b32 v111,v111,v252,s[88:89]\nv_cvt_pk_bf16_f32 v100,v100,v101\nM v[60:63],a[124:127],v[84:87],v[60:63]\nds_read_b128 a[64:67],v241 offset:16384\nv_exp_f32 v102,v102\nM a[196:199],a[208:211],v[68:71],0\n" \
"ds_read_b128 a[68:71],v241 offset:17408\nv_exp_f32 v103,v103\nM a[196:199],a[208:211],v[84:87],a[196:199]\nds_read_b128 a[72:75],v241 offset:18432\nv_cvt_pk_bf16_f32 v101,v102,v103\ns_waitcnt lgkmcnt(8)\nM v[128:131],a[0:3],a[128:131],v[224:227]\nds_read_b128 a[76:79],v241 offset:19456\n" \
"v_exp_f32 v108,v108\nM v[128:131],a[4:7],a[132:135],v[128:131]\nds_read_b128 a[80:83],v241 offset:20480\nv_exp_f32 v109,v109\nM v[128:131],a[8:11],a[136:139],v[128:131]\nds_read_b128 a[84:87],v241 offset:21504\nv_exp_f32 v110,v110\nM v[128:131],a[12:15],a[140:143],v[128:131]\n" \
"ds_read_b128 a[88:91],v241 offset:22528\nv_exp_f32 v111,v111\nM v[132:135],a[12:15],a[156:159],v[228:231]\nds_read_b128 a[92:95],v241 offset:23552\nv_cvt_pk_bf16_f32 v102,v108,v109\nv_cvt_pk_bf16_f32 v103,v110,v111\nM v[132:135],a[8:11],a[152:155],v[132:135]\nds_read_b128 a[8:11],v240 offset:51200\n" \
"v_exp_f32 v128,v128\nM v[132:135],a[4:7],a[148:151],v[132:135]\nds_read_b128 a[4:7],v240 offset:50176\nv_exp_f32 v129,v129\nM v[132:135],a[0:3],a[144:147],v[132:135]\nds_read_b128 a[0:3],v240 offset:49152\nv_cvt_pk_bf16_f32 v192,v128,v129\nM v[144:147],a[16:19],a[128:131],v[224:227]\n" \
"ds_read_b128 a[12:15],v240 offset:52224\nv_exp_f32 v130,v130\nM v[144:147],a[20:23],a[132:135],v[144:147]\nv_exp_f32 v131,v131\nds_read_b128 a[96:99],v241 offset:40960\nM v[144:147],a[24:27],a[136:139],v[144:147]\nv_cvt_pk_bf16_f32 v193,v130,v131\nv_exp_f32 v132,v132\n" \
"M v[144:147],a[28:31],a[140:143],v[144:147]\nv_exp_f32 v133,v133\nds_read_b128 v[64:67],v250\nM v[148:151],a[28:31],a[156:159],v[228:231]\nds_read_b128 a[28:31],v240 offset:56320\nv_exp_f32 v134,v134\nM v[148:151],a[24:27],a[152:155],v[148:151]\nds_read_b128 a[24:27],v240 offset:55296\n" \
"v_exp_f32 v135,v135\nM v[148:151],a[20:23],a[148:151],v[148:151]\nds_read_b128 a[20:23],v240 offset:54272\nv_cvt_pk_bf16_f32 v196,v132,v133\nv_cvt_pk_bf16_f32 v197,v134,v135\nM v[148:151],a[16:19],a[144:147],v[148:151]\nds_read_b128 a[16:19],v240 offset:53248\nv_exp_f32 v144,v144\n" \
"M v[160:163],a[32:35],a[128:131],v[224:227]\nv_exp_f32 v145,v145\nds_read_b128 v[68:71],v250\nM v[160:163],a[36:39],a[132:135],v[160:163]\nv_exp_f32 v146,v146\nv_cvt_pk_bf16_f32 v194,v144,v145\nM v[160:163],a[40:43],a[136:139],v[160:163]\nv_exp_f32 v147,v147\n" \
"s_waitcnt lgkmcnt(6)\nM v[160:163],a[44:47],a[140:143],v[160:163]\nv_cvt_pk_bf16_f32 v195,v146,v147\nds_read_b128 v[72:75],v250\nM v[164:167],a[44:47],a[156:159],v[228:231]\nds_read_b128 a[44:47],v240 offset:60416\nv_exp_f32 v148,v148\nM v[164:167],a[40:43],a[152:155],v[164:167]\n" \
"ds_read_b128 a[40:43],v240 offset:59392\nv_exp_f32 v149,v149\nM v[164:167],a[36:39],a[148:151],v[164:167]\nds_read_b128 a[36:39],v240 offset:58368\nv_exp_f32 v150,v150\nM v[164:167],a[32:35],a[144:147],v[164:167]\nds_read_b128 a[32:35],v240 offset:57344\nv_exp_f32 v151,v151\n" \
"M v[176:179],a[48:51],a[128:131],v[224:227]\nv_cvt_pk_bf16_f32 v198,v148,v149\nv_cvt_pk_bf16_f32 v199,v150,v151\nds_read_b128 v[76:79],v250\nM v[176:179],a[52:55],a[132:135],v[176:179]\nv_exp_f32 v160,v160\nds_read_b128 v[80:83],v250\nM v[176:179],a[56:59],a[136:139],v[176:179]\n" \
"v_exp_f32 v161,v161\nds_read_b128 v[84:87],v250\nM v[176:179],a[60:63],a[140:143],v[176:179]\nv_exp_f32 v162,v162\nv_cvt_pk_bf16_f32 v208,v160,v161\nM v[180:183],a[60:63],a[156:159],v[228:231]\nv_exp_f32 v163,v163\nds_read_b128 a[60:63],v240 offset:64512\n" \
"M v[180:183],a[56:59],a[152:155],v[180:183]\nv_cvt_pk_bf16_f32 v209,v162,v163\nds_read_b128 a[56:59],v240 offset:63488\nds_read_b128 v[88:91],v250\nM v[180:183],a[52:55],a[148:151],v[180:183]\nds_read_b128 a[52:55],v240 offset:62464\nv_exp_f32 v164,v164\nM v[180:183],a[48:51],a[144:147],v[180:183]\n" \
"ds_read_b128 a[48:51],v240 offset:61440\nv_exp_f32 v165,v165\nM v[0:3],a[64:67],v[96:99],v[0:3]\nv_exp_f32 v166,v166\nM v[8:11],a[68:71],v[96:99],v[8:11]\nv_exp_f32 v167,v167\nv_cvt_pk_bf16_f32 v212,v164,v165\nM v[16:19],a[72:75],v[96:99],v[16:19]\n" \
"v_cvt_pk_bf16_f32 v213,v166,v167\nv_exp_f32 v176,v176\nM v[24:27],a[76:79],v[96:99],v[24:27]\nv_exp_f32 v177,v177\nds_read_b128 v[92:95],v250\nM v[32:35],a[80:83],v[96:99],v[32:35]\nv_exp_f32 v178,v178\nv_cvt_pk_bf16_f32 v210,v176,v177\n" \
"M v[40:43],a[84:87],v[96:99],v[40:43]\nv_exp_f32 v179,v179\nds_read_b128 v[104:107],v250\nM v[48:51],a[88:91],v[96:99],v[48:51]\ns_waitcnt vmcnt(0)\ns_barrier\nv_cvt_pk_bf16_f32 v211,v178,v179\nM v[56:59],a[92:95],v[96:99],v[56:59]\n" \
"v_exp_f32 v180,v180\nds_read_b128 v[108:111],v250\nM a[192:195],a[208:211],v[96:99],a[192:195]\nv_exp_f32 v181,v181\nds_read_b128 v[96:99],v250\nM v[4:7],a[64:67],v[100:103],v[4:7]\nv_exp_f32 v182,v182\nv_cvt_pk_bf16_f32 v214,v180,v181\n" \
"M v[12:15],a[68:71],v[100:103],v[12:15]\nv_exp_f32 v183,v183\nds_read_b128 a[64:67],v241 offset:32768\nM v[20:23],a[72:75],v[100:103],v[20:23]\nv_cvt_pk_bf16_f32 v215,v182,v183\nds_read_b128 a[68:71],v241 offset:33792\nds_read_b128 v[112:115],v250\nM v[28:31],a[76:79],v[100:103],v[28:31]\n" \
"ds_read_b128 v[116:119],v250\nv_cmp_eq_u32 vcc,0,v248\nM v[36:39],a[80:83],v[100:103],v[36:39]\nds_read_b128 v[120:123],v250\ns_cbranch_vccnz .Lps4\n.Lpb4:\ns_add_u32 m0,s64,65536\nM v[44:47],a[84:87],v[100:103],v[44:47]\n" \
"ds_read_b128 v[124:127],v250\nbuffer_load_dwordx4 v240,s[48:51],s36 offen offset:0 sc1 lds\nM v[52:55],a[88:91],v[100:103],v[52:55]\nds_read_b128 a[200:203],v250\nM v[60:63],a[92:95],v[100:103],v[60:63]\nds_read_b128 a[204:207],v250\nM a[196:199],a[208:211],v[100:103],a[196:199]\nds_read_b128 v[100:103],v250\n" \
"v_add_u32 v250,0xc000,v241\nM v[136:139],a[0:3],a[160:163],0\nds_read_b128 a[100:103],v241 offset:41984\nbuffer_load_dwordx4 v240,s[48:51],s36 offen offset:1024 sc1 lds\nM v[136:139],a[4:7],a[164:167],v[136:139]\nds_read_b128 a[72:75],v241 offset:34816\nM v[136:139],a[8:11],a[168:171],v[136:139]\nds_read_b128 a[104:107],v241 offset:43008\n" \
"M v[136:139],a[12:15],a[172:175],v[136:139]\nds_read_b128 a[76:79],v241 offset:35840\nM v[140:143],a[12:15],a[188:191],0\nds_read_b128 a[108:111],v241 offset:44032\nbuffer_load_dwordx4 v240,s[48:51],s36 offen offset:2048 sc1 lds\nM v[140:143],a[8:11],a[184:187],v[140:143]\nds_read_b128 a[80:83],v241 offset:36864\nM v[140:143],a[4:7],a[180:183],v[140:143]\n" \
"ds_read_b128 a[112:115],v241 offset:45056\nv_max3_f32 v233,v136,v137,v138\nM v[140:143],a[0:3],a[176:179],v[140:143]\nds_read_b128 a[84:87],v241 offset:37888\ns_waitcnt lgkmcnt(11)\nM v[152:155],a[16:19],a[160:163],0\nds_read_b128 a[116:119],v241 offset:46080\nbuffer_load_dwordx4 v240,s[48:51],s36 offen offset:3072 sc1 lds\n" \
"M v[152:155],a[20:23],a[164:167],v[152:155]\nds_read_b128 a[88:91],v241 offset:38912\ns_add_u32 m0,s64,16384\nv_max3_f32 v237,v140,v141,v142\nM v[152:155],a[24:27],a[168:171],v[152:155]\nds_read_b128 a[120:123],v241 offset:47104\nM v[152:155],a[28:31],a[172:175],v[152:155]\nds_read_b128 a[92:95],v241 offset:39936\n" \
"M v[156:159],a[28:31],a[188:191],0\nds_read_b128 a[124:127],v241 offset:48128\nbuffer_load_dwordx4 v240,s[44:47],s65 offen offset:0 sc1 lds\nM v[156:159],a[24:27],a[184:187],v[156:159]\nM v[156:159],a[20:23],a[180:183],v[156:159]\nM v[156:159],a[16:19],a[176:179],v[156:159]\nM v[128:131],a[0:3],a[128:131],v[224:227]\nv_max3_f32 v233,v233,v139,v152\n" \
"v_max3_f32 v233,v233,v153,v154\nv_max_f32 v233,v233,v155\nM v[128:131],a[4:7],a[132:135],v[128:131]\nv_cndmask_b32 v233,v232,v233,s[90:91]\nbuffer_load_dwordx4 v240,s[44:47],s65 offen offset:1024 sc1 lds\nM v[128:131],a[8:11],a[136:139],v[128:131]\nv_max3_f32 v237,v237,v143,v156\nv_max3_f32 v237,v237,v157,v158\n" \
"v_max_f32 v237,v237,v159\nM v[128:131],a[12:15],a[140:143],v[128:131]\nv_cndmask_b32 v237,v236,v237,s[90:91]\nM v[132:135],a[12:15],a[156:159],v[228:231]\nM v[132:135],a[8:11],a[152:155],v[132:135]\nv_permlane16_swap_b32 v233,v237\nv_max_f32 v233,v233,v237\nv_mov_b32 v237,v233\n" \
"M v[132:135],a[4:7],a[148:151],v[132:135]\nbuffer_load_dwordx4 v240,s[44:47],s65 offen offset:2048 sc1 lds\nv_permlane32_swap_b32 v233,v237\nM v[132:135],a[0:3],a[144:147],v[132:135]\nv_max_f32 v233,v233,v237\nv_mov_b32 v237,v233\nds_read_b128 a[0:3],v240 offset:0\nM v[144:147],a[16:19],a[128:131],v[224:227]\n" \
"v_permlane16_swap_b32 v233,v237\nv_sub_f32 v232,0,v233\nv_sub_f32 v234,0,v233\nM v[144:147],a[20:23],a[132:135],v[144:147]\nv_sub_f32 v235,0,v233\nv_sub_f32 v233,0,v233\nv_sub_f32 v236,0,v237\nM v[144:147],a[24:27],a[136:139],v[144:147]\n" \
"v_sub_f32 v238,0,v237\nv_sub_f32 v239,0,v237\nv_sub_f32 v237,0,v237\nM v[144:147],a[28:31],a[140:143],v[144:147]\nds_read_b128 a[4:7],v240 offset:1024\nds_read_b128 a[8:11],v240 offset:2048\nv_add_f32 v136,v136,v232\nM v[148:151],a[28:31],a[156:159],v[228:231]\n" \
"ds_read_b128 a[12:15],v240 offset:3072\nbuffer_load_dwordx4 v240,s[44:47],s65 offen offset:3072 sc1 lds\nM v[148:151],a[24:27],a[152:155],v[148:151]\ns_add_u32 m0,s64,81920\nv_add_f32 v137,v137,v232\nv_add_f32 v138,v138,v232\nM v[148:151],a[20:23],a[148:151],v[148:151]\nv_add_f32 v139,v139,v232\n" \
"v_add_f32 v152,v152,v232\nv_add_f32 v153,v153,v232\nM v[148:151],a[16:19],a[144:147],v[148:151]\nds_read_b128 a[16:19],v240 offset:4096\nds_read_b128 a[20:23],v240 offset:5120\nv_add_f32 v154,v154,v232\nM v[160:163],a[32:35],a[128:131],v[224:227]\nds_read_b128 a[24:27],v240 offset:6144\n" \
"v_add_f32 v155,v155,v232\nM v[160:163],a[36:39],a[132:135],v[160:163]\nds_read_b128 a[28:31],v240 offset:7168\nbuffer_load_dwordx4 v240,s[48:51],s65 offen offset:0 sc1 lds\nM v[160:163],a[40:43],a[136:139],v[160:163]\nv_exp_f32 v136,v136\nv_add_f32 v140,v140,v236\nM v[160:163],a[44:47],a[140:143],v[160:163]\n" \
"v_exp_f32 v137,v137\nv_add_f32 v141,v141,v236\nM v[164:167],a[44:47],a[156:159],v[228:231]\nv_exp_f32 v138,v138\nv_add_f32 v142,v142,v236\nM v[164:167],a[40:43],a[152:155],v[164:167]\nbuffer_load_dwordx4 v240,s[48:51],s65 offen offset:1024 sc1 lds\nv_add_f32 v143,v143,v236\n" \
"M v[164:167],a[36:39],a[148:151],v[164:167]\nv_exp_f32 v139,v139\nv_add_f32 v156,v156,v236\nM v[164:167],a[32:35],a[144:147],v[164:167]\nv_exp_f32 v152,v152\nv_add_f32 v157,v157,v236\nM v[176:179],a[48:51],a[128:131],v[224:227]\nv_exp_f32 v153,v153\n" \
"v_add_f32 v158,v158,v236\nM v[176:179],a[52:55],a[132:135],v[176:179]\nbuffer_load_dwordx4 v240,s[48:51],s65 offen offset:2048 sc1 lds\nv_add_f32 v159,v159,v236\nM v[176:179],a[56:59],a[136:139],v[176:179]\nv_exp_f32 v154,v154\nv_cvt_pk_bf16_f32 v200,v136,v137\nM v[176:179],a[60:63],a[140:143],v[176:179]\n" \
"v_exp_f32 v155,v155\nv_cvt_pk_bf16_f32 v201,v138,v139\nM v[180:183],a[60:63],a[156:159],v[228:231]\nv_exp_f32 v128,v128\nM v[180:183],a[56:59],a[152:155],v[180:183]\nbuffer_load_dwordx4 v240,s[48:51],s65 offen offset:3072 sc1 lds\ns_add_u32 m0,s64,32768\nM v[180:183],a[52:55],a[148:151],v[180:183]\n" \
"v_exp_f32 v129,v129\nv_cvt_pk_bf16_f32 v202,v152,v153\nM v[180:183],a[48:51],a[144:147],v[180:183]\nv_exp_f32 v130,v130\nv_cvt_pk_bf16_f32 v203,v154,v155\nM v[168:171],a[32:35],a[160:163],v[232:235]\nv_exp_f32 v131,v131\nM v[168:171],a[36:39],a[164:167],v[168:171]\n" \
"buffer_load_dwordx4 v240,s[44:47],s66 offen offset:0 sc1 lds\nM v[168:171],a[40:43],a[168:171],v[168:171]\nv_exp_f32 v132,v132\nM v[168:171],a[44:47],a[172:175],v[168:171]\nv_exp_f32 v133,v133\nM v[172:175],a[44:47],a[188:191],v[236:239]\nv_exp_f32 v134,v134\nM v[172:175],a[40:43],a[184:187],v[172:175]\n" \
"buffer_load_dwordx4 v240,s[44:47],s66 offen offset:1024 sc1 lds\nM v[172:175],a[36:39],a[180:183],v[172:175]\ns_waitcnt lgkmcnt(0)\ns_barrier\nv_cndmask_b32 v168,v168,v252,s[82:83]\nM v[172:175],a[32:35],a[176:179],v[172:175]\nds_read_b128 a[32:35],v240 offset:8192\nds_read_b128 a[36:39],v240 offset:9216\n" \
"v_cndmask_b32 v169,v169,v252,s[84:85]\nM v[188:191],a[60:63],a[188:191],v[236:239]\nds_read_b128 a[40:43],v240 offset:10240\nv_exp_f32 v135,v135\nM v[188:191],a[56:59],a[184:187],v[188:191]\nds_read_b128 a[44:47],v240 offset:11264\nv_cndmask_b32 v170,v170,v252,s[86:87]\nM v[188:191],a[52:55],a[180:183],v[188:191]\n" \
"buffer_load_dwordx4 v240,s[44:47],s66 offen offset:2048 sc1 lds\nv_cndmask_b32 v171,v171,v252,s[88:89]\nM v[188:191],a[48:51],a[176:179],v[188:191]\nds_read_b128 a[48:51],v240 offset:12288\nv_exp_f32 v144,v144\nM v[0:3],a[64:67],v[192:195],v[0:3]\nds_read_b128 a[52:55],v240 offset:13312\nv_exp_f32 v145,v145\n" \
"M v[0:3],a[96:99],v[208:211],v[0:3]\nds_read_b128 a[56:59],v240 offset:14336\nv_exp_f32 v146,v146\nM v[8:11],a[68:71],v[192:195],v[8:11]\nds_read_b128 a[60:63],v240 offset:15360\nbuffer_load_dwordx4 v240,s[44:47],s66 offen offset:3072 sc1 lds\nM v[8:11],a[100:103],v[208:211],v[8:11]\nv_exp_f32 v147,v147\n" \
"s_add_u32 m0,s64,98304\nM v[16:19],a[72:75],v[192:195],v[16:19]\nv_exp_f32 v148,v148\nv_cndmask_b32 v188,v188,v252,s[82:83]\nM v[16:19],a[104:107],v[208:211],v[16:19]\nv_exp_f32 v149,v149\nv_cndmask_b32 v189,v189,v252,s[84:85]\nM v[24:27],a[76:79],v[192:195],v[24:27]\n" \
"v_exp_f32 v150,v150\nv_cndmask_b32 v190,v190,v252,s[86:87]\nM v[24:27],a[108:111],v[208:211],v[24:27]\nv_exp_f32 v151,v151\nv_cndmask_b32 v191,v191,v252,s[88:89]\nM v[32:35],a[80:83],v[192:195],v[32:35]\nv_exp_f32 v160,v160\nM v[32:35],a[112:115],v[208:211],v[32:35]\n" \
"v_exp_f32 v161,v161\nM v[40:43],a[84:87],v[192:195],v[40:43]\nv_exp_f32 v162,v162\nM v[40:43],a[116:119],v[208:211],v[40:43]\nv_exp_f32 v163,v163\nM v[48:51],a[88:91],v[192:195],v[48:51]\nv_exp_f32 v176,v176\nM v[48:51],a[120:123],v[208:211],v[48:51]\n" \
"v_exp_f32 v177,v177\nM v[56:59],a[92:95],v[192:195],v[56:59]\nv_exp_f32 v178,v178\nM v[56:59],a[124:127],v[208:211],v[56:59]\nv_exp_f32 v179,v179\nM a[192:195],a[208:211],v[192:195],a[192:195]\nv_exp_f32 v140,v140\nv_cvt_pk_bf16_f32 v192,v128,v129\n" \
"M a[192:195],a[208:211],v[208:211],a[192:195]\nv_exp_f32 v141,v141\nv_cvt_pk_bf16_f32 v193,v130,v131\nM v[4:7],a[64:67],v[196:199],v[4:7]\nv_exp_f32 v142,v142\nv_cvt_pk_bf16_f32 v194,v144,v145\nM v[4:7],a[96:99],v[212:215],v[4:7]\nv_exp_f32 v143,v143\n" \
"v_cvt_pk_bf16_f32 v195,v146,v147\nM v[12:15],a[68:71],v[196:199],v[12:15]\nv_exp_f32 v156,v156\nv_cvt_pk_bf16_f32 v208,v160,v161\nM v[12:15],a[100:103],v[212:215],v[12:15]\nv_exp_f32 v157,v157\nv_cvt_pk_bf16_f32 v209,v162,v163\nM v[20:23],a[72:75],v[196:199],v[20:23]\n" \
"v_exp_f32 v158,v158\nM v[20:23],a[104:107],v[212:215],v[20:23]\nv_exp_f32 v159,v159\nv_cvt_pk_bf16_f32 v210,v176,v177\nM v[28:31],a[76:79],v[196:199],v[28:31]\nv_exp_f32 v164,v164\nv_cvt_pk_bf16_f32 v211,v178,v179\nM v[28:31],a[108:111],v[212:215],v[28:31]\n" \
"v_exp_f32 v165,v165\nM v[36:39],a[80:83],v[196:199],v[36:39]\nv_exp_f32 v166,v166\nM v[36:39],a[112:115],v[212:215],v[36:39]\nv_exp_f32 v167,v167\nM v[44:47],a[84:87],v[196:199],v[44:47]\nv_exp_f32 v180,v180\nM v[44:47],a[116:119],v[212:215],v[44:47]\n" \
"v_exp_f32 v181,v181\nM v[52:55],a[88:91],v[196:199],v[52:55]\nv_exp_f32 v182,v182\nM v[52:55],a[120:123],v[212:215],v[52:55]\nv_exp_f32 v183,v183\nM v[60:63],a[92:95],v[196:199],v[60:63]\nv_exp_f32 v168,v168\nM v[60:63],a[124:127],v[212:215],v[60:63]\n" \
"v_exp_f32 v169,v169\nM a[196:199],a[208:211],v[196:199],a[196:199]\nv_exp_f32 v170,v170\nv_exp_f32 v171,v171\nv_cvt_pk_bf16_f32 v196,v132,v133\nv_cvt_pk_bf16_f32 v197,v134,v135\nv_cvt_pk_bf16_f32 v198,v148,v149\nv_cvt_pk_bf16_f32 v199,v150,v151\n" \
"buffer_load_dwordx4 v240,s[48:51],s66 offen offset:0 sc1 lds\nbuffer_load_dwordx4 v240,s[48:51],s66 offen offset:1024 sc1 lds\nbuffer_load_dwordx4 v240,s[48:51],s66 offen offset:2048 sc1 lds\nbuffer_load_dwordx4 v240,s[48:51],s66 offen offset:3072 sc1 lds\nM a[196:199],a[208:211],v[212:215],a[196:199]\ns_cmp_lt_u32 s62,2\ns_cbranch_scc1 .Lx01\ns_branch .Ldual\n" \
".Lpro3:\ns_barrier\nM v[64:67],a[0:3],a[128:131],0\nds_read_b128 a[64:67],v241 offset:0\nds_read_b128 a[96:99],v241 offset:8192\nM v[64:67],a[4:7],a[132:135],v[64:67]\nds_read_b128 a[68:71],v241 offset:1024\ns_mov_b32 m0,s64\n" \
"v_mov_b32 v250,143360\nM v[64:67],a[8:11],a[136:139],v[64:67]\nds_read_b128 a[100:103],v241 offset:9216\nbuffer_load_dwordx4 v240,s[44:47],s36 offen offset:0 sc1 lds\nM v[64:67],a[12:15],a[140:143],v[64:67]\nds_read_b128 a[72:75],v241 offset:2048\nv_mov_b32 v168,0\nv_mov_b32 v169,0\n" \
"M v[68:71],a[12:15],a[156:159],0\nds_read_b128 a[12:15],v240 offset:19456\nv_mov_b32 v170,0\nv_mov_b32 v171,0\nM v[68:71],a[8:11],a[152:155],v[68:71]\nds_read_b128 a[8:11],v240 offset:18432\nv_max3_f32 v225,v64,v65,v66\nv_mov_b32 v172,0xff800000\n" \
"M v[68:71],a[4:7],a[148:151],v[68:71]\nds_read_b128 a[4:7],v240 offset:17408\nbuffer_load_dwordx4 v240,s[44:47],s36 offen offset:1024 sc1 lds\nM v[68:71],a[0:3],a[144:147],v[68:71]\nds_read_b128 a[0:3],v240 offset:16384\nv_mov_b32 v173,0xff800000\nv_mov_b32 v174,0xff800000\nM v[72:75],a[16:19],a[128:131],0\n" \
"ds_read_b128 a[104:107],v241 offset:10240\nv_mov_b32 v175,0xff800000\nv_mov_b32 v184,0xff800000\nM v[72:75],a[20:23],a[132:135],v[72:75]\nv_max3_f32 v229,v68,v69,v70\nds_read_b128 a[76:79],v241 offset:3072\nv_mov_b32 v185,0xff800000\nM v[72:75],a[24:27],a[136:139],v[72:75]\n" \
"ds_read_b128 a[108:111],v241 offset:11264\nv_mov_b32 v186,0xff800000\nM v[72:75],a[28:31],a[140:143],v[72:75]\nds_read_b128 a[80:83],v241 offset:4096\nbuffer_load_dwordx4 v240,s[44:47],s36 offen offset:2048 sc1 lds\nM v[76:79],a[28:31],a[156:159],0\nds_read_b128 a[28:31],v240 offset:23552\nv_mov_b32 v187,0xff800000\n" \
"v_mov_b32 v188,0xff800000\nM v[76:79],a[24:27],a[152:155],v[76:79]\nds_read_b128 a[24:27],v240 offset:22528\nv_max3_f32 v225,v225,v67,v72\nv_max3_f32 v225,v225,v73,v74\nM v[76:79],a[20:23],a[148:151],v[76:79]\nv_max_f32 v225,v225,v75\nv_cndmask_b32 v225,v224,v225,s[90:91]\n" \
"ds_read_b128 a[20:23],v240 offset:21504\nM v[76:79],a[16:19],a[144:147],v[76:79]\nds_read_b128 a[16:19],v240 offset:20480\nbuffer_load_dwordx4 v240,s[44:47],s36 offen offset:3072 sc1 lds\nM v[80:83],a[32:35],a[128:131],0\nds_read_b128 a[112:115],v241 offset:12288\nv_mov_b32 v189,0xff800000\nv_mov_b32 v190,0xff800000\n" \
"M v[80:83],a[36:39],a[132:135],v[80:83]\nds_read_b128 a[84:87],v241 offset:5120\nv_max3_f32 v229,v229,v71,v76\nv_max3_f32 v229,v229,v77,v78\nM v[80:83],a[40:43],a[136:139],v[80:83]\nv_max_f32 v229,v229,v79\nv_cndmask_b32 v229,v228,v229,s[90:91]\nds_read_b128 a[116:119],v241 offset:13312\n" \
"M v[80:83],a[44:47],a[140:143],v[80:83]\nv_permlane16_swap_b32 v225,v229\nv_max_f32 v225,v225,v229\nv_mov_b32 v229,v225\nM v[84:87],a[44:47],a[156:159],0\nds_read_b128 a[44:47],v240 offset:27648\nv_permlane32_swap_b32 v225,v229\nv_max_f32 v225,v225,v229\n" \
"M v[84:87],a[40:43],a[152:155],v[84:87]\nv_mov_b32 v229,v225\nds_read_b128 a[40:43],v240 offset:26624\nM v[84:87],a[36:39],a[148:151],v[84:87]\nv_permlane16_swap_b32 v225,v229\nv_sub_f32 v224,0,v225\nv_sub_f32 v226,0,v225\nM v[84:87],a[32:35],a[144:147],v[84:87]\n" \
"v_sub_f32 v227,0,v225\nv_sub_f32 v225,0,v225\nv_sub_f32 v228,0,v229\nM v[88:91],a[48:51],a[128:131],0\nv_sub_f32 v230,0,v229\nv_sub_f32 v231,0,v229\nv_sub_f32 v229,0,v229\nM v[88:91],a[52:55],a[132:135],v[88:91]\n" \
"ds_read_b128 a[32:35],v240 offset:24576\nds_read_b128 a[36:39],v240 offset:25600\nv_add_f32 v64,v64,v224\nM v[88:91],a[56:59],a[136:139],v[88:91]\nv_add_f32 v65,v65,v224\nv_add_f32 v66,v66,v224\nv_add_f32 v67,v67,v224\nM v[88:91],a[60:63],a[140:143],v[88:91]\n" \
"v_exp_f32 v64,v64\nv_add_f32 v72,v72,v224\nM v[92:95],a[60:63],a[156:159],0\nds_read_b128 a[60:63],v240 offset:31744\nv_exp_f32 v65,v65\nM v[92:95],a[56:59],a[152:155],v[92:95]\nds_read_b128 a[56:59],v240 offset:30720\nv_exp_f32 v66,v66\n" \
"M v[92:95],a[52:55],a[148:151],v[92:95]\nds_read_b128 a[52:55],v240 offset:29696\nv_exp_f32 v67,v67\nM v[92:95],a[48:51],a[144:147],v[92:95]\nds_read_b128 a[48:51],v240 offset:28672\nv_add_f32 v73,v73,v224\ns_waitcnt lgkmcnt(6)\nM v[96:99],a[0:3],a[128:131],v[224:227]\n" \
"v_add_f32 v74,v74,v224\nv_add_f32 v75,v75,v224\nM v[96:99],a[4:7],a[132:135],v[96:99]\nv_add_f32 v80,v80,v224\nv_add_f32 v81,v81,v224\nv_add_f32 v82,v82,v224\nM v[96:99],a[8:11],a[136:139],v[96:99]\nv_exp_f32 v72,v72\n" \
"v_add_f32 v83,v83,v224\nM v[96:99],a[12:15],a[140:143],v[96:99]\nv_exp_f32 v73,v73\nv_cvt_pk_bf16_f32 v64,v64,v65\nM v[100:103],a[12:15],a[156:159],v[228:231]\nv_exp_f32 v74,v74\nv_add_f32 v88,v88,v224\nM v[100:103],a[8:11],a[152:155],v[100:103]\n" \
"v_exp_f32 v75,v75\nv_add_f32 v89,v89,v224\nM v[100:103],a[4:7],a[148:151],v[100:103]\nv_exp_f32 v80,v80\nv_add_f32 v90,v90,v224\nM v[100:103],a[0:3],a[144:147],v[100:103]\nv_exp_f32 v81,v81\nv_add_f32 v91,v91,v224\n" \
"M v[104:107],a[16:19],a[128:131],v[224:227]\nv_exp_f32 v82,v82\nv_cvt_pk_bf16_f32 v65,v66,v67\nM v[104:107],a[20:23],a[132:135],v[104:107]\nv_exp_f32 v83,v83\nv_cvt_pk_bf16_f32 v66,v72,v73\nM v[104:107],a[24:27],a[136:139],v[104:107]\nv_exp_f32 v88,v88\n" \
"v_cvt_pk_bf16_f32 v67,v74,v75\nM v[104:107],a[28:31],a[140:143],v[104:107]\nv_exp_f32 v89,v89\nM v[108:111],a[28:31],a[156:159],v[228:231]\nv_exp_f32 v90,v90\nv_cvt_pk_bf16_f32 v80,v80,v81\nM v[108:111],a[24:27],a[152:155],v[108:111]\nv_exp_f32 v91,v91\n" \
"v_cvt_pk_bf16_f32 v81,v82,v83\nM v[108:111],a[20:23],a[148:151],v[108:111]\nv_cvt_pk_bf16_f32 v82,v88,v89\nv_cvt_pk_bf16_f32 v83,v90,v91\nds_read_b128 a[88:91],v241 offset:6144\nM v[108:111],a[16:19],a[144:147],v[108:111]\nds_read_b128 a[120:123],v241 offset:14336\nds_read_b128 a[92:95],v241 offset:7168\n" \
"s_waitcnt lgkmcnt(3)\nM v[112:115],a[32:35],a[128:131],v[224:227]\nds_read_b128 a[124:127],v241 offset:15360\nv_add_f32 v68,v68,v228\nv_add_f32 v69,v69,v228\nM v[112:115],a[36:39],a[132:135],v[112:115]\nv_add_f32 v70,v70,v228\nv_add_f32 v71,v71,v228\n" \
"v_add_f32 v76,v76,v228\nM v[112:115],a[40:43],a[136:139],v[112:115]\nv_exp_f32 v68,v68\nv_add_f32 v77,v77,v228\nM v[112:115],a[44:47],a[140:143],v[112:115]\nv_exp_f32 v69,v69\nv_add_f32 v78,v78,v228\nM v[116:119],a[44:47],a[156:159],v[228:231]\n" \
"v_exp_f32 v70,v70\nv_add_f32 v79,v79,v228\nM v[116:119],a[40:43],a[152:155],v[116:119]\nv_exp_f32 v71,v71\nv_add_f32 v84,v84,v228\nM v[116:119],a[36:39],a[148:151],v[116:119]\nv_add_f32 v85,v85,v228\nv_add_f32 v86,v86,v228\n" \
"M v[116:119],a[32:35],a[144:147],v[116:119]\nv_exp_f32 v76,v76\nv_add_f32 v87,v87,v228\nM v[124:127],a[60:63],a[156:159],v[228:231]\nv_exp_f32 v77,v77\nv_cvt_pk_bf16_f32 v68,v68,v69\nM v[124:127],a[56:59],a[152:155],v[124:127]\nv_exp_f32 v78,v78\n" \
"v_add_f32 v92,v92,v228\nM v[124:127],a[52:55],a[148:151],v[124:127]\nv_exp_f32 v79,v79\nv_add_f32 v93,v93,v228\nM v[124:127],a[48:51],a[144:147],v[124:127]\nv_exp_f32 v84,v84\nv_add_f32 v94,v94,v228\nM v[0:3],a[64:67],v[64:67],0\n" \
"v_exp_f32 v85,v85\nv_add_f32 v95,v95,v228\nM v[0:3],a[96:99],v[80:83],v[0:3]\nv_exp_f32 v86,v86\nv_cvt_pk_bf16_f32 v69,v70,v71\nM v[8:11],a[68:71],v[64:67],0\nv_exp_f32 v87,v87\nv_cvt_pk_bf16_f32 v70,v76,v77\n" \
"M v[8:11],a[100:103],v[80:83],v[8:11]\nv_exp_f32 v92,v92\nv_cvt_pk_bf16_f32 v71,v78,v79\nM v[16:19],a[72:75],v[64:67],0\nv_exp_f32 v93,v93\nv_cvt_pk_bf16_f32 v84,v84,v85\nM v[16:19],a[104:107],v[80:83],v[16:19]\nv_exp_f32 v94,v94\n" \
"M v[24:27],a[76:79],v[64:67],0\nv_exp_f32 v95,v95\nv_cvt_pk_bf16_f32 v85,v86,v87\nM v[24:27],a[108:111],v[80:83],v[24:27]\nv_cvt_pk_bf16_f32 v86,v92,v93\nv_cvt_pk_bf16_f32 v87,v94,v95\nds_read_b128 a[0:3],v240 offset:32768\nM v[32:35],a[80:83],v[64:67],0\n" \
"ds_read_b128 a[4:7],v240 offset:33792\nds_read_b128 a[8:11],v240 offset:34816\nv_cndmask_b32 v112,v112,v252,s[82:83]\nM v[32:35],a[112:115],v[80:83],v[32:35]\nds_read_b128 a[12:15],v240 offset:35840\nv_exp_f32 v96,v96\nM v[40:43],a[84:87],v[64:67],0\nds_read_b128 a[16:19],v240 offset:36864\n" \
"v_exp_f32 v97,v97\nM v[40:43],a[116:119],v[80:83],v[40:43]\nds_read_b128 a[20:23],v240 offset:37888\nv_cndmask_b32 v113,v113,v252,s[84:85]\ns_waitcnt lgkmcnt(6)\nM v[48:51],a[88:91],v[64:67],0\nds_read_b128 a[24:27],v240 offset:38912\nv_exp_f32 v98,v98\n" \
"M v[48:51],a[120:123],v[80:83],v[48:51]\nds_read_b128 a[28:31],v240 offset:39936\nv_exp_f32 v99,v99\nM v[56:59],a[92:95],v[64:67],0\nbuffer_load_dword v248,v251,s[52:55],s80 offen sc1\nds_read_b128 a[32:35],v240 offset:40960\nM v[56:59],a[124:127],v[80:83],v[56:59]\nds_read_b128 a[36:39],v240 offset:41984\n" \
"v_exp_f32 v104,v104\nM a[192:195],a[208:211],v[64:67],0\nds_read_b128 a[40:43],v240 offset:43008\nv_cndmask_b32 v114,v114,v252,s[86:87]\nM a[192:195],a[208:211],v[80:83],a[192:195]\nds_read_b128 a[44:47],v240 offset:44032\nv_exp_f32 v105,v105\nM v[4:7],a[64:67],v[68:71],0\n" \
"ds_read_b128 a[48:51],v240 offset:45056\nv_exp_f32 v106,v106\nM v[4:7],a[96:99],v[84:87],v[4:7]\nds_read_b128 a[52:55],v240 offset:46080\nv_exp_f32 v107,v107\nM v[12:15],a[68:71],v[68:71],0\nds_read_b128 a[56:59],v240 offset:47104\nv_cndmask_b32 v115,v115,v252,s[88:89]\n" \
"v_cvt_pk_bf16_f32 v96,v96,v97\nM v[12:15],a[100:103],v[84:87],v[12:15]\nds_read_b128 a[60:63],v240 offset:48128\nv_exp_f32 v112,v112\nM v[20:23],a[72:75],v[68:71],0\nds_read_b128 a[64:67],v241 offset:16384\nv_exp_f32 v113,v113\nM v[20:23],a[104:107],v[84:87],v[20:23]\n" \
"ds_read_b128 a[96:99],v241 offset:24576\nv_exp_f32 v114,v114\nM v[28:31],a[76:79],v[68:71],0\nds_read_b128 a[68:71],v241 offset:17408\nv_exp_f32 v115,v115\nM v[28:31],a[108:111],v[84:87],v[28:31]\nds_read_b128 a[100:103],v241 offset:25600\nv_cvt_pk_bf16_f32 v97,v98,v99\n" \
"v_cvt_pk_bf16_f32 v98,v104,v105\nM v[36:39],a[80:83],v[68:71],0\nds_read_b128 a[72:75],v241 offset:18432\nv_cvt_pk_bf16_f32 v99,v106,v107\nM v[36:39],a[112:115],v[84:87],v[36:39]\nds_read_b128 a[104:107],v241 offset:26624\nv_cvt_pk_bf16_f32 v112,v112,v113\nv_cvt_pk_bf16_f32 v113,v114,v115\n" \
"M v[44:47],a[84:87],v[68:71],0\nds_read_b128 a[76:79],v241 offset:19456\nv_mov_b32 v114,0\nv_mov_b32 v115,0\nM v[44:47],a[116:119],v[84:87],v[44:47]\nds_read_b128 a[108:111],v241 offset:27648\nv_exp_f32 v100,v100\nM v[52:55],a[88:91],v[68:71],0\n" \
"ds_read_b128 a[80:83],v241 offset:20480\nv_exp_f32 v101,v101\nM v[52:55],a[120:123],v[84:87],v[52:55]\nds_read_b128 a[112:115],v241 offset:28672\nv_exp_f32 v102,v102\nM v[60:63],a[92:95],v[68:71],0\nds_read_b128 a[84:87],v241 offset:21504\nv_exp_f32 v103,v103\n" \
"M v[60:63],a[124:127],v[84:87],v[60:63]\nds_read_b128 a[116:119],v241 offset:29696\nv_exp_f32 v108,v108\nM a[196:199],a[208:211],v[68:71],0\nds_read_b128 a[88:91],v241 offset:22528\nv_exp_f32 v109,v109\nM a[196:199],a[208:211],v[84:87],a[196:199]\nds_read_b128 a[120:123],v241 offset:30720\n" \
"v_cvt_pk_bf16_f32 v100,v100,v101\ns_waitcnt lgkmcnt(8)\nM v[128:131],a[0:3],a[128:131],v[224:227]\nds_read_b128 a[92:95],v241 offset:23552\nv_exp_f32 v110,v110\nM v[128:131],a[4:7],a[132:135],v[128:131]\nds_read_b128 a[124:127],v241 offset:31744\nv_cndmask_b32 v124,v124,v252,s[82:83]\n" \
"M v[128:131],a[8:11],a[136:139],v[128:131]\nv_exp_f32 v111,v111\nv_cndmask_b32 v125,v125,v252,s[84:85]\nM v[128:131],a[12:15],a[140:143],v[128:131]\nv_exp_f32 v116,v116\nv_cndmask_b32 v126,v126,v252,s[86:87]\nM v[132:135],a[12:15],a[156:159],v[228:231]\nv_exp_f32 v117,v117\n" \
"v_cndmask_b32 v127,v127,v252,s[88:89]\nM v[132:135],a[8:11],a[152:155],v[132:135]\nv_exp_f32 v118,v118\nv_cvt_pk_bf16_f32 v101,v102,v103\nM v[132:135],a[4:7],a[148:151],v[132:135]\nv_exp_f32 v119,v119\nv_cvt_pk_bf16_f32 v102,v108,v109\nM v[132:135],a[0:3],a[144:147],v[132:135]\n" \
"v_exp_f32 v124,v124\nv_cvt_pk_bf16_f32 v103,v110,v111\nM v[144:147],a[16:19],a[128:131],v[224:227]\nv_exp_f32 v125,v125\nv_cvt_pk_bf16_f32 v116,v116,v117\nM v[144:147],a[20:23],a[132:135],v[144:147]\nv_exp_f32 v126,v126\nv_cvt_pk_bf16_f32 v117,v118,v119\n" \
"M v[144:147],a[24:27],a[136:139],v[144:147]\nv_exp_f32 v127,v127\nv_cvt_pk_bf16_f32 v118,v124,v125\nM v[144:147],a[28:31],a[140:143],v[144:147]\nv_cvt_pk_bf16_f32 v119,v126,v127\nds_read_b128 a[0:3],v240 offset:49152\nds_read_b128 a[4:7],v240 offset:50176\nM v[148:151],a[28:31],a[156:159],v[228:231]\n" \
"ds_read_b128 a[8:11],v240 offset:51200\nv_mov_b32 v191,0xff800000\nM v[148:151],a[24:27],a[152:155],v[148:151]\nds_read_b128 a[12:15],v240 offset:52224\nv_exp_f32 v128,v128\nM v[148:151],a[20:23],a[148:151],v[148:151]\nds_read_b128 a[28:31],v240 offset:56320\nv_exp_f32 v129,v129\n" \
"M v[148:151],a[16:19],a[144:147],v[148:151]\nds_read_b128 a[24:27],v240 offset:55296\nv_exp_f32 v130,v130\nM v[160:163],a[32:35],a[128:131],v[224:227]\nds_read_b128 a[20:23],v240 offset:54272\nv_exp_f32 v131,v131\nM v[160:163],a[36:39],a[132:135],v[160:163]\nds_read_b128 a[16:19],v240 offset:53248\n" \
"v_cvt_pk_bf16_f32 v192,v128,v129\nv_cvt_pk_bf16_f32 v193,v130,v131\nM v[160:163],a[40:43],a[136:139],v[160:163]\nv_exp_f32 v132,v132\nv_mov_b32 v202,0\nM v[160:163],a[44:47],a[140:143],v[160:163]\nv_exp_f32 v133,v133\nv_mov_b32 v203,0\n" \
"M v[164:167],a[44:47],a[156:159],v[228:231]\nv_exp_f32 v134,v134\nv_cvt_pk_bf16_f32 v196,v132,v133\nM v[164:167],a[40:43],a[152:155],v[164:167]\nv_exp_f32 v135,v135\nds_read_b128 a[40:43],v240 offset:59392\nM v[164:167],a[36:39],a[148:151],v[164:167]\nv_cvt_pk_bf16_f32 v197,v134,v135\n" \
"v_exp_f32 v144,v144\nM v[164:167],a[32:35],a[144:147],v[164:167]\nds_read_b128 a[32:35],v240 offset:57344\nds_read_b128 a[36:39],v240 offset:58368\nM v[176:179],a[48:51],a[128:131],v[224:227]\nv_exp_f32 v145,v145\nds_read_b128 a[44:47],v240 offset:60416\nM v[176:179],a[52:55],a[132:135],v[176:179]\n" \
"v_exp_f32 v146,v146\nv_cvt_pk_bf16_f32 v194,v144,v145\nM v[176:179],a[56:59],a[136:139],v[176:179]\nv_exp_f32 v147,v147\ns_waitcnt vmcnt(0)\nv_cmp_eq_u32 vcc,0,v248\nM v[176:179],a[60:63],a[140:143],v[176:179]\nv_cvt_pk_bf16_f32 v195,v146,v147\n" \
"v_exp_f32 v148,v148\nM v[180:183],a[60:63],a[156:159],v[228:231]\ns_waitcnt lgkmcnt(4)\ns_barrier\nds_read_b128 a[60:63],v240 offset:64512\nM v[180:183],a[56:59],a[152:155],v[180:183]\nv_exp_f32 v149,v149\nds_read_b128 a[56:59],v240 offset:63488\n" \
"M v[180:183],a[52:55],a[148:151],v[180:183]\nv_exp_f32 v150,v150\nv_cvt_pk_bf16_f32 v198,v148,v149\nM v[180:183],a[48:51],a[144:147],v[180:183]\nv_exp_f32 v151,v151\nds_read_b128 a[48:51],v240 offset:61440\nM v[0:3],a[64:67],v[96:99],v[0:3]\nv_cvt_pk_bf16_f32 v199,v150,v151\n" \
"v_exp_f32 v160,v160\nM v[0:3],a[96:99],v[112:115],v[0:3]\nv_exp_f32 v161,v161\nds_read_b128 a[52:55],v240 offset:62464\nM v[8:11],a[68:71],v[96:99],v[8:11]\nv_exp_f32 v162,v162\nM v[8:11],a[100:103],v[112:115],v[8:11]\nv_exp_f32 v163,v163\n" \
"v_cvt_pk_bf16_f32 v208,v160,v161\nM v[16:19],a[72:75],v[96:99],v[16:19]\nv_cvt_pk_bf16_f32 v209,v162,v163\nds_write_b128 v250,a[244:247]\nM v[16:19],a[104:107],v[112:115],v[16:19]\nv_exp_f32 v164,v164\nds_read_b128 v[64:67],v250\nM v[24:27],a[76:79],v[96:99],v[24:27]\n" \
"v_exp_f32 v165,v165\nds_read_b128 v[68:71],v250\nM v[24:27],a[108:111],v[112:115],v[24:27]\nv_exp_f32 v166,v166\nds_read_b128 v[72:75],v250\nM v[32:35],a[80:83],v[96:99],v[32:35]\nv_exp_f32 v167,v167\nds_read_b128 v[76:79],v250\n" \
"M v[32:35],a[112:115],v[112:115],v[32:35]\nds_read_b128 v[80:83],v250\nds_read_b128 v[84:87],v250\nv_cvt_pk_bf16_f32 v212,v164,v165\nM v[40:43],a[84:87],v[96:99],v[40:43]\nds_read_b128 v[88:91],v250\nv_cvt_pk_bf16_f32 v213,v166,v167\ns_cbranch_vccnz .Lps5\n" \
".Lpb5:\nM v[40:43],a[116:119],v[112:115],v[40:43]\nds_read_b128 v[92:95],v250\nv_exp_f32 v176,v176\nM v[48:51],a[88:91],v[96:99],v[48:51]\nds_read_b128 v[104:107],v250\nv_exp_f32 v177,v177\nM v[48:51],a[120:123],v[112:115],v[48:51]\n" \
"ds_read_b128 v[108:111],v250\nv_cvt_pk_bf16_f32 v210,v176,v177\nM v[56:59],a[92:95],v[96:99],v[56:59]\nds_read_b128 v[120:123],v250\nv_exp_f32 v178,v178\nM v[56:59],a[124:127],v[112:115],v[56:59]\nds_read_b128 v[124:127],v250\nv_exp_f32 v179,v179\n" \
"M a[192:195],a[208:211],v[96:99],a[192:195]\nds_read_b128 v[96:99],v250\nv_cvt_pk_bf16_f32 v211,v178,v179\ns_add_u32 m0,s64,65536\nM a[192:195],a[208:211],v[112:115],a[192:195]\nds_read_b128 v[112:115],v250\nv_exp_f32 v180,v180\nM v[4:7],a[64:67],v[100:103],v[4:7]\n" \
"ds_read_b128 a[200:203],v250\nv_exp_f32 v181,v181\nM v[4:7],a[96:99],v[116:119],v[4:7]\nds_read_b128 a[204:207],v250\nv_exp_f32 v182,v182\nM v[12:15],a[68:71],v[100:103],v[12:15]\nds_read_b128 a[64:67],v241 offset:32768\nv_exp_f32 v183,v183\n" \
"M v[12:15],a[100:103],v[116:119],v[12:15]\nds_read_b128 a[96:99],v241 offset:40960\nv_cvt_pk_bf16_f32 v214,v180,v181\nv_cvt_pk_bf16_f32 v215,v182,v183\nM v[20:23],a[72:75],v[100:103],v[20:23]\nds_read_b128 a[68:71],v241 offset:33792\nbuffer_load_dwordx4 v240,s[48:51],s36 offen offset:0 sc1 lds\nM v[20:23],a[104:107],v[116:119],v[20:23]\n" \
"ds_read_b128 a[100:103],v241 offset:41984\nM v[28:31],a[76:79],v[100:103],v[28:31]\nds_read_b128 a[72:75],v241 offset:34816\nM v[28:31],a[108:111],v[116:119],v[28:31]\nds_read_b128 a[104:107],v241 offset:43008\nM v[36:39],a[80:83],v[100:103],v[36:39]\nds_read_b128 a[76:79],v241 offset:35840\nbuffer_load_dwordx4 v240,s[48:51],s36 offen offset:1024 sc1 lds\n" \
"M v[36:39],a[112:115],v[116:119],v[36:39]\nds_read_b128 a[108:111],v241 offset:44032\nM v[44:47],a[84:87],v[100:103],v[44:47]\nds_read_b128 a[80:83],v241 offset:36864\nM v[44:47],a[116:119],v[116:119],v[44:47]\nds_read_b128 a[112:115],v241 offset:45056\nM v[52:55],a[88:91],v[100:103],v[52:55]\nds_read_b128 a[84:87],v241 offset:37888\n" \
"buffer_load_dwordx4 v240,s[48:51],s36 offen offset:2048 sc1 lds\nM v[52:55],a[120:123],v[116:119],v[52:55]\nds_read_b128 a[116:119],v241 offset:46080\nM v[60:63],a[92:95],v[100:103],v[60:63]\nds_read_b128 a[88:91],v241 offset:38912\nM v[60:63],a[124:127],v[116:119],v[60:63]\nds_read_b128 a[120:123],v241 offset:47104\nM a[196:199],a[208:211],v[100:103],a[196:199]\n" \
"ds_read_b128 v[100:103],v250\nbuffer_load_dwordx4 v240,s[48:51],s36 offen offset:3072 sc1 lds\nM a[196:199],a[208:211],v[116:119],a[196:199]\nds_read_b128 v[116:119],v250\ns_add_u32 m0,s64,16384\nM v[136:139],a[0:3],a[160:163],0\nds_read_b128 a[92:95],v241 offset:39936\nv_add_u32 v250,0xc000,v241\n" \
"M v[136:139],a[4:7],a[164:167],v[136:139]\nds_read_b128 a[124:127],v241 offset:48128\nM v[136:139],a[8:11],a[168:171],v[136:139]\nbuffer_load_dwordx4 v240,s[44:47],s65 offen offset:0 sc1 lds\nM v[136:139],a[12:15],a[172:175],v[136:139]\nM v[140:143],a[12:15],a[188:191],0\nM v[140:143],a[8:11],a[184:187],v[140:143]\nM v[140:143],a[4:7],a[180:183],v[140:143]\n" \
"buffer_load_dwordx4 v240,s[44:47],s65 offen offset:1024 sc1 lds\nM v[140:143],a[0:3],a[176:179],v[140:143]\nM v[156:159],a[28:31],a[188:191],0\nM v[156:159],a[24:27],a[184:187],v[156:159]\nM v[156:159],a[20:23],a[180:183],v[156:159]\nv_max3_f32 v233,v136,v137,v138\nv_max_f32 v233,v233,v139\nv_cndmask_b32 v233,v232,v233,s[90:91]\n" \
"M v[156:159],a[16:19],a[176:179],v[156:159]\nbuffer_load_dwordx4 v240,s[44:47],s65 offen offset:2048 sc1 lds\nv_max3_f32 v237,v140,v141,v142\nM v[128:131],a[0:3],a[128:131],v[224:227]\nv_cndmask_b32 v136,v136,v252,s[82:83]\nv_cndmask_b32 v137,v137,v252,s[84:85]\nv_cndmask_b32 v138,v138,v252,s[86:87]\nM v[128:131],a[4:7],a[132:135],v[128:131]\n" \
"v_cndmask_b32 v139,v139,v252,s[88:89]\nv_max3_f32 v237,v237,v143,v156\nv_max3_f32 v237,v237,v157,v158\nM v[128:131],a[8:11],a[136:139],v[128:131]\nv_max_f32 v237,v237,v159\nv_cndmask_b32 v237,v236,v237,s[90:91]\nv_cndmask_b32 v156,v156,v252,s[82:83]\nM v[128:131],a[12:15],a[140:143],v[128:131]\n" \
"v_permlane16_swap_b32 v233,v237\nv_max_f32 v233,v233,v237\nv_mov_b32 v237,v233\nM v[132:135],a[12:15],a[156:159],v[228:231]\nbuffer_load_dwordx4 v240,s[44:47],s65 offen offset:3072 sc1 lds\nv_permlane32_swap_b32 v233,v237\nM v[132:135],a[8:11],a[152:155],v[132:135]\nv_max_f32 v233,v233,v237\n" \
"s_add_u32 m0,s64,81920\nv_mov_b32 v237,v233\nM v[132:135],a[4:7],a[148:151],v[132:135]\nv_cndmask_b32 v157,v157,v252,s[84:85]\nv_permlane16_swap_b32 v233,v237\nv_sub_f32 v232,0,v233\nM v[132:135],a[0:3],a[144:147],v[132:135]\nds_read_b128 a[0:3],v240 offset:0\n" \
"ds_read_b128 a[4:7],v240 offset:1024\nv_add_f32 v136,v136,v232\nM v[144:147],a[16:19],a[128:131],v[224:227]\nds_read_b128 a[8:11],v240 offset:2048\nv_add_f32 v137,v137,v232\nM v[144:147],a[20:23],a[132:135],v[144:147]\nds_read_b128 a[12:15],v240 offset:3072\nbuffer_load_dwordx4 v240,s[48:51],s65 offen offset:0 sc1 lds\n" \
"M v[144:147],a[24:27],a[136:139],v[144:147]\nv_add_f32 v138,v138,v232\nv_add_f32 v139,v139,v232\nv_cndmask_b32 v158,v158,v252,s[86:87]\nM v[144:147],a[28:31],a[140:143],v[144:147]\nv_cndmask_b32 v159,v159,v252,s[88:89]\nv_sub_f32 v236,0,v237\nv_sub_f32 v234,0,v233\n" \
"M v[148:151],a[28:31],a[156:159],v[228:231]\nv_exp_f32 v136,v136\nv_sub_f32 v235,0,v233\nM v[148:151],a[24:27],a[152:155],v[148:151]\nbuffer_load_dwordx4 v240,s[48:51],s65 offen offset:1024 sc1 lds\nv_sub_f32 v238,0,v237\nM v[148:151],a[20:23],a[148:151],v[148:151]\nv_exp_f32 v137,v137\n" \
"v_sub_f32 v239,0,v237\nM v[148:151],a[16:19],a[144:147],v[148:151]\nds_read_b128 a[16:19],v240 offset:4096\nds_read_b128 a[20:23],v240 offset:5120\ns_waitcnt lgkmcnt(5)\nM v[160:163],a[32:35],a[128:131],v[224:227]\nds_read_b128 a[24:27],v240 offset:6144\nv_exp_f32 v138,v138\n" \
"M v[160:163],a[36:39],a[132:135],v[160:163]\nds_read_b128 a[28:31],v240 offset:7168\nbuffer_load_dwordx4 v240,s[48:51],s65 offen offset:2048 sc1 lds\nM v[160:163],a[40:43],a[136:139],v[160:163]\nv_exp_f32 v139,v139\nv_add_f32 v140,v140,v236\nM v[160:163],a[44:47],a[140:143],v[160:163]\nv_exp_f32 v128,v128\n" \
"M v[164:167],a[44:47],a[156:159],v[228:231]\ns_barrier\nv_exp_f32 v129,v129\nM v[164:167],a[40:43],a[152:155],v[164:167]\nbuffer_load_dwordx4 v240,s[48:51],s65 offen offset:3072 sc1 lds\ns_add_u32 m0,s64,32768\nM v[164:167],a[36:39],a[148:151],v[164:167]\nv_exp_f32 v130,v130\n" \
"v_add_f32 v141,v141,v236\nM v[164:167],a[32:35],a[144:147],v[164:167]\nds_read_b128 a[32:35],v240 offset:8192\nds_read_b128 a[36:39],v240 offset:9216\nv_add_f32 v142,v142,v236\nM v[176:179],a[48:51],a[128:131],v[224:227]\nds_read_b128 a[40:43],v240 offset:10240\nv_exp_f32 v131,v131\n" \
"M v[176:179],a[52:55],a[132:135],v[176:179]\nds_read_b128 a[44:47],v240 offset:11264\nbuffer_load_dwordx4 v240,s[44:47],s66 offen offset:0 sc1 lds\nM v[176:179],a[56:59],a[136:139],v[176:179]\nv_exp_f32 v132,v132\nv_add_f32 v143,v143,v236\nM v[176:179],a[60:63],a[140:143],v[176:179]\nv_exp_f32 v133,v133\n" \
"v_add_f32 v156,v156,v236\nM v[180:183],a[60:63],a[156:159],v[228:231]\nv_exp_f32 v134,v134\nv_add_f32 v157,v157,v236\nM v[180:183],a[56:59],a[152:155],v[180:183]\nbuffer_load_dwordx4 v240,s[44:47],s66 offen offset:1024 sc1 lds\nv_add_f32 v158,v158,v236\nM v[180:183],a[52:55],a[148:151],v[180:183]\n" \
"v_exp_f32 v135,v135\nM v[180:183],a[48:51],a[144:147],v[180:183]\nds_read_b128 a[48:51],v240 offset:12288\nds_read_b128 a[52:55],v240 offset:13312\nv_add_f32 v159,v159,v236\nM v[0:3],a[64:67],v[192:195],v[0:3]\nds_read_b128 a[56:59],v240 offset:14336\nv_exp_f32 v144,v144\n" \
"M v[0:3],a[96:99],v[208:211],v[0:3]\nds_read_b128 a[60:63],v240 offset:15360\nbuffer_load_dwordx4 v240,s[44:47],s66 offen offset:2048 sc1 lds\nM v[8:11],a[68:71],v[192:195],v[8:11]\nv_exp_f32 v145,v145\nv_sub_f32 v233,0,v233\nM v[8:11],a[100:103],v[208:211],v[8:11]\nv_exp_f32 v146,v146\n" \
"v_sub_f32 v237,0,v237\nM v[16:19],a[72:75],v[192:195],v[16:19]\nv_exp_f32 v147,v147\nv_cvt_pk_bf16_f32 v200,v136,v137\nM v[16:19],a[104:107],v[208:211],v[16:19]\nbuffer_load_dwordx4 v240,s[44:47],s66 offen offset:3072 sc1 lds\ns_add_u32 m0,s64,98304\nM v[24:27],a[76:79],v[192:195],v[24:27]\n" \
"v_exp_f32 v148,v148\nv_cvt_pk_bf16_f32 v201,v138,v139\nM v[24:27],a[108:111],v[208:211],v[24:27]\nv_exp_f32 v149,v149\nM v[32:35],a[80:83],v[192:195],v[32:35]\nv_exp_f32 v150,v150\nM v[32:35],a[112:115],v[208:211],v[32:35]\nv_exp_f32 v151,v151\n" \
"M v[40:43],a[84:87],v[192:195],v[40:43]\nv_exp_f32 v160,v160\nM v[40:43],a[116:119],v[208:211],v[40:43]\nv_exp_f32 v161,v161\nM v[48:51],a[88:91],v[192:195],v[48:51]\nv_exp_f32 v162,v162\nM v[48:51],a[120:123],v[208:211],v[48:51]\nv_exp_f32 v163,v163\n" \
"M v[56:59],a[92:95],v[192:195],v[56:59]\nv_exp_f32 v176,v176\nM v[56:59],a[124:127],v[208:211],v[56:59]\nv_exp_f32 v177,v177\nM a[192:195],a[208:211],v[192:195],a[192:195]\nv_exp_f32 v178,v178\nv_cvt_pk_bf16_f32 v192,v128,v129\nM a[192:195],a[208:211],v[208:211],a[192:195]\n" \
"v_exp_f32 v179,v179\nv_cvt_pk_bf16_f32 v193,v130,v131\nM v[4:7],a[64:67],v[196:199],v[4:7]\nv_exp_f32 v140,v140\nv_cvt_pk_bf16_f32 v194,v144,v145\nM v[4:7],a[96:99],v[212:215],v[4:7]\nv_exp_f32 v141,v141\nv_cvt_pk_bf16_f32 v195,v146,v147\n" \
"M v[12:15],a[68:71],v[196:199],v[12:15]\nv_exp_f32 v142,v142\nM v[12:15],a[100:103],v[212:215],v[12:15]\nv_exp_f32 v143,v143\nv_cvt_pk_bf16_f32 v208,v160,v161\nM v[20:23],a[72:75],v[196:199],v[20:23]\nv_exp_f32 v156,v156\nv_cvt_pk_bf16_f32 v209,v162,v163\n" \
"M v[20:23],a[104:107],v[212:215],v[20:23]\nv_exp_f32 v157,v157\nv_cvt_pk_bf16_f32 v210,v176,v177\nM v[28:31],a[76:79],v[196:199],v[28:31]\nv_exp_f32 v158,v158\nv_cvt_pk_bf16_f32 v211,v178,v179\nM v[28:31],a[108:111],v[212:215],v[28:31]\nv_exp_f32 v159,v159\n" \
"M v[36:39],a[80:83],v[196:199],v[36:39]\nv_exp_f32 v164,v164\nM v[36:39],a[112:115],v[212:215],v[36:39]\nv_exp_f32 v165,v165\nM v[44:47],a[84:87],v[196:199],v[44:47]\nv_exp_f32 v166,v166\nM v[44:47],a[116:119],v[212:215],v[44:47]\nv_exp_f32 v167,v167\n" \
"M v[52:55],a[88:91],v[196:199],v[52:55]\nv_exp_f32 v180,v180\nM v[52:55],a[120:123],v[212:215],v[52:55]\nv_exp_f32 v181,v181\nM v[60:63],a[92:95],v[196:199],v[60:63]\nv_exp_f32 v182,v182\nM v[60:63],a[124:127],v[212:215],v[60:63]\nv_exp_f32 v183,v183\n" \
"M a[196:199],a[208:211],v[196:199],a[196:199]\nv_cvt_pk_bf16_f32 v196,v132,v133\nv_cvt_pk_bf16_f32 v197,v134,v135\nv_cvt_pk_bf16_f32 v198,v148,v149\nv_cvt_pk_bf16_f32 v199,v150,v151\nbuffer_load_dwordx4 v240,s[48:51],s66 offen offset:0 sc1 lds\nbuffer_load_dwordx4 v240,s[48:51],s66 offen offset:1024 sc1 lds\nbuffer_load_dwordx4 v240,s[48:51],s66 offen offset:2048 sc1 lds\n" \
"buffer_load_dwordx4 v240,s[48:51],s66 offen offset:3072 sc1 lds\nM a[196:199],a[208:211],v[212:215],a[196:199]\ns_cmp_lt_u32 s62,2\ns_cbranch_scc1 .Lx01\n.Ldual:\ns_waitcnt lgkmcnt(4)\nM v[128:131],a[0:3],a[128:131],v[224:227]\ns_add_u32 m0,s94,s64\n" \
"v_cvt_pk_bf16_f32 v204,v140,v141\nM v[128:131],a[4:7],a[132:135],v[128:131]\nds_read_b128 a[64:67],v250 offset:0\nds_read_b128 a[96:99],v250 offset:8192\nM v[128:131],a[8:11],a[136:139],v[128:131]\nv_cvt_pk_bf16_f32 v205,v142,v143\nv_cvt_pk_bf16_f32 v206,v156,v157\nM v[128:131],a[12:15],a[140:143],v[128:131]\n" \
"v_cvt_pk_bf16_f32 v207,v158,v159\nv_cvt_pk_bf16_f32 v212,v164,v165\nM v[132:135],a[12:15],a[156:159],v[228:231]\nbuffer_load_dwordx4 v240,s[44:47],s95 offen offset:0 sc1 lds\nM v[132:135],a[8:11],a[152:155],v[132:135]\nv_cvt_pk_bf16_f32 v213,v166,v167\nv_cvt_pk_bf16_f32 v216,v168,v169\nM v[132:135],a[4:7],a[148:151],v[132:135]\n" \
"v_cvt_pk_bf16_f32 v217,v170,v171\nv_cvt_pk_bf16_f32 v214,v180,v181\nM v[132:135],a[0:3],a[144:147],v[132:135]\nds_read_b128 a[68:71],v250 offset:1024\nds_read_b128 a[100:103],v250 offset:9216\nM v[136:139],a[0:3],a[160:163],v[232:235]\nv_exp_f32 v172,v172\nM v[136:139],a[4:7],a[164:167],v[136:139]\n" \
"v_exp_f32 v173,v173\nM v[136:139],a[8:11],a[168:171],v[136:139]\nv_exp_f32 v174,v174\nM v[136:139],a[12:15],a[172:175],v[136:139]\nv_exp_f32 v175,v175\nM v[140:143],a[12:15],a[188:191],v[236:239]\nv_cvt_pk_bf16_f32 v220,v172,v173\nv_cvt_pk_bf16_f32 v221,v174,v175\n" \
"M v[140:143],a[8:11],a[184:187],v[140:143]\nds_read_b128 a[72:75],v250 offset:2048\nds_read_b128 a[104:107],v250 offset:10240\nM v[140:143],a[4:7],a[180:183],v[140:143]\nv_cvt_pk_bf16_f32 v215,v182,v183\ns_add_u32 s94,s94,0x4000\nM v[140:143],a[0:3],a[176:179],v[140:143]\nv_exp_f32 v184,v184\n" \
"M v[144:147],a[16:19],a[128:131],v[224:227]\nv_exp_f32 v185,v185\nM v[144:147],a[20:23],a[132:135],v[144:147]\nv_exp_f32 v186,v186\nM v[144:147],a[24:27],a[136:139],v[144:147]\nv_exp_f32 v187,v187\nM v[144:147],a[28:31],a[140:143],v[144:147]\nds_read_b128 a[76:79],v250 offset:3072\n" \
"ds_read_b128 a[108:111],v250 offset:11264\nM v[148:151],a[28:31],a[156:159],v[228:231]\nv_cvt_pk_bf16_f32 v218,v184,v185\nv_cvt_pk_bf16_f32 v219,v186,v187\nM v[148:151],a[24:27],a[152:155],v[148:151]\nv_exp_f32 v188,v188\nM v[148:151],a[20:23],a[148:151],v[148:151]\nbuffer_load_dwordx4 v240,s[44:47],s95 offen offset:1024 sc1 lds\n" \
"M v[148:151],a[16:19],a[144:147],v[148:151]\nv_exp_f32 v189,v189\nM v[152:155],a[16:19],a[160:163],v[232:235]\nv_exp_f32 v190,v190\nM v[152:155],a[20:23],a[164:167],v[152:155]\nds_read_b128 a[80:83],v250 offset:4096\nds_read_b128 a[112:115],v250 offset:12288\nM v[152:155],a[24:27],a[168:171],v[152:155]\n" \
"v_exp_f32 v191,v191\nM v[152:155],a[28:31],a[172:175],v[152:155]\nv_cvt_pk_bf16_f32 v222,v188,v189\nv_cvt_pk_bf16_f32 v223,v190,v191\nM v[156:159],a[28:31],a[188:191],v[236:239]\nv_exp_f32 v128,v128\nM v[156:159],a[24:27],a[184:187],v[156:159]\nv_exp_f32 v129,v129\n" \
"M v[156:159],a[20:23],a[180:183],v[156:159]\nv_exp_f32 v130,v130\nM v[156:159],a[16:19],a[176:179],v[156:159]\nds_read_b128 a[84:87],v250 offset:5120\nds_read_b128 a[116:119],v250 offset:13312\nM v[160:163],a[32:35],a[128:131],v[224:227]\nv_exp_f32 v131,v131\nM v[160:163],a[36:39],a[132:135],v[160:163]\n" \
"v_exp_f32 v144,v144\nM v[160:163],a[40:43],a[136:139],v[160:163]\nv_exp_f32 v145,v145\nM v[160:163],a[44:47],a[140:143],v[160:163]\nv_exp_f32 v146,v146\nM v[164:167],a[44:47],a[156:159],v[228:231]\nv_exp_f32 v147,v147\nM v[164:167],a[40:43],a[152:155],v[164:167]\n" \
"ds_read_b128 a[88:91],v250 offset:6144\nds_read_b128 a[120:123],v250 offset:14336\nM v[164:167],a[36:39],a[148:151],v[164:167]\nv_exp_f32 v132,v132\nM v[164:167],a[32:35],a[144:147],v[164:167]\nv_exp_f32 v133,v133\nM v[168:171],a[32:35],a[160:163],v[232:235]\nbuffer_load_dwordx4 v240,s[44:47],s95 offen offset:2048 sc1 lds\n" \
"M v[168:171],a[36:39],a[164:167],v[168:171]\nv_exp_f32 v134,v134\nM v[168:171],a[40:43],a[168:171],v[168:171]\nv_exp_f32 v135,v135\nM v[168:171],a[44:47],a[172:175],v[168:171]\nds_read_b128 a[92:95],v250 offset:7168\nds_read_b128 a[124:127],v250 offset:15360\nM v[172:175],a[44:47],a[188:191],v[236:239]\n" \
"v_xor_b32 v250,0x18000,v250\ns_and_b32 s94,s94,0xffff\nM v[172:175],a[40:43],a[184:187],v[172:175]\nv_exp_f32 v148,v148\nM v[172:175],a[36:39],a[180:183],v[172:175]\nv_exp_f32 v149,v149\nM v[172:175],a[32:35],a[176:179],v[172:175]\ns_waitcnt lgkmcnt(4)\n" \
"M v[176:179],a[48:51],a[128:131],v[224:227]\nv_exp_f32 v150,v150\nM v[176:179],a[52:55],a[132:135],v[176:179]\nv_exp_f32 v151,v151\nM v[176:179],a[56:59],a[136:139],v[176:179]\nv_exp_f32 v136,v136\nM v[176:179],a[60:63],a[140:143],v[176:179]\nv_exp_f32 v137,v137\n" \
"M v[180:183],a[60:63],a[156:159],v[228:231]\nv_exp_f32 v138,v138\nM v[180:183],a[56:59],a[152:155],v[180:183]\nv_exp_f32 v139,v139\nM v[180:183],a[52:55],a[148:151],v[180:183]\nv_exp_f32 v152,v152\nM v[180:183],a[48:51],a[144:147],v[180:183]\nv_exp_f32 v153,v153\n" \
"M v[184:187],a[48:51],a[160:163],v[232:235]\nv_exp_f32 v154,v154\nM v[184:187],a[52:55],a[164:167],v[184:187]\nv_exp_f32 v155,v155\nM v[184:187],a[56:59],a[168:171],v[184:187]\nbuffer_load_dwordx4 v240,s[44:47],s95 offen offset:3072 sc1 lds\nM v[184:187],a[60:63],a[172:175],v[184:187]\nv_exp_f32 v160,v160\n" \
"M v[188:191],a[60:63],a[188:191],v[236:239]\nv_exp_f32 v161,v161\nM v[188:191],a[56:59],a[184:187],v[188:191]\nv_exp_f32 v162,v162\nM v[188:191],a[52:55],a[180:183],v[188:191]\nv_exp_f32 v163,v163\nM v[188:191],a[48:51],a[176:179],v[188:191]\nv_exp_f32 v176,v176\n" \
"M v[0:3],a[64:67],v[192:195],v[0:3]\ns_waitcnt vmcnt(16) lgkmcnt(0)\ns_barrier\nM v[0:3],a[96:99],v[208:211],v[0:3]\nds_read_b128 a[0:3],v250 offset:0\nds_read_b128 a[4:7],v250 offset:1024\nM v[8:11],a[68:71],v[192:195],v[8:11]\nv_exp_f32 v177,v177\n" \
"M v[8:11],a[100:103],v[208:211],v[8:11]\nv_exp_f32 v178,v178\nM v[16:19],a[72:75],v[192:195],v[16:19]\ns_add_u32 m0,m0,0x10000\nM v[16:19],a[104:107],v[208:211],v[16:19]\nbuffer_load_dwordx4 v240,s[48:51],s95 offen offset:0 sc1 lds\nM v[24:27],a[76:79],v[192:195],v[24:27]\nv_exp_f32 v179,v179\n" \
"M v[24:27],a[108:111],v[208:211],v[24:27]\nv_exp_f32 v140,v140\nM v[32:35],a[80:83],v[192:195],v[32:35]\nv_exp_f32 v141,v141\nM v[32:35],a[112:115],v[208:211],v[32:35]\nds_read_b128 a[8:11],v250 offset:2048\nds_read_b128 a[12:15],v250 offset:3072\nM v[40:43],a[84:87],v[192:195],v[40:43]\n" \
"v_exp_f32 v142,v142\nM v[40:43],a[116:119],v[208:211],v[40:43]\nv_exp_f32 v143,v143\nM v[48:51],a[88:91],v[192:195],v[48:51]\nv_exp_f32 v156,v156\nM v[48:51],a[120:123],v[208:211],v[48:51]\nv_exp_f32 v157,v157\nM v[56:59],a[92:95],v[192:195],v[56:59]\n" \
"v_exp_f32 v158,v158\nM v[56:59],a[124:127],v[208:211],v[56:59]\nv_exp_f32 v159,v159\nM a[192:195],a[208:211],v[192:195],a[192:195]\nv_exp_f32 v164,v164\nM a[192:195],a[208:211],v[208:211],a[192:195]\nds_read_b128 a[16:19],v250 offset:4096\nds_read_b128 a[20:23],v250 offset:5120\n" \
"M v[4:7],a[64:67],v[196:199],v[4:7]\nv_exp_f32 v165,v165\nM v[4:7],a[96:99],v[212:215],v[4:7]\nv_exp_f32 v166,v166\nM v[12:15],a[68:71],v[196:199],v[12:15]\nv_exp_f32 v167,v167\nM v[12:15],a[100:103],v[212:215],v[12:15]\nbuffer_load_dwordx4 v240,s[48:51],s95 offen offset:1024 sc1 lds\n" \
"M v[20:23],a[72:75],v[196:199],v[20:23]\nv_exp_f32 v180,v180\nM v[20:23],a[104:107],v[212:215],v[20:23]\nv_exp_f32 v181,v181\nM v[28:31],a[76:79],v[196:199],v[28:31]\nv_exp_f32 v182,v182\nM v[28:31],a[108:111],v[212:215],v[28:31]\nds_read_b128 a[24:27],v250 offset:6144\n" \
"ds_read_b128 a[28:31],v250 offset:7168\nM v[36:39],a[80:83],v[196:199],v[36:39]\nv_exp_f32 v183,v183\nM v[36:39],a[112:115],v[212:215],v[36:39]\nv_exp_f32 v168,v168\nM v[44:47],a[84:87],v[196:199],v[44:47]\nv_exp_f32 v169,v169\nM v[44:47],a[116:119],v[212:215],v[44:47]\n" \
"v_exp_f32 v170,v170\nM v[52:55],a[88:91],v[196:199],v[52:55]\nv_exp_f32 v171,v171\nM v[52:55],a[120:123],v[212:215],v[52:55]\nv_cvt_pk_bf16_f32 v192,v128,v129\nv_cvt_pk_bf16_f32 v193,v130,v131\nM v[60:63],a[92:95],v[196:199],v[60:63]\nv_cvt_pk_bf16_f32 v194,v144,v145\n" \
"v_cvt_pk_bf16_f32 v195,v146,v147\nM v[60:63],a[124:127],v[212:215],v[60:63]\nds_read_b128 a[32:35],v250 offset:8192\nds_read_b128 a[36:39],v250 offset:9216\nM a[196:199],a[208:211],v[196:199],a[196:199]\nv_cvt_pk_bf16_f32 v208,v160,v161\nv_cvt_pk_bf16_f32 v209,v162,v163\nM a[196:199],a[208:211],v[212:215],a[196:199]\n" \
"v_cvt_pk_bf16_f32 v210,v176,v177\nv_cvt_pk_bf16_f32 v211,v178,v179\nM v[64:67],a[64:67],v[200:203],v[64:67]\nv_cvt_pk_bf16_f32 v196,v132,v133\nv_cvt_pk_bf16_f32 v197,v134,v135\nM v[64:67],a[96:99],v[216:219],v[64:67]\nbuffer_load_dwordx4 v240,s[48:51],s95 offen offset:2048 sc1 lds\nM v[72:75],a[68:71],v[200:203],v[72:75]\n" \
"v_cvt_pk_bf16_f32 v198,v148,v149\nv_cvt_pk_bf16_f32 v199,v150,v151\nM v[72:75],a[100:103],v[216:219],v[72:75]\nM v[80:83],a[72:75],v[200:203],v[80:83]\nM v[80:83],a[104:107],v[216:219],v[80:83]\nds_read_b128 a[40:43],v250 offset:10240\nds_read_b128 a[44:47],v250 offset:11264\nM v[88:91],a[76:79],v[200:203],v[88:91]\n" \
"M v[88:91],a[108:111],v[216:219],v[88:91]\nM v[96:99],a[80:83],v[200:203],v[96:99]\nM v[96:99],a[112:115],v[216:219],v[96:99]\nM v[104:107],a[84:87],v[200:203],v[104:107]\nM v[104:107],a[116:119],v[216:219],v[104:107]\nM v[112:115],a[88:91],v[200:203],v[112:115]\nM v[112:115],a[120:123],v[216:219],v[112:115]\nds_read_b128 a[48:51],v250 offset:12288\n" \
"ds_read_b128 a[52:55],v250 offset:13312\nM v[120:123],a[92:95],v[200:203],v[120:123]\nM v[120:123],a[124:127],v[216:219],v[120:123]\nM a[200:203],a[208:211],v[200:203],a[200:203]\nv_cvt_pk_bf16_f32 v200,v136,v137\nv_cvt_pk_bf16_f32 v201,v138,v139\nM a[200:203],a[208:211],v[216:219],a[200:203]\nbuffer_load_dwordx4 v240,s[48:51],s95 offen offset:3072 sc1 lds\n" \
"M v[68:71],a[64:67],v[204:207],v[68:71]\nv_cvt_pk_bf16_f32 v202,v152,v153\nv_cvt_pk_bf16_f32 v203,v154,v155\nM v[68:71],a[96:99],v[220:223],v[68:71]\ns_cmp_eq_u32 s95,s96\ns_cselect_b32 s72,0xc000,s98\ns_add_u32 s95,s95,s72\nM v[76:79],a[68:71],v[204:207],v[76:79]\n" \
"M v[76:79],a[100:103],v[220:223],v[76:79]\nds_read_b128 a[56:59],v250 offset:14336\nds_read_b128 a[60:63],v250 offset:15360\nM v[84:87],a[72:75],v[204:207],v[84:87]\nv_add_u32 v250,s94,v241\nM v[84:87],a[104:107],v[220:223],v[84:87]\nM v[92:95],a[76:79],v[204:207],v[92:95]\nM v[92:95],a[108:111],v[220:223],v[92:95]\n" \
"M v[100:103],a[80:83],v[204:207],v[100:103]\nM v[100:103],a[112:115],v[220:223],v[100:103]\nM v[108:111],a[84:87],v[204:207],v[108:111]\nM v[108:111],a[116:119],v[220:223],v[108:111]\nM v[116:119],a[88:91],v[204:207],v[116:119]\nbuffer_load_dword v247,v254,s[56:59],s1 offen\nM v[116:119],a[120:123],v[220:223],v[116:119]\ns_add_u32 s1,s1,s0\n" \
"M v[124:127],a[92:95],v[204:207],v[124:127]\nM v[124:127],a[124:127],v[220:223],v[124:127]\nM a[204:207],a[208:211],v[204:207],a[204:207]\nM a[204:207],a[208:211],v[220:223],a[204:207]\ns_cmp_lg_u32 s95,s97\ns_cbranch_scc1 .Ldual\n.Ldf:\ns_waitcnt lgkmcnt(4)\n" \
"M v[128:131],a[0:3],a[128:131],v[224:227]\ns_add_u32 m0,s94,s64\nv_cvt_pk_bf16_f32 v204,v140,v141\nM v[128:131],a[4:7],a[132:135],v[128:131]\nds_read_b128 a[64:67],v250 offset:0\nds_read_b128 a[96:99],v250 offset:8192\nM v[128:131],a[8:11],a[136:139],v[128:131]\nv_cvt_pk_bf16_f32 v205,v142,v143\n" \
"v_lshrrev_b32 v248,2,v249\nM v[128:131],a[12:15],a[140:143],v[128:131]\nv_mul_u32_u24 v248,3584,v248\nv_and_b32 v251,3,v249\nM v[132:135],a[12:15],a[156:159],v[228:231]\nbuffer_load_dwordx4 v240,s[44:47],s95 offen offset:0 sc1 lds\nM v[132:135],a[8:11],a[152:155],v[132:135]\ns_mul_i32 s70,s7,458752\n" \
"v_lshl_add_u32 v248,v251,3,v248\nM v[132:135],a[4:7],a[148:151],v[132:135]\ns_add_u32 s70,s70,s2\nv_add_u32 v248,s70,v248\nM v[132:135],a[0:3],a[144:147],v[132:135]\nds_read_b128 a[68:71],v250 offset:1024\nds_read_b128 a[100:103],v250 offset:9216\nM v[136:139],a[0:3],a[160:163],v[232:235]\n" \
"v_and_b32 v248,s3,v248\nv_cvt_pk_bf16_f32 v206,v156,v157\nM v[136:139],a[4:7],a[164:167],v[136:139]\nv_cvt_pk_bf16_f32 v207,v158,v159\nv_cvt_pk_bf16_f32 v212,v164,v165\nM v[136:139],a[8:11],a[168:171],v[136:139]\nv_cvt_pk_bf16_f32 v213,v166,v167\nv_cvt_pk_bf16_f32 v216,v168,v169\n" \
"M v[136:139],a[12:15],a[172:175],v[136:139]\nv_cvt_pk_bf16_f32 v217,v170,v171\ns_and_b32 s71,s3,57344\nM v[140:143],a[12:15],a[188:191],v[236:239]\nv_exp_f32 v172,v172\nM v[140:143],a[8:11],a[184:187],v[140:143]\nds_read_b128 a[72:75],v250 offset:2048\nds_read_b128 a[104:107],v250 offset:10240\n" \
"M v[140:143],a[4:7],a[180:183],v[140:143]\nglobal_load_dwordx2 a[212:213],v248,s[22:23]\nM v[140:143],a[0:3],a[176:179],v[140:143]\nv_exp_f32 v173,v173\nM v[144:147],a[16:19],a[128:131],v[224:227]\nv_exp_f32 v174,v174\nM v[144:147],a[20:23],a[132:135],v[144:147]\nv_exp_f32 v175,v175\n" \
"M v[144:147],a[24:27],a[136:139],v[144:147]\nv_cvt_pk_bf16_f32 v220,v172,v173\nv_cvt_pk_bf16_f32 v221,v174,v175\nM v[144:147],a[28:31],a[140:143],v[144:147]\nds_read_b128 a[76:79],v250 offset:3072\nds_read_b128 a[108:111],v250 offset:11264\nM v[148:151],a[28:31],a[156:159],v[228:231]\nv_cvt_pk_bf16_f32 v214,v180,v181\n" \
"v_cvt_pk_bf16_f32 v215,v182,v183\nM v[148:151],a[24:27],a[152:155],v[148:151]\nv_exp_f32 v184,v184\nM v[148:151],a[20:23],a[148:151],v[148:151]\nbuffer_load_dwordx4 v240,s[44:47],s95 offen offset:1024 sc1 lds\nM v[148:151],a[16:19],a[144:147],v[148:151]\nv_exp_f32 v185,v185\nM v[152:155],a[16:19],a[160:163],v[232:235]\n" \
"v_exp_f32 v186,v186\nM v[152:155],a[20:23],a[164:167],v[152:155]\nds_read_b128 a[80:83],v250 offset:4096\nds_read_b128 a[112:115],v250 offset:12288\nM v[152:155],a[24:27],a[168:171],v[152:155]\nv_exp_f32 v187,v187\nM v[152:155],a[28:31],a[172:175],v[152:155]\nv_cvt_pk_bf16_f32 v218,v184,v185\n" \
"v_cvt_pk_bf16_f32 v219,v186,v187\nM v[156:159],a[28:31],a[188:191],v[236:239]\nv_exp_f32 v188,v188\nM v[156:159],a[24:27],a[184:187],v[156:159]\nv_exp_f32 v189,v189\nM v[156:159],a[20:23],a[180:183],v[156:159]\nv_exp_f32 v190,v190\nM v[156:159],a[16:19],a[176:179],v[156:159]\n" \
"ds_read_b128 a[84:87],v250 offset:5120\nds_read_b128 a[116:119],v250 offset:13312\nM v[160:163],a[32:35],a[128:131],v[224:227]\nglobal_load_dwordx2 a[228:229],v248,s[24:25]\nM v[160:163],a[36:39],a[132:135],v[160:163]\nv_add_u32 v248,s71,v248\nv_cvt_pk_bf16_f32 v222,v188,v189\nM v[160:163],a[40:43],a[136:139],v[160:163]\n" \
"v_exp_f32 v191,v191\nM v[160:163],a[44:47],a[140:143],v[160:163]\nv_cvt_pk_bf16_f32 v223,v190,v191\ns_add_u32 s94,s94,0x4000\nM v[164:167],a[44:47],a[156:159],v[228:231]\nv_exp_f32 v128,v128\nM v[164:167],a[40:43],a[152:155],v[164:167]\nds_read_b128 a[88:91],v250 offset:6144\n" \
"ds_read_b128 a[120:123],v250 offset:14336\nM v[164:167],a[36:39],a[148:151],v[164:167]\nv_exp_f32 v129,v129\nM v[164:167],a[32:35],a[144:147],v[164:167]\nv_exp_f32 v130,v130\nM v[168:171],a[32:35],a[160:163],v[232:235]\nbuffer_load_dwordx4 v240,s[44:47],s95 offen offset:2048 sc1 lds\nM v[168:171],a[36:39],a[164:167],v[168:171]\n" \
"v_exp_f32 v131,v131\nM v[168:171],a[40:43],a[168:171],v[168:171]\nv_exp_f32 v144,v144\nM v[168:171],a[44:47],a[172:175],v[168:171]\nds_read_b128 a[92:95],v250 offset:7168\nds_read_b128 a[124:127],v250 offset:15360\nM v[172:175],a[44:47],a[188:191],v[236:239]\nv_xor_b32 v250,0x18000,v250\n" \
"s_and_b32 s94,s94,0xffff\nM v[172:175],a[40:43],a[184:187],v[172:175]\nv_exp_f32 v145,v145\nM v[172:175],a[36:39],a[180:183],v[172:175]\nv_exp_f32 v146,v146\nM v[172:175],a[32:35],a[176:179],v[172:175]\ns_waitcnt lgkmcnt(4)\nM v[176:179],a[48:51],a[128:131],v[224:227]\n" \
"v_exp_f32 v147,v147\nM v[176:179],a[52:55],a[132:135],v[176:179]\nv_exp_f32 v132,v132\nM v[176:179],a[56:59],a[136:139],v[176:179]\nglobal_load_dwordx2 a[214:215],v248,s[22:23]\nM v[176:179],a[60:63],a[140:143],v[176:179]\nv_exp_f32 v133,v133\nM v[180:183],a[60:63],a[156:159],v[228:231]\n" \
"v_exp_f32 v134,v134\nM v[180:183],a[56:59],a[152:155],v[180:183]\nv_exp_f32 v135,v135\nM v[180:183],a[52:55],a[148:151],v[180:183]\nv_exp_f32 v148,v148\nM v[180:183],a[48:51],a[144:147],v[180:183]\nv_exp_f32 v149,v149\nM v[184:187],a[48:51],a[160:163],v[232:235]\n" \
"v_exp_f32 v150,v150\nM v[184:187],a[52:55],a[164:167],v[184:187]\nv_exp_f32 v151,v151\nM v[184:187],a[56:59],a[168:171],v[184:187]\nbuffer_load_dwordx4 v240,s[44:47],s95 offen offset:3072 sc1 lds\nM v[184:187],a[60:63],a[172:175],v[184:187]\nv_exp_f32 v136,v136\nM v[188:191],a[60:63],a[188:191],v[236:239]\n" \
"v_exp_f32 v137,v137\nM v[188:191],a[56:59],a[184:187],v[188:191]\nv_exp_f32 v138,v138\nM v[188:191],a[52:55],a[180:183],v[188:191]\nglobal_load_dwordx2 a[230:231],v248,s[24:25]\nM v[188:191],a[48:51],a[176:179],v[188:191]\ns_add_u32 m0,m0,0x10000\nv_add_u32 v248,s71,v248\n" \
"M v[0:3],a[64:67],v[192:195],v[0:3]\ns_waitcnt vmcnt(16) lgkmcnt(0)\ns_barrier\nM v[0:3],a[96:99],v[208:211],v[0:3]\nds_read_b128 a[0:3],v250 offset:0\nds_read_b128 a[4:7],v250 offset:1024\nM v[8:11],a[68:71],v[192:195],v[8:11]\nv_exp_f32 v139,v139\n" \
"M v[8:11],a[100:103],v[208:211],v[8:11]\nv_exp_f32 v152,v152\nM v[16:19],a[72:75],v[192:195],v[16:19]\nv_exp_f32 v153,v153\nM v[16:19],a[104:107],v[208:211],v[16:19]\nbuffer_load_dwordx4 v240,s[48:51],s95 offen offset:0 sc1 lds\nM v[24:27],a[76:79],v[192:195],v[24:27]\nv_exp_f32 v154,v154\n" \
"M v[24:27],a[108:111],v[208:211],v[24:27]\nv_exp_f32 v155,v155\nM v[32:35],a[80:83],v[192:195],v[32:35]\nv_exp_f32 v160,v160\nM v[32:35],a[112:115],v[208:211],v[32:35]\nds_read_b128 a[8:11],v250 offset:2048\nds_read_b128 a[12:15],v250 offset:3072\nM v[40:43],a[84:87],v[192:195],v[40:43]\n" \
"v_exp_f32 v161,v161\nM v[40:43],a[116:119],v[208:211],v[40:43]\nv_exp_f32 v162,v162\nM v[48:51],a[88:91],v[192:195],v[48:51]\nv_exp_f32 v163,v163\nM v[48:51],a[120:123],v[208:211],v[48:51]\nglobal_load_dwordx2 a[216:217],v248,s[22:23]\nM v[56:59],a[92:95],v[192:195],v[56:59]\n" \
"v_exp_f32 v176,v176\nM v[56:59],a[124:127],v[208:211],v[56:59]\nv_exp_f32 v177,v177\nM a[192:195],a[208:211],v[192:195],a[192:195]\nv_exp_f32 v178,v178\nM a[192:195],a[208:211],v[208:211],a[192:195]\nds_read_b128 a[16:19],v250 offset:4096\nds_read_b128 a[20:23],v250 offset:5120\n" \
"M v[4:7],a[64:67],v[196:199],v[4:7]\nv_exp_f32 v179,v179\nM v[4:7],a[96:99],v[212:215],v[4:7]\nv_exp_f32 v140,v140\nM v[12:15],a[68:71],v[196:199],v[12:15]\nv_exp_f32 v141,v141\nM v[12:15],a[100:103],v[212:215],v[12:15]\nbuffer_load_dwordx4 v240,s[48:51],s95 offen offset:1024 sc1 lds\n" \
"M v[20:23],a[72:75],v[196:199],v[20:23]\nv_exp_f32 v142,v142\nM v[20:23],a[104:107],v[212:215],v[20:23]\nv_exp_f32 v143,v143\nM v[28:31],a[76:79],v[196:199],v[28:31]\nv_exp_f32 v156,v156\nM v[28:31],a[108:111],v[212:215],v[28:31]\nds_read_b128 a[24:27],v250 offset:6144\n" \
"ds_read_b128 a[28:31],v250 offset:7168\nM v[36:39],a[80:83],v[196:199],v[36:39]\nv_exp_f32 v157,v157\nM v[36:39],a[112:115],v[212:215],v[36:39]\nv_exp_f32 v158,v158\nM v[44:47],a[84:87],v[196:199],v[44:47]\nv_exp_f32 v159,v159\nM v[44:47],a[116:119],v[212:215],v[44:47]\n" \
"global_load_dwordx2 a[232:233],v248,s[24:25]\nM v[52:55],a[88:91],v[196:199],v[52:55]\nv_add_u32 v248,s71,v248\nv_cvt_pk_bf16_f32 v192,v128,v129\nM v[52:55],a[120:123],v[212:215],v[52:55]\nv_exp_f32 v164,v164\nM v[60:63],a[92:95],v[196:199],v[60:63]\nv_exp_f32 v165,v165\n" \
"M v[60:63],a[124:127],v[212:215],v[60:63]\nds_read_b128 a[32:35],v250 offset:8192\nds_read_b128 a[36:39],v250 offset:9216\nM a[196:199],a[208:211],v[196:199],a[196:199]\nv_exp_f32 v166,v166\nM a[196:199],a[208:211],v[212:215],a[196:199]\nv_exp_f32 v167,v167\nM v[64:67],a[64:67],v[200:203],v[64:67]\n" \
"v_exp_f32 v180,v180\nM v[64:67],a[96:99],v[216:219],v[64:67]\nbuffer_load_dwordx4 v240,s[48:51],s95 offen offset:2048 sc1 lds\nM v[72:75],a[68:71],v[200:203],v[72:75]\nv_exp_f32 v181,v181\nM v[72:75],a[100:103],v[216:219],v[72:75]\nv_exp_f32 v182,v182\nM v[80:83],a[72:75],v[200:203],v[80:83]\n" \
"v_exp_f32 v183,v183\nM v[80:83],a[104:107],v[216:219],v[80:83]\nds_read_b128 a[40:43],v250 offset:10240\nds_read_b128 a[44:47],v250 offset:11264\nM v[88:91],a[76:79],v[200:203],v[88:91]\nv_exp_f32 v168,v168\nM v[88:91],a[108:111],v[216:219],v[88:91]\nv_exp_f32 v169,v169\n" \
"M v[96:99],a[80:83],v[200:203],v[96:99]\nv_exp_f32 v170,v170\nM v[96:99],a[112:115],v[216:219],v[96:99]\nglobal_load_dwordx2 a[218:219],v248,s[22:23]\nM v[104:107],a[84:87],v[200:203],v[104:107]\nv_exp_f32 v171,v171\nM v[104:107],a[116:119],v[216:219],v[104:107]\nv_cvt_pk_bf16_f32 v193,v130,v131\n" \
"v_cvt_pk_bf16_f32 v194,v144,v145\nM v[112:115],a[88:91],v[200:203],v[112:115]\nv_cvt_pk_bf16_f32 v195,v146,v147\nv_cvt_pk_bf16_f32 v208,v160,v161\nM v[112:115],a[120:123],v[216:219],v[112:115]\nds_read_b128 a[48:51],v250 offset:12288\nds_read_b128 a[52:55],v250 offset:13312\nM v[120:123],a[92:95],v[200:203],v[120:123]\n" \
"v_cvt_pk_bf16_f32 v209,v162,v163\nv_cvt_pk_bf16_f32 v210,v176,v177\nM v[120:123],a[124:127],v[216:219],v[120:123]\nv_cvt_pk_bf16_f32 v211,v178,v179\nv_cvt_pk_bf16_f32 v196,v132,v133\nM a[200:203],a[208:211],v[200:203],a[200:203]\nv_cvt_pk_bf16_f32 v197,v134,v135\nv_cvt_pk_bf16_f32 v198,v148,v149\n" \
"M a[200:203],a[208:211],v[216:219],a[200:203]\nbuffer_load_dwordx4 v240,s[48:51],s95 offen offset:3072 sc1 lds\nM v[68:71],a[64:67],v[204:207],v[68:71]\nv_cvt_pk_bf16_f32 v199,v150,v151\nv_cvt_pk_bf16_f32 v200,v136,v137\nM v[68:71],a[96:99],v[220:223],v[68:71]\nv_cvt_pk_bf16_f32 v201,v138,v139\nv_cvt_pk_bf16_f32 v202,v152,v153\n" \
"M v[76:79],a[68:71],v[204:207],v[76:79]\nv_cvt_pk_bf16_f32 v203,v154,v155\ns_add_u32 s95,s95,0x4000\nM v[76:79],a[100:103],v[220:223],v[76:79]\nds_read_b128 a[56:59],v250 offset:14336\nds_read_b128 a[60:63],v250 offset:15360\nM v[84:87],a[72:75],v[204:207],v[84:87]\nv_add_u32 v250,s94,v241\n" \
"M v[84:87],a[104:107],v[220:223],v[84:87]\nM v[92:95],a[76:79],v[204:207],v[92:95]\nM v[92:95],a[108:111],v[220:223],v[92:95]\nglobal_load_dwordx2 a[234:235],v248,s[24:25]\nM v[100:103],a[80:83],v[204:207],v[100:103]\nv_add_u32 v248,s71,v248\nM v[100:103],a[112:115],v[220:223],v[100:103]\nM v[108:111],a[84:87],v[204:207],v[108:111]\n" \
"M v[108:111],a[116:119],v[220:223],v[108:111]\nM v[116:119],a[88:91],v[204:207],v[116:119]\nM v[116:119],a[120:123],v[220:223],v[116:119]\nM v[124:127],a[92:95],v[204:207],v[124:127]\nM v[124:127],a[124:127],v[220:223],v[124:127]\nM a[204:207],a[208:211],v[204:207],a[204:207]\nM a[204:207],a[208:211],v[220:223],a[204:207]\ns_waitcnt lgkmcnt(4)\n" \
"M v[128:131],a[0:3],a[128:131],v[224:227]\ns_add_u32 m0,s94,s64\nv_cvt_pk_bf16_f32 v204,v140,v141\nM v[128:131],a[4:7],a[132:135],v[128:131]\nds_read_b128 a[64:67],v250 offset:0\nds_read_b128 a[96:99],v250 offset:8192\nM v[128:131],a[8:11],a[136:139],v[128:131]\nv_cvt_pk_bf16_f32 v205,v142,v143\n" \
"v_cvt_pk_bf16_f32 v206,v156,v157\nM v[128:131],a[12:15],a[140:143],v[128:131]\nv_cvt_pk_bf16_f32 v207,v158,v159\nv_cvt_pk_bf16_f32 v212,v164,v165\nM v[132:135],a[12:15],a[156:159],v[228:231]\nbuffer_load_dwordx4 v240,s[44:47],s95 offen offset:0 sc1 lds\nM v[132:135],a[8:11],a[152:155],v[132:135]\nv_cvt_pk_bf16_f32 v213,v166,v167\n" \
"v_cvt_pk_bf16_f32 v216,v168,v169\nM v[132:135],a[4:7],a[148:151],v[132:135]\nv_cvt_pk_bf16_f32 v217,v170,v171\nv_cvt_pk_bf16_f32 v214,v180,v181\nM v[132:135],a[0:3],a[144:147],v[132:135]\nds_read_b128 a[68:71],v250 offset:1024\nds_read_b128 a[100:103],v250 offset:9216\nM v[136:139],a[0:3],a[160:163],v[232:235]\n" \
"v_exp_f32 v172,v172\nM v[136:139],a[4:7],a[164:167],v[136:139]\nv_exp_f32 v173,v173\nM v[136:139],a[8:11],a[168:171],v[136:139]\nv_exp_f32 v174,v174\nM v[136:139],a[12:15],a[172:175],v[136:139]\nv_exp_f32 v175,v175\nM v[140:143],a[12:15],a[188:191],v[236:239]\n" \
"v_cvt_pk_bf16_f32 v220,v172,v173\nv_cvt_pk_bf16_f32 v221,v174,v175\nM v[140:143],a[8:11],a[184:187],v[140:143]\nds_read_b128 a[72:75],v250 offset:2048\nds_read_b128 a[104:107],v250 offset:10240\nM v[140:143],a[4:7],a[180:183],v[140:143]\nglobal_load_dwordx2 a[220:221],v248,s[22:23]\nM v[140:143],a[0:3],a[176:179],v[140:143]\n" \
"v_cvt_pk_bf16_f32 v215,v182,v183\ns_add_u32 s94,s94,0x4000\nM v[144:147],a[16:19],a[128:131],v[224:227]\nv_exp_f32 v184,v184\nM v[144:147],a[20:23],a[132:135],v[144:147]\nv_exp_f32 v185,v185\nM v[144:147],a[24:27],a[136:139],v[144:147]\nv_exp_f32 v186,v186\n" \
"M v[144:147],a[28:31],a[140:143],v[144:147]\nds_read_b128 a[76:79],v250 offset:3072\nds_read_b128 a[108:111],v250 offset:11264\nM v[148:151],a[28:31],a[156:159],v[228:231]\nv_exp_f32 v187,v187\nM v[148:151],a[24:27],a[152:155],v[148:151]\nv_cvt_pk_bf16_f32 v218,v184,v185\nv_cvt_pk_bf16_f32 v219,v186,v187\n" \
"M v[148:151],a[20:23],a[148:151],v[148:151]\nbuffer_load_dwordx4 v240,s[44:47],s95 offen offset:1024 sc1 lds\nM v[148:151],a[16:19],a[144:147],v[148:151]\nv_exp_f32 v188,v188\nM v[152:155],a[16:19],a[160:163],v[232:235]\nv_exp_f32 v189,v189\nM v[152:155],a[20:23],a[164:167],v[152:155]\nds_read_b128 a[80:83],v250 offset:4096\n" \
"ds_read_b128 a[112:115],v250 offset:12288\nM v[152:155],a[24:27],a[168:171],v[152:155]\nv_exp_f32 v190,v190\nM v[152:155],a[28:31],a[172:175],v[152:155]\nv_exp_f32 v191,v191\nM v[156:159],a[28:31],a[188:191],v[236:239]\nv_cvt_pk_bf16_f32 v222,v188,v189\nv_cvt_pk_bf16_f32 v223,v190,v191\n" \
"M v[156:159],a[24:27],a[184:187],v[156:159]\nv_exp_f32 v128,v128\nM v[156:159],a[20:23],a[180:183],v[156:159]\nv_exp_f32 v129,v129\nM v[156:159],a[16:19],a[176:179],v[156:159]\nds_read_b128 a[84:87],v250 offset:5120\nds_read_b128 a[116:119],v250 offset:13312\nM v[160:163],a[32:35],a[128:131],v[224:227]\n" \
"global_load_dwordx2 a[236:237],v248,s[24:25]\nM v[160:163],a[36:39],a[132:135],v[160:163]\nv_add_u32 v248,s71,v248\ns_and_b32 s94,s94,0xffff\nM v[160:163],a[40:43],a[136:139],v[160:163]\nv_exp_f32 v130,v130\nM v[160:163],a[44:47],a[140:143],v[160:163]\nv_exp_f32 v131,v131\n" \
"M v[164:167],a[44:47],a[156:159],v[228:231]\nv_exp_f32 v144,v144\nM v[164:167],a[40:43],a[152:155],v[164:167]\nds_read_b128 a[88:91],v250 offset:6144\nds_read_b128 a[120:123],v250 offset:14336\nM v[164:167],a[36:39],a[148:151],v[164:167]\nv_exp_f32 v145,v145\nM v[164:167],a[32:35],a[144:147],v[164:167]\n" \
"v_exp_f32 v146,v146\nM v[168:171],a[32:35],a[160:163],v[232:235]\nbuffer_load_dwordx4 v240,s[44:47],s95 offen offset:2048 sc1 lds\nM v[168:171],a[36:39],a[164:167],v[168:171]\nv_exp_f32 v147,v147\nM v[168:171],a[40:43],a[168:171],v[168:171]\nv_exp_f32 v132,v132\nM v[168:171],a[44:47],a[172:175],v[168:171]\n" \
"ds_read_b128 a[92:95],v250 offset:7168\nds_read_b128 a[124:127],v250 offset:15360\nM v[172:175],a[44:47],a[188:191],v[236:239]\nv_exp_f32 v133,v133\nM v[172:175],a[40:43],a[184:187],v[172:175]\nv_exp_f32 v134,v134\nM v[172:175],a[36:39],a[180:183],v[172:175]\nv_exp_f32 v135,v135\n" \
"M v[172:175],a[32:35],a[176:179],v[172:175]\nv_xor_b32 v250,0x18000,v250\ns_waitcnt lgkmcnt(3)\nM v[176:179],a[48:51],a[128:131],v[224:227]\nv_exp_f32 v148,v148\nM v[176:179],a[52:55],a[132:135],v[176:179]\nv_exp_f32 v149,v149\nM v[176:179],a[56:59],a[136:139],v[176:179]\n" \
"global_load_dwordx2 a[222:223],v248,s[22:23]\nM v[176:179],a[60:63],a[140:143],v[176:179]\nv_exp_f32 v150,v150\nM v[180:183],a[60:63],a[156:159],v[228:231]\nv_exp_f32 v151,v151\nM v[180:183],a[56:59],a[152:155],v[180:183]\nv_exp_f32 v136,v136\nM v[180:183],a[52:55],a[148:151],v[180:183]\n" \
"v_exp_f32 v137,v137\nM v[180:183],a[48:51],a[144:147],v[180:183]\nv_exp_f32 v138,v138\nM v[184:187],a[48:51],a[160:163],v[232:235]\nv_exp_f32 v139,v139\nM v[184:187],a[52:55],a[164:167],v[184:187]\nv_exp_f32 v152,v152\nM v[184:187],a[56:59],a[168:171],v[184:187]\n" \
"buffer_load_dwordx4 v240,s[44:47],s95 offen offset:3072 sc1 lds\nM v[184:187],a[60:63],a[172:175],v[184:187]\nv_exp_f32 v153,v153\nM v[188:191],a[60:63],a[188:191],v[236:239]\nv_exp_f32 v154,v154\nM v[188:191],a[56:59],a[184:187],v[188:191]\nv_exp_f32 v155,v155\nM v[188:191],a[52:55],a[180:183],v[188:191]\n" \
"global_load_dwordx2 a[238:239],v248,s[24:25]\nM v[188:191],a[48:51],a[176:179],v[188:191]\ns_add_u32 m0,m0,0x10000\nv_add_u32 v248,s71,v248\nM v[0:3],a[64:67],v[192:195],v[0:3]\ns_waitcnt vmcnt(16) lgkmcnt(0)\ns_barrier\nM v[0:3],a[96:99],v[208:211],v[0:3]\n" \
"ds_read_b128 a[0:3],v250 offset:0\nds_read_b128 a[4:7],v250 offset:1024\nM v[8:11],a[68:71],v[192:195],v[8:11]\nv_exp_f32 v160,v160\nM v[8:11],a[100:103],v[208:211],v[8:11]\nv_exp_f32 v161,v161\nM v[16:19],a[72:75],v[192:195],v[16:19]\nv_exp_f32 v162,v162\n" \
"M v[16:19],a[104:107],v[208:211],v[16:19]\nbuffer_load_dwordx4 v240,s[48:51],s95 offen offset:0 sc1 lds\nM v[24:27],a[76:79],v[192:195],v[24:27]\nv_exp_f32 v163,v163\nM v[24:27],a[108:111],v[208:211],v[24:27]\nv_exp_f32 v176,v176\nM v[32:35],a[80:83],v[192:195],v[32:35]\nv_exp_f32 v177,v177\n" \
"M v[32:35],a[112:115],v[208:211],v[32:35]\nds_read_b128 a[8:11],v250 offset:2048\nds_read_b128 a[12:15],v250 offset:3072\nM v[40:43],a[84:87],v[192:195],v[40:43]\nv_exp_f32 v178,v178\nM v[40:43],a[116:119],v[208:211],v[40:43]\nv_exp_f32 v179,v179\nM v[48:51],a[88:91],v[192:195],v[48:51]\n" \
"v_exp_f32 v140,v140\nM v[48:51],a[120:123],v[208:211],v[48:51]\nglobal_load_dwordx2 a[224:225],v248,s[22:23]\nM v[56:59],a[92:95],v[192:195],v[56:59]\nv_exp_f32 v141,v141\nM v[56:59],a[124:127],v[208:211],v[56:59]\nv_exp_f32 v142,v142\nM a[192:195],a[208:211],v[192:195],a[192:195]\n" \
"v_exp_f32 v143,v143\nM a[192:195],a[208:211],v[208:211],a[192:195]\nds_read_b128 a[16:19],v250 offset:4096\nds_read_b128 a[20:23],v250 offset:5120\nM v[4:7],a[64:67],v[196:199],v[4:7]\nv_exp_f32 v156,v156\nM v[4:7],a[96:99],v[212:215],v[4:7]\nv_exp_f32 v157,v157\n" \
"M v[12:15],a[68:71],v[196:199],v[12:15]\nv_exp_f32 v158,v158\nM v[12:15],a[100:103],v[212:215],v[12:15]\nbuffer_load_dwordx4 v240,s[48:51],s95 offen offset:1024 sc1 lds\nM v[20:23],a[72:75],v[196:199],v[20:23]\nv_exp_f32 v159,v159\nM v[20:23],a[104:107],v[212:215],v[20:23]\nv_exp_f32 v164,v164\n" \
"M v[28:31],a[76:79],v[196:199],v[28:31]\nv_exp_f32 v165,v165\nM v[28:31],a[108:111],v[212:215],v[28:31]\nds_read_b128 a[24:27],v250 offset:6144\nds_read_b128 a[28:31],v250 offset:7168\nM v[36:39],a[80:83],v[196:199],v[36:39]\nv_exp_f32 v166,v166\nM v[36:39],a[112:115],v[212:215],v[36:39]\n" \
"v_exp_f32 v167,v167\nM v[44:47],a[84:87],v[196:199],v[44:47]\nv_exp_f32 v180,v180\nM v[44:47],a[116:119],v[212:215],v[44:47]\nglobal_load_dwordx2 a[240:241],v248,s[24:25]\nM v[52:55],a[88:91],v[196:199],v[52:55]\nv_add_u32 v248,s71,v248\nv_cvt_pk_bf16_f32 v192,v128,v129\n" \
"M v[52:55],a[120:123],v[212:215],v[52:55]\nv_exp_f32 v181,v181\nM v[60:63],a[92:95],v[196:199],v[60:63]\nv_exp_f32 v182,v182\nM v[60:63],a[124:127],v[212:215],v[60:63]\nds_read_b128 a[32:35],v250 offset:8192\nds_read_b128 a[36:39],v250 offset:9216\nM a[196:199],a[208:211],v[196:199],a[196:199]\n" \
"v_exp_f32 v183,v183\nM a[196:199],a[208:211],v[212:215],a[196:199]\nv_exp_f32 v168,v168\nM v[64:67],a[64:67],v[200:203],v[64:67]\nv_exp_f32 v169,v169\nM v[64:67],a[96:99],v[216:219],v[64:67]\nbuffer_load_dwordx4 v240,s[48:51],s95 offen offset:2048 sc1 lds\nM v[72:75],a[68:71],v[200:203],v[72:75]\n" \
"v_exp_f32 v170,v170\nM v[72:75],a[100:103],v[216:219],v[72:75]\nv_exp_f32 v171,v171\nM v[80:83],a[72:75],v[200:203],v[80:83]\nv_cvt_pk_bf16_f32 v193,v130,v131\nv_cvt_pk_bf16_f32 v194,v144,v145\nM v[80:83],a[104:107],v[216:219],v[80:83]\nds_read_b128 a[40:43],v250 offset:10240\n" \
"ds_read_b128 a[44:47],v250 offset:11264\nM v[88:91],a[76:79],v[200:203],v[88:91]\nv_cvt_pk_bf16_f32 v195,v146,v147\nv_cvt_pk_bf16_f32 v208,v160,v161\nM v[88:91],a[108:111],v[216:219],v[88:91]\nv_cvt_pk_bf16_f32 v209,v162,v163\nv_cvt_pk_bf16_f32 v210,v176,v177\nM v[96:99],a[80:83],v[200:203],v[96:99]\n" \
"v_cvt_pk_bf16_f32 v211,v178,v179\nv_cvt_pk_bf16_f32 v196,v132,v133\nM v[96:99],a[112:115],v[216:219],v[96:99]\nglobal_load_dwordx2 a[226:227],v248,s[22:23]\nM v[104:107],a[84:87],v[200:203],v[104:107]\nv_cvt_pk_bf16_f32 v197,v134,v135\nv_cvt_pk_bf16_f32 v198,v148,v149\nM v[104:107],a[116:119],v[216:219],v[104:107]\n" \
"v_cvt_pk_bf16_f32 v199,v150,v151\nM v[112:115],a[88:91],v[200:203],v[112:115]\nM v[112:115],a[120:123],v[216:219],v[112:115]\nds_read_b128 a[48:51],v250 offset:12288\nds_read_b128 a[52:55],v250 offset:13312\nM v[120:123],a[92:95],v[200:203],v[120:123]\nM v[120:123],a[124:127],v[216:219],v[120:123]\nM a[200:203],a[208:211],v[200:203],a[200:203]\n" \
"v_cvt_pk_bf16_f32 v200,v136,v137\nv_cvt_pk_bf16_f32 v201,v138,v139\nM a[200:203],a[208:211],v[216:219],a[200:203]\nbuffer_load_dwordx4 v240,s[48:51],s95 offen offset:3072 sc1 lds\nM v[68:71],a[64:67],v[204:207],v[68:71]\nv_cvt_pk_bf16_f32 v202,v152,v153\nv_cvt_pk_bf16_f32 v203,v154,v155\nM v[68:71],a[96:99],v[220:223],v[68:71]\n" \
"s_add_u32 s95,s95,0x4000\nM v[76:79],a[68:71],v[204:207],v[76:79]\nM v[76:79],a[100:103],v[220:223],v[76:79]\nds_read_b128 a[56:59],v250 offset:14336\nds_read_b128 a[60:63],v250 offset:15360\nM v[84:87],a[72:75],v[204:207],v[84:87]\nv_add_u32 v250,s94,v241\nM v[84:87],a[104:107],v[220:223],v[84:87]\n" \
"M v[92:95],a[76:79],v[204:207],v[92:95]\nM v[92:95],a[108:111],v[220:223],v[92:95]\nglobal_load_dwordx2 a[242:243],v248,s[24:25]\nM v[100:103],a[80:83],v[204:207],v[100:103]\nv_add_u32 v248,s71,v248\nM v[100:103],a[112:115],v[220:223],v[100:103]\nM v[108:111],a[84:87],v[204:207],v[108:111]\nM v[108:111],a[116:119],v[220:223],v[108:111]\n" \
"M v[116:119],a[88:91],v[204:207],v[116:119]\nM v[116:119],a[120:123],v[220:223],v[116:119]\nM v[124:127],a[92:95],v[204:207],v[124:127]\nM v[124:127],a[124:127],v[220:223],v[124:127]\nM a[204:207],a[208:211],v[204:207],a[204:207]\nM a[204:207],a[208:211],v[220:223],a[204:207]\ns_cmp_eq_u32 s62,7\ns_cbranch_scc0 .Ltrans\n" \
"s_waitcnt lgkmcnt(4)\nM v[136:139],a[0:3],a[160:163],v[232:235]\ns_add_u32 m0,s94,s64\nv_cvt_pk_bf16_f32 v204,v140,v141\nM v[136:139],a[4:7],a[164:167],v[136:139]\nds_read_b128 a[64:67],v250 offset:0\nds_read_b128 a[96:99],v250 offset:8192\nM v[136:139],a[8:11],a[168:171],v[136:139]\n" \
"buffer_load_dwordx4 v240,s[44:47],s95 offen offset:0 sc1 lds\nM v[136:139],a[12:15],a[172:175],v[136:139]\nv_cvt_pk_bf16_f32 v205,v142,v143\nv_cvt_pk_bf16_f32 v206,v156,v157\nM v[140:143],a[12:15],a[188:191],v[236:239]\nds_read_b128 a[68:71],v250 offset:1024\nds_read_b128 a[100:103],v250 offset:9216\nM v[140:143],a[8:11],a[184:187],v[140:143]\n" \
"buffer_store_dwordx4 a[248:251],v253,s[52:55],s33 offen offset:0 sc1\nM v[140:143],a[4:7],a[180:183],v[140:143]\nv_cvt_pk_bf16_f32 v207,v158,v159\nv_cvt_pk_bf16_f32 v216,v168,v169\nM v[140:143],a[0:3],a[176:179],v[140:143]\nds_read_b128 a[72:75],v250 offset:2048\nds_read_b128 a[104:107],v250 offset:10240\nM v[152:155],a[16:19],a[160:163],v[232:235]\n" \
"v_cvt_pk_bf16_f32 v217,v170,v171\ns_add_u32 s71,s33,16384\nM v[152:155],a[20:23],a[164:167],v[152:155]\nv_exp_f32 v172,v172\nM v[152:155],a[24:27],a[168:171],v[152:155]\nds_read_b128 a[76:79],v250 offset:3072\nds_read_b128 a[108:111],v250 offset:11264\nM v[152:155],a[28:31],a[172:175],v[152:155]\n" \
"buffer_load_dwordx4 v240,s[44:47],s95 offen offset:1024 sc1 lds\nM v[156:159],a[28:31],a[188:191],v[236:239]\nv_exp_f32 v173,v173\nM v[156:159],a[24:27],a[184:187],v[156:159]\nds_read_b128 a[80:83],v250 offset:4096\nds_read_b128 a[112:115],v250 offset:12288\nM v[156:159],a[20:23],a[180:183],v[156:159]\nbuffer_store_dwordx4 a[248:251],v253,s[52:55],s33 offen offset:1024 sc1\n" \
"M v[156:159],a[16:19],a[176:179],v[156:159]\nv_exp_f32 v174,v174\nM v[168:171],a[32:35],a[160:163],v[232:235]\nds_read_b128 a[84:87],v250 offset:5120\nds_read_b128 a[116:119],v250 offset:13312\nM v[168:171],a[36:39],a[164:167],v[168:171]\nv_exp_f32 v175,v175\nM v[168:171],a[40:43],a[168:171],v[168:171]\n" \
"v_cvt_pk_bf16_f32 v220,v172,v173\nv_cvt_pk_bf16_f32 v221,v174,v175\nM v[168:171],a[44:47],a[172:175],v[168:171]\nds_read_b128 a[88:91],v250 offset:6144\nds_read_b128 a[120:123],v250 offset:14336\nM v[172:175],a[44:47],a[188:191],v[236:239]\nbuffer_load_dwordx4 v240,s[44:47],s95 offen offset:2048 sc1 lds\nM v[172:175],a[40:43],a[184:187],v[172:175]\n" \
"v_exp_f32 v184,v184\nM v[172:175],a[36:39],a[180:183],v[172:175]\nv_exp_f32 v185,v185\nv_exp_f32 v186,v186\nv_exp_f32 v187,v187\nds_read_b128 a[92:95],v250 offset:7168\nds_read_b128 a[124:127],v250 offset:15360\nM v[172:175],a[32:35],a[176:179],v[172:175]\n" \
"buffer_store_dwordx4 a[248:251],v253,s[52:55],s33 offen offset:2048 sc1\nv_cvt_pk_bf16_f32 v218,v184,v185\nv_cvt_pk_bf16_f32 v219,v186,v187\ns_waitcnt lgkmcnt(6)\nM v[184:187],a[48:51],a[160:163],v[232:235]\nv_exp_f32 v188,v188\nM v[184:187],a[52:55],a[164:167],v[184:187]\nv_exp_f32 v189,v189\n" \
"M v[184:187],a[56:59],a[168:171],v[184:187]\nv_exp_f32 v190,v190\nv_exp_f32 v191,v191\nM v[184:187],a[60:63],a[172:175],v[184:187]\nv_cvt_pk_bf16_f32 v222,v188,v189\nv_cvt_pk_bf16_f32 v223,v190,v191\nM v[188:191],a[60:63],a[188:191],v[236:239]\nv_xor_b32 v250,0x18000,v250\n" \
"s_add_u32 s70,s78,16384\nM v[188:191],a[56:59],a[184:187],v[188:191]\nbuffer_load_dwordx4 v240,s[44:47],s95 offen offset:3072 sc1 lds\nM v[188:191],a[52:55],a[180:183],v[188:191]\ns_add_u32 m0,m0,0x10000\nv_cvt_pk_bf16_f32 v212,v164,v165\nM v[188:191],a[48:51],a[176:179],v[188:191]\nv_cvt_pk_bf16_f32 v213,v166,v167\n" \
"v_cvt_pk_bf16_f32 v214,v180,v181\nM v[64:67],a[64:67],v[200:203],v[64:67]\nbuffer_store_dwordx4 a[248:251],v253,s[52:55],s33 offen offset:3072 sc1\nM v[64:67],a[96:99],v[216:219],v[64:67]\nv_cvt_pk_bf16_f32 v215,v182,v183\ns_add_u32 s94,s94,0x4000\nM v[72:75],a[68:71],v[200:203],v[72:75]\ns_waitcnt vmcnt(16) lgkmcnt(0)\n" \
"s_barrier\nM v[72:75],a[100:103],v[216:219],v[72:75]\nds_read_b128 a[0:3],v250 offset:0\nds_read_b128 a[4:7],v250 offset:1024\nM v[80:83],a[72:75],v[200:203],v[80:83]\nv_exp_f32 v136,v136\nM v[80:83],a[104:107],v[216:219],v[80:83]\nbuffer_store_dwordx4 a[248:251],v253,s[52:55],s71 offen offset:0 sc1\n" \
"M v[88:91],a[76:79],v[200:203],v[88:91]\nv_exp_f32 v137,v137\nM v[88:91],a[108:111],v[216:219],v[88:91]\nbuffer_load_dwordx4 v240,s[48:51],s95 offen offset:0 sc1 lds\nM v[96:99],a[80:83],v[200:203],v[96:99]\nv_exp_f32 v138,v138\nM v[96:99],a[112:115],v[216:219],v[96:99]\nbuffer_store_dwordx4 a[248:251],v253,s[52:55],s71 offen offset:1024 sc1\n" \
"M v[104:107],a[84:87],v[200:203],v[104:107]\nv_exp_f32 v139,v139\nM v[104:107],a[116:119],v[216:219],v[104:107]\nds_read_b128 a[8:11],v250 offset:2048\nds_read_b128 a[12:15],v250 offset:3072\nM v[112:115],a[88:91],v[200:203],v[112:115]\nv_exp_f32 v152,v152\nM v[112:115],a[120:123],v[216:219],v[112:115]\n" \
"buffer_store_dwordx4 a[248:251],v253,s[52:55],s71 offen offset:2048 sc1\nM v[120:123],a[92:95],v[200:203],v[120:123]\nv_exp_f32 v153,v153\nM v[120:123],a[124:127],v[216:219],v[120:123]\nv_exp_f32 v154,v154\nM a[200:203],a[208:211],v[200:203],a[200:203]\nv_exp_f32 v155,v155\nM a[200:203],a[208:211],v[216:219],a[200:203]\n" \
"buffer_store_dwordx4 a[248:251],v253,s[52:55],s71 offen offset:3072 sc1\nM v[68:71],a[64:67],v[204:207],v[68:71]\nv_exp_f32 v168,v168\nM v[68:71],a[96:99],v[220:223],v[68:71]\nds_read_b128 a[16:19],v250 offset:4096\nds_read_b128 a[20:23],v250 offset:5120\nM v[76:79],a[68:71],v[204:207],v[76:79]\nv_exp_f32 v169,v169\n" \
"M v[76:79],a[100:103],v[220:223],v[76:79]\nv_exp_f32 v170,v170\nM v[84:87],a[72:75],v[204:207],v[84:87]\nv_exp_f32 v171,v171\nM v[84:87],a[104:107],v[220:223],v[84:87]\nbuffer_load_dwordx4 v240,s[48:51],s95 offen offset:1024 sc1 lds\nM v[92:95],a[76:79],v[204:207],v[92:95]\nv_exp_f32 v184,v184\n" \
"M v[92:95],a[108:111],v[220:223],v[92:95]\nbuffer_store_dwordx4 a[248:251],v253,s[52:55],s78 offen offset:0 sc1\nM v[100:103],a[80:83],v[204:207],v[100:103]\nv_exp_f32 v185,v185\nM v[100:103],a[112:115],v[220:223],v[100:103]\nds_read_b128 a[24:27],v250 offset:6144\nds_read_b128 a[28:31],v250 offset:7168\nM v[108:111],a[84:87],v[204:207],v[108:111]\n" \
"v_exp_f32 v186,v186\nM v[108:111],a[116:119],v[220:223],v[108:111]\nbuffer_store_dwordx4 a[248:251],v253,s[52:55],s78 offen offset:1024 sc1\nM v[116:119],a[88:91],v[204:207],v[116:119]\nv_exp_f32 v187,v187\nM v[116:119],a[120:123],v[220:223],v[116:119]\ns_and_b32 s94,s94,0xffff\nv_cvt_pk_bf16_f32 v200,v136,v137\n" \
"M v[124:127],a[92:95],v[204:207],v[124:127]\nv_exp_f32 v140,v140\nM v[124:127],a[124:127],v[220:223],v[124:127]\nbuffer_store_dwordx4 a[248:251],v253,s[52:55],s78 offen offset:2048 sc1\nM a[204:207],a[208:211],v[204:207],a[204:207]\nv_exp_f32 v141,v141\nM a[204:207],a[208:211],v[220:223],a[204:207]\nds_read_b128 a[32:35],v250 offset:8192\n" \
"ds_read_b128 a[36:39],v250 offset:9216\nM v[0:3],a[64:67],v[192:195],v[0:3]\nv_exp_f32 v142,v142\nM v[0:3],a[96:99],v[208:211],v[0:3]\nv_exp_f32 v143,v143\nM v[8:11],a[68:71],v[192:195],v[8:11]\nv_exp_f32 v156,v156\nM v[8:11],a[100:103],v[208:211],v[8:11]\n" \
"buffer_load_dwordx4 v240,s[48:51],s95 offen offset:2048 sc1 lds\nM v[16:19],a[72:75],v[192:195],v[16:19]\nv_exp_f32 v157,v157\nM v[16:19],a[104:107],v[208:211],v[16:19]\nbuffer_store_dwordx4 a[248:251],v253,s[52:55],s78 offen offset:3072 sc1\nM v[24:27],a[76:79],v[192:195],v[24:27]\nv_exp_f32 v158,v158\nM v[24:27],a[108:111],v[208:211],v[24:27]\n" \
"ds_read_b128 a[40:43],v250 offset:10240\nds_read_b128 a[44:47],v250 offset:11264\nM v[32:35],a[80:83],v[192:195],v[32:35]\nv_exp_f32 v159,v159\nM v[32:35],a[112:115],v[208:211],v[32:35]\nbuffer_store_dwordx4 a[248:251],v253,s[52:55],s70 offen offset:0 sc1\nM v[40:43],a[84:87],v[192:195],v[40:43]\nv_cvt_pk_bf16_f32 v201,v138,v139\n" \
"v_cvt_pk_bf16_f32 v202,v152,v153\nM v[40:43],a[116:119],v[208:211],v[40:43]\nv_cvt_pk_bf16_f32 v203,v154,v155\nv_cvt_pk_bf16_f32 v216,v168,v169\nM v[48:51],a[88:91],v[192:195],v[48:51]\nv_cvt_pk_bf16_f32 v217,v170,v171\nv_cvt_pk_bf16_f32 v218,v184,v185\nM v[48:51],a[120:123],v[208:211],v[48:51]\n" \
"buffer_store_dwordx4 a[248:251],v253,s[52:55],s70 offen offset:1024 sc1\nM v[56:59],a[92:95],v[192:195],v[56:59]\nv_cvt_pk_bf16_f32 v219,v186,v187\nM v[56:59],a[124:127],v[208:211],v[56:59]\nds_read_b128 a[48:51],v250 offset:12288\nds_read_b128 a[52:55],v250 offset:13312\nM a[192:195],a[208:211],v[192:195],a[192:195]\nM a[192:195],a[208:211],v[208:211],a[192:195]\n" \
"M v[4:7],a[64:67],v[196:199],v[4:7]\nM v[4:7],a[96:99],v[212:215],v[4:7]\nbuffer_load_dwordx4 v240,s[48:51],s95 offen offset:3072 sc1 lds\nM v[12:15],a[68:71],v[196:199],v[12:15]\ns_add_u32 s95,s95,0xffff4000\nM v[12:15],a[100:103],v[212:215],v[12:15]\nbuffer_store_dwordx4 a[248:251],v253,s[52:55],s70 offen offset:2048 sc1\nM v[20:23],a[72:75],v[196:199],v[20:23]\n" \
"M v[20:23],a[104:107],v[212:215],v[20:23]\nds_read_b128 a[56:59],v250 offset:14336\nds_read_b128 a[60:63],v250 offset:15360\nM v[28:31],a[76:79],v[196:199],v[28:31]\nv_add_u32 v250,s94,v241\nM v[28:31],a[108:111],v[212:215],v[28:31]\nbuffer_store_dwordx4 a[248:251],v253,s[52:55],s70 offen offset:3072 sc1\nM v[36:39],a[80:83],v[196:199],v[36:39]\n" \
"M v[36:39],a[112:115],v[212:215],v[36:39]\nM v[44:47],a[84:87],v[196:199],v[44:47]\nM v[44:47],a[116:119],v[212:215],v[44:47]\nM v[52:55],a[88:91],v[196:199],v[52:55]\nM v[52:55],a[120:123],v[212:215],v[52:55]\nM v[60:63],a[92:95],v[196:199],v[60:63]\nM v[60:63],a[124:127],v[212:215],v[60:63]\nM a[196:199],a[208:211],v[196:199],a[196:199]\n" \
"M a[196:199],a[208:211],v[212:215],a[196:199]\n.Lsl:\ns_waitcnt lgkmcnt(4)\nM v[136:139],a[0:3],a[160:163],v[232:235]\ns_add_u32 m0,s94,s64\nv_cvt_pk_bf16_f32 v204,v140,v141\nM v[136:139],a[4:7],a[164:167],v[136:139]\nds_read_b128 a[64:67],v250 offset:0\n" \
"ds_read_b128 a[96:99],v250 offset:8192\nM v[136:139],a[8:11],a[168:171],v[136:139]\nbuffer_load_dwordx4 v240,s[44:47],s95 offen offset:0 sc1 lds\nM v[136:139],a[12:15],a[172:175],v[136:139]\nv_cvt_pk_bf16_f32 v205,v142,v143\nv_cvt_pk_bf16_f32 v206,v156,v157\nM v[140:143],a[12:15],a[188:191],v[236:239]\nds_read_b128 a[68:71],v250 offset:1024\n" \
"ds_read_b128 a[100:103],v250 offset:9216\nM v[140:143],a[8:11],a[184:187],v[140:143]\nv_cvt_pk_bf16_f32 v207,v158,v159\ns_add_u32 s94,s94,0x4000\nM v[140:143],a[4:7],a[180:183],v[140:143]\nv_exp_f32 v172,v172\nM v[140:143],a[0:3],a[176:179],v[140:143]\nds_read_b128 a[72:75],v250 offset:2048\n" \
"ds_read_b128 a[104:107],v250 offset:10240\nM v[152:155],a[16:19],a[160:163],v[232:235]\nv_exp_f32 v173,v173\nM v[152:155],a[20:23],a[164:167],v[152:155]\nv_exp_f32 v174,v174\nM v[152:155],a[24:27],a[168:171],v[152:155]\nds_read_b128 a[76:79],v250 offset:3072\nds_read_b128 a[108:111],v250 offset:11264\n" \
"M v[152:155],a[28:31],a[172:175],v[152:155]\nbuffer_load_dwordx4 v240,s[44:47],s95 offen offset:1024 sc1 lds\nM v[156:159],a[28:31],a[188:191],v[236:239]\nv_exp_f32 v175,v175\nM v[156:159],a[24:27],a[184:187],v[156:159]\nds_read_b128 a[80:83],v250 offset:4096\nds_read_b128 a[112:115],v250 offset:12288\nM v[156:159],a[20:23],a[180:183],v[156:159]\n" \
"v_cvt_pk_bf16_f32 v220,v172,v173\nv_cvt_pk_bf16_f32 v221,v174,v175\nM v[156:159],a[16:19],a[176:179],v[156:159]\nv_exp_f32 v188,v188\nM v[168:171],a[32:35],a[160:163],v[232:235]\nds_read_b128 a[84:87],v250 offset:5120\nds_read_b128 a[116:119],v250 offset:13312\nM v[168:171],a[36:39],a[164:167],v[168:171]\n" \
"v_exp_f32 v189,v189\nM v[168:171],a[40:43],a[168:171],v[168:171]\nv_exp_f32 v190,v190\nM v[168:171],a[44:47],a[172:175],v[168:171]\nds_read_b128 a[88:91],v250 offset:6144\nds_read_b128 a[120:123],v250 offset:14336\nM v[172:175],a[44:47],a[188:191],v[236:239]\nbuffer_load_dwordx4 v240,s[44:47],s95 offen offset:2048 sc1 lds\n" \
"M v[172:175],a[40:43],a[184:187],v[172:175]\nv_exp_f32 v191,v191\nM v[172:175],a[36:39],a[180:183],v[172:175]\nds_read_b128 a[92:95],v250 offset:7168\nds_read_b128 a[124:127],v250 offset:15360\nM v[172:175],a[32:35],a[176:179],v[172:175]\nv_cvt_pk_bf16_f32 v222,v188,v189\ns_waitcnt lgkmcnt(7)\n" \
"M v[184:187],a[48:51],a[160:163],v[232:235]\nv_cvt_pk_bf16_f32 v223,v190,v191\nv_xor_b32 v250,0x18000,v250\nM v[184:187],a[52:55],a[164:167],v[184:187]\nv_exp_f32 v136,v136\nM v[184:187],a[56:59],a[168:171],v[184:187]\nv_exp_f32 v137,v137\nM v[184:187],a[60:63],a[172:175],v[184:187]\n" \
"v_exp_f32 v138,v138\nM v[188:191],a[60:63],a[188:191],v[236:239]\nv_exp_f32 v139,v139\nM v[188:191],a[56:59],a[184:187],v[188:191]\nbuffer_load_dwordx4 v240,s[44:47],s95 offen offset:3072 sc1 lds\nM v[188:191],a[52:55],a[180:183],v[188:191]\ns_add_u32 m0,m0,0x10000\ns_and_b32 s94,s94,0xffff\n" \
"M v[188:191],a[48:51],a[176:179],v[188:191]\nv_exp_f32 v152,v152\nM v[64:67],a[64:67],v[200:203],v[64:67]\nv_exp_f32 v153,v153\nM v[64:67],a[96:99],v[216:219],v[64:67]\nv_exp_f32 v154,v154\nM v[72:75],a[68:71],v[200:203],v[72:75]\ns_waitcnt vmcnt(16) lgkmcnt(0)\n" \
"s_barrier\nM v[72:75],a[100:103],v[216:219],v[72:75]\nds_read_b128 a[0:3],v250 offset:0\nds_read_b128 a[4:7],v250 offset:1024\nM v[80:83],a[72:75],v[200:203],v[80:83]\nv_exp_f32 v155,v155\nM v[80:83],a[104:107],v[216:219],v[80:83]\nbuffer_load_dwordx4 v240,s[48:51],s95 offen offset:0 sc1 lds\n" \
"M v[88:91],a[76:79],v[200:203],v[88:91]\nv_exp_f32 v168,v168\nM v[88:91],a[108:111],v[216:219],v[88:91]\nds_read_b128 a[8:11],v250 offset:2048\nds_read_b128 a[12:15],v250 offset:3072\nM v[96:99],a[80:83],v[200:203],v[96:99]\nv_exp_f32 v169,v169\nM v[96:99],a[112:115],v[216:219],v[96:99]\n" \
"v_exp_f32 v170,v170\nM v[104:107],a[84:87],v[200:203],v[104:107]\nv_exp_f32 v171,v171\nM v[104:107],a[116:119],v[216:219],v[104:107]\nds_read_b128 a[16:19],v250 offset:4096\nds_read_b128 a[20:23],v250 offset:5120\nM v[112:115],a[88:91],v[200:203],v[112:115]\nv_exp_f32 v184,v184\n" \
"M v[112:115],a[120:123],v[216:219],v[112:115]\nbuffer_load_dwordx4 v240,s[48:51],s95 offen offset:1024 sc1 lds\nM v[120:123],a[92:95],v[200:203],v[120:123]\nv_exp_f32 v185,v185\nM v[120:123],a[124:127],v[216:219],v[120:123]\nds_read_b128 a[24:27],v250 offset:6144\nds_read_b128 a[28:31],v250 offset:7168\nM a[200:203],a[208:211],v[200:203],a[200:203]\n" \
"v_exp_f32 v186,v186\nM a[200:203],a[208:211],v[216:219],a[200:203]\nv_exp_f32 v187,v187\nM v[68:71],a[64:67],v[204:207],v[68:71]\nv_exp_f32 v140,v140\nM v[68:71],a[96:99],v[220:223],v[68:71]\nds_read_b128 a[32:35],v250 offset:8192\nds_read_b128 a[36:39],v250 offset:9216\n" \
"M v[76:79],a[68:71],v[204:207],v[76:79]\nv_exp_f32 v141,v141\nM v[76:79],a[100:103],v[220:223],v[76:79]\nbuffer_load_dwordx4 v240,s[48:51],s95 offen offset:2048 sc1 lds\nM v[84:87],a[72:75],v[204:207],v[84:87]\nv_exp_f32 v142,v142\nM v[84:87],a[104:107],v[220:223],v[84:87]\nds_read_b128 a[40:43],v250 offset:10240\n" \
"ds_read_b128 a[44:47],v250 offset:11264\nM v[92:95],a[76:79],v[204:207],v[92:95]\nv_exp_f32 v143,v143\nM v[92:95],a[108:111],v[220:223],v[92:95]\nv_exp_f32 v156,v156\nM v[100:103],a[80:83],v[204:207],v[100:103]\nv_exp_f32 v157,v157\nM v[100:103],a[112:115],v[220:223],v[100:103]\n" \
"ds_read_b128 a[48:51],v250 offset:12288\nds_read_b128 a[52:55],v250 offset:13312\nM v[108:111],a[84:87],v[204:207],v[108:111]\nv_exp_f32 v158,v158\nM v[108:111],a[116:119],v[220:223],v[108:111]\nbuffer_load_dwordx4 v240,s[48:51],s95 offen offset:3072 sc1 lds\nM v[116:119],a[88:91],v[204:207],v[116:119]\nv_exp_f32 v159,v159\n" \
"M v[116:119],a[120:123],v[220:223],v[116:119]\nds_read_b128 a[56:59],v250 offset:14336\nds_read_b128 a[60:63],v250 offset:15360\nM v[124:127],a[92:95],v[204:207],v[124:127]\nv_cvt_pk_bf16_f32 v200,v136,v137\nv_cvt_pk_bf16_f32 v201,v138,v139\nM v[124:127],a[124:127],v[220:223],v[124:127]\nv_cvt_pk_bf16_f32 v202,v152,v153\n" \
"v_cvt_pk_bf16_f32 v203,v154,v155\nM a[204:207],a[208:211],v[204:207],a[204:207]\nv_cvt_pk_bf16_f32 v216,v168,v169\nv_cvt_pk_bf16_f32 v217,v170,v171\nv_cvt_pk_bf16_f32 v218,v184,v185\nv_cvt_pk_bf16_f32 v219,v186,v187\nv_add_u32 v250,s94,v241\ns_add_u32 s95,s95,0x4000\n" \
"M a[204:207],a[208:211],v[220:223],a[204:207]\ns_cmp_lg_u32 s95,s96\ns_cbranch_scc1 .Lsl\ns_waitcnt lgkmcnt(4)\nM v[128:131],a[0:3],a[128:131],v[224:227]\ns_add_u32 m0,s94,s64\nv_cvt_pk_bf16_f32 v204,v140,v141\nM v[128:131],a[4:7],a[132:135],v[128:131]\n" \
"ds_read_b128 a[64:67],v250 offset:0\nds_read_b128 a[96:99],v250 offset:8192\nM v[128:131],a[8:11],a[136:139],v[128:131]\nbuffer_load_dwordx4 v240,s[44:47],s95 offen offset:0 sc1 lds\nM v[128:131],a[12:15],a[140:143],v[128:131]\nv_exp_f32 v188,v188\nM v[132:135],a[12:15],a[156:159],v[228:231]\nds_read_b128 a[68:71],v250 offset:1024\n" \
"ds_read_b128 a[100:103],v250 offset:9216\nM v[132:135],a[8:11],a[152:155],v[132:135]\nv_exp_f32 v189,v189\nM v[132:135],a[4:7],a[148:151],v[132:135]\nv_exp_f32 v190,v190\nM v[132:135],a[0:3],a[144:147],v[132:135]\nds_read_b128 a[72:75],v250 offset:2048\nds_read_b128 a[104:107],v250 offset:10240\n" \
"M v[144:147],a[16:19],a[128:131],v[224:227]\nv_exp_f32 v191,v191\nM v[144:147],a[20:23],a[132:135],v[144:147]\nv_exp_f32 v172,v172\nM v[144:147],a[24:27],a[136:139],v[144:147]\nds_read_b128 a[76:79],v250 offset:3072\nds_read_b128 a[108:111],v250 offset:11264\nM v[144:147],a[28:31],a[140:143],v[144:147]\n" \
"buffer_load_dwordx4 v240,s[44:47],s95 offen offset:1024 sc1 lds\nM v[148:151],a[28:31],a[156:159],v[228:231]\nv_exp_f32 v173,v173\nM v[148:151],a[24:27],a[152:155],v[148:151]\nds_read_b128 a[80:83],v250 offset:4096\nds_read_b128 a[112:115],v250 offset:12288\nM v[148:151],a[20:23],a[148:151],v[148:151]\nv_exp_f32 v174,v174\n" \
"M v[148:151],a[16:19],a[144:147],v[148:151]\nv_exp_f32 v175,v175\nM v[160:163],a[32:35],a[128:131],v[224:227]\nds_read_b128 a[84:87],v250 offset:5120\nds_read_b128 a[116:119],v250 offset:13312\nM v[160:163],a[36:39],a[132:135],v[160:163]\nv_cvt_pk_bf16_f32 v205,v142,v143\nv_cvt_pk_bf16_f32 v206,v156,v157\n" \
"M v[160:163],a[40:43],a[136:139],v[160:163]\nv_cvt_pk_bf16_f32 v207,v158,v159\nv_cvt_pk_bf16_f32 v220,v172,v173\nM v[160:163],a[44:47],a[140:143],v[160:163]\nds_read_b128 a[88:91],v250 offset:6144\nds_read_b128 a[120:123],v250 offset:14336\nM v[164:167],a[44:47],a[156:159],v[228:231]\nbuffer_load_dwordx4 v240,s[44:47],s95 offen offset:2048 sc1 lds\n" \
"M v[164:167],a[40:43],a[152:155],v[164:167]\nv_cvt_pk_bf16_f32 v221,v174,v175\nv_cvt_pk_bf16_f32 v222,v188,v189\nM v[164:167],a[36:39],a[148:151],v[164:167]\nds_read_b128 a[92:95],v250 offset:7168\nds_read_b128 a[124:127],v250 offset:15360\nM v[164:167],a[32:35],a[144:147],v[164:167]\nv_xor_b32 v250,0x18000,v250\n" \
"s_waitcnt lgkmcnt(7)\nM v[176:179],a[48:51],a[128:131],v[224:227]\nv_cvt_pk_bf16_f32 v223,v190,v191\ns_add_u32 s94,s94,0x4000\nM v[176:179],a[52:55],a[132:135],v[176:179]\nv_exp_f32 v128,v128\nM v[176:179],a[56:59],a[136:139],v[176:179]\nv_exp_f32 v129,v129\n" \
"M v[176:179],a[60:63],a[140:143],v[176:179]\nv_exp_f32 v130,v130\nM v[180:183],a[60:63],a[156:159],v[228:231]\nv_exp_f32 v131,v131\nM v[180:183],a[56:59],a[152:155],v[180:183]\nbuffer_load_dwordx4 v240,s[44:47],s95 offen offset:3072 sc1 lds\nM v[180:183],a[52:55],a[148:151],v[180:183]\ns_add_u32 m0,m0,0x10000\n" \
"s_and_b32 s94,s94,0xffff\nM v[180:183],a[48:51],a[144:147],v[180:183]\nv_exp_f32 v144,v144\nM v[64:67],a[64:67],v[200:203],v[64:67]\nv_exp_f32 v145,v145\nM v[64:67],a[96:99],v[216:219],v[64:67]\nv_exp_f32 v146,v146\nM v[72:75],a[68:71],v[200:203],v[72:75]\n" \
"s_waitcnt vmcnt(16) lgkmcnt(0)\ns_barrier\nM v[72:75],a[100:103],v[216:219],v[72:75]\nds_read_b128 a[0:3],v250 offset:0\nds_read_b128 a[4:7],v250 offset:1024\nM v[80:83],a[72:75],v[200:203],v[80:83]\nv_exp_f32 v147,v147\nM v[80:83],a[104:107],v[216:219],v[80:83]\n" \
"buffer_load_dwordx4 v240,s[48:51],s95 offen offset:0 sc1 lds\nM v[88:91],a[76:79],v[200:203],v[88:91]\nv_exp_f32 v160,v160\nM v[88:91],a[108:111],v[216:219],v[88:91]\nds_read_b128 a[8:11],v250 offset:2048\nds_read_b128 a[12:15],v250 offset:3072\nM v[96:99],a[80:83],v[200:203],v[96:99]\nv_exp_f32 v161,v161\n" \
"M v[96:99],a[112:115],v[216:219],v[96:99]\nv_exp_f32 v162,v162\nM v[104:107],a[84:87],v[200:203],v[104:107]\nv_exp_f32 v163,v163\nM v[104:107],a[116:119],v[216:219],v[104:107]\nds_read_b128 a[16:19],v250 offset:4096\nds_read_b128 a[20:23],v250 offset:5120\nM v[112:115],a[88:91],v[200:203],v[112:115]\n" \
"v_exp_f32 v176,v176\nM v[112:115],a[120:123],v[216:219],v[112:115]\nbuffer_load_dwordx4 v240,s[48:51],s95 offen offset:1024 sc1 lds\nM v[120:123],a[92:95],v[200:203],v[120:123]\nv_exp_f32 v177,v177\nM v[120:123],a[124:127],v[216:219],v[120:123]\nds_read_b128 a[24:27],v250 offset:6144\nds_read_b128 a[28:31],v250 offset:7168\n" \
"M a[200:203],a[208:211],v[200:203],a[200:203]\nv_exp_f32 v178,v178\nM a[200:203],a[208:211],v[216:219],a[200:203]\nv_exp_f32 v179,v179\nM v[68:71],a[64:67],v[204:207],v[68:71]\nv_exp_f32 v132,v132\nM v[68:71],a[96:99],v[220:223],v[68:71]\nds_read_b128 a[32:35],v250 offset:8192\n" \
"ds_read_b128 a[36:39],v250 offset:9216\nM v[76:79],a[68:71],v[204:207],v[76:79]\nv_exp_f32 v133,v133\nM v[76:79],a[100:103],v[220:223],v[76:79]\nbuffer_load_dwordx4 v240,s[48:51],s95 offen offset:2048 sc1 lds\nM v[84:87],a[72:75],v[204:207],v[84:87]\nv_exp_f32 v134,v134\nM v[84:87],a[104:107],v[220:223],v[84:87]\n" \
"ds_read_b128 a[40:43],v250 offset:10240\nds_read_b128 a[44:47],v250 offset:11264\nM v[92:95],a[76:79],v[204:207],v[92:95]\nv_exp_f32 v135,v135\nM v[92:95],a[108:111],v[220:223],v[92:95]\nv_exp_f32 v148,v148\nM v[100:103],a[80:83],v[204:207],v[100:103]\nv_exp_f32 v149,v149\n" \
"M v[100:103],a[112:115],v[220:223],v[100:103]\nds_read_b128 a[48:51],v250 offset:12288\nds_read_b128 a[52:55],v250 offset:13312\nM v[108:111],a[84:87],v[204:207],v[108:111]\nv_exp_f32 v150,v150\nM v[108:111],a[116:119],v[220:223],v[108:111]\nbuffer_load_dwordx4 v240,s[48:51],s95 offen offset:3072 sc1 lds\nM v[116:119],a[88:91],v[204:207],v[116:119]\n" \
"v_exp_f32 v151,v151\nM v[116:119],a[120:123],v[220:223],v[116:119]\nds_read_b128 a[56:59],v250 offset:14336\nds_read_b128 a[60:63],v250 offset:15360\nM v[124:127],a[92:95],v[204:207],v[124:127]\nv_cvt_pk_bf16_f32 v192,v128,v129\nv_cvt_pk_bf16_f32 v193,v130,v131\nM v[124:127],a[124:127],v[220:223],v[124:127]\n" \
"v_cvt_pk_bf16_f32 v194,v144,v145\nv_cvt_pk_bf16_f32 v195,v146,v147\nM a[204:207],a[208:211],v[204:207],a[204:207]\nv_cvt_pk_bf16_f32 v208,v160,v161\nv_cvt_pk_bf16_f32 v209,v162,v163\nv_cvt_pk_bf16_f32 v210,v176,v177\nv_cvt_pk_bf16_f32 v211,v178,v179\nv_add_u32 v250,s94,v241\n" \
"s_add_u32 s95,s95,0x4000\nM a[204:207],a[208:211],v[220:223],a[204:207]\ns_branch .Lse\n.Ltrans:\ns_waitcnt lgkmcnt(4)\nM v[128:131],a[0:3],a[128:131],v[224:227]\ns_add_u32 m0,s94,s64\nv_cvt_pk_bf16_f32 v212,v164,v165\n" \
"M v[128:131],a[4:7],a[132:135],v[128:131]\nds_read_b128 a[64:67],v250 offset:0\nds_read_b128 a[96:99],v250 offset:8192\nM v[128:131],a[8:11],a[136:139],v[128:131]\nbuffer_load_dwordx4 v240,s[44:47],s95 offen offset:0 sc1 lds\nM v[128:131],a[12:15],a[140:143],v[128:131]\nv_cvt_pk_bf16_f32 v213,v166,v167\nv_cvt_pk_bf16_f32 v214,v180,v181\n" \
"M v[132:135],a[12:15],a[156:159],v[228:231]\nds_read_b128 a[68:71],v250 offset:1024\nds_read_b128 a[100:103],v250 offset:9216\nM v[132:135],a[8:11],a[152:155],v[132:135]\nbuffer_store_dwordx4 a[248:251],v253,s[52:55],s33 offen offset:0 sc1\nM v[132:135],a[4:7],a[148:151],v[132:135]\nv_cvt_pk_bf16_f32 v215,v182,v183\ns_add_u32 s71,s33,16384\n" \
"M v[132:135],a[0:3],a[144:147],v[132:135]\nds_read_b128 a[72:75],v250 offset:2048\nds_read_b128 a[104:107],v250 offset:10240\nM v[144:147],a[16:19],a[128:131],v[224:227]\nv_exp_f32 v184,v184\nM v[144:147],a[20:23],a[132:135],v[144:147]\nv_exp_f32 v185,v185\nM v[144:147],a[24:27],a[136:139],v[144:147]\n" \
"ds_read_b128 a[76:79],v250 offset:3072\nds_read_b128 a[108:111],v250 offset:11264\nM v[144:147],a[28:31],a[140:143],v[144:147]\nbuffer_load_dwordx4 v240,s[44:47],s95 offen offset:1024 sc1 lds\nM v[148:151],a[28:31],a[156:159],v[228:231]\nv_exp_f32 v186,v186\nM v[148:151],a[24:27],a[152:155],v[148:151]\nds_read_b128 a[80:83],v250 offset:4096\n" \
"ds_read_b128 a[112:115],v250 offset:12288\nM v[148:151],a[20:23],a[148:151],v[148:151]\nbuffer_store_dwordx4 a[248:251],v253,s[52:55],s33 offen offset:1024 sc1\nM v[148:151],a[16:19],a[144:147],v[148:151]\nv_exp_f32 v187,v187\nM v[160:163],a[32:35],a[128:131],v[224:227]\nds_read_b128 a[84:87],v250 offset:5120\nds_read_b128 a[116:119],v250 offset:13312\n" \
"M v[160:163],a[36:39],a[132:135],v[160:163]\nv_cvt_pk_bf16_f32 v216,v168,v169\nv_cvt_pk_bf16_f32 v217,v170,v171\nM v[160:163],a[40:43],a[136:139],v[160:163]\nv_cvt_pk_bf16_f32 v218,v184,v185\nv_cvt_pk_bf16_f32 v219,v186,v187\nM v[160:163],a[44:47],a[140:143],v[160:163]\nds_read_b128 a[88:91],v250 offset:6144\n" \
"ds_read_b128 a[120:123],v250 offset:14336\nM v[164:167],a[44:47],a[156:159],v[228:231]\nbuffer_load_dwordx4 v240,s[44:47],s95 offen offset:2048 sc1 lds\nM v[164:167],a[40:43],a[152:155],v[164:167]\ns_add_u32 s70,s78,16384\nv_cvt_pk_bf16_f32 v204,v140,v141\nM v[164:167],a[36:39],a[148:151],v[164:167]\nds_read_b128 a[92:95],v250 offset:7168\n" \
"ds_read_b128 a[124:127],v250 offset:15360\nM v[164:167],a[32:35],a[144:147],v[164:167]\nbuffer_store_dwordx4 a[248:251],v253,s[52:55],s33 offen offset:2048 sc1\ns_waitcnt lgkmcnt(6)\nM v[176:179],a[48:51],a[128:131],v[224:227]\nv_xor_b32 v250,0x18000,v250\nv_cvt_pk_bf16_f32 v205,v142,v143\nM v[176:179],a[52:55],a[132:135],v[176:179]\n" \
"v_exp_f32 v172,v172\nM v[176:179],a[56:59],a[136:139],v[176:179]\nv_exp_f32 v173,v173\nM v[176:179],a[60:63],a[140:143],v[176:179]\nv_exp_f32 v174,v174\nM v[180:183],a[60:63],a[156:159],v[228:231]\nv_exp_f32 v175,v175\nM v[180:183],a[56:59],a[152:155],v[180:183]\n" \
"buffer_load_dwordx4 v240,s[44:47],s95 offen offset:3072 sc1 lds\nM v[180:183],a[52:55],a[148:151],v[180:183]\ns_add_u32 m0,m0,0x10000\nv_cvt_pk_bf16_f32 v206,v156,v157\nM v[180:183],a[48:51],a[144:147],v[180:183]\nv_exp_f32 v188,v188\nM v[0:3],a[64:67],v[192:195],v[0:3]\nbuffer_store_dwordx4 a[248:251],v253,s[52:55],s33 offen offset:3072 sc1\n" \
"M v[0:3],a[96:99],v[208:211],v[0:3]\nv_exp_f32 v189,v189\nM v[8:11],a[68:71],v[192:195],v[8:11]\ns_waitcnt vmcnt(16) lgkmcnt(0)\ns_barrier\nM v[8:11],a[100:103],v[208:211],v[8:11]\nds_read_b128 a[0:3],v250 offset:0\nds_read_b128 a[4:7],v250 offset:1024\n" \
"M v[16:19],a[72:75],v[192:195],v[16:19]\nv_exp_f32 v190,v190\nM v[16:19],a[104:107],v[208:211],v[16:19]\nbuffer_store_dwordx4 a[248:251],v253,s[52:55],s71 offen offset:0 sc1\nM v[24:27],a[76:79],v[192:195],v[24:27]\nv_exp_f32 v191,v191\nM v[24:27],a[108:111],v[208:211],v[24:27]\nbuffer_load_dwordx4 v240,s[48:51],s95 offen offset:0 sc1 lds\n" \
"M v[32:35],a[80:83],v[192:195],v[32:35]\nv_cvt_pk_bf16_f32 v207,v158,v159\nv_cvt_pk_bf16_f32 v220,v172,v173\nM v[32:35],a[112:115],v[208:211],v[32:35]\nbuffer_store_dwordx4 a[248:251],v253,s[52:55],s71 offen offset:1024 sc1\nM v[40:43],a[84:87],v[192:195],v[40:43]\nv_cvt_pk_bf16_f32 v221,v174,v175\nv_cvt_pk_bf16_f32 v222,v188,v189\n" \
"M v[40:43],a[116:119],v[208:211],v[40:43]\nds_read_b128 a[8:11],v250 offset:2048\nds_read_b128 a[12:15],v250 offset:3072\nM v[48:51],a[88:91],v[192:195],v[48:51]\nv_cvt_pk_bf16_f32 v223,v190,v191\ns_add_u32 s94,s94,0x4000\nM v[48:51],a[120:123],v[208:211],v[48:51]\nbuffer_store_dwordx4 a[248:251],v253,s[52:55],s71 offen offset:2048 sc1\n" \
"M v[56:59],a[92:95],v[192:195],v[56:59]\nv_exp_f32 v128,v128\nM v[56:59],a[124:127],v[208:211],v[56:59]\nv_exp_f32 v129,v129\nM a[192:195],a[208:211],v[192:195],a[192:195]\nv_exp_f32 v130,v130\nM a[192:195],a[208:211],v[208:211],a[192:195]\nbuffer_store_dwordx4 a[248:251],v253,s[52:55],s71 offen offset:3072 sc1\n" \
"M v[4:7],a[64:67],v[196:199],v[4:7]\nv_exp_f32 v131,v131\nM v[4:7],a[96:99],v[212:215],v[4:7]\nds_read_b128 a[16:19],v250 offset:4096\nds_read_b128 a[20:23],v250 offset:5120\nM v[12:15],a[68:71],v[196:199],v[12:15]\nv_exp_f32 v144,v144\nM v[12:15],a[100:103],v[212:215],v[12:15]\n" \
"v_exp_f32 v145,v145\nM v[20:23],a[72:75],v[196:199],v[20:23]\nv_exp_f32 v146,v146\nM v[20:23],a[104:107],v[212:215],v[20:23]\nbuffer_load_dwordx4 v240,s[48:51],s95 offen offset:1024 sc1 lds\nM v[28:31],a[76:79],v[196:199],v[28:31]\nv_exp_f32 v147,v147\nM v[28:31],a[108:111],v[212:215],v[28:31]\n" \
"buffer_store_dwordx4 a[248:251],v253,s[52:55],s78 offen offset:0 sc1\nM v[36:39],a[80:83],v[196:199],v[36:39]\nv_exp_f32 v160,v160\nM v[36:39],a[112:115],v[212:215],v[36:39]\nds_read_b128 a[24:27],v250 offset:6144\nds_read_b128 a[28:31],v250 offset:7168\nM v[44:47],a[84:87],v[196:199],v[44:47]\nv_exp_f32 v161,v161\n" \
"M v[44:47],a[116:119],v[212:215],v[44:47]\nbuffer_store_dwordx4 a[248:251],v253,s[52:55],s78 offen offset:1024 sc1\nM v[52:55],a[88:91],v[196:199],v[52:55]\nv_exp_f32 v162,v162\nM v[52:55],a[120:123],v[212:215],v[52:55]\nv_exp_f32 v163,v163\nM v[60:63],a[92:95],v[196:199],v[60:63]\nv_exp_f32 v176,v176\n" \
"M v[60:63],a[124:127],v[212:215],v[60:63]\nbuffer_store_dwordx4 a[248:251],v253,s[52:55],s78 offen offset:2048 sc1\nM a[196:199],a[208:211],v[196:199],a[196:199]\nv_exp_f32 v177,v177\nM a[196:199],a[208:211],v[212:215],a[196:199]\nds_read_b128 a[32:35],v250 offset:8192\nds_read_b128 a[36:39],v250 offset:9216\nM v[64:67],a[64:67],v[200:203],v[64:67]\n" \
"v_exp_f32 v178,v178\nM v[64:67],a[96:99],v[216:219],v[64:67]\nv_exp_f32 v179,v179\nM v[72:75],a[68:71],v[200:203],v[72:75]\ns_and_b32 s94,s94,0xffff\nv_cvt_pk_bf16_f32 v192,v128,v129\nM v[72:75],a[100:103],v[216:219],v[72:75]\nbuffer_load_dwordx4 v240,s[48:51],s95 offen offset:2048 sc1 lds\n" \
"M v[80:83],a[72:75],v[200:203],v[80:83]\nv_exp_f32 v132,v132\nM v[80:83],a[104:107],v[216:219],v[80:83]\nbuffer_store_dwordx4 a[248:251],v253,s[52:55],s78 offen offset:3072 sc1\nM v[88:91],a[76:79],v[200:203],v[88:91]\nv_exp_f32 v133,v133\nM v[88:91],a[108:111],v[216:219],v[88:91]\nds_read_b128 a[40:43],v250 offset:10240\n" \
"ds_read_b128 a[44:47],v250 offset:11264\nM v[96:99],a[80:83],v[200:203],v[96:99]\nv_exp_f32 v134,v134\nM v[96:99],a[112:115],v[216:219],v[96:99]\nbuffer_store_dwordx4 a[248:251],v253,s[52:55],s70 offen offset:0 sc1\nM v[104:107],a[84:87],v[200:203],v[104:107]\nv_exp_f32 v135,v135\nM v[104:107],a[116:119],v[216:219],v[104:107]\n" \
"v_exp_f32 v148,v148\nM v[112:115],a[88:91],v[200:203],v[112:115]\nv_exp_f32 v149,v149\nM v[112:115],a[120:123],v[216:219],v[112:115]\nbuffer_store_dwordx4 a[248:251],v253,s[52:55],s70 offen offset:1024 sc1\nM v[120:123],a[92:95],v[200:203],v[120:123]\nv_exp_f32 v150,v150\nM v[120:123],a[124:127],v[216:219],v[120:123]\n" \
"ds_read_b128 a[48:51],v250 offset:12288\nds_read_b128 a[52:55],v250 offset:13312\nM a[200:203],a[208:211],v[200:203],a[200:203]\nv_exp_f32 v151,v151\nM a[200:203],a[208:211],v[216:219],a[200:203]\nv_cvt_pk_bf16_f32 v193,v130,v131\nv_cvt_pk_bf16_f32 v194,v144,v145\nM v[68:71],a[64:67],v[204:207],v[68:71]\n" \
"v_cvt_pk_bf16_f32 v195,v146,v147\nv_cvt_pk_bf16_f32 v208,v160,v161\nM v[68:71],a[96:99],v[220:223],v[68:71]\nbuffer_load_dwordx4 v240,s[48:51],s95 offen offset:3072 sc1 lds\nM v[76:79],a[68:71],v[204:207],v[76:79]\nv_cvt_pk_bf16_f32 v209,v162,v163\nv_cvt_pk_bf16_f32 v210,v176,v177\nM v[76:79],a[100:103],v[220:223],v[76:79]\n" \
"buffer_store_dwordx4 a[248:251],v253,s[52:55],s70 offen offset:2048 sc1\nM v[84:87],a[72:75],v[204:207],v[84:87]\nv_cvt_pk_bf16_f32 v211,v178,v179\ns_add_u32 s95,s95,0x4000\nM v[84:87],a[104:107],v[220:223],v[84:87]\nds_read_b128 a[56:59],v250 offset:14336\nds_read_b128 a[60:63],v250 offset:15360\nM v[92:95],a[76:79],v[204:207],v[92:95]\n" \
"v_add_u32 v250,s94,v241\nM v[92:95],a[108:111],v[220:223],v[92:95]\nbuffer_store_dwordx4 a[248:251],v253,s[52:55],s70 offen offset:3072 sc1\nM v[100:103],a[80:83],v[204:207],v[100:103]\nM v[100:103],a[112:115],v[220:223],v[100:103]\nM v[108:111],a[84:87],v[204:207],v[108:111]\nM v[108:111],a[116:119],v[220:223],v[108:111]\nM v[116:119],a[88:91],v[204:207],v[116:119]\n" \
"M v[116:119],a[120:123],v[220:223],v[116:119]\nM v[124:127],a[92:95],v[204:207],v[124:127]\nM v[124:127],a[124:127],v[220:223],v[124:127]\nM a[204:207],a[208:211],v[204:207],a[204:207]\nM a[204:207],a[208:211],v[220:223],a[204:207]\n.Lse:\ns_waitcnt lgkmcnt(4)\nM v[128:131],a[0:3],a[128:131],v[224:227]\n" \
"s_add_u32 m0,s94,s64\nv_accvgpr_read_b32 v232,a200\nv_cvt_pk_bf16_f32 v196,v132,v133\nv_cvt_pk_bf16_f32 v197,v134,v135\nM v[128:131],a[4:7],a[132:135],v[128:131]\nds_read_b128 a[64:67],v250 offset:0\nds_read_b128 a[96:99],v250 offset:8192\nv_accvgpr_read_b32 v236,a204\n" \
"v_add_f32 v233,v232,v236\nM v[128:131],a[8:11],a[136:139],v[128:131]\nbuffer_load_dwordx4 v240,s[44:47],s95 offen offset:0 sc1 lds\nv_rcp_f32 v232,v232\nM v[128:131],a[12:15],a[140:143],v[128:131]\nv_rcp_f32 v236,v236\nv_add_f32 v233,v233,v232\nv_add_f32 v233,v233,v236\n" \
"M v[132:135],a[12:15],a[156:159],v[228:231]\nds_read_b128 a[68:71],v250 offset:1024\nds_read_b128 a[100:103],v250 offset:9216\nv_cmp_ngt_f32 vcc,s28,v233\nv_mul_f32 v64,v64,v232\nM v[132:135],a[8:11],a[152:155],v[132:135]\ns_and_b64 vcc,vcc,s[90:91]\ns_cselect_b32 s39,0x8000000,s16\n" \
"s_cselect_b32 s16,s16,0x8000000\ns_cselect_b32 s71,2,1\ns_addc_u32 s99,s99,0\nM v[132:135],a[4:7],a[148:151],v[132:135]\nv_mul_f32 v65,v65,v232\ns_add_u32 s37,s12,s67\nv_mul_f32 v66,v66,v232\nv_mul_f32 v67,v67,v232\n" \
"M v[132:135],a[0:3],a[144:147],v[132:135]\nds_read_b128 a[72:75],v250 offset:2048\nds_read_b128 a[104:107],v250 offset:10240\nv_cvt_pk_bf16_f32 v64,v64,v65\ns_add_u32 s37,s37,1048576\nM v[144:147],a[16:19],a[128:131],v[224:227]\nv_mul_f32 v72,v72,v232\nv_mul_f32 v73,v73,v232\n" \
"v_mul_f32 v74,v74,v232\nv_mul_f32 v75,v75,v232\nv_cvt_pk_bf16_f32 v65,v66,v67\nM v[144:147],a[20:23],a[132:135],v[144:147]\ns_or_b32 s37,s37,s39\nv_cvt_pk_bf16_f32 v66,v72,v73\nv_cvt_pk_bf16_f32 v67,v74,v75\nbuffer_store_dwordx4 v[64:67],v240,s[52:55],s37 offen offset:0 sc1\n" \
"M v[144:147],a[24:27],a[136:139],v[144:147]\nds_read_b128 a[76:79],v250 offset:3072\nds_read_b128 a[108:111],v250 offset:11264\nv_cvt_pk_bf16_f32 v198,v148,v149\nv_cvt_pk_bf16_f32 v199,v150,v151\nv_mul_f32 v68,v68,v236\nM v[144:147],a[28:31],a[140:143],v[144:147]\nbuffer_load_dwordx4 v240,s[44:47],s95 offen offset:1024 sc1 lds\n" \
"v_mul_f32 v69,v69,v236\nv_mul_f32 v70,v70,v236\nM v[148:151],a[28:31],a[156:159],v[228:231]\nv_mul_f32 v71,v71,v236\nv_cvt_pk_bf16_f32 v68,v68,v69\nv_mul_f32 v76,v76,v236\nv_mul_f32 v77,v77,v236\nM v[148:151],a[24:27],a[152:155],v[148:151]\n" \
"ds_read_b128 a[80:83],v250 offset:4096\nds_read_b128 a[112:115],v250 offset:12288\nv_mul_f32 v78,v78,v236\nv_mul_f32 v79,v79,v236\nM v[148:151],a[20:23],a[148:151],v[148:151]\nv_cvt_pk_bf16_f32 v69,v70,v71\nv_cvt_pk_bf16_f32 v70,v76,v77\nv_cvt_pk_bf16_f32 v71,v78,v79\n" \
"v_mul_f32 v80,v80,v232\nM v[148:151],a[16:19],a[144:147],v[148:151]\nv_exp_f32 v164,v164\nv_exp_f32 v165,v165\nM v[160:163],a[32:35],a[128:131],v[224:227]\nds_read_b128 a[84:87],v250 offset:5120\nds_read_b128 a[116:119],v250 offset:13312\nv_exp_f32 v166,v166\n" \
"v_cvt_pk_bf16_f32 v212,v164,v165\nM v[160:163],a[36:39],a[132:135],v[160:163]\nbuffer_store_dwordx4 v[68:71],v240,s[52:55],s37 offen offset:1024 sc1\nv_exp_f32 v167,v167\nM v[160:163],a[40:43],a[136:139],v[160:163]\nv_cvt_pk_bf16_f32 v213,v166,v167\nv_mul_f32 v81,v81,v232\nv_mul_f32 v82,v82,v232\n" \
"v_mul_f32 v83,v83,v232\nM v[160:163],a[44:47],a[140:143],v[160:163]\nds_read_b128 a[88:91],v250 offset:6144\nds_read_b128 a[120:123],v250 offset:14336\nv_cvt_pk_bf16_f32 v80,v80,v81\nv_mul_f32 v88,v88,v232\nM v[164:167],a[44:47],a[156:159],v[228:231]\nbuffer_load_dwordx4 v240,s[44:47],s95 offen offset:2048 sc1 lds\n" \
"v_mul_f32 v89,v89,v232\nv_mul_f32 v90,v90,v232\nM v[164:167],a[40:43],a[152:155],v[164:167]\nv_mul_f32 v91,v91,v232\nv_cvt_pk_bf16_f32 v81,v82,v83\nv_cvt_pk_bf16_f32 v82,v88,v89\nv_cvt_pk_bf16_f32 v83,v90,v91\nv_mul_f32 v84,v84,v236\n" \
"M v[164:167],a[36:39],a[148:151],v[164:167]\nds_read_b128 a[92:95],v250 offset:7168\nds_read_b128 a[124:127],v250 offset:15360\nv_exp_f32 v180,v180\nM v[164:167],a[32:35],a[144:147],v[164:167]\nv_exp_f32 v181,v181\nv_mul_f32 v85,v85,v236\ns_waitcnt lgkmcnt(4)\n" \
"M v[176:179],a[48:51],a[128:131],v[224:227]\nv_exp_f32 v182,v182\nv_exp_f32 v183,v183\nM v[176:179],a[52:55],a[132:135],v[176:179]\nbuffer_store_dwordx4 v[80:83],v240,s[52:55],s37 offen offset:2048 sc1\nv_cvt_pk_bf16_f32 v214,v180,v181\nv_cvt_pk_bf16_f32 v215,v182,v183\nM v[176:179],a[56:59],a[136:139],v[176:179]\n" \
"v_mul_f32 v86,v86,v236\nv_mul_f32 v87,v87,v236\nv_cvt_pk_bf16_f32 v84,v84,v85\nv_mul_f32 v92,v92,v236\nM v[176:179],a[60:63],a[140:143],v[176:179]\nv_mul_f32 v93,v93,v236\nv_mul_f32 v94,v94,v236\nv_mul_f32 v95,v95,v236\n" \
"v_cvt_pk_bf16_f32 v85,v86,v87\nv_cvt_pk_bf16_f32 v86,v92,v93\nM v[180:183],a[60:63],a[156:159],v[228:231]\nv_cvt_pk_bf16_f32 v87,v94,v95\nv_xor_b32 v250,0x18000,v250\nv_mul_f32 v96,v96,v232\nv_mul_f32 v97,v97,v232\nM v[180:183],a[56:59],a[152:155],v[180:183]\n" \
"buffer_load_dwordx4 v240,s[44:47],s95 offen offset:3072 sc1 lds\ns_add_u32 m0,m0,0x10000\nv_mul_f32 v98,v98,v232\nM v[180:183],a[52:55],a[148:151],v[180:183]\nv_mul_f32 v99,v99,v232\nv_cvt_pk_bf16_f32 v96,v96,v97\nv_mul_f32 v104,v104,v232\nv_mul_f32 v105,v105,v232\n" \
"M v[180:183],a[48:51],a[144:147],v[180:183]\nv_mul_f32 v106,v106,v232\nv_mul_f32 v107,v107,v232\nv_cvt_pk_bf16_f32 v97,v98,v99\ns_add_u32 s38,s37,4096\nM v[0:3],a[64:67],v[192:195],v[0:3]\nv_cvt_pk_bf16_f32 v98,v104,v105\nv_cvt_pk_bf16_f32 v99,v106,v107\n" \
"v_mul_f32 v100,v100,v236\nv_mul_f32 v101,v101,v236\nv_mul_f32 v102,v102,v236\nM v[0:3],a[96:99],v[208:211],v[0:3]\nbuffer_store_dwordx4 v[84:87],v240,s[52:55],s37 offen offset:3072 sc1\nv_mul_f32 v103,v103,v236\nv_cvt_pk_bf16_f32 v100,v100,v101\nM v[8:11],a[68:71],v[192:195],v[8:11]\n" \
"s_waitcnt vmcnt(16) lgkmcnt(0)\ns_barrier\nv_mul_f32 v108,v108,v236\nv_mul_f32 v109,v109,v236\nM v[8:11],a[100:103],v[208:211],v[8:11]\nds_read_b128 a[0:3],v250 offset:0\nds_read_b128 a[4:7],v250 offset:1024\nv_mul_f32 v110,v110,v236\n" \
"v_mul_f32 v111,v111,v236\nM v[16:19],a[72:75],v[192:195],v[16:19]\nv_cvt_pk_bf16_f32 v101,v102,v103\nv_cvt_pk_bf16_f32 v102,v108,v109\nv_cvt_pk_bf16_f32 v103,v110,v111\nv_mul_f32 v112,v112,v232\nM v[16:19],a[104:107],v[208:211],v[16:19]\nbuffer_load_dwordx4 v240,s[48:51],s95 offen offset:0 sc1 lds\n" \
"v_mul_f32 v113,v113,v232\nv_mul_f32 v114,v114,v232\nv_mul_f32 v115,v115,v232\nM v[24:27],a[76:79],v[192:195],v[24:27]\nv_cvt_pk_bf16_f32 v112,v112,v113\nv_mul_f32 v120,v120,v232\nv_mul_f32 v121,v121,v232\nv_mul_f32 v122,v122,v232\n" \
"M v[24:27],a[108:111],v[208:211],v[24:27]\nds_read_b128 a[8:11],v250 offset:2048\nds_read_b128 a[12:15],v250 offset:3072\nv_mul_f32 v123,v123,v232\nv_cvt_pk_bf16_f32 v113,v114,v115\nM v[32:35],a[80:83],v[192:195],v[32:35]\nv_cvt_pk_bf16_f32 v114,v120,v121\nv_cvt_pk_bf16_f32 v115,v122,v123\n" \
"v_mul_f32 v116,v116,v236\nv_mul_f32 v117,v117,v236\nM v[32:35],a[112:115],v[208:211],v[32:35]\nbuffer_store_dwordx4 v[96:99],v240,s[52:55],s38 offen offset:0 sc1\nv_mul_f32 v118,v118,v236\nv_mul_f32 v119,v119,v236\nM v[40:43],a[84:87],v[192:195],v[40:43]\nv_cvt_pk_bf16_f32 v116,v116,v117\n" \
"v_mul_f32 v124,v124,v236\nv_mul_f32 v125,v125,v236\nv_mul_f32 v126,v126,v236\nM v[40:43],a[116:119],v[208:211],v[40:43]\nds_read_b128 a[16:19],v250 offset:4096\nds_read_b128 a[20:23],v250 offset:5120\nv_mul_f32 v127,v127,v236\nv_cvt_pk_bf16_f32 v117,v118,v119\n" \
"v_cvt_pk_bf16_f32 v118,v124,v125\nM v[48:51],a[88:91],v[192:195],v[48:51]\nv_cvt_pk_bf16_f32 v119,v126,v127\nv_exp_f32 v128,v128\ns_add_u32 s94,s94,0x4000\nM v[48:51],a[120:123],v[208:211],v[48:51]\nbuffer_load_dwordx4 v240,s[48:51],s95 offen offset:1024 sc1 lds\nv_exp_f32 v129,v129\n" \
"M v[56:59],a[92:95],v[192:195],v[56:59]\nv_exp_f32 v130,v130\nv_exp_f32 v131,v131\nM v[56:59],a[124:127],v[208:211],v[56:59]\nds_read_b128 a[24:27],v250 offset:6144\nds_read_b128 a[28:31],v250 offset:7168\nv_exp_f32 v144,v144\nM a[192:195],a[208:211],v[192:195],a[192:195]\n" \
"v_exp_f32 v145,v145\nv_exp_f32 v146,v146\ns_lshl2_add_u32 s77,s61,s79\nM a[192:195],a[208:211],v[208:211],a[192:195]\nbuffer_store_dwordx4 v[100:103],v240,s[52:55],s38 offen offset:1024 sc1\nv_exp_f32 v147,v147\nM v[4:7],a[64:67],v[196:199],v[4:7]\nv_exp_f32 v160,v160\n" \
"v_exp_f32 v161,v161\nM v[4:7],a[96:99],v[212:215],v[4:7]\nds_read_b128 a[32:35],v250 offset:8192\nds_read_b128 a[36:39],v250 offset:9216\nv_exp_f32 v162,v162\nM v[12:15],a[68:71],v[196:199],v[12:15]\nv_exp_f32 v163,v163\nv_exp_f32 v176,v176\n" \
"M v[12:15],a[100:103],v[212:215],v[12:15]\nbuffer_load_dwordx4 v240,s[48:51],s95 offen offset:2048 sc1 lds\nv_exp_f32 v177,v177\nM v[20:23],a[72:75],v[196:199],v[20:23]\nv_exp_f32 v178,v178\nv_exp_f32 v179,v179\ns_and_b32 s94,s94,0xffff\nM v[20:23],a[104:107],v[212:215],v[20:23]\n" \
"ds_read_b128 a[40:43],v250 offset:10240\nds_read_b128 a[44:47],v250 offset:11264\ns_add_u32 s77,s77,4096\nv_mov_b32 v233,s71\nM v[28:31],a[76:79],v[196:199],v[28:31]\nv_exp_f32 v132,v132\nv_exp_f32 v133,v133\nM v[28:31],a[108:111],v[212:215],v[28:31]\n" \
"buffer_store_dwordx4 v[112:115],v240,s[52:55],s38 offen offset:2048 sc1\nv_exp_f32 v134,v134\nM v[36:39],a[80:83],v[196:199],v[36:39]\nv_exp_f32 v135,v135\nv_exp_f32 v148,v148\nM v[36:39],a[112:115],v[212:215],v[36:39]\nds_read_b128 a[48:51],v250 offset:12288\nds_read_b128 a[52:55],v250 offset:13312\n" \
"v_exp_f32 v149,v149\nv_cvt_pk_bf16_f32 v192,v128,v129\nM v[44:47],a[84:87],v[196:199],v[44:47]\nv_exp_f32 v150,v150\nv_exp_f32 v151,v151\nM v[44:47],a[116:119],v[212:215],v[44:47]\nbuffer_load_dwordx4 v240,s[48:51],s95 offen offset:3072 sc1 lds\nv_cvt_pk_bf16_f32 v193,v130,v131\n" \
"v_cvt_pk_bf16_f32 v194,v144,v145\nM v[52:55],a[88:91],v[196:199],v[52:55]\nv_cvt_pk_bf16_f32 v195,v146,v147\nv_cvt_pk_bf16_f32 v208,v160,v161\nv_cvt_pk_bf16_f32 v209,v162,v163\nv_cvt_pk_bf16_f32 v210,v176,v177\nM v[52:55],a[120:123],v[212:215],v[52:55]\nds_read_b128 a[56:59],v250 offset:14336\n" \
"ds_read_b128 a[60:63],v250 offset:15360\nv_cvt_pk_bf16_f32 v211,v178,v179\nv_add_u32 v250,s94,v241\nM v[60:63],a[92:95],v[196:199],v[60:63]\ns_add_u32 s95,s95,0x4000\nM v[60:63],a[124:127],v[212:215],v[60:63]\nbuffer_store_dwordx4 v[116:119],v240,s[52:55],s38 offen offset:3072 sc1\nM a[196:199],a[208:211],v[196:199],a[196:199]\n" \
"M a[196:199],a[208:211],v[212:215],a[196:199]\ns_cmp_ge_u32 s95,s15\ns_cbranch_scc1 .Lsx\n.Lsingle:\ns_waitcnt lgkmcnt(4)\nM v[128:131],a[0:3],a[128:131],v[224:227]\ns_xor_b32 m0,m0,0x14000\nv_cvt_pk_bf16_f32 v196,v132,v133\n" \
"M v[128:131],a[4:7],a[132:135],v[128:131]\nds_read_b128 a[64:67],v250 offset:0\nds_read_b128 a[96:99],v250 offset:8192\nM v[128:131],a[8:11],a[136:139],v[128:131]\nbuffer_load_dwordx4 v240,s[44:47],s95 offen offset:0 sc1 lds\nM v[128:131],a[12:15],a[140:143],v[128:131]\nv_cvt_pk_bf16_f32 v197,v134,v135\nv_cvt_pk_bf16_f32 v198,v148,v149\n" \
"M v[132:135],a[12:15],a[156:159],v[228:231]\nds_read_b128 a[68:71],v250 offset:1024\nds_read_b128 a[100:103],v250 offset:9216\nM v[132:135],a[8:11],a[152:155],v[132:135]\nv_exp_f32 v164,v164\nM v[132:135],a[4:7],a[148:151],v[132:135]\nv_exp_f32 v165,v165\nM v[132:135],a[0:3],a[144:147],v[132:135]\n" \
"ds_read_b128 a[72:75],v250 offset:2048\nds_read_b128 a[104:107],v250 offset:10240\nM v[144:147],a[16:19],a[128:131],v[224:227]\nv_cvt_pk_bf16_f32 v199,v150,v151\nv_cvt_pk_bf16_f32 v212,v164,v165\nM v[144:147],a[20:23],a[132:135],v[144:147]\nv_exp_f32 v166,v166\nM v[144:147],a[24:27],a[136:139],v[144:147]\n" \
"ds_read_b128 a[76:79],v250 offset:3072\nds_read_b128 a[108:111],v250 offset:11264\nM v[144:147],a[28:31],a[140:143],v[144:147]\nbuffer_load_dwordx4 v240,s[44:47],s95 offen offset:1024 sc1 lds\nM v[148:151],a[28:31],a[156:159],v[228:231]\nv_exp_f32 v167,v167\nM v[148:151],a[24:27],a[152:155],v[148:151]\nds_read_b128 a[80:83],v250 offset:4096\n" \
"ds_read_b128 a[112:115],v250 offset:12288\nM v[148:151],a[20:23],a[148:151],v[148:151]\nv_exp_f32 v180,v180\nM v[148:151],a[16:19],a[144:147],v[148:151]\nv_exp_f32 v181,v181\nM v[160:163],a[32:35],a[128:131],v[224:227]\nds_read_b128 a[84:87],v250 offset:5120\nds_read_b128 a[116:119],v250 offset:13312\n" \
"M v[160:163],a[36:39],a[132:135],v[160:163]\nv_cvt_pk_bf16_f32 v213,v166,v167\nv_cvt_pk_bf16_f32 v214,v180,v181\nM v[160:163],a[40:43],a[136:139],v[160:163]\nv_exp_f32 v182,v182\nM v[160:163],a[44:47],a[140:143],v[160:163]\nds_read_b128 a[88:91],v250 offset:6144\nds_read_b128 a[120:123],v250 offset:14336\n" \
"M v[164:167],a[44:47],a[156:159],v[228:231]\nbuffer_load_dwordx4 v240,s[44:47],s95 offen offset:2048 sc1 lds\nM v[164:167],a[40:43],a[152:155],v[164:167]\nv_exp_f32 v183,v183\nM v[164:167],a[36:39],a[148:151],v[164:167]\nds_read_b128 a[92:95],v250 offset:7168\nds_read_b128 a[124:127],v250 offset:15360\nM v[164:167],a[32:35],a[144:147],v[164:167]\n" \
"v_cvt_pk_bf16_f32 v215,v182,v183\ns_waitcnt lgkmcnt(7)\nM v[176:179],a[48:51],a[128:131],v[224:227]\nv_exp_f32 v128,v128\nM v[176:179],a[52:55],a[132:135],v[176:179]\nv_exp_f32 v129,v129\nM v[176:179],a[56:59],a[136:139],v[176:179]\nv_exp_f32 v130,v130\n" \
"M v[176:179],a[60:63],a[140:143],v[176:179]\nv_exp_f32 v131,v131\nM v[180:183],a[60:63],a[156:159],v[228:231]\nv_exp_f32 v132,v132\nM v[180:183],a[56:59],a[152:155],v[180:183]\nbuffer_load_dwordx4 v240,s[44:47],s95 offen offset:3072 sc1 lds\nM v[180:183],a[52:55],a[148:151],v[180:183]\nv_xor_b32 v250,0x18000,v250\n" \
"s_xor_b32 m0,m0,0x10000\nM v[180:183],a[48:51],a[144:147],v[180:183]\nv_exp_f32 v133,v133\nM v[0:3],a[64:67],v[192:195],v[0:3]\nv_exp_f32 v134,v134\nM v[0:3],a[96:99],v[208:211],v[0:3]\nv_exp_f32 v135,v135\nM v[8:11],a[68:71],v[192:195],v[8:11]\n" \
"s_waitcnt vmcnt(16) lgkmcnt(0)\ns_barrier\nM v[8:11],a[100:103],v[208:211],v[8:11]\nds_read_b128 a[0:3],v250 offset:0\nds_read_b128 a[4:7],v250 offset:1024\nM v[16:19],a[72:75],v[192:195],v[16:19]\nv_exp_f32 v144,v144\nM v[16:19],a[104:107],v[208:211],v[16:19]\n" \
"buffer_load_dwordx4 v240,s[48:51],s95 offen offset:0 sc1 lds\nM v[24:27],a[76:79],v[192:195],v[24:27]\nv_exp_f32 v145,v145\nM v[24:27],a[108:111],v[208:211],v[24:27]\nds_read_b128 a[8:11],v250 offset:2048\nds_read_b128 a[12:15],v250 offset:3072\nM v[32:35],a[80:83],v[192:195],v[32:35]\nv_exp_f32 v146,v146\n" \
"M v[32:35],a[112:115],v[208:211],v[32:35]\nv_exp_f32 v147,v147\nM v[40:43],a[84:87],v[192:195],v[40:43]\nv_exp_f32 v148,v148\nM v[40:43],a[116:119],v[208:211],v[40:43]\nds_read_b128 a[16:19],v250 offset:4096\nds_read_b128 a[20:23],v250 offset:5120\nM v[48:51],a[88:91],v[192:195],v[48:51]\n" \
"v_exp_f32 v149,v149\nM v[48:51],a[120:123],v[208:211],v[48:51]\nbuffer_load_dwordx4 v240,s[48:51],s95 offen offset:1024 sc1 lds\nM v[56:59],a[92:95],v[192:195],v[56:59]\nv_exp_f32 v150,v150\nM v[56:59],a[124:127],v[208:211],v[56:59]\nds_read_b128 a[24:27],v250 offset:6144\nds_read_b128 a[28:31],v250 offset:7168\n" \
"M a[192:195],a[208:211],v[192:195],a[192:195]\nv_cvt_pk_bf16_f32 v192,v128,v129\nv_cvt_pk_bf16_f32 v193,v130,v131\nM a[192:195],a[208:211],v[208:211],a[192:195]\nv_cvt_pk_bf16_f32 v194,v144,v145\nv_cvt_pk_bf16_f32 v195,v146,v147\nM v[4:7],a[64:67],v[196:199],v[4:7]\nv_exp_f32 v151,v151\n" \
"M v[4:7],a[96:99],v[212:215],v[4:7]\nds_read_b128 a[32:35],v250 offset:8192\nds_read_b128 a[36:39],v250 offset:9216\nM v[12:15],a[68:71],v[196:199],v[12:15]\nv_exp_f32 v160,v160\nM v[12:15],a[100:103],v[212:215],v[12:15]\nbuffer_load_dwordx4 v240,s[48:51],s95 offen offset:2048 sc1 lds\nM v[20:23],a[72:75],v[196:199],v[20:23]\n" \
"v_exp_f32 v161,v161\nM v[20:23],a[104:107],v[212:215],v[20:23]\nds_read_b128 a[40:43],v250 offset:10240\nds_read_b128 a[44:47],v250 offset:11264\nM v[28:31],a[76:79],v[196:199],v[28:31]\nv_exp_f32 v162,v162\nM v[28:31],a[108:111],v[212:215],v[28:31]\nv_exp_f32 v163,v163\n" \
"M v[36:39],a[80:83],v[196:199],v[36:39]\nv_cvt_pk_bf16_f32 v208,v160,v161\nv_cvt_pk_bf16_f32 v209,v162,v163\nM v[36:39],a[112:115],v[212:215],v[36:39]\nds_read_b128 a[48:51],v250 offset:12288\nds_read_b128 a[52:55],v250 offset:13312\nM v[44:47],a[84:87],v[196:199],v[44:47]\nv_exp_f32 v164,v164\n" \
"M v[44:47],a[116:119],v[212:215],v[44:47]\nbuffer_load_dwordx4 v240,s[48:51],s95 offen offset:3072 sc1 lds\nM v[52:55],a[88:91],v[196:199],v[52:55]\ns_add_u32 s95,s95,0x4000\ns_xor_b32 m0,m0,0x1c000\nM v[52:55],a[120:123],v[212:215],v[52:55]\nds_read_b128 a[56:59],v250 offset:14336\nds_read_b128 a[60:63],v250 offset:15360\n" \
"M v[60:63],a[92:95],v[196:199],v[60:63]\nv_exp_f32 v165,v165\nM v[60:63],a[124:127],v[212:215],v[60:63]\nv_exp_f32 v166,v166\nM a[196:199],a[208:211],v[196:199],a[196:199]\nv_xor_b32 v250,0x14000,v250\nv_cvt_pk_bf16_f32 v196,v132,v133\nM a[196:199],a[208:211],v[212:215],a[196:199]\n" \
"v_cvt_pk_bf16_f32 v197,v134,v135\ns_waitcnt lgkmcnt(4)\nM v[128:131],a[0:3],a[128:131],v[224:227]\nv_cvt_pk_bf16_f32 v198,v148,v149\nv_cvt_pk_bf16_f32 v199,v150,v151\nM v[128:131],a[4:7],a[132:135],v[128:131]\nds_read_b128 a[64:67],v250 offset:0\nds_read_b128 a[96:99],v250 offset:8192\n" \
"M v[128:131],a[8:11],a[136:139],v[128:131]\nbuffer_load_dwordx4 v240,s[44:47],s95 offen offset:0 sc1 lds\nM v[128:131],a[12:15],a[140:143],v[128:131]\nv_exp_f32 v167,v167\nM v[132:135],a[12:15],a[156:159],v[228:231]\nds_read_b128 a[68:71],v250 offset:1024\nds_read_b128 a[100:103],v250 offset:9216\nM v[132:135],a[8:11],a[152:155],v[132:135]\n" \
"v_cvt_pk_bf16_f32 v212,v164,v165\nv_cvt_pk_bf16_f32 v213,v166,v167\nM v[132:135],a[4:7],a[148:151],v[132:135]\nv_exp_f32 v176,v176\nM v[132:135],a[0:3],a[144:147],v[132:135]\nds_read_b128 a[72:75],v250 offset:2048\nds_read_b128 a[104:107],v250 offset:10240\nM v[144:147],a[16:19],a[128:131],v[224:227]\n" \
"v_exp_f32 v177,v177\nM v[144:147],a[20:23],a[132:135],v[144:147]\nv_exp_f32 v178,v178\nM v[144:147],a[24:27],a[136:139],v[144:147]\nds_read_b128 a[76:79],v250 offset:3072\nds_read_b128 a[108:111],v250 offset:11264\nM v[144:147],a[28:31],a[140:143],v[144:147]\nbuffer_load_dwordx4 v240,s[44:47],s95 offen offset:1024 sc1 lds\n" \
"M v[148:151],a[28:31],a[156:159],v[228:231]\nv_exp_f32 v179,v179\nM v[148:151],a[24:27],a[152:155],v[148:151]\nds_read_b128 a[80:83],v250 offset:4096\nds_read_b128 a[112:115],v250 offset:12288\nM v[148:151],a[20:23],a[148:151],v[148:151]\nv_cvt_pk_bf16_f32 v210,v176,v177\nv_cvt_pk_bf16_f32 v211,v178,v179\n" \
"M v[148:151],a[16:19],a[144:147],v[148:151]\nv_exp_f32 v180,v180\nM v[160:163],a[32:35],a[128:131],v[224:227]\nds_read_b128 a[84:87],v250 offset:5120\nds_read_b128 a[116:119],v250 offset:13312\nM v[160:163],a[36:39],a[132:135],v[160:163]\nv_exp_f32 v181,v181\nM v[160:163],a[40:43],a[136:139],v[160:163]\n" \
"v_exp_f32 v182,v182\nM v[160:163],a[44:47],a[140:143],v[160:163]\nds_read_b128 a[88:91],v250 offset:6144\nds_read_b128 a[120:123],v250 offset:14336\nM v[164:167],a[44:47],a[156:159],v[228:231]\nbuffer_load_dwordx4 v240,s[44:47],s95 offen offset:2048 sc1 lds\nM v[164:167],a[40:43],a[152:155],v[164:167]\nv_exp_f32 v183,v183\n" \
"M v[164:167],a[36:39],a[148:151],v[164:167]\nds_read_b128 a[92:95],v250 offset:7168\nds_read_b128 a[124:127],v250 offset:15360\nM v[164:167],a[32:35],a[144:147],v[164:167]\nv_cvt_pk_bf16_f32 v214,v180,v181\ns_waitcnt lgkmcnt(7)\nM v[176:179],a[48:51],a[128:131],v[224:227]\nv_cvt_pk_bf16_f32 v215,v182,v183\n" \
"v_xor_b32 v250,0x18000,v250\nM v[176:179],a[52:55],a[132:135],v[176:179]\nv_exp_f32 v128,v128\nM v[176:179],a[56:59],a[136:139],v[176:179]\nv_exp_f32 v129,v129\nM v[176:179],a[60:63],a[140:143],v[176:179]\nv_exp_f32 v130,v130\nM v[180:183],a[60:63],a[156:159],v[228:231]\n" \
"v_exp_f32 v131,v131\nM v[180:183],a[56:59],a[152:155],v[180:183]\nbuffer_load_dwordx4 v240,s[44:47],s95 offen offset:3072 sc1 lds\nM v[180:183],a[52:55],a[148:151],v[180:183]\nv_exp_f32 v144,v144\nM v[180:183],a[48:51],a[144:147],v[180:183]\nv_exp_f32 v145,v145\nM v[0:3],a[64:67],v[192:195],v[0:3]\n" \
"v_exp_f32 v146,v146\nM v[0:3],a[96:99],v[208:211],v[0:3]\nv_exp_f32 v147,v147\nM v[8:11],a[68:71],v[192:195],v[8:11]\ns_waitcnt vmcnt(16) lgkmcnt(0)\ns_barrier\nM v[8:11],a[100:103],v[208:211],v[8:11]\nds_read_b128 a[0:3],v250 offset:0\n" \
"ds_read_b128 a[4:7],v250 offset:1024\nM v[16:19],a[72:75],v[192:195],v[16:19]\ns_xor_b32 m0,m0,0x10000\nM v[16:19],a[104:107],v[208:211],v[16:19]\nbuffer_load_dwordx4 v240,s[48:51],s95 offen offset:0 sc1 lds\nM v[24:27],a[76:79],v[192:195],v[24:27]\nv_exp_f32 v160,v160\nM v[24:27],a[108:111],v[208:211],v[24:27]\n" \
"ds_read_b128 a[8:11],v250 offset:2048\nds_read_b128 a[12:15],v250 offset:3072\nM v[32:35],a[80:83],v[192:195],v[32:35]\nv_exp_f32 v161,v161\nM v[32:35],a[112:115],v[208:211],v[32:35]\nv_exp_f32 v162,v162\nM v[40:43],a[84:87],v[192:195],v[40:43]\nv_exp_f32 v163,v163\n" \
"M v[40:43],a[116:119],v[208:211],v[40:43]\nds_read_b128 a[16:19],v250 offset:4096\nds_read_b128 a[20:23],v250 offset:5120\nM v[48:51],a[88:91],v[192:195],v[48:51]\nv_exp_f32 v176,v176\nM v[48:51],a[120:123],v[208:211],v[48:51]\nbuffer_load_dwordx4 v240,s[48:51],s95 offen offset:1024 sc1 lds\nM v[56:59],a[92:95],v[192:195],v[56:59]\n" \
"v_exp_f32 v177,v177\nM v[56:59],a[124:127],v[208:211],v[56:59]\nds_read_b128 a[24:27],v250 offset:6144\nds_read_b128 a[28:31],v250 offset:7168\nM a[192:195],a[208:211],v[192:195],a[192:195]\nv_exp_f32 v178,v178\nM a[192:195],a[208:211],v[208:211],a[192:195]\nv_exp_f32 v179,v179\n" \
"M v[4:7],a[64:67],v[196:199],v[4:7]\nv_exp_f32 v132,v132\nM v[4:7],a[96:99],v[212:215],v[4:7]\nds_read_b128 a[32:35],v250 offset:8192\nds_read_b128 a[36:39],v250 offset:9216\nM v[12:15],a[68:71],v[196:199],v[12:15]\nv_exp_f32 v133,v133\nM v[12:15],a[100:103],v[212:215],v[12:15]\n" \
"buffer_load_dwordx4 v240,s[48:51],s95 offen offset:2048 sc1 lds\nM v[20:23],a[72:75],v[196:199],v[20:23]\nv_exp_f32 v134,v134\nM v[20:23],a[104:107],v[212:215],v[20:23]\nds_read_b128 a[40:43],v250 offset:10240\nds_read_b128 a[44:47],v250 offset:11264\nM v[28:31],a[76:79],v[196:199],v[28:31]\nv_exp_f32 v135,v135\n" \
"M v[28:31],a[108:111],v[212:215],v[28:31]\nv_exp_f32 v148,v148\nM v[36:39],a[80:83],v[196:199],v[36:39]\nv_exp_f32 v149,v149\nM v[36:39],a[112:115],v[212:215],v[36:39]\nds_read_b128 a[48:51],v250 offset:12288\nds_read_b128 a[52:55],v250 offset:13312\nM v[44:47],a[84:87],v[196:199],v[44:47]\n" \
"v_exp_f32 v150,v150\nM v[44:47],a[116:119],v[212:215],v[44:47]\nbuffer_load_dwordx4 v240,s[48:51],s95 offen offset:3072 sc1 lds\nM v[52:55],a[88:91],v[196:199],v[52:55]\nv_exp_f32 v151,v151\nM v[52:55],a[120:123],v[212:215],v[52:55]\nds_read_b128 a[56:59],v250 offset:14336\nds_read_b128 a[60:63],v250 offset:15360\n" \
"M v[60:63],a[92:95],v[196:199],v[60:63]\nv_cvt_pk_bf16_f32 v192,v128,v129\nv_cvt_pk_bf16_f32 v193,v130,v131\nM v[60:63],a[124:127],v[212:215],v[60:63]\nv_cvt_pk_bf16_f32 v194,v144,v145\nv_cvt_pk_bf16_f32 v195,v146,v147\nM a[196:199],a[208:211],v[196:199],a[196:199]\nv_cvt_pk_bf16_f32 v208,v160,v161\n" \
"v_cvt_pk_bf16_f32 v209,v162,v163\nv_cvt_pk_bf16_f32 v210,v176,v177\nv_cvt_pk_bf16_f32 v211,v178,v179\nv_xor_b32 v250,0x1c000,v250\ns_add_u32 s95,s95,0x4000\nM a[196:199],a[208:211],v[212:215],a[196:199]\ns_cmp_lg_u32 s95,s15\ns_cbranch_scc1 .Lsingle\n" \
"s_and_b32 s94,m0,0x8000\ns_or_b32 s94,s94,0x4000\n.Lsx:\n.Lsw:\ns_waitcnt lgkmcnt(4)\nM v[128:131],a[0:3],a[128:131],v[224:227]\ns_lshl4_add_u32 s69,s12,s68\ns_add_u32 m0,s64,131072\n" \
"M v[128:131],a[4:7],a[132:135],v[128:131]\nds_read_b128 a[64:67],v250 offset:0\nds_read_b128 a[96:99],v250 offset:8192\nv_cvt_pk_bf16_f32 v196,v132,v133\nM v[128:131],a[8:11],a[136:139],v[128:131]\nbuffer_load_dwordx4 v243,s[40:43],s69 offen offset:0 lds\nM v[128:131],a[12:15],a[140:143],v[128:131]\nv_cvt_pk_bf16_f32 v197,v134,v135\n" \
"v_cvt_pk_bf16_f32 v198,v148,v149\nv_cvt_pk_bf16_f32 v199,v150,v151\nM v[132:135],a[12:15],a[156:159],v[228:231]\nds_read_b128 a[68:71],v250 offset:1024\nds_read_b128 a[100:103],v250 offset:9216\nM v[132:135],a[8:11],a[152:155],v[132:135]\nbuffer_load_dwordx4 v244,s[40:43],s69 offen offset:1024 lds\ns_add_u32 s94,s94,0x4000\n" \
"M v[132:135],a[4:7],a[148:151],v[132:135]\nv_exp_f32 v164,v164\nM v[132:135],a[0:3],a[144:147],v[132:135]\nds_read_b128 a[72:75],v250 offset:2048\nds_read_b128 a[104:107],v250 offset:10240\ns_and_b32 s94,s94,0xffff\nM v[144:147],a[16:19],a[128:131],v[224:227]\nbuffer_load_dwordx4 v245,s[40:43],s69 offen offset:2048 lds\n" \
"M v[144:147],a[20:23],a[132:135],v[144:147]\nv_exp_f32 v165,v165\ns_add_u32 s95,s95,0x4000\nM v[144:147],a[24:27],a[136:139],v[144:147]\nds_read_b128 a[76:79],v250 offset:3072\nds_read_b128 a[108:111],v250 offset:11264\nM v[144:147],a[28:31],a[140:143],v[144:147]\nbuffer_load_dwordx4 v246,s[40:43],s69 offen offset:3072 lds\n" \
"s_lshl4_add_u32 s69,s12,s68\nM v[148:151],a[28:31],a[156:159],v[228:231]\ns_add_u32 s69,s69,0x10000\ns_add_u32 m0,s64,147456\nM v[148:151],a[24:27],a[152:155],v[148:151]\nds_read_b128 a[80:83],v250 offset:4096\nds_read_b128 a[112:115],v250 offset:12288\nv_cvt_pk_bf16_f32 v212,v164,v165\n" \
"M v[148:151],a[20:23],a[148:151],v[148:151]\nv_exp_f32 v166,v166\nM v[148:151],a[16:19],a[144:147],v[148:151]\nv_exp_f32 v167,v167\nM v[160:163],a[32:35],a[128:131],v[224:227]\nds_read_b128 a[84:87],v250 offset:5120\nds_read_b128 a[116:119],v250 offset:13312\nv_cvt_pk_bf16_f32 v213,v166,v167\n" \
"M v[160:163],a[36:39],a[132:135],v[160:163]\nbuffer_load_dwordx4 v243,s[40:43],s69 offen offset:0 lds\nM v[160:163],a[40:43],a[136:139],v[160:163]\nv_exp_f32 v180,v180\nM v[160:163],a[44:47],a[140:143],v[160:163]\nds_read_b128 a[88:91],v250 offset:6144\nds_read_b128 a[120:123],v250 offset:14336\nM v[164:167],a[44:47],a[156:159],v[228:231]\n" \
"v_exp_f32 v181,v181\nM v[164:167],a[40:43],a[152:155],v[164:167]\nbuffer_load_dwordx4 v244,s[40:43],s69 offen offset:1024 lds\nM v[164:167],a[36:39],a[148:151],v[164:167]\nds_read_b128 a[92:95],v250 offset:7168\nds_read_b128 a[124:127],v250 offset:15360\nv_cvt_pk_bf16_f32 v214,v180,v181\nM v[164:167],a[32:35],a[144:147],v[164:167]\n" \
"v_xor_b32 v250,0x18000,v250\ns_waitcnt lgkmcnt(6)\nM v[176:179],a[48:51],a[128:131],v[224:227]\ns_waitcnt vmcnt(6)\nbuffer_store_dword v233,off,s[52:55],s77 sc1\nM v[176:179],a[52:55],a[132:135],v[176:179]\nbuffer_load_dwordx4 v245,s[40:43],s69 offen offset:2048 lds\nM v[176:179],a[56:59],a[136:139],v[176:179]\n" \
"v_exp_f32 v182,v182\nv_exp_f32 v183,v183\nM v[176:179],a[60:63],a[140:143],v[176:179]\nv_cvt_pk_bf16_f32 v215,v182,v183\nM v[180:183],a[60:63],a[156:159],v[228:231]\nv_exp_f32 v128,v128\nM v[180:183],a[56:59],a[152:155],v[180:183]\nbuffer_load_dwordx4 v246,s[40:43],s69 offen offset:3072 lds\n" \
"M v[180:183],a[52:55],a[148:151],v[180:183]\nv_exp_f32 v129,v129\nM v[180:183],a[48:51],a[144:147],v[180:183]\ns_lshl4_add_u32 s69,s10,s68\nv_exp_f32 v130,v130\nM v[0:3],a[64:67],v[192:195],v[0:3]\nv_exp_f32 v131,v131\nM v[0:3],a[96:99],v[208:211],v[0:3]\n" \
"s_add_u32 m0,s64,0\nv_exp_f32 v144,v144\nM v[8:11],a[68:71],v[192:195],v[8:11]\ns_waitcnt vmcnt(16) lgkmcnt(0)\ns_barrier\nM v[8:11],a[100:103],v[208:211],v[8:11]\nds_read_b128 a[0:3],v250 offset:0\nds_read_b128 a[4:7],v250 offset:1024\n" \
"M v[16:19],a[72:75],v[192:195],v[16:19]\nv_exp_f32 v145,v145\nM v[16:19],a[104:107],v[208:211],v[16:19]\nbuffer_load_dwordx4 v243,s[40:43],s69 offen offset:0 lds\nM v[24:27],a[76:79],v[192:195],v[24:27]\nv_exp_f32 v146,v146\nM v[24:27],a[108:111],v[208:211],v[24:27]\nds_read_b128 a[8:11],v250 offset:2048\n" \
"ds_read_b128 a[12:15],v250 offset:3072\nM v[32:35],a[80:83],v[192:195],v[32:35]\nv_exp_f32 v147,v147\nM v[32:35],a[112:115],v[208:211],v[32:35]\nbuffer_load_dwordx4 v244,s[40:43],s69 offen offset:1024 lds\nM v[40:43],a[84:87],v[192:195],v[40:43]\nv_exp_f32 v160,v160\nM v[40:43],a[116:119],v[208:211],v[40:43]\n" \
"ds_read_b128 a[16:19],v250 offset:4096\nds_read_b128 a[20:23],v250 offset:5120\nM v[48:51],a[88:91],v[192:195],v[48:51]\nv_exp_f32 v161,v161\nM v[48:51],a[120:123],v[208:211],v[48:51]\nbuffer_load_dwordx4 v245,s[40:43],s69 offen offset:2048 lds\nM v[56:59],a[92:95],v[192:195],v[56:59]\nv_exp_f32 v162,v162\n" \
"M v[56:59],a[124:127],v[208:211],v[56:59]\nds_read_b128 a[24:27],v250 offset:6144\nds_read_b128 a[28:31],v250 offset:7168\nM a[192:195],a[208:211],v[192:195],a[192:195]\nv_exp_f32 v163,v163\nv_cvt_pk_bf16_f32 v192,v128,v129\nM a[192:195],a[208:211],v[208:211],a[192:195]\nbuffer_load_dwordx4 v246,s[40:43],s69 offen offset:3072 lds\n" \
"M v[4:7],a[64:67],v[196:199],v[4:7]\ns_lshl4_add_u32 s69,s10,s68\ns_add_u32 s69,s69,0x10000\ns_add_u32 m0,s64,32768\nM v[4:7],a[96:99],v[212:215],v[4:7]\nds_read_b128 a[32:35],v250 offset:8192\nds_read_b128 a[36:39],v250 offset:9216\nM v[12:15],a[68:71],v[196:199],v[12:15]\n" \
"v_exp_f32 v176,v176\nv_cvt_pk_bf16_f32 v193,v130,v131\nM v[12:15],a[100:103],v[212:215],v[12:15]\nbuffer_load_dwordx4 v243,s[40:43],s69 offen offset:0 lds\nM v[20:23],a[72:75],v[196:199],v[20:23]\nv_exp_f32 v177,v177\nv_cvt_pk_bf16_f32 v194,v144,v145\nM v[20:23],a[104:107],v[212:215],v[20:23]\n" \
"ds_read_b128 a[40:43],v250 offset:10240\nds_read_b128 a[44:47],v250 offset:11264\nM v[28:31],a[76:79],v[196:199],v[28:31]\nv_exp_f32 v178,v178\nv_cvt_pk_bf16_f32 v195,v146,v147\nM v[28:31],a[108:111],v[212:215],v[28:31]\nbuffer_load_dwordx4 v244,s[40:43],s69 offen offset:1024 lds\nM v[36:39],a[80:83],v[196:199],v[36:39]\n" \
"v_exp_f32 v179,v179\nv_cvt_pk_bf16_f32 v208,v160,v161\nM v[36:39],a[112:115],v[212:215],v[36:39]\nds_read_b128 a[48:51],v250 offset:12288\nds_read_b128 a[52:55],v250 offset:13312\nM v[44:47],a[84:87],v[196:199],v[44:47]\nv_exp_f32 v132,v132\nv_cvt_pk_bf16_f32 v209,v162,v163\n" \
"M v[44:47],a[116:119],v[212:215],v[44:47]\nbuffer_load_dwordx4 v245,s[40:43],s69 offen offset:2048 lds\nv_cvt_pk_bf16_f32 v210,v176,v177\nM v[52:55],a[88:91],v[196:199],v[52:55]\nv_exp_f32 v133,v133\nM v[52:55],a[120:123],v[212:215],v[52:55]\nds_read_b128 a[56:59],v250 offset:14336\nds_read_b128 a[60:63],v250 offset:15360\n" \
"v_cvt_pk_bf16_f32 v211,v178,v179\nM v[60:63],a[92:95],v[196:199],v[60:63]\nv_exp_f32 v134,v134\nM v[60:63],a[124:127],v[212:215],v[60:63]\nbuffer_load_dwordx4 v246,s[40:43],s69 offen offset:3072 lds\nv_add_u32 v250,s94,v241\nM a[196:199],a[208:211],v[196:199],a[196:199]\nv_exp_f32 v135,v135\n" \
"M a[196:199],a[208:211],v[212:215],a[196:199]\nv_exp_f32 v148,v148\nv_exp_f32 v149,v149\nv_exp_f32 v150,v150\nv_exp_f32 v151,v151\n.Lod:\ns_waitcnt lgkmcnt(2)\nM v[128:131],a[0:3],a[128:131],v[224:227]\n" \
"v_cvt_pk_bf16_f32 v196,v132,v133\nv_cvt_pk_bf16_f32 v197,v134,v135\nM v[128:131],a[4:7],a[132:135],v[128:131]\nds_read_b128 a[64:67],v250 offset:0\nds_read_b128 a[96:99],v250 offset:8192\nM v[128:131],a[8:11],a[136:139],v[128:131]\ns_lshl2_add_u32 s39,s62,s7\ns_lshl_b32 s39,s39,5\n" \
"M v[128:131],a[12:15],a[140:143],v[128:131]\ns_add_u32 s39,s39,s63\nv_cvt_pk_bf16_f32 v198,v148,v149\nv_cvt_pk_bf16_f32 v199,v150,v151\nM v[132:135],a[12:15],a[156:159],v[228:231]\nds_read_b128 a[68:71],v250 offset:1024\nds_read_b128 a[100:103],v250 offset:9216\nM v[132:135],a[8:11],a[152:155],v[132:135]\n" \
"s_lshl2_add_u32 s39,s39,73728\nv_and_b32 v251,3,v249\nM v[132:135],a[4:7],a[148:151],v[132:135]\ns_waitcnt vmcnt(16)\nbuffer_store_dword v252,off,s[52:55],s39 sc1\nM v[132:135],a[0:3],a[144:147],v[132:135]\nds_read_b128 a[72:75],v250 offset:2048\nds_read_b128 a[104:107],v250 offset:10240\n" \
"v_lshlrev_b32 v251,2,v251\nM v[144:147],a[16:19],a[128:131],v[224:227]\nv_exp_f32 v164,v164\nM v[144:147],a[20:23],a[132:135],v[144:147]\nv_exp_f32 v165,v165\nM v[144:147],a[24:27],a[136:139],v[144:147]\nds_read_b128 a[76:79],v250 offset:3072\nds_read_b128 a[108:111],v250 offset:11264\n" \
"M v[144:147],a[28:31],a[140:143],v[144:147]\nv_exp_f32 v166,v166\nv_cvt_pk_bf16_f32 v212,v164,v165\nM v[148:151],a[28:31],a[156:159],v[228:231]\nv_exp_f32 v167,v167\nM v[148:151],a[24:27],a[152:155],v[148:151]\nds_read_b128 a[80:83],v250 offset:4096\nds_read_b128 a[112:115],v250 offset:12288\n" \
"M v[148:151],a[20:23],a[148:151],v[148:151]\nv_cvt_pk_bf16_f32 v213,v166,v167\ns_add_u32 s80,s79,4096\nM v[148:151],a[16:19],a[144:147],v[148:151]\nv_exp_f32 v180,v180\ns_add_u32 s94,s94,0x4000\nM v[160:163],a[32:35],a[128:131],v[224:227]\nds_read_b128 a[84:87],v250 offset:5120\n" \
"ds_read_b128 a[116:119],v250 offset:13312\nM v[160:163],a[36:39],a[132:135],v[160:163]\nv_exp_f32 v181,v181\nM v[160:163],a[40:43],a[136:139],v[160:163]\nv_exp_f32 v182,v182\nM v[160:163],a[44:47],a[140:143],v[160:163]\nds_read_b128 a[88:91],v250 offset:6144\nds_read_b128 a[120:123],v250 offset:14336\n" \
"v_cvt_pk_bf16_f32 v214,v180,v181\nM v[164:167],a[44:47],a[156:159],v[228:231]\nbuffer_load_dword v248,v251,s[52:55],s80 offen sc1\nM v[164:167],a[40:43],a[152:155],v[164:167]\nv_exp_f32 v183,v183\nM v[164:167],a[36:39],a[148:151],v[164:167]\nds_read_b128 a[92:95],v250 offset:7168\nds_read_b128 a[124:127],v250 offset:15360\n" \
"M v[164:167],a[32:35],a[144:147],v[164:167]\nv_cvt_pk_bf16_f32 v215,v182,v183\ns_and_b32 s94,s94,0xffff\nv_add_u32 v250,s94,v241\nM v[176:179],a[48:51],a[128:131],v[224:227]\nv_exp_f32 v128,v128\nM v[176:179],a[52:55],a[132:135],v[176:179]\ns_add_u32 s65,s10,s67\n" \
"s_waitcnt lgkmcnt(4)\nM v[176:179],a[56:59],a[136:139],v[176:179]\nv_exp_f32 v129,v129\nM v[176:179],a[60:63],a[140:143],v[176:179]\nv_exp_f32 v130,v130\ns_add_u32 s65,s65,1048576\nM v[180:183],a[60:63],a[156:159],v[228:231]\nv_exp_f32 v131,v131\n" \
"M v[180:183],a[56:59],a[152:155],v[180:183]\nv_exp_f32 v144,v144\nM v[180:183],a[52:55],a[148:151],v[180:183]\nv_exp_f32 v145,v145\nM v[180:183],a[48:51],a[144:147],v[180:183]\nv_exp_f32 v146,v146\nv_add_u32 v226,131072,v242\nM v[0:3],a[64:67],v[192:195],v[0:3]\n" \
"v_exp_f32 v147,v147\nM v[0:3],a[96:99],v[208:211],v[0:3]\nv_exp_f32 v160,v160\nM v[8:11],a[68:71],v[192:195],v[8:11]\ns_waitcnt lgkmcnt(0)\ns_barrier\nM v[8:11],a[100:103],v[208:211],v[8:11]\ns_lshl4_add_u32 s69,s11,s68\n" \
"s_add_u32 m0,s64,16384\nv_xor_b32 v227,32,v226\nM v[16:19],a[72:75],v[192:195],v[16:19]\nv_exp_f32 v161,v161\nM v[16:19],a[104:107],v[208:211],v[16:19]\nv_exp_f32 v162,v162\nM v[24:27],a[76:79],v[192:195],v[24:27]\nv_exp_f32 v163,v163\n" \
"M v[24:27],a[108:111],v[208:211],v[24:27]\nv_exp_f32 v176,v176\ns_add_u32 s37,s12,s67\nM v[32:35],a[80:83],v[192:195],v[32:35]\nbuffer_load_dwordx4 v243,s[40:43],s69 offen offset:0 lds\nM v[32:35],a[112:115],v[208:211],v[32:35]\nv_exp_f32 v177,v177\nM v[40:43],a[84:87],v[192:195],v[40:43]\n" \
"v_exp_f32 v178,v178\nM v[40:43],a[116:119],v[208:211],v[40:43]\nv_exp_f32 v179,v179\ns_add_u32 s38,s37,4096\nM v[48:51],a[88:91],v[192:195],v[48:51]\nv_exp_f32 v132,v132\nM v[48:51],a[120:123],v[208:211],v[48:51]\nv_exp_f32 v133,v133\n" \
"M v[56:59],a[92:95],v[192:195],v[56:59]\nv_exp_f32 v134,v134\nM v[56:59],a[124:127],v[208:211],v[56:59]\nv_exp_f32 v135,v135\ns_lshl2_add_u32 s76,s61,s79\nM a[192:195],a[208:211],v[192:195],a[192:195]\nbuffer_load_dwordx4 v244,s[40:43],s69 offen offset:1024 lds\nM a[192:195],a[208:211],v[208:211],a[192:195]\n" \
"v_cvt_pk_bf16_f32 v192,v128,v129\nv_cvt_pk_bf16_f32 v193,v130,v131\nM v[4:7],a[64:67],v[196:199],v[4:7]\nds_read_b128 a[64:67],v250 offset:0\nv_cvt_pk_bf16_f32 v194,v144,v145\nM v[4:7],a[96:99],v[212:215],v[4:7]\nds_read_b128 a[96:99],v250 offset:8192\nv_cvt_pk_bf16_f32 v195,v146,v147\n" \
"v_cvt_pk_bf16_f32 v208,v160,v161\nM v[12:15],a[68:71],v[196:199],v[12:15]\nds_read_b128 a[68:71],v250 offset:1024\nv_cvt_pk_bf16_f32 v209,v162,v163\nM v[12:15],a[100:103],v[212:215],v[12:15]\nds_read_b128 a[100:103],v250 offset:9216\nv_cvt_pk_bf16_f32 v210,v176,v177\nM v[20:23],a[72:75],v[196:199],v[20:23]\n" \
"ds_read_b128 a[72:75],v250 offset:2048\nv_cvt_pk_bf16_f32 v211,v178,v179\nM v[20:23],a[104:107],v[212:215],v[20:23]\nds_read_b128 a[104:107],v250 offset:10240\nv_exp_f32 v148,v148\nM v[28:31],a[76:79],v[196:199],v[28:31]\nbuffer_load_dwordx4 v245,s[40:43],s69 offen offset:2048 lds\nM v[28:31],a[108:111],v[212:215],v[28:31]\n" \
"ds_read_b128 a[76:79],v250 offset:3072\nds_read_b128 a[108:111],v250 offset:11264\nM v[36:39],a[80:83],v[196:199],v[36:39]\nds_read_b128 a[80:83],v250 offset:4096\nv_lshlrev_b32 v238,1,v240\nM v[36:39],a[112:115],v[212:215],v[36:39]\nds_read_b128 a[112:115],v250 offset:12288\nv_exp_f32 v149,v149\n" \
"M v[44:47],a[84:87],v[196:199],v[44:47]\nds_read_b128 a[84:87],v250 offset:5120\nv_and_b32 v254,32,v249\nM v[44:47],a[116:119],v[212:215],v[44:47]\nds_read_b128 a[116:119],v250 offset:13312\ns_lshr_b32 s70,s60,1\nM v[52:55],a[88:91],v[196:199],v[52:55]\nds_read_b128 a[88:91],v250 offset:6144\n" \
"v_lshlrev_b32 v254,3,v254\nM v[52:55],a[120:123],v[212:215],v[52:55]\nds_read_b128 a[120:123],v250 offset:14336\nv_exp_f32 v150,v150\nM v[60:63],a[92:95],v[196:199],v[60:63]\nbuffer_load_dwordx4 v246,s[40:43],s69 offen offset:3072 lds\nM v[60:63],a[124:127],v[212:215],v[60:63]\ns_waitcnt vmcnt(4)\n" \
"v_and_b32 v248,s17,v248\nM a[196:199],a[208:211],v[196:199],a[196:199]\nv_cmp_eq_u32 vcc,0,v248\nds_read_b128 a[92:95],v250 offset:7168\nM a[196:199],a[208:211],v[212:215],a[196:199]\ns_cbranch_vccnz .Lps6\n.Lpb6:\ns_lshl4_add_u32 s69,s11,s68\n" \
"s_waitcnt lgkmcnt(4)\nM v[0:3],a[64:67],v[192:195],v[0:3]\nbuffer_load_dwordx4 v[72:75],v240,s[52:55],s65 offen offset:0 sc1\nM v[0:3],a[96:99],v[208:211],v[0:3]\ns_add_u32 s69,s69,0x10000\ns_add_u32 m0,s64,49152\nM v[8:11],a[68:71],v[192:195],v[8:11]\nds_read_b128 a[124:127],v250 offset:15360\n" \
"v_cvt_pk_bf16_f32 v196,v132,v133\nM v[8:11],a[100:103],v[208:211],v[8:11]\nv_exp_f32 v151,v151\nv_cvt_pk_bf16_f32 v197,v134,v135\nM v[16:19],a[72:75],v[192:195],v[16:19]\nbuffer_load_dwordx4 v243,s[40:43],s69 offen offset:0 lds\nM v[16:19],a[104:107],v[208:211],v[16:19]\nv_exp_f32 v164,v164\n" \
"M v[24:27],a[76:79],v[192:195],v[24:27]\nv_exp_f32 v165,v165\nM v[24:27],a[108:111],v[208:211],v[24:27]\nv_exp_f32 v166,v166\nv_cvt_pk_bf16_f32 v198,v148,v149\nM v[32:35],a[80:83],v[192:195],v[32:35]\nbuffer_load_dwordx4 v[76:79],v240,s[52:55],s65 offen offset:1024 sc1\nM v[32:35],a[112:115],v[208:211],v[32:35]\n" \
"v_exp_f32 v167,v167\nM v[40:43],a[84:87],v[192:195],v[40:43]\nv_cvt_pk_bf16_f32 v199,v150,v151\ns_waitcnt lgkmcnt(0)\nM v[40:43],a[116:119],v[208:211],v[40:43]\nv_exp_f32 v180,v180\nv_cvt_pk_bf16_f32 v212,v164,v165\nM v[48:51],a[88:91],v[192:195],v[48:51]\n" \
"buffer_load_dwordx4 v244,s[40:43],s69 offen offset:1024 lds\nM v[48:51],a[120:123],v[208:211],v[48:51]\nv_exp_f32 v181,v181\nM v[56:59],a[92:95],v[192:195],v[56:59]\nv_exp_f32 v182,v182\nM v[56:59],a[124:127],v[208:211],v[56:59]\nv_exp_f32 v183,v183\nv_cvt_pk_bf16_f32 v213,v166,v167\n" \
"M a[192:195],a[208:211],v[192:195],a[192:195]\nbuffer_load_dwordx4 v[88:91],v240,s[52:55],s65 offen offset:2048 sc1\nM a[192:195],a[208:211],v[208:211],a[192:195]\nv_cvt_pk_bf16_f32 v214,v180,v181\nv_cvt_pk_bf16_f32 v215,v182,v183\nM v[4:7],a[64:67],v[196:199],v[4:7]\ns_barrier\nds_read_b64_tr_b16 v[200:201],v226 offset:0\n" \
"M v[4:7],a[96:99],v[212:215],v[4:7]\nds_read_b64_tr_b16 v[202:203],v226 offset:4096\nds_read_b64_tr_b16 v[204:205],v226 offset:8192\nds_read_b64_tr_b16 v[206:207],v226 offset:12288\nM v[12:15],a[68:71],v[196:199],v[12:15]\nbuffer_load_dwordx4 v245,s[40:43],s69 offen offset:2048 lds\nM v[12:15],a[100:103],v[212:215],v[12:15]\nds_read_b64_tr_b16 v[216:217],v227 offset:0\n" \
"ds_read_b64_tr_b16 v[218:219],v227 offset:4096\nM v[20:23],a[72:75],v[196:199],v[20:23]\nds_read_b64_tr_b16 v[220:221],v227 offset:8192\nds_read_b64_tr_b16 v[222:223],v227 offset:12288\nM v[20:23],a[104:107],v[212:215],v[20:23]\nds_read_b64_tr_b16 v[192:193],v226 offset:512\nds_read_b64_tr_b16 v[194:195],v226 offset:4608\nds_read_b64_tr_b16 v[208:209],v226 offset:8704\n" \
"M v[28:31],a[76:79],v[196:199],v[28:31]\nbuffer_load_dwordx4 v[92:95],v240,s[52:55],s65 offen offset:3072 sc1\nM v[28:31],a[108:111],v[212:215],v[28:31]\ns_add_u32 s65,s65,4096\nds_read_b64_tr_b16 v[210:211],v226 offset:12800\nM v[36:39],a[80:83],v[196:199],v[36:39]\nv_accvgpr_read_b32 v224,a192\nv_cmp_eq_u32 vcc,2,v248\n" \
"M v[36:39],a[112:115],v[212:215],v[36:39]\ns_cmp_lg_u64 vcc,0\ns_addc_u32 s99,s99,0\nv_xor_b32 v250,0x8000,v250\nM v[44:47],a[84:87],v[196:199],v[44:47]\nbuffer_load_dwordx4 v246,s[40:43],s69 offen offset:3072 lds\nM v[44:47],a[116:119],v[212:215],v[44:47]\nv_add_u32 v229,s64,v250\n" \
"s_lshl_b32 s70,s70,7\nM v[52:55],a[88:91],v[196:199],v[52:55]\nv_lshl_add_u32 v254,v249,2,v254\nv_xor_b32 v254,s70,v254\nM v[52:55],a[120:123],v[212:215],v[52:55]\ns_mov_b32 s97,4096\ns_lshl_b32 s80,s62,9\ns_add_u32 s80,s80,73728\n" \
"M v[60:63],a[92:95],v[196:199],v[60:63]\nbuffer_load_dwordx4 v[104:107],v240,s[52:55],s65 offen offset:0 sc1\nM v[60:63],a[124:127],v[212:215],v[60:63]\nv_and_b32 v236,15,v249\ns_lshl2_add_u32 s74,s63,s7\nM a[196:199],a[208:211],v[196:199],a[196:199]\nds_read_b64_tr_b16 v[196:197],v227 offset:512\nds_read_b64_tr_b16 v[198:199],v227 offset:4608\n" \
"M a[196:199],a[208:211],v[212:215],a[196:199]\nds_read_b64_tr_b16 v[212:213],v227 offset:8704\nds_read_b64_tr_b16 v[214:215],v227 offset:12800\ns_waitcnt lgkmcnt(4)\nM v[128:131],v[64:67],v[200:203],0\ns_lshl_b32 s72,s62,7\nv_lshrrev_b32 v235,4,v249\nv_lshlrev_b32 v236,1,v236\n" \
"s_add_u32 s95,s74,s72\nM v[128:131],v[80:83],v[204:207],v[128:131]\nv_accvgpr_read_b32 v228,a196\nv_add_f32 v225,v224,v228\nv_rcp_f32 v224,v224\nv_lshl_or_b32 v235,v235,8,v236\nM v[132:135],v[68:71],v[200:203],0\nv_mul_f32 v0,v0,v224\n" \
"v_mul_f32 v1,v1,v224\nv_mul_f32 v2,v2,v224\nv_mul_f32 v3,v3,v224\nM v[132:135],v[84:87],v[204:207],v[132:135]\nv_cvt_pk_bf16_f32 v0,v0,v1\nv_mul_f32 v8,v8,v224\nv_mul_f32 v9,v9,v224\nv_mul_f32 v10,v10,v224\n" \
"v_mul_f32 v11,v11,v224\nM v[136:139],v[64:67],v[216:219],0\nbuffer_load_dwordx4 v[108:111],v240,s[52:55],s65 offen offset:1024 sc1\nv_cvt_pk_bf16_f32 v1,v2,v3\nds_read_b64_tr_b16 v[200:201],v226 offset:1024\nM v[136:139],v[80:83],v[220:223],v[136:139]\nds_read_b64_tr_b16 v[202:203],v226 offset:5120\nv_cvt_pk_bf16_f32 v2,v8,v9\n" \
"v_cvt_pk_bf16_f32 v3,v10,v11\nds_read_b64_tr_b16 v[204:205],v226 offset:9216\nds_read_b64_tr_b16 v[206:207],v226 offset:13312\nM v[140:143],v[68:71],v[216:219],0\nbuffer_store_dwordx4 v[0:3],v240,s[52:55],s37 offen offset:0 sc1\nv_rcp_f32 v228,v228\nM v[140:143],v[84:87],v[220:223],v[140:143]\nv_mul_f32 v4,v4,v228\n" \
"v_mul_f32 v5,v5,v228\nds_read_b64_tr_b16 v[216:217],v227 offset:1024\nds_read_b64_tr_b16 v[218:219],v227 offset:5120\nv_mul_f32 v6,v6,v228\nM v[144:147],v[64:67],v[192:195],0\nv_mul_f32 v7,v7,v228\nv_cvt_pk_bf16_f32 v4,v4,v5\nv_mul_f32 v12,v12,v228\n" \
"v_mul_f32 v13,v13,v228\nM v[144:147],v[80:83],v[208:211],v[144:147]\nv_mul_f32 v14,v14,v228\nv_mul_f32 v15,v15,v228\nv_cvt_pk_bf16_f32 v5,v6,v7\nds_read_b64_tr_b16 v[220:221],v227 offset:9216\nds_read_b64_tr_b16 v[222:223],v227 offset:13312\nM v[148:151],v[68:71],v[192:195],0\n" \
"v_cvt_pk_bf16_f32 v6,v12,v13\nv_cvt_pk_bf16_f32 v7,v14,v15\nds_read_b64_tr_b16 v[192:193],v226 offset:1536\nds_read_b64_tr_b16 v[194:195],v226 offset:5632\nM v[148:151],v[84:87],v[208:211],v[148:151]\nbuffer_store_dwordx4 v[4:7],v240,s[52:55],s37 offen offset:1024 sc1\nv_mul_f32 v16,v16,v224\nv_mul_f32 v17,v17,v224\n" \
"s_waitcnt lgkmcnt(6)\nM v[152:155],v[64:67],v[196:199],0\nbuffer_load_dwordx4 v[120:123],v240,s[52:55],s65 offen offset:2048 sc1\nds_read_b64_tr_b16 v[208:209],v226 offset:9728\nds_read_b64_tr_b16 v[210:211],v226 offset:13824\nM v[152:155],v[80:83],v[212:215],v[152:155]\nv_mul_f32 v18,v18,v224\nv_mul_f32 v19,v19,v224\n" \
"v_cvt_pk_bf16_f32 v16,v16,v17\nv_mul_f32 v24,v24,v224\nv_mul_f32 v25,v25,v224\nM v[156:159],v[68:71],v[196:199],0\nv_mul_f32 v26,v26,v224\nv_mul_f32 v27,v27,v224\nv_cvt_pk_bf16_f32 v17,v18,v19\nv_cvt_pk_bf16_f32 v18,v24,v25\n" \
"M v[156:159],v[84:87],v[212:215],v[156:159]\nv_cvt_pk_bf16_f32 v19,v26,v27\nds_read_b64_tr_b16 v[196:197],v227 offset:1536\nds_read_b64_tr_b16 v[198:199],v227 offset:5632\nds_read_b64_tr_b16 v[212:213],v227 offset:9728\nv_mul_f32 v20,v20,v228\nM v[160:163],v[64:67],v[200:203],0\nbuffer_store_dwordx4 v[16:19],v240,s[52:55],s37 offen offset:2048 sc1\n" \
"ds_read_b64_tr_b16 v[214:215],v227 offset:13824\nv_mul_f32 v21,v21,v228\nM v[160:163],v[80:83],v[204:207],v[160:163]\nv_mul_f32 v22,v22,v228\nv_mul_f32 v23,v23,v228\nv_cvt_pk_bf16_f32 v20,v20,v21\nv_mul_f32 v28,v28,v228\nv_mul_f32 v29,v29,v228\n" \
"M v[164:167],v[68:71],v[200:203],0\nv_mul_f32 v30,v30,v228\nv_mul_f32 v31,v31,v228\nv_cvt_pk_bf16_f32 v21,v22,v23\nv_cvt_pk_bf16_f32 v22,v28,v29\nM v[164:167],v[84:87],v[204:207],v[164:167]\nv_cvt_pk_bf16_f32 v23,v30,v31\nds_read_b64_tr_b16 v[200:201],v226 offset:16384\n" \
"ds_read_b64_tr_b16 v[202:203],v226 offset:20480\nds_read_b64_tr_b16 v[204:205],v226 offset:24576\ns_waitcnt lgkmcnt(6)\nM v[168:171],v[64:67],v[216:219],0\nbuffer_load_dwordx4 v[124:127],v240,s[52:55],s65 offen offset:3072 sc1\nds_read_b64_tr_b16 v[206:207],v226 offset:28672\nv_mul_f32 v32,v32,v224\nM v[168:171],v[80:83],v[220:223],v[168:171]\n" \
"buffer_store_dwordx4 v[20:23],v240,s[52:55],s37 offen offset:3072 sc1\nv_mul_f32 v33,v33,v224\nv_mul_f32 v34,v34,v224\nv_mul_f32 v35,v35,v224\nM v[172:175],v[68:71],v[216:219],0\nv_cvt_pk_bf16_f32 v32,v32,v33\nv_mul_f32 v40,v40,v224\nv_mul_f32 v41,v41,v224\n" \
"v_mul_f32 v42,v42,v224\nM v[172:175],v[84:87],v[220:223],v[172:175]\nv_mul_f32 v43,v43,v224\nv_cvt_pk_bf16_f32 v33,v34,v35\nds_read_b64_tr_b16 v[216:217],v227 offset:16384\nds_read_b64_tr_b16 v[218:219],v227 offset:20480\nv_cvt_pk_bf16_f32 v34,v40,v41\nM v[176:179],v[64:67],v[192:195],0\n" \
"v_cvt_pk_bf16_f32 v35,v42,v43\nds_read_b64_tr_b16 v[220:221],v227 offset:24576\nds_read_b64_tr_b16 v[222:223],v227 offset:28672\ns_add_u32 s65,s11,s67\nM v[176:179],v[80:83],v[208:211],v[176:179]\ns_add_u32 s65,s65,1048576\nv_mul_f32 v36,v36,v228\nv_mul_f32 v37,v37,v228\n" \
"v_mul_f32 v38,v38,v228\nv_mul_f32 v39,v39,v228\nM v[180:183],v[68:71],v[192:195],0\nbuffer_store_dwordx4 v[32:35],v240,s[52:55],s38 offen offset:0 sc1\nds_read_b64_tr_b16 v[192:193],v226 offset:16896\nds_read_b64_tr_b16 v[194:195],v226 offset:20992\nM v[180:183],v[84:87],v[208:211],v[180:183]\nv_cvt_pk_bf16_f32 v36,v36,v37\n" \
"v_mul_f32 v44,v44,v228\nv_mul_f32 v45,v45,v228\nv_mul_f32 v46,v46,v228\ns_waitcnt lgkmcnt(6)\nM v[184:187],v[64:67],v[196:199],0\nbuffer_load_dwordx4 a[0:3],v240,s[52:55],s65 offen offset:0 sc1\nv_mul_f32 v47,v47,v228\nv_cvt_pk_bf16_f32 v37,v38,v39\n" \
"M v[184:187],v[80:83],v[212:215],v[184:187]\nds_read_b64_tr_b16 v[208:209],v226 offset:25088\nds_read_b64_tr_b16 v[210:211],v226 offset:29184\nv_cvt_pk_bf16_f32 v38,v44,v45\nv_cvt_pk_bf16_f32 v39,v46,v47\nv_add_f32 v225,v225,v224\nM v[188:191],v[68:71],v[196:199],0\nds_read_b64_tr_b16 v[196:197],v227 offset:16896\n" \
"ds_read_b64_tr_b16 v[198:199],v227 offset:20992\nv_add_f32 v225,v225,v228\nv_cmp_ngt_f32 vcc,s28,v225\nM v[188:191],v[84:87],v[212:215],v[188:191]\nbuffer_store_dwordx4 v[36:39],v240,s[52:55],s38 offen offset:1024 sc1\nv_mul_f32 v48,v48,v224\nv_mul_f32 v49,v49,v224\nds_read_b64_tr_b16 v[212:213],v227 offset:25088\n" \
"M v[128:131],v[96:99],v[200:203],v[128:131]\nds_read_b64_tr_b16 v[214:215],v227 offset:29184\ns_and_b64 vcc,vcc,s[90:91]\ns_addc_u32 s99,s99,0\nv_mul_f32 v50,v50,v224\nM v[128:131],v[112:115],v[204:207],v[128:131]\nv_mul_f32 v51,v51,v224\nv_cvt_pk_bf16_f32 v48,v48,v49\n" \
"v_mul_f32 v56,v56,v224\nv_mul_f32 v57,v57,v224\nv_mul_f32 v58,v58,v224\nM v[132:135],v[100:103],v[200:203],v[132:135]\nv_mul_f32 v59,v59,v224\nv_cvt_pk_bf16_f32 v49,v50,v51\nv_cvt_pk_bf16_f32 v50,v56,v57\nv_cvt_pk_bf16_f32 v51,v58,v59\n" \
"M v[132:135],v[116:119],v[204:207],v[132:135]\nv_mov_b32 v230,s99\nds_write_b32 v229,v230\nds_read_b64_tr_b16 v[200:201],v226 offset:17408\nds_read_b64_tr_b16 v[202:203],v226 offset:21504\ns_waitcnt lgkmcnt(5)\nM v[136:139],v[96:99],v[216:219],v[136:139]\nbuffer_store_dwordx4 v[48:51],v240,s[52:55],s38 offen offset:2048 sc1\n" \
"buffer_load_dwordx4 a[4:7],v240,s[52:55],s65 offen offset:1024 sc1\nM v[136:139],v[112:115],v[220:223],v[136:139]\nds_read_b64_tr_b16 v[204:205],v226 offset:25600\nds_read_b64_tr_b16 v[206:207],v226 offset:29696\nv_mul_f32 v52,v52,v228\nv_mul_f32 v53,v53,v228\nv_mul_f32 v54,v54,v228\nM v[140:143],v[100:103],v[216:219],v[140:143]\n" \
"v_mul_f32 v55,v55,v228\nv_cvt_pk_bf16_f32 v52,v52,v53\nv_mul_f32 v60,v60,v228\nv_mul_f32 v61,v61,v228\nM v[140:143],v[116:119],v[220:223],v[140:143]\nv_mul_f32 v62,v62,v228\nv_mul_f32 v63,v63,v228\nv_cvt_pk_bf16_f32 v53,v54,v55\n" \
"v_cvt_pk_bf16_f32 v54,v60,v61\nv_cvt_pk_bf16_f32 v55,v62,v63\nM v[144:147],v[96:99],v[192:195],v[144:147]\nds_read_b64_tr_b16 v[216:217],v227 offset:17408\nds_read_b64_tr_b16 v[218:219],v227 offset:21504\nds_read_b64_tr_b16 v[220:221],v227 offset:25600\ns_lshl4_add_u32 s69,s14,s68\nM v[144:147],v[112:115],v[208:211],v[144:147]\n" \
"buffer_store_dwordx4 v[52:55],v240,s[52:55],s38 offen offset:3072 sc1\nds_read_b64_tr_b16 v[222:223],v227 offset:29696\ns_add_u32 m0,s64,65536\nv_lshl_add_u32 v229,v251,10,v250\nM v[148:151],v[100:103],v[192:195],v[148:151]\nds_read_b64_tr_b16 v[192:193],v226 offset:17920\nds_read_b64_tr_b16 v[194:195],v226 offset:22016\ns_lshl_b32 s95,s95,10\n" \
"v_add_u32 v236,s95,v235\nM v[148:151],v[116:119],v[208:211],v[148:151]\nds_read_b64_tr_b16 v[208:209],v226 offset:26112\nds_read_b64_tr_b16 v[210:211],v226 offset:30208\nv_add_u32 v226,0,v242\ns_sub_u32 s74,s74,s72\ns_add_u32 s74,s74,1920\nM v[152:155],v[96:99],v[196:199],v[152:155]\n" \
"buffer_load_dwordx4 a[8:11],v240,s[52:55],s65 offen offset:2048 sc1\ns_lshl_b32 s74,s74,10\ns_waitcnt lgkmcnt(7)\nM v[152:155],v[112:115],v[212:215],v[152:155]\nv_add_u32 v237,s74,v235\ns_lshl_b32 s72,s7,9\nv_lshl_add_u32 v239,v249,3,s72\ns_add_u32 s38,s33,16384\n" \
"v_bfe_u32 v228,v249,2,2\nM v[156:159],v[100:103],v[196:199],v[156:159]\ns_barrier\nbuffer_load_dwordx4 v243,s[40:43],s69 offen offset:0 lds\nds_read_b64_tr_b16 v[196:197],v227 offset:17920\nM v[156:159],v[116:119],v[212:215],v[156:159]\nds_read_b64_tr_b16 v[198:199],v227 offset:22016\nds_read_b32 v230,v229\n" \
"ds_read_b64_tr_b16 v[212:213],v227 offset:26112\nv_lshrrev_b32 v229,4,v249\ns_add_u32 s95,s95,54525952\nM v[160:163],v[96:99],v[200:203],v[160:163]\nds_read_b64_tr_b16 v[214:215],v227 offset:30208\nv_xor_b32 v227,32,v226\ns_add_u32 s74,s74,54525952\nM v[160:163],v[112:115],v[204:207],v[160:163]\n" \
"M v[164:167],v[100:103],v[200:203],v[164:167]\nds_read_b64_tr_b16 v[200:201],v226 offset:0\nds_read_b64_tr_b16 v[202:203],v226 offset:4096\nM v[164:167],v[116:119],v[204:207],v[164:167]\nds_read_b64_tr_b16 v[204:205],v226 offset:8192\ns_waitcnt lgkmcnt(8)\nM v[168:171],v[96:99],v[216:219],v[168:171]\nbuffer_load_dwordx4 a[12:15],v240,s[52:55],s65 offen offset:3072 sc1\n" \
"M v[168:171],v[112:115],v[220:223],v[168:171]\nds_read_b64_tr_b16 v[206:207],v226 offset:12288\ns_add_u32 s65,s65,4096\nM v[172:175],v[100:103],v[216:219],v[172:175]\nbuffer_load_dwordx4 v244,s[40:43],s69 offen offset:1024 lds\nM v[172:175],v[116:119],v[220:223],v[172:175]\nds_read_b64_tr_b16 v[216:217],v227 offset:0\nds_read_b64_tr_b16 v[218:219],v227 offset:4096\n" \
"M v[176:179],v[96:99],v[192:195],v[176:179]\nds_read_b64_tr_b16 v[220:221],v227 offset:8192\nds_read_b64_tr_b16 v[222:223],v227 offset:12288\nM v[176:179],v[112:115],v[208:211],v[176:179]\ns_waitcnt lgkmcnt(8)\nv_cmp_ne_u32 vcc,0,v230\nM v[180:183],v[100:103],v[192:195],v[180:183]\ns_cbranch_vccnz .Lredo\n" \
"ds_read_b64_tr_b16 v[192:193],v226 offset:512\nM v[180:183],v[116:119],v[208:211],v[180:183]\nds_read_b64_tr_b16 v[194:195],v226 offset:4608\nds_read_b64_tr_b16 v[208:209],v226 offset:8704\nM v[184:187],v[96:99],v[196:199],v[184:187]\nbuffer_load_dwordx4 a[16:19],v240,s[52:55],s65 offen offset:0 sc1\nM v[184:187],v[112:115],v[212:215],v[184:187]\nds_read_b64_tr_b16 v[210:211],v226 offset:12800\n" \
"s_lshl_b32 s70,s2,9\nM v[188:191],v[100:103],v[196:199],v[188:191]\nbuffer_load_dwordx4 v245,s[40:43],s69 offen offset:2048 lds\nM v[188:191],v[116:119],v[212:215],v[188:191]\nds_read_b64_tr_b16 v[196:197],v227 offset:512\ns_waitcnt vmcnt(27) lgkmcnt(6)\nM v[128:131],v[72:75],v[200:203],v[128:131]\nds_read_b64_tr_b16 v[198:199],v227 offset:4608\n" \
"s_waitcnt vmcnt(23)\nM v[128:131],v[88:91],v[204:207],v[128:131]\nds_read_b64_tr_b16 v[212:213],v227 offset:8704\nds_read_b64_tr_b16 v[214:215],v227 offset:12800\nM v[132:135],v[76:79],v[200:203],v[132:135]\nds_read_b64_tr_b16 v[200:201],v226 offset:1024\ns_waitcnt vmcnt(21)\nM v[132:135],v[92:95],v[204:207],v[132:135]\n" \
"ds_read_b64_tr_b16 v[202:203],v226 offset:5120\nds_read_b64_tr_b16 v[204:205],v226 offset:9216\nM v[136:139],v[72:75],v[216:219],v[136:139]\nbuffer_load_dwordx4 a[20:23],v240,s[52:55],s65 offen offset:1024 sc1\ns_waitcnt lgkmcnt(7)\nM v[136:139],v[88:91],v[220:223],v[136:139]\nds_read_b64_tr_b16 v[206:207],v226 offset:13312\ns_lshl_b32 s71,s7,8\n" \
"M v[140:143],v[76:79],v[216:219],v[140:143]\nbuffer_load_dwordx4 v246,s[40:43],s69 offen offset:3072 lds\nM v[140:143],v[92:95],v[220:223],v[140:143]\nds_read_b64_tr_b16 v[216:217],v227 offset:1024\nds_read_b64_tr_b16 v[218:219],v227 offset:5120\nM v[144:147],v[72:75],v[192:195],v[144:147]\nds_read_b64_tr_b16 v[220:221],v227 offset:9216\nds_read_b64_tr_b16 v[222:223],v227 offset:13312\n" \
"M v[144:147],v[88:91],v[208:211],v[144:147]\ns_lshl4_add_u32 s69,s14,s68\ns_add_u32 s69,s69,0x10000\nM v[148:151],v[76:79],v[192:195],v[148:151]\nds_read_b64_tr_b16 v[192:193],v226 offset:1536\nds_read_b64_tr_b16 v[194:195],v226 offset:5632\nM v[148:151],v[92:95],v[208:211],v[148:151]\ns_add_u32 m0,s64,98304\n" \
"s_waitcnt lgkmcnt(7)\nM v[152:155],v[72:75],v[196:199],v[152:155]\nbuffer_load_dwordx4 a[24:27],v240,s[52:55],s65 offen offset:2048 sc1\nM v[152:155],v[88:91],v[212:215],v[152:155]\nds_read_b64_tr_b16 v[208:209],v226 offset:9728\nds_read_b64_tr_b16 v[210:211],v226 offset:13824\nM v[156:159],v[76:79],v[196:199],v[156:159]\nbuffer_load_dwordx4 v243,s[40:43],s69 offen offset:0 lds\n" \
"M v[156:159],v[92:95],v[212:215],v[156:159]\nds_read_b64_tr_b16 v[196:197],v227 offset:1536\nds_read_b64_tr_b16 v[198:199],v227 offset:5632\nM v[160:163],v[72:75],v[200:203],v[160:163]\nds_read_b64_tr_b16 v[212:213],v227 offset:9728\ns_waitcnt lgkmcnt(7)\nM v[160:163],v[88:91],v[204:207],v[160:163]\nds_read_b64_tr_b16 v[214:215],v227 offset:13824\n" \
"v_lshl_add_u32 v241,v249,2,s71\nM v[164:167],v[76:79],v[200:203],v[164:167]\nds_read_b64_tr_b16 v[200:201],v226 offset:32768\nds_read_b64_tr_b16 v[202:203],v226 offset:36864\nM v[164:167],v[92:95],v[204:207],v[164:167]\nds_read_b64_tr_b16 v[204:205],v226 offset:40960\nds_read_b64_tr_b16 v[206:207],v226 offset:45056\nM v[168:171],v[72:75],v[216:219],v[168:171]\n" \
"buffer_load_dwordx4 a[28:31],v240,s[52:55],s65 offen offset:3072 sc1\nM v[168:171],v[88:91],v[220:223],v[168:171]\ns_add_u32 s65,s14,s67\ns_add_u32 s65,s65,1048576\nM v[172:175],v[76:79],v[216:219],v[172:175]\nbuffer_load_dwordx4 v244,s[40:43],s69 offen offset:1024 lds\nM v[172:175],v[92:95],v[220:223],v[172:175]\nds_read_b64_tr_b16 v[216:217],v227 offset:32768\n" \
"s_waitcnt lgkmcnt(6)\nM v[176:179],v[72:75],v[192:195],v[176:179]\nds_read_b64_tr_b16 v[218:219],v227 offset:36864\nds_read_b64_tr_b16 v[220:221],v227 offset:40960\nM v[176:179],v[88:91],v[208:211],v[176:179]\nds_read_b64_tr_b16 v[222:223],v227 offset:45056\ns_add_u32 s70,s70,4096\nM v[180:183],v[76:79],v[192:195],v[180:183]\n" \
"ds_read_b64_tr_b16 v[192:193],v226 offset:33280\nds_read_b64_tr_b16 v[194:195],v226 offset:37376\nM v[180:183],v[92:95],v[208:211],v[180:183]\nds_read_b64_tr_b16 v[208:209],v226 offset:41472\nds_read_b64_tr_b16 v[210:211],v226 offset:45568\nM v[184:187],v[72:75],v[196:199],v[184:187]\nbuffer_load_dwordx4 a[32:35],v240,s[52:55],s65 offen offset:0 sc1\ns_waitcnt lgkmcnt(8)\n" \
"M v[184:187],v[88:91],v[212:215],v[184:187]\nv_add_u32 v230,s70,v241\nv_add_u32 v231,8192,v230\nM v[188:191],v[76:79],v[196:199],v[188:191]\nbuffer_load_dwordx4 v245,s[40:43],s69 offen offset:2048 lds\nM v[188:191],v[92:95],v[212:215],v[188:191]\nds_read_b64_tr_b16 v[196:197],v227 offset:33280\ns_waitcnt vmcnt(26)\n" \
"M v[128:131],v[104:107],v[200:203],v[128:131]\nds_read_b64_tr_b16 v[198:199],v227 offset:37376\ns_waitcnt vmcnt(23)\nM v[128:131],v[120:123],v[204:207],v[128:131]\nds_read_b64_tr_b16 v[212:213],v227 offset:41472\nds_read_b64_tr_b16 v[214:215],v227 offset:45568\nM v[132:135],v[108:111],v[200:203],v[132:135]\nds_read_b64_tr_b16 v[200:201],v226 offset:33792\n" \
"s_waitcnt vmcnt(21)\nM v[132:135],v[124:127],v[204:207],v[132:135]\nds_read_b64_tr_b16 v[202:203],v226 offset:37888\ns_waitcnt lgkmcnt(6)\nM v[136:139],v[104:107],v[216:219],v[136:139]\nbuffer_load_dwordx4 a[36:39],v240,s[52:55],s65 offen offset:1024 sc1\nM v[136:139],v[120:123],v[220:223],v[136:139]\nds_read_b64_tr_b16 v[204:205],v226 offset:41984\n" \
"ds_read_b64_tr_b16 v[206:207],v226 offset:46080\nM v[140:143],v[108:111],v[216:219],v[140:143]\nbuffer_load_dwordx4 v246,s[40:43],s69 offen offset:3072 lds\nM v[140:143],v[124:127],v[220:223],v[140:143]\nds_read_b64_tr_b16 v[216:217],v227 offset:33792\nds_read_b64_tr_b16 v[218:219],v227 offset:37888\nM v[144:147],v[104:107],v[192:195],v[144:147]\nds_read_b64_tr_b16 v[220:221],v227 offset:41984\n" \
"ds_read_b64_tr_b16 v[222:223],v227 offset:46080\nM v[144:147],v[120:123],v[208:211],v[144:147]\nv_mov_b32 v250,83968\nv_accvgpr_write_b32 a194,0\nM v[148:151],v[108:111],v[192:195],v[148:151]\nds_read_b64_tr_b16 v[192:193],v226 offset:34304\nds_read_b64_tr_b16 v[194:195],v226 offset:38400\nM v[148:151],v[124:127],v[208:211],v[148:151]\n" \
"ds_read_b64_tr_b16 v[208:209],v226 offset:42496\ns_waitcnt lgkmcnt(9)\nM v[152:155],v[104:107],v[196:199],v[152:155]\nbuffer_load_dwordx4 a[40:43],v240,s[52:55],s65 offen offset:2048 sc1\nM v[152:155],v[120:123],v[212:215],v[152:155]\nds_read_b64_tr_b16 v[210:211],v226 offset:46592\nv_accvgpr_write_b32 a195,0\nM v[156:159],v[108:111],v[196:199],v[156:159]\n" \
"s_waitcnt vmcnt(16)\nbuffer_store_dword v252,off,s[52:55],s76 sc1\ns_barrier\nM v[156:159],v[124:127],v[212:215],v[156:159]\nds_read_b64_tr_b16 v[196:197],v227 offset:34304\nds_read_b64_tr_b16 v[198:199],v227 offset:38400\nM v[160:163],v[104:107],v[200:203],v[160:163]\nds_read_b64_tr_b16 v[212:213],v227 offset:42496\n" \
"s_waitcnt lgkmcnt(7)\nM v[160:163],v[120:123],v[204:207],v[160:163]\nds_read_b64_tr_b16 v[214:215],v227 offset:46592\nv_accvgpr_write_b32 a196,0\nM v[164:167],v[108:111],v[200:203],v[164:167]\nds_read_b64_tr_b16 v[200:201],v226 offset:16384\nds_read_b64_tr_b16 v[202:203],v226 offset:20480\nM v[164:167],v[124:127],v[204:207],v[164:167]\n" \
"ds_read_b64_tr_b16 v[204:205],v226 offset:24576\nds_read_b64_tr_b16 v[206:207],v226 offset:28672\nM v[168:171],v[104:107],v[216:219],v[168:171]\nbuffer_load_dwordx4 a[44:47],v240,s[52:55],s65 offen offset:3072 sc1\nM v[168:171],v[120:123],v[220:223],v[168:171]\ns_add_u32 s65,s65,4096\nv_accvgpr_write_b32 a197,0\nM v[172:175],v[108:111],v[216:219],v[172:175]\n" \
"ds_read_b64_tr_b16 v[216:217],v227 offset:16384\nds_read_b64_tr_b16 v[218:219],v227 offset:20480\nM v[172:175],v[124:127],v[220:223],v[172:175]\nds_read_b64_tr_b16 v[220:221],v227 offset:24576\ns_waitcnt lgkmcnt(8)\nM v[176:179],v[104:107],v[192:195],v[176:179]\nds_read_b64_tr_b16 v[222:223],v227 offset:28672\ns_load_dwordx2 s[44:45],s[4:5],0x68\n" \
"M v[176:179],v[120:123],v[208:211],v[176:179]\nds_write_b128 v250,a[244:247]\nM v[180:183],v[108:111],v[192:195],v[180:183]\nds_read_b64_tr_b16 v[192:193],v226 offset:16896\nds_read_b64_tr_b16 v[194:195],v226 offset:20992\nM v[180:183],v[124:127],v[208:211],v[180:183]\nds_read_b64_tr_b16 v[208:209],v226 offset:25088\nds_read_b64_tr_b16 v[210:211],v226 offset:29184\n" \
"M v[184:187],v[104:107],v[196:199],v[184:187]\nbuffer_load_dwordx4 a[48:51],v240,s[52:55],s65 offen offset:0 sc1\ns_waitcnt lgkmcnt(9)\nM v[184:187],v[120:123],v[212:215],v[184:187]\ns_mov_b32 s46,2097152\nM v[188:191],v[108:111],v[196:199],v[188:191]\nds_read_b64_tr_b16 v[196:197],v227 offset:16896\nds_read_b64_tr_b16 v[198:199],v227 offset:20992\n" \
"M v[188:191],v[124:127],v[212:215],v[188:191]\nds_read_b64_tr_b16 v[212:213],v227 offset:25088\nds_read_b64_tr_b16 v[214:215],v227 offset:29184\nM v[128:131],a[0:3],v[200:203],v[128:131]\nM v[128:131],a[8:11],v[204:207],v[128:131]\nM v[132:135],a[4:7],v[200:203],v[132:135]\nbuffer_load_dwordx4 a[160:163],v238,s[52:55],0 offen offset:0 sc1\ns_waitcnt vmcnt(18)\n" \
"M v[132:135],a[12:15],v[204:207],v[132:135]\nds_read_b64_tr_b16 v[200:201],v226 offset:17408\ns_waitcnt lgkmcnt(5)\nM v[136:139],a[0:3],v[216:219],v[136:139]\nbuffer_load_dwordx4 a[52:55],v240,s[52:55],s65 offen offset:1024 sc1\nM v[136:139],a[8:11],v[220:223],v[136:139]\nds_read_b64_tr_b16 v[202:203],v226 offset:21504\nds_read_b64_tr_b16 v[204:205],v226 offset:25600\n" \
"M v[140:143],a[4:7],v[216:219],v[140:143]\nds_read_b64_tr_b16 v[206:207],v226 offset:29696\nds_read_b64_tr_b16 v[216:217],v227 offset:17408\nM v[140:143],a[12:15],v[220:223],v[140:143]\nglobal_load_dword a200,v230,s[26:27] offset:-4096\nM v[144:147],a[0:3],v[192:195],v[144:147]\nbuffer_load_dwordx4 a[164:167],v238,s[52:55],0 offen offset:16 sc1\nM v[144:147],a[8:11],v[208:211],v[144:147]\n" \
"ds_read_b64_tr_b16 v[218:219],v227 offset:21504\nglobal_load_dword a201,v230,s[26:27] offset:-3072\nM v[148:151],a[4:7],v[192:195],v[148:151]\nds_read_b64_tr_b16 v[220:221],v227 offset:25600\nds_read_b64_tr_b16 v[222:223],v227 offset:29696\nM v[148:151],a[12:15],v[208:211],v[148:151]\nglobal_load_dword a202,v230,s[26:27] offset:-2048\ns_waitcnt lgkmcnt(7)\n" \
"M v[152:155],a[0:3],v[196:199],v[152:155]\nbuffer_load_dwordx4 a[56:59],v240,s[52:55],s65 offen offset:2048 sc1\nM v[152:155],a[8:11],v[212:215],v[152:155]\nds_read_b64_tr_b16 v[192:193],v226 offset:17920\nds_read_b64_tr_b16 v[194:195],v226 offset:22016\nglobal_load_dword a203,v230,s[26:27] offset:-1024\nM v[156:159],a[4:7],v[196:199],v[156:159]\nds_read_b64_tr_b16 v[208:209],v226 offset:26112\n" \
"ds_read_b64_tr_b16 v[210:211],v226 offset:30208\nbuffer_load_dwordx4 a[168:171],v238,s[52:55],0 offen offset:2048 sc1\nM v[156:159],a[12:15],v[212:215],v[156:159]\nglobal_load_dword a204,v230,s[26:27] offset:0\ns_waitcnt lgkmcnt(6)\nM v[160:163],a[0:3],v[200:203],v[160:163]\nds_read_b64_tr_b16 v[196:197],v227 offset:17920\nds_read_b64_tr_b16 v[198:199],v227 offset:22016\n" \
"M v[160:163],a[8:11],v[204:207],v[160:163]\nglobal_load_dword a205,v230,s[26:27] offset:1024\nM v[164:167],a[4:7],v[200:203],v[164:167]\nds_read_b64_tr_b16 v[212:213],v227 offset:26112\nds_read_b64_tr_b16 v[214:215],v227 offset:30208\nM v[164:167],a[12:15],v[204:207],v[164:167]\nglobal_load_dword a206,v230,s[26:27] offset:2048\nM v[168:171],a[0:3],v[216:219],v[168:171]\n" \
"buffer_load_dwordx4 a[60:63],v240,s[52:55],s65 offen offset:3072 sc1\nbuffer_load_dwordx4 a[172:175],v238,s[52:55],0 offen offset:2064 sc1\ns_waitcnt lgkmcnt(4)\nM v[168:171],a[8:11],v[220:223],v[168:171]\nds_read_b64_tr_b16 v[200:201],v226 offset:49152\nds_read_b64_tr_b16 v[202:203],v226 offset:53248\nglobal_load_dword a207,v230,s[26:27] offset:3072\nM v[172:175],a[4:7],v[216:219],v[172:175]\n" \
"ds_read_b64_tr_b16 v[204:205],v226 offset:57344\nds_read_b64_tr_b16 v[206:207],v226 offset:61440\nM v[172:175],a[12:15],v[220:223],v[172:175]\nglobal_load_dword a208,v231,s[26:27] offset:-4096\nM v[176:179],a[0:3],v[192:195],v[176:179]\nds_read_b64_tr_b16 v[216:217],v227 offset:49152\nds_read_b64_tr_b16 v[218:219],v227 offset:53248\nM v[176:179],a[8:11],v[208:211],v[176:179]\n" \
"global_load_dword a209,v231,s[26:27] offset:-3072\nM v[180:183],a[4:7],v[192:195],v[180:183]\nds_read_b64_tr_b16 v[220:221],v227 offset:57344\nds_read_b64_tr_b16 v[222:223],v227 offset:61440\nbuffer_load_dwordx4 a[176:179],v238,s[52:55],s97 offen offset:0 sc1\nM v[180:183],a[12:15],v[208:211],v[180:183]\nglobal_load_dword a210,v231,s[26:27] offset:-2048\ns_waitcnt lgkmcnt(6)\n" \
"M v[184:187],a[0:3],v[196:199],v[184:187]\nds_read_b64_tr_b16 v[192:193],v226 offset:49664\nds_read_b64_tr_b16 v[194:195],v226 offset:53760\nM v[184:187],a[8:11],v[212:215],v[184:187]\nglobal_load_dword a211,v231,s[26:27] offset:-1024\nM v[188:191],a[4:7],v[196:199],v[188:191]\nds_read_b64_tr_b16 v[208:209],v226 offset:57856\nds_read_b64_tr_b16 v[210:211],v226 offset:61952\n" \
"M v[188:191],a[12:15],v[212:215],v[188:191]\nglobal_load_dword a252,v231,s[26:27] offset:0\ns_waitcnt vmcnt(36)\nM v[128:131],a[16:19],v[200:203],v[128:131]\nbuffer_load_dwordx4 a[180:183],v238,s[52:55],s97 offen offset:16 sc1\ns_waitcnt vmcnt(33) lgkmcnt(4)\nM v[128:131],a[24:27],v[204:207],v[128:131]\nds_read_b64_tr_b16 v[196:197],v227 offset:49664\n" \
"ds_read_b64_tr_b16 v[198:199],v227 offset:53760\nglobal_load_dword a253,v231,s[26:27] offset:1024\nM v[132:135],a[20:23],v[200:203],v[132:135]\nds_read_b64_tr_b16 v[212:213],v227 offset:57856\nds_read_b64_tr_b16 v[214:215],v227 offset:61952\ns_waitcnt vmcnt(32)\nM v[132:135],a[28:31],v[204:207],v[132:135]\nglobal_load_dword a254,v231,s[26:27] offset:2048\n" \
"M v[136:139],a[16:19],v[216:219],v[136:139]\nds_read_b64_tr_b16 v[200:201],v226 offset:50176\nds_read_b64_tr_b16 v[202:203],v226 offset:54272\nM v[136:139],a[24:27],v[220:223],v[136:139]\nglobal_load_dword a255,v231,s[26:27] offset:3072\nM v[140:143],a[20:23],v[216:219],v[140:143]\nds_read_b64_tr_b16 v[204:205],v226 offset:58368\nds_read_b64_tr_b16 v[206:207],v226 offset:62464\n" \
"buffer_load_dwordx4 a[184:187],v238,s[52:55],s97 offen offset:2048 sc1\nM v[140:143],a[28:31],v[220:223],v[140:143]\nds_read_b64_tr_b16 v[216:217],v227 offset:50176\ns_waitcnt lgkmcnt(7)\nM v[144:147],a[16:19],v[192:195],v[144:147]\nbuffer_load_dword v248,v254,s[52:55],s80 offen sc1\nM v[144:147],a[24:27],v[208:211],v[144:147]\nds_read_b64_tr_b16 v[218:219],v227 offset:54272\n" \
"ds_read_b64_tr_b16 v[220:221],v227 offset:58368\nM v[148:151],a[20:23],v[192:195],v[148:151]\nds_read_b64_tr_b16 v[222:223],v227 offset:62464\nds_read_b64_tr_b16 v[192:193],v226 offset:50688\nM v[148:151],a[28:31],v[208:211],v[148:151]\nglobal_load_ushort v64,v236,s[8:9] offset:0\nM v[152:155],a[16:19],v[196:199],v[152:155]\nbuffer_load_dwordx4 a[188:191],v238,s[52:55],s97 offen offset:2064 sc1\n" \
"s_waitcnt lgkmcnt(7)\nM v[152:155],a[24:27],v[212:215],v[152:155]\nds_read_b64_tr_b16 v[194:195],v226 offset:54784\nglobal_load_ushort v80,v237,s[8:9] offset:0\nM v[156:159],a[20:23],v[196:199],v[156:159]\ns_waitcnt vmcnt(33)\ns_barrier\nds_read_b64_tr_b16 v[208:209],v226 offset:58880\n" \
"ds_read_b64_tr_b16 v[210:211],v226 offset:62976\nM v[156:159],a[28:31],v[212:215],v[156:159]\nglobal_load_ushort v96,v235,s[20:21] offset:0\nM v[160:163],a[16:19],v[200:203],v[160:163]\nds_read_b64_tr_b16 v[196:197],v227 offset:50688\ns_waitcnt lgkmcnt(5)\nM v[160:163],a[24:27],v[204:207],v[160:163]\nds_read_b64_tr_b16 v[198:199],v227 offset:54784\n" \
"global_load_ushort v65,v236,s[8:9] offset:32\nM v[164:167],a[20:23],v[200:203],v[164:167]\nds_read_b64_tr_b16 v[212:213],v227 offset:58880\nds_read_b64_tr_b16 v[214:215],v227 offset:62976\nM v[164:167],a[28:31],v[204:207],v[164:167]\nglobal_load_ushort v81,v237,s[8:9] offset:32\nM v[168:171],a[16:19],v[216:219],v[168:171]\nv_add_u32 v226,65536,v242\n" \
"ds_read_b64_tr_b16 v[200:201],v226 offset:0\nM v[168:171],a[24:27],v[220:223],v[168:171]\nds_read_b64_tr_b16 v[202:203],v226 offset:4096\nglobal_load_ushort v97,v235,s[20:21] offset:32\nM v[172:175],a[20:23],v[216:219],v[172:175]\nds_read_b64_tr_b16 v[204:205],v226 offset:8192\nds_read_b64_tr_b16 v[206:207],v226 offset:12288\nM v[172:175],a[28:31],v[220:223],v[172:175]\n" \
"global_load_ushort v66,v236,s[8:9] offset:64\ns_waitcnt lgkmcnt(7)\nM v[176:179],a[16:19],v[192:195],v[176:179]\nv_xor_b32 v227,32,v226\nds_read_b64_tr_b16 v[216:217],v227 offset:0\nM v[176:179],a[24:27],v[208:211],v[176:179]\nds_read_b64_tr_b16 v[218:219],v227 offset:4096\nglobal_load_ushort v82,v237,s[8:9] offset:64\n" \
"M v[180:183],a[20:23],v[192:195],v[180:183]\nds_read_b64_tr_b16 v[220:221],v227 offset:8192\nds_read_b64_tr_b16 v[222:223],v227 offset:12288\nM v[180:183],a[28:31],v[208:211],v[180:183]\nglobal_load_ushort v98,v235,s[20:21] offset:64\ns_waitcnt lgkmcnt(6)\nM v[184:187],a[16:19],v[196:199],v[184:187]\nds_read_b64_tr_b16 v[192:193],v226 offset:512\n" \
"ds_read_b64_tr_b16 v[194:195],v226 offset:4608\nM v[184:187],a[24:27],v[212:215],v[184:187]\nglobal_load_ushort v67,v236,s[8:9] offset:96\nM v[188:191],a[20:23],v[196:199],v[188:191]\nds_read_b64_tr_b16 v[208:209],v226 offset:8704\nds_read_b64_tr_b16 v[210:211],v226 offset:12800\nM v[188:191],a[28:31],v[212:215],v[188:191]\nglobal_load_ushort v83,v237,s[8:9] offset:96\n" \
"M v[128:131],a[32:35],v[200:203],v[128:131]\nds_read_b64_tr_b16 v[196:197],v227 offset:512\ns_waitcnt lgkmcnt(7)\nM v[128:131],a[40:43],v[204:207],v[128:131]\nds_read_b64_tr_b16 v[198:199],v227 offset:4608\nglobal_load_ushort v99,v235,s[20:21] offset:96\nM v[132:135],a[36:39],v[200:203],v[132:135]\nds_read_b64_tr_b16 v[212:213],v227 offset:8704\n" \
"ds_read_b64_tr_b16 v[214:215],v227 offset:12800\ns_waitcnt vmcnt(32)\nM v[132:135],a[44:47],v[204:207],v[132:135]\nglobal_load_ushort v68,v236,s[8:9] offset:128\nM v[136:139],a[32:35],v[216:219],v[136:139]\nds_read_b64_tr_b16 v[200:201],v226 offset:1024\ns_waitcnt lgkmcnt(7)\nM v[136:139],a[40:43],v[220:223],v[136:139]\n" \
"ds_read_b64_tr_b16 v[202:203],v226 offset:5120\nglobal_load_ushort v84,v237,s[8:9] offset:128\nM v[140:143],a[36:39],v[216:219],v[140:143]\nds_read_b64_tr_b16 v[204:205],v226 offset:9216\nds_read_b64_tr_b16 v[206:207],v226 offset:13312\nM v[140:143],a[44:47],v[220:223],v[140:143]\nglobal_load_ushort v100,v235,s[20:21] offset:128\nM v[144:147],a[32:35],v[192:195],v[144:147]\n" \
"ds_read_b64_tr_b16 v[216:217],v227 offset:1024\ns_waitcnt lgkmcnt(7)\nM v[144:147],a[40:43],v[208:211],v[144:147]\nds_read_b64_tr_b16 v[218:219],v227 offset:5120\nglobal_load_ushort v69,v236,s[8:9] offset:160\nM v[148:151],a[36:39],v[192:195],v[148:151]\nds_read_b64_tr_b16 v[220:221],v227 offset:9216\nds_read_b64_tr_b16 v[222:223],v227 offset:13312\n" \
"M v[148:151],a[44:47],v[208:211],v[148:151]\nglobal_load_ushort v85,v237,s[8:9] offset:160\nM v[152:155],a[32:35],v[196:199],v[152:155]\nds_read_b64_tr_b16 v[192:193],v226 offset:1536\ns_waitcnt lgkmcnt(7)\nM v[152:155],a[40:43],v[212:215],v[152:155]\nds_read_b64_tr_b16 v[194:195],v226 offset:5632\nglobal_load_ushort v101,v235,s[20:21] offset:160\n" \
"M v[156:159],a[36:39],v[196:199],v[156:159]\nds_read_b64_tr_b16 v[208:209],v226 offset:9728\nds_read_b64_tr_b16 v[210:211],v226 offset:13824\nM v[156:159],a[44:47],v[212:215],v[156:159]\nglobal_load_ushort v70,v236,s[8:9] offset:192\nM v[160:163],a[32:35],v[200:203],v[160:163]\nds_read_b64_tr_b16 v[196:197],v227 offset:1536\ns_waitcnt lgkmcnt(7)\n" \
"M v[160:163],a[40:43],v[204:207],v[160:163]\nds_read_b64_tr_b16 v[198:199],v227 offset:5632\nglobal_load_ushort v86,v237,s[8:9] offset:192\nM v[164:167],a[36:39],v[200:203],v[164:167]\nds_read_b64_tr_b16 v[212:213],v227 offset:9728\nds_read_b64_tr_b16 v[214:215],v227 offset:13824\nM v[164:167],a[44:47],v[204:207],v[164:167]\ns_waitcnt vmcnt(21)\n" \
"v_cmp_eq_u32 vcc,0,v248\nglobal_load_ushort v102,v235,s[20:21] offset:192\nM v[168:171],a[32:35],v[216:219],v[168:171]\ns_cbranch_vccnz .Lps7\n.Lpb7:\nbuffer_load_dword v248,v251,s[52:55],s79 offen sc1\ns_waitcnt lgkmcnt(4)\nM v[168:171],a[40:43],v[220:223],v[168:171]\n" \
"ds_read_b64_tr_b16 v[200:201],v226 offset:32768\nds_read_b64_tr_b16 v[202:203],v226 offset:36864\nglobal_load_ushort v71,v236,s[8:9] offset:224\nM v[172:175],a[36:39],v[216:219],v[172:175]\nds_read_b64_tr_b16 v[204:205],v226 offset:40960\nds_read_b64_tr_b16 v[206:207],v226 offset:45056\nM v[172:175],a[44:47],v[220:223],v[172:175]\nglobal_load_ushort v87,v237,s[8:9] offset:224\n" \
"M v[176:179],a[32:35],v[192:195],v[176:179]\nds_read_b64_tr_b16 v[216:217],v227 offset:32768\nds_read_b64_tr_b16 v[218:219],v227 offset:36864\nM v[176:179],a[40:43],v[208:211],v[176:179]\nglobal_load_ushort v103,v235,s[20:21] offset:224\nM v[180:183],a[36:39],v[192:195],v[180:183]\nds_read_b64_tr_b16 v[220:221],v227 offset:40960\nds_read_b64_tr_b16 v[222:223],v227 offset:45056\n" \
"M v[180:183],a[44:47],v[208:211],v[180:183]\nbuffer_load_dwordx2 v[112:113],v239,s[52:55],0 offen offset:0 sc1\ns_waitcnt lgkmcnt(8)\nM v[184:187],a[32:35],v[196:199],v[184:187]\nds_read_b64_tr_b16 v[192:193],v226 offset:33280\nds_read_b64_tr_b16 v[194:195],v226 offset:37376\nM v[184:187],a[40:43],v[212:215],v[184:187]\nbuffer_load_dwordx2 v[114:115],v239,s[52:55],0 offen offset:2048 sc1\n" \
"M v[188:191],a[36:39],v[196:199],v[188:191]\nds_read_b64_tr_b16 v[208:209],v226 offset:41472\nds_read_b64_tr_b16 v[210:211],v226 offset:45568\nM v[188:191],a[44:47],v[212:215],v[188:191]\nbuffer_load_dwordx2 v[116:117],v239,s[52:55],s97 offen offset:0 sc1\ns_waitcnt lgkmcnt(6)\nM v[128:131],a[48:51],v[200:203],v[128:131]\nds_read_b64_tr_b16 v[196:197],v227 offset:33280\n" \
"ds_read_b64_tr_b16 v[198:199],v227 offset:37376\nM v[128:131],a[56:59],v[204:207],v[128:131]\nbuffer_load_dwordx2 v[118:119],v239,s[52:55],s97 offen offset:2048 sc1\nM v[132:135],a[52:55],v[200:203],v[132:135]\nds_read_b64_tr_b16 v[212:213],v227 offset:41472\nds_read_b64_tr_b16 v[214:215],v227 offset:45568\nM v[132:135],a[60:63],v[204:207],v[132:135]\nds_read_b64_tr_b16 v[200:201],v226 offset:33792\n" \
"ds_read_b64_tr_b16 v[202:203],v226 offset:37888\nM v[136:139],a[48:51],v[216:219],v[136:139]\nds_read_b64_tr_b16 v[204:205],v226 offset:41984\ns_waitcnt lgkmcnt(9)\nM v[136:139],a[56:59],v[220:223],v[136:139]\nds_read_b64_tr_b16 v[206:207],v226 offset:46080\ns_add_u32 s65,s10,s67\nM v[140:143],a[52:55],v[216:219],v[140:143]\n" \
"ds_read_b64_tr_b16 v[216:217],v227 offset:33792\nds_read_b64_tr_b16 v[218:219],v227 offset:37888\nM v[140:143],a[60:63],v[220:223],v[140:143]\nds_read_b64_tr_b16 v[220:221],v227 offset:41984\nds_read_b64_tr_b16 v[222:223],v227 offset:46080\nM v[144:147],a[48:51],v[192:195],v[144:147]\nv_cvt_pk_bf16_f32 v128,v128,v136\ns_waitcnt lgkmcnt(10)\n" \
"M v[144:147],a[56:59],v[208:211],v[144:147]\nv_cvt_pk_bf16_f32 v129,v129,v137\nv_cvt_pk_bf16_f32 v130,v130,v138\nM v[148:151],a[52:55],v[192:195],v[148:151]\nds_read_b64_tr_b16 v[192:193],v226 offset:34304\nds_read_b64_tr_b16 v[194:195],v226 offset:38400\nM v[148:151],a[60:63],v[208:211],v[148:151]\nds_read_b64_tr_b16 v[208:209],v226 offset:42496\n" \
"ds_read_b64_tr_b16 v[210:211],v226 offset:46592\nM v[152:155],a[48:51],v[196:199],v[152:155]\nv_add_u32 v226,131072,v242\ns_waitcnt lgkmcnt(8)\nM v[152:155],a[56:59],v[212:215],v[152:155]\nv_cvt_pk_bf16_f32 v131,v131,v139\nv_min_u32 v136,-2,v128\nM v[156:159],a[52:55],v[196:199],v[156:159]\n" \
"ds_read_b64_tr_b16 v[196:197],v227 offset:34304\nds_read_b64_tr_b16 v[198:199],v227 offset:38400\nM v[156:159],a[60:63],v[212:215],v[156:159]\nds_read_b64_tr_b16 v[212:213],v227 offset:42496\nds_read_b64_tr_b16 v[214:215],v227 offset:46592\nM v[160:163],a[48:51],v[200:203],v[160:163]\nv_xor_b32 v227,32,v226\nv_cvt_pk_bf16_f32 v144,v144,v152\n" \
"M v[160:163],a[56:59],v[204:207],v[160:163]\nv_min_u32 v137,-2,v144\nv_cvt_pk_bf16_f32 v145,v145,v153\nM v[164:167],a[52:55],v[200:203],v[164:167]\nds_read_b64_tr_b16 v[200:201],v226 offset:0\nds_read_b64_tr_b16 v[202:203],v226 offset:4096\nM v[164:167],a[60:63],v[204:207],v[164:167]\nds_read_b64_tr_b16 v[204:205],v226 offset:8192\n" \
"s_waitcnt lgkmcnt(7)\nM v[168:171],a[48:51],v[216:219],v[168:171]\nds_read_b64_tr_b16 v[206:207],v226 offset:12288\nv_cvt_pk_bf16_f32 v146,v146,v154\nM v[168:171],a[56:59],v[220:223],v[168:171]\nv_cvt_pk_bf16_f32 v147,v147,v155\nv_min_u32 v152,-2,v129\nM v[172:175],a[52:55],v[216:219],v[172:175]\n" \
"s_waitcnt vmcnt(7)\nv_cmp_eq_u32 vcc,0,v248\nM v[172:175],a[60:63],v[220:223],v[172:175]\ns_cbranch_vccnz .Lps8\n.Lpb8:\nds_read_b64_tr_b16 v[216:217],v227 offset:0\nM v[176:179],a[48:51],v[192:195],v[176:179]\nbuffer_load_dwordx4 a[64:67],v240,s[52:55],s65 offen offset:0 sc1\n" \
"M v[176:179],a[56:59],v[208:211],v[176:179]\nds_read_b64_tr_b16 v[218:219],v227 offset:4096\nds_read_b64_tr_b16 v[220:221],v227 offset:8192\nM v[180:183],a[52:55],v[192:195],v[180:183]\nds_read_b64_tr_b16 v[222:223],v227 offset:12288\nds_read_b64_tr_b16 v[192:193],v226 offset:512\nM v[180:183],a[60:63],v[208:211],v[180:183]\nds_read_b64_tr_b16 v[194:195],v226 offset:4608\n" \
"s_waitcnt lgkmcnt(6)\nM v[184:187],a[48:51],v[196:199],v[184:187]\nbuffer_load_dwordx4 a[68:71],v240,s[52:55],s65 offen offset:1024 sc1\nM v[184:187],a[56:59],v[212:215],v[184:187]\nds_read_b64_tr_b16 v[208:209],v226 offset:8704\nds_read_b64_tr_b16 v[210:211],v226 offset:12800\nM v[188:191],a[52:55],v[196:199],v[188:191]\nds_read_b64_tr_b16 v[196:197],v227 offset:512\n" \
"ds_read_b64_tr_b16 v[198:199],v227 offset:4608\nM v[188:191],a[60:63],v[212:215],v[188:191]\nds_read_b64_tr_b16 v[212:213],v227 offset:8704\nds_read_b64_tr_b16 v[214:215],v227 offset:12800\nM v[8:11],v[0:3],v[200:203],0\nbuffer_load_dwordx4 a[72:75],v240,s[52:55],s65 offen offset:2048 sc1\nM v[8:11],v[16:19],v[204:207],v[8:11]\nv_cvt_pk_bf16_f32 v160,v160,v168\n" \
"v_cvt_pk_bf16_f32 v176,v176,v184\nM v[12:15],v[4:7],v[200:203],0\nds_read_b64_tr_b16 v[200:201],v226 offset:1024\nds_read_b64_tr_b16 v[202:203],v226 offset:5120\nM v[12:15],v[20:23],v[204:207],v[12:15]\nds_read_b64_tr_b16 v[204:205],v226 offset:9216\ns_waitcnt lgkmcnt(9)\nM v[24:27],v[0:3],v[216:219],0\n" \
"buffer_load_dwordx4 a[76:79],v240,s[52:55],s65 offen offset:3072 sc1\nM v[24:27],v[16:19],v[220:223],v[24:27]\nds_read_b64_tr_b16 v[206:207],v226 offset:13312\ns_add_u32 s65,s65,4096\nM v[28:31],v[4:7],v[216:219],0\nds_read_b64_tr_b16 v[216:217],v227 offset:1024\nds_read_b64_tr_b16 v[218:219],v227 offset:5120\nM v[28:31],v[20:23],v[220:223],v[28:31]\n" \
"ds_read_b64_tr_b16 v[220:221],v227 offset:9216\nds_read_b64_tr_b16 v[222:223],v227 offset:13312\nM v[40:43],v[0:3],v[192:195],0\nbuffer_load_dwordx4 a[80:83],v240,s[52:55],s65 offen offset:0 sc1\ns_waitcnt lgkmcnt(8)\nM v[40:43],v[16:19],v[208:211],v[40:43]\nv_min_u32 v138,-2,v160\nv_min_u32 v139,-2,v176\n" \
"M v[44:47],v[4:7],v[192:195],0\nds_read_b64_tr_b16 v[192:193],v226 offset:1536\nds_read_b64_tr_b16 v[194:195],v226 offset:5632\nM v[44:47],v[20:23],v[208:211],v[44:47]\nds_read_b64_tr_b16 v[208:209],v226 offset:9728\nds_read_b64_tr_b16 v[210:211],v226 offset:13824\nM v[56:59],v[0:3],v[196:199],0\nbuffer_load_dwordx4 a[84:87],v240,s[52:55],s65 offen offset:1024 sc1\n" \
"M v[56:59],v[16:19],v[212:215],v[56:59]\nv_cvt_pk_bf16_f32 v161,v161,v169\nv_cvt_pk_bf16_f32 v177,v177,v185\nM v[60:63],v[4:7],v[196:199],0\nbuffer_store_dwordx4 v[136:139],v253,s[52:55],s33 offen offset:0 sc1\nM v[60:63],v[20:23],v[212:215],v[60:63]\nds_read_b64_tr_b16 v[196:197],v227 offset:1536\ns_waitcnt lgkmcnt(5)\n" \
"M v[72:75],v[0:3],v[200:203],0\nbuffer_load_dwordx4 a[88:91],v240,s[52:55],s65 offen offset:2048 sc1\nM v[72:75],v[16:19],v[204:207],v[72:75]\nds_read_b64_tr_b16 v[198:199],v227 offset:5632\nds_read_b64_tr_b16 v[212:213],v227 offset:9728\nM v[76:79],v[4:7],v[200:203],0\nds_read_b64_tr_b16 v[214:215],v227 offset:13824\nds_read_b64_tr_b16 v[200:201],v226 offset:16384\n" \
"M v[76:79],v[20:23],v[204:207],v[76:79]\nds_read_b64_tr_b16 v[202:203],v226 offset:20480\nds_read_b64_tr_b16 v[204:205],v226 offset:24576\nM v[88:91],v[0:3],v[216:219],0\nbuffer_load_dwordx4 a[92:95],v240,s[52:55],s65 offen offset:3072 sc1\nM v[88:91],v[16:19],v[220:223],v[88:91]\nds_read_b64_tr_b16 v[206:207],v226 offset:28672\ns_add_u32 s65,s11,s67\n" \
"M v[92:95],v[4:7],v[216:219],0\nds_read_b64_tr_b16 v[216:217],v227 offset:16384\nds_read_b64_tr_b16 v[218:219],v227 offset:20480\nM v[92:95],v[20:23],v[220:223],v[92:95]\nds_read_b64_tr_b16 v[220:221],v227 offset:24576\ns_waitcnt lgkmcnt(10)\nM v[104:107],v[0:3],v[192:195],0\nbuffer_load_dwordx4 a[96:99],v240,s[52:55],s65 offen offset:0 sc1\n" \
"M v[104:107],v[16:19],v[208:211],v[104:107]\nds_read_b64_tr_b16 v[222:223],v227 offset:28672\nv_min_u32 v153,-2,v145\nM v[108:111],v[4:7],v[192:195],0\nds_read_b64_tr_b16 v[192:193],v226 offset:16896\nds_read_b64_tr_b16 v[194:195],v226 offset:20992\nM v[108:111],v[20:23],v[208:211],v[108:111]\nv_min_u32 v154,-2,v161\n" \
"s_waitcnt lgkmcnt(7)\nM v[120:123],v[0:3],v[196:199],0\nbuffer_load_dwordx4 a[100:103],v240,s[52:55],s65 offen offset:1024 sc1\nM v[120:123],v[16:19],v[212:215],v[120:123]\nv_min_u32 v155,-2,v177\nds_read_b64_tr_b16 v[208:209],v226 offset:25088\nM v[124:127],v[4:7],v[196:199],0\nds_read_b64_tr_b16 v[210:211],v226 offset:29184\n" \
"buffer_store_dwordx4 v[152:155],v253,s[52:55],s33 offen offset:1024 sc1\nM v[124:127],v[20:23],v[212:215],v[124:127]\nds_read_b64_tr_b16 v[196:197],v227 offset:16896\nds_read_b64_tr_b16 v[198:199],v227 offset:20992\nM v[8:11],v[32:35],v[200:203],v[8:11]\nbuffer_load_dwordx4 a[104:107],v240,s[52:55],s65 offen offset:2048 sc1\ns_waitcnt lgkmcnt(6)\nM v[8:11],v[48:51],v[204:207],v[8:11]\n" \
"ds_read_b64_tr_b16 v[212:213],v227 offset:25088\nds_read_b64_tr_b16 v[214:215],v227 offset:29184\nM v[12:15],v[36:39],v[200:203],v[12:15]\nds_read_b64_tr_b16 v[200:201],v226 offset:17408\nds_read_b64_tr_b16 v[202:203],v226 offset:21504\nM v[12:15],v[52:55],v[204:207],v[12:15]\nds_read_b64_tr_b16 v[204:205],v226 offset:25600\nds_read_b64_tr_b16 v[206:207],v226 offset:29696\n" \
"M v[24:27],v[32:35],v[216:219],v[24:27]\nbuffer_load_dwordx4 a[108:111],v240,s[52:55],s65 offen offset:3072 sc1\nM v[24:27],v[48:51],v[220:223],v[24:27]\ns_add_u32 s65,s65,4096\nv_cvt_pk_bf16_f32 v162,v162,v170\nM v[28:31],v[36:39],v[216:219],v[28:31]\nds_read_b64_tr_b16 v[216:217],v227 offset:17408\nds_read_b64_tr_b16 v[218:219],v227 offset:21504\n" \
"M v[28:31],v[52:55],v[220:223],v[28:31]\nds_read_b64_tr_b16 v[220:221],v227 offset:25600\ns_waitcnt lgkmcnt(9)\nM v[40:43],v[32:35],v[192:195],v[40:43]\nbuffer_load_dwordx4 a[112:115],v240,s[52:55],s65 offen offset:0 sc1\nM v[40:43],v[48:51],v[208:211],v[40:43]\nds_read_b64_tr_b16 v[222:223],v227 offset:29696\nv_cvt_pk_bf16_f32 v163,v163,v171\n" \
"M v[44:47],v[36:39],v[192:195],v[44:47]\nds_read_b64_tr_b16 v[192:193],v226 offset:17920\nds_read_b64_tr_b16 v[194:195],v226 offset:22016\nM v[44:47],v[52:55],v[208:211],v[44:47]\nv_cvt_pk_bf16_f32 v178,v178,v186\nv_min_u32 v168,-2,v130\nM v[56:59],v[32:35],v[196:199],v[56:59]\nbuffer_load_dwordx4 a[116:119],v240,s[52:55],s65 offen offset:1024 sc1\n" \
"s_waitcnt lgkmcnt(6)\nM v[56:59],v[48:51],v[212:215],v[56:59]\nv_min_u32 v169,-2,v146\nv_min_u32 v170,-2,v162\nM v[60:63],v[36:39],v[196:199],v[60:63]\nv_min_u32 v171,-2,v178\nds_read_b64_tr_b16 v[208:209],v226 offset:26112\nds_read_b64_tr_b16 v[210:211],v226 offset:30208\n" \
"buffer_store_dwordx4 v[168:171],v253,s[52:55],s33 offen offset:2048 sc1\nM v[60:63],v[52:55],v[212:215],v[60:63]\nds_read_b64_tr_b16 v[196:197],v227 offset:17920\nds_read_b64_tr_b16 v[198:199],v227 offset:22016\nM v[72:75],v[32:35],v[200:203],v[72:75]\nbuffer_load_dwordx4 a[120:123],v240,s[52:55],s65 offen offset:2048 sc1\nM v[72:75],v[48:51],v[204:207],v[72:75]\nds_read_b64_tr_b16 v[212:213],v227 offset:26112\n" \
"ds_read_b64_tr_b16 v[214:215],v227 offset:30208\nM v[76:79],v[36:39],v[200:203],v[76:79]\nv_add_u32 v226,0,v242\nds_read_b64_tr_b16 v[200:201],v226 offset:0\nM v[76:79],v[52:55],v[204:207],v[76:79]\nds_read_b64_tr_b16 v[202:203],v226 offset:4096\ns_waitcnt lgkmcnt(8)\nM v[88:91],v[32:35],v[216:219],v[88:91]\n" \
"buffer_load_dwordx4 a[124:127],v240,s[52:55],s65 offen offset:3072 sc1\nM v[88:91],v[48:51],v[220:223],v[88:91]\nds_read_b64_tr_b16 v[204:205],v226 offset:8192\nds_read_b64_tr_b16 v[206:207],v226 offset:12288\nM v[92:95],v[36:39],v[216:219],v[92:95]\ns_add_u32 s65,s14,s67\nv_xor_b32 v227,32,v226\nM v[92:95],v[52:55],v[220:223],v[92:95]\n" \
"ds_read_b64_tr_b16 v[216:217],v227 offset:0\nds_read_b64_tr_b16 v[218:219],v227 offset:4096\nM v[104:107],v[32:35],v[192:195],v[104:107]\nbuffer_load_dwordx4 a[128:131],v240,s[52:55],s65 offen offset:0 sc1\ns_waitcnt lgkmcnt(6)\nM v[104:107],v[48:51],v[208:211],v[104:107]\nds_read_b64_tr_b16 v[220:221],v227 offset:8192\nds_read_b64_tr_b16 v[222:223],v227 offset:12288\n" \
"M v[108:111],v[36:39],v[192:195],v[108:111]\nv_cvt_pk_bf16_f32 v179,v179,v187\nds_read_b64_tr_b16 v[192:193],v226 offset:512\nM v[108:111],v[52:55],v[208:211],v[108:111]\nds_read_b64_tr_b16 v[194:195],v226 offset:4608\nv_min_u32 v184,-2,v131\nM v[120:123],v[32:35],v[196:199],v[120:123]\nbuffer_load_dwordx4 a[132:135],v240,s[52:55],s65 offen offset:1024 sc1\n" \
"M v[120:123],v[48:51],v[212:215],v[120:123]\nv_min_u32 v185,-2,v147\nv_min_u32 v186,-2,v163\nM v[124:127],v[36:39],v[196:199],v[124:127]\nv_min_u32 v187,-2,v179\nbuffer_store_dwordx4 v[184:187],v253,s[52:55],s33 offen offset:3072 sc1\nds_read_b64_tr_b16 v[208:209],v226 offset:8704\nds_read_b64_tr_b16 v[210:211],v226 offset:12800\n" \
"M v[124:127],v[52:55],v[212:215],v[124:127]\nds_read_b64_tr_b16 v[196:197],v227 offset:512\ns_waitcnt vmcnt(18) lgkmcnt(7)\nM v[8:11],a[64:67],v[200:203],v[8:11]\nbuffer_load_dwordx4 a[136:139],v240,s[52:55],s65 offen offset:2048 sc1\nM v[8:11],a[72:75],v[204:207],v[8:11]\nds_read_b64_tr_b16 v[198:199],v227 offset:4608\nds_read_b64_tr_b16 v[212:213],v227 offset:8704\n" \
"M v[12:15],a[68:71],v[200:203],v[12:15]\nds_read_b64_tr_b16 v[214:215],v227 offset:12800\nds_read_b64_tr_b16 v[200:201],v226 offset:1024\nM v[12:15],a[76:79],v[204:207],v[12:15]\nds_read_b64_tr_b16 v[202:203],v226 offset:5120\nds_read_b64_tr_b16 v[204:205],v226 offset:9216\nM v[24:27],a[64:67],v[216:219],v[24:27]\nbuffer_load_dwordx4 a[140:143],v240,s[52:55],s65 offen offset:3072 sc1\n" \
"s_waitcnt lgkmcnt(9)\nM v[24:27],a[72:75],v[220:223],v[24:27]\nds_read_b64_tr_b16 v[206:207],v226 offset:13312\ns_add_u32 s65,s65,4096\nM v[28:31],a[68:71],v[216:219],v[28:31]\nds_read_b64_tr_b16 v[216:217],v227 offset:1024\nds_read_b64_tr_b16 v[218:219],v227 offset:5120\nM v[28:31],a[76:79],v[220:223],v[28:31]\n" \
"ds_read_b64_tr_b16 v[220:221],v227 offset:9216\nds_read_b64_tr_b16 v[222:223],v227 offset:13312\nM v[40:43],a[64:67],v[192:195],v[40:43]\nbuffer_load_dwordx4 a[144:147],v240,s[52:55],s65 offen offset:0 sc1\ns_waitcnt lgkmcnt(9)\nM v[40:43],a[72:75],v[208:211],v[40:43]\nv_cvt_pk_bf16_f32 v132,v132,v140\nv_cvt_pk_bf16_f32 v133,v133,v141\n" \
"M v[44:47],a[68:71],v[192:195],v[44:47]\nv_cvt_pk_bf16_f32 v134,v134,v142\nv_cvt_pk_bf16_f32 v135,v135,v143\nM v[44:47],a[76:79],v[208:211],v[44:47]\nv_cvt_pk_bf16_f32 v148,v148,v156\nv_cvt_pk_bf16_f32 v164,v164,v172\nM v[56:59],a[64:67],v[196:199],v[56:59]\nbuffer_load_dwordx4 a[148:151],v240,s[52:55],s65 offen offset:1024 sc1\n" \
"s_waitcnt lgkmcnt(4)\nM v[56:59],a[72:75],v[212:215],v[56:59]\nv_cvt_pk_bf16_f32 v180,v180,v188\nds_read_b64_tr_b16 v[192:193],v226 offset:1536\nds_read_b64_tr_b16 v[194:195],v226 offset:5632\nM v[60:63],a[68:71],v[196:199],v[60:63]\nv_min_u32 v140,-2,v132\nv_min_u32 v141,-2,v148\n" \
"v_min_u32 v142,-2,v164\nv_min_u32 v143,-2,v180\nbuffer_store_dwordx4 v[140:143],v253,s[52:55],s38 offen offset:0 sc1\nds_read_b64_tr_b16 v[208:209],v226 offset:9728\nds_read_b64_tr_b16 v[210:211],v226 offset:13824\nM v[60:63],a[76:79],v[212:215],v[60:63]\nds_read_b64_tr_b16 v[196:197],v227 offset:1536\nds_read_b64_tr_b16 v[198:199],v227 offset:5632\n" \
"M v[72:75],a[64:67],v[200:203],v[72:75]\nbuffer_load_dwordx4 a[152:155],v240,s[52:55],s65 offen offset:2048 sc1\nM v[72:75],a[72:75],v[204:207],v[72:75]\nds_read_b64_tr_b16 v[212:213],v227 offset:9728\nds_read_b64_tr_b16 v[214:215],v227 offset:13824\nM v[76:79],a[68:71],v[200:203],v[76:79]\nds_read_b64_tr_b16 v[200:201],v226 offset:32768\nds_read_b64_tr_b16 v[202:203],v226 offset:36864\n" \
"M v[76:79],a[76:79],v[204:207],v[76:79]\nds_read_b64_tr_b16 v[204:205],v226 offset:40960\ns_waitcnt lgkmcnt(9)\nM v[88:91],a[64:67],v[216:219],v[88:91]\nbuffer_load_dwordx4 a[156:159],v240,s[52:55],s65 offen offset:3072 sc1\nM v[88:91],a[72:75],v[220:223],v[88:91]\nds_read_b64_tr_b16 v[206:207],v226 offset:45056\nv_cvt_pk_bf16_f32 v149,v149,v157\n" \
"M v[92:95],a[68:71],v[216:219],v[92:95]\nds_read_b64_tr_b16 v[216:217],v227 offset:32768\nds_read_b64_tr_b16 v[218:219],v227 offset:36864\nM v[92:95],a[76:79],v[220:223],v[92:95]\nds_read_b64_tr_b16 v[220:221],v227 offset:40960\nds_read_b64_tr_b16 v[222:223],v227 offset:45056\nM v[104:107],a[64:67],v[192:195],v[104:107]\nv_cvt_pk_bf16_f32 v150,v150,v158\n" \
"s_waitcnt lgkmcnt(9)\nM v[104:107],a[72:75],v[208:211],v[104:107]\nv_cvt_pk_bf16_f32 v151,v151,v159\nv_cvt_pk_bf16_f32 v165,v165,v173\nM v[108:111],a[68:71],v[192:195],v[108:111]\nv_cvt_pk_bf16_f32 v181,v181,v189\nds_read_b64_tr_b16 v[192:193],v226 offset:33280\nM v[108:111],a[76:79],v[208:211],v[108:111]\n" \
"ds_read_b64_tr_b16 v[194:195],v226 offset:37376\nv_min_u32 v156,-2,v133\nM v[120:123],a[64:67],v[196:199],v[120:123]\nv_min_u32 v157,-2,v149\ns_waitcnt lgkmcnt(6)\nM v[120:123],a[72:75],v[212:215],v[120:123]\nv_min_u32 v158,-2,v165\nv_min_u32 v159,-2,v181\n" \
"M v[124:127],a[68:71],v[196:199],v[124:127]\nbuffer_store_dwordx4 v[156:159],v253,s[52:55],s38 offen offset:1024 sc1\nds_read_b64_tr_b16 v[208:209],v226 offset:41472\nds_read_b64_tr_b16 v[210:211],v226 offset:45568\nM v[124:127],a[76:79],v[212:215],v[124:127]\nds_read_b64_tr_b16 v[196:197],v227 offset:33280\ns_waitcnt vmcnt(24)\nM v[8:11],a[80:83],v[200:203],v[8:11]\n" \
"ds_read_b64_tr_b16 v[198:199],v227 offset:37376\ns_waitcnt vmcnt(19)\nM v[8:11],a[88:91],v[204:207],v[8:11]\nds_read_b64_tr_b16 v[212:213],v227 offset:41472\nds_read_b64_tr_b16 v[214:215],v227 offset:45568\nM v[12:15],a[84:87],v[200:203],v[12:15]\nds_read_b64_tr_b16 v[200:201],v226 offset:33792\nds_read_b64_tr_b16 v[202:203],v226 offset:37888\n" \
"M v[12:15],a[92:95],v[204:207],v[12:15]\nds_read_b64_tr_b16 v[204:205],v226 offset:41984\ns_waitcnt lgkmcnt(9)\nM v[24:27],a[80:83],v[216:219],v[24:27]\nds_read_b64_tr_b16 v[206:207],v226 offset:46080\nv_cvt_pk_bf16_f32 v166,v166,v174\nM v[24:27],a[88:91],v[220:223],v[24:27]\nv_cvt_pk_bf16_f32 v167,v167,v175\n" \
"v_cvt_pk_bf16_f32 v182,v182,v190\nM v[28:31],a[84:87],v[216:219],v[28:31]\nds_read_b64_tr_b16 v[216:217],v227 offset:33792\nds_read_b64_tr_b16 v[218:219],v227 offset:37888\nM v[28:31],a[92:95],v[220:223],v[28:31]\nds_read_b64_tr_b16 v[220:221],v227 offset:41984\nds_read_b64_tr_b16 v[222:223],v227 offset:46080\nM v[40:43],a[80:83],v[192:195],v[40:43]\n" \
"v_min_u32 v172,-2,v134\ns_waitcnt lgkmcnt(9)\nM v[40:43],a[88:91],v[208:211],v[40:43]\nv_min_u32 v173,-2,v150\nv_min_u32 v174,-2,v166\nM v[44:47],a[84:87],v[192:195],v[44:47]\nds_read_b64_tr_b16 v[192:193],v226 offset:34304\nds_read_b64_tr_b16 v[194:195],v226 offset:38400\n" \
"M v[44:47],a[92:95],v[208:211],v[44:47]\nv_min_u32 v175,-2,v182\nds_read_b64_tr_b16 v[208:209],v226 offset:42496\nM v[56:59],a[80:83],v[196:199],v[56:59]\nds_read_b64_tr_b16 v[210:211],v226 offset:46592\ns_waitcnt lgkmcnt(8)\nM v[56:59],a[88:91],v[212:215],v[56:59]\nv_cvt_pk_bf16_f32 v183,v183,v191\n" \
"v_min_u32 v188,-2,v135\nM v[60:63],a[84:87],v[196:199],v[60:63]\nbuffer_store_dwordx4 v[172:175],v253,s[52:55],s38 offen offset:2048 sc1\nM v[60:63],a[92:95],v[212:215],v[60:63]\nds_read_b64_tr_b16 v[196:197],v227 offset:34304\nds_read_b64_tr_b16 v[198:199],v227 offset:38400\nM v[72:75],a[80:83],v[200:203],v[72:75]\nds_read_b64_tr_b16 v[212:213],v227 offset:42496\n" \
"ds_read_b64_tr_b16 v[214:215],v227 offset:46592\nM v[72:75],a[88:91],v[204:207],v[72:75]\nv_min_u32 v189,-2,v151\nv_min_u32 v190,-2,v167\nM v[76:79],a[84:87],v[200:203],v[76:79]\nds_read_b64_tr_b16 v[200:201],v226 offset:16384\nds_read_b64_tr_b16 v[202:203],v226 offset:20480\nM v[76:79],a[92:95],v[204:207],v[76:79]\n" \
"ds_read_b64_tr_b16 v[204:205],v226 offset:24576\ns_waitcnt lgkmcnt(7)\nM v[88:91],a[80:83],v[216:219],v[88:91]\nds_read_b64_tr_b16 v[206:207],v226 offset:28672\nv_min_u32 v191,-2,v183\nM v[88:91],a[88:91],v[220:223],v[88:91]\nv_cmp_eq_u32 vcc,v228,v229\nv_and_b32 v228,1,v249\n" \
"M v[92:95],a[84:87],v[216:219],v[92:95]\nds_read_b64_tr_b16 v[216:217],v227 offset:16384\nds_read_b64_tr_b16 v[218:219],v227 offset:20480\nM v[92:95],a[92:95],v[220:223],v[92:95]\nds_read_b64_tr_b16 v[220:221],v227 offset:24576\nds_read_b64_tr_b16 v[222:223],v227 offset:28672\nM v[104:107],a[80:83],v[192:195],v[104:107]\nv_lshlrev_b32 v228,4,v228\n" \
"v_mov_b32 v229,0x3f80\nM v[104:107],a[88:91],v[208:211],v[104:107]\nv_lshlrev_b32 v228,v228,v229\nv_cndmask_b32 v228,0,v228,vcc\nM v[108:111],a[84:87],v[192:195],v[108:111]\nds_read_b64_tr_b16 v[192:193],v226 offset:16896\nds_read_b64_tr_b16 v[194:195],v226 offset:20992\nM v[108:111],a[92:95],v[208:211],v[108:111]\n" \
"ds_read_b64_tr_b16 v[208:209],v226 offset:25088\ns_waitcnt lgkmcnt(7)\nM v[120:123],a[80:83],v[196:199],v[120:123]\nds_read_b64_tr_b16 v[210:211],v226 offset:29184\nv_and_b32 v229,2,v249\nM v[120:123],a[88:91],v[212:215],v[120:123]\nv_cmp_eq_u32 vcc,0,v229\nv_and_b32 v244,15,v249\n" \
"M v[124:127],a[84:87],v[196:199],v[124:127]\nbuffer_store_dwordx4 v[188:191],v253,s[52:55],s38 offen offset:3072 sc1\nM v[124:127],a[92:95],v[212:215],v[124:127]\nds_read_b64_tr_b16 v[196:197],v227 offset:16896\nds_read_b64_tr_b16 v[198:199],v227 offset:20992\nM v[8:11],a[96:99],v[200:203],v[8:11]\nds_read_b64_tr_b16 v[212:213],v227 offset:25088\ns_waitcnt vmcnt(16)\n" \
"M v[8:11],a[104:107],v[204:207],v[8:11]\nds_read_b64_tr_b16 v[214:215],v227 offset:29184\nv_cndmask_b32 v229,0,v228,vcc\nM v[12:15],a[100:103],v[200:203],v[12:15]\nds_read_b64_tr_b16 v[200:201],v226 offset:17408\nds_read_b64_tr_b16 v[202:203],v226 offset:21504\nM v[12:15],a[108:111],v[204:207],v[12:15]\nds_read_b64_tr_b16 v[204:205],v226 offset:25600\n" \
"s_waitcnt lgkmcnt(7)\nM v[24:27],a[96:99],v[216:219],v[24:27]\nds_read_b64_tr_b16 v[206:207],v226 offset:29696\nv_and_b32 v245,0x300,v240\nM v[24:27],a[104:107],v[220:223],v[24:27]\nv_lshlrev_b32 v246,11,v249\nv_cmp_lt_u32 vcc,7,v249\nM v[28:31],a[100:103],v[216:219],v[28:31]\n" \
"ds_read_b64_tr_b16 v[216:217],v227 offset:17408\nds_read_b64_tr_b16 v[218:219],v227 offset:21504\nM v[28:31],a[108:111],v[220:223],v[28:31]\nds_read_b64_tr_b16 v[220:221],v227 offset:25600\nds_read_b64_tr_b16 v[222:223],v227 offset:29696\nM v[40:43],a[96:99],v[192:195],v[40:43]\nv_mov_b32 v230,0x40000000\nv_lshl_or_b32 v244,v244,1,v245\n" \
"M v[40:43],a[104:107],v[208:211],v[40:43]\ns_add_u32 s70,s64,114688\nv_cndmask_b32 v246,v246,v230,vcc\nM v[44:47],a[100:103],v[192:195],v[44:47]\nds_read_b64_tr_b16 v[192:193],v226 offset:17920\nds_read_b64_tr_b16 v[194:195],v226 offset:22016\nM v[44:47],a[108:111],v[208:211],v[44:47]\nds_read_b64_tr_b16 v[208:209],v226 offset:26112\n" \
"s_waitcnt lgkmcnt(7)\nM v[56:59],a[96:99],v[196:199],v[56:59]\nds_read_b64_tr_b16 v[210:211],v226 offset:30208\nv_add_u32 v244,s70,v244\nM v[56:59],a[104:107],v[212:215],v[56:59]\nv_add_u32 v245,s70,v240\ns_lshl_b32 s80,s60,11\nM v[60:63],a[100:103],v[196:199],v[60:63]\n" \
"ds_read_b64_tr_b16 v[196:197],v227 offset:17920\nds_read_b64_tr_b16 v[198:199],v227 offset:22016\nM v[60:63],a[108:111],v[212:215],v[60:63]\nds_read_b64_tr_b16 v[212:213],v227 offset:26112\nds_read_b64_tr_b16 v[214:215],v227 offset:30208\nM v[72:75],a[96:99],v[200:203],v[72:75]\nv_and_b32 v230,3,v249\nv_lshrrev_b32 v231,2,v249\n" \
"M v[72:75],a[104:107],v[204:207],v[72:75]\nv_accvgpr_read_b32 v32,a176\nv_accvgpr_read_b32 v33,a177\nM v[76:79],a[100:103],v[200:203],v[76:79]\nds_read_b64_tr_b16 v[200:201],v226 offset:49152\nds_read_b64_tr_b16 v[202:203],v226 offset:53248\nM v[76:79],a[108:111],v[204:207],v[76:79]\nds_read_b64_tr_b16 v[204:205],v226 offset:57344\n" \
"s_waitcnt lgkmcnt(7)\nM v[88:91],a[96:99],v[216:219],v[88:91]\nds_read_b64_tr_b16 v[206:207],v226 offset:61440\nv_accvgpr_read_b32 v34,a178\nM v[88:91],a[104:107],v[220:223],v[88:91]\nv_accvgpr_read_b32 v35,a179\nv_accvgpr_read_b32 v36,a180\nM v[92:95],a[100:103],v[216:219],v[92:95]\n" \
"ds_read_b64_tr_b16 v[216:217],v227 offset:49152\nds_read_b64_tr_b16 v[218:219],v227 offset:53248\nM v[92:95],a[108:111],v[220:223],v[92:95]\nds_read_b64_tr_b16 v[220:221],v227 offset:57344\nds_read_b64_tr_b16 v[222:223],v227 offset:61440\nM v[104:107],a[96:99],v[192:195],v[104:107]\nv_accvgpr_read_b32 v37,a181\nv_accvgpr_read_b32 v38,a182\n" \
"M v[104:107],a[104:107],v[208:211],v[104:107]\nv_accvgpr_read_b32 v39,a183\ns_lshl3_add_u32 s37,s6,s75\nM v[108:111],a[100:103],v[192:195],v[108:111]\nds_read_b64_tr_b16 v[192:193],v226 offset:49664\nds_read_b64_tr_b16 v[194:195],v226 offset:53760\nM v[108:111],a[108:111],v[208:211],v[108:111]\nds_read_b64_tr_b16 v[208:209],v226 offset:57856\n" \
"s_waitcnt lgkmcnt(7)\nM v[120:123],a[96:99],v[196:199],v[120:123]\nds_read_b64_tr_b16 v[210:211],v226 offset:61952\ns_add_u32 s80,s80,557056\nM v[120:123],a[104:107],v[212:215],v[120:123]\nv_lshl_add_u32 v231,v230,5,v231\ns_lshl_b32 s70,s7,8\nM v[124:127],a[100:103],v[196:199],v[124:127]\n" \
"ds_read_b64_tr_b16 v[196:197],v227 offset:49664\nds_read_b64_tr_b16 v[198:199],v227 offset:53760\nM v[124:127],a[108:111],v[212:215],v[124:127]\nds_read_b64_tr_b16 v[212:213],v227 offset:57856\nds_read_b64_tr_b16 v[214:215],v227 offset:61952\nM v[8:11],a[112:115],v[200:203],v[8:11]\nv_add_f32 v32,1.0,v32\ns_waitcnt vmcnt(11)\n" \
"M v[8:11],a[120:123],v[204:207],v[8:11]\nv_add_f32 v33,1.0,v33\nv_add_f32 v34,1.0,v34\nM v[12:15],a[116:119],v[200:203],v[12:15]\nds_read_b64_tr_b16 v[200:201],v226 offset:50176\nds_read_b64_tr_b16 v[202:203],v226 offset:54272\nM v[12:15],a[124:127],v[204:207],v[12:15]\nds_read_b64_tr_b16 v[204:205],v226 offset:58368\n" \
"s_waitcnt lgkmcnt(7)\nM v[24:27],a[112:115],v[216:219],v[24:27]\nds_read_b64_tr_b16 v[206:207],v226 offset:62464\nv_add_f32 v35,1.0,v35\nM v[24:27],a[120:123],v[220:223],v[24:27]\nv_add_f32 v36,1.0,v36\nv_add_f32 v37,1.0,v37\nM v[28:31],a[116:119],v[216:219],v[28:31]\n" \
"ds_read_b64_tr_b16 v[216:217],v227 offset:50176\nds_read_b64_tr_b16 v[218:219],v227 offset:54272\nM v[28:31],a[124:127],v[220:223],v[28:31]\nds_read_b64_tr_b16 v[220:221],v227 offset:58368\nds_read_b64_tr_b16 v[222:223],v227 offset:62464\nM v[40:43],a[112:115],v[192:195],v[40:43]\nv_add_f32 v38,1.0,v38\nv_add_f32 v39,1.0,v39\n" \
"M v[40:43],a[120:123],v[208:211],v[40:43]\nv_add_f32 v116,1.0,v116\nv_add_f32 v117,1.0,v117\nM v[44:47],a[116:119],v[192:195],v[44:47]\nds_read_b64_tr_b16 v[192:193],v226 offset:50688\nds_read_b64_tr_b16 v[194:195],v226 offset:54784\nM v[44:47],a[124:127],v[208:211],v[44:47]\nds_read_b64_tr_b16 v[208:209],v226 offset:58880\n" \
"s_waitcnt lgkmcnt(7)\nM v[56:59],a[112:115],v[196:199],v[56:59]\nds_read_b64_tr_b16 v[210:211],v226 offset:62976\nv_add_u32 v226,65536,v242\nM v[56:59],a[120:123],v[212:215],v[56:59]\ns_add_u32 s37,s37,557056\nv_lshlrev_b32 v251,5,v249\nM v[60:63],a[116:119],v[196:199],v[60:63]\n" \
"ds_read_b64_tr_b16 v[196:197],v227 offset:50688\nds_read_b64_tr_b16 v[198:199],v227 offset:54784\nM v[60:63],a[124:127],v[212:215],v[60:63]\nds_read_b64_tr_b16 v[212:213],v227 offset:58880\nds_read_b64_tr_b16 v[214:215],v227 offset:62976\nM v[72:75],a[112:115],v[200:203],v[72:75]\nv_xor_b32 v227,32,v226\ns_or_b32 s80,s80,s75\n" \
"M v[72:75],a[120:123],v[204:207],v[72:75]\nv_lshl_add_u32 v248,v231,1,s70\nv_accvgpr_read_b32 v0,a160\nM v[76:79],a[116:119],v[200:203],v[76:79]\nds_read_b64_tr_b16 v[200:201],v226 offset:0\nds_read_b64_tr_b16 v[202:203],v226 offset:4096\nM v[76:79],a[124:127],v[204:207],v[76:79]\nds_read_b64_tr_b16 v[204:205],v226 offset:8192\n" \
"s_waitcnt lgkmcnt(7)\nM v[88:91],a[112:115],v[216:219],v[88:91]\nds_read_b64_tr_b16 v[206:207],v226 offset:12288\nv_accvgpr_read_b32 v1,a161\nM v[88:91],a[120:123],v[220:223],v[88:91]\nv_accvgpr_read_b32 v2,a162\nv_accvgpr_read_b32 v3,a163\nM v[92:95],a[116:119],v[216:219],v[92:95]\n" \
"ds_read_b64_tr_b16 v[216:217],v227 offset:0\nds_read_b64_tr_b16 v[218:219],v227 offset:4096\nM v[92:95],a[124:127],v[220:223],v[92:95]\nds_read_b64_tr_b16 v[220:221],v227 offset:8192\nds_read_b64_tr_b16 v[222:223],v227 offset:12288\nM v[104:107],a[112:115],v[192:195],v[104:107]\nv_accvgpr_read_b32 v4,a164\nv_accvgpr_read_b32 v5,a165\n" \
"M v[104:107],a[120:123],v[208:211],v[104:107]\nv_accvgpr_read_b32 v6,a166\nv_accvgpr_read_b32 v7,a167\nM v[108:111],a[116:119],v[192:195],v[108:111]\nds_read_b64_tr_b16 v[192:193],v226 offset:512\nds_read_b64_tr_b16 v[194:195],v226 offset:4608\nM v[108:111],a[124:127],v[208:211],v[108:111]\nds_read_b64_tr_b16 v[208:209],v226 offset:8704\n" \
"s_waitcnt lgkmcnt(7)\nM v[120:123],a[112:115],v[196:199],v[120:123]\nds_read_b64_tr_b16 v[210:211],v226 offset:12800\nv_accvgpr_read_b32 v16,a168\nM v[120:123],a[120:123],v[212:215],v[120:123]\nv_accvgpr_read_b32 v17,a169\nv_accvgpr_read_b32 v18,a170\nM v[124:127],a[116:119],v[196:199],v[124:127]\n" \
"ds_read_b64_tr_b16 v[196:197],v227 offset:512\nds_read_b64_tr_b16 v[198:199],v227 offset:4608\nM v[124:127],a[124:127],v[212:215],v[124:127]\nds_read_b64_tr_b16 v[212:213],v227 offset:8704\nds_read_b64_tr_b16 v[214:215],v227 offset:12800\nM v[8:11],a[128:131],v[200:203],v[8:11]\nv_accvgpr_read_b32 v19,a171\ns_waitcnt vmcnt(6)\n" \
"M v[8:11],a[136:139],v[204:207],v[8:11]\nv_accvgpr_read_b32 v20,a172\nv_accvgpr_read_b32 v21,a173\nM v[12:15],a[132:135],v[200:203],v[12:15]\nds_read_b64_tr_b16 v[200:201],v226 offset:1024\nds_read_b64_tr_b16 v[202:203],v226 offset:5120\nM v[12:15],a[140:143],v[204:207],v[12:15]\nds_read_b64_tr_b16 v[204:205],v226 offset:9216\n" \
"s_waitcnt lgkmcnt(7)\nM v[24:27],a[128:131],v[216:219],v[24:27]\nds_read_b64_tr_b16 v[206:207],v226 offset:13312\nv_accvgpr_read_b32 v22,a174\nM v[24:27],a[136:139],v[220:223],v[24:27]\nv_accvgpr_read_b32 v23,a175\nv_accvgpr_read_b32 v48,a184\nM v[28:31],a[132:135],v[216:219],v[28:31]\n" \
"ds_read_b64_tr_b16 v[216:217],v227 offset:1024\nds_read_b64_tr_b16 v[218:219],v227 offset:5120\nM v[28:31],a[140:143],v[220:223],v[28:31]\nds_read_b64_tr_b16 v[220:221],v227 offset:9216\nds_read_b64_tr_b16 v[222:223],v227 offset:13312\nM v[40:43],a[128:131],v[192:195],v[40:43]\nv_accvgpr_read_b32 v49,a185\nv_accvgpr_read_b32 v50,a186\n" \
"M v[40:43],a[136:139],v[208:211],v[40:43]\nv_accvgpr_read_b32 v51,a187\nv_accvgpr_read_b32 v52,a188\nM v[44:47],a[132:135],v[192:195],v[44:47]\nds_read_b64_tr_b16 v[192:193],v226 offset:1536\nds_read_b64_tr_b16 v[194:195],v226 offset:5632\nM v[44:47],a[140:143],v[208:211],v[44:47]\nds_read_b64_tr_b16 v[208:209],v226 offset:9728\n" \
"s_waitcnt lgkmcnt(7)\nM v[56:59],a[128:131],v[196:199],v[56:59]\nds_read_b64_tr_b16 v[210:211],v226 offset:13824\nv_accvgpr_read_b32 v53,a189\nM v[56:59],a[136:139],v[212:215],v[56:59]\nv_accvgpr_read_b32 v54,a190\nv_accvgpr_read_b32 v55,a191\nM v[60:63],a[132:135],v[196:199],v[60:63]\n" \
"ds_read_b64_tr_b16 v[196:197],v227 offset:1536\nds_read_b64_tr_b16 v[198:199],v227 offset:5632\nM v[60:63],a[140:143],v[212:215],v[60:63]\nds_read_b64_tr_b16 v[212:213],v227 offset:9728\nds_read_b64_tr_b16 v[214:215],v227 offset:13824\nM v[72:75],a[128:131],v[200:203],v[72:75]\nv_lshlrev_b32 v64,16,v64\nv_lshlrev_b32 v65,16,v65\n" \
"M v[72:75],a[136:139],v[204:207],v[72:75]\nv_lshlrev_b32 v66,16,v66\nv_lshlrev_b32 v67,16,v67\nM v[76:79],a[132:135],v[200:203],v[76:79]\nds_read_b64_tr_b16 v[200:201],v226 offset:32768\nds_read_b64_tr_b16 v[202:203],v226 offset:36864\nM v[76:79],a[140:143],v[204:207],v[76:79]\nds_read_b64_tr_b16 v[204:205],v226 offset:40960\n" \
"s_waitcnt lgkmcnt(7)\nM v[88:91],a[128:131],v[216:219],v[88:91]\nds_read_b64_tr_b16 v[206:207],v226 offset:45056\nv_lshlrev_b32 v68,16,v68\nM v[88:91],a[136:139],v[220:223],v[88:91]\nv_lshlrev_b32 v69,16,v69\nv_lshlrev_b32 v70,16,v70\nM v[92:95],a[132:135],v[216:219],v[92:95]\n" \
"ds_read_b64_tr_b16 v[216:217],v227 offset:32768\nds_read_b64_tr_b16 v[218:219],v227 offset:36864\nM v[92:95],a[140:143],v[220:223],v[92:95]\nds_read_b64_tr_b16 v[220:221],v227 offset:40960\nds_read_b64_tr_b16 v[222:223],v227 offset:45056\nM v[104:107],a[128:131],v[192:195],v[104:107]\nv_lshlrev_b32 v71,16,v71\nv_lshlrev_b32 v80,16,v80\n" \
"M v[104:107],a[136:139],v[208:211],v[104:107]\nv_lshlrev_b32 v81,16,v81\nv_lshlrev_b32 v82,16,v82\nM v[108:111],a[132:135],v[192:195],v[108:111]\nds_read_b64_tr_b16 v[192:193],v226 offset:33280\nds_read_b64_tr_b16 v[194:195],v226 offset:37376\nM v[108:111],a[140:143],v[208:211],v[108:111]\nds_read_b64_tr_b16 v[208:209],v226 offset:41472\n" \
"s_waitcnt lgkmcnt(7)\nM v[120:123],a[128:131],v[196:199],v[120:123]\nds_read_b64_tr_b16 v[210:211],v226 offset:45568\nv_lshlrev_b32 v83,16,v83\nM v[120:123],a[136:139],v[212:215],v[120:123]\nv_lshlrev_b32 v84,16,v84\nv_lshlrev_b32 v85,16,v85\nM v[124:127],a[132:135],v[196:199],v[124:127]\n" \
"ds_read_b64_tr_b16 v[196:197],v227 offset:33280\nds_read_b64_tr_b16 v[198:199],v227 offset:37376\nM v[124:127],a[140:143],v[212:215],v[124:127]\nds_read_b64_tr_b16 v[212:213],v227 offset:41472\nds_read_b64_tr_b16 v[214:215],v227 offset:45568\nM v[8:11],a[144:147],v[200:203],v[8:11]\nv_lshlrev_b32 v86,16,v86\ns_waitcnt vmcnt(3)\n" \
"M v[8:11],a[152:155],v[204:207],v[8:11]\nv_lshlrev_b32 v87,16,v87\nv_lshlrev_b32 v96,16,v96\nM v[12:15],a[148:151],v[200:203],v[12:15]\nds_read_b64_tr_b16 v[200:201],v226 offset:33792\nds_read_b64_tr_b16 v[202:203],v226 offset:37888\nM v[12:15],a[156:159],v[204:207],v[12:15]\nds_read_b64_tr_b16 v[204:205],v226 offset:41984\n" \
"s_waitcnt lgkmcnt(7)\nM v[24:27],a[144:147],v[216:219],v[24:27]\nds_read_b64_tr_b16 v[206:207],v226 offset:46080\nv_lshlrev_b32 v97,16,v97\nM v[24:27],a[152:155],v[220:223],v[24:27]\nv_lshlrev_b32 v98,16,v98\nv_lshlrev_b32 v99,16,v99\nM v[28:31],a[148:151],v[216:219],v[28:31]\n" \
"v_lshlrev_b32 v100,16,v100\nv_lshlrev_b32 v101,16,v101\nM v[28:31],a[156:159],v[220:223],v[28:31]\nv_lshlrev_b32 v102,16,v102\nv_lshlrev_b32 v103,16,v103\nM v[40:43],a[144:147],v[192:195],v[40:43]\nv_cvt_pk_bf16_f32 v32,0,v32\nv_cvt_pk_bf16_f32 v33,0,v33\n" \
"v_cvt_pk_bf16_f32 v34,0,v34\nv_cvt_pk_bf16_f32 v35,0,v35\nv_cvt_pk_bf16_f32 v36,0,v36\nv_cvt_pk_bf16_f32 v37,0,v37\nv_cvt_pk_bf16_f32 v38,0,v38\nv_cvt_pk_bf16_f32 v39,0,v39\nv_cvt_pk_bf16_f32 v116,0,v116\nv_cvt_pk_bf16_f32 v117,0,v117\n" \
"M v[40:43],a[152:155],v[208:211],v[40:43]\nds_read_b64_tr_b16 v[216:217],v227 offset:33792\nds_read_b64_tr_b16 v[218:219],v227 offset:37888\nM v[44:47],a[148:151],v[192:195],v[44:47]\nds_read_b64_tr_b16 v[220:221],v227 offset:41984\nds_read_b64_tr_b16 v[222:223],v227 offset:46080\nM v[44:47],a[156:159],v[208:211],v[44:47]\nds_read_b64_tr_b16 v[192:193],v226 offset:34304\n" \
"s_waitcnt lgkmcnt(5)\nM v[56:59],a[144:147],v[196:199],v[56:59]\nds_read_b64_tr_b16 v[194:195],v226 offset:38400\nds_read_b64_tr_b16 v[208:209],v226 offset:42496\nM v[56:59],a[152:155],v[212:215],v[56:59]\nds_read_b64_tr_b16 v[210:211],v226 offset:46592\nv_cvt_pk_bf16_f32 v8,v8,v24\nM v[60:63],a[148:151],v[196:199],v[60:63]\n" \
"ds_read_b64_tr_b16 v[196:197],v227 offset:34304\nds_read_b64_tr_b16 v[198:199],v227 offset:38400\nM v[60:63],a[156:159],v[212:215],v[60:63]\nds_read_b64_tr_b16 v[212:213],v227 offset:42496\nds_read_b64_tr_b16 v[214:215],v227 offset:46592\nM v[72:75],a[144:147],v[200:203],v[72:75]\nv_cvt_pk_bf16_f32 v9,v9,v25\nv_cvt_pk_bf16_f32 v10,v10,v26\n" \
"M v[72:75],a[152:155],v[204:207],v[72:75]\nv_cvt_pk_bf16_f32 v11,v11,v27\nv_cvt_pk_bf16_f32 v40,v40,v56\nM v[76:79],a[148:151],v[200:203],v[76:79]\nv_cvt_pk_bf16_f32 v41,v41,v57\nv_cvt_pk_bf16_f32 v42,v42,v58\nM v[76:79],a[156:159],v[204:207],v[76:79]\nv_cvt_pk_bf16_f32 v43,v43,v59\n" \
"s_waitcnt lgkmcnt(6)\nM v[88:91],a[144:147],v[216:219],v[88:91]\nv_cvt_pk_bf16_f32 v12,v12,v28\nv_cvt_pk_bf16_f32 v13,v13,v29\nM v[88:91],a[152:155],v[220:223],v[88:91]\nv_cvt_pk_bf16_f32 v14,v14,v30\nv_cvt_pk_bf16_f32 v15,v15,v31\nM v[92:95],a[148:151],v[216:219],v[92:95]\n" \
"v_cvt_pk_bf16_f32 v44,v44,v60\nv_cvt_pk_bf16_f32 v45,v45,v61\nM v[92:95],a[156:159],v[220:223],v[92:95]\nv_cvt_pk_bf16_f32 v46,v46,v62\nv_cvt_pk_bf16_f32 v47,v47,v63\nM v[104:107],a[144:147],v[192:195],v[104:107]\nv_cvt_pk_bf16_f32 v72,v72,v88\ns_waitcnt lgkmcnt(0)\n" \
"M v[104:107],a[152:155],v[208:211],v[104:107]\nv_cvt_pk_bf16_f32 v73,v73,v89\nv_cvt_pk_bf16_f32 v74,v74,v90\nM v[108:111],a[148:151],v[192:195],v[108:111]\nv_cvt_pk_bf16_f32 v75,v75,v91\nv_cvt_pk_bf16_f32 v76,v76,v92\nM v[108:111],a[156:159],v[208:211],v[108:111]\nv_cvt_pk_bf16_f32 v77,v77,v93\n" \
"v_cvt_pk_bf16_f32 v78,v78,v94\nM v[120:123],a[144:147],v[196:199],v[120:123]\nv_cvt_pk_bf16_f32 v79,v79,v95\nv_xor_b32 v228,v228,v229\nM v[120:123],a[152:155],v[212:215],v[120:123]\ns_add_u32 s38,s78,16384\nv_min_u32 v24,-2,v8\nM v[124:127],a[148:151],v[196:199],v[124:127]\n" \
"v_min_u32 v25,-2,v40\nv_min_u32 v26,-2,v72\nM v[124:127],a[156:159],v[212:215],v[124:127]\nv_min_u32 v56,-2,v9\nv_min_u32 v57,-2,v41\nv_cvt_pk_bf16_f32 v104,v104,v120\nv_cvt_pk_bf16_f32 v105,v105,v121\nv_cvt_pk_bf16_f32 v106,v106,v122\n" \
"v_cvt_pk_bf16_f32 v107,v107,v123\nv_min_u32 v27,-2,v104\nv_min_u32 v58,-2,v73\nv_cvt_pk_bf16_f32 v108,v108,v124\nv_cvt_pk_bf16_f32 v109,v109,v125\nv_cvt_pk_bf16_f32 v110,v110,v126\nv_cvt_pk_bf16_f32 v111,v111,v127\nv_min_u32 v59,-2,v105\n" \
"v_min_u32 v88,-2,v10\nv_min_u32 v89,-2,v42\nv_min_u32 v90,-2,v74\nv_min_u32 v91,-2,v106\nv_min_u32 v120,-2,v11\nv_min_u32 v121,-2,v43\nv_min_u32 v122,-2,v75\nv_min_u32 v123,-2,v107\n" \
"v_min_u32 v28,-2,v12\nv_min_u32 v29,-2,v44\nv_min_u32 v30,-2,v76\nv_min_u32 v31,-2,v108\nv_min_u32 v60,-2,v13\nv_min_u32 v61,-2,v45\nv_min_u32 v62,-2,v77\nv_min_u32 v63,-2,v109\n" \
"v_min_u32 v92,-2,v14\nv_min_u32 v93,-2,v46\nv_min_u32 v94,-2,v78\nv_min_u32 v95,-2,v110\nv_min_u32 v124,-2,v15\nv_min_u32 v125,-2,v47\nv_min_u32 v126,-2,v79\nv_min_u32 v127,-2,v111\n" \
"v_accvgpr_write_b32 a192,v229\nv_accvgpr_write_b32 a193,v228\nv_accvgpr_write_b32 a198,v229\nv_accvgpr_write_b32 a199,v228\nbuffer_store_dwordx4 v[24:27],v253,s[52:55],s78 offen offset:0 sc1\nbuffer_store_dwordx4 v[56:59],v253,s[52:55],s78 offen offset:1024 sc1\nbuffer_store_dwordx4 v[88:91],v253,s[52:55],s78 offen offset:2048 sc1\nbuffer_store_dwordx4 v[120:123],v253,s[52:55],s78 offen offset:3072 sc1\n" \
"buffer_store_dwordx4 v[28:31],v253,s[52:55],s38 offen offset:0 sc1\nbuffer_store_dwordx4 v[60:63],v253,s[52:55],s38 offen offset:1024 sc1\nbuffer_store_dwordx4 v[92:95],v253,s[52:55],s38 offen offset:2048 sc1\nbuffer_store_dwordx4 v[124:127],v253,s[52:55],s38 offen offset:3072 sc1\n.Ltail:\ns_add_u32 s72,s95,0\nbuffer_load_dwordx4 v[160:163],v240,s[52:55],s72 offen sc1\ns_add_u32 s72,s95,2097152\n" \
"buffer_load_dwordx4 v[164:167],v240,s[52:55],s72 offen sc1\ns_add_u32 s72,s95,4194304\nbuffer_load_dwordx4 v[168:171],v240,s[52:55],s72 offen sc1\ns_add_u32 s72,s95,6291456\nbuffer_load_dwordx4 v[172:175],v240,s[52:55],s72 offen sc1\ns_add_u32 s72,s95,8388608\nbuffer_load_dwordx4 v[176:179],v240,s[52:55],s72 offen sc1\ns_add_u32 s72,s95,10485760\n" \
"buffer_load_dwordx4 v[180:183],v240,s[52:55],s72 offen sc1\ns_add_u32 s72,s95,12582912\nbuffer_load_dwordx4 v[184:187],v240,s[52:55],s72 offen sc1\ns_add_u32 s72,s95,14680064\nbuffer_load_dwordx4 v[188:191],v240,s[52:55],s72 offen sc1\n.Ltail2:\ns_add_u32 s72,s74,0\nbuffer_load_dwordx4 v[128:131],v240,s[52:55],s72 offen sc1\n" \
"s_add_u32 s72,s74,2097152\nbuffer_load_dwordx4 v[132:135],v240,s[52:55],s72 offen sc1\ns_add_u32 s72,s74,4194304\nbuffer_load_dwordx4 v[136:139],v240,s[52:55],s72 offen sc1\ns_add_u32 s72,s74,6291456\nbuffer_load_dwordx4 v[140:143],v240,s[52:55],s72 offen sc1\ns_add_u32 s72,s74,8388608\nbuffer_load_dwordx4 v[144:147],v240,s[52:55],s72 offen sc1\n" \
"s_add_u32 s72,s74,10485760\nbuffer_load_dwordx4 v[148:151],v240,s[52:55],s72 offen sc1\ns_add_u32 s72,s74,12582912\nbuffer_load_dwordx4 v[152:155],v240,s[52:55],s72 offen sc1\ns_add_u32 s72,s74,14680064\nbuffer_load_dwordx4 v[156:159],v240,s[52:55],s72 offen sc1\ns_mov_b32 s94,0\ns_mov_b32 s29,1\n" \
"s_cmp_eq_u32 s3,0\ns_cbranch_scc1 .Lnp\ns_mov_b32 s70,0x05040100\ns_mov_b32 s71,0x07060302\nv_accvgpr_read_b32 v228,a212\nv_accvgpr_read_b32 v229,a213\nv_accvgpr_read_b32 v230,a214\nv_accvgpr_read_b32 v231,a215\n" \
"v_perm_b32 v192,v230,v228,s70\nv_perm_b32 v193,v230,v228,s71\nv_perm_b32 v194,v231,v229,s70\nv_perm_b32 v195,v231,v229,s71\nv_accvgpr_read_b32 v228,a216\nv_accvgpr_read_b32 v229,a217\nv_accvgpr_read_b32 v230,a218\nv_accvgpr_read_b32 v231,a219\n" \
"v_perm_b32 v196,v230,v228,s70\nv_perm_b32 v197,v230,v228,s71\nv_perm_b32 v198,v231,v229,s70\nv_perm_b32 v199,v231,v229,s71\nv_accvgpr_read_b32 v228,a220\nv_accvgpr_read_b32 v229,a221\nv_accvgpr_read_b32 v230,a222\nv_accvgpr_read_b32 v231,a223\n" \
"v_perm_b32 v200,v230,v228,s70\nv_perm_b32 v201,v230,v228,s71\nv_perm_b32 v202,v231,v229,s70\nv_perm_b32 v203,v231,v229,s71\nv_accvgpr_read_b32 v228,a224\nv_accvgpr_read_b32 v229,a225\nv_accvgpr_read_b32 v230,a226\nv_accvgpr_read_b32 v231,a227\n" \
"v_perm_b32 v204,v230,v228,s70\nv_perm_b32 v205,v230,v228,s71\nv_perm_b32 v206,v231,v229,s70\nv_perm_b32 v207,v231,v229,s71\nv_accvgpr_read_b32 v228,a228\nv_accvgpr_read_b32 v229,a229\nv_accvgpr_read_b32 v230,a230\nv_accvgpr_read_b32 v231,a231\n" \
"v_perm_b32 v208,v230,v228,s70\nv_perm_b32 v209,v230,v228,s71\nv_perm_b32 v210,v231,v229,s70\nv_perm_b32 v211,v231,v229,s71\nv_accvgpr_read_b32 v228,a232\nv_accvgpr_read_b32 v229,a233\nv_accvgpr_read_b32 v230,a234\nv_accvgpr_read_b32 v231,a235\n" \
"v_perm_b32 v212,v230,v228,s70\nv_perm_b32 v213,v230,v228,s71\nv_perm_b32 v214,v231,v229,s70\nv_perm_b32 v215,v231,v229,s71\nv_accvgpr_read_b32 v228,a236\nv_accvgpr_read_b32 v229,a237\nv_accvgpr_read_b32 v230,a238\nv_accvgpr_read_b32 v231,a239\n" \
"v_perm_b32 v216,v230,v228,s70\nv_perm_b32 v217,v230,v228,s71\nv_perm_b32 v218,v231,v229,s70\nv_perm_b32 v219,v231,v229,s71\nv_accvgpr_read_b32 v228,a240\nv_accvgpr_read_b32 v229,a241\nv_accvgpr_read_b32 v230,a242\nv_accvgpr_read_b32 v231,a243\n" \
"v_perm_b32 v220,v230,v228,s70\nv_perm_b32 v221,v230,v228,s71\nv_perm_b32 v222,v231,v229,s70\nv_perm_b32 v223,v231,v229,s71\nv_and_b32 v228,3,v249\nv_lshrrev_b32 v229,4,v249\nv_and_b32 v230,1,v229\nv_lshrrev_b32 v229,1,v229\n" \
"v_lshlrev_b32 v230,5,v230\nv_lshl_or_b32 v230,v228,6,v230\nv_lshl_or_b32 v230,v229,8,v230\ns_lshl2_add_u32 s70,s7,84032\nv_add_u32 v252,s70,v230\nv_and_b32 v230,0xf0,v240\nv_add_u32 v253,84032,v230\n.Lnp:\n" \
".Lrow1:\ns_waitcnt vmcnt(15)\nM v[72:75],a[192:195],v[160:163],0\nM v[76:79],a[196:199],v[160:163],0\ns_waitcnt vmcnt(14)\nM v[72:75],a[192:195],v[164:167],v[72:75]\nM v[76:79],a[196:199],v[164:167],v[76:79]\ns_waitcnt vmcnt(13)\n" \
"M v[72:75],a[192:195],v[168:171],v[72:75]\nM v[76:79],a[196:199],v[168:171],v[76:79]\ns_waitcnt vmcnt(12)\nM v[72:75],a[192:195],v[172:175],v[72:75]\nM v[76:79],a[196:199],v[172:175],v[76:79]\ns_waitcnt vmcnt(11)\nM v[72:75],a[192:195],v[176:179],v[72:75]\nM v[76:79],a[196:199],v[176:179],v[76:79]\n" \
"s_waitcnt vmcnt(10)\nM v[72:75],a[192:195],v[180:183],v[72:75]\nM v[76:79],a[196:199],v[180:183],v[76:79]\ns_waitcnt vmcnt(9)\nM v[72:75],a[192:195],v[184:187],v[72:75]\nM v[76:79],a[196:199],v[184:187],v[76:79]\ns_waitcnt vmcnt(8)\nM v[72:75],a[192:195],v[188:191],v[72:75]\n" \
"M v[76:79],a[196:199],v[188:191],v[76:79]\ns_sub_u32 s38,56622112,s95\ns_lshr_b32 s38,s38,5\ns_nop 4\nv_cvt_pk_bf16_f32 v72,0,v72\nv_cvt_pk_bf16_f32 v73,0,v73\nv_cvt_pk_bf16_f32 v74,0,v74\nv_cvt_pk_bf16_f32 v75,0,v75\n" \
"v_cvt_pk_bf16_f32 v76,0,v76\nv_cvt_pk_bf16_f32 v77,0,v77\nv_cvt_pk_bf16_f32 v78,0,v78\nv_cvt_pk_bf16_f32 v79,0,v79\nv_pk_mul_f32 v[72:73],v[72:73],v[0:1]\nv_pk_mul_f32 v[74:75],v[74:75],v[2:3]\nv_pk_mul_f32 v[76:77],v[76:77],v[4:5]\nv_pk_mul_f32 v[78:79],v[78:79],v[6:7]\n" \
"v_cvt_pk_bf16_f32 v72,0,v72\nv_cvt_pk_bf16_f32 v73,0,v73\nv_cvt_pk_bf16_f32 v74,0,v74\nv_cvt_pk_bf16_f32 v75,0,v75\nv_cvt_pk_bf16_f32 v76,0,v76\nv_cvt_pk_bf16_f32 v77,0,v77\nv_cvt_pk_bf16_f32 v78,0,v78\nv_cvt_pk_bf16_f32 v79,0,v79\n" \
"v_pk_add_f32 v[72:73],v[72:73],v[64:65]\nv_pk_add_f32 v[74:75],v[74:75],v[66:67]\nv_pk_add_f32 v[76:77],v[76:77],v[68:69]\nv_pk_add_f32 v[78:79],v[78:79],v[70:71]\nv_cvt_pk_bf16_f32 v72,0,v72\nv_cvt_pk_bf16_f32 v73,0,v73\nv_cvt_pk_bf16_f32 v74,0,v74\nv_cvt_pk_bf16_f32 v75,0,v75\n" \
"v_cvt_pk_bf16_f32 v76,0,v76\nv_cvt_pk_bf16_f32 v77,0,v77\nv_cvt_pk_bf16_f32 v78,0,v78\nv_cvt_pk_bf16_f32 v79,0,v79\nv_readfirstlane_b32 s70,v72\ns_nop 1\nv_subrev_f32 v104,s70,v72\nv_subrev_f32 v105,s70,v73\n" \
"v_subrev_f32 v106,s70,v74\nv_subrev_f32 v107,s70,v75\nv_subrev_f32 v108,s70,v76\nv_subrev_f32 v109,s70,v77\nv_subrev_f32 v110,s70,v78\nv_subrev_f32 v111,s70,v79\nv_pk_mul_f32 v[124:125],v[104:105],v[104:105]\nv_pk_add_f32 v[120:121],v[104:105],v[106:107]\n" \
"v_pk_fma_f32 v[124:125],v[106:107],v[106:107],v[124:125]\nv_pk_fma_f32 v[124:125],v[108:109],v[108:109],v[124:125]\nv_pk_fma_f32 v[124:125],v[110:111],v[110:111],v[124:125]\nv_pk_add_f32 v[108:109],v[108:109],v[110:111]\nv_pk_add_f32 v[120:121],v[120:121],v[108:109]\nv_add_f32 v120,v120,v121\nv_add_f32 v121,v124,v125\ns_nop 1\n" \
"v_permlane16_swap_b32 v120,v121\nv_add_f32 v120,v120,v121\nv_mov_b32 v121,v120\ns_nop 1\nv_permlane32_swap_b32 v120,v121\nv_add_f32 v120,v120,v121\ns_nop 1\nv_add_f32_dpp v120,v120,v120 quad_perm:[1,0,3,2] row_mask:0xf bank_mask:0xf\n" \
"s_nop 1\nv_add_f32_dpp v120,v120,v120 quad_perm:[2,3,0,1] row_mask:0xf bank_mask:0xf\ns_nop 1\nv_add_f32_dpp v120,v120,v120 row_half_mirror row_mask:0xf bank_mask:0xf\ns_nop 1\nv_add_f32_dpp v120,v120,v120 row_mirror row_mask:0xf bank_mask:0xf\nv_mul_f32 v120,0x3b000000,v120\nv_mov_b32 v121,v120\n" \
"v_cmp_u_f32 vcc,v120,v120\ns_nop 0\nv_permlane16_swap_b32 v120,v121\ns_cbranch_vccnz .Lrs1\n.Lrb1:\nv_fma_f32 v121,-v120,v120,v121\nv_add_f32 v122,s70,v120\nv_max_f32 v121,0,v121\n" \
"v_cvt_pk_bf16_f32 v122,0,v122\nv_cvt_pk_bf16_f32 v121,0,v121\nv_add_f32 v121,0x3727c5ac,v121\nv_cvt_pk_bf16_f32 v121,0,v121\nv_sqrt_f32 v121,v121\nv_pk_add_f32 v[88:89],v[72:73],v[122:123] op_sel_hi:[1,0] neg_lo:[0,1] neg_hi:[0,1]\nv_cvt_pk_bf16_f32 v121,0,v121\nv_rcp_f32 v123,v121\n" \
"v_cvt_pk_bf16_f32 v88,0,v88\nv_pk_add_f32 v[90:91],v[74:75],v[122:123] op_sel_hi:[1,0] neg_lo:[0,1] neg_hi:[0,1]\nv_pk_add_f32 v[92:93],v[76:77],v[122:123] op_sel_hi:[1,0] neg_lo:[0,1] neg_hi:[0,1]\nv_pk_add_f32 v[94:95],v[78:79],v[122:123] op_sel_hi:[1,0] neg_lo:[0,1] neg_hi:[0,1]\nv_cvt_pk_bf16_f32 v89,0,v89\nv_pk_mul_f32 v[88:89],v[88:89],v[122:123] op_sel:[0,1]\nv_cvt_pk_bf16_f32 v88,0,v88\nv_cvt_pk_bf16_f32 v89,0,v89\n" \
"v_pk_mul_f32 v[88:89],v[88:89],v[32:33]\nv_cvt_pk_bf16_f32 v88,0,v88\nv_cvt_pk_bf16_f32 v89,0,v89\nv_pk_add_f32 v[88:89],v[88:89],v[16:17]\nv_cvt_pk_bf16_f32 v88,0,v88\nv_cvt_pk_bf16_f32 v89,0,v89\nv_pk_mul_f32 v[124:125],v[88:89],v[96:97]\nv_cvt_pk_bf16_f32 v90,0,v90\n" \
"v_cvt_pk_bf16_f32 v91,0,v91\nv_pk_mul_f32 v[90:91],v[90:91],v[122:123] op_sel:[0,1]\nv_cvt_pk_bf16_f32 v90,0,v90\nv_cvt_pk_bf16_f32 v91,0,v91\nv_pk_mul_f32 v[90:91],v[90:91],v[34:35]\nv_cvt_pk_bf16_f32 v90,0,v90\nv_cvt_pk_bf16_f32 v91,0,v91\nv_pk_add_f32 v[90:91],v[90:91],v[18:19]\n" \
"v_cvt_pk_bf16_f32 v90,0,v90\nv_cvt_pk_bf16_f32 v91,0,v91\nv_pk_fma_f32 v[124:125],v[90:91],v[98:99],v[124:125]\nv_cvt_pk_bf16_f32 v92,0,v92\nv_cvt_pk_bf16_f32 v93,0,v93\nv_pk_mul_f32 v[92:93],v[92:93],v[122:123] op_sel:[0,1]\nv_cvt_pk_bf16_f32 v92,0,v92\nv_cvt_pk_bf16_f32 v93,0,v93\n" \
"v_pk_mul_f32 v[92:93],v[92:93],v[36:37]\nv_cvt_pk_bf16_f32 v92,0,v92\nv_cvt_pk_bf16_f32 v93,0,v93\nv_pk_add_f32 v[92:93],v[92:93],v[20:21]\nv_cvt_pk_bf16_f32 v92,0,v92\nv_cvt_pk_bf16_f32 v93,0,v93\nv_pk_fma_f32 v[124:125],v[92:93],v[100:101],v[124:125]\nv_cvt_pk_bf16_f32 v94,0,v94\n" \
"v_cvt_pk_bf16_f32 v95,0,v95\nv_pk_mul_f32 v[94:95],v[94:95],v[122:123] op_sel:[0,1]\nv_cvt_pk_bf16_f32 v94,0,v94\nv_cvt_pk_bf16_f32 v95,0,v95\nv_pk_mul_f32 v[94:95],v[94:95],v[38:39]\nv_cvt_pk_bf16_f32 v94,0,v94\nv_cvt_pk_bf16_f32 v95,0,v95\nv_pk_add_f32 v[94:95],v[94:95],v[22:23]\n" \
"v_cvt_pk_bf16_f32 v94,0,v94\nv_cvt_pk_bf16_f32 v95,0,v95\nv_pk_fma_f32 v[124:125],v[94:95],v[102:103],v[124:125]\nv_add_f32 v124,v124,v125\nv_mov_b32 v125,v124\nv_lshrrev_b32 v104,16,v121\nv_or_b32 v104,v104,v122\nv_permlane16_swap_b32 v124,v125\n" \
"v_add_f32 v124,v124,v125\nv_mov_b32 v125,v124\ns_nop 1\nv_permlane32_swap_b32 v124,v125\nv_add_f32 v124,v124,v125\ns_nop 1\nv_add_f32_dpp v124,v124,v124 quad_perm:[1,0,3,2] row_mask:0xf bank_mask:0xf\ns_nop 1\n" \
"v_add_f32_dpp v124,v124,v124 quad_perm:[2,3,0,1] row_mask:0xf bank_mask:0xf\ns_nop 1\nv_add_f32_dpp v124,v124,v124 row_half_mirror row_mask:0xf bank_mask:0xf\ns_nop 1\nv_add_f32_dpp v124,v124,v124 row_mirror row_mask:0xf bank_mask:0xf\nv_cvt_pk_bf16_f32 v124,0,v124\ns_nop 0\nv_readfirstlane_b32 s77,v124\n" \
"s_nop 0\ns_ashr_i32 s71,s77,31\ns_and_b32 s72,s77,0x7fffffff\ns_or_b32 s71,s71,0x80000000\ns_xor_b32 s77,s77,s71\ns_and_b32 s77,s77,0xffff0000\ns_cmp_gt_u32 s72,0x7f800000\ns_cselect_b32 s77,0,s77\n" \
"s_or_b32 s77,s77,s38\nv_mov_b32 v105,s77\nds_max_u64 v250,v[104:105]\n.Lrow0:\ns_waitcnt vmcnt(7)\nM v[8:11],a[192:195],v[128:131],0\nM v[12:15],a[196:199],v[128:131],0\ns_waitcnt vmcnt(6)\n" \
"M v[8:11],a[192:195],v[132:135],v[8:11]\nM v[12:15],a[196:199],v[132:135],v[12:15]\ns_waitcnt vmcnt(5)\nM v[8:11],a[192:195],v[136:139],v[8:11]\nM v[12:15],a[196:199],v[136:139],v[12:15]\ns_waitcnt vmcnt(4)\nM v[8:11],a[192:195],v[140:143],v[8:11]\nM v[12:15],a[196:199],v[140:143],v[12:15]\n" \
"s_waitcnt vmcnt(3)\nM v[8:11],a[192:195],v[144:147],v[8:11]\nM v[12:15],a[196:199],v[144:147],v[12:15]\ns_waitcnt vmcnt(2)\nM v[8:11],a[192:195],v[148:151],v[8:11]\nM v[12:15],a[196:199],v[148:151],v[12:15]\ns_waitcnt vmcnt(1)\nM v[8:11],a[192:195],v[152:155],v[8:11]\n" \
"M v[12:15],a[196:199],v[152:155],v[12:15]\ns_waitcnt vmcnt(0)\nM v[8:11],a[192:195],v[156:159],v[8:11]\nM v[12:15],a[196:199],v[156:159],v[12:15]\ns_sub_u32 s39,56622112,s74\ns_lshr_b32 s39,s39,5\ns_nop 4\nv_cvt_pk_bf16_f32 v8,0,v8\n" \
"v_cvt_pk_bf16_f32 v9,0,v9\nv_cvt_pk_bf16_f32 v10,0,v10\nv_cvt_pk_bf16_f32 v11,0,v11\nv_cvt_pk_bf16_f32 v12,0,v12\nv_cvt_pk_bf16_f32 v13,0,v13\nv_cvt_pk_bf16_f32 v14,0,v14\nv_cvt_pk_bf16_f32 v15,0,v15\nv_pk_mul_f32 v[8:9],v[8:9],v[0:1]\n" \
"v_pk_mul_f32 v[10:11],v[10:11],v[2:3]\nv_pk_mul_f32 v[12:13],v[12:13],v[4:5]\nv_pk_mul_f32 v[14:15],v[14:15],v[6:7]\nv_cvt_pk_bf16_f32 v8,0,v8\nv_cvt_pk_bf16_f32 v9,0,v9\nv_cvt_pk_bf16_f32 v10,0,v10\nv_cvt_pk_bf16_f32 v11,0,v11\nv_cvt_pk_bf16_f32 v12,0,v12\n" \
"v_cvt_pk_bf16_f32 v13,0,v13\nv_cvt_pk_bf16_f32 v14,0,v14\nv_cvt_pk_bf16_f32 v15,0,v15\nv_pk_add_f32 v[8:9],v[8:9],v[80:81]\nv_pk_add_f32 v[10:11],v[10:11],v[82:83]\nv_pk_add_f32 v[12:13],v[12:13],v[84:85]\nv_pk_add_f32 v[14:15],v[14:15],v[86:87]\nv_cvt_pk_bf16_f32 v8,0,v8\n" \
"v_cvt_pk_bf16_f32 v9,0,v9\nv_cvt_pk_bf16_f32 v10,0,v10\nv_cvt_pk_bf16_f32 v11,0,v11\nv_cvt_pk_bf16_f32 v12,0,v12\nv_cvt_pk_bf16_f32 v13,0,v13\nv_cvt_pk_bf16_f32 v14,0,v14\nv_cvt_pk_bf16_f32 v15,0,v15\nv_readfirstlane_b32 s70,v8\n" \
"s_nop 1\nv_subrev_f32 v40,s70,v8\nv_subrev_f32 v41,s70,v9\nv_subrev_f32 v42,s70,v10\nv_subrev_f32 v43,s70,v11\nv_subrev_f32 v44,s70,v12\nv_subrev_f32 v45,s70,v13\nv_subrev_f32 v46,s70,v14\n" \
"v_subrev_f32 v47,s70,v15\nv_pk_mul_f32 v[60:61],v[40:41],v[40:41]\nv_pk_add_f32 v[56:57],v[40:41],v[42:43]\nv_pk_fma_f32 v[60:61],v[42:43],v[42:43],v[60:61]\nv_pk_fma_f32 v[60:61],v[44:45],v[44:45],v[60:61]\nv_pk_fma_f32 v[60:61],v[46:47],v[46:47],v[60:61]\nv_pk_add_f32 v[44:45],v[44:45],v[46:47]\nv_pk_add_f32 v[56:57],v[56:57],v[44:45]\n" \
"v_add_f32 v56,v56,v57\nv_add_f32 v57,v60,v61\ns_nop 1\nv_permlane16_swap_b32 v56,v57\nv_add_f32 v56,v56,v57\nv_mov_b32 v57,v56\ns_nop 1\nv_permlane32_swap_b32 v56,v57\n" \
"v_add_f32 v56,v56,v57\ns_nop 1\nv_add_f32_dpp v56,v56,v56 quad_perm:[1,0,3,2] row_mask:0xf bank_mask:0xf\ns_nop 1\nv_add_f32_dpp v56,v56,v56 quad_perm:[2,3,0,1] row_mask:0xf bank_mask:0xf\ns_nop 1\nv_add_f32_dpp v56,v56,v56 row_half_mirror row_mask:0xf bank_mask:0xf\ns_nop 1\n" \
"v_add_f32_dpp v56,v56,v56 row_mirror row_mask:0xf bank_mask:0xf\nv_mul_f32 v56,0x3b000000,v56\nv_mov_b32 v57,v56\nv_cmp_u_f32 vcc,v56,v56\ns_nop 0\nv_permlane16_swap_b32 v56,v57\ns_cbranch_vccnz .Lrs0\n.Lrb0:\n" \
"buffer_load_dwordx4 v[232:235],v251,s[52:55],s80 offen sc1\nbuffer_load_dwordx4 v[236:239],v251,s[52:55],s80 offen offset:16 sc1\nv_fma_f32 v57,-v56,v56,v57\nv_add_f32 v58,s70,v56\nv_max_f32 v57,0,v57\nv_cvt_pk_bf16_f32 v58,0,v58\nv_cvt_pk_bf16_f32 v57,0,v57\nv_add_f32 v57,0x3727c5ac,v57\n" \
"v_cvt_pk_bf16_f32 v57,0,v57\nv_sqrt_f32 v57,v57\nv_pk_add_f32 v[24:25],v[8:9],v[58:59] op_sel_hi:[1,0] neg_lo:[0,1] neg_hi:[0,1]\nv_cvt_pk_bf16_f32 v57,0,v57\nv_rcp_f32 v59,v57\nv_cvt_pk_bf16_f32 v24,0,v24\nv_pk_add_f32 v[26:27],v[10:11],v[58:59] op_sel_hi:[1,0] neg_lo:[0,1] neg_hi:[0,1]\nv_pk_add_f32 v[28:29],v[12:13],v[58:59] op_sel_hi:[1,0] neg_lo:[0,1] neg_hi:[0,1]\n" \
"v_pk_add_f32 v[30:31],v[14:15],v[58:59] op_sel_hi:[1,0] neg_lo:[0,1] neg_hi:[0,1]\nv_cvt_pk_bf16_f32 v25,0,v25\nv_pk_mul_f32 v[24:25],v[24:25],v[58:59] op_sel:[0,1]\nv_cvt_pk_bf16_f32 v24,0,v24\nv_cvt_pk_bf16_f32 v25,0,v25\nv_pk_mul_f32 v[24:25],v[24:25],v[32:33]\nv_cvt_pk_bf16_f32 v24,0,v24\nv_cvt_pk_bf16_f32 v25,0,v25\n" \
"v_pk_add_f32 v[24:25],v[24:25],v[16:17]\nv_cvt_pk_bf16_f32 v24,0,v24\nv_cvt_pk_bf16_f32 v25,0,v25\nv_pk_mul_f32 v[60:61],v[24:25],v[96:97]\nv_cvt_pk_bf16_f32 v26,0,v26\nv_cvt_pk_bf16_f32 v27,0,v27\nv_pk_mul_f32 v[26:27],v[26:27],v[58:59] op_sel:[0,1]\nv_cvt_pk_bf16_f32 v26,0,v26\n" \
"v_cvt_pk_bf16_f32 v27,0,v27\nv_pk_mul_f32 v[26:27],v[26:27],v[34:35]\nv_cvt_pk_bf16_f32 v26,0,v26\nv_cvt_pk_bf16_f32 v27,0,v27\nv_pk_add_f32 v[26:27],v[26:27],v[18:19]\nv_cvt_pk_bf16_f32 v26,0,v26\nv_cvt_pk_bf16_f32 v27,0,v27\nv_pk_fma_f32 v[60:61],v[26:27],v[98:99],v[60:61]\n" \
"v_cvt_pk_bf16_f32 v28,0,v28\nv_cvt_pk_bf16_f32 v29,0,v29\nv_pk_mul_f32 v[28:29],v[28:29],v[58:59] op_sel:[0,1]\nv_cvt_pk_bf16_f32 v28,0,v28\nv_cvt_pk_bf16_f32 v29,0,v29\nv_pk_mul_f32 v[28:29],v[28:29],v[36:37]\nv_cvt_pk_bf16_f32 v28,0,v28\nv_cvt_pk_bf16_f32 v29,0,v29\n" \
"v_pk_add_f32 v[28:29],v[28:29],v[20:21]\nv_cvt_pk_bf16_f32 v28,0,v28\nv_cvt_pk_bf16_f32 v29,0,v29\nv_pk_fma_f32 v[60:61],v[28:29],v[100:101],v[60:61]\nv_cvt_pk_bf16_f32 v30,0,v30\nv_cvt_pk_bf16_f32 v31,0,v31\nv_pk_mul_f32 v[30:31],v[30:31],v[58:59] op_sel:[0,1]\nv_cvt_pk_bf16_f32 v30,0,v30\n" \
"v_cvt_pk_bf16_f32 v31,0,v31\nv_pk_mul_f32 v[30:31],v[30:31],v[38:39]\nv_cvt_pk_bf16_f32 v30,0,v30\nv_cvt_pk_bf16_f32 v31,0,v31\nv_pk_add_f32 v[30:31],v[30:31],v[22:23]\nv_cvt_pk_bf16_f32 v30,0,v30\nv_cvt_pk_bf16_f32 v31,0,v31\nv_pk_fma_f32 v[60:61],v[30:31],v[102:103],v[60:61]\n" \
"v_add_f32 v60,v60,v61\nv_mov_b32 v61,v60\nv_lshrrev_b32 v40,16,v57\nv_or_b32 v40,v40,v58\nv_permlane16_swap_b32 v60,v61\nv_add_f32 v60,v60,v61\nv_mov_b32 v61,v60\ns_nop 1\n" \
"v_permlane32_swap_b32 v60,v61\nv_add_f32 v60,v60,v61\ns_nop 1\nv_add_f32_dpp v60,v60,v60 quad_perm:[1,0,3,2] row_mask:0xf bank_mask:0xf\ns_nop 1\nv_add_f32_dpp v60,v60,v60 quad_perm:[2,3,0,1] row_mask:0xf bank_mask:0xf\ns_nop 1\nv_add_f32_dpp v60,v60,v60 row_half_mirror row_mask:0xf bank_mask:0xf\n" \
"s_nop 1\nv_add_f32_dpp v60,v60,v60 row_mirror row_mask:0xf bank_mask:0xf\nv_cvt_pk_bf16_f32 v60,0,v60\ns_nop 0\nv_readfirstlane_b32 s76,v60\ns_nop 0\ns_ashr_i32 s71,s76,31\ns_and_b32 s72,s76,0x7fffffff\n" \
"s_or_b32 s71,s71,0x80000000\ns_xor_b32 s76,s76,s71\ns_and_b32 s76,s76,0xffff0000\ns_cmp_gt_u32 s72,0x7f800000\ns_cselect_b32 s76,0,s76\ns_or_b32 s76,s76,s39\nv_mov_b32 v41,s76\nds_max_u64 v250,v[40:41]\n" \
"s_waitcnt lgkmcnt(0)\ns_barrier\nds_read_b64 v[224:225],v250\ns_waitcnt lgkmcnt(0)\nbuffer_store_dwordx2 v[224:225],v246,s[52:55],s37 offen sc1\nv_readfirstlane_b32 s97,v225\ns_sub_u32 s95,s95,54525952\ns_sub_u32 s74,s74,54525952\n" \
"s_mov_b32 s29,0\ns_cmp_eq_u32 s97,s77\ns_cselect_b32 s65,s95,0x40000000\ns_cselect_b32 s95,0x40000000,s95\ns_cmp_eq_u32 s97,s76\ns_cselect_b32 s66,s74,0x40000000\ns_cselect_b32 s74,0x40000000,s74\ns_lshr_b32 s70,s6,7\n" \
"s_or_b32 s38,s7,s70\ns_cmp_lg_u32 s3,0\ns_waitcnt vmcnt(1)\ns_cbranch_scc1 .Lkp\nv_pk_mul_f32 v[88:89],v[88:89],v[48:49]\nv_pk_mul_f32 v[90:91],v[90:91],v[50:51]\nv_pk_mul_f32 v[92:93],v[92:93],v[52:53]\nv_pk_mul_f32 v[94:95],v[94:95],v[54:55]\n" \
"v_cvt_pk_bf16_f32 v88,0,v88\nv_cvt_pk_bf16_f32 v89,0,v89\nv_cvt_pk_bf16_f32 v90,0,v90\nv_cvt_pk_bf16_f32 v91,0,v91\nv_cvt_pk_bf16_f32 v92,0,v92\nv_cvt_pk_bf16_f32 v93,0,v93\nv_cvt_pk_bf16_f32 v94,0,v94\nv_cvt_pk_bf16_f32 v95,0,v95\n" \
"v_pk_add_f32 v[88:89],v[88:89],v[72:73]\nv_pk_add_f32 v[90:91],v[90:91],v[74:75]\nv_pk_add_f32 v[92:93],v[92:93],v[76:77]\nv_pk_add_f32 v[94:95],v[94:95],v[78:79]\nv_cvt_pk_bf16_f32 v88,0,v88\nds_write_b16_d16_hi v244,v88 offset:0\nv_cvt_pk_bf16_f32 v89,0,v89\nds_write_b16_d16_hi v244,v89 offset:32\n" \
"v_cvt_pk_bf16_f32 v90,0,v90\nds_write_b16_d16_hi v244,v90 offset:64\nv_cvt_pk_bf16_f32 v91,0,v91\nds_write_b16_d16_hi v244,v91 offset:96\nv_cvt_pk_bf16_f32 v92,0,v92\nds_write_b16_d16_hi v244,v92 offset:128\nv_cvt_pk_bf16_f32 v93,0,v93\nds_write_b16_d16_hi v244,v93 offset:160\n" \
"v_cvt_pk_bf16_f32 v94,0,v94\nds_write_b16_d16_hi v244,v94 offset:192\nv_cvt_pk_bf16_f32 v95,0,v95\nds_write_b16_d16_hi v244,v95 offset:224\nds_read_b128 v[64:67],v245 offset:0\nv_pk_mul_f32 v[24:25],v[24:25],v[48:49]\nv_pk_mul_f32 v[26:27],v[26:27],v[50:51]\nv_pk_mul_f32 v[28:29],v[28:29],v[52:53]\n" \
"v_pk_mul_f32 v[30:31],v[30:31],v[54:55]\nv_cvt_pk_bf16_f32 v24,0,v24\nv_cvt_pk_bf16_f32 v25,0,v25\nv_cvt_pk_bf16_f32 v26,0,v26\nv_cvt_pk_bf16_f32 v27,0,v27\nv_cvt_pk_bf16_f32 v28,0,v28\nv_cvt_pk_bf16_f32 v29,0,v29\nv_cvt_pk_bf16_f32 v30,0,v30\n" \
"v_cvt_pk_bf16_f32 v31,0,v31\nv_pk_add_f32 v[24:25],v[24:25],v[8:9]\nv_pk_add_f32 v[26:27],v[26:27],v[10:11]\nv_pk_add_f32 v[28:29],v[28:29],v[12:13]\nv_pk_add_f32 v[30:31],v[30:31],v[14:15]\nv_cvt_pk_bf16_f32 v24,0,v24\nds_write_b16_d16_hi v244,v24 offset:1024\nv_cvt_pk_bf16_f32 v25,0,v25\n" \
"ds_write_b16_d16_hi v244,v25 offset:1056\nv_cvt_pk_bf16_f32 v26,0,v26\nds_write_b16_d16_hi v244,v26 offset:1088\nv_cvt_pk_bf16_f32 v27,0,v27\nds_write_b16_d16_hi v244,v27 offset:1120\nv_cvt_pk_bf16_f32 v28,0,v28\nds_write_b16_d16_hi v244,v28 offset:1152\nv_cvt_pk_bf16_f32 v29,0,v29\n" \
"ds_write_b16_d16_hi v244,v29 offset:1184\nv_cvt_pk_bf16_f32 v30,0,v30\nds_write_b16_d16_hi v244,v30 offset:1216\nv_cvt_pk_bf16_f32 v31,0,v31\nds_write_b16_d16_hi v244,v31 offset:1248\nds_read_b128 v[68:71],v245 offset:1024\ns_waitcnt lgkmcnt(0)\nbuffer_store_dwordx4 v[64:67],v240,s[44:47],s95 offen sc1\n" \
"buffer_store_dwordx4 v[68:71],v240,s[44:47],s74 offen sc1\ns_cmp_eq_u32 s7,0\ns_cselect_b32 s70,1,0\ns_xor_b32 s71,s65,s66\ns_or_b32 s70,s70,s71\ns_cbranch_scc0 .Lfin\ns_lshr_b32 s70,s6,3\ns_and_b32 s70,s70,15\n" \
"s_lshl4_add_u32 s98,s60,s70\nv_lshrrev_b32 v242,4,v249\ns_lshl_b32 s71,s98,2\nv_add_u32 v242,s71,v242\nv_and_b32 v244,15,v242\nv_bfe_u32 v245,v242,4,3\nv_lshl_add_u32 v244,v244,3,v245\nv_and_b32 v245,0x180,v242\n" \
"v_add_lshl_u32 v243,v244,v245,1\nv_lshlrev_b32 v244,1,v243\ns_mov_b32 s70,4096\nv_lshlrev_b32 v242,1,v242\nbuffer_load_dword v62,v244,s[52:55],0 offen offset:0 sc1\nbuffer_load_dword v63,v244,s[52:55],0 offen offset:2048 sc1\nbuffer_load_dword v126,v244,s[52:55],s70 offen offset:0 sc1\nbuffer_load_dword v127,v244,s[52:55],s70 offen offset:2048 sc1\n" \
".Lkp:\ns_cmp_lg_u32 s7,0\ns_cbranch_scc1 .Lkl\nv_max3_u32 v228,v233,v235,v237\nv_min3_u32 v229,v233,v235,v237\nv_max_u32 v228,v228,v239\nv_min_u32 v229,v229,v239\nv_cmp_lt_u32 vcc,s94,v228\n" \
"v_cmp_eq_u32 s[90:91],0,v229\ns_cbranch_vccnz .Lnew\ns_cmp_eq_u64 s[90:91],0\ns_cbranch_scc1 .Lv0\ns_sub_u32 s73,s73,1\ns_cselect_b32 s73,0,s73\ns_cbranch_scc1 .Lv0\ns_sleep 3\n" \
"buffer_load_dwordx4 v[232:235],v251,s[52:55],s80 offen sc1\nbuffer_load_dwordx4 v[236:239],v251,s[52:55],s80 offen offset:16 sc1\ns_waitcnt vmcnt(0)\ns_branch .Lkp\n.Lv0:\nv_mov_b32 v230,s96\ns_or_b32 s72,s94,2\nv_mov_b32 v231,s72\n" \
"ds_write_b64 v250,v[230:231] offset:8\ns_branch .Lver\n.Lkl:\nds_read_b64 v[230:231],v250 offset:8\ns_mov_b32 s91,0\ns_waitcnt lgkmcnt(0)\nv_readfirstlane_b32 s72,v231\nv_readfirstlane_b32 s96,v230\n" \
"s_and_b32 s90,s72,2\ns_xor_b32 s72,s72,s90\ns_xor_b32 s90,s90,2\ns_cmp_lg_u32 s72,s94\ns_mov_b32 s94,s72\ns_cbranch_scc1 .Lsp\ns_cmp_eq_u32 s90,0\ns_cbranch_scc1 .Lver\n" \
"s_sub_u32 s73,s73,1\ns_cselect_b32 s73,0,s73\ns_cbranch_scc1 .Lver\ns_sleep 1\n.Lyield:\ns_branch .Lkl\n.Lnew:\nv_mov_b32 v230,v228\n" \
"v_cmp_eq_u32 vcc,v228,v233\ns_cmp_eq_u64 s[90:91],0\ns_cselect_b32 s71,2,0\nv_max_u32_dpp v230,v230,v230 quad_perm:[1,0,3,2] row_mask:0xf bank_mask:0xf\nv_cndmask_b32 v229,v238,v232,vcc\nv_cmp_eq_u32 vcc,v228,v235\nv_max_u32_dpp v230,v230,v230 quad_perm:[2,3,0,1] row_mask:0xf bank_mask:0xf\ns_nop 1\n" \
"v_max_u32_dpp v230,v230,v230 row_half_mirror row_mask:0xf bank_mask:0xf\nv_cndmask_b32 v229,v229,v234,vcc\nv_cmp_eq_u32 vcc,v228,v237\nv_max_u32_dpp v230,v230,v230 row_mirror row_mask:0xf bank_mask:0xf\nv_mov_b32 v231,v230\nv_cndmask_b32 v229,v229,v236,vcc\ns_nop 0\nv_permlane16_swap_b32 v230,v231\n" \
"v_max_u32 v230,v230,v231\nv_mov_b32 v231,v230\ns_nop 1\nv_permlane32_swap_b32 v230,v231\nv_max_u32 v230,v230,v231\ns_nop 0\nv_readfirstlane_b32 s94,v230\ns_nop 1\n" \
"v_cmp_eq_u32 vcc,s94,v228\ns_nop 0\ns_ff1_i32_b64 s72,vcc\nv_readlane_b32 s96,v229,s72\ns_nop 1\nv_mov_b32 v230,s96\ns_or_b32 s72,s94,s71\nv_mov_b32 v231,s72\n" \
"ds_write_b64 v250,v[230:231] offset:8\n.Lsp:\ns_not_b32 s15,s94\ns_bfe_u32 s15,s15,0xb0005\ns_lshl_b32 s15,s15,10\ns_cmp_eq_u32 s3,0\ns_cbranch_scc1 .Lkp\ns_add_u32 s72,s15,54525952\n" \
"buffer_load_dword v41,v241,s[52:55],s72 offen sc1\ns_add_u32 s72,s15,56623104\nbuffer_load_dword v43,v241,s[52:55],s72 offen sc1\ns_add_u32 s72,s15,58720256\nbuffer_load_dword v45,v241,s[52:55],s72 offen sc1\ns_add_u32 s72,s15,60817408\nbuffer_load_dword v47,v241,s[52:55],s72 offen sc1\ns_add_u32 s72,s15,62914560\n" \
"buffer_load_dword v105,v241,s[52:55],s72 offen sc1\ns_add_u32 s72,s15,65011712\nbuffer_load_dword v107,v241,s[52:55],s72 offen sc1\ns_add_u32 s72,s15,67108864\nbuffer_load_dword v109,v241,s[52:55],s72 offen sc1\ns_add_u32 s72,s15,69206016\nbuffer_load_dword v111,v241,s[52:55],s72 offen sc1\nv_add_u32 v66,s15,v248\n" \
"global_load_ushort v64,v66,s[8:9]\nglobal_load_ushort v65,v66,s[8:9] offset:32\nbuffer_load_dwordx4 v[232:235],v251,s[52:55],s80 offen sc1\nbuffer_load_dwordx4 v[236:239],v251,s[52:55],s80 offen offset:16 sc1\nv_accvgpr_read_b32 v161,a200\nv_lshlrev_b32 v160,16,v161\nv_and_b32 v161,0xffff0000,v161\nv_accvgpr_read_b32 v163,a201\n" \
"v_lshlrev_b32 v162,16,v163\nv_and_b32 v163,0xffff0000,v163\nv_accvgpr_read_b32 v165,a202\nv_lshlrev_b32 v164,16,v165\nv_and_b32 v165,0xffff0000,v165\nv_accvgpr_read_b32 v167,a203\nv_lshlrev_b32 v166,16,v167\nv_and_b32 v167,0xffff0000,v167\n" \
"v_accvgpr_read_b32 v169,a204\nv_lshlrev_b32 v168,16,v169\nv_and_b32 v169,0xffff0000,v169\nv_accvgpr_read_b32 v171,a205\nv_lshlrev_b32 v170,16,v171\nv_and_b32 v171,0xffff0000,v171\nv_accvgpr_read_b32 v173,a206\nv_lshlrev_b32 v172,16,v173\n" \
"v_and_b32 v173,0xffff0000,v173\nv_accvgpr_read_b32 v175,a207\nv_lshlrev_b32 v174,16,v175\nv_and_b32 v175,0xffff0000,v175\nv_accvgpr_read_b32 v177,a208\nv_lshlrev_b32 v176,16,v177\nv_and_b32 v177,0xffff0000,v177\nv_accvgpr_read_b32 v179,a209\n" \
"v_lshlrev_b32 v178,16,v179\nv_and_b32 v179,0xffff0000,v179\nv_accvgpr_read_b32 v181,a210\nv_lshlrev_b32 v180,16,v181\nv_and_b32 v181,0xffff0000,v181\nv_accvgpr_read_b32 v183,a211\nv_lshlrev_b32 v182,16,v183\nv_and_b32 v183,0xffff0000,v183\n" \
"v_accvgpr_read_b32 v185,a252\nv_lshlrev_b32 v184,16,v185\nv_and_b32 v185,0xffff0000,v185\nv_accvgpr_read_b32 v187,a253\nv_lshlrev_b32 v186,16,v187\nv_and_b32 v187,0xffff0000,v187\nv_accvgpr_read_b32 v189,a254\nv_lshlrev_b32 v188,16,v189\n" \
"v_and_b32 v189,0xffff0000,v189\nv_accvgpr_read_b32 v191,a255\nv_lshlrev_b32 v190,16,v191\nv_and_b32 v191,0xffff0000,v191\ns_and_b32 s82,s96,0xffff0000\ns_lshl_b32 s83,s96,16\nv_rcp_f32 v68,s83\ns_waitcnt vmcnt(11)\n" \
"v_lshlrev_b32 v40,16,v41\nv_and_b32 v41,0xffff0000,v41\ns_waitcnt vmcnt(10)\nv_lshlrev_b32 v42,16,v43\nv_and_b32 v43,0xffff0000,v43\nv_pk_add_f32 v[40:41],v[40:41],v[42:43]\ns_waitcnt vmcnt(9)\nv_lshlrev_b32 v44,16,v45\n" \
"v_and_b32 v45,0xffff0000,v45\ns_waitcnt vmcnt(8)\nv_lshlrev_b32 v46,16,v47\nv_and_b32 v47,0xffff0000,v47\nv_pk_add_f32 v[44:45],v[44:45],v[46:47]\nv_pk_add_f32 v[40:41],v[40:41],v[44:45]\ns_waitcnt vmcnt(7)\nv_lshlrev_b32 v104,16,v105\n" \
"v_and_b32 v105,0xffff0000,v105\ns_waitcnt vmcnt(6)\nv_lshlrev_b32 v106,16,v107\nv_and_b32 v107,0xffff0000,v107\nv_pk_add_f32 v[104:105],v[104:105],v[106:107]\ns_waitcnt vmcnt(5)\nv_lshlrev_b32 v108,16,v109\nv_and_b32 v109,0xffff0000,v109\n" \
"s_waitcnt vmcnt(4)\nv_lshlrev_b32 v110,16,v111\nv_and_b32 v111,0xffff0000,v111\nv_pk_add_f32 v[108:109],v[108:109],v[110:111]\nv_pk_add_f32 v[104:105],v[104:105],v[108:109]\nv_pk_add_f32 v[40:41],v[40:41],v[104:105]\nv_cvt_pk_bf16_f32 v40,0,v40\nv_cvt_pk_bf16_f32 v41,0,v41\n" \
"v_pk_mul_f32 v[40:41],v[40:41],v[112:113]\nv_cvt_pk_bf16_f32 v40,0,v40\nv_cvt_pk_bf16_f32 v41,0,v41\ns_waitcnt vmcnt(3)\nv_lshlrev_b32 v64,16,v64\ns_waitcnt vmcnt(2)\nv_lshlrev_b32 v65,16,v65\nv_pk_add_f32 v[40:41],v[40:41],v[64:65]\n" \
"v_cvt_pk_bf16_f32 v40,0,v40\nv_cvt_pk_bf16_f32 v41,0,v41\nv_pk_add_f32 v[40:41],v[40:41],s[82:83] op_sel_hi:[1,0] neg_lo:[0,1] neg_hi:[0,1]\nv_cvt_pk_bf16_f32 v40,0,v40\nv_cvt_pk_bf16_f32 v41,0,v41\nv_pk_mul_f32 v[40:41],v[40:41],v[68:69] op_sel_hi:[1,0]\nv_cvt_pk_bf16_f32 v40,0,v40\nv_cvt_pk_bf16_f32 v41,0,v41\n" \
"v_pk_mul_f32 v[40:41],v[40:41],v[116:117]\nv_cvt_pk_bf16_f32 v40,0,v40\nv_cvt_pk_bf16_f32 v41,0,v41\nv_pk_add_f32 v[40:41],v[40:41],v[114:115]\nv_cvt_pk_bf16_f32 v40,v40,v41\ns_nop 1\nv_mov_b32_dpp v44,v40 quad_perm:[0,0,0,0] row_mask:0xf bank_mask:0xf\nv_mov_b32_dpp v45,v40 quad_perm:[1,1,1,1] row_mask:0xf bank_mask:0xf\n" \
"v_mov_b32_dpp v46,v40 quad_perm:[2,2,2,2] row_mask:0xf bank_mask:0xf\nv_mov_b32_dpp v47,v40 quad_perm:[3,3,3,3] row_mask:0xf bank_mask:0xf\nv_dot2_f32_bf16 v120,v192,v44,0\nv_dot2_f32_bf16 v121,v193,v44,0\nv_dot2_f32_bf16 v122,v194,v44,0\nv_dot2_f32_bf16 v123,v195,v44,0\nv_dot2_f32_bf16 v124,v208,v44,0\nv_dot2_f32_bf16 v125,v209,v44,0\n" \
"v_dot2_f32_bf16 v126,v210,v44,0\nv_dot2_f32_bf16 v127,v211,v44,0\nv_dot2_f32_bf16 v120,v196,v45,v120\nv_dot2_f32_bf16 v121,v197,v45,v121\nv_dot2_f32_bf16 v122,v198,v45,v122\nv_dot2_f32_bf16 v123,v199,v45,v123\nv_dot2_f32_bf16 v124,v212,v45,v124\nv_dot2_f32_bf16 v125,v213,v45,v125\n" \
"v_dot2_f32_bf16 v126,v214,v45,v126\nv_dot2_f32_bf16 v127,v215,v45,v127\nv_dot2_f32_bf16 v120,v200,v46,v120\nv_dot2_f32_bf16 v121,v201,v46,v121\nv_dot2_f32_bf16 v122,v202,v46,v122\nv_dot2_f32_bf16 v123,v203,v46,v123\nv_dot2_f32_bf16 v124,v216,v46,v124\nv_dot2_f32_bf16 v125,v217,v46,v125\n" \
"v_dot2_f32_bf16 v126,v218,v46,v126\nv_dot2_f32_bf16 v127,v219,v46,v127\nv_dot2_f32_bf16 v120,v204,v47,v120\nv_dot2_f32_bf16 v121,v205,v47,v121\nv_dot2_f32_bf16 v122,v206,v47,v122\nv_dot2_f32_bf16 v123,v207,v47,v123\nv_dot2_f32_bf16 v124,v220,v47,v124\nv_dot2_f32_bf16 v125,v221,v47,v125\n" \
"v_dot2_f32_bf16 v126,v222,v47,v126\nv_dot2_f32_bf16 v127,v223,v47,v127\nv_permlane32_swap_b32 v120,v124\nv_add_f32 v120,v120,v124\nv_permlane32_swap_b32 v121,v125\nv_add_f32 v121,v121,v125\nv_permlane32_swap_b32 v122,v126\nv_add_f32 v122,v122,v126\n" \
"v_permlane32_swap_b32 v123,v127\nv_add_f32 v123,v123,v127\nv_permlane16_swap_b32 v120,v122\nv_add_f32 v120,v120,v122\nv_permlane16_swap_b32 v121,v123\nv_add_f32 v121,v121,v123\nv_add_f32_dpp v120,v120,v120 row_ror:4 row_mask:0xf bank_mask:0xf\ns_nop 1\n" \
"v_add_f32_dpp v120,v120,v120 row_ror:8 row_mask:0xf bank_mask:0xf\nv_add_f32_dpp v121,v121,v121 row_ror:4 row_mask:0xf bank_mask:0xf\ns_nop 1\nv_add_f32_dpp v121,v121,v121 row_ror:8 row_mask:0xf bank_mask:0xf\ns_cmp_eq_u64 s[90:91],0\ns_cbranch_scc1 .Lver\ns_waitcnt vmcnt(0)\ns_branch .Lkp\n" \
".Lver:\ns_cmp_eq_u32 s3,0\ns_cbranch_scc1 .Lnf\nds_write_b32 v252,v120\nds_write_b32 v252,v121 offset:16\ns_lshl_b32 s70,s2,6\ns_add_u32 s70,s70,98304\nv_lshlrev_b32 v241,1,v241\n" \
"s_waitcnt lgkmcnt(0)\ns_barrier\nds_read_b128 v[40:43],v253\nds_read_b128 v[44:47],v253 offset:256\ns_waitcnt lgkmcnt(1)\nv_pk_add_f32 v[40:41],v[40:41],v[42:43]\nv_add_f32 v40,v40,v41\nv_cvt_pk_bf16_f32 v40,0,v40\n" \
"v_mul_f32 v41,0xbfb8aa3b,v40\nv_exp_f32 v41,v41\ns_waitcnt lgkmcnt(0)\nv_pk_add_f32 v[44:45],v[44:45],v[46:47]\nv_add_f32 v41,1.0,v41\nv_rcp_f32 v41,v41\nv_add_f32 v44,v44,v45\nv_cvt_pk_bf16_f32 v44,0,v44\n" \
"v_mul_f32 v40,v40,v41\nv_cvt_pk_bf16_f32 v40,0,v40\nv_mul_f32 v40,v40,v44\nv_cvt_pk_bf16_f32 v40,0,v40\ns_nop 0\nv_readlane_b32 s8,v40,0\nv_readlane_b32 s9,v40,1\nv_readlane_b32 s10,v40,2\n" \
"v_readlane_b32 s11,v40,3\nv_readlane_b32 s12,v40,4\nv_readlane_b32 s13,v40,5\nv_readlane_b32 s14,v40,6\nv_readlane_b32 s15,v40,7\nv_readlane_b32 s16,v40,8\nv_readlane_b32 s17,v40,9\nv_readlane_b32 s18,v40,10\n" \
"v_readlane_b32 s19,v40,11\nv_readlane_b32 s20,v40,12\nv_readlane_b32 s21,v40,13\nv_readlane_b32 s22,v40,14\nv_readlane_b32 s23,v40,15\nv_pk_mul_f32 v[120:121],v[160:161],s[8:9] op_sel_hi:[1,0]\nv_pk_mul_f32 v[122:123],v[162:163],s[8:9] op_sel:[0,1]\nv_pk_fma_f32 v[120:121],v[164:165],s[10:11],v[120:121] op_sel_hi:[1,0,1]\n" \
"v_pk_fma_f32 v[122:123],v[166:167],s[10:11],v[122:123] op_sel:[0,1,0]\nv_pk_fma_f32 v[120:121],v[168:169],s[12:13],v[120:121] op_sel_hi:[1,0,1]\nv_pk_fma_f32 v[122:123],v[170:171],s[12:13],v[122:123] op_sel:[0,1,0]\nv_pk_fma_f32 v[120:121],v[172:173],s[14:15],v[120:121] op_sel_hi:[1,0,1]\nv_pk_fma_f32 v[122:123],v[174:175],s[14:15],v[122:123] op_sel:[0,1,0]\nv_pk_fma_f32 v[120:121],v[176:177],s[16:17],v[120:121] op_sel_hi:[1,0,1]\nv_pk_fma_f32 v[122:123],v[178:179],s[16:17],v[122:123] op_sel:[0,1,0]\nv_pk_fma_f32 v[120:121],v[180:181],s[18:19],v[120:121] op_sel_hi:[1,0,1]\n" \
"v_pk_fma_f32 v[122:123],v[182:183],s[18:19],v[122:123] op_sel:[0,1,0]\nv_pk_fma_f32 v[120:121],v[184:185],s[20:21],v[120:121] op_sel_hi:[1,0,1]\nv_pk_fma_f32 v[122:123],v[186:187],s[20:21],v[122:123] op_sel:[0,1,0]\nv_pk_fma_f32 v[120:121],v[188:189],s[22:23],v[120:121] op_sel_hi:[1,0,1]\nv_pk_fma_f32 v[122:123],v[190:191],s[22:23],v[122:123] op_sel:[0,1,0]\nv_pk_add_f32 v[120:121],v[120:121],v[122:123]\nv_min_u32 v120,-2,v120\nv_min_u32 v121,-2,v121\n" \
"buffer_store_dwordx2 v[120:121],v241,s[52:55],s70 offen sc1\nv_pk_mul_f32 v[88:89],v[88:89],v[48:49]\nv_pk_mul_f32 v[90:91],v[90:91],v[50:51]\nv_pk_mul_f32 v[92:93],v[92:93],v[52:53]\nv_pk_mul_f32 v[94:95],v[94:95],v[54:55]\nv_cvt_pk_bf16_f32 v88,0,v88\nv_cvt_pk_bf16_f32 v89,0,v89\nv_cvt_pk_bf16_f32 v90,0,v90\n" \
"v_cvt_pk_bf16_f32 v91,0,v91\nv_cvt_pk_bf16_f32 v92,0,v92\nv_cvt_pk_bf16_f32 v93,0,v93\nv_cvt_pk_bf16_f32 v94,0,v94\nv_cvt_pk_bf16_f32 v95,0,v95\nv_pk_add_f32 v[88:89],v[88:89],v[72:73]\nv_pk_add_f32 v[90:91],v[90:91],v[74:75]\nv_pk_add_f32 v[92:93],v[92:93],v[76:77]\n" \
"v_pk_add_f32 v[94:95],v[94:95],v[78:79]\nv_cvt_pk_bf16_f32 v88,0,v88\nds_write_b16_d16_hi v244,v88 offset:0\nv_cvt_pk_bf16_f32 v89,0,v89\nds_write_b16_d16_hi v244,v89 offset:32\nv_cvt_pk_bf16_f32 v90,0,v90\nds_write_b16_d16_hi v244,v90 offset:64\nv_cvt_pk_bf16_f32 v91,0,v91\n" \
"ds_write_b16_d16_hi v244,v91 offset:96\nv_cvt_pk_bf16_f32 v92,0,v92\nds_write_b16_d16_hi v244,v92 offset:128\nv_cvt_pk_bf16_f32 v93,0,v93\nds_write_b16_d16_hi v244,v93 offset:160\nv_cvt_pk_bf16_f32 v94,0,v94\nds_write_b16_d16_hi v244,v94 offset:192\nv_cvt_pk_bf16_f32 v95,0,v95\n" \
"ds_write_b16_d16_hi v244,v95 offset:224\nds_read_b128 v[64:67],v245 offset:0\nv_pk_mul_f32 v[24:25],v[24:25],v[48:49]\nv_pk_mul_f32 v[26:27],v[26:27],v[50:51]\nv_pk_mul_f32 v[28:29],v[28:29],v[52:53]\nv_pk_mul_f32 v[30:31],v[30:31],v[54:55]\nv_cvt_pk_bf16_f32 v24,0,v24\nv_cvt_pk_bf16_f32 v25,0,v25\n" \
"v_cvt_pk_bf16_f32 v26,0,v26\nv_cvt_pk_bf16_f32 v27,0,v27\nv_cvt_pk_bf16_f32 v28,0,v28\nv_cvt_pk_bf16_f32 v29,0,v29\nv_cvt_pk_bf16_f32 v30,0,v30\nv_cvt_pk_bf16_f32 v31,0,v31\nv_pk_add_f32 v[24:25],v[24:25],v[8:9]\nv_pk_add_f32 v[26:27],v[26:27],v[10:11]\n" \
"v_pk_add_f32 v[28:29],v[28:29],v[12:13]\nv_pk_add_f32 v[30:31],v[30:31],v[14:15]\nv_cvt_pk_bf16_f32 v24,0,v24\nds_write_b16_d16_hi v244,v24 offset:1024\nv_cvt_pk_bf16_f32 v25,0,v25\nds_write_b16_d16_hi v244,v25 offset:1056\nv_cvt_pk_bf16_f32 v26,0,v26\nds_write_b16_d16_hi v244,v26 offset:1088\n" \
"v_cvt_pk_bf16_f32 v27,0,v27\nds_write_b16_d16_hi v244,v27 offset:1120\nv_cvt_pk_bf16_f32 v28,0,v28\nds_write_b16_d16_hi v244,v28 offset:1152\nv_cvt_pk_bf16_f32 v29,0,v29\nds_write_b16_d16_hi v244,v29 offset:1184\nv_cvt_pk_bf16_f32 v30,0,v30\nds_write_b16_d16_hi v244,v30 offset:1216\n" \
"v_cvt_pk_bf16_f32 v31,0,v31\nds_write_b16_d16_hi v244,v31 offset:1248\nds_read_b128 v[68:71],v245 offset:1024\ns_waitcnt lgkmcnt(0)\nbuffer_store_dwordx4 v[64:67],v240,s[44:47],s95 offen sc1\nbuffer_store_dwordx4 v[68:71],v240,s[44:47],s74 offen sc1\n.Lnf:\ns_cmp_eq_u32 s94,s97\n" \
"s_cbranch_scc1 .Lwin\nbuffer_store_dwordx4 v[64:67],v240,s[44:47],s65 offen sc1\nbuffer_store_dwordx4 v[68:71],v240,s[44:47],s66 offen sc1\n.Lwin:\ns_cmp_lg_u32 s38,0\ns_cbranch_scc1 .Lfin\ns_add_u32 s72,s15,54525952\nbuffer_load_ushort v128,v243,s[52:55],s72 offen sc1\n" \
"s_add_u32 s72,s15,56623104\nbuffer_load_ushort v129,v243,s[52:55],s72 offen sc1\ns_add_u32 s72,s15,58720256\nbuffer_load_ushort v130,v243,s[52:55],s72 offen sc1\ns_add_u32 s72,s15,60817408\nbuffer_load_ushort v131,v243,s[52:55],s72 offen sc1\ns_add_u32 s72,s15,62914560\nbuffer_load_ushort v132,v243,s[52:55],s72 offen sc1\n" \
"s_add_u32 s72,s15,65011712\nbuffer_load_ushort v133,v243,s[52:55],s72 offen sc1\ns_add_u32 s72,s15,67108864\nbuffer_load_ushort v134,v243,s[52:55],s72 offen sc1\ns_add_u32 s72,s15,69206016\nbuffer_load_ushort v135,v243,s[52:55],s72 offen sc1\nv_add_u32 v242,s15,v242\nglobal_load_ushort v144,v242,s[8:9]\n" \
"v_min_u32 v168,55,v249\nv_lshlrev_b32 v168,11,v168\ns_lshl4_add_u32 s70,s98,98304\ns_add_u32 s71,s70,114688\ns_and_b32 s82,s96,0xffff0000\ns_lshl_b32 s83,s96,16\nv_rcp_f32 v148,s83\ns_ashr_i32 s37,s94,31\n" \
"s_not_b32 s37,s37\ns_or_b32 s37,s37,0x80000000\ns_xor_b32 s37,s37,s94\ns_and_b32 s37,s37,0xffff0000\nv_mov_b32 v149,0xbfb8aa3b\nv_mul_f32 v149,s37,v149\nv_exp_f32 v149,v149\nv_and_b32 v150,15,v249\n" \
"v_add_f32 v149,1.0,v149\nv_rcp_f32 v149,v149\nv_cmp_eq_u32 vcc,0,v150\nv_cvt_pk_bf16_f32 v149,0,v149\nv_mov_b32 v150,0x40000000\nv_cndmask_b32 v150,v150,v242,vcc\nv_cmp_gt_u32 s[90:91],56,v249\ns_waitcnt vmcnt(8)\n" \
"v_lshlrev_b32 v128,16,v128\ns_waitcnt vmcnt(7)\nv_lshlrev_b32 v129,16,v129\nv_add_f32 v128,v128,v129\ns_waitcnt vmcnt(6)\nv_lshlrev_b32 v130,16,v130\ns_waitcnt vmcnt(5)\nv_lshlrev_b32 v131,16,v131\n" \
"v_add_f32 v130,v130,v131\nv_add_f32 v128,v128,v130\ns_waitcnt vmcnt(4)\nv_lshlrev_b32 v132,16,v132\ns_waitcnt vmcnt(3)\nv_lshlrev_b32 v133,16,v133\nv_add_f32 v132,v132,v133\ns_waitcnt vmcnt(2)\n" \
"v_lshlrev_b32 v134,16,v134\ns_waitcnt vmcnt(1)\nv_lshlrev_b32 v135,16,v135\nv_add_f32 v134,v134,v135\nv_add_f32 v132,v132,v134\nv_add_f32 v128,v128,v132\nv_cvt_pk_bf16_f32 v128,0,v128\nv_mul_f32 v128,v128,v62\n" \
"v_cvt_pk_bf16_f32 v128,0,v128\ns_waitcnt vmcnt(0)\nv_add_f32 v126,1.0,v126\nv_cvt_pk_bf16_f32 v126,0,v126\nv_lshlrev_b32 v144,16,v144\nv_add_f32 v128,v128,v144\nv_cvt_pk_bf16_f32 v128,0,v128\nv_subrev_f32 v129,s82,v128\n" \
"v_cvt_pk_bf16_f32 v129,0,v129\nv_mul_f32 v129,v129,v148\nv_cvt_pk_bf16_f32 v129,0,v129\nv_mul_f32 v129,v129,v126\nv_cvt_pk_bf16_f32 v129,0,v129\nv_add_f32 v129,v129,v63\nv_cvt_pk_bf16_f32 v129,0,v129\n.Lp2:\n" \
"buffer_load_dwordx4 v[160:163],v168,s[52:55],s70 offen sc1\nbuffer_load_dwordx4 v[164:167],v168,s[52:55],s71 offen sc1\ns_waitcnt vmcnt(0)\nv_max3_u32 v228,v160,v161,v162\nv_max3_u32 v229,v163,v164,v165\nv_max3_u32 v228,v228,v166,v167\nv_max_u32 v228,v228,v229\nv_pk_add_f32 v[160:161],v[160:161],v[164:165]\n" \
"v_cmp_eq_u32 vcc,-1,v228\nv_pk_add_f32 v[162:163],v[162:163],v[166:167]\ns_sub_u32 s73,s73,1\ns_cselect_b32 s73,0,s73\ns_cselect_b64 vcc,0,vcc\ns_cmp_lg_u64 vcc,0\ns_cselect_b32 s72,0x8000000,0\nv_cndmask_b32 v160,0,v160,s[90:91]\n" \
"v_cndmask_b32 v161,0,v161,s[90:91]\nv_cndmask_b32 v162,0,v162,s[90:91]\nv_cndmask_b32 v163,0,v163,s[90:91]\ns_nop 0\nv_permlane32_swap_b32 v160,v162\nv_permlane32_swap_b32 v161,v163\nv_pk_add_f32 v[160:161],v[160:161],v[162:163]\ns_nop 1\n" \
"v_permlane16_swap_b32 v160,v161\nv_add_f32 v160,v160,v161\ns_nop 1\nv_add_f32_dpp v160,v160,v160 quad_perm:[1,0,3,2] row_mask:0xf bank_mask:0xf\ns_nop 1\nv_add_f32_dpp v160,v160,v160 quad_perm:[2,3,0,1] row_mask:0xf bank_mask:0xf\ns_nop 1\nv_add_f32_dpp v160,v160,v160 row_half_mirror row_mask:0xf bank_mask:0xf\n" \
"s_nop 1\nv_add_f32_dpp v160,v160,v160 row_mirror row_mask:0xf bank_mask:0xf\nv_cvt_pk_bf16_f32 v160,0,v160\nv_mul_f32 v160,v160,v149\nv_cvt_pk_bf16_f32 v160,0,v160\nv_add_f32 v160,v160,v129\nv_cvt_pk_bf16_f32 v160,0,v160\nv_mul_f32 v160,v160,v127\n" \
"v_cvt_pk_bf16_f32 v160,0,v160\nv_add_f32 v160,v160,v128\nv_cvt_pk_bf16_f32 v160,0,v160\nbuffer_store_short_d16_hi v160,v150,s[44:47],s72 offen sc1\n.Lp2s:\ns_cbranch_scc1 .Lp2\ns_branch .Lfin\n.Lhs:\n" \
"s_sub_u32 s73,s73,1\ns_cselect_b32 s73,0,s73\ns_cbranch_scc1 .Lhb\ns_sleep 2\nbuffer_load_dwordx2 v[166:167],v165,s[52:55],s80 offen sc1\ns_waitcnt vmcnt(0)\nv_xor_b32 v166,v166,v230\nv_xor_b32 v167,v167,v231\n" \
"v_or_b32 v166,v166,v167\nv_cmp_ne_u32 vcc,0,v166\ns_nop 0\ns_cbranch_vccnz .Lhs\ns_branch .Lhb\n.Lps0:\ns_sub_u32 s73,s73,1\ns_cselect_b32 s73,0,s73\n" \
"s_cbranch_scc1 .Lpb0\ns_sleep 1\nbuffer_load_dword v248,v228,s[52:55],s80 offen sc1\ns_waitcnt vmcnt(0)\nv_cmp_eq_u32 vcc,0,v248\ns_nop 0\ns_cbranch_vccnz .Lps0\ns_branch .Lpb0\n" \
".Lps1:\ns_sub_u32 s73,s73,1\ns_cselect_b32 s73,0,s73\ns_cbranch_scc1 .Lpb1\ns_sleep 1\nbuffer_load_dword v248,v228,s[52:55],s80 offen sc1\ns_waitcnt vmcnt(0)\nv_cmp_eq_u32 vcc,0,v248\n" \
"s_nop 0\ns_cbranch_vccnz .Lps1\ns_branch .Lpb1\n.Lps2:\ns_sub_u32 s73,s73,1\ns_cselect_b32 s73,0,s73\ns_cbranch_scc1 .Lpb2\ns_sleep 1\n" \
"buffer_load_dword v248,v251,s[52:55],s80 offen sc1\ns_waitcnt vmcnt(0)\nv_cmp_eq_u32 vcc,0,v248\ns_nop 0\ns_cbranch_vccnz .Lps2\ns_branch .Lpb2\n.Lps3:\ns_sub_u32 s73,s73,1\n" \
"s_cselect_b32 s73,0,s73\ns_cbranch_scc1 .Lpb3\ns_sleep 1\nbuffer_load_dword v248,v251,s[52:55],s80 offen sc1\ns_waitcnt vmcnt(0)\nv_cmp_eq_u32 vcc,0,v248\ns_nop 0\ns_cbranch_vccnz .Lps3\n" \
"s_branch .Lpb3\n.Lps4:\ns_sub_u32 s73,s73,1\ns_cselect_b32 s73,0,s73\ns_cbranch_scc1 .Lpb4\ns_sleep 1\nbuffer_load_dword v248,v251,s[52:55],s80 offen sc1\ns_waitcnt vmcnt(0)\n" \
"v_cmp_eq_u32 vcc,0,v248\ns_nop 0\ns_cbranch_vccnz .Lps4\ns_branch .Lpb4\n.Lps5:\ns_sub_u32 s73,s73,1\ns_cselect_b32 s73,0,s73\ns_cbranch_scc1 .Lpb5\n" \
"s_sleep 1\nbuffer_load_dword v248,v251,s[52:55],s80 offen sc1\ns_waitcnt vmcnt(0)\nv_cmp_eq_u32 vcc,0,v248\ns_nop 0\ns_cbranch_vccnz .Lps5\ns_branch .Lpb5\n.Lx01:\n" \
"s_cmp_eq_u32 s62,0\ns_cbranch_scc1 .Ltrans\ns_branch .Ldf\n.Lps6:\ns_sub_u32 s73,s73,1\ns_cselect_b32 s73,0,s73\ns_cbranch_scc1 .Lpb6\ns_sleep 1\n" \
"buffer_load_dword v248,v251,s[52:55],s80 offen sc1\ns_waitcnt vmcnt(0)\nv_and_b32 v248,s17,v248\nv_cmp_eq_u32 vcc,0,v248\ns_nop 0\ns_cbranch_vccnz .Lps6\ns_branch .Lpb6\n.Lps7:\n" \
"s_sub_u32 s73,s73,1\ns_cselect_b32 s73,0,s73\ns_cbranch_scc1 .Lpb7\ns_sleep 1\nbuffer_load_dword v248,v254,s[52:55],s80 offen sc1\ns_waitcnt vmcnt(0)\nv_cmp_eq_u32 vcc,0,v248\ns_nop 0\n" \
"s_cbranch_vccnz .Lps7\ns_branch .Lpb7\n.Lps8:\ns_sub_u32 s73,s73,1\ns_cselect_b32 s73,0,s73\ns_cbranch_scc1 .Lpb8\ns_sleep 1\nbuffer_load_dword v248,v251,s[52:55],s79 offen sc1\n" \
"s_waitcnt vmcnt(0)\nv_cmp_eq_u32 vcc,0,v248\ns_nop 0\ns_cbranch_vccnz .Lps8\ns_branch .Lpb8\n.Lrs1:\ns_mov_b32 s71,0\n.Lrl1:\n" \
"s_mov_b32 s97,0\nv_max3_u32 v124,v160,v161,v162\nv_max3_u32 v124,v124,v163,v163\nv_cmp_eq_u32 vcc,-1,v124\ns_add_u32 s72,s95,0\ns_cbranch_vccz .Lrz10\ns_mov_b64 exec,vcc\ns_mov_b32 s97,1\n" \
"buffer_load_dwordx4 v[160:163],v240,s[52:55],s72 offen sc1\ns_mov_b64 exec,-1\n.Lrz10:\nv_max3_u32 v124,v164,v165,v166\nv_max3_u32 v124,v124,v167,v167\nv_cmp_eq_u32 vcc,-1,v124\ns_add_u32 s72,s95,2097152\ns_cbranch_vccz .Lrz11\n" \
"s_mov_b64 exec,vcc\ns_mov_b32 s97,1\nbuffer_load_dwordx4 v[164:167],v240,s[52:55],s72 offen sc1\ns_mov_b64 exec,-1\n.Lrz11:\nv_max3_u32 v124,v168,v169,v170\nv_max3_u32 v124,v124,v171,v171\nv_cmp_eq_u32 vcc,-1,v124\n" \
"s_add_u32 s72,s95,4194304\ns_cbranch_vccz .Lrz12\ns_mov_b64 exec,vcc\ns_mov_b32 s97,1\nbuffer_load_dwordx4 v[168:171],v240,s[52:55],s72 offen sc1\ns_mov_b64 exec,-1\n.Lrz12:\nv_max3_u32 v124,v172,v173,v174\n" \
"v_max3_u32 v124,v124,v175,v175\nv_cmp_eq_u32 vcc,-1,v124\ns_add_u32 s72,s95,6291456\ns_cbranch_vccz .Lrz13\ns_mov_b64 exec,vcc\ns_mov_b32 s97,1\nbuffer_load_dwordx4 v[172:175],v240,s[52:55],s72 offen sc1\ns_mov_b64 exec,-1\n" \
".Lrz13:\nv_max3_u32 v124,v176,v177,v178\nv_max3_u32 v124,v124,v179,v179\nv_cmp_eq_u32 vcc,-1,v124\ns_add_u32 s72,s95,8388608\ns_cbranch_vccz .Lrz14\ns_mov_b64 exec,vcc\ns_mov_b32 s97,1\n" \
"buffer_load_dwordx4 v[176:179],v240,s[52:55],s72 offen sc1\ns_mov_b64 exec,-1\n.Lrz14:\nv_max3_u32 v124,v180,v181,v182\nv_max3_u32 v124,v124,v183,v183\nv_cmp_eq_u32 vcc,-1,v124\ns_add_u32 s72,s95,10485760\ns_cbranch_vccz .Lrz15\n" \
"s_mov_b64 exec,vcc\ns_mov_b32 s97,1\nbuffer_load_dwordx4 v[180:183],v240,s[52:55],s72 offen sc1\ns_mov_b64 exec,-1\n.Lrz15:\nv_max3_u32 v124,v184,v185,v186\nv_max3_u32 v124,v124,v187,v187\nv_cmp_eq_u32 vcc,-1,v124\n" \
"s_add_u32 s72,s95,12582912\ns_cbranch_vccz .Lrz16\ns_mov_b64 exec,vcc\ns_mov_b32 s97,1\nbuffer_load_dwordx4 v[184:187],v240,s[52:55],s72 offen sc1\ns_mov_b64 exec,-1\n.Lrz16:\nv_max3_u32 v124,v188,v189,v190\n" \
"v_max3_u32 v124,v124,v191,v191\nv_cmp_eq_u32 vcc,-1,v124\ns_add_u32 s72,s95,14680064\ns_cbranch_vccz .Lrz17\ns_mov_b64 exec,vcc\ns_mov_b32 s97,1\nbuffer_load_dwordx4 v[188:191],v240,s[52:55],s72 offen sc1\ns_mov_b64 exec,-1\n" \
".Lrz17:\ns_cmp_lg_u32 s97,0\ns_cbranch_scc1 .Lrm1\ns_cmp_eq_u32 s71,0\ns_cbranch_scc1 .Lrb1\ns_branch .Lrow1\n.Lrm1:\ns_mov_b32 s71,1\n" \
"s_waitcnt vmcnt(0)\ns_sub_u32 s73,s73,1\ns_cselect_b32 s73,0,s73\ns_cbranch_scc1 .Lrb1\ns_sleep 2\ns_branch .Lrl1\n.Lrs0:\ns_mov_b32 s71,0\n" \
".Lrl0:\ns_mov_b32 s97,0\nv_max3_u32 v60,v128,v129,v130\nv_max3_u32 v60,v60,v131,v131\nv_cmp_eq_u32 vcc,-1,v60\ns_add_u32 s72,s74,0\ns_cbranch_vccz .Lrz00\ns_mov_b64 exec,vcc\n" \
"s_mov_b32 s97,1\nbuffer_load_dwordx4 v[128:131],v240,s[52:55],s72 offen sc1\ns_mov_b64 exec,-1\n.Lrz00:\nv_max3_u32 v60,v132,v133,v134\nv_max3_u32 v60,v60,v135,v135\nv_cmp_eq_u32 vcc,-1,v60\ns_add_u32 s72,s74,2097152\n" \
"s_cbranch_vccz .Lrz01\ns_mov_b64 exec,vcc\ns_mov_b32 s97,1\nbuffer_load_dwordx4 v[132:135],v240,s[52:55],s72 offen sc1\ns_mov_b64 exec,-1\n.Lrz01:\nv_max3_u32 v60,v136,v137,v138\nv_max3_u32 v60,v60,v139,v139\n" \
"v_cmp_eq_u32 vcc,-1,v60\ns_add_u32 s72,s74,4194304\ns_cbranch_vccz .Lrz02\ns_mov_b64 exec,vcc\ns_mov_b32 s97,1\nbuffer_load_dwordx4 v[136:139],v240,s[52:55],s72 offen sc1\ns_mov_b64 exec,-1\n.Lrz02:\n" \
"v_max3_u32 v60,v140,v141,v142\nv_max3_u32 v60,v60,v143,v143\nv_cmp_eq_u32 vcc,-1,v60\ns_add_u32 s72,s74,6291456\ns_cbranch_vccz .Lrz03\ns_mov_b64 exec,vcc\ns_mov_b32 s97,1\nbuffer_load_dwordx4 v[140:143],v240,s[52:55],s72 offen sc1\n" \
"s_mov_b64 exec,-1\n.Lrz03:\nv_max3_u32 v60,v144,v145,v146\nv_max3_u32 v60,v60,v147,v147\nv_cmp_eq_u32 vcc,-1,v60\ns_add_u32 s72,s74,8388608\ns_cbranch_vccz .Lrz04\ns_mov_b64 exec,vcc\n" \
"s_mov_b32 s97,1\nbuffer_load_dwordx4 v[144:147],v240,s[52:55],s72 offen sc1\ns_mov_b64 exec,-1\n.Lrz04:\nv_max3_u32 v60,v148,v149,v150\nv_max3_u32 v60,v60,v151,v151\nv_cmp_eq_u32 vcc,-1,v60\ns_add_u32 s72,s74,10485760\n" \
"s_cbranch_vccz .Lrz05\ns_mov_b64 exec,vcc\ns_mov_b32 s97,1\nbuffer_load_dwordx4 v[148:151],v240,s[52:55],s72 offen sc1\ns_mov_b64 exec,-1\n.Lrz05:\nv_max3_u32 v60,v152,v153,v154\nv_max3_u32 v60,v60,v155,v155\n" \
"v_cmp_eq_u32 vcc,-1,v60\ns_add_u32 s72,s74,12582912\ns_cbranch_vccz .Lrz06\ns_mov_b64 exec,vcc\ns_mov_b32 s97,1\nbuffer_load_dwordx4 v[152:155],v240,s[52:55],s72 offen sc1\ns_mov_b64 exec,-1\n.Lrz06:\n" \
"v_max3_u32 v60,v156,v157,v158\nv_max3_u32 v60,v60,v159,v159\nv_cmp_eq_u32 vcc,-1,v60\ns_add_u32 s72,s74,14680064\ns_cbranch_vccz .Lrz07\ns_mov_b64 exec,vcc\ns_mov_b32 s97,1\nbuffer_load_dwordx4 v[156:159],v240,s[52:55],s72 offen sc1\n" \
"s_mov_b64 exec,-1\n.Lrz07:\ns_cmp_lg_u32 s97,0\ns_cbranch_scc1 .Lrm0\ns_cmp_eq_u32 s71,0\ns_cbranch_scc1 .Lrb0\ns_branch .Lrow0\n.Lrm0:\n" \
"s_mov_b32 s71,1\ns_waitcnt vmcnt(0)\ns_sub_u32 s73,s73,1\ns_cselect_b32 s73,0,s73\ns_cbranch_scc1 .Lrb0\ns_sleep 2\ns_branch .Lrl0\n.Lredo:\n" \
"s_waitcnt vmcnt(0) lgkmcnt(0)\ns_barrier\ns_mov_b64 s[90:91],0\ns_mov_b32 s99,0\ns_mov_b32 s17,1\ns_lshl_b32 s70,s63,19\ns_add_u32 s37,s70,3145728\ns_lshl_b32 s71,s62,15\n" \
"s_add_u32 s72,s37,s64\ns_add_u32 s72,s72,491520\ns_sub_u32 s72,s72,s71\ns_add_u32 m0,s64,0\ns_nop 0\nbuffer_load_dwordx4 v240,s[52:55],s72 offen offset:0 sc1 lds\nbuffer_load_dwordx4 v240,s[52:55],s72 offen offset:1024 sc1 lds\nbuffer_load_dwordx4 v240,s[52:55],s72 offen offset:2048 sc1 lds\n" \
"buffer_load_dwordx4 v240,s[52:55],s72 offen offset:3072 sc1 lds\ns_add_u32 m0,s64,65536\ns_add_u32 s72,s72,16777216\nbuffer_load_dwordx4 v240,s[52:55],s72 offen offset:0 sc1 lds\nbuffer_load_dwordx4 v240,s[52:55],s72 offen offset:1024 sc1 lds\nbuffer_load_dwordx4 v240,s[52:55],s72 offen offset:2048 sc1 lds\nbuffer_load_dwordx4 v240,s[52:55],s72 offen offset:3072 sc1 lds\ns_add_u32 s72,s37,s64\n" \
"s_add_u32 s72,s72,507904\ns_sub_u32 s72,s72,s71\ns_add_u32 m0,s64,16384\ns_nop 0\nbuffer_load_dwordx4 v240,s[52:55],s72 offen offset:0 sc1 lds\nbuffer_load_dwordx4 v240,s[52:55],s72 offen offset:1024 sc1 lds\nbuffer_load_dwordx4 v240,s[52:55],s72 offen offset:2048 sc1 lds\nbuffer_load_dwordx4 v240,s[52:55],s72 offen offset:3072 sc1 lds\n" \
"s_add_u32 m0,s64,81920\ns_add_u32 s72,s72,16777216\nbuffer_load_dwordx4 v240,s[52:55],s72 offen offset:0 sc1 lds\nbuffer_load_dwordx4 v240,s[52:55],s72 offen offset:1024 sc1 lds\nbuffer_load_dwordx4 v240,s[52:55],s72 offen offset:2048 sc1 lds\nbuffer_load_dwordx4 v240,s[52:55],s72 offen offset:3072 sc1 lds\ns_add_u32 s72,s37,s64\ns_add_u32 s72,s72,16384\n" \
"s_add_u32 s72,s72,s71\ns_add_u32 m0,s64,32768\ns_nop 0\nbuffer_load_dwordx4 v240,s[52:55],s72 offen offset:0 sc1 lds\nbuffer_load_dwordx4 v240,s[52:55],s72 offen offset:1024 sc1 lds\nbuffer_load_dwordx4 v240,s[52:55],s72 offen offset:2048 sc1 lds\nbuffer_load_dwordx4 v240,s[52:55],s72 offen offset:3072 sc1 lds\ns_add_u32 m0,s64,98304\n" \
"s_add_u32 s72,s72,16777216\nbuffer_load_dwordx4 v240,s[52:55],s72 offen offset:0 sc1 lds\nbuffer_load_dwordx4 v240,s[52:55],s72 offen offset:1024 sc1 lds\nbuffer_load_dwordx4 v240,s[52:55],s72 offen offset:2048 sc1 lds\nbuffer_load_dwordx4 v240,s[52:55],s72 offen offset:3072 sc1 lds\ns_add_u32 s72,s37,s64\ns_add_u32 s72,s72,0\ns_add_u32 s72,s72,s71\n" \
"s_add_u32 m0,s64,49152\ns_nop 0\nbuffer_load_dwordx4 v240,s[52:55],s72 offen offset:0 sc1 lds\nbuffer_load_dwordx4 v240,s[52:55],s72 offen offset:1024 sc1 lds\nbuffer_load_dwordx4 v240,s[52:55],s72 offen offset:2048 sc1 lds\nbuffer_load_dwordx4 v240,s[52:55],s72 offen offset:3072 sc1 lds\ns_add_u32 m0,s64,114688\ns_add_u32 s72,s72,16777216\n" \
"buffer_load_dwordx4 v240,s[52:55],s72 offen offset:0 sc1 lds\nbuffer_load_dwordx4 v240,s[52:55],s72 offen offset:1024 sc1 lds\nbuffer_load_dwordx4 v240,s[52:55],s72 offen offset:2048 sc1 lds\nbuffer_load_dwordx4 v240,s[52:55],s72 offen offset:3072 sc1 lds\nv_and_b32 v251,15,v249\nv_lshrrev_b32 v250,4,v249\nv_lshlrev_b32 v250,2,v250\nv_sub_u32 v251,v251,v250\n" \
"s_lshl_b32 s70,s62,7\ns_lshl_b32 s72,s7,5\ns_sub_u32 s38,s72,s70\ns_add_u32 s38,s38,1920\ns_sub_u32 s39,s70,s72\ns_add_u32 s39,s39,96\nv_add_u32 v225,s38,v251\nv_add_u32 v225,0,v225\n" \
"v_mov_b32 v224,0xff800000\nv_add_u32 v229,s38,v251\nv_add_u32 v229,16,v229\nv_mov_b32 v228,0xff800000\nv_add_u32 v233,s39,v251\nv_add_u32 v233,0,v233\nv_mov_b32 v232,0xff800000\nv_add_u32 v237,s39,v251\n" \
"v_add_u32 v237,16,v237\nv_mov_b32 v236,0xff800000\ns_sub_u32 s38,524288,s71\ns_add_u32 s38,s38,s37\n.Lrl:\nbuffer_load_dwordx4 a[0:3],v240,s[52:55],s37 offen offset:0 sc1\nbuffer_load_dwordx4 a[4:7],v240,s[52:55],s37 offen offset:1024 sc1\nbuffer_load_dwordx4 a[8:11],v240,s[52:55],s37 offen offset:2048 sc1\n" \
"buffer_load_dwordx4 a[12:15],v240,s[52:55],s37 offen offset:3072 sc1\ns_add_u32 s37,s37,4096\nbuffer_load_dwordx4 a[16:19],v240,s[52:55],s37 offen offset:0 sc1\nbuffer_load_dwordx4 a[20:23],v240,s[52:55],s37 offen offset:1024 sc1\nbuffer_load_dwordx4 a[24:27],v240,s[52:55],s37 offen offset:2048 sc1\nbuffer_load_dwordx4 a[28:31],v240,s[52:55],s37 offen offset:3072 sc1\ns_add_u32 s37,s37,4096\nbuffer_load_dwordx4 a[32:35],v240,s[52:55],s37 offen offset:0 sc1\n" \
"buffer_load_dwordx4 a[36:39],v240,s[52:55],s37 offen offset:1024 sc1\nbuffer_load_dwordx4 a[40:43],v240,s[52:55],s37 offen offset:2048 sc1\nbuffer_load_dwordx4 a[44:47],v240,s[52:55],s37 offen offset:3072 sc1\ns_add_u32 s37,s37,4096\nbuffer_load_dwordx4 a[48:51],v240,s[52:55],s37 offen offset:0 sc1\nbuffer_load_dwordx4 a[52:55],v240,s[52:55],s37 offen offset:1024 sc1\nbuffer_load_dwordx4 a[56:59],v240,s[52:55],s37 offen offset:2048 sc1\nbuffer_load_dwordx4 a[60:63],v240,s[52:55],s37 offen offset:3072 sc1\n" \
"s_add_u32 s37,s37,4096\ns_waitcnt vmcnt(15)\nM v[128:131],a[0:3],a[128:131],0\ns_waitcnt vmcnt(14)\nM v[128:131],a[4:7],a[132:135],v[128:131]\ns_waitcnt vmcnt(13)\nM v[128:131],a[8:11],a[136:139],v[128:131]\ns_waitcnt vmcnt(12)\n" \
"M v[128:131],a[12:15],a[140:143],v[128:131]\nM v[132:135],a[0:3],a[144:147],0\nM v[132:135],a[4:7],a[148:151],v[132:135]\nM v[132:135],a[8:11],a[152:155],v[132:135]\nM v[132:135],a[12:15],a[156:159],v[132:135]\nM v[136:139],a[0:3],a[160:163],0\nM v[136:139],a[4:7],a[164:167],v[136:139]\nM v[136:139],a[8:11],a[168:171],v[136:139]\n" \
"M v[136:139],a[12:15],a[172:175],v[136:139]\nM v[140:143],a[0:3],a[176:179],0\nM v[140:143],a[4:7],a[180:183],v[140:143]\nM v[140:143],a[8:11],a[184:187],v[140:143]\nM v[140:143],a[12:15],a[188:191],v[140:143]\ns_waitcnt vmcnt(11)\nM v[144:147],a[16:19],a[128:131],0\ns_waitcnt vmcnt(10)\n" \
"M v[144:147],a[20:23],a[132:135],v[144:147]\ns_waitcnt vmcnt(9)\nM v[144:147],a[24:27],a[136:139],v[144:147]\ns_waitcnt vmcnt(8)\nM v[144:147],a[28:31],a[140:143],v[144:147]\nM v[148:151],a[16:19],a[144:147],0\nM v[148:151],a[20:23],a[148:151],v[148:151]\nM v[148:151],a[24:27],a[152:155],v[148:151]\n" \
"M v[148:151],a[28:31],a[156:159],v[148:151]\nM v[152:155],a[16:19],a[160:163],0\nM v[152:155],a[20:23],a[164:167],v[152:155]\nM v[152:155],a[24:27],a[168:171],v[152:155]\nM v[152:155],a[28:31],a[172:175],v[152:155]\nM v[156:159],a[16:19],a[176:179],0\nM v[156:159],a[20:23],a[180:183],v[156:159]\nM v[156:159],a[24:27],a[184:187],v[156:159]\n" \
"M v[156:159],a[28:31],a[188:191],v[156:159]\ns_waitcnt vmcnt(7)\nM v[160:163],a[32:35],a[128:131],0\ns_waitcnt vmcnt(6)\nM v[160:163],a[36:39],a[132:135],v[160:163]\ns_waitcnt vmcnt(5)\nM v[160:163],a[40:43],a[136:139],v[160:163]\ns_waitcnt vmcnt(4)\n" \
"M v[160:163],a[44:47],a[140:143],v[160:163]\nM v[164:167],a[32:35],a[144:147],0\nM v[164:167],a[36:39],a[148:151],v[164:167]\nM v[164:167],a[40:43],a[152:155],v[164:167]\nM v[164:167],a[44:47],a[156:159],v[164:167]\nM v[168:171],a[32:35],a[160:163],0\nM v[168:171],a[36:39],a[164:167],v[168:171]\nM v[168:171],a[40:43],a[168:171],v[168:171]\n" \
"M v[168:171],a[44:47],a[172:175],v[168:171]\nM v[172:175],a[32:35],a[176:179],0\nM v[172:175],a[36:39],a[180:183],v[172:175]\nM v[172:175],a[40:43],a[184:187],v[172:175]\nM v[172:175],a[44:47],a[188:191],v[172:175]\ns_waitcnt vmcnt(3)\nM v[176:179],a[48:51],a[128:131],0\ns_waitcnt vmcnt(2)\n" \
"M v[176:179],a[52:55],a[132:135],v[176:179]\ns_waitcnt vmcnt(1)\nM v[176:179],a[56:59],a[136:139],v[176:179]\ns_waitcnt vmcnt(0)\nM v[176:179],a[60:63],a[140:143],v[176:179]\nM v[180:183],a[48:51],a[144:147],0\nM v[180:183],a[52:55],a[148:151],v[180:183]\nM v[180:183],a[56:59],a[152:155],v[180:183]\n" \
"M v[180:183],a[60:63],a[156:159],v[180:183]\nM v[184:187],a[48:51],a[160:163],0\nM v[184:187],a[52:55],a[164:167],v[184:187]\nM v[184:187],a[56:59],a[168:171],v[184:187]\nM v[184:187],a[60:63],a[172:175],v[184:187]\nM v[188:191],a[48:51],a[176:179],0\nM v[188:191],a[52:55],a[180:183],v[188:191]\nM v[188:191],a[56:59],a[184:187],v[188:191]\n" \
"M v[188:191],a[60:63],a[188:191],v[188:191]\nv_cmp_le_i32 vcc,0,v225\ns_nop 1\nv_cndmask_b32 v128,v252,v128,vcc\nv_max_f32 v224,v224,v128\nv_cmp_le_i32 vcc,1,v225\ns_nop 1\nv_cndmask_b32 v129,v252,v129,vcc\n" \
"v_max_f32 v224,v224,v129\nv_cmp_le_i32 vcc,2,v225\ns_nop 1\nv_cndmask_b32 v130,v252,v130,vcc\nv_max_f32 v224,v224,v130\nv_cmp_le_i32 vcc,3,v225\ns_nop 1\nv_cndmask_b32 v131,v252,v131,vcc\n" \
"v_max_f32 v224,v224,v131\nv_cmp_le_i32 vcc,0,v229\ns_nop 1\nv_cndmask_b32 v132,v252,v132,vcc\nv_max_f32 v228,v228,v132\nv_cmp_le_i32 vcc,1,v229\ns_nop 1\nv_cndmask_b32 v133,v252,v133,vcc\n" \
"v_max_f32 v228,v228,v133\nv_cmp_le_i32 vcc,2,v229\ns_nop 1\nv_cndmask_b32 v134,v252,v134,vcc\nv_max_f32 v228,v228,v134\nv_cmp_le_i32 vcc,3,v229\ns_nop 1\nv_cndmask_b32 v135,v252,v135,vcc\n" \
"v_max_f32 v228,v228,v135\nv_cmp_le_i32 vcc,0,v233\ns_nop 1\nv_cndmask_b32 v136,v252,v136,vcc\nv_max_f32 v232,v232,v136\nv_cmp_le_i32 vcc,1,v233\ns_nop 1\nv_cndmask_b32 v137,v252,v137,vcc\n" \
"v_max_f32 v232,v232,v137\nv_cmp_le_i32 vcc,2,v233\ns_nop 1\nv_cndmask_b32 v138,v252,v138,vcc\nv_max_f32 v232,v232,v138\nv_cmp_le_i32 vcc,3,v233\ns_nop 1\nv_cndmask_b32 v139,v252,v139,vcc\n" \
"v_max_f32 v232,v232,v139\nv_cmp_le_i32 vcc,0,v237\ns_nop 1\nv_cndmask_b32 v140,v252,v140,vcc\nv_max_f32 v236,v236,v140\nv_cmp_le_i32 vcc,1,v237\ns_nop 1\nv_cndmask_b32 v141,v252,v141,vcc\n" \
"v_max_f32 v236,v236,v141\nv_cmp_le_i32 vcc,2,v237\ns_nop 1\nv_cndmask_b32 v142,v252,v142,vcc\nv_max_f32 v236,v236,v142\nv_cmp_le_i32 vcc,3,v237\ns_nop 1\nv_cndmask_b32 v143,v252,v143,vcc\n" \
"v_max_f32 v236,v236,v143\nv_cmp_le_i32 vcc,16,v225\ns_nop 1\nv_cndmask_b32 v144,v252,v144,vcc\nv_max_f32 v224,v224,v144\nv_cmp_le_i32 vcc,17,v225\ns_nop 1\nv_cndmask_b32 v145,v252,v145,vcc\n" \
"v_max_f32 v224,v224,v145\nv_cmp_le_i32 vcc,18,v225\ns_nop 1\nv_cndmask_b32 v146,v252,v146,vcc\nv_max_f32 v224,v224,v146\nv_cmp_le_i32 vcc,19,v225\ns_nop 1\nv_cndmask_b32 v147,v252,v147,vcc\n" \
"v_max_f32 v224,v224,v147\nv_cmp_le_i32 vcc,16,v229\ns_nop 1\nv_cndmask_b32 v148,v252,v148,vcc\nv_max_f32 v228,v228,v148\nv_cmp_le_i32 vcc,17,v229\ns_nop 1\nv_cndmask_b32 v149,v252,v149,vcc\n" \
"v_max_f32 v228,v228,v149\nv_cmp_le_i32 vcc,18,v229\ns_nop 1\nv_cndmask_b32 v150,v252,v150,vcc\nv_max_f32 v228,v228,v150\nv_cmp_le_i32 vcc,19,v229\ns_nop 1\nv_cndmask_b32 v151,v252,v151,vcc\n" \
"v_max_f32 v228,v228,v151\nv_cmp_le_i32 vcc,16,v233\ns_nop 1\nv_cndmask_b32 v152,v252,v152,vcc\nv_max_f32 v232,v232,v152\nv_cmp_le_i32 vcc,17,v233\ns_nop 1\nv_cndmask_b32 v153,v252,v153,vcc\n" \
"v_max_f32 v232,v232,v153\nv_cmp_le_i32 vcc,18,v233\ns_nop 1\nv_cndmask_b32 v154,v252,v154,vcc\nv_max_f32 v232,v232,v154\nv_cmp_le_i32 vcc,19,v233\ns_nop 1\nv_cndmask_b32 v155,v252,v155,vcc\n" \
"v_max_f32 v232,v232,v155\nv_cmp_le_i32 vcc,16,v237\ns_nop 1\nv_cndmask_b32 v156,v252,v156,vcc\nv_max_f32 v236,v236,v156\nv_cmp_le_i32 vcc,17,v237\ns_nop 1\nv_cndmask_b32 v157,v252,v157,vcc\n" \
"v_max_f32 v236,v236,v157\nv_cmp_le_i32 vcc,18,v237\ns_nop 1\nv_cndmask_b32 v158,v252,v158,vcc\nv_max_f32 v236,v236,v158\nv_cmp_le_i32 vcc,19,v237\ns_nop 1\nv_cndmask_b32 v159,v252,v159,vcc\n" \
"v_max_f32 v236,v236,v159\nv_cmp_le_i32 vcc,32,v225\ns_nop 1\nv_cndmask_b32 v160,v252,v160,vcc\nv_max_f32 v224,v224,v160\nv_cmp_le_i32 vcc,33,v225\ns_nop 1\nv_cndmask_b32 v161,v252,v161,vcc\n" \
"v_max_f32 v224,v224,v161\nv_cmp_le_i32 vcc,34,v225\ns_nop 1\nv_cndmask_b32 v162,v252,v162,vcc\nv_max_f32 v224,v224,v162\nv_cmp_le_i32 vcc,35,v225\ns_nop 1\nv_cndmask_b32 v163,v252,v163,vcc\n" \
"v_max_f32 v224,v224,v163\nv_cmp_le_i32 vcc,32,v229\ns_nop 1\nv_cndmask_b32 v164,v252,v164,vcc\nv_max_f32 v228,v228,v164\nv_cmp_le_i32 vcc,33,v229\ns_nop 1\nv_cndmask_b32 v165,v252,v165,vcc\n" \
"v_max_f32 v228,v228,v165\nv_cmp_le_i32 vcc,34,v229\ns_nop 1\nv_cndmask_b32 v166,v252,v166,vcc\nv_max_f32 v228,v228,v166\nv_cmp_le_i32 vcc,35,v229\ns_nop 1\nv_cndmask_b32 v167,v252,v167,vcc\n" \
"v_max_f32 v228,v228,v167\nv_cmp_le_i32 vcc,32,v233\ns_nop 1\nv_cndmask_b32 v168,v252,v168,vcc\nv_max_f32 v232,v232,v168\nv_cmp_le_i32 vcc,33,v233\ns_nop 1\nv_cndmask_b32 v169,v252,v169,vcc\n" \
"v_max_f32 v232,v232,v169\nv_cmp_le_i32 vcc,34,v233\ns_nop 1\nv_cndmask_b32 v170,v252,v170,vcc\nv_max_f32 v232,v232,v170\nv_cmp_le_i32 vcc,35,v233\ns_nop 1\nv_cndmask_b32 v171,v252,v171,vcc\n" \
"v_max_f32 v232,v232,v171\nv_cmp_le_i32 vcc,32,v237\ns_nop 1\nv_cndmask_b32 v172,v252,v172,vcc\nv_max_f32 v236,v236,v172\nv_cmp_le_i32 vcc,33,v237\ns_nop 1\nv_cndmask_b32 v173,v252,v173,vcc\n" \
"v_max_f32 v236,v236,v173\nv_cmp_le_i32 vcc,34,v237\ns_nop 1\nv_cndmask_b32 v174,v252,v174,vcc\nv_max_f32 v236,v236,v174\nv_cmp_le_i32 vcc,35,v237\ns_nop 1\nv_cndmask_b32 v175,v252,v175,vcc\n" \
"v_max_f32 v236,v236,v175\nv_cmp_le_i32 vcc,48,v225\ns_nop 1\nv_cndmask_b32 v176,v252,v176,vcc\nv_max_f32 v224,v224,v176\nv_cmp_le_i32 vcc,49,v225\ns_nop 1\nv_cndmask_b32 v177,v252,v177,vcc\n" \
"v_max_f32 v224,v224,v177\nv_cmp_le_i32 vcc,50,v225\ns_nop 1\nv_cndmask_b32 v178,v252,v178,vcc\nv_max_f32 v224,v224,v178\nv_cmp_le_i32 vcc,51,v225\ns_nop 1\nv_cndmask_b32 v179,v252,v179,vcc\n" \
"v_max_f32 v224,v224,v179\nv_cmp_le_i32 vcc,48,v229\ns_nop 1\nv_cndmask_b32 v180,v252,v180,vcc\nv_max_f32 v228,v228,v180\nv_cmp_le_i32 vcc,49,v229\ns_nop 1\nv_cndmask_b32 v181,v252,v181,vcc\n" \
"v_max_f32 v228,v228,v181\nv_cmp_le_i32 vcc,50,v229\ns_nop 1\nv_cndmask_b32 v182,v252,v182,vcc\nv_max_f32 v228,v228,v182\nv_cmp_le_i32 vcc,51,v229\ns_nop 1\nv_cndmask_b32 v183,v252,v183,vcc\n" \
"v_max_f32 v228,v228,v183\nv_cmp_le_i32 vcc,48,v233\ns_nop 1\nv_cndmask_b32 v184,v252,v184,vcc\nv_max_f32 v232,v232,v184\nv_cmp_le_i32 vcc,49,v233\ns_nop 1\nv_cndmask_b32 v185,v252,v185,vcc\n" \
"v_max_f32 v232,v232,v185\nv_cmp_le_i32 vcc,50,v233\ns_nop 1\nv_cndmask_b32 v186,v252,v186,vcc\nv_max_f32 v232,v232,v186\nv_cmp_le_i32 vcc,51,v233\ns_nop 1\nv_cndmask_b32 v187,v252,v187,vcc\n" \
"v_max_f32 v232,v232,v187\nv_cmp_le_i32 vcc,48,v237\ns_nop 1\nv_cndmask_b32 v188,v252,v188,vcc\nv_max_f32 v236,v236,v188\nv_cmp_le_i32 vcc,49,v237\ns_nop 1\nv_cndmask_b32 v189,v252,v189,vcc\n" \
"v_max_f32 v236,v236,v189\nv_cmp_le_i32 vcc,50,v237\ns_nop 1\nv_cndmask_b32 v190,v252,v190,vcc\nv_max_f32 v236,v236,v190\nv_cmp_le_i32 vcc,51,v237\ns_nop 1\nv_cndmask_b32 v191,v252,v191,vcc\n" \
"v_max_f32 v236,v236,v191\nv_subrev_u32 v225,64,v225\nv_subrev_u32 v229,64,v229\nv_subrev_u32 v233,64,v233\nv_subrev_u32 v237,64,v237\ns_cmp_lt_u32 s37,s38\ns_cbranch_scc1 .Lrl\ns_waitcnt vmcnt(0)\n" \
"s_barrier\nds_read_b128 a[0:3],v240 offset:0\nds_read_b128 a[4:7],v240 offset:1024\nds_read_b128 a[8:11],v240 offset:2048\nds_read_b128 a[12:15],v240 offset:3072\nds_read_b128 a[16:19],v240 offset:4096\nds_read_b128 a[20:23],v240 offset:5120\nds_read_b128 a[24:27],v240 offset:6144\n" \
"ds_read_b128 a[28:31],v240 offset:7168\nds_read_b128 a[32:35],v240 offset:8192\nds_read_b128 a[36:39],v240 offset:9216\nds_read_b128 a[40:43],v240 offset:10240\nds_read_b128 a[44:47],v240 offset:11264\nds_read_b128 a[48:51],v240 offset:12288\nds_read_b128 a[52:55],v240 offset:13312\nds_read_b128 a[56:59],v240 offset:14336\n" \
"ds_read_b128 a[60:63],v240 offset:15360\ns_cmp_eq_u32 s62,0\ns_cselect_b32 s36,0x8000,0\ns_add_u32 s36,s36,s64\ns_cmp_eq_u32 s36,s96\ns_cselect_b32 s72,0xc000,s98\ns_add_u32 s65,s36,s72\ns_cmp_eq_u32 s65,s96\n" \
"s_cselect_b32 s72,0xc000,s98\ns_add_u32 s66,s65,s72\ns_cmp_eq_u32 s66,s96\ns_cselect_b32 s72,0xc000,s98\ns_add_u32 s95,s66,s72\ns_add_u32 s97,s96,65536\ns_add_u32 s15,s64,s46\ns_cmp_gt_i32 s62,-1\n" \
"s_cselect_b32 s72,s98,0xfffe4000\ns_add_u32 s15,s15,s72\ns_mov_b32 s94,0xc000\ns_cmp_eq_u32 s62,7\ns_cselect_b32 s72,98304,0\ns_sub_u32 s97,s97,s72\nv_and_b32 v251,31,v249\nv_lshlrev_b32 v251,2,v251\n" \
"s_lshl_b32 s80,s63,7\ns_add_u32 s80,s80,86016\nv_add_u32 v250,0xc000,v241\ns_waitcnt lgkmcnt(0)\ns_branch .Lattn\n.Lfin:\n" \
""
constexpr size_t WS_DBG = (size_t)51 << 20;
constexpr unsigned LDS_BYTES = 163840;
struct Args {
    const u16 *x, *ckv, *w_q, *w_uk, *w_uv, *w_o, *w_r, *wg, *wu, *wd, *cvec, *w_mod;
    char* ws; u16* out;
};
typedef const __attribute__((address_space(4))) Args* kargs_t;
#define STAMPD(i) do {} while (0)
#define C8(p, a) p #a "0", p #a "1", p #a "2", p #a "3", p #a "4", p #a "5", p #a "6", p #a "7", p #a "8", p #a "9"
#define C100(p, a) C8(p, a##0), C8(p, a##1), C8(p, a##2), C8(p, a##3), C8(p, a##4), C8(p, a##5), C8(p, a##6), C8(p, a##7), C8(p, a##8), C8(p, a##9)
#define C10_99(p) C8(p, 1), C8(p, 2), C8(p, 3), C8(p, 4), C8(p, 5), C8(p, 6), C8(p, 7), C8(p, 8), C8(p, 9)
#define C200_249(p) C8(p, 20), C8(p, 21), C8(p, 22), C8(p, 23), C8(p, 24)
#define BODY_CLOB "memory", "vcc", "scc", "s0", "s1", "s2", "s3", "s8", "s9", C8("s", 1), C8("s", 2), "s30", "s31", "s33", "s34", "s35", "s36", "s37", "s38", "s39", C8("s", 4), C8("s", 5), C8("s", 6), C8("s", 7), C8("s", 8), C8("s", 9), \
    C8("v", ), C10_99("v"), C100("v", 1), C200_249("v"), "v250", "v251", "v252", "v253", "v254", C8("a", ), C10_99("a"), C100("a", 1), C200_249("a"), "a250", "a251", "a252", "a253", "a254", "a255"

__global__ __launch_bounds__(256, 1) void k_all(const Args a_) {
    kargs_t ap = (kargs_t)__builtin_amdgcn_kernarg_segment_ptr(); unsigned bid = blockIdx.x, wid = rfl(threadIdx.x >> 6), sv_;
    asm volatile(BODY_ASM : "+{s[4:5]}"(ap), "+{s6}"(bid), "+{s7}"(wid), "={v255}"(sv_) :: BODY_CLOB);
    asm volatile(".Lcode_e:");
}
extern "C" void kernel_launch(void* const* d_in, const int* in_sizes, int n_in, void* d_out, int out_size, void* d_ws, size_t ws_size, hipStream_t stream) {
    Args a;
    a.x = (const u16*)d_in[0]; a.ckv = (const u16*)d_in[1]; a.w_q = (const u16*)d_in[2]; a.w_uk = (const u16*)d_in[3]; a.w_uv = (const u16*)d_in[4]; a.w_o = (const u16*)d_in[5];
    a.w_r = (const u16*)d_in[7]; a.wg = (const u16*)d_in[8]; a.wu = (const u16*)d_in[9]; a.wd = (const u16*)d_in[10]; a.cvec = (const u16*)d_in[11]; a.w_mod = (const u16*)d_in[12];
    a.ws = (char*)d_ws;
    a.out = (u16*)d_out;
    (void)hipFuncSetAttribute((const void*)k_all, hipFuncAttributeMaxDynamicSharedMemorySize, LDS_BYTES);
    k_all<<<256, 256, LDS_BYTES, stream>>>(a);
}
```

```cpp
#include <hip/hip_runtime.h>
#include <hip/hip_bf16.h>
#include <stdint.h>
typedef unsigned short u16;
#define DI __device__ __forceinline__
DI unsigned lane_id() { unsigned l; asm volatile("v_mbcnt_lo_u32_b32 %0, -1, 0\n\tv_mbcnt_hi_u32_b32 %0, -1, %0" : "=v"(l)); return l; }
DI unsigned rfl(unsigned x) { return __builtin_amdgcn_readfirstlane(x); }
#define BODY_ASM ".macro M a,b,c,d\nv_mfma_f32_16x16x32_bf16 \\a,\\b,\\c,\\d\n.endm\n" \
".Lcode_b:\ns_load_dwordx16 s[8:23],s[4:5],0x0\ns_load_dwordx8 s[24:31],s[4:5],0x40\ns_load_dwordx2 s[34:35],s[4:5],0x60\nv_mbcnt_lo_u32_b32 v249,-1,0\nv_mbcnt_hi_u32_b32 v249,-1,v249\ns_bfe_u32 s61,s6,0x20003\ns_lshl_b32 s64,s7,12\n" \
"s_and_b32 s60,s6,7\ns_lshr_b32 s62,s6,5\ns_lshl2_add_u32 s63,s60,s61\nv_lshlrev_b32 v240,4,v249\nv_lshrrev_b32 v0,3,v249\nv_and_b32 v2,7,v249\nv_bfe_u32 v6,v249,2,3\nv_lshrrev_b32 v7,5,v249\n" \
"v_and_b32 v8,48,v240\nv_lshlrev_b32 v6,13,v6\nv_lshl_or_b32 v7,v7,6,v8\nv_or_b32 v6,0x1000,v6\nv_or_b32 v243,v6,v7\nv_add_u32 v244,0xfffffc80,v243\nv_xor_b32 v6,32,v243\nv_add_u32 v245,63488,v6\n" \
"v_add_u32 v246,62592,v6\ns_lshl_b32 s68,s7,17\ns_lshl_b32 s70,s63,8\ns_add_u32 s68,s68,s70\ns_mov_b32 s42,4198400\ns_mov_b32 s43,0x20000\nv_lshrrev_b32 v6,4,v249\nv_xor_b32 v7,v6,v2\n" \
"v_lshlrev_b32 v7,4,v7\nv_lshl_or_b32 v224,v0,10,v7\nv_xor_b32 v7,64,v224\nv_add_u32 v225,7168,v7\nv_add_u32 v226,14336,v224\nv_add_u32 v227,21504,v7\ns_lshl_b32 s70,s62,1\ns_sub_u32 s71,s7,s70\n" \
"s_add_u32 s71,s71,30\ns_sub_u32 s72,s70,s7\ns_add_u32 s72,s72,3\ns_cmp_lt_u32 s7,2\ns_cselect_b32 s74,s71,s72\ns_lshl_b32 s66,s74,16\ns_mul_i32 s65,s7,24576\ns_mov_b32 s38,2097152\n" \
"s_mov_b32 s39,0x20000\ns_xor_b32 s94,s61,s62\ns_and_b32 s94,s94,1\ns_lshl_b32 s99,s94,19\ns_or_b32 s99,s99,s68\ns_waitcnt lgkmcnt(0)\ns_sub_u32 s40,s14,0x1000\ns_subb_u32 s41,s15,0\n" \
"s_mov_b64 s[36:37],s[10:11]\ns_add_u32 m0,s64,147456\ns_add_u32 s69,s68,0\nbuffer_load_dwordx4 v243,s[40:43],s69 offen offset:0 lds\nbuffer_load_dwordx4 v244,s[40:43],s69 offen offset:1024 lds\nbuffer_load_dwordx4 v245,s[40:43],s69 offen offset:2048 lds\nbuffer_load_dwordx4 v246,s[40:43],s69 offen offset:3072 lds\ns_add_u32 m0,s65,0\n" \
"s_xor_b32 s67,s66,0\nbuffer_load_dwordx4 v224,s[36:39],s67 offen offset:0 lds\nbuffer_load_dwordx4 v225,s[36:39],s67 offen offset:1024 lds\nbuffer_load_dwordx4 v226,s[36:39],s67 offen offset:2048 lds\nbuffer_load_dwordx4 v227,s[36:39],s67 offen offset:3072 lds\ns_add_u32 m0,s65,4096\ns_xor_b32 s67,s66,32768\nbuffer_load_dwordx4 v224,s[36:39],s67 offen offset:0 lds\n" \
"buffer_load_dwordx4 v225,s[36:39],s67 offen offset:1024 lds\nbuffer_load_dwordx4 v226,s[36:39],s67 offen offset:2048 lds\nbuffer_load_dwordx4 v227,s[36:39],s67 offen offset:3072 lds\ns_cmp_lt_u32 s6,192\ns_cselect_b32 s70,s6,0\ns_mul_i32 s71,s70,171\ns_lshr_b32 s71,s71,13\ns_mul_i32 s72,s71,48\n" \
"s_sub_u32 s70,s70,s72\ns_lshl2_add_u32 s72,s71,s7\ns_lshl_b32 s72,s72,5\nv_lshl_add_u32 v1,v0,2,s72\nv_mul_u32_u24 v3,6144,v1\nv_lshl_add_u32 v3,v2,4,v3\ns_lshl_b32 s72,s70,7\ns_add_u32 s72,s72,3072\n" \
"v_add_u32 v3,s72,v3\nv_add_u32 v4,12288,v3\nv_lshlrev_b32 v1,1,v1\nglobal_load_dwordx2 v[168:169],v1,s[28:29]\nglobal_load_dwordx4 v[152:155],v3,s[30:31] offset:-3072\nglobal_load_dwordx4 v[156:159],v3,s[30:31] offset:3072\nglobal_load_dwordx4 v[160:163],v4,s[30:31] offset:-3072\nglobal_load_dwordx4 v[164:167],v4,s[30:31] offset:3072\n" \
"s_mul_i32 s71,s71,12288\ns_lshl_b32 s70,s70,8\ns_add_u32 s79,s70,s71\ns_add_u32 s79,s79,786432\ns_cmp_eq_u32 s7,0\ns_cselect_b32 s75,0,0x8000000\ns_cmp_lt_u32 s6,192\ns_cselect_b32 s70,s75,0x8000000\n" \
"s_or_b32 s79,s79,s70\ns_mov_b64 s[52:53],s[34:35]\ns_mov_b32 s54,71303168\ns_mov_b32 s55,0x20000\ns_mov_b32 s70,94208\nbuffer_load_dwordx2 v[230:231],off,s[52:55],s70 sc1\ns_getpc_b64 s[56:57]\n.Lpc0:\n" \
"s_and_b32 s57,s57,0xffff\ns_mov_b32 s58,.Lcode_e-.Lpc0\ns_mov_b32 s59,0x20000\ns_lshr_b32 s70,s6,3\nv_and_b32 v251,1,v249\ns_lshl2_add_u32 s78,s70,s7\nv_lshlrev_b32 v251,14,v251\ns_lshl_b32 s78,s78,7\n" \
"buffer_load_dword v247,v251,s[56:59],s78 offen\nv_and_b32 v34,15,v249\nv_lshrrev_b32 v35,1,v34\nv_xor_b32 v35,v35,v6\nv_lshlrev_b32 v35,4,v35\nv_lshl_or_b32 v35,v34,7,v35\nv_add_u32 v228,s65,v35\nv_xor_b32 v229,64,v228\n" \
"v_bfe_u32 v34,v249,2,2\nv_and_b32 v35,3,v249\nv_lshlrev_b32 v35,3,v35\nv_lshl_or_b32 v35,v34,6,v35\nv_lshl_or_b32 v35,v6,11,v35\nv_and_b32 v34,16,v249\nv_lshl_or_b32 v35,v34,1,v35\nv_add_u32 v241,98304,v35\n" \
"v_xor_b32 v242,32,v241\ns_mov_b32 s73,0x400000\nv_mov_b32 v252,0xff800000\ns_mov_b32 s82,0x05040100\ns_mov_b32 s83,0x07060302\nv_lshlrev_b32 v190,5,v2\nv_lshl_or_b32 v190,v6,3,v190\ns_lshl_b32 s72,s7,8\n" \
"s_add_u32 s72,s72,114688\nv_add_u32 v190,s72,v190\ns_lshl_b32 s72,s7,3\ns_add_u32 s72,s72,115712\nv_lshl_add_u32 v191,v2,5,s72\nv_lshlrev_b32 v251,5,v2\nv_add_u32 v251,115712,v251\nv_lshlrev_b32 v250,2,v249\n" \
"v_add_u32 v250,114688,v250\ns_lshl_b32 s70,s62,7\ns_xor_b32 s71,s63,12\ns_lshl3_add_u32 s71,s71,s70\ns_lshl_b32 s72,s63,3\ns_sub_u32 s72,s72,s70\ns_add_u32 s72,s72,1792\ns_cmp_lt_u32 s63,16\n" \
"s_cselect_b32 s71,s71,s72\ns_lshl_b32 s71,s71,10\ns_lshl_b32 s72,s7,7\ns_add_u32 s71,s71,s72\nv_lshlrev_b32 v5,10,v2\nv_lshl_add_u32 v5,v0,4,v5\nv_add_u32 v5,s71,v5\ns_lshl_b32 s70,s6,11\n" \
"s_cmp_lt_u32 s6,128\ns_cselect_b32 s70,s70,0x8000000\ns_lshl_b32 s71,s7,9\ns_add_u32 s70,s70,s71\ns_add_u32 s80,s70,98304\nv_mov_b32 v28,-1\nv_mov_b32 v29,-1\nv_lshlrev_b32 v30,3,v249\n" \
"s_lshl_b32 s70,s6,7\ns_cmp_lt_u32 s6,224\ns_cselect_b32 s71,65536,0x40000000\ns_cmp_lt_u32 s6,128\ns_cselect_b32 s72,556928,0x40000000\nv_mov_b32 v31,s71\nv_mov_b32 v32,s72\nv_cmp_lt_u32 vcc,31,v249\n" \
"s_or_b32 s70,s70,s75\nv_mov_b32 v33,0\nv_cndmask_b32 v31,v31,v32,vcc\nv_lshl_add_u32 v31,v249,2,v31\ns_add_u32 m0,s64,98304\ns_add_u32 s69,s68,524288\nbuffer_load_dwordx4 v243,s[40:43],s69 offen offset:0 lds\nbuffer_load_dwordx4 v244,s[40:43],s69 offen offset:1024 lds\n" \
"buffer_load_dwordx4 v245,s[40:43],s69 offen offset:2048 lds\nbuffer_load_dwordx4 v246,s[40:43],s69 offen offset:3072 lds\ns_add_u32 m0,s65,8192\ns_xor_b32 s67,s66,128\nbuffer_load_dwordx4 v224,s[36:39],s67 offen offset:0 lds\nbuffer_load_dwordx4 v225,s[36:39],s67 offen offset:1024 lds\nbuffer_load_dwordx4 v226,s[36:39],s67 offen offset:2048 lds\nbuffer_load_dwordx4 v227,s[36:39],s67 offen offset:3072 lds\n" \
"s_add_u32 m0,s65,12288\ns_xor_b32 s67,s66,32896\nbuffer_load_dwordx4 v224,s[36:39],s67 offen offset:0 lds\nbuffer_load_dwordx4 v225,s[36:39],s67 offen offset:1024 lds\nbuffer_load_dwordx4 v226,s[36:39],s67 offen offset:2048 lds\nbuffer_load_dwordx4 v227,s[36:39],s67 offen offset:3072 lds\nglobal_load_dwordx4 v[128:131],v5,s[8:9]\nglobal_load_dwordx4 v[132:135],v5,s[8:9] offset:512\n" \
"buffer_store_dwordx2 v[28:29],v30,s[52:55],s80 offen sc1\nbuffer_store_dword v33,v31,s[52:55],s70 offen sc1\ns_waitcnt vmcnt(23)\ns_barrier\ns_add_u32 m0,s64,131072\ns_add_u32 s69,s68,1048576\nds_read_b128 a[0:3],v228 offset:0\nds_read_b128 a[4:7],v228 offset:2048\n" \
"ds_read_b128 a[8:11],v228 offset:4096\nds_read_b128 a[12:15],v228 offset:6144\nds_read_b128 a[16:19],v229 offset:0\nds_read_b128 a[20:23],v229 offset:2048\nds_read_b128 a[24:27],v229 offset:4096\nds_read_b128 a[28:31],v229 offset:6144\nds_read_b64_tr_b16 v[192:193],v241 offset:49152\nds_read_b64_tr_b16 v[194:195],v241 offset:49408\n" \
"ds_read_b64_tr_b16 v[196:197],v241 offset:57344\nds_read_b64_tr_b16 v[198:199],v241 offset:57600\nds_read_b64_tr_b16 v[200:201],v242 offset:49152\nds_read_b64_tr_b16 v[202:203],v242 offset:49408\nds_read_b64_tr_b16 v[204:205],v242 offset:57344\nds_read_b64_tr_b16 v[206:207],v242 offset:57600\nds_read_b64_tr_b16 v[208:209],v241 offset:49664\nds_read_b64_tr_b16 v[210:211],v241 offset:49920\n" \
"ds_read_b64_tr_b16 v[212:213],v241 offset:57856\nds_read_b64_tr_b16 v[214:215],v241 offset:58112\nds_read_b64_tr_b16 v[216:217],v242 offset:49664\nds_read_b64_tr_b16 v[218:219],v242 offset:49920\nds_read_b64_tr_b16 v[220:221],v242 offset:57856\nds_read_b64_tr_b16 v[222:223],v242 offset:58112\ns_waitcnt vmcnt(22)\nv_lshlrev_b32 v170,16,v168\n" \
"v_and_b32 v171,0xffff0000,v168\nv_lshlrev_b32 v172,16,v169\nv_and_b32 v173,0xffff0000,v169\nv_mul_f32 v184,0xbfb8aa3b,v170\nv_mul_f32 v185,0xbfb8aa3b,v171\nv_mul_f32 v186,0xbfb8aa3b,v172\nv_mul_f32 v187,0xbfb8aa3b,v173\nv_exp_f32 v184,v184\n" \
"v_exp_f32 v185,v185\nv_exp_f32 v186,v186\nv_exp_f32 v187,v187\nv_add_f32 v184,1.0,v184\nv_add_f32 v185,1.0,v185\nv_add_f32 v186,1.0,v186\nv_add_f32 v187,1.0,v187\nv_rcp_f32 v184,v184\n" \
"v_rcp_f32 v185,v185\nv_rcp_f32 v186,v186\nv_rcp_f32 v187,v187\nv_mul_f32 v170,v170,v184\nv_mul_f32 v171,v171,v185\nv_mul_f32 v172,v172,v186\nv_mul_f32 v173,v173,v187\nv_cvt_pk_bf16_f32 v170,v170,v171\n" \
"v_cvt_pk_bf16_f32 v171,v172,v173\ns_waitcnt lgkmcnt(5)\nM v[0:3],v[192:195],a[0:3],0\nbuffer_load_dwordx4 v243,s[40:43],s69 offen offset:0 lds\nM v[0:3],v[196:199],a[16:19],v[0:3]\nv_bfe_u32 v253,v249,1,2\ns_lshl2_add_u32 s76,s62,s7\nv_and_b32 v248,1,v249\n" \
"v_lshlrev_b32 v253,13,v253\ns_mul_i32 s76,s76,32768\ns_lshl_b32 s77,s63,8\nM v[4:7],v[192:195],a[4:7],0\nv_lshl_or_b32 v253,v248,7,v253\ns_add_u32 s76,s76,s77\nv_and_b32 v254,7,v249\nv_add_u32 v253,s76,v253\n" \
"M v[4:7],v[196:199],a[20:23],v[4:7]\nv_and_b32 v234,7,v249\ns_lshl3_add_u32 s72,s6,s75\nM v[64:67],v[192:195],a[8:11],0\nv_lshlrev_b32 v234,11,v234\ns_add_u32 s72,s72,573440\nM v[64:67],v[196:199],a[24:27],v[64:67]\nbuffer_load_dwordx4 v244,s[40:43],s69 offen offset:1024 lds\n" \
"M v[68:71],v[192:195],a[12:15],0\ns_waitcnt vmcnt(22)\nv_perm_b32 v182,v156,v152,s82\nv_perm_b32 v183,v156,v152,s83\nv_perm_b32 v184,v157,v153,s82\nv_perm_b32 v185,v157,v153,s83\nv_perm_b32 v186,v158,v154,s82\nv_perm_b32 v187,v158,v154,s83\n" \
"v_perm_b32 v188,v159,v155,s82\nv_perm_b32 v189,v159,v155,s83\nv_dot2_f32_bf16 v174,v182,v170,0\nv_dot2_f32_bf16 v175,v183,v170,0\nv_dot2_f32_bf16 v176,v184,v170,0\nv_dot2_f32_bf16 v177,v185,v170,0\nv_dot2_f32_bf16 v178,v186,v170,0\nv_dot2_f32_bf16 v179,v187,v170,0\n" \
"v_dot2_f32_bf16 v180,v188,v170,0\nv_dot2_f32_bf16 v181,v189,v170,0\ns_waitcnt vmcnt(20)\nv_perm_b32 v182,v164,v160,s82\nv_perm_b32 v183,v164,v160,s83\nv_perm_b32 v184,v165,v161,s82\nv_perm_b32 v185,v165,v161,s83\nv_perm_b32 v186,v166,v162,s82\n" \
"v_perm_b32 v187,v166,v162,s83\nv_perm_b32 v188,v167,v163,s82\nv_perm_b32 v189,v167,v163,s83\nv_dot2_f32_bf16 v174,v182,v171,v174\nv_dot2_f32_bf16 v175,v183,v171,v175\nv_dot2_f32_bf16 v176,v184,v171,v176\nv_dot2_f32_bf16 v177,v185,v171,v177\nv_dot2_f32_bf16 v178,v186,v171,v178\n" \
"v_dot2_f32_bf16 v179,v187,v171,v179\nv_dot2_f32_bf16 v180,v188,v171,v180\nv_dot2_f32_bf16 v181,v189,v171,v181\nv_permlane32_swap_b32 v174,v178\nv_permlane32_swap_b32 v175,v179\nv_permlane32_swap_b32 v176,v180\nv_permlane32_swap_b32 v177,v181\nv_add_f32 v174,v174,v178\n" \
"v_add_f32 v175,v175,v179\nv_add_f32 v176,v176,v180\nv_add_f32 v177,v177,v181\nds_read_b64_tr_b16 v[192:193],v241 offset:50176\nv_permlane16_swap_b32 v174,v176\nv_permlane16_swap_b32 v175,v177\nv_add_f32 v174,v174,v176\nv_add_f32 v175,v175,v177\n" \
"ds_read_b64_tr_b16 v[194:195],v241 offset:50432\nv_add_f32_dpp v174,v174,v174 row_ror:8 row_mask:0xf bank_mask:0xf\nv_add_f32_dpp v175,v175,v175 row_ror:8 row_mask:0xf bank_mask:0xf\nds_write_b64 v190,v[174:175]\nM v[68:71],v[196:199],a[28:31],v[68:71]\nds_read_b64_tr_b16 v[196:197],v241 offset:58368\nds_read_b64_tr_b16 v[198:199],v241 offset:58624\nM v[8:11],v[200:203],a[0:3],0\n" \
"s_waitcnt vmcnt(18)\nv_readfirstlane_b32 s92,v230\nv_readfirstlane_b32 s93,v231\nM v[8:11],v[204:207],a[16:19],v[8:11]\ns_add_u32 s70,s92,1\ns_addc_u32 s71,s93,0\nM v[12:15],v[200:203],a[4:7],0\nbuffer_load_dwordx4 v245,s[40:43],s69 offen offset:2048 lds\n" \
"M v[12:15],v[204:207],a[20:23],v[12:15]\ns_add_u32 s92,s92,0x7f4a7c15\ns_addc_u32 s93,s93,0x9e3779b9\nM v[72:75],v[200:203],a[8:11],0\nv_mov_b32 v230,s92\nv_mov_b32 v231,s93\nM v[72:75],v[204:207],a[24:27],v[72:75]\ns_lshl2_add_u32 s80,s60,s7\n" \
"s_lshl_b32 s80,s80,9\nM v[76:79],v[200:203],a[12:15],0\nds_read_b64_tr_b16 v[200:201],v242 offset:50176\nds_read_b64_tr_b16 v[202:203],v242 offset:50432\nM v[76:79],v[204:207],a[28:31],v[76:79]\nbuffer_load_dwordx4 v246,s[40:43],s69 offen offset:3072 lds\nds_read_b64_tr_b16 v[204:205],v242 offset:58368\nds_read_b64_tr_b16 v[206:207],v242 offset:58624\n" \
"M v[16:19],v[208:211],a[0:3],0\ns_add_u32 s80,s80,573440\ns_waitcnt lgkmcnt(6)\nM v[16:19],v[212:215],a[16:19],v[16:19]\ns_add_u32 m0,s65,16384\ns_xor_b32 s67,s66,256\nM v[20:23],v[208:211],a[4:7],0\nv_mov_b32 v232,s70\n" \
"v_mov_b32 v233,s71\nM v[20:23],v[212:215],a[20:23],v[20:23]\ns_lshr_b32 s70,s64,2\ns_add_u32 s71,s70,786432\nM v[80:83],v[208:211],a[8:11],0\nbuffer_load_dwordx4 v224,s[36:39],s67 offen offset:0 lds\nM v[80:83],v[212:215],a[24:27],v[80:83]\nv_lshlrev_b32 v254,14,v254\n" \
"s_add_u32 s78,s78,32768\nM v[84:87],v[208:211],a[12:15],0\nds_read_b64_tr_b16 v[208:209],v241 offset:50688\nds_read_b64_tr_b16 v[210:211],v241 offset:50944\nM v[84:87],v[212:215],a[28:31],v[84:87]\nds_read_b64_tr_b16 v[212:213],v241 offset:58880\nds_read_b64_tr_b16 v[214:215],v241 offset:59136\nM v[24:27],v[216:219],a[0:3],0\n" \
"s_mov_b32 s98,0x4000\ns_mov_b32 s0,28672\nM v[24:27],v[220:223],a[16:19],v[24:27]\nbuffer_load_dwordx4 v225,s[36:39],s67 offen offset:1024 lds\nM v[28:31],v[216:219],a[4:7],0\ns_add_u32 s10,s61,1\ns_add_u32 s11,s61,2\nM v[28:31],v[220:223],a[20:23],v[28:31]\n" \
"s_add_u32 s14,s61,3\ns_and_b32 s10,s10,3\nM v[88:91],v[216:219],a[8:11],0\ns_and_b32 s11,s11,3\ns_and_b32 s14,s14,3\nM v[88:91],v[220:223],a[24:27],v[88:91]\ns_mov_b32 s47,0x20000\ns_mov_b32 s51,0x20000\n" \
"M v[92:95],v[216:219],a[12:15],0\nbuffer_load_dwordx4 v226,s[36:39],s67 offen offset:2048 lds\nds_read_b64_tr_b16 v[216:217],v242 offset:50688\nds_read_b64_tr_b16 v[218:219],v242 offset:50944\nM v[92:95],v[220:223],a[28:31],v[92:95]\nds_read_b64_tr_b16 v[220:221],v242 offset:58880\nds_read_b64_tr_b16 v[222:223],v242 offset:59136\nM v[32:35],v[192:195],a[0:3],0\n" \
"s_lshl_b32 s10,s10,13\ns_waitcnt lgkmcnt(6)\nM v[32:35],v[196:199],a[16:19],v[32:35]\ns_lshl_b32 s11,s11,13\ns_lshl_b32 s14,s14,13\nM v[36:39],v[192:195],a[4:7],0\ns_mov_b32 s82,0xf0000\ns_mov_b32 s83,0xfff00ff\n" \
"M v[36:39],v[196:199],a[20:23],v[36:39]\nbuffer_load_dwordx4 v227,s[36:39],s67 offen offset:3072 lds\nM v[96:99],v[192:195],a[8:11],0\ns_mov_b32 s84,0x1f0001\ns_mov_b32 s85,0x1fff01ff\nM v[96:99],v[196:199],a[24:27],v[96:99]\ns_add_u32 m0,s65,20480\ns_xor_b32 s67,s66,33024\n" \
"M v[100:103],v[192:195],a[12:15],0\ns_mov_b32 s86,0x3f0003\ns_mov_b32 s87,0x3fff03ff\nM v[100:103],v[196:199],a[28:31],v[100:103]\ns_mov_b32 s88,0x7f0007\ns_mov_b32 s89,0x7fff07ff\nM v[40:43],v[200:203],a[0:3],0\nbuffer_load_dwordx4 v224,s[36:39],s67 offen offset:0 lds\n" \
"s_waitcnt vmcnt(11)\nv_lshlrev_b32 v136,16,v128\nv_and_b32 v137,0xffff0000,v128\nv_lshlrev_b32 v138,16,v129\nv_and_b32 v139,0xffff0000,v129\nv_lshlrev_b32 v140,16,v130\nv_and_b32 v141,0xffff0000,v130\nv_lshlrev_b32 v142,16,v131\n" \
"v_and_b32 v143,0xffff0000,v131\nv_lshlrev_b32 v144,16,v132\nv_and_b32 v145,0xffff0000,v132\nv_lshlrev_b32 v146,16,v133\nv_and_b32 v147,0xffff0000,v133\nv_lshlrev_b32 v148,16,v134\nv_and_b32 v149,0xffff0000,v134\nv_lshlrev_b32 v150,16,v135\n" \
"v_and_b32 v151,0xffff0000,v135\nv_add_f32 v168,v136,v140\nv_add_f32 v128,v144,v148\nv_add_f32 v169,v137,v141\nv_add_f32 v129,v145,v149\nv_add_f32 v170,v138,v142\nv_add_f32 v130,v146,v150\nv_add_f32 v171,v139,v143\n" \
"v_add_f32 v131,v147,v151\nv_add_f32 v168,v168,v128\nv_add_f32 v169,v169,v129\nv_add_f32 v170,v170,v130\nv_add_f32 v171,v171,v131\nv_add_f32 v168,v168,v169\nv_add_f32 v170,v170,v171\nv_add_f32 v168,v168,v170\n" \
"M v[40:43],v[204:207],a[16:19],v[40:43]\ns_mov_b64 s[90:91],-1\nv_add_f32_dpp v168,v168,v168 row_ror:8 row_mask:0xf bank_mask:0xf\nv_mov_b32 v170,v168\nM v[44:47],v[200:203],a[4:7],0\ns_mov_b32 s28,0x5f800000\nv_permlane16_swap_b32 v168,v170\nv_add_f32 v168,v168,v170\n" \
"v_mov_b32 v170,v168\ns_nop 1\nv_permlane32_swap_b32 v168,v170\nv_add_f32 v168,v168,v170\nv_mul_f32 v168,0x3c000000,v168\nv_sub_f32 v152,v136,v168\nv_sub_f32 v153,v137,v168\nv_sub_f32 v154,v138,v168\n" \
"v_sub_f32 v155,v139,v168\nv_sub_f32 v156,v140,v168\nv_sub_f32 v157,v141,v168\nv_sub_f32 v158,v142,v168\nv_sub_f32 v159,v143,v168\nv_mul_f32 v128,v152,v152\nv_mul_f32 v129,v153,v153\nv_mul_f32 v130,v154,v154\n" \
"v_mul_f32 v131,v155,v155\nv_sub_f32 v160,v144,v168\nv_sub_f32 v161,v145,v168\nv_sub_f32 v162,v146,v168\nv_sub_f32 v163,v147,v168\nv_fmac_f32 v128,v156,v156\nv_fmac_f32 v129,v157,v157\nv_fmac_f32 v130,v158,v158\n" \
"v_fmac_f32 v131,v159,v159\nv_sub_f32 v164,v148,v168\nv_sub_f32 v165,v149,v168\nv_sub_f32 v166,v150,v168\nv_sub_f32 v167,v151,v168\nv_fmac_f32 v128,v160,v160\nv_fmac_f32 v129,v161,v161\nv_fmac_f32 v130,v162,v162\n" \
"v_fmac_f32 v131,v163,v163\nv_fmac_f32 v128,v164,v164\nv_fmac_f32 v129,v165,v165\nv_fmac_f32 v130,v166,v166\nv_fmac_f32 v131,v167,v167\nv_add_f32 v128,v128,v129\nv_add_f32 v130,v130,v131\nv_add_f32 v169,v128,v130\n" \
"s_nop 1\nv_add_f32_dpp v169,v169,v169 row_ror:8 row_mask:0xf bank_mask:0xf\nv_mov_b32 v170,v169\ns_nop 1\nv_permlane16_swap_b32 v169,v170\nv_add_f32 v169,v169,v170\nv_mov_b32 v170,v169\ns_nop 1\n" \
"v_permlane32_swap_b32 v169,v170\nv_add_f32 v169,v169,v170\nds_write_b64 v191,v[168:169]\nM v[44:47],v[204:207],a[20:23],v[44:47]\nv_and_b32 v169,3,v249\nv_mul_u32_u24 v169,12288,v169\nM v[104:107],v[200:203],a[8:11],0\nv_and_b32 v170,28,v249\n" \
"v_add_u32 v169,v169,v170\nM v[104:107],v[204:207],a[24:27],v[104:107]\nbuffer_load_dwordx4 v225,s[36:39],s67 offen offset:1024 lds\nM v[108:111],v[200:203],a[12:15],0\nv_lshlrev_b32 v170,3,v170\nM v[108:111],v[204:207],a[28:31],v[108:111]\nM v[48:51],v[208:211],a[0:3],0\ns_waitcnt lgkmcnt(1)\n" \
"M v[48:51],v[212:215],a[16:19],v[48:51]\nM v[52:55],v[208:211],a[4:7],0\nbuffer_load_dwordx4 v226,s[36:39],s67 offen offset:2048 lds\nM v[52:55],v[212:215],a[20:23],v[52:55]\nM v[112:115],v[208:211],a[8:11],0\ns_barrier\nds_read_b128 a[32:35],v228 offset:8192\nds_read_b128 a[36:39],v228 offset:10240\n" \
"ds_read_b128 a[40:43],v228 offset:12288\nds_read_b128 a[44:47],v228 offset:14336\nds_read_b128 a[48:51],v229 offset:8192\nds_read_b128 a[52:55],v229 offset:10240\nds_read_b128 a[56:59],v229 offset:12288\nds_read_b128 a[60:63],v229 offset:14336\nds_read_b32 v184,v250 offset:0\nds_read_b32 v185,v250 offset:256\n" \
"ds_read_b32 v186,v250 offset:512\nds_read_b32 v187,v250 offset:768\ns_waitcnt lgkmcnt(2)\nv_add_f32 v184,v184,v185\ns_waitcnt lgkmcnt(0)\nv_add_f32 v186,v186,v187\nv_lshlrev_b32 v185,2,v249\nv_add_f32 v184,v184,v186\n" \
"buffer_store_dword v184,v185,s[52:55],s79 offen sc1\nds_read_b64_tr_b16 v[192:193],v241 offset:0\nds_read_b64_tr_b16 v[194:195],v241 offset:256\nds_read_b64_tr_b16 v[196:197],v241 offset:8192\nds_read_b64_tr_b16 v[198:199],v241 offset:8448\nds_read_b64_tr_b16 v[200:201],v242 offset:0\nds_read_b64_tr_b16 v[202:203],v242 offset:256\nds_read_b64_tr_b16 v[204:205],v242 offset:8192\n" \
"ds_read_b64_tr_b16 v[206:207],v242 offset:8448\nM v[112:115],v[212:215],a[24:27],v[112:115]\nv_bfe_u32 v250,v249,3,2\nM v[116:119],v[208:211],a[12:15],0\nds_read_b64_tr_b16 v[208:209],v241 offset:512\nds_read_b64_tr_b16 v[210:211],v241 offset:768\nM v[116:119],v[212:215],a[28:31],v[116:119]\nbuffer_load_dwordx4 v227,s[36:39],s67 offen offset:3072 lds\n" \
"ds_read_b64_tr_b16 v[212:213],v241 offset:8704\nds_read_b64_tr_b16 v[214:215],v241 offset:8960\nM v[56:59],v[216:219],a[0:3],0\nM v[56:59],v[220:223],a[16:19],v[56:59]\ns_add_u32 m0,s65,0\ns_xor_b32 s67,s66,384\nM v[60:63],v[216:219],a[4:7],0\nM v[60:63],v[220:223],a[20:23],v[60:63]\n" \
"M v[120:123],v[216:219],a[8:11],0\nbuffer_load_dwordx4 v224,s[36:39],s67 offen offset:0 lds\nM v[120:123],v[220:223],a[24:27],v[120:123]\nM v[124:127],v[216:219],a[12:15],0\nds_read_b64_tr_b16 v[216:217],v242 offset:512\nds_read_b64_tr_b16 v[218:219],v242 offset:768\nM v[124:127],v[220:223],a[28:31],v[124:127]\nds_read_b64_tr_b16 v[220:221],v242 offset:8704\n" \
"ds_read_b64_tr_b16 v[222:223],v242 offset:8960\ns_waitcnt lgkmcnt(11)\nM v[0:3],v[192:195],a[32:35],v[0:3]\nM v[0:3],v[196:199],a[48:51],v[0:3]\nbuffer_load_dwordx4 v225,s[36:39],s67 offen offset:1024 lds\nM v[4:7],v[192:195],a[36:39],v[4:7]\nM v[4:7],v[196:199],a[52:55],v[4:7]\nM v[64:67],v[192:195],a[40:43],v[64:67]\n" \
"M v[64:67],v[196:199],a[56:59],v[64:67]\nM v[68:71],v[192:195],a[44:47],v[68:71]\nbuffer_load_dwordx4 v226,s[36:39],s67 offen offset:2048 lds\nds_read_b64_tr_b16 v[192:193],v241 offset:1024\nds_read_b64_tr_b16 v[194:195],v241 offset:1280\nM v[68:71],v[196:199],a[60:63],v[68:71]\nds_read_b64_tr_b16 v[196:197],v241 offset:9216\nds_read_b64_tr_b16 v[198:199],v241 offset:9472\n" \
"s_waitcnt lgkmcnt(8)\nM v[8:11],v[200:203],a[32:35],v[8:11]\nM v[8:11],v[204:207],a[48:51],v[8:11]\nM v[12:15],v[200:203],a[36:39],v[12:15]\nM v[12:15],v[204:207],a[52:55],v[12:15]\nbuffer_load_dwordx4 v227,s[36:39],s67 offen offset:3072 lds\nM v[72:75],v[200:203],a[40:43],v[72:75]\nM v[72:75],v[204:207],a[56:59],v[72:75]\n" \
"s_add_u32 m0,s65,4096\ns_xor_b32 s67,s66,33152\nM v[76:79],v[200:203],a[44:47],v[76:79]\nds_read_b64_tr_b16 v[200:201],v242 offset:1024\nds_read_b64_tr_b16 v[202:203],v242 offset:1280\nM v[76:79],v[204:207],a[60:63],v[76:79]\nds_read_b64_tr_b16 v[204:205],v242 offset:9216\nds_read_b64_tr_b16 v[206:207],v242 offset:9472\n" \
"M v[16:19],v[208:211],a[32:35],v[16:19]\nbuffer_load_dwordx4 v224,s[36:39],s67 offen offset:0 lds\nM v[16:19],v[212:215],a[48:51],v[16:19]\nM v[20:23],v[208:211],a[36:39],v[20:23]\nM v[20:23],v[212:215],a[52:55],v[20:23]\nM v[80:83],v[208:211],a[40:43],v[80:83]\nM v[80:83],v[212:215],a[56:59],v[80:83]\nbuffer_load_dwordx4 v225,s[36:39],s67 offen offset:1024 lds\n" \
"M v[84:87],v[208:211],a[44:47],v[84:87]\nds_read_b64_tr_b16 v[208:209],v241 offset:1536\nds_read_b64_tr_b16 v[210:211],v241 offset:1792\nM v[84:87],v[212:215],a[60:63],v[84:87]\nds_read_b64_tr_b16 v[212:213],v241 offset:9728\nds_read_b64_tr_b16 v[214:215],v241 offset:9984\ns_waitcnt lgkmcnt(8)\nM v[24:27],v[216:219],a[32:35],v[24:27]\n" \
"M v[24:27],v[220:223],a[48:51],v[24:27]\nM v[28:31],v[216:219],a[36:39],v[28:31]\nbuffer_load_dwordx4 v226,s[36:39],s67 offen offset:2048 lds\nM v[28:31],v[220:223],a[52:55],v[28:31]\nM v[88:91],v[216:219],a[40:43],v[88:91]\nM v[88:91],v[220:223],a[56:59],v[88:91]\nM v[92:95],v[216:219],a[44:47],v[92:95]\nds_read_b64_tr_b16 v[216:217],v242 offset:1536\n" \
"ds_read_b64_tr_b16 v[218:219],v242 offset:1792\nM v[92:95],v[220:223],a[60:63],v[92:95]\nbuffer_load_dwordx4 v227,s[36:39],s67 offen offset:3072 lds\nds_read_b64_tr_b16 v[220:221],v242 offset:9728\nds_read_b64_tr_b16 v[222:223],v242 offset:9984\nM v[32:35],v[192:195],a[32:35],v[32:35]\nM v[32:35],v[196:199],a[48:51],v[32:35]\ns_add_u32 m0,s64,147456\n" \
"s_add_u32 s69,s68,1572864\nM v[36:39],v[192:195],a[36:39],v[36:39]\nM v[36:39],v[196:199],a[52:55],v[36:39]\nM v[96:99],v[192:195],a[40:43],v[96:99]\nbuffer_load_dwordx4 v243,s[40:43],s69 offen offset:0 lds\nM v[96:99],v[196:199],a[56:59],v[96:99]\nM v[100:103],v[192:195],a[44:47],v[100:103]\nM v[100:103],v[196:199],a[60:63],v[100:103]\n" \
"s_waitcnt lgkmcnt(4)\nM v[40:43],v[200:203],a[32:35],v[40:43]\nM v[40:43],v[204:207],a[48:51],v[40:43]\nbuffer_load_dwordx4 v244,s[40:43],s69 offen offset:1024 lds\nM v[44:47],v[200:203],a[36:39],v[44:47]\nM v[44:47],v[204:207],a[52:55],v[44:47]\nM v[104:107],v[200:203],a[40:43],v[104:107]\nM v[104:107],v[204:207],a[56:59],v[104:107]\n" \
"M v[108:111],v[200:203],a[44:47],v[108:111]\nbuffer_load_dwordx4 v245,s[40:43],s69 offen offset:2048 lds\nM v[108:111],v[204:207],a[60:63],v[108:111]\nM v[48:51],v[208:211],a[32:35],v[48:51]\nM v[48:51],v[212:215],a[48:51],v[48:51]\nM v[52:55],v[208:211],a[36:39],v[52:55]\nM v[52:55],v[212:215],a[52:55],v[52:55]\nbuffer_load_dwordx4 v246,s[40:43],s69 offen offset:3072 lds\n" \
"M v[112:115],v[208:211],a[40:43],v[112:115]\ns_waitcnt vmcnt(12) lgkmcnt(0)\ns_barrier\nds_read_b128 a[64:67],v228 offset:16384\nds_read_b128 a[68:71],v228 offset:18432\nds_read_b128 a[72:75],v228 offset:20480\nds_read_b128 a[76:79],v228 offset:22528\nds_read_b128 a[80:83],v229 offset:16384\n" \
"ds_read_b128 a[84:87],v229 offset:18432\nds_read_b128 a[88:91],v229 offset:20480\nds_read_b128 a[92:95],v229 offset:22528\nM v[112:115],v[212:215],a[56:59],v[112:115]\ns_add_u32 m0,s65,8192\ns_xor_b32 s67,s66,512\nds_read_b64_tr_b16 v[192:193],v241 offset:32768\nds_read_b64_tr_b16 v[194:195],v241 offset:33024\n" \
"M v[116:119],v[208:211],a[44:47],v[116:119]\nbuffer_store_dwordx2 v[230:231],v234,s[52:55],s72 offen sc1\nds_read_b64_tr_b16 v[196:197],v241 offset:40960\nds_read_b64_tr_b16 v[198:199],v241 offset:41216\nM v[116:119],v[212:215],a[60:63],v[116:119]\ns_cmp_eq_u32 s6,0\ns_cselect_b32 s72,s75,0x8000000\nM v[56:59],v[216:219],a[32:35],v[56:59]\n" \
"buffer_load_dwordx4 v224,s[36:39],s67 offen offset:0 lds\nM v[56:59],v[220:223],a[48:51],v[56:59]\ns_add_u32 s72,s72,94208\nM v[60:63],v[216:219],a[36:39],v[60:63]\nM v[60:63],v[220:223],a[52:55],v[60:63]\nds_read_b64_tr_b16 v[200:201],v242 offset:32768\nds_read_b64_tr_b16 v[202:203],v242 offset:33024\nds_read_b64_tr_b16 v[204:205],v242 offset:40960\n" \
"ds_read_b64_tr_b16 v[206:207],v242 offset:41216\nds_read_b64_tr_b16 v[208:209],v241 offset:33280\nds_read_b64_tr_b16 v[210:211],v241 offset:33536\nds_read_b64_tr_b16 v[212:213],v241 offset:41472\nds_read_b64_tr_b16 v[214:215],v241 offset:41728\nM v[120:123],v[216:219],a[40:43],v[120:123]\nM v[120:123],v[220:223],a[56:59],v[120:123]\nbuffer_load_dwordx4 v225,s[36:39],s67 offen offset:1024 lds\n" \
"M v[124:127],v[216:219],a[44:47],v[124:127]\nds_read_b64_tr_b16 v[216:217],v242 offset:33280\nds_read_b64_tr_b16 v[218:219],v242 offset:33536\nM v[124:127],v[220:223],a[60:63],v[124:127]\nds_read_b64_tr_b16 v[220:221],v242 offset:41472\nds_read_b64_tr_b16 v[222:223],v242 offset:41728\ns_waitcnt lgkmcnt(14)\nM v[0:3],v[192:195],a[64:67],v[0:3]\n" \
"s_waitcnt lgkmcnt(12)\nM v[0:3],v[196:199],a[80:83],v[0:3]\nM v[4:7],v[192:195],a[68:71],v[4:7]\nbuffer_load_dwordx4 v226,s[36:39],s67 offen offset:2048 lds\nM v[4:7],v[196:199],a[84:87],v[4:7]\nds_read_b128 v[156:159],v251\nM v[64:67],v[192:195],a[72:75],v[64:67]\nds_read_b128 v[160:163],v251 offset:16\n" \
"v_lshrrev_b32 v251,3,v249\nM v[64:67],v[196:199],a[88:91],v[64:67]\nM v[68:71],v[192:195],a[76:79],v[68:71]\nds_read_b64_tr_b16 v[192:193],v241 offset:33792\nds_read_b64_tr_b16 v[194:195],v241 offset:34048\nM v[68:71],v[196:199],a[92:95],v[68:71]\nbuffer_load_dwordx4 v227,s[36:39],s67 offen offset:3072 lds\nds_read_b64_tr_b16 v[196:197],v241 offset:41984\n" \
"ds_read_b64_tr_b16 v[198:199],v241 offset:42240\ns_waitcnt lgkmcnt(10)\nM v[8:11],v[200:203],a[64:67],v[8:11]\nM v[8:11],v[204:207],a[80:83],v[8:11]\ns_add_u32 m0,s65,12288\ns_xor_b32 s67,s66,33280\nM v[12:15],v[200:203],a[68:71],v[12:15]\nM v[12:15],v[204:207],a[84:87],v[12:15]\n" \
"M v[72:75],v[200:203],a[72:75],v[72:75]\nbuffer_load_dwordx4 v224,s[36:39],s67 offen offset:0 lds\nM v[72:75],v[204:207],a[88:91],v[72:75]\ns_waitcnt lgkmcnt(5)\nv_add_f32 v164,v156,v158\nM v[76:79],v[200:203],a[76:79],v[76:79]\nds_read_b64_tr_b16 v[200:201],v242 offset:33792\nds_read_b64_tr_b16 v[202:203],v242 offset:34048\n" \
"M v[76:79],v[204:207],a[92:95],v[76:79]\nds_read_b64_tr_b16 v[204:205],v242 offset:41984\nds_read_b64_tr_b16 v[206:207],v242 offset:42240\nM v[16:19],v[208:211],a[64:67],v[16:19]\ns_waitcnt lgkmcnt(8)\nv_add_f32 v165,v160,v162\nM v[16:19],v[212:215],a[80:83],v[16:19]\nbuffer_load_dwordx4 v225,s[36:39],s67 offen offset:1024 lds\n" \
"M v[20:23],v[208:211],a[68:71],v[20:23]\nv_add_f32 v164,v164,v165\nv_lshlrev_b32 v165,3,v249\nM v[20:23],v[212:215],a[84:87],v[20:23]\nv_mul_f32 v164,0x3e800000,v164\nv_sub_f32 v156,v156,v164\nM v[80:83],v[208:211],a[72:75],v[80:83]\nv_sub_f32 v158,v158,v164\n" \
"v_mul_f32 v156,v156,v156\nM v[80:83],v[212:215],a[88:91],v[80:83]\nv_sub_f32 v160,v160,v164\nv_fmac_f32 v156,v158,v158\nM v[84:87],v[208:211],a[76:79],v[84:87]\nbuffer_load_dwordx4 v226,s[36:39],s67 offen offset:2048 lds\nds_read_b64_tr_b16 v[208:209],v241 offset:34304\nds_read_b64_tr_b16 v[210:211],v241 offset:34560\n" \
"M v[84:87],v[212:215],a[92:95],v[84:87]\nds_read_b64_tr_b16 v[212:213],v241 offset:42496\nds_read_b64_tr_b16 v[214:215],v241 offset:42752\nM v[24:27],v[216:219],a[64:67],v[24:27]\nv_add_f32 v157,v157,v159\nv_add_f32 v161,v161,v163\nM v[24:27],v[220:223],a[80:83],v[24:27]\nv_sub_f32 v162,v162,v164\n" \
"v_fmac_f32 v156,v160,v160\nM v[28:31],v[216:219],a[68:71],v[28:31]\nglobal_load_dword v247,v253,s[16:17]\nM v[28:31],v[220:223],a[84:87],v[28:31]\nbuffer_load_dwordx4 v227,s[36:39],s67 offen offset:3072 lds\nM v[88:91],v[216:219],a[72:75],v[88:91]\nv_add_u32 v253,0x100000,v253\nv_add_f32 v157,v157,v161\n" \
"M v[88:91],v[220:223],a[88:91],v[88:91]\ns_add_u32 m0,s64,98304\ns_add_u32 s69,s68,2097152\nM v[92:95],v[216:219],a[76:79],v[92:95]\nds_read_b64_tr_b16 v[216:217],v242 offset:34304\nds_read_b64_tr_b16 v[218:219],v242 offset:34560\nM v[92:95],v[220:223],a[92:95],v[92:95]\nds_read_b64_tr_b16 v[220:221],v242 offset:42496\n" \
"ds_read_b64_tr_b16 v[222:223],v242 offset:42752\ns_waitcnt lgkmcnt(8)\nM v[32:35],v[192:195],a[64:67],v[32:35]\nbuffer_load_dwordx4 v243,s[40:43],s69 offen offset:0 lds\nM v[32:35],v[196:199],a[80:83],v[32:35]\nv_fmac_f32 v156,v162,v162\nv_fmac_f32 v157,0x43000000,v156\nM v[36:39],v[192:195],a[68:71],v[36:39]\n" \
"v_mul_f32 v157,0x3b000000,v157\nv_cvt_pk_bf16_f32 v157,0,v157\nM v[36:39],v[196:199],a[84:87],v[36:39]\nv_add_f32 v157,0x3727c5ac,v157\nv_cvt_pk_bf16_f32 v157,0,v157\nM v[96:99],v[192:195],a[72:75],v[96:99]\nv_sqrt_f32 v157,v157\nM v[96:99],v[196:199],a[88:91],v[96:99]\n" \
"buffer_load_dwordx4 v244,s[40:43],s69 offen offset:1024 lds\nM v[100:103],v[192:195],a[76:79],v[100:103]\nv_cvt_pk_bf16_f32 v164,0,v164\nv_cvt_pk_bf16_f32 v157,0,v157\nM v[100:103],v[196:199],a[92:95],v[100:103]\nv_rcp_f32 v157,v157\nM v[40:43],v[200:203],a[64:67],v[40:43]\nv_sub_f32 v136,v136,v164\n" \
"v_sub_f32 v137,v137,v164\nM v[40:43],v[204:207],a[80:83],v[40:43]\nv_sub_f32 v138,v138,v164\nv_sub_f32 v139,v139,v164\nM v[44:47],v[200:203],a[68:71],v[44:47]\nbuffer_load_dwordx4 v245,s[40:43],s69 offen offset:2048 lds\nM v[44:47],v[204:207],a[84:87],v[44:47]\nv_sub_f32 v140,v140,v164\n" \
"v_sub_f32 v141,v141,v164\nM v[104:107],v[200:203],a[72:75],v[104:107]\nv_sub_f32 v142,v142,v164\nv_sub_f32 v143,v143,v164\nM v[104:107],v[204:207],a[88:91],v[104:107]\nv_sub_f32 v144,v144,v164\nv_sub_f32 v145,v145,v164\nM v[108:111],v[200:203],a[76:79],v[108:111]\n" \
"v_sub_f32 v146,v146,v164\nv_sub_f32 v147,v147,v164\nM v[108:111],v[204:207],a[92:95],v[108:111]\nbuffer_load_dwordx4 v246,s[40:43],s69 offen offset:3072 lds\ns_waitcnt lgkmcnt(0)\nM v[48:51],v[208:211],a[64:67],v[48:51]\nv_sub_f32 v148,v148,v164\nv_sub_f32 v149,v149,v164\n" \
"M v[48:51],v[212:215],a[80:83],v[48:51]\ns_add_u32 m0,s65,16384\ns_xor_b32 s67,s66,640\nM v[52:55],v[208:211],a[68:71],v[52:55]\nv_sub_f32 v150,v150,v164\nv_sub_f32 v151,v151,v164\nM v[52:55],v[212:215],a[84:87],v[52:55]\nv_cvt_pk_bf16_f32 v136,0,v136\n" \
"v_cvt_pk_bf16_f32 v137,0,v137\nM v[112:115],v[208:211],a[72:75],v[112:115]\nbuffer_load_dwordx4 v224,s[36:39],s67 offen offset:0 lds\ns_waitcnt vmcnt(15)\ns_barrier\nds_read_b128 a[96:99],v228 offset:0\nds_read_b128 a[100:103],v228 offset:2048\nds_read_b128 a[104:107],v228 offset:4096\n" \
"ds_read_b128 a[108:111],v228 offset:6144\nds_read_b128 a[112:115],v229 offset:0\nds_read_b128 a[116:119],v229 offset:2048\nds_read_b128 a[120:123],v229 offset:4096\nds_read_b128 a[124:127],v229 offset:6144\nM v[112:115],v[212:215],a[88:91],v[112:115]\nds_read_b64_tr_b16 v[192:193],v241 offset:49152\nds_read_b64_tr_b16 v[194:195],v241 offset:49408\n" \
"M v[116:119],v[208:211],a[76:79],v[116:119]\nds_read_b64_tr_b16 v[196:197],v241 offset:57344\nds_read_b64_tr_b16 v[198:199],v241 offset:57600\nM v[116:119],v[212:215],a[92:95],v[116:119]\nv_cvt_pk_bf16_f32 v138,0,v138\nv_cvt_pk_bf16_f32 v139,0,v139\nM v[56:59],v[216:219],a[64:67],v[56:59]\nv_cvt_pk_bf16_f32 v140,0,v140\n" \
"v_cvt_pk_bf16_f32 v141,0,v141\nM v[56:59],v[220:223],a[80:83],v[56:59]\nbuffer_load_dwordx4 v225,s[36:39],s67 offen offset:1024 lds\nM v[60:63],v[216:219],a[68:71],v[60:63]\nv_cvt_pk_bf16_f32 v142,0,v142\nv_cvt_pk_bf16_f32 v143,0,v143\nM v[60:63],v[220:223],a[84:87],v[60:63]\nv_cvt_pk_bf16_f32 v144,0,v144\n" \
"v_cvt_pk_bf16_f32 v145,0,v145\nM v[120:123],v[216:219],a[72:75],v[120:123]\nv_cvt_pk_bf16_f32 v146,0,v146\nv_cvt_pk_bf16_f32 v147,0,v147\nM v[120:123],v[220:223],a[88:91],v[120:123]\nds_read_b64_tr_b16 v[200:201],v242 offset:49152\nds_read_b64_tr_b16 v[202:203],v242 offset:49408\nM v[124:127],v[216:219],a[76:79],v[124:127]\n" \
"buffer_load_dwordx4 v226,s[36:39],s67 offen offset:2048 lds\nds_read_b64_tr_b16 v[204:205],v242 offset:57344\nds_read_b64_tr_b16 v[206:207],v242 offset:57600\nM v[124:127],v[220:223],a[92:95],v[124:127]\nds_read_b64_tr_b16 v[208:209],v241 offset:49664\ns_waitcnt lgkmcnt(7)\nM v[0:3],v[192:195],a[96:99],v[0:3]\nds_read_b64_tr_b16 v[210:211],v241 offset:49920\n" \
"s_waitcnt lgkmcnt(6)\nM v[0:3],v[196:199],a[112:115],v[0:3]\nds_read_b64_tr_b16 v[212:213],v241 offset:57856\nds_read_b64_tr_b16 v[214:215],v241 offset:58112\nM v[4:7],v[192:195],a[100:103],v[4:7]\nglobal_load_dword v247,v253,s[16:17]\nM v[4:7],v[196:199],a[116:119],v[4:7]\nbuffer_load_dwordx4 v227,s[36:39],s67 offen offset:3072 lds\n" \
"M v[64:67],v[192:195],a[104:107],v[64:67]\nds_read_b64_tr_b16 v[216:217],v242 offset:49664\nds_read_b64_tr_b16 v[218:219],v242 offset:49920\nM v[64:67],v[196:199],a[120:123],v[64:67]\ns_add_u32 m0,s65,20480\ns_xor_b32 s67,s66,33408\nM v[68:71],v[192:195],a[108:111],v[68:71]\nds_read_b64_tr_b16 v[220:221],v242 offset:57856\n" \
"ds_read_b64_tr_b16 v[222:223],v242 offset:58112\nM v[68:71],v[196:199],a[124:127],v[68:71]\nds_read_b64_tr_b16 v[192:193],v241 offset:50176\ns_waitcnt lgkmcnt(11)\nM v[8:11],v[200:203],a[96:99],v[8:11]\nbuffer_load_dwordx4 v224,s[36:39],s67 offen offset:0 lds\ns_waitcnt lgkmcnt(9)\nM v[8:11],v[204:207],a[112:115],v[8:11]\n" \
"ds_read_b64_tr_b16 v[194:195],v241 offset:50432\nds_read_b64_tr_b16 v[196:197],v241 offset:58368\nM v[12:15],v[200:203],a[100:103],v[12:15]\nds_read_b64_tr_b16 v[198:199],v241 offset:58624\nv_add_u32 v253,0x100000,v253\nM v[12:15],v[204:207],a[116:119],v[12:15]\nv_cvt_pk_bf16_f32 v148,0,v148\nv_cvt_pk_bf16_f32 v149,0,v149\n" \
"M v[72:75],v[200:203],a[104:107],v[72:75]\nv_cvt_pk_bf16_f32 v150,0,v150\nv_cvt_pk_bf16_f32 v151,0,v151\nM v[72:75],v[204:207],a[120:123],v[72:75]\nbuffer_load_dwordx4 v225,s[36:39],s67 offen offset:1024 lds\nM v[76:79],v[200:203],a[108:111],v[76:79]\nds_read_b64_tr_b16 v[200:201],v242 offset:50176\nds_read_b64_tr_b16 v[202:203],v242 offset:50432\n" \
"M v[76:79],v[204:207],a[124:127],v[76:79]\nds_read_b64_tr_b16 v[204:205],v242 offset:58368\ns_waitcnt lgkmcnt(9)\nM v[16:19],v[208:211],a[96:99],v[16:19]\nds_read_b64_tr_b16 v[206:207],v242 offset:58624\nv_mul_f32 v136,v136,v157\nM v[16:19],v[212:215],a[112:115],v[16:19]\nv_mul_f32 v137,v137,v157\n" \
"v_mul_f32 v138,v138,v157\nM v[20:23],v[208:211],a[100:103],v[20:23]\nbuffer_load_dwordx4 v226,s[36:39],s67 offen offset:2048 lds\nM v[20:23],v[212:215],a[116:119],v[20:23]\nv_mul_f32 v139,v139,v157\nv_mul_f32 v140,v140,v157\nM v[80:83],v[208:211],a[104:107],v[80:83]\nv_mul_f32 v141,v141,v157\n" \
"v_mul_f32 v142,v142,v157\nM v[80:83],v[212:215],a[120:123],v[80:83]\nv_mul_f32 v143,v143,v157\nv_mul_f32 v144,v144,v157\nM v[84:87],v[208:211],a[108:111],v[84:87]\nds_read_b64_tr_b16 v[208:209],v241 offset:50688\nds_read_b64_tr_b16 v[210:211],v241 offset:50944\nM v[84:87],v[212:215],a[124:127],v[84:87]\n" \
"buffer_load_dwordx4 v227,s[36:39],s67 offen offset:3072 lds\nM v[24:27],v[216:219],a[96:99],v[24:27]\nds_read_b64_tr_b16 v[212:213],v241 offset:58880\ns_waitcnt lgkmcnt(6)\nM v[24:27],v[220:223],a[112:115],v[24:27]\ns_add_u32 m0,s64,114688\ns_add_u32 s69,s68,2621440\nM v[28:31],v[216:219],a[100:103],v[28:31]\n" \
"ds_read_b64_tr_b16 v[214:215],v241 offset:59136\nv_mul_f32 v145,v145,v157\nM v[28:31],v[220:223],a[116:119],v[28:31]\nv_mul_f32 v146,v146,v157\nv_mul_f32 v147,v147,v157\nM v[88:91],v[216:219],a[104:107],v[88:91]\nbuffer_load_dwordx4 v243,s[40:43],s69 offen offset:0 lds\nM v[88:91],v[220:223],a[120:123],v[88:91]\n" \
"v_mul_f32 v148,v148,v157\nv_mul_f32 v149,v149,v157\nM v[92:95],v[216:219],a[108:111],v[92:95]\nds_read_b64_tr_b16 v[216:217],v242 offset:50688\nds_read_b64_tr_b16 v[218:219],v242 offset:50944\nM v[92:95],v[220:223],a[124:127],v[92:95]\nds_read_b64_tr_b16 v[220:221],v242 offset:58880\nds_read_b64_tr_b16 v[222:223],v242 offset:59136\n" \
"M v[32:35],v[192:195],a[96:99],v[32:35]\nbuffer_load_dwordx2 v[166:167],v165,s[52:55],s80 offen sc1\nM v[32:35],v[196:199],a[112:115],v[32:35]\nbuffer_load_dwordx4 v244,s[40:43],s69 offen offset:1024 lds\nM v[36:39],v[192:195],a[100:103],v[36:39]\nv_mul_f32 v150,v150,v157\nv_mul_f32 v151,v151,v157\nM v[36:39],v[196:199],a[116:119],v[36:39]\n" \
"v_cvt_pk_bf16_f32 v144,0,v144\nv_cvt_pk_bf16_f32 v145,0,v145\nM v[96:99],v[192:195],a[104:107],v[96:99]\nv_cvt_pk_bf16_f32 v136,0,v136\nv_cvt_pk_bf16_f32 v137,0,v137\nM v[96:99],v[196:199],a[120:123],v[96:99]\nv_cvt_pk_bf16_f32 v138,0,v138\nv_cvt_pk_bf16_f32 v139,0,v139\n" \
"M v[100:103],v[192:195],a[108:111],v[100:103]\nbuffer_load_dwordx4 v245,s[40:43],s69 offen offset:2048 lds\nM v[100:103],v[196:199],a[124:127],v[100:103]\nv_cvt_pk_bf16_f32 v140,0,v140\ns_waitcnt lgkmcnt(4)\nM v[40:43],v[200:203],a[96:99],v[40:43]\nv_cvt_pk_bf16_f32 v141,0,v141\nv_cvt_pk_bf16_f32 v142,0,v142\n" \
"M v[40:43],v[204:207],a[112:115],v[40:43]\nv_cvt_pk_bf16_f32 v143,0,v143\nv_cvt_pk_bf16_f32 v146,0,v146\nM v[44:47],v[200:203],a[100:103],v[44:47]\nglobal_load_dword v247,v253,s[16:17]\nM v[44:47],v[204:207],a[116:119],v[44:47]\nbuffer_load_dwordx4 v246,s[40:43],s69 offen offset:3072 lds\nM v[104:107],v[200:203],a[104:107],v[104:107]\n" \
"v_add_u32 v253,0x100000,v253\nv_cvt_pk_bf16_f32 v147,0,v147\nM v[104:107],v[204:207],a[120:123],v[104:107]\ns_add_u32 m0,s65,0\ns_xor_b32 s67,s66,768\nM v[108:111],v[200:203],a[108:111],v[108:111]\nv_cvt_pk_bf16_f32 v148,0,v148\nv_cvt_pk_bf16_f32 v149,0,v149\n" \
"M v[108:111],v[204:207],a[124:127],v[108:111]\nv_cvt_pk_bf16_f32 v150,0,v150\nv_cvt_pk_bf16_f32 v151,0,v151\nM v[48:51],v[208:211],a[96:99],v[48:51]\nbuffer_load_dwordx4 v224,s[36:39],s67 offen offset:0 lds\nM v[48:51],v[212:215],a[112:115],v[48:51]\nM v[52:55],v[208:211],a[100:103],v[52:55]\nM v[52:55],v[212:215],a[116:119],v[52:55]\n" \
"M v[112:115],v[208:211],a[104:107],v[112:115]\ns_waitcnt vmcnt(16) lgkmcnt(0)\ns_barrier\nM v[112:115],v[212:215],a[120:123],v[112:115]\nbuffer_load_dwordx4 v225,s[36:39],s67 offen offset:1024 lds\nds_read_b128 a[128:131],v228 offset:8192\nds_read_b64_tr_b16 v[192:193],v241 offset:0\nds_read_b64_tr_b16 v[194:195],v241 offset:256\n" \
"M v[116:119],v[208:211],a[108:111],v[116:119]\nds_read_b128 a[144:147],v229 offset:8192\nds_read_b64_tr_b16 v[196:197],v241 offset:8192\nds_read_b64_tr_b16 v[198:199],v241 offset:8448\nM v[116:119],v[212:215],a[124:127],v[116:119]\nds_read_b128 a[132:135],v228 offset:10240\nM v[56:59],v[216:219],a[96:99],v[56:59]\nds_read_b128 a[148:151],v229 offset:10240\n" \
"M v[56:59],v[220:223],a[112:115],v[56:59]\nds_read_b128 a[136:139],v228 offset:12288\nM v[60:63],v[216:219],a[100:103],v[60:63]\nbuffer_load_dwordx4 v226,s[36:39],s67 offen offset:2048 lds\nds_read_b128 a[152:155],v229 offset:12288\nM v[60:63],v[220:223],a[116:119],v[60:63]\nds_read_b128 a[140:143],v228 offset:14336\nM v[120:123],v[216:219],a[104:107],v[120:123]\n" \
"ds_read_b128 a[156:159],v229 offset:14336\nM v[120:123],v[220:223],a[120:123],v[120:123]\nds_read_b64_tr_b16 v[200:201],v242 offset:0\nds_read_b64_tr_b16 v[202:203],v242 offset:256\nM v[124:127],v[216:219],a[108:111],v[124:127]\nds_read_b64_tr_b16 v[204:205],v242 offset:8192\nds_read_b64_tr_b16 v[206:207],v242 offset:8448\nM v[124:127],v[220:223],a[124:127],v[124:127]\n" \
"buffer_load_dwordx4 v227,s[36:39],s67 offen offset:3072 lds\ns_waitcnt lgkmcnt(13)\nM v[0:3],v[192:195],a[128:131],v[0:3]\nds_read_b64_tr_b16 v[208:209],v241 offset:512\ns_waitcnt lgkmcnt(11)\nM v[0:3],v[196:199],a[144:147],v[0:3]\ns_add_u32 m0,s65,4096\ns_xor_b32 s67,s66,33536\n" \
"s_waitcnt lgkmcnt(10)\nM v[4:7],v[192:195],a[132:135],v[4:7]\nds_read_b64_tr_b16 v[210:211],v241 offset:768\ns_waitcnt lgkmcnt(10)\nM v[4:7],v[196:199],a[148:151],v[4:7]\nds_read_b64_tr_b16 v[212:213],v241 offset:8704\ns_waitcnt lgkmcnt(10)\nM v[64:67],v[192:195],a[136:139],v[64:67]\n" \
"buffer_load_dwordx4 v224,s[36:39],s67 offen offset:0 lds\ns_waitcnt lgkmcnt(9)\nM v[64:67],v[196:199],a[152:155],v[64:67]\nds_read_b64_tr_b16 v[214:215],v241 offset:8960\ns_waitcnt lgkmcnt(9)\nM v[68:71],v[192:195],a[140:143],v[68:71]\nds_read_b64_tr_b16 v[216:217],v242 offset:512\ns_waitcnt lgkmcnt(9)\n" \
"M v[68:71],v[196:199],a[156:159],v[68:71]\nds_read_b64_tr_b16 v[218:219],v242 offset:768\ns_waitcnt lgkmcnt(8)\nM v[8:11],v[200:203],a[128:131],v[8:11]\nds_read_b64_tr_b16 v[220:221],v242 offset:8704\ns_waitcnt lgkmcnt(7)\nM v[8:11],v[204:207],a[144:147],v[8:11]\nbuffer_load_dwordx4 v225,s[36:39],s67 offen offset:1024 lds\n" \
"M v[12:15],v[200:203],a[132:135],v[12:15]\nds_read_b64_tr_b16 v[222:223],v242 offset:8960\nds_read_b64_tr_b16 v[192:193],v241 offset:1024\nM v[12:15],v[204:207],a[148:151],v[12:15]\nds_read_b64_tr_b16 v[194:195],v241 offset:1280\nds_read_b64_tr_b16 v[196:197],v241 offset:9216\nM v[72:75],v[200:203],a[136:139],v[72:75]\nds_read_b64_tr_b16 v[198:199],v241 offset:9472\n" \
"M v[72:75],v[204:207],a[152:155],v[72:75]\nM v[76:79],v[200:203],a[140:143],v[76:79]\nbuffer_load_dwordx4 v226,s[36:39],s67 offen offset:2048 lds\nM v[76:79],v[204:207],a[156:159],v[76:79]\nds_read_b64_tr_b16 v[200:201],v242 offset:1024\ns_waitcnt lgkmcnt(10)\nM v[16:19],v[208:211],a[128:131],v[16:19]\nds_read_b64_tr_b16 v[202:203],v242 offset:1280\n" \
"s_waitcnt lgkmcnt(10)\nM v[16:19],v[212:215],a[144:147],v[16:19]\nds_read_b64_tr_b16 v[204:205],v242 offset:9216\nds_read_b64_tr_b16 v[206:207],v242 offset:9472\nM v[20:23],v[208:211],a[132:135],v[20:23]\nglobal_load_dword v247,v253,s[16:17]\nM v[20:23],v[212:215],a[148:151],v[20:23]\nbuffer_load_dwordx4 v227,s[36:39],s67 offen offset:3072 lds\n" \
"M v[80:83],v[208:211],a[136:139],v[80:83]\nv_add_u32 v253,0xffd00000,v253\nM v[80:83],v[212:215],a[152:155],v[80:83]\ns_add_u32 m0,s64,131072\ns_add_u32 s69,s68,3145728\nM v[84:87],v[208:211],a[140:143],v[84:87]\nds_read_b64_tr_b16 v[208:209],v241 offset:1536\nds_read_b64_tr_b16 v[210:211],v241 offset:1792\n" \
"M v[84:87],v[212:215],a[156:159],v[84:87]\nds_read_b64_tr_b16 v[212:213],v241 offset:9728\ns_waitcnt lgkmcnt(7)\nM v[24:27],v[216:219],a[128:131],v[24:27]\nbuffer_load_dwordx4 v243,s[40:43],s69 offen offset:0 lds\nM v[24:27],v[220:223],a[144:147],v[24:27]\nds_read_b64_tr_b16 v[214:215],v241 offset:9984\nM v[28:31],v[216:219],a[132:135],v[28:31]\n" \
"M v[28:31],v[220:223],a[148:151],v[28:31]\nM v[88:91],v[216:219],a[136:139],v[88:91]\nM v[88:91],v[220:223],a[152:155],v[88:91]\nbuffer_load_dwordx4 v244,s[40:43],s69 offen offset:1024 lds\nM v[92:95],v[216:219],a[140:143],v[92:95]\nds_read_b64_tr_b16 v[216:217],v242 offset:1536\nds_read_b64_tr_b16 v[218:219],v242 offset:1792\nM v[92:95],v[220:223],a[156:159],v[92:95]\n" \
"ds_read_b64_tr_b16 v[220:221],v242 offset:9728\nds_read_b64_tr_b16 v[222:223],v242 offset:9984\nM v[32:35],v[192:195],a[128:131],v[32:35]\nM v[32:35],v[196:199],a[144:147],v[32:35]\nM v[36:39],v[192:195],a[132:135],v[36:39]\nbuffer_load_dwordx4 v245,s[40:43],s69 offen offset:2048 lds\nM v[36:39],v[196:199],a[148:151],v[36:39]\nM v[96:99],v[192:195],a[136:139],v[96:99]\n" \
"s_waitcnt vmcnt(16)\nv_xor_b32 v166,v166,v230\nM v[96:99],v[196:199],a[152:155],v[96:99]\nv_xor_b32 v167,v167,v231\nv_or_b32 v166,v166,v167\nM v[100:103],v[192:195],a[140:143],v[100:103]\nv_cmp_ne_u32 vcc,0,v166\nM v[100:103],v[196:199],a[156:159],v[100:103]\n" \
"buffer_load_dwordx4 v246,s[40:43],s69 offen offset:3072 lds\ns_waitcnt lgkmcnt(4)\nM v[40:43],v[200:203],a[128:131],v[40:43]\ns_cbranch_vccnz .Lhs\n.Lhb:\nv_bfe_u32 v231,v249,2,3\nM v[40:43],v[204:207],a[144:147],v[40:43]\ns_add_u32 m0,s65,8192\n" \
"s_xor_b32 s67,s66,896\nM v[44:47],v[200:203],a[132:135],v[44:47]\nv_mul_u32_u24 v231,3584,v231\nv_and_b32 v230,31,v249\nM v[44:47],v[204:207],a[148:151],v[44:47]\nv_lshlrev_b32 v230,7,v230\nM v[104:107],v[200:203],a[136:139],v[104:107]\nbuffer_load_dwordx4 v224,s[36:39],s67 offen offset:0 lds\n" \
"M v[104:107],v[204:207],a[152:155],v[104:107]\nM v[108:111],v[200:203],a[140:143],v[108:111]\nM v[108:111],v[204:207],a[156:159],v[108:111]\nM v[48:51],v[208:211],a[128:131],v[48:51]\nM v[48:51],v[212:215],a[144:147],v[48:51]\nbuffer_load_dwordx4 v225,s[36:39],s67 offen offset:1024 lds\nM v[52:55],v[208:211],a[132:135],v[52:55]\nM v[52:55],v[212:215],a[148:151],v[52:55]\n" \
"M v[112:115],v[208:211],a[136:139],v[112:115]\ns_waitcnt vmcnt(15) lgkmcnt(0)\ns_barrier\nM v[112:115],v[212:215],a[152:155],v[112:115]\nds_read_b128 a[160:163],v228 offset:16384\nds_read_b64_tr_b16 v[192:193],v241 offset:16384\nds_read_b64_tr_b16 v[194:195],v241 offset:16640\nM v[116:119],v[208:211],a[140:143],v[116:119]\n" \
"ds_read_b128 a[176:179],v229 offset:16384\nds_read_b64_tr_b16 v[196:197],v241 offset:24576\nds_read_b64_tr_b16 v[198:199],v241 offset:24832\nM v[116:119],v[212:215],a[156:159],v[116:119]\nds_read_b128 a[164:167],v228 offset:18432\nM v[56:59],v[216:219],a[128:131],v[56:59]\nbuffer_load_dwordx4 v[152:155],v240,s[52:55],s71 offen sc1\nds_read_b128 a[180:183],v229 offset:18432\n" \
"M v[56:59],v[220:223],a[144:147],v[56:59]\nbuffer_load_dwordx4 v226,s[36:39],s67 offen offset:2048 lds\ns_add_u32 s71,s70,798720\nds_read_b128 a[168:171],v228 offset:20480\nbuffer_load_dwordx4 v[156:159],v240,s[52:55],s71 offen sc1\nM v[60:63],v[216:219],a[132:135],v[60:63]\nds_read_b128 a[184:187],v229 offset:20480\ns_add_u32 s71,s70,811008\n" \
"M v[60:63],v[220:223],a[148:151],v[60:63]\nbuffer_load_dwordx4 v227,s[36:39],s67 offen offset:3072 lds\nbuffer_load_dwordx4 v[160:163],v240,s[52:55],s71 offen sc1\nds_read_b128 a[172:175],v228 offset:22528\nM v[120:123],v[216:219],a[136:139],v[120:123]\ns_add_u32 s71,s70,823296\nds_read_b128 a[188:191],v229 offset:22528\nbuffer_load_dwordx4 v[164:167],v240,s[52:55],s71 offen sc1\n" \
"M v[120:123],v[220:223],a[152:155],v[120:123]\ns_add_u32 m0,s65,12288\ns_xor_b32 s67,s66,33664\nds_read_b64_tr_b16 v[200:201],v242 offset:16384\nds_read_b64_tr_b16 v[202:203],v242 offset:16640\nM v[124:127],v[216:219],a[140:143],v[124:127]\nds_read_b64_tr_b16 v[204:205],v242 offset:24576\nds_read_b64_tr_b16 v[206:207],v242 offset:24832\n" \
"M v[124:127],v[220:223],a[156:159],v[124:127]\ns_lshl_b32 s71,s6,5\ns_waitcnt lgkmcnt(9)\nM v[0:3],v[192:195],a[160:163],v[0:3]\nds_read_b64_tr_b16 v[208:209],v241 offset:16896\ns_add_u32 s71,s71,790528\nM v[0:3],v[196:199],a[176:179],v[0:3]\nds_read_b64_tr_b16 v[210:211],v241 offset:17152\n" \
"ds_read_b64_tr_b16 v[212:213],v241 offset:25088\nM v[4:7],v[192:195],a[164:167],v[4:7]\nds_read_b64_tr_b16 v[214:215],v241 offset:25344\ns_waitcnt lgkmcnt(11)\nM v[4:7],v[196:199],a[180:183],v[4:7]\nds_read_b64_tr_b16 v[216:217],v242 offset:16896\nds_read_b64_tr_b16 v[218:219],v242 offset:17152\nM v[64:67],v[192:195],a[168:171],v[64:67]\n" \
"ds_read_b64_tr_b16 v[220:221],v242 offset:25088\ns_waitcnt lgkmcnt(13)\nM v[64:67],v[196:199],a[184:187],v[64:67]\nds_read_b64_tr_b16 v[222:223],v242 offset:25344\ns_waitcnt lgkmcnt(13)\nM v[68:71],v[192:195],a[172:175],v[68:71]\nds_read_b64_tr_b16 v[192:193],v241 offset:17408\ns_waitcnt lgkmcnt(13)\n" \
"M v[68:71],v[196:199],a[188:191],v[68:71]\nds_read_b64_tr_b16 v[194:195],v241 offset:17664\ns_waitcnt lgkmcnt(12)\nM v[8:11],v[200:203],a[160:163],v[8:11]\nds_read_b64_tr_b16 v[196:197],v241 offset:25600\ns_waitcnt lgkmcnt(11)\nM v[8:11],v[204:207],a[176:179],v[8:11]\ns_cmp_lt_u32 s7,2\n" \
"s_cselect_b32 s70,0,1.0\nM v[12:15],v[200:203],a[164:167],v[12:15]\nbuffer_load_dword v247,v254,s[56:59],s78 offen\nM v[12:15],v[204:207],a[180:183],v[12:15]\nds_read_b64_tr_b16 v[198:199],v241 offset:25856\nv_and_b32 v254,3,v249\nM v[72:75],v[200:203],a[168:171],v[72:75]\ns_cmp_eq_u32 s62,5\n" \
"s_cselect_b64 s[56:57],s[22:23],s[24:25]\nM v[72:75],v[204:207],a[184:187],v[72:75]\nv_lshl_add_u32 v231,v254,7,v231\nM v[76:79],v[200:203],a[172:175],v[76:79]\nglobal_load_dword v247,v253,s[12:13]\nM v[76:79],v[204:207],a[188:191],v[76:79]\nds_read_b64_tr_b16 v[200:201],v242 offset:17408\ns_waitcnt lgkmcnt(6)\n" \
"M v[16:19],v[208:211],a[160:163],v[16:19]\nds_read_b64_tr_b16 v[202:203],v242 offset:17664\nds_read_b64_tr_b16 v[204:205],v242 offset:25600\nM v[16:19],v[212:215],a[176:179],v[16:19]\nds_read_b64_tr_b16 v[206:207],v242 offset:25856\ns_add_u32 s69,s68,3670016\nM v[20:23],v[208:211],a[164:167],v[20:23]\nbuffer_load_dwordx4 v224,s[36:39],s67 offen offset:0 lds\n" \
"M v[20:23],v[212:215],a[180:183],v[20:23]\nv_add_u32 v253,0x100000,v253\nM v[80:83],v[208:211],a[168:171],v[80:83]\nM v[80:83],v[212:215],a[184:187],v[80:83]\nM v[84:87],v[208:211],a[172:175],v[84:87]\nbuffer_load_dwordx4 v225,s[36:39],s67 offen offset:1024 lds\nM v[84:87],v[212:215],a[188:191],v[84:87]\nds_read_b64_tr_b16 v[208:209],v241 offset:17920\n" \
"ds_read_b64_tr_b16 v[210:211],v241 offset:18176\nM v[24:27],v[216:219],a[160:163],v[24:27]\nds_read_b64_tr_b16 v[212:213],v241 offset:26112\ns_waitcnt lgkmcnt(7)\nM v[24:27],v[220:223],a[176:179],v[24:27]\nds_read_b64_tr_b16 v[214:215],v241 offset:26368\nM v[28:31],v[216:219],a[164:167],v[28:31]\nbuffer_load_dwordx4 v226,s[36:39],s67 offen offset:2048 lds\n" \
"M v[28:31],v[220:223],a[180:183],v[28:31]\nM v[88:91],v[216:219],a[168:171],v[88:91]\nM v[88:91],v[220:223],a[184:187],v[88:91]\nM v[92:95],v[216:219],a[172:175],v[92:95]\nbuffer_load_dwordx4 v227,s[36:39],s67 offen offset:3072 lds\nM v[92:95],v[220:223],a[188:191],v[92:95]\ns_add_u32 m0,s64,147456\nds_read_b64_tr_b16 v[216:217],v242 offset:17920\n" \
"M v[32:35],v[192:195],a[160:163],v[32:35]\nds_read_b64_tr_b16 v[218:219],v242 offset:18176\nds_read_b64_tr_b16 v[220:221],v242 offset:26112\nM v[32:35],v[196:199],a[176:179],v[32:35]\ns_cmp_eq_u32 s62,0\ns_cselect_b32 s36,0x8000,0\nM v[36:39],v[192:195],a[164:167],v[36:39]\nds_read_b64_tr_b16 v[222:223],v242 offset:26368\n" \
"s_add_u32 s36,s36,s64\nM v[36:39],v[196:199],a[180:183],v[36:39]\nbuffer_load_dwordx4 v243,s[40:43],s69 offen offset:0 lds\nM v[96:99],v[192:195],a[168:171],v[96:99]\nM v[96:99],v[196:199],a[184:187],v[96:99]\nM v[100:103],v[192:195],a[172:175],v[100:103]\nM v[100:103],v[196:199],a[188:191],v[100:103]\ns_waitcnt lgkmcnt(4)\n" \
"M v[40:43],v[200:203],a[160:163],v[40:43]\nbuffer_load_dwordx4 v244,s[40:43],s69 offen offset:1024 lds\nM v[40:43],v[204:207],a[176:179],v[40:43]\nM v[44:47],v[200:203],a[164:167],v[44:47]\nM v[44:47],v[204:207],a[180:183],v[44:47]\nM v[104:107],v[200:203],a[168:171],v[104:107]\nbuffer_load_dwordx4 v245,s[40:43],s69 offen offset:2048 lds\nM v[104:107],v[204:207],a[184:187],v[104:107]\n" \
"M v[108:111],v[200:203],a[172:175],v[108:111]\nM v[108:111],v[204:207],a[188:191],v[108:111]\nM v[48:51],v[208:211],a[160:163],v[48:51]\nbuffer_load_dwordx4 v246,s[40:43],s69 offen offset:3072 lds\nM v[48:51],v[212:215],a[176:179],v[48:51]\ns_sub_u32 s40,s16,0x1000\ns_subb_u32 s41,s17,0\nM v[52:55],v[208:211],a[164:167],v[52:55]\n" \
"s_add_u32 m0,s64,98304\ns_add_u32 s69,s68,0\nM v[52:55],v[212:215],a[180:183],v[52:55]\ns_mov_b32 s16,0\ns_mov_b32 s17,3\nM v[112:115],v[208:211],a[168:171],v[112:115]\ns_waitcnt vmcnt(18) lgkmcnt(0)\ns_barrier\n" \
"M v[112:115],v[212:215],a[184:187],v[112:115]\nds_read_b128 a[192:195],v228 offset:0\nds_read_b64_tr_b16 v[192:193],v241 offset:32768\nds_read_b64_tr_b16 v[194:195],v241 offset:33024\nM v[116:119],v[208:211],a[172:175],v[116:119]\nglobal_load_dword v247,v253,s[12:13]\nds_read_b128 a[208:211],v229 offset:0\nds_read_b64_tr_b16 v[196:197],v241 offset:40960\n" \
"ds_read_b64_tr_b16 v[198:199],v241 offset:41216\nM v[116:119],v[212:215],a[188:191],v[116:119]\nds_read_b128 a[196:199],v228 offset:2048\nv_add_u32 v253,0x100000,v253\nM v[56:59],v[216:219],a[160:163],v[56:59]\nds_read_b128 a[212:215],v229 offset:2048\nM v[56:59],v[220:223],a[176:179],v[56:59]\nds_read_b128 a[200:203],v228 offset:4096\n" \
"M v[60:63],v[216:219],a[164:167],v[60:63]\nds_read_b128 a[216:219],v229 offset:4096\nM v[60:63],v[220:223],a[180:183],v[60:63]\nds_read_b128 a[204:207],v228 offset:6144\nM v[120:123],v[216:219],a[168:171],v[120:123]\nds_read_b128 a[220:223],v229 offset:6144\nM v[120:123],v[220:223],a[184:187],v[120:123]\nds_read_b64_tr_b16 v[200:201],v242 offset:32768\n" \
"ds_read_b64_tr_b16 v[202:203],v242 offset:33024\nM v[124:127],v[216:219],a[172:175],v[124:127]\nds_read_b64_tr_b16 v[204:205],v242 offset:40960\nds_read_b64_tr_b16 v[206:207],v242 offset:41216\nM v[124:127],v[220:223],a[188:191],v[124:127]\ns_waitcnt vmcnt(14)\nv_add_f32 v152,v152,v156\ns_waitcnt lgkmcnt(13)\n" \
"M v[0:3],v[192:195],a[192:195],v[0:3]\nds_read_b64_tr_b16 v[208:209],v241 offset:33280\ns_waitcnt lgkmcnt(11)\nM v[0:3],v[196:199],a[208:211],v[0:3]\nds_read_b64_tr_b16 v[210:211],v241 offset:33536\ns_waitcnt lgkmcnt(11)\nM v[4:7],v[192:195],a[196:199],v[4:7]\nds_read_b64_tr_b16 v[212:213],v241 offset:41472\n" \
"s_waitcnt lgkmcnt(11)\nM v[4:7],v[196:199],a[212:215],v[4:7]\nds_read_b64_tr_b16 v[214:215],v241 offset:41728\ns_waitcnt lgkmcnt(11)\nM v[64:67],v[192:195],a[200:203],v[64:67]\nds_read_b64_tr_b16 v[216:217],v242 offset:33280\ns_waitcnt lgkmcnt(11)\nM v[64:67],v[196:199],a[216:219],v[64:67]\n" \
"ds_read_b64_tr_b16 v[218:219],v242 offset:33536\ns_waitcnt lgkmcnt(11)\nM v[68:71],v[192:195],a[204:207],v[68:71]\nds_read_b64_tr_b16 v[220:221],v242 offset:41472\ns_waitcnt lgkmcnt(11)\nM v[68:71],v[196:199],a[220:223],v[68:71]\nds_read_b64_tr_b16 v[222:223],v242 offset:41728\ns_waitcnt lgkmcnt(10)\n" \
"M v[8:11],v[200:203],a[192:195],v[8:11]\nds_read_b64_tr_b16 v[192:193],v241 offset:33792\ns_waitcnt lgkmcnt(9)\nM v[8:11],v[204:207],a[208:211],v[8:11]\nds_read_b64_tr_b16 v[194:195],v241 offset:34048\nds_read_b64_tr_b16 v[196:197],v241 offset:41984\nM v[12:15],v[200:203],a[196:199],v[12:15]\nds_read_b64_tr_b16 v[198:199],v241 offset:42240\n" \
"s_waitcnt vmcnt(11)\nv_add_f32 v160,v160,v164\nM v[12:15],v[204:207],a[212:215],v[12:15]\nv_add_f32 v153,v153,v157\nv_add_f32 v161,v161,v165\nM v[72:75],v[200:203],a[200:203],v[72:75]\nv_add_f32 v154,v154,v158\nv_add_f32 v162,v162,v166\n" \
"M v[72:75],v[204:207],a[216:219],v[72:75]\nv_add_f32 v155,v155,v159\nv_add_f32 v163,v163,v167\nM v[76:79],v[200:203],a[204:207],v[76:79]\nds_read_b64_tr_b16 v[200:201],v242 offset:33792\nds_read_b64_tr_b16 v[202:203],v242 offset:34048\nM v[76:79],v[204:207],a[220:223],v[76:79]\nds_read_b64_tr_b16 v[204:205],v242 offset:41984\n" \
"s_waitcnt lgkmcnt(9)\nM v[16:19],v[208:211],a[192:195],v[16:19]\nds_read_b64_tr_b16 v[206:207],v242 offset:42240\nv_add_f32 v152,v152,v160\nM v[16:19],v[212:215],a[208:211],v[16:19]\nbuffer_load_dword v168,v169,s[52:55],s71 offen sc1\nM v[20:23],v[208:211],a[196:199],v[20:23]\nv_add_f32 v153,v153,v161\n" \
"v_add_f32 v154,v154,v162\nM v[20:23],v[212:215],a[212:215],v[20:23]\nv_add_f32 v155,v155,v163\nv_cvt_pk_bf16_f32 v152,0,v152\nM v[80:83],v[208:211],a[200:203],v[80:83]\nv_cvt_pk_bf16_f32 v153,0,v153\nv_cvt_pk_bf16_f32 v154,0,v154\nM v[80:83],v[212:215],a[216:219],v[80:83]\n" \
"v_cvt_pk_bf16_f32 v155,0,v155\nv_add_f32 v152,s70,v152\nM v[84:87],v[208:211],a[204:207],v[84:87]\nds_read_b64_tr_b16 v[208:209],v241 offset:34304\nds_read_b64_tr_b16 v[210:211],v241 offset:34560\nM v[84:87],v[212:215],a[220:223],v[84:87]\nds_read_b64_tr_b16 v[212:213],v241 offset:42496\nds_read_b64_tr_b16 v[214:215],v241 offset:42752\n" \
"M v[24:27],v[216:219],a[192:195],v[24:27]\nv_add_f32 v153,s70,v153\ns_waitcnt lgkmcnt(7)\nM v[24:27],v[220:223],a[208:211],v[24:27]\nv_add_f32 v154,s70,v154\nv_add_f32 v155,s70,v155\nM v[28:31],v[216:219],a[196:199],v[28:31]\nglobal_load_dword v247,v253,s[12:13]\n" \
"M v[28:31],v[220:223],a[212:215],v[28:31]\ns_lshr_b32 s71,s64,2\nv_cvt_pk_bf16_f32 v152,0,v152\nM v[88:91],v[216:219],a[200:203],v[88:91]\nv_cvt_pk_bf16_f32 v153,0,v153\nv_cvt_pk_bf16_f32 v154,0,v154\nM v[88:91],v[220:223],a[216:219],v[88:91]\nv_cvt_pk_bf16_f32 v155,0,v155\n" \
"v_add_u32 v171,s71,v240\nM v[92:95],v[216:219],a[204:207],v[92:95]\nds_write_b128 v171,v[152:155] offset:16384\nM v[92:95],v[220:223],a[220:223],v[92:95]\nds_read_b64_tr_b16 v[216:217],v242 offset:34304\nds_read_b64_tr_b16 v[218:219],v242 offset:34560\nM v[32:35],v[192:195],a[192:195],v[32:35]\nds_read_b64_tr_b16 v[220:221],v242 offset:42496\n" \
"ds_read_b64_tr_b16 v[222:223],v242 offset:42752\nM v[32:35],v[196:199],a[208:211],v[32:35]\nv_add_u32 v253,0x100000,v253\ns_lshl_b32 s70,s7,8\nM v[36:39],v[192:195],a[196:199],v[36:39]\nbuffer_load_dwordx4 v243,s[40:43],s69 offen offset:0 lds\nM v[36:39],v[196:199],a[212:215],v[36:39]\nv_lshl_add_u32 v251,v251,5,s70\n" \
"s_lshl_b32 s71,s6,5\nM v[96:99],v[192:195],a[200:203],v[96:99]\ns_and_b32 s71,s71,0xfffffe00\ns_xor_b32 s70,s63,16\nM v[96:99],v[196:199],a[216:219],v[96:99]\ns_lshr_b32 s70,s70,2\ns_lshl3_add_u32 s70,s62,s70\nM v[100:103],v[192:195],a[204:207],v[100:103]\n" \
"buffer_load_dwordx4 v244,s[40:43],s69 offen offset:1024 lds\nM v[100:103],v[196:199],a[220:223],v[100:103]\ns_lshl_b32 s70,s70,15\ns_waitcnt lgkmcnt(5)\nM v[40:43],v[200:203],a[192:195],v[40:43]\ns_add_u32 s70,s70,s64\nM v[40:43],v[204:207],a[208:211],v[40:43]\nM v[44:47],v[200:203],a[196:199],v[44:47]\n" \
"buffer_load_dwordx4 v245,s[40:43],s69 offen offset:2048 lds\nM v[44:47],v[204:207],a[212:215],v[44:47]\nM v[104:107],v[200:203],a[200:203],v[104:107]\nM v[104:107],v[204:207],a[216:219],v[104:107]\nM v[108:111],v[200:203],a[204:207],v[108:111]\nbuffer_load_dwordx4 v246,s[40:43],s69 offen offset:3072 lds\nM v[108:111],v[204:207],a[220:223],v[108:111]\ns_add_u32 m0,s64,114688\n" \
"s_add_u32 s69,s68,524288\nM v[48:51],v[208:211],a[192:195],v[48:51]\nM v[48:51],v[212:215],a[208:211],v[48:51]\nM v[52:55],v[208:211],a[196:199],v[52:55]\nbuffer_store_dwordx2 v[232:233],off,s[52:55],s72 sc1\nM v[52:55],v[212:215],a[212:215],v[52:55]\ns_and_b32 s72,s6,1\ns_lshl_b32 s72,s72,8\n" \
"M v[112:115],v[208:211],a[200:203],v[112:115]\ns_waitcnt vmcnt(8) lgkmcnt(0)\ns_barrier\nM v[112:115],v[212:215],a[216:219],v[112:115]\nds_read_b128 a[224:227],v228 offset:8192\nds_read_b64_tr_b16 v[192:193],v241 offset:49152\nds_read_b64_tr_b16 v[194:195],v241 offset:49408\nM v[116:119],v[208:211],a[204:207],v[116:119]\n" \
"ds_read_b128 a[240:243],v229 offset:8192\nds_read_b64_tr_b16 v[196:197],v241 offset:57344\nds_read_b64_tr_b16 v[198:199],v241 offset:57600\nM v[116:119],v[212:215],a[220:223],v[116:119]\nds_read_b128 a[228:231],v228 offset:10240\ns_add_u32 s71,s71,s72\nM v[56:59],v[216:219],a[192:195],v[56:59]\nds_read_b128 a[244:247],v229 offset:10240\n" \
"s_and_b32 s72,s6,14\nM v[56:59],v[220:223],a[208:211],v[56:59]\nds_read_b128 a[232:235],v228 offset:12288\ns_lshl1_add_u32 s71,s72,s71\nM v[60:63],v[216:219],a[196:199],v[60:63]\nds_read_b128 a[248:251],v229 offset:12288\ns_or_b32 s71,s71,s75\nM v[60:63],v[220:223],a[212:215],v[60:63]\n" \
"ds_read_b128 a[236:239],v228 offset:14336\ns_mov_b32 s72,0x3e0293ee\nM v[120:123],v[216:219],a[200:203],v[120:123]\nds_read_b128 a[252:255],v229 offset:14336\nv_and_b32 v228,15,v249\nM v[120:123],v[220:223],a[216:219],v[120:123]\nds_read_b64_tr_b16 v[200:201],v242 offset:49152\nds_read_b64_tr_b16 v[202:203],v242 offset:49408\n" \
"M v[124:127],v[216:219],a[204:207],v[124:127]\nds_read_b64_tr_b16 v[204:205],v242 offset:57344\nds_read_b64_tr_b16 v[206:207],v242 offset:57600\nM v[124:127],v[220:223],a[220:223],v[124:127]\nv_and_b32 v229,16,v249\ns_waitcnt lgkmcnt(13)\nM v[0:3],v[192:195],a[224:227],v[0:3]\nds_read_b64_tr_b16 v[208:209],v241 offset:49664\n" \
"s_waitcnt lgkmcnt(11)\nM v[0:3],v[196:199],a[240:243],v[0:3]\nds_read_b64_tr_b16 v[210:211],v241 offset:49920\ns_waitcnt lgkmcnt(11)\nM v[4:7],v[192:195],a[228:231],v[4:7]\nds_read_b64_tr_b16 v[212:213],v241 offset:57856\ns_waitcnt lgkmcnt(11)\nM v[4:7],v[196:199],a[244:247],v[4:7]\n" \
"ds_read_b64_tr_b16 v[214:215],v241 offset:58112\ns_waitcnt lgkmcnt(11)\nM v[64:67],v[192:195],a[232:235],v[64:67]\nds_read_b64_tr_b16 v[216:217],v242 offset:49664\ns_waitcnt lgkmcnt(11)\nM v[64:67],v[196:199],a[248:251],v[64:67]\nds_read_b64_tr_b16 v[218:219],v242 offset:49920\ns_waitcnt lgkmcnt(11)\n" \
"M v[68:71],v[192:195],a[236:239],v[68:71]\nglobal_load_dword v247,v253,s[12:13]\ns_waitcnt lgkmcnt(10)\nM v[68:71],v[196:199],a[252:255],v[68:71]\nds_read_b64_tr_b16 v[220:221],v242 offset:57856\ns_waitcnt lgkmcnt(9)\nM v[8:11],v[200:203],a[224:227],v[8:11]\nds_read_b64_tr_b16 v[222:223],v242 offset:58112\n" \
"s_waitcnt lgkmcnt(8)\nM v[8:11],v[204:207],a[240:243],v[8:11]\nds_read_b64_tr_b16 v[192:193],v241 offset:50176\nds_read_b64_tr_b16 v[194:195],v241 offset:50432\nM v[12:15],v[200:203],a[228:231],v[12:15]\nds_read_b64_tr_b16 v[196:197],v241 offset:58368\nds_read_b64_tr_b16 v[198:199],v241 offset:58624\nM v[12:15],v[204:207],a[244:247],v[12:15]\n" \
"ds_read_b128 v[176:179],v251 offset:19456\nds_read_b128 v[160:163],v251 offset:17408\nM v[72:75],v[200:203],a[232:235],v[72:75]\nds_read_b128 v[172:175],v251 offset:18448\nv_cvt_pk_bf16_f32 v128,v0,v1\nM v[72:75],v[204:207],a[248:251],v[72:75]\nds_read_b128 v[152:155],v251 offset:16384\nv_cvt_pk_bf16_f32 v129,v2,v3\n" \
"M v[76:79],v[200:203],a[236:239],v[76:79]\nds_read_b64_tr_b16 v[200:201],v242 offset:50176\nv_cvt_pk_bf16_f32 v130,v8,v9\nM v[76:79],v[204:207],a[252:255],v[76:79]\nds_read_b64_tr_b16 v[202:203],v242 offset:50432\ns_waitcnt lgkmcnt(12)\nM v[16:19],v[208:211],a[224:227],v[16:19]\nds_read_b64_tr_b16 v[204:205],v242 offset:58368\n" \
"ds_read_b64_tr_b16 v[206:207],v242 offset:58624\nM v[16:19],v[212:215],a[240:243],v[16:19]\nds_read_b128 v[156:159],v251 offset:16400\nds_read_b128 v[180:183],v251 offset:19472\nM v[20:23],v[208:211],a[228:231],v[20:23]\nds_read_b128 v[164:167],v251 offset:17424\nv_cvt_pk_bf16_f32 v131,v10,v11\nM v[20:23],v[212:215],a[244:247],v[20:23]\n" \
"buffer_load_dwordx4 v243,s[40:43],s69 offen offset:0 lds\nM v[80:83],v[208:211],a[232:235],v[80:83]\nv_cvt_pk_bf16_f32 v132,v16,v17\nv_cvt_pk_bf16_f32 v133,v18,v19\nM v[80:83],v[212:215],a[248:251],v[80:83]\ns_waitcnt lgkmcnt(10)\nv_mul_f32 v144,v144,v176\nM v[84:87],v[208:211],a[236:239],v[84:87]\n" \
"ds_read_b64_tr_b16 v[208:209],v241 offset:50688\nds_read_b64_tr_b16 v[210:211],v241 offset:50944\nM v[84:87],v[212:215],a[252:255],v[84:87]\nds_read_b64_tr_b16 v[212:213],v241 offset:58880\nds_read_b64_tr_b16 v[214:215],v241 offset:59136\nM v[24:27],v[216:219],a[224:227],v[24:27]\ns_waitcnt vmcnt(8)\nv_add_f32_dpp v168,v168,v168 quad_perm:[1,0,3,2] row_mask:0xf bank_mask:0xf\n" \
"M v[24:27],v[220:223],a[240:243],v[24:27]\nv_mul_f32 v145,v145,v177\nv_add_f32_dpp v168,v168,v168 quad_perm:[2,3,0,1] row_mask:0xf bank_mask:0xf\nM v[28:31],v[216:219],a[228:231],v[28:31]\nv_cvt_pk_bf16_f32 v168,0,v168\nv_cvt_pk_bf16_f32 v144,0,v144\nM v[28:31],v[220:223],a[244:247],v[28:31]\nbuffer_store_dword v168,v170,s[52:55],s71 offen sc1\n" \
"M v[88:91],v[216:219],a[232:235],v[88:91]\nds_read_b128 v[168:171],v251 offset:18432\nv_cvt_pk_bf16_f32 v145,0,v145\nM v[88:91],v[220:223],a[248:251],v[88:91]\ns_waitcnt lgkmcnt(5)\nv_add_f32 v144,v144,v160\nM v[92:95],v[216:219],a[236:239],v[92:95]\nds_read_b64_tr_b16 v[216:217],v242 offset:50688\n" \
"ds_read_b64_tr_b16 v[218:219],v242 offset:50944\nM v[92:95],v[220:223],a[252:255],v[92:95]\nds_read_b64_tr_b16 v[220:221],v242 offset:58880\nds_read_b64_tr_b16 v[222:223],v242 offset:59136\nM v[32:35],v[192:195],a[224:227],v[32:35]\nv_add_f32 v145,v145,v161\nv_cvt_pk_bf16_f32 v144,0,v144\nM v[32:35],v[196:199],a[240:243],v[32:35]\n" \
"v_cvt_pk_bf16_f32 v145,0,v145\nv_mul_f32 v144,s72,v144\nM v[36:39],v[192:195],a[228:231],v[36:39]\nv_mul_f32 v145,s72,v145\nv_cvt_pk_bf16_f32 v236,v144,v145\nM v[36:39],v[196:199],a[244:247],v[36:39]\nv_cvt_pk_bf16_f32 v144,v4,v5\nv_cvt_pk_bf16_f32 v145,v6,v7\n" \
"M v[96:99],v[192:195],a[232:235],v[96:99]\nv_cvt_pk_bf16_f32 v160,v64,v65\nv_cvt_pk_bf16_f32 v161,v66,v67\nM v[96:99],v[196:199],a[248:251],v[96:99]\ns_and_b32 s71,s63,2\nv_mul_f32 v140,v140,v172\nM v[100:103],v[192:195],a[236:239],v[100:103]\ns_waitcnt lgkmcnt(4)\n" \
"v_mul_f32 v136,v136,v168\nM v[100:103],v[196:199],a[252:255],v[100:103]\nv_mul_f32 v137,v137,v169\nv_mul_f32 v138,v138,v170\nM v[40:43],v[200:203],a[224:227],v[40:43]\nv_mul_f32 v139,v139,v171\nv_mul_f32 v141,v141,v173\nM v[40:43],v[204:207],a[240:243],v[40:43]\n" \
"v_mul_f32 v142,v142,v174\nv_mul_f32 v143,v143,v175\nM v[44:47],v[200:203],a[228:231],v[44:47]\ns_lshl_b32 s71,s71,9\nv_cvt_pk_bf16_f32 v176,v68,v69\nM v[44:47],v[204:207],a[244:247],v[44:47]\nv_cvt_pk_bf16_f32 v177,v70,v71\nv_cvt_pk_bf16_f32 v136,0,v136\n" \
"M v[104:107],v[200:203],a[232:235],v[104:107]\nv_cvt_pk_bf16_f32 v137,0,v137\nv_cvt_pk_bf16_f32 v138,0,v138\nM v[104:107],v[204:207],a[248:251],v[104:107]\nv_cvt_pk_bf16_f32 v139,0,v139\nv_cvt_pk_bf16_f32 v140,0,v140\nM v[108:111],v[200:203],a[236:239],v[108:111]\nv_cvt_pk_bf16_f32 v141,0,v141\n" \
"v_cvt_pk_bf16_f32 v142,0,v142\nM v[108:111],v[204:207],a[252:255],v[108:111]\nv_cvt_pk_bf16_f32 v143,0,v143\nv_lshrrev_b32 v251,5,v249\nM v[48:51],v[208:211],a[224:227],v[48:51]\ns_add_u32 s70,s70,s71\nv_add_f32 v136,v136,v152\nM v[48:51],v[212:215],a[240:243],v[48:51]\n" \
"v_add_f32 v137,v137,v153\nv_add_f32 v138,v138,v154\nM v[52:55],v[208:211],a[228:231],v[52:55]\nv_add_f32 v139,v139,v155\nv_add_f32 v140,v140,v156\nM v[52:55],v[212:215],a[244:247],v[52:55]\nv_add_f32 v141,v141,v157\nv_add_f32 v142,v142,v158\n" \
"M v[112:115],v[208:211],a[232:235],v[112:115]\ns_waitcnt vmcnt(4) lgkmcnt(0)\ns_barrier\nM v[112:115],v[212:215],a[248:251],v[112:115]\nds_read_b64_tr_b16 v[192:193],v241 offset:0\nds_read_b64_tr_b16 v[194:195],v241 offset:256\nM v[116:119],v[208:211],a[236:239],v[116:119]\nds_read_b64_tr_b16 v[196:197],v241 offset:8192\n" \
"ds_read_b64_tr_b16 v[198:199],v241 offset:8448\nM v[116:119],v[212:215],a[252:255],v[116:119]\nds_read_b64_tr_b16 v[200:201],v242 offset:0\nds_read_b64_tr_b16 v[202:203],v242 offset:256\nM v[56:59],v[216:219],a[224:227],v[56:59]\nds_read_b64_tr_b16 v[204:205],v242 offset:8192\nds_read_b64_tr_b16 v[206:207],v242 offset:8448\nM v[56:59],v[220:223],a[240:243],v[56:59]\n" \
"ds_read_b64_tr_b16 v[208:209],v241 offset:512\nds_read_b64_tr_b16 v[210:211],v241 offset:768\nM v[60:63],v[216:219],a[228:231],v[60:63]\nv_add_f32 v143,v143,v159\nv_mul_f32 v146,v146,v178\nM v[60:63],v[220:223],a[244:247],v[60:63]\nv_mul_f32 v147,v147,v179\nv_lshlrev_b32 v251,11,v251\n" \
"M v[120:123],v[216:219],a[232:235],v[120:123]\ns_and_b32 s71,s63,1\nds_read_b64_tr_b16 v[212:213],v241 offset:8704\nM v[120:123],v[220:223],a[248:251],v[120:123]\nds_read_b64_tr_b16 v[214:215],v241 offset:8960\nv_cvt_pk_bf16_f32 v136,0,v136\nM v[124:127],v[216:219],a[236:239],v[124:127]\nv_cvt_pk_bf16_f32 v137,0,v137\n" \
"v_cvt_pk_bf16_f32 v138,0,v138\nM v[124:127],v[220:223],a[252:255],v[124:127]\nv_cvt_pk_bf16_f32 v139,0,v139\ns_waitcnt lgkmcnt(10)\nM v[0:3],a[0:3],v[192:195],0\nv_cvt_pk_bf16_f32 v140,0,v140\ns_waitcnt lgkmcnt(8)\nM v[0:3],a[16:19],v[196:199],v[0:3]\n" \
"v_cvt_pk_bf16_f32 v141,0,v141\nv_cvt_pk_bf16_f32 v142,0,v142\nM v[4:7],a[4:7],v[192:195],0\nv_cvt_pk_bf16_f32 v143,0,v143\nv_cvt_pk_bf16_f32 v146,0,v146\nM v[4:7],a[20:23],v[196:199],v[4:7]\nv_cvt_pk_bf16_f32 v147,0,v147\nv_lshl_or_b32 v251,v250,8,v251\n" \
"M v[64:67],a[8:11],v[192:195],0\ns_lshl_b32 s71,s71,7\nv_mul_f32 v136,s72,v136\nM v[64:67],a[24:27],v[196:199],v[64:67]\nv_mul_f32 v137,s72,v137\nv_mul_f32 v138,s72,v138\nM v[68:71],a[12:15],v[192:195],0\nv_mul_f32 v139,s72,v139\n" \
"v_mul_f32 v140,s72,v140\nM v[68:71],a[28:31],v[196:199],v[68:71]\nv_mul_f32 v141,s72,v141\nv_mul_f32 v142,s72,v142\nv_mul_f32 v143,s72,v143\nv_add_f32 v146,v146,v162\nv_add_f32 v147,v147,v163\nv_and_b32 v250,0x70,v240\n" \
"s_add_u32 s70,s70,s71\ns_waitcnt lgkmcnt(0)\nM v[8:11],a[0:3],v[200:203],0\nv_cvt_pk_bf16_f32 v232,v136,v137\nv_cvt_pk_bf16_f32 v233,v138,v139\nv_cvt_pk_bf16_f32 v234,v140,v141\nv_cvt_pk_bf16_f32 v235,v142,v143\nv_cvt_pk_bf16_f32 v146,0,v146\n" \
"v_cvt_pk_bf16_f32 v147,0,v147\nv_or_b32 v251,v251,v250\ns_add_u32 s70,s70,1048576\nv_mul_f32 v146,s72,v146\nv_mul_f32 v147,s72,v147\nbuffer_store_dwordx4 v[232:235],v251,s[52:55],s70 offen sc1\nM v[8:11],a[16:19],v[204:207],v[8:11]\nv_cvt_pk_bf16_f32 v237,v146,v147\n" \
"v_mul_f32 v148,v148,v180\nv_mul_f32 v149,v149,v181\nv_mul_f32 v150,v150,v182\nv_mul_f32 v151,v151,v183\nv_cvt_pk_bf16_f32 v146,v12,v13\nv_cvt_pk_bf16_f32 v147,v14,v15\nM v[12:15],a[4:7],v[200:203],0\nv_cvt_pk_bf16_f32 v148,0,v148\n" \
"v_cvt_pk_bf16_f32 v149,0,v149\nv_cvt_pk_bf16_f32 v150,0,v150\nv_cvt_pk_bf16_f32 v151,0,v151\nv_add_f32 v148,v148,v164\nv_add_f32 v149,v149,v165\nv_add_f32 v150,v150,v166\nv_add_f32 v151,v151,v167\nM v[12:15],a[20:23],v[204:207],v[12:15]\n" \
"v_cvt_pk_bf16_f32 v148,0,v148\nv_cvt_pk_bf16_f32 v149,0,v149\nv_cvt_pk_bf16_f32 v150,0,v150\nv_cvt_pk_bf16_f32 v151,0,v151\nv_mul_f32 v148,s72,v148\nv_mul_f32 v149,s72,v149\nv_mul_f32 v150,s72,v150\nv_mul_f32 v151,s72,v151\n" \
"v_cvt_pk_bf16_f32 v162,v72,v73\nv_cvt_pk_bf16_f32 v163,v74,v75\nM v[72:75],a[8:11],v[200:203],0\nv_cvt_pk_bf16_f32 v238,v148,v149\nv_cvt_pk_bf16_f32 v239,v150,v151\ns_add_u32 s71,s70,16384\nbuffer_load_dwordx4 v244,s[40:43],s69 offen offset:1024 lds\nbuffer_load_dwordx4 v245,s[40:43],s69 offen offset:2048 lds\n" \
"buffer_load_dwordx4 v246,s[40:43],s69 offen offset:3072 lds\nbuffer_store_dwordx4 v[236:239],v251,s[52:55],s71 offen sc1\nM v[72:75],a[24:27],v[204:207],v[72:75]\nv_cvt_pk_bf16_f32 v178,v76,v77\nv_cvt_pk_bf16_f32 v179,v78,v79\nds_read_b64_tr_b16 v[216:217],v242 offset:512\nds_read_b64_tr_b16 v[218:219],v242 offset:768\nM v[76:79],a[12:15],v[200:203],0\n" \
"ds_read_b64_tr_b16 v[220:221],v242 offset:8704\nds_read_b64_tr_b16 v[222:223],v242 offset:8960\nM v[76:79],a[28:31],v[204:207],v[76:79]\nv_cvt_pk_bf16_f32 v148,v20,v21\nv_cvt_pk_bf16_f32 v149,v22,v23\nM v[16:19],a[0:3],v[208:211],0\nv_cvt_pk_bf16_f32 v164,v80,v81\nv_cvt_pk_bf16_f32 v165,v82,v83\n" \
"M v[16:19],a[16:19],v[212:215],v[16:19]\nv_cvt_pk_bf16_f32 v180,v84,v85\nv_cvt_pk_bf16_f32 v181,v86,v87\nM v[20:23],a[4:7],v[208:211],0\nds_read_b64_tr_b16 v[192:193],v241 offset:1024\nds_read_b64_tr_b16 v[194:195],v241 offset:1280\nM v[20:23],a[20:23],v[212:215],v[20:23]\nds_read_b64_tr_b16 v[196:197],v241 offset:9216\n" \
"ds_read_b64_tr_b16 v[198:199],v241 offset:9472\nM v[80:83],a[8:11],v[208:211],0\nv_cvt_pk_bf16_f32 v134,v24,v25\nv_cvt_pk_bf16_f32 v135,v26,v27\nM v[80:83],a[24:27],v[212:215],v[80:83]\nv_cvt_pk_bf16_f32 v150,v28,v29\nv_cvt_pk_bf16_f32 v151,v30,v31\nM v[84:87],a[12:15],v[208:211],0\n" \
"v_cvt_pk_bf16_f32 v166,v88,v89\nv_cvt_pk_bf16_f32 v167,v90,v91\nM v[84:87],a[28:31],v[212:215],v[84:87]\nv_cvt_pk_bf16_f32 v182,v92,v93\ns_waitcnt lgkmcnt(6)\nM v[24:27],a[0:3],v[216:219],0\nv_cvt_pk_bf16_f32 v183,v94,v95\ns_waitcnt lgkmcnt(4)\n" \
"M v[24:27],a[16:19],v[220:223],v[24:27]\nds_read_b64_tr_b16 v[200:201],v242 offset:1024\nds_read_b64_tr_b16 v[202:203],v242 offset:1280\nM v[28:31],a[4:7],v[216:219],0\nds_read_b64_tr_b16 v[204:205],v242 offset:9216\nds_read_b64_tr_b16 v[206:207],v242 offset:9472\nM v[28:31],a[20:23],v[220:223],v[28:31]\nv_cvt_pk_bf16_f32 v136,v32,v33\n" \
"v_cvt_pk_bf16_f32 v137,v34,v35\nM v[88:91],a[8:11],v[216:219],0\nv_cvt_pk_bf16_f32 v152,v36,v37\nv_cvt_pk_bf16_f32 v153,v38,v39\nM v[88:91],a[24:27],v[220:223],v[88:91]\nv_cvt_pk_bf16_f32 v168,v96,v97\nv_cvt_pk_bf16_f32 v169,v98,v99\nM v[92:95],a[12:15],v[216:219],0\n" \
"s_add_u32 m0,s64,131072\ns_add_u32 s69,s68,1048576\nM v[92:95],a[28:31],v[220:223],v[92:95]\nv_cvt_pk_bf16_f32 v184,v100,v101\ns_waitcnt lgkmcnt(4)\nM v[32:35],a[0:3],v[192:195],0\nv_cvt_pk_bf16_f32 v185,v102,v103\nds_read_b64_tr_b16 v[208:209],v241 offset:1536\n" \
"M v[32:35],a[16:19],v[196:199],v[32:35]\nds_read_b64_tr_b16 v[210:211],v241 offset:1792\nds_read_b64_tr_b16 v[212:213],v241 offset:9728\nM v[36:39],a[4:7],v[192:195],0\nds_read_b64_tr_b16 v[214:215],v241 offset:9984\nv_cvt_pk_bf16_f32 v138,v40,v41\nM v[36:39],a[20:23],v[196:199],v[36:39]\nv_cvt_pk_bf16_f32 v139,v42,v43\n" \
"v_cvt_pk_bf16_f32 v154,v44,v45\nM v[96:99],a[8:11],v[192:195],0\nv_cvt_pk_bf16_f32 v155,v46,v47\nds_read_b64_tr_b16 v[216:217],v242 offset:1536\nM v[96:99],a[24:27],v[196:199],v[96:99]\nds_read_b64_tr_b16 v[218:219],v242 offset:1792\nds_read_b64_tr_b16 v[220:221],v242 offset:9728\nM v[100:103],a[12:15],v[192:195],0\n" \
"ds_read_b64_tr_b16 v[222:223],v242 offset:9984\nv_cvt_pk_bf16_f32 v170,v104,v105\nM v[100:103],a[28:31],v[196:199],v[100:103]\nv_cvt_pk_bf16_f32 v171,v106,v107\ns_waitcnt lgkmcnt(8)\nM v[40:43],a[0:3],v[200:203],0\nv_cvt_pk_bf16_f32 v186,v108,v109\nv_cvt_pk_bf16_f32 v187,v110,v111\n" \
"M v[40:43],a[16:19],v[204:207],v[40:43]\nv_cvt_pk_bf16_f32 v140,v48,v49\nv_cvt_pk_bf16_f32 v141,v50,v51\nM v[44:47],a[4:7],v[200:203],0\nv_cvt_pk_bf16_f32 v156,v52,v53\nv_cvt_pk_bf16_f32 v157,v54,v55\nM v[44:47],a[20:23],v[204:207],v[44:47]\nv_cvt_pk_bf16_f32 v172,v112,v113\n" \
"v_cvt_pk_bf16_f32 v173,v114,v115\nM v[104:107],a[8:11],v[200:203],0\nv_cvt_pk_bf16_f32 v188,v116,v117\nv_cvt_pk_bf16_f32 v189,v118,v119\nM v[104:107],a[24:27],v[204:207],v[104:107]\nv_cvt_pk_bf16_f32 v142,v56,v57\nv_cvt_pk_bf16_f32 v143,v58,v59\nM v[108:111],a[12:15],v[200:203],0\n" \
"v_cvt_pk_bf16_f32 v158,v60,v61\nv_cvt_pk_bf16_f32 v159,v62,v63\nM v[108:111],a[28:31],v[204:207],v[108:111]\nv_cvt_pk_bf16_f32 v174,v120,v121\ns_waitcnt lgkmcnt(2)\nM v[48:51],a[0:3],v[208:211],0\nv_cvt_pk_bf16_f32 v175,v122,v123\nv_cvt_pk_bf16_f32 v190,v124,v125\n" \
"M v[48:51],a[16:19],v[212:215],v[48:51]\nv_cvt_pk_bf16_f32 v191,v126,v127\ns_lshl_b32 s70,s62,5\nM v[52:55],a[4:7],v[208:211],0\ns_add_u32 s70,s70,s63\ns_lshl2_add_u32 s70,s70,s7\nM v[52:55],a[20:23],v[212:215],v[52:55]\ns_lshl2_add_u32 s79,s70,90112\n" \
"s_lshl_b32 s70,s60,4\nM v[112:115],a[8:11],v[208:211],0\ns_waitcnt vmcnt(1) lgkmcnt(0)\ns_barrier\nM v[112:115],a[24:27],v[212:215],v[112:115]\nds_read_b64_tr_b16 v[192:193],v241 offset:16384\nds_read_b64_tr_b16 v[194:195],v241 offset:16640\nM v[116:119],a[12:15],v[208:211],0\n" \
"ds_read_b64_tr_b16 v[196:197],v241 offset:24576\nds_read_b64_tr_b16 v[198:199],v241 offset:24832\nM v[116:119],a[28:31],v[212:215],v[116:119]\nds_read_b64_tr_b16 v[200:201],v242 offset:16384\nds_read_b64_tr_b16 v[202:203],v242 offset:16640\nM v[56:59],a[0:3],v[216:219],0\nds_read_b64_tr_b16 v[204:205],v242 offset:24576\nds_read_b64_tr_b16 v[206:207],v242 offset:24832\n" \
"M v[56:59],a[16:19],v[220:223],v[56:59]\nds_read_b64_tr_b16 v[208:209],v241 offset:16896\nds_read_b64_tr_b16 v[210:211],v241 offset:17152\nM v[60:63],a[4:7],v[216:219],0\nds_read_b64_tr_b16 v[212:213],v241 offset:25088\nds_read_b64_tr_b16 v[214:215],v241 offset:25344\nM v[60:63],a[20:23],v[220:223],v[60:63]\nbuffer_load_dwordx4 v243,s[40:43],s69 offen offset:0 lds\n" \
"M v[120:123],a[8:11],v[216:219],0\ns_sub_u32 s70,s70,s62\ns_add_u32 s70,s70,15\nM v[120:123],a[24:27],v[220:223],v[120:123]\ns_lshl2_add_u32 s70,s70,s7\ns_lshl_b32 s70,s70,15\nM v[124:127],a[12:15],v[216:219],0\nds_read_b64_tr_b16 v[216:217],v242 offset:16896\n" \
"ds_read_b64_tr_b16 v[218:219],v242 offset:17152\nM v[124:127],a[28:31],v[220:223],v[124:127]\ns_lshl_b32 s71,s61,8\ns_waitcnt lgkmcnt(12)\nM v[0:3],a[32:35],v[192:195],v[0:3]\nds_read_b64_tr_b16 v[220:221],v242 offset:25088\ns_waitcnt lgkmcnt(11)\nM v[0:3],a[48:51],v[196:199],v[0:3]\n" \
"buffer_load_dwordx4 v244,s[40:43],s69 offen offset:1024 lds\nM v[4:7],a[36:39],v[192:195],v[4:7]\nds_read_b64_tr_b16 v[222:223],v242 offset:25344\ns_add_u32 s70,s70,s71\nM v[4:7],a[52:55],v[196:199],v[4:7]\ns_add_u32 s78,s70,54525952\ns_lshl3_add_u32 s70,s62,s7\nM v[64:67],a[40:43],v[192:195],v[64:67]\n" \
"s_lshl_b32 s76,s70,15\ns_lshl_b32 s80,s70,6\nM v[64:67],a[56:59],v[196:199],v[64:67]\nbuffer_load_dwordx4 v245,s[40:43],s69 offen offset:2048 lds\nM v[68:71],a[44:47],v[192:195],v[68:71]\ns_add_u32 s76,s76,1048576\ns_add_u32 s77,s76,131072\nM v[68:71],a[60:63],v[196:199],v[68:71]\n" \
"s_lshl_b32 s70,s94,12\ns_waitcnt lgkmcnt(4)\nM v[8:11],a[32:35],v[200:203],v[8:11]\ns_or_b32 s76,s76,s70\ns_or_b32 s77,s77,s70\nM v[8:11],a[48:51],v[204:207],v[8:11]\nbuffer_load_dwordx4 v246,s[40:43],s69 offen offset:3072 lds\nM v[12:15],a[36:39],v[200:203],v[12:15]\n" \
"s_lshl_b32 s70,s63,5\ns_add_u32 s70,s70,s74\nM v[12:15],a[52:55],v[204:207],v[12:15]\ns_lshl_b32 s74,s70,14\ns_add_u32 s74,s74,3145728\nM v[72:75],a[40:43],v[200:203],v[72:75]\nds_read_b64_tr_b16 v[192:193],v241 offset:17408\nds_read_b64_tr_b16 v[194:195],v241 offset:17664\n" \
"M v[72:75],a[56:59],v[204:207],v[72:75]\nds_read_b64_tr_b16 v[196:197],v241 offset:25600\nds_read_b64_tr_b16 v[198:199],v241 offset:25856\nM v[76:79],a[44:47],v[200:203],v[76:79]\nds_read_b64_tr_b16 v[200:201],v242 offset:17408\nds_read_b64_tr_b16 v[202:203],v242 offset:17664\nM v[76:79],a[60:63],v[204:207],v[76:79]\nds_read_b64_tr_b16 v[204:205],v242 offset:25600\n" \
"ds_read_b64_tr_b16 v[206:207],v242 offset:25856\nM v[16:19],a[32:35],v[208:211],v[16:19]\nbuffer_store_dwordx4 v[128:131],v240,s[52:55],s74 offen offset:0 sc1\nM v[16:19],a[48:51],v[212:215],v[16:19]\ns_add_u32 m0,s64,147456\ns_add_u32 s69,s68,1572864\nM v[20:23],a[36:39],v[208:211],v[20:23]\nv_lshlrev_b32 v228,2,v228\n" \
"v_lshl_or_b32 v228,v229,4,v228\nM v[20:23],a[52:55],v[212:215],v[20:23]\ns_add_u32 s80,s80,90112\ns_lshl2_add_u32 s29,s70,81920\nM v[80:83],a[40:43],v[208:211],v[80:83]\nbuffer_load_dwordx4 v243,s[40:43],s69 offen offset:0 lds\nM v[80:83],a[56:59],v[212:215],v[80:83]\ns_lshl_b32 s70,s7,14\n" \
"v_add_u32 v250,s70,v240\nM v[84:87],a[44:47],v[208:211],v[84:87]\nds_read_b64_tr_b16 v[208:209],v241 offset:17920\nds_read_b64_tr_b16 v[210:211],v241 offset:18176\nM v[84:87],a[60:63],v[212:215],v[84:87]\nds_read_b64_tr_b16 v[212:213],v241 offset:26112\ns_waitcnt lgkmcnt(8)\nM v[24:27],a[32:35],v[216:219],v[24:27]\n" \
"ds_read_b64_tr_b16 v[214:215],v241 offset:26368\ns_lshl_b32 s70,s63,19\nM v[24:27],a[48:51],v[220:223],v[24:27]\nbuffer_load_dwordx4 v244,s[40:43],s69 offen offset:1024 lds\nM v[28:31],a[36:39],v[216:219],v[28:31]\nds_write_b64 v250,v[128:129] offset:0\ns_add_u32 s72,s78,16384\nM v[28:31],a[52:55],v[220:223],v[28:31]\n" \
"ds_write_b64 v250,v[130:131] offset:8\ns_mul_i32 s71,s60,14\nM v[88:91],a[40:43],v[216:219],v[88:91]\nds_write_b64 v250,v[132:133] offset:1024\nv_and_b32 v229,15,v249\nM v[88:91],a[56:59],v[220:223],v[88:91]\nbuffer_load_dwordx4 v245,s[40:43],s69 offen offset:2048 lds\nM v[92:95],a[44:47],v[216:219],v[92:95]\n" \
"ds_read_b64_tr_b16 v[216:217],v242 offset:17920\nds_read_b64_tr_b16 v[218:219],v242 offset:18176\nM v[92:95],a[60:63],v[220:223],v[92:95]\nds_read_b64_tr_b16 v[220:221],v242 offset:26112\nds_read_b64_tr_b16 v[222:223],v242 offset:26368\nM v[32:35],a[32:35],v[192:195],v[32:35]\nv_lshlrev_b32 v229,7,v229\ns_waitcnt lgkmcnt(10)\n" \
"M v[32:35],a[48:51],v[196:199],v[32:35]\nbuffer_load_dwordx4 v246,s[40:43],s69 offen offset:3072 lds\nM v[36:39],a[36:39],v[192:195],v[36:39]\ns_add_u32 m0,s64,98304\ns_add_u32 s69,s68,2097152\nM v[36:39],a[52:55],v[196:199],v[36:39]\nds_write_b64 v250,v[134:135] offset:1032\nv_add_u32 v253,0xffd00000,v253\n" \
"M v[96:99],a[40:43],v[192:195],v[96:99]\ns_add_u32 s44,s34,s70\ns_addc_u32 s45,s35,0\nM v[96:99],a[56:59],v[196:199],v[96:99]\nbuffer_store_dwordx4 v[132:135],v240,s[52:55],s74 offen offset:1024 sc1\nM v[100:103],a[44:47],v[192:195],v[100:103]\ns_lshl_b32 s70,s62,15\ns_add_u32 s96,s70,s64\n" \
"M v[100:103],a[60:63],v[196:199],v[100:103]\ns_sub_u32 s96,s96,16384\ns_sub_u32 s46,491520,s70\nM v[40:43],a[32:35],v[200:203],v[40:43]\ns_cmp_eq_u32 s36,s96\ns_cselect_b32 s72,0xc000,s98\ns_add_u32 s65,s36,s72\nM v[40:43],a[48:51],v[204:207],v[40:43]\n" \
"buffer_load_dwordx4 v243,s[40:43],s69 offen offset:0 lds\nM v[44:47],a[36:39],v[200:203],v[44:47]\ns_cmp_eq_u32 s65,s96\ns_cselect_b32 s72,0xc000,s98\ns_add_u32 s66,s65,s72\nM v[44:47],a[52:55],v[204:207],v[44:47]\ns_lshl2_add_u32 s70,s62,0\ns_sub_u32 s70,s70,s7\n" \
"M v[104:107],a[40:43],v[200:203],v[104:107]\ns_lshl_b32 s70,s70,16\ns_add_u32 s33,s78,s70\nM v[104:107],a[56:59],v[204:207],v[104:107]\nbuffer_load_dwordx4 v244,s[40:43],s69 offen offset:1024 lds\nM v[108:111],a[44:47],v[200:203],v[108:111]\nds_write_b64 v250,v[136:137] offset:2048\ns_lshl3_add_u32 s70,s62,s7\n" \
"M v[108:111],a[60:63],v[204:207],v[108:111]\ns_cmp_eq_u32 s66,s96\ns_cselect_b32 s72,0xc000,s98\ns_add_u32 s95,s66,s72\ns_waitcnt lgkmcnt(1)\nM v[48:51],a[32:35],v[208:211],v[48:51]\ns_cmp_eq_u32 s62,7\ns_cselect_b32 s46,229376,s46\n" \
"M v[48:51],a[48:51],v[212:215],v[48:51]\ns_waitcnt vmcnt(12)\nbuffer_store_dword v252,off,s[52:55],s79 sc1\nM v[52:55],a[36:39],v[208:211],v[52:55]\nbuffer_load_dwordx4 v245,s[40:43],s69 offen offset:2048 lds\nM v[52:55],a[52:55],v[212:215],v[52:55]\ns_lshl3_add_u32 s70,s70,s60\ns_lshl_b32 s67,s70,15\n" \
"M v[112:115],a[40:43],v[208:211],v[112:115]\ns_waitcnt vmcnt(10)\ns_barrier\nM v[112:115],a[56:59],v[212:215],v[112:115]\nds_read_b64_tr_b16 v[192:193],v241 offset:32768\nds_read_b64_tr_b16 v[194:195],v241 offset:33024\nM v[116:119],a[44:47],v[208:211],v[116:119]\nds_read_b64_tr_b16 v[196:197],v241 offset:40960\n" \
"ds_read_b64_tr_b16 v[198:199],v241 offset:41216\nM v[116:119],a[60:63],v[212:215],v[116:119]\nbuffer_store_dwordx4 v[136:139],v240,s[52:55],s74 offen offset:2048 sc1\nM v[56:59],a[32:35],v[216:219],v[56:59]\nds_read_b64_tr_b16 v[200:201],v242 offset:32768\nds_read_b64_tr_b16 v[202:203],v242 offset:33024\nM v[56:59],a[48:51],v[220:223],v[56:59]\nds_read_b64_tr_b16 v[204:205],v242 offset:40960\n" \
"ds_read_b64_tr_b16 v[206:207],v242 offset:41216\nM v[60:63],a[36:39],v[216:219],v[60:63]\nds_read_b64_tr_b16 v[208:209],v241 offset:33280\nds_read_b64_tr_b16 v[210:211],v241 offset:33536\nM v[60:63],a[52:55],v[220:223],v[60:63]\nds_read_b64_tr_b16 v[212:213],v241 offset:41472\nds_read_b64_tr_b16 v[214:215],v241 offset:41728\nM v[120:123],a[40:43],v[216:219],v[120:123]\n" \
"buffer_load_dwordx4 v246,s[40:43],s69 offen offset:3072 lds\nM v[120:123],a[56:59],v[220:223],v[120:123]\ns_add_u32 m0,s64,114688\ns_add_u32 s69,s68,2621440\nM v[124:127],a[44:47],v[216:219],v[124:127]\nds_read_b64_tr_b16 v[216:217],v242 offset:33280\nds_read_b64_tr_b16 v[218:219],v242 offset:33536\nM v[124:127],a[60:63],v[220:223],v[124:127]\n" \
"ds_read_b64_tr_b16 v[220:221],v242 offset:41472\ns_waitcnt lgkmcnt(13)\nM v[0:3],a[64:67],v[192:195],v[0:3]\nds_read_b64_tr_b16 v[222:223],v242 offset:41728\ns_waitcnt lgkmcnt(12)\nM v[0:3],a[80:83],v[196:199],v[0:3]\nbuffer_load_dwordx4 v243,s[40:43],s69 offen offset:0 lds\nM v[4:7],a[68:71],v[192:195],v[4:7]\n" \
"ds_write_b64 v250,v[138:139] offset:2056\ns_add_u32 s15,s64,s46\nM v[4:7],a[84:87],v[196:199],v[4:7]\ns_cmp_gt_i32 s62,-1\ns_cselect_b32 s72,s98,0xfffe4000\nM v[64:67],a[72:75],v[192:195],v[64:67]\ns_lshl3_add_u32 s70,s62,s60\ns_add_u32 s15,s15,s72\n" \
"M v[64:67],a[88:91],v[196:199],v[64:67]\nbuffer_load_dwordx4 v244,s[40:43],s69 offen offset:1024 lds\nM v[68:71],a[76:79],v[192:195],v[68:71]\nds_read_b64_tr_b16 v[192:193],v241 offset:33792\nds_read_b64_tr_b16 v[194:195],v241 offset:34048\nM v[68:71],a[92:95],v[196:199],v[68:71]\nds_read_b64_tr_b16 v[196:197],v241 offset:41984\ns_waitcnt lgkmcnt(8)\n" \
"M v[8:11],a[64:67],v[200:203],v[8:11]\nds_read_b64_tr_b16 v[198:199],v241 offset:42240\ns_lshl2_add_u32 s70,s70,s7\nM v[8:11],a[80:83],v[204:207],v[8:11]\nbuffer_store_dwordx4 v[140:143],v240,s[52:55],s74 offen offset:3072 sc1\nM v[12:15],a[68:71],v[200:203],v[12:15]\ns_add_u32 s74,s74,4096\ns_lshl4_add_u32 s79,s70,65536\n" \
"M v[12:15],a[84:87],v[204:207],v[12:15]\nds_write_b64 v250,v[140:141] offset:3072\ns_add_u32 s97,s96,65536\nM v[72:75],a[72:75],v[200:203],v[72:75]\ns_cmp_eq_u32 s62,7\ns_cselect_b32 s72,98304,0\nM v[72:75],a[88:91],v[204:207],v[72:75]\nbuffer_load_dwordx4 v245,s[40:43],s69 offen offset:2048 lds\n" \
"M v[76:79],a[76:79],v[200:203],v[76:79]\nds_read_b64_tr_b16 v[200:201],v242 offset:33792\nds_read_b64_tr_b16 v[202:203],v242 offset:34048\nM v[76:79],a[92:95],v[204:207],v[76:79]\nds_read_b64_tr_b16 v[204:205],v242 offset:41984\nds_read_b64_tr_b16 v[206:207],v242 offset:42240\nM v[16:19],a[64:67],v[208:211],v[16:19]\ns_lshr_b32 s70,s6,3\n" \
"s_sub_u32 s97,s97,s72\nM v[16:19],a[80:83],v[212:215],v[16:19]\nbuffer_load_dwordx4 v246,s[40:43],s69 offen offset:3072 lds\nM v[20:23],a[68:71],v[208:211],v[20:23]\nds_write_b64 v250,v[142:143] offset:3080\ns_sub_u32 s70,s70,16\nM v[20:23],a[84:87],v[212:215],v[20:23]\ns_add_u32 s2,s70,s71\n" \
"s_lshl_b32 s72,s60,1\nM v[80:83],a[72:75],v[208:211],v[80:83]\ns_mul_i32 s71,s60,448\ns_add_u32 s72,s72,s62\nM v[80:83],a[88:91],v[212:215],v[80:83]\ns_and_b32 s71,s71,0xffffff80\ns_sub_u32 s72,s72,6\nM v[84:87],a[76:79],v[208:211],v[84:87]\n" \
"ds_read_b64_tr_b16 v[208:209],v241 offset:34304\nds_read_b64_tr_b16 v[210:211],v241 offset:34560\nM v[84:87],a[92:95],v[212:215],v[84:87]\nds_read_b64_tr_b16 v[212:213],v241 offset:42496\ns_waitcnt lgkmcnt(8)\nM v[24:27],a[64:67],v[216:219],v[24:27]\nbuffer_store_dwordx4 v[144:147],v240,s[52:55],s74 offen offset:0 sc1\nM v[24:27],a[80:83],v[220:223],v[24:27]\n" \
"ds_read_b64_tr_b16 v[214:215],v241 offset:42752\ns_sub_u32 s58,1835008,s71\nM v[28:31],a[68:71],v[216:219],v[28:31]\ns_lshl_b32 s72,s72,18\ns_lshl_b32 s2,s2,5\nM v[28:31],a[84:87],v[220:223],v[28:31]\ns_cmp_ge_u32 s62,6\ns_cselect_b64 s[56:57],s[18:19],s[56:57]\n" \
"s_cselect_b32 s71,s72,s71\ns_cselect_b32 s58,0x40000,s58\ns_cselect_b32 s0,2048,s0\nM v[88:91],a[72:75],v[216:219],v[88:91]\ns_mul_i32 s72,s60,229376\nv_lshrrev_b32 v253,4,v249\nM v[88:91],a[88:91],v[220:223],v[88:91]\ns_cmp_eq_u32 s62,3\n" \
"s_cselect_b64 s[56:57],s[26:27],s[56:57]\ns_cselect_b32 s71,s72,s71\ns_cselect_b32 s58,229376,s58\ns_cselect_b32 s0,4096,s0\nM v[92:95],a[76:79],v[216:219],v[92:95]\nds_read_b64_tr_b16 v[216:217],v242 offset:34304\nds_read_b64_tr_b16 v[218:219],v242 offset:34560\nM v[92:95],a[92:95],v[220:223],v[92:95]\n" \
"ds_read_b64_tr_b16 v[220:221],v242 offset:42496\nds_read_b64_tr_b16 v[222:223],v242 offset:42752\nM v[32:35],a[64:67],v[192:195],v[32:35]\nds_write_b64 v250,v[144:145] offset:4096\nv_and_b32 v251,0xf0,v240\nM v[32:35],a[80:83],v[196:199],v[32:35]\ns_cmp_lt_u32 s70,14\ns_cselect_b32 s2,s2,0\n" \
"s_cselect_b32 s3,-1,0\nM v[36:39],a[68:71],v[192:195],v[36:39]\ns_lshl2_add_u32 s70,s61,s7\ns_mul_i32 s1,s70,s0\nM v[36:39],a[84:87],v[196:199],v[36:39]\ns_add_u32 s48,s44,19922944\ns_addc_u32 s49,s45,0\nM v[96:99],a[72:75],v[192:195],v[96:99]\n" \
"v_lshl_or_b32 v253,v253,12,v251\ns_lshl_b32 s70,s61,8\nM v[96:99],a[88:91],v[196:199],v[96:99]\ns_add_u32 s44,s44,3145728\ns_addc_u32 s45,s45,0\nM v[100:103],a[76:79],v[192:195],v[100:103]\ns_mov_b32 s50,s46\ns_mov_b32 s94,0xc000\n" \
"M v[100:103],a[92:95],v[196:199],v[100:103]\ns_sub_u32 s33,s33,1867776\ns_waitcnt lgkmcnt(5)\nM v[40:43],a[64:67],v[200:203],v[40:43]\ns_add_u32 s67,s67,36700160\ns_lshl_b32 s0,s0,4\nM v[40:43],a[80:83],v[204:207],v[40:43]\ns_cmp_lt_u32 s62,3\n" \
"s_cselect_b32 s58,0,s58\nM v[44:47],a[68:71],v[200:203],v[44:47]\nbuffer_store_dwordx4 v[148:151],v240,s[52:55],s74 offen offset:1024 sc1\nM v[44:47],a[84:87],v[204:207],v[44:47]\nds_write_b64 v250,v[146:147] offset:4104\nM v[104:107],a[72:75],v[200:203],v[104:107]\ns_add_u32 s56,s56,s71\ns_addc_u32 s57,s57,0\n" \
"M v[104:107],a[88:91],v[204:207],v[104:107]\nM v[108:111],a[76:79],v[200:203],v[108:111]\nM v[108:111],a[92:95],v[204:207],v[108:111]\nM v[48:51],a[64:67],v[208:211],v[48:51]\nM v[48:51],a[80:83],v[212:215],v[48:51]\nM v[52:55],a[68:71],v[208:211],v[52:55]\ns_add_u32 m0,s64,131072\ns_add_u32 s69,s68,3145728\n" \
"M v[52:55],a[84:87],v[212:215],v[52:55]\nM v[112:115],a[72:75],v[208:211],v[112:115]\ns_waitcnt vmcnt(14) lgkmcnt(1)\ns_barrier\nM v[112:115],a[88:91],v[212:215],v[112:115]\nds_read_b64_tr_b16 v[192:193],v241 offset:49152\nds_read_b64_tr_b16 v[194:195],v241 offset:49408\nM v[116:119],a[76:79],v[208:211],v[116:119]\n" \
"ds_read_b64_tr_b16 v[196:197],v241 offset:57344\nds_read_b64_tr_b16 v[198:199],v241 offset:57600\nM v[116:119],a[92:95],v[212:215],v[116:119]\nds_read_b64_tr_b16 v[200:201],v242 offset:49152\nds_read_b64_tr_b16 v[202:203],v242 offset:49408\nM v[56:59],a[64:67],v[216:219],v[56:59]\nds_read_b64_tr_b16 v[204:205],v242 offset:57344\nds_read_b64_tr_b16 v[206:207],v242 offset:57600\n" \
"M v[56:59],a[80:83],v[220:223],v[56:59]\nds_read_b64_tr_b16 v[208:209],v241 offset:49664\nds_read_b64_tr_b16 v[210:211],v241 offset:49920\nM v[60:63],a[68:71],v[216:219],v[60:63]\nds_read_b64_tr_b16 v[212:213],v241 offset:57856\nds_read_b64_tr_b16 v[214:215],v241 offset:58112\nM v[60:63],a[84:87],v[220:223],v[60:63]\nbuffer_load_dwordx4 v243,s[40:43],s69 offen offset:0 lds\n" \
"M v[120:123],a[72:75],v[216:219],v[120:123]\nds_write_b64 v250,v[148:149] offset:5120\nM v[120:123],a[88:91],v[220:223],v[120:123]\nM v[124:127],a[76:79],v[216:219],v[124:127]\nds_read_b64_tr_b16 v[216:217],v242 offset:49664\nds_read_b64_tr_b16 v[218:219],v242 offset:49920\nM v[124:127],a[92:95],v[220:223],v[124:127]\nds_read_b64_tr_b16 v[220:221],v242 offset:57856\n" \
"s_waitcnt lgkmcnt(14)\nM v[0:3],a[96:99],v[192:195],v[0:3]\nbuffer_store_dwordx4 v[152:155],v240,s[52:55],s74 offen offset:2048 sc1\ns_waitcnt lgkmcnt(12)\nM v[0:3],a[112:115],v[196:199],v[0:3]\nds_read_b64_tr_b16 v[222:223],v242 offset:58112\nM v[4:7],a[100:103],v[192:195],v[4:7]\nds_write_b64 v250,v[150:151] offset:5128\n" \
"M v[4:7],a[116:119],v[196:199],v[4:7]\nM v[64:67],a[104:107],v[192:195],v[64:67]\nbuffer_load_dwordx4 v244,s[40:43],s69 offen offset:1024 lds\nM v[64:67],a[120:123],v[196:199],v[64:67]\nM v[68:71],a[108:111],v[192:195],v[68:71]\nds_read_b64_tr_b16 v[192:193],v241 offset:50176\nds_read_b64_tr_b16 v[194:195],v241 offset:50432\nM v[68:71],a[124:127],v[196:199],v[68:71]\n" \
"ds_read_b64_tr_b16 v[196:197],v241 offset:58368\ns_waitcnt lgkmcnt(9)\nM v[8:11],a[96:99],v[200:203],v[8:11]\nbuffer_load_dwordx4 v245,s[40:43],s69 offen offset:2048 lds\nM v[8:11],a[112:115],v[204:207],v[8:11]\nds_read_b64_tr_b16 v[198:199],v241 offset:58624\nM v[12:15],a[100:103],v[200:203],v[12:15]\nM v[12:15],a[116:119],v[204:207],v[12:15]\n" \
"M v[72:75],a[104:107],v[200:203],v[72:75]\nbuffer_load_dwordx4 v246,s[40:43],s69 offen offset:3072 lds\nM v[72:75],a[120:123],v[204:207],v[72:75]\nM v[76:79],a[108:111],v[200:203],v[76:79]\nds_read_b64_tr_b16 v[200:201],v242 offset:50176\nds_read_b64_tr_b16 v[202:203],v242 offset:50432\nM v[76:79],a[124:127],v[204:207],v[76:79]\nds_read_b64_tr_b16 v[204:205],v242 offset:58368\n" \
"ds_read_b64_tr_b16 v[206:207],v242 offset:58624\nM v[16:19],a[96:99],v[208:211],v[16:19]\nbuffer_store_dwordx4 v[156:159],v240,s[52:55],s74 offen offset:3072 sc1\nM v[16:19],a[112:115],v[212:215],v[16:19]\nds_write_b64 v250,v[152:153] offset:6144\ns_add_u32 s74,s74,4096\nM v[20:23],a[100:103],v[208:211],v[20:23]\nM v[20:23],a[116:119],v[212:215],v[20:23]\n" \
"M v[80:83],a[104:107],v[208:211],v[80:83]\nM v[80:83],a[120:123],v[212:215],v[80:83]\nM v[84:87],a[108:111],v[208:211],v[84:87]\nds_read_b64_tr_b16 v[208:209],v241 offset:50688\nds_read_b64_tr_b16 v[210:211],v241 offset:50944\nM v[84:87],a[124:127],v[212:215],v[84:87]\nds_read_b64_tr_b16 v[212:213],v241 offset:58880\ns_waitcnt lgkmcnt(8)\n" \
"M v[24:27],a[96:99],v[216:219],v[24:27]\nds_read_b64_tr_b16 v[214:215],v241 offset:59136\nM v[24:27],a[112:115],v[220:223],v[24:27]\nM v[28:31],a[100:103],v[216:219],v[28:31]\nds_write_b64 v250,v[154:155] offset:6152\nM v[28:31],a[116:119],v[220:223],v[28:31]\nM v[88:91],a[104:107],v[216:219],v[88:91]\nM v[88:91],a[120:123],v[220:223],v[88:91]\n" \
"M v[92:95],a[108:111],v[216:219],v[92:95]\nds_read_b64_tr_b16 v[216:217],v242 offset:50688\nds_read_b64_tr_b16 v[218:219],v242 offset:50944\nM v[92:95],a[124:127],v[220:223],v[92:95]\nds_read_b64_tr_b16 v[220:221],v242 offset:58880\nds_read_b64_tr_b16 v[222:223],v242 offset:59136\nM v[32:35],a[96:99],v[192:195],v[32:35]\nbuffer_store_dwordx4 v[160:163],v240,s[52:55],s74 offen offset:0 sc1\n" \
"M v[32:35],a[112:115],v[196:199],v[32:35]\nM v[36:39],a[100:103],v[192:195],v[36:39]\nM v[36:39],a[116:119],v[196:199],v[36:39]\nM v[96:99],a[104:107],v[192:195],v[96:99]\nM v[96:99],a[120:123],v[196:199],v[96:99]\nM v[100:103],a[108:111],v[192:195],v[100:103]\nds_write_b64 v250,v[156:157] offset:7168\nM v[100:103],a[124:127],v[196:199],v[100:103]\n" \
"s_waitcnt lgkmcnt(7)\nM v[40:43],a[96:99],v[200:203],v[40:43]\nM v[40:43],a[112:115],v[204:207],v[40:43]\nM v[44:47],a[100:103],v[200:203],v[44:47]\nM v[44:47],a[116:119],v[204:207],v[44:47]\nM v[104:107],a[104:107],v[200:203],v[104:107]\nM v[104:107],a[120:123],v[204:207],v[104:107]\nM v[108:111],a[108:111],v[200:203],v[108:111]\n" \
"M v[108:111],a[124:127],v[204:207],v[108:111]\nM v[48:51],a[96:99],v[208:211],v[48:51]\ns_waitcnt lgkmcnt(3)\nM v[48:51],a[112:115],v[212:215],v[48:51]\nM v[52:55],a[100:103],v[208:211],v[52:55]\nbuffer_store_dwordx4 v[164:167],v240,s[52:55],s74 offen offset:1024 sc1\nM v[52:55],a[116:119],v[212:215],v[52:55]\ns_add_u32 m0,s64,147456\n" \
"s_add_u32 s69,s68,3670016\nM v[112:115],a[104:107],v[208:211],v[112:115]\ns_waitcnt vmcnt(15) lgkmcnt(1)\ns_barrier\nM v[112:115],a[120:123],v[212:215],v[112:115]\nds_read_b64_tr_b16 v[192:193],v241 offset:0\nds_read_b64_tr_b16 v[194:195],v241 offset:256\nM v[116:119],a[108:111],v[208:211],v[116:119]\n" \
"ds_read_b64_tr_b16 v[196:197],v241 offset:8192\nds_read_b64_tr_b16 v[198:199],v241 offset:8448\nM v[116:119],a[124:127],v[212:215],v[116:119]\nds_read_b64_tr_b16 v[200:201],v242 offset:0\nds_read_b64_tr_b16 v[202:203],v242 offset:256\nM v[56:59],a[96:99],v[216:219],v[56:59]\nds_read_b64_tr_b16 v[204:205],v242 offset:8192\nds_read_b64_tr_b16 v[206:207],v242 offset:8448\n" \
"M v[56:59],a[112:115],v[220:223],v[56:59]\nds_read_b64_tr_b16 v[208:209],v241 offset:512\nds_read_b64_tr_b16 v[210:211],v241 offset:768\nM v[60:63],a[100:103],v[216:219],v[60:63]\nds_read_b64_tr_b16 v[212:213],v241 offset:8704\nds_read_b64_tr_b16 v[214:215],v241 offset:8960\nM v[60:63],a[116:119],v[220:223],v[60:63]\nbuffer_load_dwordx4 v243,s[40:43],s69 offen offset:0 lds\n" \
"M v[120:123],a[104:107],v[216:219],v[120:123]\nds_write_b64 v250,v[158:159] offset:7176\ns_lshl_b32 s68,s60,19\nM v[120:123],a[120:123],v[220:223],v[120:123]\ns_lshl2_add_u32 s68,s64,s68\ns_add_u32 s68,s68,s70\nM v[124:127],a[108:111],v[216:219],v[124:127]\nds_read_b64_tr_b16 v[216:217],v242 offset:512\n" \
"ds_read_b64_tr_b16 v[218:219],v242 offset:768\nM v[124:127],a[124:127],v[220:223],v[124:127]\nds_read_b64_tr_b16 v[220:221],v242 offset:8704\ns_waitcnt lgkmcnt(14)\nM v[0:3],a[128:131],v[192:195],v[0:3]\nds_read_b64_tr_b16 v[222:223],v242 offset:8960\ns_waitcnt lgkmcnt(13)\nM v[0:3],a[144:147],v[196:199],v[0:3]\n" \
"buffer_load_dwordx4 v244,s[40:43],s69 offen offset:1024 lds\nM v[4:7],a[132:135],v[192:195],v[4:7]\nds_write_b64 v250,v[160:161] offset:8192\nM v[4:7],a[148:151],v[196:199],v[4:7]\nM v[64:67],a[136:139],v[192:195],v[64:67]\nM v[64:67],a[152:155],v[196:199],v[64:67]\nbuffer_store_dwordx4 v[168:171],v240,s[52:55],s74 offen offset:2048 sc1\nM v[68:71],a[140:143],v[192:195],v[68:71]\n" \
"ds_read_b64_tr_b16 v[192:193],v241 offset:1024\nds_read_b64_tr_b16 v[194:195],v241 offset:1280\nM v[68:71],a[156:159],v[196:199],v[68:71]\nds_read_b64_tr_b16 v[196:197],v241 offset:9216\ns_waitcnt lgkmcnt(9)\nM v[8:11],a[128:131],v[200:203],v[8:11]\nds_read_b64_tr_b16 v[198:199],v241 offset:9472\nM v[8:11],a[144:147],v[204:207],v[8:11]\n" \
"buffer_load_dwordx4 v245,s[40:43],s69 offen offset:2048 lds\nM v[12:15],a[132:135],v[200:203],v[12:15]\nds_write_b64 v250,v[162:163] offset:8200\nM v[12:15],a[148:151],v[204:207],v[12:15]\nM v[72:75],a[136:139],v[200:203],v[72:75]\nM v[72:75],a[152:155],v[204:207],v[72:75]\nbuffer_load_dwordx4 v246,s[40:43],s69 offen offset:3072 lds\nM v[76:79],a[140:143],v[200:203],v[76:79]\n" \
"ds_read_b64_tr_b16 v[200:201],v242 offset:1024\nds_read_b64_tr_b16 v[202:203],v242 offset:1280\nM v[76:79],a[156:159],v[204:207],v[76:79]\nds_read_b64_tr_b16 v[204:205],v242 offset:9216\nds_read_b64_tr_b16 v[206:207],v242 offset:9472\nM v[16:19],a[128:131],v[208:211],v[16:19]\nM v[16:19],a[144:147],v[212:215],v[16:19]\nM v[20:23],a[132:135],v[208:211],v[20:23]\n" \
"M v[20:23],a[148:151],v[212:215],v[20:23]\nM v[80:83],a[136:139],v[208:211],v[80:83]\nM v[80:83],a[152:155],v[212:215],v[80:83]\nM v[84:87],a[140:143],v[208:211],v[84:87]\nbuffer_load_dword v248,v228,s[52:55],s80 offen sc1\nM v[84:87],a[156:159],v[212:215],v[84:87]\nds_read_b64_tr_b16 v[208:209],v241 offset:1536\ns_waitcnt lgkmcnt(6)\n" \
"M v[24:27],a[128:131],v[216:219],v[24:27]\nbuffer_store_dwordx4 v[172:175],v240,s[52:55],s74 offen offset:3072 sc1\nM v[24:27],a[144:147],v[220:223],v[24:27]\nds_read_b64_tr_b16 v[210:211],v241 offset:1792\nds_read_b64_tr_b16 v[212:213],v241 offset:9728\nM v[28:31],a[132:135],v[216:219],v[28:31]\nds_read_b64_tr_b16 v[214:215],v241 offset:9984\ns_add_u32 s74,s74,4096\n" \
"M v[28:31],a[148:151],v[220:223],v[28:31]\nds_write_b64 v250,v[164:165] offset:9216\nM v[88:91],a[136:139],v[216:219],v[88:91]\nM v[88:91],a[152:155],v[220:223],v[88:91]\nM v[92:95],a[140:143],v[216:219],v[92:95]\nds_read_b64_tr_b16 v[216:217],v242 offset:1536\nds_read_b64_tr_b16 v[218:219],v242 offset:1792\nM v[92:95],a[156:159],v[220:223],v[92:95]\n" \
"ds_read_b64_tr_b16 v[220:221],v242 offset:9728\nds_read_b64_tr_b16 v[222:223],v242 offset:9984\nM v[32:35],a[128:131],v[192:195],v[32:35]\nM v[32:35],a[144:147],v[196:199],v[32:35]\nM v[36:39],a[132:135],v[192:195],v[36:39]\nds_write_b64 v250,v[166:167] offset:9224\nM v[36:39],a[148:151],v[196:199],v[36:39]\nM v[96:99],a[136:139],v[192:195],v[96:99]\n" \
"M v[96:99],a[152:155],v[196:199],v[96:99]\nM v[100:103],a[140:143],v[192:195],v[100:103]\nM v[100:103],a[156:159],v[196:199],v[100:103]\ns_waitcnt lgkmcnt(9)\nM v[40:43],a[128:131],v[200:203],v[40:43]\nbuffer_store_dwordx4 v[176:179],v240,s[52:55],s74 offen offset:0 sc1\nM v[40:43],a[144:147],v[204:207],v[40:43]\nM v[44:47],a[132:135],v[200:203],v[44:47]\n" \
"M v[44:47],a[148:151],v[204:207],v[44:47]\nM v[104:107],a[136:139],v[200:203],v[104:107]\nM v[104:107],a[152:155],v[204:207],v[104:107]\nM v[108:111],a[140:143],v[200:203],v[108:111]\nds_write_b64 v250,v[168:169] offset:10240\nM v[108:111],a[156:159],v[204:207],v[108:111]\ns_waitcnt lgkmcnt(4)\nM v[48:51],a[128:131],v[208:211],v[48:51]\n" \
"M v[48:51],a[144:147],v[212:215],v[48:51]\nM v[52:55],a[132:135],v[208:211],v[52:55]\ns_sub_u32 s40,s12,0x1000\ns_subb_u32 s41,s13,0\nM v[52:55],a[148:151],v[212:215],v[52:55]\ns_add_u32 m0,s64,98304\ns_xor_b32 s69,s99,0\nM v[112:115],a[136:139],v[208:211],v[112:115]\n" \
"s_waitcnt vmcnt(18) lgkmcnt(1)\ns_barrier\nM v[112:115],a[152:155],v[212:215],v[112:115]\nds_read_b64_tr_b16 v[192:193],v241 offset:16384\nds_read_b64_tr_b16 v[194:195],v241 offset:16640\nM v[116:119],a[140:143],v[208:211],v[116:119]\nds_read_b64_tr_b16 v[196:197],v241 offset:24576\nds_read_b64_tr_b16 v[198:199],v241 offset:24832\n" \
"M v[116:119],a[156:159],v[212:215],v[116:119]\nds_read_b64_tr_b16 v[200:201],v242 offset:16384\nds_read_b64_tr_b16 v[202:203],v242 offset:16640\nM v[56:59],a[128:131],v[216:219],v[56:59]\nds_read_b64_tr_b16 v[204:205],v242 offset:24576\nds_read_b64_tr_b16 v[206:207],v242 offset:24832\nM v[56:59],a[144:147],v[220:223],v[56:59]\nds_read_b64_tr_b16 v[208:209],v241 offset:16896\n" \
"ds_read_b64_tr_b16 v[210:211],v241 offset:17152\nM v[60:63],a[132:135],v[216:219],v[60:63]\nbuffer_store_dwordx4 v[180:183],v240,s[52:55],s74 offen offset:1024 sc1\nM v[60:63],a[148:151],v[220:223],v[60:63]\nds_read_b64_tr_b16 v[212:213],v241 offset:25088\nds_read_b64_tr_b16 v[214:215],v241 offset:25344\nM v[120:123],a[136:139],v[216:219],v[120:123]\nds_write_b64 v250,v[170:171] offset:10248\n" \
"s_lshl_b32 s12,s61,13\nM v[120:123],a[152:155],v[220:223],v[120:123]\nM v[124:127],a[140:143],v[216:219],v[124:127]\nds_read_b64_tr_b16 v[216:217],v242 offset:16896\nds_read_b64_tr_b16 v[218:219],v242 offset:17152\nM v[124:127],a[156:159],v[220:223],v[124:127]\nds_read_b64_tr_b16 v[220:221],v242 offset:25088\ns_waitcnt lgkmcnt(14)\n" \
"M v[0:3],a[160:163],v[192:195],v[0:3]\nds_read_b64_tr_b16 v[222:223],v242 offset:25344\ns_waitcnt lgkmcnt(13)\nM v[0:3],a[176:179],v[196:199],v[0:3]\nbuffer_load_dwordx4 v243,s[40:43],s69 offen offset:0 lds\nM v[4:7],a[164:167],v[192:195],v[4:7]\nM v[4:7],a[180:183],v[196:199],v[4:7]\nM v[64:67],a[168:171],v[192:195],v[64:67]\n" \
"M v[64:67],a[184:187],v[196:199],v[64:67]\nbuffer_load_dwordx4 v244,s[40:43],s69 offen offset:1024 lds\nM v[68:71],a[172:175],v[192:195],v[68:71]\nds_read_b64_tr_b16 v[192:193],v241 offset:17408\nds_read_b64_tr_b16 v[194:195],v241 offset:17664\nM v[68:71],a[188:191],v[196:199],v[68:71]\nds_read_b64_tr_b16 v[196:197],v241 offset:25600\ns_waitcnt lgkmcnt(9)\n" \
"M v[8:11],a[160:163],v[200:203],v[8:11]\nds_write_b64 v250,v[172:173] offset:11264\nds_read_b64_tr_b16 v[198:199],v241 offset:25856\nM v[8:11],a[176:179],v[204:207],v[8:11]\nbuffer_load_dwordx4 v245,s[40:43],s69 offen offset:2048 lds\nM v[12:15],a[164:167],v[200:203],v[12:15]\nM v[12:15],a[180:183],v[204:207],v[12:15]\nM v[72:75],a[168:171],v[200:203],v[72:75]\n" \
"M v[72:75],a[184:187],v[204:207],v[72:75]\nbuffer_store_dwordx4 v[184:187],v240,s[52:55],s74 offen offset:2048 sc1\nM v[76:79],a[172:175],v[200:203],v[76:79]\ns_waitcnt vmcnt(7)\nv_cmp_eq_u32 vcc,0,v248\nM v[76:79],a[188:191],v[204:207],v[76:79]\ns_cbranch_vccnz .Lps0\n.Lpb0:\n" \
"ds_read_b64_tr_b16 v[200:201],v242 offset:17408\nM v[16:19],a[160:163],v[208:211],v[16:19]\nds_read_b64_tr_b16 v[202:203],v242 offset:17664\ns_waitcnt lgkmcnt(7)\nM v[16:19],a[176:179],v[212:215],v[16:19]\nds_read_b64_tr_b16 v[204:205],v242 offset:25600\nds_read_b64_tr_b16 v[206:207],v242 offset:25856\nM v[20:23],a[164:167],v[208:211],v[20:23]\n" \
"buffer_load_dwordx4 a[0:3],v240,s[52:55],s76 offen offset:0 sc1\nM v[20:23],a[180:183],v[212:215],v[20:23]\nds_write_b64 v250,v[174:175] offset:11272\nv_and_b32 v228,31,v249\nM v[80:83],a[168:171],v[208:211],v[80:83]\ns_lshl_b32 s80,s63,7\nv_lshlrev_b32 v228,2,v228\nM v[80:83],a[184:187],v[212:215],v[80:83]\n" \
"s_add_u32 s80,s80,81920\nv_mov_b32 v251,v228\nM v[84:87],a[172:175],v[208:211],v[84:87]\nds_read_b64_tr_b16 v[208:209],v241 offset:17920\nds_read_b64_tr_b16 v[210:211],v241 offset:18176\nM v[84:87],a[188:191],v[212:215],v[84:87]\nds_read_b64_tr_b16 v[212:213],v241 offset:26112\nds_read_b64_tr_b16 v[214:215],v241 offset:26368\n" \
"M v[24:27],a[160:163],v[216:219],v[24:27]\nbuffer_load_dwordx4 a[16:19],v240,s[52:55],s76 offen offset:2048 sc1\nM v[24:27],a[176:179],v[220:223],v[24:27]\nM v[28:31],a[164:167],v[216:219],v[28:31]\nM v[28:31],a[180:183],v[220:223],v[28:31]\nM v[88:91],a[168:171],v[216:219],v[88:91]\nbuffer_load_dwordx4 a[4:7],v240,s[52:55],s76 offen offset:1024 sc1\nM v[88:91],a[184:187],v[220:223],v[88:91]\n" \
"M v[92:95],a[172:175],v[216:219],v[92:95]\nds_read_b64_tr_b16 v[216:217],v242 offset:17920\nds_read_b64_tr_b16 v[218:219],v242 offset:18176\nM v[92:95],a[188:191],v[220:223],v[92:95]\nds_read_b64_tr_b16 v[220:221],v242 offset:26112\ns_waitcnt lgkmcnt(7)\nM v[32:35],a[160:163],v[192:195],v[32:35]\nbuffer_store_dwordx4 v[188:191],v240,s[52:55],s74 offen offset:3072 sc1\n" \
"M v[32:35],a[176:179],v[196:199],v[32:35]\nds_read_b64_tr_b16 v[222:223],v242 offset:26368\ns_add_u32 s74,s74,16764928\nM v[36:39],a[164:167],v[192:195],v[36:39]\nds_write_b64 v250,v[176:177] offset:12288\nM v[36:39],a[180:183],v[196:199],v[36:39]\nM v[96:99],a[168:171],v[192:195],v[96:99]\nbuffer_load_dwordx4 a[20:23],v240,s[52:55],s76 offen offset:3072 sc1\n" \
"M v[96:99],a[184:187],v[196:199],v[96:99]\ns_xor_b32 s76,s76,4096\nM v[100:103],a[172:175],v[192:195],v[100:103]\nM v[100:103],a[188:191],v[196:199],v[100:103]\nM v[40:43],a[160:163],v[200:203],v[40:43]\nbuffer_load_dwordx4 a[8:11],v240,s[52:55],s77 offen offset:0 sc1\nM v[40:43],a[176:179],v[204:207],v[40:43]\nM v[44:47],a[164:167],v[200:203],v[44:47]\n" \
"ds_write_b64 v250,v[178:179] offset:12296\nM v[44:47],a[180:183],v[204:207],v[44:47]\nM v[104:107],a[168:171],v[200:203],v[104:107]\nbuffer_load_dwordx4 a[24:27],v240,s[52:55],s77 offen offset:2048 sc1\nM v[104:107],a[184:187],v[204:207],v[104:107]\nM v[108:111],a[172:175],v[200:203],v[108:111]\nM v[108:111],a[188:191],v[204:207],v[108:111]\ns_waitcnt lgkmcnt(3)\n" \
"M v[48:51],a[160:163],v[208:211],v[48:51]\nbuffer_load_dwordx4 a[12:15],v240,s[52:55],s77 offen offset:1024 sc1\nM v[48:51],a[176:179],v[212:215],v[48:51]\nM v[52:55],a[164:167],v[208:211],v[52:55]\nM v[52:55],a[180:183],v[212:215],v[52:55]\nM v[112:115],a[168:171],v[208:211],v[112:115]\ns_waitcnt lgkmcnt(1)\ns_barrier\n" \
"M v[112:115],a[184:187],v[212:215],v[112:115]\nds_read_b64_tr_b16 v[192:193],v241 offset:32768\nds_read_b64_tr_b16 v[194:195],v241 offset:33024\nM v[116:119],a[172:175],v[208:211],v[116:119]\nds_read_b64_tr_b16 v[196:197],v241 offset:40960\nds_read_b64_tr_b16 v[198:199],v241 offset:41216\nM v[116:119],a[188:191],v[212:215],v[116:119]\nds_read_b64_tr_b16 v[200:201],v242 offset:32768\n" \
"ds_read_b64_tr_b16 v[202:203],v242 offset:33024\nM v[56:59],a[160:163],v[216:219],v[56:59]\nds_read_b64_tr_b16 v[204:205],v242 offset:40960\nds_read_b64_tr_b16 v[206:207],v242 offset:41216\nM v[56:59],a[176:179],v[220:223],v[56:59]\nds_read_b64_tr_b16 v[208:209],v241 offset:33280\nds_read_b64_tr_b16 v[210:211],v241 offset:33536\nM v[60:63],a[164:167],v[216:219],v[60:63]\n" \
"ds_read_b64_tr_b16 v[212:213],v241 offset:41472\nds_read_b64_tr_b16 v[214:215],v241 offset:41728\nM v[60:63],a[180:183],v[220:223],v[60:63]\nbuffer_load_dwordx4 a[28:31],v240,s[52:55],s77 offen offset:3072 sc1\nM v[120:123],a[168:171],v[216:219],v[120:123]\nds_write_b64 v250,v[180:181] offset:13312\ns_xor_b32 s77,s77,4096\nM v[120:123],a[184:187],v[220:223],v[120:123]\n" \
"M v[124:127],a[172:175],v[216:219],v[124:127]\nds_read_b64_tr_b16 v[216:217],v242 offset:33280\nds_read_b64_tr_b16 v[218:219],v242 offset:33536\nM v[124:127],a[188:191],v[220:223],v[124:127]\nds_read_b64_tr_b16 v[220:221],v242 offset:41472\ns_waitcnt lgkmcnt(14)\nM v[0:3],a[192:195],v[192:195],v[0:3]\nds_read_b64_tr_b16 v[222:223],v242 offset:41728\n" \
"s_waitcnt lgkmcnt(13)\nM v[0:3],a[208:211],v[196:199],v[0:3]\nbuffer_load_dwordx4 v246,s[40:43],s69 offen offset:3072 lds\nM v[4:7],a[196:199],v[192:195],v[4:7]\ns_add_u32 m0,s64,114688\ns_xor_b32 s69,s99,524288\nM v[4:7],a[212:215],v[196:199],v[4:7]\nds_write_b64 v250,v[182:183] offset:13320\n" \
"M v[64:67],a[200:203],v[192:195],v[64:67]\nM v[64:67],a[216:219],v[196:199],v[64:67]\nbuffer_load_dwordx4 a[32:35],v240,s[52:55],s76 offen offset:0 sc1\nM v[68:71],a[204:207],v[192:195],v[68:71]\nds_read_b64_tr_b16 v[192:193],v241 offset:33792\nds_read_b64_tr_b16 v[194:195],v241 offset:34048\nM v[68:71],a[220:223],v[196:199],v[68:71]\nds_read_b64_tr_b16 v[196:197],v241 offset:41984\n" \
"s_waitcnt lgkmcnt(9)\nM v[8:11],a[192:195],v[200:203],v[8:11]\nds_read_b64_tr_b16 v[198:199],v241 offset:42240\nM v[8:11],a[208:211],v[204:207],v[8:11]\nbuffer_load_dwordx4 a[48:51],v240,s[52:55],s76 offen offset:2048 sc1\nM v[12:15],a[196:199],v[200:203],v[12:15]\nM v[12:15],a[212:215],v[204:207],v[12:15]\nM v[72:75],a[200:203],v[200:203],v[72:75]\n" \
"M v[72:75],a[216:219],v[204:207],v[72:75]\nbuffer_load_dwordx4 a[36:39],v240,s[52:55],s76 offen offset:1024 sc1\nM v[76:79],a[204:207],v[200:203],v[76:79]\nds_read_b64_tr_b16 v[200:201],v242 offset:33792\nds_read_b64_tr_b16 v[202:203],v242 offset:34048\nM v[76:79],a[220:223],v[204:207],v[76:79]\nds_read_b64_tr_b16 v[204:205],v242 offset:41984\nds_read_b64_tr_b16 v[206:207],v242 offset:42240\n" \
"M v[16:19],a[192:195],v[208:211],v[16:19]\nds_write_b64 v250,v[184:185] offset:14336\nM v[16:19],a[208:211],v[212:215],v[16:19]\nbuffer_load_dwordx4 a[52:55],v240,s[52:55],s76 offen offset:3072 sc1\nM v[20:23],a[196:199],v[208:211],v[20:23]\ns_xor_b32 s76,s76,12288\nM v[20:23],a[212:215],v[212:215],v[20:23]\nM v[80:83],a[200:203],v[208:211],v[80:83]\n" \
"M v[80:83],a[216:219],v[212:215],v[80:83]\nbuffer_load_dwordx4 a[40:43],v240,s[52:55],s77 offen offset:0 sc1\nM v[84:87],a[204:207],v[208:211],v[84:87]\nds_read_b64_tr_b16 v[208:209],v241 offset:34304\nds_read_b64_tr_b16 v[210:211],v241 offset:34560\nM v[84:87],a[220:223],v[212:215],v[84:87]\nds_read_b64_tr_b16 v[212:213],v241 offset:42496\ns_waitcnt lgkmcnt(8)\n" \
"M v[24:27],a[192:195],v[216:219],v[24:27]\nds_read_b64_tr_b16 v[214:215],v241 offset:42752\nM v[24:27],a[208:211],v[220:223],v[24:27]\nbuffer_load_dwordx4 a[56:59],v240,s[52:55],s77 offen offset:2048 sc1\nM v[28:31],a[196:199],v[216:219],v[28:31]\nds_write_b64 v250,v[186:187] offset:14344\nM v[28:31],a[212:215],v[220:223],v[28:31]\nM v[88:91],a[200:203],v[216:219],v[88:91]\n" \
"M v[88:91],a[216:219],v[220:223],v[88:91]\nbuffer_load_dwordx4 a[44:47],v240,s[52:55],s77 offen offset:1024 sc1\nM v[92:95],a[204:207],v[216:219],v[92:95]\nds_read_b64_tr_b16 v[216:217],v242 offset:34304\nds_read_b64_tr_b16 v[218:219],v242 offset:34560\nM v[92:95],a[220:223],v[220:223],v[92:95]\nds_read_b64_tr_b16 v[220:221],v242 offset:42496\nds_read_b64_tr_b16 v[222:223],v242 offset:42752\n" \
"M v[32:35],a[192:195],v[192:195],v[32:35]\nM v[32:35],a[208:211],v[196:199],v[32:35]\nbuffer_load_dwordx4 a[60:63],v240,s[52:55],s77 offen offset:3072 sc1\nM v[36:39],a[196:199],v[192:195],v[36:39]\ns_xor_b32 s77,s77,12288\nM v[36:39],a[212:215],v[196:199],v[36:39]\nM v[96:99],a[200:203],v[192:195],v[96:99]\nM v[96:99],a[216:219],v[196:199],v[96:99]\n" \
"buffer_load_dwordx4 v243,s[40:43],s69 offen offset:0 lds\nM v[100:103],a[204:207],v[192:195],v[100:103]\nds_write_b64 v250,v[188:189] offset:15360\nM v[100:103],a[220:223],v[196:199],v[100:103]\ns_waitcnt lgkmcnt(6)\nM v[40:43],a[192:195],v[200:203],v[40:43]\nM v[40:43],a[208:211],v[204:207],v[40:43]\nbuffer_load_dwordx4 v244,s[40:43],s69 offen offset:1024 lds\n" \
"M v[44:47],a[196:199],v[200:203],v[44:47]\nM v[44:47],a[212:215],v[204:207],v[44:47]\nM v[104:107],a[200:203],v[200:203],v[104:107]\nM v[104:107],a[216:219],v[204:207],v[104:107]\nbuffer_load_dwordx4 v245,s[40:43],s69 offen offset:2048 lds\nM v[108:111],a[204:207],v[200:203],v[108:111]\nM v[108:111],a[220:223],v[204:207],v[108:111]\nM v[48:51],a[192:195],v[208:211],v[48:51]\n" \
"M v[48:51],a[208:211],v[212:215],v[48:51]\nbuffer_load_dwordx4 v246,s[40:43],s69 offen offset:3072 lds\nM v[52:55],a[196:199],v[208:211],v[52:55]\ns_add_u32 m0,s64,131072\ns_xor_b32 s69,s99,1048576\nM v[52:55],a[212:215],v[212:215],v[52:55]\nds_write_b64 v250,v[190:191] offset:15368\nv_add_u32 v250,65536,v250\n" \
"M v[112:115],a[200:203],v[208:211],v[112:115]\ns_waitcnt lgkmcnt(2)\ns_barrier\nM v[112:115],a[216:219],v[212:215],v[112:115]\nds_read_b64_tr_b16 v[192:193],v241 offset:49152\nds_read_b64_tr_b16 v[194:195],v241 offset:49408\nM v[116:119],a[204:207],v[208:211],v[116:119]\nds_read_b64_tr_b16 v[196:197],v241 offset:57344\n" \
"ds_read_b64_tr_b16 v[198:199],v241 offset:57600\nM v[116:119],a[220:223],v[212:215],v[116:119]\nds_read_b64_tr_b16 v[200:201],v242 offset:49152\nds_read_b64_tr_b16 v[202:203],v242 offset:49408\nM v[56:59],a[192:195],v[216:219],v[56:59]\nds_read_b64_tr_b16 v[204:205],v242 offset:57344\nds_read_b64_tr_b16 v[206:207],v242 offset:57600\nM v[56:59],a[208:211],v[220:223],v[56:59]\n" \
"ds_read_b64_tr_b16 v[208:209],v241 offset:49664\nds_read_b64_tr_b16 v[210:211],v241 offset:49920\nM v[60:63],a[196:199],v[216:219],v[60:63]\nds_read_b64_tr_b16 v[212:213],v241 offset:57856\nds_read_b64_tr_b16 v[214:215],v241 offset:58112\nM v[60:63],a[212:215],v[220:223],v[60:63]\nbuffer_load_dwordx4 a[64:67],v240,s[52:55],s76 offen offset:0 sc1\nM v[120:123],a[200:203],v[216:219],v[120:123]\n" \
"M v[120:123],a[216:219],v[220:223],v[120:123]\nM v[124:127],a[204:207],v[216:219],v[124:127]\nds_read_b64_tr_b16 v[216:217],v242 offset:49664\nds_read_b64_tr_b16 v[218:219],v242 offset:49920\nM v[124:127],a[220:223],v[220:223],v[124:127]\nds_read_b64_tr_b16 v[220:221],v242 offset:57856\ns_waitcnt lgkmcnt(13)\nM v[0:3],a[224:227],v[192:195],v[0:3]\n" \
"ds_read_b64_tr_b16 v[222:223],v242 offset:58112\ns_waitcnt lgkmcnt(12)\nM v[0:3],a[240:243],v[196:199],v[0:3]\nbuffer_load_dwordx4 a[68:71],v240,s[52:55],s76 offen offset:1024 sc1\nM v[4:7],a[228:231],v[192:195],v[4:7]\nM v[4:7],a[244:247],v[196:199],v[4:7]\nM v[64:67],a[232:235],v[192:195],v[64:67]\nM v[64:67],a[248:251],v[196:199],v[64:67]\n" \
"buffer_load_dwordx4 a[80:83],v240,s[52:55],s76 offen offset:2048 sc1\nM v[68:71],a[236:239],v[192:195],v[68:71]\nds_read_b64_tr_b16 v[192:193],v241 offset:50176\nds_read_b64_tr_b16 v[194:195],v241 offset:50432\nM v[68:71],a[252:255],v[196:199],v[68:71]\nds_read_b64_tr_b16 v[196:197],v241 offset:58368\ns_waitcnt lgkmcnt(7)\nM v[8:11],a[224:227],v[200:203],v[8:11]\n" \
"ds_read_b64_tr_b16 v[198:199],v241 offset:58624\nv_cvt_pk_bf16_f32 v128,v0,v1\nM v[8:11],a[240:243],v[204:207],v[8:11]\nv_cvt_pk_bf16_f32 v129,v2,v3\nv_cvt_pk_bf16_f32 v130,v4,v5\nM v[12:15],a[228:231],v[200:203],v[12:15]\nv_cvt_pk_bf16_f32 v131,v6,v7\nv_cvt_pk_bf16_f32 v160,v64,v65\n" \
"M v[12:15],a[244:247],v[204:207],v[12:15]\nv_cvt_pk_bf16_f32 v161,v66,v67\nv_cvt_pk_bf16_f32 v162,v68,v69\nM v[72:75],a[232:235],v[200:203],v[72:75]\nv_cvt_pk_bf16_f32 v163,v70,v71\nv_cvt_pk_bf16_f32 v132,v8,v9\nM v[72:75],a[248:251],v[204:207],v[72:75]\nbuffer_load_dwordx4 a[84:87],v240,s[52:55],s76 offen offset:3072 sc1\n" \
"M v[76:79],a[236:239],v[200:203],v[76:79]\nds_read_b64_tr_b16 v[200:201],v242 offset:50176\nds_read_b64_tr_b16 v[202:203],v242 offset:50432\nM v[76:79],a[252:255],v[204:207],v[76:79]\nds_read_b64_tr_b16 v[204:205],v242 offset:58368\nds_read_b64_tr_b16 v[206:207],v242 offset:58624\nM v[16:19],a[224:227],v[208:211],v[16:19]\nv_cvt_pk_bf16_f32 v133,v10,v11\n" \
"v_cvt_pk_bf16_f32 v134,v12,v13\nM v[16:19],a[240:243],v[212:215],v[16:19]\nv_cvt_pk_bf16_f32 v135,v14,v15\nv_cvt_pk_bf16_f32 v164,v72,v73\nM v[20:23],a[228:231],v[208:211],v[20:23]\nv_cvt_pk_bf16_f32 v165,v74,v75\nv_cvt_pk_bf16_f32 v166,v76,v77\nM v[20:23],a[244:247],v[212:215],v[20:23]\n" \
"v_cvt_pk_bf16_f32 v167,v78,v79\ns_xor_b32 s76,s76,4096\nM v[80:83],a[232:235],v[208:211],v[80:83]\nv_cvt_pk_bf16_f32 v136,v16,v17\nv_cvt_pk_bf16_f32 v137,v18,v19\nM v[80:83],a[248:251],v[212:215],v[80:83]\nbuffer_load_dwordx4 a[72:75],v240,s[52:55],s77 offen offset:0 sc1\nM v[84:87],a[236:239],v[208:211],v[84:87]\n" \
"ds_read_b64_tr_b16 v[208:209],v241 offset:50688\nds_read_b64_tr_b16 v[210:211],v241 offset:50944\nM v[84:87],a[252:255],v[212:215],v[84:87]\nds_read_b64_tr_b16 v[212:213],v241 offset:58880\ns_waitcnt lgkmcnt(7)\nM v[24:27],a[224:227],v[216:219],v[24:27]\nds_read_b64_tr_b16 v[214:215],v241 offset:59136\nv_cvt_pk_bf16_f32 v138,v20,v21\n" \
"M v[24:27],a[240:243],v[220:223],v[24:27]\nv_cvt_pk_bf16_f32 v139,v22,v23\nv_cvt_pk_bf16_f32 v168,v80,v81\nM v[28:31],a[228:231],v[216:219],v[28:31]\nv_cvt_pk_bf16_f32 v169,v82,v83\nv_cvt_pk_bf16_f32 v170,v84,v85\nM v[28:31],a[244:247],v[220:223],v[28:31]\nbuffer_load_dwordx4 a[88:91],v240,s[52:55],s77 offen offset:2048 sc1\n" \
"M v[88:91],a[232:235],v[216:219],v[88:91]\nv_cvt_pk_bf16_f32 v171,v86,v87\nv_cvt_pk_bf16_f32 v140,v24,v25\nM v[88:91],a[248:251],v[220:223],v[88:91]\nv_cvt_pk_bf16_f32 v141,v26,v27\nM v[92:95],a[236:239],v[216:219],v[92:95]\nds_read_b64_tr_b16 v[216:217],v242 offset:50688\nds_read_b64_tr_b16 v[218:219],v242 offset:50944\n" \
"M v[92:95],a[252:255],v[220:223],v[92:95]\nds_read_b64_tr_b16 v[220:221],v242 offset:58880\nds_read_b64_tr_b16 v[222:223],v242 offset:59136\nM v[32:35],a[224:227],v[192:195],v[32:35]\nv_cvt_pk_bf16_f32 v142,v28,v29\nv_cvt_pk_bf16_f32 v143,v30,v31\nM v[32:35],a[240:243],v[196:199],v[32:35]\nv_cvt_pk_bf16_f32 v172,v88,v89\n" \
"v_cvt_pk_bf16_f32 v173,v90,v91\nM v[36:39],a[228:231],v[192:195],v[36:39]\nv_cvt_pk_bf16_f32 v174,v92,v93\nv_cvt_pk_bf16_f32 v175,v94,v95\nM v[36:39],a[244:247],v[196:199],v[36:39]\nbuffer_load_dwordx4 a[76:79],v240,s[52:55],s77 offen offset:1024 sc1\nM v[96:99],a[232:235],v[192:195],v[96:99]\nv_cvt_pk_bf16_f32 v144,v32,v33\n" \
"v_cvt_pk_bf16_f32 v145,v34,v35\nM v[96:99],a[248:251],v[196:199],v[96:99]\nM v[100:103],a[236:239],v[192:195],v[100:103]\nM v[100:103],a[252:255],v[196:199],v[100:103]\ns_waitcnt vmcnt(20)\nbuffer_store_dword v252,off,s[52:55],s29 sc1\ns_waitcnt lgkmcnt(4)\nM v[40:43],a[224:227],v[200:203],v[40:43]\n" \
"v_cvt_pk_bf16_f32 v146,v36,v37\nv_cvt_pk_bf16_f32 v147,v38,v39\nM v[40:43],a[240:243],v[204:207],v[40:43]\nv_cvt_pk_bf16_f32 v176,v96,v97\nv_cvt_pk_bf16_f32 v177,v98,v99\nM v[44:47],a[228:231],v[200:203],v[44:47]\nv_cvt_pk_bf16_f32 v178,v100,v101\nv_cvt_pk_bf16_f32 v179,v102,v103\n" \
"M v[44:47],a[244:247],v[204:207],v[44:47]\nbuffer_load_dwordx4 a[92:95],v240,s[52:55],s77 offen offset:3072 sc1\nM v[104:107],a[232:235],v[200:203],v[104:107]\nv_cvt_pk_bf16_f32 v148,v40,v41\nv_cvt_pk_bf16_f32 v149,v42,v43\nM v[104:107],a[248:251],v[204:207],v[104:107]\ns_xor_b32 s77,s77,4096\ns_add_u32 s29,s29,4096\n" \
"M v[108:111],a[236:239],v[200:203],v[108:111]\nv_cvt_pk_bf16_f32 v150,v44,v45\nv_cvt_pk_bf16_f32 v151,v46,v47\nM v[108:111],a[252:255],v[204:207],v[108:111]\nbuffer_load_dwordx4 v243,s[40:43],s69 offen offset:0 lds\nM v[48:51],a[224:227],v[208:211],v[48:51]\nv_cvt_pk_bf16_f32 v180,v104,v105\nv_cvt_pk_bf16_f32 v181,v106,v107\n" \
"M v[48:51],a[240:243],v[212:215],v[48:51]\nM v[52:55],a[228:231],v[208:211],v[52:55]\nM v[52:55],a[244:247],v[212:215],v[52:55]\nbuffer_load_dwordx4 v244,s[40:43],s69 offen offset:1024 lds\nM v[112:115],a[232:235],v[208:211],v[112:115]\ns_waitcnt vmcnt(15) lgkmcnt(0)\ns_barrier\nM v[112:115],a[248:251],v[212:215],v[112:115]\n" \
"ds_read_b64_tr_b16 v[192:193],v241 offset:0\nds_read_b64_tr_b16 v[194:195],v241 offset:256\nM v[116:119],a[236:239],v[208:211],v[116:119]\nds_read_b64_tr_b16 v[196:197],v241 offset:8192\nds_read_b64_tr_b16 v[198:199],v241 offset:8448\nM v[116:119],a[252:255],v[212:215],v[116:119]\nds_read_b64_tr_b16 v[200:201],v242 offset:0\nds_read_b64_tr_b16 v[202:203],v242 offset:256\n" \
"M v[56:59],a[224:227],v[216:219],v[56:59]\nds_read_b64_tr_b16 v[204:205],v242 offset:8192\nds_read_b64_tr_b16 v[206:207],v242 offset:8448\nM v[56:59],a[240:243],v[220:223],v[56:59]\nds_read_b64_tr_b16 v[208:209],v241 offset:512\nds_read_b64_tr_b16 v[210:211],v241 offset:768\nM v[60:63],a[228:231],v[216:219],v[60:63]\nds_read_b64_tr_b16 v[212:213],v241 offset:8704\n" \
"ds_read_b64_tr_b16 v[214:215],v241 offset:8960\nM v[60:63],a[244:247],v[220:223],v[60:63]\nv_cvt_pk_bf16_f32 v182,v108,v109\nv_cvt_pk_bf16_f32 v183,v110,v111\nM v[120:123],a[232:235],v[216:219],v[120:123]\nv_cvt_pk_bf16_f32 v152,v48,v49\nv_cvt_pk_bf16_f32 v153,v50,v51\nM v[120:123],a[248:251],v[220:223],v[120:123]\n" \
"v_cvt_pk_bf16_f32 v154,v52,v53\nv_cvt_pk_bf16_f32 v155,v54,v55\nM v[124:127],a[236:239],v[216:219],v[124:127]\nds_read_b64_tr_b16 v[216:217],v242 offset:512\nds_read_b64_tr_b16 v[218:219],v242 offset:768\nM v[124:127],a[252:255],v[220:223],v[124:127]\nds_read_b64_tr_b16 v[220:221],v242 offset:8704\ns_waitcnt lgkmcnt(13)\n" \
"M v[0:3],v[192:195],a[0:3],0\nds_read_b64_tr_b16 v[222:223],v242 offset:8960\ns_waitcnt lgkmcnt(12)\nM v[0:3],v[196:199],a[16:19],v[0:3]\nv_cvt_pk_bf16_f32 v184,v112,v113\nv_cvt_pk_bf16_f32 v185,v114,v115\nM v[4:7],v[192:195],a[4:7],0\nv_cvt_pk_bf16_f32 v186,v116,v117\n" \
"v_cvt_pk_bf16_f32 v187,v118,v119\nM v[4:7],v[196:199],a[20:23],v[4:7]\nv_cvt_pk_bf16_f32 v156,v56,v57\nv_cvt_pk_bf16_f32 v157,v58,v59\nM v[64:67],v[192:195],a[8:11],0\nv_cvt_pk_bf16_f32 v158,v60,v61\nv_cvt_pk_bf16_f32 v159,v62,v63\nM v[64:67],v[196:199],a[24:27],v[64:67]\n" \
"v_cvt_pk_bf16_f32 v188,v120,v121\nv_cvt_pk_bf16_f32 v189,v122,v123\nM v[68:71],v[192:195],a[12:15],0\nds_read_b64_tr_b16 v[192:193],v241 offset:1024\nds_read_b64_tr_b16 v[194:195],v241 offset:1280\nM v[68:71],v[196:199],a[28:31],v[68:71]\nds_read_b64_tr_b16 v[196:197],v241 offset:9216\ns_waitcnt lgkmcnt(7)\n" \
"M v[8:11],v[200:203],a[0:3],0\nds_read_b64_tr_b16 v[198:199],v241 offset:9472\nv_cvt_pk_bf16_f32 v190,v124,v125\nM v[8:11],v[204:207],a[16:19],v[8:11]\nbuffer_load_dwordx4 v245,s[40:43],s69 offen offset:2048 lds\nM v[12:15],v[200:203],a[4:7],0\nv_cvt_pk_bf16_f32 v191,v126,v127\nM v[12:15],v[204:207],a[20:23],v[12:15]\n" \
"M v[72:75],v[200:203],a[8:11],0\nM v[72:75],v[204:207],a[24:27],v[72:75]\nbuffer_load_dwordx4 v246,s[40:43],s69 offen offset:3072 lds\nM v[76:79],v[200:203],a[12:15],0\nds_read_b64_tr_b16 v[200:201],v242 offset:1024\nds_read_b64_tr_b16 v[202:203],v242 offset:1280\nM v[76:79],v[204:207],a[28:31],v[76:79]\nds_read_b64_tr_b16 v[204:205],v242 offset:9216\n" \
"ds_read_b64_tr_b16 v[206:207],v242 offset:9472\nM v[16:19],v[208:211],a[0:3],0\ns_add_u32 m0,s64,147456\ns_xor_b32 s69,s99,1572864\nM v[16:19],v[212:215],a[16:19],v[16:19]\nbuffer_load_dwordx4 a[96:99],v240,s[52:55],s76 offen offset:0 sc1\nM v[20:23],v[208:211],a[4:7],0\nM v[20:23],v[212:215],a[20:23],v[20:23]\n" \
"M v[80:83],v[208:211],a[8:11],0\nM v[80:83],v[212:215],a[24:27],v[80:83]\nbuffer_store_dwordx4 v[128:131],v240,s[52:55],s74 offen offset:0 sc1\nM v[84:87],v[208:211],a[12:15],0\nds_read_b64_tr_b16 v[208:209],v241 offset:1536\nds_read_b64_tr_b16 v[210:211],v241 offset:1792\nM v[84:87],v[212:215],a[28:31],v[84:87]\nds_read_b64_tr_b16 v[212:213],v241 offset:9728\n" \
"s_waitcnt lgkmcnt(7)\nM v[24:27],v[216:219],a[0:3],0\nds_read_b64_tr_b16 v[214:215],v241 offset:9984\nM v[24:27],v[220:223],a[16:19],v[24:27]\nbuffer_load_dwordx4 a[112:115],v240,s[52:55],s76 offen offset:2048 sc1\nM v[28:31],v[216:219],a[4:7],0\nM v[28:31],v[220:223],a[20:23],v[28:31]\nM v[88:91],v[216:219],a[8:11],0\n" \
"M v[88:91],v[220:223],a[24:27],v[88:91]\nbuffer_load_dwordx4 a[100:103],v240,s[52:55],s76 offen offset:1024 sc1\nM v[92:95],v[216:219],a[12:15],0\nds_read_b64_tr_b16 v[216:217],v242 offset:1536\nds_read_b64_tr_b16 v[218:219],v242 offset:1792\nM v[92:95],v[220:223],a[28:31],v[92:95]\nds_read_b64_tr_b16 v[220:221],v242 offset:9728\nds_read_b64_tr_b16 v[222:223],v242 offset:9984\n" \
"M v[32:35],v[192:195],a[0:3],0\nM v[32:35],v[196:199],a[16:19],v[32:35]\nbuffer_load_dwordx4 a[116:119],v240,s[52:55],s76 offen offset:3072 sc1\nM v[36:39],v[192:195],a[4:7],0\ns_xor_b32 s76,s76,28672\nM v[36:39],v[196:199],a[20:23],v[36:39]\nM v[96:99],v[192:195],a[8:11],0\nM v[96:99],v[196:199],a[24:27],v[96:99]\n" \
"buffer_load_dwordx4 a[104:107],v240,s[52:55],s77 offen offset:0 sc1\nM v[100:103],v[192:195],a[12:15],0\nM v[100:103],v[196:199],a[28:31],v[100:103]\ns_waitcnt lgkmcnt(4)\nM v[40:43],v[200:203],a[0:3],0\nM v[40:43],v[204:207],a[16:19],v[40:43]\nbuffer_load_dwordx4 a[120:123],v240,s[52:55],s77 offen offset:2048 sc1\nM v[44:47],v[200:203],a[4:7],0\n" \
"M v[44:47],v[204:207],a[20:23],v[44:47]\nM v[104:107],v[200:203],a[8:11],0\nM v[104:107],v[204:207],a[24:27],v[104:107]\nbuffer_load_dwordx4 a[108:111],v240,s[52:55],s77 offen offset:1024 sc1\nM v[108:111],v[200:203],a[12:15],0\nM v[108:111],v[204:207],a[28:31],v[108:111]\nM v[48:51],v[208:211],a[0:3],0\nM v[48:51],v[212:215],a[16:19],v[48:51]\n" \
"buffer_load_dwordx4 a[124:127],v240,s[52:55],s77 offen offset:3072 sc1\nM v[52:55],v[208:211],a[4:7],0\ns_xor_b32 s77,s77,28672\nM v[52:55],v[212:215],a[20:23],v[52:55]\nM v[112:115],v[208:211],a[8:11],0\ns_waitcnt vmcnt(15) lgkmcnt(0)\ns_barrier\nM v[112:115],v[212:215],a[24:27],v[112:115]\n" \
"ds_read_b64_tr_b16 v[192:193],v241 offset:16384\nds_read_b64_tr_b16 v[194:195],v241 offset:16640\nM v[116:119],v[208:211],a[12:15],0\nds_read_b64_tr_b16 v[196:197],v241 offset:24576\nds_read_b64_tr_b16 v[198:199],v241 offset:24832\nbuffer_store_dwordx4 v[132:135],v240,s[52:55],s74 offen offset:1024 sc1\nM v[116:119],v[212:215],a[28:31],v[116:119]\nds_read_b64_tr_b16 v[200:201],v242 offset:16384\n" \
"ds_read_b64_tr_b16 v[202:203],v242 offset:16640\nM v[56:59],v[216:219],a[0:3],0\nds_read_b64_tr_b16 v[204:205],v242 offset:24576\nds_read_b64_tr_b16 v[206:207],v242 offset:24832\nM v[56:59],v[220:223],a[16:19],v[56:59]\nds_read_b64_tr_b16 v[208:209],v241 offset:16896\nds_read_b64_tr_b16 v[210:211],v241 offset:17152\nM v[60:63],v[216:219],a[4:7],0\n" \
"ds_read_b64_tr_b16 v[212:213],v241 offset:25088\nds_read_b64_tr_b16 v[214:215],v241 offset:25344\nM v[60:63],v[220:223],a[20:23],v[60:63]\nbuffer_load_dwordx4 v243,s[40:43],s69 offen offset:0 lds\nM v[120:123],v[216:219],a[8:11],0\nM v[120:123],v[220:223],a[24:27],v[120:123]\nM v[124:127],v[216:219],a[12:15],0\nds_read_b64_tr_b16 v[216:217],v242 offset:16896\n" \
"ds_read_b64_tr_b16 v[218:219],v242 offset:17152\nM v[124:127],v[220:223],a[28:31],v[124:127]\nds_read_b64_tr_b16 v[220:221],v242 offset:25088\ns_waitcnt lgkmcnt(13)\nM v[0:3],v[192:195],a[32:35],v[0:3]\nds_read_b64_tr_b16 v[222:223],v242 offset:25344\ns_waitcnt lgkmcnt(12)\nM v[0:3],v[196:199],a[48:51],v[0:3]\n" \
"buffer_load_dwordx4 v244,s[40:43],s69 offen offset:1024 lds\nM v[4:7],v[192:195],a[36:39],v[4:7]\nM v[4:7],v[196:199],a[52:55],v[4:7]\nM v[64:67],v[192:195],a[40:43],v[64:67]\nM v[64:67],v[196:199],a[56:59],v[64:67]\nbuffer_load_dwordx4 v245,s[40:43],s69 offen offset:2048 lds\nM v[68:71],v[192:195],a[44:47],v[68:71]\nds_read_b64_tr_b16 v[192:193],v241 offset:17408\n" \
"ds_read_b64_tr_b16 v[194:195],v241 offset:17664\nM v[68:71],v[196:199],a[60:63],v[68:71]\nds_read_b64_tr_b16 v[196:197],v241 offset:25600\ns_waitcnt lgkmcnt(7)\nM v[8:11],v[200:203],a[32:35],v[8:11]\nds_read_b64_tr_b16 v[198:199],v241 offset:25856\nM v[8:11],v[204:207],a[48:51],v[8:11]\nbuffer_load_dwordx4 v246,s[40:43],s69 offen offset:3072 lds\n" \
"M v[12:15],v[200:203],a[36:39],v[12:15]\ns_add_u32 m0,s64,98304\ns_xor_b32 s69,s99,2097152\nM v[12:15],v[204:207],a[52:55],v[12:15]\nM v[72:75],v[200:203],a[40:43],v[72:75]\nM v[72:75],v[204:207],a[56:59],v[72:75]\nbuffer_load_dwordx4 a[128:131],v240,s[52:55],s76 offen offset:0 sc1\nM v[76:79],v[200:203],a[44:47],v[76:79]\n" \
"ds_read_b64_tr_b16 v[200:201],v242 offset:17408\nds_read_b64_tr_b16 v[202:203],v242 offset:17664\nM v[76:79],v[204:207],a[60:63],v[76:79]\nds_read_b64_tr_b16 v[204:205],v242 offset:25600\nds_read_b64_tr_b16 v[206:207],v242 offset:25856\nM v[16:19],v[208:211],a[32:35],v[16:19]\nM v[16:19],v[212:215],a[48:51],v[16:19]\nbuffer_store_dwordx4 v[136:139],v240,s[52:55],s74 offen offset:2048 sc1\n" \
"M v[20:23],v[208:211],a[36:39],v[20:23]\nM v[20:23],v[212:215],a[52:55],v[20:23]\nM v[80:83],v[208:211],a[40:43],v[80:83]\nM v[80:83],v[212:215],a[56:59],v[80:83]\nbuffer_load_dwordx4 a[144:147],v240,s[52:55],s76 offen offset:2048 sc1\nM v[84:87],v[208:211],a[44:47],v[84:87]\nds_read_b64_tr_b16 v[208:209],v241 offset:17920\nds_read_b64_tr_b16 v[210:211],v241 offset:18176\n" \
"M v[84:87],v[212:215],a[60:63],v[84:87]\nds_read_b64_tr_b16 v[212:213],v241 offset:26112\ns_waitcnt lgkmcnt(7)\nM v[24:27],v[216:219],a[32:35],v[24:27]\nds_read_b64_tr_b16 v[214:215],v241 offset:26368\nM v[24:27],v[220:223],a[48:51],v[24:27]\nbuffer_load_dwordx4 a[132:135],v240,s[52:55],s76 offen offset:1024 sc1\nM v[28:31],v[216:219],a[36:39],v[28:31]\n" \
"M v[28:31],v[220:223],a[52:55],v[28:31]\nM v[88:91],v[216:219],a[40:43],v[88:91]\nM v[88:91],v[220:223],a[56:59],v[88:91]\nbuffer_load_dwordx4 a[148:151],v240,s[52:55],s76 offen offset:3072 sc1\nM v[92:95],v[216:219],a[44:47],v[92:95]\nds_read_b64_tr_b16 v[216:217],v242 offset:17920\nds_read_b64_tr_b16 v[218:219],v242 offset:18176\nM v[92:95],v[220:223],a[60:63],v[92:95]\n" \
"ds_read_b64_tr_b16 v[220:221],v242 offset:26112\nds_read_b64_tr_b16 v[222:223],v242 offset:26368\nM v[32:35],v[192:195],a[32:35],v[32:35]\ns_xor_b32 s76,s76,4096\nM v[32:35],v[196:199],a[48:51],v[32:35]\nbuffer_load_dwordx4 a[136:139],v240,s[52:55],s77 offen offset:0 sc1\nM v[36:39],v[192:195],a[36:39],v[36:39]\nM v[36:39],v[196:199],a[52:55],v[36:39]\n" \
"M v[96:99],v[192:195],a[40:43],v[96:99]\nM v[96:99],v[196:199],a[56:59],v[96:99]\nbuffer_load_dwordx4 a[152:155],v240,s[52:55],s77 offen offset:2048 sc1\nM v[100:103],v[192:195],a[44:47],v[100:103]\nM v[100:103],v[196:199],a[60:63],v[100:103]\ns_waitcnt lgkmcnt(4)\nM v[40:43],v[200:203],a[32:35],v[40:43]\nM v[40:43],v[204:207],a[48:51],v[40:43]\n" \
"buffer_load_dwordx4 a[140:143],v240,s[52:55],s77 offen offset:1024 sc1\nM v[44:47],v[200:203],a[36:39],v[44:47]\nM v[44:47],v[204:207],a[52:55],v[44:47]\nM v[104:107],v[200:203],a[40:43],v[104:107]\nM v[104:107],v[204:207],a[56:59],v[104:107]\nbuffer_load_dwordx4 a[156:159],v240,s[52:55],s77 offen offset:3072 sc1\nM v[108:111],v[200:203],a[44:47],v[108:111]\ns_xor_b32 s77,s77,4096\n" \
"M v[108:111],v[204:207],a[60:63],v[108:111]\nM v[48:51],v[208:211],a[32:35],v[48:51]\nM v[48:51],v[212:215],a[48:51],v[48:51]\nbuffer_store_dwordx4 v[140:143],v240,s[52:55],s74 offen offset:3072 sc1\nM v[52:55],v[208:211],a[36:39],v[52:55]\ns_add_u32 s74,s74,4096\nM v[52:55],v[212:215],a[52:55],v[52:55]\nM v[112:115],v[208:211],a[40:43],v[112:115]\n" \
"s_waitcnt vmcnt(23) lgkmcnt(0)\ns_barrier\nM v[112:115],v[212:215],a[56:59],v[112:115]\nds_read_b64_tr_b16 v[192:193],v241 offset:32768\nds_read_b64_tr_b16 v[194:195],v241 offset:33024\nM v[116:119],v[208:211],a[44:47],v[116:119]\nds_read_b64_tr_b16 v[196:197],v241 offset:40960\nds_read_b64_tr_b16 v[198:199],v241 offset:41216\n" \
"M v[116:119],v[212:215],a[60:63],v[116:119]\nds_read_b64_tr_b16 v[200:201],v242 offset:32768\nds_read_b64_tr_b16 v[202:203],v242 offset:33024\nM v[56:59],v[216:219],a[32:35],v[56:59]\nds_read_b64_tr_b16 v[204:205],v242 offset:40960\nds_read_b64_tr_b16 v[206:207],v242 offset:41216\nM v[56:59],v[220:223],a[48:51],v[56:59]\nds_read_b64_tr_b16 v[208:209],v241 offset:33280\n" \
"ds_read_b64_tr_b16 v[210:211],v241 offset:33536\nM v[60:63],v[216:219],a[36:39],v[60:63]\nds_read_b64_tr_b16 v[212:213],v241 offset:41472\nds_read_b64_tr_b16 v[214:215],v241 offset:41728\nM v[60:63],v[220:223],a[52:55],v[60:63]\nbuffer_load_dwordx4 v243,s[40:43],s69 offen offset:0 lds\nM v[120:123],v[216:219],a[40:43],v[120:123]\nM v[120:123],v[220:223],a[56:59],v[120:123]\n" \
"M v[124:127],v[216:219],a[44:47],v[124:127]\nds_read_b64_tr_b16 v[216:217],v242 offset:33280\nds_read_b64_tr_b16 v[218:219],v242 offset:33536\nM v[124:127],v[220:223],a[60:63],v[124:127]\nds_read_b64_tr_b16 v[220:221],v242 offset:41472\ns_waitcnt lgkmcnt(13)\nM v[0:3],v[192:195],a[64:67],v[0:3]\nds_read_b64_tr_b16 v[222:223],v242 offset:41728\n" \
"s_waitcnt lgkmcnt(12)\nM v[0:3],v[196:199],a[80:83],v[0:3]\nbuffer_load_dwordx4 v244,s[40:43],s69 offen offset:1024 lds\nM v[4:7],v[192:195],a[68:71],v[4:7]\nM v[4:7],v[196:199],a[84:87],v[4:7]\nM v[64:67],v[192:195],a[72:75],v[64:67]\nM v[64:67],v[196:199],a[88:91],v[64:67]\nbuffer_load_dwordx4 v245,s[40:43],s69 offen offset:2048 lds\n" \
"M v[68:71],v[192:195],a[76:79],v[68:71]\nds_read_b64_tr_b16 v[192:193],v241 offset:33792\nds_read_b64_tr_b16 v[194:195],v241 offset:34048\nM v[68:71],v[196:199],a[92:95],v[68:71]\nds_read_b64_tr_b16 v[196:197],v241 offset:41984\ns_waitcnt lgkmcnt(7)\nM v[8:11],v[200:203],a[64:67],v[8:11]\nds_read_b64_tr_b16 v[198:199],v241 offset:42240\n" \
"M v[8:11],v[204:207],a[80:83],v[8:11]\nbuffer_load_dwordx4 v246,s[40:43],s69 offen offset:3072 lds\nM v[12:15],v[200:203],a[68:71],v[12:15]\ns_add_u32 m0,s64,114688\ns_xor_b32 s69,s99,2621440\nM v[12:15],v[204:207],a[84:87],v[12:15]\nM v[72:75],v[200:203],a[72:75],v[72:75]\nM v[72:75],v[204:207],a[88:91],v[72:75]\n" \
"buffer_store_dwordx4 v[144:147],v240,s[52:55],s74 offen offset:0 sc1\nM v[76:79],v[200:203],a[76:79],v[76:79]\nds_read_b64_tr_b16 v[200:201],v242 offset:33792\nds_read_b64_tr_b16 v[202:203],v242 offset:34048\nM v[76:79],v[204:207],a[92:95],v[76:79]\nds_read_b64_tr_b16 v[204:205],v242 offset:41984\nds_read_b64_tr_b16 v[206:207],v242 offset:42240\nM v[16:19],v[208:211],a[64:67],v[16:19]\n" \
"M v[16:19],v[212:215],a[80:83],v[16:19]\nbuffer_load_dwordx4 a[160:163],v240,s[52:55],s76 offen offset:0 sc1\nM v[20:23],v[208:211],a[68:71],v[20:23]\nM v[20:23],v[212:215],a[84:87],v[20:23]\nM v[80:83],v[208:211],a[72:75],v[80:83]\nM v[80:83],v[212:215],a[88:91],v[80:83]\nbuffer_load_dwordx4 a[176:179],v240,s[52:55],s76 offen offset:2048 sc1\nM v[84:87],v[208:211],a[76:79],v[84:87]\n" \
"ds_read_b64_tr_b16 v[208:209],v241 offset:34304\nds_read_b64_tr_b16 v[210:211],v241 offset:34560\nM v[84:87],v[212:215],a[92:95],v[84:87]\nds_read_b64_tr_b16 v[212:213],v241 offset:42496\ns_waitcnt lgkmcnt(7)\nM v[24:27],v[216:219],a[64:67],v[24:27]\nds_read_b64_tr_b16 v[214:215],v241 offset:42752\nM v[24:27],v[220:223],a[80:83],v[24:27]\n" \
"buffer_load_dwordx4 a[164:167],v240,s[52:55],s76 offen offset:1024 sc1\nM v[28:31],v[216:219],a[68:71],v[28:31]\nM v[28:31],v[220:223],a[84:87],v[28:31]\nM v[88:91],v[216:219],a[72:75],v[88:91]\nM v[88:91],v[220:223],a[88:91],v[88:91]\nbuffer_load_dwordx4 a[180:183],v240,s[52:55],s76 offen offset:3072 sc1\nM v[92:95],v[216:219],a[76:79],v[92:95]\nds_read_b64_tr_b16 v[216:217],v242 offset:34304\n" \
"ds_read_b64_tr_b16 v[218:219],v242 offset:34560\nM v[92:95],v[220:223],a[92:95],v[92:95]\nds_read_b64_tr_b16 v[220:221],v242 offset:42496\nds_read_b64_tr_b16 v[222:223],v242 offset:42752\nM v[32:35],v[192:195],a[64:67],v[32:35]\ns_xor_b32 s76,s76,12288\nM v[32:35],v[196:199],a[80:83],v[32:35]\nbuffer_load_dwordx4 a[168:171],v240,s[52:55],s77 offen offset:0 sc1\n" \
"M v[36:39],v[192:195],a[68:71],v[36:39]\nM v[36:39],v[196:199],a[84:87],v[36:39]\nM v[96:99],v[192:195],a[72:75],v[96:99]\nM v[96:99],v[196:199],a[88:91],v[96:99]\nbuffer_load_dwordx4 a[184:187],v240,s[52:55],s77 offen offset:2048 sc1\nM v[100:103],v[192:195],a[76:79],v[100:103]\nM v[100:103],v[196:199],a[92:95],v[100:103]\ns_waitcnt lgkmcnt(4)\n" \
"M v[40:43],v[200:203],a[64:67],v[40:43]\nM v[40:43],v[204:207],a[80:83],v[40:43]\nbuffer_load_dwordx4 a[172:175],v240,s[52:55],s77 offen offset:1024 sc1\nM v[44:47],v[200:203],a[68:71],v[44:47]\nM v[44:47],v[204:207],a[84:87],v[44:47]\nM v[104:107],v[200:203],a[72:75],v[104:107]\nM v[104:107],v[204:207],a[88:91],v[104:107]\nbuffer_store_dwordx4 v[148:151],v240,s[52:55],s74 offen offset:1024 sc1\n" \
"M v[108:111],v[200:203],a[76:79],v[108:111]\nM v[108:111],v[204:207],a[92:95],v[108:111]\nM v[48:51],v[208:211],a[64:67],v[48:51]\nM v[48:51],v[212:215],a[80:83],v[48:51]\nbuffer_load_dwordx4 a[188:191],v240,s[52:55],s77 offen offset:3072 sc1\nM v[52:55],v[208:211],a[68:71],v[52:55]\ns_xor_b32 s77,s77,12288\nM v[52:55],v[212:215],a[84:87],v[52:55]\n" \
"M v[112:115],v[208:211],a[72:75],v[112:115]\ns_waitcnt vmcnt(23) lgkmcnt(0)\ns_barrier\nM v[112:115],v[212:215],a[88:91],v[112:115]\nds_read_b64_tr_b16 v[192:193],v241 offset:49152\nds_read_b64_tr_b16 v[194:195],v241 offset:49408\nM v[116:119],v[208:211],a[76:79],v[116:119]\nds_read_b64_tr_b16 v[196:197],v241 offset:57344\n" \
"ds_read_b64_tr_b16 v[198:199],v241 offset:57600\nM v[116:119],v[212:215],a[92:95],v[116:119]\nds_read_b64_tr_b16 v[200:201],v242 offset:49152\nds_read_b64_tr_b16 v[202:203],v242 offset:49408\nM v[56:59],v[216:219],a[64:67],v[56:59]\nds_read_b64_tr_b16 v[204:205],v242 offset:57344\nds_read_b64_tr_b16 v[206:207],v242 offset:57600\nM v[56:59],v[220:223],a[80:83],v[56:59]\n" \
"ds_read_b64_tr_b16 v[208:209],v241 offset:49664\nds_read_b64_tr_b16 v[210:211],v241 offset:49920\nM v[60:63],v[216:219],a[68:71],v[60:63]\nds_read_b64_tr_b16 v[212:213],v241 offset:57856\nds_read_b64_tr_b16 v[214:215],v241 offset:58112\nM v[60:63],v[220:223],a[84:87],v[60:63]\nbuffer_load_dwordx4 v243,s[40:43],s69 offen offset:0 lds\nM v[120:123],v[216:219],a[72:75],v[120:123]\n" \
"M v[120:123],v[220:223],a[88:91],v[120:123]\nM v[124:127],v[216:219],a[76:79],v[124:127]\nds_read_b64_tr_b16 v[216:217],v242 offset:49664\nds_read_b64_tr_b16 v[218:219],v242 offset:49920\nM v[124:127],v[220:223],a[92:95],v[124:127]\nds_read_b64_tr_b16 v[220:221],v242 offset:57856\ns_waitcnt lgkmcnt(13)\nM v[0:3],v[192:195],a[96:99],v[0:3]\n" \
"ds_read_b64_tr_b16 v[222:223],v242 offset:58112\ns_waitcnt lgkmcnt(12)\nM v[0:3],v[196:199],a[112:115],v[0:3]\nbuffer_load_dwordx4 v244,s[40:43],s69 offen offset:1024 lds\nM v[4:7],v[192:195],a[100:103],v[4:7]\nM v[4:7],v[196:199],a[116:119],v[4:7]\nM v[64:67],v[192:195],a[104:107],v[64:67]\nM v[64:67],v[196:199],a[120:123],v[64:67]\n" \
"buffer_load_dwordx4 v245,s[40:43],s69 offen offset:2048 lds\nM v[68:71],v[192:195],a[108:111],v[68:71]\nds_read_b64_tr_b16 v[192:193],v241 offset:50176\nds_read_b64_tr_b16 v[194:195],v241 offset:50432\nM v[68:71],v[196:199],a[124:127],v[68:71]\nds_read_b64_tr_b16 v[196:197],v241 offset:58368\ns_waitcnt lgkmcnt(7)\nM v[8:11],v[200:203],a[96:99],v[8:11]\n" \
"ds_read_b64_tr_b16 v[198:199],v241 offset:58624\nM v[8:11],v[204:207],a[112:115],v[8:11]\nbuffer_store_dwordx4 v[152:155],v240,s[52:55],s74 offen offset:2048 sc1\nM v[12:15],v[200:203],a[100:103],v[12:15]\nM v[12:15],v[204:207],a[116:119],v[12:15]\nM v[72:75],v[200:203],a[104:107],v[72:75]\nM v[72:75],v[204:207],a[120:123],v[72:75]\nbuffer_load_dwordx4 v246,s[40:43],s69 offen offset:3072 lds\n" \
"M v[76:79],v[200:203],a[108:111],v[76:79]\nds_read_b64_tr_b16 v[200:201],v242 offset:50176\nds_read_b64_tr_b16 v[202:203],v242 offset:50432\nM v[76:79],v[204:207],a[124:127],v[76:79]\nds_read_b64_tr_b16 v[204:205],v242 offset:58368\nds_read_b64_tr_b16 v[206:207],v242 offset:58624\nM v[16:19],v[208:211],a[96:99],v[16:19]\ns_add_u32 m0,s64,131072\n" \
"s_xor_b32 s69,s99,3145728\nM v[16:19],v[212:215],a[112:115],v[16:19]\nbuffer_load_dwordx4 a[192:195],v240,s[52:55],s76 offen offset:0 sc1\nM v[20:23],v[208:211],a[100:103],v[20:23]\nM v[20:23],v[212:215],a[116:119],v[20:23]\nM v[80:83],v[208:211],a[104:107],v[80:83]\nM v[80:83],v[212:215],a[120:123],v[80:83]\nbuffer_load_dwordx4 a[208:211],v240,s[52:55],s76 offen offset:2048 sc1\n" \
"M v[84:87],v[208:211],a[108:111],v[84:87]\nds_read_b64_tr_b16 v[208:209],v241 offset:50688\nds_read_b64_tr_b16 v[210:211],v241 offset:50944\nM v[84:87],v[212:215],a[124:127],v[84:87]\nds_read_b64_tr_b16 v[212:213],v241 offset:58880\ns_waitcnt lgkmcnt(7)\nM v[24:27],v[216:219],a[96:99],v[24:27]\nds_read_b64_tr_b16 v[214:215],v241 offset:59136\n" \
"M v[24:27],v[220:223],a[112:115],v[24:27]\nbuffer_load_dwordx4 a[196:199],v240,s[52:55],s76 offen offset:1024 sc1\nM v[28:31],v[216:219],a[100:103],v[28:31]\nM v[28:31],v[220:223],a[116:119],v[28:31]\nM v[88:91],v[216:219],a[104:107],v[88:91]\nM v[88:91],v[220:223],a[120:123],v[88:91]\nbuffer_load_dwordx4 a[212:215],v240,s[52:55],s76 offen offset:3072 sc1\nM v[92:95],v[216:219],a[108:111],v[92:95]\n" \
"ds_read_b64_tr_b16 v[216:217],v242 offset:50688\nds_read_b64_tr_b16 v[218:219],v242 offset:50944\nM v[92:95],v[220:223],a[124:127],v[92:95]\nds_read_b64_tr_b16 v[220:221],v242 offset:58880\nds_read_b64_tr_b16 v[222:223],v242 offset:59136\nM v[32:35],v[192:195],a[96:99],v[32:35]\ns_xor_b32 s76,s76,4096\nM v[32:35],v[196:199],a[112:115],v[32:35]\n" \
"buffer_load_dwordx4 a[200:203],v240,s[52:55],s77 offen offset:0 sc1\nM v[36:39],v[192:195],a[100:103],v[36:39]\nM v[36:39],v[196:199],a[116:119],v[36:39]\nM v[96:99],v[192:195],a[104:107],v[96:99]\nM v[96:99],v[196:199],a[120:123],v[96:99]\nbuffer_load_dwordx4 a[216:219],v240,s[52:55],s77 offen offset:2048 sc1\nM v[100:103],v[192:195],a[108:111],v[100:103]\nM v[100:103],v[196:199],a[124:127],v[100:103]\n" \
"s_waitcnt lgkmcnt(4)\nM v[40:43],v[200:203],a[96:99],v[40:43]\nM v[40:43],v[204:207],a[112:115],v[40:43]\nbuffer_store_dwordx4 v[156:159],v240,s[52:55],s74 offen offset:3072 sc1\nM v[44:47],v[200:203],a[100:103],v[44:47]\ns_add_u32 s74,s74,4096\nM v[44:47],v[204:207],a[116:119],v[44:47]\nM v[104:107],v[200:203],a[104:107],v[104:107]\n" \
"M v[104:107],v[204:207],a[120:123],v[104:107]\nbuffer_load_dwordx4 a[204:207],v240,s[52:55],s77 offen offset:1024 sc1\nM v[108:111],v[200:203],a[108:111],v[108:111]\nM v[108:111],v[204:207],a[124:127],v[108:111]\nM v[48:51],v[208:211],a[96:99],v[48:51]\nM v[48:51],v[212:215],a[112:115],v[48:51]\nbuffer_load_dwordx4 a[220:223],v240,s[52:55],s77 offen offset:3072 sc1\nM v[52:55],v[208:211],a[100:103],v[52:55]\n" \
"s_xor_b32 s77,s77,4096\nM v[52:55],v[212:215],a[116:119],v[52:55]\nM v[112:115],v[208:211],a[104:107],v[112:115]\ns_waitcnt vmcnt(24) lgkmcnt(0)\ns_barrier\nM v[112:115],v[212:215],a[120:123],v[112:115]\nds_read_b64_tr_b16 v[192:193],v241 offset:0\nds_read_b64_tr_b16 v[194:195],v241 offset:256\n" \
"M v[116:119],v[208:211],a[108:111],v[116:119]\nds_read_b64_tr_b16 v[196:197],v241 offset:8192\nds_read_b64_tr_b16 v[198:199],v241 offset:8448\nM v[116:119],v[212:215],a[124:127],v[116:119]\nds_read_b64_tr_b16 v[200:201],v242 offset:0\nds_read_b64_tr_b16 v[202:203],v242 offset:256\nM v[56:59],v[216:219],a[96:99],v[56:59]\nds_read_b64_tr_b16 v[204:205],v242 offset:8192\n" \
"ds_read_b64_tr_b16 v[206:207],v242 offset:8448\nM v[56:59],v[220:223],a[112:115],v[56:59]\nds_read_b64_tr_b16 v[208:209],v241 offset:512\nds_read_b64_tr_b16 v[210:211],v241 offset:768\nM v[60:63],v[216:219],a[100:103],v[60:63]\nds_read_b64_tr_b16 v[212:213],v241 offset:8704\nds_read_b64_tr_b16 v[214:215],v241 offset:8960\nM v[60:63],v[220:223],a[116:119],v[60:63]\n" \
"buffer_load_dwordx4 v243,s[40:43],s69 offen offset:0 lds\nM v[120:123],v[216:219],a[104:107],v[120:123]\nM v[120:123],v[220:223],a[120:123],v[120:123]\nM v[124:127],v[216:219],a[108:111],v[124:127]\nds_read_b64_tr_b16 v[216:217],v242 offset:512\nds_read_b64_tr_b16 v[218:219],v242 offset:768\nM v[124:127],v[220:223],a[124:127],v[124:127]\nds_read_b64_tr_b16 v[220:221],v242 offset:8704\n" \
"s_waitcnt lgkmcnt(13)\nM v[0:3],v[192:195],a[128:131],v[0:3]\nds_read_b64_tr_b16 v[222:223],v242 offset:8960\ns_waitcnt lgkmcnt(12)\nM v[0:3],v[196:199],a[144:147],v[0:3]\nbuffer_load_dwordx4 v244,s[40:43],s69 offen offset:1024 lds\nM v[4:7],v[192:195],a[132:135],v[4:7]\nM v[4:7],v[196:199],a[148:151],v[4:7]\n" \
"M v[64:67],v[192:195],a[136:139],v[64:67]\nM v[64:67],v[196:199],a[152:155],v[64:67]\nbuffer_store_dwordx4 v[160:163],v240,s[52:55],s74 offen offset:0 sc1\nM v[68:71],v[192:195],a[140:143],v[68:71]\nds_read_b64_tr_b16 v[192:193],v241 offset:1024\nds_read_b64_tr_b16 v[194:195],v241 offset:1280\nM v[68:71],v[196:199],a[156:159],v[68:71]\nds_read_b64_tr_b16 v[196:197],v241 offset:9216\n" \
"s_waitcnt lgkmcnt(7)\nM v[8:11],v[200:203],a[128:131],v[8:11]\nds_read_b64_tr_b16 v[198:199],v241 offset:9472\nM v[8:11],v[204:207],a[144:147],v[8:11]\nbuffer_load_dwordx4 v245,s[40:43],s69 offen offset:2048 lds\nM v[12:15],v[200:203],a[132:135],v[12:15]\nM v[12:15],v[204:207],a[148:151],v[12:15]\nM v[72:75],v[200:203],a[136:139],v[72:75]\n" \
"M v[72:75],v[204:207],a[152:155],v[72:75]\nbuffer_load_dwordx4 v246,s[40:43],s69 offen offset:3072 lds\nM v[76:79],v[200:203],a[140:143],v[76:79]\nds_read_b64_tr_b16 v[200:201],v242 offset:1024\nds_read_b64_tr_b16 v[202:203],v242 offset:1280\nM v[76:79],v[204:207],a[156:159],v[76:79]\nds_read_b64_tr_b16 v[204:205],v242 offset:9216\nds_read_b64_tr_b16 v[206:207],v242 offset:9472\n" \
"M v[16:19],v[208:211],a[128:131],v[16:19]\ns_add_u32 m0,s64,147456\ns_xor_b32 s69,s99,3670016\nM v[16:19],v[212:215],a[144:147],v[16:19]\nbuffer_load_dwordx4 a[224:227],v240,s[52:55],s76 offen offset:0 sc1\nM v[20:23],v[208:211],a[132:135],v[20:23]\ns_mov_b32 s99,0\nM v[20:23],v[212:215],a[148:151],v[20:23]\n" \
"M v[80:83],v[208:211],a[136:139],v[80:83]\nds_read_b128 a[0:3],v240 offset:0\nM v[80:83],v[212:215],a[152:155],v[80:83]\nbuffer_load_dword v248,v228,s[52:55],s80 offen sc1\nM v[84:87],v[208:211],a[140:143],v[84:87]\nds_read_b64_tr_b16 v[208:209],v241 offset:1536\nds_read_b64_tr_b16 v[210:211],v241 offset:1792\nM v[84:87],v[212:215],a[156:159],v[84:87]\n" \
"ds_read_b64_tr_b16 v[212:213],v241 offset:9728\ns_waitcnt lgkmcnt(8)\nM v[24:27],v[216:219],a[128:131],v[24:27]\nbuffer_load_dwordx4 a[240:243],v240,s[52:55],s76 offen offset:2048 sc1\nM v[24:27],v[220:223],a[144:147],v[24:27]\nds_read_b64_tr_b16 v[214:215],v241 offset:9984\nM v[28:31],v[216:219],a[132:135],v[28:31]\nds_read_b128 a[4:7],v240 offset:1024\n" \
"M v[28:31],v[220:223],a[148:151],v[28:31]\nM v[88:91],v[216:219],a[136:139],v[88:91]\nbuffer_load_dwordx4 a[228:231],v240,s[52:55],s76 offen offset:1024 sc1\nM v[88:91],v[220:223],a[152:155],v[88:91]\nM v[92:95],v[216:219],a[140:143],v[92:95]\nds_read_b64_tr_b16 v[216:217],v242 offset:1536\nds_read_b64_tr_b16 v[218:219],v242 offset:1792\nM v[92:95],v[220:223],a[156:159],v[92:95]\n" \
"ds_read_b64_tr_b16 v[220:221],v242 offset:9728\nds_read_b64_tr_b16 v[222:223],v242 offset:9984\nM v[32:35],v[192:195],a[128:131],v[32:35]\nbuffer_load_dwordx4 a[244:247],v240,s[52:55],s76 offen offset:3072 sc1\nM v[32:35],v[196:199],a[144:147],v[32:35]\nds_read_b128 a[8:11],v240 offset:2048\nM v[36:39],v[192:195],a[132:135],v[36:39]\nM v[36:39],v[196:199],a[148:151],v[36:39]\n" \
"M v[96:99],v[192:195],a[136:139],v[96:99]\nbuffer_store_dwordx4 v[164:167],v240,s[52:55],s74 offen offset:1024 sc1\nM v[96:99],v[196:199],a[152:155],v[96:99]\nM v[100:103],v[192:195],a[140:143],v[100:103]\nds_read_b128 a[12:15],v240 offset:3072\nM v[100:103],v[196:199],a[156:159],v[100:103]\ns_waitcnt lgkmcnt(7)\nM v[40:43],v[200:203],a[128:131],v[40:43]\n" \
"buffer_load_dwordx4 a[232:235],v240,s[52:55],s77 offen offset:0 sc1\nM v[40:43],v[204:207],a[144:147],v[40:43]\nM v[44:47],v[200:203],a[132:135],v[44:47]\nM v[44:47],v[204:207],a[148:151],v[44:47]\nM v[104:107],v[200:203],a[136:139],v[104:107]\nbuffer_load_dwordx4 a[248:251],v240,s[52:55],s77 offen offset:2048 sc1\nM v[104:107],v[204:207],a[152:155],v[104:107]\nds_read_b128 a[16:19],v240 offset:4096\n" \
"M v[108:111],v[200:203],a[140:143],v[108:111]\nM v[108:111],v[204:207],a[156:159],v[108:111]\nM v[48:51],v[208:211],a[128:131],v[48:51]\nbuffer_load_dwordx4 a[236:239],v240,s[52:55],s77 offen offset:1024 sc1\nM v[48:51],v[212:215],a[144:147],v[48:51]\nM v[52:55],v[208:211],a[132:135],v[52:55]\nds_read_b128 a[20:23],v240 offset:5120\nM v[52:55],v[212:215],a[148:151],v[52:55]\n" \
"M v[112:115],v[208:211],a[136:139],v[112:115]\ns_waitcnt vmcnt(17) lgkmcnt(3)\ns_barrier\nM v[112:115],v[212:215],a[152:155],v[112:115]\nds_read_b64_tr_b16 v[192:193],v241 offset:16384\nds_read_b64_tr_b16 v[194:195],v241 offset:16640\nM v[116:119],v[208:211],a[140:143],v[116:119]\nds_read_b64_tr_b16 v[196:197],v241 offset:24576\n" \
"ds_read_b64_tr_b16 v[198:199],v241 offset:24832\nM v[116:119],v[212:215],a[156:159],v[116:119]\nds_read_b64_tr_b16 v[200:201],v242 offset:16384\nds_read_b64_tr_b16 v[202:203],v242 offset:16640\nM v[56:59],v[216:219],a[128:131],v[56:59]\nds_read_b64_tr_b16 v[204:205],v242 offset:24576\nds_read_b64_tr_b16 v[206:207],v242 offset:24832\nM v[56:59],v[220:223],a[144:147],v[56:59]\n" \
"ds_read_b64_tr_b16 v[208:209],v241 offset:16896\nds_read_b64_tr_b16 v[210:211],v241 offset:17152\nM v[60:63],v[216:219],a[132:135],v[60:63]\nds_read_b64_tr_b16 v[212:213],v241 offset:25088\nds_read_b64_tr_b16 v[214:215],v241 offset:25344\nM v[60:63],v[220:223],a[148:151],v[60:63]\nbuffer_load_dwordx4 a[252:255],v240,s[52:55],s77 offen offset:3072 sc1\nM v[120:123],v[216:219],a[136:139],v[120:123]\n" \
"ds_read_b128 a[24:27],v240 offset:6144\nM v[120:123],v[220:223],a[152:155],v[120:123]\nM v[124:127],v[216:219],a[140:143],v[124:127]\nds_read_b64_tr_b16 v[216:217],v242 offset:16896\nds_read_b64_tr_b16 v[218:219],v242 offset:17152\nM v[124:127],v[220:223],a[156:159],v[124:127]\nds_read_b64_tr_b16 v[220:221],v242 offset:25088\ns_waitcnt lgkmcnt(14)\n" \
"M v[0:3],v[192:195],a[160:163],v[0:3]\nds_read_b64_tr_b16 v[222:223],v242 offset:25344\ns_waitcnt lgkmcnt(13)\nM v[0:3],v[196:199],a[176:179],v[0:3]\nbuffer_store_dwordx4 v[168:171],v240,s[52:55],s74 offen offset:2048 sc1\nM v[4:7],v[192:195],a[164:167],v[4:7]\nds_read_b128 a[28:31],v240 offset:7168\nM v[4:7],v[196:199],a[180:183],v[4:7]\n" \
"M v[64:67],v[192:195],a[168:171],v[64:67]\nds_read_b128 a[32:35],v240 offset:8192\nM v[64:67],v[196:199],a[184:187],v[64:67]\nbuffer_load_dwordx4 v243,s[40:43],s69 offen offset:0 lds\nM v[68:71],v[192:195],a[172:175],v[68:71]\nds_read_b64_tr_b16 v[192:193],v241 offset:17408\nds_read_b64_tr_b16 v[194:195],v241 offset:17664\nM v[68:71],v[196:199],a[188:191],v[68:71]\n" \
"ds_read_b64_tr_b16 v[196:197],v241 offset:25600\ns_waitcnt lgkmcnt(10)\nM v[8:11],v[200:203],a[160:163],v[8:11]\nds_read_b64_tr_b16 v[198:199],v241 offset:25856\nM v[8:11],v[204:207],a[176:179],v[8:11]\nbuffer_load_dwordx4 v244,s[40:43],s69 offen offset:1024 lds\nM v[12:15],v[200:203],a[164:167],v[12:15]\nds_read_b128 a[36:39],v240 offset:9216\n" \
"M v[12:15],v[204:207],a[180:183],v[12:15]\nM v[72:75],v[200:203],a[168:171],v[72:75]\nM v[72:75],v[204:207],a[184:187],v[72:75]\nbuffer_load_dwordx4 v245,s[40:43],s69 offen offset:2048 lds\nM v[76:79],v[200:203],a[172:175],v[76:79]\nds_read_b64_tr_b16 v[200:201],v242 offset:17408\nds_read_b64_tr_b16 v[202:203],v242 offset:17664\nM v[76:79],v[204:207],a[188:191],v[76:79]\n" \
"ds_read_b64_tr_b16 v[204:205],v242 offset:25600\nds_read_b64_tr_b16 v[206:207],v242 offset:25856\nM v[16:19],v[208:211],a[160:163],v[16:19]\nds_read_b128 a[40:43],v240 offset:10240\nM v[16:19],v[212:215],a[176:179],v[16:19]\nbuffer_load_dwordx4 v246,s[40:43],s69 offen offset:3072 lds\nM v[20:23],v[208:211],a[164:167],v[20:23]\ns_sub_u32 s40,s18,0x1000\n" \
"s_subb_u32 s41,s19,0\nM v[20:23],v[212:215],a[180:183],v[20:23]\ns_waitcnt vmcnt(13)\nv_cmp_eq_u32 vcc,0,v248\nM v[80:83],v[208:211],a[168:171],v[80:83]\ns_cbranch_vccnz .Lps1\n.Lpb1:\ns_add_u32 s80,s80,4096\n" \
"M v[80:83],v[212:215],a[184:187],v[80:83]\ns_cmp_ge_u32 s62,6\ns_cselect_b64 vcc,-1,0\nM v[84:87],v[208:211],a[172:175],v[84:87]\nds_read_b64_tr_b16 v[208:209],v241 offset:17920\nds_read_b64_tr_b16 v[210:211],v241 offset:18176\nM v[84:87],v[212:215],a[188:191],v[84:87]\nds_read_b64_tr_b16 v[212:213],v241 offset:26112\n" \
"s_waitcnt lgkmcnt(8)\nM v[24:27],v[216:219],a[160:163],v[24:27]\nds_read_b64_tr_b16 v[214:215],v241 offset:26368\nv_cndmask_b32 v254,v231,v229,vcc\nM v[24:27],v[220:223],a[176:179],v[24:27]\ns_cmp_eq_u32 s62,3\ns_cselect_b64 vcc,-1,0\nM v[28:31],v[216:219],a[164:167],v[28:31]\n" \
"v_cndmask_b32 v254,v254,v230,vcc\nv_bfe_u32 v230,v249,2,3\nM v[28:31],v[220:223],a[180:183],v[28:31]\nv_and_b32 v231,0x70,v243\nv_lshl_or_b32 v230,v230,10,v231\nM v[88:91],v[216:219],a[168:171],v[88:91]\nv_and_b32 v231,16,v249\nv_lshlrev_b32 v231,1,v231\n" \
"M v[88:91],v[220:223],a[184:187],v[88:91]\nv_xor_b32 v230,v230,v231\nv_add_u32 v243,4096,v230\nM v[92:95],v[216:219],a[172:175],v[92:95]\nbuffer_store_dwordx4 v[172:175],v240,s[52:55],s74 offen offset:3072 sc1\nM v[92:95],v[220:223],a[188:191],v[92:95]\nds_read_b64_tr_b16 v[216:217],v242 offset:17920\nds_read_b64_tr_b16 v[218:219],v242 offset:18176\n" \
"M v[32:35],v[192:195],a[160:163],v[32:35]\nds_read_b64_tr_b16 v[220:221],v242 offset:26112\nds_read_b64_tr_b16 v[222:223],v242 offset:26368\nM v[32:35],v[196:199],a[176:179],v[32:35]\ns_add_u32 s74,s74,4096\nv_add_u32 v244,3200,v230\nM v[36:39],v[192:195],a[164:167],v[36:39]\nv_add_u32 v245,10240,v230\n" \
"v_add_u32 v246,9344,v230\nM v[36:39],v[196:199],a[180:183],v[36:39]\nv_and_b32 v229,3,v249\nv_cmp_eq_u32 vcc,0,v229\nM v[96:99],v[192:195],a[168:171],v[96:99]\nv_mov_b32 v230,0x3f803f80\nv_cndmask_b32 v229,0,v230,vcc\nM v[96:99],v[196:199],a[184:187],v[96:99]\n" \
"v_and_b32 v230,0xff,v241\nv_and_b32 v231,16,v249\nM v[100:103],v[192:195],a[172:175],v[100:103]\nv_lshl_or_b32 v230,v231,4,v230\nv_and_b32 v231,32,v249\nM v[100:103],v[196:199],a[188:191],v[100:103]\nds_read_b128 a[44:47],v240 offset:11264\ns_waitcnt lgkmcnt(5)\n" \
"M v[40:43],v[200:203],a[160:163],v[40:43]\nds_read_b128 a[48:51],v240 offset:12288\nds_read_b128 a[52:55],v240 offset:13312\nM v[40:43],v[204:207],a[176:179],v[40:43]\nds_read_b128 a[56:59],v240 offset:14336\nM v[44:47],v[200:203],a[164:167],v[44:47]\nM v[44:47],v[204:207],a[180:183],v[44:47]\nM v[104:107],v[200:203],a[168:171],v[104:107]\n" \
"M v[104:107],v[204:207],a[184:187],v[104:107]\nM v[108:111],v[200:203],a[172:175],v[108:111]\nds_read_b128 a[60:63],v240 offset:15360\nM v[108:111],v[204:207],a[188:191],v[108:111]\nM v[48:51],v[208:211],a[160:163],v[48:51]\nM v[48:51],v[212:215],a[176:179],v[48:51]\nM v[52:55],v[208:211],a[164:167],v[52:55]\nM v[52:55],v[212:215],a[180:183],v[52:55]\n" \
"M v[112:115],v[208:211],a[168:171],v[112:115]\ns_waitcnt lgkmcnt(5)\ns_barrier\nM v[112:115],v[212:215],a[184:187],v[112:115]\nds_read_b64_tr_b16 v[192:193],v241 offset:32768\nds_read_b64_tr_b16 v[194:195],v241 offset:33024\nM v[116:119],v[208:211],a[172:175],v[116:119]\nds_read_b64_tr_b16 v[196:197],v241 offset:40960\n" \
"ds_read_b64_tr_b16 v[198:199],v241 offset:41216\nM v[116:119],v[212:215],a[188:191],v[116:119]\nds_read_b64_tr_b16 v[200:201],v242 offset:32768\nds_read_b64_tr_b16 v[202:203],v242 offset:33024\nM v[56:59],v[216:219],a[160:163],v[56:59]\nds_read_b64_tr_b16 v[204:205],v242 offset:40960\nds_read_b64_tr_b16 v[206:207],v242 offset:41216\nM v[56:59],v[220:223],a[176:179],v[56:59]\n" \
"ds_read_b64_tr_b16 v[208:209],v241 offset:33280\nds_read_b64_tr_b16 v[210:211],v241 offset:33536\nM v[60:63],v[216:219],a[164:167],v[60:63]\nds_read_b64_tr_b16 v[212:213],v241 offset:41472\nds_read_b64_tr_b16 v[214:215],v241 offset:41728\nM v[60:63],v[220:223],a[180:183],v[60:63]\nds_write_b64 v250,v[128:129] offset:0\nM v[120:123],v[216:219],a[168:171],v[120:123]\n" \
"buffer_store_dwordx4 v[176:179],v240,s[52:55],s74 offen offset:0 sc1\nM v[120:123],v[220:223],a[184:187],v[120:123]\nds_write_b64 v250,v[130:131] offset:8\nM v[124:127],v[216:219],a[172:175],v[124:127]\nds_read_b64_tr_b16 v[216:217],v242 offset:33280\nds_read_b64_tr_b16 v[218:219],v242 offset:33536\nM v[124:127],v[220:223],a[188:191],v[124:127]\nds_read_b64_tr_b16 v[220:221],v242 offset:41472\n" \
"s_waitcnt lgkmcnt(15)\nM v[0:3],v[192:195],a[192:195],v[0:3]\nds_read_b64_tr_b16 v[222:223],v242 offset:41728\ns_waitcnt lgkmcnt(14)\nM v[0:3],v[196:199],a[208:211],v[0:3]\nds_write_b64 v250,v[132:133] offset:1024\nM v[4:7],v[192:195],a[196:199],v[4:7]\nds_write_b64 v250,v[134:135] offset:1032\n" \
"M v[4:7],v[196:199],a[212:215],v[4:7]\nds_write_b64 v250,v[136:137] offset:2048\nM v[64:67],v[192:195],a[200:203],v[64:67]\nM v[64:67],v[196:199],a[216:219],v[64:67]\nds_write_b64 v250,v[138:139] offset:2056\nM v[68:71],v[192:195],a[204:207],v[68:71]\nds_read_b64_tr_b16 v[192:193],v241 offset:33792\nds_read_b64_tr_b16 v[194:195],v241 offset:34048\n" \
"M v[68:71],v[196:199],a[220:223],v[68:71]\nds_read_b64_tr_b16 v[196:197],v241 offset:41984\ns_waitcnt lgkmcnt(12)\nM v[8:11],v[200:203],a[192:195],v[8:11]\nds_write_b64 v250,v[140:141] offset:3072\nds_read_b64_tr_b16 v[198:199],v241 offset:42240\nM v[8:11],v[204:207],a[208:211],v[8:11]\nM v[12:15],v[200:203],a[196:199],v[12:15]\n" \
"M v[12:15],v[204:207],a[212:215],v[12:15]\nds_write_b64 v250,v[142:143] offset:3080\nM v[72:75],v[200:203],a[200:203],v[72:75]\nM v[72:75],v[204:207],a[216:219],v[72:75]\nM v[76:79],v[200:203],a[204:207],v[76:79]\nds_read_b64_tr_b16 v[200:201],v242 offset:33792\nds_read_b64_tr_b16 v[202:203],v242 offset:34048\nM v[76:79],v[204:207],a[220:223],v[76:79]\n" \
"ds_read_b64_tr_b16 v[204:205],v242 offset:41984\nds_read_b64_tr_b16 v[206:207],v242 offset:42240\nM v[16:19],v[208:211],a[192:195],v[16:19]\nds_write_b64 v250,v[144:145] offset:4096\nM v[16:19],v[212:215],a[208:211],v[16:19]\nds_write_b64 v250,v[146:147] offset:4104\nM v[20:23],v[208:211],a[196:199],v[20:23]\nM v[20:23],v[212:215],a[212:215],v[20:23]\n" \
"M v[80:83],v[208:211],a[200:203],v[80:83]\nds_write_b64 v250,v[148:149] offset:5120\nM v[80:83],v[212:215],a[216:219],v[80:83]\nM v[84:87],v[208:211],a[204:207],v[84:87]\nds_read_b64_tr_b16 v[208:209],v241 offset:34304\nds_read_b64_tr_b16 v[210:211],v241 offset:34560\nM v[84:87],v[212:215],a[220:223],v[84:87]\nds_read_b64_tr_b16 v[212:213],v241 offset:42496\n" \
"s_waitcnt lgkmcnt(11)\nM v[24:27],v[216:219],a[192:195],v[24:27]\nds_write_b64 v250,v[150:151] offset:5128\nds_read_b64_tr_b16 v[214:215],v241 offset:42752\nM v[24:27],v[220:223],a[208:211],v[24:27]\nM v[28:31],v[216:219],a[196:199],v[28:31]\nbuffer_store_dwordx4 v[180:183],v240,s[52:55],s74 offen offset:1024 sc1\nM v[28:31],v[220:223],a[212:215],v[28:31]\n" \
"ds_write_b64 v250,v[152:153] offset:6144\nM v[88:91],v[216:219],a[200:203],v[88:91]\nM v[88:91],v[220:223],a[216:219],v[88:91]\nds_write_b64 v250,v[154:155] offset:6152\nM v[92:95],v[216:219],a[204:207],v[92:95]\nds_read_b64_tr_b16 v[216:217],v242 offset:34304\nds_read_b64_tr_b16 v[218:219],v242 offset:34560\nM v[92:95],v[220:223],a[220:223],v[92:95]\n" \
"ds_read_b64_tr_b16 v[220:221],v242 offset:42496\nds_read_b64_tr_b16 v[222:223],v242 offset:42752\nM v[32:35],v[192:195],a[192:195],v[32:35]\nds_write_b64 v250,v[156:157] offset:7168\nM v[32:35],v[196:199],a[208:211],v[32:35]\nM v[36:39],v[192:195],a[196:199],v[36:39]\nM v[36:39],v[196:199],a[212:215],v[36:39]\nds_write_b64 v250,v[158:159] offset:7176\n" \
"M v[96:99],v[192:195],a[200:203],v[96:99]\nM v[96:99],v[196:199],a[216:219],v[96:99]\nM v[100:103],v[192:195],a[204:207],v[100:103]\nds_write_b64 v250,v[160:161] offset:8192\nM v[100:103],v[196:199],a[220:223],v[100:103]\ns_waitcnt lgkmcnt(9)\nM v[40:43],v[200:203],a[192:195],v[40:43]\nM v[40:43],v[204:207],a[208:211],v[40:43]\n" \
"ds_write_b64 v250,v[162:163] offset:8200\nM v[44:47],v[200:203],a[196:199],v[44:47]\nM v[44:47],v[204:207],a[212:215],v[44:47]\nM v[104:107],v[200:203],a[200:203],v[104:107]\nds_write_b64 v250,v[164:165] offset:9216\nM v[104:107],v[204:207],a[216:219],v[104:107]\nM v[108:111],v[200:203],a[204:207],v[108:111]\nM v[108:111],v[204:207],a[220:223],v[108:111]\n" \
"ds_write_b64 v250,v[166:167] offset:9224\nM v[48:51],v[208:211],a[192:195],v[48:51]\nM v[48:51],v[212:215],a[208:211],v[48:51]\nM v[52:55],v[208:211],a[196:199],v[52:55]\nds_write_b64 v250,v[168:169] offset:10240\nM v[52:55],v[212:215],a[212:215],v[52:55]\nM v[112:115],v[208:211],a[200:203],v[112:115]\ns_waitcnt vmcnt(3) lgkmcnt(5)\n" \
"s_barrier\nM v[112:115],v[212:215],a[216:219],v[112:115]\nds_read_b64_tr_b16 v[192:193],v241 offset:49152\nds_read_b64_tr_b16 v[194:195],v241 offset:49408\nM v[116:119],v[208:211],a[204:207],v[116:119]\nds_read_b64_tr_b16 v[196:197],v241 offset:57344\nds_read_b64_tr_b16 v[198:199],v241 offset:57600\nM v[116:119],v[212:215],a[220:223],v[116:119]\n" \
"ds_read_b64_tr_b16 v[200:201],v242 offset:49152\nds_read_b64_tr_b16 v[202:203],v242 offset:49408\nM v[56:59],v[216:219],a[192:195],v[56:59]\nbuffer_store_dwordx4 v[184:187],v240,s[52:55],s74 offen offset:2048 sc1\nM v[56:59],v[220:223],a[208:211],v[56:59]\nds_read_b64_tr_b16 v[204:205],v242 offset:57344\nds_read_b64_tr_b16 v[206:207],v242 offset:57600\nM v[60:63],v[216:219],a[196:199],v[60:63]\n" \
"ds_read_b64_tr_b16 v[208:209],v241 offset:49664\nds_read_b64_tr_b16 v[210:211],v241 offset:49920\nM v[60:63],v[220:223],a[212:215],v[60:63]\nds_read_b64_tr_b16 v[212:213],v241 offset:57856\nds_read_b64_tr_b16 v[214:215],v241 offset:58112\nM v[120:123],v[216:219],a[200:203],v[120:123]\nds_write_b64 v250,v[170:171] offset:10248\nv_accvgpr_write_b32 a208,v229\n" \
"M v[120:123],v[220:223],a[216:219],v[120:123]\nds_write_b64 v250,v[172:173] offset:11264\nv_accvgpr_write_b32 a209,v229\nM v[124:127],v[216:219],a[204:207],v[124:127]\nds_read_b64_tr_b16 v[216:217],v242 offset:49664\nds_read_b64_tr_b16 v[218:219],v242 offset:49920\nM v[124:127],v[220:223],a[220:223],v[124:127]\nds_read_b64_tr_b16 v[220:221],v242 offset:57856\n" \
"s_waitcnt lgkmcnt(15)\nM v[0:3],v[192:195],a[224:227],v[0:3]\nds_read_b64_tr_b16 v[222:223],v242 offset:58112\ns_waitcnt lgkmcnt(14)\nM v[0:3],v[196:199],a[240:243],v[0:3]\nds_write_b64 v250,v[174:175] offset:11272\nv_accvgpr_write_b32 a210,v229\nM v[4:7],v[192:195],a[228:231],v[4:7]\n" \
"ds_write_b64 v250,v[176:177] offset:12288\nv_accvgpr_write_b32 a211,v229\nM v[4:7],v[196:199],a[244:247],v[4:7]\nds_write_b64 v250,v[178:179] offset:12296\nM v[64:67],v[192:195],a[232:235],v[64:67]\nv_cvt_pk_bf16_f32 v0,v0,v1\nv_cvt_pk_bf16_f32 v1,v2,v3\nM v[64:67],v[196:199],a[248:251],v[64:67]\n" \
"ds_write_b64 v250,v[180:181] offset:13312\nv_accvgpr_write_b32 a128,v0\nM v[68:71],v[192:195],a[236:239],v[68:71]\nds_read_b64_tr_b16 v[192:193],v241 offset:50176\nds_read_b64_tr_b16 v[194:195],v241 offset:50432\nM v[68:71],v[196:199],a[252:255],v[68:71]\nds_read_b64_tr_b16 v[196:197],v241 offset:58368\ns_waitcnt lgkmcnt(12)\n" \
"M v[8:11],v[200:203],a[224:227],v[8:11]\nds_read_b64_tr_b16 v[198:199],v241 offset:58624\nv_cvt_pk_bf16_f32 v4,v4,v5\nM v[8:11],v[204:207],a[240:243],v[8:11]\nv_cvt_pk_bf16_f32 v64,v64,v65\nv_cvt_pk_bf16_f32 v5,v6,v7\nM v[12:15],v[200:203],a[228:231],v[12:15]\nv_cvt_pk_bf16_f32 v68,v68,v69\n" \
"v_cvt_pk_bf16_f32 v65,v66,v67\nM v[12:15],v[204:207],a[244:247],v[12:15]\nds_write_b64 v250,v[182:183] offset:13320\nv_cvt_pk_bf16_f32 v69,v70,v71\nM v[72:75],v[200:203],a[232:235],v[72:75]\nds_write_b64 v250,v[184:185] offset:14336\nv_cvt_pk_bf16_f32 v2,v8,v9\nM v[72:75],v[204:207],a[248:251],v[72:75]\n" \
"ds_write_b64 v250,v[186:187] offset:14344\nv_cvt_pk_bf16_f32 v3,v10,v11\nM v[76:79],v[200:203],a[236:239],v[76:79]\nds_read_b64_tr_b16 v[200:201],v242 offset:50176\nds_read_b64_tr_b16 v[202:203],v242 offset:50432\nM v[76:79],v[204:207],a[252:255],v[76:79]\nds_read_b64_tr_b16 v[204:205],v242 offset:58368\nds_read_b64_tr_b16 v[206:207],v242 offset:58624\n" \
"M v[16:19],v[208:211],a[224:227],v[16:19]\nds_write_b64 v250,v[188:189] offset:15360\nv_cvt_pk_bf16_f32 v6,v12,v13\nM v[16:19],v[212:215],a[240:243],v[16:19]\nv_cvt_pk_bf16_f32 v7,v14,v15\nv_cvt_pk_bf16_f32 v66,v72,v73\nM v[20:23],v[208:211],a[228:231],v[20:23]\nv_cvt_pk_bf16_f32 v67,v74,v75\n" \
"v_cvt_pk_bf16_f32 v70,v76,v77\nM v[20:23],v[212:215],a[244:247],v[20:23]\nds_write_b64 v250,v[190:191] offset:15368\nv_cvt_pk_bf16_f32 v71,v78,v79\nM v[80:83],v[208:211],a[232:235],v[80:83]\nv_cvt_pk_bf16_f32 v16,v16,v17\nv_cvt_pk_bf16_f32 v17,v18,v19\nM v[80:83],v[212:215],a[248:251],v[80:83]\n" \
"v_add_u32 v250,114688,v240\nv_accvgpr_write_b32 a129,v1\nM v[84:87],v[208:211],a[236:239],v[84:87]\nbuffer_store_dwordx4 v[188:191],v240,s[52:55],s74 offen offset:3072 sc1\nM v[84:87],v[212:215],a[252:255],v[84:87]\nds_read_b64_tr_b16 v[208:209],v241 offset:50688\ns_waitcnt lgkmcnt(7)\nM v[24:27],v[216:219],a[224:227],v[24:27]\n" \
"ds_read_b64_tr_b16 v[210:211],v241 offset:50944\nds_read_b64_tr_b16 v[212:213],v241 offset:58880\nM v[24:27],v[220:223],a[240:243],v[24:27]\nds_read_b64_tr_b16 v[214:215],v241 offset:59136\nv_cvt_pk_bf16_f32 v20,v20,v21\nM v[28:31],v[216:219],a[228:231],v[28:31]\nv_cvt_pk_bf16_f32 v80,v80,v81\nv_cvt_pk_bf16_f32 v84,v84,v85\n" \
"M v[28:31],v[220:223],a[244:247],v[28:31]\nv_cvt_pk_bf16_f32 v21,v22,v23\nv_cvt_pk_bf16_f32 v81,v82,v83\nM v[88:91],v[216:219],a[232:235],v[88:91]\nv_cvt_pk_bf16_f32 v85,v86,v87\nv_cvt_pk_bf16_f32 v18,v24,v25\nM v[88:91],v[220:223],a[248:251],v[88:91]\nv_cvt_pk_bf16_f32 v19,v26,v27\n" \
"v_add_u32 v241,65536,v240\nM v[92:95],v[216:219],a[236:239],v[92:95]\nds_read_b64_tr_b16 v[216:217],v242 offset:50688\nds_read_b64_tr_b16 v[218:219],v242 offset:50944\nM v[92:95],v[220:223],a[252:255],v[92:95]\nds_read_b64_tr_b16 v[220:221],v242 offset:58880\nds_read_b64_tr_b16 v[222:223],v242 offset:59136\nM v[32:35],v[192:195],a[224:227],v[32:35]\n" \
"v_cvt_pk_bf16_f32 v22,v28,v29\nv_cvt_pk_bf16_f32 v23,v30,v31\nM v[32:35],v[196:199],a[240:243],v[32:35]\nv_cvt_pk_bf16_f32 v82,v88,v89\nv_cvt_pk_bf16_f32 v83,v90,v91\nM v[36:39],v[192:195],a[228:231],v[36:39]\nv_cvt_pk_bf16_f32 v86,v92,v93\nv_cvt_pk_bf16_f32 v87,v94,v95\n" \
"M v[36:39],v[196:199],a[244:247],v[36:39]\nv_lshl_or_b32 v242,v231,6,v230\nv_accvgpr_write_b32 a130,v2\nM v[96:99],v[192:195],a[232:235],v[96:99]\nv_cvt_pk_bf16_f32 v32,v32,v33\nv_cvt_pk_bf16_f32 v33,v34,v35\nM v[96:99],v[196:199],a[248:251],v[96:99]\nv_accvgpr_write_b32 a131,v3\n" \
"v_accvgpr_write_b32 a144,v4\nM v[100:103],v[192:195],a[236:239],v[100:103]\nv_cvt_pk_bf16_f32 v36,v36,v37\nv_cvt_pk_bf16_f32 v37,v38,v39\nM v[100:103],v[196:199],a[252:255],v[100:103]\nv_accvgpr_write_b32 a145,v5\ns_waitcnt lgkmcnt(4)\nM v[40:43],v[200:203],a[224:227],v[40:43]\n" \
"v_cvt_pk_bf16_f32 v96,v96,v97\nv_cvt_pk_bf16_f32 v97,v98,v99\nM v[40:43],v[204:207],a[240:243],v[40:43]\nv_accvgpr_write_b32 a146,v6\nv_accvgpr_write_b32 a147,v7\nM v[44:47],v[200:203],a[228:231],v[44:47]\nv_cvt_pk_bf16_f32 v100,v100,v101\nv_cvt_pk_bf16_f32 v101,v102,v103\n" \
"M v[44:47],v[204:207],a[244:247],v[44:47]\nv_accvgpr_write_b32 a160,v64\nv_accvgpr_write_b32 a161,v65\nM v[104:107],v[200:203],a[232:235],v[104:107]\nv_cvt_pk_bf16_f32 v34,v40,v41\nv_cvt_pk_bf16_f32 v35,v42,v43\nM v[104:107],v[204:207],a[248:251],v[104:107]\nv_accvgpr_write_b32 a162,v66\n" \
"v_accvgpr_write_b32 a163,v67\nM v[108:111],v[200:203],a[236:239],v[108:111]\nv_cvt_pk_bf16_f32 v38,v44,v45\nv_cvt_pk_bf16_f32 v39,v46,v47\nM v[108:111],v[204:207],a[252:255],v[108:111]\nv_accvgpr_write_b32 a176,v68\nv_accvgpr_write_b32 a177,v69\nM v[48:51],v[208:211],a[224:227],v[48:51]\n" \
"v_cvt_pk_bf16_f32 v98,v104,v105\nv_cvt_pk_bf16_f32 v99,v106,v107\nM v[48:51],v[212:215],a[240:243],v[48:51]\nv_accvgpr_write_b32 a178,v70\nv_accvgpr_write_b32 a179,v71\nM v[52:55],v[208:211],a[228:231],v[52:55]\nv_cvt_pk_bf16_f32 v102,v108,v109\nv_cvt_pk_bf16_f32 v103,v110,v111\n" \
"M v[52:55],v[212:215],a[244:247],v[52:55]\nv_accvgpr_write_b32 a132,v16\nv_accvgpr_write_b32 a133,v17\nM v[112:115],v[208:211],a[232:235],v[112:115]\nv_cvt_pk_bf16_f32 v48,v48,v49\nv_cvt_pk_bf16_f32 v49,v50,v51\nM v[112:115],v[212:215],a[248:251],v[112:115]\nv_accvgpr_write_b32 a134,v18\n" \
"v_accvgpr_write_b32 a135,v19\nM v[116:119],v[208:211],a[236:239],v[116:119]\nv_cvt_pk_bf16_f32 v52,v52,v53\nv_cvt_pk_bf16_f32 v53,v54,v55\nM v[116:119],v[212:215],a[252:255],v[116:119]\nv_accvgpr_write_b32 a148,v20\ns_waitcnt lgkmcnt(0)\nM v[56:59],v[216:219],a[224:227],v[56:59]\n" \
"v_cvt_pk_bf16_f32 v112,v112,v113\nv_cvt_pk_bf16_f32 v113,v114,v115\nM v[56:59],v[220:223],a[240:243],v[56:59]\nv_accvgpr_write_b32 a149,v21\nv_accvgpr_write_b32 a150,v22\nM v[60:63],v[216:219],a[228:231],v[60:63]\nv_cvt_pk_bf16_f32 v116,v116,v117\nv_cvt_pk_bf16_f32 v117,v118,v119\n" \
"M v[60:63],v[220:223],a[244:247],v[60:63]\nv_accvgpr_write_b32 a244,0\nv_accvgpr_write_b32 a245,0\nM v[120:123],v[216:219],a[232:235],v[120:123]\nv_cvt_pk_bf16_f32 v50,v56,v57\nv_cvt_pk_bf16_f32 v51,v58,v59\nM v[120:123],v[220:223],a[248:251],v[120:123]\nv_accvgpr_write_b32 a248,-1\n" \
"v_accvgpr_write_b32 a249,-1\nM v[124:127],v[216:219],a[236:239],v[124:127]\nv_cvt_pk_bf16_f32 v54,v60,v61\nv_cvt_pk_bf16_f32 v55,v62,v63\nM v[124:127],v[220:223],a[252:255],v[124:127]\ns_waitcnt vmcnt(0)\nbuffer_store_dword v252,off,s[52:55],s29 sc1\nv_cvt_pk_bf16_f32 v114,v120,v121\n" \
"v_cvt_pk_bf16_f32 v115,v122,v123\nv_accvgpr_write_b32 a246,0\nv_accvgpr_write_b32 a250,-1\nv_accvgpr_write_b32 a247,0\nv_accvgpr_write_b32 a251,-1\nv_cvt_pk_bf16_f32 v118,v124,v125\nv_cvt_pk_bf16_f32 v119,v126,v127\nv_accvgpr_write_b32 a151,v23\n" \
"v_accvgpr_write_b32 a164,v80\nv_accvgpr_write_b32 a165,v81\nv_accvgpr_write_b32 a166,v82\nv_accvgpr_write_b32 a167,v83\nv_accvgpr_write_b32 a180,v84\nv_accvgpr_write_b32 a181,v85\nv_accvgpr_write_b32 a182,v86\nv_accvgpr_write_b32 a183,v87\n" \
"v_accvgpr_write_b32 a136,v32\nv_accvgpr_write_b32 a137,v33\nv_accvgpr_write_b32 a138,v34\nv_accvgpr_write_b32 a139,v35\nv_accvgpr_write_b32 a152,v36\nv_accvgpr_write_b32 a153,v37\nv_accvgpr_write_b32 a154,v38\nv_accvgpr_write_b32 a155,v39\n" \
"v_accvgpr_write_b32 a168,v96\nv_accvgpr_write_b32 a169,v97\nv_accvgpr_write_b32 a170,v98\nv_accvgpr_write_b32 a171,v99\nv_accvgpr_write_b32 a184,v100\nv_accvgpr_write_b32 a185,v101\nv_accvgpr_write_b32 a186,v102\nv_accvgpr_write_b32 a187,v103\n" \
"v_accvgpr_write_b32 a140,v48\nv_accvgpr_write_b32 a141,v49\nv_accvgpr_write_b32 a142,v50\nv_accvgpr_write_b32 a143,v51\nv_accvgpr_write_b32 a156,v52\nv_accvgpr_write_b32 a157,v53\nv_accvgpr_write_b32 a158,v54\nv_accvgpr_write_b32 a159,v55\n" \
"v_accvgpr_write_b32 a172,v112\nv_accvgpr_write_b32 a173,v113\nv_accvgpr_write_b32 a174,v114\nv_accvgpr_write_b32 a175,v115\nv_accvgpr_write_b32 a188,v116\nv_accvgpr_write_b32 a189,v117\nv_accvgpr_write_b32 a190,v118\nv_accvgpr_write_b32 a191,v119\n" \
"s_waitcnt lgkmcnt(0)\n.Lattn:\ns_cmp_lt_u32 s7,2\ns_cbranch_scc0 .Lp23\ns_cmp_eq_u32 s7,0\ns_cbranch_scc0 .Lpro1\n.Lpro0:\ns_barrier\n" \
"M v[64:67],a[0:3],a[128:131],0\nds_read_b128 a[32:35],v240 offset:40960\nds_read_b128 a[36:39],v240 offset:41984\nM v[64:67],a[4:7],a[132:135],v[64:67]\nds_read_b128 a[40:43],v240 offset:43008\ns_mov_b32 m0,s64\nv_mov_b32 v218,0\nM v[64:67],a[8:11],a[136:139],v[64:67]\n" \
"ds_read_b128 a[44:47],v240 offset:44032\nv_mov_b32 v219,0\nM v[64:67],a[12:15],a[140:143],v[64:67]\nds_read_b128 a[48:51],v240 offset:45056\nbuffer_load_dwordx4 v240,s[44:47],s36 offen offset:0 sc1 lds\nM v[68:71],a[12:15],a[156:159],0\nds_read_b128 a[12:15],v240 offset:35840\nM v[68:71],a[8:11],a[152:155],v[68:71]\n" \
"ds_read_b128 a[8:11],v240 offset:34816\nM v[68:71],a[4:7],a[148:151],v[68:71]\nds_read_b128 a[4:7],v240 offset:33792\nv_max3_f32 v225,v64,v65,v66\nM v[68:71],a[0:3],a[144:147],v[68:71]\nds_read_b128 a[0:3],v240 offset:32768\nv_max_f32 v225,v225,v67\nv_cndmask_b32 v225,v224,v225,s[90:91]\n" \
"M v[76:79],a[28:31],a[156:159],0\nds_read_b128 a[28:31],v240 offset:39936\nv_cndmask_b32 v64,v64,v252,s[82:83]\nM v[76:79],a[24:27],a[152:155],v[76:79]\nds_read_b128 a[24:27],v240 offset:38912\nv_max3_f32 v229,v68,v69,v70\nv_cndmask_b32 v65,v65,v252,s[84:85]\nM v[76:79],a[20:23],a[148:151],v[76:79]\n" \
"ds_read_b128 a[20:23],v240 offset:37888\nv_cndmask_b32 v66,v66,v252,s[86:87]\nM v[76:79],a[16:19],a[144:147],v[76:79]\nds_read_b128 a[16:19],v240 offset:36864\nv_cndmask_b32 v67,v67,v252,s[88:89]\ns_waitcnt lgkmcnt(4)\nM v[136:139],a[0:3],a[160:163],0\nbuffer_load_dwordx4 v240,s[44:47],s36 offen offset:1024 sc1 lds\n" \
"M v[136:139],a[4:7],a[164:167],v[136:139]\nds_read_b128 a[52:55],v240 offset:46080\nds_read_b128 a[56:59],v240 offset:47104\nv_max3_f32 v229,v229,v71,v76\nM v[136:139],a[8:11],a[168:171],v[136:139]\nv_max3_f32 v229,v229,v77,v78\nv_max_f32 v229,v229,v79\nv_cndmask_b32 v229,v228,v229,s[90:91]\n" \
"M v[136:139],a[12:15],a[172:175],v[136:139]\nds_read_b128 a[60:63],v240 offset:48128\nv_permlane16_swap_b32 v225,v229\nM v[140:143],a[12:15],a[188:191],0\nv_max_f32 v225,v225,v229\nv_mov_b32 v229,v225\nds_read_b128 a[64:67],v241 offset:0\nM v[140:143],a[8:11],a[184:187],v[140:143]\n" \
"v_permlane32_swap_b32 v225,v229\nv_max_f32 v225,v225,v229\nM v[140:143],a[4:7],a[180:183],v[140:143]\nv_mov_b32 v229,v225\nv_max3_f32 v233,v136,v137,v138\nds_read_b128 a[68:71],v241 offset:1024\nM v[140:143],a[0:3],a[176:179],v[140:143]\nv_permlane16_swap_b32 v225,v229\n" \
"s_waitcnt lgkmcnt(5)\nM v[152:155],a[16:19],a[160:163],0\nv_sub_f32 v224,0,v225\nv_sub_f32 v226,0,v225\nv_sub_f32 v227,0,v225\nM v[152:155],a[20:23],a[164:167],v[152:155]\nv_sub_f32 v225,0,v225\nv_sub_f32 v228,0,v229\n" \
"M v[152:155],a[24:27],a[168:171],v[152:155]\nv_sub_f32 v230,0,v229\nv_sub_f32 v231,0,v229\nv_sub_f32 v229,0,v229\nM v[152:155],a[28:31],a[172:175],v[152:155]\nv_max3_f32 v237,v140,v141,v142\nds_read_b128 a[72:75],v241 offset:2048\nM v[156:159],a[28:31],a[188:191],0\n" \
"buffer_load_dwordx4 v240,s[44:47],s36 offen offset:2048 sc1 lds\nds_read_b128 a[76:79],v241 offset:3072\nM v[156:159],a[24:27],a[184:187],v[156:159]\nds_read_b128 a[80:83],v241 offset:4096\nds_read_b128 a[84:87],v241 offset:5120\nM v[156:159],a[20:23],a[180:183],v[156:159]\nv_max3_f32 v233,v233,v139,v152\nv_max3_f32 v233,v233,v153,v154\n" \
"v_max_f32 v233,v233,v155\nM v[156:159],a[16:19],a[176:179],v[156:159]\nv_cndmask_b32 v233,v232,v233,s[90:91]\nds_read_b128 a[88:91],v241 offset:6144\nv_add_f32 v64,v64,v224\nM v[128:131],a[0:3],a[128:131],v[224:227]\nbuffer_load_dwordx4 v240,s[44:47],s36 offen offset:3072 sc1 lds\nM v[128:131],a[4:7],a[132:135],v[128:131]\n" \
"v_add_f32 v65,v65,v224\nv_add_f32 v66,v66,v224\nv_max3_f32 v237,v237,v143,v156\nM v[128:131],a[8:11],a[136:139],v[128:131]\nv_max3_f32 v237,v237,v157,v158\nv_max_f32 v237,v237,v159\nM v[128:131],a[12:15],a[140:143],v[128:131]\nv_cndmask_b32 v237,v236,v237,s[90:91]\n" \
"v_add_f32 v67,v67,v224\nds_read_b128 a[92:95],v241 offset:7168\nM v[132:135],a[12:15],a[156:159],v[228:231]\nv_permlane16_swap_b32 v233,v237\nv_max_f32 v233,v233,v237\nM v[132:135],a[8:11],a[152:155],v[132:135]\nv_mov_b32 v237,v233\nv_exp_f32 v64,v64\n" \
"M v[132:135],a[4:7],a[148:151],v[132:135]\nv_permlane32_swap_b32 v233,v237\nv_max_f32 v233,v233,v237\nM v[132:135],a[0:3],a[144:147],v[132:135]\nv_mov_b32 v237,v233\nv_exp_f32 v65,v65\nM v[144:147],a[16:19],a[128:131],v[224:227]\nv_permlane16_swap_b32 v233,v237\n" \
"v_sub_f32 v232,0,v233\nM v[144:147],a[20:23],a[132:135],v[144:147]\nv_sub_f32 v234,0,v233\nv_sub_f32 v235,0,v233\nv_sub_f32 v233,0,v233\nM v[144:147],a[24:27],a[136:139],v[144:147]\nv_sub_f32 v236,0,v237\nv_sub_f32 v238,0,v237\n" \
"M v[144:147],a[28:31],a[140:143],v[144:147]\nv_sub_f32 v239,0,v237\nv_sub_f32 v237,0,v237\nv_cvt_pk_bf16_f32 v64,v64,v65\nM v[148:151],a[28:31],a[156:159],v[228:231]\nv_exp_f32 v66,v66\nv_add_f32 v68,v68,v228\nM v[148:151],a[24:27],a[152:155],v[148:151]\n" \
"v_exp_f32 v67,v67\nM v[148:151],a[20:23],a[148:151],v[148:151]\nv_cvt_pk_bf16_f32 v65,v66,v67\nv_mov_b32 v66,0\nv_mov_b32 v67,0\nM v[148:151],a[16:19],a[144:147],v[148:151]\nv_add_f32 v69,v69,v228\nv_cndmask_b32 v76,v76,v252,s[82:83]\n" \
"M v[160:163],a[32:35],a[128:131],v[224:227]\nv_cndmask_b32 v77,v77,v252,s[84:85]\nv_cndmask_b32 v78,v78,v252,s[86:87]\nv_cndmask_b32 v79,v79,v252,s[88:89]\nM v[160:163],a[36:39],a[132:135],v[160:163]\nv_add_f32 v70,v70,v228\nv_add_f32 v71,v71,v228\nM v[160:163],a[40:43],a[136:139],v[160:163]\n" \
"v_exp_f32 v68,v68\nv_add_f32 v76,v76,v228\nM v[160:163],a[44:47],a[140:143],v[160:163]\nv_exp_f32 v69,v69\nM v[164:167],a[44:47],a[156:159],v[228:231]\nv_exp_f32 v70,v70\nv_add_f32 v77,v77,v228\nM v[164:167],a[40:43],a[152:155],v[164:167]\n" \
"v_exp_f32 v71,v71\nM v[164:167],a[36:39],a[148:151],v[164:167]\nv_add_f32 v78,v78,v228\nv_add_f32 v79,v79,v228\nv_cvt_pk_bf16_f32 v68,v68,v69\nM v[164:167],a[32:35],a[144:147],v[164:167]\nv_exp_f32 v76,v76\nM v[176:179],a[48:51],a[128:131],v[224:227]\n" \
"v_exp_f32 v77,v77\ns_waitcnt lgkmcnt(0)\nM v[176:179],a[52:55],a[132:135],v[176:179]\nv_exp_f32 v78,v78\nv_cvt_pk_bf16_f32 v69,v70,v71\nM v[176:179],a[56:59],a[136:139],v[176:179]\nv_exp_f32 v79,v79\nM v[176:179],a[60:63],a[140:143],v[176:179]\n" \
"v_cvt_pk_bf16_f32 v70,v76,v77\nv_cvt_pk_bf16_f32 v71,v78,v79\nds_read_b128 a[0:3],v240 offset:49152\nM v[180:183],a[60:63],a[156:159],v[228:231]\nds_read_b128 a[4:7],v240 offset:50176\nds_read_b128 a[8:11],v240 offset:51200\nM v[180:183],a[56:59],a[152:155],v[180:183]\nds_read_b128 a[12:15],v240 offset:52224\n" \
"v_exp_f32 v128,v128\nM v[180:183],a[52:55],a[148:151],v[180:183]\nds_read_b128 a[16:19],v240 offset:53248\nv_add_f32 v136,v136,v232\nM v[180:183],a[48:51],a[144:147],v[180:183]\nv_exp_f32 v129,v129\nds_read_b128 a[20:23],v240 offset:54272\nM v[168:171],a[32:35],a[160:163],v[232:235]\n" \
"v_exp_f32 v130,v130\nM v[168:171],a[36:39],a[164:167],v[168:171]\nv_exp_f32 v131,v131\nv_cvt_pk_bf16_f32 v192,v128,v129\nM v[168:171],a[40:43],a[168:171],v[168:171]\nv_cvt_pk_bf16_f32 v193,v130,v131\nds_read_b128 a[24:27],v240 offset:55296\nM v[168:171],a[44:47],a[172:175],v[168:171]\n" \
"ds_read_b128 a[28:31],v240 offset:56320\nv_exp_f32 v132,v132\nM v[172:175],a[44:47],a[188:191],v[236:239]\nv_exp_f32 v133,v133\nM v[172:175],a[40:43],a[184:187],v[172:175]\nv_exp_f32 v134,v134\nv_cvt_pk_bf16_f32 v196,v132,v133\nM v[172:175],a[36:39],a[180:183],v[172:175]\n" \
"buffer_load_dword v248,v251,s[52:55],s80 offen sc1\nv_add_f32 v137,v137,v232\nM v[172:175],a[32:35],a[176:179],v[172:175]\nv_exp_f32 v135,v135\nM v[188:191],a[60:63],a[188:191],v[236:239]\nv_cvt_pk_bf16_f32 v197,v134,v135\nv_exp_f32 v144,v144\nM v[188:191],a[56:59],a[184:187],v[188:191]\n" \
"v_exp_f32 v145,v145\nM v[188:191],a[52:55],a[180:183],v[188:191]\nv_exp_f32 v146,v146\nv_cvt_pk_bf16_f32 v194,v144,v145\nM v[188:191],a[48:51],a[176:179],v[188:191]\nv_exp_f32 v147,v147\nM v[0:3],a[64:67],v[64:67],0\nv_cvt_pk_bf16_f32 v195,v146,v147\n" \
"v_exp_f32 v148,v148\nM v[8:11],a[68:71],v[64:67],0\nv_exp_f32 v149,v149\nM v[16:19],a[72:75],v[64:67],0\nv_exp_f32 v150,v150\nv_cvt_pk_bf16_f32 v198,v148,v149\nM v[24:27],a[76:79],v[64:67],0\nv_exp_f32 v151,v151\n" \
"M v[32:35],a[80:83],v[64:67],0\nv_cvt_pk_bf16_f32 v199,v150,v151\nv_add_f32 v138,v138,v232\nv_add_f32 v139,v139,v232\nM v[40:43],a[84:87],v[64:67],0\nv_exp_f32 v136,v136\nM v[48:51],a[88:91],v[64:67],0\nv_exp_f32 v137,v137\n" \
"v_add_f32 v140,v140,v236\nM v[56:59],a[92:95],v[64:67],0\nv_exp_f32 v138,v138\nv_cvt_pk_bf16_f32 v200,v136,v137\nM a[192:195],a[208:211],v[64:67],0\nv_exp_f32 v139,v139\nM v[4:7],a[64:67],v[68:71],0\nv_cvt_pk_bf16_f32 v201,v138,v139\n" \
"v_add_f32 v141,v141,v236\nv_add_f32 v142,v142,v236\nM v[12:15],a[68:71],v[68:71],0\nv_add_f32 v143,v143,v236\nds_read_b128 a[32:35],v240 offset:57344\nM v[20:23],a[72:75],v[68:71],0\nv_exp_f32 v140,v140\nv_add_f32 v152,v152,v232\n" \
"M v[28:31],a[76:79],v[68:71],0\nv_exp_f32 v141,v141\nM v[36:39],a[80:83],v[68:71],0\nv_exp_f32 v142,v142\nv_cvt_pk_bf16_f32 v204,v140,v141\nM v[44:47],a[84:87],v[68:71],0\nv_exp_f32 v143,v143\nM v[52:55],a[88:91],v[68:71],0\n" \
"v_cvt_pk_bf16_f32 v205,v142,v143\nv_add_f32 v153,v153,v232\nv_add_f32 v154,v154,v232\nM v[60:63],a[92:95],v[68:71],0\nv_add_f32 v155,v155,v232\nds_read_b128 a[36:39],v240 offset:58368\nM a[196:199],a[208:211],v[68:71],0\nv_exp_f32 v152,v152\n" \
"s_waitcnt lgkmcnt(2)\nM v[128:131],a[0:3],a[128:131],v[224:227]\nv_exp_f32 v153,v153\nM v[128:131],a[4:7],a[132:135],v[128:131]\nv_exp_f32 v154,v154\nv_cvt_pk_bf16_f32 v202,v152,v153\nM v[128:131],a[8:11],a[136:139],v[128:131]\nv_exp_f32 v155,v155\n" \
"M v[128:131],a[12:15],a[140:143],v[128:131]\nv_cvt_pk_bf16_f32 v203,v154,v155\nds_read_b128 a[40:43],v240 offset:59392\nds_read_b128 a[44:47],v240 offset:60416\nM v[132:135],a[12:15],a[156:159],v[228:231]\nv_add_f32 v156,v156,v236\nv_add_f32 v157,v157,v236\nv_add_f32 v158,v158,v236\n" \
"M v[132:135],a[8:11],a[152:155],v[132:135]\nv_add_f32 v159,v159,v236\nds_read_b128 a[48:51],v240 offset:61440\nM v[132:135],a[4:7],a[148:151],v[132:135]\nv_exp_f32 v156,v156\nds_read_b128 a[52:55],v240 offset:62464\nM v[132:135],a[0:3],a[144:147],v[132:135]\nv_exp_f32 v157,v157\n" \
"M v[144:147],a[16:19],a[128:131],v[224:227]\nv_exp_f32 v158,v158\nv_cvt_pk_bf16_f32 v206,v156,v157\nM v[144:147],a[20:23],a[132:135],v[144:147]\nv_exp_f32 v159,v159\nM v[144:147],a[24:27],a[136:139],v[144:147]\nv_cvt_pk_bf16_f32 v207,v158,v159\nv_exp_f32 v160,v160\n" \
"M v[144:147],a[28:31],a[140:143],v[144:147]\nv_exp_f32 v161,v161\nM v[148:151],a[28:31],a[156:159],v[228:231]\nv_exp_f32 v162,v162\nv_cvt_pk_bf16_f32 v208,v160,v161\nM v[148:151],a[24:27],a[152:155],v[148:151]\nv_exp_f32 v163,v163\nM v[148:151],a[20:23],a[148:151],v[148:151]\n" \
"v_cvt_pk_bf16_f32 v209,v162,v163\nds_read_b128 a[56:59],v240 offset:63488\nds_read_b128 a[60:63],v240 offset:64512\nM v[148:151],a[16:19],a[144:147],v[148:151]\nv_exp_f32 v164,v164\nM v[136:139],a[0:3],a[160:163],v[232:235]\nv_exp_f32 v165,v165\nv_cndmask_b32 v168,v168,v252,s[82:83]\n" \
"M v[136:139],a[4:7],a[164:167],v[136:139]\nv_exp_f32 v166,v166\nv_cvt_pk_bf16_f32 v212,v164,v165\nM v[136:139],a[8:11],a[168:171],v[136:139]\nv_exp_f32 v167,v167\nM v[136:139],a[12:15],a[172:175],v[136:139]\nv_cvt_pk_bf16_f32 v213,v166,v167\nv_exp_f32 v176,v176\n" \
"M v[140:143],a[12:15],a[188:191],v[236:239]\nv_exp_f32 v177,v177\nM v[140:143],a[8:11],a[184:187],v[140:143]\nv_exp_f32 v178,v178\nv_cvt_pk_bf16_f32 v210,v176,v177\nM v[140:143],a[4:7],a[180:183],v[140:143]\nv_exp_f32 v179,v179\nM v[140:143],a[0:3],a[176:179],v[140:143]\n" \
"v_cvt_pk_bf16_f32 v211,v178,v179\nv_exp_f32 v180,v180\nM v[152:155],a[16:19],a[160:163],v[232:235]\nv_exp_f32 v181,v181\nM v[152:155],a[20:23],a[164:167],v[152:155]\nv_exp_f32 v182,v182\nv_cvt_pk_bf16_f32 v214,v180,v181\nM v[152:155],a[24:27],a[168:171],v[152:155]\n" \
"s_waitcnt vmcnt(1)\ns_barrier\nM v[152:155],a[28:31],a[172:175],v[152:155]\nv_exp_f32 v183,v183\nv_cndmask_b32 v169,v169,v252,s[84:85]\nM v[156:159],a[28:31],a[188:191],v[236:239]\nv_cvt_pk_bf16_f32 v215,v182,v183\nv_cndmask_b32 v170,v170,v252,s[86:87]\n" \
"M v[156:159],a[24:27],a[184:187],v[156:159]\nv_cndmask_b32 v171,v171,v252,s[88:89]\nv_exp_f32 v168,v168\nM v[156:159],a[20:23],a[180:183],v[156:159]\nv_exp_f32 v169,v169\nds_read_b128 a[64:67],v241 offset:32768\nM v[156:159],a[16:19],a[176:179],v[156:159]\nv_cvt_pk_bf16_f32 v216,v168,v169\n" \
"s_waitcnt lgkmcnt(1)\nM v[160:163],a[32:35],a[128:131],v[224:227]\nv_exp_f32 v170,v170\nds_read_b128 a[96:99],v241 offset:40960\nM v[160:163],a[36:39],a[132:135],v[160:163]\nv_exp_f32 v171,v171\nM v[160:163],a[40:43],a[136:139],v[160:163]\nv_cvt_pk_bf16_f32 v217,v170,v171\n" \
"v_exp_f32 v172,v172\nM v[160:163],a[44:47],a[140:143],v[160:163]\nv_exp_f32 v173,v173\nM v[164:167],a[44:47],a[156:159],v[228:231]\nv_exp_f32 v174,v174\nv_cvt_pk_bf16_f32 v220,v172,v173\nM v[164:167],a[40:43],a[152:155],v[164:167]\nv_exp_f32 v175,v175\n" \
"M v[164:167],a[36:39],a[148:151],v[164:167]\nv_cvt_pk_bf16_f32 v221,v174,v175\nds_read_b128 a[68:71],v241 offset:33792\nds_read_b128 a[100:103],v241 offset:41984\nM v[164:167],a[32:35],a[144:147],v[164:167]\nds_read_b128 a[72:75],v241 offset:34816\nv_cndmask_b32 v188,v188,v252,s[82:83]\nM v[176:179],a[48:51],a[128:131],v[224:227]\n" \
"v_cndmask_b32 v189,v189,v252,s[84:85]\nv_cndmask_b32 v190,v190,v252,s[86:87]\nv_cndmask_b32 v191,v191,v252,s[88:89]\nM v[176:179],a[52:55],a[132:135],v[176:179]\nv_exp_f32 v188,v188\nM v[176:179],a[56:59],a[136:139],v[176:179]\nv_exp_f32 v189,v189\nds_read_b128 a[104:107],v241 offset:43008\n" \
"M v[176:179],a[60:63],a[140:143],v[176:179]\nv_exp_f32 v190,v190\nv_cvt_pk_bf16_f32 v222,v188,v189\nM v[180:183],a[60:63],a[156:159],v[228:231]\nv_exp_f32 v191,v191\nM v[180:183],a[56:59],a[152:155],v[180:183]\nv_cvt_pk_bf16_f32 v223,v190,v191\nds_read_b128 a[76:79],v241 offset:35840\n" \
"ds_read_b128 a[108:111],v241 offset:44032\nM v[180:183],a[52:55],a[148:151],v[180:183]\nds_read_b128 a[80:83],v241 offset:36864\ns_waitcnt vmcnt(0)\nv_cmp_eq_u32 vcc,0,v248\nM v[180:183],a[48:51],a[144:147],v[180:183]\nds_read_b128 a[112:115],v241 offset:45056\ns_cbranch_vccnz .Lps2\n" \
".Lpb2:\ns_add_u32 m0,s64,65536\nM v[168:171],a[32:35],a[160:163],v[232:235]\nbuffer_load_dwordx4 v240,s[48:51],s36 offen offset:0 sc1 lds\nM v[168:171],a[36:39],a[164:167],v[168:171]\nds_read_b128 a[84:87],v241 offset:37888\nv_exp_f32 v128,v128\nM v[168:171],a[40:43],a[168:171],v[168:171]\n" \
"ds_read_b128 a[116:119],v241 offset:46080\nds_read_b128 a[88:91],v241 offset:38912\nM v[168:171],a[44:47],a[172:175],v[168:171]\nds_read_b128 a[120:123],v241 offset:47104\nv_exp_f32 v129,v129\nM v[172:175],a[44:47],a[188:191],v[236:239]\nbuffer_load_dwordx4 v240,s[48:51],s36 offen offset:1024 sc1 lds\nM v[172:175],a[40:43],a[184:187],v[172:175]\n" \
"ds_read_b128 a[92:95],v241 offset:39936\nv_exp_f32 v130,v130\nM v[172:175],a[36:39],a[180:183],v[172:175]\nds_read_b128 a[124:127],v241 offset:48128\nds_read_b128 a[0:3],v240 offset:0\nM v[172:175],a[32:35],a[176:179],v[172:175]\nds_read_b128 a[4:7],v240 offset:1024\nv_exp_f32 v131,v131\n" \
"M v[184:187],a[48:51],a[160:163],v[232:235]\nds_read_b128 a[8:11],v240 offset:2048\nbuffer_load_dwordx4 v240,s[48:51],s36 offen offset:2048 sc1 lds\nM v[184:187],a[52:55],a[164:167],v[184:187]\nv_exp_f32 v132,v132\nM v[184:187],a[56:59],a[168:171],v[184:187]\nds_read_b128 a[12:15],v240 offset:3072\nv_exp_f32 v133,v133\n" \
"M v[184:187],a[60:63],a[172:175],v[184:187]\nds_read_b128 a[16:19],v240 offset:4096\nds_read_b128 a[20:23],v240 offset:5120\nM v[188:191],a[60:63],a[188:191],v[236:239]\nds_read_b128 a[24:27],v240 offset:6144\nbuffer_load_dwordx4 v240,s[48:51],s36 offen offset:3072 sc1 lds\nM v[188:191],a[56:59],a[184:187],v[188:191]\nds_read_b128 a[28:31],v240 offset:7168\n" \
"s_add_u32 m0,s64,16384\nM v[188:191],a[52:55],a[180:183],v[188:191]\nds_read_b128 a[32:35],v240 offset:8192\nv_exp_f32 v134,v134\nM v[188:191],a[48:51],a[176:179],v[188:191]\nds_read_b128 a[36:39],v240 offset:9216\ns_waitcnt lgkmcnt(9)\nM v[0:3],a[64:67],v[192:195],v[0:3]\n" \
"ds_read_b128 a[40:43],v240 offset:10240\nbuffer_load_dwordx4 v240,s[44:47],s65 offen offset:0 sc1 lds\nM v[0:3],a[96:99],v[208:211],v[0:3]\nv_exp_f32 v135,v135\nM v[8:11],a[68:71],v[192:195],v[8:11]\nds_read_b128 a[44:47],v240 offset:11264\nv_exp_f32 v144,v144\nM v[8:11],a[100:103],v[208:211],v[8:11]\n" \
"ds_read_b128 a[48:51],v240 offset:12288\nds_read_b128 a[52:55],v240 offset:13312\nM v[16:19],a[72:75],v[192:195],v[16:19]\nbuffer_load_dwordx4 v240,s[44:47],s65 offen offset:1024 sc1 lds\nds_read_b128 a[56:59],v240 offset:14336\nM v[16:19],a[104:107],v[208:211],v[16:19]\nds_read_b128 a[60:63],v240 offset:15360\nv_exp_f32 v145,v145\n" \
"M v[24:27],a[76:79],v[192:195],v[24:27]\nv_exp_f32 v146,v146\nM v[24:27],a[108:111],v[208:211],v[24:27]\nv_exp_f32 v147,v147\nM v[32:35],a[80:83],v[192:195],v[32:35]\nbuffer_load_dwordx4 v240,s[44:47],s65 offen offset:2048 sc1 lds\nM v[32:35],a[112:115],v[208:211],v[32:35]\nv_exp_f32 v148,v148\n" \
"M v[40:43],a[84:87],v[192:195],v[40:43]\nv_exp_f32 v149,v149\nM v[40:43],a[116:119],v[208:211],v[40:43]\nv_exp_f32 v150,v150\nM v[48:51],a[88:91],v[192:195],v[48:51]\nbuffer_load_dwordx4 v240,s[44:47],s65 offen offset:3072 sc1 lds\nM v[48:51],a[120:123],v[208:211],v[48:51]\ns_add_u32 m0,s64,81920\n" \
"v_exp_f32 v151,v151\nM v[56:59],a[92:95],v[192:195],v[56:59]\nv_exp_f32 v136,v136\nM v[56:59],a[124:127],v[208:211],v[56:59]\nv_exp_f32 v137,v137\nM a[192:195],a[208:211],v[192:195],a[192:195]\nbuffer_load_dwordx4 v240,s[48:51],s65 offen offset:0 sc1 lds\nM a[192:195],a[208:211],v[208:211],a[192:195]\n" \
"v_exp_f32 v138,v138\nv_cvt_pk_bf16_f32 v192,v128,v129\nM v[4:7],a[64:67],v[196:199],v[4:7]\nv_exp_f32 v139,v139\nM v[4:7],a[96:99],v[212:215],v[4:7]\nv_exp_f32 v152,v152\nv_cvt_pk_bf16_f32 v193,v130,v131\nM v[12:15],a[68:71],v[196:199],v[12:15]\n" \
"buffer_load_dwordx4 v240,s[48:51],s65 offen offset:1024 sc1 lds\nv_cvt_pk_bf16_f32 v194,v144,v145\nM v[12:15],a[100:103],v[212:215],v[12:15]\nv_exp_f32 v153,v153\nM v[20:23],a[72:75],v[196:199],v[20:23]\nv_exp_f32 v154,v154\nv_cvt_pk_bf16_f32 v195,v146,v147\nM v[20:23],a[104:107],v[212:215],v[20:23]\n" \
"v_exp_f32 v155,v155\nM v[28:31],a[76:79],v[196:199],v[28:31]\nbuffer_load_dwordx4 v240,s[48:51],s65 offen offset:2048 sc1 lds\nM v[28:31],a[108:111],v[212:215],v[28:31]\nv_exp_f32 v160,v160\nM v[36:39],a[80:83],v[196:199],v[36:39]\ns_barrier\nv_exp_f32 v161,v161\n" \
"M v[36:39],a[112:115],v[212:215],v[36:39]\nv_exp_f32 v162,v162\nM v[44:47],a[84:87],v[196:199],v[44:47]\nbuffer_load_dwordx4 v240,s[48:51],s65 offen offset:3072 sc1 lds\ns_add_u32 m0,s64,32768\nM v[44:47],a[116:119],v[212:215],v[44:47]\nv_exp_f32 v163,v163\nM v[52:55],a[88:91],v[196:199],v[52:55]\n" \
"v_exp_f32 v176,v176\nv_cvt_pk_bf16_f32 v208,v160,v161\nM v[52:55],a[120:123],v[212:215],v[52:55]\nv_exp_f32 v177,v177\nM v[60:63],a[92:95],v[196:199],v[60:63]\nbuffer_load_dwordx4 v240,s[44:47],s66 offen offset:0 sc1 lds\nv_cvt_pk_bf16_f32 v209,v162,v163\nM v[60:63],a[124:127],v[212:215],v[60:63]\n" \
"v_exp_f32 v178,v178\nv_cvt_pk_bf16_f32 v210,v176,v177\nM a[196:199],a[208:211],v[196:199],a[196:199]\nv_exp_f32 v179,v179\nM a[196:199],a[208:211],v[212:215],a[196:199]\nv_exp_f32 v140,v140\nv_cvt_pk_bf16_f32 v196,v132,v133\nM v[64:67],a[64:67],v[200:203],0\n" \
"buffer_load_dwordx4 v240,s[44:47],s66 offen offset:1024 sc1 lds\nM v[64:67],a[96:99],v[216:219],v[64:67]\nv_exp_f32 v141,v141\nv_cvt_pk_bf16_f32 v197,v134,v135\nM v[72:75],a[68:71],v[200:203],0\nv_exp_f32 v142,v142\nM v[72:75],a[100:103],v[216:219],v[72:75]\nv_exp_f32 v143,v143\n" \
"v_cvt_pk_bf16_f32 v198,v148,v149\nM v[80:83],a[72:75],v[200:203],0\nbuffer_load_dwordx4 v240,s[44:47],s66 offen offset:2048 sc1 lds\nM v[80:83],a[104:107],v[216:219],v[80:83]\nv_exp_f32 v156,v156\nv_cvt_pk_bf16_f32 v199,v150,v151\nM v[88:91],a[76:79],v[200:203],0\nv_exp_f32 v157,v157\n" \
"M v[88:91],a[108:111],v[216:219],v[88:91]\nv_exp_f32 v158,v158\nv_cvt_pk_bf16_f32 v211,v178,v179\nM v[96:99],a[80:83],v[200:203],0\nbuffer_load_dwordx4 v240,s[44:47],s66 offen offset:3072 sc1 lds\nM v[96:99],a[112:115],v[216:219],v[96:99]\ns_add_u32 m0,s64,98304\nv_exp_f32 v159,v159\n" \
"M v[104:107],a[84:87],v[200:203],0\nv_exp_f32 v164,v164\nM v[104:107],a[116:119],v[216:219],v[104:107]\nv_exp_f32 v165,v165\nM v[112:115],a[88:91],v[200:203],0\nv_exp_f32 v166,v166\nM v[112:115],a[120:123],v[216:219],v[112:115]\nv_exp_f32 v167,v167\n" \
"M v[120:123],a[92:95],v[200:203],0\nv_exp_f32 v180,v180\nM v[120:123],a[124:127],v[216:219],v[120:123]\nv_exp_f32 v181,v181\nM a[200:203],a[208:211],v[200:203],0\nv_exp_f32 v182,v182\nv_cvt_pk_bf16_f32 v200,v136,v137\nM a[200:203],a[208:211],v[216:219],a[200:203]\n" \
"v_exp_f32 v183,v183\nM v[68:71],a[64:67],v[204:207],0\nv_exp_f32 v168,v168\nv_cvt_pk_bf16_f32 v201,v138,v139\nM v[68:71],a[96:99],v[220:223],v[68:71]\nv_exp_f32 v169,v169\nM v[76:79],a[68:71],v[204:207],0\nv_exp_f32 v170,v170\n" \
"v_cvt_pk_bf16_f32 v202,v152,v153\nM v[76:79],a[100:103],v[220:223],v[76:79]\nv_exp_f32 v171,v171\nM v[84:87],a[72:75],v[204:207],0\nv_cvt_pk_bf16_f32 v203,v154,v155\nbuffer_load_dwordx4 v240,s[48:51],s66 offen offset:0 sc1 lds\nM v[84:87],a[104:107],v[220:223],v[84:87]\nM v[92:95],a[76:79],v[204:207],0\n" \
"M v[92:95],a[108:111],v[220:223],v[92:95]\nM v[100:103],a[80:83],v[204:207],0\nbuffer_load_dwordx4 v240,s[48:51],s66 offen offset:1024 sc1 lds\nM v[100:103],a[112:115],v[220:223],v[100:103]\nM v[108:111],a[84:87],v[204:207],0\nM v[108:111],a[116:119],v[220:223],v[108:111]\nM v[116:119],a[88:91],v[204:207],0\nbuffer_load_dwordx4 v240,s[48:51],s66 offen offset:2048 sc1 lds\n" \
"M v[116:119],a[120:123],v[220:223],v[116:119]\nM v[124:127],a[92:95],v[204:207],0\nM v[124:127],a[124:127],v[220:223],v[124:127]\nM a[204:207],a[208:211],v[204:207],0\nbuffer_load_dwordx4 v240,s[48:51],s66 offen offset:3072 sc1 lds\nM a[204:207],a[208:211],v[220:223],a[204:207]\ns_cmp_lt_u32 s62,2\ns_cbranch_scc1 .Lx01\n" \
"s_branch .Ldual\n.Lpro1:\ns_barrier\nM v[64:67],a[0:3],a[128:131],0\nds_read_b128 a[64:67],v241 offset:0\nds_read_b128 a[96:99],v241 offset:8192\nM v[64:67],a[4:7],a[132:135],v[64:67]\nds_read_b128 a[68:71],v241 offset:1024\n" \
"s_mov_b32 m0,s64\nv_mov_b32 v202,0\nM v[64:67],a[8:11],a[136:139],v[64:67]\nbuffer_load_dwordx4 v240,s[44:47],s36 offen offset:0 sc1 lds\nM v[64:67],a[12:15],a[140:143],v[64:67]\nds_read_b128 a[100:103],v241 offset:9216\nds_read_b128 a[72:75],v241 offset:2048\nv_mov_b32 v203,0\n" \
"M v[68:71],a[12:15],a[156:159],0\nds_read_b128 a[12:15],v240 offset:35840\nM v[68:71],a[8:11],a[152:155],v[68:71]\nds_read_b128 a[8:11],v240 offset:34816\nM v[68:71],a[4:7],a[148:151],v[68:71]\nds_read_b128 a[4:7],v240 offset:33792\nbuffer_load_dwordx4 v240,s[44:47],s36 offen offset:1024 sc1 lds\nM v[68:71],a[0:3],a[144:147],v[68:71]\n" \
"ds_read_b128 a[0:3],v240 offset:32768\nv_max3_f32 v225,v64,v65,v66\nM v[72:75],a[16:19],a[128:131],0\nds_read_b128 a[104:107],v241 offset:10240\nM v[72:75],a[20:23],a[132:135],v[72:75]\nds_read_b128 a[76:79],v241 offset:3072\nM v[72:75],a[24:27],a[136:139],v[72:75]\nbuffer_load_dwordx4 v240,s[44:47],s36 offen offset:2048 sc1 lds\n" \
"M v[72:75],a[28:31],a[140:143],v[72:75]\nv_max3_f32 v229,v68,v69,v70\nds_read_b128 a[108:111],v241 offset:11264\nds_read_b128 a[80:83],v241 offset:4096\nM v[76:79],a[28:31],a[156:159],0\nds_read_b128 a[28:31],v240 offset:39936\nM v[76:79],a[24:27],a[152:155],v[76:79]\nds_read_b128 a[24:27],v240 offset:38912\n" \
"M v[76:79],a[20:23],a[148:151],v[76:79]\nds_read_b128 a[20:23],v240 offset:37888\nv_max3_f32 v225,v225,v67,v72\nv_max3_f32 v225,v225,v73,v74\nM v[76:79],a[16:19],a[144:147],v[76:79]\nds_read_b128 a[16:19],v240 offset:36864\nv_max_f32 v225,v225,v75\nv_cndmask_b32 v225,v224,v225,s[90:91]\n" \
"M v[80:83],a[32:35],a[128:131],0\nbuffer_load_dwordx4 v240,s[44:47],s36 offen offset:3072 sc1 lds\nM v[80:83],a[36:39],a[132:135],v[80:83]\nds_read_b128 a[112:115],v241 offset:12288\nds_read_b128 a[84:87],v241 offset:5120\nv_max3_f32 v229,v229,v71,v76\nM v[80:83],a[40:43],a[136:139],v[80:83]\nv_max3_f32 v229,v229,v77,v78\n" \
"v_max_f32 v229,v229,v79\nv_cndmask_b32 v229,v228,v229,s[90:91]\nM v[80:83],a[44:47],a[140:143],v[80:83]\nds_read_b128 a[116:119],v241 offset:13312\nv_permlane16_swap_b32 v225,v229\nM v[84:87],a[44:47],a[156:159],0\nv_max_f32 v225,v225,v229\nv_mov_b32 v229,v225\n" \
"ds_read_b128 a[44:47],v240 offset:44032\nM v[84:87],a[40:43],a[152:155],v[84:87]\nv_permlane32_swap_b32 v225,v229\nv_max_f32 v225,v225,v229\nM v[84:87],a[36:39],a[148:151],v[84:87]\nv_mov_b32 v229,v225\nds_read_b128 a[36:39],v240 offset:41984\nds_read_b128 a[40:43],v240 offset:43008\n" \
"M v[84:87],a[32:35],a[144:147],v[84:87]\nv_permlane16_swap_b32 v225,v229\nv_sub_f32 v224,0,v225\nv_sub_f32 v226,0,v225\nM v[92:95],a[60:63],a[156:159],0\nv_sub_f32 v227,0,v225\nv_sub_f32 v225,0,v225\nM v[92:95],a[56:59],a[152:155],v[92:95]\n" \
"v_sub_f32 v228,0,v229\nv_sub_f32 v230,0,v229\nv_sub_f32 v231,0,v229\nM v[92:95],a[52:55],a[148:151],v[92:95]\nv_sub_f32 v229,0,v229\nds_read_b128 a[32:35],v240 offset:40960\nds_read_b128 a[52:55],v240 offset:46080\nM v[92:95],a[48:51],a[144:147],v[92:95]\n" \
"ds_read_b128 a[48:51],v240 offset:45056\ns_waitcnt lgkmcnt(7)\nM v[136:139],a[0:3],a[160:163],0\nds_read_b128 a[56:59],v240 offset:47104\nv_add_f32 v64,v64,v224\nv_add_f32 v65,v65,v224\nM v[136:139],a[4:7],a[164:167],v[136:139]\nds_read_b128 a[60:63],v240 offset:48128\n" \
"v_add_f32 v66,v66,v224\nM v[136:139],a[8:11],a[168:171],v[136:139]\nv_add_f32 v67,v67,v224\nv_exp_f32 v64,v64\nM v[136:139],a[12:15],a[172:175],v[136:139]\nv_exp_f32 v65,v65\nv_cndmask_b32 v80,v80,v252,s[82:83]\nM v[140:143],a[12:15],a[188:191],0\n" \
"v_cndmask_b32 v81,v81,v252,s[84:85]\nv_add_f32 v72,v72,v224\nM v[140:143],a[8:11],a[184:187],v[140:143]\nv_exp_f32 v66,v66\nv_add_f32 v73,v73,v224\nM v[140:143],a[4:7],a[180:183],v[140:143]\nv_exp_f32 v67,v67\nv_add_f32 v74,v74,v224\n" \
"M v[140:143],a[0:3],a[176:179],v[140:143]\nv_add_f32 v75,v75,v224\nv_cndmask_b32 v82,v82,v252,s[86:87]\nM v[156:159],a[28:31],a[188:191],0\nv_cndmask_b32 v83,v83,v252,s[88:89]\nv_add_f32 v80,v80,v224\nv_add_f32 v81,v81,v224\nM v[156:159],a[24:27],a[184:187],v[156:159]\n" \
"v_exp_f32 v72,v72\nv_add_f32 v82,v82,v224\nM v[156:159],a[20:23],a[180:183],v[156:159]\nv_exp_f32 v73,v73\nM v[156:159],a[16:19],a[176:179],v[156:159]\nv_exp_f32 v74,v74\nv_add_f32 v83,v83,v224\nM v[128:131],a[0:3],a[128:131],v[224:227]\n" \
"v_exp_f32 v75,v75\nM v[128:131],a[4:7],a[132:135],v[128:131]\nv_exp_f32 v80,v80\nv_cvt_pk_bf16_f32 v64,v64,v65\nM v[128:131],a[8:11],a[136:139],v[128:131]\nv_exp_f32 v81,v81\nv_cvt_pk_bf16_f32 v65,v66,v67\nM v[128:131],a[12:15],a[140:143],v[128:131]\n" \
"v_exp_f32 v82,v82\nM v[132:135],a[12:15],a[156:159],v[228:231]\nv_exp_f32 v83,v83\nv_cvt_pk_bf16_f32 v66,v72,v73\nM v[132:135],a[8:11],a[152:155],v[132:135]\nv_cvt_pk_bf16_f32 v67,v74,v75\nv_cvt_pk_bf16_f32 v80,v80,v81\nv_cvt_pk_bf16_f32 v81,v82,v83\n" \
"M v[132:135],a[4:7],a[148:151],v[132:135]\nv_mov_b32 v82,0\nv_mov_b32 v83,0\nM v[132:135],a[0:3],a[144:147],v[132:135]\nds_read_b128 a[88:91],v241 offset:6144\nds_read_b128 a[120:123],v241 offset:14336\nv_add_f32 v68,v68,v228\nM v[144:147],a[16:19],a[128:131],v[224:227]\n" \
"ds_read_b128 a[92:95],v241 offset:7168\nv_add_f32 v69,v69,v228\nv_add_f32 v70,v70,v228\nM v[144:147],a[20:23],a[132:135],v[144:147]\nds_read_b128 a[124:127],v241 offset:15360\nv_add_f32 v71,v71,v228\nM v[144:147],a[24:27],a[136:139],v[144:147]\nv_exp_f32 v68,v68\n" \
"v_add_f32 v76,v76,v228\nM v[144:147],a[28:31],a[140:143],v[144:147]\nv_exp_f32 v69,v69\nM v[148:151],a[28:31],a[156:159],v[228:231]\nv_exp_f32 v70,v70\nv_add_f32 v77,v77,v228\nM v[148:151],a[24:27],a[152:155],v[148:151]\nv_exp_f32 v71,v71\n" \
"v_add_f32 v78,v78,v228\nM v[148:151],a[20:23],a[148:151],v[148:151]\nv_add_f32 v79,v79,v228\nv_add_f32 v84,v84,v228\nM v[148:151],a[16:19],a[144:147],v[148:151]\nv_add_f32 v85,v85,v228\nv_cndmask_b32 v92,v92,v252,s[82:83]\ns_waitcnt lgkmcnt(4)\n" \
"M v[160:163],a[32:35],a[128:131],v[224:227]\nv_exp_f32 v76,v76\nv_cndmask_b32 v93,v93,v252,s[84:85]\nM v[160:163],a[36:39],a[132:135],v[160:163]\nv_exp_f32 v77,v77\nM v[160:163],a[40:43],a[136:139],v[160:163]\nv_exp_f32 v78,v78\nv_cndmask_b32 v94,v94,v252,s[86:87]\n" \
"M v[160:163],a[44:47],a[140:143],v[160:163]\nv_exp_f32 v79,v79\nM v[164:167],a[44:47],a[156:159],v[228:231]\nv_cndmask_b32 v95,v95,v252,s[88:89]\nv_add_f32 v86,v86,v228\nv_add_f32 v87,v87,v228\nM v[164:167],a[40:43],a[152:155],v[164:167]\nv_exp_f32 v84,v84\n" \
"v_cvt_pk_bf16_f32 v68,v68,v69\nM v[164:167],a[36:39],a[148:151],v[164:167]\nv_exp_f32 v85,v85\nM v[164:167],a[32:35],a[144:147],v[164:167]\nv_exp_f32 v86,v86\nv_add_f32 v92,v92,v228\nM v[176:179],a[48:51],a[128:131],v[224:227]\nv_exp_f32 v87,v87\n" \
"v_add_f32 v93,v93,v228\nM v[176:179],a[52:55],a[132:135],v[176:179]\nv_add_f32 v94,v94,v228\nv_add_f32 v95,v95,v228\nM v[176:179],a[56:59],a[136:139],v[176:179]\nbuffer_load_dword v248,v251,s[52:55],s80 offen sc1\nv_cvt_pk_bf16_f32 v69,v70,v71\nM v[176:179],a[60:63],a[140:143],v[176:179]\n" \
"v_exp_f32 v92,v92\nv_cvt_pk_bf16_f32 v70,v76,v77\nM v[180:183],a[60:63],a[156:159],v[228:231]\nv_exp_f32 v93,v93\nM v[180:183],a[56:59],a[152:155],v[180:183]\nv_exp_f32 v94,v94\nv_cvt_pk_bf16_f32 v71,v78,v79\nM v[180:183],a[52:55],a[148:151],v[180:183]\n" \
"v_exp_f32 v95,v95\nM v[180:183],a[48:51],a[144:147],v[180:183]\nv_cvt_pk_bf16_f32 v84,v84,v85\nv_cvt_pk_bf16_f32 v85,v86,v87\nv_cvt_pk_bf16_f32 v86,v92,v93\nM v[0:3],a[64:67],v[64:67],0\nv_cvt_pk_bf16_f32 v87,v94,v95\nds_read_b128 a[0:3],v240 offset:49152\n" \
"ds_read_b128 a[4:7],v240 offset:50176\nM v[0:3],a[96:99],v[80:83],v[0:3]\nds_read_b128 a[8:11],v240 offset:51200\nv_max3_f32 v237,v140,v141,v142\nM v[8:11],a[68:71],v[64:67],0\nds_read_b128 a[12:15],v240 offset:52224\nv_exp_f32 v128,v128\nM v[8:11],a[100:103],v[80:83],v[8:11]\n" \
"ds_read_b128 a[16:19],v240 offset:53248\nv_exp_f32 v129,v129\nM v[16:19],a[72:75],v[64:67],0\nv_exp_f32 v130,v130\nM v[16:19],a[104:107],v[80:83],v[16:19]\nv_exp_f32 v131,v131\nds_read_b128 a[20:23],v240 offset:54272\nM v[24:27],a[76:79],v[64:67],0\n" \
"v_cvt_pk_bf16_f32 v192,v128,v129\nv_cvt_pk_bf16_f32 v193,v130,v131\nds_read_b128 a[24:27],v240 offset:55296\nM v[24:27],a[108:111],v[80:83],v[24:27]\nds_read_b128 a[28:31],v240 offset:56320\nv_max3_f32 v237,v237,v143,v156\nM v[32:35],a[80:83],v[64:67],0\nv_exp_f32 v132,v132\n" \
"v_max3_f32 v233,v136,v137,v138\nM v[32:35],a[112:115],v[80:83],v[32:35]\nv_exp_f32 v133,v133\nM v[40:43],a[84:87],v[64:67],0\nv_exp_f32 v134,v134\nv_cvt_pk_bf16_f32 v196,v132,v133\nM v[40:43],a[116:119],v[80:83],v[40:43]\nv_exp_f32 v135,v135\n" \
"s_waitcnt lgkmcnt(4)\nM v[48:51],a[88:91],v[64:67],0\nv_cvt_pk_bf16_f32 v197,v134,v135\nv_max3_f32 v237,v237,v157,v158\nM v[48:51],a[120:123],v[80:83],v[48:51]\nv_exp_f32 v144,v144\nv_max_f32 v233,v233,v139\nM v[56:59],a[92:95],v[64:67],0\n" \
"v_exp_f32 v145,v145\nv_max_f32 v237,v237,v159\nM v[56:59],a[124:127],v[80:83],v[56:59]\nv_exp_f32 v146,v146\nM a[192:195],a[208:211],v[64:67],0\nv_exp_f32 v147,v147\nv_cvt_pk_bf16_f32 v194,v144,v145\nM a[192:195],a[208:211],v[80:83],a[192:195]\n" \
"v_cvt_pk_bf16_f32 v195,v146,v147\nv_cndmask_b32 v233,v232,v233,s[90:91]\nM v[4:7],a[64:67],v[68:71],0\nv_cndmask_b32 v237,v236,v237,s[90:91]\nv_exp_f32 v148,v148\nM v[4:7],a[96:99],v[84:87],v[4:7]\nv_permlane16_swap_b32 v233,v237\nv_max_f32 v233,v233,v237\n" \
"v_mov_b32 v237,v233\nM v[12:15],a[68:71],v[68:71],0\nv_exp_f32 v149,v149\nM v[12:15],a[100:103],v[84:87],v[12:15]\nv_exp_f32 v150,v150\nv_permlane32_swap_b32 v233,v237\nM v[20:23],a[72:75],v[68:71],0\nv_exp_f32 v151,v151\n" \
"v_cvt_pk_bf16_f32 v198,v148,v149\nM v[20:23],a[104:107],v[84:87],v[20:23]\nv_cvt_pk_bf16_f32 v199,v150,v151\nv_max_f32 v233,v233,v237\nM v[28:31],a[76:79],v[68:71],0\nv_mov_b32 v237,v233\nv_cndmask_b32 v136,v136,v252,s[82:83]\nv_cndmask_b32 v137,v137,v252,s[84:85]\n" \
"M v[28:31],a[108:111],v[84:87],v[28:31]\nv_permlane16_swap_b32 v233,v237\nv_cndmask_b32 v138,v138,v252,s[86:87]\nv_cndmask_b32 v139,v139,v252,s[88:89]\nM v[36:39],a[80:83],v[68:71],0\nv_sub_f32 v232,0,v233\nv_sub_f32 v234,0,v233\nM v[36:39],a[112:115],v[84:87],v[36:39]\n" \
"v_sub_f32 v235,0,v233\nv_add_f32 v136,v136,v232\nv_add_f32 v137,v137,v232\nM v[44:47],a[84:87],v[68:71],0\nv_add_f32 v138,v138,v232\nv_add_f32 v139,v139,v232\nM v[44:47],a[116:119],v[84:87],v[44:47]\nv_sub_f32 v233,0,v233\n" \
"v_exp_f32 v136,v136\nM v[52:55],a[88:91],v[68:71],0\nv_exp_f32 v137,v137\nv_sub_f32 v236,0,v237\nM v[52:55],a[120:123],v[84:87],v[52:55]\nv_exp_f32 v138,v138\nM v[60:63],a[92:95],v[68:71],0\nv_exp_f32 v139,v139\n" \
"v_cvt_pk_bf16_f32 v200,v136,v137\nM v[60:63],a[124:127],v[84:87],v[60:63]\nv_cvt_pk_bf16_f32 v201,v138,v139\nv_sub_f32 v238,0,v237\nv_sub_f32 v239,0,v237\nM a[196:199],a[208:211],v[68:71],0\nv_add_f32 v140,v140,v236\nv_add_f32 v141,v141,v236\n" \
"M a[196:199],a[208:211],v[84:87],a[196:199]\nv_add_f32 v142,v142,v236\nv_add_f32 v143,v143,v236\nv_sub_f32 v237,0,v237\nM v[128:131],a[0:3],a[128:131],v[224:227]\nv_exp_f32 v140,v140\nds_read_b128 a[32:35],v240 offset:57344\nM v[128:131],a[4:7],a[132:135],v[128:131]\n" \
"v_exp_f32 v141,v141\nM v[128:131],a[8:11],a[136:139],v[128:131]\nv_exp_f32 v142,v142\nv_cvt_pk_bf16_f32 v204,v140,v141\nM v[128:131],a[12:15],a[140:143],v[128:131]\nv_exp_f32 v143,v143\nM v[132:135],a[12:15],a[156:159],v[228:231]\nv_cvt_pk_bf16_f32 v205,v142,v143\n" \
"ds_read_b128 a[36:39],v240 offset:58368\nds_read_b128 a[40:43],v240 offset:59392\nM v[132:135],a[8:11],a[152:155],v[132:135]\nds_read_b128 a[44:47],v240 offset:60416\nv_cndmask_b32 v156,v156,v252,s[82:83]\nv_cndmask_b32 v157,v157,v252,s[84:85]\nM v[132:135],a[4:7],a[148:151],v[132:135]\nv_cndmask_b32 v158,v158,v252,s[86:87]\n" \
"v_cndmask_b32 v159,v159,v252,s[88:89]\nM v[132:135],a[0:3],a[144:147],v[132:135]\nv_add_f32 v156,v156,v236\nv_add_f32 v157,v157,v236\ns_waitcnt lgkmcnt(4)\nM v[144:147],a[16:19],a[128:131],v[224:227]\ns_waitcnt vmcnt(1)\ns_barrier\n" \
"v_add_f32 v158,v158,v236\nM v[144:147],a[20:23],a[132:135],v[144:147]\nv_add_f32 v159,v159,v236\nds_read_b128 a[48:51],v240 offset:61440\nM v[144:147],a[24:27],a[136:139],v[144:147]\nv_exp_f32 v156,v156\nds_read_b128 a[52:55],v240 offset:62464\nM v[144:147],a[28:31],a[140:143],v[144:147]\n" \
"v_exp_f32 v157,v157\nM v[148:151],a[28:31],a[156:159],v[228:231]\nv_exp_f32 v158,v158\nv_cvt_pk_bf16_f32 v206,v156,v157\nM v[148:151],a[24:27],a[152:155],v[148:151]\nv_exp_f32 v159,v159\nds_read_b128 a[56:59],v240 offset:63488\nM v[148:151],a[20:23],a[148:151],v[148:151]\n" \
"v_cvt_pk_bf16_f32 v207,v158,v159\nds_read_b128 a[60:63],v240 offset:64512\nM v[148:151],a[16:19],a[144:147],v[148:151]\nv_exp_f32 v160,v160\nds_read_b128 a[64:67],v241 offset:32768\nM v[136:139],a[0:3],a[160:163],v[232:235]\nv_exp_f32 v161,v161\nds_read_b128 a[96:99],v241 offset:40960\n" \
"M v[136:139],a[4:7],a[164:167],v[136:139]\nv_exp_f32 v162,v162\nM v[136:139],a[8:11],a[168:171],v[136:139]\nv_exp_f32 v163,v163\nv_cvt_pk_bf16_f32 v208,v160,v161\nM v[136:139],a[12:15],a[172:175],v[136:139]\nv_cvt_pk_bf16_f32 v209,v162,v163\nv_exp_f32 v164,v164\n" \
"M v[140:143],a[12:15],a[188:191],v[236:239]\nv_exp_f32 v165,v165\nM v[140:143],a[8:11],a[184:187],v[140:143]\nv_exp_f32 v166,v166\nv_cvt_pk_bf16_f32 v212,v164,v165\nM v[140:143],a[4:7],a[180:183],v[140:143]\nv_exp_f32 v167,v167\nM v[140:143],a[0:3],a[176:179],v[140:143]\n" \
"v_cvt_pk_bf16_f32 v213,v166,v167\nv_exp_f32 v176,v176\nM v[152:155],a[16:19],a[160:163],v[232:235]\nv_exp_f32 v177,v177\nds_read_b128 a[68:71],v241 offset:33792\nM v[152:155],a[20:23],a[164:167],v[152:155]\nv_exp_f32 v178,v178\nM v[152:155],a[24:27],a[168:171],v[152:155]\n" \
"v_exp_f32 v179,v179\nv_cvt_pk_bf16_f32 v210,v176,v177\nM v[152:155],a[28:31],a[172:175],v[152:155]\nv_cvt_pk_bf16_f32 v211,v178,v179\nv_exp_f32 v180,v180\nM v[156:159],a[28:31],a[188:191],v[236:239]\nv_exp_f32 v181,v181\nM v[156:159],a[24:27],a[184:187],v[156:159]\n" \
"v_exp_f32 v182,v182\nv_cvt_pk_bf16_f32 v214,v180,v181\nM v[156:159],a[20:23],a[180:183],v[156:159]\nv_exp_f32 v183,v183\nds_read_b128 a[100:103],v241 offset:41984\nM v[156:159],a[16:19],a[176:179],v[156:159]\nv_cvt_pk_bf16_f32 v215,v182,v183\ns_waitcnt lgkmcnt(2)\n" \
"M v[160:163],a[32:35],a[128:131],v[224:227]\nds_read_b128 a[72:75],v241 offset:34816\nds_read_b128 a[104:107],v241 offset:43008\ns_waitcnt vmcnt(0)\nv_cmp_eq_u32 vcc,0,v248\nM v[160:163],a[36:39],a[132:135],v[160:163]\nds_read_b128 a[76:79],v241 offset:35840\ns_cbranch_vccnz .Lps3\n" \
".Lpb3:\nM v[160:163],a[40:43],a[136:139],v[160:163]\nds_read_b128 a[108:111],v241 offset:44032\nv_exp_f32 v128,v128\nM v[160:163],a[44:47],a[140:143],v[160:163]\nds_read_b128 a[80:83],v241 offset:36864\nv_exp_f32 v129,v129\nM v[164:167],a[44:47],a[156:159],v[228:231]\n" \
"ds_read_b128 a[112:115],v241 offset:45056\ns_add_u32 m0,s64,65536\nM v[164:167],a[40:43],a[152:155],v[164:167]\nds_read_b128 a[84:87],v241 offset:37888\nbuffer_load_dwordx4 v240,s[48:51],s36 offen offset:0 sc1 lds\nM v[164:167],a[36:39],a[148:151],v[164:167]\nds_read_b128 a[116:119],v241 offset:46080\nv_exp_f32 v130,v130\n" \
"M v[164:167],a[32:35],a[144:147],v[164:167]\nv_exp_f32 v131,v131\nM v[176:179],a[48:51],a[128:131],v[224:227]\nds_read_b128 a[88:91],v241 offset:38912\nv_exp_f32 v132,v132\nM v[176:179],a[52:55],a[132:135],v[176:179]\nds_read_b128 a[120:123],v241 offset:47104\nds_read_b128 a[92:95],v241 offset:39936\n" \
"M v[176:179],a[56:59],a[136:139],v[176:179]\nds_read_b128 a[124:127],v241 offset:48128\nbuffer_load_dwordx4 v240,s[48:51],s36 offen offset:1024 sc1 lds\nM v[176:179],a[60:63],a[140:143],v[176:179]\nds_read_b128 a[0:3],v240 offset:0\nv_exp_f32 v133,v133\nM v[180:183],a[60:63],a[156:159],v[228:231]\nv_exp_f32 v134,v134\n" \
"M v[180:183],a[56:59],a[152:155],v[180:183]\nds_read_b128 a[4:7],v240 offset:1024\nv_exp_f32 v135,v135\nM v[180:183],a[52:55],a[148:151],v[180:183]\nds_read_b128 a[8:11],v240 offset:2048\nbuffer_load_dwordx4 v240,s[48:51],s36 offen offset:2048 sc1 lds\nM v[180:183],a[48:51],a[144:147],v[180:183]\nds_read_b128 a[12:15],v240 offset:3072\n" \
"ds_read_b128 a[16:19],v240 offset:4096\nM v[168:171],a[32:35],a[160:163],v[232:235]\nds_read_b128 a[20:23],v240 offset:5120\nv_exp_f32 v144,v144\nM v[168:171],a[36:39],a[164:167],v[168:171]\nds_read_b128 a[24:27],v240 offset:6144\nv_exp_f32 v145,v145\nM v[168:171],a[40:43],a[168:171],v[168:171]\n" \
"buffer_load_dwordx4 v240,s[48:51],s36 offen offset:3072 sc1 lds\nM v[168:171],a[44:47],a[172:175],v[168:171]\nds_read_b128 a[28:31],v240 offset:7168\nv_exp_f32 v146,v146\nM v[172:175],a[44:47],a[188:191],v[236:239]\nv_exp_f32 v147,v147\nM v[172:175],a[40:43],a[184:187],v[172:175]\ns_add_u32 m0,s64,16384\n" \
"v_exp_f32 v148,v148\nM v[172:175],a[36:39],a[180:183],v[172:175]\nbuffer_load_dwordx4 v240,s[44:47],s65 offen offset:0 sc1 lds\nM v[172:175],a[32:35],a[176:179],v[172:175]\nds_read_b128 a[32:35],v240 offset:8192\nds_read_b128 a[36:39],v240 offset:9216\nM v[184:187],a[48:51],a[160:163],v[232:235]\nds_read_b128 a[40:43],v240 offset:10240\n" \
"v_exp_f32 v149,v149\nM v[184:187],a[52:55],a[164:167],v[184:187]\nds_read_b128 a[44:47],v240 offset:11264\nv_exp_f32 v150,v150\nM v[184:187],a[56:59],a[168:171],v[184:187]\nbuffer_load_dwordx4 v240,s[44:47],s65 offen offset:1024 sc1 lds\nM v[184:187],a[60:63],a[172:175],v[184:187]\nv_exp_f32 v151,v151\n" \
"M v[188:191],a[60:63],a[188:191],v[236:239]\nv_exp_f32 v136,v136\nM v[188:191],a[56:59],a[184:187],v[188:191]\nv_exp_f32 v137,v137\nM v[188:191],a[52:55],a[180:183],v[188:191]\nbuffer_load_dwordx4 v240,s[44:47],s65 offen offset:2048 sc1 lds\nM v[188:191],a[48:51],a[176:179],v[188:191]\nds_read_b128 a[48:51],v240 offset:12288\n" \
"ds_read_b128 a[52:55],v240 offset:13312\nM v[0:3],a[64:67],v[192:195],v[0:3]\nds_read_b128 a[56:59],v240 offset:14336\nv_exp_f32 v138,v138\nM v[0:3],a[96:99],v[208:211],v[0:3]\nv_exp_f32 v139,v139\ns_waitcnt lgkmcnt(4)\nM v[8:11],a[68:71],v[192:195],v[8:11]\n" \
"buffer_load_dwordx4 v240,s[44:47],s65 offen offset:3072 sc1 lds\nM v[8:11],a[100:103],v[208:211],v[8:11]\ns_add_u32 m0,s64,81920\nv_exp_f32 v152,v152\nM v[16:19],a[72:75],v[192:195],v[16:19]\nds_read_b128 a[60:63],v240 offset:15360\nv_exp_f32 v153,v153\nM v[16:19],a[104:107],v[208:211],v[16:19]\n" \
"v_exp_f32 v154,v154\nM v[24:27],a[76:79],v[192:195],v[24:27]\nbuffer_load_dwordx4 v240,s[48:51],s65 offen offset:0 sc1 lds\nM v[24:27],a[108:111],v[208:211],v[24:27]\nv_exp_f32 v155,v155\nM v[32:35],a[80:83],v[192:195],v[32:35]\ns_barrier\nv_exp_f32 v160,v160\n" \
"M v[32:35],a[112:115],v[208:211],v[32:35]\nv_exp_f32 v161,v161\nM v[40:43],a[84:87],v[192:195],v[40:43]\nbuffer_load_dwordx4 v240,s[48:51],s65 offen offset:1024 sc1 lds\nM v[40:43],a[116:119],v[208:211],v[40:43]\nv_exp_f32 v162,v162\nM v[48:51],a[88:91],v[192:195],v[48:51]\nv_exp_f32 v163,v163\n" \
"M v[48:51],a[120:123],v[208:211],v[48:51]\nv_exp_f32 v176,v176\nM v[56:59],a[92:95],v[192:195],v[56:59]\nbuffer_load_dwordx4 v240,s[48:51],s65 offen offset:2048 sc1 lds\nM v[56:59],a[124:127],v[208:211],v[56:59]\nv_exp_f32 v177,v177\nM a[192:195],a[208:211],v[192:195],a[192:195]\nv_exp_f32 v178,v178\n" \
"M a[192:195],a[208:211],v[208:211],a[192:195]\nv_exp_f32 v179,v179\nv_cvt_pk_bf16_f32 v192,v128,v129\nM v[4:7],a[64:67],v[196:199],v[4:7]\nbuffer_load_dwordx4 v240,s[48:51],s65 offen offset:3072 sc1 lds\nM v[4:7],a[96:99],v[212:215],v[4:7]\ns_add_u32 m0,s64,32768\nv_exp_f32 v140,v140\n" \
"M v[12:15],a[68:71],v[196:199],v[12:15]\nv_exp_f32 v141,v141\nv_cvt_pk_bf16_f32 v193,v130,v131\nM v[12:15],a[100:103],v[212:215],v[12:15]\nv_exp_f32 v142,v142\nM v[20:23],a[72:75],v[196:199],v[20:23]\nbuffer_load_dwordx4 v240,s[44:47],s66 offen offset:0 sc1 lds\nv_cvt_pk_bf16_f32 v194,v144,v145\n" \
"M v[20:23],a[104:107],v[212:215],v[20:23]\nv_exp_f32 v143,v143\nv_cvt_pk_bf16_f32 v195,v146,v147\nM v[28:31],a[76:79],v[196:199],v[28:31]\nv_exp_f32 v156,v156\nM v[28:31],a[108:111],v[212:215],v[28:31]\nv_exp_f32 v157,v157\nv_cvt_pk_bf16_f32 v208,v160,v161\n" \
"M v[36:39],a[80:83],v[196:199],v[36:39]\nbuffer_load_dwordx4 v240,s[44:47],s66 offen offset:1024 sc1 lds\nM v[36:39],a[112:115],v[212:215],v[36:39]\nv_exp_f32 v158,v158\nv_cvt_pk_bf16_f32 v209,v162,v163\nM v[44:47],a[84:87],v[196:199],v[44:47]\nv_exp_f32 v159,v159\nv_cvt_pk_bf16_f32 v210,v176,v177\n" \
"M v[44:47],a[116:119],v[212:215],v[44:47]\nv_exp_f32 v164,v164\nM v[52:55],a[88:91],v[196:199],v[52:55]\nbuffer_load_dwordx4 v240,s[44:47],s66 offen offset:2048 sc1 lds\nv_cvt_pk_bf16_f32 v211,v178,v179\nM v[52:55],a[120:123],v[212:215],v[52:55]\nv_exp_f32 v165,v165\nM v[60:63],a[92:95],v[196:199],v[60:63]\n" \
"v_exp_f32 v166,v166\nM v[60:63],a[124:127],v[212:215],v[60:63]\nv_exp_f32 v167,v167\nM a[196:199],a[208:211],v[196:199],a[196:199]\nbuffer_load_dwordx4 v240,s[44:47],s66 offen offset:3072 sc1 lds\ns_add_u32 m0,s64,98304\nM a[196:199],a[208:211],v[212:215],a[196:199]\nv_exp_f32 v180,v180\n" \
"M v[64:67],a[64:67],v[200:203],0\nv_exp_f32 v181,v181\nv_cvt_pk_bf16_f32 v196,v132,v133\nM v[72:75],a[68:71],v[200:203],0\nv_exp_f32 v182,v182\nM v[80:83],a[72:75],v[200:203],0\nv_exp_f32 v183,v183\nv_cvt_pk_bf16_f32 v197,v134,v135\n" \
"M v[88:91],a[76:79],v[200:203],0\nv_exp_f32 v168,v168\nv_cvt_pk_bf16_f32 v198,v148,v149\nM v[96:99],a[80:83],v[200:203],0\nv_exp_f32 v169,v169\nM v[104:107],a[84:87],v[200:203],0\nv_exp_f32 v170,v170\nv_cvt_pk_bf16_f32 v199,v150,v151\n" \
"M v[112:115],a[88:91],v[200:203],0\nv_exp_f32 v171,v171\nM v[120:123],a[92:95],v[200:203],0\nbuffer_load_dwordx4 v240,s[48:51],s66 offen offset:0 sc1 lds\nM a[200:203],a[208:211],v[200:203],0\nv_cvt_pk_bf16_f32 v200,v136,v137\nv_cvt_pk_bf16_f32 v201,v138,v139\nv_cvt_pk_bf16_f32 v202,v152,v153\n" \
"M v[68:71],a[64:67],v[204:207],0\nv_cvt_pk_bf16_f32 v203,v154,v155\nM v[76:79],a[68:71],v[204:207],0\nM v[84:87],a[72:75],v[204:207],0\nbuffer_load_dwordx4 v240,s[48:51],s66 offen offset:1024 sc1 lds\nM v[92:95],a[76:79],v[204:207],0\nM v[100:103],a[80:83],v[204:207],0\nM v[108:111],a[84:87],v[204:207],0\n" \
"M v[116:119],a[88:91],v[204:207],0\nbuffer_load_dwordx4 v240,s[48:51],s66 offen offset:2048 sc1 lds\nM v[124:127],a[92:95],v[204:207],0\nbuffer_load_dwordx4 v240,s[48:51],s66 offen offset:3072 sc1 lds\nM a[204:207],a[208:211],v[204:207],0\ns_cmp_lt_u32 s62,2\ns_cbranch_scc1 .Lx01\ns_branch .Ldual\n" \
".Lp23:\ns_cmp_eq_u32 s7,2\ns_cbranch_scc0 .Lpro3\n.Lpro2:\ns_barrier\nM v[64:67],a[0:3],a[128:131],0\nds_read_b128 a[64:67],v241 offset:0\nds_read_b128 a[96:99],v241 offset:8192\n" \
"M v[64:67],a[4:7],a[132:135],v[64:67]\nds_read_b128 a[68:71],v241 offset:1024\ns_mov_b32 m0,s64\nv_mov_b32 v250,139264\nM v[64:67],a[8:11],a[136:139],v[64:67]\nds_read_b128 a[100:103],v241 offset:9216\nbuffer_load_dwordx4 v240,s[44:47],s36 offen offset:0 sc1 lds\nM v[64:67],a[12:15],a[140:143],v[64:67]\n" \
"ds_read_b128 a[72:75],v241 offset:2048\nv_mov_b32 v184,0xff800000\nv_mov_b32 v185,0xff800000\nM v[68:71],a[12:15],a[156:159],0\nds_read_b128 a[12:15],v240 offset:19456\nv_mov_b32 v186,0xff800000\nv_mov_b32 v187,0xff800000\nM v[68:71],a[8:11],a[152:155],v[68:71]\n" \
"ds_read_b128 a[8:11],v240 offset:18432\nv_max3_f32 v225,v64,v65,v66\nM v[68:71],a[4:7],a[148:151],v[68:71]\nds_read_b128 a[4:7],v240 offset:17408\nbuffer_load_dwordx4 v240,s[44:47],s36 offen offset:1024 sc1 lds\nM v[68:71],a[0:3],a[144:147],v[68:71]\nds_read_b128 a[0:3],v240 offset:16384\nM v[72:75],a[16:19],a[128:131],0\n" \
"ds_read_b128 a[104:107],v241 offset:10240\nM v[72:75],a[20:23],a[132:135],v[72:75]\nds_read_b128 a[76:79],v241 offset:3072\nM v[72:75],a[24:27],a[136:139],v[72:75]\nds_read_b128 a[108:111],v241 offset:11264\nbuffer_load_dwordx4 v240,s[44:47],s36 offen offset:2048 sc1 lds\nM v[72:75],a[28:31],a[140:143],v[72:75]\nv_max3_f32 v229,v68,v69,v70\n" \
"ds_write_b128 v250,a[244:247]\nM v[76:79],a[28:31],a[156:159],0\nds_read_b128 a[28:31],v240 offset:23552\nM v[76:79],a[24:27],a[152:155],v[76:79]\nds_read_b128 a[24:27],v240 offset:22528\nM v[76:79],a[20:23],a[148:151],v[76:79]\nds_read_b128 a[20:23],v240 offset:21504\nv_max3_f32 v225,v225,v67,v72\n" \
"v_max3_f32 v225,v225,v73,v74\nM v[76:79],a[16:19],a[144:147],v[76:79]\nv_max_f32 v225,v225,v75\nv_cndmask_b32 v225,v224,v225,s[90:91]\nds_read_b128 a[16:19],v240 offset:20480\nM v[80:83],a[32:35],a[128:131],0\nds_read_b128 a[80:83],v241 offset:4096\nbuffer_load_dwordx4 v240,s[44:47],s36 offen offset:3072 sc1 lds\n" \
"M v[80:83],a[36:39],a[132:135],v[80:83]\nds_read_b128 a[112:115],v241 offset:12288\nv_max3_f32 v229,v229,v71,v76\nv_max3_f32 v229,v229,v77,v78\nM v[80:83],a[40:43],a[136:139],v[80:83]\nv_max_f32 v229,v229,v79\nv_cndmask_b32 v229,v228,v229,s[90:91]\nds_read_b128 a[84:87],v241 offset:5120\n" \
"M v[80:83],a[44:47],a[140:143],v[80:83]\nv_permlane16_swap_b32 v225,v229\nv_max_f32 v225,v225,v229\nv_mov_b32 v229,v225\nM v[84:87],a[44:47],a[156:159],0\nds_read_b128 a[116:119],v241 offset:13312\nv_permlane32_swap_b32 v225,v229\nv_max_f32 v225,v225,v229\n" \
"M v[84:87],a[40:43],a[152:155],v[84:87]\nv_mov_b32 v229,v225\nds_read_b128 a[88:91],v241 offset:6144\nds_read_b128 a[120:123],v241 offset:14336\nM v[84:87],a[36:39],a[148:151],v[84:87]\nv_permlane16_swap_b32 v225,v229\nv_sub_f32 v224,0,v225\nv_sub_f32 v226,0,v225\n" \
"M v[84:87],a[32:35],a[144:147],v[84:87]\nv_sub_f32 v227,0,v225\nv_sub_f32 v225,0,v225\nv_sub_f32 v228,0,v229\nM v[88:91],a[48:51],a[128:131],0\nv_sub_f32 v230,0,v229\nv_sub_f32 v231,0,v229\nM v[88:91],a[52:55],a[132:135],v[88:91]\n" \
"v_sub_f32 v229,0,v229\nv_add_f32 v64,v64,v224\nv_add_f32 v65,v65,v224\nM v[88:91],a[56:59],a[136:139],v[88:91]\nv_add_f32 v66,v66,v224\nv_add_f32 v67,v67,v224\nv_add_f32 v72,v72,v224\nM v[88:91],a[60:63],a[140:143],v[88:91]\n" \
"v_exp_f32 v64,v64\nv_add_f32 v73,v73,v224\nM v[92:95],a[60:63],a[156:159],0\nv_exp_f32 v65,v65\nv_add_f32 v74,v74,v224\nM v[92:95],a[56:59],a[152:155],v[92:95]\nv_exp_f32 v66,v66\nv_add_f32 v75,v75,v224\n" \
"M v[92:95],a[52:55],a[148:151],v[92:95]\nv_exp_f32 v67,v67\nv_add_f32 v80,v80,v224\nM v[92:95],a[48:51],a[144:147],v[92:95]\nv_add_f32 v81,v81,v224\nv_add_f32 v82,v82,v224\ns_waitcnt lgkmcnt(0)\nM v[96:99],a[0:3],a[128:131],v[224:227]\n" \
"v_exp_f32 v72,v72\nv_add_f32 v83,v83,v224\nM v[96:99],a[4:7],a[132:135],v[96:99]\nv_exp_f32 v73,v73\nv_cvt_pk_bf16_f32 v64,v64,v65\nM v[96:99],a[8:11],a[136:139],v[96:99]\nv_exp_f32 v74,v74\nv_add_f32 v88,v88,v224\n" \
"M v[96:99],a[12:15],a[140:143],v[96:99]\nv_exp_f32 v75,v75\nv_add_f32 v89,v89,v224\nM v[100:103],a[12:15],a[156:159],v[228:231]\nv_exp_f32 v80,v80\nv_add_f32 v90,v90,v224\nM v[100:103],a[8:11],a[152:155],v[100:103]\nv_exp_f32 v81,v81\n" \
"M v[100:103],a[4:7],a[148:151],v[100:103]\nv_exp_f32 v82,v82\nv_add_f32 v91,v91,v224\nM v[100:103],a[0:3],a[144:147],v[100:103]\nv_exp_f32 v83,v83\nv_cvt_pk_bf16_f32 v65,v66,v67\nM v[108:111],a[28:31],a[156:159],v[228:231]\nv_exp_f32 v88,v88\n" \
"v_cvt_pk_bf16_f32 v66,v72,v73\nM v[108:111],a[24:27],a[152:155],v[108:111]\nv_exp_f32 v89,v89\nv_cvt_pk_bf16_f32 v67,v74,v75\nM v[108:111],a[20:23],a[148:151],v[108:111]\nv_exp_f32 v90,v90\nv_exp_f32 v91,v91\nv_cvt_pk_bf16_f32 v80,v80,v81\n" \
"M v[108:111],a[16:19],a[144:147],v[108:111]\nv_cvt_pk_bf16_f32 v81,v82,v83\nv_cvt_pk_bf16_f32 v82,v88,v89\nv_cvt_pk_bf16_f32 v83,v90,v91\nM v[0:3],a[64:67],v[64:67],0\nds_read_b128 a[92:95],v241 offset:7168\nds_read_b128 a[124:127],v241 offset:15360\nv_add_f32 v68,v68,v228\n" \
"M v[0:3],a[96:99],v[80:83],v[0:3]\nv_add_f32 v69,v69,v228\nv_add_f32 v70,v70,v228\nv_add_f32 v71,v71,v228\nM v[8:11],a[68:71],v[64:67],0\nv_exp_f32 v68,v68\nv_add_f32 v76,v76,v228\nM v[8:11],a[100:103],v[80:83],v[8:11]\n" \
"v_exp_f32 v69,v69\nv_add_f32 v77,v77,v228\nM v[16:19],a[72:75],v[64:67],0\nv_exp_f32 v70,v70\nv_add_f32 v78,v78,v228\nM v[16:19],a[104:107],v[80:83],v[16:19]\nv_exp_f32 v71,v71\nM v[24:27],a[76:79],v[64:67],0\n" \
"v_add_f32 v79,v79,v228\nv_add_f32 v84,v84,v228\nv_add_f32 v85,v85,v228\nM v[24:27],a[108:111],v[80:83],v[24:27]\nv_exp_f32 v76,v76\nv_add_f32 v86,v86,v228\nM v[32:35],a[80:83],v[64:67],0\nv_exp_f32 v77,v77\n" \
"v_add_f32 v87,v87,v228\nM v[32:35],a[112:115],v[80:83],v[32:35]\nv_exp_f32 v78,v78\nv_cvt_pk_bf16_f32 v68,v68,v69\nM v[40:43],a[84:87],v[64:67],0\nv_exp_f32 v79,v79\nv_add_f32 v92,v92,v228\nM v[40:43],a[116:119],v[80:83],v[40:43]\n" \
"v_exp_f32 v84,v84\nv_add_f32 v93,v93,v228\nM v[48:51],a[88:91],v[64:67],0\nv_exp_f32 v85,v85\nv_add_f32 v94,v94,v228\nM v[48:51],a[120:123],v[80:83],v[48:51]\nv_exp_f32 v86,v86\ns_waitcnt lgkmcnt(0)\n" \
"M v[56:59],a[92:95],v[64:67],0\nv_exp_f32 v87,v87\nv_add_f32 v95,v95,v228\nM v[56:59],a[124:127],v[80:83],v[56:59]\nv_cvt_pk_bf16_f32 v69,v70,v71\nv_exp_f32 v92,v92\nM a[192:195],a[208:211],v[64:67],0\nv_exp_f32 v93,v93\n" \
"v_exp_f32 v94,v94\nv_exp_f32 v95,v95\nv_cvt_pk_bf16_f32 v70,v76,v77\nv_cvt_pk_bf16_f32 v71,v78,v79\nv_cvt_pk_bf16_f32 v84,v84,v85\nM a[192:195],a[208:211],v[80:83],a[192:195]\nv_cvt_pk_bf16_f32 v85,v86,v87\nv_cvt_pk_bf16_f32 v86,v92,v93\n" \
"v_cvt_pk_bf16_f32 v87,v94,v95\nM v[4:7],a[64:67],v[68:71],0\nds_read_b128 a[0:3],v240 offset:32768\nds_read_b128 a[4:7],v240 offset:33792\nM v[4:7],a[96:99],v[84:87],v[4:7]\nds_read_b128 a[8:11],v240 offset:34816\nv_cndmask_b32 v96,v96,v252,s[82:83]\nv_cndmask_b32 v97,v97,v252,s[84:85]\n" \
"M v[12:15],a[68:71],v[68:71],0\nds_read_b128 a[12:15],v240 offset:35840\nv_cndmask_b32 v98,v98,v252,s[86:87]\nv_cndmask_b32 v99,v99,v252,s[88:89]\nM v[12:15],a[100:103],v[84:87],v[12:15]\nds_read_b128 a[16:19],v240 offset:36864\nv_exp_f32 v96,v96\nM v[20:23],a[72:75],v[68:71],0\n" \
"ds_read_b128 a[20:23],v240 offset:37888\nv_exp_f32 v97,v97\nM v[20:23],a[104:107],v[84:87],v[20:23]\nds_read_b128 a[24:27],v240 offset:38912\nv_exp_f32 v98,v98\nM v[28:31],a[76:79],v[68:71],0\nbuffer_load_dword v248,v251,s[52:55],s80 offen sc1\nds_read_b128 a[28:31],v240 offset:39936\n" \
"M v[28:31],a[108:111],v[84:87],v[28:31]\nds_read_b128 a[32:35],v240 offset:40960\nv_exp_f32 v99,v99\nM v[36:39],a[80:83],v[68:71],0\nds_read_b128 a[36:39],v240 offset:41984\nv_cvt_pk_bf16_f32 v96,v96,v97\nv_cvt_pk_bf16_f32 v97,v98,v99\nM v[36:39],a[112:115],v[84:87],v[36:39]\n" \
"ds_read_b128 a[40:43],v240 offset:43008\nv_mov_b32 v98,0\nv_mov_b32 v99,0\nM v[44:47],a[84:87],v[68:71],0\nds_read_b128 a[44:47],v240 offset:44032\nv_exp_f32 v100,v100\nM v[44:47],a[116:119],v[84:87],v[44:47]\nds_read_b128 a[48:51],v240 offset:45056\n" \
"v_exp_f32 v101,v101\nM v[52:55],a[88:91],v[68:71],0\nds_read_b128 a[52:55],v240 offset:46080\nv_cndmask_b32 v108,v108,v252,s[82:83]\nM v[52:55],a[120:123],v[84:87],v[52:55]\nds_read_b128 a[56:59],v240 offset:47104\nv_cndmask_b32 v109,v109,v252,s[84:85]\nv_cndmask_b32 v110,v110,v252,s[86:87]\n" \
"M v[60:63],a[92:95],v[68:71],0\nds_read_b128 a[60:63],v240 offset:48128\nv_cndmask_b32 v111,v111,v252,s[88:89]\nv_cvt_pk_bf16_f32 v100,v100,v101\nM v[60:63],a[124:127],v[84:87],v[60:63]\nds_read_b128 a[64:67],v241 offset:16384\nv_exp_f32 v102,v102\nM a[196:199],a[208:211],v[68:71],0\n" \
"ds_read_b128 a[68:71],v241 offset:17408\nv_exp_f32 v103,v103\nM a[196:199],a[208:211],v[84:87],a[196:199]\nds_read_b128 a[72:75],v241 offset:18432\nv_cvt_pk_bf16_f32 v101,v102,v103\ns_waitcnt lgkmcnt(8)\nM v[128:131],a[0:3],a[128:131],v[224:227]\nds_read_b128 a[76:79],v241 offset:19456\n" \
"v_exp_f32 v108,v108\nM v[128:131],a[4:7],a[132:135],v[128:131]\nds_read_b128 a[80:83],v241 offset:20480\nv_exp_f32 v109,v109\nM v[128:131],a[8:11],a[136:139],v[128:131]\nds_read_b128 a[84:87],v241 offset:21504\nv_exp_f32 v110,v110\nM v[128:131],a[12:15],a[140:143],v[128:131]\n" \
"ds_read_b128 a[88:91],v241 offset:22528\nv_exp_f32 v111,v111\nM v[132:135],a[12:15],a[156:159],v[228:231]\nds_read_b128 a[92:95],v241 offset:23552\nv_cvt_pk_bf16_f32 v102,v108,v109\nv_cvt_pk_bf16_f32 v103,v110,v111\nM v[132:135],a[8:11],a[152:155],v[132:135]\nds_read_b128 a[8:11],v240 offset:51200\n" \
"v_exp_f32 v128,v128\nM v[132:135],a[4:7],a[148:151],v[132:135]\nds_read_b128 a[4:7],v240 offset:50176\nv_exp_f32 v129,v129\nM v[132:135],a[0:3],a[144:147],v[132:135]\nds_read_b128 a[0:3],v240 offset:49152\nv_cvt_pk_bf16_f32 v192,v128,v129\nM v[144:147],a[16:19],a[128:131],v[224:227]\n" \
"ds_read_b128 a[12:15],v240 offset:52224\nv_exp_f32 v130,v130\nM v[144:147],a[20:23],a[132:135],v[144:147]\nv_exp_f32 v131,v131\nds_read_b128 a[96:99],v241 offset:40960\nM v[144:147],a[24:27],a[136:139],v[144:147]\nv_cvt_pk_bf16_f32 v193,v130,v131\nv_exp_f32 v132,v132\n" \
"M v[144:147],a[28:31],a[140:143],v[144:147]\nv_exp_f32 v133,v133\nds_read_b128 v[64:67],v250\nM v[148:151],a[28:31],a[156:159],v[228:231]\nds_read_b128 a[28:31],v240 offset:56320\nv_exp_f32 v134,v134\nM v[148:151],a[24:27],a[152:155],v[148:151]\nds_read_b128 a[24:27],v240 offset:55296\n" \
"v_exp_f32 v135,v135\nM v[148:151],a[20:23],a[148:151],v[148:151]\nds_read_b128 a[20:23],v240 offset:54272\nv_cvt_pk_bf16_f32 v196,v132,v133\nv_cvt_pk_bf16_f32 v197,v134,v135\nM v[148:151],a[16:19],a[144:147],v[148:151]\nds_read_b128 a[16:19],v240 offset:53248\nv_exp_f32 v144,v144\n" \
"M v[160:163],a[32:35],a[128:131],v[224:227]\nv_exp_f32 v145,v145\nds_read_b128 v[68:71],v250\nM v[160:163],a[36:39],a[132:135],v[160:163]\nv_exp_f32 v146,v146\nv_cvt_pk_bf16_f32 v194,v144,v145\nM v[160:163],a[40:43],a[136:139],v[160:163]\nv_exp_f32 v147,v147\n" \
"s_waitcnt lgkmcnt(6)\nM v[160:163],a[44:47],a[140:143],v[160:163]\nv_cvt_pk_bf16_f32 v195,v146,v147\nds_read_b128 v[72:75],v250\nM v[164:167],a[44:47],a[156:159],v[228:231]\nds_read_b128 a[44:47],v240 offset:60416\nv_exp_f32 v148,v148\nM v[164:167],a[40:43],a[152:155],v[164:167]\n" \
"ds_read_b128 a[40:43],v240 offset:59392\nv_exp_f32 v149,v149\nM v[164:167],a[36:39],a[148:151],v[164:167]\nds_read_b128 a[36:39],v240 offset:58368\nv_exp_f32 v150,v150\nM v[164:167],a[32:35],a[144:147],v[164:167]\nds_read_b128 a[32:35],v240 offset:57344\nv_exp_f32 v151,v151\n" \
"M v[176:179],a[48:51],a[128:131],v[224:227]\nv_cvt_pk_bf16_f32 v198,v148,v149\nv_cvt_pk_bf16_f32 v199,v150,v151\nds_read_b128 v[76:79],v250\nM v[176:179],a[52:55],a[132:135],v[176:179]\nv_exp_f32 v160,v160\nds_read_b128 v[80:83],v250\nM v[176:179],a[56:59],a[136:139],v[176:179]\n" \
"v_exp_f32 v161,v161\nds_read_b128 v[84:87],v250\nM v[176:179],a[60:63],a[140:143],v[176:179]\nv_exp_f32 v162,v162\nv_cvt_pk_bf16_f32 v208,v160,v161\nM v[180:183],a[60:63],a[156:159],v[228:231]\nv_exp_f32 v163,v163\nds_read_b128 a[60:63],v240 offset:64512\n" \
"M v[180:183],a[56:59],a[152:155],v[180:183]\nv_cvt_pk_bf16_f32 v209,v162,v163\nds_read_b128 a[56:59],v240 offset:63488\nds_read_b128 v[88:91],v250\nM v[180:183],a[52:55],a[148:151],v[180:183]\nds_read_b128 a[52:55],v240 offset:62464\nv_exp_f32 v164,v164\nM v[180:183],a[48:51],a[144:147],v[180:183]\n" \
"ds_read_b128 a[48:51],v240 offset:61440\nv_exp_f32 v165,v165\nM v[0:3],a[64:67],v[96:99],v[0:3]\nv_exp_f32 v166,v166\nM v[8:11],a[68:71],v[96:99],v[8:11]\nv_exp_f32 v167,v167\nv_cvt_pk_bf16_f32 v212,v164,v165\nM v[16:19],a[72:75],v[96:99],v[16:19]\n" \
"v_cvt_pk_bf16_f32 v213,v166,v167\nv_exp_f32 v176,v176\nM v[24:27],a[76:79],v[96:99],v[24:27]\nv_exp_f32 v177,v177\nds_read_b128 v[92:95],v250\nM v[32:35],a[80:83],v[96:99],v[32:35]\nv_exp_f32 v178,v178\nv_cvt_pk_bf16_f32 v210,v176,v177\n" \
"M v[40:43],a[84:87],v[96:99],v[40:43]\nv_exp_f32 v179,v179\nds_read_b128 v[104:107],v250\nM v[48:51],a[88:91],v[96:99],v[48:51]\ns_waitcnt vmcnt(0)\ns_barrier\nv_cvt_pk_bf16_f32 v211,v178,v179\nM v[56:59],a[92:95],v[96:99],v[56:59]\n" \
"v_exp_f32 v180,v180\nds_read_b128 v[108:111],v250\nM a[192:195],a[208:211],v[96:99],a[192:195]\nv_exp_f32 v181,v181\nds_read_b128 v[96:99],v250\nM v[4:7],a[64:67],v[100:103],v[4:7]\nv_exp_f32 v182,v182\nv_cvt_pk_bf16_f32 v214,v180,v181\n" \
"M v[12:15],a[68:71],v[100:103],v[12:15]\nv_exp_f32 v183,v183\nds_read_b128 a[64:67],v241 offset:32768\nM v[20:23],a[72:75],v[100:103],v[20:23]\nv_cvt_pk_bf16_f32 v215,v182,v183\nds_read_b128 a[68:71],v241 offset:33792\nds_read_b128 v[112:115],v250\nM v[28:31],a[76:79],v[100:103],v[28:31]\n" \
"ds_read_b128 v[116:119],v250\nv_cmp_eq_u32 vcc,0,v248\nM v[36:39],a[80:83],v[100:103],v[36:39]\nds_read_b128 v[120:123],v250\ns_cbranch_vccnz .Lps4\n.Lpb4:\ns_add_u32 m0,s64,65536\nM v[44:47],a[84:87],v[100:103],v[44:47]\n" \
"ds_read_b128 v[124:127],v250\nbuffer_load_dwordx4 v240,s[48:51],s36 offen offset:0 sc1 lds\nM v[52:55],a[88:91],v[100:103],v[52:55]\nds_read_b128 a[200:203],v250\nM v[60:63],a[92:95],v[100:103],v[60:63]\nds_read_b128 a[204:207],v250\nM a[196:199],a[208:211],v[100:103],a[196:199]\nds_read_b128 v[100:103],v250\n" \
"v_add_u32 v250,0xc000,v241\nM v[136:139],a[0:3],a[160:163],0\nds_read_b128 a[100:103],v241 offset:41984\nbuffer_load_dwordx4 v240,s[48:51],s36 offen offset:1024 sc1 lds\nM v[136:139],a[4:7],a[164:167],v[136:139]\nds_read_b128 a[72:75],v241 offset:34816\nM v[136:139],a[8:11],a[168:171],v[136:139]\nds_read_b128 a[104:107],v241 offset:43008\n" \
"M v[136:139],a[12:15],a[172:175],v[136:139]\nds_read_b128 a[76:79],v241 offset:35840\nM v[140:143],a[12:15],a[188:191],0\nds_read_b128 a[108:111],v241 offset:44032\nbuffer_load_dwordx4 v240,s[48:51],s36 offen offset:2048 sc1 lds\nM v[140:143],a[8:11],a[184:187],v[140:143]\nds_read_b128 a[80:83],v241 offset:36864\nM v[140:143],a[4:7],a[180:183],v[140:143]\n" \
"ds_read_b128 a[112:115],v241 offset:45056\nv_max3_f32 v233,v136,v137,v138\nM v[140:143],a[0:3],a[176:179],v[140:143]\nds_read_b128 a[84:87],v241 offset:37888\ns_waitcnt lgkmcnt(11)\nM v[152:155],a[16:19],a[160:163],0\nds_read_b128 a[116:119],v241 offset:46080\nbuffer_load_dwordx4 v240,s[48:51],s36 offen offset:3072 sc1 lds\n" \
"M v[152:155],a[20:23],a[164:167],v[152:155]\nds_read_b128 a[88:91],v241 offset:38912\ns_add_u32 m0,s64,16384\nv_max3_f32 v237,v140,v141,v142\nM v[152:155],a[24:27],a[168:171],v[152:155]\nds_read_b128 a[120:123],v241 offset:47104\nM v[152:155],a[28:31],a[172:175],v[152:155]\nds_read_b128 a[92:95],v241 offset:39936\n" \
"M v[156:159],a[28:31],a[188:191],0\nds_read_b128 a[124:127],v241 offset:48128\nbuffer_load_dwordx4 v240,s[44:47],s65 offen offset:0 sc1 lds\nM v[156:159],a[24:27],a[184:187],v[156:159]\nM v[156:159],a[20:23],a[180:183],v[156:159]\nM v[156:159],a[16:19],a[176:179],v[156:159]\nM v[128:131],a[0:3],a[128:131],v[224:227]\nv_max3_f32 v233,v233,v139,v152\n" \
"v_max3_f32 v233,v233,v153,v154\nv_max_f32 v233,v233,v155\nM v[128:131],a[4:7],a[132:135],v[128:131]\nv_cndmask_b32 v233,v232,v233,s[90:91]\nbuffer_load_dwordx4 v240,s[44:47],s65 offen offset:1024 sc1 lds\nM v[128:131],a[8:11],a[136:139],v[128:131]\nv_max3_f32 v237,v237,v143,v156\nv_max3_f32 v237,v237,v157,v158\n" \
"v_max_f32 v237,v237,v159\nM v[128:131],a[12:15],a[140:143],v[128:131]\nv_cndmask_b32 v237,v236,v237,s[90:91]\nM v[132:135],a[12:15],a[156:159],v[228:231]\nM v[132:135],a[8:11],a[152:155],v[132:135]\nv_permlane16_swap_b32 v233,v237\nv_max_f32 v233,v233,v237\nv_mov_b32 v237,v233\n" \
"M v[132:135],a[4:7],a[148:151],v[132:135]\nbuffer_load_dwordx4 v240,s[44:47],s65 offen offset:2048 sc1 lds\nv_permlane32_swap_b32 v233,v237\nM v[132:135],a[0:3],a[144:147],v[132:135]\nv_max_f32 v233,v233,v237\nv_mov_b32 v237,v233\nds_read_b128 a[0:3],v240 offset:0\nM v[144:147],a[16:19],a[128:131],v[224:227]\n" \
"v_permlane16_swap_b32 v233,v237\nv_sub_f32 v232,0,v233\nv_sub_f32 v234,0,v233\nM v[144:147],a[20:23],a[132:135],v[144:147]\nv_sub_f32 v235,0,v233\nv_sub_f32 v233,0,v233\nv_sub_f32 v236,0,v237\nM v[144:147],a[24:27],a[136:139],v[144:147]\n" \
"v_sub_f32 v238,0,v237\nv_sub_f32 v239,0,v237\nv_sub_f32 v237,0,v237\nM v[144:147],a[28:31],a[140:143],v[144:147]\nds_read_b128 a[4:7],v240 offset:1024\nds_read_b128 a[8:11],v240 offset:2048\nv_add_f32 v136,v136,v232\nM v[148:151],a[28:31],a[156:159],v[228:231]\n" \
"ds_read_b128 a[12:15],v240 offset:3072\nbuffer_load_dwordx4 v240,s[44:47],s65 offen offset:3072 sc1 lds\nM v[148:151],a[24:27],a[152:155],v[148:151]\ns_add_u32 m0,s64,81920\nv_add_f32 v137,v137,v232\nv_add_f32 v138,v138,v232\nM v[148:151],a[20:23],a[148:151],v[148:151]\nv_add_f32 v139,v139,v232\n" \
"v_add_f32 v152,v152,v232\nv_add_f32 v153,v153,v232\nM v[148:151],a[16:19],a[144:147],v[148:151]\nds_read_b128 a[16:19],v240 offset:4096\nds_read_b128 a[20:23],v240 offset:5120\nv_add_f32 v154,v154,v232\nM v[160:163],a[32:35],a[128:131],v[224:227]\nds_read_b128 a[24:27],v240 offset:6144\n" \
"v_add_f32 v155,v155,v232\nM v[160:163],a[36:39],a[132:135],v[160:163]\nds_read_b128 a[28:31],v240 offset:7168\nbuffer_load_dwordx4 v240,s[48:51],s65 offen offset:0 sc1 lds\nM v[160:163],a[40:43],a[136:139],v[160:163]\nv_exp_f32 v136,v136\nv_add_f32 v140,v140,v236\nM v[160:163],a[44:47],a[140:143],v[160:163]\n" \
"v_exp_f32 v137,v137\nv_add_f32 v141,v141,v236\nM v[164:167],a[44:47],a[156:159],v[228:231]\nv_exp_f32 v138,v138\nv_add_f32 v142,v142,v236\nM v[164:167],a[40:43],a[152:155],v[164:167]\nbuffer_load_dwordx4 v240,s[48:51],s65 offen offset:1024 sc1 lds\nv_add_f32 v143,v143,v236\n" \
"M v[164:167],a[36:39],a[148:151],v[164:167]\nv_exp_f32 v139,v139\nv_add_f32 v156,v156,v236\nM v[164:167],a[32:35],a[144:147],v[164:167]\nv_exp_f32 v152,v152\nv_add_f32 v157,v157,v236\nM v[176:179],a[48:51],a[128:131],v[224:227]\nv_exp_f32 v153,v153\n" \
"v_add_f32 v158,v158,v236\nM v[176:179],a[52:55],a[132:135],v[176:179]\nbuffer_load_dwordx4 v240,s[48:51],s65 offen offset:2048 sc1 lds\nv_add_f32 v159,v159,v236\nM v[176:179],a[56:59],a[136:139],v[176:179]\nv_exp_f32 v154,v154\nv_cvt_pk_bf16_f32 v200,v136,v137\nM v[176:179],a[60:63],a[140:143],v[176:179]\n" \
"v_exp_f32 v155,v155\nv_cvt_pk_bf16_f32 v201,v138,v139\nM v[180:183],a[60:63],a[156:159],v[228:231]\nv_exp_f32 v128,v128\nM v[180:183],a[56:59],a[152:155],v[180:183]\nbuffer_load_dwordx4 v240,s[48:51],s65 offen offset:3072 sc1 lds\ns_add_u32 m0,s64,32768\nM v[180:183],a[52:55],a[148:151],v[180:183]\n" \
"v_exp_f32 v129,v129\nv_cvt_pk_bf16_f32 v202,v152,v153\nM v[180:183],a[48:51],a[144:147],v[180:183]\nv_exp_f32 v130,v130\nv_cvt_pk_bf16_f32 v203,v154,v155\nM v[168:171],a[32:35],a[160:163],v[232:235]\nv_exp_f32 v131,v131\nM v[168:171],a[36:39],a[164:167],v[168:171]\n" \
"buffer_load_dwordx4 v240,s[44:47],s66 offen offset:0 sc1 lds\nM v[168:171],a[40:43],a[168:171],v[168:171]\nv_exp_f32 v132,v132\nM v[168:171],a[44:47],a[172:175],v[168:171]\nv_exp_f32 v133,v133\nM v[172:175],a[44:47],a[188:191],v[236:239]\nv_exp_f32 v134,v134\nM v[172:175],a[40:43],a[184:187],v[172:175]\n" \
"buffer_load_dwordx4 v240,s[44:47],s66 offen offset:1024 sc1 lds\nM v[172:175],a[36:39],a[180:183],v[172:175]\ns_waitcnt lgkmcnt(0)\ns_barrier\nv_cndmask_b32 v168,v168,v252,s[82:83]\nM v[172:175],a[32:35],a[176:179],v[172:175]\nds_read_b128 a[32:35],v240 offset:8192\nds_read_b128 a[36:39],v240 offset:9216\n" \
"v_cndmask_b32 v169,v169,v252,s[84:85]\nM v[188:191],a[60:63],a[188:191],v[236:239]\nds_read_b128 a[40:43],v240 offset:10240\nv_exp_f32 v135,v135\nM v[188:191],a[56:59],a[184:187],v[188:191]\nds_read_b128 a[44:47],v240 offset:11264\nv_cndmask_b32 v170,v170,v252,s[86:87]\nM v[188:191],a[52:55],a[180:183],v[188:191]\n" \
"buffer_load_dwordx4 v240,s[44:47],s66 offen offset:2048 sc1 lds\nv_cndmask_b32 v171,v171,v252,s[88:89]\nM v[188:191],a[48:51],a[176:179],v[188:191]\nds_read_b128 a[48:51],v240 offset:12288\nv_exp_f32 v144,v144\nM v[0:3],a[64:67],v[192:195],v[0:3]\nds_read_b128 a[52:55],v240 offset:13312\nv_exp_f32 v145,v145\n" \
"M v[0:3],a[96:99],v[208:211],v[0:3]\nds_read_b128 a[56:59],v240 offset:14336\nv_exp_f32 v146,v146\nM v[8:11],a[68:71],v[192:195],v[8:11]\nds_read_b128 a[60:63],v240 offset:15360\nbuffer_load_dwordx4 v240,s[44:47],s66 offen offset:3072 sc1 lds\nM v[8:11],a[100:103],v[208:211],v[8:11]\nv_exp_f32 v147,v147\n" \
"s_add_u32 m0,s64,98304\nM v[16:19],a[72:75],v[192:195],v[16:19]\nv_exp_f32 v148,v148\nv_cndmask_b32 v188,v188,v252,s[82:83]\nM v[16:19],a[104:107],v[208:211],v[16:19]\nv_exp_f32 v149,v149\nv_cndmask_b32 v189,v189,v252,s[84:85]\nM v[24:27],a[76:79],v[192:195],v[24:27]\n" \
"v_exp_f32 v150,v150\nv_cndmask_b32 v190,v190,v252,s[86:87]\nM v[24:27],a[108:111],v[208:211],v[24:27]\nv_exp_f32 v151,v151\nv_cndmask_b32 v191,v191,v252,s[88:89]\nM v[32:35],a[80:83],v[192:195],v[32:35]\nv_exp_f32 v160,v160\nM v[32:35],a[112:115],v[208:211],v[32:35]\n" \
"v_exp_f32 v161,v161\nM v[40:43],a[84:87],v[192:195],v[40:43]\nv_exp_f32 v162,v162\nM v[40:43],a[116:119],v[208:211],v[40:43]\nv_exp_f32 v163,v163\nM v[48:51],a[88:91],v[192:195],v[48:51]\nv_exp_f32 v176,v176\nM v[48:51],a[120:123],v[208:211],v[48:51]\n" \
"v_exp_f32 v177,v177\nM v[56:59],a[92:95],v[192:195],v[56:59]\nv_exp_f32 v178,v178\nM v[56:59],a[124:127],v[208:211],v[56:59]\nv_exp_f32 v179,v179\nM a[192:195],a[208:211],v[192:195],a[192:195]\nv_exp_f32 v140,v140\nv_cvt_pk_bf16_f32 v192,v128,v129\n" \
"M a[192:195],a[208:211],v[208:211],a[192:195]\nv_exp_f32 v141,v141\nv_cvt_pk_bf16_f32 v193,v130,v131\nM v[4:7],a[64:67],v[196:199],v[4:7]\nv_exp_f32 v142,v142\nv_cvt_pk_bf16_f32 v194,v144,v145\nM v[4:7],a[96:99],v[212:215],v[4:7]\nv_exp_f32 v143,v143\n" \
"v_cvt_pk_bf16_f32 v195,v146,v147\nM v[12:15],a[68:71],v[196:199],v[12:15]\nv_exp_f32 v156,v156\nv_cvt_pk_bf16_f32 v208,v160,v161\nM v[12:15],a[100:103],v[212:215],v[12:15]\nv_exp_f32 v157,v157\nv_cvt_pk_bf16_f32 v209,v162,v163\nM v[20:23],a[72:75],v[196:199],v[20:23]\n" \
"v_exp_f32 v158,v158\nM v[20:23],a[104:107],v[212:215],v[20:23]\nv_exp_f32 v159,v159\nv_cvt_pk_bf16_f32 v210,v176,v177\nM v[28:31],a[76:79],v[196:199],v[28:31]\nv_exp_f32 v164,v164\nv_cvt_pk_bf16_f32 v211,v178,v179\nM v[28:31],a[108:111],v[212:215],v[28:31]\n" \
"v_exp_f32 v165,v165\nM v[36:39],a[80:83],v[196:199],v[36:39]\nv_exp_f32 v166,v166\nM v[36:39],a[112:115],v[212:215],v[36:39]\nv_exp_f32 v167,v167\nM v[44:47],a[84:87],v[196:199],v[44:47]\nv_exp_f32 v180,v180\nM v[44:47],a[116:119],v[212:215],v[44:47]\n" \
"v_exp_f32 v181,v181\nM v[52:55],a[88:91],v[196:199],v[52:55]\nv_exp_f32 v182,v182\nM v[52:55],a[120:123],v[212:215],v[52:55]\nv_exp_f32 v183,v183\nM v[60:63],a[92:95],v[196:199],v[60:63]\nv_exp_f32 v168,v168\nM v[60:63],a[124:127],v[212:215],v[60:63]\n" \
"v_exp_f32 v169,v169\nM a[196:199],a[208:211],v[196:199],a[196:199]\nv_exp_f32 v170,v170\nv_exp_f32 v171,v171\nv_cvt_pk_bf16_f32 v196,v132,v133\nv_cvt_pk_bf16_f32 v197,v134,v135\nv_cvt_pk_bf16_f32 v198,v148,v149\nv_cvt_pk_bf16_f32 v199,v150,v151\n" \
"buffer_load_dwordx4 v240,s[48:51],s66 offen offset:0 sc1 lds\nbuffer_load_dwordx4 v240,s[48:51],s66 offen offset:1024 sc1 lds\nbuffer_load_dwordx4 v240,s[48:51],s66 offen offset:2048 sc1 lds\nbuffer_load_dwordx4 v240,s[48:51],s66 offen offset:3072 sc1 lds\nM a[196:199],a[208:211],v[212:215],a[196:199]\ns_cmp_lt_u32 s62,2\ns_cbranch_scc1 .Lx01\ns_branch .Ldual\n" \
".Lpro3:\ns_barrier\nM v[64:67],a[0:3],a[128:131],0\nds_read_b128 a[64:67],v241 offset:0\nds_read_b128 a[96:99],v241 offset:8192\nM v[64:67],a[4:7],a[132:135],v[64:67]\nds_read_b128 a[68:71],v241 offset:1024\ns_mov_b32 m0,s64\n" \
"v_mov_b32 v250,143360\nM v[64:67],a[8:11],a[136:139],v[64:67]\nds_read_b128 a[100:103],v241 offset:9216\nbuffer_load_dwordx4 v240,s[44:47],s36 offen offset:0 sc1 lds\nM v[64:67],a[12:15],a[140:143],v[64:67]\nds_read_b128 a[72:75],v241 offset:2048\nv_mov_b32 v168,0\nv_mov_b32 v169,0\n" \
"M v[68:71],a[12:15],a[156:159],0\nds_read_b128 a[12:15],v240 offset:19456\nv_mov_b32 v170,0\nv_mov_b32 v171,0\nM v[68:71],a[8:11],a[152:155],v[68:71]\nds_read_b128 a[8:11],v240 offset:18432\nv_max3_f32 v225,v64,v65,v66\nv_mov_b32 v172,0xff800000\n" \
"M v[68:71],a[4:7],a[148:151],v[68:71]\nds_read_b128 a[4:7],v240 offset:17408\nbuffer_load_dwordx4 v240,s[44:47],s36 offen offset:1024 sc1 lds\nM v[68:71],a[0:3],a[144:147],v[68:71]\nds_read_b128 a[0:3],v240 offset:16384\nv_mov_b32 v173,0xff800000\nv_mov_b32 v174,0xff800000\nM v[72:75],a[16:19],a[128:131],0\n" \
"ds_read_b128 a[104:107],v241 offset:10240\nv_mov_b32 v175,0xff800000\nv_mov_b32 v184,0xff800000\nM v[72:75],a[20:23],a[132:135],v[72:75]\nv_max3_f32 v229,v68,v69,v70\nds_read_b128 a[76:79],v241 offset:3072\nv_mov_b32 v185,0xff800000\nM v[72:75],a[24:27],a[136:139],v[72:75]\n" \
"ds_read_b128 a[108:111],v241 offset:11264\nv_mov_b32 v186,0xff800000\nM v[72:75],a[28:31],a[140:143],v[72:75]\nds_read_b128 a[80:83],v241 offset:4096\nbuffer_load_dwordx4 v240,s[44:47],s36 offen offset:2048 sc1 lds\nM v[76:79],a[28:31],a[156:159],0\nds_read_b128 a[28:31],v240 offset:23552\nv_mov_b32 v187,0xff800000\n" \
"v_mov_b32 v188,0xff800000\nM v[76:79],a[24:27],a[152:155],v[76:79]\nds_read_b128 a[24:27],v240 offset:22528\nv_max3_f32 v225,v225,v67,v72\nv_max3_f32 v225,v225,v73,v74\nM v[76:79],a[20:23],a[148:151],v[76:79]\nv_max_f32 v225,v225,v75\nv_cndmask_b32 v225,v224,v225,s[90:91]\n" \
"ds_read_b128 a[20:23],v240 offset:21504\nM v[76:79],a[16:19],a[144:147],v[76:79]\nds_read_b128 a[16:19],v240 offset:20480\nbuffer_load_dwordx4 v240,s[44:47],s36 offen offset:3072 sc1 lds\nM v[80:83],a[32:35],a[128:131],0\nds_read_b128 a[112:115],v241 offset:12288\nv_mov_b32 v189,0xff800000\nv_mov_b32 v190,0xff800000\n" \
"M v[80:83],a[36:39],a[132:135],v[80:83]\nds_read_b128 a[84:87],v241 offset:5120\nv_max3_f32 v229,v229,v71,v76\nv_max3_f32 v229,v229,v77,v78\nM v[80:83],a[40:43],a[136:139],v[80:83]\nv_max_f32 v229,v229,v79\nv_cndmask_b32 v229,v228,v229,s[90:91]\nds_read_b128 a[116:119],v241 offset:13312\n" \
"M v[80:83],a[44:47],a[140:143],v[80:83]\nv_permlane16_swap_b32 v225,v229\nv_max_f32 v225,v225,v229\nv_mov_b32 v229,v225\nM v[84:87],a[44:47],a[156:159],0\nds_read_b128 a[44:47],v240 offset:27648\nv_permlane32_swap_b32 v225,v229\nv_max_f32 v225,v225,v229\n" \
"M v[84:87],a[40:43],a[152:155],v[84:87]\nv_mov_b32 v229,v225\nds_read_b128 a[40:43],v240 offset:26624\nM v[84:87],a[36:39],a[148:151],v[84:87]\nv_permlane16_swap_b32 v225,v229\nv_sub_f32 v224,0,v225\nv_sub_f32 v226,0,v225\nM v[84:87],a[32:35],a[144:147],v[84:87]\n" \
"v_sub_f32 v227,0,v225\nv_sub_f32 v225,0,v225\nv_sub_f32 v228,0,v229\nM v[88:91],a[48:51],a[128:131],0\nv_sub_f32 v230,0,v229\nv_sub_f32 v231,0,v229\nv_sub_f32 v229,0,v229\nM v[88:91],a[52:55],a[132:135],v[88:91]\n" \
"ds_read_b128 a[32:35],v240 offset:24576\nds_read_b128 a[36:39],v240 offset:25600\nv_add_f32 v64,v64,v224\nM v[88:91],a[56:59],a[136:139],v[88:91]\nv_add_f32 v65,v65,v224\nv_add_f32 v66,v66,v224\nv_add_f32 v67,v67,v224\nM v[88:91],a[60:63],a[140:143],v[88:91]\n" \
"v_exp_f32 v64,v64\nv_add_f32 v72,v72,v224\nM v[92:95],a[60:63],a[156:159],0\nds_read_b128 a[60:63],v240 offset:31744\nv_exp_f32 v65,v65\nM v[92:95],a[56:59],a[152:155],v[92:95]\nds_read_b128 a[56:59],v240 offset:30720\nv_exp_f32 v66,v66\n" \
"M v[92:95],a[52:55],a[148:151],v[92:95]\nds_read_b128 a[52:55],v240 offset:29696\nv_exp_f32 v67,v67\nM v[92:95],a[48:51],a[144:147],v[92:95]\nds_read_b128 a[48:51],v240 offset:28672\nv_add_f32 v73,v73,v224\ns_waitcnt lgkmcnt(6)\nM v[96:99],a[0:3],a[128:131],v[224:227]\n" \
"v_add_f32 v74,v74,v224\nv_add_f32 v75,v75,v224\nM v[96:99],a[4:7],a[132:135],v[96:99]\nv_add_f32 v80,v80,v224\nv_add_f32 v81,v81,v224\nv_add_f32 v82,v82,v224\nM v[96:99],a[8:11],a[136:139],v[96:99]\nv_exp_f32 v72,v72\n" \
"v_add_f32 v83,v83,v224\nM v[96:99],a[12:15],a[140:143],v[96:99]\nv_exp_f32 v73,v73\nv_cvt_pk_bf16_f32 v64,v64,v65\nM v[100:103],a[12:15],a[156:159],v[228:231]\nv_exp_f32 v74,v74\nv_add_f32 v88,v88,v224\nM v[100:103],a[8:11],a[152:155],v[100:103]\n" \
"v_exp_f32 v75,v75\nv_add_f32 v89,v89,v224\nM v[100:103],a[4:7],a[148:151],v[100:103]\nv_exp_f32 v80,v80\nv_add_f32 v90,v90,v224\nM v[100:103],a[0:3],a[144:147],v[100:103]\nv_exp_f32 v81,v81\nv_add_f32 v91,v91,v224\n" \
"M v[104:107],a[16:19],a[128:131],v[224:227]\nv_exp_f32 v82,v82\nv_cvt_pk_bf16_f32 v65,v66,v67\nM v[104:107],a[20:23],a[132:135],v[104:107]\nv_exp_f32 v83,v83\nv_cvt_pk_bf16_f32 v66,v72,v73\nM v[104:107],a[24:27],a[136:139],v[104:107]\nv_exp_f32 v88,v88\n" \
"v_cvt_pk_bf16_f32 v67,v74,v75\nM v[104:107],a[28:31],a[140:143],v[104:107]\nv_exp_f32 v89,v89\nM v[108:111],a[28:31],a[156:159],v[228:231]\nv_exp_f32 v90,v90\nv_cvt_pk_bf16_f32 v80,v80,v81\nM v[108:111],a[24:27],a[152:155],v[108:111]\nv_exp_f32 v91,v91\n" \
"v_cvt_pk_bf16_f32 v81,v82,v83\nM v[108:111],a[20:23],a[148:151],v[108:111]\nv_cvt_pk_bf16_f32 v82,v88,v89\nv_cvt_pk_bf16_f32 v83,v90,v91\nds_read_b128 a[88:91],v241 offset:6144\nM v[108:111],a[16:19],a[144:147],v[108:111]\nds_read_b128 a[120:123],v241 offset:14336\nds_read_b128 a[92:95],v241 offset:7168\n" \
"s_waitcnt lgkmcnt(3)\nM v[112:115],a[32:35],a[128:131],v[224:227]\nds_read_b128 a[124:127],v241 offset:15360\nv_add_f32 v68,v68,v228\nv_add_f32 v69,v69,v228\nM v[112:115],a[36:39],a[132:135],v[112:115]\nv_add_f32 v70,v70,v228\nv_add_f32 v71,v71,v228\n" \
"v_add_f32 v76,v76,v228\nM v[112:115],a[40:43],a[136:139],v[112:115]\nv_exp_f32 v68,v68\nv_add_f32 v77,v77,v228\nM v[112:115],a[44:47],a[140:143],v[112:115]\nv_exp_f32 v69,v69\nv_add_f32 v78,v78,v228\nM v[116:119],a[44:47],a[156:159],v[228:231]\n" \
"v_exp_f32 v70,v70\nv_add_f32 v79,v79,v228\nM v[116:119],a[40:43],a[152:155],v[116:119]\nv_exp_f32 v71,v71\nv_add_f32 v84,v84,v228\nM v[116:119],a[36:39],a[148:151],v[116:119]\nv_add_f32 v85,v85,v228\nv_add_f32 v86,v86,v228\n" \
"M v[116:119],a[32:35],a[144:147],v[116:119]\nv_exp_f32 v76,v76\nv_add_f32 v87,v87,v228\nM v[124:127],a[60:63],a[156:159],v[228:231]\nv_exp_f32 v77,v77\nv_cvt_pk_bf16_f32 v68,v68,v69\nM v[124:127],a[56:59],a[152:155],v[124:127]\nv_exp_f32 v78,v78\n" \
"v_add_f32 v92,v92,v228\nM v[124:127],a[52:55],a[148:151],v[124:127]\nv_exp_f32 v79,v79\nv_add_f32 v93,v93,v228\nM v[124:127],a[48:51],a[144:147],v[124:127]\nv_exp_f32 v84,v84\nv_add_f32 v94,v94,v228\nM v[0:3],a[64:67],v[64:67],0\n" \
"v_exp_f32 v85,v85\nv_add_f32 v95,v95,v228\nM v[0:3],a[96:99],v[80:83],v[0:3]\nv_exp_f32 v86,v86\nv_cvt_pk_bf16_f32 v69,v70,v71\nM v[8:11],a[68:71],v[64:67],0\nv_exp_f32 v87,v87\nv_cvt_pk_bf16_f32 v70,v76,v77\n" \
"M v[8:11],a[100:103],v[80:83],v[8:11]\nv_exp_f32 v92,v92\nv_cvt_pk_bf16_f32 v71,v78,v79\nM v[16:19],a[72:75],v[64:67],0\nv_exp_f32 v93,v93\nv_cvt_pk_bf16_f32 v84,v84,v85\nM v[16:19],a[104:107],v[80:83],v[16:19]\nv_exp_f32 v94,v94\n" \
"M v[24:27],a[76:79],v[64:67],0\nv_exp_f32 v95,v95\nv_cvt_pk_bf16_f32 v85,v86,v87\nM v[24:27],a[108:111],v[80:83],v[24:27]\nv_cvt_pk_bf16_f32 v86,v92,v93\nv_cvt_pk_bf16_f32 v87,v94,v95\nds_read_b128 a[0:3],v240 offset:32768\nM v[32:35],a[80:83],v[64:67],0\n" \
"ds_read_b128 a[4:7],v240 offset:33792\nds_read_b128 a[8:11],v240 offset:34816\nv_cndmask_b32 v112,v112,v252,s[82:83]\nM v[32:35],a[112:115],v[80:83],v[32:35]\nds_read_b128 a[12:15],v240 offset:35840\nv_exp_f32 v96,v96\nM v[40:43],a[84:87],v[64:67],0\nds_read_b128 a[16:19],v240 offset:36864\n" \
"v_exp_f32 v97,v97\nM v[40:43],a[116:119],v[80:83],v[40:43]\nds_read_b128 a[20:23],v240 offset:37888\nv_cndmask_b32 v113,v113,v252,s[84:85]\ns_waitcnt lgkmcnt(6)\nM v[48:51],a[88:91],v[64:67],0\nds_read_b128 a[24:27],v240 offset:38912\nv_exp_f32 v98,v98\n" \
"M v[48:51],a[120:123],v[80:83],v[48:51]\nds_read_b128 a[28:31],v240 offset:39936\nv_exp_f32 v99,v99\nM v[56:59],a[92:95],v[64:67],0\nbuffer_load_dword v248,v251,s[52:55],s80 offen sc1\nds_read_b128 a[32:35],v240 offset:40960\nM v[56:59],a[124:127],v[80:83],v[56:59]\nds_read_b128 a[36:39],v240 offset:41984\n" \
"v_exp_f32 v104,v104\nM a[192:195],a[208:211],v[64:67],0\nds_read_b128 a[40:43],v240 offset:43008\nv_cndmask_b32 v114,v114,v252,s[86:87]\nM a[192:195],a[208:211],v[80:83],a[192:195]\nds_read_b128 a[44:47],v240 offset:44032\nv_exp_f32 v105,v105\nM v[4:7],a[64:67],v[68:71],0\n" \
"ds_read_b128 a[48:51],v240 offset:45056\nv_exp_f32 v106,v106\nM v[4:7],a[96:99],v[84:87],v[4:7]\nds_read_b128 a[52:55],v240 offset:46080\nv_exp_f32 v107,v107\nM v[12:15],a[68:71],v[68:71],0\nds_read_b128 a[56:59],v240 offset:47104\nv_cndmask_b32 v115,v115,v252,s[88:89]\n" \
"v_cvt_pk_bf16_f32 v96,v96,v97\nM v[12:15],a[100:103],v[84:87],v[12:15]\nds_read_b128 a[60:63],v240 offset:48128\nv_exp_f32 v112,v112\nM v[20:23],a[72:75],v[68:71],0\nds_read_b128 a[64:67],v241 offset:16384\nv_exp_f32 v113,v113\nM v[20:23],a[104:107],v[84:87],v[20:23]\n" \
"ds_read_b128 a[96:99],v241 offset:24576\nv_exp_f32 v114,v114\nM v[28:31],a[76:79],v[68:71],0\nds_read_b128 a[68:71],v241 offset:17408\nv_exp_f32 v115,v115\nM v[28:31],a[108:111],v[84:87],v[28:31]\nds_read_b128 a[100:103],v241 offset:25600\nv_cvt_pk_bf16_f32 v97,v98,v99\n" \
"v_cvt_pk_bf16_f32 v98,v104,v105\nM v[36:39],a[80:83],v[68:71],0\nds_read_b128 a[72:75],v241 offset:18432\nv_cvt_pk_bf16_f32 v99,v106,v107\nM v[36:39],a[112:115],v[84:87],v[36:39]\nds_read_b128 a[104:107],v241 offset:26624\nv_cvt_pk_bf16_f32 v112,v112,v113\nv_cvt_pk_bf16_f32 v113,v114,v115\n" \
"M v[44:47],a[84:87],v[68:71],0\nds_read_b128 a[76:79],v241 offset:19456\nv_mov_b32 v114,0\nv_mov_b32 v115,0\nM v[44:47],a[116:119],v[84:87],v[44:47]\nds_read_b128 a[108:111],v241 offset:27648\nv_exp_f32 v100,v100\nM v[52:55],a[88:91],v[68:71],0\n" \
"ds_read_b128 a[80:83],v241 offset:20480\nv_exp_f32 v101,v101\nM v[52:55],a[120:123],v[84:87],v[52:55]\nds_read_b128 a[112:115],v241 offset:28672\nv_exp_f32 v102,v102\nM v[60:63],a[92:95],v[68:71],0\nds_read_b128 a[84:87],v241 offset:21504\nv_exp_f32 v103,v103\n" \
"M v[60:63],a[124:127],v[84:87],v[60:63]\nds_read_b128 a[116:119],v241 offset:29696\nv_exp_f32 v108,v108\nM a[196:199],a[208:211],v[68:71],0\nds_read_b128 a[88:91],v241 offset:22528\nv_exp_f32 v109,v109\nM a[196:199],a[208:211],v[84:87],a[196:199]\nds_read_b128 a[120:123],v241 offset:30720\n" \
"v_cvt_pk_bf16_f32 v100,v100,v101\ns_waitcnt lgkmcnt(8)\nM v[128:131],a[0:3],a[128:131],v[224:227]\nds_read_b128 a[92:95],v241 offset:23552\nv_exp_f32 v110,v110\nM v[128:131],a[4:7],a[132:135],v[128:131]\nds_read_b128 a[124:127],v241 offset:31744\nv_cndmask_b32 v124,v124,v252,s[82:83]\n" \
"M v[128:131],a[8:11],a[136:139],v[128:131]\nv_exp_f32 v111,v111\nv_cndmask_b32 v125,v125,v252,s[84:85]\nM v[128:131],a[12:15],a[140:143],v[128:131]\nv_exp_f32 v116,v116\nv_cndmask_b32 v126,v126,v252,s[86:87]\nM v[132:135],a[12:15],a[156:159],v[228:231]\nv_exp_f32 v117,v117\n" \
"v_cndmask_b32 v127,v127,v252,s[88:89]\nM v[132:135],a[8:11],a[152:155],v[132:135]\nv_exp_f32 v118,v118\nv_cvt_pk_bf16_f32 v101,v102,v103\nM v[132:135],a[4:7],a[148:151],v[132:135]\nv_exp_f32 v119,v119\nv_cvt_pk_bf16_f32 v102,v108,v109\nM v[132:135],a[0:3],a[144:147],v[132:135]\n" \
"v_exp_f32 v124,v124\nv_cvt_pk_bf16_f32 v103,v110,v111\nM v[144:147],a[16:19],a[128:131],v[224:227]\nv_exp_f32 v125,v125\nv_cvt_pk_bf16_f32 v116,v116,v117\nM v[144:147],a[20:23],a[132:135],v[144:147]\nv_exp_f32 v126,v126\nv_cvt_pk_bf16_f32 v117,v118,v119\n" \
"M v[144:147],a[24:27],a[136:139],v[144:147]\nv_exp_f32 v127,v127\nv_cvt_pk_bf16_f32 v118,v124,v125\nM v[144:147],a[28:31],a[140:143],v[144:147]\nv_cvt_pk_bf16_f32 v119,v126,v127\nds_read_b128 a[0:3],v240 offset:49152\nds_read_b128 a[4:7],v240 offset:50176\nM v[148:151],a[28:31],a[156:159],v[228:231]\n" \
"ds_read_b128 a[8:11],v240 offset:51200\nv_mov_b32 v191,0xff800000\nM v[148:151],a[24:27],a[152:155],v[148:151]\nds_read_b128 a[12:15],v240 offset:52224\nv_exp_f32 v128,v128\nM v[148:151],a[20:23],a[148:151],v[148:151]\nds_read_b128 a[28:31],v240 offset:56320\nv_exp_f32 v129,v129\n" \
"M v[148:151],a[16:19],a[144:147],v[148:151]\nds_read_b128 a[24:27],v240 offset:55296\nv_exp_f32 v130,v130\nM v[160:163],a[32:35],a[128:131],v[224:227]\nds_read_b128 a[20:23],v240 offset:54272\nv_exp_f32 v131,v131\nM v[160:163],a[36:39],a[132:135],v[160:163]\nds_read_b128 a[16:19],v240 offset:53248\n" \
"v_cvt_pk_bf16_f32 v192,v128,v129\nv_cvt_pk_bf16_f32 v193,v130,v131\nM v[160:163],a[40:43],a[136:139],v[160:163]\nv_exp_f32 v132,v132\nv_mov_b32 v202,0\nM v[160:163],a[44:47],a[140:143],v[160:163]\nv_exp_f32 v133,v133\nv_mov_b32 v203,0\n" \
"M v[164:167],a[44:47],a[156:159],v[228:231]\nv_exp_f32 v134,v134\nv_cvt_pk_bf16_f32 v196,v132,v133\nM v[164:167],a[40:43],a[152:155],v[164:167]\nv_exp_f32 v135,v135\nds_read_b128 a[40:43],v240 offset:59392\nM v[164:167],a[36:39],a[148:151],v[164:167]\nv_cvt_pk_bf16_f32 v197,v134,v135\n" \
"v_exp_f32 v144,v144\nM v[164:167],a[32:35],a[144:147],v[164:167]\nds_read_b128 a[32:35],v240 offset:57344\nds_read_b128 a[36:39],v240 offset:58368\nM v[176:179],a[48:51],a[128:131],v[224:227]\nv_exp_f32 v145,v145\nds_read_b128 a[44:47],v240 offset:60416\nM v[176:179],a[52:55],a[132:135],v[176:179]\n" \
"v_exp_f32 v146,v146\nv_cvt_pk_bf16_f32 v194,v144,v145\nM v[176:179],a[56:59],a[136:139],v[176:179]\nv_exp_f32 v147,v147\ns_waitcnt vmcnt(0)\nv_cmp_eq_u32 vcc,0,v248\nM v[176:179],a[60:63],a[140:143],v[176:179]\nv_cvt_pk_bf16_f32 v195,v146,v147\n" \
"v_exp_f32 v148,v148\nM v[180:183],a[60:63],a[156:159],v[228:231]\ns_waitcnt lgkmcnt(4)\ns_barrier\nds_read_b128 a[60:63],v240 offset:64512\nM v[180:183],a[56:59],a[152:155],v[180:183]\nv_exp_f32 v149,v149\nds_read_b128 a[56:59],v240 offset:63488\n" \
"M v[180:183],a[52:55],a[148:151],v[180:183]\nv_exp_f32 v150,v150\nv_cvt_pk_bf16_f32 v198,v148,v149\nM v[180:183],a[48:51],a[144:147],v[180:183]\nv_exp_f32 v151,v151\nds_read_b128 a[48:51],v240 offset:61440\nM v[0:3],a[64:67],v[96:99],v[0:3]\nv_cvt_pk_bf16_f32 v199,v150,v151\n" \
"v_exp_f32 v160,v160\nM v[0:3],a[96:99],v[112:115],v[0:3]\nv_exp_f32 v161,v161\nds_read_b128 a[52:55],v240 offset:62464\nM v[8:11],a[68:71],v[96:99],v[8:11]\nv_exp_f32 v162,v162\nM v[8:11],a[100:103],v[112:115],v[8:11]\nv_exp_f32 v163,v163\n" \
"v_cvt_pk_bf16_f32 v208,v160,v161\nM v[16:19],a[72:75],v[96:99],v[16:19]\nv_cvt_pk_bf16_f32 v209,v162,v163\nds_write_b128 v250,a[244:247]\nM v[16:19],a[104:107],v[112:115],v[16:19]\nv_exp_f32 v164,v164\nds_read_b128 v[64:67],v250\nM v[24:27],a[76:79],v[96:99],v[24:27]\n" \
"v_exp_f32 v165,v165\nds_read_b128 v[68:71],v250\nM v[24:27],a[108:111],v[112:115],v[24:27]\nv_exp_f32 v166,v166\nds_read_b128 v[72:75],v250\nM v[32:35],a[80:83],v[96:99],v[32:35]\nv_exp_f32 v167,v167\nds_read_b128 v[76:79],v250\n" \
"M v[32:35],a[112:115],v[112:115],v[32:35]\nds_read_b128 v[80:83],v250\nds_read_b128 v[84:87],v250\nv_cvt_pk_bf16_f32 v212,v164,v165\nM v[40:43],a[84:87],v[96:99],v[40:43]\nds_read_b128 v[88:91],v250\nv_cvt_pk_bf16_f32 v213,v166,v167\ns_cbranch_vccnz .Lps5\n" \
".Lpb5:\nM v[40:43],a[116:119],v[112:115],v[40:43]\nds_read_b128 v[92:95],v250\nv_exp_f32 v176,v176\nM v[48:51],a[88:91],v[96:99],v[48:51]\nds_read_b128 v[104:107],v250\nv_exp_f32 v177,v177\nM v[48:51],a[120:123],v[112:115],v[48:51]\n" \
"ds_read_b128 v[108:111],v250\nv_cvt_pk_bf16_f32 v210,v176,v177\nM v[56:59],a[92:95],v[96:99],v[56:59]\nds_read_b128 v[120:123],v250\nv_exp_f32 v178,v178\nM v[56:59],a[124:127],v[112:115],v[56:59]\nds_read_b128 v[124:127],v250\nv_exp_f32 v179,v179\n" \
"M a[192:195],a[208:211],v[96:99],a[192:195]\nds_read_b128 v[96:99],v250\nv_cvt_pk_bf16_f32 v211,v178,v179\ns_add_u32 m0,s64,65536\nM a[192:195],a[208:211],v[112:115],a[192:195]\nds_read_b128 v[112:115],v250\nv_exp_f32 v180,v180\nM v[4:7],a[64:67],v[100:103],v[4:7]\n" \
"ds_read_b128 a[200:203],v250\nv_exp_f32 v181,v181\nM v[4:7],a[96:99],v[116:119],v[4:7]\nds_read_b128 a[204:207],v250\nv_exp_f32 v182,v182\nM v[12:15],a[68:71],v[100:103],v[12:15]\nds_read_b128 a[64:67],v241 offset:32768\nv_exp_f32 v183,v183\n" \
"M v[12:15],a[100:103],v[116:119],v[12:15]\nds_read_b128 a[96:99],v241 offset:40960\nv_cvt_pk_bf16_f32 v214,v180,v181\nv_cvt_pk_bf16_f32 v215,v182,v183\nM v[20:23],a[72:75],v[100:103],v[20:23]\nds_read_b128 a[68:71],v241 offset:33792\nbuffer_load_dwordx4 v240,s[48:51],s36 offen offset:0 sc1 lds\nM v[20:23],a[104:107],v[116:119],v[20:23]\n" \
"ds_read_b128 a[100:103],v241 offset:41984\nM v[28:31],a[76:79],v[100:103],v[28:31]\nds_read_b128 a[72:75],v241 offset:34816\nM v[28:31],a[108:111],v[116:119],v[28:31]\nds_read_b128 a[104:107],v241 offset:43008\nM v[36:39],a[80:83],v[100:103],v[36:39]\nds_read_b128 a[76:79],v241 offset:35840\nbuffer_load_dwordx4 v240,s[48:51],s36 offen offset:1024 sc1 lds\n" \
"M v[36:39],a[112:115],v[116:119],v[36:39]\nds_read_b128 a[108:111],v241 offset:44032\nM v[44:47],a[84:87],v[100:103],v[44:47]\nds_read_b128 a[80:83],v241 offset:36864\nM v[44:47],a[116:119],v[116:119],v[44:47]\nds_read_b128 a[112:115],v241 offset:45056\nM v[52:55],a[88:91],v[100:103],v[52:55]\nds_read_b128 a[84:87],v241 offset:37888\n" \
"buffer_load_dwordx4 v240,s[48:51],s36 offen offset:2048 sc1 lds\nM v[52:55],a[120:123],v[116:119],v[52:55]\nds_read_b128 a[116:119],v241 offset:46080\nM v[60:63],a[92:95],v[100:103],v[60:63]\nds_read_b128 a[88:91],v241 offset:38912\nM v[60:63],a[124:127],v[116:119],v[60:63]\nds_read_b128 a[120:123],v241 offset:47104\nM a[196:199],a[208:211],v[100:103],a[196:199]\n" \
"ds_read_b128 v[100:103],v250\nbuffer_load_dwordx4 v240,s[48:51],s36 offen offset:3072 sc1 lds\nM a[196:199],a[208:211],v[116:119],a[196:199]\nds_read_b128 v[116:119],v250\ns_add_u32 m0,s64,16384\nM v[136:139],a[0:3],a[160:163],0\nds_read_b128 a[92:95],v241 offset:39936\nv_add_u32 v250,0xc000,v241\n" \
"M v[136:139],a[4:7],a[164:167],v[136:139]\nds_read_b128 a[124:127],v241 offset:48128\nM v[136:139],a[8:11],a[168:171],v[136:139]\nbuffer_load_dwordx4 v240,s[44:47],s65 offen offset:0 sc1 lds\nM v[136:139],a[12:15],a[172:175],v[136:139]\nM v[140:143],a[12:15],a[188:191],0\nM v[140:143],a[8:11],a[184:187],v[140:143]\nM v[140:143],a[4:7],a[180:183],v[140:143]\n" \
"buffer_load_dwordx4 v240,s[44:47],s65 offen offset:1024 sc1 lds\nM v[140:143],a[0:3],a[176:179],v[140:143]\nM v[156:159],a[28:31],a[188:191],0\nM v[156:159],a[24:27],a[184:187],v[156:159]\nM v[156:159],a[20:23],a[180:183],v[156:159]\nv_max3_f32 v233,v136,v137,v138\nv_max_f32 v233,v233,v139\nv_cndmask_b32 v233,v232,v233,s[90:91]\n" \
"M v[156:159],a[16:19],a[176:179],v[156:159]\nbuffer_load_dwordx4 v240,s[44:47],s65 offen offset:2048 sc1 lds\nv_max3_f32 v237,v140,v141,v142\nM v[128:131],a[0:3],a[128:131],v[224:227]\nv_cndmask_b32 v136,v136,v252,s[82:83]\nv_cndmask_b32 v137,v137,v252,s[84:85]\nv_cndmask_b32 v138,v138,v252,s[86:87]\nM v[128:131],a[4:7],a[132:135],v[128:131]\n" \
"v_cndmask_b32 v139,v139,v252,s[88:89]\nv_max3_f32 v237,v237,v143,v156\nv_max3_f32 v237,v237,v157,v158\nM v[128:131],a[8:11],a[136:139],v[128:131]\nv_max_f32 v237,v237,v159\nv_cndmask_b32 v237,v236,v237,s[90:91]\nv_cndmask_b32 v156,v156,v252,s[82:83]\nM v[128:131],a[12:15],a[140:143],v[128:131]\n" \
"v_permlane16_swap_b32 v233,v237\nv_max_f32 v233,v233,v237\nv_mov_b32 v237,v233\nM v[132:135],a[12:15],a[156:159],v[228:231]\nbuffer_load_dwordx4 v240,s[44:47],s65 offen offset:3072 sc1 lds\nv_permlane32_swap_b32 v233,v237\nM v[132:135],a[8:11],a[152:155],v[132:135]\nv_max_f32 v233,v233,v237\n" \
"s_add_u32 m0,s64,81920\nv_mov_b32 v237,v233\nM v[132:135],a[4:7],a[148:151],v[132:135]\nv_cndmask_b32 v157,v157,v252,s[84:85]\nv_permlane16_swap_b32 v233,v237\nv_sub_f32 v232,0,v233\nM v[132:135],a[0:3],a[144:147],v[132:135]\nds_read_b128 a[0:3],v240 offset:0\n" \
"ds_read_b128 a[4:7],v240 offset:1024\nv_add_f32 v136,v136,v232\nM v[144:147],a[16:19],a[128:131],v[224:227]\nds_read_b128 a[8:11],v240 offset:2048\nv_add_f32 v137,v137,v232\nM v[144:147],a[20:23],a[132:135],v[144:147]\nds_read_b128 a[12:15],v240 offset:3072\nbuffer_load_dwordx4 v240,s[48:51],s65 offen offset:0 sc1 lds\n" \
"M v[144:147],a[24:27],a[136:139],v[144:147]\nv_add_f32 v138,v138,v232\nv_add_f32 v139,v139,v232\nv_cndmask_b32 v158,v158,v252,s[86:87]\nM v[144:147],a[28:31],a[140:143],v[144:147]\nv_cndmask_b32 v159,v159,v252,s[88:89]\nv_sub_f32 v236,0,v237\nv_sub_f32 v234,0,v233\n" \
"M v[148:151],a[28:31],a[156:159],v[228:231]\nv_exp_f32 v136,v136\nv_sub_f32 v235,0,v233\nM v[148:151],a[24:27],a[152:155],v[148:151]\nbuffer_load_dwordx4 v240,s[48:51],s65 offen offset:1024 sc1 lds\nv_sub_f32 v238,0,v237\nM v[148:151],a[20:23],a[148:151],v[148:151]\nv_exp_f32 v137,v137\n" \
"v_sub_f32 v239,0,v237\nM v[148:151],a[16:19],a[144:147],v[148:151]\nds_read_b128 a[16:19],v240 offset:4096\nds_read_b128 a[20:23],v240 offset:5120\ns_waitcnt lgkmcnt(5)\nM v[160:163],a[32:35],a[128:131],v[224:227]\nds_read_b128 a[24:27],v240 offset:6144\nv_exp_f32 v138,v138\n" \
"M v[160:163],a[36:39],a[132:135],v[160:163]\nds_read_b128 a[28:31],v240 offset:7168\nbuffer_load_dwordx4 v240,s[48:51],s65 offen offset:2048 sc1 lds\nM v[160:163],a[40:43],a[136:139],v[160:163]\nv_exp_f32 v139,v139\nv_add_f32 v140,v140,v236\nM v[160:163],a[44:47],a[140:143],v[160:163]\nv_exp_f32 v128,v128\n" \
"M v[164:167],a[44:47],a[156:159],v[228:231]\ns_barrier\nv_exp_f32 v129,v129\nM v[164:167],a[40:43],a[152:155],v[164:167]\nbuffer_load_dwordx4 v240,s[48:51],s65 offen offset:3072 sc1 lds\ns_add_u32 m0,s64,32768\nM v[164:167],a[36:39],a[148:151],v[164:167]\nv_exp_f32 v130,v130\n" \
"v_add_f32 v141,v141,v236\nM v[164:167],a[32:35],a[144:147],v[164:167]\nds_read_b128 a[32:35],v240 offset:8192\nds_read_b128 a[36:39],v240 offset:9216\nv_add_f32 v142,v142,v236\nM v[176:179],a[48:51],a[128:131],v[224:227]\nds_read_b128 a[40:43],v240 offset:10240\nv_exp_f32 v131,v131\n" \
"M v[176:179],a[52:55],a[132:135],v[176:179]\nds_read_b128 a[44:47],v240 offset:11264\nbuffer_load_dwordx4 v240,s[44:47],s66 offen offset:0 sc1 lds\nM v[176:179],a[56:59],a[136:139],v[176:179]\nv_exp_f32 v132,v132\nv_add_f32 v143,v143,v236\nM v[176:179],a[60:63],a[140:143],v[176:179]\nv_exp_f32 v133,v133\n" \
"v_add_f32 v156,v156,v236\nM v[180:183],a[60:63],a[156:159],v[228:231]\nv_exp_f32 v134,v134\nv_add_f32 v157,v157,v236\nM v[180:183],a[56:59],a[152:155],v[180:183]\nbuffer_load_dwordx4 v240,s[44:47],s66 offen offset:1024 sc1 lds\nv_add_f32 v158,v158,v236\nM v[180:183],a[52:55],a[148:151],v[180:183]\n" \
"v_exp_f32 v135,v135\nM v[180:183],a[48:51],a[144:147],v[180:183]\nds_read_b128 a[48:51],v240 offset:12288\nds_read_b128 a[52:55],v240 offset:13312\nv_add_f32 v159,v159,v236\nM v[0:3],a[64:67],v[192:195],v[0:3]\nds_read_b128 a[56:59],v240 offset:14336\nv_exp_f32 v144,v144\n" \
"M v[0:3],a[96:99],v[208:211],v[0:3]\nds_read_b128 a[60:63],v240 offset:15360\nbuffer_load_dwordx4 v240,s[44:47],s66 offen offset:2048 sc1 lds\nM v[8:11],a[68:71],v[192:195],v[8:11]\nv_exp_f32 v145,v145\nv_sub_f32 v233,0,v233\nM v[8:11],a[100:103],v[208:211],v[8:11]\nv_exp_f32 v146,v146\n" \
"v_sub_f32 v237,0,v237\nM v[16:19],a[72:75],v[192:195],v[16:19]\nv_exp_f32 v147,v147\nv_cvt_pk_bf16_f32 v200,v136,v137\nM v[16:19],a[104:107],v[208:211],v[16:19]\nbuffer_load_dwordx4 v240,s[44:47],s66 offen offset:3072 sc1 lds\ns_add_u32 m0,s64,98304\nM v[24:27],a[76:79],v[192:195],v[24:27]\n" \
"v_exp_f32 v148,v148\nv_cvt_pk_bf16_f32 v201,v138,v139\nM v[24:27],a[108:111],v[208:211],v[24:27]\nv_exp_f32 v149,v149\nM v[32:35],a[80:83],v[192:195],v[32:35]\nv_exp_f32 v150,v150\nM v[32:35],a[112:115],v[208:211],v[32:35]\nv_exp_f32 v151,v151\n" \
"M v[40:43],a[84:87],v[192:195],v[40:43]\nv_exp_f32 v160,v160\nM v[40:43],a[116:119],v[208:211],v[40:43]\nv_exp_f32 v161,v161\nM v[48:51],a[88:91],v[192:195],v[48:51]\nv_exp_f32 v162,v162\nM v[48:51],a[120:123],v[208:211],v[48:51]\nv_exp_f32 v163,v163\n" \
"M v[56:59],a[92:95],v[192:195],v[56:59]\nv_exp_f32 v176,v176\nM v[56:59],a[124:127],v[208:211],v[56:59]\nv_exp_f32 v177,v177\nM a[192:195],a[208:211],v[192:195],a[192:195]\nv_exp_f32 v178,v178\nv_cvt_pk_bf16_f32 v192,v128,v129\nM a[192:195],a[208:211],v[208:211],a[192:195]\n" \
"v_exp_f32 v179,v179\nv_cvt_pk_bf16_f32 v193,v130,v131\nM v[4:7],a[64:67],v[196:199],v[4:7]\nv_exp_f32 v140,v140\nv_cvt_pk_bf16_f32 v194,v144,v145\nM v[4:7],a[96:99],v[212:215],v[4:7]\nv_exp_f32 v141,v141\nv_cvt_pk_bf16_f32 v195,v146,v147\n" \
"M v[12:15],a[68:71],v[196:199],v[12:15]\nv_exp_f32 v142,v142\nM v[12:15],a[100:103],v[212:215],v[12:15]\nv_exp_f32 v143,v143\nv_cvt_pk_bf16_f32 v208,v160,v161\nM v[20:23],a[72:75],v[196:199],v[20:23]\nv_exp_f32 v156,v156\nv_cvt_pk_bf16_f32 v209,v162,v163\n" \
"M v[20:23],a[104:107],v[212:215],v[20:23]\nv_exp_f32 v157,v157\nv_cvt_pk_bf16_f32 v210,v176,v177\nM v[28:31],a[76:79],v[196:199],v[28:31]\nv_exp_f32 v158,v158\nv_cvt_pk_bf16_f32 v211,v178,v179\nM v[28:31],a[108:111],v[212:215],v[28:31]\nv_exp_f32 v159,v159\n" \
"M v[36:39],a[80:83],v[196:199],v[36:39]\nv_exp_f32 v164,v164\nM v[36:39],a[112:115],v[212:215],v[36:39]\nv_exp_f32 v165,v165\nM v[44:47],a[84:87],v[196:199],v[44:47]\nv_exp_f32 v166,v166\nM v[44:47],a[116:119],v[212:215],v[44:47]\nv_exp_f32 v167,v167\n" \
"M v[52:55],a[88:91],v[196:199],v[52:55]\nv_exp_f32 v180,v180\nM v[52:55],a[120:123],v[212:215],v[52:55]\nv_exp_f32 v181,v181\nM v[60:63],a[92:95],v[196:199],v[60:63]\nv_exp_f32 v182,v182\nM v[60:63],a[124:127],v[212:215],v[60:63]\nv_exp_f32 v183,v183\n" \
"M a[196:199],a[208:211],v[196:199],a[196:199]\nv_cvt_pk_bf16_f32 v196,v132,v133\nv_cvt_pk_bf16_f32 v197,v134,v135\nv_cvt_pk_bf16_f32 v198,v148,v149\nv_cvt_pk_bf16_f32 v199,v150,v151\nbuffer_load_dwordx4 v240,s[48:51],s66 offen offset:0 sc1 lds\nbuffer_load_dwordx4 v240,s[48:51],s66 offen offset:1024 sc1 lds\nbuffer_load_dwordx4 v240,s[48:51],s66 offen offset:2048 sc1 lds\n" \
"buffer_load_dwordx4 v240,s[48:51],s66 offen offset:3072 sc1 lds\nM a[196:199],a[208:211],v[212:215],a[196:199]\ns_cmp_lt_u32 s62,2\ns_cbranch_scc1 .Lx01\n.Ldual:\ns_waitcnt lgkmcnt(4)\nM v[128:131],a[0:3],a[128:131],v[224:227]\ns_add_u32 m0,s94,s64\n" \
"v_cvt_pk_bf16_f32 v204,v140,v141\nM v[128:131],a[4:7],a[132:135],v[128:131]\nds_read_b128 a[64:67],v250 offset:0\nds_read_b128 a[96:99],v250 offset:8192\nM v[128:131],a[8:11],a[136:139],v[128:131]\nv_cvt_pk_bf16_f32 v205,v142,v143\nv_cvt_pk_bf16_f32 v206,v156,v157\nM v[128:131],a[12:15],a[140:143],v[128:131]\n" \
"v_cvt_pk_bf16_f32 v207,v158,v159\nv_cvt_pk_bf16_f32 v212,v164,v165\nM v[132:135],a[12:15],a[156:159],v[228:231]\nbuffer_load_dwordx4 v240,s[44:47],s95 offen offset:0 sc1 lds\nM v[132:135],a[8:11],a[152:155],v[132:135]\nv_cvt_pk_bf16_f32 v213,v166,v167\nv_cvt_pk_bf16_f32 v216,v168,v169\nM v[132:135],a[4:7],a[148:151],v[132:135]\n" \
"v_cvt_pk_bf16_f32 v217,v170,v171\nv_cvt_pk_bf16_f32 v214,v180,v181\nM v[132:135],a[0:3],a[144:147],v[132:135]\nds_read_b128 a[68:71],v250 offset:1024\nds_read_b128 a[100:103],v250 offset:9216\nM v[136:139],a[0:3],a[160:163],v[232:235]\nv_exp_f32 v172,v172\nM v[136:139],a[4:7],a[164:167],v[136:139]\n" \
"v_exp_f32 v173,v173\nM v[136:139],a[8:11],a[168:171],v[136:139]\nv_exp_f32 v174,v174\nM v[136:139],a[12:15],a[172:175],v[136:139]\nv_exp_f32 v175,v175\nM v[140:143],a[12:15],a[188:191],v[236:239]\nv_cvt_pk_bf16_f32 v220,v172,v173\nv_cvt_pk_bf16_f32 v221,v174,v175\n" \
"M v[140:143],a[8:11],a[184:187],v[140:143]\nds_read_b128 a[72:75],v250 offset:2048\nds_read_b128 a[104:107],v250 offset:10240\nM v[140:143],a[4:7],a[180:183],v[140:143]\nv_cvt_pk_bf16_f32 v215,v182,v183\ns_add_u32 s94,s94,0x4000\nM v[140:143],a[0:3],a[176:179],v[140:143]\nv_exp_f32 v184,v184\n" \
"M v[144:147],a[16:19],a[128:131],v[224:227]\nv_exp_f32 v185,v185\nM v[144:147],a[20:23],a[132:135],v[144:147]\nv_exp_f32 v186,v186\nM v[144:147],a[24:27],a[136:139],v[144:147]\nv_exp_f32 v187,v187\nM v[144:147],a[28:31],a[140:143],v[144:147]\nds_read_b128 a[76:79],v250 offset:3072\n" \
"ds_read_b128 a[108:111],v250 offset:11264\nM v[148:151],a[28:31],a[156:159],v[228:231]\nv_cvt_pk_bf16_f32 v218,v184,v185\nv_cvt_pk_bf16_f32 v219,v186,v187\nM v[148:151],a[24:27],a[152:155],v[148:151]\nv_exp_f32 v188,v188\nM v[148:151],a[20:23],a[148:151],v[148:151]\nbuffer_load_dwordx4 v240,s[44:47],s95 offen offset:1024 sc1 lds\n" \
"M v[148:151],a[16:19],a[144:147],v[148:151]\nv_exp_f32 v189,v189\nM v[152:155],a[16:19],a[160:163],v[232:235]\nv_exp_f32 v190,v190\nM v[152:155],a[20:23],a[164:167],v[152:155]\nds_read_b128 a[80:83],v250 offset:4096\nds_read_b128 a[112:115],v250 offset:12288\nM v[152:155],a[24:27],a[168:171],v[152:155]\n" \
"v_exp_f32 v191,v191\nM v[152:155],a[28:31],a[172:175],v[152:155]\nv_cvt_pk_bf16_f32 v222,v188,v189\nv_cvt_pk_bf16_f32 v223,v190,v191\nM v[156:159],a[28:31],a[188:191],v[236:239]\nv_exp_f32 v128,v128\nM v[156:159],a[24:27],a[184:187],v[156:159]\nv_exp_f32 v129,v129\n" \
"M v[156:159],a[20:23],a[180:183],v[156:159]\nv_exp_f32 v130,v130\nM v[156:159],a[16:19],a[176:179],v[156:159]\nds_read_b128 a[84:87],v250 offset:5120\nds_read_b128 a[116:119],v250 offset:13312\nM v[160:163],a[32:35],a[128:131],v[224:227]\nv_exp_f32 v131,v131\nM v[160:163],a[36:39],a[132:135],v[160:163]\n" \
"v_exp_f32 v144,v144\nM v[160:163],a[40:43],a[136:139],v[160:163]\nv_exp_f32 v145,v145\nM v[160:163],a[44:47],a[140:143],v[160:163]\nv_exp_f32 v146,v146\nM v[164:167],a[44:47],a[156:159],v[228:231]\nv_exp_f32 v147,v147\nM v[164:167],a[40:43],a[152:155],v[164:167]\n" \
"ds_read_b128 a[88:91],v250 offset:6144\nds_read_b128 a[120:123],v250 offset:14336\nM v[164:167],a[36:39],a[148:151],v[164:167]\nv_exp_f32 v132,v132\nM v[164:167],a[32:35],a[144:147],v[164:167]\nv_exp_f32 v133,v133\nM v[168:171],a[32:35],a[160:163],v[232:235]\nbuffer_load_dwordx4 v240,s[44:47],s95 offen offset:2048 sc1 lds\n" \
"M v[168:171],a[36:39],a[164:167],v[168:171]\nv_exp_f32 v134,v134\nM v[168:171],a[40:43],a[168:171],v[168:171]\nv_exp_f32 v135,v135\nM v[168:171],a[44:47],a[172:175],v[168:171]\nds_read_b128 a[92:95],v250 offset:7168\nds_read_b128 a[124:127],v250 offset:15360\nM v[172:175],a[44:47],a[188:191],v[236:239]\n" \
"v_xor_b32 v250,0x18000,v250\ns_and_b32 s94,s94,0xffff\nM v[172:175],a[40:43],a[184:187],v[172:175]\nv_exp_f32 v148,v148\nM v[172:175],a[36:39],a[180:183],v[172:175]\nv_exp_f32 v149,v149\nM v[172:175],a[32:35],a[176:179],v[172:175]\ns_waitcnt lgkmcnt(4)\n" \
"M v[176:179],a[48:51],a[128:131],v[224:227]\nv_exp_f32 v150,v150\nM v[176:179],a[52:55],a[132:135],v[176:179]\nv_exp_f32 v151,v151\nM v[176:179],a[56:59],a[136:139],v[176:179]\nv_exp_f32 v136,v136\nM v[176:179],a[60:63],a[140:143],v[176:179]\nv_exp_f32 v137,v137\n" \
"M v[180:183],a[60:63],a[156:159],v[228:231]\nv_exp_f32 v138,v138\nM v[180:183],a[56:59],a[152:155],v[180:183]\nv_exp_f32 v139,v139\nM v[180:183],a[52:55],a[148:151],v[180:183]\nv_exp_f32 v152,v152\nM v[180:183],a[48:51],a[144:147],v[180:183]\nv_exp_f32 v153,v153\n" \
"M v[184:187],a[48:51],a[160:163],v[232:235]\nv_exp_f32 v154,v154\nM v[184:187],a[52:55],a[164:167],v[184:187]\nv_exp_f32 v155,v155\nM v[184:187],a[56:59],a[168:171],v[184:187]\nbuffer_load_dwordx4 v240,s[44:47],s95 offen offset:3072 sc1 lds\nM v[184:187],a[60:63],a[172:175],v[184:187]\nv_exp_f32 v160,v160\n" \
"M v[188:191],a[60:63],a[188:191],v[236:239]\nv_exp_f32 v161,v161\nM v[188:191],a[56:59],a[184:187],v[188:191]\nv_exp_f32 v162,v162\nM v[188:191],a[52:55],a[180:183],v[188:191]\nv_exp_f32 v163,v163\nM v[188:191],a[48:51],a[176:179],v[188:191]\nv_exp_f32 v176,v176\n" \
"M v[0:3],a[64:67],v[192:195],v[0:3]\ns_waitcnt vmcnt(16) lgkmcnt(0)\ns_barrier\nM v[0:3],a[96:99],v[208:211],v[0:3]\nds_read_b128 a[0:3],v250 offset:0\nds_read_b128 a[4:7],v250 offset:1024\nM v[8:11],a[68:71],v[192:195],v[8:11]\nv_exp_f32 v177,v177\n" \
"M v[8:11],a[100:103],v[208:211],v[8:11]\nv_exp_f32 v178,v178\nM v[16:19],a[72:75],v[192:195],v[16:19]\ns_add_u32 m0,m0,0x10000\nM v[16:19],a[104:107],v[208:211],v[16:19]\nbuffer_load_dwordx4 v240,s[48:51],s95 offen offset:0 sc1 lds\nM v[24:27],a[76:79],v[192:195],v[24:27]\nv_exp_f32 v179,v179\n" \
"M v[24:27],a[108:111],v[208:211],v[24:27]\nv_exp_f32 v140,v140\nM v[32:35],a[80:83],v[192:195],v[32:35]\nv_exp_f32 v141,v141\nM v[32:35],a[112:115],v[208:211],v[32:35]\nds_read_b128 a[8:11],v250 offset:2048\nds_read_b128 a[12:15],v250 offset:3072\nM v[40:43],a[84:87],v[192:195],v[40:43]\n" \
"v_exp_f32 v142,v142\nM v[40:43],a[116:119],v[208:211],v[40:43]\nv_exp_f32 v143,v143\nM v[48:51],a[88:91],v[192:195],v[48:51]\nv_exp_f32 v156,v156\nM v[48:51],a[120:123],v[208:211],v[48:51]\nv_exp_f32 v157,v157\nM v[56:59],a[92:95],v[192:195],v[56:59]\n" \
"v_exp_f32 v158,v158\nM v[56:59],a[124:127],v[208:211],v[56:59]\nv_exp_f32 v159,v159\nM a[192:195],a[208:211],v[192:195],a[192:195]\nv_exp_f32 v164,v164\nM a[192:195],a[208:211],v[208:211],a[192:195]\nds_read_b128 a[16:19],v250 offset:4096\nds_read_b128 a[20:23],v250 offset:5120\n" \
"M v[4:7],a[64:67],v[196:199],v[4:7]\nv_exp_f32 v165,v165\nM v[4:7],a[96:99],v[212:215],v[4:7]\nv_exp_f32 v166,v166\nM v[12:15],a[68:71],v[196:199],v[12:15]\nv_exp_f32 v167,v167\nM v[12:15],a[100:103],v[212:215],v[12:15]\nbuffer_load_dwordx4 v240,s[48:51],s95 offen offset:1024 sc1 lds\n" \
"M v[20:23],a[72:75],v[196:199],v[20:23]\nv_exp_f32 v180,v180\nM v[20:23],a[104:107],v[212:215],v[20:23]\nv_exp_f32 v181,v181\nM v[28:31],a[76:79],v[196:199],v[28:31]\nv_exp_f32 v182,v182\nM v[28:31],a[108:111],v[212:215],v[28:31]\nds_read_b128 a[24:27],v250 offset:6144\n" \
"ds_read_b128 a[28:31],v250 offset:7168\nM v[36:39],a[80:83],v[196:199],v[36:39]\nv_exp_f32 v183,v183\nM v[36:39],a[112:115],v[212:215],v[36:39]\nv_exp_f32 v168,v168\nM v[44:47],a[84:87],v[196:199],v[44:47]\nv_exp_f32 v169,v169\nM v[44:47],a[116:119],v[212:215],v[44:47]\n" \
"v_exp_f32 v170,v170\nM v[52:55],a[88:91],v[196:199],v[52:55]\nv_exp_f32 v171,v171\nM v[52:55],a[120:123],v[212:215],v[52:55]\nv_cvt_pk_bf16_f32 v192,v128,v129\nv_cvt_pk_bf16_f32 v193,v130,v131\nM v[60:63],a[92:95],v[196:199],v[60:63]\nv_cvt_pk_bf16_f32 v194,v144,v145\n" \
"v_cvt_pk_bf16_f32 v195,v146,v147\nM v[60:63],a[124:127],v[212:215],v[60:63]\nds_read_b128 a[32:35],v250 offset:8192\nds_read_b128 a[36:39],v250 offset:9216\nM a[196:199],a[208:211],v[196:199],a[196:199]\nv_cvt_pk_bf16_f32 v208,v160,v161\nv_cvt_pk_bf16_f32 v209,v162,v163\nM a[196:199],a[208:211],v[212:215],a[196:199]\n" \
"v_cvt_pk_bf16_f32 v210,v176,v177\nv_cvt_pk_bf16_f32 v211,v178,v179\nM v[64:67],a[64:67],v[200:203],v[64:67]\nv_cvt_pk_bf16_f32 v196,v132,v133\nv_cvt_pk_bf16_f32 v197,v134,v135\nM v[64:67],a[96:99],v[216:219],v[64:67]\nbuffer_load_dwordx4 v240,s[48:51],s95 offen offset:2048 sc1 lds\nM v[72:75],a[68:71],v[200:203],v[72:75]\n" \
"v_cvt_pk_bf16_f32 v198,v148,v149\nv_cvt_pk_bf16_f32 v199,v150,v151\nM v[72:75],a[100:103],v[216:219],v[72:75]\nM v[80:83],a[72:75],v[200:203],v[80:83]\nM v[80:83],a[104:107],v[216:219],v[80:83]\nds_read_b128 a[40:43],v250 offset:10240\nds_read_b128 a[44:47],v250 offset:11264\nM v[88:91],a[76:79],v[200:203],v[88:91]\n" \
"M v[88:91],a[108:111],v[216:219],v[88:91]\nM v[96:99],a[80:83],v[200:203],v[96:99]\nM v[96:99],a[112:115],v[216:219],v[96:99]\nM v[104:107],a[84:87],v[200:203],v[104:107]\nM v[104:107],a[116:119],v[216:219],v[104:107]\nM v[112:115],a[88:91],v[200:203],v[112:115]\nM v[112:115],a[120:123],v[216:219],v[112:115]\nds_read_b128 a[48:51],v250 offset:12288\n" \
"ds_read_b128 a[52:55],v250 offset:13312\nM v[120:123],a[92:95],v[200:203],v[120:123]\nM v[120:123],a[124:127],v[216:219],v[120:123]\nM a[200:203],a[208:211],v[200:203],a[200:203]\nv_cvt_pk_bf16_f32 v200,v136,v137\nv_cvt_pk_bf16_f32 v201,v138,v139\nM a[200:203],a[208:211],v[216:219],a[200:203]\nbuffer_load_dwordx4 v240,s[48:51],s95 offen offset:3072 sc1 lds\n" \
"M v[68:71],a[64:67],v[204:207],v[68:71]\nv_cvt_pk_bf16_f32 v202,v152,v153\nv_cvt_pk_bf16_f32 v203,v154,v155\nM v[68:71],a[96:99],v[220:223],v[68:71]\ns_cmp_eq_u32 s95,s96\ns_cselect_b32 s72,0xc000,s98\ns_add_u32 s95,s95,s72\nM v[76:79],a[68:71],v[204:207],v[76:79]\n" \
"M v[76:79],a[100:103],v[220:223],v[76:79]\nds_read_b128 a[56:59],v250 offset:14336\nds_read_b128 a[60:63],v250 offset:15360\nM v[84:87],a[72:75],v[204:207],v[84:87]\nv_add_u32 v250,s94,v241\nM v[84:87],a[104:107],v[220:223],v[84:87]\nM v[92:95],a[76:79],v[204:207],v[92:95]\nM v[92:95],a[108:111],v[220:223],v[92:95]\n" \
"M v[100:103],a[80:83],v[204:207],v[100:103]\nM v[100:103],a[112:115],v[220:223],v[100:103]\nM v[108:111],a[84:87],v[204:207],v[108:111]\nM v[108:111],a[116:119],v[220:223],v[108:111]\nM v[116:119],a[88:91],v[204:207],v[116:119]\nbuffer_load_dword v247,v254,s[56:59],s1 offen\nM v[116:119],a[120:123],v[220:223],v[116:119]\ns_add_u32 s1,s1,s0\n" \
"M v[124:127],a[92:95],v[204:207],v[124:127]\nM v[124:127],a[124:127],v[220:223],v[124:127]\nM a[204:207],a[208:211],v[204:207],a[204:207]\nM a[204:207],a[208:211],v[220:223],a[204:207]\ns_cmp_lg_u32 s95,s97\ns_cbranch_scc1 .Ldual\n.Ldf:\ns_waitcnt lgkmcnt(4)\n" \
"M v[128:131],a[0:3],a[128:131],v[224:227]\ns_add_u32 m0,s94,s64\nv_cvt_pk_bf16_f32 v204,v140,v141\nM v[128:131],a[4:7],a[132:135],v[128:131]\nds_read_b128 a[64:67],v250 offset:0\nds_read_b128 a[96:99],v250 offset:8192\nM v[128:131],a[8:11],a[136:139],v[128:131]\nv_cvt_pk_bf16_f32 v205,v142,v143\n" \
"v_lshrrev_b32 v248,2,v249\nM v[128:131],a[12:15],a[140:143],v[128:131]\nv_mul_u32_u24 v248,3584,v248\nv_and_b32 v251,3,v249\nM v[132:135],a[12:15],a[156:159],v[228:231]\nbuffer_load_dwordx4 v240,s[44:47],s95 offen offset:0 sc1 lds\nM v[132:135],a[8:11],a[152:155],v[132:135]\ns_mul_i32 s70,s7,458752\n" \
"v_lshl_add_u32 v248,v251,3,v248\nM v[132:135],a[4:7],a[148:151],v[132:135]\ns_add_u32 s70,s70,s2\nv_add_u32 v248,s70,v248\nM v[132:135],a[0:3],a[144:147],v[132:135]\nds_read_b128 a[68:71],v250 offset:1024\nds_read_b128 a[100:103],v250 offset:9216\nM v[136:139],a[0:3],a[160:163],v[232:235]\n" \
"v_and_b32 v248,s3,v248\nv_cvt_pk_bf16_f32 v206,v156,v157\nM v[136:139],a[4:7],a[164:167],v[136:139]\nv_cvt_pk_bf16_f32 v207,v158,v159\nv_cvt_pk_bf16_f32 v212,v164,v165\nM v[136:139],a[8:11],a[168:171],v[136:139]\nv_cvt_pk_bf16_f32 v213,v166,v167\nv_cvt_pk_bf16_f32 v216,v168,v169\n" \
"M v[136:139],a[12:15],a[172:175],v[136:139]\nv_cvt_pk_bf16_f32 v217,v170,v171\ns_and_b32 s71,s3,57344\nM v[140:143],a[12:15],a[188:191],v[236:239]\nv_exp_f32 v172,v172\nM v[140:143],a[8:11],a[184:187],v[140:143]\nds_read_b128 a[72:75],v250 offset:2048\nds_read_b128 a[104:107],v250 offset:10240\n" \
"M v[140:143],a[4:7],a[180:183],v[140:143]\nglobal_load_dwordx2 a[212:213],v248,s[22:23]\nM v[140:143],a[0:3],a[176:179],v[140:143]\nv_exp_f32 v173,v173\nM v[144:147],a[16:19],a[128:131],v[224:227]\nv_exp_f32 v174,v174\nM v[144:147],a[20:23],a[132:135],v[144:147]\nv_exp_f32 v175,v175\n" \
"M v[144:147],a[24:27],a[136:139],v[144:147]\nv_cvt_pk_bf16_f32 v220,v172,v173\nv_cvt_pk_bf16_f32 v221,v174,v175\nM v[144:147],a[28:31],a[140:143],v[144:147]\nds_read_b128 a[76:79],v250 offset:3072\nds_read_b128 a[108:111],v250 offset:11264\nM v[148:151],a[28:31],a[156:159],v[228:231]\nv_cvt_pk_bf16_f32 v214,v180,v181\n" \
"v_cvt_pk_bf16_f32 v215,v182,v183\nM v[148:151],a[24:27],a[152:155],v[148:151]\nv_exp_f32 v184,v184\nM v[148:151],a[20:23],a[148:151],v[148:151]\nbuffer_load_dwordx4 v240,s[44:47],s95 offen offset:1024 sc1 lds\nM v[148:151],a[16:19],a[144:147],v[148:151]\nv_exp_f32 v185,v185\nM v[152:155],a[16:19],a[160:163],v[232:235]\n" \
"v_exp_f32 v186,v186\nM v[152:155],a[20:23],a[164:167],v[152:155]\nds_read_b128 a[80:83],v250 offset:4096\nds_read_b128 a[112:115],v250 offset:12288\nM v[152:155],a[24:27],a[168:171],v[152:155]\nv_exp_f32 v187,v187\nM v[152:155],a[28:31],a[172:175],v[152:155]\nv_cvt_pk_bf16_f32 v218,v184,v185\n" \
"v_cvt_pk_bf16_f32 v219,v186,v187\nM v[156:159],a[28:31],a[188:191],v[236:239]\nv_exp_f32 v188,v188\nM v[156:159],a[24:27],a[184:187],v[156:159]\nv_exp_f32 v189,v189\nM v[156:159],a[20:23],a[180:183],v[156:159]\nv_exp_f32 v190,v190\nM v[156:159],a[16:19],a[176:179],v[156:159]\n" \
"ds_read_b128 a[84:87],v250 offset:5120\nds_read_b128 a[116:119],v250 offset:13312\nM v[160:163],a[32:35],a[128:131],v[224:227]\nglobal_load_dwordx2 a[228:229],v248,s[24:25]\nM v[160:163],a[36:39],a[132:135],v[160:163]\nv_add_u32 v248,s71,v248\nv_cvt_pk_bf16_f32 v222,v188,v189\nM v[160:163],a[40:43],a[136:139],v[160:163]\n" \
"v_exp_f32 v191,v191\nM v[160:163],a[44:47],a[140:143],v[160:163]\nv_cvt_pk_bf16_f32 v223,v190,v191\ns_add_u32 s94,s94,0x4000\nM v[164:167],a[44:47],a[156:159],v[228:231]\nv_exp_f32 v128,v128\nM v[164:167],a[40:43],a[152:155],v[164:167]\nds_read_b128 a[88:91],v250 offset:6144\n" \
"ds_read_b128 a[120:123],v250 offset:14336\nM v[164:167],a[36:39],a[148:151],v[164:167]\nv_exp_f32 v129,v129\nM v[164:167],a[32:35],a[144:147],v[164:167]\nv_exp_f32 v130,v130\nM v[168:171],a[32:35],a[160:163],v[232:235]\nbuffer_load_dwordx4 v240,s[44:47],s95 offen offset:2048 sc1 lds\nM v[168:171],a[36:39],a[164:167],v[168:171]\n" \
"v_exp_f32 v131,v131\nM v[168:171],a[40:43],a[168:171],v[168:171]\nv_exp_f32 v144,v144\nM v[168:171],a[44:47],a[172:175],v[168:171]\nds_read_b128 a[92:95],v250 offset:7168\nds_read_b128 a[124:127],v250 offset:15360\nM v[172:175],a[44:47],a[188:191],v[236:239]\nv_xor_b32 v250,0x18000,v250\n" \
"s_and_b32 s94,s94,0xffff\nM v[172:175],a[40:43],a[184:187],v[172:175]\nv_exp_f32 v145,v145\nM v[172:175],a[36:39],a[180:183],v[172:175]\nv_exp_f32 v146,v146\nM v[172:175],a[32:35],a[176:179],v[172:175]\ns_waitcnt lgkmcnt(4)\nM v[176:179],a[48:51],a[128:131],v[224:227]\n" \
"v_exp_f32 v147,v147\nM v[176:179],a[52:55],a[132:135],v[176:179]\nv_exp_f32 v132,v132\nM v[176:179],a[56:59],a[136:139],v[176:179]\nglobal_load_dwordx2 a[214:215],v248,s[22:23]\nM v[176:179],a[60:63],a[140:143],v[176:179]\nv_exp_f32 v133,v133\nM v[180:183],a[60:63],a[156:159],v[228:231]\n" \
"v_exp_f32 v134,v134\nM v[180:183],a[56:59],a[152:155],v[180:183]\nv_exp_f32 v135,v135\nM v[180:183],a[52:55],a[148:151],v[180:183]\nv_exp_f32 v148,v148\nM v[180:183],a[48:51],a[144:147],v[180:183]\nv_exp_f32 v149,v149\nM v[184:187],a[48:51],a[160:163],v[232:235]\n" \
"v_exp_f32 v150,v150\nM v[184:187],a[52:55],a[164:167],v[184:187]\nv_exp_f32 v151,v151\nM v[184:187],a[56:59],a[168:171],v[184:187]\nbuffer_load_dwordx4 v240,s[44:47],s95 offen offset:3072 sc1 lds\nM v[184:187],a[60:63],a[172:175],v[184:187]\nv_exp_f32 v136,v136\nM v[188:191],a[60:63],a[188:191],v[236:239]\n" \
"v_exp_f32 v137,v137\nM v[188:191],a[56:59],a[184:187],v[188:191]\nv_exp_f32 v138,v138\nM v[188:191],a[52:55],a[180:183],v[188:191]\nglobal_load_dwordx2 a[230:231],v248,s[24:25]\nM v[188:191],a[48:51],a[176:179],v[188:191]\ns_add_u32 m0,m0,0x10000\nv_add_u32 v248,s71,v248\n" \
"M v[0:3],a[64:67],v[192:195],v[0:3]\ns_waitcnt vmcnt(16) lgkmcnt(0)\ns_barrier\nM v[0:3],a[96:99],v[208:211],v[0:3]\nds_read_b128 a[0:3],v250 offset:0\nds_read_b128 a[4:7],v250 offset:1024\nM v[8:11],a[68:71],v[192:195],v[8:11]\nv_exp_f32 v139,v139\n" \
"M v[8:11],a[100:103],v[208:211],v[8:11]\nv_exp_f32 v152,v152\nM v[16:19],a[72:75],v[192:195],v[16:19]\nv_exp_f32 v153,v153\nM v[16:19],a[104:107],v[208:211],v[16:19]\nbuffer_load_dwordx4 v240,s[48:51],s95 offen offset:0 sc1 lds\nM v[24:27],a[76:79],v[192:195],v[24:27]\nv_exp_f32 v154,v154\n" \
"M v[24:27],a[108:111],v[208:211],v[24:27]\nv_exp_f32 v155,v155\nM v[32:35],a[80:83],v[192:195],v[32:35]\nv_exp_f32 v160,v160\nM v[32:35],a[112:115],v[208:211],v[32:35]\nds_read_b128 a[8:11],v250 offset:2048\nds_read_b128 a[12:15],v250 offset:3072\nM v[40:43],a[84:87],v[192:195],v[40:43]\n" \
"v_exp_f32 v161,v161\nM v[40:43],a[116:119],v[208:211],v[40:43]\nv_exp_f32 v162,v162\nM v[48:51],a[88:91],v[192:195],v[48:51]\nv_exp_f32 v163,v163\nM v[48:51],a[120:123],v[208:211],v[48:51]\nglobal_load_dwordx2 a[216:217],v248,s[22:23]\nM v[56:59],a[92:95],v[192:195],v[56:59]\n" \
"v_exp_f32 v176,v176\nM v[56:59],a[124:127],v[208:211],v[56:59]\nv_exp_f32 v177,v177\nM a[192:195],a[208:211],v[192:195],a[192:195]\nv_exp_f32 v178,v178\nM a[192:195],a[208:211],v[208:211],a[192:195]\nds_read_b128 a[16:19],v250 offset:4096\nds_read_b128 a[20:23],v250 offset:5120\n" \
"M v[4:7],a[64:67],v[196:199],v[4:7]\nv_exp_f32 v179,v179\nM v[4:7],a[96:99],v[212:215],v[4:7]\nv_exp_f32 v140,v140\nM v[12:15],a[68:71],v[196:199],v[12:15]\nv_exp_f32 v141,v141\nM v[12:15],a[100:103],v[212:215],v[12:15]\nbuffer_load_dwordx4 v240,s[48:51],s95 offen offset:1024 sc1 lds\n" \
"M v[20:23],a[72:75],v[196:199],v[20:23]\nv_exp_f32 v142,v142\nM v[20:23],a[104:107],v[212:215],v[20:23]\nv_exp_f32 v143,v143\nM v[28:31],a[76:79],v[196:199],v[28:31]\nv_exp_f32 v156,v156\nM v[28:31],a[108:111],v[212:215],v[28:31]\nds_read_b128 a[24:27],v250 offset:6144\n" \
"ds_read_b128 a[28:31],v250 offset:7168\nM v[36:39],a[80:83],v[196:199],v[36:39]\nv_exp_f32 v157,v157\nM v[36:39],a[112:115],v[212:215],v[36:39]\nv_exp_f32 v158,v158\nM v[44:47],a[84:87],v[196:199],v[44:47]\nv_exp_f32 v159,v159\nM v[44:47],a[116:119],v[212:215],v[44:47]\n" \
"global_load_dwordx2 a[232:233],v248,s[24:25]\nM v[52:55],a[88:91],v[196:199],v[52:55]\nv_add_u32 v248,s71,v248\nv_cvt_pk_bf16_f32 v192,v128,v129\nM v[52:55],a[120:123],v[212:215],v[52:55]\nv_exp_f32 v164,v164\nM v[60:63],a[92:95],v[196:199],v[60:63]\nv_exp_f32 v165,v165\n" \
"M v[60:63],a[124:127],v[212:215],v[60:63]\nds_read_b128 a[32:35],v250 offset:8192\nds_read_b128 a[36:39],v250 offset:9216\nM a[196:199],a[208:211],v[196:199],a[196:199]\nv_exp_f32 v166,v166\nM a[196:199],a[208:211],v[212:215],a[196:199]\nv_exp_f32 v167,v167\nM v[64:67],a[64:67],v[200:203],v[64:67]\n" \
"v_exp_f32 v180,v180\nM v[64:67],a[96:99],v[216:219],v[64:67]\nbuffer_load_dwordx4 v240,s[48:51],s95 offen offset:2048 sc1 lds\nM v[72:75],a[68:71],v[200:203],v[72:75]\nv_exp_f32 v181,v181\nM v[72:75],a[100:103],v[216:219],v[72:75]\nv_exp_f32 v182,v182\nM v[80:83],a[72:75],v[200:203],v[80:83]\n" \
"v_exp_f32 v183,v183\nM v[80:83],a[104:107],v[216:219],v[80:83]\nds_read_b128 a[40:43],v250 offset:10240\nds_read_b128 a[44:47],v250 offset:11264\nM v[88:91],a[76:79],v[200:203],v[88:91]\nv_exp_f32 v168,v168\nM v[88:91],a[108:111],v[216:219],v[88:91]\nv_exp_f32 v169,v169\n" \
"M v[96:99],a[80:83],v[200:203],v[96:99]\nv_exp_f32 v170,v170\nM v[96:99],a[112:115],v[216:219],v[96:99]\nglobal_load_dwordx2 a[218:219],v248,s[22:23]\nM v[104:107],a[84:87],v[200:203],v[104:107]\nv_exp_f32 v171,v171\nM v[104:107],a[116:119],v[216:219],v[104:107]\nv_cvt_pk_bf16_f32 v193,v130,v131\n" \
"v_cvt_pk_bf16_f32 v194,v144,v145\nM v[112:115],a[88:91],v[200:203],v[112:115]\nv_cvt_pk_bf16_f32 v195,v146,v147\nv_cvt_pk_bf16_f32 v208,v160,v161\nM v[112:115],a[120:123],v[216:219],v[112:115]\nds_read_b128 a[48:51],v250 offset:12288\nds_read_b128 a[52:55],v250 offset:13312\nM v[120:123],a[92:95],v[200:203],v[120:123]\n" \
"v_cvt_pk_bf16_f32 v209,v162,v163\nv_cvt_pk_bf16_f32 v210,v176,v177\nM v[120:123],a[124:127],v[216:219],v[120:123]\nv_cvt_pk_bf16_f32 v211,v178,v179\nv_cvt_pk_bf16_f32 v196,v132,v133\nM a[200:203],a[208:211],v[200:203],a[200:203]\nv_cvt_pk_bf16_f32 v197,v134,v135\nv_cvt_pk_bf16_f32 v198,v148,v149\n" \
"M a[200:203],a[208:211],v[216:219],a[200:203]\nbuffer_load_dwordx4 v240,s[48:51],s95 offen offset:3072 sc1 lds\nM v[68:71],a[64:67],v[204:207],v[68:71]\nv_cvt_pk_bf16_f32 v199,v150,v151\nv_cvt_pk_bf16_f32 v200,v136,v137\nM v[68:71],a[96:99],v[220:223],v[68:71]\nv_cvt_pk_bf16_f32 v201,v138,v139\nv_cvt_pk_bf16_f32 v202,v152,v153\n" \
"M v[76:79],a[68:71],v[204:207],v[76:79]\nv_cvt_pk_bf16_f32 v203,v154,v155\ns_add_u32 s95,s95,0x4000\nM v[76:79],a[100:103],v[220:223],v[76:79]\nds_read_b128 a[56:59],v250 offset:14336\nds_read_b128 a[60:63],v250 offset:15360\nM v[84:87],a[72:75],v[204:207],v[84:87]\nv_add_u32 v250,s94,v241\n" \
"M v[84:87],a[104:107],v[220:223],v[84:87]\nM v[92:95],a[76:79],v[204:207],v[92:95]\nM v[92:95],a[108:111],v[220:223],v[92:95]\nglobal_load_dwordx2 a[234:235],v248,s[24:25]\nM v[100:103],a[80:83],v[204:207],v[100:103]\nv_add_u32 v248,s71,v248\nM v[100:103],a[112:115],v[220:223],v[100:103]\nM v[108:111],a[84:87],v[204:207],v[108:111]\n" \
"M v[108:111],a[116:119],v[220:223],v[108:111]\nM v[116:119],a[88:91],v[204:207],v[116:119]\nM v[116:119],a[120:123],v[220:223],v[116:119]\nM v[124:127],a[92:95],v[204:207],v[124:127]\nM v[124:127],a[124:127],v[220:223],v[124:127]\nM a[204:207],a[208:211],v[204:207],a[204:207]\nM a[204:207],a[208:211],v[220:223],a[204:207]\ns_waitcnt lgkmcnt(4)\n" \
"M v[128:131],a[0:3],a[128:131],v[224:227]\ns_add_u32 m0,s94,s64\nv_cvt_pk_bf16_f32 v204,v140,v141\nM v[128:131],a[4:7],a[132:135],v[128:131]\nds_read_b128 a[64:67],v250 offset:0\nds_read_b128 a[96:99],v250 offset:8192\nM v[128:131],a[8:11],a[136:139],v[128:131]\nv_cvt_pk_bf16_f32 v205,v142,v143\n" \
"v_cvt_pk_bf16_f32 v206,v156,v157\nM v[128:131],a[12:15],a[140:143],v[128:131]\nv_cvt_pk_bf16_f32 v207,v158,v159\nv_cvt_pk_bf16_f32 v212,v164,v165\nM v[132:135],a[12:15],a[156:159],v[228:231]\nbuffer_load_dwordx4 v240,s[44:47],s95 offen offset:0 sc1 lds\nM v[132:135],a[8:11],a[152:155],v[132:135]\nv_cvt_pk_bf16_f32 v213,v166,v167\n" \
"v_cvt_pk_bf16_f32 v216,v168,v169\nM v[132:135],a[4:7],a[148:151],v[132:135]\nv_cvt_pk_bf16_f32 v217,v170,v171\nv_cvt_pk_bf16_f32 v214,v180,v181\nM v[132:135],a[0:3],a[144:147],v[132:135]\nds_read_b128 a[68:71],v250 offset:1024\nds_read_b128 a[100:103],v250 offset:9216\nM v[136:139],a[0:3],a[160:163],v[232:235]\n" \
"v_exp_f32 v172,v172\nM v[136:139],a[4:7],a[164:167],v[136:139]\nv_exp_f32 v173,v173\nM v[136:139],a[8:11],a[168:171],v[136:139]\nv_exp_f32 v174,v174\nM v[136:139],a[12:15],a[172:175],v[136:139]\nv_exp_f32 v175,v175\nM v[140:143],a[12:15],a[188:191],v[236:239]\n" \
"v_cvt_pk_bf16_f32 v220,v172,v173\nv_cvt_pk_bf16_f32 v221,v174,v175\nM v[140:143],a[8:11],a[184:187],v[140:143]\nds_read_b128 a[72:75],v250 offset:2048\nds_read_b128 a[104:107],v250 offset:10240\nM v[140:143],a[4:7],a[180:183],v[140:143]\nglobal_load_dwordx2 a[220:221],v248,s[22:23]\nM v[140:143],a[0:3],a[176:179],v[140:143]\n" \
"v_cvt_pk_bf16_f32 v215,v182,v183\ns_add_u32 s94,s94,0x4000\nM v[144:147],a[16:19],a[128:131],v[224:227]\nv_exp_f32 v184,v184\nM v[144:147],a[20:23],a[132:135],v[144:147]\nv_exp_f32 v185,v185\nM v[144:147],a[24:27],a[136:139],v[144:147]\nv_exp_f32 v186,v186\n" \
"M v[144:147],a[28:31],a[140:143],v[144:147]\nds_read_b128 a[76:79],v250 offset:3072\nds_read_b128 a[108:111],v250 offset:11264\nM v[148:151],a[28:31],a[156:159],v[228:231]\nv_exp_f32 v187,v187\nM v[148:151],a[24:27],a[152:155],v[148:151]\nv_cvt_pk_bf16_f32 v218,v184,v185\nv_cvt_pk_bf16_f32 v219,v186,v187\n" \
"M v[148:151],a[20:23],a[148:151],v[148:151]\nbuffer_load_dwordx4 v240,s[44:47],s95 offen offset:1024 sc1 lds\nM v[148:151],a[16:19],a[144:147],v[148:151]\nv_exp_f32 v188,v188\nM v[152:155],a[16:19],a[160:163],v[232:235]\nv_exp_f32 v189,v189\nM v[152:155],a[20:23],a[164:167],v[152:155]\nds_read_b128 a[80:83],v250 offset:4096\n" \
"ds_read_b128 a[112:115],v250 offset:12288\nM v[152:155],a[24:27],a[168:171],v[152:155]\nv_exp_f32 v190,v190\nM v[152:155],a[28:31],a[172:175],v[152:155]\nv_exp_f32 v191,v191\nM v[156:159],a[28:31],a[188:191],v[236:239]\nv_cvt_pk_bf16_f32 v222,v188,v189\nv_cvt_pk_bf16_f32 v223,v190,v191\n" \
"M v[156:159],a[24:27],a[184:187],v[156:159]\nv_exp_f32 v128,v128\nM v[156:159],a[20:23],a[180:183],v[156:159]\nv_exp_f32 v129,v129\nM v[156:159],a[16:19],a[176:179],v[156:159]\nds_read_b128 a[84:87],v250 offset:5120\nds_read_b128 a[116:119],v250 offset:13312\nM v[160:163],a[32:35],a[128:131],v[224:227]\n" \
"global_load_dwordx2 a[236:237],v248,s[24:25]\nM v[160:163],a[36:39],a[132:135],v[160:163]\nv_add_u32 v248,s71,v248\ns_and_b32 s94,s94,0xffff\nM v[160:163],a[40:43],a[136:139],v[160:163]\nv_exp_f32 v130,v130\nM v[160:163],a[44:47],a[140:143],v[160:163]\nv_exp_f32 v131,v131\n" \
"M v[164:167],a[44:47],a[156:159],v[228:231]\nv_exp_f32 v144,v144\nM v[164:167],a[40:43],a[152:155],v[164:167]\nds_read_b128 a[88:91],v250 offset:6144\nds_read_b128 a[120:123],v250 offset:14336\nM v[164:167],a[36:39],a[148:151],v[164:167]\nv_exp_f32 v145,v145\nM v[164:167],a[32:35],a[144:147],v[164:167]\n" \
"v_exp_f32 v146,v146\nM v[168:171],a[32:35],a[160:163],v[232:235]\nbuffer_load_dwordx4 v240,s[44:47],s95 offen offset:2048 sc1 lds\nM v[168:171],a[36:39],a[164:167],v[168:171]\nv_exp_f32 v147,v147\nM v[168:171],a[40:43],a[168:171],v[168:171]\nv_exp_f32 v132,v132\nM v[168:171],a[44:47],a[172:175],v[168:171]\n" \
"ds_read_b128 a[92:95],v250 offset:7168\nds_read_b128 a[124:127],v250 offset:15360\nM v[172:175],a[44:47],a[188:191],v[236:239]\nv_exp_f32 v133,v133\nM v[172:175],a[40:43],a[184:187],v[172:175]\nv_exp_f32 v134,v134\nM v[172:175],a[36:39],a[180:183],v[172:175]\nv_exp_f32 v135,v135\n" \
"M v[172:175],a[32:35],a[176:179],v[172:175]\nv_xor_b32 v250,0x18000,v250\ns_waitcnt lgkmcnt(3)\nM v[176:179],a[48:51],a[128:131],v[224:227]\nv_exp_f32 v148,v148\nM v[176:179],a[52:55],a[132:135],v[176:179]\nv_exp_f32 v149,v149\nM v[176:179],a[56:59],a[136:139],v[176:179]\n" \
"global_load_dwordx2 a[222:223],v248,s[22:23]\nM v[176:179],a[60:63],a[140:143],v[176:179]\nv_exp_f32 v150,v150\nM v[180:183],a[60:63],a[156:159],v[228:231]\nv_exp_f32 v151,v151\nM v[180:183],a[56:59],a[152:155],v[180:183]\nv_exp_f32 v136,v136\nM v[180:183],a[52:55],a[148:151],v[180:183]\n" \
"v_exp_f32 v137,v137\nM v[180:183],a[48:51],a[144:147],v[180:183]\nv_exp_f32 v138,v138\nM v[184:187],a[48:51],a[160:163],v[232:235]\nv_exp_f32 v139,v139\nM v[184:187],a[52:55],a[164:167],v[184:187]\nv_exp_f32 v152,v152\nM v[184:187],a[56:59],a[168:171],v[184:187]\n" \
"buffer_load_dwordx4 v240,s[44:47],s95 offen offset:3072 sc1 lds\nM v[184:187],a[60:63],a[172:175],v[184:187]\nv_exp_f32 v153,v153\nM v[188:191],a[60:63],a[188:191],v[236:239]\nv_exp_f32 v154,v154\nM v[188:191],a[56:59],a[184:187],v[188:191]\nv_exp_f32 v155,v155\nM v[188:191],a[52:55],a[180:183],v[188:191]\n" \
"global_load_dwordx2 a[238:239],v248,s[24:25]\nM v[188:191],a[48:51],a[176:179],v[188:191]\ns_add_u32 m0,m0,0x10000\nv_add_u32 v248,s71,v248\nM v[0:3],a[64:67],v[192:195],v[0:3]\ns_waitcnt vmcnt(16) lgkmcnt(0)\ns_barrier\nM v[0:3],a[96:99],v[208:211],v[0:3]\n" \
"ds_read_b128 a[0:3],v250 offset:0\nds_read_b128 a[4:7],v250 offset:1024\nM v[8:11],a[68:71],v[192:195],v[8:11]\nv_exp_f32 v160,v160\nM v[8:11],a[100:103],v[208:211],v[8:11]\nv_exp_f32 v161,v161\nM v[16:19],a[72:75],v[192:195],v[16:19]\nv_exp_f32 v162,v162\n" \
"M v[16:19],a[104:107],v[208:211],v[16:19]\nbuffer_load_dwordx4 v240,s[48:51],s95 offen offset:0 sc1 lds\nM v[24:27],a[76:79],v[192:195],v[24:27]\nv_exp_f32 v163,v163\nM v[24:27],a[108:111],v[208:211],v[24:27]\nv_exp_f32 v176,v176\nM v[32:35],a[80:83],v[192:195],v[32:35]\nv_exp_f32 v177,v177\n" \
"M v[32:35],a[112:115],v[208:211],v[32:35]\nds_read_b128 a[8:11],v250 offset:2048\nds_read_b128 a[12:15],v250 offset:3072\nM v[40:43],a[84:87],v[192:195],v[40:43]\nv_exp_f32 v178,v178\nM v[40:43],a[116:119],v[208:211],v[40:43]\nv_exp_f32 v179,v179\nM v[48:51],a[88:91],v[192:195],v[48:51]\n" \
"v_exp_f32 v140,v140\nM v[48:51],a[120:123],v[208:211],v[48:51]\nglobal_load_dwordx2 a[224:225],v248,s[22:23]\nM v[56:59],a[92:95],v[192:195],v[56:59]\nv_exp_f32 v141,v141\nM v[56:59],a[124:127],v[208:211],v[56:59]\nv_exp_f32 v142,v142\nM a[192:195],a[208:211],v[192:195],a[192:195]\n" \
"v_exp_f32 v143,v143\nM a[192:195],a[208:211],v[208:211],a[192:195]\nds_read_b128 a[16:19],v250 offset:4096\nds_read_b128 a[20:23],v250 offset:5120\nM v[4:7],a[64:67],v[196:199],v[4:7]\nv_exp_f32 v156,v156\nM v[4:7],a[96:99],v[212:215],v[4:7]\nv_exp_f32 v157,v157\n" \
"M v[12:15],a[68:71],v[196:199],v[12:15]\nv_exp_f32 v158,v158\nM v[12:15],a[100:103],v[212:215],v[12:15]\nbuffer_load_dwordx4 v240,s[48:51],s95 offen offset:1024 sc1 lds\nM v[20:23],a[72:75],v[196:199],v[20:23]\nv_exp_f32 v159,v159\nM v[20:23],a[104:107],v[212:215],v[20:23]\nv_exp_f32 v164,v164\n" \
"M v[28:31],a[76:79],v[196:199],v[28:31]\nv_exp_f32 v165,v165\nM v[28:31],a[108:111],v[212:215],v[28:31]\nds_read_b128 a[24:27],v250 offset:6144\nds_read_b128 a[28:31],v250 offset:7168\nM v[36:39],a[80:83],v[196:199],v[36:39]\nv_exp_f32 v166,v166\nM v[36:39],a[112:115],v[212:215],v[36:39]\n" \
"v_exp_f32 v167,v167\nM v[44:47],a[84:87],v[196:199],v[44:47]\nv_exp_f32 v180,v180\nM v[44:47],a[116:119],v[212:215],v[44:47]\nglobal_load_dwordx2 a[240:241],v248,s[24:25]\nM v[52:55],a[88:91],v[196:199],v[52:55]\nv_add_u32 v248,s71,v248\nv_cvt_pk_bf16_f32 v192,v128,v129\n" \
"M v[52:55],a[120:123],v[212:215],v[52:55]\nv_exp_f32 v181,v181\nM v[60:63],a[92:95],v[196:199],v[60:63]\nv_exp_f32 v182,v182\nM v[60:63],a[124:127],v[212:215],v[60:63]\nds_read_b128 a[32:35],v250 offset:8192\nds_read_b128 a[36:39],v250 offset:9216\nM a[196:199],a[208:211],v[196:199],a[196:199]\n" \
"v_exp_f32 v183,v183\nM a[196:199],a[208:211],v[212:215],a[196:199]\nv_exp_f32 v168,v168\nM v[64:67],a[64:67],v[200:203],v[64:67]\nv_exp_f32 v169,v169\nM v[64:67],a[96:99],v[216:219],v[64:67]\nbuffer_load_dwordx4 v240,s[48:51],s95 offen offset:2048 sc1 lds\nM v[72:75],a[68:71],v[200:203],v[72:75]\n" \
"v_exp_f32 v170,v170\nM v[72:75],a[100:103],v[216:219],v[72:75]\nv_exp_f32 v171,v171\nM v[80:83],a[72:75],v[200:203],v[80:83]\nv_cvt_pk_bf16_f32 v193,v130,v131\nv_cvt_pk_bf16_f32 v194,v144,v145\nM v[80:83],a[104:107],v[216:219],v[80:83]\nds_read_b128 a[40:43],v250 offset:10240\n" \
"ds_read_b128 a[44:47],v250 offset:11264\nM v[88:91],a[76:79],v[200:203],v[88:91]\nv_cvt_pk_bf16_f32 v195,v146,v147\nv_cvt_pk_bf16_f32 v208,v160,v161\nM v[88:91],a[108:111],v[216:219],v[88:91]\nv_cvt_pk_bf16_f32 v209,v162,v163\nv_cvt_pk_bf16_f32 v210,v176,v177\nM v[96:99],a[80:83],v[200:203],v[96:99]\n" \
"v_cvt_pk_bf16_f32 v211,v178,v179\nv_cvt_pk_bf16_f32 v196,v132,v133\nM v[96:99],a[112:115],v[216:219],v[96:99]\nglobal_load_dwordx2 a[226:227],v248,s[22:23]\nM v[104:107],a[84:87],v[200:203],v[104:107]\nv_cvt_pk_bf16_f32 v197,v134,v135\nv_cvt_pk_bf16_f32 v198,v148,v149\nM v[104:107],a[116:119],v[216:219],v[104:107]\n" \
"v_cvt_pk_bf16_f32 v199,v150,v151\nM v[112:115],a[88:91],v[200:203],v[112:115]\nM v[112:115],a[120:123],v[216:219],v[112:115]\nds_read_b128 a[48:51],v250 offset:12288\nds_read_b128 a[52:55],v250 offset:13312\nM v[120:123],a[92:95],v[200:203],v[120:123]\nM v[120:123],a[124:127],v[216:219],v[120:123]\nM a[200:203],a[208:211],v[200:203],a[200:203]\n" \
"v_cvt_pk_bf16_f32 v200,v136,v137\nv_cvt_pk_bf16_f32 v201,v138,v139\nM a[200:203],a[208:211],v[216:219],a[200:203]\nbuffer_load_dwordx4 v240,s[48:51],s95 offen offset:3072 sc1 lds\nM v[68:71],a[64:67],v[204:207],v[68:71]\nv_cvt_pk_bf16_f32 v202,v152,v153\nv_cvt_pk_bf16_f32 v203,v154,v155\nM v[68:71],a[96:99],v[220:223],v[68:71]\n" \
"s_add_u32 s95,s95,0x4000\nM v[76:79],a[68:71],v[204:207],v[76:79]\nM v[76:79],a[100:103],v[220:223],v[76:79]\nds_read_b128 a[56:59],v250 offset:14336\nds_read_b128 a[60:63],v250 offset:15360\nM v[84:87],a[72:75],v[204:207],v[84:87]\nv_add_u32 v250,s94,v241\nM v[84:87],a[104:107],v[220:223],v[84:87]\n" \
"M v[92:95],a[76:79],v[204:207],v[92:95]\nM v[92:95],a[108:111],v[220:223],v[92:95]\nglobal_load_dwordx2 a[242:243],v248,s[24:25]\nM v[100:103],a[80:83],v[204:207],v[100:103]\nv_add_u32 v248,s71,v248\nM v[100:103],a[112:115],v[220:223],v[100:103]\nM v[108:111],a[84:87],v[204:207],v[108:111]\nM v[108:111],a[116:119],v[220:223],v[108:111]\n" \
"M v[116:119],a[88:91],v[204:207],v[116:119]\nM v[116:119],a[120:123],v[220:223],v[116:119]\nM v[124:127],a[92:95],v[204:207],v[124:127]\nM v[124:127],a[124:127],v[220:223],v[124:127]\nM a[204:207],a[208:211],v[204:207],a[204:207]\nM a[204:207],a[208:211],v[220:223],a[204:207]\ns_cmp_eq_u32 s62,7\ns_cbranch_scc0 .Ltrans\n" \
"s_waitcnt lgkmcnt(4)\nM v[136:139],a[0:3],a[160:163],v[232:235]\ns_add_u32 m0,s94,s64\nv_cvt_pk_bf16_f32 v204,v140,v141\nM v[136:139],a[4:7],a[164:167],v[136:139]\nds_read_b128 a[64:67],v250 offset:0\nds_read_b128 a[96:99],v250 offset:8192\nM v[136:139],a[8:11],a[168:171],v[136:139]\n" \
"buffer_load_dwordx4 v240,s[44:47],s95 offen offset:0 sc1 lds\nM v[136:139],a[12:15],a[172:175],v[136:139]\nv_cvt_pk_bf16_f32 v205,v142,v143\nv_cvt_pk_bf16_f32 v206,v156,v157\nM v[140:143],a[12:15],a[188:191],v[236:239]\nds_read_b128 a[68:71],v250 offset:1024\nds_read_b128 a[100:103],v250 offset:9216\nM v[140:143],a[8:11],a[184:187],v[140:143]\n" \
"buffer_store_dwordx4 a[248:251],v253,s[52:55],s33 offen offset:0 sc1\nM v[140:143],a[4:7],a[180:183],v[140:143]\nv_cvt_pk_bf16_f32 v207,v158,v159\nv_cvt_pk_bf16_f32 v216,v168,v169\nM v[140:143],a[0:3],a[176:179],v[140:143]\nds_read_b128 a[72:75],v250 offset:2048\nds_read_b128 a[104:107],v250 offset:10240\nM v[152:155],a[16:19],a[160:163],v[232:235]\n" \
"v_cvt_pk_bf16_f32 v217,v170,v171\ns_add_u32 s71,s33,16384\nM v[152:155],a[20:23],a[164:167],v[152:155]\nv_exp_f32 v172,v172\nM v[152:155],a[24:27],a[168:171],v[152:155]\nds_read_b128 a[76:79],v250 offset:3072\nds_read_b128 a[108:111],v250 offset:11264\nM v[152:155],a[28:31],a[172:175],v[152:155]\n" \
"buffer_load_dwordx4 v240,s[44:47],s95 offen offset:1024 sc1 lds\nM v[156:159],a[28:31],a[188:191],v[236:239]\nv_exp_f32 v173,v173\nM v[156:159],a[24:27],a[184:187],v[156:159]\nds_read_b128 a[80:83],v250 offset:4096\nds_read_b128 a[112:115],v250 offset:12288\nM v[156:159],a[20:23],a[180:183],v[156:159]\nbuffer_store_dwordx4 a[248:251],v253,s[52:55],s33 offen offset:1024 sc1\n" \
"M v[156:159],a[16:19],a[176:179],v[156:159]\nv_exp_f32 v174,v174\nM v[168:171],a[32:35],a[160:163],v[232:235]\nds_read_b128 a[84:87],v250 offset:5120\nds_read_b128 a[116:119],v250 offset:13312\nM v[168:171],a[36:39],a[164:167],v[168:171]\nv_exp_f32 v175,v175\nM v[168:171],a[40:43],a[168:171],v[168:171]\n" \
"v_cvt_pk_bf16_f32 v220,v172,v173\nv_cvt_pk_bf16_f32 v221,v174,v175\nM v[168:171],a[44:47],a[172:175],v[168:171]\nds_read_b128 a[88:91],v250 offset:6144\nds_read_b128 a[120:123],v250 offset:14336\nM v[172:175],a[44:47],a[188:191],v[236:239]\nbuffer_load_dwordx4 v240,s[44:47],s95 offen offset:2048 sc1 lds\nM v[172:175],a[40:43],a[184:187],v[172:175]\n" \
"v_exp_f32 v184,v184\nM v[172:175],a[36:39],a[180:183],v[172:175]\nv_exp_f32 v185,v185\nv_exp_f32 v186,v186\nv_exp_f32 v187,v187\nds_read_b128 a[92:95],v250 offset:7168\nds_read_b128 a[124:127],v250 offset:15360\nM v[172:175],a[32:35],a[176:179],v[172:175]\n" \
"buffer_store_dwordx4 a[248:251],v253,s[52:55],s33 offen offset:2048 sc1\nv_cvt_pk_bf16_f32 v218,v184,v185\nv_cvt_pk_bf16_f32 v219,v186,v187\ns_waitcnt lgkmcnt(6)\nM v[184:187],a[48:51],a[160:163],v[232:235]\nv_exp_f32 v188,v188\nM v[184:187],a[52:55],a[164:167],v[184:187]\nv_exp_f32 v189,v189\n" \
"M v[184:187],a[56:59],a[168:171],v[184:187]\nv_exp_f32 v190,v190\nv_exp_f32 v191,v191\nM v[184:187],a[60:63],a[172:175],v[184:187]\nv_cvt_pk_bf16_f32 v222,v188,v189\nv_cvt_pk_bf16_f32 v223,v190,v191\nM v[188:191],a[60:63],a[188:191],v[236:239]\nv_xor_b32 v250,0x18000,v250\n" \
"s_add_u32 s70,s78,16384\nM v[188:191],a[56:59],a[184:187],v[188:191]\nbuffer_load_dwordx4 v240,s[44:47],s95 offen offset:3072 sc1 lds\nM v[188:191],a[52:55],a[180:183],v[188:191]\ns_add_u32 m0,m0,0x10000\nv_cvt_pk_bf16_f32 v212,v164,v165\nM v[188:191],a[48:51],a[176:179],v[188:191]\nv_cvt_pk_bf16_f32 v213,v166,v167\n" \
"v_cvt_pk_bf16_f32 v214,v180,v181\nM v[64:67],a[64:67],v[200:203],v[64:67]\nbuffer_store_dwordx4 a[248:251],v253,s[52:55],s33 offen offset:3072 sc1\nM v[64:67],a[96:99],v[216:219],v[64:67]\nv_cvt_pk_bf16_f32 v215,v182,v183\ns_add_u32 s94,s94,0x4000\nM v[72:75],a[68:71],v[200:203],v[72:75]\ns_waitcnt vmcnt(16) lgkmcnt(0)\n" \
"s_barrier\nM v[72:75],a[100:103],v[216:219],v[72:75]\nds_read_b128 a[0:3],v250 offset:0\nds_read_b128 a[4:7],v250 offset:1024\nM v[80:83],a[72:75],v[200:203],v[80:83]\nv_exp_f32 v136,v136\nM v[80:83],a[104:107],v[216:219],v[80:83]\nbuffer_store_dwordx4 a[248:251],v253,s[52:55],s71 offen offset:0 sc1\n" \
"M v[88:91],a[76:79],v[200:203],v[88:91]\nv_exp_f32 v137,v137\nM v[88:91],a[108:111],v[216:219],v[88:91]\nbuffer_load_dwordx4 v240,s[48:51],s95 offen offset:0 sc1 lds\nM v[96:99],a[80:83],v[200:203],v[96:99]\nv_exp_f32 v138,v138\nM v[96:99],a[112:115],v[216:219],v[96:99]\nbuffer_store_dwordx4 a[248:251],v253,s[52:55],s71 offen offset:1024 sc1\n" \
"M v[104:107],a[84:87],v[200:203],v[104:107]\nv_exp_f32 v139,v139\nM v[104:107],a[116:119],v[216:219],v[104:107]\nds_read_b128 a[8:11],v250 offset:2048\nds_read_b128 a[12:15],v250 offset:3072\nM v[112:115],a[88:91],v[200:203],v[112:115]\nv_exp_f32 v152,v152\nM v[112:115],a[120:123],v[216:219],v[112:115]\n" \
"buffer_store_dwordx4 a[248:251],v253,s[52:55],s71 offen offset:2048 sc1\nM v[120:123],a[92:95],v[200:203],v[120:123]\nv_exp_f32 v153,v153\nM v[120:123],a[124:127],v[216:219],v[120:123]\nv_exp_f32 v154,v154\nM a[200:203],a[208:211],v[200:203],a[200:203]\nv_exp_f32 v155,v155\nM a[200:203],a[208:211],v[216:219],a[200:203]\n" \
"buffer_store_dwordx4 a[248:251],v253,s[52:55],s71 offen offset:3072 sc1\nM v[68:71],a[64:67],v[204:207],v[68:71]\nv_exp_f32 v168,v168\nM v[68:71],a[96:99],v[220:223],v[68:71]\nds_read_b128 a[16:19],v250 offset:4096\nds_read_b128 a[20:23],v250 offset:5120\nM v[76:79],a[68:71],v[204:207],v[76:79]\nv_exp_f32 v169,v169\n" \
"M v[76:79],a[100:103],v[220:223],v[76:79]\nv_exp_f32 v170,v170\nM v[84:87],a[72:75],v[204:207],v[84:87]\nv_exp_f32 v171,v171\nM v[84:87],a[104:107],v[220:223],v[84:87]\nbuffer_load_dwordx4 v240,s[48:51],s95 offen offset:1024 sc1 lds\nM v[92:95],a[76:79],v[204:207],v[92:95]\nv_exp_f32 v184,v184\n" \
"M v[92:95],a[108:111],v[220:223],v[92:95]\nbuffer_store_dwordx4 a[248:251],v253,s[52:55],s78 offen offset:0 sc1\nM v[100:103],a[80:83],v[204:207],v[100:103]\nv_exp_f32 v185,v185\nM v[100:103],a[112:115],v[220:223],v[100:103]\nds_read_b128 a[24:27],v250 offset:6144\nds_read_b128 a[28:31],v250 offset:7168\nM v[108:111],a[84:87],v[204:207],v[108:111]\n" \
"v_exp_f32 v186,v186\nM v[108:111],a[116:119],v[220:223],v[108:111]\nbuffer_store_dwordx4 a[248:251],v253,s[52:55],s78 offen offset:1024 sc1\nM v[116:119],a[88:91],v[204:207],v[116:119]\nv_exp_f32 v187,v187\nM v[116:119],a[120:123],v[220:223],v[116:119]\ns_and_b32 s94,s94,0xffff\nv_cvt_pk_bf16_f32 v200,v136,v137\n" \
"M v[124:127],a[92:95],v[204:207],v[124:127]\nv_exp_f32 v140,v140\nM v[124:127],a[124:127],v[220:223],v[124:127]\nbuffer_store_dwordx4 a[248:251],v253,s[52:55],s78 offen offset:2048 sc1\nM a[204:207],a[208:211],v[204:207],a[204:207]\nv_exp_f32 v141,v141\nM a[204:207],a[208:211],v[220:223],a[204:207]\nds_read_b128 a[32:35],v250 offset:8192\n" \
"ds_read_b128 a[36:39],v250 offset:9216\nM v[0:3],a[64:67],v[192:195],v[0:3]\nv_exp_f32 v142,v142\nM v[0:3],a[96:99],v[208:211],v[0:3]\nv_exp_f32 v143,v143\nM v[8:11],a[68:71],v[192:195],v[8:11]\nv_exp_f32 v156,v156\nM v[8:11],a[100:103],v[208:211],v[8:11]\n" \
"buffer_load_dwordx4 v240,s[48:51],s95 offen offset:2048 sc1 lds\nM v[16:19],a[72:75],v[192:195],v[16:19]\nv_exp_f32 v157,v157\nM v[16:19],a[104:107],v[208:211],v[16:19]\nbuffer_store_dwordx4 a[248:251],v253,s[52:55],s78 offen offset:3072 sc1\nM v[24:27],a[76:79],v[192:195],v[24:27]\nv_exp_f32 v158,v158\nM v[24:27],a[108:111],v[208:211],v[24:27]\n" \
"ds_read_b128 a[40:43],v250 offset:10240\nds_read_b128 a[44:47],v250 offset:11264\nM v[32:35],a[80:83],v[192:195],v[32:35]\nv_exp_f32 v159,v159\nM v[32:35],a[112:115],v[208:211],v[32:35]\nbuffer_store_dwordx4 a[248:251],v253,s[52:55],s70 offen offset:0 sc1\nM v[40:43],a[84:87],v[192:195],v[40:43]\nv_cvt_pk_bf16_f32 v201,v138,v139\n" \
"v_cvt_pk_bf16_f32 v202,v152,v153\nM v[40:43],a[116:119],v[208:211],v[40:43]\nv_cvt_pk_bf16_f32 v203,v154,v155\nv_cvt_pk_bf16_f32 v216,v168,v169\nM v[48:51],a[88:91],v[192:195],v[48:51]\nv_cvt_pk_bf16_f32 v217,v170,v171\nv_cvt_pk_bf16_f32 v218,v184,v185\nM v[48:51],a[120:123],v[208:211],v[48:51]\n" \
"buffer_store_dwordx4 a[248:251],v253,s[52:55],s70 offen offset:1024 sc1\nM v[56:59],a[92:95],v[192:195],v[56:59]\nv_cvt_pk_bf16_f32 v219,v186,v187\nM v[56:59],a[124:127],v[208:211],v[56:59]\nds_read_b128 a[48:51],v250 offset:12288\nds_read_b128 a[52:55],v250 offset:13312\nM a[192:195],a[208:211],v[192:195],a[192:195]\nM a[192:195],a[208:211],v[208:211],a[192:195]\n" \
"M v[4:7],a[64:67],v[196:199],v[4:7]\nM v[4:7],a[96:99],v[212:215],v[4:7]\nbuffer_load_dwordx4 v240,s[48:51],s95 offen offset:3072 sc1 lds\nM v[12:15],a[68:71],v[196:199],v[12:15]\ns_add_u32 s95,s95,0xffff4000\nM v[12:15],a[100:103],v[212:215],v[12:15]\nbuffer_store_dwordx4 a[248:251],v253,s[52:55],s70 offen offset:2048 sc1\nM v[20:23],a[72:75],v[196:199],v[20:23]\n" \
"M v[20:23],a[104:107],v[212:215],v[20:23]\nds_read_b128 a[56:59],v250 offset:14336\nds_read_b128 a[60:63],v250 offset:15360\nM v[28:31],a[76:79],v[196:199],v[28:31]\nv_add_u32 v250,s94,v241\nM v[28:31],a[108:111],v[212:215],v[28:31]\nbuffer_store_dwordx4 a[248:251],v253,s[52:55],s70 offen offset:3072 sc1\nM v[36:39],a[80:83],v[196:199],v[36:39]\n" \
"M v[36:39],a[112:115],v[212:215],v[36:39]\nM v[44:47],a[84:87],v[196:199],v[44:47]\nM v[44:47],a[116:119],v[212:215],v[44:47]\nM v[52:55],a[88:91],v[196:199],v[52:55]\nM v[52:55],a[120:123],v[212:215],v[52:55]\nM v[60:63],a[92:95],v[196:199],v[60:63]\nM v[60:63],a[124:127],v[212:215],v[60:63]\nM a[196:199],a[208:211],v[196:199],a[196:199]\n" \
"M a[196:199],a[208:211],v[212:215],a[196:199]\n.Lsl:\ns_waitcnt lgkmcnt(4)\nM v[136:139],a[0:3],a[160:163],v[232:235]\ns_add_u32 m0,s94,s64\nv_cvt_pk_bf16_f32 v204,v140,v141\nM v[136:139],a[4:7],a[164:167],v[136:139]\nds_read_b128 a[64:67],v250 offset:0\n" \
"ds_read_b128 a[96:99],v250 offset:8192\nM v[136:139],a[8:11],a[168:171],v[136:139]\nbuffer_load_dwordx4 v240,s[44:47],s95 offen offset:0 sc1 lds\nM v[136:139],a[12:15],a[172:175],v[136:139]\nv_cvt_pk_bf16_f32 v205,v142,v143\nv_cvt_pk_bf16_f32 v206,v156,v157\nM v[140:143],a[12:15],a[188:191],v[236:239]\nds_read_b128 a[68:71],v250 offset:1024\n" \
"ds_read_b128 a[100:103],v250 offset:9216\nM v[140:143],a[8:11],a[184:187],v[140:143]\nv_cvt_pk_bf16_f32 v207,v158,v159\ns_add_u32 s94,s94,0x4000\nM v[140:143],a[4:7],a[180:183],v[140:143]\nv_exp_f32 v172,v172\nM v[140:143],a[0:3],a[176:179],v[140:143]\nds_read_b128 a[72:75],v250 offset:2048\n" \
"ds_read_b128 a[104:107],v250 offset:10240\nM v[152:155],a[16:19],a[160:163],v[232:235]\nv_exp_f32 v173,v173\nM v[152:155],a[20:23],a[164:167],v[152:155]\nv_exp_f32 v174,v174\nM v[152:155],a[24:27],a[168:171],v[152:155]\nds_read_b128 a[76:79],v250 offset:3072\nds_read_b128 a[108:111],v250 offset:11264\n" \
"M v[152:155],a[28:31],a[172:175],v[152:155]\nbuffer_load_dwordx4 v240,s[44:47],s95 offen offset:1024 sc1 lds\nM v[156:159],a[28:31],a[188:191],v[236:239]\nv_exp_f32 v175,v175\nM v[156:159],a[24:27],a[184:187],v[156:159]\nds_read_b128 a[80:83],v250 offset:4096\nds_read_b128 a[112:115],v250 offset:12288\nM v[156:159],a[20:23],a[180:183],v[156:159]\n" \
"v_cvt_pk_bf16_f32 v220,v172,v173\nv_cvt_pk_bf16_f32 v221,v174,v175\nM v[156:159],a[16:19],a[176:179],v[156:159]\nv_exp_f32 v188,v188\nM v[168:171],a[32:35],a[160:163],v[232:235]\nds_read_b128 a[84:87],v250 offset:5120\nds_read_b128 a[116:119],v250 offset:13312\nM v[168:171],a[36:39],a[164:167],v[168:171]\n" \
"v_exp_f32 v189,v189\nM v[168:171],a[40:43],a[168:171],v[168:171]\nv_exp_f32 v190,v190\nM v[168:171],a[44:47],a[172:175],v[168:171]\nds_read_b128 a[88:91],v250 offset:6144\nds_read_b128 a[120:123],v250 offset:14336\nM v[172:175],a[44:47],a[188:191],v[236:239]\nbuffer_load_dwordx4 v240,s[44:47],s95 offen offset:2048 sc1 lds\n" \
"M v[172:175],a[40:43],a[184:187],v[172:175]\nv_exp_f32 v191,v191\nM v[172:175],a[36:39],a[180:183],v[172:175]\nds_read_b128 a[92:95],v250 offset:7168\nds_read_b128 a[124:127],v250 offset:15360\nM v[172:175],a[32:35],a[176:179],v[172:175]\nv_cvt_pk_bf16_f32 v222,v188,v189\ns_waitcnt lgkmcnt(7)\n" \
"M v[184:187],a[48:51],a[160:163],v[232:235]\nv_cvt_pk_bf16_f32 v223,v190,v191\nv_xor_b32 v250,0x18000,v250\nM v[184:187],a[52:55],a[164:167],v[184:187]\nv_exp_f32 v136,v136\nM v[184:187],a[56:59],a[168:171],v[184:187]\nv_exp_f32 v137,v137\nM v[184:187],a[60:63],a[172:175],v[184:187]\n" \
"v_exp_f32 v138,v138\nM v[188:191],a[60:63],a[188:191],v[236:239]\nv_exp_f32 v139,v139\nM v[188:191],a[56:59],a[184:187],v[188:191]\nbuffer_load_dwordx4 v240,s[44:47],s95 offen offset:3072 sc1 lds\nM v[188:191],a[52:55],a[180:183],v[188:191]\ns_add_u32 m0,m0,0x10000\ns_and_b32 s94,s94,0xffff\n" \
"M v[188:191],a[48:51],a[176:179],v[188:191]\nv_exp_f32 v152,v152\nM v[64:67],a[64:67],v[200:203],v[64:67]\nv_exp_f32 v153,v153\nM v[64:67],a[96:99],v[216:219],v[64:67]\nv_exp_f32 v154,v154\nM v[72:75],a[68:71],v[200:203],v[72:75]\ns_waitcnt vmcnt(16) lgkmcnt(0)\n" \
"s_barrier\nM v[72:75],a[100:103],v[216:219],v[72:75]\nds_read_b128 a[0:3],v250 offset:0\nds_read_b128 a[4:7],v250 offset:1024\nM v[80:83],a[72:75],v[200:203],v[80:83]\nv_exp_f32 v155,v155\nM v[80:83],a[104:107],v[216:219],v[80:83]\nbuffer_load_dwordx4 v240,s[48:51],s95 offen offset:0 sc1 lds\n" \
"M v[88:91],a[76:79],v[200:203],v[88:91]\nv_exp_f32 v168,v168\nM v[88:91],a[108:111],v[216:219],v[88:91]\nds_read_b128 a[8:11],v250 offset:2048\nds_read_b128 a[12:15],v250 offset:3072\nM v[96:99],a[80:83],v[200:203],v[96:99]\nv_exp_f32 v169,v169\nM v[96:99],a[112:115],v[216:219],v[96:99]\n" \
"v_exp_f32 v170,v170\nM v[104:107],a[84:87],v[200:203],v[104:107]\nv_exp_f32 v171,v171\nM v[104:107],a[116:119],v[216:219],v[104:107]\nds_read_b128 a[16:19],v250 offset:4096\nds_read_b128 a[20:23],v250 offset:5120\nM v[112:115],a[88:91],v[200:203],v[112:115]\nv_exp_f32 v184,v184\n" \
"M v[112:115],a[120:123],v[216:219],v[112:115]\nbuffer_load_dwordx4 v240,s[48:51],s95 offen offset:1024 sc1 lds\nM v[120:123],a[92:95],v[200:203],v[120:123]\nv_exp_f32 v185,v185\nM v[120:123],a[124:127],v[216:219],v[120:123]\nds_read_b128 a[24:27],v250 offset:6144\nds_read_b128 a[28:31],v250 offset:7168\nM a[200:203],a[208:211],v[200:203],a[200:203]\n" \
"v_exp_f32 v186,v186\nM a[200:203],a[208:211],v[216:219],a[200:203]\nv_exp_f32 v187,v187\nM v[68:71],a[64:67],v[204:207],v[68:71]\nv_exp_f32 v140,v140\nM v[68:71],a[96:99],v[220:223],v[68:71]\nds_read_b128 a[32:35],v250 offset:8192\nds_read_b128 a[36:39],v250 offset:9216\n" \
"M v[76:79],a[68:71],v[204:207],v[76:79]\nv_exp_f32 v141,v141\nM v[76:79],a[100:103],v[220:223],v[76:79]\nbuffer_load_dwordx4 v240,s[48:51],s95 offen offset:2048 sc1 lds\nM v[84:87],a[72:75],v[204:207],v[84:87]\nv_exp_f32 v142,v142\nM v[84:87],a[104:107],v[220:223],v[84:87]\nds_read_b128 a[40:43],v250 offset:10240\n" \
"ds_read_b128 a[44:47],v250 offset:11264\nM v[92:95],a[76:79],v[204:207],v[92:95]\nv_exp_f32 v143,v143\nM v[92:95],a[108:111],v[220:223],v[92:95]\nv_exp_f32 v156,v156\nM v[100:103],a[80:83],v[204:207],v[100:103]\nv_exp_f32 v157,v157\nM v[100:103],a[112:115],v[220:223],v[100:103]\n" \
"ds_read_b128 a[48:51],v250 offset:12288\nds_read_b128 a[52:55],v250 offset:13312\nM v[108:111],a[84:87],v[204:207],v[108:111]\nv_exp_f32 v158,v158\nM v[108:111],a[116:119],v[220:223],v[108:111]\nbuffer_load_dwordx4 v240,s[48:51],s95 offen offset:3072 sc1 lds\nM v[116:119],a[88:91],v[204:207],v[116:119]\nv_exp_f32 v159,v159\n" \
"M v[116:119],a[120:123],v[220:223],v[116:119]\nds_read_b128 a[56:59],v250 offset:14336\nds_read_b128 a[60:63],v250 offset:15360\nM v[124:127],a[92:95],v[204:207],v[124:127]\nv_cvt_pk_bf16_f32 v200,v136,v137\nv_cvt_pk_bf16_f32 v201,v138,v139\nM v[124:127],a[124:127],v[220:223],v[124:127]\nv_cvt_pk_bf16_f32 v202,v152,v153\n" \
"v_cvt_pk_bf16_f32 v203,v154,v155\nM a[204:207],a[208:211],v[204:207],a[204:207]\nv_cvt_pk_bf16_f32 v216,v168,v169\nv_cvt_pk_bf16_f32 v217,v170,v171\nv_cvt_pk_bf16_f32 v218,v184,v185\nv_cvt_pk_bf16_f32 v219,v186,v187\nv_add_u32 v250,s94,v241\ns_add_u32 s95,s95,0x4000\n" \
"M a[204:207],a[208:211],v[220:223],a[204:207]\ns_cmp_lg_u32 s95,s96\ns_cbranch_scc1 .Lsl\ns_waitcnt lgkmcnt(4)\nM v[128:131],a[0:3],a[128:131],v[224:227]\ns_add_u32 m0,s94,s64\nv_cvt_pk_bf16_f32 v204,v140,v141\nM v[128:131],a[4:7],a[132:135],v[128:131]\n" \
"ds_read_b128 a[64:67],v250 offset:0\nds_read_b128 a[96:99],v250 offset:8192\nM v[128:131],a[8:11],a[136:139],v[128:131]\nbuffer_load_dwordx4 v240,s[44:47],s95 offen offset:0 sc1 lds\nM v[128:131],a[12:15],a[140:143],v[128:131]\nv_exp_f32 v188,v188\nM v[132:135],a[12:15],a[156:159],v[228:231]\nds_read_b128 a[68:71],v250 offset:1024\n" \
"ds_read_b128 a[100:103],v250 offset:9216\nM v[132:135],a[8:11],a[152:155],v[132:135]\nv_exp_f32 v189,v189\nM v[132:135],a[4:7],a[148:151],v[132:135]\nv_exp_f32 v190,v190\nM v[132:135],a[0:3],a[144:147],v[132:135]\nds_read_b128 a[72:75],v250 offset:2048\nds_read_b128 a[104:107],v250 offset:10240\n" \
"M v[144:147],a[16:19],a[128:131],v[224:227]\nv_exp_f32 v191,v191\nM v[144:147],a[20:23],a[132:135],v[144:147]\nv_exp_f32 v172,v172\nM v[144:147],a[24:27],a[136:139],v[144:147]\nds_read_b128 a[76:79],v250 offset:3072\nds_read_b128 a[108:111],v250 offset:11264\nM v[144:147],a[28:31],a[140:143],v[144:147]\n" \
"buffer_load_dwordx4 v240,s[44:47],s95 offen offset:1024 sc1 lds\nM v[148:151],a[28:31],a[156:159],v[228:231]\nv_exp_f32 v173,v173\nM v[148:151],a[24:27],a[152:155],v[148:151]\nds_read_b128 a[80:83],v250 offset:4096\nds_read_b128 a[112:115],v250 offset:12288\nM v[148:151],a[20:23],a[148:151],v[148:151]\nv_exp_f32 v174,v174\n" \
"M v[148:151],a[16:19],a[144:147],v[148:151]\nv_exp_f32 v175,v175\nM v[160:163],a[32:35],a[128:131],v[224:227]\nds_read_b128 a[84:87],v250 offset:5120\nds_read_b128 a[116:119],v250 offset:13312\nM v[160:163],a[36:39],a[132:135],v[160:163]\nv_cvt_pk_bf16_f32 v205,v142,v143\nv_cvt_pk_bf16_f32 v206,v156,v157\n" \
"M v[160:163],a[40:43],a[136:139],v[160:163]\nv_cvt_pk_bf16_f32 v207,v158,v159\nv_cvt_pk_bf16_f32 v220,v172,v173\nM v[160:163],a[44:47],a[140:143],v[160:163]\nds_read_b128 a[88:91],v250 offset:6144\nds_read_b128 a[120:123],v250 offset:14336\nM v[164:167],a[44:47],a[156:159],v[228:231]\nbuffer_load_dwordx4 v240,s[44:47],s95 offen offset:2048 sc1 lds\n" \
"M v[164:167],a[40:43],a[152:155],v[164:167]\nv_cvt_pk_bf16_f32 v221,v174,v175\nv_cvt_pk_bf16_f32 v222,v188,v189\nM v[164:167],a[36:39],a[148:151],v[164:167]\nds_read_b128 a[92:95],v250 offset:7168\nds_read_b128 a[124:127],v250 offset:15360\nM v[164:167],a[32:35],a[144:147],v[164:167]\nv_xor_b32 v250,0x18000,v250\n" \
"s_waitcnt lgkmcnt(7)\nM v[176:179],a[48:51],a[128:131],v[224:227]\nv_cvt_pk_bf16_f32 v223,v190,v191\ns_add_u32 s94,s94,0x4000\nM v[176:179],a[52:55],a[132:135],v[176:179]\nv_exp_f32 v128,v128\nM v[176:179],a[56:59],a[136:139],v[176:179]\nv_exp_f32 v129,v129\n" \
"M v[176:179],a[60:63],a[140:143],v[176:179]\nv_exp_f32 v130,v130\nM v[180:183],a[60:63],a[156:159],v[228:231]\nv_exp_f32 v131,v131\nM v[180:183],a[56:59],a[152:155],v[180:183]\nbuffer_load_dwordx4 v240,s[44:47],s95 offen offset:3072 sc1 lds\nM v[180:183],a[52:55],a[148:151],v[180:183]\ns_add_u32 m0,m0,0x10000\n" \
"s_and_b32 s94,s94,0xffff\nM v[180:183],a[48:51],a[144:147],v[180:183]\nv_exp_f32 v144,v144\nM v[64:67],a[64:67],v[200:203],v[64:67]\nv_exp_f32 v145,v145\nM v[64:67],a[96:99],v[216:219],v[64:67]\nv_exp_f32 v146,v146\nM v[72:75],a[68:71],v[200:203],v[72:75]\n" \
"s_waitcnt vmcnt(16) lgkmcnt(0)\ns_barrier\nM v[72:75],a[100:103],v[216:219],v[72:75]\nds_read_b128 a[0:3],v250 offset:0\nds_read_b128 a[4:7],v250 offset:1024\nM v[80:83],a[72:75],v[200:203],v[80:83]\nv_exp_f32 v147,v147\nM v[80:83],a[104:107],v[216:219],v[80:83]\n" \
"buffer_load_dwordx4 v240,s[48:51],s95 offen offset:0 sc1 lds\nM v[88:91],a[76:79],v[200:203],v[88:91]\nv_exp_f32 v160,v160\nM v[88:91],a[108:111],v[216:219],v[88:91]\nds_read_b128 a[8:11],v250 offset:2048\nds_read_b128 a[12:15],v250 offset:3072\nM v[96:99],a[80:83],v[200:203],v[96:99]\nv_exp_f32 v161,v161\n" \
"M v[96:99],a[112:115],v[216:219],v[96:99]\nv_exp_f32 v162,v162\nM v[104:107],a[84:87],v[200:203],v[104:107]\nv_exp_f32 v163,v163\nM v[104:107],a[116:119],v[216:219],v[104:107]\nds_read_b128 a[16:19],v250 offset:4096\nds_read_b128 a[20:23],v250 offset:5120\nM v[112:115],a[88:91],v[200:203],v[112:115]\n" \
"v_exp_f32 v176,v176\nM v[112:115],a[120:123],v[216:219],v[112:115]\nbuffer_load_dwordx4 v240,s[48:51],s95 offen offset:1024 sc1 lds\nM v[120:123],a[92:95],v[200:203],v[120:123]\nv_exp_f32 v177,v177\nM v[120:123],a[124:127],v[216:219],v[120:123]\nds_read_b128 a[24:27],v250 offset:6144\nds_read_b128 a[28:31],v250 offset:7168\n" \
"M a[200:203],a[208:211],v[200:203],a[200:203]\nv_exp_f32 v178,v178\nM a[200:203],a[208:211],v[216:219],a[200:203]\nv_exp_f32 v179,v179\nM v[68:71],a[64:67],v[204:207],v[68:71]\nv_exp_f32 v132,v132\nM v[68:71],a[96:99],v[220:223],v[68:71]\nds_read_b128 a[32:35],v250 offset:8192\n" \
"ds_read_b128 a[36:39],v250 offset:9216\nM v[76:79],a[68:71],v[204:207],v[76:79]\nv_exp_f32 v133,v133\nM v[76:79],a[100:103],v[220:223],v[76:79]\nbuffer_load_dwordx4 v240,s[48:51],s95 offen offset:2048 sc1 lds\nM v[84:87],a[72:75],v[204:207],v[84:87]\nv_exp_f32 v134,v134\nM v[84:87],a[104:107],v[220:223],v[84:87]\n" \
"ds_read_b128 a[40:43],v250 offset:10240\nds_read_b128 a[44:47],v250 offset:11264\nM v[92:95],a[76:79],v[204:207],v[92:95]\nv_exp_f32 v135,v135\nM v[92:95],a[108:111],v[220:223],v[92:95]\nv_exp_f32 v148,v148\nM v[100:103],a[80:83],v[204:207],v[100:103]\nv_exp_f32 v149,v149\n" \
"M v[100:103],a[112:115],v[220:223],v[100:103]\nds_read_b128 a[48:51],v250 offset:12288\nds_read_b128 a[52:55],v250 offset:13312\nM v[108:111],a[84:87],v[204:207],v[108:111]\nv_exp_f32 v150,v150\nM v[108:111],a[116:119],v[220:223],v[108:111]\nbuffer_load_dwordx4 v240,s[48:51],s95 offen offset:3072 sc1 lds\nM v[116:119],a[88:91],v[204:207],v[116:119]\n" \
"v_exp_f32 v151,v151\nM v[116:119],a[120:123],v[220:223],v[116:119]\nds_read_b128 a[56:59],v250 offset:14336\nds_read_b128 a[60:63],v250 offset:15360\nM v[124:127],a[92:95],v[204:207],v[124:127]\nv_cvt_pk_bf16_f32 v192,v128,v129\nv_cvt_pk_bf16_f32 v193,v130,v131\nM v[124:127],a[124:127],v[220:223],v[124:127]\n" \
"v_cvt_pk_bf16_f32 v194,v144,v145\nv_cvt_pk_bf16_f32 v195,v146,v147\nM a[204:207],a[208:211],v[204:207],a[204:207]\nv_cvt_pk_bf16_f32 v208,v160,v161\nv_cvt_pk_bf16_f32 v209,v162,v163\nv_cvt_pk_bf16_f32 v210,v176,v177\nv_cvt_pk_bf16_f32 v211,v178,v179\nv_add_u32 v250,s94,v241\n" \
"s_add_u32 s95,s95,0x4000\nM a[204:207],a[208:211],v[220:223],a[204:207]\ns_branch .Lse\n.Ltrans:\ns_waitcnt lgkmcnt(4)\nM v[128:131],a[0:3],a[128:131],v[224:227]\ns_add_u32 m0,s94,s64\nv_cvt_pk_bf16_f32 v212,v164,v165\n" \
"M v[128:131],a[4:7],a[132:135],v[128:131]\nds_read_b128 a[64:67],v250 offset:0\nds_read_b128 a[96:99],v250 offset:8192\nM v[128:131],a[8:11],a[136:139],v[128:131]\nbuffer_load_dwordx4 v240,s[44:47],s95 offen offset:0 sc1 lds\nM v[128:131],a[12:15],a[140:143],v[128:131]\nv_cvt_pk_bf16_f32 v213,v166,v167\nv_cvt_pk_bf16_f32 v214,v180,v181\n" \
"M v[132:135],a[12:15],a[156:159],v[228:231]\nds_read_b128 a[68:71],v250 offset:1024\nds_read_b128 a[100:103],v250 offset:9216\nM v[132:135],a[8:11],a[152:155],v[132:135]\nbuffer_store_dwordx4 a[248:251],v253,s[52:55],s33 offen offset:0 sc1\nM v[132:135],a[4:7],a[148:151],v[132:135]\nv_cvt_pk_bf16_f32 v215,v182,v183\ns_add_u32 s71,s33,16384\n" \
"M v[132:135],a[0:3],a[144:147],v[132:135]\nds_read_b128 a[72:75],v250 offset:2048\nds_read_b128 a[104:107],v250 offset:10240\nM v[144:147],a[16:19],a[128:131],v[224:227]\nv_exp_f32 v184,v184\nM v[144:147],a[20:23],a[132:135],v[144:147]\nv_exp_f32 v185,v185\nM v[144:147],a[24:27],a[136:139],v[144:147]\n" \
"ds_read_b128 a[76:79],v250 offset:3072\nds_read_b128 a[108:111],v250 offset:11264\nM v[144:147],a[28:31],a[140:143],v[144:147]\nbuffer_load_dwordx4 v240,s[44:47],s95 offen offset:1024 sc1 lds\nM v[148:151],a[28:31],a[156:159],v[228:231]\nv_exp_f32 v186,v186\nM v[148:151],a[24:27],a[152:155],v[148:151]\nds_read_b128 a[80:83],v250 offset:4096\n" \
"ds_read_b128 a[112:115],v250 offset:12288\nM v[148:151],a[20:23],a[148:151],v[148:151]\nbuffer_store_dwordx4 a[248:251],v253,s[52:55],s33 offen offset:1024 sc1\nM v[148:151],a[16:19],a[144:147],v[148:151]\nv_exp_f32 v187,v187\nM v[160:163],a[32:35],a[128:131],v[224:227]\nds_read_b128 a[84:87],v250 offset:5120\nds_read_b128 a[116:119],v250 offset:13312\n" \
"M v[160:163],a[36:39],a[132:135],v[160:163]\nv_cvt_pk_bf16_f32 v216,v168,v169\nv_cvt_pk_bf16_f32 v217,v170,v171\nM v[160:163],a[40:43],a[136:139],v[160:163]\nv_cvt_pk_bf16_f32 v218,v184,v185\nv_cvt_pk_bf16_f32 v219,v186,v187\nM v[160:163],a[44:47],a[140:143],v[160:163]\nds_read_b128 a[88:91],v250 offset:6144\n" \
"ds_read_b128 a[120:123],v250 offset:14336\nM v[164:167],a[44:47],a[156:159],v[228:231]\nbuffer_load_dwordx4 v240,s[44:47],s95 offen offset:2048 sc1 lds\nM v[164:167],a[40:43],a[152:155],v[164:167]\ns_add_u32 s70,s78,16384\nv_cvt_pk_bf16_f32 v204,v140,v141\nM v[164:167],a[36:39],a[148:151],v[164:167]\nds_read_b128 a[92:95],v250 offset:7168\n" \
"ds_read_b128 a[124:127],v250 offset:15360\nM v[164:167],a[32:35],a[144:147],v[164:167]\nbuffer_store_dwordx4 a[248:251],v253,s[52:55],s33 offen offset:2048 sc1\ns_waitcnt lgkmcnt(6)\nM v[176:179],a[48:51],a[128:131],v[224:227]\nv_xor_b32 v250,0x18000,v250\nv_cvt_pk_bf16_f32 v205,v142,v143\nM v[176:179],a[52:55],a[132:135],v[176:179]\n" \
"v_exp_f32 v172,v172\nM v[176:179],a[56:59],a[136:139],v[176:179]\nv_exp_f32 v173,v173\nM v[176:179],a[60:63],a[140:143],v[176:179]\nv_exp_f32 v174,v174\nM v[180:183],a[60:63],a[156:159],v[228:231]\nv_exp_f32 v175,v175\nM v[180:183],a[56:59],a[152:155],v[180:183]\n" \
"buffer_load_dwordx4 v240,s[44:47],s95 offen offset:3072 sc1 lds\nM v[180:183],a[52:55],a[148:151],v[180:183]\ns_add_u32 m0,m0,0x10000\nv_cvt_pk_bf16_f32 v206,v156,v157\nM v[180:183],a[48:51],a[144:147],v[180:183]\nv_exp_f32 v188,v188\nM v[0:3],a[64:67],v[192:195],v[0:3]\nbuffer_store_dwordx4 a[248:251],v253,s[52:55],s33 offen offset:3072 sc1\n" \
"M v[0:3],a[96:99],v[208:211],v[0:3]\nv_exp_f32 v189,v189\nM v[8:11],a[68:71],v[192:195],v[8:11]\ns_waitcnt vmcnt(16) lgkmcnt(0)\ns_barrier\nM v[8:11],a[100:103],v[208:211],v[8:11]\nds_read_b128 a[0:3],v250 offset:0\nds_read_b128 a[4:7],v250 offset:1024\n" \
"M v[16:19],a[72:75],v[192:195],v[16:19]\nv_exp_f32 v190,v190\nM v[16:19],a[104:107],v[208:211],v[16:19]\nbuffer_store_dwordx4 a[248:251],v253,s[52:55],s71 offen offset:0 sc1\nM v[24:27],a[76:79],v[192:195],v[24:27]\nv_exp_f32 v191,v191\nM v[24:27],a[108:111],v[208:211],v[24:27]\nbuffer_load_dwordx4 v240,s[48:51],s95 offen offset:0 sc1 lds\n" \
"M v[32:35],a[80:83],v[192:195],v[32:35]\nv_cvt_pk_bf16_f32 v207,v158,v159\nv_cvt_pk_bf16_f32 v220,v172,v173\nM v[32:35],a[112:115],v[208:211],v[32:35]\nbuffer_store_dwordx4 a[248:251],v253,s[52:55],s71 offen offset:1024 sc1\nM v[40:43],a[84:87],v[192:195],v[40:43]\nv_cvt_pk_bf16_f32 v221,v174,v175\nv_cvt_pk_bf16_f32 v222,v188,v189\n" \
"M v[40:43],a[116:119],v[208:211],v[40:43]\nds_read_b128 a[8:11],v250 offset:2048\nds_read_b128 a[12:15],v250 offset:3072\nM v[48:51],a[88:91],v[192:195],v[48:51]\nv_cvt_pk_bf16_f32 v223,v190,v191\ns_add_u32 s94,s94,0x4000\nM v[48:51],a[120:123],v[208:211],v[48:51]\nbuffer_store_dwordx4 a[248:251],v253,s[52:55],s71 offen offset:2048 sc1\n" \
"M v[56:59],a[92:95],v[192:195],v[56:59]\nv_exp_f32 v128,v128\nM v[56:59],a[124:127],v[208:211],v[56:59]\nv_exp_f32 v129,v129\nM a[192:195],a[208:211],v[192:195],a[192:195]\nv_exp_f32 v130,v130\nM a[192:195],a[208:211],v[208:211],a[192:195]\nbuffer_store_dwordx4 a[248:251],v253,s[52:55],s71 offen offset:3072 sc1\n" \
"M v[4:7],a[64:67],v[196:199],v[4:7]\nv_exp_f32 v131,v131\nM v[4:7],a[96:99],v[212:215],v[4:7]\nds_read_b128 a[16:19],v250 offset:4096\nds_read_b128 a[20:23],v250 offset:5120\nM v[12:15],a[68:71],v[196:199],v[12:15]\nv_exp_f32 v144,v144\nM v[12:15],a[100:103],v[212:215],v[12:15]\n" \
"v_exp_f32 v145,v145\nM v[20:23],a[72:75],v[196:199],v[20:23]\nv_exp_f32 v146,v146\nM v[20:23],a[104:107],v[212:215],v[20:23]\nbuffer_load_dwordx4 v240,s[48:51],s95 offen offset:1024 sc1 lds\nM v[28:31],a[76:79],v[196:199],v[28:31]\nv_exp_f32 v147,v147\nM v[28:31],a[108:111],v[212:215],v[28:31]\n" \
"buffer_store_dwordx4 a[248:251],v253,s[52:55],s78 offen offset:0 sc1\nM v[36:39],a[80:83],v[196:199],v[36:39]\nv_exp_f32 v160,v160\nM v[36:39],a[112:115],v[212:215],v[36:39]\nds_read_b128 a[24:27],v250 offset:6144\nds_read_b128 a[28:31],v250 offset:7168\nM v[44:47],a[84:87],v[196:199],v[44:47]\nv_exp_f32 v161,v161\n" \
"M v[44:47],a[116:119],v[212:215],v[44:47]\nbuffer_store_dwordx4 a[248:251],v253,s[52:55],s78 offen offset:1024 sc1\nM v[52:55],a[88:91],v[196:199],v[52:55]\nv_exp_f32 v162,v162\nM v[52:55],a[120:123],v[212:215],v[52:55]\nv_exp_f32 v163,v163\nM v[60:63],a[92:95],v[196:199],v[60:63]\nv_exp_f32 v176,v176\n" \
"M v[60:63],a[124:127],v[212:215],v[60:63]\nbuffer_store_dwordx4 a[248:251],v253,s[52:55],s78 offen offset:2048 sc1\nM a[196:199],a[208:211],v[196:199],a[196:199]\nv_exp_f32 v177,v177\nM a[196:199],a[208:211],v[212:215],a[196:199]\nds_read_b128 a[32:35],v250 offset:8192\nds_read_b128 a[36:39],v250 offset:9216\nM v[64:67],a[64:67],v[200:203],v[64:67]\n" \
"v_exp_f32 v178,v178\nM v[64:67],a[96:99],v[216:219],v[64:67]\nv_exp_f32 v179,v179\nM v[72:75],a[68:71],v[200:203],v[72:75]\ns_and_b32 s94,s94,0xffff\nv_cvt_pk_bf16_f32 v192,v128,v129\nM v[72:75],a[100:103],v[216:219],v[72:75]\nbuffer_load_dwordx4 v240,s[48:51],s95 offen offset:2048 sc1 lds\n" \
"M v[80:83],a[72:75],v[200:203],v[80:83]\nv_exp_f32 v132,v132\nM v[80:83],a[104:107],v[216:219],v[80:83]\nbuffer_store_dwordx4 a[248:251],v253,s[52:55],s78 offen offset:3072 sc1\nM v[88:91],a[76:79],v[200:203],v[88:91]\nv_exp_f32 v133,v133\nM v[88:91],a[108:111],v[216:219],v[88:91]\nds_read_b128 a[40:43],v250 offset:10240\n" \
"ds_read_b128 a[44:47],v250 offset:11264\nM v[96:99],a[80:83],v[200:203],v[96:99]\nv_exp_f32 v134,v134\nM v[96:99],a[112:115],v[216:219],v[96:99]\nbuffer_store_dwordx4 a[248:251],v253,s[52:55],s70 offen offset:0 sc1\nM v[104:107],a[84:87],v[200:203],v[104:107]\nv_exp_f32 v135,v135\nM v[104:107],a[116:119],v[216:219],v[104:107]\n" \
"v_exp_f32 v148,v148\nM v[112:115],a[88:91],v[200:203],v[112:115]\nv_exp_f32 v149,v149\nM v[112:115],a[120:123],v[216:219],v[112:115]\nbuffer_store_dwordx4 a[248:251],v253,s[52:55],s70 offen offset:1024 sc1\nM v[120:123],a[92:95],v[200:203],v[120:123]\nv_exp_f32 v150,v150\nM v[120:123],a[124:127],v[216:219],v[120:123]\n" \
"ds_read_b128 a[48:51],v250 offset:12288\nds_read_b128 a[52:55],v250 offset:13312\nM a[200:203],a[208:211],v[200:203],a[200:203]\nv_exp_f32 v151,v151\nM a[200:203],a[208:211],v[216:219],a[200:203]\nv_cvt_pk_bf16_f32 v193,v130,v131\nv_cvt_pk_bf16_f32 v194,v144,v145\nM v[68:71],a[64:67],v[204:207],v[68:71]\n" \
"v_cvt_pk_bf16_f32 v195,v146,v147\nv_cvt_pk_bf16_f32 v208,v160,v161\nM v[68:71],a[96:99],v[220:223],v[68:71]\nbuffer_load_dwordx4 v240,s[48:51],s95 offen offset:3072 sc1 lds\nM v[76:79],a[68:71],v[204:207],v[76:79]\nv_cvt_pk_bf16_f32 v209,v162,v163\nv_cvt_pk_bf16_f32 v210,v176,v177\nM v[76:79],a[100:103],v[220:223],v[76:79]\n" \
"buffer_store_dwordx4 a[248:251],v253,s[52:55],s70 offen offset:2048 sc1\nM v[84:87],a[72:75],v[204:207],v[84:87]\nv_cvt_pk_bf16_f32 v211,v178,v179\ns_add_u32 s95,s95,0x4000\nM v[84:87],a[104:107],v[220:223],v[84:87]\nds_read_b128 a[56:59],v250 offset:14336\nds_read_b128 a[60:63],v250 offset:15360\nM v[92:95],a[76:79],v[204:207],v[92:95]\n" \
"v_add_u32 v250,s94,v241\nM v[92:95],a[108:111],v[220:223],v[92:95]\nbuffer_store_dwordx4 a[248:251],v253,s[52:55],s70 offen offset:3072 sc1\nM v[100:103],a[80:83],v[204:207],v[100:103]\nM v[100:103],a[112:115],v[220:223],v[100:103]\nM v[108:111],a[84:87],v[204:207],v[108:111]\nM v[108:111],a[116:119],v[220:223],v[108:111]\nM v[116:119],a[88:91],v[204:207],v[116:119]\n" \
"M v[116:119],a[120:123],v[220:223],v[116:119]\nM v[124:127],a[92:95],v[204:207],v[124:127]\nM v[124:127],a[124:127],v[220:223],v[124:127]\nM a[204:207],a[208:211],v[204:207],a[204:207]\nM a[204:207],a[208:211],v[220:223],a[204:207]\n.Lse:\ns_waitcnt lgkmcnt(4)\nM v[128:131],a[0:3],a[128:131],v[224:227]\n" \
"s_add_u32 m0,s94,s64\nv_accvgpr_read_b32 v232,a200\nv_cvt_pk_bf16_f32 v196,v132,v133\nv_cvt_pk_bf16_f32 v197,v134,v135\nM v[128:131],a[4:7],a[132:135],v[128:131]\nds_read_b128 a[64:67],v250 offset:0\nds_read_b128 a[96:99],v250 offset:8192\nv_accvgpr_read_b32 v236,a204\n" \
"v_add_f32 v233,v232,v236\nM v[128:131],a[8:11],a[136:139],v[128:131]\nbuffer_load_dwordx4 v240,s[44:47],s95 offen offset:0 sc1 lds\nv_rcp_f32 v232,v232\nM v[128:131],a[12:15],a[140:143],v[128:131]\nv_rcp_f32 v236,v236\nv_add_f32 v233,v233,v232\nv_add_f32 v233,v233,v236\n" \
"M v[132:135],a[12:15],a[156:159],v[228:231]\nds_read_b128 a[68:71],v250 offset:1024\nds_read_b128 a[100:103],v250 offset:9216\nv_cmp_ngt_f32 vcc,s28,v233\nv_mul_f32 v64,v64,v232\nM v[132:135],a[8:11],a[152:155],v[132:135]\ns_and_b64 vcc,vcc,s[90:91]\ns_cselect_b32 s39,0x8000000,s16\n" \
"s_cselect_b32 s16,s16,0x8000000\ns_cselect_b32 s71,2,1\ns_addc_u32 s99,s99,0\nM v[132:135],a[4:7],a[148:151],v[132:135]\nv_mul_f32 v65,v65,v232\ns_add_u32 s37,s12,s67\nv_mul_f32 v66,v66,v232\nv_mul_f32 v67,v67,v232\n" \
"M v[132:135],a[0:3],a[144:147],v[132:135]\nds_read_b128 a[72:75],v250 offset:2048\nds_read_b128 a[104:107],v250 offset:10240\nv_cvt_pk_bf16_f32 v64,v64,v65\ns_add_u32 s37,s37,1048576\nM v[144:147],a[16:19],a[128:131],v[224:227]\nv_mul_f32 v72,v72,v232\nv_mul_f32 v73,v73,v232\n" \
"v_mul_f32 v74,v74,v232\nv_mul_f32 v75,v75,v232\nv_cvt_pk_bf16_f32 v65,v66,v67\nM v[144:147],a[20:23],a[132:135],v[144:147]\ns_or_b32 s37,s37,s39\nv_cvt_pk_bf16_f32 v66,v72,v73\nv_cvt_pk_bf16_f32 v67,v74,v75\nbuffer_store_dwordx4 v[64:67],v240,s[52:55],s37 offen offset:0 sc1\n" \
"M v[144:147],a[24:27],a[136:139],v[144:147]\nds_read_b128 a[76:79],v250 offset:3072\nds_read_b128 a[108:111],v250 offset:11264\nv_cvt_pk_bf16_f32 v198,v148,v149\nv_cvt_pk_bf16_f32 v199,v150,v151\nv_mul_f32 v68,v68,v236\nM v[144:147],a[28:31],a[140:143],v[144:147]\nbuffer_load_dwordx4 v240,s[44:47],s95 offen offset:1024 sc1 lds\n" \
"v_mul_f32 v69,v69,v236\nv_mul_f32 v70,v70,v236\nM v[148:151],a[28:31],a[156:159],v[228:231]\nv_mul_f32 v71,v71,v236\nv_cvt_pk_bf16_f32 v68,v68,v69\nv_mul_f32 v76,v76,v236\nv_mul_f32 v77,v77,v236\nM v[148:151],a[24:27],a[152:155],v[148:151]\n" \
"ds_read_b128 a[80:83],v250 offset:4096\nds_read_b128 a[112:115],v250 offset:12288\nv_mul_f32 v78,v78,v236\nv_mul_f32 v79,v79,v236\nM v[148:151],a[20:23],a[148:151],v[148:151]\nv_cvt_pk_bf16_f32 v69,v70,v71\nv_cvt_pk_bf16_f32 v70,v76,v77\nv_cvt_pk_bf16_f32 v71,v78,v79\n" \
"v_mul_f32 v80,v80,v232\nM v[148:151],a[16:19],a[144:147],v[148:151]\nv_exp_f32 v164,v164\nv_exp_f32 v165,v165\nM v[160:163],a[32:35],a[128:131],v[224:227]\nds_read_b128 a[84:87],v250 offset:5120\nds_read_b128 a[116:119],v250 offset:13312\nv_exp_f32 v166,v166\n" \
"v_cvt_pk_bf16_f32 v212,v164,v165\nM v[160:163],a[36:39],a[132:135],v[160:163]\nbuffer_store_dwordx4 v[68:71],v240,s[52:55],s37 offen offset:1024 sc1\nv_exp_f32 v167,v167\nM v[160:163],a[40:43],a[136:139],v[160:163]\nv_cvt_pk_bf16_f32 v213,v166,v167\nv_mul_f32 v81,v81,v232\nv_mul_f32 v82,v82,v232\n" \
"v_mul_f32 v83,v83,v232\nM v[160:163],a[44:47],a[140:143],v[160:163]\nds_read_b128 a[88:91],v250 offset:6144\nds_read_b128 a[120:123],v250 offset:14336\nv_cvt_pk_bf16_f32 v80,v80,v81\nv_mul_f32 v88,v88,v232\nM v[164:167],a[44:47],a[156:159],v[228:231]\nbuffer_load_dwordx4 v240,s[44:47],s95 offen offset:2048 sc1 lds\n" \
"v_mul_f32 v89,v89,v232\nv_mul_f32 v90,v90,v232\nM v[164:167],a[40:43],a[152:155],v[164:167]\nv_mul_f32 v91,v91,v232\nv_cvt_pk_bf16_f32 v81,v82,v83\nv_cvt_pk_bf16_f32 v82,v88,v89\nv_cvt_pk_bf16_f32 v83,v90,v91\nv_mul_f32 v84,v84,v236\n" \
"M v[164:167],a[36:39],a[148:151],v[164:167]\nds_read_b128 a[92:95],v250 offset:7168\nds_read_b128 a[124:127],v250 offset:15360\nv_exp_f32 v180,v180\nM v[164:167],a[32:35],a[144:147],v[164:167]\nv_exp_f32 v181,v181\nv_mul_f32 v85,v85,v236\ns_waitcnt lgkmcnt(4)\n" \
"M v[176:179],a[48:51],a[128:131],v[224:227]\nv_exp_f32 v182,v182\nv_exp_f32 v183,v183\nM v[176:179],a[52:55],a[132:135],v[176:179]\nbuffer_store_dwordx4 v[80:83],v240,s[52:55],s37 offen offset:2048 sc1\nv_cvt_pk_bf16_f32 v214,v180,v181\nv_cvt_pk_bf16_f32 v215,v182,v183\nM v[176:179],a[56:59],a[136:139],v[176:179]\n" \
"v_mul_f32 v86,v86,v236\nv_mul_f32 v87,v87,v236\nv_cvt_pk_bf16_f32 v84,v84,v85\nv_mul_f32 v92,v92,v236\nM v[176:179],a[60:63],a[140:143],v[176:179]\nv_mul_f32 v93,v93,v236\nv_mul_f32 v94,v94,v236\nv_mul_f32 v95,v95,v236\n" \
"v_cvt_pk_bf16_f32 v85,v86,v87\nv_cvt_pk_bf16_f32 v86,v92,v93\nM v[180:183],a[60:63],a[156:159],v[228:231]\nv_cvt_pk_bf16_f32 v87,v94,v95\nv_xor_b32 v250,0x18000,v250\nv_mul_f32 v96,v96,v232\nv_mul_f32 v97,v97,v232\nM v[180:183],a[56:59],a[152:155],v[180:183]\n" \
"buffer_load_dwordx4 v240,s[44:47],s95 offen offset:3072 sc1 lds\ns_add_u32 m0,m0,0x10000\nv_mul_f32 v98,v98,v232\nM v[180:183],a[52:55],a[148:151],v[180:183]\nv_mul_f32 v99,v99,v232\nv_cvt_pk_bf16_f32 v96,v96,v97\nv_mul_f32 v104,v104,v232\nv_mul_f32 v105,v105,v232\n" \
"M v[180:183],a[48:51],a[144:147],v[180:183]\nv_mul_f32 v106,v106,v232\nv_mul_f32 v107,v107,v232\nv_cvt_pk_bf16_f32 v97,v98,v99\ns_add_u32 s38,s37,4096\nM v[0:3],a[64:67],v[192:195],v[0:3]\nv_cvt_pk_bf16_f32 v98,v104,v105\nv_cvt_pk_bf16_f32 v99,v106,v107\n" \
"v_mul_f32 v100,v100,v236\nv_mul_f32 v101,v101,v236\nv_mul_f32 v102,v102,v236\nM v[0:3],a[96:99],v[208:211],v[0:3]\nbuffer_store_dwordx4 v[84:87],v240,s[52:55],s37 offen offset:3072 sc1\nv_mul_f32 v103,v103,v236\nv_cvt_pk_bf16_f32 v100,v100,v101\nM v[8:11],a[68:71],v[192:195],v[8:11]\n" \
"s_waitcnt vmcnt(16) lgkmcnt(0)\ns_barrier\nv_mul_f32 v108,v108,v236\nv_mul_f32 v109,v109,v236\nM v[8:11],a[100:103],v[208:211],v[8:11]\nds_read_b128 a[0:3],v250 offset:0\nds_read_b128 a[4:7],v250 offset:1024\nv_mul_f32 v110,v110,v236\n" \
"v_mul_f32 v111,v111,v236\nM v[16:19],a[72:75],v[192:195],v[16:19]\nv_cvt_pk_bf16_f32 v101,v102,v103\nv_cvt_pk_bf16_f32 v102,v108,v109\nv_cvt_pk_bf16_f32 v103,v110,v111\nv_mul_f32 v112,v112,v232\nM v[16:19],a[104:107],v[208:211],v[16:19]\nbuffer_load_dwordx4 v240,s[48:51],s95 offen offset:0 sc1 lds\n" \
"v_mul_f32 v113,v113,v232\nv_mul_f32 v114,v114,v232\nv_mul_f32 v115,v115,v232\nM v[24:27],a[76:79],v[192:195],v[24:27]\nv_cvt_pk_bf16_f32 v112,v112,v113\nv_mul_f32 v120,v120,v232\nv_mul_f32 v121,v121,v232\nv_mul_f32 v122,v122,v232\n" \
"M v[24:27],a[108:111],v[208:211],v[24:27]\nds_read_b128 a[8:11],v250 offset:2048\nds_read_b128 a[12:15],v250 offset:3072\nv_mul_f32 v123,v123,v232\nv_cvt_pk_bf16_f32 v113,v114,v115\nM v[32:35],a[80:83],v[192:195],v[32:35]\nv_cvt_pk_bf16_f32 v114,v120,v121\nv_cvt_pk_bf16_f32 v115,v122,v123\n" \
"v_mul_f32 v116,v116,v236\nv_mul_f32 v117,v117,v236\nM v[32:35],a[112:115],v[208:211],v[32:35]\nbuffer_store_dwordx4 v[96:99],v240,s[52:55],s38 offen offset:0 sc1\nv_mul_f32 v118,v118,v236\nv_mul_f32 v119,v119,v236\nM v[40:43],a[84:87],v[192:195],v[40:43]\nv_cvt_pk_bf16_f32 v116,v116,v117\n" \
"v_mul_f32 v124,v124,v236\nv_mul_f32 v125,v125,v236\nv_mul_f32 v126,v126,v236\nM v[40:43],a[116:119],v[208:211],v[40:43]\nds_read_b128 a[16:19],v250 offset:4096\nds_read_b128 a[20:23],v250 offset:5120\nv_mul_f32 v127,v127,v236\nv_cvt_pk_bf16_f32 v117,v118,v119\n" \
"v_cvt_pk_bf16_f32 v118,v124,v125\nM v[48:51],a[88:91],v[192:195],v[48:51]\nv_cvt_pk_bf16_f32 v119,v126,v127\nv_exp_f32 v128,v128\ns_add_u32 s94,s94,0x4000\nM v[48:51],a[120:123],v[208:211],v[48:51]\nbuffer_load_dwordx4 v240,s[48:51],s95 offen offset:1024 sc1 lds\nv_exp_f32 v129,v129\n" \
"M v[56:59],a[92:95],v[192:195],v[56:59]\nv_exp_f32 v130,v130\nv_exp_f32 v131,v131\nM v[56:59],a[124:127],v[208:211],v[56:59]\nds_read_b128 a[24:27],v250 offset:6144\nds_read_b128 a[28:31],v250 offset:7168\nv_exp_f32 v144,v144\nM a[192:195],a[208:211],v[192:195],a[192:195]\n" \
"v_exp_f32 v145,v145\nv_exp_f32 v146,v146\ns_lshl2_add_u32 s77,s61,s79\nM a[192:195],a[208:211],v[208:211],a[192:195]\nbuffer_store_dwordx4 v[100:103],v240,s[52:55],s38 offen offset:1024 sc1\nv_exp_f32 v147,v147\nM v[4:7],a[64:67],v[196:199],v[4:7]\nv_exp_f32 v160,v160\n" \
"v_exp_f32 v161,v161\nM v[4:7],a[96:99],v[212:215],v[4:7]\nds_read_b128 a[32:35],v250 offset:8192\nds_read_b128 a[36:39],v250 offset:9216\nv_exp_f32 v162,v162\nM v[12:15],a[68:71],v[196:199],v[12:15]\nv_exp_f32 v163,v163\nv_exp_f32 v176,v176\n" \
"M v[12:15],a[100:103],v[212:215],v[12:15]\nbuffer_load_dwordx4 v240,s[48:51],s95 offen offset:2048 sc1 lds\nv_exp_f32 v177,v177\nM v[20:23],a[72:75],v[196:199],v[20:23]\nv_exp_f32 v178,v178\nv_exp_f32 v179,v179\ns_and_b32 s94,s94,0xffff\nM v[20:23],a[104:107],v[212:215],v[20:23]\n" \
"ds_read_b128 a[40:43],v250 offset:10240\nds_read_b128 a[44:47],v250 offset:11264\ns_add_u32 s77,s77,4096\nv_mov_b32 v233,s71\nM v[28:31],a[76:79],v[196:199],v[28:31]\nv_exp_f32 v132,v132\nv_exp_f32 v133,v133\nM v[28:31],a[108:111],v[212:215],v[28:31]\n" \
"buffer_store_dwordx4 v[112:115],v240,s[52:55],s38 offen offset:2048 sc1\nv_exp_f32 v134,v134\nM v[36:39],a[80:83],v[196:199],v[36:39]\nv_exp_f32 v135,v135\nv_exp_f32 v148,v148\nM v[36:39],a[112:115],v[212:215],v[36:39]\nds_read_b128 a[48:51],v250 offset:12288\nds_read_b128 a[52:55],v250 offset:13312\n" \
"v_exp_f32 v149,v149\nv_cvt_pk_bf16_f32 v192,v128,v129\nM v[44:47],a[84:87],v[196:199],v[44:47]\nv_exp_f32 v150,v150\nv_exp_f32 v151,v151\nM v[44:47],a[116:119],v[212:215],v[44:47]\nbuffer_load_dwordx4 v240,s[48:51],s95 offen offset:3072 sc1 lds\nv_cvt_pk_bf16_f32 v193,v130,v131\n" \
"v_cvt_pk_bf16_f32 v194,v144,v145\nM v[52:55],a[88:91],v[196:199],v[52:55]\nv_cvt_pk_bf16_f32 v195,v146,v147\nv_cvt_pk_bf16_f32 v208,v160,v161\nv_cvt_pk_bf16_f32 v209,v162,v163\nv_cvt_pk_bf16_f32 v210,v176,v177\nM v[52:55],a[120:123],v[212:215],v[52:55]\nds_read_b128 a[56:59],v250 offset:14336\n" \
"ds_read_b128 a[60:63],v250 offset:15360\nv_cvt_pk_bf16_f32 v211,v178,v179\nv_add_u32 v250,s94,v241\nM v[60:63],a[92:95],v[196:199],v[60:63]\ns_add_u32 s95,s95,0x4000\nM v[60:63],a[124:127],v[212:215],v[60:63]\nbuffer_store_dwordx4 v[116:119],v240,s[52:55],s38 offen offset:3072 sc1\nM a[196:199],a[208:211],v[196:199],a[196:199]\n" \
"M a[196:199],a[208:211],v[212:215],a[196:199]\ns_cmp_ge_u32 s95,s15\ns_cbranch_scc1 .Lsx\n.Lsingle:\ns_waitcnt lgkmcnt(4)\nM v[128:131],a[0:3],a[128:131],v[224:227]\ns_xor_b32 m0,m0,0x14000\nv_cvt_pk_bf16_f32 v196,v132,v133\n" \
"M v[128:131],a[4:7],a[132:135],v[128:131]\nds_read_b128 a[64:67],v250 offset:0\nds_read_b128 a[96:99],v250 offset:8192\nM v[128:131],a[8:11],a[136:139],v[128:131]\nbuffer_load_dwordx4 v240,s[44:47],s95 offen offset:0 sc1 lds\nM v[128:131],a[12:15],a[140:143],v[128:131]\nv_cvt_pk_bf16_f32 v197,v134,v135\nv_cvt_pk_bf16_f32 v198,v148,v149\n" \
"M v[132:135],a[12:15],a[156:159],v[228:231]\nds_read_b128 a[68:71],v250 offset:1024\nds_read_b128 a[100:103],v250 offset:9216\nM v[132:135],a[8:11],a[152:155],v[132:135]\nv_exp_f32 v164,v164\nM v[132:135],a[4:7],a[148:151],v[132:135]\nv_exp_f32 v165,v165\nM v[132:135],a[0:3],a[144:147],v[132:135]\n" \
"ds_read_b128 a[72:75],v250 offset:2048\nds_read_b128 a[104:107],v250 offset:10240\nM v[144:147],a[16:19],a[128:131],v[224:227]\nv_cvt_pk_bf16_f32 v199,v150,v151\nv_cvt_pk_bf16_f32 v212,v164,v165\nM v[144:147],a[20:23],a[132:135],v[144:147]\nv_exp_f32 v166,v166\nM v[144:147],a[24:27],a[136:139],v[144:147]\n" \
"ds_read_b128 a[76:79],v250 offset:3072\nds_read_b128 a[108:111],v250 offset:11264\nM v[144:147],a[28:31],a[140:143],v[144:147]\nbuffer_load_dwordx4 v240,s[44:47],s95 offen offset:1024 sc1 lds\nM v[148:151],a[28:31],a[156:159],v[228:231]\nv_exp_f32 v167,v167\nM v[148:151],a[24:27],a[152:155],v[148:151]\nds_read_b128 a[80:83],v250 offset:4096\n" \
"ds_read_b128 a[112:115],v250 offset:12288\nM v[148:151],a[20:23],a[148:151],v[148:151]\nv_exp_f32 v180,v180\nM v[148:151],a[16:19],a[144:147],v[148:151]\nv_exp_f32 v181,v181\nM v[160:163],a[32:35],a[128:131],v[224:227]\nds_read_b128 a[84:87],v250 offset:5120\nds_read_b128 a[116:119],v250 offset:13312\n" \
"M v[160:163],a[36:39],a[132:135],v[160:163]\nv_cvt_pk_bf16_f32 v213,v166,v167\nv_cvt_pk_bf16_f32 v214,v180,v181\nM v[160:163],a[40:43],a[136:139],v[160:163]\nv_exp_f32 v182,v182\nM v[160:163],a[44:47],a[140:143],v[160:163]\nds_read_b128 a[88:91],v250 offset:6144\nds_read_b128 a[120:123],v250 offset:14336\n" \
"M v[164:167],a[44:47],a[156:159],v[228:231]\nbuffer_load_dwordx4 v240,s[44:47],s95 offen offset:2048 sc1 lds\nM v[164:167],a[40:43],a[152:155],v[164:167]\nv_exp_f32 v183,v183\nM v[164:167],a[36:39],a[148:151],v[164:167]\nds_read_b128 a[92:95],v250 offset:7168\nds_read_b128 a[124:127],v250 offset:15360\nM v[164:167],a[32:35],a[144:147],v[164:167]\n" \
"v_cvt_pk_bf16_f32 v215,v182,v183\ns_waitcnt lgkmcnt(7)\nM v[176:179],a[48:51],a[128:131],v[224:227]\nv_exp_f32 v128,v128\nM v[176:179],a[52:55],a[132:135],v[176:179]\nv_exp_f32 v129,v129\nM v[176:179],a[56:59],a[136:139],v[176:179]\nv_exp_f32 v130,v130\n" \
"M v[176:179],a[60:63],a[140:143],v[176:179]\nv_exp_f32 v131,v131\nM v[180:183],a[60:63],a[156:159],v[228:231]\nv_exp_f32 v132,v132\nM v[180:183],a[56:59],a[152:155],v[180:183]\nbuffer_load_dwordx4 v240,s[44:47],s95 offen offset:3072 sc1 lds\nM v[180:183],a[52:55],a[148:151],v[180:183]\nv_xor_b32 v250,0x18000,v250\n" \
"s_xor_b32 m0,m0,0x10000\nM v[180:183],a[48:51],a[144:147],v[180:183]\nv_exp_f32 v133,v133\nM v[0:3],a[64:67],v[192:195],v[0:3]\nv_exp_f32 v134,v134\nM v[0:3],a[96:99],v[208:211],v[0:3]\nv_exp_f32 v135,v135\nM v[8:11],a[68:71],v[192:195],v[8:11]\n" \
"s_waitcnt vmcnt(16) lgkmcnt(0)\ns_barrier\nM v[8:11],a[100:103],v[208:211],v[8:11]\nds_read_b128 a[0:3],v250 offset:0\nds_read_b128 a[4:7],v250 offset:1024\nM v[16:19],a[72:75],v[192:195],v[16:19]\nv_exp_f32 v144,v144\nM v[16:19],a[104:107],v[208:211],v[16:19]\n" \
"buffer_load_dwordx4 v240,s[48:51],s95 offen offset:0 sc1 lds\nM v[24:27],a[76:79],v[192:195],v[24:27]\nv_exp_f32 v145,v145\nM v[24:27],a[108:111],v[208:211],v[24:27]\nds_read_b128 a[8:11],v250 offset:2048\nds_read_b128 a[12:15],v250 offset:3072\nM v[32:35],a[80:83],v[192:195],v[32:35]\nv_exp_f32 v146,v146\n" \
"M v[32:35],a[112:115],v[208:211],v[32:35]\nv_exp_f32 v147,v147\nM v[40:43],a[84:87],v[192:195],v[40:43]\nv_exp_f32 v148,v148\nM v[40:43],a[116:119],v[208:211],v[40:43]\nds_read_b128 a[16:19],v250 offset:4096\nds_read_b128 a[20:23],v250 offset:5120\nM v[48:51],a[88:91],v[192:195],v[48:51]\n" \
"v_exp_f32 v149,v149\nM v[48:51],a[120:123],v[208:211],v[48:51]\nbuffer_load_dwordx4 v240,s[48:51],s95 offen offset:1024 sc1 lds\nM v[56:59],a[92:95],v[192:195],v[56:59]\nv_exp_f32 v150,v150\nM v[56:59],a[124:127],v[208:211],v[56:59]\nds_read_b128 a[24:27],v250 offset:6144\nds_read_b128 a[28:31],v250 offset:7168\n" \
"M a[192:195],a[208:211],v[192:195],a[192:195]\nv_cvt_pk_bf16_f32 v192,v128,v129\nv_cvt_pk_bf16_f32 v193,v130,v131\nM a[192:195],a[208:211],v[208:211],a[192:195]\nv_cvt_pk_bf16_f32 v194,v144,v145\nv_cvt_pk_bf16_f32 v195,v146,v147\nM v[4:7],a[64:67],v[196:199],v[4:7]\nv_exp_f32 v151,v151\n" \
"M v[4:7],a[96:99],v[212:215],v[4:7]\nds_read_b128 a[32:35],v250 offset:8192\nds_read_b128 a[36:39],v250 offset:9216\nM v[12:15],a[68:71],v[196:199],v[12:15]\nv_exp_f32 v160,v160\nM v[12:15],a[100:103],v[212:215],v[12:15]\nbuffer_load_dwordx4 v240,s[48:51],s95 offen offset:2048 sc1 lds\nM v[20:23],a[72:75],v[196:199],v[20:23]\n" \
"v_exp_f32 v161,v161\nM v[20:23],a[104:107],v[212:215],v[20:23]\nds_read_b128 a[40:43],v250 offset:10240\nds_read_b128 a[44:47],v250 offset:11264\nM v[28:31],a[76:79],v[196:199],v[28:31]\nv_exp_f32 v162,v162\nM v[28:31],a[108:111],v[212:215],v[28:31]\nv_exp_f32 v163,v163\n" \
"M v[36:39],a[80:83],v[196:199],v[36:39]\nv_cvt_pk_bf16_f32 v208,v160,v161\nv_cvt_pk_bf16_f32 v209,v162,v163\nM v[36:39],a[112:115],v[212:215],v[36:39]\nds_read_b128 a[48:51],v250 offset:12288\nds_read_b128 a[52:55],v250 offset:13312\nM v[44:47],a[84:87],v[196:199],v[44:47]\nv_exp_f32 v164,v164\n" \
"M v[44:47],a[116:119],v[212:215],v[44:47]\nbuffer_load_dwordx4 v240,s[48:51],s95 offen offset:3072 sc1 lds\nM v[52:55],a[88:91],v[196:199],v[52:55]\ns_add_u32 s95,s95,0x4000\ns_xor_b32 m0,m0,0x1c000\nM v[52:55],a[120:123],v[212:215],v[52:55]\nds_read_b128 a[56:59],v250 offset:14336\nds_read_b128 a[60:63],v250 offset:15360\n" \
"M v[60:63],a[92:95],v[196:199],v[60:63]\nv_exp_f32 v165,v165\nM v[60:63],a[124:127],v[212:215],v[60:63]\nv_exp_f32 v166,v166\nM a[196:199],a[208:211],v[196:199],a[196:199]\nv_xor_b32 v250,0x14000,v250\nv_cvt_pk_bf16_f32 v196,v132,v133\nM a[196:199],a[208:211],v[212:215],a[196:199]\n" \
"v_cvt_pk_bf16_f32 v197,v134,v135\ns_waitcnt lgkmcnt(4)\nM v[128:131],a[0:3],a[128:131],v[224:227]\nv_cvt_pk_bf16_f32 v198,v148,v149\nv_cvt_pk_bf16_f32 v199,v150,v151\nM v[128:131],a[4:7],a[132:135],v[128:131]\nds_read_b128 a[64:67],v250 offset:0\nds_read_b128 a[96:99],v250 offset:8192\n" \
"M v[128:131],a[8:11],a[136:139],v[128:131]\nbuffer_load_dwordx4 v240,s[44:47],s95 offen offset:0 sc1 lds\nM v[128:131],a[12:15],a[140:143],v[128:131]\nv_exp_f32 v167,v167\nM v[132:135],a[12:15],a[156:159],v[228:231]\nds_read_b128 a[68:71],v250 offset:1024\nds_read_b128 a[100:103],v250 offset:9216\nM v[132:135],a[8:11],a[152:155],v[132:135]\n" \
"v_cvt_pk_bf16_f32 v212,v164,v165\nv_cvt_pk_bf16_f32 v213,v166,v167\nM v[132:135],a[4:7],a[148:151],v[132:135]\nv_exp_f32 v176,v176\nM v[132:135],a[0:3],a[144:147],v[132:135]\nds_read_b128 a[72:75],v250 offset:2048\nds_read_b128 a[104:107],v250 offset:10240\nM v[144:147],a[16:19],a[128:131],v[224:227]\n" \
"v_exp_f32 v177,v177\nM v[144:147],a[20:23],a[132:135],v[144:147]\nv_exp_f32 v178,v178\nM v[144:147],a[24:27],a[136:139],v[144:147]\nds_read_b128 a[76:79],v250 offset:3072\nds_read_b128 a[108:111],v250 offset:11264\nM v[144:147],a[28:31],a[140:143],v[144:147]\nbuffer_load_dwordx4 v240,s[44:47],s95 offen offset:1024 sc1 lds\n" \
"M v[148:151],a[28:31],a[156:159],v[228:231]\nv_exp_f32 v179,v179\nM v[148:151],a[24:27],a[152:155],v[148:151]\nds_read_b128 a[80:83],v250 offset:4096\nds_read_b128 a[112:115],v250 offset:12288\nM v[148:151],a[20:23],a[148:151],v[148:151]\nv_cvt_pk_bf16_f32 v210,v176,v177\nv_cvt_pk_bf16_f32 v211,v178,v179\n" \
"M v[148:151],a[16:19],a[144:147],v[148:151]\nv_exp_f32 v180,v180\nM v[160:163],a[32:35],a[128:131],v[224:227]\nds_read_b128 a[84:87],v250 offset:5120\nds_read_b128 a[116:119],v250 offset:13312\nM v[160:163],a[36:39],a[132:135],v[160:163]\nv_exp_f32 v181,v181\nM v[160:163],a[40:43],a[136:139],v[160:163]\n" \
"v_exp_f32 v182,v182\nM v[160:163],a[44:47],a[140:143],v[160:163]\nds_read_b128 a[88:91],v250 offset:6144\nds_read_b128 a[120:123],v250 offset:14336\nM v[164:167],a[44:47],a[156:159],v[228:231]\nbuffer_load_dwordx4 v240,s[44:47],s95 offen offset:2048 sc1 lds\nM v[164:167],a[40:43],a[152:155],v[164:167]\nv_exp_f32 v183,v183\n" \
"M v[164:167],a[36:39],a[148:151],v[164:167]\nds_read_b128 a[92:95],v250 offset:7168\nds_read_b128 a[124:127],v250 offset:15360\nM v[164:167],a[32:35],a[144:147],v[164:167]\nv_cvt_pk_bf16_f32 v214,v180,v181\ns_waitcnt lgkmcnt(7)\nM v[176:179],a[48:51],a[128:131],v[224:227]\nv_cvt_pk_bf16_f32 v215,v182,v183\n" \
"v_xor_b32 v250,0x18000,v250\nM v[176:179],a[52:55],a[132:135],v[176:179]\nv_exp_f32 v128,v128\nM v[176:179],a[56:59],a[136:139],v[176:179]\nv_exp_f32 v129,v129\nM v[176:179],a[60:63],a[140:143],v[176:179]\nv_exp_f32 v130,v130\nM v[180:183],a[60:63],a[156:159],v[228:231]\n" \
"v_exp_f32 v131,v131\nM v[180:183],a[56:59],a[152:155],v[180:183]\nbuffer_load_dwordx4 v240,s[44:47],s95 offen offset:3072 sc1 lds\nM v[180:183],a[52:55],a[148:151],v[180:183]\nv_exp_f32 v144,v144\nM v[180:183],a[48:51],a[144:147],v[180:183]\nv_exp_f32 v145,v145\nM v[0:3],a[64:67],v[192:195],v[0:3]\n" \
"v_exp_f32 v146,v146\nM v[0:3],a[96:99],v[208:211],v[0:3]\nv_exp_f32 v147,v147\nM v[8:11],a[68:71],v[192:195],v[8:11]\ns_waitcnt vmcnt(16) lgkmcnt(0)\ns_barrier\nM v[8:11],a[100:103],v[208:211],v[8:11]\nds_read_b128 a[0:3],v250 offset:0\n" \
"ds_read_b128 a[4:7],v250 offset:1024\nM v[16:19],a[72:75],v[192:195],v[16:19]\ns_xor_b32 m0,m0,0x10000\nM v[16:19],a[104:107],v[208:211],v[16:19]\nbuffer_load_dwordx4 v240,s[48:51],s95 offen offset:0 sc1 lds\nM v[24:27],a[76:79],v[192:195],v[24:27]\nv_exp_f32 v160,v160\nM v[24:27],a[108:111],v[208:211],v[24:27]\n" \
"ds_read_b128 a[8:11],v250 offset:2048\nds_read_b128 a[12:15],v250 offset:3072\nM v[32:35],a[80:83],v[192:195],v[32:35]\nv_exp_f32 v161,v161\nM v[32:35],a[112:115],v[208:211],v[32:35]\nv_exp_f32 v162,v162\nM v[40:43],a[84:87],v[192:195],v[40:43]\nv_exp_f32 v163,v163\n" \
"M v[40:43],a[116:119],v[208:211],v[40:43]\nds_read_b128 a[16:19],v250 offset:4096\nds_read_b128 a[20:23],v250 offset:5120\nM v[48:51],a[88:91],v[192:195],v[48:51]\nv_exp_f32 v176,v176\nM v[48:51],a[120:123],v[208:211],v[48:51]\nbuffer_load_dwordx4 v240,s[48:51],s95 offen offset:1024 sc1 lds\nM v[56:59],a[92:95],v[192:195],v[56:59]\n" \
"v_exp_f32 v177,v177\nM v[56:59],a[124:127],v[208:211],v[56:59]\nds_read_b128 a[24:27],v250 offset:6144\nds_read_b128 a[28:31],v250 offset:7168\nM a[192:195],a[208:211],v[192:195],a[192:195]\nv_exp_f32 v178,v178\nM a[192:195],a[208:211],v[208:211],a[192:195]\nv_exp_f32 v179,v179\n" \
"M v[4:7],a[64:67],v[196:199],v[4:7]\nv_exp_f32 v132,v132\nM v[4:7],a[96:99],v[212:215],v[4:7]\nds_read_b128 a[32:35],v250 offset:8192\nds_read_b128 a[36:39],v250 offset:9216\nM v[12:15],a[68:71],v[196:199],v[12:15]\nv_exp_f32 v133,v133\nM v[12:15],a[100:103],v[212:215],v[12:15]\n" \
"buffer_load_dwordx4 v240,s[48:51],s95 offen offset:2048 sc1 lds\nM v[20:23],a[72:75],v[196:199],v[20:23]\nv_exp_f32 v134,v134\nM v[20:23],a[104:107],v[212:215],v[20:23]\nds_read_b128 a[40:43],v250 offset:10240\nds_read_b128 a[44:47],v250 offset:11264\nM v[28:31],a[76:79],v[196:199],v[28:31]\nv_exp_f32 v135,v135\n" \
"M v[28:31],a[108:111],v[212:215],v[28:31]\nv_exp_f32 v148,v148\nM v[36:39],a[80:83],v[196:199],v[36:39]\nv_exp_f32 v149,v149\nM v[36:39],a[112:115],v[212:215],v[36:39]\nds_read_b128 a[48:51],v250 offset:12288\nds_read_b128 a[52:55],v250 offset:13312\nM v[44:47],a[84:87],v[196:199],v[44:47]\n" \
"v_exp_f32 v150,v150\nM v[44:47],a[116:119],v[212:215],v[44:47]\nbuffer_load_dwordx4 v240,s[48:51],s95 offen offset:3072 sc1 lds\nM v[52:55],a[88:91],v[196:199],v[52:55]\nv_exp_f32 v151,v151\nM v[52:55],a[120:123],v[212:215],v[52:55]\nds_read_b128 a[56:59],v250 offset:14336\nds_read_b128 a[60:63],v250 offset:15360\n" \
"M v[60:63],a[92:95],v[196:199],v[60:63]\nv_cvt_pk_bf16_f32 v192,v128,v129\nv_cvt_pk_bf16_f32 v193,v130,v131\nM v[60:63],a[124:127],v[212:215],v[60:63]\nv_cvt_pk_bf16_f32 v194,v144,v145\nv_cvt_pk_bf16_f32 v195,v146,v147\nM a[196:199],a[208:211],v[196:199],a[196:199]\nv_cvt_pk_bf16_f32 v208,v160,v161\n" \
"v_cvt_pk_bf16_f32 v209,v162,v163\nv_cvt_pk_bf16_f32 v210,v176,v177\nv_cvt_pk_bf16_f32 v211,v178,v179\nv_xor_b32 v250,0x1c000,v250\ns_add_u32 s95,s95,0x4000\nM a[196:199],a[208:211],v[212:215],a[196:199]\ns_cmp_lg_u32 s95,s15\ns_cbranch_scc1 .Lsingle\n" \
"s_and_b32 s94,m0,0x8000\ns_or_b32 s94,s94,0x4000\n.Lsx:\n.Lsw:\ns_waitcnt lgkmcnt(4)\nM v[128:131],a[0:3],a[128:131],v[224:227]\ns_lshl4_add_u32 s69,s12,s68\ns_add_u32 m0,s64,131072\n" \
"M v[128:131],a[4:7],a[132:135],v[128:131]\nds_read_b128 a[64:67],v250 offset:0\nds_read_b128 a[96:99],v250 offset:8192\nv_cvt_pk_bf16_f32 v196,v132,v133\nM v[128:131],a[8:11],a[136:139],v[128:131]\nbuffer_load_dwordx4 v243,s[40:43],s69 offen offset:0 lds\nM v[128:131],a[12:15],a[140:143],v[128:131]\nv_cvt_pk_bf16_f32 v197,v134,v135\n" \
"v_cvt_pk_bf16_f32 v198,v148,v149\nv_cvt_pk_bf16_f32 v199,v150,v151\nM v[132:135],a[12:15],a[156:159],v[228:231]\nds_read_b128 a[68:71],v250 offset:1024\nds_read_b128 a[100:103],v250 offset:9216\nM v[132:135],a[8:11],a[152:155],v[132:135]\nbuffer_load_dwordx4 v244,s[40:43],s69 offen offset:1024 lds\ns_add_u32 s94,s94,0x4000\n" \
"M v[132:135],a[4:7],a[148:151],v[132:135]\nv_exp_f32 v164,v164\nM v[132:135],a[0:3],a[144:147],v[132:135]\nds_read_b128 a[72:75],v250 offset:2048\nds_read_b128 a[104:107],v250 offset:10240\ns_and_b32 s94,s94,0xffff\nM v[144:147],a[16:19],a[128:131],v[224:227]\nbuffer_load_dwordx4 v245,s[40:43],s69 offen offset:2048 lds\n" \
"M v[144:147],a[20:23],a[132:135],v[144:147]\nv_exp_f32 v165,v165\ns_add_u32 s95,s95,0x4000\nM v[144:147],a[24:27],a[136:139],v[144:147]\nds_read_b128 a[76:79],v250 offset:3072\nds_read_b128 a[108:111],v250 offset:11264\nM v[144:147],a[28:31],a[140:143],v[144:147]\nbuffer_load_dwordx4 v246,s[40:43],s69 offen offset:3072 lds\n" \
"s_lshl4_add_u32 s69,s12,s68\nM v[148:151],a[28:31],a[156:159],v[228:231]\ns_add_u32 s69,s69,0x10000\ns_add_u32 m0,s64,147456\nM v[148:151],a[24:27],a[152:155],v[148:151]\nds_read_b128 a[80:83],v250 offset:4096\nds_read_b128 a[112:115],v250 offset:12288\nv_cvt_pk_bf16_f32 v212,v164,v165\n" \
"M v[148:151],a[20:23],a[148:151],v[148:151]\nv_exp_f32 v166,v166\nM v[148:151],a[16:19],a[144:147],v[148:151]\nv_exp_f32 v167,v167\nM v[160:163],a[32:35],a[128:131],v[224:227]\nds_read_b128 a[84:87],v250 offset:5120\nds_read_b128 a[116:119],v250 offset:13312\nv_cvt_pk_bf16_f32 v213,v166,v167\n" \
"M v[160:163],a[36:39],a[132:135],v[160:163]\nbuffer_load_dwordx4 v243,s[40:43],s69 offen offset:0 lds\nM v[160:163],a[40:43],a[136:139],v[160:163]\nv_exp_f32 v180,v180\nM v[160:163],a[44:47],a[140:143],v[160:163]\nds_read_b128 a[88:91],v250 offset:6144\nds_read_b128 a[120:123],v250 offset:14336\nM v[164:167],a[44:47],a[156:159],v[228:231]\n" \
"v_exp_f32 v181,v181\nM v[164:167],a[40:43],a[152:155],v[164:167]\nbuffer_load_dwordx4 v244,s[40:43],s69 offen offset:1024 lds\nM v[164:167],a[36:39],a[148:151],v[164:167]\nds_read_b128 a[92:95],v250 offset:7168\nds_read_b128 a[124:127],v250 offset:15360\nv_cvt_pk_bf16_f32 v214,v180,v181\nM v[164:167],a[32:35],a[144:147],v[164:167]\n" \
"v_xor_b32 v250,0x18000,v250\ns_waitcnt lgkmcnt(6)\nM v[176:179],a[48:51],a[128:131],v[224:227]\ns_waitcnt vmcnt(6)\nbuffer_store_dword v233,off,s[52:55],s77 sc1\nM v[176:179],a[52:55],a[132:135],v[176:179]\nbuffer_load_dwordx4 v245,s[40:43],s69 offen offset:2048 lds\nM v[176:179],a[56:59],a[136:139],v[176:179]\n" \
"v_exp_f32 v182,v182\nv_exp_f32 v183,v183\nM v[176:179],a[60:63],a[140:143],v[176:179]\nv_cvt_pk_bf16_f32 v215,v182,v183\nM v[180:183],a[60:63],a[156:159],v[228:231]\nv_exp_f32 v128,v128\nM v[180:183],a[56:59],a[152:155],v[180:183]\nbuffer_load_dwordx4 v246,s[40:43],s69 offen offset:3072 lds\n" \
"M v[180:183],a[52:55],a[148:151],v[180:183]\nv_exp_f32 v129,v129\nM v[180:183],a[48:51],a[144:147],v[180:183]\ns_lshl4_add_u32 s69,s10,s68\nv_exp_f32 v130,v130\nM v[0:3],a[64:67],v[192:195],v[0:3]\nv_exp_f32 v131,v131\nM v[0:3],a[96:99],v[208:211],v[0:3]\n" \
"s_add_u32 m0,s64,0\nv_exp_f32 v144,v144\nM v[8:11],a[68:71],v[192:195],v[8:11]\ns_waitcnt vmcnt(16) lgkmcnt(0)\ns_barrier\nM v[8:11],a[100:103],v[208:211],v[8:11]\nds_read_b128 a[0:3],v250 offset:0\nds_read_b128 a[4:7],v250 offset:1024\n" \
"M v[16:19],a[72:75],v[192:195],v[16:19]\nv_exp_f32 v145,v145\nM v[16:19],a[104:107],v[208:211],v[16:19]\nbuffer_load_dwordx4 v243,s[40:43],s69 offen offset:0 lds\nM v[24:27],a[76:79],v[192:195],v[24:27]\nv_exp_f32 v146,v146\nM v[24:27],a[108:111],v[208:211],v[24:27]\nds_read_b128 a[8:11],v250 offset:2048\n" \
"ds_read_b128 a[12:15],v250 offset:3072\nM v[32:35],a[80:83],v[192:195],v[32:35]\nv_exp_f32 v147,v147\nM v[32:35],a[112:115],v[208:211],v[32:35]\nbuffer_load_dwordx4 v244,s[40:43],s69 offen offset:1024 lds\nM v[40:43],a[84:87],v[192:195],v[40:43]\nv_exp_f32 v160,v160\nM v[40:43],a[116:119],v[208:211],v[40:43]\n" \
"ds_read_b128 a[16:19],v250 offset:4096\nds_read_b128 a[20:23],v250 offset:5120\nM v[48:51],a[88:91],v[192:195],v[48:51]\nv_exp_f32 v161,v161\nM v[48:51],a[120:123],v[208:211],v[48:51]\nbuffer_load_dwordx4 v245,s[40:43],s69 offen offset:2048 lds\nM v[56:59],a[92:95],v[192:195],v[56:59]\nv_exp_f32 v162,v162\n" \
"M v[56:59],a[124:127],v[208:211],v[56:59]\nds_read_b128 a[24:27],v250 offset:6144\nds_read_b128 a[28:31],v250 offset:7168\nM a[192:195],a[208:211],v[192:195],a[192:195]\nv_exp_f32 v163,v163\nv_cvt_pk_bf16_f32 v192,v128,v129\nM a[192:195],a[208:211],v[208:211],a[192:195]\nbuffer_load_dwordx4 v246,s[40:43],s69 offen offset:3072 lds\n" \
"M v[4:7],a[64:67],v[196:199],v[4:7]\ns_lshl4_add_u32 s69,s10,s68\ns_add_u32 s69,s69,0x10000\ns_add_u32 m0,s64,32768\nM v[4:7],a[96:99],v[212:215],v[4:7]\nds_read_b128 a[32:35],v250 offset:8192\nds_read_b128 a[36:39],v250 offset:9216\nM v[12:15],a[68:71],v[196:199],v[12:15]\n" \
"v_exp_f32 v176,v176\nv_cvt_pk_bf16_f32 v193,v130,v131\nM v[12:15],a[100:103],v[212:215],v[12:15]\nbuffer_load_dwordx4 v243,s[40:43],s69 offen offset:0 lds\nM v[20:23],a[72:75],v[196:199],v[20:23]\nv_exp_f32 v177,v177\nv_cvt_pk_bf16_f32 v194,v144,v145\nM v[20:23],a[104:107],v[212:215],v[20:23]\n" \
"ds_read_b128 a[40:43],v250 offset:10240\nds_read_b128 a[44:47],v250 offset:11264\nM v[28:31],a[76:79],v[196:199],v[28:31]\nv_exp_f32 v178,v178\nv_cvt_pk_bf16_f32 v195,v146,v147\nM v[28:31],a[108:111],v[212:215],v[28:31]\nbuffer_load_dwordx4 v244,s[40:43],s69 offen offset:1024 lds\nM v[36:39],a[80:83],v[196:199],v[36:39]\n" \
"v_exp_f32 v179,v179\nv_cvt_pk_bf16_f32 v208,v160,v161\nM v[36:39],a[112:115],v[212:215],v[36:39]\nds_read_b128 a[48:51],v250 offset:12288\nds_read_b128 a[52:55],v250 offset:13312\nM v[44:47],a[84:87],v[196:199],v[44:47]\nv_exp_f32 v132,v132\nv_cvt_pk_bf16_f32 v209,v162,v163\n" \
"M v[44:47],a[116:119],v[212:215],v[44:47]\nbuffer_load_dwordx4 v245,s[40:43],s69 offen offset:2048 lds\nv_cvt_pk_bf16_f32 v210,v176,v177\nM v[52:55],a[88:91],v[196:199],v[52:55]\nv_exp_f32 v133,v133\nM v[52:55],a[120:123],v[212:215],v[52:55]\nds_read_b128 a[56:59],v250 offset:14336\nds_read_b128 a[60:63],v250 offset:15360\n" \
"v_cvt_pk_bf16_f32 v211,v178,v179\nM v[60:63],a[92:95],v[196:199],v[60:63]\nv_exp_f32 v134,v134\nM v[60:63],a[124:127],v[212:215],v[60:63]\nbuffer_load_dwordx4 v246,s[40:43],s69 offen offset:3072 lds\nv_add_u32 v250,s94,v241\nM a[196:199],a[208:211],v[196:199],a[196:199]\nv_exp_f32 v135,v135\n" \
"M a[196:199],a[208:211],v[212:215],a[196:199]\nv_exp_f32 v148,v148\nv_exp_f32 v149,v149\nv_exp_f32 v150,v150\nv_exp_f32 v151,v151\n.Lod:\ns_waitcnt lgkmcnt(2)\nM v[128:131],a[0:3],a[128:131],v[224:227]\n" \
"v_cvt_pk_bf16_f32 v196,v132,v133\nv_cvt_pk_bf16_f32 v197,v134,v135\nM v[128:131],a[4:7],a[132:135],v[128:131]\nds_read_b128 a[64:67],v250 offset:0\nds_read_b128 a[96:99],v250 offset:8192\nM v[128:131],a[8:11],a[136:139],v[128:131]\ns_lshl2_add_u32 s39,s62,s7\ns_lshl_b32 s39,s39,5\n" \
"M v[128:131],a[12:15],a[140:143],v[128:131]\ns_add_u32 s39,s39,s63\nv_cvt_pk_bf16_f32 v198,v148,v149\nv_cvt_pk_bf16_f32 v199,v150,v151\nM v[132:135],a[12:15],a[156:159],v[228:231]\nds_read_b128 a[68:71],v250 offset:1024\nds_read_b128 a[100:103],v250 offset:9216\nM v[132:135],a[8:11],a[152:155],v[132:135]\n" \
"s_lshl2_add_u32 s39,s39,73728\nv_and_b32 v251,3,v249\nM v[132:135],a[4:7],a[148:151],v[132:135]\ns_waitcnt vmcnt(16)\nbuffer_store_dword v252,off,s[52:55],s39 sc1\nM v[132:135],a[0:3],a[144:147],v[132:135]\nds_read_b128 a[72:75],v250 offset:2048\nds_read_b128 a[104:107],v250 offset:10240\n" \
"v_lshlrev_b32 v251,2,v251\nM v[144:147],a[16:19],a[128:131],v[224:227]\nv_exp_f32 v164,v164\nM v[144:147],a[20:23],a[132:135],v[144:147]\nv_exp_f32 v165,v165\nM v[144:147],a[24:27],a[136:139],v[144:147]\nds_read_b128 a[76:79],v250 offset:3072\nds_read_b128 a[108:111],v250 offset:11264\n" \
"M v[144:147],a[28:31],a[140:143],v[144:147]\nv_exp_f32 v166,v166\nv_cvt_pk_bf16_f32 v212,v164,v165\nM v[148:151],a[28:31],a[156:159],v[228:231]\nv_exp_f32 v167,v167\nM v[148:151],a[24:27],a[152:155],v[148:151]\nds_read_b128 a[80:83],v250 offset:4096\nds_read_b128 a[112:115],v250 offset:12288\n" \
"M v[148:151],a[20:23],a[148:151],v[148:151]\nv_cvt_pk_bf16_f32 v213,v166,v167\ns_add_u32 s80,s79,4096\nM v[148:151],a[16:19],a[144:147],v[148:151]\nv_exp_f32 v180,v180\ns_add_u32 s94,s94,0x4000\nM v[160:163],a[32:35],a[128:131],v[224:227]\nds_read_b128 a[84:87],v250 offset:5120\n" \
"ds_read_b128 a[116:119],v250 offset:13312\nM v[160:163],a[36:39],a[132:135],v[160:163]\nv_exp_f32 v181,v181\nM v[160:163],a[40:43],a[136:139],v[160:163]\nv_exp_f32 v182,v182\nM v[160:163],a[44:47],a[140:143],v[160:163]\nds_read_b128 a[88:91],v250 offset:6144\nds_read_b128 a[120:123],v250 offset:14336\n" \
"v_cvt_pk_bf16_f32 v214,v180,v181\nM v[164:167],a[44:47],a[156:159],v[228:231]\nbuffer_load_dword v248,v251,s[52:55],s80 offen sc1\nM v[164:167],a[40:43],a[152:155],v[164:167]\nv_exp_f32 v183,v183\nM v[164:167],a[36:39],a[148:151],v[164:167]\nds_read_b128 a[92:95],v250 offset:7168\nds_read_b128 a[124:127],v250 offset:15360\n" \
"M v[164:167],a[32:35],a[144:147],v[164:167]\nv_cvt_pk_bf16_f32 v215,v182,v183\ns_and_b32 s94,s94,0xffff\nv_add_u32 v250,s94,v241\nM v[176:179],a[48:51],a[128:131],v[224:227]\nv_exp_f32 v128,v128\nM v[176:179],a[52:55],a[132:135],v[176:179]\ns_add_u32 s65,s10,s67\n" \
"s_waitcnt lgkmcnt(4)\nM v[176:179],a[56:59],a[136:139],v[176:179]\nv_exp_f32 v129,v129\nM v[176:179],a[60:63],a[140:143],v[176:179]\nv_exp_f32 v130,v130\ns_add_u32 s65,s65,1048576\nM v[180:183],a[60:63],a[156:159],v[228:231]\nv_exp_f32 v131,v131\n" \
"M v[180:183],a[56:59],a[152:155],v[180:183]\nv_exp_f32 v144,v144\nM v[180:183],a[52:55],a[148:151],v[180:183]\nv_exp_f32 v145,v145\nM v[180:183],a[48:51],a[144:147],v[180:183]\nv_exp_f32 v146,v146\nv_add_u32 v226,131072,v242\nM v[0:3],a[64:67],v[192:195],v[0:3]\n" \
"v_exp_f32 v147,v147\nM v[0:3],a[96:99],v[208:211],v[0:3]\nv_exp_f32 v160,v160\nM v[8:11],a[68:71],v[192:195],v[8:11]\ns_waitcnt lgkmcnt(0)\ns_barrier\nM v[8:11],a[100:103],v[208:211],v[8:11]\ns_lshl4_add_u32 s69,s11,s68\n" \
"s_add_u32 m0,s64,16384\nv_xor_b32 v227,32,v226\nM v[16:19],a[72:75],v[192:195],v[16:19]\nv_exp_f32 v161,v161\nM v[16:19],a[104:107],v[208:211],v[16:19]\nv_exp_f32 v162,v162\nM v[24:27],a[76:79],v[192:195],v[24:27]\nv_exp_f32 v163,v163\n" \
"M v[24:27],a[108:111],v[208:211],v[24:27]\nv_exp_f32 v176,v176\ns_add_u32 s37,s12,s67\nM v[32:35],a[80:83],v[192:195],v[32:35]\nbuffer_load_dwordx4 v243,s[40:43],s69 offen offset:0 lds\nM v[32:35],a[112:115],v[208:211],v[32:35]\nv_exp_f32 v177,v177\nM v[40:43],a[84:87],v[192:195],v[40:43]\n" \
"v_exp_f32 v178,v178\nM v[40:43],a[116:119],v[208:211],v[40:43]\nv_exp_f32 v179,v179\ns_add_u32 s38,s37,4096\nM v[48:51],a[88:91],v[192:195],v[48:51]\nv_exp_f32 v132,v132\nM v[48:51],a[120:123],v[208:211],v[48:51]\nv_exp_f32 v133,v133\n" \
"M v[56:59],a[92:95],v[192:195],v[56:59]\nv_exp_f32 v134,v134\nM v[56:59],a[124:127],v[208:211],v[56:59]\nv_exp_f32 v135,v135\ns_lshl2_add_u32 s74,s63,s7\nM a[192:195],a[208:211],v[192:195],a[192:195]\nbuffer_load_dwordx4 v244,s[40:43],s69 offen offset:1024 lds\nM a[192:195],a[208:211],v[208:211],a[192:195]\n" \
"v_cvt_pk_bf16_f32 v192,v128,v129\nv_cvt_pk_bf16_f32 v193,v130,v131\nM v[4:7],a[64:67],v[196:199],v[4:7]\nds_read_b128 a[64:67],v250 offset:0\nv_cvt_pk_bf16_f32 v194,v144,v145\nM v[4:7],a[96:99],v[212:215],v[4:7]\nds_read_b128 a[96:99],v250 offset:8192\nv_cvt_pk_bf16_f32 v195,v146,v147\n" \
"v_cvt_pk_bf16_f32 v208,v160,v161\nM v[12:15],a[68:71],v[196:199],v[12:15]\nds_read_b128 a[68:71],v250 offset:1024\nv_cvt_pk_bf16_f32 v209,v162,v163\nM v[12:15],a[100:103],v[212:215],v[12:15]\nds_read_b128 a[100:103],v250 offset:9216\nv_cvt_pk_bf16_f32 v210,v176,v177\nM v[20:23],a[72:75],v[196:199],v[20:23]\n" \
"ds_read_b128 a[72:75],v250 offset:2048\nv_cvt_pk_bf16_f32 v211,v178,v179\nM v[20:23],a[104:107],v[212:215],v[20:23]\nds_read_b128 a[104:107],v250 offset:10240\nv_exp_f32 v148,v148\nM v[28:31],a[76:79],v[196:199],v[28:31]\nbuffer_load_dwordx4 v245,s[40:43],s69 offen offset:2048 lds\nM v[28:31],a[108:111],v[212:215],v[28:31]\n" \
"ds_read_b128 a[76:79],v250 offset:3072\nds_read_b128 a[108:111],v250 offset:11264\nM v[36:39],a[80:83],v[196:199],v[36:39]\nds_read_b128 a[80:83],v250 offset:4096\ns_lshl_b32 s72,s62,7\nM v[36:39],a[112:115],v[212:215],v[36:39]\nds_read_b128 a[112:115],v250 offset:12288\nv_exp_f32 v149,v149\n" \
"M v[44:47],a[84:87],v[196:199],v[44:47]\nds_read_b128 a[84:87],v250 offset:5120\ns_add_u32 s95,s74,s72\nM v[44:47],a[116:119],v[212:215],v[44:47]\nds_read_b128 a[116:119],v250 offset:13312\ns_lshl2_add_u32 s76,s61,s79\nM v[52:55],a[88:91],v[196:199],v[52:55]\nds_read_b128 a[88:91],v250 offset:6144\n" \
"s_sub_u32 s74,s74,s72\nM v[52:55],a[120:123],v[212:215],v[52:55]\nds_read_b128 a[120:123],v250 offset:14336\nv_exp_f32 v150,v150\nM v[60:63],a[92:95],v[196:199],v[60:63]\nbuffer_load_dwordx4 v246,s[40:43],s69 offen offset:3072 lds\nM v[60:63],a[124:127],v[212:215],v[60:63]\ns_waitcnt vmcnt(4)\n" \
"v_and_b32 v248,s17,v248\nM a[196:199],a[208:211],v[196:199],a[196:199]\nv_cmp_eq_u32 vcc,0,v248\nds_read_b128 a[92:95],v250 offset:7168\nM a[196:199],a[208:211],v[212:215],a[196:199]\ns_cbranch_vccnz .Lps6\n.Lpb6:\ns_lshl4_add_u32 s69,s11,s68\n" \
"s_waitcnt lgkmcnt(4)\nM v[0:3],a[64:67],v[192:195],v[0:3]\nbuffer_load_dwordx4 v[72:75],v240,s[52:55],s65 offen offset:0 sc1\nM v[0:3],a[96:99],v[208:211],v[0:3]\ns_add_u32 s69,s69,0x10000\ns_add_u32 m0,s64,49152\nM v[8:11],a[68:71],v[192:195],v[8:11]\nds_read_b128 a[124:127],v250 offset:15360\n" \
"v_cvt_pk_bf16_f32 v196,v132,v133\nM v[8:11],a[100:103],v[208:211],v[8:11]\nv_exp_f32 v151,v151\nv_cvt_pk_bf16_f32 v197,v134,v135\nM v[16:19],a[72:75],v[192:195],v[16:19]\nbuffer_load_dwordx4 v243,s[40:43],s69 offen offset:0 lds\nM v[16:19],a[104:107],v[208:211],v[16:19]\nv_exp_f32 v164,v164\n" \
"M v[24:27],a[76:79],v[192:195],v[24:27]\nv_exp_f32 v165,v165\nM v[24:27],a[108:111],v[208:211],v[24:27]\nv_exp_f32 v166,v166\nv_cvt_pk_bf16_f32 v198,v148,v149\nM v[32:35],a[80:83],v[192:195],v[32:35]\nbuffer_load_dwordx4 v[76:79],v240,s[52:55],s65 offen offset:1024 sc1\nM v[32:35],a[112:115],v[208:211],v[32:35]\n" \
"v_exp_f32 v167,v167\nM v[40:43],a[84:87],v[192:195],v[40:43]\nv_cvt_pk_bf16_f32 v199,v150,v151\ns_waitcnt lgkmcnt(0)\nM v[40:43],a[116:119],v[208:211],v[40:43]\nv_exp_f32 v180,v180\nv_cvt_pk_bf16_f32 v212,v164,v165\nM v[48:51],a[88:91],v[192:195],v[48:51]\n" \
"buffer_load_dwordx4 v244,s[40:43],s69 offen offset:1024 lds\nM v[48:51],a[120:123],v[208:211],v[48:51]\nv_exp_f32 v181,v181\nM v[56:59],a[92:95],v[192:195],v[56:59]\nv_exp_f32 v182,v182\nM v[56:59],a[124:127],v[208:211],v[56:59]\nv_exp_f32 v183,v183\nv_cvt_pk_bf16_f32 v213,v166,v167\n" \
"M a[192:195],a[208:211],v[192:195],a[192:195]\nbuffer_load_dwordx4 v[88:91],v240,s[52:55],s65 offen offset:2048 sc1\nM a[192:195],a[208:211],v[208:211],a[192:195]\nv_cvt_pk_bf16_f32 v214,v180,v181\nv_cvt_pk_bf16_f32 v215,v182,v183\nM v[4:7],a[64:67],v[196:199],v[4:7]\ns_barrier\nds_read_b64_tr_b16 v[200:201],v226 offset:0\n" \
"M v[4:7],a[96:99],v[212:215],v[4:7]\nds_read_b64_tr_b16 v[202:203],v226 offset:4096\nds_read_b64_tr_b16 v[204:205],v226 offset:8192\nds_read_b64_tr_b16 v[206:207],v226 offset:12288\nM v[12:15],a[68:71],v[196:199],v[12:15]\nbuffer_load_dwordx4 v245,s[40:43],s69 offen offset:2048 lds\nM v[12:15],a[100:103],v[212:215],v[12:15]\nds_read_b64_tr_b16 v[216:217],v227 offset:0\n" \
"ds_read_b64_tr_b16 v[218:219],v227 offset:4096\nM v[20:23],a[72:75],v[196:199],v[20:23]\nds_read_b64_tr_b16 v[220:221],v227 offset:8192\nds_read_b64_tr_b16 v[222:223],v227 offset:12288\nM v[20:23],a[104:107],v[212:215],v[20:23]\nds_read_b64_tr_b16 v[192:193],v226 offset:512\nds_read_b64_tr_b16 v[194:195],v226 offset:4608\nds_read_b64_tr_b16 v[208:209],v226 offset:8704\n" \
"M v[28:31],a[76:79],v[196:199],v[28:31]\nbuffer_load_dwordx4 v[92:95],v240,s[52:55],s65 offen offset:3072 sc1\nM v[28:31],a[108:111],v[212:215],v[28:31]\ns_add_u32 s65,s65,4096\nds_read_b64_tr_b16 v[210:211],v226 offset:12800\nM v[36:39],a[80:83],v[196:199],v[36:39]\nv_accvgpr_read_b32 v224,a192\nv_cmp_eq_u32 vcc,2,v248\n" \
"M v[36:39],a[112:115],v[212:215],v[36:39]\ns_cmp_lg_u64 vcc,0\ns_addc_u32 s99,s99,0\nv_xor_b32 v250,0x8000,v250\nM v[44:47],a[84:87],v[196:199],v[44:47]\nbuffer_load_dwordx4 v246,s[40:43],s69 offen offset:3072 lds\nM v[44:47],a[116:119],v[212:215],v[44:47]\nv_add_u32 v229,s64,v250\n" \
"s_lshl_b32 s72,s7,9\nM v[52:55],a[88:91],v[196:199],v[52:55]\nv_lshl_add_u32 v239,v249,3,s72\ns_lshr_b32 s72,s64,1\nM v[52:55],a[120:123],v[212:215],v[52:55]\nv_and_b32 v254,32,v249\ns_lshr_b32 s70,s60,1\nv_lshlrev_b32 v254,3,v254\n" \
"M v[60:63],a[92:95],v[196:199],v[60:63]\nbuffer_load_dwordx4 v[104:107],v240,s[52:55],s65 offen offset:0 sc1\nM v[60:63],a[124:127],v[212:215],v[60:63]\ns_lshl_b32 s70,s70,7\nv_lshl_add_u32 v254,v249,2,v254\nM a[196:199],a[208:211],v[196:199],a[196:199]\nds_read_b64_tr_b16 v[196:197],v227 offset:512\nds_read_b64_tr_b16 v[198:199],v227 offset:4608\n" \
"M a[196:199],a[208:211],v[212:215],a[196:199]\nds_read_b64_tr_b16 v[212:213],v227 offset:8704\nds_read_b64_tr_b16 v[214:215],v227 offset:12800\ns_waitcnt lgkmcnt(4)\nM v[128:131],v[64:67],v[200:203],0\ns_lshl_b32 s80,s62,9\nv_xor_b32 v254,s70,v254\ns_add_u32 s80,s80,73728\n" \
"v_and_b32 v236,15,v249\nM v[128:131],v[80:83],v[204:207],v[128:131]\nv_accvgpr_read_b32 v228,a196\nv_add_f32 v225,v224,v228\nv_rcp_f32 v224,v224\nv_lshrrev_b32 v235,4,v249\nM v[132:135],v[68:71],v[200:203],0\nv_mul_f32 v0,v0,v224\n" \
"v_mul_f32 v1,v1,v224\nv_mul_f32 v2,v2,v224\nv_mul_f32 v3,v3,v224\nM v[132:135],v[84:87],v[204:207],v[132:135]\nv_cvt_pk_bf16_f32 v0,v0,v1\nv_mul_f32 v8,v8,v224\nv_mul_f32 v9,v9,v224\nv_mul_f32 v10,v10,v224\n" \
"v_mul_f32 v11,v11,v224\nM v[136:139],v[64:67],v[216:219],0\nbuffer_load_dwordx4 v[108:111],v240,s[52:55],s65 offen offset:1024 sc1\nv_cvt_pk_bf16_f32 v1,v2,v3\nds_read_b64_tr_b16 v[200:201],v226 offset:1024\nM v[136:139],v[80:83],v[220:223],v[136:139]\nds_read_b64_tr_b16 v[202:203],v226 offset:5120\nv_cvt_pk_bf16_f32 v2,v8,v9\n" \
"v_cvt_pk_bf16_f32 v3,v10,v11\nds_read_b64_tr_b16 v[204:205],v226 offset:9216\nds_read_b64_tr_b16 v[206:207],v226 offset:13312\nM v[140:143],v[68:71],v[216:219],0\nbuffer_store_dwordx4 v[0:3],v240,s[52:55],s37 offen offset:0 sc1\nv_rcp_f32 v228,v228\nM v[140:143],v[84:87],v[220:223],v[140:143]\nv_mul_f32 v4,v4,v228\n" \
"v_mul_f32 v5,v5,v228\nds_read_b64_tr_b16 v[216:217],v227 offset:1024\nds_read_b64_tr_b16 v[218:219],v227 offset:5120\nv_mul_f32 v6,v6,v228\nM v[144:147],v[64:67],v[192:195],0\nv_mul_f32 v7,v7,v228\nv_cvt_pk_bf16_f32 v4,v4,v5\nv_mul_f32 v12,v12,v228\n" \
"v_mul_f32 v13,v13,v228\nM v[144:147],v[80:83],v[208:211],v[144:147]\nv_mul_f32 v14,v14,v228\nv_mul_f32 v15,v15,v228\nv_cvt_pk_bf16_f32 v5,v6,v7\nds_read_b64_tr_b16 v[220:221],v227 offset:9216\nds_read_b64_tr_b16 v[222:223],v227 offset:13312\nM v[148:151],v[68:71],v[192:195],0\n" \
"v_cvt_pk_bf16_f32 v6,v12,v13\nv_cvt_pk_bf16_f32 v7,v14,v15\nds_read_b64_tr_b16 v[192:193],v226 offset:1536\nds_read_b64_tr_b16 v[194:195],v226 offset:5632\nM v[148:151],v[84:87],v[208:211],v[148:151]\nbuffer_store_dwordx4 v[4:7],v240,s[52:55],s37 offen offset:1024 sc1\nv_mul_f32 v16,v16,v224\nv_mul_f32 v17,v17,v224\n" \
"s_waitcnt lgkmcnt(6)\nM v[152:155],v[64:67],v[196:199],0\nbuffer_load_dwordx4 v[120:123],v240,s[52:55],s65 offen offset:2048 sc1\nds_read_b64_tr_b16 v[208:209],v226 offset:9728\nds_read_b64_tr_b16 v[210:211],v226 offset:13824\nM v[152:155],v[80:83],v[212:215],v[152:155]\nv_mul_f32 v18,v18,v224\nv_mul_f32 v19,v19,v224\n" \
"v_cvt_pk_bf16_f32 v16,v16,v17\nv_mul_f32 v24,v24,v224\nv_mul_f32 v25,v25,v224\nM v[156:159],v[68:71],v[196:199],0\nv_mul_f32 v26,v26,v224\nv_mul_f32 v27,v27,v224\nv_cvt_pk_bf16_f32 v17,v18,v19\nv_cvt_pk_bf16_f32 v18,v24,v25\n" \
"M v[156:159],v[84:87],v[212:215],v[156:159]\nv_cvt_pk_bf16_f32 v19,v26,v27\nds_read_b64_tr_b16 v[196:197],v227 offset:1536\nds_read_b64_tr_b16 v[198:199],v227 offset:5632\nds_read_b64_tr_b16 v[212:213],v227 offset:9728\nv_mul_f32 v20,v20,v228\nM v[160:163],v[64:67],v[200:203],0\nbuffer_store_dwordx4 v[16:19],v240,s[52:55],s37 offen offset:2048 sc1\n" \
"ds_read_b64_tr_b16 v[214:215],v227 offset:13824\nv_mul_f32 v21,v21,v228\nM v[160:163],v[80:83],v[204:207],v[160:163]\nv_mul_f32 v22,v22,v228\nv_mul_f32 v23,v23,v228\nv_cvt_pk_bf16_f32 v20,v20,v21\nv_mul_f32 v28,v28,v228\nv_mul_f32 v29,v29,v228\n" \
"M v[164:167],v[68:71],v[200:203],0\nv_mul_f32 v30,v30,v228\nv_mul_f32 v31,v31,v228\nv_cvt_pk_bf16_f32 v21,v22,v23\nv_cvt_pk_bf16_f32 v22,v28,v29\nM v[164:167],v[84:87],v[204:207],v[164:167]\nv_cvt_pk_bf16_f32 v23,v30,v31\nds_read_b64_tr_b16 v[200:201],v226 offset:16384\n" \
"ds_read_b64_tr_b16 v[202:203],v226 offset:20480\nds_read_b64_tr_b16 v[204:205],v226 offset:24576\ns_waitcnt lgkmcnt(6)\nM v[168:171],v[64:67],v[216:219],0\nbuffer_load_dwordx4 v[124:127],v240,s[52:55],s65 offen offset:3072 sc1\nds_read_b64_tr_b16 v[206:207],v226 offset:28672\nv_mul_f32 v32,v32,v224\nM v[168:171],v[80:83],v[220:223],v[168:171]\n" \
"buffer_store_dwordx4 v[20:23],v240,s[52:55],s37 offen offset:3072 sc1\nv_mul_f32 v33,v33,v224\nv_mul_f32 v34,v34,v224\nv_mul_f32 v35,v35,v224\nM v[172:175],v[68:71],v[216:219],0\nv_cvt_pk_bf16_f32 v32,v32,v33\nv_mul_f32 v40,v40,v224\nv_mul_f32 v41,v41,v224\n" \
"v_mul_f32 v42,v42,v224\nM v[172:175],v[84:87],v[220:223],v[172:175]\nv_mul_f32 v43,v43,v224\nv_cvt_pk_bf16_f32 v33,v34,v35\nds_read_b64_tr_b16 v[216:217],v227 offset:16384\nds_read_b64_tr_b16 v[218:219],v227 offset:20480\nv_cvt_pk_bf16_f32 v34,v40,v41\nM v[176:179],v[64:67],v[192:195],0\n" \
"v_cvt_pk_bf16_f32 v35,v42,v43\nds_read_b64_tr_b16 v[220:221],v227 offset:24576\nds_read_b64_tr_b16 v[222:223],v227 offset:28672\ns_add_u32 s65,s11,s67\nM v[176:179],v[80:83],v[208:211],v[176:179]\ns_add_u32 s65,s65,1048576\nv_mul_f32 v36,v36,v228\nv_mul_f32 v37,v37,v228\n" \
"v_mul_f32 v38,v38,v228\nv_mul_f32 v39,v39,v228\nM v[180:183],v[68:71],v[192:195],0\nbuffer_store_dwordx4 v[32:35],v240,s[52:55],s38 offen offset:0 sc1\nds_read_b64_tr_b16 v[192:193],v226 offset:16896\nds_read_b64_tr_b16 v[194:195],v226 offset:20992\nM v[180:183],v[84:87],v[208:211],v[180:183]\nv_cvt_pk_bf16_f32 v36,v36,v37\n" \
"v_mul_f32 v44,v44,v228\nv_mul_f32 v45,v45,v228\nv_mul_f32 v46,v46,v228\ns_waitcnt lgkmcnt(6)\nM v[184:187],v[64:67],v[196:199],0\nbuffer_load_dwordx4 a[0:3],v240,s[52:55],s65 offen offset:0 sc1\nv_mul_f32 v47,v47,v228\nv_cvt_pk_bf16_f32 v37,v38,v39\n" \
"M v[184:187],v[80:83],v[212:215],v[184:187]\nds_read_b64_tr_b16 v[208:209],v226 offset:25088\nds_read_b64_tr_b16 v[210:211],v226 offset:29184\nv_cvt_pk_bf16_f32 v38,v44,v45\nv_cvt_pk_bf16_f32 v39,v46,v47\nv_add_f32 v225,v225,v224\nM v[188:191],v[68:71],v[196:199],0\nds_read_b64_tr_b16 v[196:197],v227 offset:16896\n" \
"ds_read_b64_tr_b16 v[198:199],v227 offset:20992\nv_add_f32 v225,v225,v228\nv_cmp_ngt_f32 vcc,s28,v225\nM v[188:191],v[84:87],v[212:215],v[188:191]\nbuffer_store_dwordx4 v[36:39],v240,s[52:55],s38 offen offset:1024 sc1\nv_mul_f32 v48,v48,v224\nv_mul_f32 v49,v49,v224\nds_read_b64_tr_b16 v[212:213],v227 offset:25088\n" \
"M v[128:131],v[96:99],v[200:203],v[128:131]\nds_read_b64_tr_b16 v[214:215],v227 offset:29184\ns_and_b64 vcc,vcc,s[90:91]\ns_addc_u32 s99,s99,0\nv_mul_f32 v50,v50,v224\nM v[128:131],v[112:115],v[204:207],v[128:131]\nv_mul_f32 v51,v51,v224\nv_cvt_pk_bf16_f32 v48,v48,v49\n" \
"v_mul_f32 v56,v56,v224\nv_mul_f32 v57,v57,v224\nv_mul_f32 v58,v58,v224\nM v[132:135],v[100:103],v[200:203],v[132:135]\nv_mul_f32 v59,v59,v224\nv_cvt_pk_bf16_f32 v49,v50,v51\nv_cvt_pk_bf16_f32 v50,v56,v57\nv_cvt_pk_bf16_f32 v51,v58,v59\n" \
"M v[132:135],v[116:119],v[204:207],v[132:135]\nv_mov_b32 v230,s99\nds_write_b32 v229,v230\nds_read_b64_tr_b16 v[200:201],v226 offset:17408\nds_read_b64_tr_b16 v[202:203],v226 offset:21504\ns_waitcnt lgkmcnt(5)\nM v[136:139],v[96:99],v[216:219],v[136:139]\nbuffer_store_dwordx4 v[48:51],v240,s[52:55],s38 offen offset:2048 sc1\n" \
"buffer_load_dwordx4 a[4:7],v240,s[52:55],s65 offen offset:1024 sc1\nM v[136:139],v[112:115],v[220:223],v[136:139]\nds_read_b64_tr_b16 v[204:205],v226 offset:25600\nds_read_b64_tr_b16 v[206:207],v226 offset:29696\nv_mul_f32 v52,v52,v228\nv_mul_f32 v53,v53,v228\nv_mul_f32 v54,v54,v228\nM v[140:143],v[100:103],v[216:219],v[140:143]\n" \
"v_mul_f32 v55,v55,v228\nv_cvt_pk_bf16_f32 v52,v52,v53\nv_mul_f32 v60,v60,v228\nv_mul_f32 v61,v61,v228\nM v[140:143],v[116:119],v[220:223],v[140:143]\nv_mul_f32 v62,v62,v228\nv_mul_f32 v63,v63,v228\nv_cvt_pk_bf16_f32 v53,v54,v55\n" \
"v_cvt_pk_bf16_f32 v54,v60,v61\nv_cvt_pk_bf16_f32 v55,v62,v63\nM v[144:147],v[96:99],v[192:195],v[144:147]\nds_read_b64_tr_b16 v[216:217],v227 offset:17408\nds_read_b64_tr_b16 v[218:219],v227 offset:21504\nds_read_b64_tr_b16 v[220:221],v227 offset:25600\ns_lshl4_add_u32 s69,s14,s68\nM v[144:147],v[112:115],v[208:211],v[144:147]\n" \
"buffer_store_dwordx4 v[52:55],v240,s[52:55],s38 offen offset:3072 sc1\nds_read_b64_tr_b16 v[222:223],v227 offset:29696\ns_add_u32 m0,s64,65536\nv_lshl_add_u32 v229,v251,10,v250\nM v[148:151],v[100:103],v[192:195],v[148:151]\nds_read_b64_tr_b16 v[192:193],v226 offset:17920\nds_read_b64_tr_b16 v[194:195],v226 offset:22016\nv_lshlrev_b32 v236,1,v236\n" \
"v_lshl_or_b32 v235,v235,8,v236\nM v[148:151],v[116:119],v[208:211],v[148:151]\nds_read_b64_tr_b16 v[208:209],v226 offset:26112\nds_read_b64_tr_b16 v[210:211],v226 offset:30208\nv_add_u32 v226,0,v242\ns_lshl_b32 s95,s95,10\nv_add_u32 v236,s95,v235\nM v[152:155],v[96:99],v[196:199],v[152:155]\n" \
"buffer_load_dwordx4 a[8:11],v240,s[52:55],s65 offen offset:2048 sc1\ns_add_u32 s74,s74,1920\ns_waitcnt lgkmcnt(7)\nM v[152:155],v[112:115],v[212:215],v[152:155]\ns_lshl_b32 s74,s74,10\nv_add_u32 v237,s74,v235\ns_add_u32 s38,s33,16384\nv_lshlrev_b32 v238,1,v240\n" \
"v_add_u32 v238,83968,v238\nM v[156:159],v[100:103],v[196:199],v[156:159]\ns_barrier\nbuffer_load_dwordx4 v243,s[40:43],s69 offen offset:0 lds\nds_read_b64_tr_b16 v[196:197],v227 offset:17920\nM v[156:159],v[116:119],v[212:215],v[156:159]\nds_read_b64_tr_b16 v[198:199],v227 offset:22016\nds_read_b32 v230,v229\n" \
"ds_read_b64_tr_b16 v[212:213],v227 offset:26112\nv_add_u32 v239,83968,v239\nv_bfe_u32 v228,v249,2,2\nM v[160:163],v[96:99],v[200:203],v[160:163]\nds_read_b64_tr_b16 v[214:215],v227 offset:30208\nv_xor_b32 v227,32,v226\nv_lshrrev_b32 v229,4,v249\ns_mov_b32 s97,4096\n" \
"M v[160:163],v[112:115],v[204:207],v[160:163]\ns_add_u32 s95,s95,54525952\ns_add_u32 s74,s74,54525952\nM v[164:167],v[100:103],v[200:203],v[164:167]\nds_read_b64_tr_b16 v[200:201],v226 offset:0\nds_read_b64_tr_b16 v[202:203],v226 offset:4096\nM v[164:167],v[116:119],v[204:207],v[164:167]\nds_read_b64_tr_b16 v[204:205],v226 offset:8192\n" \
"s_waitcnt lgkmcnt(8)\nM v[168:171],v[96:99],v[216:219],v[168:171]\nbuffer_load_dwordx4 a[12:15],v240,s[52:55],s65 offen offset:3072 sc1\nM v[168:171],v[112:115],v[220:223],v[168:171]\nds_read_b64_tr_b16 v[206:207],v226 offset:12288\ns_add_u32 s65,s65,4096\nM v[172:175],v[100:103],v[216:219],v[172:175]\nbuffer_load_dwordx4 v244,s[40:43],s69 offen offset:1024 lds\n" \
"M v[172:175],v[116:119],v[220:223],v[172:175]\nds_read_b64_tr_b16 v[216:217],v227 offset:0\nds_read_b64_tr_b16 v[218:219],v227 offset:4096\nM v[176:179],v[96:99],v[192:195],v[176:179]\nds_read_b64_tr_b16 v[220:221],v227 offset:8192\nds_read_b64_tr_b16 v[222:223],v227 offset:12288\nM v[176:179],v[112:115],v[208:211],v[176:179]\ns_waitcnt lgkmcnt(8)\n" \
"v_cmp_ne_u32 vcc,0,v230\nM v[180:183],v[100:103],v[192:195],v[180:183]\ns_cbranch_vccnz .Lredo\nds_read_b64_tr_b16 v[192:193],v226 offset:512\nM v[180:183],v[116:119],v[208:211],v[180:183]\nds_read_b64_tr_b16 v[194:195],v226 offset:4608\nds_read_b64_tr_b16 v[208:209],v226 offset:8704\nM v[184:187],v[96:99],v[196:199],v[184:187]\n" \
"buffer_load_dwordx4 a[16:19],v240,s[52:55],s65 offen offset:0 sc1\nM v[184:187],v[112:115],v[212:215],v[184:187]\nds_read_b64_tr_b16 v[210:211],v226 offset:12800\ns_and_b32 s58,s3,16384\nM v[188:191],v[100:103],v[196:199],v[188:191]\nbuffer_load_dwordx4 v245,s[40:43],s69 offen offset:2048 lds\nM v[188:191],v[116:119],v[212:215],v[188:191]\nds_read_b64_tr_b16 v[196:197],v227 offset:512\n" \
"s_waitcnt vmcnt(27) lgkmcnt(6)\nM v[128:131],v[72:75],v[200:203],v[128:131]\nds_read_b64_tr_b16 v[198:199],v227 offset:4608\ns_waitcnt vmcnt(23)\nM v[128:131],v[88:91],v[204:207],v[128:131]\nds_read_b64_tr_b16 v[212:213],v227 offset:8704\nds_read_b64_tr_b16 v[214:215],v227 offset:12800\nM v[132:135],v[76:79],v[200:203],v[132:135]\n" \
"ds_read_b64_tr_b16 v[200:201],v226 offset:1024\ns_waitcnt vmcnt(21)\nM v[132:135],v[92:95],v[204:207],v[132:135]\nds_read_b64_tr_b16 v[202:203],v226 offset:5120\nds_read_b64_tr_b16 v[204:205],v226 offset:9216\nM v[136:139],v[72:75],v[216:219],v[136:139]\nbuffer_load_dwordx4 a[20:23],v240,s[52:55],s65 offen offset:1024 sc1\ns_waitcnt lgkmcnt(7)\n" \
"M v[136:139],v[88:91],v[220:223],v[136:139]\nds_read_b64_tr_b16 v[206:207],v226 offset:13312\nv_mov_b32 v250,82944\nM v[140:143],v[76:79],v[216:219],v[140:143]\nbuffer_load_dwordx4 v246,s[40:43],s69 offen offset:3072 lds\nM v[140:143],v[92:95],v[220:223],v[140:143]\nds_read_b64_tr_b16 v[216:217],v227 offset:1024\nds_read_b64_tr_b16 v[218:219],v227 offset:5120\n" \
"M v[144:147],v[72:75],v[192:195],v[144:147]\nds_read_b64_tr_b16 v[220:221],v227 offset:9216\nds_read_b64_tr_b16 v[222:223],v227 offset:13312\nM v[144:147],v[88:91],v[208:211],v[144:147]\ns_lshl4_add_u32 s69,s14,s68\ns_add_u32 s69,s69,0x10000\nM v[148:151],v[76:79],v[192:195],v[148:151]\nds_read_b64_tr_b16 v[192:193],v226 offset:1536\n" \
"ds_read_b64_tr_b16 v[194:195],v226 offset:5632\nM v[148:151],v[92:95],v[208:211],v[148:151]\ns_add_u32 m0,s64,98304\ns_waitcnt lgkmcnt(7)\nM v[152:155],v[72:75],v[196:199],v[152:155]\nbuffer_load_dwordx4 a[24:27],v240,s[52:55],s65 offen offset:2048 sc1\nM v[152:155],v[88:91],v[212:215],v[152:155]\nds_read_b64_tr_b16 v[208:209],v226 offset:9728\n" \
"ds_read_b64_tr_b16 v[210:211],v226 offset:13824\nM v[156:159],v[76:79],v[196:199],v[156:159]\nbuffer_load_dwordx4 v243,s[40:43],s69 offen offset:0 lds\nM v[156:159],v[92:95],v[212:215],v[156:159]\nds_read_b64_tr_b16 v[196:197],v227 offset:1536\nds_read_b64_tr_b16 v[198:199],v227 offset:5632\nM v[160:163],v[72:75],v[200:203],v[160:163]\nds_read_b64_tr_b16 v[212:213],v227 offset:9728\n" \
"s_waitcnt lgkmcnt(7)\nM v[160:163],v[88:91],v[204:207],v[160:163]\nds_read_b64_tr_b16 v[214:215],v227 offset:13824\ns_lshl_b32 s70,s2,9\nM v[164:167],v[76:79],v[200:203],v[164:167]\nds_read_b64_tr_b16 v[200:201],v226 offset:32768\nds_read_b64_tr_b16 v[202:203],v226 offset:36864\nM v[164:167],v[92:95],v[204:207],v[164:167]\n" \
"ds_read_b64_tr_b16 v[204:205],v226 offset:40960\nds_read_b64_tr_b16 v[206:207],v226 offset:45056\nM v[168:171],v[72:75],v[216:219],v[168:171]\nbuffer_load_dwordx4 a[28:31],v240,s[52:55],s65 offen offset:3072 sc1\nM v[168:171],v[88:91],v[220:223],v[168:171]\ns_add_u32 s65,s14,s67\ns_add_u32 s65,s65,1048576\nM v[172:175],v[76:79],v[216:219],v[172:175]\n" \
"buffer_load_dwordx4 v244,s[40:43],s69 offen offset:1024 lds\nM v[172:175],v[92:95],v[220:223],v[172:175]\nds_read_b64_tr_b16 v[216:217],v227 offset:32768\ns_waitcnt lgkmcnt(6)\nM v[176:179],v[72:75],v[192:195],v[176:179]\nds_read_b64_tr_b16 v[218:219],v227 offset:36864\nds_read_b64_tr_b16 v[220:221],v227 offset:40960\nM v[176:179],v[88:91],v[208:211],v[176:179]\n" \
"ds_read_b64_tr_b16 v[222:223],v227 offset:45056\ns_lshl_b32 s71,s7,8\nM v[180:183],v[76:79],v[192:195],v[180:183]\nds_read_b64_tr_b16 v[192:193],v226 offset:33280\nds_read_b64_tr_b16 v[194:195],v226 offset:37376\nM v[180:183],v[92:95],v[208:211],v[180:183]\nds_read_b64_tr_b16 v[208:209],v226 offset:41472\nds_read_b64_tr_b16 v[210:211],v226 offset:45568\n" \
"M v[184:187],v[72:75],v[196:199],v[184:187]\nbuffer_load_dwordx4 a[32:35],v240,s[52:55],s65 offen offset:0 sc1\ns_waitcnt lgkmcnt(8)\nM v[184:187],v[88:91],v[212:215],v[184:187]\nds_write_b128 v250,a[244:247]\nM v[188:191],v[76:79],v[196:199],v[188:191]\nbuffer_load_dwordx4 v245,s[40:43],s69 offen offset:2048 lds\nM v[188:191],v[92:95],v[212:215],v[188:191]\n" \
"ds_read_b64_tr_b16 v[196:197],v227 offset:33280\ns_waitcnt vmcnt(26)\nM v[128:131],v[104:107],v[200:203],v[128:131]\nds_read_b64_tr_b16 v[198:199],v227 offset:37376\ns_waitcnt vmcnt(23)\nM v[128:131],v[120:123],v[204:207],v[128:131]\nds_read_b64_tr_b16 v[212:213],v227 offset:41472\nds_read_b64_tr_b16 v[214:215],v227 offset:45568\n" \
"M v[132:135],v[108:111],v[200:203],v[132:135]\nds_read_b64_tr_b16 v[200:201],v226 offset:33792\ns_waitcnt vmcnt(21)\nM v[132:135],v[124:127],v[204:207],v[132:135]\nds_read_b64_tr_b16 v[202:203],v226 offset:37888\ns_waitcnt lgkmcnt(7)\nM v[136:139],v[104:107],v[216:219],v[136:139]\nbuffer_load_dwordx4 a[36:39],v240,s[52:55],s65 offen offset:1024 sc1\n" \
"M v[136:139],v[120:123],v[220:223],v[136:139]\nds_read_b64_tr_b16 v[204:205],v226 offset:41984\nds_read_b64_tr_b16 v[206:207],v226 offset:46080\nM v[140:143],v[108:111],v[216:219],v[140:143]\nbuffer_load_dwordx4 v246,s[40:43],s69 offen offset:3072 lds\nM v[140:143],v[124:127],v[220:223],v[140:143]\nds_read_b64_tr_b16 v[216:217],v227 offset:33792\nds_read_b64_tr_b16 v[218:219],v227 offset:37888\n" \
"M v[144:147],v[104:107],v[192:195],v[144:147]\nds_read_b64_tr_b16 v[220:221],v227 offset:41984\nds_read_b64_tr_b16 v[222:223],v227 offset:46080\nM v[144:147],v[120:123],v[208:211],v[144:147]\nv_lshl_add_u32 v241,v249,2,s71\ns_add_u32 s70,s70,4096\nM v[148:151],v[108:111],v[192:195],v[148:151]\nds_read_b64_tr_b16 v[192:193],v226 offset:34304\n" \
"ds_read_b64_tr_b16 v[194:195],v226 offset:38400\nM v[148:151],v[124:127],v[208:211],v[148:151]\nds_read_b64_tr_b16 v[208:209],v226 offset:42496\ns_waitcnt lgkmcnt(9)\nM v[152:155],v[104:107],v[196:199],v[152:155]\nbuffer_load_dwordx4 a[40:43],v240,s[52:55],s65 offen offset:2048 sc1\nM v[152:155],v[120:123],v[212:215],v[152:155]\nds_read_b64_tr_b16 v[210:211],v226 offset:46592\n" \
"s_add_u32 m0,s64,83968\nM v[156:159],v[108:111],v[196:199],v[156:159]\ns_waitcnt vmcnt(16)\nbuffer_store_dword v252,off,s[52:55],s76 sc1\ns_barrier\nM v[156:159],v[124:127],v[212:215],v[156:159]\nds_read_b64_tr_b16 v[196:197],v227 offset:34304\nds_read_b64_tr_b16 v[198:199],v227 offset:38400\n" \
"M v[160:163],v[104:107],v[200:203],v[160:163]\nbuffer_load_dwordx4 v240,s[52:55],s72 offen offset:0 sc1 lds\ns_waitcnt lgkmcnt(6)\nM v[160:163],v[120:123],v[204:207],v[160:163]\nds_read_b64_tr_b16 v[212:213],v227 offset:42496\nds_read_b64_tr_b16 v[214:215],v227 offset:46592\nM v[164:167],v[108:111],v[200:203],v[164:167]\nds_read_b64_tr_b16 v[200:201],v226 offset:16384\n" \
"ds_read_b64_tr_b16 v[202:203],v226 offset:20480\nM v[164:167],v[124:127],v[204:207],v[164:167]\nds_read_b64_tr_b16 v[204:205],v226 offset:24576\nds_read_b64_tr_b16 v[206:207],v226 offset:28672\nM v[168:171],v[104:107],v[216:219],v[168:171]\nbuffer_load_dwordx4 v240,s[52:55],s72 offen offset:1024 sc1 lds\ns_lshl_b32 s72,s2,9\ns_add_u32 s56,s26,s72\n" \
"s_addc_u32 s57,s27,0\ns_add_u32 m0,s64,114688\nbuffer_load_dwordx4 a[44:47],v240,s[52:55],s65 offen offset:3072 sc1\nM v[168:171],v[120:123],v[220:223],v[168:171]\ns_add_u32 s65,s65,4096\nv_add_u32 v230,s70,v241\nM v[172:175],v[108:111],v[216:219],v[172:175]\nds_read_b64_tr_b16 v[216:217],v227 offset:16384\n" \
"ds_read_b64_tr_b16 v[218:219],v227 offset:20480\nM v[172:175],v[124:127],v[220:223],v[172:175]\nds_read_b64_tr_b16 v[220:221],v227 offset:24576\ns_waitcnt lgkmcnt(7)\nM v[176:179],v[104:107],v[192:195],v[176:179]\nbuffer_load_dwordx4 v240,s[56:59],s64 offen offset:0 lds\nM v[176:179],v[120:123],v[208:211],v[176:179]\nds_read_b64_tr_b16 v[222:223],v227 offset:28672\n" \
"v_add_u32 v231,8192,v230\nM v[180:183],v[108:111],v[192:195],v[180:183]\nds_read_b64_tr_b16 v[192:193],v226 offset:16896\nds_read_b64_tr_b16 v[194:195],v226 offset:20992\nM v[180:183],v[124:127],v[208:211],v[180:183]\nds_read_b64_tr_b16 v[208:209],v226 offset:25088\nds_read_b64_tr_b16 v[210:211],v226 offset:29184\nM v[184:187],v[104:107],v[196:199],v[184:187]\n" \
"buffer_load_dwordx4 v240,s[56:59],s64 offen offset:1024 lds\nbuffer_load_dwordx4 a[48:51],v240,s[52:55],s65 offen offset:0 sc1\nM v[184:187],v[120:123],v[212:215],v[184:187]\nv_accvgpr_write_b32 a194,0\nv_accvgpr_write_b32 a195,0\nM v[188:191],v[108:111],v[196:199],v[188:191]\nds_read_b64_tr_b16 v[196:197],v227 offset:16896\nds_read_b64_tr_b16 v[198:199],v227 offset:20992\n" \
"M v[188:191],v[124:127],v[212:215],v[188:191]\nds_read_b64_tr_b16 v[212:213],v227 offset:25088\ns_waitcnt lgkmcnt(8)\nM v[128:131],a[0:3],v[200:203],v[128:131]\nbuffer_load_dwordx4 v240,s[56:59],s64 offen offset:2048 lds\nM v[128:131],a[8:11],v[204:207],v[128:131]\nds_read_b64_tr_b16 v[214:215],v227 offset:29184\nv_accvgpr_write_b32 a196,0\n" \
"M v[132:135],a[4:7],v[200:203],v[132:135]\nds_read_b64_tr_b16 v[200:201],v226 offset:17408\ns_waitcnt vmcnt(22)\nM v[132:135],a[12:15],v[204:207],v[132:135]\nds_read_b64_tr_b16 v[202:203],v226 offset:21504\nds_read_b64_tr_b16 v[204:205],v226 offset:25600\nM v[136:139],a[0:3],v[216:219],v[136:139]\nbuffer_load_dwordx4 v240,s[56:59],s64 offen offset:3072 lds\n" \
"buffer_load_dwordx4 a[52:55],v240,s[52:55],s65 offen offset:1024 sc1\ns_waitcnt lgkmcnt(7)\nM v[136:139],a[8:11],v[220:223],v[136:139]\nds_read_b64_tr_b16 v[206:207],v226 offset:29696\nv_accvgpr_write_b32 a197,0\nM v[140:143],a[4:7],v[216:219],v[140:143]\nds_read_b64_tr_b16 v[216:217],v227 offset:17408\nds_read_b64_tr_b16 v[218:219],v227 offset:21504\n" \
"M v[140:143],a[12:15],v[220:223],v[140:143]\nds_read_b64_tr_b16 v[220:221],v227 offset:25600\nds_read_b64_tr_b16 v[222:223],v227 offset:29696\nM v[144:147],a[0:3],v[192:195],v[144:147]\ns_load_dwordx2 s[44:45],s[4:5],0x68\ns_mov_b32 s46,2097152\nM v[144:147],a[8:11],v[208:211],v[144:147]\nM v[148:151],a[4:7],v[192:195],v[148:151]\n" \
"ds_read_b64_tr_b16 v[192:193],v226 offset:17920\nds_read_b64_tr_b16 v[194:195],v226 offset:22016\nM v[148:151],a[12:15],v[208:211],v[148:151]\nds_read_b64_tr_b16 v[208:209],v226 offset:26112\ns_waitcnt lgkmcnt(8)\nM v[152:155],a[0:3],v[196:199],v[152:155]\nbuffer_load_dwordx4 a[56:59],v240,s[52:55],s65 offen offset:2048 sc1\nM v[152:155],a[8:11],v[212:215],v[152:155]\n" \
"ds_read_b64_tr_b16 v[210:211],v226 offset:30208\nM v[156:159],a[4:7],v[196:199],v[156:159]\nds_read_b64_tr_b16 v[196:197],v227 offset:17920\nds_read_b64_tr_b16 v[198:199],v227 offset:22016\nM v[156:159],a[12:15],v[212:215],v[156:159]\nds_read_b64_tr_b16 v[212:213],v227 offset:26112\nds_read_b64_tr_b16 v[214:215],v227 offset:30208\nM v[160:163],a[0:3],v[200:203],v[160:163]\n" \
"s_waitcnt lgkmcnt(8)\nM v[160:163],a[8:11],v[204:207],v[160:163]\nM v[164:167],a[4:7],v[200:203],v[164:167]\nds_read_b64_tr_b16 v[200:201],v226 offset:49152\nds_read_b64_tr_b16 v[202:203],v226 offset:53248\nM v[164:167],a[12:15],v[204:207],v[164:167]\nds_read_b64_tr_b16 v[204:205],v226 offset:57344\nds_read_b64_tr_b16 v[206:207],v226 offset:61440\n" \
"M v[168:171],a[0:3],v[216:219],v[168:171]\nbuffer_load_dwordx4 a[60:63],v240,s[52:55],s65 offen offset:3072 sc1\nM v[168:171],a[8:11],v[220:223],v[168:171]\ns_add_u32 s65,s10,s67\nM v[172:175],a[4:7],v[216:219],v[172:175]\nds_read_b64_tr_b16 v[216:217],v227 offset:49152\nds_read_b64_tr_b16 v[218:219],v227 offset:53248\nM v[172:175],a[12:15],v[220:223],v[172:175]\n" \
"ds_read_b64_tr_b16 v[220:221],v227 offset:57344\ns_waitcnt lgkmcnt(8)\nM v[176:179],a[0:3],v[192:195],v[176:179]\nds_read_b64_tr_b16 v[222:223],v227 offset:61440\nM v[176:179],a[8:11],v[208:211],v[176:179]\nM v[180:183],a[4:7],v[192:195],v[180:183]\nds_read_b64_tr_b16 v[192:193],v226 offset:49664\nds_read_b64_tr_b16 v[194:195],v226 offset:53760\n" \
"M v[180:183],a[12:15],v[208:211],v[180:183]\nds_read_b64_tr_b16 v[208:209],v226 offset:57856\nds_read_b64_tr_b16 v[210:211],v226 offset:61952\nM v[184:187],a[0:3],v[196:199],v[184:187]\ns_waitcnt lgkmcnt(8)\nM v[184:187],a[8:11],v[212:215],v[184:187]\nM v[188:191],a[4:7],v[196:199],v[188:191]\nds_read_b64_tr_b16 v[196:197],v227 offset:49664\n" \
"ds_read_b64_tr_b16 v[198:199],v227 offset:53760\nM v[188:191],a[12:15],v[212:215],v[188:191]\nds_read_b64_tr_b16 v[212:213],v227 offset:57856\ns_waitcnt vmcnt(24)\nM v[128:131],a[16:19],v[200:203],v[128:131]\nds_read_b64_tr_b16 v[214:215],v227 offset:61952\ns_waitcnt vmcnt(20)\nM v[128:131],a[24:27],v[204:207],v[128:131]\n" \
"M v[132:135],a[20:23],v[200:203],v[132:135]\nds_read_b64_tr_b16 v[200:201],v226 offset:50176\ns_waitcnt vmcnt(18)\nM v[132:135],a[28:31],v[204:207],v[132:135]\nds_read_b64_tr_b16 v[202:203],v226 offset:54272\ns_waitcnt lgkmcnt(8)\nM v[136:139],a[16:19],v[216:219],v[136:139]\nds_read_b64_tr_b16 v[204:205],v226 offset:58368\n" \
"ds_read_b64_tr_b16 v[206:207],v226 offset:62464\nM v[136:139],a[24:27],v[220:223],v[136:139]\nM v[140:143],a[20:23],v[216:219],v[140:143]\nds_read_b64_tr_b16 v[216:217],v227 offset:50176\nds_read_b64_tr_b16 v[218:219],v227 offset:54272\nM v[140:143],a[28:31],v[220:223],v[140:143]\nds_read_b64_tr_b16 v[220:221],v227 offset:58368\nds_read_b64_tr_b16 v[222:223],v227 offset:62464\n" \
"M v[144:147],a[16:19],v[192:195],v[144:147]\nbuffer_load_dword v248,v254,s[52:55],s80 offen sc1\ns_waitcnt lgkmcnt(8)\nM v[144:147],a[24:27],v[208:211],v[144:147]\nM v[148:151],a[20:23],v[192:195],v[148:151]\nglobal_load_ushort v64,v236,s[8:9] offset:0\nM v[148:151],a[28:31],v[208:211],v[148:151]\nds_read_b64_tr_b16 v[192:193],v226 offset:50688\n" \
"ds_read_b64_tr_b16 v[194:195],v226 offset:54784\nM v[152:155],a[16:19],v[196:199],v[152:155]\nglobal_load_ushort v80,v237,s[8:9] offset:0\nM v[152:155],a[24:27],v[212:215],v[152:155]\nds_read_b64_tr_b16 v[208:209],v226 offset:58880\nds_read_b64_tr_b16 v[210:211],v226 offset:62976\nM v[156:159],a[20:23],v[196:199],v[156:159]\ns_waitcnt vmcnt(6)\n" \
"s_barrier\nM v[156:159],a[28:31],v[212:215],v[156:159]\nglobal_load_ushort v96,v235,s[20:21] offset:0\ns_waitcnt lgkmcnt(4)\nM v[160:163],a[16:19],v[200:203],v[160:163]\nds_read_b64_tr_b16 v[196:197],v227 offset:50688\nds_read_b64_tr_b16 v[198:199],v227 offset:54784\nM v[160:163],a[24:27],v[204:207],v[160:163]\n" \
"global_load_ushort v65,v236,s[8:9] offset:32\nM v[164:167],a[20:23],v[200:203],v[164:167]\nds_read_b64_tr_b16 v[212:213],v227 offset:58880\nds_read_b64_tr_b16 v[214:215],v227 offset:62976\nM v[164:167],a[28:31],v[204:207],v[164:167]\nglobal_load_ushort v81,v237,s[8:9] offset:32\nM v[168:171],a[16:19],v[216:219],v[168:171]\nv_add_u32 v226,65536,v242\n" \
"ds_read_b64_tr_b16 v[200:201],v226 offset:0\nM v[168:171],a[24:27],v[220:223],v[168:171]\nds_read_b64_tr_b16 v[202:203],v226 offset:4096\nglobal_load_ushort v97,v235,s[20:21] offset:32\nM v[172:175],a[20:23],v[216:219],v[172:175]\nds_read_b64_tr_b16 v[204:205],v226 offset:8192\nds_read_b64_tr_b16 v[206:207],v226 offset:12288\nM v[172:175],a[28:31],v[220:223],v[172:175]\n" \
"global_load_ushort v66,v236,s[8:9] offset:64\ns_waitcnt lgkmcnt(6)\nM v[176:179],a[16:19],v[192:195],v[176:179]\nv_xor_b32 v227,32,v226\nds_read_b64_tr_b16 v[216:217],v227 offset:0\nM v[176:179],a[24:27],v[208:211],v[176:179]\nds_read_b64_tr_b16 v[218:219],v227 offset:4096\nglobal_load_ushort v82,v237,s[8:9] offset:64\n" \
"M v[180:183],a[20:23],v[192:195],v[180:183]\nds_read_b64_tr_b16 v[220:221],v227 offset:8192\nds_read_b64_tr_b16 v[222:223],v227 offset:12288\nM v[180:183],a[28:31],v[208:211],v[180:183]\nglobal_load_ushort v98,v235,s[20:21] offset:64\nM v[184:187],a[16:19],v[196:199],v[184:187]\nds_read_b64_tr_b16 v[192:193],v226 offset:512\ns_waitcnt lgkmcnt(7)\n" \
"M v[184:187],a[24:27],v[212:215],v[184:187]\nds_read_b64_tr_b16 v[194:195],v226 offset:4608\nglobal_load_ushort v67,v236,s[8:9] offset:96\nM v[188:191],a[20:23],v[196:199],v[188:191]\nds_read_b64_tr_b16 v[208:209],v226 offset:8704\nds_read_b64_tr_b16 v[210:211],v226 offset:12800\nM v[188:191],a[28:31],v[212:215],v[188:191]\nglobal_load_ushort v83,v237,s[8:9] offset:96\n" \
"M v[128:131],a[32:35],v[200:203],v[128:131]\nds_read_b64_tr_b16 v[196:197],v227 offset:512\ns_waitcnt lgkmcnt(7)\nM v[128:131],a[40:43],v[204:207],v[128:131]\nds_read_b64_tr_b16 v[198:199],v227 offset:4608\nglobal_load_ushort v99,v235,s[20:21] offset:96\nM v[132:135],a[36:39],v[200:203],v[132:135]\nds_read_b64_tr_b16 v[212:213],v227 offset:8704\n" \
"ds_read_b64_tr_b16 v[214:215],v227 offset:12800\nM v[132:135],a[44:47],v[204:207],v[132:135]\nglobal_load_ushort v68,v236,s[8:9] offset:128\nM v[136:139],a[32:35],v[216:219],v[136:139]\nds_read_b64_tr_b16 v[200:201],v226 offset:1024\ns_waitcnt lgkmcnt(7)\nM v[136:139],a[40:43],v[220:223],v[136:139]\nds_read_b64_tr_b16 v[202:203],v226 offset:5120\n" \
"global_load_ushort v84,v237,s[8:9] offset:128\nM v[140:143],a[36:39],v[216:219],v[140:143]\nds_read_b64_tr_b16 v[204:205],v226 offset:9216\nds_read_b64_tr_b16 v[206:207],v226 offset:13312\nM v[140:143],a[44:47],v[220:223],v[140:143]\nglobal_load_ushort v100,v235,s[20:21] offset:128\nM v[144:147],a[32:35],v[192:195],v[144:147]\nds_read_b64_tr_b16 v[216:217],v227 offset:1024\n" \
"s_waitcnt lgkmcnt(7)\nM v[144:147],a[40:43],v[208:211],v[144:147]\nds_read_b64_tr_b16 v[218:219],v227 offset:5120\nglobal_load_ushort v69,v236,s[8:9] offset:160\nM v[148:151],a[36:39],v[192:195],v[148:151]\nds_read_b64_tr_b16 v[220:221],v227 offset:9216\nds_read_b64_tr_b16 v[222:223],v227 offset:13312\nM v[148:151],a[44:47],v[208:211],v[148:151]\n" \
"global_load_ushort v85,v237,s[8:9] offset:160\nM v[152:155],a[32:35],v[196:199],v[152:155]\nds_read_b64_tr_b16 v[192:193],v226 offset:1536\ns_waitcnt lgkmcnt(7)\nM v[152:155],a[40:43],v[212:215],v[152:155]\nds_read_b64_tr_b16 v[194:195],v226 offset:5632\nglobal_load_ushort v101,v235,s[20:21] offset:160\nM v[156:159],a[36:39],v[196:199],v[156:159]\n" \
"ds_read_b64_tr_b16 v[208:209],v226 offset:9728\nds_read_b64_tr_b16 v[210:211],v226 offset:13824\nM v[156:159],a[44:47],v[212:215],v[156:159]\nglobal_load_ushort v70,v236,s[8:9] offset:192\nM v[160:163],a[32:35],v[200:203],v[160:163]\nds_read_b64_tr_b16 v[196:197],v227 offset:1536\ns_waitcnt lgkmcnt(7)\nM v[160:163],a[40:43],v[204:207],v[160:163]\n" \
"ds_read_b64_tr_b16 v[198:199],v227 offset:5632\nglobal_load_ushort v86,v237,s[8:9] offset:192\nM v[164:167],a[36:39],v[200:203],v[164:167]\nds_read_b64_tr_b16 v[212:213],v227 offset:9728\nds_read_b64_tr_b16 v[214:215],v227 offset:13824\nM v[164:167],a[44:47],v[204:207],v[164:167]\ns_waitcnt vmcnt(20)\nv_cmp_eq_u32 vcc,0,v248\n" \
"global_load_ushort v102,v235,s[20:21] offset:192\nM v[168:171],a[32:35],v[216:219],v[168:171]\ns_cbranch_vccnz .Lps7\n.Lpb7:\nbuffer_load_dword v248,v251,s[52:55],s79 offen sc1\ns_waitcnt lgkmcnt(4)\nM v[168:171],a[40:43],v[220:223],v[168:171]\nds_read_b64_tr_b16 v[200:201],v226 offset:32768\n" \
"ds_read_b64_tr_b16 v[202:203],v226 offset:36864\nglobal_load_ushort v71,v236,s[8:9] offset:224\nM v[172:175],a[36:39],v[216:219],v[172:175]\nds_read_b64_tr_b16 v[204:205],v226 offset:40960\nds_read_b64_tr_b16 v[206:207],v226 offset:45056\nM v[172:175],a[44:47],v[220:223],v[172:175]\nglobal_load_ushort v87,v237,s[8:9] offset:224\nM v[176:179],a[32:35],v[192:195],v[176:179]\n" \
"ds_read_b64_tr_b16 v[216:217],v227 offset:32768\nds_read_b64_tr_b16 v[218:219],v227 offset:36864\nM v[176:179],a[40:43],v[208:211],v[176:179]\nglobal_load_ushort v103,v235,s[20:21] offset:224\nM v[180:183],a[36:39],v[192:195],v[180:183]\nds_read_b64_tr_b16 v[220:221],v227 offset:40960\nds_read_b64_tr_b16 v[222:223],v227 offset:45056\nM v[180:183],a[44:47],v[208:211],v[180:183]\n" \
"ds_read_b64_tr_b16 v[192:193],v226 offset:33280\ns_waitcnt lgkmcnt(9)\nM v[184:187],a[32:35],v[196:199],v[184:187]\nds_read_b64_tr_b16 v[194:195],v226 offset:37376\nds_read_b64_tr_b16 v[208:209],v226 offset:41472\nM v[184:187],a[40:43],v[212:215],v[184:187]\nds_read_b64_tr_b16 v[210:211],v226 offset:45568\nds_read_b64 v[112:113],v239 offset:0\n" \
"M v[188:191],a[36:39],v[196:199],v[188:191]\nds_read_b64_tr_b16 v[196:197],v227 offset:33280\nds_read_b64_tr_b16 v[198:199],v227 offset:37376\nM v[188:191],a[44:47],v[212:215],v[188:191]\nds_read_b64_tr_b16 v[212:213],v227 offset:41472\ns_waitcnt lgkmcnt(11)\nM v[128:131],a[48:51],v[200:203],v[128:131]\nds_read_b64_tr_b16 v[214:215],v227 offset:45568\n" \
"ds_read_b64 v[114:115],v239 offset:4096\nM v[128:131],a[56:59],v[204:207],v[128:131]\nds_read_b64 v[116:117],v239 offset:8192\nds_read_b64 v[118:119],v239 offset:12288\nM v[132:135],a[52:55],v[200:203],v[132:135]\nds_read_b64_tr_b16 v[200:201],v226 offset:33792\nds_read_b64_tr_b16 v[202:203],v226 offset:37888\nM v[132:135],a[60:63],v[204:207],v[132:135]\n" \
"ds_read_b64_tr_b16 v[204:205],v226 offset:41984\ns_waitcnt lgkmcnt(13)\nM v[136:139],a[48:51],v[216:219],v[136:139]\nds_read_b64_tr_b16 v[206:207],v226 offset:46080\nM v[136:139],a[56:59],v[220:223],v[136:139]\nM v[140:143],a[52:55],v[216:219],v[140:143]\nds_read_b64_tr_b16 v[216:217],v227 offset:33792\nds_read_b64_tr_b16 v[218:219],v227 offset:37888\n" \
"M v[140:143],a[60:63],v[220:223],v[140:143]\nds_read_b64_tr_b16 v[220:221],v227 offset:41984\nds_read_b64_tr_b16 v[222:223],v227 offset:46080\nM v[144:147],a[48:51],v[192:195],v[144:147]\ns_waitcnt lgkmcnt(11)\nM v[144:147],a[56:59],v[208:211],v[144:147]\nv_cvt_pk_bf16_f32 v128,v128,v136\nv_cvt_pk_bf16_f32 v129,v129,v137\n" \
"M v[148:151],a[52:55],v[192:195],v[148:151]\nds_read_b64_tr_b16 v[192:193],v226 offset:34304\nds_read_b64_tr_b16 v[194:195],v226 offset:38400\nM v[148:151],a[60:63],v[208:211],v[148:151]\nds_read_b64_tr_b16 v[208:209],v226 offset:42496\nds_read_b64_tr_b16 v[210:211],v226 offset:46592\nM v[152:155],a[48:51],v[196:199],v[152:155]\nv_add_u32 v226,131072,v242\n" \
"v_cvt_pk_bf16_f32 v130,v130,v138\nM v[152:155],a[56:59],v[212:215],v[152:155]\nv_cvt_pk_bf16_f32 v131,v131,v139\nv_min_u32 v136,-2,v128\nM v[156:159],a[52:55],v[196:199],v[156:159]\nds_read_b64_tr_b16 v[196:197],v227 offset:34304\nds_read_b64_tr_b16 v[198:199],v227 offset:38400\nM v[156:159],a[60:63],v[212:215],v[156:159]\n" \
"ds_read_b64_tr_b16 v[212:213],v227 offset:42496\ns_waitcnt lgkmcnt(7)\nM v[160:163],a[48:51],v[200:203],v[160:163]\nds_read_b64_tr_b16 v[214:215],v227 offset:46592\nv_xor_b32 v227,32,v226\nM v[160:163],a[56:59],v[204:207],v[160:163]\nv_cvt_pk_bf16_f32 v144,v144,v152\nv_min_u32 v137,-2,v144\n" \
"M v[164:167],a[52:55],v[200:203],v[164:167]\nds_read_b64_tr_b16 v[200:201],v226 offset:0\nds_read_b64_tr_b16 v[202:203],v226 offset:4096\nM v[164:167],a[60:63],v[204:207],v[164:167]\nds_read_b64_tr_b16 v[204:205],v226 offset:8192\nds_read_b64_tr_b16 v[206:207],v226 offset:12288\nM v[168:171],a[48:51],v[216:219],v[168:171]\nv_cvt_pk_bf16_f32 v145,v145,v153\n" \
"v_cvt_pk_bf16_f32 v146,v146,v154\nM v[168:171],a[56:59],v[220:223],v[168:171]\nv_cvt_pk_bf16_f32 v147,v147,v155\nv_min_u32 v152,-2,v129\nM v[172:175],a[52:55],v[216:219],v[172:175]\ns_waitcnt vmcnt(3)\nv_cmp_eq_u32 vcc,0,v248\nM v[172:175],a[60:63],v[220:223],v[172:175]\n" \
"s_cbranch_vccnz .Lps8\n.Lpb8:\ns_waitcnt lgkmcnt(4)\nM v[176:179],a[48:51],v[192:195],v[176:179]\nbuffer_load_dwordx4 a[64:67],v240,s[52:55],s65 offen offset:0 sc1\nM v[176:179],a[56:59],v[208:211],v[176:179]\nds_read_b64_tr_b16 v[216:217],v227 offset:0\nds_read_b64_tr_b16 v[218:219],v227 offset:4096\n" \
"M v[180:183],a[52:55],v[192:195],v[180:183]\nds_read_b64_tr_b16 v[220:221],v227 offset:8192\nds_read_b64_tr_b16 v[222:223],v227 offset:12288\nM v[180:183],a[60:63],v[208:211],v[180:183]\nds_read_b64_tr_b16 v[192:193],v226 offset:512\nds_read_b64_tr_b16 v[194:195],v226 offset:4608\nM v[184:187],a[48:51],v[196:199],v[184:187]\nbuffer_load_dwordx4 a[68:71],v240,s[52:55],s65 offen offset:1024 sc1\n" \
"M v[184:187],a[56:59],v[212:215],v[184:187]\nds_read_b64_tr_b16 v[208:209],v226 offset:8704\nds_read_b64_tr_b16 v[210:211],v226 offset:12800\nM v[188:191],a[52:55],v[196:199],v[188:191]\nds_read_b64_tr_b16 v[196:197],v227 offset:512\nds_read_b64_tr_b16 v[198:199],v227 offset:4608\nM v[188:191],a[60:63],v[212:215],v[188:191]\nds_read_b64_tr_b16 v[212:213],v227 offset:8704\n" \
"s_waitcnt lgkmcnt(11)\nM v[8:11],v[0:3],v[200:203],0\nbuffer_load_dwordx4 a[72:75],v240,s[52:55],s65 offen offset:2048 sc1\nM v[8:11],v[16:19],v[204:207],v[8:11]\nds_read_b64_tr_b16 v[214:215],v227 offset:12800\nv_cvt_pk_bf16_f32 v160,v160,v168\nM v[12:15],v[4:7],v[200:203],0\nds_read_b64_tr_b16 v[200:201],v226 offset:1024\n" \
"ds_read_b64_tr_b16 v[202:203],v226 offset:5120\nM v[12:15],v[20:23],v[204:207],v[12:15]\nds_read_b64_tr_b16 v[204:205],v226 offset:9216\ns_waitcnt lgkmcnt(9)\nM v[24:27],v[0:3],v[216:219],0\nbuffer_load_dwordx4 a[76:79],v240,s[52:55],s65 offen offset:3072 sc1\nM v[24:27],v[16:19],v[220:223],v[24:27]\nds_read_b64_tr_b16 v[206:207],v226 offset:13312\n" \
"s_add_u32 s65,s65,4096\nM v[28:31],v[4:7],v[216:219],0\nds_read_b64_tr_b16 v[216:217],v227 offset:1024\nds_read_b64_tr_b16 v[218:219],v227 offset:5120\nM v[28:31],v[20:23],v[220:223],v[28:31]\nds_read_b64_tr_b16 v[220:221],v227 offset:9216\nds_read_b64_tr_b16 v[222:223],v227 offset:13312\nM v[40:43],v[0:3],v[192:195],0\n" \
"buffer_load_dwordx4 a[80:83],v240,s[52:55],s65 offen offset:0 sc1\ns_waitcnt lgkmcnt(8)\nM v[40:43],v[16:19],v[208:211],v[40:43]\nv_cvt_pk_bf16_f32 v176,v176,v184\nv_min_u32 v138,-2,v160\nM v[44:47],v[4:7],v[192:195],0\nds_read_b64_tr_b16 v[192:193],v226 offset:1536\nds_read_b64_tr_b16 v[194:195],v226 offset:5632\n" \
"M v[44:47],v[20:23],v[208:211],v[44:47]\nv_min_u32 v139,-2,v176\nds_read_b64_tr_b16 v[208:209],v226 offset:9728\nM v[56:59],v[0:3],v[196:199],0\nbuffer_load_dwordx4 a[84:87],v240,s[52:55],s65 offen offset:1024 sc1\nM v[56:59],v[16:19],v[212:215],v[56:59]\nds_read_b64_tr_b16 v[210:211],v226 offset:13824\nv_cvt_pk_bf16_f32 v161,v161,v169\n" \
"M v[60:63],v[4:7],v[196:199],0\nbuffer_store_dwordx4 v[136:139],v253,s[52:55],s33 offen offset:0 sc1\nM v[60:63],v[20:23],v[212:215],v[60:63]\nds_read_b64_tr_b16 v[196:197],v227 offset:1536\ns_waitcnt lgkmcnt(5)\nM v[72:75],v[0:3],v[200:203],0\nbuffer_load_dwordx4 a[88:91],v240,s[52:55],s65 offen offset:2048 sc1\nM v[72:75],v[16:19],v[204:207],v[72:75]\n" \
"ds_read_b64_tr_b16 v[198:199],v227 offset:5632\nds_read_b64_tr_b16 v[212:213],v227 offset:9728\nM v[76:79],v[4:7],v[200:203],0\nds_read_b64_tr_b16 v[214:215],v227 offset:13824\nds_read_b64_tr_b16 v[200:201],v226 offset:16384\nM v[76:79],v[20:23],v[204:207],v[76:79]\nds_read_b64_tr_b16 v[202:203],v226 offset:20480\nds_read_b64_tr_b16 v[204:205],v226 offset:24576\n" \
"M v[88:91],v[0:3],v[216:219],0\nbuffer_load_dwordx4 a[92:95],v240,s[52:55],s65 offen offset:3072 sc1\nM v[88:91],v[16:19],v[220:223],v[88:91]\nds_read_b64_tr_b16 v[206:207],v226 offset:28672\ns_add_u32 s65,s11,s67\nM v[92:95],v[4:7],v[216:219],0\nds_read_b64_tr_b16 v[216:217],v227 offset:16384\nds_read_b64_tr_b16 v[218:219],v227 offset:20480\n" \
"M v[92:95],v[20:23],v[220:223],v[92:95]\nds_read_b64_tr_b16 v[220:221],v227 offset:24576\ns_waitcnt lgkmcnt(10)\nM v[104:107],v[0:3],v[192:195],0\nbuffer_load_dwordx4 a[96:99],v240,s[52:55],s65 offen offset:0 sc1\nM v[104:107],v[16:19],v[208:211],v[104:107]\nds_read_b64_tr_b16 v[222:223],v227 offset:28672\nv_cvt_pk_bf16_f32 v177,v177,v185\n" \
"M v[108:111],v[4:7],v[192:195],0\nds_read_b64_tr_b16 v[192:193],v226 offset:16896\nds_read_b64_tr_b16 v[194:195],v226 offset:20992\nM v[108:111],v[20:23],v[208:211],v[108:111]\nv_min_u32 v153,-2,v145\ns_waitcnt lgkmcnt(7)\nM v[120:123],v[0:3],v[196:199],0\nbuffer_load_dwordx4 a[100:103],v240,s[52:55],s65 offen offset:1024 sc1\n" \
"M v[120:123],v[16:19],v[212:215],v[120:123]\nv_min_u32 v154,-2,v161\nv_min_u32 v155,-2,v177\nM v[124:127],v[4:7],v[196:199],0\nds_read_b64_tr_b16 v[208:209],v226 offset:25088\nds_read_b64_tr_b16 v[210:211],v226 offset:29184\nbuffer_store_dwordx4 v[152:155],v253,s[52:55],s33 offen offset:1024 sc1\nM v[124:127],v[20:23],v[212:215],v[124:127]\n" \
"ds_read_b64_tr_b16 v[196:197],v227 offset:16896\nds_read_b64_tr_b16 v[198:199],v227 offset:20992\nM v[8:11],v[32:35],v[200:203],v[8:11]\nbuffer_load_dwordx4 a[104:107],v240,s[52:55],s65 offen offset:2048 sc1\ns_waitcnt lgkmcnt(6)\nM v[8:11],v[48:51],v[204:207],v[8:11]\nds_read_b64_tr_b16 v[212:213],v227 offset:25088\nds_read_b64_tr_b16 v[214:215],v227 offset:29184\n" \
"M v[12:15],v[36:39],v[200:203],v[12:15]\nds_read_b64_tr_b16 v[200:201],v226 offset:17408\nds_read_b64_tr_b16 v[202:203],v226 offset:21504\nM v[12:15],v[52:55],v[204:207],v[12:15]\nds_read_b64_tr_b16 v[204:205],v226 offset:25600\nds_read_b64_tr_b16 v[206:207],v226 offset:29696\nM v[24:27],v[32:35],v[216:219],v[24:27]\nbuffer_load_dwordx4 a[108:111],v240,s[52:55],s65 offen offset:3072 sc1\n" \
"M v[24:27],v[48:51],v[220:223],v[24:27]\ns_add_u32 s65,s65,4096\nv_cvt_pk_bf16_f32 v162,v162,v170\nM v[28:31],v[36:39],v[216:219],v[28:31]\nds_read_b64_tr_b16 v[216:217],v227 offset:17408\nds_read_b64_tr_b16 v[218:219],v227 offset:21504\nM v[28:31],v[52:55],v[220:223],v[28:31]\nds_read_b64_tr_b16 v[220:221],v227 offset:25600\n" \
"s_waitcnt lgkmcnt(9)\nM v[40:43],v[32:35],v[192:195],v[40:43]\nbuffer_load_dwordx4 a[112:115],v240,s[52:55],s65 offen offset:0 sc1\nM v[40:43],v[48:51],v[208:211],v[40:43]\nds_read_b64_tr_b16 v[222:223],v227 offset:29696\nv_cvt_pk_bf16_f32 v163,v163,v171\nM v[44:47],v[36:39],v[192:195],v[44:47]\nds_read_b64_tr_b16 v[192:193],v226 offset:17920\n" \
"ds_read_b64_tr_b16 v[194:195],v226 offset:22016\nM v[44:47],v[52:55],v[208:211],v[44:47]\nv_cvt_pk_bf16_f32 v178,v178,v186\nv_min_u32 v168,-2,v130\nM v[56:59],v[32:35],v[196:199],v[56:59]\nbuffer_load_dwordx4 a[116:119],v240,s[52:55],s65 offen offset:1024 sc1\ns_waitcnt lgkmcnt(6)\nM v[56:59],v[48:51],v[212:215],v[56:59]\n" \
"v_min_u32 v169,-2,v146\nv_min_u32 v170,-2,v162\nM v[60:63],v[36:39],v[196:199],v[60:63]\nv_min_u32 v171,-2,v178\nds_read_b64_tr_b16 v[208:209],v226 offset:26112\nds_read_b64_tr_b16 v[210:211],v226 offset:30208\nbuffer_store_dwordx4 v[168:171],v253,s[52:55],s33 offen offset:2048 sc1\nM v[60:63],v[52:55],v[212:215],v[60:63]\n" \
"ds_read_b64_tr_b16 v[196:197],v227 offset:17920\nds_read_b64_tr_b16 v[198:199],v227 offset:22016\nM v[72:75],v[32:35],v[200:203],v[72:75]\nbuffer_load_dwordx4 a[120:123],v240,s[52:55],s65 offen offset:2048 sc1\nM v[72:75],v[48:51],v[204:207],v[72:75]\nds_read_b64_tr_b16 v[212:213],v227 offset:26112\nds_read_b64_tr_b16 v[214:215],v227 offset:30208\nM v[76:79],v[36:39],v[200:203],v[76:79]\n" \
"v_add_u32 v226,0,v242\nds_read_b64_tr_b16 v[200:201],v226 offset:0\nM v[76:79],v[52:55],v[204:207],v[76:79]\nds_read_b64_tr_b16 v[202:203],v226 offset:4096\ns_waitcnt lgkmcnt(8)\nM v[88:91],v[32:35],v[216:219],v[88:91]\nbuffer_load_dwordx4 a[124:127],v240,s[52:55],s65 offen offset:3072 sc1\nM v[88:91],v[48:51],v[220:223],v[88:91]\n" \
"ds_read_b64_tr_b16 v[204:205],v226 offset:8192\nds_read_b64_tr_b16 v[206:207],v226 offset:12288\nM v[92:95],v[36:39],v[216:219],v[92:95]\ns_add_u32 s65,s14,s67\nv_xor_b32 v227,32,v226\nM v[92:95],v[52:55],v[220:223],v[92:95]\nds_read_b64_tr_b16 v[216:217],v227 offset:0\nds_read_b64_tr_b16 v[218:219],v227 offset:4096\n" \
"M v[104:107],v[32:35],v[192:195],v[104:107]\nbuffer_load_dwordx4 a[128:131],v240,s[52:55],s65 offen offset:0 sc1\ns_waitcnt lgkmcnt(6)\nM v[104:107],v[48:51],v[208:211],v[104:107]\nds_read_b64_tr_b16 v[220:221],v227 offset:8192\nds_read_b64_tr_b16 v[222:223],v227 offset:12288\nM v[108:111],v[36:39],v[192:195],v[108:111]\nv_cvt_pk_bf16_f32 v179,v179,v187\n" \
"ds_read_b64_tr_b16 v[192:193],v226 offset:512\nM v[108:111],v[52:55],v[208:211],v[108:111]\nds_read_b64_tr_b16 v[194:195],v226 offset:4608\nv_min_u32 v184,-2,v131\nM v[120:123],v[32:35],v[196:199],v[120:123]\nbuffer_load_dwordx4 a[132:135],v240,s[52:55],s65 offen offset:1024 sc1\nM v[120:123],v[48:51],v[212:215],v[120:123]\nv_min_u32 v185,-2,v147\n" \
"v_min_u32 v186,-2,v163\nM v[124:127],v[36:39],v[196:199],v[124:127]\nv_min_u32 v187,-2,v179\nbuffer_store_dwordx4 v[184:187],v253,s[52:55],s33 offen offset:3072 sc1\nds_read_b64_tr_b16 v[208:209],v226 offset:8704\nds_read_b64_tr_b16 v[210:211],v226 offset:12800\nM v[124:127],v[52:55],v[212:215],v[124:127]\nds_read_b64_tr_b16 v[196:197],v227 offset:512\n" \
"s_waitcnt vmcnt(18) lgkmcnt(7)\nM v[8:11],a[64:67],v[200:203],v[8:11]\nbuffer_load_dwordx4 a[136:139],v240,s[52:55],s65 offen offset:2048 sc1\nM v[8:11],a[72:75],v[204:207],v[8:11]\nds_read_b64_tr_b16 v[198:199],v227 offset:4608\nds_read_b64_tr_b16 v[212:213],v227 offset:8704\nM v[12:15],a[68:71],v[200:203],v[12:15]\nds_read_b64_tr_b16 v[214:215],v227 offset:12800\n" \
"ds_read_b64_tr_b16 v[200:201],v226 offset:1024\nM v[12:15],a[76:79],v[204:207],v[12:15]\nds_read_b64_tr_b16 v[202:203],v226 offset:5120\nds_read_b64_tr_b16 v[204:205],v226 offset:9216\nM v[24:27],a[64:67],v[216:219],v[24:27]\nbuffer_load_dwordx4 a[140:143],v240,s[52:55],s65 offen offset:3072 sc1\ns_waitcnt lgkmcnt(9)\nM v[24:27],a[72:75],v[220:223],v[24:27]\n" \
"ds_read_b64_tr_b16 v[206:207],v226 offset:13312\ns_add_u32 s65,s65,4096\nM v[28:31],a[68:71],v[216:219],v[28:31]\nds_read_b64_tr_b16 v[216:217],v227 offset:1024\nds_read_b64_tr_b16 v[218:219],v227 offset:5120\nM v[28:31],a[76:79],v[220:223],v[28:31]\nds_read_b64_tr_b16 v[220:221],v227 offset:9216\nds_read_b64_tr_b16 v[222:223],v227 offset:13312\n" \
"M v[40:43],a[64:67],v[192:195],v[40:43]\nbuffer_load_dwordx4 a[144:147],v240,s[52:55],s65 offen offset:0 sc1\ns_waitcnt lgkmcnt(9)\nM v[40:43],a[72:75],v[208:211],v[40:43]\nv_cvt_pk_bf16_f32 v132,v132,v140\nv_cvt_pk_bf16_f32 v133,v133,v141\nM v[44:47],a[68:71],v[192:195],v[44:47]\nv_cvt_pk_bf16_f32 v134,v134,v142\n" \
"v_cvt_pk_bf16_f32 v135,v135,v143\nM v[44:47],a[76:79],v[208:211],v[44:47]\nv_cvt_pk_bf16_f32 v148,v148,v156\nv_cvt_pk_bf16_f32 v164,v164,v172\nM v[56:59],a[64:67],v[196:199],v[56:59]\nbuffer_load_dwordx4 a[148:151],v240,s[52:55],s65 offen offset:1024 sc1\ns_waitcnt lgkmcnt(4)\nM v[56:59],a[72:75],v[212:215],v[56:59]\n" \
"v_cvt_pk_bf16_f32 v180,v180,v188\nds_read_b64_tr_b16 v[192:193],v226 offset:1536\nds_read_b64_tr_b16 v[194:195],v226 offset:5632\nM v[60:63],a[68:71],v[196:199],v[60:63]\nv_min_u32 v140,-2,v132\nv_min_u32 v141,-2,v148\nv_min_u32 v142,-2,v164\nv_min_u32 v143,-2,v180\n" \
"buffer_store_dwordx4 v[140:143],v253,s[52:55],s38 offen offset:0 sc1\nds_read_b64_tr_b16 v[208:209],v226 offset:9728\nds_read_b64_tr_b16 v[210:211],v226 offset:13824\nM v[60:63],a[76:79],v[212:215],v[60:63]\nds_read_b64_tr_b16 v[196:197],v227 offset:1536\nds_read_b64_tr_b16 v[198:199],v227 offset:5632\nM v[72:75],a[64:67],v[200:203],v[72:75]\nbuffer_load_dwordx4 a[152:155],v240,s[52:55],s65 offen offset:2048 sc1\n" \
"M v[72:75],a[72:75],v[204:207],v[72:75]\nds_read_b64_tr_b16 v[212:213],v227 offset:9728\nds_read_b64_tr_b16 v[214:215],v227 offset:13824\nM v[76:79],a[68:71],v[200:203],v[76:79]\nds_read_b64_tr_b16 v[200:201],v226 offset:32768\nds_read_b64_tr_b16 v[202:203],v226 offset:36864\nM v[76:79],a[76:79],v[204:207],v[76:79]\nds_read_b64_tr_b16 v[204:205],v226 offset:40960\n" \
"s_waitcnt lgkmcnt(9)\nM v[88:91],a[64:67],v[216:219],v[88:91]\nbuffer_load_dwordx4 a[156:159],v240,s[52:55],s65 offen offset:3072 sc1\nM v[88:91],a[72:75],v[220:223],v[88:91]\nds_read_b64_tr_b16 v[206:207],v226 offset:45056\nv_cvt_pk_bf16_f32 v149,v149,v157\nM v[92:95],a[68:71],v[216:219],v[92:95]\nds_read_b64_tr_b16 v[216:217],v227 offset:32768\n" \
"ds_read_b64_tr_b16 v[218:219],v227 offset:36864\nM v[92:95],a[76:79],v[220:223],v[92:95]\nds_read_b64_tr_b16 v[220:221],v227 offset:40960\nds_read_b64_tr_b16 v[222:223],v227 offset:45056\nM v[104:107],a[64:67],v[192:195],v[104:107]\nv_cvt_pk_bf16_f32 v150,v150,v158\ns_waitcnt lgkmcnt(9)\nM v[104:107],a[72:75],v[208:211],v[104:107]\n" \
"v_cvt_pk_bf16_f32 v151,v151,v159\nv_cvt_pk_bf16_f32 v165,v165,v173\nM v[108:111],a[68:71],v[192:195],v[108:111]\nv_cvt_pk_bf16_f32 v181,v181,v189\nds_read_b64_tr_b16 v[192:193],v226 offset:33280\nM v[108:111],a[76:79],v[208:211],v[108:111]\nds_read_b64_tr_b16 v[194:195],v226 offset:37376\nv_min_u32 v156,-2,v133\n" \
"M v[120:123],a[64:67],v[196:199],v[120:123]\nv_min_u32 v157,-2,v149\ns_waitcnt lgkmcnt(6)\nM v[120:123],a[72:75],v[212:215],v[120:123]\nv_min_u32 v158,-2,v165\nv_min_u32 v159,-2,v181\nM v[124:127],a[68:71],v[196:199],v[124:127]\nbuffer_store_dwordx4 v[156:159],v253,s[52:55],s38 offen offset:1024 sc1\n" \
"ds_read_b64_tr_b16 v[208:209],v226 offset:41472\nds_read_b64_tr_b16 v[210:211],v226 offset:45568\nM v[124:127],a[76:79],v[212:215],v[124:127]\nds_read_b64_tr_b16 v[196:197],v227 offset:33280\ns_waitcnt vmcnt(24)\nM v[8:11],a[80:83],v[200:203],v[8:11]\nds_read_b64_tr_b16 v[198:199],v227 offset:37376\ns_waitcnt vmcnt(19)\n" \
"M v[8:11],a[88:91],v[204:207],v[8:11]\nds_read_b64_tr_b16 v[212:213],v227 offset:41472\nds_read_b64_tr_b16 v[214:215],v227 offset:45568\nM v[12:15],a[84:87],v[200:203],v[12:15]\nds_read_b64_tr_b16 v[200:201],v226 offset:33792\nds_read_b64_tr_b16 v[202:203],v226 offset:37888\nM v[12:15],a[92:95],v[204:207],v[12:15]\nds_read_b64_tr_b16 v[204:205],v226 offset:41984\n" \
"s_waitcnt lgkmcnt(9)\nM v[24:27],a[80:83],v[216:219],v[24:27]\nds_read_b64_tr_b16 v[206:207],v226 offset:46080\nv_cvt_pk_bf16_f32 v166,v166,v174\nM v[24:27],a[88:91],v[220:223],v[24:27]\nv_cvt_pk_bf16_f32 v167,v167,v175\nv_cvt_pk_bf16_f32 v182,v182,v190\nM v[28:31],a[84:87],v[216:219],v[28:31]\n" \
"ds_read_b64_tr_b16 v[216:217],v227 offset:33792\nds_read_b64_tr_b16 v[218:219],v227 offset:37888\nM v[28:31],a[92:95],v[220:223],v[28:31]\nds_read_b64_tr_b16 v[220:221],v227 offset:41984\nds_read_b64_tr_b16 v[222:223],v227 offset:46080\nM v[40:43],a[80:83],v[192:195],v[40:43]\nv_min_u32 v172,-2,v134\ns_waitcnt lgkmcnt(9)\n" \
"M v[40:43],a[88:91],v[208:211],v[40:43]\nv_min_u32 v173,-2,v150\nv_min_u32 v174,-2,v166\nM v[44:47],a[84:87],v[192:195],v[44:47]\nds_read_b64_tr_b16 v[192:193],v226 offset:34304\nds_read_b64_tr_b16 v[194:195],v226 offset:38400\nM v[44:47],a[92:95],v[208:211],v[44:47]\nv_min_u32 v175,-2,v182\n" \
"ds_read_b64_tr_b16 v[208:209],v226 offset:42496\nM v[56:59],a[80:83],v[196:199],v[56:59]\nds_read_b64_tr_b16 v[210:211],v226 offset:46592\ns_waitcnt lgkmcnt(8)\nM v[56:59],a[88:91],v[212:215],v[56:59]\nv_cvt_pk_bf16_f32 v183,v183,v191\nv_min_u32 v188,-2,v135\nM v[60:63],a[84:87],v[196:199],v[60:63]\n" \
"buffer_store_dwordx4 v[172:175],v253,s[52:55],s38 offen offset:2048 sc1\nM v[60:63],a[92:95],v[212:215],v[60:63]\nds_read_b64_tr_b16 v[196:197],v227 offset:34304\nds_read_b64_tr_b16 v[198:199],v227 offset:38400\nM v[72:75],a[80:83],v[200:203],v[72:75]\nds_read_b64_tr_b16 v[212:213],v227 offset:42496\nds_read_b64_tr_b16 v[214:215],v227 offset:46592\nM v[72:75],a[88:91],v[204:207],v[72:75]\n" \
"v_min_u32 v189,-2,v151\nv_min_u32 v190,-2,v167\nM v[76:79],a[84:87],v[200:203],v[76:79]\nds_read_b64_tr_b16 v[200:201],v226 offset:16384\nds_read_b64_tr_b16 v[202:203],v226 offset:20480\nM v[76:79],a[92:95],v[204:207],v[76:79]\nds_read_b64_tr_b16 v[204:205],v226 offset:24576\ns_waitcnt lgkmcnt(7)\n" \
"M v[88:91],a[80:83],v[216:219],v[88:91]\nds_read_b64_tr_b16 v[206:207],v226 offset:28672\nv_min_u32 v191,-2,v183\nM v[88:91],a[88:91],v[220:223],v[88:91]\nds_read_b128 v[0:3],v238 offset:0\nds_read_b128 v[4:7],v238 offset:16\nM v[92:95],a[84:87],v[216:219],v[92:95]\nds_read_b64_tr_b16 v[216:217],v227 offset:16384\n" \
"v_cmp_eq_u32 vcc,v228,v229\nM v[92:95],a[92:95],v[220:223],v[92:95]\nds_read_b64_tr_b16 v[218:219],v227 offset:20480\nds_read_b64_tr_b16 v[220:221],v227 offset:24576\nM v[104:107],a[80:83],v[192:195],v[104:107]\nds_read_b64_tr_b16 v[222:223],v227 offset:28672\nds_read_b128 v[16:19],v238 offset:4096\nM v[104:107],a[88:91],v[208:211],v[104:107]\n" \
"ds_read_b128 v[20:23],v238 offset:4112\nv_and_b32 v228,1,v249\nM v[108:111],a[84:87],v[192:195],v[108:111]\nds_read_b64_tr_b16 v[192:193],v226 offset:16896\nv_lshlrev_b32 v228,4,v228\nM v[108:111],a[92:95],v[208:211],v[108:111]\nds_read_b64_tr_b16 v[194:195],v226 offset:20992\ns_waitcnt lgkmcnt(10)\n" \
"M v[120:123],a[80:83],v[196:199],v[120:123]\nds_read_b64_tr_b16 v[208:209],v226 offset:25088\nds_read_b64_tr_b16 v[210:211],v226 offset:29184\nM v[120:123],a[88:91],v[212:215],v[120:123]\nds_read_b128 v[32:35],v238 offset:8192\nds_read_b128 v[36:39],v238 offset:8208\nM v[124:127],a[84:87],v[196:199],v[124:127]\nbuffer_store_dwordx4 v[188:191],v253,s[52:55],s38 offen offset:3072 sc1\n" \
"M v[124:127],a[92:95],v[212:215],v[124:127]\nds_read_b64_tr_b16 v[196:197],v227 offset:16896\nds_read_b64_tr_b16 v[198:199],v227 offset:20992\nM v[8:11],a[96:99],v[200:203],v[8:11]\nds_read_b64_tr_b16 v[212:213],v227 offset:25088\ns_waitcnt vmcnt(16)\nM v[8:11],a[104:107],v[204:207],v[8:11]\nds_read_b64_tr_b16 v[214:215],v227 offset:29184\n" \
"ds_read_b128 v[48:51],v238 offset:12288\nM v[12:15],a[100:103],v[200:203],v[12:15]\nds_read_b64_tr_b16 v[200:201],v226 offset:17408\nds_read_b64_tr_b16 v[202:203],v226 offset:21504\nM v[12:15],a[108:111],v[204:207],v[12:15]\nds_read_b64_tr_b16 v[204:205],v226 offset:25600\ns_waitcnt lgkmcnt(11)\nM v[24:27],a[96:99],v[216:219],v[24:27]\n" \
"ds_read_b64_tr_b16 v[206:207],v226 offset:29696\nds_read_b128 v[52:55],v238 offset:12304\nM v[24:27],a[104:107],v[220:223],v[24:27]\nv_mov_b32 v229,0x3f80\nv_lshlrev_b32 v228,v228,v229\nM v[28:31],a[100:103],v[216:219],v[28:31]\nds_read_b64_tr_b16 v[216:217],v227 offset:17408\nds_read_b64_tr_b16 v[218:219],v227 offset:21504\n" \
"M v[28:31],a[108:111],v[220:223],v[28:31]\nds_read_b64_tr_b16 v[220:221],v227 offset:25600\nds_read_b64_tr_b16 v[222:223],v227 offset:29696\nM v[40:43],a[96:99],v[192:195],v[40:43]\nv_cndmask_b32 v228,0,v228,vcc\ns_waitcnt lgkmcnt(10)\nM v[40:43],a[104:107],v[208:211],v[40:43]\nv_and_b32 v229,2,v249\n" \
"v_cmp_eq_u32 vcc,0,v229\nM v[44:47],a[100:103],v[192:195],v[44:47]\nds_read_b64_tr_b16 v[192:193],v226 offset:17920\nds_read_b64_tr_b16 v[194:195],v226 offset:22016\nM v[44:47],a[108:111],v[208:211],v[44:47]\nds_read_b64_tr_b16 v[208:209],v226 offset:26112\nds_read_b64_tr_b16 v[210:211],v226 offset:30208\nM v[56:59],a[96:99],v[196:199],v[56:59]\n" \
"v_cndmask_b32 v229,0,v228,vcc\nv_and_b32 v244,15,v249\nM v[56:59],a[104:107],v[212:215],v[56:59]\nv_and_b32 v245,0x300,v240\nv_lshlrev_b32 v246,11,v249\nM v[60:63],a[100:103],v[196:199],v[60:63]\nds_read_b64_tr_b16 v[196:197],v227 offset:17920\nds_read_b64_tr_b16 v[198:199],v227 offset:22016\n" \
"M v[60:63],a[108:111],v[212:215],v[60:63]\nds_read_b64_tr_b16 v[212:213],v227 offset:26112\ns_waitcnt lgkmcnt(7)\nM v[72:75],a[96:99],v[200:203],v[72:75]\nds_read_b64_tr_b16 v[214:215],v227 offset:30208\nv_cmp_lt_u32 vcc,7,v249\nM v[72:75],a[104:107],v[204:207],v[72:75]\nv_mov_b32 v230,0x40000000\n" \
"v_lshl_or_b32 v244,v244,1,v245\nM v[76:79],a[100:103],v[200:203],v[76:79]\nds_read_b64_tr_b16 v[200:201],v226 offset:49152\nds_read_b64_tr_b16 v[202:203],v226 offset:53248\nM v[76:79],a[108:111],v[204:207],v[76:79]\nds_read_b64_tr_b16 v[204:205],v226 offset:57344\nds_read_b64_tr_b16 v[206:207],v226 offset:61440\nM v[88:91],a[96:99],v[216:219],v[88:91]\n" \
"s_add_u32 s70,s64,114688\nv_cndmask_b32 v246,v246,v230,vcc\nM v[88:91],a[104:107],v[220:223],v[88:91]\nv_add_u32 v244,s70,v244\nv_add_u32 v245,s70,v240\nM v[92:95],a[100:103],v[216:219],v[92:95]\nds_read_b64_tr_b16 v[216:217],v227 offset:49152\nds_read_b64_tr_b16 v[218:219],v227 offset:53248\n" \
"M v[92:95],a[108:111],v[220:223],v[92:95]\nds_read_b64_tr_b16 v[220:221],v227 offset:57344\ns_waitcnt lgkmcnt(7)\nM v[104:107],a[96:99],v[192:195],v[104:107]\nds_read_b64_tr_b16 v[222:223],v227 offset:61440\ns_lshl_b32 s80,s60,11\nM v[104:107],a[104:107],v[208:211],v[104:107]\nv_and_b32 v230,3,v249\n" \
"v_lshrrev_b32 v231,2,v249\nM v[108:111],a[100:103],v[192:195],v[108:111]\nds_read_b64_tr_b16 v[192:193],v226 offset:49664\nds_read_b64_tr_b16 v[194:195],v226 offset:53760\nM v[108:111],a[108:111],v[208:211],v[108:111]\nds_read_b64_tr_b16 v[208:209],v226 offset:57856\nds_read_b64_tr_b16 v[210:211],v226 offset:61952\nM v[120:123],a[96:99],v[196:199],v[120:123]\n" \
"s_lshl3_add_u32 s37,s6,s75\ns_add_u32 s80,s80,557056\nM v[120:123],a[104:107],v[212:215],v[120:123]\nv_lshl_add_u32 v231,v230,5,v231\ns_lshl_b32 s70,s7,8\nM v[124:127],a[100:103],v[196:199],v[124:127]\nds_read_b64_tr_b16 v[196:197],v227 offset:49664\nds_read_b64_tr_b16 v[198:199],v227 offset:53760\n" \
"M v[124:127],a[108:111],v[212:215],v[124:127]\nds_read_b64_tr_b16 v[212:213],v227 offset:57856\ns_waitcnt lgkmcnt(7)\nM v[8:11],a[112:115],v[200:203],v[8:11]\nds_read_b64_tr_b16 v[214:215],v227 offset:61952\ns_waitcnt vmcnt(11)\nM v[8:11],a[120:123],v[204:207],v[8:11]\nv_add_f32 v32,1.0,v32\n" \
"v_add_f32 v33,1.0,v33\nM v[12:15],a[116:119],v[200:203],v[12:15]\nds_read_b64_tr_b16 v[200:201],v226 offset:50176\nds_read_b64_tr_b16 v[202:203],v226 offset:54272\nM v[12:15],a[124:127],v[204:207],v[12:15]\nds_read_b64_tr_b16 v[204:205],v226 offset:58368\nds_read_b64_tr_b16 v[206:207],v226 offset:62464\nM v[24:27],a[112:115],v[216:219],v[24:27]\n" \
"v_add_f32 v34,1.0,v34\nv_add_f32 v35,1.0,v35\nM v[24:27],a[120:123],v[220:223],v[24:27]\nv_add_f32 v36,1.0,v36\nv_add_f32 v37,1.0,v37\nM v[28:31],a[116:119],v[216:219],v[28:31]\nds_read_b64_tr_b16 v[216:217],v227 offset:50176\nds_read_b64_tr_b16 v[218:219],v227 offset:54272\n" \
"M v[28:31],a[124:127],v[220:223],v[28:31]\nds_read_b64_tr_b16 v[220:221],v227 offset:58368\ns_waitcnt lgkmcnt(7)\nM v[40:43],a[112:115],v[192:195],v[40:43]\nds_read_b64_tr_b16 v[222:223],v227 offset:62464\nv_add_f32 v38,1.0,v38\nM v[40:43],a[120:123],v[208:211],v[40:43]\nv_add_f32 v39,1.0,v39\n" \
"v_add_f32 v116,1.0,v116\nM v[44:47],a[116:119],v[192:195],v[44:47]\nds_read_b64_tr_b16 v[192:193],v226 offset:50688\nds_read_b64_tr_b16 v[194:195],v226 offset:54784\nM v[44:47],a[124:127],v[208:211],v[44:47]\nds_read_b64_tr_b16 v[208:209],v226 offset:58880\nds_read_b64_tr_b16 v[210:211],v226 offset:62976\nM v[56:59],a[112:115],v[196:199],v[56:59]\n" \
"v_add_u32 v226,65536,v242\nv_add_f32 v117,1.0,v117\nM v[56:59],a[120:123],v[212:215],v[56:59]\ns_add_u32 s37,s37,557056\nv_lshlrev_b32 v251,5,v249\nM v[60:63],a[116:119],v[196:199],v[60:63]\nds_read_b64_tr_b16 v[196:197],v227 offset:50688\nds_read_b64_tr_b16 v[198:199],v227 offset:54784\n" \
"M v[60:63],a[124:127],v[212:215],v[60:63]\nds_read_b64_tr_b16 v[212:213],v227 offset:58880\ns_waitcnt lgkmcnt(7)\nM v[72:75],a[112:115],v[200:203],v[72:75]\nds_read_b64_tr_b16 v[214:215],v227 offset:62976\nv_xor_b32 v227,32,v226\nM v[72:75],a[120:123],v[204:207],v[72:75]\ns_or_b32 s80,s80,s75\n" \
"v_lshl_add_u32 v248,v231,1,s70\nM v[76:79],a[116:119],v[200:203],v[76:79]\nds_read_b64_tr_b16 v[200:201],v226 offset:0\nds_read_b64_tr_b16 v[202:203],v226 offset:4096\nM v[76:79],a[124:127],v[204:207],v[76:79]\nds_read_b64_tr_b16 v[204:205],v226 offset:8192\nds_read_b64_tr_b16 v[206:207],v226 offset:12288\nM v[88:91],a[112:115],v[216:219],v[88:91]\n" \
"v_lshlrev_b32 v64,16,v64\nv_lshlrev_b32 v65,16,v65\nM v[88:91],a[120:123],v[220:223],v[88:91]\nv_lshlrev_b32 v66,16,v66\nv_lshlrev_b32 v67,16,v67\nM v[92:95],a[116:119],v[216:219],v[92:95]\nds_read_b64_tr_b16 v[216:217],v227 offset:0\nds_read_b64_tr_b16 v[218:219],v227 offset:4096\n" \
"M v[92:95],a[124:127],v[220:223],v[92:95]\nds_read_b64_tr_b16 v[220:221],v227 offset:8192\ns_waitcnt lgkmcnt(7)\nM v[104:107],a[112:115],v[192:195],v[104:107]\nds_read_b64_tr_b16 v[222:223],v227 offset:12288\nv_lshlrev_b32 v68,16,v68\nM v[104:107],a[120:123],v[208:211],v[104:107]\nv_lshlrev_b32 v69,16,v69\n" \
"v_lshlrev_b32 v70,16,v70\nM v[108:111],a[116:119],v[192:195],v[108:111]\nds_read_b64_tr_b16 v[192:193],v226 offset:512\nds_read_b64_tr_b16 v[194:195],v226 offset:4608\nM v[108:111],a[124:127],v[208:211],v[108:111]\nds_read_b64_tr_b16 v[208:209],v226 offset:8704\nds_read_b64_tr_b16 v[210:211],v226 offset:12800\nM v[120:123],a[112:115],v[196:199],v[120:123]\n" \
"v_lshlrev_b32 v71,16,v71\nv_lshlrev_b32 v80,16,v80\nM v[120:123],a[120:123],v[212:215],v[120:123]\nv_lshlrev_b32 v81,16,v81\nv_lshlrev_b32 v82,16,v82\nM v[124:127],a[116:119],v[196:199],v[124:127]\nds_read_b64_tr_b16 v[196:197],v227 offset:512\nds_read_b64_tr_b16 v[198:199],v227 offset:4608\n" \
"M v[124:127],a[124:127],v[212:215],v[124:127]\nds_read_b64_tr_b16 v[212:213],v227 offset:8704\ns_waitcnt lgkmcnt(7)\nM v[8:11],a[128:131],v[200:203],v[8:11]\nds_read_b64_tr_b16 v[214:215],v227 offset:12800\ns_waitcnt vmcnt(6)\nM v[8:11],a[136:139],v[204:207],v[8:11]\nv_lshlrev_b32 v83,16,v83\n" \
"v_lshlrev_b32 v84,16,v84\nM v[12:15],a[132:135],v[200:203],v[12:15]\nds_read_b64_tr_b16 v[200:201],v226 offset:1024\nds_read_b64_tr_b16 v[202:203],v226 offset:5120\nM v[12:15],a[140:143],v[204:207],v[12:15]\nds_read_b64_tr_b16 v[204:205],v226 offset:9216\nds_read_b64_tr_b16 v[206:207],v226 offset:13312\nM v[24:27],a[128:131],v[216:219],v[24:27]\n" \
"v_lshlrev_b32 v85,16,v85\nv_lshlrev_b32 v86,16,v86\nM v[24:27],a[136:139],v[220:223],v[24:27]\nv_lshlrev_b32 v87,16,v87\nv_lshlrev_b32 v96,16,v96\nM v[28:31],a[132:135],v[216:219],v[28:31]\nds_read_b64_tr_b16 v[216:217],v227 offset:1024\nds_read_b64_tr_b16 v[218:219],v227 offset:5120\n" \
"M v[28:31],a[140:143],v[220:223],v[28:31]\nds_read_b64_tr_b16 v[220:221],v227 offset:9216\ns_waitcnt lgkmcnt(7)\nM v[40:43],a[128:131],v[192:195],v[40:43]\nds_read_b64_tr_b16 v[222:223],v227 offset:13312\nv_lshlrev_b32 v97,16,v97\nM v[40:43],a[136:139],v[208:211],v[40:43]\nv_lshlrev_b32 v98,16,v98\n" \
"v_lshlrev_b32 v99,16,v99\nM v[44:47],a[132:135],v[192:195],v[44:47]\nds_read_b64_tr_b16 v[192:193],v226 offset:1536\nds_read_b64_tr_b16 v[194:195],v226 offset:5632\nM v[44:47],a[140:143],v[208:211],v[44:47]\nds_read_b64_tr_b16 v[208:209],v226 offset:9728\nds_read_b64_tr_b16 v[210:211],v226 offset:13824\nM v[56:59],a[128:131],v[196:199],v[56:59]\n" \
"v_lshlrev_b32 v100,16,v100\nv_lshlrev_b32 v101,16,v101\nM v[56:59],a[136:139],v[212:215],v[56:59]\nv_lshlrev_b32 v102,16,v102\nv_lshlrev_b32 v103,16,v103\nM v[60:63],a[132:135],v[196:199],v[60:63]\nds_read_b64_tr_b16 v[196:197],v227 offset:1536\nds_read_b64_tr_b16 v[198:199],v227 offset:5632\n" \
"M v[60:63],a[140:143],v[212:215],v[60:63]\nds_read_b64_tr_b16 v[212:213],v227 offset:9728\ns_waitcnt lgkmcnt(7)\nM v[72:75],a[128:131],v[200:203],v[72:75]\nds_read_b64_tr_b16 v[214:215],v227 offset:13824\nv_cvt_pk_bf16_f32 v32,0,v32\nM v[72:75],a[136:139],v[204:207],v[72:75]\nv_cvt_pk_bf16_f32 v33,0,v33\n" \
"v_cvt_pk_bf16_f32 v34,0,v34\nM v[76:79],a[132:135],v[200:203],v[76:79]\nds_read_b64_tr_b16 v[200:201],v226 offset:32768\nds_read_b64_tr_b16 v[202:203],v226 offset:36864\nM v[76:79],a[140:143],v[204:207],v[76:79]\nds_read_b64_tr_b16 v[204:205],v226 offset:40960\nds_read_b64_tr_b16 v[206:207],v226 offset:45056\nM v[88:91],a[128:131],v[216:219],v[88:91]\n" \
"v_cvt_pk_bf16_f32 v35,0,v35\nv_cvt_pk_bf16_f32 v36,0,v36\nM v[88:91],a[136:139],v[220:223],v[88:91]\nv_cvt_pk_bf16_f32 v37,0,v37\nv_cvt_pk_bf16_f32 v38,0,v38\nM v[92:95],a[132:135],v[216:219],v[92:95]\nds_read_b64_tr_b16 v[216:217],v227 offset:32768\nds_read_b64_tr_b16 v[218:219],v227 offset:36864\n" \
"M v[92:95],a[140:143],v[220:223],v[92:95]\nds_read_b64_tr_b16 v[220:221],v227 offset:40960\ns_waitcnt lgkmcnt(7)\nM v[104:107],a[128:131],v[192:195],v[104:107]\nds_read_b64_tr_b16 v[222:223],v227 offset:45056\nv_cvt_pk_bf16_f32 v39,0,v39\nM v[104:107],a[136:139],v[208:211],v[104:107]\nv_cvt_pk_bf16_f32 v116,0,v116\n" \
"v_cvt_pk_bf16_f32 v117,0,v117\nM v[108:111],a[132:135],v[192:195],v[108:111]\nds_read_b64_tr_b16 v[192:193],v226 offset:33280\nds_read_b64_tr_b16 v[194:195],v226 offset:37376\nM v[108:111],a[140:143],v[208:211],v[108:111]\nds_read_b64_tr_b16 v[208:209],v226 offset:41472\nds_read_b64_tr_b16 v[210:211],v226 offset:45568\nM v[120:123],a[128:131],v[196:199],v[120:123]\n" \
"v_xor_b32 v228,v228,v229\ns_add_u32 s38,s78,16384\nM v[120:123],a[136:139],v[212:215],v[120:123]\nv_accvgpr_write_b32 a192,v229\nv_accvgpr_write_b32 a193,v228\nM v[124:127],a[132:135],v[196:199],v[124:127]\nds_read_b64_tr_b16 v[196:197],v227 offset:33280\nds_read_b64_tr_b16 v[198:199],v227 offset:37376\n" \
"M v[124:127],a[140:143],v[212:215],v[124:127]\nds_read_b64_tr_b16 v[212:213],v227 offset:41472\ns_waitcnt lgkmcnt(7)\nM v[8:11],a[144:147],v[200:203],v[8:11]\nds_read_b64_tr_b16 v[214:215],v227 offset:45568\ns_waitcnt vmcnt(3)\nM v[8:11],a[152:155],v[204:207],v[8:11]\nv_accvgpr_write_b32 a198,v229\n" \
"v_accvgpr_write_b32 a199,v228\nM v[12:15],a[148:151],v[200:203],v[12:15]\nds_read_b64_tr_b16 v[200:201],v226 offset:33792\nds_read_b64_tr_b16 v[202:203],v226 offset:37888\nM v[12:15],a[156:159],v[204:207],v[12:15]\nds_read_b64_tr_b16 v[204:205],v226 offset:41984\nds_read_b64_tr_b16 v[206:207],v226 offset:46080\nM v[24:27],a[144:147],v[216:219],v[24:27]\n" \
"M v[24:27],a[152:155],v[220:223],v[24:27]\nM v[28:31],a[148:151],v[216:219],v[28:31]\nds_read_b64_tr_b16 v[216:217],v227 offset:33792\nds_read_b64_tr_b16 v[218:219],v227 offset:37888\nM v[28:31],a[156:159],v[220:223],v[28:31]\nds_read_b64_tr_b16 v[220:221],v227 offset:41984\ns_waitcnt lgkmcnt(8)\nM v[40:43],a[144:147],v[192:195],v[40:43]\n" \
"ds_read_b64_tr_b16 v[222:223],v227 offset:46080\nv_cvt_pk_bf16_f32 v8,v8,v24\nM v[40:43],a[152:155],v[208:211],v[40:43]\nv_cvt_pk_bf16_f32 v9,v9,v25\nv_cvt_pk_bf16_f32 v10,v10,v26\nM v[44:47],a[148:151],v[192:195],v[44:47]\nds_read_b64_tr_b16 v[192:193],v226 offset:34304\nds_read_b64_tr_b16 v[194:195],v226 offset:38400\n" \
"M v[44:47],a[156:159],v[208:211],v[44:47]\nds_read_b64_tr_b16 v[208:209],v226 offset:42496\nds_read_b64_tr_b16 v[210:211],v226 offset:46592\nM v[56:59],a[144:147],v[196:199],v[56:59]\nv_cvt_pk_bf16_f32 v11,v11,v27\ns_waitcnt lgkmcnt(8)\nM v[56:59],a[152:155],v[212:215],v[56:59]\nv_cvt_pk_bf16_f32 v12,v12,v28\n" \
"v_cvt_pk_bf16_f32 v13,v13,v29\nM v[60:63],a[148:151],v[196:199],v[60:63]\nds_read_b64_tr_b16 v[196:197],v227 offset:34304\nds_read_b64_tr_b16 v[198:199],v227 offset:38400\nM v[60:63],a[156:159],v[212:215],v[60:63]\nds_read_b64_tr_b16 v[212:213],v227 offset:42496\nds_read_b64_tr_b16 v[214:215],v227 offset:46592\nM v[72:75],a[144:147],v[200:203],v[72:75]\n" \
"v_cvt_pk_bf16_f32 v40,v40,v56\nv_cvt_pk_bf16_f32 v41,v41,v57\nM v[72:75],a[152:155],v[204:207],v[72:75]\nv_cvt_pk_bf16_f32 v42,v42,v58\nv_cvt_pk_bf16_f32 v43,v43,v59\nM v[76:79],a[148:151],v[200:203],v[76:79]\nv_cvt_pk_bf16_f32 v14,v14,v30\nv_cvt_pk_bf16_f32 v15,v15,v31\n" \
"M v[76:79],a[156:159],v[204:207],v[76:79]\nv_cvt_pk_bf16_f32 v44,v44,v60\ns_waitcnt lgkmcnt(4)\nM v[88:91],a[144:147],v[216:219],v[88:91]\nv_cvt_pk_bf16_f32 v45,v45,v61\nv_cvt_pk_bf16_f32 v46,v46,v62\nM v[88:91],a[152:155],v[220:223],v[88:91]\nv_cvt_pk_bf16_f32 v47,v47,v63\n" \
"v_min_u32 v24,-2,v8\nM v[92:95],a[148:151],v[216:219],v[92:95]\nv_min_u32 v25,-2,v40\nv_min_u32 v56,-2,v9\nM v[92:95],a[156:159],v[220:223],v[92:95]\nv_min_u32 v57,-2,v41\nv_min_u32 v28,-2,v12\nM v[104:107],a[144:147],v[192:195],v[104:107]\n" \
"v_cvt_pk_bf16_f32 v72,v72,v88\nv_cvt_pk_bf16_f32 v73,v73,v89\nM v[104:107],a[152:155],v[208:211],v[104:107]\nv_cvt_pk_bf16_f32 v74,v74,v90\nv_cvt_pk_bf16_f32 v75,v75,v91\nM v[108:111],a[148:151],v[192:195],v[108:111]\nv_cvt_pk_bf16_f32 v76,v76,v92\nv_cvt_pk_bf16_f32 v77,v77,v93\n" \
"M v[108:111],a[156:159],v[208:211],v[108:111]\nv_cvt_pk_bf16_f32 v78,v78,v94\ns_waitcnt lgkmcnt(0)\nM v[120:123],a[144:147],v[196:199],v[120:123]\nv_cvt_pk_bf16_f32 v79,v79,v95\nv_min_u32 v26,-2,v72\nM v[120:123],a[152:155],v[212:215],v[120:123]\nv_min_u32 v58,-2,v73\n" \
"v_min_u32 v88,-2,v10\nM v[124:127],a[148:151],v[196:199],v[124:127]\nv_min_u32 v89,-2,v42\nv_min_u32 v90,-2,v74\nM v[124:127],a[156:159],v[212:215],v[124:127]\nv_min_u32 v29,-2,v44\nv_min_u32 v30,-2,v76\nv_cvt_pk_bf16_f32 v104,v104,v120\n" \
"v_cvt_pk_bf16_f32 v105,v105,v121\nv_cvt_pk_bf16_f32 v106,v106,v122\nv_cvt_pk_bf16_f32 v107,v107,v123\nv_min_u32 v27,-2,v104\nv_min_u32 v59,-2,v105\nv_cvt_pk_bf16_f32 v108,v108,v124\nv_cvt_pk_bf16_f32 v109,v109,v125\nv_cvt_pk_bf16_f32 v110,v110,v126\n" \
"v_cvt_pk_bf16_f32 v111,v111,v127\nv_min_u32 v91,-2,v106\nv_min_u32 v120,-2,v11\nv_min_u32 v121,-2,v43\nv_min_u32 v122,-2,v75\nv_min_u32 v123,-2,v107\nv_min_u32 v31,-2,v108\nv_min_u32 v60,-2,v13\n" \
"v_min_u32 v61,-2,v45\nv_min_u32 v62,-2,v77\nv_min_u32 v63,-2,v109\nv_min_u32 v92,-2,v14\nv_min_u32 v93,-2,v46\nv_min_u32 v94,-2,v78\nv_min_u32 v95,-2,v110\nv_min_u32 v124,-2,v15\n" \
"v_min_u32 v125,-2,v47\nv_min_u32 v126,-2,v79\nv_min_u32 v127,-2,v111\nbuffer_store_dwordx4 v[24:27],v253,s[52:55],s78 offen offset:0 sc1\nbuffer_store_dwordx4 v[56:59],v253,s[52:55],s78 offen offset:1024 sc1\nbuffer_store_dwordx4 v[88:91],v253,s[52:55],s78 offen offset:2048 sc1\nbuffer_store_dwordx4 v[120:123],v253,s[52:55],s78 offen offset:3072 sc1\nbuffer_store_dwordx4 v[28:31],v253,s[52:55],s38 offen offset:0 sc1\n" \
"buffer_store_dwordx4 v[60:63],v253,s[52:55],s38 offen offset:1024 sc1\nbuffer_store_dwordx4 v[92:95],v253,s[52:55],s38 offen offset:2048 sc1\nbuffer_store_dwordx4 v[124:127],v253,s[52:55],s38 offen offset:3072 sc1\n.Ltail:\ns_add_u32 s72,s95,0\nbuffer_load_dwordx4 v[160:163],v240,s[52:55],s72 offen sc1\ns_add_u32 s72,s95,2097152\nbuffer_load_dwordx4 v[164:167],v240,s[52:55],s72 offen sc1\n" \
"s_add_u32 s72,s95,4194304\nbuffer_load_dwordx4 v[168:171],v240,s[52:55],s72 offen sc1\ns_add_u32 s72,s95,6291456\nbuffer_load_dwordx4 v[172:175],v240,s[52:55],s72 offen sc1\ns_add_u32 s72,s95,8388608\nbuffer_load_dwordx4 v[176:179],v240,s[52:55],s72 offen sc1\ns_add_u32 s72,s95,10485760\nbuffer_load_dwordx4 v[180:183],v240,s[52:55],s72 offen sc1\n" \
"s_add_u32 s72,s95,12582912\nbuffer_load_dwordx4 v[184:187],v240,s[52:55],s72 offen sc1\ns_add_u32 s72,s95,14680064\nbuffer_load_dwordx4 v[188:191],v240,s[52:55],s72 offen sc1\n.Ltail2:\ns_add_u32 s72,s74,0\nbuffer_load_dwordx4 v[128:131],v240,s[52:55],s72 offen sc1\ns_add_u32 s72,s74,2097152\n" \
"buffer_load_dwordx4 v[132:135],v240,s[52:55],s72 offen sc1\ns_add_u32 s72,s74,4194304\nbuffer_load_dwordx4 v[136:139],v240,s[52:55],s72 offen sc1\ns_add_u32 s72,s74,6291456\nbuffer_load_dwordx4 v[140:143],v240,s[52:55],s72 offen sc1\ns_add_u32 s72,s74,8388608\nbuffer_load_dwordx4 v[144:147],v240,s[52:55],s72 offen sc1\ns_add_u32 s72,s74,10485760\n" \
"buffer_load_dwordx4 v[148:151],v240,s[52:55],s72 offen sc1\ns_add_u32 s72,s74,12582912\nbuffer_load_dwordx4 v[152:155],v240,s[52:55],s72 offen sc1\ns_add_u32 s72,s74,14680064\nbuffer_load_dwordx4 v[156:159],v240,s[52:55],s72 offen sc1\ns_mov_b32 s94,0\ns_mov_b32 s29,1\ns_cmp_eq_u32 s3,0\n" \
"s_cbranch_scc1 .Lnp\ns_mov_b32 s70,0x05040100\ns_mov_b32 s71,0x07060302\nv_accvgpr_read_b32 v228,a212\nv_accvgpr_read_b32 v229,a213\nv_accvgpr_read_b32 v230,a214\nv_accvgpr_read_b32 v231,a215\nv_perm_b32 v192,v230,v228,s70\n" \
"v_perm_b32 v193,v230,v228,s71\nv_perm_b32 v194,v231,v229,s70\nv_perm_b32 v195,v231,v229,s71\nv_accvgpr_read_b32 v228,a216\nv_accvgpr_read_b32 v229,a217\nv_accvgpr_read_b32 v230,a218\nv_accvgpr_read_b32 v231,a219\nv_perm_b32 v196,v230,v228,s70\n" \
"v_perm_b32 v197,v230,v228,s71\nv_perm_b32 v198,v231,v229,s70\nv_perm_b32 v199,v231,v229,s71\nv_accvgpr_read_b32 v228,a220\nv_accvgpr_read_b32 v229,a221\nv_accvgpr_read_b32 v230,a222\nv_accvgpr_read_b32 v231,a223\nv_perm_b32 v200,v230,v228,s70\n" \
"v_perm_b32 v201,v230,v228,s71\nv_perm_b32 v202,v231,v229,s70\nv_perm_b32 v203,v231,v229,s71\nv_accvgpr_read_b32 v228,a224\nv_accvgpr_read_b32 v229,a225\nv_accvgpr_read_b32 v230,a226\nv_accvgpr_read_b32 v231,a227\nv_perm_b32 v204,v230,v228,s70\n" \
"v_perm_b32 v205,v230,v228,s71\nv_perm_b32 v206,v231,v229,s70\nv_perm_b32 v207,v231,v229,s71\nv_accvgpr_read_b32 v228,a228\nv_accvgpr_read_b32 v229,a229\nv_accvgpr_read_b32 v230,a230\nv_accvgpr_read_b32 v231,a231\nv_perm_b32 v208,v230,v228,s70\n" \
"v_perm_b32 v209,v230,v228,s71\nv_perm_b32 v210,v231,v229,s70\nv_perm_b32 v211,v231,v229,s71\nv_accvgpr_read_b32 v228,a232\nv_accvgpr_read_b32 v229,a233\nv_accvgpr_read_b32 v230,a234\nv_accvgpr_read_b32 v231,a235\nv_perm_b32 v212,v230,v228,s70\n" \
"v_perm_b32 v213,v230,v228,s71\nv_perm_b32 v214,v231,v229,s70\nv_perm_b32 v215,v231,v229,s71\nv_accvgpr_read_b32 v228,a236\nv_accvgpr_read_b32 v229,a237\nv_accvgpr_read_b32 v230,a238\nv_accvgpr_read_b32 v231,a239\nv_perm_b32 v216,v230,v228,s70\n" \
"v_perm_b32 v217,v230,v228,s71\nv_perm_b32 v218,v231,v229,s70\nv_perm_b32 v219,v231,v229,s71\nv_accvgpr_read_b32 v228,a240\nv_accvgpr_read_b32 v229,a241\nv_accvgpr_read_b32 v230,a242\nv_accvgpr_read_b32 v231,a243\nv_perm_b32 v220,v230,v228,s70\n" \
"v_perm_b32 v221,v230,v228,s71\nv_perm_b32 v222,v231,v229,s70\nv_perm_b32 v223,v231,v229,s71\nv_and_b32 v228,3,v249\nv_lshrrev_b32 v229,4,v249\nv_and_b32 v230,1,v229\nv_lshrrev_b32 v229,1,v229\nv_lshlrev_b32 v230,5,v230\n" \
"v_lshl_or_b32 v230,v228,6,v230\nv_lshl_or_b32 v230,v229,8,v230\ns_lshl2_add_u32 s70,s7,83008\nv_add_u32 v252,s70,v230\nv_and_b32 v230,0xf0,v240\nv_add_u32 v253,83008,v230\n.Lnp:\n.Lrow1:\n" \
"s_waitcnt vmcnt(15)\nM v[72:75],a[192:195],v[160:163],0\nM v[76:79],a[196:199],v[160:163],0\ns_waitcnt vmcnt(14)\nM v[72:75],a[192:195],v[164:167],v[72:75]\nM v[76:79],a[196:199],v[164:167],v[76:79]\ns_waitcnt vmcnt(13)\nM v[72:75],a[192:195],v[168:171],v[72:75]\n" \
"M v[76:79],a[196:199],v[168:171],v[76:79]\ns_waitcnt vmcnt(12)\nM v[72:75],a[192:195],v[172:175],v[72:75]\nM v[76:79],a[196:199],v[172:175],v[76:79]\ns_waitcnt vmcnt(11)\nM v[72:75],a[192:195],v[176:179],v[72:75]\nM v[76:79],a[196:199],v[176:179],v[76:79]\ns_waitcnt vmcnt(10)\n" \
"M v[72:75],a[192:195],v[180:183],v[72:75]\nM v[76:79],a[196:199],v[180:183],v[76:79]\ns_waitcnt vmcnt(9)\nM v[72:75],a[192:195],v[184:187],v[72:75]\nM v[76:79],a[196:199],v[184:187],v[76:79]\ns_waitcnt vmcnt(8)\nM v[72:75],a[192:195],v[188:191],v[72:75]\nM v[76:79],a[196:199],v[188:191],v[76:79]\n" \
"s_sub_u32 s38,56622112,s95\ns_lshr_b32 s38,s38,5\ns_nop 4\nv_cvt_pk_bf16_f32 v72,0,v72\nv_cvt_pk_bf16_f32 v73,0,v73\nv_cvt_pk_bf16_f32 v74,0,v74\nv_cvt_pk_bf16_f32 v75,0,v75\nv_cvt_pk_bf16_f32 v76,0,v76\n" \
"v_cvt_pk_bf16_f32 v77,0,v77\nv_cvt_pk_bf16_f32 v78,0,v78\nv_cvt_pk_bf16_f32 v79,0,v79\nv_pk_mul_f32 v[72:73],v[72:73],v[0:1]\nv_pk_mul_f32 v[74:75],v[74:75],v[2:3]\nv_pk_mul_f32 v[76:77],v[76:77],v[4:5]\nv_pk_mul_f32 v[78:79],v[78:79],v[6:7]\nv_cvt_pk_bf16_f32 v72,0,v72\n" \
"v_cvt_pk_bf16_f32 v73,0,v73\nv_cvt_pk_bf16_f32 v74,0,v74\nv_cvt_pk_bf16_f32 v75,0,v75\nv_cvt_pk_bf16_f32 v76,0,v76\nv_cvt_pk_bf16_f32 v77,0,v77\nv_cvt_pk_bf16_f32 v78,0,v78\nv_cvt_pk_bf16_f32 v79,0,v79\nv_pk_add_f32 v[72:73],v[72:73],v[64:65]\n" \
"v_pk_add_f32 v[74:75],v[74:75],v[66:67]\nv_pk_add_f32 v[76:77],v[76:77],v[68:69]\nv_pk_add_f32 v[78:79],v[78:79],v[70:71]\nv_cvt_pk_bf16_f32 v72,0,v72\nv_cvt_pk_bf16_f32 v73,0,v73\nv_cvt_pk_bf16_f32 v74,0,v74\nv_cvt_pk_bf16_f32 v75,0,v75\nv_cvt_pk_bf16_f32 v76,0,v76\n" \
"v_cvt_pk_bf16_f32 v77,0,v77\nv_cvt_pk_bf16_f32 v78,0,v78\nv_cvt_pk_bf16_f32 v79,0,v79\nv_readfirstlane_b32 s70,v72\ns_nop 1\nv_subrev_f32 v104,s70,v72\nv_subrev_f32 v105,s70,v73\nv_subrev_f32 v106,s70,v74\n" \
"v_subrev_f32 v107,s70,v75\nv_subrev_f32 v108,s70,v76\nv_subrev_f32 v109,s70,v77\nv_subrev_f32 v110,s70,v78\nv_subrev_f32 v111,s70,v79\nv_pk_mul_f32 v[124:125],v[104:105],v[104:105]\nv_pk_add_f32 v[120:121],v[104:105],v[106:107]\nv_pk_fma_f32 v[124:125],v[106:107],v[106:107],v[124:125]\n" \
"v_pk_fma_f32 v[124:125],v[108:109],v[108:109],v[124:125]\nv_pk_fma_f32 v[124:125],v[110:111],v[110:111],v[124:125]\nv_pk_add_f32 v[108:109],v[108:109],v[110:111]\nv_pk_add_f32 v[120:121],v[120:121],v[108:109]\nv_add_f32 v120,v120,v121\nv_add_f32 v121,v124,v125\ns_nop 1\nv_permlane16_swap_b32 v120,v121\n" \
"v_add_f32 v120,v120,v121\nv_mov_b32 v121,v120\ns_nop 1\nv_permlane32_swap_b32 v120,v121\nv_add_f32 v120,v120,v121\ns_nop 1\nv_add_f32_dpp v120,v120,v120 quad_perm:[1,0,3,2] row_mask:0xf bank_mask:0xf\ns_nop 1\n" \
"v_add_f32_dpp v120,v120,v120 quad_perm:[2,3,0,1] row_mask:0xf bank_mask:0xf\ns_nop 1\nv_add_f32_dpp v120,v120,v120 row_half_mirror row_mask:0xf bank_mask:0xf\ns_nop 1\nv_add_f32_dpp v120,v120,v120 row_mirror row_mask:0xf bank_mask:0xf\nv_mul_f32 v120,0x3b000000,v120\nv_mov_b32 v121,v120\nv_cmp_u_f32 vcc,v120,v120\n" \
"s_nop 0\nv_permlane16_swap_b32 v120,v121\ns_cbranch_vccnz .Lrs1\n.Lrb1:\nv_fma_f32 v121,-v120,v120,v121\nv_add_f32 v122,s70,v120\nv_max_f32 v121,0,v121\nv_cvt_pk_bf16_f32 v122,0,v122\n" \
"v_cvt_pk_bf16_f32 v121,0,v121\nv_add_f32 v121,0x3727c5ac,v121\nv_cvt_pk_bf16_f32 v121,0,v121\nv_sqrt_f32 v121,v121\nv_pk_add_f32 v[88:89],v[72:73],v[122:123] op_sel_hi:[1,0] neg_lo:[0,1] neg_hi:[0,1]\nv_cvt_pk_bf16_f32 v121,0,v121\nv_rcp_f32 v123,v121\nv_cvt_pk_bf16_f32 v88,0,v88\n" \
"v_pk_add_f32 v[90:91],v[74:75],v[122:123] op_sel_hi:[1,0] neg_lo:[0,1] neg_hi:[0,1]\nv_pk_add_f32 v[92:93],v[76:77],v[122:123] op_sel_hi:[1,0] neg_lo:[0,1] neg_hi:[0,1]\nv_pk_add_f32 v[94:95],v[78:79],v[122:123] op_sel_hi:[1,0] neg_lo:[0,1] neg_hi:[0,1]\nv_cvt_pk_bf16_f32 v89,0,v89\nv_pk_mul_f32 v[88:89],v[88:89],v[122:123] op_sel:[0,1]\nv_cvt_pk_bf16_f32 v88,0,v88\nv_cvt_pk_bf16_f32 v89,0,v89\nv_pk_mul_f32 v[88:89],v[88:89],v[32:33]\n" \
"v_cvt_pk_bf16_f32 v88,0,v88\nv_cvt_pk_bf16_f32 v89,0,v89\nv_pk_add_f32 v[88:89],v[88:89],v[16:17]\nv_cvt_pk_bf16_f32 v88,0,v88\nv_cvt_pk_bf16_f32 v89,0,v89\nv_pk_mul_f32 v[124:125],v[88:89],v[96:97]\nv_cvt_pk_bf16_f32 v90,0,v90\nv_cvt_pk_bf16_f32 v91,0,v91\n" \
"v_pk_mul_f32 v[90:91],v[90:91],v[122:123] op_sel:[0,1]\nv_cvt_pk_bf16_f32 v90,0,v90\nv_cvt_pk_bf16_f32 v91,0,v91\nv_pk_mul_f32 v[90:91],v[90:91],v[34:35]\nv_cvt_pk_bf16_f32 v90,0,v90\nv_cvt_pk_bf16_f32 v91,0,v91\nv_pk_add_f32 v[90:91],v[90:91],v[18:19]\nv_cvt_pk_bf16_f32 v90,0,v90\n" \
"v_cvt_pk_bf16_f32 v91,0,v91\nv_pk_fma_f32 v[124:125],v[90:91],v[98:99],v[124:125]\nv_cvt_pk_bf16_f32 v92,0,v92\nv_cvt_pk_bf16_f32 v93,0,v93\nv_pk_mul_f32 v[92:93],v[92:93],v[122:123] op_sel:[0,1]\nv_cvt_pk_bf16_f32 v92,0,v92\nv_cvt_pk_bf16_f32 v93,0,v93\nv_pk_mul_f32 v[92:93],v[92:93],v[36:37]\n" \
"v_cvt_pk_bf16_f32 v92,0,v92\nv_cvt_pk_bf16_f32 v93,0,v93\nv_pk_add_f32 v[92:93],v[92:93],v[20:21]\nv_cvt_pk_bf16_f32 v92,0,v92\nv_cvt_pk_bf16_f32 v93,0,v93\nv_pk_fma_f32 v[124:125],v[92:93],v[100:101],v[124:125]\nv_cvt_pk_bf16_f32 v94,0,v94\nv_cvt_pk_bf16_f32 v95,0,v95\n" \
"v_pk_mul_f32 v[94:95],v[94:95],v[122:123] op_sel:[0,1]\nv_cvt_pk_bf16_f32 v94,0,v94\nv_cvt_pk_bf16_f32 v95,0,v95\nv_pk_mul_f32 v[94:95],v[94:95],v[38:39]\nv_cvt_pk_bf16_f32 v94,0,v94\nv_cvt_pk_bf16_f32 v95,0,v95\nv_pk_add_f32 v[94:95],v[94:95],v[22:23]\nv_cvt_pk_bf16_f32 v94,0,v94\n" \
"v_cvt_pk_bf16_f32 v95,0,v95\nv_pk_fma_f32 v[124:125],v[94:95],v[102:103],v[124:125]\nv_add_f32 v124,v124,v125\nv_mov_b32 v125,v124\nv_lshrrev_b32 v104,16,v121\nv_or_b32 v104,v104,v122\nv_permlane16_swap_b32 v124,v125\nv_add_f32 v124,v124,v125\n" \
"v_mov_b32 v125,v124\ns_nop 1\nv_permlane32_swap_b32 v124,v125\nv_add_f32 v124,v124,v125\ns_nop 1\nv_add_f32_dpp v124,v124,v124 quad_perm:[1,0,3,2] row_mask:0xf bank_mask:0xf\ns_nop 1\nv_add_f32_dpp v124,v124,v124 quad_perm:[2,3,0,1] row_mask:0xf bank_mask:0xf\n" \
"s_nop 1\nv_add_f32_dpp v124,v124,v124 row_half_mirror row_mask:0xf bank_mask:0xf\ns_nop 1\nv_add_f32_dpp v124,v124,v124 row_mirror row_mask:0xf bank_mask:0xf\nv_cvt_pk_bf16_f32 v124,0,v124\ns_nop 0\nv_readfirstlane_b32 s77,v124\ns_nop 0\n" \
"s_ashr_i32 s71,s77,31\ns_and_b32 s72,s77,0x7fffffff\ns_or_b32 s71,s71,0x80000000\ns_xor_b32 s77,s77,s71\ns_and_b32 s77,s77,0xffff0000\ns_cmp_gt_u32 s72,0x7f800000\ns_cselect_b32 s77,0,s77\ns_or_b32 s77,s77,s38\n" \
"v_mov_b32 v105,s77\nds_max_u64 v250,v[104:105]\n.Lrow0:\ns_waitcnt vmcnt(7)\nM v[8:11],a[192:195],v[128:131],0\nM v[12:15],a[196:199],v[128:131],0\ns_waitcnt vmcnt(6)\nM v[8:11],a[192:195],v[132:135],v[8:11]\n" \
"M v[12:15],a[196:199],v[132:135],v[12:15]\ns_waitcnt vmcnt(5)\nM v[8:11],a[192:195],v[136:139],v[8:11]\nM v[12:15],a[196:199],v[136:139],v[12:15]\ns_waitcnt vmcnt(4)\nM v[8:11],a[192:195],v[140:143],v[8:11]\nM v[12:15],a[196:199],v[140:143],v[12:15]\ns_waitcnt vmcnt(3)\n" \
"M v[8:11],a[192:195],v[144:147],v[8:11]\nM v[12:15],a[196:199],v[144:147],v[12:15]\ns_waitcnt vmcnt(2)\nM v[8:11],a[192:195],v[148:151],v[8:11]\nM v[12:15],a[196:199],v[148:151],v[12:15]\ns_waitcnt vmcnt(1)\nM v[8:11],a[192:195],v[152:155],v[8:11]\nM v[12:15],a[196:199],v[152:155],v[12:15]\n" \
"s_waitcnt vmcnt(0)\nM v[8:11],a[192:195],v[156:159],v[8:11]\nM v[12:15],a[196:199],v[156:159],v[12:15]\ns_sub_u32 s39,56622112,s74\ns_lshr_b32 s39,s39,5\ns_nop 4\nv_cvt_pk_bf16_f32 v8,0,v8\nv_cvt_pk_bf16_f32 v9,0,v9\n" \
"v_cvt_pk_bf16_f32 v10,0,v10\nv_cvt_pk_bf16_f32 v11,0,v11\nv_cvt_pk_bf16_f32 v12,0,v12\nv_cvt_pk_bf16_f32 v13,0,v13\nv_cvt_pk_bf16_f32 v14,0,v14\nv_cvt_pk_bf16_f32 v15,0,v15\nv_pk_mul_f32 v[8:9],v[8:9],v[0:1]\nv_pk_mul_f32 v[10:11],v[10:11],v[2:3]\n" \
"v_pk_mul_f32 v[12:13],v[12:13],v[4:5]\nv_pk_mul_f32 v[14:15],v[14:15],v[6:7]\nv_cvt_pk_bf16_f32 v8,0,v8\nv_cvt_pk_bf16_f32 v9,0,v9\nv_cvt_pk_bf16_f32 v10,0,v10\nv_cvt_pk_bf16_f32 v11,0,v11\nv_cvt_pk_bf16_f32 v12,0,v12\nv_cvt_pk_bf16_f32 v13,0,v13\n" \
"v_cvt_pk_bf16_f32 v14,0,v14\nv_cvt_pk_bf16_f32 v15,0,v15\nv_pk_add_f32 v[8:9],v[8:9],v[80:81]\nv_pk_add_f32 v[10:11],v[10:11],v[82:83]\nv_pk_add_f32 v[12:13],v[12:13],v[84:85]\nv_pk_add_f32 v[14:15],v[14:15],v[86:87]\nv_cvt_pk_bf16_f32 v8,0,v8\nv_cvt_pk_bf16_f32 v9,0,v9\n" \
"v_cvt_pk_bf16_f32 v10,0,v10\nv_cvt_pk_bf16_f32 v11,0,v11\nv_cvt_pk_bf16_f32 v12,0,v12\nv_cvt_pk_bf16_f32 v13,0,v13\nv_cvt_pk_bf16_f32 v14,0,v14\nv_cvt_pk_bf16_f32 v15,0,v15\nv_readfirstlane_b32 s70,v8\ns_nop 1\n" \
"v_subrev_f32 v40,s70,v8\nv_subrev_f32 v41,s70,v9\nv_subrev_f32 v42,s70,v10\nv_subrev_f32 v43,s70,v11\nv_subrev_f32 v44,s70,v12\nv_subrev_f32 v45,s70,v13\nv_subrev_f32 v46,s70,v14\nv_subrev_f32 v47,s70,v15\n" \
"v_pk_mul_f32 v[60:61],v[40:41],v[40:41]\nv_pk_add_f32 v[56:57],v[40:41],v[42:43]\nv_pk_fma_f32 v[60:61],v[42:43],v[42:43],v[60:61]\nv_pk_fma_f32 v[60:61],v[44:45],v[44:45],v[60:61]\nv_pk_fma_f32 v[60:61],v[46:47],v[46:47],v[60:61]\nv_pk_add_f32 v[44:45],v[44:45],v[46:47]\nv_pk_add_f32 v[56:57],v[56:57],v[44:45]\nv_add_f32 v56,v56,v57\n" \
"v_add_f32 v57,v60,v61\ns_nop 1\nv_permlane16_swap_b32 v56,v57\nv_add_f32 v56,v56,v57\nv_mov_b32 v57,v56\ns_nop 1\nv_permlane32_swap_b32 v56,v57\nv_add_f32 v56,v56,v57\n" \
"s_nop 1\nv_add_f32_dpp v56,v56,v56 quad_perm:[1,0,3,2] row_mask:0xf bank_mask:0xf\ns_nop 1\nv_add_f32_dpp v56,v56,v56 quad_perm:[2,3,0,1] row_mask:0xf bank_mask:0xf\ns_nop 1\nv_add_f32_dpp v56,v56,v56 row_half_mirror row_mask:0xf bank_mask:0xf\ns_nop 1\nv_add_f32_dpp v56,v56,v56 row_mirror row_mask:0xf bank_mask:0xf\n" \
"v_mul_f32 v56,0x3b000000,v56\nv_mov_b32 v57,v56\nv_cmp_u_f32 vcc,v56,v56\ns_nop 0\nv_permlane16_swap_b32 v56,v57\ns_cbranch_vccnz .Lrs0\n.Lrb0:\nbuffer_load_dwordx4 v[232:235],v251,s[52:55],s80 offen sc1\n" \
"buffer_load_dwordx4 v[236:239],v251,s[52:55],s80 offen offset:16 sc1\nv_fma_f32 v57,-v56,v56,v57\nv_add_f32 v58,s70,v56\nv_max_f32 v57,0,v57\nv_cvt_pk_bf16_f32 v58,0,v58\nv_cvt_pk_bf16_f32 v57,0,v57\nv_add_f32 v57,0x3727c5ac,v57\nv_cvt_pk_bf16_f32 v57,0,v57\n" \
"v_sqrt_f32 v57,v57\nv_pk_add_f32 v[24:25],v[8:9],v[58:59] op_sel_hi:[1,0] neg_lo:[0,1] neg_hi:[0,1]\nv_cvt_pk_bf16_f32 v57,0,v57\nv_rcp_f32 v59,v57\nv_cvt_pk_bf16_f32 v24,0,v24\nv_pk_add_f32 v[26:27],v[10:11],v[58:59] op_sel_hi:[1,0] neg_lo:[0,1] neg_hi:[0,1]\nv_pk_add_f32 v[28:29],v[12:13],v[58:59] op_sel_hi:[1,0] neg_lo:[0,1] neg_hi:[0,1]\nv_pk_add_f32 v[30:31],v[14:15],v[58:59] op_sel_hi:[1,0] neg_lo:[0,1] neg_hi:[0,1]\n" \
"v_cvt_pk_bf16_f32 v25,0,v25\nv_pk_mul_f32 v[24:25],v[24:25],v[58:59] op_sel:[0,1]\nv_cvt_pk_bf16_f32 v24,0,v24\nv_cvt_pk_bf16_f32 v25,0,v25\nv_pk_mul_f32 v[24:25],v[24:25],v[32:33]\nv_cvt_pk_bf16_f32 v24,0,v24\nv_cvt_pk_bf16_f32 v25,0,v25\nv_pk_add_f32 v[24:25],v[24:25],v[16:17]\n" \
"v_cvt_pk_bf16_f32 v24,0,v24\nv_cvt_pk_bf16_f32 v25,0,v25\nv_pk_mul_f32 v[60:61],v[24:25],v[96:97]\nv_cvt_pk_bf16_f32 v26,0,v26\nv_cvt_pk_bf16_f32 v27,0,v27\nv_pk_mul_f32 v[26:27],v[26:27],v[58:59] op_sel:[0,1]\nv_cvt_pk_bf16_f32 v26,0,v26\nv_cvt_pk_bf16_f32 v27,0,v27\n" \
"v_pk_mul_f32 v[26:27],v[26:27],v[34:35]\nv_cvt_pk_bf16_f32 v26,0,v26\nv_cvt_pk_bf16_f32 v27,0,v27\nv_pk_add_f32 v[26:27],v[26:27],v[18:19]\nv_cvt_pk_bf16_f32 v26,0,v26\nv_cvt_pk_bf16_f32 v27,0,v27\nv_pk_fma_f32 v[60:61],v[26:27],v[98:99],v[60:61]\nv_cvt_pk_bf16_f32 v28,0,v28\n" \
"v_cvt_pk_bf16_f32 v29,0,v29\nv_pk_mul_f32 v[28:29],v[28:29],v[58:59] op_sel:[0,1]\nv_cvt_pk_bf16_f32 v28,0,v28\nv_cvt_pk_bf16_f32 v29,0,v29\nv_pk_mul_f32 v[28:29],v[28:29],v[36:37]\nv_cvt_pk_bf16_f32 v28,0,v28\nv_cvt_pk_bf16_f32 v29,0,v29\nv_pk_add_f32 v[28:29],v[28:29],v[20:21]\n" \
"v_cvt_pk_bf16_f32 v28,0,v28\nv_cvt_pk_bf16_f32 v29,0,v29\nv_pk_fma_f32 v[60:61],v[28:29],v[100:101],v[60:61]\nv_cvt_pk_bf16_f32 v30,0,v30\nv_cvt_pk_bf16_f32 v31,0,v31\nv_pk_mul_f32 v[30:31],v[30:31],v[58:59] op_sel:[0,1]\nv_cvt_pk_bf16_f32 v30,0,v30\nv_cvt_pk_bf16_f32 v31,0,v31\n" \
"v_pk_mul_f32 v[30:31],v[30:31],v[38:39]\nv_cvt_pk_bf16_f32 v30,0,v30\nv_cvt_pk_bf16_f32 v31,0,v31\nv_pk_add_f32 v[30:31],v[30:31],v[22:23]\nv_cvt_pk_bf16_f32 v30,0,v30\nv_cvt_pk_bf16_f32 v31,0,v31\nv_pk_fma_f32 v[60:61],v[30:31],v[102:103],v[60:61]\nv_add_f32 v60,v60,v61\n" \
"v_mov_b32 v61,v60\nv_lshrrev_b32 v40,16,v57\nv_or_b32 v40,v40,v58\nv_permlane16_swap_b32 v60,v61\nv_add_f32 v60,v60,v61\nv_mov_b32 v61,v60\ns_nop 1\nv_permlane32_swap_b32 v60,v61\n" \
"v_add_f32 v60,v60,v61\ns_nop 1\nv_add_f32_dpp v60,v60,v60 quad_perm:[1,0,3,2] row_mask:0xf bank_mask:0xf\ns_nop 1\nv_add_f32_dpp v60,v60,v60 quad_perm:[2,3,0,1] row_mask:0xf bank_mask:0xf\ns_nop 1\nv_add_f32_dpp v60,v60,v60 row_half_mirror row_mask:0xf bank_mask:0xf\ns_nop 1\n" \
"v_add_f32_dpp v60,v60,v60 row_mirror row_mask:0xf bank_mask:0xf\nv_cvt_pk_bf16_f32 v60,0,v60\ns_nop 0\nv_readfirstlane_b32 s76,v60\ns_nop 0\ns_ashr_i32 s71,s76,31\ns_and_b32 s72,s76,0x7fffffff\ns_or_b32 s71,s71,0x80000000\n" \
"s_xor_b32 s76,s76,s71\ns_and_b32 s76,s76,0xffff0000\ns_cmp_gt_u32 s72,0x7f800000\ns_cselect_b32 s76,0,s76\ns_or_b32 s76,s76,s39\nv_mov_b32 v41,s76\nds_max_u64 v250,v[40:41]\ns_waitcnt lgkmcnt(0)\n" \
"s_barrier\nds_read_b64 v[224:225],v250\ns_waitcnt lgkmcnt(0)\nbuffer_store_dwordx2 v[224:225],v246,s[52:55],s37 offen sc1\nv_readfirstlane_b32 s97,v225\ns_sub_u32 s95,s95,54525952\ns_sub_u32 s74,s74,54525952\ns_mov_b32 s29,0\n" \
"s_cmp_eq_u32 s97,s77\ns_cselect_b32 s65,s95,0x40000000\ns_cselect_b32 s95,0x40000000,s95\ns_cmp_eq_u32 s97,s76\ns_cselect_b32 s66,s74,0x40000000\ns_cselect_b32 s74,0x40000000,s74\ns_lshr_b32 s70,s6,7\ns_or_b32 s38,s7,s70\n" \
"s_cmp_lg_u32 s3,0\ns_waitcnt vmcnt(1)\ns_cbranch_scc1 .Lkp\nv_pk_mul_f32 v[88:89],v[88:89],v[48:49]\nv_pk_mul_f32 v[90:91],v[90:91],v[50:51]\nv_pk_mul_f32 v[92:93],v[92:93],v[52:53]\nv_pk_mul_f32 v[94:95],v[94:95],v[54:55]\nv_cvt_pk_bf16_f32 v88,0,v88\n" \
"v_cvt_pk_bf16_f32 v89,0,v89\nv_cvt_pk_bf16_f32 v90,0,v90\nv_cvt_pk_bf16_f32 v91,0,v91\nv_cvt_pk_bf16_f32 v92,0,v92\nv_cvt_pk_bf16_f32 v93,0,v93\nv_cvt_pk_bf16_f32 v94,0,v94\nv_cvt_pk_bf16_f32 v95,0,v95\nv_pk_add_f32 v[88:89],v[88:89],v[72:73]\n" \
"v_pk_add_f32 v[90:91],v[90:91],v[74:75]\nv_pk_add_f32 v[92:93],v[92:93],v[76:77]\nv_pk_add_f32 v[94:95],v[94:95],v[78:79]\nv_cvt_pk_bf16_f32 v88,0,v88\nds_write_b16_d16_hi v244,v88 offset:0\nv_cvt_pk_bf16_f32 v89,0,v89\nds_write_b16_d16_hi v244,v89 offset:32\nv_cvt_pk_bf16_f32 v90,0,v90\n" \
"ds_write_b16_d16_hi v244,v90 offset:64\nv_cvt_pk_bf16_f32 v91,0,v91\nds_write_b16_d16_hi v244,v91 offset:96\nv_cvt_pk_bf16_f32 v92,0,v92\nds_write_b16_d16_hi v244,v92 offset:128\nv_cvt_pk_bf16_f32 v93,0,v93\nds_write_b16_d16_hi v244,v93 offset:160\nv_cvt_pk_bf16_f32 v94,0,v94\n" \
"ds_write_b16_d16_hi v244,v94 offset:192\nv_cvt_pk_bf16_f32 v95,0,v95\nds_write_b16_d16_hi v244,v95 offset:224\nds_read_b128 v[64:67],v245 offset:0\nv_pk_mul_f32 v[24:25],v[24:25],v[48:49]\nv_pk_mul_f32 v[26:27],v[26:27],v[50:51]\nv_pk_mul_f32 v[28:29],v[28:29],v[52:53]\nv_pk_mul_f32 v[30:31],v[30:31],v[54:55]\n" \
"v_cvt_pk_bf16_f32 v24,0,v24\nv_cvt_pk_bf16_f32 v25,0,v25\nv_cvt_pk_bf16_f32 v26,0,v26\nv_cvt_pk_bf16_f32 v27,0,v27\nv_cvt_pk_bf16_f32 v28,0,v28\nv_cvt_pk_bf16_f32 v29,0,v29\nv_cvt_pk_bf16_f32 v30,0,v30\nv_cvt_pk_bf16_f32 v31,0,v31\n" \
"v_pk_add_f32 v[24:25],v[24:25],v[8:9]\nv_pk_add_f32 v[26:27],v[26:27],v[10:11]\nv_pk_add_f32 v[28:29],v[28:29],v[12:13]\nv_pk_add_f32 v[30:31],v[30:31],v[14:15]\nv_cvt_pk_bf16_f32 v24,0,v24\nds_write_b16_d16_hi v244,v24 offset:1024\nv_cvt_pk_bf16_f32 v25,0,v25\nds_write_b16_d16_hi v244,v25 offset:1056\n" \
"v_cvt_pk_bf16_f32 v26,0,v26\nds_write_b16_d16_hi v244,v26 offset:1088\nv_cvt_pk_bf16_f32 v27,0,v27\nds_write_b16_d16_hi v244,v27 offset:1120\nv_cvt_pk_bf16_f32 v28,0,v28\nds_write_b16_d16_hi v244,v28 offset:1152\nv_cvt_pk_bf16_f32 v29,0,v29\nds_write_b16_d16_hi v244,v29 offset:1184\n" \
"v_cvt_pk_bf16_f32 v30,0,v30\nds_write_b16_d16_hi v244,v30 offset:1216\nv_cvt_pk_bf16_f32 v31,0,v31\nds_write_b16_d16_hi v244,v31 offset:1248\nds_read_b128 v[68:71],v245 offset:1024\ns_waitcnt lgkmcnt(0)\nbuffer_store_dwordx4 v[64:67],v240,s[44:47],s95 offen sc1\nbuffer_store_dwordx4 v[68:71],v240,s[44:47],s74 offen sc1\n" \
"s_cmp_eq_u32 s7,0\ns_cselect_b32 s70,1,0\ns_xor_b32 s71,s65,s66\ns_or_b32 s70,s70,s71\ns_cbranch_scc0 .Lfin\ns_lshr_b32 s70,s6,3\ns_and_b32 s70,s70,15\ns_lshl4_add_u32 s98,s60,s70\n" \
"v_lshrrev_b32 v242,4,v249\ns_lshl_b32 s71,s98,2\nv_add_u32 v242,s71,v242\nv_and_b32 v244,15,v242\nv_bfe_u32 v245,v242,4,3\nv_lshl_add_u32 v244,v244,3,v245\nv_and_b32 v245,0x180,v242\nv_add_lshl_u32 v243,v244,v245,1\n" \
"v_lshlrev_b32 v244,1,v243\ns_mov_b32 s70,4096\nv_lshlrev_b32 v242,1,v242\nbuffer_load_dword v62,v244,s[52:55],0 offen offset:0 sc1\nbuffer_load_dword v63,v244,s[52:55],0 offen offset:2048 sc1\nbuffer_load_dword v126,v244,s[52:55],s70 offen offset:0 sc1\nbuffer_load_dword v127,v244,s[52:55],s70 offen offset:2048 sc1\n.Lkp:\n" \
"s_cmp_lg_u32 s7,0\ns_cbranch_scc1 .Lkl\nv_max3_u32 v228,v233,v235,v237\nv_min3_u32 v229,v233,v235,v237\nv_max_u32 v228,v228,v239\nv_min_u32 v229,v229,v239\nv_cmp_lt_u32 vcc,s94,v228\nv_cmp_eq_u32 s[90:91],0,v229\n" \
"s_cbranch_vccnz .Lnew\ns_cmp_eq_u64 s[90:91],0\ns_cbranch_scc1 .Lv0\ns_sub_u32 s73,s73,1\ns_cselect_b32 s73,0,s73\ns_cbranch_scc1 .Lv0\ns_sleep 3\nbuffer_load_dwordx4 v[232:235],v251,s[52:55],s80 offen sc1\n" \
"buffer_load_dwordx4 v[236:239],v251,s[52:55],s80 offen offset:16 sc1\ns_waitcnt vmcnt(0)\ns_branch .Lkp\n.Lv0:\nv_mov_b32 v230,s96\ns_or_b32 s72,s94,2\nv_mov_b32 v231,s72\nds_write_b64 v250,v[230:231] offset:8\n" \
"s_branch .Lver\n.Lkl:\nds_read_b64 v[230:231],v250 offset:8\ns_mov_b32 s91,0\ns_waitcnt lgkmcnt(0)\nv_readfirstlane_b32 s72,v231\nv_readfirstlane_b32 s96,v230\ns_and_b32 s90,s72,2\n" \
"s_xor_b32 s72,s72,s90\ns_xor_b32 s90,s90,2\ns_cmp_lg_u32 s72,s94\ns_mov_b32 s94,s72\ns_cbranch_scc1 .Lsp\ns_cmp_eq_u32 s90,0\ns_cbranch_scc1 .Lver\ns_sub_u32 s73,s73,1\n" \
"s_cselect_b32 s73,0,s73\ns_cbranch_scc1 .Lver\ns_sleep 1\n.Lyield:\ns_branch .Lkl\n.Lnew:\nv_mov_b32 v230,v228\nv_cmp_eq_u32 vcc,v228,v233\n" \
"s_cmp_eq_u64 s[90:91],0\ns_cselect_b32 s71,2,0\nv_max_u32_dpp v230,v230,v230 quad_perm:[1,0,3,2] row_mask:0xf bank_mask:0xf\nv_cndmask_b32 v229,v238,v232,vcc\nv_cmp_eq_u32 vcc,v228,v235\nv_max_u32_dpp v230,v230,v230 quad_perm:[2,3,0,1] row_mask:0xf bank_mask:0xf\ns_nop 1\nv_max_u32_dpp v230,v230,v230 row_half_mirror row_mask:0xf bank_mask:0xf\n" \
"v_cndmask_b32 v229,v229,v234,vcc\nv_cmp_eq_u32 vcc,v228,v237\nv_max_u32_dpp v230,v230,v230 row_mirror row_mask:0xf bank_mask:0xf\nv_mov_b32 v231,v230\nv_cndmask_b32 v229,v229,v236,vcc\ns_nop 0\nv_permlane16_swap_b32 v230,v231\nv_max_u32 v230,v230,v231\n" \
"v_mov_b32 v231,v230\ns_nop 1\nv_permlane32_swap_b32 v230,v231\nv_max_u32 v230,v230,v231\ns_nop 0\nv_readfirstlane_b32 s94,v230\ns_nop 1\nv_cmp_eq_u32 vcc,s94,v228\n" \
"s_nop 0\ns_ff1_i32_b64 s72,vcc\nv_readlane_b32 s96,v229,s72\ns_nop 1\nv_mov_b32 v230,s96\ns_or_b32 s72,s94,s71\nv_mov_b32 v231,s72\nds_write_b64 v250,v[230:231] offset:8\n" \
".Lsp:\ns_not_b32 s15,s94\ns_bfe_u32 s15,s15,0xb0005\ns_lshl_b32 s15,s15,10\ns_cmp_eq_u32 s3,0\ns_cbranch_scc1 .Lkp\nv_add_u32 v70,114688,v241\ns_add_u32 s72,s15,54525952\n" \
"buffer_load_dword v41,v241,s[52:55],s72 offen sc1\ns_add_u32 s72,s15,56623104\nbuffer_load_dword v43,v241,s[52:55],s72 offen sc1\ns_add_u32 s72,s15,58720256\nbuffer_load_dword v45,v241,s[52:55],s72 offen sc1\ns_add_u32 s72,s15,60817408\nbuffer_load_dword v47,v241,s[52:55],s72 offen sc1\ns_add_u32 s72,s15,62914560\n" \
"buffer_load_dword v105,v241,s[52:55],s72 offen sc1\ns_add_u32 s72,s15,65011712\nbuffer_load_dword v107,v241,s[52:55],s72 offen sc1\ns_add_u32 s72,s15,67108864\nbuffer_load_dword v109,v241,s[52:55],s72 offen sc1\ns_add_u32 s72,s15,69206016\nbuffer_load_dword v111,v241,s[52:55],s72 offen sc1\nv_add_u32 v66,s15,v248\n" \
"global_load_ushort v64,v66,s[8:9]\nglobal_load_ushort v65,v66,s[8:9] offset:32\nbuffer_load_dwordx4 v[232:235],v251,s[52:55],s80 offen sc1\nbuffer_load_dwordx4 v[236:239],v251,s[52:55],s80 offen offset:16 sc1\nds_read_b32 v161,v70 offset:0\nds_read_b32 v163,v70 offset:1024\nds_read_b32 v165,v70 offset:2048\nds_read_b32 v167,v70 offset:3072\n" \
"ds_read_b32 v169,v70 offset:4096\nds_read_b32 v171,v70 offset:5120\nds_read_b32 v173,v70 offset:6144\nds_read_b32 v175,v70 offset:7168\nds_read_b32 v177,v70 offset:8192\nds_read_b32 v179,v70 offset:9216\nds_read_b32 v181,v70 offset:10240\nds_read_b32 v183,v70 offset:11264\n" \
"ds_read_b32 v185,v70 offset:12288\nds_read_b32 v187,v70 offset:13312\nds_read_b32 v189,v70 offset:14336\nds_read_b32 v191,v70 offset:15360\ns_waitcnt lgkmcnt(15)\nv_lshlrev_b32 v160,16,v161\nv_and_b32 v161,0xffff0000,v161\ns_waitcnt lgkmcnt(14)\n" \
"v_lshlrev_b32 v162,16,v163\nv_and_b32 v163,0xffff0000,v163\ns_waitcnt lgkmcnt(13)\nv_lshlrev_b32 v164,16,v165\nv_and_b32 v165,0xffff0000,v165\ns_waitcnt lgkmcnt(12)\nv_lshlrev_b32 v166,16,v167\nv_and_b32 v167,0xffff0000,v167\n" \
"s_waitcnt lgkmcnt(11)\nv_lshlrev_b32 v168,16,v169\nv_and_b32 v169,0xffff0000,v169\ns_waitcnt lgkmcnt(10)\nv_lshlrev_b32 v170,16,v171\nv_and_b32 v171,0xffff0000,v171\ns_waitcnt lgkmcnt(9)\nv_lshlrev_b32 v172,16,v173\n" \
"v_and_b32 v173,0xffff0000,v173\ns_waitcnt lgkmcnt(8)\nv_lshlrev_b32 v174,16,v175\nv_and_b32 v175,0xffff0000,v175\ns_waitcnt lgkmcnt(7)\nv_lshlrev_b32 v176,16,v177\nv_and_b32 v177,0xffff0000,v177\ns_waitcnt lgkmcnt(6)\n" \
"v_lshlrev_b32 v178,16,v179\nv_and_b32 v179,0xffff0000,v179\ns_waitcnt lgkmcnt(5)\nv_lshlrev_b32 v180,16,v181\nv_and_b32 v181,0xffff0000,v181\ns_waitcnt lgkmcnt(4)\nv_lshlrev_b32 v182,16,v183\nv_and_b32 v183,0xffff0000,v183\n" \
"s_waitcnt lgkmcnt(3)\nv_lshlrev_b32 v184,16,v185\nv_and_b32 v185,0xffff0000,v185\ns_waitcnt lgkmcnt(1)\nv_lshlrev_b32 v186,16,v187\nv_and_b32 v187,0xffff0000,v187\nv_lshlrev_b32 v188,16,v189\nv_and_b32 v189,0xffff0000,v189\n" \
"s_waitcnt lgkmcnt(0)\nv_lshlrev_b32 v190,16,v191\nv_and_b32 v191,0xffff0000,v191\ns_and_b32 s82,s96,0xffff0000\ns_lshl_b32 s83,s96,16\nv_rcp_f32 v68,s83\ns_waitcnt vmcnt(11)\nv_lshlrev_b32 v40,16,v41\n" \
"v_and_b32 v41,0xffff0000,v41\ns_waitcnt vmcnt(10)\nv_lshlrev_b32 v42,16,v43\nv_and_b32 v43,0xffff0000,v43\nv_pk_add_f32 v[40:41],v[40:41],v[42:43]\ns_waitcnt vmcnt(9)\nv_lshlrev_b32 v44,16,v45\nv_and_b32 v45,0xffff0000,v45\n" \
"s_waitcnt vmcnt(8)\nv_lshlrev_b32 v46,16,v47\nv_and_b32 v47,0xffff0000,v47\nv_pk_add_f32 v[44:45],v[44:45],v[46:47]\nv_pk_add_f32 v[40:41],v[40:41],v[44:45]\ns_waitcnt vmcnt(7)\nv_lshlrev_b32 v104,16,v105\nv_and_b32 v105,0xffff0000,v105\n" \
"s_waitcnt vmcnt(6)\nv_lshlrev_b32 v106,16,v107\nv_and_b32 v107,0xffff0000,v107\nv_pk_add_f32 v[104:105],v[104:105],v[106:107]\ns_waitcnt vmcnt(5)\nv_lshlrev_b32 v108,16,v109\nv_and_b32 v109,0xffff0000,v109\ns_waitcnt vmcnt(4)\n" \
"v_lshlrev_b32 v110,16,v111\nv_and_b32 v111,0xffff0000,v111\nv_pk_add_f32 v[108:109],v[108:109],v[110:111]\nv_pk_add_f32 v[104:105],v[104:105],v[108:109]\nv_pk_add_f32 v[40:41],v[40:41],v[104:105]\nv_cvt_pk_bf16_f32 v40,0,v40\nv_cvt_pk_bf16_f32 v41,0,v41\nv_pk_mul_f32 v[40:41],v[40:41],v[112:113]\n" \
"v_cvt_pk_bf16_f32 v40,0,v40\nv_cvt_pk_bf16_f32 v41,0,v41\ns_waitcnt vmcnt(3)\nv_lshlrev_b32 v64,16,v64\ns_waitcnt vmcnt(2)\nv_lshlrev_b32 v65,16,v65\nv_pk_add_f32 v[40:41],v[40:41],v[64:65]\nv_cvt_pk_bf16_f32 v40,0,v40\n" \
"v_cvt_pk_bf16_f32 v41,0,v41\nv_pk_add_f32 v[40:41],v[40:41],s[82:83] op_sel_hi:[1,0] neg_lo:[0,1] neg_hi:[0,1]\nv_cvt_pk_bf16_f32 v40,0,v40\nv_cvt_pk_bf16_f32 v41,0,v41\nv_pk_mul_f32 v[40:41],v[40:41],v[68:69] op_sel_hi:[1,0]\nv_cvt_pk_bf16_f32 v40,0,v40\nv_cvt_pk_bf16_f32 v41,0,v41\nv_pk_mul_f32 v[40:41],v[40:41],v[116:117]\n" \
"v_cvt_pk_bf16_f32 v40,0,v40\nv_cvt_pk_bf16_f32 v41,0,v41\nv_pk_add_f32 v[40:41],v[40:41],v[114:115]\nv_cvt_pk_bf16_f32 v40,v40,v41\ns_nop 1\nv_mov_b32_dpp v44,v40 quad_perm:[0,0,0,0] row_mask:0xf bank_mask:0xf\nv_mov_b32_dpp v45,v40 quad_perm:[1,1,1,1] row_mask:0xf bank_mask:0xf\nv_mov_b32_dpp v46,v40 quad_perm:[2,2,2,2] row_mask:0xf bank_mask:0xf\n" \
"v_mov_b32_dpp v47,v40 quad_perm:[3,3,3,3] row_mask:0xf bank_mask:0xf\nv_dot2_f32_bf16 v120,v192,v44,0\nv_dot2_f32_bf16 v121,v193,v44,0\nv_dot2_f32_bf16 v122,v194,v44,0\nv_dot2_f32_bf16 v123,v195,v44,0\nv_dot2_f32_bf16 v124,v208,v44,0\nv_dot2_f32_bf16 v125,v209,v44,0\nv_dot2_f32_bf16 v126,v210,v44,0\n" \
"v_dot2_f32_bf16 v127,v211,v44,0\nv_dot2_f32_bf16 v120,v196,v45,v120\nv_dot2_f32_bf16 v121,v197,v45,v121\nv_dot2_f32_bf16 v122,v198,v45,v122\nv_dot2_f32_bf16 v123,v199,v45,v123\nv_dot2_f32_bf16 v124,v212,v45,v124\nv_dot2_f32_bf16 v125,v213,v45,v125\nv_dot2_f32_bf16 v126,v214,v45,v126\n" \
"v_dot2_f32_bf16 v127,v215,v45,v127\nv_dot2_f32_bf16 v120,v200,v46,v120\nv_dot2_f32_bf16 v121,v201,v46,v121\nv_dot2_f32_bf16 v122,v202,v46,v122\nv_dot2_f32_bf16 v123,v203,v46,v123\nv_dot2_f32_bf16 v124,v216,v46,v124\nv_dot2_f32_bf16 v125,v217,v46,v125\nv_dot2_f32_bf16 v126,v218,v46,v126\n" \
"v_dot2_f32_bf16 v127,v219,v46,v127\nv_dot2_f32_bf16 v120,v204,v47,v120\nv_dot2_f32_bf16 v121,v205,v47,v121\nv_dot2_f32_bf16 v122,v206,v47,v122\nv_dot2_f32_bf16 v123,v207,v47,v123\nv_dot2_f32_bf16 v124,v220,v47,v124\nv_dot2_f32_bf16 v125,v221,v47,v125\nv_dot2_f32_bf16 v126,v222,v47,v126\n" \
"v_dot2_f32_bf16 v127,v223,v47,v127\nv_permlane32_swap_b32 v120,v124\nv_add_f32 v120,v120,v124\nv_permlane32_swap_b32 v121,v125\nv_add_f32 v121,v121,v125\nv_permlane32_swap_b32 v122,v126\nv_add_f32 v122,v122,v126\nv_permlane32_swap_b32 v123,v127\n" \
"v_add_f32 v123,v123,v127\nv_permlane16_swap_b32 v120,v122\nv_add_f32 v120,v120,v122\nv_permlane16_swap_b32 v121,v123\nv_add_f32 v121,v121,v123\nv_add_f32_dpp v120,v120,v120 row_ror:4 row_mask:0xf bank_mask:0xf\ns_nop 1\nv_add_f32_dpp v120,v120,v120 row_ror:8 row_mask:0xf bank_mask:0xf\n" \
"v_add_f32_dpp v121,v121,v121 row_ror:4 row_mask:0xf bank_mask:0xf\ns_nop 1\nv_add_f32_dpp v121,v121,v121 row_ror:8 row_mask:0xf bank_mask:0xf\ns_cmp_eq_u64 s[90:91],0\ns_cbranch_scc1 .Lver\ns_waitcnt vmcnt(0)\ns_branch .Lkp\n.Lver:\n" \
"s_cmp_eq_u32 s3,0\ns_cbranch_scc1 .Lnf\nds_write_b32 v252,v120\nds_write_b32 v252,v121 offset:16\ns_lshr_b32 s70,s2,1\nv_and_b32 v64,4,v241\nv_lshrrev_b32 v241,3,v241\nv_lshlrev_b32 v241,11,v241\n" \
"v_lshl_or_b32 v241,v64,1,v241\ns_add_u32 s70,s70,98304\ns_waitcnt lgkmcnt(0)\ns_barrier\nds_read_b128 v[40:43],v253\nds_read_b128 v[44:47],v253 offset:256\ns_waitcnt lgkmcnt(1)\nv_pk_add_f32 v[40:41],v[40:41],v[42:43]\n" \
"v_add_f32 v40,v40,v41\nv_cvt_pk_bf16_f32 v40,0,v40\nv_mul_f32 v41,0xbfb8aa3b,v40\nv_exp_f32 v41,v41\ns_waitcnt lgkmcnt(0)\nv_pk_add_f32 v[44:45],v[44:45],v[46:47]\nv_add_f32 v41,1.0,v41\nv_rcp_f32 v41,v41\n" \
"v_add_f32 v44,v44,v45\nv_cvt_pk_bf16_f32 v44,0,v44\nv_mul_f32 v40,v40,v41\nv_cvt_pk_bf16_f32 v40,0,v40\nv_mul_f32 v40,v40,v44\nv_cvt_pk_bf16_f32 v40,0,v40\ns_nop 0\nv_readlane_b32 s8,v40,0\n" \
"v_readlane_b32 s9,v40,1\nv_readlane_b32 s10,v40,2\nv_readlane_b32 s11,v40,3\nv_readlane_b32 s12,v40,4\nv_readlane_b32 s13,v40,5\nv_readlane_b32 s14,v40,6\nv_readlane_b32 s15,v40,7\nv_readlane_b32 s16,v40,8\n" \
"v_readlane_b32 s17,v40,9\nv_readlane_b32 s18,v40,10\nv_readlane_b32 s19,v40,11\nv_readlane_b32 s20,v40,12\nv_readlane_b32 s21,v40,13\nv_readlane_b32 s22,v40,14\nv_readlane_b32 s23,v40,15\nv_pk_mul_f32 v[120:121],v[160:161],s[8:9] op_sel_hi:[1,0]\n" \
"v_pk_mul_f32 v[122:123],v[162:163],s[8:9] op_sel:[0,1]\nv_pk_fma_f32 v[120:121],v[164:165],s[10:11],v[120:121] op_sel_hi:[1,0,1]\nv_pk_fma_f32 v[122:123],v[166:167],s[10:11],v[122:123] op_sel:[0,1,0]\nv_pk_fma_f32 v[120:121],v[168:169],s[12:13],v[120:121] op_sel_hi:[1,0,1]\nv_pk_fma_f32 v[122:123],v[170:171],s[12:13],v[122:123] op_sel:[0,1,0]\nv_pk_fma_f32 v[120:121],v[172:173],s[14:15],v[120:121] op_sel_hi:[1,0,1]\nv_pk_fma_f32 v[122:123],v[174:175],s[14:15],v[122:123] op_sel:[0,1,0]\nv_pk_fma_f32 v[120:121],v[176:177],s[16:17],v[120:121] op_sel_hi:[1,0,1]\n" \
"v_pk_fma_f32 v[122:123],v[178:179],s[16:17],v[122:123] op_sel:[0,1,0]\nv_pk_fma_f32 v[120:121],v[180:181],s[18:19],v[120:121] op_sel_hi:[1,0,1]\nv_pk_fma_f32 v[122:123],v[182:183],s[18:19],v[122:123] op_sel:[0,1,0]\nv_pk_fma_f32 v[120:121],v[184:185],s[20:21],v[120:121] op_sel_hi:[1,0,1]\nv_pk_fma_f32 v[122:123],v[186:187],s[20:21],v[122:123] op_sel:[0,1,0]\nv_pk_fma_f32 v[120:121],v[188:189],s[22:23],v[120:121] op_sel_hi:[1,0,1]\nv_pk_fma_f32 v[122:123],v[190:191],s[22:23],v[122:123] op_sel:[0,1,0]\nv_pk_add_f32 v[120:121],v[120:121],v[122:123]\n" \
"v_min_u32 v120,-2,v120\nv_min_u32 v121,-2,v121\nbuffer_store_dwordx2 v[120:121],v241,s[52:55],s70 offen sc1\nv_pk_mul_f32 v[88:89],v[88:89],v[48:49]\nv_pk_mul_f32 v[90:91],v[90:91],v[50:51]\nv_pk_mul_f32 v[92:93],v[92:93],v[52:53]\nv_pk_mul_f32 v[94:95],v[94:95],v[54:55]\nv_cvt_pk_bf16_f32 v88,0,v88\n" \
"v_cvt_pk_bf16_f32 v89,0,v89\nv_cvt_pk_bf16_f32 v90,0,v90\nv_cvt_pk_bf16_f32 v91,0,v91\nv_cvt_pk_bf16_f32 v92,0,v92\nv_cvt_pk_bf16_f32 v93,0,v93\nv_cvt_pk_bf16_f32 v94,0,v94\nv_cvt_pk_bf16_f32 v95,0,v95\nv_pk_add_f32 v[88:89],v[88:89],v[72:73]\n" \
"v_pk_add_f32 v[90:91],v[90:91],v[74:75]\nv_pk_add_f32 v[92:93],v[92:93],v[76:77]\nv_pk_add_f32 v[94:95],v[94:95],v[78:79]\nv_cvt_pk_bf16_f32 v88,0,v88\nds_write_b16_d16_hi v244,v88 offset:0\nv_cvt_pk_bf16_f32 v89,0,v89\nds_write_b16_d16_hi v244,v89 offset:32\nv_cvt_pk_bf16_f32 v90,0,v90\n" \
"ds_write_b16_d16_hi v244,v90 offset:64\nv_cvt_pk_bf16_f32 v91,0,v91\nds_write_b16_d16_hi v244,v91 offset:96\nv_cvt_pk_bf16_f32 v92,0,v92\nds_write_b16_d16_hi v244,v92 offset:128\nv_cvt_pk_bf16_f32 v93,0,v93\nds_write_b16_d16_hi v244,v93 offset:160\nv_cvt_pk_bf16_f32 v94,0,v94\n" \
"ds_write_b16_d16_hi v244,v94 offset:192\nv_cvt_pk_bf16_f32 v95,0,v95\nds_write_b16_d16_hi v244,v95 offset:224\nds_read_b128 v[64:67],v245 offset:0\nv_pk_mul_f32 v[24:25],v[24:25],v[48:49]\nv_pk_mul_f32 v[26:27],v[26:27],v[50:51]\nv_pk_mul_f32 v[28:29],v[28:29],v[52:53]\nv_pk_mul_f32 v[30:31],v[30:31],v[54:55]\n" \
"v_cvt_pk_bf16_f32 v24,0,v24\nv_cvt_pk_bf16_f32 v25,0,v25\nv_cvt_pk_bf16_f32 v26,0,v26\nv_cvt_pk_bf16_f32 v27,0,v27\nv_cvt_pk_bf16_f32 v28,0,v28\nv_cvt_pk_bf16_f32 v29,0,v29\nv_cvt_pk_bf16_f32 v30,0,v30\nv_cvt_pk_bf16_f32 v31,0,v31\n" \
"v_pk_add_f32 v[24:25],v[24:25],v[8:9]\nv_pk_add_f32 v[26:27],v[26:27],v[10:11]\nv_pk_add_f32 v[28:29],v[28:29],v[12:13]\nv_pk_add_f32 v[30:31],v[30:31],v[14:15]\nv_cvt_pk_bf16_f32 v24,0,v24\nds_write_b16_d16_hi v244,v24 offset:1024\nv_cvt_pk_bf16_f32 v25,0,v25\nds_write_b16_d16_hi v244,v25 offset:1056\n" \
"v_cvt_pk_bf16_f32 v26,0,v26\nds_write_b16_d16_hi v244,v26 offset:1088\nv_cvt_pk_bf16_f32 v27,0,v27\nds_write_b16_d16_hi v244,v27 offset:1120\nv_cvt_pk_bf16_f32 v28,0,v28\nds_write_b16_d16_hi v244,v28 offset:1152\nv_cvt_pk_bf16_f32 v29,0,v29\nds_write_b16_d16_hi v244,v29 offset:1184\n" \
"v_cvt_pk_bf16_f32 v30,0,v30\nds_write_b16_d16_hi v244,v30 offset:1216\nv_cvt_pk_bf16_f32 v31,0,v31\nds_write_b16_d16_hi v244,v31 offset:1248\nds_read_b128 v[68:71],v245 offset:1024\ns_waitcnt lgkmcnt(0)\nbuffer_store_dwordx4 v[64:67],v240,s[44:47],s95 offen sc1\nbuffer_store_dwordx4 v[68:71],v240,s[44:47],s74 offen sc1\n" \
".Lnf:\ns_cmp_eq_u32 s94,s97\ns_cbranch_scc1 .Lwin\nbuffer_store_dwordx4 v[64:67],v240,s[44:47],s65 offen sc1\nbuffer_store_dwordx4 v[68:71],v240,s[44:47],s66 offen sc1\n.Lwin:\ns_cmp_lg_u32 s38,0\ns_cbranch_scc1 .Lfin\n" \
"s_add_u32 s72,s15,54525952\nbuffer_load_ushort v128,v243,s[52:55],s72 offen sc1\ns_add_u32 s72,s15,56623104\nbuffer_load_ushort v129,v243,s[52:55],s72 offen sc1\ns_add_u32 s72,s15,58720256\nbuffer_load_ushort v130,v243,s[52:55],s72 offen sc1\ns_add_u32 s72,s15,60817408\nbuffer_load_ushort v131,v243,s[52:55],s72 offen sc1\n" \
"s_add_u32 s72,s15,62914560\nbuffer_load_ushort v132,v243,s[52:55],s72 offen sc1\ns_add_u32 s72,s15,65011712\nbuffer_load_ushort v133,v243,s[52:55],s72 offen sc1\ns_add_u32 s72,s15,67108864\nbuffer_load_ushort v134,v243,s[52:55],s72 offen sc1\ns_add_u32 s72,s15,69206016\nbuffer_load_ushort v135,v243,s[52:55],s72 offen sc1\n" \
"v_add_u32 v242,s15,v242\nglobal_load_ushort v144,v242,s[8:9]\nv_min_u32 v168,55,v249\nv_lshlrev_b32 v168,4,v168\ns_lshl_b32 s70,s98,11\ns_add_u32 s70,s70,98304\ns_add_u32 s71,s70,896\ns_and_b32 s82,s96,0xffff0000\n" \
"s_lshl_b32 s83,s96,16\nv_rcp_f32 v148,s83\ns_ashr_i32 s37,s94,31\ns_not_b32 s37,s37\ns_or_b32 s37,s37,0x80000000\ns_xor_b32 s37,s37,s94\ns_and_b32 s37,s37,0xffff0000\nv_mov_b32 v149,0xbfb8aa3b\n" \
"v_mul_f32 v149,s37,v149\nv_exp_f32 v149,v149\nv_and_b32 v150,15,v249\nv_add_f32 v149,1.0,v149\nv_rcp_f32 v149,v149\nv_cmp_eq_u32 vcc,0,v150\nv_cvt_pk_bf16_f32 v149,0,v149\nv_mov_b32 v150,0x40000000\n" \
"v_cndmask_b32 v150,v150,v242,vcc\nv_cmp_gt_u32 s[90:91],56,v249\ns_waitcnt vmcnt(8)\nv_lshlrev_b32 v128,16,v128\ns_waitcnt vmcnt(7)\nv_lshlrev_b32 v129,16,v129\nv_add_f32 v128,v128,v129\ns_waitcnt vmcnt(6)\n" \
"v_lshlrev_b32 v130,16,v130\ns_waitcnt vmcnt(5)\nv_lshlrev_b32 v131,16,v131\nv_add_f32 v130,v130,v131\nv_add_f32 v128,v128,v130\ns_waitcnt vmcnt(4)\nv_lshlrev_b32 v132,16,v132\ns_waitcnt vmcnt(3)\n" \
"v_lshlrev_b32 v133,16,v133\nv_add_f32 v132,v132,v133\ns_waitcnt vmcnt(2)\nv_lshlrev_b32 v134,16,v134\ns_waitcnt vmcnt(1)\nv_lshlrev_b32 v135,16,v135\nv_add_f32 v134,v134,v135\nv_add_f32 v132,v132,v134\n" \
"v_add_f32 v128,v128,v132\nv_cvt_pk_bf16_f32 v128,0,v128\nv_mul_f32 v128,v128,v62\nv_cvt_pk_bf16_f32 v128,0,v128\ns_waitcnt vmcnt(0)\nv_add_f32 v126,1.0,v126\nv_cvt_pk_bf16_f32 v126,0,v126\nv_lshlrev_b32 v144,16,v144\n" \
"v_add_f32 v128,v128,v144\nv_cvt_pk_bf16_f32 v128,0,v128\nv_subrev_f32 v129,s82,v128\nv_cvt_pk_bf16_f32 v129,0,v129\nv_mul_f32 v129,v129,v148\nv_cvt_pk_bf16_f32 v129,0,v129\nv_mul_f32 v129,v129,v126\nv_cvt_pk_bf16_f32 v129,0,v129\n" \
"v_add_f32 v129,v129,v63\nv_cvt_pk_bf16_f32 v129,0,v129\n.Lp2:\nbuffer_load_dwordx4 v[160:163],v168,s[52:55],s70 offen sc1\nbuffer_load_dwordx4 v[164:167],v168,s[52:55],s71 offen sc1\ns_waitcnt vmcnt(0)\nv_max3_u32 v228,v160,v161,v162\nv_max3_u32 v229,v163,v164,v165\n" \
"v_max3_u32 v228,v228,v166,v167\nv_max_u32 v228,v228,v229\nv_pk_add_f32 v[160:161],v[160:161],v[164:165]\nv_cmp_eq_u32 vcc,-1,v228\nv_pk_add_f32 v[162:163],v[162:163],v[166:167]\ns_sub_u32 s73,s73,1\ns_cselect_b32 s73,0,s73\ns_cselect_b64 vcc,0,vcc\n" \
"s_cmp_lg_u64 vcc,0\ns_cselect_b32 s72,0x8000000,0\nv_cndmask_b32 v160,0,v160,s[90:91]\nv_cndmask_b32 v161,0,v161,s[90:91]\nv_cndmask_b32 v162,0,v162,s[90:91]\nv_cndmask_b32 v163,0,v163,s[90:91]\ns_nop 0\nv_permlane32_swap_b32 v160,v162\n" \
"v_permlane32_swap_b32 v161,v163\nv_pk_add_f32 v[160:161],v[160:161],v[162:163]\ns_nop 1\nv_permlane16_swap_b32 v160,v161\nv_add_f32 v160,v160,v161\ns_nop 1\nv_add_f32_dpp v160,v160,v160 quad_perm:[1,0,3,2] row_mask:0xf bank_mask:0xf\ns_nop 1\n" \
"v_add_f32_dpp v160,v160,v160 quad_perm:[2,3,0,1] row_mask:0xf bank_mask:0xf\ns_nop 1\nv_add_f32_dpp v160,v160,v160 row_half_mirror row_mask:0xf bank_mask:0xf\ns_nop 1\nv_add_f32_dpp v160,v160,v160 row_mirror row_mask:0xf bank_mask:0xf\nv_cvt_pk_bf16_f32 v160,0,v160\nv_mul_f32 v160,v160,v149\nv_cvt_pk_bf16_f32 v160,0,v160\n" \
"v_add_f32 v160,v160,v129\nv_cvt_pk_bf16_f32 v160,0,v160\nv_mul_f32 v160,v160,v127\nv_cvt_pk_bf16_f32 v160,0,v160\nv_add_f32 v160,v160,v128\nv_cvt_pk_bf16_f32 v160,0,v160\nbuffer_store_short_d16_hi v160,v150,s[44:47],s72 offen sc1\n.Lp2s:\n" \
"s_cbranch_scc1 .Lp2\ns_branch .Lfin\n.Lhs:\ns_sub_u32 s73,s73,1\ns_cselect_b32 s73,0,s73\ns_cbranch_scc1 .Lhb\ns_sleep 2\nbuffer_load_dwordx2 v[166:167],v165,s[52:55],s80 offen sc1\n" \
"s_waitcnt vmcnt(0)\nv_xor_b32 v166,v166,v230\nv_xor_b32 v167,v167,v231\nv_or_b32 v166,v166,v167\nv_cmp_ne_u32 vcc,0,v166\ns_nop 0\ns_cbranch_vccnz .Lhs\ns_branch .Lhb\n" \
".Lps0:\ns_sub_u32 s73,s73,1\ns_cselect_b32 s73,0,s73\ns_cbranch_scc1 .Lpb0\ns_sleep 1\nbuffer_load_dword v248,v228,s[52:55],s80 offen sc1\ns_waitcnt vmcnt(0)\nv_cmp_eq_u32 vcc,0,v248\n" \
"s_nop 0\ns_cbranch_vccnz .Lps0\ns_branch .Lpb0\n.Lps1:\ns_sub_u32 s73,s73,1\ns_cselect_b32 s73,0,s73\ns_cbranch_scc1 .Lpb1\ns_sleep 1\n" \
"buffer_load_dword v248,v228,s[52:55],s80 offen sc1\ns_waitcnt vmcnt(0)\nv_cmp_eq_u32 vcc,0,v248\ns_nop 0\ns_cbranch_vccnz .Lps1\ns_branch .Lpb1\n.Lps2:\ns_sub_u32 s73,s73,1\n" \
"s_cselect_b32 s73,0,s73\ns_cbranch_scc1 .Lpb2\ns_sleep 1\nbuffer_load_dword v248,v251,s[52:55],s80 offen sc1\ns_waitcnt vmcnt(0)\nv_cmp_eq_u32 vcc,0,v248\ns_nop 0\ns_cbranch_vccnz .Lps2\n" \
"s_branch .Lpb2\n.Lps3:\ns_sub_u32 s73,s73,1\ns_cselect_b32 s73,0,s73\ns_cbranch_scc1 .Lpb3\ns_sleep 1\nbuffer_load_dword v248,v251,s[52:55],s80 offen sc1\ns_waitcnt vmcnt(0)\n" \
"v_cmp_eq_u32 vcc,0,v248\ns_nop 0\ns_cbranch_vccnz .Lps3\ns_branch .Lpb3\n.Lps4:\ns_sub_u32 s73,s73,1\ns_cselect_b32 s73,0,s73\ns_cbranch_scc1 .Lpb4\n" \
"s_sleep 1\nbuffer_load_dword v248,v251,s[52:55],s80 offen sc1\ns_waitcnt vmcnt(0)\nv_cmp_eq_u32 vcc,0,v248\ns_nop 0\ns_cbranch_vccnz .Lps4\ns_branch .Lpb4\n.Lps5:\n" \
"s_sub_u32 s73,s73,1\ns_cselect_b32 s73,0,s73\ns_cbranch_scc1 .Lpb5\ns_sleep 1\nbuffer_load_dword v248,v251,s[52:55],s80 offen sc1\ns_waitcnt vmcnt(0)\nv_cmp_eq_u32 vcc,0,v248\ns_nop 0\n" \
"s_cbranch_vccnz .Lps5\ns_branch .Lpb5\n.Lx01:\ns_cmp_eq_u32 s62,0\ns_cbranch_scc1 .Ltrans\ns_branch .Ldf\n.Lps6:\ns_sub_u32 s73,s73,1\n" \
"s_cselect_b32 s73,0,s73\ns_cbranch_scc1 .Lpb6\ns_sleep 1\nbuffer_load_dword v248,v251,s[52:55],s80 offen sc1\ns_waitcnt vmcnt(0)\nv_and_b32 v248,s17,v248\nv_cmp_eq_u32 vcc,0,v248\ns_nop 0\n" \
"s_cbranch_vccnz .Lps6\ns_branch .Lpb6\n.Lps7:\ns_sub_u32 s73,s73,1\ns_cselect_b32 s73,0,s73\ns_cbranch_scc1 .Lpb7\ns_sleep 1\nbuffer_load_dword v248,v254,s[52:55],s80 offen sc1\n" \
"s_waitcnt vmcnt(0)\nv_cmp_eq_u32 vcc,0,v248\ns_nop 0\ns_cbranch_vccnz .Lps7\ns_branch .Lpb7\n.Lps8:\ns_sub_u32 s73,s73,1\ns_cselect_b32 s73,0,s73\n" \
"s_cbranch_scc1 .Lpb8\ns_sleep 1\nbuffer_load_dword v248,v251,s[52:55],s79 offen sc1\ns_waitcnt vmcnt(0)\nv_cmp_eq_u32 vcc,0,v248\ns_nop 0\ns_cbranch_vccnz .Lps8\ns_branch .Lpb8\n" \
".Lrs1:\ns_mov_b32 s71,0\n.Lrl1:\ns_mov_b32 s97,0\nv_max3_u32 v124,v160,v161,v162\nv_max3_u32 v124,v124,v163,v163\nv_cmp_eq_u32 vcc,-1,v124\ns_add_u32 s72,s95,0\n" \
"s_cbranch_vccz .Lrz10\ns_mov_b64 exec,vcc\ns_mov_b32 s97,1\nbuffer_load_dwordx4 v[160:163],v240,s[52:55],s72 offen sc1\ns_mov_b64 exec,-1\n.Lrz10:\nv_max3_u32 v124,v164,v165,v166\nv_max3_u32 v124,v124,v167,v167\n" \
"v_cmp_eq_u32 vcc,-1,v124\ns_add_u32 s72,s95,2097152\ns_cbranch_vccz .Lrz11\ns_mov_b64 exec,vcc\ns_mov_b32 s97,1\nbuffer_load_dwordx4 v[164:167],v240,s[52:55],s72 offen sc1\ns_mov_b64 exec,-1\n.Lrz11:\n" \
"v_max3_u32 v124,v168,v169,v170\nv_max3_u32 v124,v124,v171,v171\nv_cmp_eq_u32 vcc,-1,v124\ns_add_u32 s72,s95,4194304\ns_cbranch_vccz .Lrz12\ns_mov_b64 exec,vcc\ns_mov_b32 s97,1\nbuffer_load_dwordx4 v[168:171],v240,s[52:55],s72 offen sc1\n" \
"s_mov_b64 exec,-1\n.Lrz12:\nv_max3_u32 v124,v172,v173,v174\nv_max3_u32 v124,v124,v175,v175\nv_cmp_eq_u32 vcc,-1,v124\ns_add_u32 s72,s95,6291456\ns_cbranch_vccz .Lrz13\ns_mov_b64 exec,vcc\n" \
"s_mov_b32 s97,1\nbuffer_load_dwordx4 v[172:175],v240,s[52:55],s72 offen sc1\ns_mov_b64 exec,-1\n.Lrz13:\nv_max3_u32 v124,v176,v177,v178\nv_max3_u32 v124,v124,v179,v179\nv_cmp_eq_u32 vcc,-1,v124\ns_add_u32 s72,s95,8388608\n" \
"s_cbranch_vccz .Lrz14\ns_mov_b64 exec,vcc\ns_mov_b32 s97,1\nbuffer_load_dwordx4 v[176:179],v240,s[52:55],s72 offen sc1\ns_mov_b64 exec,-1\n.Lrz14:\nv_max3_u32 v124,v180,v181,v182\nv_max3_u32 v124,v124,v183,v183\n" \
"v_cmp_eq_u32 vcc,-1,v124\ns_add_u32 s72,s95,10485760\ns_cbranch_vccz .Lrz15\ns_mov_b64 exec,vcc\ns_mov_b32 s97,1\nbuffer_load_dwordx4 v[180:183],v240,s[52:55],s72 offen sc1\ns_mov_b64 exec,-1\n.Lrz15:\n" \
"v_max3_u32 v124,v184,v185,v186\nv_max3_u32 v124,v124,v187,v187\nv_cmp_eq_u32 vcc,-1,v124\ns_add_u32 s72,s95,12582912\ns_cbranch_vccz .Lrz16\ns_mov_b64 exec,vcc\ns_mov_b32 s97,1\nbuffer_load_dwordx4 v[184:187],v240,s[52:55],s72 offen sc1\n" \
"s_mov_b64 exec,-1\n.Lrz16:\nv_max3_u32 v124,v188,v189,v190\nv_max3_u32 v124,v124,v191,v191\nv_cmp_eq_u32 vcc,-1,v124\ns_add_u32 s72,s95,14680064\ns_cbranch_vccz .Lrz17\ns_mov_b64 exec,vcc\n" \
"s_mov_b32 s97,1\nbuffer_load_dwordx4 v[188:191],v240,s[52:55],s72 offen sc1\ns_mov_b64 exec,-1\n.Lrz17:\ns_cmp_lg_u32 s97,0\ns_cbranch_scc1 .Lrm1\ns_cmp_eq_u32 s71,0\ns_cbranch_scc1 .Lrb1\n" \
"s_branch .Lrow1\n.Lrm1:\ns_mov_b32 s71,1\ns_waitcnt vmcnt(0)\ns_sub_u32 s73,s73,1\ns_cselect_b32 s73,0,s73\ns_cbranch_scc1 .Lrb1\ns_sleep 2\n" \
"s_branch .Lrl1\n.Lrs0:\ns_mov_b32 s71,0\n.Lrl0:\ns_mov_b32 s97,0\nv_max3_u32 v60,v128,v129,v130\nv_max3_u32 v60,v60,v131,v131\nv_cmp_eq_u32 vcc,-1,v60\n" \
"s_add_u32 s72,s74,0\ns_cbranch_vccz .Lrz00\ns_mov_b64 exec,vcc\ns_mov_b32 s97,1\nbuffer_load_dwordx4 v[128:131],v240,s[52:55],s72 offen sc1\ns_mov_b64 exec,-1\n.Lrz00:\nv_max3_u32 v60,v132,v133,v134\n" \
"v_max3_u32 v60,v60,v135,v135\nv_cmp_eq_u32 vcc,-1,v60\ns_add_u32 s72,s74,2097152\ns_cbranch_vccz .Lrz01\ns_mov_b64 exec,vcc\ns_mov_b32 s97,1\nbuffer_load_dwordx4 v[132:135],v240,s[52:55],s72 offen sc1\ns_mov_b64 exec,-1\n" \
".Lrz01:\nv_max3_u32 v60,v136,v137,v138\nv_max3_u32 v60,v60,v139,v139\nv_cmp_eq_u32 vcc,-1,v60\ns_add_u32 s72,s74,4194304\ns_cbranch_vccz .Lrz02\ns_mov_b64 exec,vcc\ns_mov_b32 s97,1\n" \
"buffer_load_dwordx4 v[136:139],v240,s[52:55],s72 offen sc1\ns_mov_b64 exec,-1\n.Lrz02:\nv_max3_u32 v60,v140,v141,v142\nv_max3_u32 v60,v60,v143,v143\nv_cmp_eq_u32 vcc,-1,v60\ns_add_u32 s72,s74,6291456\ns_cbranch_vccz .Lrz03\n" \
"s_mov_b64 exec,vcc\ns_mov_b32 s97,1\nbuffer_load_dwordx4 v[140:143],v240,s[52:55],s72 offen sc1\ns_mov_b64 exec,-1\n.Lrz03:\nv_max3_u32 v60,v144,v145,v146\nv_max3_u32 v60,v60,v147,v147\nv_cmp_eq_u32 vcc,-1,v60\n" \
"s_add_u32 s72,s74,8388608\ns_cbranch_vccz .Lrz04\ns_mov_b64 exec,vcc\ns_mov_b32 s97,1\nbuffer_load_dwordx4 v[144:147],v240,s[52:55],s72 offen sc1\ns_mov_b64 exec,-1\n.Lrz04:\nv_max3_u32 v60,v148,v149,v150\n" \
"v_max3_u32 v60,v60,v151,v151\nv_cmp_eq_u32 vcc,-1,v60\ns_add_u32 s72,s74,10485760\ns_cbranch_vccz .Lrz05\ns_mov_b64 exec,vcc\ns_mov_b32 s97,1\nbuffer_load_dwordx4 v[148:151],v240,s[52:55],s72 offen sc1\ns_mov_b64 exec,-1\n" \
".Lrz05:\nv_max3_u32 v60,v152,v153,v154\nv_max3_u32 v60,v60,v155,v155\nv_cmp_eq_u32 vcc,-1,v60\ns_add_u32 s72,s74,12582912\ns_cbranch_vccz .Lrz06\ns_mov_b64 exec,vcc\ns_mov_b32 s97,1\n" \
"buffer_load_dwordx4 v[152:155],v240,s[52:55],s72 offen sc1\ns_mov_b64 exec,-1\n.Lrz06:\nv_max3_u32 v60,v156,v157,v158\nv_max3_u32 v60,v60,v159,v159\nv_cmp_eq_u32 vcc,-1,v60\ns_add_u32 s72,s74,14680064\ns_cbranch_vccz .Lrz07\n" \
"s_mov_b64 exec,vcc\ns_mov_b32 s97,1\nbuffer_load_dwordx4 v[156:159],v240,s[52:55],s72 offen sc1\ns_mov_b64 exec,-1\n.Lrz07:\ns_cmp_lg_u32 s97,0\ns_cbranch_scc1 .Lrm0\ns_cmp_eq_u32 s71,0\n" \
"s_cbranch_scc1 .Lrb0\ns_branch .Lrow0\n.Lrm0:\ns_mov_b32 s71,1\ns_waitcnt vmcnt(0)\ns_sub_u32 s73,s73,1\ns_cselect_b32 s73,0,s73\ns_cbranch_scc1 .Lrb0\n" \
"s_sleep 2\ns_branch .Lrl0\n.Lredo:\ns_waitcnt vmcnt(0) lgkmcnt(0)\ns_barrier\ns_mov_b64 s[90:91],0\ns_mov_b32 s99,0\ns_mov_b32 s17,1\n" \
"s_lshl_b32 s70,s63,19\ns_add_u32 s37,s70,3145728\ns_lshl_b32 s71,s62,15\ns_add_u32 s72,s37,s64\ns_add_u32 s72,s72,491520\ns_sub_u32 s72,s72,s71\ns_add_u32 m0,s64,0\ns_nop 0\n" \
"buffer_load_dwordx4 v240,s[52:55],s72 offen offset:0 sc1 lds\nbuffer_load_dwordx4 v240,s[52:55],s72 offen offset:1024 sc1 lds\nbuffer_load_dwordx4 v240,s[52:55],s72 offen offset:2048 sc1 lds\nbuffer_load_dwordx4 v240,s[52:55],s72 offen offset:3072 sc1 lds\ns_add_u32 m0,s64,65536\ns_add_u32 s72,s72,16777216\nbuffer_load_dwordx4 v240,s[52:55],s72 offen offset:0 sc1 lds\nbuffer_load_dwordx4 v240,s[52:55],s72 offen offset:1024 sc1 lds\n" \
"buffer_load_dwordx4 v240,s[52:55],s72 offen offset:2048 sc1 lds\nbuffer_load_dwordx4 v240,s[52:55],s72 offen offset:3072 sc1 lds\ns_add_u32 s72,s37,s64\ns_add_u32 s72,s72,507904\ns_sub_u32 s72,s72,s71\ns_add_u32 m0,s64,16384\ns_nop 0\nbuffer_load_dwordx4 v240,s[52:55],s72 offen offset:0 sc1 lds\n" \
"buffer_load_dwordx4 v240,s[52:55],s72 offen offset:1024 sc1 lds\nbuffer_load_dwordx4 v240,s[52:55],s72 offen offset:2048 sc1 lds\nbuffer_load_dwordx4 v240,s[52:55],s72 offen offset:3072 sc1 lds\ns_add_u32 m0,s64,81920\ns_add_u32 s72,s72,16777216\nbuffer_load_dwordx4 v240,s[52:55],s72 offen offset:0 sc1 lds\nbuffer_load_dwordx4 v240,s[52:55],s72 offen offset:1024 sc1 lds\nbuffer_load_dwordx4 v240,s[52:55],s72 offen offset:2048 sc1 lds\n" \
"buffer_load_dwordx4 v240,s[52:55],s72 offen offset:3072 sc1 lds\ns_add_u32 s72,s37,s64\ns_add_u32 s72,s72,16384\ns_add_u32 s72,s72,s71\ns_add_u32 m0,s64,32768\ns_nop 0\nbuffer_load_dwordx4 v240,s[52:55],s72 offen offset:0 sc1 lds\nbuffer_load_dwordx4 v240,s[52:55],s72 offen offset:1024 sc1 lds\n" \
"buffer_load_dwordx4 v240,s[52:55],s72 offen offset:2048 sc1 lds\nbuffer_load_dwordx4 v240,s[52:55],s72 offen offset:3072 sc1 lds\ns_add_u32 m0,s64,98304\ns_add_u32 s72,s72,16777216\nbuffer_load_dwordx4 v240,s[52:55],s72 offen offset:0 sc1 lds\nbuffer_load_dwordx4 v240,s[52:55],s72 offen offset:1024 sc1 lds\nbuffer_load_dwordx4 v240,s[52:55],s72 offen offset:2048 sc1 lds\nbuffer_load_dwordx4 v240,s[52:55],s72 offen offset:3072 sc1 lds\n" \
"s_add_u32 s72,s37,s64\ns_add_u32 s72,s72,0\ns_add_u32 s72,s72,s71\ns_add_u32 m0,s64,49152\ns_nop 0\nbuffer_load_dwordx4 v240,s[52:55],s72 offen offset:0 sc1 lds\nbuffer_load_dwordx4 v240,s[52:55],s72 offen offset:1024 sc1 lds\nbuffer_load_dwordx4 v240,s[52:55],s72 offen offset:2048 sc1 lds\n" \
"buffer_load_dwordx4 v240,s[52:55],s72 offen offset:3072 sc1 lds\ns_add_u32 m0,s64,114688\ns_add_u32 s72,s72,16777216\nbuffer_load_dwordx4 v240,s[52:55],s72 offen offset:0 sc1 lds\nbuffer_load_dwordx4 v240,s[52:55],s72 offen offset:1024 sc1 lds\nbuffer_load_dwordx4 v240,s[52:55],s72 offen offset:2048 sc1 lds\nbuffer_load_dwordx4 v240,s[52:55],s72 offen offset:3072 sc1 lds\nv_and_b32 v251,15,v249\n" \
"v_lshrrev_b32 v250,4,v249\nv_lshlrev_b32 v250,2,v250\nv_sub_u32 v251,v251,v250\ns_lshl_b32 s70,s62,7\ns_lshl_b32 s72,s7,5\ns_sub_u32 s38,s72,s70\ns_add_u32 s38,s38,1920\ns_sub_u32 s39,s70,s72\n" \
"s_add_u32 s39,s39,96\nv_add_u32 v225,s38,v251\nv_add_u32 v225,0,v225\nv_mov_b32 v224,0xff800000\nv_add_u32 v229,s38,v251\nv_add_u32 v229,16,v229\nv_mov_b32 v228,0xff800000\nv_add_u32 v233,s39,v251\n" \
"v_add_u32 v233,0,v233\nv_mov_b32 v232,0xff800000\nv_add_u32 v237,s39,v251\nv_add_u32 v237,16,v237\nv_mov_b32 v236,0xff800000\ns_sub_u32 s38,524288,s71\ns_add_u32 s38,s38,s37\n.Lrl:\n" \
"buffer_load_dwordx4 a[0:3],v240,s[52:55],s37 offen offset:0 sc1\nbuffer_load_dwordx4 a[4:7],v240,s[52:55],s37 offen offset:1024 sc1\nbuffer_load_dwordx4 a[8:11],v240,s[52:55],s37 offen offset:2048 sc1\nbuffer_load_dwordx4 a[12:15],v240,s[52:55],s37 offen offset:3072 sc1\ns_add_u32 s37,s37,4096\nbuffer_load_dwordx4 a[16:19],v240,s[52:55],s37 offen offset:0 sc1\nbuffer_load_dwordx4 a[20:23],v240,s[52:55],s37 offen offset:1024 sc1\nbuffer_load_dwordx4 a[24:27],v240,s[52:55],s37 offen offset:2048 sc1\n" \
"buffer_load_dwordx4 a[28:31],v240,s[52:55],s37 offen offset:3072 sc1\ns_add_u32 s37,s37,4096\nbuffer_load_dwordx4 a[32:35],v240,s[52:55],s37 offen offset:0 sc1\nbuffer_load_dwordx4 a[36:39],v240,s[52:55],s37 offen offset:1024 sc1\nbuffer_load_dwordx4 a[40:43],v240,s[52:55],s37 offen offset:2048 sc1\nbuffer_load_dwordx4 a[44:47],v240,s[52:55],s37 offen offset:3072 sc1\ns_add_u32 s37,s37,4096\nbuffer_load_dwordx4 a[48:51],v240,s[52:55],s37 offen offset:0 sc1\n" \
"buffer_load_dwordx4 a[52:55],v240,s[52:55],s37 offen offset:1024 sc1\nbuffer_load_dwordx4 a[56:59],v240,s[52:55],s37 offen offset:2048 sc1\nbuffer_load_dwordx4 a[60:63],v240,s[52:55],s37 offen offset:3072 sc1\ns_add_u32 s37,s37,4096\ns_waitcnt vmcnt(15)\nM v[128:131],a[0:3],a[128:131],0\ns_waitcnt vmcnt(14)\nM v[128:131],a[4:7],a[132:135],v[128:131]\n" \
"s_waitcnt vmcnt(13)\nM v[128:131],a[8:11],a[136:139],v[128:131]\ns_waitcnt vmcnt(12)\nM v[128:131],a[12:15],a[140:143],v[128:131]\nM v[132:135],a[0:3],a[144:147],0\nM v[132:135],a[4:7],a[148:151],v[132:135]\nM v[132:135],a[8:11],a[152:155],v[132:135]\nM v[132:135],a[12:15],a[156:159],v[132:135]\n" \
"M v[136:139],a[0:3],a[160:163],0\nM v[136:139],a[4:7],a[164:167],v[136:139]\nM v[136:139],a[8:11],a[168:171],v[136:139]\nM v[136:139],a[12:15],a[172:175],v[136:139]\nM v[140:143],a[0:3],a[176:179],0\nM v[140:143],a[4:7],a[180:183],v[140:143]\nM v[140:143],a[8:11],a[184:187],v[140:143]\nM v[140:143],a[12:15],a[188:191],v[140:143]\n" \
"s_waitcnt vmcnt(11)\nM v[144:147],a[16:19],a[128:131],0\ns_waitcnt vmcnt(10)\nM v[144:147],a[20:23],a[132:135],v[144:147]\ns_waitcnt vmcnt(9)\nM v[144:147],a[24:27],a[136:139],v[144:147]\ns_waitcnt vmcnt(8)\nM v[144:147],a[28:31],a[140:143],v[144:147]\n" \
"M v[148:151],a[16:19],a[144:147],0\nM v[148:151],a[20:23],a[148:151],v[148:151]\nM v[148:151],a[24:27],a[152:155],v[148:151]\nM v[148:151],a[28:31],a[156:159],v[148:151]\nM v[152:155],a[16:19],a[160:163],0\nM v[152:155],a[20:23],a[164:167],v[152:155]\nM v[152:155],a[24:27],a[168:171],v[152:155]\nM v[152:155],a[28:31],a[172:175],v[152:155]\n" \
"M v[156:159],a[16:19],a[176:179],0\nM v[156:159],a[20:23],a[180:183],v[156:159]\nM v[156:159],a[24:27],a[184:187],v[156:159]\nM v[156:159],a[28:31],a[188:191],v[156:159]\ns_waitcnt vmcnt(7)\nM v[160:163],a[32:35],a[128:131],0\ns_waitcnt vmcnt(6)\nM v[160:163],a[36:39],a[132:135],v[160:163]\n" \
"s_waitcnt vmcnt(5)\nM v[160:163],a[40:43],a[136:139],v[160:163]\ns_waitcnt vmcnt(4)\nM v[160:163],a[44:47],a[140:143],v[160:163]\nM v[164:167],a[32:35],a[144:147],0\nM v[164:167],a[36:39],a[148:151],v[164:167]\nM v[164:167],a[40:43],a[152:155],v[164:167]\nM v[164:167],a[44:47],a[156:159],v[164:167]\n" \
"M v[168:171],a[32:35],a[160:163],0\nM v[168:171],a[36:39],a[164:167],v[168:171]\nM v[168:171],a[40:43],a[168:171],v[168:171]\nM v[168:171],a[44:47],a[172:175],v[168:171]\nM v[172:175],a[32:35],a[176:179],0\nM v[172:175],a[36:39],a[180:183],v[172:175]\nM v[172:175],a[40:43],a[184:187],v[172:175]\nM v[172:175],a[44:47],a[188:191],v[172:175]\n" \
"s_waitcnt vmcnt(3)\nM v[176:179],a[48:51],a[128:131],0\ns_waitcnt vmcnt(2)\nM v[176:179],a[52:55],a[132:135],v[176:179]\ns_waitcnt vmcnt(1)\nM v[176:179],a[56:59],a[136:139],v[176:179]\ns_waitcnt vmcnt(0)\nM v[176:179],a[60:63],a[140:143],v[176:179]\n" \
"M v[180:183],a[48:51],a[144:147],0\nM v[180:183],a[52:55],a[148:151],v[180:183]\nM v[180:183],a[56:59],a[152:155],v[180:183]\nM v[180:183],a[60:63],a[156:159],v[180:183]\nM v[184:187],a[48:51],a[160:163],0\nM v[184:187],a[52:55],a[164:167],v[184:187]\nM v[184:187],a[56:59],a[168:171],v[184:187]\nM v[184:187],a[60:63],a[172:175],v[184:187]\n" \
"M v[188:191],a[48:51],a[176:179],0\nM v[188:191],a[52:55],a[180:183],v[188:191]\nM v[188:191],a[56:59],a[184:187],v[188:191]\nM v[188:191],a[60:63],a[188:191],v[188:191]\nv_cmp_le_i32 vcc,0,v225\ns_nop 1\nv_cndmask_b32 v128,v252,v128,vcc\nv_max_f32 v224,v224,v128\n" \
"v_cmp_le_i32 vcc,1,v225\ns_nop 1\nv_cndmask_b32 v129,v252,v129,vcc\nv_max_f32 v224,v224,v129\nv_cmp_le_i32 vcc,2,v225\ns_nop 1\nv_cndmask_b32 v130,v252,v130,vcc\nv_max_f32 v224,v224,v130\n" \
"v_cmp_le_i32 vcc,3,v225\ns_nop 1\nv_cndmask_b32 v131,v252,v131,vcc\nv_max_f32 v224,v224,v131\nv_cmp_le_i32 vcc,0,v229\ns_nop 1\nv_cndmask_b32 v132,v252,v132,vcc\nv_max_f32 v228,v228,v132\n" \
"v_cmp_le_i32 vcc,1,v229\ns_nop 1\nv_cndmask_b32 v133,v252,v133,vcc\nv_max_f32 v228,v228,v133\nv_cmp_le_i32 vcc,2,v229\ns_nop 1\nv_cndmask_b32 v134,v252,v134,vcc\nv_max_f32 v228,v228,v134\n" \
"v_cmp_le_i32 vcc,3,v229\ns_nop 1\nv_cndmask_b32 v135,v252,v135,vcc\nv_max_f32 v228,v228,v135\nv_cmp_le_i32 vcc,0,v233\ns_nop 1\nv_cndmask_b32 v136,v252,v136,vcc\nv_max_f32 v232,v232,v136\n" \
"v_cmp_le_i32 vcc,1,v233\ns_nop 1\nv_cndmask_b32 v137,v252,v137,vcc\nv_max_f32 v232,v232,v137\nv_cmp_le_i32 vcc,2,v233\ns_nop 1\nv_cndmask_b32 v138,v252,v138,vcc\nv_max_f32 v232,v232,v138\n" \
"v_cmp_le_i32 vcc,3,v233\ns_nop 1\nv_cndmask_b32 v139,v252,v139,vcc\nv_max_f32 v232,v232,v139\nv_cmp_le_i32 vcc,0,v237\ns_nop 1\nv_cndmask_b32 v140,v252,v140,vcc\nv_max_f32 v236,v236,v140\n" \
"v_cmp_le_i32 vcc,1,v237\ns_nop 1\nv_cndmask_b32 v141,v252,v141,vcc\nv_max_f32 v236,v236,v141\nv_cmp_le_i32 vcc,2,v237\ns_nop 1\nv_cndmask_b32 v142,v252,v142,vcc\nv_max_f32 v236,v236,v142\n" \
"v_cmp_le_i32 vcc,3,v237\ns_nop 1\nv_cndmask_b32 v143,v252,v143,vcc\nv_max_f32 v236,v236,v143\nv_cmp_le_i32 vcc,16,v225\ns_nop 1\nv_cndmask_b32 v144,v252,v144,vcc\nv_max_f32 v224,v224,v144\n" \
"v_cmp_le_i32 vcc,17,v225\ns_nop 1\nv_cndmask_b32 v145,v252,v145,vcc\nv_max_f32 v224,v224,v145\nv_cmp_le_i32 vcc,18,v225\ns_nop 1\nv_cndmask_b32 v146,v252,v146,vcc\nv_max_f32 v224,v224,v146\n" \
"v_cmp_le_i32 vcc,19,v225\ns_nop 1\nv_cndmask_b32 v147,v252,v147,vcc\nv_max_f32 v224,v224,v147\nv_cmp_le_i32 vcc,16,v229\ns_nop 1\nv_cndmask_b32 v148,v252,v148,vcc\nv_max_f32 v228,v228,v148\n" \
"v_cmp_le_i32 vcc,17,v229\ns_nop 1\nv_cndmask_b32 v149,v252,v149,vcc\nv_max_f32 v228,v228,v149\nv_cmp_le_i32 vcc,18,v229\ns_nop 1\nv_cndmask_b32 v150,v252,v150,vcc\nv_max_f32 v228,v228,v150\n" \
"v_cmp_le_i32 vcc,19,v229\ns_nop 1\nv_cndmask_b32 v151,v252,v151,vcc\nv_max_f32 v228,v228,v151\nv_cmp_le_i32 vcc,16,v233\ns_nop 1\nv_cndmask_b32 v152,v252,v152,vcc\nv_max_f32 v232,v232,v152\n" \
"v_cmp_le_i32 vcc,17,v233\ns_nop 1\nv_cndmask_b32 v153,v252,v153,vcc\nv_max_f32 v232,v232,v153\nv_cmp_le_i32 vcc,18,v233\ns_nop 1\nv_cndmask_b32 v154,v252,v154,vcc\nv_max_f32 v232,v232,v154\n" \
"v_cmp_le_i32 vcc,19,v233\ns_nop 1\nv_cndmask_b32 v155,v252,v155,vcc\nv_max_f32 v232,v232,v155\nv_cmp_le_i32 vcc,16,v237\ns_nop 1\nv_cndmask_b32 v156,v252,v156,vcc\nv_max_f32 v236,v236,v156\n" \
"v_cmp_le_i32 vcc,17,v237\ns_nop 1\nv_cndmask_b32 v157,v252,v157,vcc\nv_max_f32 v236,v236,v157\nv_cmp_le_i32 vcc,18,v237\ns_nop 1\nv_cndmask_b32 v158,v252,v158,vcc\nv_max_f32 v236,v236,v158\n" \
"v_cmp_le_i32 vcc,19,v237\ns_nop 1\nv_cndmask_b32 v159,v252,v159,vcc\nv_max_f32 v236,v236,v159\nv_cmp_le_i32 vcc,32,v225\ns_nop 1\nv_cndmask_b32 v160,v252,v160,vcc\nv_max_f32 v224,v224,v160\n" \
"v_cmp_le_i32 vcc,33,v225\ns_nop 1\nv_cndmask_b32 v161,v252,v161,vcc\nv_max_f32 v224,v224,v161\nv_cmp_le_i32 vcc,34,v225\ns_nop 1\nv_cndmask_b32 v162,v252,v162,vcc\nv_max_f32 v224,v224,v162\n" \
"v_cmp_le_i32 vcc,35,v225\ns_nop 1\nv_cndmask_b32 v163,v252,v163,vcc\nv_max_f32 v224,v224,v163\nv_cmp_le_i32 vcc,32,v229\ns_nop 1\nv_cndmask_b32 v164,v252,v164,vcc\nv_max_f32 v228,v228,v164\n" \
"v_cmp_le_i32 vcc,33,v229\ns_nop 1\nv_cndmask_b32 v165,v252,v165,vcc\nv_max_f32 v228,v228,v165\nv_cmp_le_i32 vcc,34,v229\ns_nop 1\nv_cndmask_b32 v166,v252,v166,vcc\nv_max_f32 v228,v228,v166\n" \
"v_cmp_le_i32 vcc,35,v229\ns_nop 1\nv_cndmask_b32 v167,v252,v167,vcc\nv_max_f32 v228,v228,v167\nv_cmp_le_i32 vcc,32,v233\ns_nop 1\nv_cndmask_b32 v168,v252,v168,vcc\nv_max_f32 v232,v232,v168\n" \
"v_cmp_le_i32 vcc,33,v233\ns_nop 1\nv_cndmask_b32 v169,v252,v169,vcc\nv_max_f32 v232,v232,v169\nv_cmp_le_i32 vcc,34,v233\ns_nop 1\nv_cndmask_b32 v170,v252,v170,vcc\nv_max_f32 v232,v232,v170\n" \
"v_cmp_le_i32 vcc,35,v233\ns_nop 1\nv_cndmask_b32 v171,v252,v171,vcc\nv_max_f32 v232,v232,v171\nv_cmp_le_i32 vcc,32,v237\ns_nop 1\nv_cndmask_b32 v172,v252,v172,vcc\nv_max_f32 v236,v236,v172\n" \
"v_cmp_le_i32 vcc,33,v237\ns_nop 1\nv_cndmask_b32 v173,v252,v173,vcc\nv_max_f32 v236,v236,v173\nv_cmp_le_i32 vcc,34,v237\ns_nop 1\nv_cndmask_b32 v174,v252,v174,vcc\nv_max_f32 v236,v236,v174\n" \
"v_cmp_le_i32 vcc,35,v237\ns_nop 1\nv_cndmask_b32 v175,v252,v175,vcc\nv_max_f32 v236,v236,v175\nv_cmp_le_i32 vcc,48,v225\ns_nop 1\nv_cndmask_b32 v176,v252,v176,vcc\nv_max_f32 v224,v224,v176\n" \
"v_cmp_le_i32 vcc,49,v225\ns_nop 1\nv_cndmask_b32 v177,v252,v177,vcc\nv_max_f32 v224,v224,v177\nv_cmp_le_i32 vcc,50,v225\ns_nop 1\nv_cndmask_b32 v178,v252,v178,vcc\nv_max_f32 v224,v224,v178\n" \
"v_cmp_le_i32 vcc,51,v225\ns_nop 1\nv_cndmask_b32 v179,v252,v179,vcc\nv_max_f32 v224,v224,v179\nv_cmp_le_i32 vcc,48,v229\ns_nop 1\nv_cndmask_b32 v180,v252,v180,vcc\nv_max_f32 v228,v228,v180\n" \
"v_cmp_le_i32 vcc,49,v229\ns_nop 1\nv_cndmask_b32 v181,v252,v181,vcc\nv_max_f32 v228,v228,v181\nv_cmp_le_i32 vcc,50,v229\ns_nop 1\nv_cndmask_b32 v182,v252,v182,vcc\nv_max_f32 v228,v228,v182\n" \
"v_cmp_le_i32 vcc,51,v229\ns_nop 1\nv_cndmask_b32 v183,v252,v183,vcc\nv_max_f32 v228,v228,v183\nv_cmp_le_i32 vcc,48,v233\ns_nop 1\nv_cndmask_b32 v184,v252,v184,vcc\nv_max_f32 v232,v232,v184\n" \
"v_cmp_le_i32 vcc,49,v233\ns_nop 1\nv_cndmask_b32 v185,v252,v185,vcc\nv_max_f32 v232,v232,v185\nv_cmp_le_i32 vcc,50,v233\ns_nop 1\nv_cndmask_b32 v186,v252,v186,vcc\nv_max_f32 v232,v232,v186\n" \
"v_cmp_le_i32 vcc,51,v233\ns_nop 1\nv_cndmask_b32 v187,v252,v187,vcc\nv_max_f32 v232,v232,v187\nv_cmp_le_i32 vcc,48,v237\ns_nop 1\nv_cndmask_b32 v188,v252,v188,vcc\nv_max_f32 v236,v236,v188\n" \
"v_cmp_le_i32 vcc,49,v237\ns_nop 1\nv_cndmask_b32 v189,v252,v189,vcc\nv_max_f32 v236,v236,v189\nv_cmp_le_i32 vcc,50,v237\ns_nop 1\nv_cndmask_b32 v190,v252,v190,vcc\nv_max_f32 v236,v236,v190\n" \
"v_cmp_le_i32 vcc,51,v237\ns_nop 1\nv_cndmask_b32 v191,v252,v191,vcc\nv_max_f32 v236,v236,v191\nv_subrev_u32 v225,64,v225\nv_subrev_u32 v229,64,v229\nv_subrev_u32 v233,64,v233\nv_subrev_u32 v237,64,v237\n" \
"s_cmp_lt_u32 s37,s38\ns_cbranch_scc1 .Lrl\ns_waitcnt vmcnt(0)\ns_barrier\nds_read_b128 a[0:3],v240 offset:0\nds_read_b128 a[4:7],v240 offset:1024\nds_read_b128 a[8:11],v240 offset:2048\nds_read_b128 a[12:15],v240 offset:3072\n" \
"ds_read_b128 a[16:19],v240 offset:4096\nds_read_b128 a[20:23],v240 offset:5120\nds_read_b128 a[24:27],v240 offset:6144\nds_read_b128 a[28:31],v240 offset:7168\nds_read_b128 a[32:35],v240 offset:8192\nds_read_b128 a[36:39],v240 offset:9216\nds_read_b128 a[40:43],v240 offset:10240\nds_read_b128 a[44:47],v240 offset:11264\n" \
"ds_read_b128 a[48:51],v240 offset:12288\nds_read_b128 a[52:55],v240 offset:13312\nds_read_b128 a[56:59],v240 offset:14336\nds_read_b128 a[60:63],v240 offset:15360\ns_cmp_eq_u32 s62,0\ns_cselect_b32 s36,0x8000,0\ns_add_u32 s36,s36,s64\ns_cmp_eq_u32 s36,s96\n" \
"s_cselect_b32 s72,0xc000,s98\ns_add_u32 s65,s36,s72\ns_cmp_eq_u32 s65,s96\ns_cselect_b32 s72,0xc000,s98\ns_add_u32 s66,s65,s72\ns_cmp_eq_u32 s66,s96\ns_cselect_b32 s72,0xc000,s98\ns_add_u32 s95,s66,s72\n" \
"s_add_u32 s97,s96,65536\ns_add_u32 s15,s64,s46\ns_cmp_gt_i32 s62,-1\ns_cselect_b32 s72,s98,0xfffe4000\ns_add_u32 s15,s15,s72\ns_mov_b32 s94,0xc000\ns_cmp_eq_u32 s62,7\ns_cselect_b32 s72,98304,0\n" \
"s_sub_u32 s97,s97,s72\nv_and_b32 v251,31,v249\nv_lshlrev_b32 v251,2,v251\ns_lshl_b32 s80,s63,7\ns_add_u32 s80,s80,86016\nv_add_u32 v250,0xc000,v241\ns_waitcnt lgkmcnt(0)\ns_branch .Lattn\n" \
".Lfin:\n" \
""
constexpr size_t WS_DBG = (size_t)51 << 20;
constexpr unsigned LDS_BYTES = 163840;
struct Args {
    const u16 *x, *ckv, *w_q, *w_uk, *w_uv, *w_o, *w_r, *wg, *wu, *wd, *cvec, *w_mod;
    char* ws; u16* out;
};
typedef const __attribute__((address_space(4))) Args* kargs_t;
#define STAMPD(i) do {} while (0)
#define C8(p, a) p #a "0", p #a "1", p #a "2", p #a "3", p #a "4", p #a "5", p #a "6", p #a "7", p #a "8", p #a "9"
#define C100(p, a) C8(p, a##0), C8(p, a##1), C8(p, a##2), C8(p, a##3), C8(p, a##4), C8(p, a##5), C8(p, a##6), C8(p, a##7), C8(p, a##8), C8(p, a##9)
#define C10_99(p) C8(p, 1), C8(p, 2), C8(p, 3), C8(p, 4), C8(p, 5), C8(p, 6), C8(p, 7), C8(p, 8), C8(p, 9)
#define C200_249(p) C8(p, 20), C8(p, 21), C8(p, 22), C8(p, 23), C8(p, 24)
#define BODY_CLOB "memory", "vcc", "scc", "s0", "s1", "s2", "s3", "s8", "s9", C8("s", 1), C8("s", 2), "s30", "s31", "s33", "s34", "s35", "s36", "s37", "s38", "s39", C8("s", 4), C8("s", 5), C8("s", 6), C8("s", 7), C8("s", 8), C8("s", 9), \
    C8("v", ), C10_99("v"), C100("v", 1), C200_249("v"), "v250", "v251", "v252", "v253", "v254", C8("a", ), C10_99("a"), C100("a", 1), C200_249("a"), "a250", "a251", "a252", "a253", "a254", "a255"

__global__ __launch_bounds__(256, 1) void k_all(const Args a_) {
    kargs_t ap = (kargs_t)__builtin_amdgcn_kernarg_segment_ptr(); unsigned bid = blockIdx.x, wid = rfl(threadIdx.x >> 6), sv_;
    asm volatile(BODY_ASM : "+{s[4:5]}"(ap), "+{s6}"(bid), "+{s7}"(wid), "={v255}"(sv_) :: BODY_CLOB);
    asm volatile(".Lcode_e:");
}
extern "C" void kernel_launch(void* const* d_in, const int* in_sizes, int n_in, void* d_out, int out_size, void* d_ws, size_t ws_size, hipStream_t stream) {
    Args a;
    a.x = (const u16*)d_in[0]; a.ckv = (const u16*)d_in[1]; a.w_q = (const u16*)d_in[2]; a.w_uk = (const u16*)d_in[3]; a.w_uv = (const u16*)d_in[4]; a.w_o = (const u16*)d_in[5];
    a.w_r = (const u16*)d_in[7]; a.wg = (const u16*)d_in[8]; a.wu = (const u16*)d_in[9]; a.wd = (const u16*)d_in[10]; a.cvec = (const u16*)d_in[11]; a.w_mod = (const u16*)d_in[12];
    a.ws = (char*)d_ws;
    a.out = (u16*)d_out;
    (void)hipFuncSetAttribute((const void*)k_all, hipFuncAttributeMaxDynamicSharedMemorySize, LDS_BYTES);
    k_all<<<256, 256, LDS_BYTES, stream>>>(a);
}
```

```cpp
#include <hip/hip_runtime.h>
#include <hip/hip_bf16.h>
#include <stdint.h>
typedef unsigned short u16;
#define DI __device__ __forceinline__
DI unsigned lane_id() { unsigned l; asm volatile("v_mbcnt_lo_u32_b32 %0, -1, 0\n\tv_mbcnt_hi_u32_b32 %0, -1, %0" : "=v"(l)); return l; }
DI unsigned rfl(unsigned x) { return __builtin_amdgcn_readfirstlane(x); }
#define BODY_ASM ".macro M a,b,c,d\nv_mfma_f32_16x16x32_bf16 \\a,\\b,\\c,\\d\n.endm\n" \
".Lcode_b:\ns_load_dwordx16 s[8:23],s[4:5],0x0\ns_load_dwordx8 s[24:31],s[4:5],0x40\ns_load_dwordx2 s[34:35],s[4:5],0x60\nv_mbcnt_lo_u32_b32 v249,-1,0\nv_mbcnt_hi_u32_b32 v249,-1,v249\ns_bfe_u32 s61,s6,0x20003\ns_lshl_b32 s64,s7,12\n" \
"s_and_b32 s60,s6,7\ns_lshr_b32 s62,s6,5\ns_lshl2_add_u32 s63,s60,s61\nv_lshlrev_b32 v240,4,v249\nv_lshrrev_b32 v0,3,v249\nv_and_b32 v2,7,v249\nv_bfe_u32 v6,v249,2,3\nv_lshrrev_b32 v7,5,v249\n" \
"v_and_b32 v8,48,v240\nv_lshlrev_b32 v6,13,v6\nv_lshl_or_b32 v7,v7,6,v8\nv_or_b32 v6,0x1000,v6\nv_or_b32 v243,v6,v7\nv_add_u32 v244,0xfffffc80,v243\nv_xor_b32 v6,32,v243\nv_add_u32 v245,63488,v6\n" \
"v_add_u32 v246,62592,v6\ns_lshl_b32 s68,s7,17\ns_lshl_b32 s70,s63,8\ns_add_u32 s68,s68,s70\ns_mov_b32 s42,4198400\ns_mov_b32 s43,0x20000\nv_lshrrev_b32 v6,4,v249\nv_xor_b32 v7,v6,v2\n" \
"v_lshlrev_b32 v7,4,v7\nv_lshl_or_b32 v224,v0,10,v7\nv_xor_b32 v7,64,v224\nv_add_u32 v225,7168,v7\nv_add_u32 v226,14336,v224\nv_add_u32 v227,21504,v7\ns_lshl_b32 s70,s62,1\ns_sub_u32 s71,s7,s70\n" \
"s_add_u32 s71,s71,30\ns_sub_u32 s72,s70,s7\ns_add_u32 s72,s72,3\ns_cmp_lt_u32 s7,2\ns_cselect_b32 s74,s71,s72\ns_lshl_b32 s66,s74,16\ns_mul_i32 s65,s7,24576\ns_mov_b32 s38,2097152\n" \
"s_mov_b32 s39,0x20000\ns_xor_b32 s94,s61,s62\ns_and_b32 s94,s94,1\ns_lshl_b32 s99,s94,19\ns_or_b32 s99,s99,s68\ns_waitcnt lgkmcnt(0)\ns_sub_u32 s40,s14,0x1000\ns_subb_u32 s41,s15,0\n" \
"s_mov_b64 s[36:37],s[10:11]\ns_add_u32 m0,s64,147456\ns_add_u32 s69,s68,0\nbuffer_load_dwordx4 v243,s[40:43],s69 offen offset:0 lds\nbuffer_load_dwordx4 v244,s[40:43],s69 offen offset:1024 lds\nbuffer_load_dwordx4 v245,s[40:43],s69 offen offset:2048 lds\nbuffer_load_dwordx4 v246,s[40:43],s69 offen offset:3072 lds\ns_add_u32 m0,s65,0\n" \
"s_xor_b32 s67,s66,0\nbuffer_load_dwordx4 v224,s[36:39],s67 offen offset:0 lds\nbuffer_load_dwordx4 v225,s[36:39],s67 offen offset:1024 lds\nbuffer_load_dwordx4 v226,s[36:39],s67 offen offset:2048 lds\nbuffer_load_dwordx4 v227,s[36:39],s67 offen offset:3072 lds\ns_add_u32 m0,s65,4096\ns_xor_b32 s67,s66,32768\nbuffer_load_dwordx4 v224,s[36:39],s67 offen offset:0 lds\n" \
"buffer_load_dwordx4 v225,s[36:39],s67 offen offset:1024 lds\nbuffer_load_dwordx4 v226,s[36:39],s67 offen offset:2048 lds\nbuffer_load_dwordx4 v227,s[36:39],s67 offen offset:3072 lds\ns_cmp_lt_u32 s6,192\ns_cselect_b32 s70,s6,0\ns_mul_i32 s71,s70,171\ns_lshr_b32 s71,s71,13\ns_mul_i32 s72,s71,48\n" \
"s_sub_u32 s70,s70,s72\ns_lshl2_add_u32 s72,s71,s7\ns_lshl_b32 s72,s72,5\nv_lshl_add_u32 v1,v0,2,s72\nv_mul_u32_u24 v3,6144,v1\nv_lshl_add_u32 v3,v2,4,v3\ns_lshl_b32 s72,s70,7\ns_add_u32 s72,s72,3072\n" \
"v_add_u32 v3,s72,v3\nv_add_u32 v4,12288,v3\nv_lshlrev_b32 v1,1,v1\nglobal_load_dwordx2 v[168:169],v1,s[28:29]\nglobal_load_dwordx4 v[152:155],v3,s[30:31] offset:-3072\nglobal_load_dwordx4 v[156:159],v3,s[30:31] offset:3072\nglobal_load_dwordx4 v[160:163],v4,s[30:31] offset:-3072\nglobal_load_dwordx4 v[164:167],v4,s[30:31] offset:3072\n" \
"s_mul_i32 s71,s71,12288\ns_lshl_b32 s70,s70,8\ns_add_u32 s79,s70,s71\ns_add_u32 s79,s79,786432\ns_cmp_eq_u32 s7,0\ns_cselect_b32 s75,0,0x8000000\ns_cmp_lt_u32 s6,192\ns_cselect_b32 s70,s75,0x8000000\n" \
"s_or_b32 s79,s79,s70\ns_mov_b64 s[52:53],s[34:35]\ns_mov_b32 s54,71303168\ns_mov_b32 s55,0x20000\ns_mov_b32 s70,94208\nbuffer_load_dwordx2 v[230:231],off,s[52:55],s70 sc1\ns_getpc_b64 s[56:57]\n.Lpc0:\n" \
"s_and_b32 s57,s57,0xffff\ns_mov_b32 s58,.Lcode_e-.Lpc0\ns_mov_b32 s59,0x20000\ns_lshr_b32 s70,s6,3\nv_and_b32 v251,1,v249\ns_lshl2_add_u32 s78,s70,s7\nv_lshlrev_b32 v251,14,v251\ns_lshl_b32 s78,s78,7\n" \
"buffer_load_dword v247,v251,s[56:59],s78 offen\nv_and_b32 v34,15,v249\nv_lshrrev_b32 v35,1,v34\nv_xor_b32 v35,v35,v6\nv_lshlrev_b32 v35,4,v35\nv_lshl_or_b32 v35,v34,7,v35\nv_add_u32 v228,s65,v35\nv_xor_b32 v229,64,v228\n" \
"v_bfe_u32 v34,v249,2,2\nv_and_b32 v35,3,v249\nv_lshlrev_b32 v35,3,v35\nv_lshl_or_b32 v35,v34,6,v35\nv_lshl_or_b32 v35,v6,11,v35\nv_and_b32 v34,16,v249\nv_lshl_or_b32 v35,v34,1,v35\nv_add_u32 v241,98304,v35\n" \
"v_xor_b32 v242,32,v241\ns_mov_b32 s73,0x400000\nv_mov_b32 v252,0xff800000\ns_mov_b32 s82,0x05040100\ns_mov_b32 s83,0x07060302\nv_lshlrev_b32 v190,5,v2\nv_lshl_or_b32 v190,v6,3,v190\ns_lshl_b32 s72,s7,8\n" \
"s_add_u32 s72,s72,114688\nv_add_u32 v190,s72,v190\ns_lshl_b32 s72,s7,3\ns_add_u32 s72,s72,115712\nv_lshl_add_u32 v191,v2,5,s72\nv_lshlrev_b32 v251,5,v2\nv_add_u32 v251,115712,v251\nv_lshlrev_b32 v250,2,v249\n" \
"v_add_u32 v250,114688,v250\ns_lshl_b32 s70,s62,7\ns_xor_b32 s71,s63,12\ns_lshl3_add_u32 s71,s71,s70\ns_lshl_b32 s72,s63,3\ns_sub_u32 s72,s72,s70\ns_add_u32 s72,s72,1792\ns_cmp_lt_u32 s63,16\n" \
"s_cselect_b32 s71,s71,s72\ns_lshl_b32 s71,s71,10\ns_lshl_b32 s72,s7,7\ns_add_u32 s71,s71,s72\nv_lshlrev_b32 v5,10,v2\nv_lshl_add_u32 v5,v0,4,v5\nv_add_u32 v5,s71,v5\ns_lshl_b32 s70,s6,11\n" \
"s_cmp_lt_u32 s6,128\ns_cselect_b32 s70,s70,0x8000000\ns_lshl_b32 s71,s7,9\ns_add_u32 s70,s70,s71\ns_add_u32 s80,s70,98304\nv_mov_b32 v28,-1\nv_mov_b32 v29,-1\nv_lshlrev_b32 v30,3,v249\n" \
"s_lshl_b32 s70,s6,7\ns_cmp_lt_u32 s6,224\ns_cselect_b32 s71,65536,0x40000000\ns_cmp_lt_u32 s6,128\ns_cselect_b32 s72,556928,0x40000000\nv_mov_b32 v31,s71\nv_mov_b32 v32,s72\nv_cmp_lt_u32 vcc,31,v249\n" \
"s_or_b32 s70,s70,s75\nv_mov_b32 v33,0\nv_cndmask_b32 v31,v31,v32,vcc\nv_lshl_add_u32 v31,v249,2,v31\ns_add_u32 m0,s64,98304\ns_add_u32 s69,s68,524288\nbuffer_load_dwordx4 v243,s[40:43],s69 offen offset:0 lds\nbuffer_load_dwordx4 v244,s[40:43],s69 offen offset:1024 lds\n" \
"buffer_load_dwordx4 v245,s[40:43],s69 offen offset:2048 lds\nbuffer_load_dwordx4 v246,s[40:43],s69 offen offset:3072 lds\ns_add_u32 m0,s65,8192\ns_xor_b32 s67,s66,128\nbuffer_load_dwordx4 v224,s[36:39],s67 offen offset:0 lds\nbuffer_load_dwordx4 v225,s[36:39],s67 offen offset:1024 lds\nbuffer_load_dwordx4 v226,s[36:39],s67 offen offset:2048 lds\nbuffer_load_dwordx4 v227,s[36:39],s67 offen offset:3072 lds\n" \
"s_add_u32 m0,s65,12288\ns_xor_b32 s67,s66,32896\nbuffer_load_dwordx4 v224,s[36:39],s67 offen offset:0 lds\nbuffer_load_dwordx4 v225,s[36:39],s67 offen offset:1024 lds\nbuffer_load_dwordx4 v226,s[36:39],s67 offen offset:2048 lds\nbuffer_load_dwordx4 v227,s[36:39],s67 offen offset:3072 lds\nglobal_load_dwordx4 v[128:131],v5,s[8:9]\nglobal_load_dwordx4 v[132:135],v5,s[8:9] offset:512\n" \
"buffer_store_dwordx2 v[28:29],v30,s[52:55],s80 offen sc1\nbuffer_store_dword v33,v31,s[52:55],s70 offen sc1\ns_waitcnt vmcnt(23)\ns_barrier\ns_add_u32 m0,s64,131072\ns_add_u32 s69,s68,1048576\nds_read_b128 a[0:3],v228 offset:0\nds_read_b128 a[4:7],v228 offset:2048\n" \
"ds_read_b128 a[8:11],v228 offset:4096\nds_read_b128 a[12:15],v228 offset:6144\nds_read_b128 a[16:19],v229 offset:0\nds_read_b128 a[20:23],v229 offset:2048\nds_read_b128 a[24:27],v229 offset:4096\nds_read_b128 a[28:31],v229 offset:6144\nds_read_b64_tr_b16 v[192:193],v241 offset:49152\nds_read_b64_tr_b16 v[194:195],v241 offset:49408\n" \
"ds_read_b64_tr_b16 v[196:197],v241 offset:57344\nds_read_b64_tr_b16 v[198:199],v241 offset:57600\nds_read_b64_tr_b16 v[200:201],v242 offset:49152\nds_read_b64_tr_b16 v[202:203],v242 offset:49408\nds_read_b64_tr_b16 v[204:205],v242 offset:57344\nds_read_b64_tr_b16 v[206:207],v242 offset:57600\nds_read_b64_tr_b16 v[208:209],v241 offset:49664\nds_read_b64_tr_b16 v[210:211],v241 offset:49920\n" \
"ds_read_b64_tr_b16 v[212:213],v241 offset:57856\nds_read_b64_tr_b16 v[214:215],v241 offset:58112\nds_read_b64_tr_b16 v[216:217],v242 offset:49664\nds_read_b64_tr_b16 v[218:219],v242 offset:49920\nds_read_b64_tr_b16 v[220:221],v242 offset:57856\nds_read_b64_tr_b16 v[222:223],v242 offset:58112\ns_waitcnt vmcnt(22)\nv_lshlrev_b32 v170,16,v168\n" \
"v_and_b32 v171,0xffff0000,v168\nv_lshlrev_b32 v172,16,v169\nv_and_b32 v173,0xffff0000,v169\nv_mul_f32 v184,0xbfb8aa3b,v170\nv_mul_f32 v185,0xbfb8aa3b,v171\nv_mul_f32 v186,0xbfb8aa3b,v172\nv_mul_f32 v187,0xbfb8aa3b,v173\nv_exp_f32 v184,v184\n" \
"v_exp_f32 v185,v185\nv_exp_f32 v186,v186\nv_exp_f32 v187,v187\nv_add_f32 v184,1.0,v184\nv_add_f32 v185,1.0,v185\nv_add_f32 v186,1.0,v186\nv_add_f32 v187,1.0,v187\nv_rcp_f32 v184,v184\n" \
"v_rcp_f32 v185,v185\nv_rcp_f32 v186,v186\nv_rcp_f32 v187,v187\nv_mul_f32 v170,v170,v184\nv_mul_f32 v171,v171,v185\nv_mul_f32 v172,v172,v186\nv_mul_f32 v173,v173,v187\nv_cvt_pk_bf16_f32 v170,v170,v171\n" \
"v_cvt_pk_bf16_f32 v171,v172,v173\ns_waitcnt lgkmcnt(5)\nM v[0:3],v[192:195],a[0:3],0\nbuffer_load_dwordx4 v243,s[40:43],s69 offen offset:0 lds\nM v[0:3],v[196:199],a[16:19],v[0:3]\nv_bfe_u32 v253,v249,1,2\ns_lshl2_add_u32 s76,s62,s7\nv_and_b32 v248,1,v249\n" \
"v_lshlrev_b32 v253,13,v253\ns_mul_i32 s76,s76,32768\ns_lshl_b32 s77,s63,8\nM v[4:7],v[192:195],a[4:7],0\nv_lshl_or_b32 v253,v248,7,v253\ns_add_u32 s76,s76,s77\nv_and_b32 v254,7,v249\nv_add_u32 v253,s76,v253\n" \
"M v[4:7],v[196:199],a[20:23],v[4:7]\nv_and_b32 v234,7,v249\ns_lshl3_add_u32 s72,s6,s75\nM v[64:67],v[192:195],a[8:11],0\nv_lshlrev_b32 v234,11,v234\ns_add_u32 s72,s72,573440\nM v[64:67],v[196:199],a[24:27],v[64:67]\nbuffer_load_dwordx4 v244,s[40:43],s69 offen offset:1024 lds\n" \
"M v[68:71],v[192:195],a[12:15],0\ns_waitcnt vmcnt(22)\nv_perm_b32 v182,v156,v152,s82\nv_perm_b32 v183,v156,v152,s83\nv_perm_b32 v184,v157,v153,s82\nv_perm_b32 v185,v157,v153,s83\nv_perm_b32 v186,v158,v154,s82\nv_perm_b32 v187,v158,v154,s83\n" \
"v_perm_b32 v188,v159,v155,s82\nv_perm_b32 v189,v159,v155,s83\nv_dot2_f32_bf16 v174,v182,v170,0\nv_dot2_f32_bf16 v175,v183,v170,0\nv_dot2_f32_bf16 v176,v184,v170,0\nv_dot2_f32_bf16 v177,v185,v170,0\nv_dot2_f32_bf16 v178,v186,v170,0\nv_dot2_f32_bf16 v179,v187,v170,0\n" \
"v_dot2_f32_bf16 v180,v188,v170,0\nv_dot2_f32_bf16 v181,v189,v170,0\ns_waitcnt vmcnt(20)\nv_perm_b32 v182,v164,v160,s82\nv_perm_b32 v183,v164,v160,s83\nv_perm_b32 v184,v165,v161,s82\nv_perm_b32 v185,v165,v161,s83\nv_perm_b32 v186,v166,v162,s82\n" \
"v_perm_b32 v187,v166,v162,s83\nv_perm_b32 v188,v167,v163,s82\nv_perm_b32 v189,v167,v163,s83\nv_dot2_f32_bf16 v174,v182,v171,v174\nv_dot2_f32_bf16 v175,v183,v171,v175\nv_dot2_f32_bf16 v176,v184,v171,v176\nv_dot2_f32_bf16 v177,v185,v171,v177\nv_dot2_f32_bf16 v178,v186,v171,v178\n" \
"v_dot2_f32_bf16 v179,v187,v171,v179\nv_dot2_f32_bf16 v180,v188,v171,v180\nv_dot2_f32_bf16 v181,v189,v171,v181\nv_permlane32_swap_b32 v174,v178\nv_permlane32_swap_b32 v175,v179\nv_permlane32_swap_b32 v176,v180\nv_permlane32_swap_b32 v177,v181\nv_add_f32 v174,v174,v178\n" \
"v_add_f32 v175,v175,v179\nv_add_f32 v176,v176,v180\nv_add_f32 v177,v177,v181\nds_read_b64_tr_b16 v[192:193],v241 offset:50176\nv_permlane16_swap_b32 v174,v176\nv_permlane16_swap_b32 v175,v177\nv_add_f32 v174,v174,v176\nv_add_f32 v175,v175,v177\n" \
"ds_read_b64_tr_b16 v[194:195],v241 offset:50432\nv_add_f32_dpp v174,v174,v174 row_ror:8 row_mask:0xf bank_mask:0xf\nv_add_f32_dpp v175,v175,v175 row_ror:8 row_mask:0xf bank_mask:0xf\nds_write_b64 v190,v[174:175]\nM v[68:71],v[196:199],a[28:31],v[68:71]\nds_read_b64_tr_b16 v[196:197],v241 offset:58368\nds_read_b64_tr_b16 v[198:199],v241 offset:58624\nM v[8:11],v[200:203],a[0:3],0\n" \
"s_waitcnt vmcnt(18)\nv_readfirstlane_b32 s92,v230\nv_readfirstlane_b32 s93,v231\nM v[8:11],v[204:207],a[16:19],v[8:11]\ns_add_u32 s70,s92,1\ns_addc_u32 s71,s93,0\nM v[12:15],v[200:203],a[4:7],0\nbuffer_load_dwordx4 v245,s[40:43],s69 offen offset:2048 lds\n" \
"M v[12:15],v[204:207],a[20:23],v[12:15]\ns_add_u32 s92,s92,0x7f4a7c15\ns_addc_u32 s93,s93,0x9e3779b9\nM v[72:75],v[200:203],a[8:11],0\nv_mov_b32 v230,s92\nv_mov_b32 v231,s93\nM v[72:75],v[204:207],a[24:27],v[72:75]\ns_lshl2_add_u32 s80,s60,s7\n" \
"s_lshl_b32 s80,s80,9\nM v[76:79],v[200:203],a[12:15],0\nds_read_b64_tr_b16 v[200:201],v242 offset:50176\nds_read_b64_tr_b16 v[202:203],v242 offset:50432\nM v[76:79],v[204:207],a[28:31],v[76:79]\nbuffer_load_dwordx4 v246,s[40:43],s69 offen offset:3072 lds\nds_read_b64_tr_b16 v[204:205],v242 offset:58368\nds_read_b64_tr_b16 v[206:207],v242 offset:58624\n" \
"M v[16:19],v[208:211],a[0:3],0\ns_add_u32 s80,s80,573440\ns_waitcnt lgkmcnt(6)\nM v[16:19],v[212:215],a[16:19],v[16:19]\ns_add_u32 m0,s65,16384\ns_xor_b32 s67,s66,256\nM v[20:23],v[208:211],a[4:7],0\nv_mov_b32 v232,s70\n" \
"v_mov_b32 v233,s71\nM v[20:23],v[212:215],a[20:23],v[20:23]\ns_lshr_b32 s70,s64,2\ns_add_u32 s71,s70,786432\nM v[80:83],v[208:211],a[8:11],0\nbuffer_load_dwordx4 v224,s[36:39],s67 offen offset:0 lds\nM v[80:83],v[212:215],a[24:27],v[80:83]\nv_lshlrev_b32 v254,14,v254\n" \
"s_add_u32 s78,s78,32768\nM v[84:87],v[208:211],a[12:15],0\nds_read_b64_tr_b16 v[208:209],v241 offset:50688\nds_read_b64_tr_b16 v[210:211],v241 offset:50944\nM v[84:87],v[212:215],a[28:31],v[84:87]\nds_read_b64_tr_b16 v[212:213],v241 offset:58880\nds_read_b64_tr_b16 v[214:215],v241 offset:59136\nM v[24:27],v[216:219],a[0:3],0\n" \
"s_mov_b32 s98,0x4000\ns_mov_b32 s0,28672\nM v[24:27],v[220:223],a[16:19],v[24:27]\nbuffer_load_dwordx4 v225,s[36:39],s67 offen offset:1024 lds\nM v[28:31],v[216:219],a[4:7],0\ns_add_u32 s10,s61,1\ns_add_u32 s11,s61,2\nM v[28:31],v[220:223],a[20:23],v[28:31]\n" \
"s_add_u32 s14,s61,3\ns_and_b32 s10,s10,3\nM v[88:91],v[216:219],a[8:11],0\ns_and_b32 s11,s11,3\ns_and_b32 s14,s14,3\nM v[88:91],v[220:223],a[24:27],v[88:91]\ns_mov_b32 s47,0x20000\ns_mov_b32 s51,0x20000\n" \
"M v[92:95],v[216:219],a[12:15],0\nbuffer_load_dwordx4 v226,s[36:39],s67 offen offset:2048 lds\nds_read_b64_tr_b16 v[216:217],v242 offset:50688\nds_read_b64_tr_b16 v[218:219],v242 offset:50944\nM v[92:95],v[220:223],a[28:31],v[92:95]\nds_read_b64_tr_b16 v[220:221],v242 offset:58880\nds_read_b64_tr_b16 v[222:223],v242 offset:59136\nM v[32:35],v[192:195],a[0:3],0\n" \
"s_lshl_b32 s10,s10,13\ns_waitcnt lgkmcnt(6)\nM v[32:35],v[196:199],a[16:19],v[32:35]\ns_lshl_b32 s11,s11,13\ns_lshl_b32 s14,s14,13\nM v[36:39],v[192:195],a[4:7],0\ns_mov_b32 s82,0xf0000\ns_mov_b32 s83,0xfff00ff\n" \
"M v[36:39],v[196:199],a[20:23],v[36:39]\nbuffer_load_dwordx4 v227,s[36:39],s67 offen offset:3072 lds\nM v[96:99],v[192:195],a[8:11],0\ns_mov_b32 s84,0x1f0001\ns_mov_b32 s85,0x1fff01ff\nM v[96:99],v[196:199],a[24:27],v[96:99]\ns_add_u32 m0,s65,20480\ns_xor_b32 s67,s66,33024\n" \
"M v[100:103],v[192:195],a[12:15],0\ns_mov_b32 s86,0x3f0003\ns_mov_b32 s87,0x3fff03ff\nM v[100:103],v[196:199],a[28:31],v[100:103]\ns_mov_b32 s88,0x7f0007\ns_mov_b32 s89,0x7fff07ff\nM v[40:43],v[200:203],a[0:3],0\nbuffer_load_dwordx4 v224,s[36:39],s67 offen offset:0 lds\n" \
"s_waitcnt vmcnt(11)\nv_lshlrev_b32 v136,16,v128\nv_and_b32 v137,0xffff0000,v128\nv_lshlrev_b32 v138,16,v129\nv_and_b32 v139,0xffff0000,v129\nv_lshlrev_b32 v140,16,v130\nv_and_b32 v141,0xffff0000,v130\nv_lshlrev_b32 v142,16,v131\n" \
"v_and_b32 v143,0xffff0000,v131\nv_lshlrev_b32 v144,16,v132\nv_and_b32 v145,0xffff0000,v132\nv_lshlrev_b32 v146,16,v133\nv_and_b32 v147,0xffff0000,v133\nv_lshlrev_b32 v148,16,v134\nv_and_b32 v149,0xffff0000,v134\nv_lshlrev_b32 v150,16,v135\n" \
"v_and_b32 v151,0xffff0000,v135\nv_add_f32 v168,v136,v140\nv_add_f32 v128,v144,v148\nv_add_f32 v169,v137,v141\nv_add_f32 v129,v145,v149\nv_add_f32 v170,v138,v142\nv_add_f32 v130,v146,v150\nv_add_f32 v171,v139,v143\n" \
"v_add_f32 v131,v147,v151\nv_add_f32 v168,v168,v128\nv_add_f32 v169,v169,v129\nv_add_f32 v170,v170,v130\nv_add_f32 v171,v171,v131\nv_add_f32 v168,v168,v169\nv_add_f32 v170,v170,v171\nv_add_f32 v168,v168,v170\n" \
"M v[40:43],v[204:207],a[16:19],v[40:43]\ns_mov_b64 s[90:91],-1\nv_add_f32_dpp v168,v168,v168 row_ror:8 row_mask:0xf bank_mask:0xf\nv_mov_b32 v170,v168\nM v[44:47],v[200:203],a[4:7],0\ns_mov_b32 s28,0x5f800000\nv_permlane16_swap_b32 v168,v170\nv_add_f32 v168,v168,v170\n" \
"v_mov_b32 v170,v168\ns_nop 1\nv_permlane32_swap_b32 v168,v170\nv_add_f32 v168,v168,v170\nv_mul_f32 v168,0x3c000000,v168\nv_sub_f32 v152,v136,v168\nv_sub_f32 v153,v137,v168\nv_sub_f32 v154,v138,v168\n" \
"v_sub_f32 v155,v139,v168\nv_sub_f32 v156,v140,v168\nv_sub_f32 v157,v141,v168\nv_sub_f32 v158,v142,v168\nv_sub_f32 v159,v143,v168\nv_mul_f32 v128,v152,v152\nv_mul_f32 v129,v153,v153\nv_mul_f32 v130,v154,v154\n" \
"v_mul_f32 v131,v155,v155\nv_sub_f32 v160,v144,v168\nv_sub_f32 v161,v145,v168\nv_sub_f32 v162,v146,v168\nv_sub_f32 v163,v147,v168\nv_fmac_f32 v128,v156,v156\nv_fmac_f32 v129,v157,v157\nv_fmac_f32 v130,v158,v158\n" \
"v_fmac_f32 v131,v159,v159\nv_sub_f32 v164,v148,v168\nv_sub_f32 v165,v149,v168\nv_sub_f32 v166,v150,v168\nv_sub_f32 v167,v151,v168\nv_fmac_f32 v128,v160,v160\nv_fmac_f32 v129,v161,v161\nv_fmac_f32 v130,v162,v162\n" \
"v_fmac_f32 v131,v163,v163\nv_fmac_f32 v128,v164,v164\nv_fmac_f32 v129,v165,v165\nv_fmac_f32 v130,v166,v166\nv_fmac_f32 v131,v167,v167\nv_add_f32 v128,v128,v129\nv_add_f32 v130,v130,v131\nv_add_f32 v169,v128,v130\n" \
"s_nop 1\nv_add_f32_dpp v169,v169,v169 row_ror:8 row_mask:0xf bank_mask:0xf\nv_mov_b32 v170,v169\ns_nop 1\nv_permlane16_swap_b32 v169,v170\nv_add_f32 v169,v169,v170\nv_mov_b32 v170,v169\ns_nop 1\n" \
"v_permlane32_swap_b32 v169,v170\nv_add_f32 v169,v169,v170\nds_write_b64 v191,v[168:169]\nM v[44:47],v[204:207],a[20:23],v[44:47]\nv_and_b32 v169,3,v249\nv_mul_u32_u24 v169,12288,v169\nM v[104:107],v[200:203],a[8:11],0\nv_and_b32 v170,28,v249\n" \
"v_add_u32 v169,v169,v170\nM v[104:107],v[204:207],a[24:27],v[104:107]\nbuffer_load_dwordx4 v225,s[36:39],s67 offen offset:1024 lds\nM v[108:111],v[200:203],a[12:15],0\nv_lshlrev_b32 v170,3,v170\nM v[108:111],v[204:207],a[28:31],v[108:111]\nM v[48:51],v[208:211],a[0:3],0\ns_waitcnt lgkmcnt(1)\n" \
"M v[48:51],v[212:215],a[16:19],v[48:51]\nM v[52:55],v[208:211],a[4:7],0\nbuffer_load_dwordx4 v226,s[36:39],s67 offen offset:2048 lds\nM v[52:55],v[212:215],a[20:23],v[52:55]\nM v[112:115],v[208:211],a[8:11],0\ns_barrier\nds_read_b128 a[32:35],v228 offset:8192\nds_read_b128 a[36:39],v228 offset:10240\n" \
"ds_read_b128 a[40:43],v228 offset:12288\nds_read_b128 a[44:47],v228 offset:14336\nds_read_b128 a[48:51],v229 offset:8192\nds_read_b128 a[52:55],v229 offset:10240\nds_read_b128 a[56:59],v229 offset:12288\nds_read_b128 a[60:63],v229 offset:14336\nds_read_b32 v184,v250 offset:0\nds_read_b32 v185,v250 offset:256\n" \
"ds_read_b32 v186,v250 offset:512\nds_read_b32 v187,v250 offset:768\ns_waitcnt lgkmcnt(2)\nv_add_f32 v184,v184,v185\ns_waitcnt lgkmcnt(0)\nv_add_f32 v186,v186,v187\nv_lshlrev_b32 v185,2,v249\nv_add_f32 v184,v184,v186\n" \
"buffer_store_dword v184,v185,s[52:55],s79 offen sc1\nds_read_b64_tr_b16 v[192:193],v241 offset:0\nds_read_b64_tr_b16 v[194:195],v241 offset:256\nds_read_b64_tr_b16 v[196:197],v241 offset:8192\nds_read_b64_tr_b16 v[198:199],v241 offset:8448\nds_read_b64_tr_b16 v[200:201],v242 offset:0\nds_read_b64_tr_b16 v[202:203],v242 offset:256\nds_read_b64_tr_b16 v[204:205],v242 offset:8192\n" \
"ds_read_b64_tr_b16 v[206:207],v242 offset:8448\nM v[112:115],v[212:215],a[24:27],v[112:115]\nv_bfe_u32 v250,v249,3,2\nM v[116:119],v[208:211],a[12:15],0\nds_read_b64_tr_b16 v[208:209],v241 offset:512\nds_read_b64_tr_b16 v[210:211],v241 offset:768\nM v[116:119],v[212:215],a[28:31],v[116:119]\nbuffer_load_dwordx4 v227,s[36:39],s67 offen offset:3072 lds\n" \
"ds_read_b64_tr_b16 v[212:213],v241 offset:8704\nds_read_b64_tr_b16 v[214:215],v241 offset:8960\nM v[56:59],v[216:219],a[0:3],0\nM v[56:59],v[220:223],a[16:19],v[56:59]\ns_add_u32 m0,s65,0\ns_xor_b32 s67,s66,384\nM v[60:63],v[216:219],a[4:7],0\nM v[60:63],v[220:223],a[20:23],v[60:63]\n" \
"M v[120:123],v[216:219],a[8:11],0\nbuffer_load_dwordx4 v224,s[36:39],s67 offen offset:0 lds\nM v[120:123],v[220:223],a[24:27],v[120:123]\nM v[124:127],v[216:219],a[12:15],0\nds_read_b64_tr_b16 v[216:217],v242 offset:512\nds_read_b64_tr_b16 v[218:219],v242 offset:768\nM v[124:127],v[220:223],a[28:31],v[124:127]\nds_read_b64_tr_b16 v[220:221],v242 offset:8704\n" \
"ds_read_b64_tr_b16 v[222:223],v242 offset:8960\ns_waitcnt lgkmcnt(11)\nM v[0:3],v[192:195],a[32:35],v[0:3]\nM v[0:3],v[196:199],a[48:51],v[0:3]\nbuffer_load_dwordx4 v225,s[36:39],s67 offen offset:1024 lds\nM v[4:7],v[192:195],a[36:39],v[4:7]\nM v[4:7],v[196:199],a[52:55],v[4:7]\nM v[64:67],v[192:195],a[40:43],v[64:67]\n" \
"M v[64:67],v[196:199],a[56:59],v[64:67]\nM v[68:71],v[192:195],a[44:47],v[68:71]\nbuffer_load_dwordx4 v226,s[36:39],s67 offen offset:2048 lds\nds_read_b64_tr_b16 v[192:193],v241 offset:1024\nds_read_b64_tr_b16 v[194:195],v241 offset:1280\nM v[68:71],v[196:199],a[60:63],v[68:71]\nds_read_b64_tr_b16 v[196:197],v241 offset:9216\nds_read_b64_tr_b16 v[198:199],v241 offset:9472\n" \
"s_waitcnt lgkmcnt(8)\nM v[8:11],v[200:203],a[32:35],v[8:11]\nM v[8:11],v[204:207],a[48:51],v[8:11]\nM v[12:15],v[200:203],a[36:39],v[12:15]\nM v[12:15],v[204:207],a[52:55],v[12:15]\nbuffer_load_dwordx4 v227,s[36:39],s67 offen offset:3072 lds\nM v[72:75],v[200:203],a[40:43],v[72:75]\nM v[72:75],v[204:207],a[56:59],v[72:75]\n" \
"s_add_u32 m0,s65,4096\ns_xor_b32 s67,s66,33152\nM v[76:79],v[200:203],a[44:47],v[76:79]\nds_read_b64_tr_b16 v[200:201],v242 offset:1024\nds_read_b64_tr_b16 v[202:203],v242 offset:1280\nM v[76:79],v[204:207],a[60:63],v[76:79]\nds_read_b64_tr_b16 v[204:205],v242 offset:9216\nds_read_b64_tr_b16 v[206:207],v242 offset:9472\n" \
"M v[16:19],v[208:211],a[32:35],v[16:19]\nbuffer_load_dwordx4 v224,s[36:39],s67 offen offset:0 lds\nM v[16:19],v[212:215],a[48:51],v[16:19]\nM v[20:23],v[208:211],a[36:39],v[20:23]\nM v[20:23],v[212:215],a[52:55],v[20:23]\nM v[80:83],v[208:211],a[40:43],v[80:83]\nM v[80:83],v[212:215],a[56:59],v[80:83]\nbuffer_load_dwordx4 v225,s[36:39],s67 offen offset:1024 lds\n" \
"M v[84:87],v[208:211],a[44:47],v[84:87]\nds_read_b64_tr_b16 v[208:209],v241 offset:1536\nds_read_b64_tr_b16 v[210:211],v241 offset:1792\nM v[84:87],v[212:215],a[60:63],v[84:87]\nds_read_b64_tr_b16 v[212:213],v241 offset:9728\nds_read_b64_tr_b16 v[214:215],v241 offset:9984\ns_waitcnt lgkmcnt(8)\nM v[24:27],v[216:219],a[32:35],v[24:27]\n" \
"M v[24:27],v[220:223],a[48:51],v[24:27]\nM v[28:31],v[216:219],a[36:39],v[28:31]\nbuffer_load_dwordx4 v226,s[36:39],s67 offen offset:2048 lds\nM v[28:31],v[220:223],a[52:55],v[28:31]\nM v[88:91],v[216:219],a[40:43],v[88:91]\nM v[88:91],v[220:223],a[56:59],v[88:91]\nM v[92:95],v[216:219],a[44:47],v[92:95]\nds_read_b64_tr_b16 v[216:217],v242 offset:1536\n" \
"ds_read_b64_tr_b16 v[218:219],v242 offset:1792\nM v[92:95],v[220:223],a[60:63],v[92:95]\nbuffer_load_dwordx4 v227,s[36:39],s67 offen offset:3072 lds\nds_read_b64_tr_b16 v[220:221],v242 offset:9728\nds_read_b64_tr_b16 v[222:223],v242 offset:9984\nM v[32:35],v[192:195],a[32:35],v[32:35]\nM v[32:35],v[196:199],a[48:51],v[32:35]\ns_add_u32 m0,s64,147456\n" \
"s_add_u32 s69,s68,1572864\nM v[36:39],v[192:195],a[36:39],v[36:39]\nM v[36:39],v[196:199],a[52:55],v[36:39]\nM v[96:99],v[192:195],a[40:43],v[96:99]\nbuffer_load_dwordx4 v243,s[40:43],s69 offen offset:0 lds\nM v[96:99],v[196:199],a[56:59],v[96:99]\nM v[100:103],v[192:195],a[44:47],v[100:103]\nM v[100:103],v[196:199],a[60:63],v[100:103]\n" \
"s_waitcnt lgkmcnt(4)\nM v[40:43],v[200:203],a[32:35],v[40:43]\nM v[40:43],v[204:207],a[48:51],v[40:43]\nbuffer_load_dwordx4 v244,s[40:43],s69 offen offset:1024 lds\nM v[44:47],v[200:203],a[36:39],v[44:47]\nM v[44:47],v[204:207],a[52:55],v[44:47]\nM v[104:107],v[200:203],a[40:43],v[104:107]\nM v[104:107],v[204:207],a[56:59],v[104:107]\n" \
"M v[108:111],v[200:203],a[44:47],v[108:111]\nbuffer_load_dwordx4 v245,s[40:43],s69 offen offset:2048 lds\nM v[108:111],v[204:207],a[60:63],v[108:111]\nM v[48:51],v[208:211],a[32:35],v[48:51]\nM v[48:51],v[212:215],a[48:51],v[48:51]\nM v[52:55],v[208:211],a[36:39],v[52:55]\nM v[52:55],v[212:215],a[52:55],v[52:55]\nbuffer_load_dwordx4 v246,s[40:43],s69 offen offset:3072 lds\n" \
"M v[112:115],v[208:211],a[40:43],v[112:115]\ns_waitcnt vmcnt(12) lgkmcnt(0)\ns_barrier\nds_read_b128 a[64:67],v228 offset:16384\nds_read_b128 a[68:71],v228 offset:18432\nds_read_b128 a[72:75],v228 offset:20480\nds_read_b128 a[76:79],v228 offset:22528\nds_read_b128 a[80:83],v229 offset:16384\n" \
"ds_read_b128 a[84:87],v229 offset:18432\nds_read_b128 a[88:91],v229 offset:20480\nds_read_b128 a[92:95],v229 offset:22528\nM v[112:115],v[212:215],a[56:59],v[112:115]\ns_add_u32 m0,s65,8192\ns_xor_b32 s67,s66,512\nds_read_b64_tr_b16 v[192:193],v241 offset:32768\nds_read_b64_tr_b16 v[194:195],v241 offset:33024\n" \
"M v[116:119],v[208:211],a[44:47],v[116:119]\nbuffer_store_dwordx2 v[230:231],v234,s[52:55],s72 offen sc1\nds_read_b64_tr_b16 v[196:197],v241 offset:40960\nds_read_b64_tr_b16 v[198:199],v241 offset:41216\nM v[116:119],v[212:215],a[60:63],v[116:119]\ns_cmp_eq_u32 s6,0\ns_cselect_b32 s72,s75,0x8000000\nM v[56:59],v[216:219],a[32:35],v[56:59]\n" \
"buffer_load_dwordx4 v224,s[36:39],s67 offen offset:0 lds\nM v[56:59],v[220:223],a[48:51],v[56:59]\ns_add_u32 s72,s72,94208\nM v[60:63],v[216:219],a[36:39],v[60:63]\nM v[60:63],v[220:223],a[52:55],v[60:63]\nds_read_b64_tr_b16 v[200:201],v242 offset:32768\nds_read_b64_tr_b16 v[202:203],v242 offset:33024\nds_read_b64_tr_b16 v[204:205],v242 offset:40960\n" \
"ds_read_b64_tr_b16 v[206:207],v242 offset:41216\nds_read_b64_tr_b16 v[208:209],v241 offset:33280\nds_read_b64_tr_b16 v[210:211],v241 offset:33536\nds_read_b64_tr_b16 v[212:213],v241 offset:41472\nds_read_b64_tr_b16 v[214:215],v241 offset:41728\nM v[120:123],v[216:219],a[40:43],v[120:123]\nM v[120:123],v[220:223],a[56:59],v[120:123]\nbuffer_load_dwordx4 v225,s[36:39],s67 offen offset:1024 lds\n" \
"M v[124:127],v[216:219],a[44:47],v[124:127]\nds_read_b64_tr_b16 v[216:217],v242 offset:33280\nds_read_b64_tr_b16 v[218:219],v242 offset:33536\nM v[124:127],v[220:223],a[60:63],v[124:127]\nds_read_b64_tr_b16 v[220:221],v242 offset:41472\nds_read_b64_tr_b16 v[222:223],v242 offset:41728\ns_waitcnt lgkmcnt(14)\nM v[0:3],v[192:195],a[64:67],v[0:3]\n" \
"s_waitcnt lgkmcnt(12)\nM v[0:3],v[196:199],a[80:83],v[0:3]\nM v[4:7],v[192:195],a[68:71],v[4:7]\nbuffer_load_dwordx4 v226,s[36:39],s67 offen offset:2048 lds\nM v[4:7],v[196:199],a[84:87],v[4:7]\nds_read_b128 v[156:159],v251\nM v[64:67],v[192:195],a[72:75],v[64:67]\nds_read_b128 v[160:163],v251 offset:16\n" \
"v_lshrrev_b32 v251,3,v249\nM v[64:67],v[196:199],a[88:91],v[64:67]\nM v[68:71],v[192:195],a[76:79],v[68:71]\nds_read_b64_tr_b16 v[192:193],v241 offset:33792\nds_read_b64_tr_b16 v[194:195],v241 offset:34048\nM v[68:71],v[196:199],a[92:95],v[68:71]\nbuffer_load_dwordx4 v227,s[36:39],s67 offen offset:3072 lds\nds_read_b64_tr_b16 v[196:197],v241 offset:41984\n" \
"ds_read_b64_tr_b16 v[198:199],v241 offset:42240\ns_waitcnt lgkmcnt(10)\nM v[8:11],v[200:203],a[64:67],v[8:11]\nM v[8:11],v[204:207],a[80:83],v[8:11]\ns_add_u32 m0,s65,12288\ns_xor_b32 s67,s66,33280\nM v[12:15],v[200:203],a[68:71],v[12:15]\nM v[12:15],v[204:207],a[84:87],v[12:15]\n" \
"M v[72:75],v[200:203],a[72:75],v[72:75]\nbuffer_load_dwordx4 v224,s[36:39],s67 offen offset:0 lds\nM v[72:75],v[204:207],a[88:91],v[72:75]\ns_waitcnt lgkmcnt(5)\nv_add_f32 v164,v156,v158\nM v[76:79],v[200:203],a[76:79],v[76:79]\nds_read_b64_tr_b16 v[200:201],v242 offset:33792\nds_read_b64_tr_b16 v[202:203],v242 offset:34048\n" \
"M v[76:79],v[204:207],a[92:95],v[76:79]\nds_read_b64_tr_b16 v[204:205],v242 offset:41984\nds_read_b64_tr_b16 v[206:207],v242 offset:42240\nM v[16:19],v[208:211],a[64:67],v[16:19]\ns_waitcnt lgkmcnt(8)\nv_add_f32 v165,v160,v162\nM v[16:19],v[212:215],a[80:83],v[16:19]\nbuffer_load_dwordx4 v225,s[36:39],s67 offen offset:1024 lds\n" \
"M v[20:23],v[208:211],a[68:71],v[20:23]\nv_add_f32 v164,v164,v165\nv_lshlrev_b32 v165,3,v249\nM v[20:23],v[212:215],a[84:87],v[20:23]\nv_mul_f32 v164,0x3e800000,v164\nv_sub_f32 v156,v156,v164\nM v[80:83],v[208:211],a[72:75],v[80:83]\nv_sub_f32 v158,v158,v164\n" \
"v_mul_f32 v156,v156,v156\nM v[80:83],v[212:215],a[88:91],v[80:83]\nv_sub_f32 v160,v160,v164\nv_fmac_f32 v156,v158,v158\nM v[84:87],v[208:211],a[76:79],v[84:87]\nbuffer_load_dwordx4 v226,s[36:39],s67 offen offset:2048 lds\nds_read_b64_tr_b16 v[208:209],v241 offset:34304\nds_read_b64_tr_b16 v[210:211],v241 offset:34560\n" \
"M v[84:87],v[212:215],a[92:95],v[84:87]\nds_read_b64_tr_b16 v[212:213],v241 offset:42496\nds_read_b64_tr_b16 v[214:215],v241 offset:42752\nM v[24:27],v[216:219],a[64:67],v[24:27]\nv_add_f32 v157,v157,v159\nv_add_f32 v161,v161,v163\nM v[24:27],v[220:223],a[80:83],v[24:27]\nv_sub_f32 v162,v162,v164\n" \
"v_fmac_f32 v156,v160,v160\nM v[28:31],v[216:219],a[68:71],v[28:31]\nglobal_load_dword v247,v253,s[16:17]\nM v[28:31],v[220:223],a[84:87],v[28:31]\nbuffer_load_dwordx4 v227,s[36:39],s67 offen offset:3072 lds\nM v[88:91],v[216:219],a[72:75],v[88:91]\nv_add_u32 v253,0x100000,v253\nv_add_f32 v157,v157,v161\n" \
"M v[88:91],v[220:223],a[88:91],v[88:91]\ns_add_u32 m0,s64,98304\ns_add_u32 s69,s68,2097152\nM v[92:95],v[216:219],a[76:79],v[92:95]\nds_read_b64_tr_b16 v[216:217],v242 offset:34304\nds_read_b64_tr_b16 v[218:219],v242 offset:34560\nM v[92:95],v[220:223],a[92:95],v[92:95]\nds_read_b64_tr_b16 v[220:221],v242 offset:42496\n" \
"ds_read_b64_tr_b16 v[222:223],v242 offset:42752\ns_waitcnt lgkmcnt(8)\nM v[32:35],v[192:195],a[64:67],v[32:35]\nbuffer_load_dwordx4 v243,s[40:43],s69 offen offset:0 lds\nM v[32:35],v[196:199],a[80:83],v[32:35]\nv_fmac_f32 v156,v162,v162\nv_fmac_f32 v157,0x43000000,v156\nM v[36:39],v[192:195],a[68:71],v[36:39]\n" \
"v_mul_f32 v157,0x3b000000,v157\nv_cvt_pk_bf16_f32 v157,0,v157\nM v[36:39],v[196:199],a[84:87],v[36:39]\nv_add_f32 v157,0x3727c5ac,v157\nv_cvt_pk_bf16_f32 v157,0,v157\nM v[96:99],v[192:195],a[72:75],v[96:99]\nv_sqrt_f32 v157,v157\nM v[96:99],v[196:199],a[88:91],v[96:99]\n" \
"buffer_load_dwordx4 v244,s[40:43],s69 offen offset:1024 lds\nM v[100:103],v[192:195],a[76:79],v[100:103]\nv_cvt_pk_bf16_f32 v164,0,v164\nv_cvt_pk_bf16_f32 v157,0,v157\nM v[100:103],v[196:199],a[92:95],v[100:103]\nv_rcp_f32 v157,v157\nM v[40:43],v[200:203],a[64:67],v[40:43]\nv_sub_f32 v136,v136,v164\n" \
"v_sub_f32 v137,v137,v164\nM v[40:43],v[204:207],a[80:83],v[40:43]\nv_sub_f32 v138,v138,v164\nv_sub_f32 v139,v139,v164\nM v[44:47],v[200:203],a[68:71],v[44:47]\nbuffer_load_dwordx4 v245,s[40:43],s69 offen offset:2048 lds\nM v[44:47],v[204:207],a[84:87],v[44:47]\nv_sub_f32 v140,v140,v164\n" \
"v_sub_f32 v141,v141,v164\nM v[104:107],v[200:203],a[72:75],v[104:107]\nv_sub_f32 v142,v142,v164\nv_sub_f32 v143,v143,v164\nM v[104:107],v[204:207],a[88:91],v[104:107]\nv_sub_f32 v144,v144,v164\nv_sub_f32 v145,v145,v164\nM v[108:111],v[200:203],a[76:79],v[108:111]\n" \
"v_sub_f32 v146,v146,v164\nv_sub_f32 v147,v147,v164\nM v[108:111],v[204:207],a[92:95],v[108:111]\nbuffer_load_dwordx4 v246,s[40:43],s69 offen offset:3072 lds\ns_waitcnt lgkmcnt(0)\nM v[48:51],v[208:211],a[64:67],v[48:51]\nv_sub_f32 v148,v148,v164\nv_sub_f32 v149,v149,v164\n" \
"M v[48:51],v[212:215],a[80:83],v[48:51]\ns_add_u32 m0,s65,16384\ns_xor_b32 s67,s66,640\nM v[52:55],v[208:211],a[68:71],v[52:55]\nv_sub_f32 v150,v150,v164\nv_sub_f32 v151,v151,v164\nM v[52:55],v[212:215],a[84:87],v[52:55]\nv_cvt_pk_bf16_f32 v136,0,v136\n" \
"v_cvt_pk_bf16_f32 v137,0,v137\nM v[112:115],v[208:211],a[72:75],v[112:115]\nbuffer_load_dwordx4 v224,s[36:39],s67 offen offset:0 lds\ns_waitcnt vmcnt(15)\ns_barrier\nds_read_b128 a[96:99],v228 offset:0\nds_read_b128 a[100:103],v228 offset:2048\nds_read_b128 a[104:107],v228 offset:4096\n" \
"ds_read_b128 a[108:111],v228 offset:6144\nds_read_b128 a[112:115],v229 offset:0\nds_read_b128 a[116:119],v229 offset:2048\nds_read_b128 a[120:123],v229 offset:4096\nds_read_b128 a[124:127],v229 offset:6144\nM v[112:115],v[212:215],a[88:91],v[112:115]\nds_read_b64_tr_b16 v[192:193],v241 offset:49152\nds_read_b64_tr_b16 v[194:195],v241 offset:49408\n" \
"M v[116:119],v[208:211],a[76:79],v[116:119]\nds_read_b64_tr_b16 v[196:197],v241 offset:57344\nds_read_b64_tr_b16 v[198:199],v241 offset:57600\nM v[116:119],v[212:215],a[92:95],v[116:119]\nv_cvt_pk_bf16_f32 v138,0,v138\nv_cvt_pk_bf16_f32 v139,0,v139\nM v[56:59],v[216:219],a[64:67],v[56:59]\nv_cvt_pk_bf16_f32 v140,0,v140\n" \
"v_cvt_pk_bf16_f32 v141,0,v141\nM v[56:59],v[220:223],a[80:83],v[56:59]\nbuffer_load_dwordx4 v225,s[36:39],s67 offen offset:1024 lds\nM v[60:63],v[216:219],a[68:71],v[60:63]\nv_cvt_pk_bf16_f32 v142,0,v142\nv_cvt_pk_bf16_f32 v143,0,v143\nM v[60:63],v[220:223],a[84:87],v[60:63]\nv_cvt_pk_bf16_f32 v144,0,v144\n" \
"v_cvt_pk_bf16_f32 v145,0,v145\nM v[120:123],v[216:219],a[72:75],v[120:123]\nv_cvt_pk_bf16_f32 v146,0,v146\nv_cvt_pk_bf16_f32 v147,0,v147\nM v[120:123],v[220:223],a[88:91],v[120:123]\nds_read_b64_tr_b16 v[200:201],v242 offset:49152\nds_read_b64_tr_b16 v[202:203],v242 offset:49408\nM v[124:127],v[216:219],a[76:79],v[124:127]\n" \
"buffer_load_dwordx4 v226,s[36:39],s67 offen offset:2048 lds\nds_read_b64_tr_b16 v[204:205],v242 offset:57344\nds_read_b64_tr_b16 v[206:207],v242 offset:57600\nM v[124:127],v[220:223],a[92:95],v[124:127]\nds_read_b64_tr_b16 v[208:209],v241 offset:49664\ns_waitcnt lgkmcnt(7)\nM v[0:3],v[192:195],a[96:99],v[0:3]\nds_read_b64_tr_b16 v[210:211],v241 offset:49920\n" \
"s_waitcnt lgkmcnt(6)\nM v[0:3],v[196:199],a[112:115],v[0:3]\nds_read_b64_tr_b16 v[212:213],v241 offset:57856\nds_read_b64_tr_b16 v[214:215],v241 offset:58112\nM v[4:7],v[192:195],a[100:103],v[4:7]\nglobal_load_dword v247,v253,s[16:17]\nM v[4:7],v[196:199],a[116:119],v[4:7]\nbuffer_load_dwordx4 v227,s[36:39],s67 offen offset:3072 lds\n" \
"M v[64:67],v[192:195],a[104:107],v[64:67]\nds_read_b64_tr_b16 v[216:217],v242 offset:49664\nds_read_b64_tr_b16 v[218:219],v242 offset:49920\nM v[64:67],v[196:199],a[120:123],v[64:67]\ns_add_u32 m0,s65,20480\ns_xor_b32 s67,s66,33408\nM v[68:71],v[192:195],a[108:111],v[68:71]\nds_read_b64_tr_b16 v[220:221],v242 offset:57856\n" \
"ds_read_b64_tr_b16 v[222:223],v242 offset:58112\nM v[68:71],v[196:199],a[124:127],v[68:71]\nds_read_b64_tr_b16 v[192:193],v241 offset:50176\ns_waitcnt lgkmcnt(11)\nM v[8:11],v[200:203],a[96:99],v[8:11]\nbuffer_load_dwordx4 v224,s[36:39],s67 offen offset:0 lds\ns_waitcnt lgkmcnt(9)\nM v[8:11],v[204:207],a[112:115],v[8:11]\n" \
"ds_read_b64_tr_b16 v[194:195],v241 offset:50432\nds_read_b64_tr_b16 v[196:197],v241 offset:58368\nM v[12:15],v[200:203],a[100:103],v[12:15]\nds_read_b64_tr_b16 v[198:199],v241 offset:58624\nv_add_u32 v253,0x100000,v253\nM v[12:15],v[204:207],a[116:119],v[12:15]\nv_cvt_pk_bf16_f32 v148,0,v148\nv_cvt_pk_bf16_f32 v149,0,v149\n" \
"M v[72:75],v[200:203],a[104:107],v[72:75]\nv_cvt_pk_bf16_f32 v150,0,v150\nv_cvt_pk_bf16_f32 v151,0,v151\nM v[72:75],v[204:207],a[120:123],v[72:75]\nbuffer_load_dwordx4 v225,s[36:39],s67 offen offset:1024 lds\nM v[76:79],v[200:203],a[108:111],v[76:79]\nds_read_b64_tr_b16 v[200:201],v242 offset:50176\nds_read_b64_tr_b16 v[202:203],v242 offset:50432\n" \
"M v[76:79],v[204:207],a[124:127],v[76:79]\nds_read_b64_tr_b16 v[204:205],v242 offset:58368\ns_waitcnt lgkmcnt(9)\nM v[16:19],v[208:211],a[96:99],v[16:19]\nds_read_b64_tr_b16 v[206:207],v242 offset:58624\nv_mul_f32 v136,v136,v157\nM v[16:19],v[212:215],a[112:115],v[16:19]\nv_mul_f32 v137,v137,v157\n" \
"v_mul_f32 v138,v138,v157\nM v[20:23],v[208:211],a[100:103],v[20:23]\nbuffer_load_dwordx4 v226,s[36:39],s67 offen offset:2048 lds\nM v[20:23],v[212:215],a[116:119],v[20:23]\nv_mul_f32 v139,v139,v157\nv_mul_f32 v140,v140,v157\nM v[80:83],v[208:211],a[104:107],v[80:83]\nv_mul_f32 v141,v141,v157\n" \
"v_mul_f32 v142,v142,v157\nM v[80:83],v[212:215],a[120:123],v[80:83]\nv_mul_f32 v143,v143,v157\nv_mul_f32 v144,v144,v157\nM v[84:87],v[208:211],a[108:111],v[84:87]\nds_read_b64_tr_b16 v[208:209],v241 offset:50688\nds_read_b64_tr_b16 v[210:211],v241 offset:50944\nM v[84:87],v[212:215],a[124:127],v[84:87]\n" \
"buffer_load_dwordx4 v227,s[36:39],s67 offen offset:3072 lds\nM v[24:27],v[216:219],a[96:99],v[24:27]\nds_read_b64_tr_b16 v[212:213],v241 offset:58880\ns_waitcnt lgkmcnt(6)\nM v[24:27],v[220:223],a[112:115],v[24:27]\ns_add_u32 m0,s64,114688\ns_add_u32 s69,s68,2621440\nM v[28:31],v[216:219],a[100:103],v[28:31]\n" \
"ds_read_b64_tr_b16 v[214:215],v241 offset:59136\nv_mul_f32 v145,v145,v157\nM v[28:31],v[220:223],a[116:119],v[28:31]\nv_mul_f32 v146,v146,v157\nv_mul_f32 v147,v147,v157\nM v[88:91],v[216:219],a[104:107],v[88:91]\nbuffer_load_dwordx4 v243,s[40:43],s69 offen offset:0 lds\nM v[88:91],v[220:223],a[120:123],v[88:91]\n" \
"v_mul_f32 v148,v148,v157\nv_mul_f32 v149,v149,v157\nM v[92:95],v[216:219],a[108:111],v[92:95]\nds_read_b64_tr_b16 v[216:217],v242 offset:50688\nds_read_b64_tr_b16 v[218:219],v242 offset:50944\nM v[92:95],v[220:223],a[124:127],v[92:95]\nds_read_b64_tr_b16 v[220:221],v242 offset:58880\nds_read_b64_tr_b16 v[222:223],v242 offset:59136\n" \
"M v[32:35],v[192:195],a[96:99],v[32:35]\nbuffer_load_dwordx2 v[166:167],v165,s[52:55],s80 offen sc1\nM v[32:35],v[196:199],a[112:115],v[32:35]\nbuffer_load_dwordx4 v244,s[40:43],s69 offen offset:1024 lds\nM v[36:39],v[192:195],a[100:103],v[36:39]\nv_mul_f32 v150,v150,v157\nv_mul_f32 v151,v151,v157\nM v[36:39],v[196:199],a[116:119],v[36:39]\n" \
"v_cvt_pk_bf16_f32 v144,0,v144\nv_cvt_pk_bf16_f32 v145,0,v145\nM v[96:99],v[192:195],a[104:107],v[96:99]\nv_cvt_pk_bf16_f32 v136,0,v136\nv_cvt_pk_bf16_f32 v137,0,v137\nM v[96:99],v[196:199],a[120:123],v[96:99]\nv_cvt_pk_bf16_f32 v138,0,v138\nv_cvt_pk_bf16_f32 v139,0,v139\n" \
"M v[100:103],v[192:195],a[108:111],v[100:103]\nbuffer_load_dwordx4 v245,s[40:43],s69 offen offset:2048 lds\nM v[100:103],v[196:199],a[124:127],v[100:103]\nv_cvt_pk_bf16_f32 v140,0,v140\ns_waitcnt lgkmcnt(4)\nM v[40:43],v[200:203],a[96:99],v[40:43]\nv_cvt_pk_bf16_f32 v141,0,v141\nv_cvt_pk_bf16_f32 v142,0,v142\n" \
"M v[40:43],v[204:207],a[112:115],v[40:43]\nv_cvt_pk_bf16_f32 v143,0,v143\nv_cvt_pk_bf16_f32 v146,0,v146\nM v[44:47],v[200:203],a[100:103],v[44:47]\nglobal_load_dword v247,v253,s[16:17]\nM v[44:47],v[204:207],a[116:119],v[44:47]\nbuffer_load_dwordx4 v246,s[40:43],s69 offen offset:3072 lds\nM v[104:107],v[200:203],a[104:107],v[104:107]\n" \
"v_add_u32 v253,0x100000,v253\nv_cvt_pk_bf16_f32 v147,0,v147\nM v[104:107],v[204:207],a[120:123],v[104:107]\ns_add_u32 m0,s65,0\ns_xor_b32 s67,s66,768\nM v[108:111],v[200:203],a[108:111],v[108:111]\nv_cvt_pk_bf16_f32 v148,0,v148\nv_cvt_pk_bf16_f32 v149,0,v149\n" \
"M v[108:111],v[204:207],a[124:127],v[108:111]\nv_cvt_pk_bf16_f32 v150,0,v150\nv_cvt_pk_bf16_f32 v151,0,v151\nM v[48:51],v[208:211],a[96:99],v[48:51]\nbuffer_load_dwordx4 v224,s[36:39],s67 offen offset:0 lds\nM v[48:51],v[212:215],a[112:115],v[48:51]\nM v[52:55],v[208:211],a[100:103],v[52:55]\nM v[52:55],v[212:215],a[116:119],v[52:55]\n" \
"M v[112:115],v[208:211],a[104:107],v[112:115]\ns_waitcnt vmcnt(16) lgkmcnt(0)\ns_barrier\nM v[112:115],v[212:215],a[120:123],v[112:115]\nbuffer_load_dwordx4 v225,s[36:39],s67 offen offset:1024 lds\nds_read_b128 a[128:131],v228 offset:8192\nds_read_b64_tr_b16 v[192:193],v241 offset:0\nds_read_b64_tr_b16 v[194:195],v241 offset:256\n" \
"M v[116:119],v[208:211],a[108:111],v[116:119]\nds_read_b128 a[144:147],v229 offset:8192\nds_read_b64_tr_b16 v[196:197],v241 offset:8192\nds_read_b64_tr_b16 v[198:199],v241 offset:8448\nM v[116:119],v[212:215],a[124:127],v[116:119]\nds_read_b128 a[132:135],v228 offset:10240\nM v[56:59],v[216:219],a[96:99],v[56:59]\nds_read_b128 a[148:151],v229 offset:10240\n" \
"M v[56:59],v[220:223],a[112:115],v[56:59]\nds_read_b128 a[136:139],v228 offset:12288\nM v[60:63],v[216:219],a[100:103],v[60:63]\nbuffer_load_dwordx4 v226,s[36:39],s67 offen offset:2048 lds\nds_read_b128 a[152:155],v229 offset:12288\nM v[60:63],v[220:223],a[116:119],v[60:63]\nds_read_b128 a[140:143],v228 offset:14336\nM v[120:123],v[216:219],a[104:107],v[120:123]\n" \
"ds_read_b128 a[156:159],v229 offset:14336\nM v[120:123],v[220:223],a[120:123],v[120:123]\nds_read_b64_tr_b16 v[200:201],v242 offset:0\nds_read_b64_tr_b16 v[202:203],v242 offset:256\nM v[124:127],v[216:219],a[108:111],v[124:127]\nds_read_b64_tr_b16 v[204:205],v242 offset:8192\nds_read_b64_tr_b16 v[206:207],v242 offset:8448\nM v[124:127],v[220:223],a[124:127],v[124:127]\n" \
"buffer_load_dwordx4 v227,s[36:39],s67 offen offset:3072 lds\ns_waitcnt lgkmcnt(13)\nM v[0:3],v[192:195],a[128:131],v[0:3]\nds_read_b64_tr_b16 v[208:209],v241 offset:512\ns_waitcnt lgkmcnt(11)\nM v[0:3],v[196:199],a[144:147],v[0:3]\ns_add_u32 m0,s65,4096\ns_xor_b32 s67,s66,33536\n" \
"s_waitcnt lgkmcnt(10)\nM v[4:7],v[192:195],a[132:135],v[4:7]\nds_read_b64_tr_b16 v[210:211],v241 offset:768\ns_waitcnt lgkmcnt(10)\nM v[4:7],v[196:199],a[148:151],v[4:7]\nds_read_b64_tr_b16 v[212:213],v241 offset:8704\ns_waitcnt lgkmcnt(10)\nM v[64:67],v[192:195],a[136:139],v[64:67]\n" \
"buffer_load_dwordx4 v224,s[36:39],s67 offen offset:0 lds\ns_waitcnt lgkmcnt(9)\nM v[64:67],v[196:199],a[152:155],v[64:67]\nds_read_b64_tr_b16 v[214:215],v241 offset:8960\ns_waitcnt lgkmcnt(9)\nM v[68:71],v[192:195],a[140:143],v[68:71]\nds_read_b64_tr_b16 v[216:217],v242 offset:512\ns_waitcnt lgkmcnt(9)\n" \
"M v[68:71],v[196:199],a[156:159],v[68:71]\nds_read_b64_tr_b16 v[218:219],v242 offset:768\ns_waitcnt lgkmcnt(8)\nM v[8:11],v[200:203],a[128:131],v[8:11]\nds_read_b64_tr_b16 v[220:221],v242 offset:8704\ns_waitcnt lgkmcnt(7)\nM v[8:11],v[204:207],a[144:147],v[8:11]\nbuffer_load_dwordx4 v225,s[36:39],s67 offen offset:1024 lds\n" \
"M v[12:15],v[200:203],a[132:135],v[12:15]\nds_read_b64_tr_b16 v[222:223],v242 offset:8960\nds_read_b64_tr_b16 v[192:193],v241 offset:1024\nM v[12:15],v[204:207],a[148:151],v[12:15]\nds_read_b64_tr_b16 v[194:195],v241 offset:1280\nds_read_b64_tr_b16 v[196:197],v241 offset:9216\nM v[72:75],v[200:203],a[136:139],v[72:75]\nds_read_b64_tr_b16 v[198:199],v241 offset:9472\n" \
"M v[72:75],v[204:207],a[152:155],v[72:75]\nM v[76:79],v[200:203],a[140:143],v[76:79]\nbuffer_load_dwordx4 v226,s[36:39],s67 offen offset:2048 lds\nM v[76:79],v[204:207],a[156:159],v[76:79]\nds_read_b64_tr_b16 v[200:201],v242 offset:1024\ns_waitcnt lgkmcnt(10)\nM v[16:19],v[208:211],a[128:131],v[16:19]\nds_read_b64_tr_b16 v[202:203],v242 offset:1280\n" \
"s_waitcnt lgkmcnt(10)\nM v[16:19],v[212:215],a[144:147],v[16:19]\nds_read_b64_tr_b16 v[204:205],v242 offset:9216\nds_read_b64_tr_b16 v[206:207],v242 offset:9472\nM v[20:23],v[208:211],a[132:135],v[20:23]\nglobal_load_dword v247,v253,s[16:17]\nM v[20:23],v[212:215],a[148:151],v[20:23]\nbuffer_load_dwordx4 v227,s[36:39],s67 offen offset:3072 lds\n" \
"M v[80:83],v[208:211],a[136:139],v[80:83]\nv_add_u32 v253,0xffd00000,v253\nM v[80:83],v[212:215],a[152:155],v[80:83]\ns_add_u32 m0,s64,131072\ns_add_u32 s69,s68,3145728\nM v[84:87],v[208:211],a[140:143],v[84:87]\nds_read_b64_tr_b16 v[208:209],v241 offset:1536\nds_read_b64_tr_b16 v[210:211],v241 offset:1792\n" \
"M v[84:87],v[212:215],a[156:159],v[84:87]\nds_read_b64_tr_b16 v[212:213],v241 offset:9728\ns_waitcnt lgkmcnt(7)\nM v[24:27],v[216:219],a[128:131],v[24:27]\nbuffer_load_dwordx4 v243,s[40:43],s69 offen offset:0 lds\nM v[24:27],v[220:223],a[144:147],v[24:27]\nds_read_b64_tr_b16 v[214:215],v241 offset:9984\nM v[28:31],v[216:219],a[132:135],v[28:31]\n" \
"M v[28:31],v[220:223],a[148:151],v[28:31]\nM v[88:91],v[216:219],a[136:139],v[88:91]\nM v[88:91],v[220:223],a[152:155],v[88:91]\nbuffer_load_dwordx4 v244,s[40:43],s69 offen offset:1024 lds\nM v[92:95],v[216:219],a[140:143],v[92:95]\nds_read_b64_tr_b16 v[216:217],v242 offset:1536\nds_read_b64_tr_b16 v[218:219],v242 offset:1792\nM v[92:95],v[220:223],a[156:159],v[92:95]\n" \
"ds_read_b64_tr_b16 v[220:221],v242 offset:9728\nds_read_b64_tr_b16 v[222:223],v242 offset:9984\nM v[32:35],v[192:195],a[128:131],v[32:35]\nM v[32:35],v[196:199],a[144:147],v[32:35]\nM v[36:39],v[192:195],a[132:135],v[36:39]\nbuffer_load_dwordx4 v245,s[40:43],s69 offen offset:2048 lds\nM v[36:39],v[196:199],a[148:151],v[36:39]\nM v[96:99],v[192:195],a[136:139],v[96:99]\n" \
"s_waitcnt vmcnt(16)\nv_xor_b32 v166,v166,v230\nM v[96:99],v[196:199],a[152:155],v[96:99]\nv_xor_b32 v167,v167,v231\nv_or_b32 v166,v166,v167\nM v[100:103],v[192:195],a[140:143],v[100:103]\nv_cmp_ne_u32 vcc,0,v166\nM v[100:103],v[196:199],a[156:159],v[100:103]\n" \
"buffer_load_dwordx4 v246,s[40:43],s69 offen offset:3072 lds\ns_waitcnt lgkmcnt(4)\nM v[40:43],v[200:203],a[128:131],v[40:43]\ns_cbranch_vccnz .Lhs\n.Lhb:\nv_bfe_u32 v231,v249,2,3\nM v[40:43],v[204:207],a[144:147],v[40:43]\ns_add_u32 m0,s65,8192\n" \
"s_xor_b32 s67,s66,896\nM v[44:47],v[200:203],a[132:135],v[44:47]\nv_mul_u32_u24 v231,3584,v231\nv_and_b32 v230,31,v249\nM v[44:47],v[204:207],a[148:151],v[44:47]\nv_lshlrev_b32 v230,7,v230\nM v[104:107],v[200:203],a[136:139],v[104:107]\nbuffer_load_dwordx4 v224,s[36:39],s67 offen offset:0 lds\n" \
"M v[104:107],v[204:207],a[152:155],v[104:107]\nM v[108:111],v[200:203],a[140:143],v[108:111]\nM v[108:111],v[204:207],a[156:159],v[108:111]\nM v[48:51],v[208:211],a[128:131],v[48:51]\nM v[48:51],v[212:215],a[144:147],v[48:51]\nbuffer_load_dwordx4 v225,s[36:39],s67 offen offset:1024 lds\nM v[52:55],v[208:211],a[132:135],v[52:55]\nM v[52:55],v[212:215],a[148:151],v[52:55]\n" \
"M v[112:115],v[208:211],a[136:139],v[112:115]\ns_waitcnt vmcnt(15) lgkmcnt(0)\ns_barrier\nM v[112:115],v[212:215],a[152:155],v[112:115]\nds_read_b128 a[160:163],v228 offset:16384\nds_read_b64_tr_b16 v[192:193],v241 offset:16384\nds_read_b64_tr_b16 v[194:195],v241 offset:16640\nM v[116:119],v[208:211],a[140:143],v[116:119]\n" \
"ds_read_b128 a[176:179],v229 offset:16384\nds_read_b64_tr_b16 v[196:197],v241 offset:24576\nds_read_b64_tr_b16 v[198:199],v241 offset:24832\nM v[116:119],v[212:215],a[156:159],v[116:119]\nds_read_b128 a[164:167],v228 offset:18432\nM v[56:59],v[216:219],a[128:131],v[56:59]\nbuffer_load_dwordx4 v[152:155],v240,s[52:55],s71 offen sc1\nds_read_b128 a[180:183],v229 offset:18432\n" \
"M v[56:59],v[220:223],a[144:147],v[56:59]\nbuffer_load_dwordx4 v226,s[36:39],s67 offen offset:2048 lds\ns_add_u32 s71,s70,798720\nds_read_b128 a[168:171],v228 offset:20480\nbuffer_load_dwordx4 v[156:159],v240,s[52:55],s71 offen sc1\nM v[60:63],v[216:219],a[132:135],v[60:63]\nds_read_b128 a[184:187],v229 offset:20480\ns_add_u32 s71,s70,811008\n" \
"M v[60:63],v[220:223],a[148:151],v[60:63]\nbuffer_load_dwordx4 v227,s[36:39],s67 offen offset:3072 lds\nbuffer_load_dwordx4 v[160:163],v240,s[52:55],s71 offen sc1\nds_read_b128 a[172:175],v228 offset:22528\nM v[120:123],v[216:219],a[136:139],v[120:123]\ns_add_u32 s71,s70,823296\nds_read_b128 a[188:191],v229 offset:22528\nbuffer_load_dwordx4 v[164:167],v240,s[52:55],s71 offen sc1\n" \
"M v[120:123],v[220:223],a[152:155],v[120:123]\ns_add_u32 m0,s65,12288\ns_xor_b32 s67,s66,33664\nds_read_b64_tr_b16 v[200:201],v242 offset:16384\nds_read_b64_tr_b16 v[202:203],v242 offset:16640\nM v[124:127],v[216:219],a[140:143],v[124:127]\nds_read_b64_tr_b16 v[204:205],v242 offset:24576\nds_read_b64_tr_b16 v[206:207],v242 offset:24832\n" \
"M v[124:127],v[220:223],a[156:159],v[124:127]\ns_lshl_b32 s71,s6,5\ns_waitcnt lgkmcnt(9)\nM v[0:3],v[192:195],a[160:163],v[0:3]\nds_read_b64_tr_b16 v[208:209],v241 offset:16896\ns_add_u32 s71,s71,790528\nM v[0:3],v[196:199],a[176:179],v[0:3]\nds_read_b64_tr_b16 v[210:211],v241 offset:17152\n" \
"ds_read_b64_tr_b16 v[212:213],v241 offset:25088\nM v[4:7],v[192:195],a[164:167],v[4:7]\nds_read_b64_tr_b16 v[214:215],v241 offset:25344\ns_waitcnt lgkmcnt(11)\nM v[4:7],v[196:199],a[180:183],v[4:7]\nds_read_b64_tr_b16 v[216:217],v242 offset:16896\nds_read_b64_tr_b16 v[218:219],v242 offset:17152\nM v[64:67],v[192:195],a[168:171],v[64:67]\n" \
"ds_read_b64_tr_b16 v[220:221],v242 offset:25088\ns_waitcnt lgkmcnt(13)\nM v[64:67],v[196:199],a[184:187],v[64:67]\nds_read_b64_tr_b16 v[222:223],v242 offset:25344\ns_waitcnt lgkmcnt(13)\nM v[68:71],v[192:195],a[172:175],v[68:71]\nds_read_b64_tr_b16 v[192:193],v241 offset:17408\ns_waitcnt lgkmcnt(13)\n" \
"M v[68:71],v[196:199],a[188:191],v[68:71]\nds_read_b64_tr_b16 v[194:195],v241 offset:17664\ns_waitcnt lgkmcnt(12)\nM v[8:11],v[200:203],a[160:163],v[8:11]\nds_read_b64_tr_b16 v[196:197],v241 offset:25600\ns_waitcnt lgkmcnt(11)\nM v[8:11],v[204:207],a[176:179],v[8:11]\ns_cmp_lt_u32 s7,2\n" \
"s_cselect_b32 s70,0,1.0\nM v[12:15],v[200:203],a[164:167],v[12:15]\nbuffer_load_dword v247,v254,s[56:59],s78 offen\nM v[12:15],v[204:207],a[180:183],v[12:15]\nds_read_b64_tr_b16 v[198:199],v241 offset:25856\nv_and_b32 v254,3,v249\nM v[72:75],v[200:203],a[168:171],v[72:75]\ns_cmp_eq_u32 s62,5\n" \
"s_cselect_b64 s[56:57],s[22:23],s[24:25]\nM v[72:75],v[204:207],a[184:187],v[72:75]\nv_lshl_add_u32 v231,v254,7,v231\nM v[76:79],v[200:203],a[172:175],v[76:79]\nglobal_load_dword v247,v253,s[12:13]\nM v[76:79],v[204:207],a[188:191],v[76:79]\nds_read_b64_tr_b16 v[200:201],v242 offset:17408\ns_waitcnt lgkmcnt(6)\n" \
"M v[16:19],v[208:211],a[160:163],v[16:19]\nds_read_b64_tr_b16 v[202:203],v242 offset:17664\nds_read_b64_tr_b16 v[204:205],v242 offset:25600\nM v[16:19],v[212:215],a[176:179],v[16:19]\nds_read_b64_tr_b16 v[206:207],v242 offset:25856\ns_add_u32 s69,s68,3670016\nM v[20:23],v[208:211],a[164:167],v[20:23]\nbuffer_load_dwordx4 v224,s[36:39],s67 offen offset:0 lds\n" \
"M v[20:23],v[212:215],a[180:183],v[20:23]\nv_add_u32 v253,0x100000,v253\nM v[80:83],v[208:211],a[168:171],v[80:83]\nM v[80:83],v[212:215],a[184:187],v[80:83]\nM v[84:87],v[208:211],a[172:175],v[84:87]\nbuffer_load_dwordx4 v225,s[36:39],s67 offen offset:1024 lds\nM v[84:87],v[212:215],a[188:191],v[84:87]\nds_read_b64_tr_b16 v[208:209],v241 offset:17920\n" \
"ds_read_b64_tr_b16 v[210:211],v241 offset:18176\nM v[24:27],v[216:219],a[160:163],v[24:27]\nds_read_b64_tr_b16 v[212:213],v241 offset:26112\ns_waitcnt lgkmcnt(7)\nM v[24:27],v[220:223],a[176:179],v[24:27]\nds_read_b64_tr_b16 v[214:215],v241 offset:26368\nM v[28:31],v[216:219],a[164:167],v[28:31]\nbuffer_load_dwordx4 v226,s[36:39],s67 offen offset:2048 lds\n" \
"M v[28:31],v[220:223],a[180:183],v[28:31]\nM v[88:91],v[216:219],a[168:171],v[88:91]\nM v[88:91],v[220:223],a[184:187],v[88:91]\nM v[92:95],v[216:219],a[172:175],v[92:95]\nbuffer_load_dwordx4 v227,s[36:39],s67 offen offset:3072 lds\nM v[92:95],v[220:223],a[188:191],v[92:95]\ns_add_u32 m0,s64,147456\nds_read_b64_tr_b16 v[216:217],v242 offset:17920\n" \
"M v[32:35],v[192:195],a[160:163],v[32:35]\nds_read_b64_tr_b16 v[218:219],v242 offset:18176\nds_read_b64_tr_b16 v[220:221],v242 offset:26112\nM v[32:35],v[196:199],a[176:179],v[32:35]\ns_cmp_eq_u32 s62,0\ns_cselect_b32 s36,0x8000,0\nM v[36:39],v[192:195],a[164:167],v[36:39]\nds_read_b64_tr_b16 v[222:223],v242 offset:26368\n" \
"s_add_u32 s36,s36,s64\nM v[36:39],v[196:199],a[180:183],v[36:39]\nbuffer_load_dwordx4 v243,s[40:43],s69 offen offset:0 lds\nM v[96:99],v[192:195],a[168:171],v[96:99]\nM v[96:99],v[196:199],a[184:187],v[96:99]\nM v[100:103],v[192:195],a[172:175],v[100:103]\nM v[100:103],v[196:199],a[188:191],v[100:103]\ns_waitcnt lgkmcnt(4)\n" \
"M v[40:43],v[200:203],a[160:163],v[40:43]\nbuffer_load_dwordx4 v244,s[40:43],s69 offen offset:1024 lds\nM v[40:43],v[204:207],a[176:179],v[40:43]\nM v[44:47],v[200:203],a[164:167],v[44:47]\nM v[44:47],v[204:207],a[180:183],v[44:47]\nM v[104:107],v[200:203],a[168:171],v[104:107]\nbuffer_load_dwordx4 v245,s[40:43],s69 offen offset:2048 lds\nM v[104:107],v[204:207],a[184:187],v[104:107]\n" \
"M v[108:111],v[200:203],a[172:175],v[108:111]\nM v[108:111],v[204:207],a[188:191],v[108:111]\nM v[48:51],v[208:211],a[160:163],v[48:51]\nbuffer_load_dwordx4 v246,s[40:43],s69 offen offset:3072 lds\nM v[48:51],v[212:215],a[176:179],v[48:51]\ns_sub_u32 s40,s16,0x1000\ns_subb_u32 s41,s17,0\nM v[52:55],v[208:211],a[164:167],v[52:55]\n" \
"s_add_u32 m0,s64,98304\ns_add_u32 s69,s68,0\nM v[52:55],v[212:215],a[180:183],v[52:55]\ns_mov_b32 s16,0\ns_mov_b32 s17,3\nM v[112:115],v[208:211],a[168:171],v[112:115]\ns_waitcnt vmcnt(18) lgkmcnt(0)\ns_barrier\n" \
"M v[112:115],v[212:215],a[184:187],v[112:115]\nds_read_b128 a[192:195],v228 offset:0\nds_read_b64_tr_b16 v[192:193],v241 offset:32768\nds_read_b64_tr_b16 v[194:195],v241 offset:33024\nM v[116:119],v[208:211],a[172:175],v[116:119]\nglobal_load_dword v247,v253,s[12:13]\nds_read_b128 a[208:211],v229 offset:0\nds_read_b64_tr_b16 v[196:197],v241 offset:40960\n" \
"ds_read_b64_tr_b16 v[198:199],v241 offset:41216\nM v[116:119],v[212:215],a[188:191],v[116:119]\nds_read_b128 a[196:199],v228 offset:2048\nv_add_u32 v253,0x100000,v253\nM v[56:59],v[216:219],a[160:163],v[56:59]\nds_read_b128 a[212:215],v229 offset:2048\nM v[56:59],v[220:223],a[176:179],v[56:59]\nds_read_b128 a[200:203],v228 offset:4096\n" \
"M v[60:63],v[216:219],a[164:167],v[60:63]\nds_read_b128 a[216:219],v229 offset:4096\nM v[60:63],v[220:223],a[180:183],v[60:63]\nds_read_b128 a[204:207],v228 offset:6144\nM v[120:123],v[216:219],a[168:171],v[120:123]\nds_read_b128 a[220:223],v229 offset:6144\nM v[120:123],v[220:223],a[184:187],v[120:123]\nds_read_b64_tr_b16 v[200:201],v242 offset:32768\n" \
"ds_read_b64_tr_b16 v[202:203],v242 offset:33024\nM v[124:127],v[216:219],a[172:175],v[124:127]\nds_read_b64_tr_b16 v[204:205],v242 offset:40960\nds_read_b64_tr_b16 v[206:207],v242 offset:41216\nM v[124:127],v[220:223],a[188:191],v[124:127]\ns_waitcnt vmcnt(14)\nv_add_f32 v152,v152,v156\ns_waitcnt lgkmcnt(13)\n" \
"M v[0:3],v[192:195],a[192:195],v[0:3]\nds_read_b64_tr_b16 v[208:209],v241 offset:33280\ns_waitcnt lgkmcnt(11)\nM v[0:3],v[196:199],a[208:211],v[0:3]\nds_read_b64_tr_b16 v[210:211],v241 offset:33536\ns_waitcnt lgkmcnt(11)\nM v[4:7],v[192:195],a[196:199],v[4:7]\nds_read_b64_tr_b16 v[212:213],v241 offset:41472\n" \
"s_waitcnt lgkmcnt(11)\nM v[4:7],v[196:199],a[212:215],v[4:7]\nds_read_b64_tr_b16 v[214:215],v241 offset:41728\ns_waitcnt lgkmcnt(11)\nM v[64:67],v[192:195],a[200:203],v[64:67]\nds_read_b64_tr_b16 v[216:217],v242 offset:33280\ns_waitcnt lgkmcnt(11)\nM v[64:67],v[196:199],a[216:219],v[64:67]\n" \
"ds_read_b64_tr_b16 v[218:219],v242 offset:33536\ns_waitcnt lgkmcnt(11)\nM v[68:71],v[192:195],a[204:207],v[68:71]\nds_read_b64_tr_b16 v[220:221],v242 offset:41472\ns_waitcnt lgkmcnt(11)\nM v[68:71],v[196:199],a[220:223],v[68:71]\nds_read_b64_tr_b16 v[222:223],v242 offset:41728\ns_waitcnt lgkmcnt(10)\n" \
"M v[8:11],v[200:203],a[192:195],v[8:11]\nds_read_b64_tr_b16 v[192:193],v241 offset:33792\ns_waitcnt lgkmcnt(9)\nM v[8:11],v[204:207],a[208:211],v[8:11]\nds_read_b64_tr_b16 v[194:195],v241 offset:34048\nds_read_b64_tr_b16 v[196:197],v241 offset:41984\nM v[12:15],v[200:203],a[196:199],v[12:15]\nds_read_b64_tr_b16 v[198:199],v241 offset:42240\n" \
"s_waitcnt vmcnt(11)\nv_add_f32 v160,v160,v164\nM v[12:15],v[204:207],a[212:215],v[12:15]\nv_add_f32 v153,v153,v157\nv_add_f32 v161,v161,v165\nM v[72:75],v[200:203],a[200:203],v[72:75]\nv_add_f32 v154,v154,v158\nv_add_f32 v162,v162,v166\n" \
"M v[72:75],v[204:207],a[216:219],v[72:75]\nv_add_f32 v155,v155,v159\nv_add_f32 v163,v163,v167\nM v[76:79],v[200:203],a[204:207],v[76:79]\nds_read_b64_tr_b16 v[200:201],v242 offset:33792\nds_read_b64_tr_b16 v[202:203],v242 offset:34048\nM v[76:79],v[204:207],a[220:223],v[76:79]\nds_read_b64_tr_b16 v[204:205],v242 offset:41984\n" \
"s_waitcnt lgkmcnt(9)\nM v[16:19],v[208:211],a[192:195],v[16:19]\nds_read_b64_tr_b16 v[206:207],v242 offset:42240\nv_add_f32 v152,v152,v160\nM v[16:19],v[212:215],a[208:211],v[16:19]\nbuffer_load_dword v168,v169,s[52:55],s71 offen sc1\nM v[20:23],v[208:211],a[196:199],v[20:23]\nv_add_f32 v153,v153,v161\n" \
"v_add_f32 v154,v154,v162\nM v[20:23],v[212:215],a[212:215],v[20:23]\nv_add_f32 v155,v155,v163\nv_cvt_pk_bf16_f32 v152,0,v152\nM v[80:83],v[208:211],a[200:203],v[80:83]\nv_cvt_pk_bf16_f32 v153,0,v153\nv_cvt_pk_bf16_f32 v154,0,v154\nM v[80:83],v[212:215],a[216:219],v[80:83]\n" \
"v_cvt_pk_bf16_f32 v155,0,v155\nv_add_f32 v152,s70,v152\nM v[84:87],v[208:211],a[204:207],v[84:87]\nds_read_b64_tr_b16 v[208:209],v241 offset:34304\nds_read_b64_tr_b16 v[210:211],v241 offset:34560\nM v[84:87],v[212:215],a[220:223],v[84:87]\nds_read_b64_tr_b16 v[212:213],v241 offset:42496\nds_read_b64_tr_b16 v[214:215],v241 offset:42752\n" \
"M v[24:27],v[216:219],a[192:195],v[24:27]\nv_add_f32 v153,s70,v153\ns_waitcnt lgkmcnt(7)\nM v[24:27],v[220:223],a[208:211],v[24:27]\nv_add_f32 v154,s70,v154\nv_add_f32 v155,s70,v155\nM v[28:31],v[216:219],a[196:199],v[28:31]\nglobal_load_dword v247,v253,s[12:13]\n" \
"M v[28:31],v[220:223],a[212:215],v[28:31]\ns_lshr_b32 s71,s64,2\nv_cvt_pk_bf16_f32 v152,0,v152\nM v[88:91],v[216:219],a[200:203],v[88:91]\nv_cvt_pk_bf16_f32 v153,0,v153\nv_cvt_pk_bf16_f32 v154,0,v154\nM v[88:91],v[220:223],a[216:219],v[88:91]\nv_cvt_pk_bf16_f32 v155,0,v155\n" \
"v_add_u32 v171,s71,v240\nM v[92:95],v[216:219],a[204:207],v[92:95]\nds_write_b128 v171,v[152:155] offset:16384\nM v[92:95],v[220:223],a[220:223],v[92:95]\nds_read_b64_tr_b16 v[216:217],v242 offset:34304\nds_read_b64_tr_b16 v[218:219],v242 offset:34560\nM v[32:35],v[192:195],a[192:195],v[32:35]\nds_read_b64_tr_b16 v[220:221],v242 offset:42496\n" \
"ds_read_b64_tr_b16 v[222:223],v242 offset:42752\nM v[32:35],v[196:199],a[208:211],v[32:35]\nv_add_u32 v253,0x100000,v253\ns_lshl_b32 s70,s7,8\nM v[36:39],v[192:195],a[196:199],v[36:39]\nbuffer_load_dwordx4 v243,s[40:43],s69 offen offset:0 lds\nM v[36:39],v[196:199],a[212:215],v[36:39]\nv_lshl_add_u32 v251,v251,5,s70\n" \
"s_lshl_b32 s71,s6,5\nM v[96:99],v[192:195],a[200:203],v[96:99]\ns_and_b32 s71,s71,0xfffffe00\ns_xor_b32 s70,s63,16\nM v[96:99],v[196:199],a[216:219],v[96:99]\ns_lshr_b32 s70,s70,2\ns_lshl3_add_u32 s70,s62,s70\nM v[100:103],v[192:195],a[204:207],v[100:103]\n" \
"buffer_load_dwordx4 v244,s[40:43],s69 offen offset:1024 lds\nM v[100:103],v[196:199],a[220:223],v[100:103]\ns_lshl_b32 s70,s70,15\ns_waitcnt lgkmcnt(5)\nM v[40:43],v[200:203],a[192:195],v[40:43]\ns_add_u32 s70,s70,s64\nM v[40:43],v[204:207],a[208:211],v[40:43]\nM v[44:47],v[200:203],a[196:199],v[44:47]\n" \
"buffer_load_dwordx4 v245,s[40:43],s69 offen offset:2048 lds\nM v[44:47],v[204:207],a[212:215],v[44:47]\nM v[104:107],v[200:203],a[200:203],v[104:107]\nM v[104:107],v[204:207],a[216:219],v[104:107]\nM v[108:111],v[200:203],a[204:207],v[108:111]\nbuffer_load_dwordx4 v246,s[40:43],s69 offen offset:3072 lds\nM v[108:111],v[204:207],a[220:223],v[108:111]\ns_add_u32 m0,s64,114688\n" \
"s_add_u32 s69,s68,524288\nM v[48:51],v[208:211],a[192:195],v[48:51]\nM v[48:51],v[212:215],a[208:211],v[48:51]\nM v[52:55],v[208:211],a[196:199],v[52:55]\nbuffer_store_dwordx2 v[232:233],off,s[52:55],s72 sc1\nM v[52:55],v[212:215],a[212:215],v[52:55]\ns_and_b32 s72,s6,1\ns_lshl_b32 s72,s72,8\n" \
"M v[112:115],v[208:211],a[200:203],v[112:115]\ns_waitcnt vmcnt(8) lgkmcnt(0)\ns_barrier\nM v[112:115],v[212:215],a[216:219],v[112:115]\nds_read_b128 a[224:227],v228 offset:8192\nds_read_b64_tr_b16 v[192:193],v241 offset:49152\nds_read_b64_tr_b16 v[194:195],v241 offset:49408\nM v[116:119],v[208:211],a[204:207],v[116:119]\n" \
"ds_read_b128 a[240:243],v229 offset:8192\nds_read_b64_tr_b16 v[196:197],v241 offset:57344\nds_read_b64_tr_b16 v[198:199],v241 offset:57600\nM v[116:119],v[212:215],a[220:223],v[116:119]\nds_read_b128 a[228:231],v228 offset:10240\ns_add_u32 s71,s71,s72\nM v[56:59],v[216:219],a[192:195],v[56:59]\nds_read_b128 a[244:247],v229 offset:10240\n" \
"s_and_b32 s72,s6,14\nM v[56:59],v[220:223],a[208:211],v[56:59]\nds_read_b128 a[232:235],v228 offset:12288\ns_lshl1_add_u32 s71,s72,s71\nM v[60:63],v[216:219],a[196:199],v[60:63]\nds_read_b128 a[248:251],v229 offset:12288\ns_or_b32 s71,s71,s75\nM v[60:63],v[220:223],a[212:215],v[60:63]\n" \
"ds_read_b128 a[236:239],v228 offset:14336\ns_mov_b32 s72,0x3e0293ee\nM v[120:123],v[216:219],a[200:203],v[120:123]\nds_read_b128 a[252:255],v229 offset:14336\nv_and_b32 v228,15,v249\nM v[120:123],v[220:223],a[216:219],v[120:123]\nds_read_b64_tr_b16 v[200:201],v242 offset:49152\nds_read_b64_tr_b16 v[202:203],v242 offset:49408\n" \
"M v[124:127],v[216:219],a[204:207],v[124:127]\nds_read_b64_tr_b16 v[204:205],v242 offset:57344\nds_read_b64_tr_b16 v[206:207],v242 offset:57600\nM v[124:127],v[220:223],a[220:223],v[124:127]\nv_and_b32 v229,16,v249\ns_waitcnt lgkmcnt(13)\nM v[0:3],v[192:195],a[224:227],v[0:3]\nds_read_b64_tr_b16 v[208:209],v241 offset:49664\n" \
"s_waitcnt lgkmcnt(11)\nM v[0:3],v[196:199],a[240:243],v[0:3]\nds_read_b64_tr_b16 v[210:211],v241 offset:49920\ns_waitcnt lgkmcnt(11)\nM v[4:7],v[192:195],a[228:231],v[4:7]\nds_read_b64_tr_b16 v[212:213],v241 offset:57856\ns_waitcnt lgkmcnt(11)\nM v[4:7],v[196:199],a[244:247],v[4:7]\n" \
"ds_read_b64_tr_b16 v[214:215],v241 offset:58112\ns_waitcnt lgkmcnt(11)\nM v[64:67],v[192:195],a[232:235],v[64:67]\nds_read_b64_tr_b16 v[216:217],v242 offset:49664\ns_waitcnt lgkmcnt(11)\nM v[64:67],v[196:199],a[248:251],v[64:67]\nds_read_b64_tr_b16 v[218:219],v242 offset:49920\ns_waitcnt lgkmcnt(11)\n" \
"M v[68:71],v[192:195],a[236:239],v[68:71]\nglobal_load_dword v247,v253,s[12:13]\ns_waitcnt lgkmcnt(10)\nM v[68:71],v[196:199],a[252:255],v[68:71]\nds_read_b64_tr_b16 v[220:221],v242 offset:57856\ns_waitcnt lgkmcnt(9)\nM v[8:11],v[200:203],a[224:227],v[8:11]\nds_read_b64_tr_b16 v[222:223],v242 offset:58112\n" \
"s_waitcnt lgkmcnt(8)\nM v[8:11],v[204:207],a[240:243],v[8:11]\nds_read_b64_tr_b16 v[192:193],v241 offset:50176\nds_read_b64_tr_b16 v[194:195],v241 offset:50432\nM v[12:15],v[200:203],a[228:231],v[12:15]\nds_read_b64_tr_b16 v[196:197],v241 offset:58368\nds_read_b64_tr_b16 v[198:199],v241 offset:58624\nM v[12:15],v[204:207],a[244:247],v[12:15]\n" \
"ds_read_b128 v[176:179],v251 offset:19456\nds_read_b128 v[160:163],v251 offset:17408\nM v[72:75],v[200:203],a[232:235],v[72:75]\nds_read_b128 v[172:175],v251 offset:18448\nv_cvt_pk_bf16_f32 v128,v0,v1\nM v[72:75],v[204:207],a[248:251],v[72:75]\nds_read_b128 v[152:155],v251 offset:16384\nv_cvt_pk_bf16_f32 v129,v2,v3\n" \
"M v[76:79],v[200:203],a[236:239],v[76:79]\nds_read_b64_tr_b16 v[200:201],v242 offset:50176\nv_cvt_pk_bf16_f32 v130,v8,v9\nM v[76:79],v[204:207],a[252:255],v[76:79]\nds_read_b64_tr_b16 v[202:203],v242 offset:50432\ns_waitcnt lgkmcnt(12)\nM v[16:19],v[208:211],a[224:227],v[16:19]\nds_read_b64_tr_b16 v[204:205],v242 offset:58368\n" \
"ds_read_b64_tr_b16 v[206:207],v242 offset:58624\nM v[16:19],v[212:215],a[240:243],v[16:19]\nds_read_b128 v[156:159],v251 offset:16400\nds_read_b128 v[180:183],v251 offset:19472\nM v[20:23],v[208:211],a[228:231],v[20:23]\nds_read_b128 v[164:167],v251 offset:17424\nv_cvt_pk_bf16_f32 v131,v10,v11\nM v[20:23],v[212:215],a[244:247],v[20:23]\n" \
"buffer_load_dwordx4 v243,s[40:43],s69 offen offset:0 lds\nM v[80:83],v[208:211],a[232:235],v[80:83]\nv_cvt_pk_bf16_f32 v132,v16,v17\nv_cvt_pk_bf16_f32 v133,v18,v19\nM v[80:83],v[212:215],a[248:251],v[80:83]\ns_waitcnt lgkmcnt(10)\nv_mul_f32 v144,v144,v176\nM v[84:87],v[208:211],a[236:239],v[84:87]\n" \
"ds_read_b64_tr_b16 v[208:209],v241 offset:50688\nds_read_b64_tr_b16 v[210:211],v241 offset:50944\nM v[84:87],v[212:215],a[252:255],v[84:87]\nds_read_b64_tr_b16 v[212:213],v241 offset:58880\nds_read_b64_tr_b16 v[214:215],v241 offset:59136\nM v[24:27],v[216:219],a[224:227],v[24:27]\ns_waitcnt vmcnt(8)\nv_add_f32_dpp v168,v168,v168 quad_perm:[1,0,3,2] row_mask:0xf bank_mask:0xf\n" \
"M v[24:27],v[220:223],a[240:243],v[24:27]\nv_mul_f32 v145,v145,v177\nv_add_f32_dpp v168,v168,v168 quad_perm:[2,3,0,1] row_mask:0xf bank_mask:0xf\nM v[28:31],v[216:219],a[228:231],v[28:31]\nv_cvt_pk_bf16_f32 v168,0,v168\nv_cvt_pk_bf16_f32 v144,0,v144\nM v[28:31],v[220:223],a[244:247],v[28:31]\nbuffer_store_dword v168,v170,s[52:55],s71 offen sc1\n" \
"M v[88:91],v[216:219],a[232:235],v[88:91]\nds_read_b128 v[168:171],v251 offset:18432\nv_cvt_pk_bf16_f32 v145,0,v145\nM v[88:91],v[220:223],a[248:251],v[88:91]\ns_waitcnt lgkmcnt(5)\nv_add_f32 v144,v144,v160\nM v[92:95],v[216:219],a[236:239],v[92:95]\nds_read_b64_tr_b16 v[216:217],v242 offset:50688\n" \
"ds_read_b64_tr_b16 v[218:219],v242 offset:50944\nM v[92:95],v[220:223],a[252:255],v[92:95]\nds_read_b64_tr_b16 v[220:221],v242 offset:58880\nds_read_b64_tr_b16 v[222:223],v242 offset:59136\nM v[32:35],v[192:195],a[224:227],v[32:35]\nv_add_f32 v145,v145,v161\nv_cvt_pk_bf16_f32 v144,0,v144\nM v[32:35],v[196:199],a[240:243],v[32:35]\n" \
"v_cvt_pk_bf16_f32 v145,0,v145\nv_mul_f32 v144,s72,v144\nM v[36:39],v[192:195],a[228:231],v[36:39]\nv_mul_f32 v145,s72,v145\nv_cvt_pk_bf16_f32 v236,v144,v145\nM v[36:39],v[196:199],a[244:247],v[36:39]\nv_cvt_pk_bf16_f32 v144,v4,v5\nv_cvt_pk_bf16_f32 v145,v6,v7\n" \
"M v[96:99],v[192:195],a[232:235],v[96:99]\nv_cvt_pk_bf16_f32 v160,v64,v65\nv_cvt_pk_bf16_f32 v161,v66,v67\nM v[96:99],v[196:199],a[248:251],v[96:99]\ns_and_b32 s71,s63,2\nv_mul_f32 v140,v140,v172\nM v[100:103],v[192:195],a[236:239],v[100:103]\ns_waitcnt lgkmcnt(4)\n" \
"v_mul_f32 v136,v136,v168\nM v[100:103],v[196:199],a[252:255],v[100:103]\nv_mul_f32 v137,v137,v169\nv_mul_f32 v138,v138,v170\nM v[40:43],v[200:203],a[224:227],v[40:43]\nv_mul_f32 v139,v139,v171\nv_mul_f32 v141,v141,v173\nM v[40:43],v[204:207],a[240:243],v[40:43]\n" \
"v_mul_f32 v142,v142,v174\nv_mul_f32 v143,v143,v175\nM v[44:47],v[200:203],a[228:231],v[44:47]\ns_lshl_b32 s71,s71,9\nv_cvt_pk_bf16_f32 v176,v68,v69\nM v[44:47],v[204:207],a[244:247],v[44:47]\nv_cvt_pk_bf16_f32 v177,v70,v71\nv_cvt_pk_bf16_f32 v136,0,v136\n" \
"M v[104:107],v[200:203],a[232:235],v[104:107]\nv_cvt_pk_bf16_f32 v137,0,v137\nv_cvt_pk_bf16_f32 v138,0,v138\nM v[104:107],v[204:207],a[248:251],v[104:107]\nv_cvt_pk_bf16_f32 v139,0,v139\nv_cvt_pk_bf16_f32 v140,0,v140\nM v[108:111],v[200:203],a[236:239],v[108:111]\nv_cvt_pk_bf16_f32 v141,0,v141\n" \
"v_cvt_pk_bf16_f32 v142,0,v142\nM v[108:111],v[204:207],a[252:255],v[108:111]\nv_cvt_pk_bf16_f32 v143,0,v143\nv_lshrrev_b32 v251,5,v249\nM v[48:51],v[208:211],a[224:227],v[48:51]\ns_add_u32 s70,s70,s71\nv_add_f32 v136,v136,v152\nM v[48:51],v[212:215],a[240:243],v[48:51]\n" \
"v_add_f32 v137,v137,v153\nv_add_f32 v138,v138,v154\nM v[52:55],v[208:211],a[228:231],v[52:55]\nv_add_f32 v139,v139,v155\nv_add_f32 v140,v140,v156\nM v[52:55],v[212:215],a[244:247],v[52:55]\nv_add_f32 v141,v141,v157\nv_add_f32 v142,v142,v158\n" \
"M v[112:115],v[208:211],a[232:235],v[112:115]\ns_waitcnt vmcnt(4) lgkmcnt(0)\ns_barrier\nM v[112:115],v[212:215],a[248:251],v[112:115]\nds_read_b64_tr_b16 v[192:193],v241 offset:0\nds_read_b64_tr_b16 v[194:195],v241 offset:256\nM v[116:119],v[208:211],a[236:239],v[116:119]\nds_read_b64_tr_b16 v[196:197],v241 offset:8192\n" \
"ds_read_b64_tr_b16 v[198:199],v241 offset:8448\nM v[116:119],v[212:215],a[252:255],v[116:119]\nds_read_b64_tr_b16 v[200:201],v242 offset:0\nds_read_b64_tr_b16 v[202:203],v242 offset:256\nM v[56:59],v[216:219],a[224:227],v[56:59]\nds_read_b64_tr_b16 v[204:205],v242 offset:8192\nds_read_b64_tr_b16 v[206:207],v242 offset:8448\nM v[56:59],v[220:223],a[240:243],v[56:59]\n" \
"ds_read_b64_tr_b16 v[208:209],v241 offset:512\nds_read_b64_tr_b16 v[210:211],v241 offset:768\nM v[60:63],v[216:219],a[228:231],v[60:63]\nv_add_f32 v143,v143,v159\nv_mul_f32 v146,v146,v178\nM v[60:63],v[220:223],a[244:247],v[60:63]\nv_mul_f32 v147,v147,v179\nv_lshlrev_b32 v251,11,v251\n" \
"M v[120:123],v[216:219],a[232:235],v[120:123]\ns_and_b32 s71,s63,1\nds_read_b64_tr_b16 v[212:213],v241 offset:8704\nM v[120:123],v[220:223],a[248:251],v[120:123]\nds_read_b64_tr_b16 v[214:215],v241 offset:8960\nv_cvt_pk_bf16_f32 v136,0,v136\nM v[124:127],v[216:219],a[236:239],v[124:127]\nv_cvt_pk_bf16_f32 v137,0,v137\n" \
"v_cvt_pk_bf16_f32 v138,0,v138\nM v[124:127],v[220:223],a[252:255],v[124:127]\nv_cvt_pk_bf16_f32 v139,0,v139\ns_waitcnt lgkmcnt(10)\nM v[0:3],a[0:3],v[192:195],0\nv_cvt_pk_bf16_f32 v140,0,v140\ns_waitcnt lgkmcnt(8)\nM v[0:3],a[16:19],v[196:199],v[0:3]\n" \
"v_cvt_pk_bf16_f32 v141,0,v141\nv_cvt_pk_bf16_f32 v142,0,v142\nM v[4:7],a[4:7],v[192:195],0\nv_cvt_pk_bf16_f32 v143,0,v143\nv_cvt_pk_bf16_f32 v146,0,v146\nM v[4:7],a[20:23],v[196:199],v[4:7]\nv_cvt_pk_bf16_f32 v147,0,v147\nv_lshl_or_b32 v251,v250,8,v251\n" \
"M v[64:67],a[8:11],v[192:195],0\ns_lshl_b32 s71,s71,7\nv_mul_f32 v136,s72,v136\nM v[64:67],a[24:27],v[196:199],v[64:67]\nv_mul_f32 v137,s72,v137\nv_mul_f32 v138,s72,v138\nM v[68:71],a[12:15],v[192:195],0\nv_mul_f32 v139,s72,v139\n" \
"v_mul_f32 v140,s72,v140\nM v[68:71],a[28:31],v[196:199],v[68:71]\nv_mul_f32 v141,s72,v141\nv_mul_f32 v142,s72,v142\nv_mul_f32 v143,s72,v143\nv_add_f32 v146,v146,v162\nv_add_f32 v147,v147,v163\nv_and_b32 v250,0x70,v240\n" \
"s_add_u32 s70,s70,s71\ns_waitcnt lgkmcnt(0)\nM v[8:11],a[0:3],v[200:203],0\nv_cvt_pk_bf16_f32 v232,v136,v137\nv_cvt_pk_bf16_f32 v233,v138,v139\nv_cvt_pk_bf16_f32 v234,v140,v141\nv_cvt_pk_bf16_f32 v235,v142,v143\nv_cvt_pk_bf16_f32 v146,0,v146\n" \
"v_cvt_pk_bf16_f32 v147,0,v147\nv_or_b32 v251,v251,v250\ns_add_u32 s70,s70,1048576\nv_mul_f32 v146,s72,v146\nv_mul_f32 v147,s72,v147\nbuffer_store_dwordx4 v[232:235],v251,s[52:55],s70 offen sc1\nM v[8:11],a[16:19],v[204:207],v[8:11]\nv_cvt_pk_bf16_f32 v237,v146,v147\n" \
"v_mul_f32 v148,v148,v180\nv_mul_f32 v149,v149,v181\nv_mul_f32 v150,v150,v182\nv_mul_f32 v151,v151,v183\nv_cvt_pk_bf16_f32 v146,v12,v13\nv_cvt_pk_bf16_f32 v147,v14,v15\nM v[12:15],a[4:7],v[200:203],0\nv_cvt_pk_bf16_f32 v148,0,v148\n" \
"v_cvt_pk_bf16_f32 v149,0,v149\nv_cvt_pk_bf16_f32 v150,0,v150\nv_cvt_pk_bf16_f32 v151,0,v151\nv_add_f32 v148,v148,v164\nv_add_f32 v149,v149,v165\nv_add_f32 v150,v150,v166\nv_add_f32 v151,v151,v167\nM v[12:15],a[20:23],v[204:207],v[12:15]\n" \
"v_cvt_pk_bf16_f32 v148,0,v148\nv_cvt_pk_bf16_f32 v149,0,v149\nv_cvt_pk_bf16_f32 v150,0,v150\nv_cvt_pk_bf16_f32 v151,0,v151\nv_mul_f32 v148,s72,v148\nv_mul_f32 v149,s72,v149\nv_mul_f32 v150,s72,v150\nv_mul_f32 v151,s72,v151\n" \
"v_cvt_pk_bf16_f32 v162,v72,v73\nv_cvt_pk_bf16_f32 v163,v74,v75\nM v[72:75],a[8:11],v[200:203],0\nv_cvt_pk_bf16_f32 v238,v148,v149\nv_cvt_pk_bf16_f32 v239,v150,v151\ns_add_u32 s71,s70,16384\nbuffer_load_dwordx4 v244,s[40:43],s69 offen offset:1024 lds\nbuffer_load_dwordx4 v245,s[40:43],s69 offen offset:2048 lds\n" \
"buffer_load_dwordx4 v246,s[40:43],s69 offen offset:3072 lds\nbuffer_store_dwordx4 v[236:239],v251,s[52:55],s71 offen sc1\nM v[72:75],a[24:27],v[204:207],v[72:75]\nv_cvt_pk_bf16_f32 v178,v76,v77\nv_cvt_pk_bf16_f32 v179,v78,v79\nds_read_b64_tr_b16 v[216:217],v242 offset:512\nds_read_b64_tr_b16 v[218:219],v242 offset:768\nM v[76:79],a[12:15],v[200:203],0\n" \
"ds_read_b64_tr_b16 v[220:221],v242 offset:8704\nds_read_b64_tr_b16 v[222:223],v242 offset:8960\nM v[76:79],a[28:31],v[204:207],v[76:79]\nv_cvt_pk_bf16_f32 v148,v20,v21\nv_cvt_pk_bf16_f32 v149,v22,v23\nM v[16:19],a[0:3],v[208:211],0\nv_cvt_pk_bf16_f32 v164,v80,v81\nv_cvt_pk_bf16_f32 v165,v82,v83\n" \
"M v[16:19],a[16:19],v[212:215],v[16:19]\nv_cvt_pk_bf16_f32 v180,v84,v85\nv_cvt_pk_bf16_f32 v181,v86,v87\nM v[20:23],a[4:7],v[208:211],0\nds_read_b64_tr_b16 v[192:193],v241 offset:1024\nds_read_b64_tr_b16 v[194:195],v241 offset:1280\nM v[20:23],a[20:23],v[212:215],v[20:23]\nds_read_b64_tr_b16 v[196:197],v241 offset:9216\n" \
"ds_read_b64_tr_b16 v[198:199],v241 offset:9472\nM v[80:83],a[8:11],v[208:211],0\nv_cvt_pk_bf16_f32 v134,v24,v25\nv_cvt_pk_bf16_f32 v135,v26,v27\nM v[80:83],a[24:27],v[212:215],v[80:83]\nv_cvt_pk_bf16_f32 v150,v28,v29\nv_cvt_pk_bf16_f32 v151,v30,v31\nM v[84:87],a[12:15],v[208:211],0\n" \
"v_cvt_pk_bf16_f32 v166,v88,v89\nv_cvt_pk_bf16_f32 v167,v90,v91\nM v[84:87],a[28:31],v[212:215],v[84:87]\nv_cvt_pk_bf16_f32 v182,v92,v93\ns_waitcnt lgkmcnt(6)\nM v[24:27],a[0:3],v[216:219],0\nv_cvt_pk_bf16_f32 v183,v94,v95\ns_waitcnt lgkmcnt(4)\n" \
"M v[24:27],a[16:19],v[220:223],v[24:27]\nds_read_b64_tr_b16 v[200:201],v242 offset:1024\nds_read_b64_tr_b16 v[202:203],v242 offset:1280\nM v[28:31],a[4:7],v[216:219],0\nds_read_b64_tr_b16 v[204:205],v242 offset:9216\nds_read_b64_tr_b16 v[206:207],v242 offset:9472\nM v[28:31],a[20:23],v[220:223],v[28:31]\nv_cvt_pk_bf16_f32 v136,v32,v33\n" \
"v_cvt_pk_bf16_f32 v137,v34,v35\nM v[88:91],a[8:11],v[216:219],0\nv_cvt_pk_bf16_f32 v152,v36,v37\nv_cvt_pk_bf16_f32 v153,v38,v39\nM v[88:91],a[24:27],v[220:223],v[88:91]\nv_cvt_pk_bf16_f32 v168,v96,v97\nv_cvt_pk_bf16_f32 v169,v98,v99\nM v[92:95],a[12:15],v[216:219],0\n" \
"s_add_u32 m0,s64,131072\ns_add_u32 s69,s68,1048576\nM v[92:95],a[28:31],v[220:223],v[92:95]\nv_cvt_pk_bf16_f32 v184,v100,v101\ns_waitcnt lgkmcnt(4)\nM v[32:35],a[0:3],v[192:195],0\nv_cvt_pk_bf16_f32 v185,v102,v103\nds_read_b64_tr_b16 v[208:209],v241 offset:1536\n" \
"M v[32:35],a[16:19],v[196:199],v[32:35]\nds_read_b64_tr_b16 v[210:211],v241 offset:1792\nds_read_b64_tr_b16 v[212:213],v241 offset:9728\nM v[36:39],a[4:7],v[192:195],0\nds_read_b64_tr_b16 v[214:215],v241 offset:9984\nv_cvt_pk_bf16_f32 v138,v40,v41\nM v[36:39],a[20:23],v[196:199],v[36:39]\nv_cvt_pk_bf16_f32 v139,v42,v43\n" \
"v_cvt_pk_bf16_f32 v154,v44,v45\nM v[96:99],a[8:11],v[192:195],0\nv_cvt_pk_bf16_f32 v155,v46,v47\nds_read_b64_tr_b16 v[216:217],v242 offset:1536\nM v[96:99],a[24:27],v[196:199],v[96:99]\nds_read_b64_tr_b16 v[218:219],v242 offset:1792\nds_read_b64_tr_b16 v[220:221],v242 offset:9728\nM v[100:103],a[12:15],v[192:195],0\n" \
"ds_read_b64_tr_b16 v[222:223],v242 offset:9984\nv_cvt_pk_bf16_f32 v170,v104,v105\nM v[100:103],a[28:31],v[196:199],v[100:103]\nv_cvt_pk_bf16_f32 v171,v106,v107\ns_waitcnt lgkmcnt(8)\nM v[40:43],a[0:3],v[200:203],0\nv_cvt_pk_bf16_f32 v186,v108,v109\nv_cvt_pk_bf16_f32 v187,v110,v111\n" \
"M v[40:43],a[16:19],v[204:207],v[40:43]\nv_cvt_pk_bf16_f32 v140,v48,v49\nv_cvt_pk_bf16_f32 v141,v50,v51\nM v[44:47],a[4:7],v[200:203],0\nv_cvt_pk_bf16_f32 v156,v52,v53\nv_cvt_pk_bf16_f32 v157,v54,v55\nM v[44:47],a[20:23],v[204:207],v[44:47]\nv_cvt_pk_bf16_f32 v172,v112,v113\n" \
"v_cvt_pk_bf16_f32 v173,v114,v115\nM v[104:107],a[8:11],v[200:203],0\nv_cvt_pk_bf16_f32 v188,v116,v117\nv_cvt_pk_bf16_f32 v189,v118,v119\nM v[104:107],a[24:27],v[204:207],v[104:107]\nv_cvt_pk_bf16_f32 v142,v56,v57\nv_cvt_pk_bf16_f32 v143,v58,v59\nM v[108:111],a[12:15],v[200:203],0\n" \
"v_cvt_pk_bf16_f32 v158,v60,v61\nv_cvt_pk_bf16_f32 v159,v62,v63\nM v[108:111],a[28:31],v[204:207],v[108:111]\nv_cvt_pk_bf16_f32 v174,v120,v121\ns_waitcnt lgkmcnt(2)\nM v[48:51],a[0:3],v[208:211],0\nv_cvt_pk_bf16_f32 v175,v122,v123\nv_cvt_pk_bf16_f32 v190,v124,v125\n" \
"M v[48:51],a[16:19],v[212:215],v[48:51]\nv_cvt_pk_bf16_f32 v191,v126,v127\ns_lshl_b32 s70,s62,5\nM v[52:55],a[4:7],v[208:211],0\ns_add_u32 s70,s70,s63\ns_lshl2_add_u32 s70,s70,s7\nM v[52:55],a[20:23],v[212:215],v[52:55]\ns_lshl2_add_u32 s79,s70,90112\n" \
"s_lshl_b32 s70,s60,4\nM v[112:115],a[8:11],v[208:211],0\ns_waitcnt vmcnt(1) lgkmcnt(0)\ns_barrier\nM v[112:115],a[24:27],v[212:215],v[112:115]\nds_read_b64_tr_b16 v[192:193],v241 offset:16384\nds_read_b64_tr_b16 v[194:195],v241 offset:16640\nM v[116:119],a[12:15],v[208:211],0\n" \
"ds_read_b64_tr_b16 v[196:197],v241 offset:24576\nds_read_b64_tr_b16 v[198:199],v241 offset:24832\nM v[116:119],a[28:31],v[212:215],v[116:119]\nds_read_b64_tr_b16 v[200:201],v242 offset:16384\nds_read_b64_tr_b16 v[202:203],v242 offset:16640\nM v[56:59],a[0:3],v[216:219],0\nds_read_b64_tr_b16 v[204:205],v242 offset:24576\nds_read_b64_tr_b16 v[206:207],v242 offset:24832\n" \
"M v[56:59],a[16:19],v[220:223],v[56:59]\nds_read_b64_tr_b16 v[208:209],v241 offset:16896\nds_read_b64_tr_b16 v[210:211],v241 offset:17152\nM v[60:63],a[4:7],v[216:219],0\nds_read_b64_tr_b16 v[212:213],v241 offset:25088\nds_read_b64_tr_b16 v[214:215],v241 offset:25344\nM v[60:63],a[20:23],v[220:223],v[60:63]\nbuffer_load_dwordx4 v243,s[40:43],s69 offen offset:0 lds\n" \
"M v[120:123],a[8:11],v[216:219],0\ns_sub_u32 s70,s70,s62\ns_add_u32 s70,s70,15\nM v[120:123],a[24:27],v[220:223],v[120:123]\ns_lshl2_add_u32 s70,s70,s7\ns_lshl_b32 s70,s70,15\nM v[124:127],a[12:15],v[216:219],0\nds_read_b64_tr_b16 v[216:217],v242 offset:16896\n" \
"ds_read_b64_tr_b16 v[218:219],v242 offset:17152\nM v[124:127],a[28:31],v[220:223],v[124:127]\ns_lshl_b32 s71,s61,8\ns_waitcnt lgkmcnt(12)\nM v[0:3],a[32:35],v[192:195],v[0:3]\nds_read_b64_tr_b16 v[220:221],v242 offset:25088\ns_waitcnt lgkmcnt(11)\nM v[0:3],a[48:51],v[196:199],v[0:3]\n" \
"buffer_load_dwordx4 v244,s[40:43],s69 offen offset:1024 lds\nM v[4:7],a[36:39],v[192:195],v[4:7]\nds_read_b64_tr_b16 v[222:223],v242 offset:25344\ns_add_u32 s70,s70,s71\nM v[4:7],a[52:55],v[196:199],v[4:7]\ns_add_u32 s78,s70,54525952\ns_lshl3_add_u32 s70,s62,s7\nM v[64:67],a[40:43],v[192:195],v[64:67]\n" \
"s_lshl_b32 s76,s70,15\ns_lshl_b32 s80,s70,6\nM v[64:67],a[56:59],v[196:199],v[64:67]\nbuffer_load_dwordx4 v245,s[40:43],s69 offen offset:2048 lds\nM v[68:71],a[44:47],v[192:195],v[68:71]\ns_add_u32 s76,s76,1048576\ns_add_u32 s77,s76,131072\nM v[68:71],a[60:63],v[196:199],v[68:71]\n" \
"s_lshl_b32 s70,s94,12\ns_waitcnt lgkmcnt(4)\nM v[8:11],a[32:35],v[200:203],v[8:11]\ns_or_b32 s76,s76,s70\ns_or_b32 s77,s77,s70\nM v[8:11],a[48:51],v[204:207],v[8:11]\nbuffer_load_dwordx4 v246,s[40:43],s69 offen offset:3072 lds\nM v[12:15],a[36:39],v[200:203],v[12:15]\n" \
"s_lshl_b32 s70,s63,5\ns_add_u32 s70,s70,s74\nM v[12:15],a[52:55],v[204:207],v[12:15]\ns_lshl_b32 s74,s70,14\ns_add_u32 s74,s74,3145728\nM v[72:75],a[40:43],v[200:203],v[72:75]\nds_read_b64_tr_b16 v[192:193],v241 offset:17408\nds_read_b64_tr_b16 v[194:195],v241 offset:17664\n" \
"M v[72:75],a[56:59],v[204:207],v[72:75]\nds_read_b64_tr_b16 v[196:197],v241 offset:25600\nds_read_b64_tr_b16 v[198:199],v241 offset:25856\nM v[76:79],a[44:47],v[200:203],v[76:79]\nds_read_b64_tr_b16 v[200:201],v242 offset:17408\nds_read_b64_tr_b16 v[202:203],v242 offset:17664\nM v[76:79],a[60:63],v[204:207],v[76:79]\nds_read_b64_tr_b16 v[204:205],v242 offset:25600\n" \
"ds_read_b64_tr_b16 v[206:207],v242 offset:25856\nM v[16:19],a[32:35],v[208:211],v[16:19]\nbuffer_store_dwordx4 v[128:131],v240,s[52:55],s74 offen offset:0 sc1\nM v[16:19],a[48:51],v[212:215],v[16:19]\ns_add_u32 m0,s64,147456\ns_add_u32 s69,s68,1572864\nM v[20:23],a[36:39],v[208:211],v[20:23]\nv_lshlrev_b32 v228,2,v228\n" \
"v_lshl_or_b32 v228,v229,4,v228\nM v[20:23],a[52:55],v[212:215],v[20:23]\ns_add_u32 s80,s80,90112\ns_lshl2_add_u32 s29,s70,81920\nM v[80:83],a[40:43],v[208:211],v[80:83]\nbuffer_load_dwordx4 v243,s[40:43],s69 offen offset:0 lds\nM v[80:83],a[56:59],v[212:215],v[80:83]\ns_lshl_b32 s70,s7,14\n" \
"v_add_u32 v250,s70,v240\nM v[84:87],a[44:47],v[208:211],v[84:87]\nds_read_b64_tr_b16 v[208:209],v241 offset:17920\nds_read_b64_tr_b16 v[210:211],v241 offset:18176\nM v[84:87],a[60:63],v[212:215],v[84:87]\nds_read_b64_tr_b16 v[212:213],v241 offset:26112\ns_waitcnt lgkmcnt(8)\nM v[24:27],a[32:35],v[216:219],v[24:27]\n" \
"ds_read_b64_tr_b16 v[214:215],v241 offset:26368\ns_lshl_b32 s70,s63,19\nM v[24:27],a[48:51],v[220:223],v[24:27]\nbuffer_load_dwordx4 v244,s[40:43],s69 offen offset:1024 lds\nM v[28:31],a[36:39],v[216:219],v[28:31]\nds_write_b64 v250,v[128:129] offset:0\ns_add_u32 s72,s78,16384\nM v[28:31],a[52:55],v[220:223],v[28:31]\n" \
"ds_write_b64 v250,v[130:131] offset:8\ns_mul_i32 s71,s60,14\nM v[88:91],a[40:43],v[216:219],v[88:91]\nds_write_b64 v250,v[132:133] offset:1024\nv_and_b32 v229,15,v249\nM v[88:91],a[56:59],v[220:223],v[88:91]\nbuffer_load_dwordx4 v245,s[40:43],s69 offen offset:2048 lds\nM v[92:95],a[44:47],v[216:219],v[92:95]\n" \
"ds_read_b64_tr_b16 v[216:217],v242 offset:17920\nds_read_b64_tr_b16 v[218:219],v242 offset:18176\nM v[92:95],a[60:63],v[220:223],v[92:95]\nds_read_b64_tr_b16 v[220:221],v242 offset:26112\nds_read_b64_tr_b16 v[222:223],v242 offset:26368\nM v[32:35],a[32:35],v[192:195],v[32:35]\nv_lshlrev_b32 v229,7,v229\ns_waitcnt lgkmcnt(10)\n" \
"M v[32:35],a[48:51],v[196:199],v[32:35]\nbuffer_load_dwordx4 v246,s[40:43],s69 offen offset:3072 lds\nM v[36:39],a[36:39],v[192:195],v[36:39]\ns_add_u32 m0,s64,98304\ns_add_u32 s69,s68,2097152\nM v[36:39],a[52:55],v[196:199],v[36:39]\nds_write_b64 v250,v[134:135] offset:1032\nv_add_u32 v253,0xffd00000,v253\n" \
"M v[96:99],a[40:43],v[192:195],v[96:99]\ns_add_u32 s44,s34,s70\ns_addc_u32 s45,s35,0\nM v[96:99],a[56:59],v[196:199],v[96:99]\nbuffer_store_dwordx4 v[132:135],v240,s[52:55],s74 offen offset:1024 sc1\nM v[100:103],a[44:47],v[192:195],v[100:103]\ns_lshl_b32 s70,s62,15\ns_add_u32 s96,s70,s64\n" \
"M v[100:103],a[60:63],v[196:199],v[100:103]\ns_sub_u32 s96,s96,16384\ns_sub_u32 s46,491520,s70\nM v[40:43],a[32:35],v[200:203],v[40:43]\ns_cmp_eq_u32 s36,s96\ns_cselect_b32 s72,0xc000,s98\ns_add_u32 s65,s36,s72\nM v[40:43],a[48:51],v[204:207],v[40:43]\n" \
"buffer_load_dwordx4 v243,s[40:43],s69 offen offset:0 lds\nM v[44:47],a[36:39],v[200:203],v[44:47]\ns_cmp_eq_u32 s65,s96\ns_cselect_b32 s72,0xc000,s98\ns_add_u32 s66,s65,s72\nM v[44:47],a[52:55],v[204:207],v[44:47]\ns_cmp_eq_u32 s66,s96\ns_cselect_b32 s72,0xc000,s98\n" \
"s_add_u32 s95,s66,s72\nM v[104:107],a[40:43],v[200:203],v[104:107]\ns_lshl2_add_u32 s70,s62,0\ns_sub_u32 s70,s70,s7\nM v[104:107],a[56:59],v[204:207],v[104:107]\nbuffer_load_dwordx4 v244,s[40:43],s69 offen offset:1024 lds\nM v[108:111],a[44:47],v[200:203],v[108:111]\nds_write_b64 v250,v[136:137] offset:2048\n" \
"s_lshl_b32 s70,s70,16\nM v[108:111],a[60:63],v[204:207],v[108:111]\ns_add_u32 s33,s78,s70\ns_waitcnt lgkmcnt(1)\nM v[48:51],a[32:35],v[208:211],v[48:51]\ns_cmp_eq_u32 s62,7\ns_cselect_b32 s46,229376,s46\nM v[48:51],a[48:51],v[212:215],v[48:51]\n" \
"s_waitcnt vmcnt(12)\nbuffer_store_dword v252,off,s[52:55],s79 sc1\nM v[52:55],a[36:39],v[208:211],v[52:55]\nbuffer_load_dwordx4 v245,s[40:43],s69 offen offset:2048 lds\nM v[52:55],a[52:55],v[212:215],v[52:55]\ns_cmp_gt_i32 s62,-1\ns_cselect_b32 s72,s98,0xfffe4000\nM v[112:115],a[40:43],v[208:211],v[112:115]\n" \
"s_waitcnt vmcnt(10)\ns_barrier\nM v[112:115],a[56:59],v[212:215],v[112:115]\nds_read_b64_tr_b16 v[192:193],v241 offset:32768\nds_read_b64_tr_b16 v[194:195],v241 offset:33024\nM v[116:119],a[44:47],v[208:211],v[116:119]\nds_read_b64_tr_b16 v[196:197],v241 offset:40960\nds_read_b64_tr_b16 v[198:199],v241 offset:41216\n" \
"M v[116:119],a[60:63],v[212:215],v[116:119]\nbuffer_store_dwordx4 v[136:139],v240,s[52:55],s74 offen offset:2048 sc1\nM v[56:59],a[32:35],v[216:219],v[56:59]\nds_read_b64_tr_b16 v[200:201],v242 offset:32768\nds_read_b64_tr_b16 v[202:203],v242 offset:33024\nM v[56:59],a[48:51],v[220:223],v[56:59]\nds_read_b64_tr_b16 v[204:205],v242 offset:40960\nds_read_b64_tr_b16 v[206:207],v242 offset:41216\n" \
"M v[60:63],a[36:39],v[216:219],v[60:63]\nds_read_b64_tr_b16 v[208:209],v241 offset:33280\nds_read_b64_tr_b16 v[210:211],v241 offset:33536\nM v[60:63],a[52:55],v[220:223],v[60:63]\nds_read_b64_tr_b16 v[212:213],v241 offset:41472\nds_read_b64_tr_b16 v[214:215],v241 offset:41728\nM v[120:123],a[40:43],v[216:219],v[120:123]\nbuffer_load_dwordx4 v246,s[40:43],s69 offen offset:3072 lds\n" \
"M v[120:123],a[56:59],v[220:223],v[120:123]\ns_add_u32 m0,s64,114688\ns_add_u32 s69,s68,2621440\nM v[124:127],a[44:47],v[216:219],v[124:127]\nds_read_b64_tr_b16 v[216:217],v242 offset:33280\nds_read_b64_tr_b16 v[218:219],v242 offset:33536\nM v[124:127],a[60:63],v[220:223],v[124:127]\nds_read_b64_tr_b16 v[220:221],v242 offset:41472\n" \
"s_waitcnt lgkmcnt(13)\nM v[0:3],a[64:67],v[192:195],v[0:3]\nds_read_b64_tr_b16 v[222:223],v242 offset:41728\ns_waitcnt lgkmcnt(12)\nM v[0:3],a[80:83],v[196:199],v[0:3]\nbuffer_load_dwordx4 v243,s[40:43],s69 offen offset:0 lds\nM v[4:7],a[68:71],v[192:195],v[4:7]\nds_write_b64 v250,v[138:139] offset:2056\n" \
"s_add_u32 s15,s64,s46\nM v[4:7],a[84:87],v[196:199],v[4:7]\ns_lshl3_add_u32 s70,s62,s7\ns_add_u32 s15,s15,s72\nM v[64:67],a[72:75],v[192:195],v[64:67]\ns_lshl3_add_u32 s70,s70,s60\ns_lshl_b32 s67,s70,15\nM v[64:67],a[88:91],v[196:199],v[64:67]\n" \
"buffer_load_dwordx4 v244,s[40:43],s69 offen offset:1024 lds\nM v[68:71],a[76:79],v[192:195],v[68:71]\nds_read_b64_tr_b16 v[192:193],v241 offset:33792\nds_read_b64_tr_b16 v[194:195],v241 offset:34048\nM v[68:71],a[92:95],v[196:199],v[68:71]\nds_read_b64_tr_b16 v[196:197],v241 offset:41984\ns_waitcnt lgkmcnt(8)\nM v[8:11],a[64:67],v[200:203],v[8:11]\n" \
"ds_read_b64_tr_b16 v[198:199],v241 offset:42240\ns_add_u32 s97,s96,65536\nM v[8:11],a[80:83],v[204:207],v[8:11]\nbuffer_store_dwordx4 v[140:143],v240,s[52:55],s74 offen offset:3072 sc1\nM v[12:15],a[68:71],v[200:203],v[12:15]\ns_add_u32 s74,s74,4096\ns_lshl3_add_u32 s70,s62,s60\nM v[12:15],a[84:87],v[204:207],v[12:15]\n" \
"ds_write_b64 v250,v[140:141] offset:3072\ns_lshl2_add_u32 s70,s70,s7\nM v[72:75],a[72:75],v[200:203],v[72:75]\ns_cmp_eq_u32 s62,7\ns_cselect_b32 s72,98304,0\nM v[72:75],a[88:91],v[204:207],v[72:75]\nbuffer_load_dwordx4 v245,s[40:43],s69 offen offset:2048 lds\nM v[76:79],a[76:79],v[200:203],v[76:79]\n" \
"ds_read_b64_tr_b16 v[200:201],v242 offset:33792\nds_read_b64_tr_b16 v[202:203],v242 offset:34048\nM v[76:79],a[92:95],v[204:207],v[76:79]\nds_read_b64_tr_b16 v[204:205],v242 offset:41984\nds_read_b64_tr_b16 v[206:207],v242 offset:42240\nM v[16:19],a[64:67],v[208:211],v[16:19]\ns_sub_u32 s97,s97,s72\ns_lshl4_add_u32 s79,s70,65536\n" \
"M v[16:19],a[80:83],v[212:215],v[16:19]\nbuffer_load_dwordx4 v246,s[40:43],s69 offen offset:3072 lds\nM v[20:23],a[68:71],v[208:211],v[20:23]\nds_write_b64 v250,v[142:143] offset:3080\ns_lshr_b32 s70,s6,3\nM v[20:23],a[84:87],v[212:215],v[20:23]\ns_cmp_lt_u32 s62,4\ns_cselect_b32 s72,32768,0\n" \
"M v[80:83],a[72:75],v[208:211],v[80:83]\ns_add_u32 s97,s97,s72\ns_sub_u32 s70,s70,16\nM v[80:83],a[88:91],v[212:215],v[80:83]\ns_add_u32 s2,s70,s71\ns_lshl_b32 s72,s60,1\nM v[84:87],a[76:79],v[208:211],v[84:87]\nds_read_b64_tr_b16 v[208:209],v241 offset:34304\n" \
"ds_read_b64_tr_b16 v[210:211],v241 offset:34560\nM v[84:87],a[92:95],v[212:215],v[84:87]\nds_read_b64_tr_b16 v[212:213],v241 offset:42496\ns_waitcnt lgkmcnt(8)\nM v[24:27],a[64:67],v[216:219],v[24:27]\nbuffer_store_dwordx4 v[144:147],v240,s[52:55],s74 offen offset:0 sc1\nM v[24:27],a[80:83],v[220:223],v[24:27]\nds_read_b64_tr_b16 v[214:215],v241 offset:42752\n" \
"s_mul_i32 s71,s60,448\nM v[28:31],a[68:71],v[216:219],v[28:31]\ns_add_u32 s72,s72,s62\ns_and_b32 s71,s71,0xffffff80\nM v[28:31],a[84:87],v[220:223],v[28:31]\ns_sub_u32 s72,s72,6\ns_sub_u32 s58,1835008,s71\nM v[88:91],a[72:75],v[216:219],v[88:91]\n" \
"s_lshl_b32 s72,s72,18\ns_lshl_b32 s2,s2,5\nM v[88:91],a[88:91],v[220:223],v[88:91]\ns_cmp_ge_u32 s62,6\ns_cselect_b64 s[56:57],s[18:19],s[56:57]\ns_cselect_b32 s71,s72,s71\ns_cselect_b32 s58,0x40000,s58\ns_cselect_b32 s0,2048,s0\n" \
"M v[92:95],a[76:79],v[216:219],v[92:95]\nds_read_b64_tr_b16 v[216:217],v242 offset:34304\nds_read_b64_tr_b16 v[218:219],v242 offset:34560\nM v[92:95],a[92:95],v[220:223],v[92:95]\nds_read_b64_tr_b16 v[220:221],v242 offset:42496\nds_read_b64_tr_b16 v[222:223],v242 offset:42752\nM v[32:35],a[64:67],v[192:195],v[32:35]\nds_write_b64 v250,v[144:145] offset:4096\n" \
"s_mul_i32 s72,s60,229376\nM v[32:35],a[80:83],v[196:199],v[32:35]\ns_cmp_eq_u32 s62,3\ns_cselect_b64 s[56:57],s[26:27],s[56:57]\ns_cselect_b32 s71,s72,s71\ns_cselect_b32 s58,229376,s58\ns_cselect_b32 s0,4096,s0\nM v[36:39],a[68:71],v[192:195],v[36:39]\n" \
"s_cmp_lt_u32 s70,14\ns_cselect_b32 s2,s2,0\ns_cselect_b32 s3,-1,0\nM v[36:39],a[84:87],v[196:199],v[36:39]\ns_lshl2_add_u32 s70,s61,s7\nv_lshrrev_b32 v253,4,v249\nM v[96:99],a[72:75],v[192:195],v[96:99]\nv_and_b32 v251,0xf0,v240\n" \
"s_mul_i32 s1,s70,s0\nM v[96:99],a[88:91],v[196:199],v[96:99]\ns_add_u32 s48,s44,19922944\ns_addc_u32 s49,s45,0\nM v[100:103],a[76:79],v[192:195],v[100:103]\nv_lshl_or_b32 v253,v253,12,v251\ns_lshl_b32 s70,s61,8\nM v[100:103],a[92:95],v[196:199],v[100:103]\n" \
"s_mov_b32 s50,s46\ns_waitcnt lgkmcnt(4)\nM v[40:43],a[64:67],v[200:203],v[40:43]\ns_add_u32 s44,s44,3145728\ns_addc_u32 s45,s45,0\nM v[40:43],a[80:83],v[204:207],v[40:43]\ns_mov_b32 s94,0xc000\ns_sub_u32 s33,s33,1867776\n" \
"M v[44:47],a[68:71],v[200:203],v[44:47]\nbuffer_store_dwordx4 v[148:151],v240,s[52:55],s74 offen offset:1024 sc1\nM v[44:47],a[84:87],v[204:207],v[44:47]\nds_write_b64 v250,v[146:147] offset:4104\ns_add_u32 s67,s67,36700160\nM v[104:107],a[72:75],v[200:203],v[104:107]\ns_cmp_lt_u32 s62,3\ns_cselect_b32 s58,0,s58\n" \
"M v[104:107],a[88:91],v[204:207],v[104:107]\ns_add_u32 s56,s56,s71\ns_addc_u32 s57,s57,0\nM v[108:111],a[76:79],v[200:203],v[108:111]\ns_lshl_b32 s0,s0,4\nM v[108:111],a[92:95],v[204:207],v[108:111]\nM v[48:51],a[64:67],v[208:211],v[48:51]\nM v[48:51],a[80:83],v[212:215],v[48:51]\n" \
"M v[52:55],a[68:71],v[208:211],v[52:55]\ns_add_u32 m0,s64,131072\ns_add_u32 s69,s68,3145728\nM v[52:55],a[84:87],v[212:215],v[52:55]\nM v[112:115],a[72:75],v[208:211],v[112:115]\ns_waitcnt vmcnt(14) lgkmcnt(1)\ns_barrier\nM v[112:115],a[88:91],v[212:215],v[112:115]\n" \
"ds_read_b64_tr_b16 v[192:193],v241 offset:49152\nds_read_b64_tr_b16 v[194:195],v241 offset:49408\nM v[116:119],a[76:79],v[208:211],v[116:119]\nds_read_b64_tr_b16 v[196:197],v241 offset:57344\nds_read_b64_tr_b16 v[198:199],v241 offset:57600\nM v[116:119],a[92:95],v[212:215],v[116:119]\nds_read_b64_tr_b16 v[200:201],v242 offset:49152\nds_read_b64_tr_b16 v[202:203],v242 offset:49408\n" \
"M v[56:59],a[64:67],v[216:219],v[56:59]\nds_read_b64_tr_b16 v[204:205],v242 offset:57344\nds_read_b64_tr_b16 v[206:207],v242 offset:57600\nM v[56:59],a[80:83],v[220:223],v[56:59]\nds_read_b64_tr_b16 v[208:209],v241 offset:49664\nds_read_b64_tr_b16 v[210:211],v241 offset:49920\nM v[60:63],a[68:71],v[216:219],v[60:63]\nds_read_b64_tr_b16 v[212:213],v241 offset:57856\n" \
"ds_read_b64_tr_b16 v[214:215],v241 offset:58112\nM v[60:63],a[84:87],v[220:223],v[60:63]\nbuffer_load_dwordx4 v243,s[40:43],s69 offen offset:0 lds\nM v[120:123],a[72:75],v[216:219],v[120:123]\nds_write_b64 v250,v[148:149] offset:5120\nM v[120:123],a[88:91],v[220:223],v[120:123]\nM v[124:127],a[76:79],v[216:219],v[124:127]\nds_read_b64_tr_b16 v[216:217],v242 offset:49664\n" \
"ds_read_b64_tr_b16 v[218:219],v242 offset:49920\nM v[124:127],a[92:95],v[220:223],v[124:127]\nds_read_b64_tr_b16 v[220:221],v242 offset:57856\ns_waitcnt lgkmcnt(14)\nM v[0:3],a[96:99],v[192:195],v[0:3]\nbuffer_store_dwordx4 v[152:155],v240,s[52:55],s74 offen offset:2048 sc1\ns_waitcnt lgkmcnt(12)\nM v[0:3],a[112:115],v[196:199],v[0:3]\n" \
"ds_read_b64_tr_b16 v[222:223],v242 offset:58112\nM v[4:7],a[100:103],v[192:195],v[4:7]\nds_write_b64 v250,v[150:151] offset:5128\nM v[4:7],a[116:119],v[196:199],v[4:7]\nM v[64:67],a[104:107],v[192:195],v[64:67]\nbuffer_load_dwordx4 v244,s[40:43],s69 offen offset:1024 lds\nM v[64:67],a[120:123],v[196:199],v[64:67]\nM v[68:71],a[108:111],v[192:195],v[68:71]\n" \
"ds_read_b64_tr_b16 v[192:193],v241 offset:50176\nds_read_b64_tr_b16 v[194:195],v241 offset:50432\nM v[68:71],a[124:127],v[196:199],v[68:71]\nds_read_b64_tr_b16 v[196:197],v241 offset:58368\ns_waitcnt lgkmcnt(9)\nM v[8:11],a[96:99],v[200:203],v[8:11]\nbuffer_load_dwordx4 v245,s[40:43],s69 offen offset:2048 lds\nM v[8:11],a[112:115],v[204:207],v[8:11]\n" \
"ds_read_b64_tr_b16 v[198:199],v241 offset:58624\nM v[12:15],a[100:103],v[200:203],v[12:15]\nM v[12:15],a[116:119],v[204:207],v[12:15]\nM v[72:75],a[104:107],v[200:203],v[72:75]\nbuffer_load_dwordx4 v246,s[40:43],s69 offen offset:3072 lds\nM v[72:75],a[120:123],v[204:207],v[72:75]\nM v[76:79],a[108:111],v[200:203],v[76:79]\nds_read_b64_tr_b16 v[200:201],v242 offset:50176\n" \
"ds_read_b64_tr_b16 v[202:203],v242 offset:50432\nM v[76:79],a[124:127],v[204:207],v[76:79]\nds_read_b64_tr_b16 v[204:205],v242 offset:58368\nds_read_b64_tr_b16 v[206:207],v242 offset:58624\nM v[16:19],a[96:99],v[208:211],v[16:19]\nbuffer_store_dwordx4 v[156:159],v240,s[52:55],s74 offen offset:3072 sc1\nM v[16:19],a[112:115],v[212:215],v[16:19]\nds_write_b64 v250,v[152:153] offset:6144\n" \
"s_add_u32 s74,s74,4096\nM v[20:23],a[100:103],v[208:211],v[20:23]\nM v[20:23],a[116:119],v[212:215],v[20:23]\nM v[80:83],a[104:107],v[208:211],v[80:83]\nM v[80:83],a[120:123],v[212:215],v[80:83]\nM v[84:87],a[108:111],v[208:211],v[84:87]\nds_read_b64_tr_b16 v[208:209],v241 offset:50688\nds_read_b64_tr_b16 v[210:211],v241 offset:50944\n" \
"M v[84:87],a[124:127],v[212:215],v[84:87]\nds_read_b64_tr_b16 v[212:213],v241 offset:58880\ns_waitcnt lgkmcnt(8)\nM v[24:27],a[96:99],v[216:219],v[24:27]\nds_read_b64_tr_b16 v[214:215],v241 offset:59136\nM v[24:27],a[112:115],v[220:223],v[24:27]\nM v[28:31],a[100:103],v[216:219],v[28:31]\nds_write_b64 v250,v[154:155] offset:6152\n" \
"M v[28:31],a[116:119],v[220:223],v[28:31]\nM v[88:91],a[104:107],v[216:219],v[88:91]\nM v[88:91],a[120:123],v[220:223],v[88:91]\nM v[92:95],a[108:111],v[216:219],v[92:95]\nds_read_b64_tr_b16 v[216:217],v242 offset:50688\nds_read_b64_tr_b16 v[218:219],v242 offset:50944\nM v[92:95],a[124:127],v[220:223],v[92:95]\nds_read_b64_tr_b16 v[220:221],v242 offset:58880\n" \
"ds_read_b64_tr_b16 v[222:223],v242 offset:59136\nM v[32:35],a[96:99],v[192:195],v[32:35]\nbuffer_store_dwordx4 v[160:163],v240,s[52:55],s74 offen offset:0 sc1\nM v[32:35],a[112:115],v[196:199],v[32:35]\nM v[36:39],a[100:103],v[192:195],v[36:39]\nM v[36:39],a[116:119],v[196:199],v[36:39]\nM v[96:99],a[104:107],v[192:195],v[96:99]\nM v[96:99],a[120:123],v[196:199],v[96:99]\n" \
"M v[100:103],a[108:111],v[192:195],v[100:103]\nds_write_b64 v250,v[156:157] offset:7168\nM v[100:103],a[124:127],v[196:199],v[100:103]\ns_waitcnt lgkmcnt(7)\nM v[40:43],a[96:99],v[200:203],v[40:43]\nM v[40:43],a[112:115],v[204:207],v[40:43]\nM v[44:47],a[100:103],v[200:203],v[44:47]\nM v[44:47],a[116:119],v[204:207],v[44:47]\n" \
"M v[104:107],a[104:107],v[200:203],v[104:107]\nM v[104:107],a[120:123],v[204:207],v[104:107]\nM v[108:111],a[108:111],v[200:203],v[108:111]\nM v[108:111],a[124:127],v[204:207],v[108:111]\nM v[48:51],a[96:99],v[208:211],v[48:51]\ns_waitcnt lgkmcnt(3)\nM v[48:51],a[112:115],v[212:215],v[48:51]\nM v[52:55],a[100:103],v[208:211],v[52:55]\n" \
"buffer_store_dwordx4 v[164:167],v240,s[52:55],s74 offen offset:1024 sc1\nM v[52:55],a[116:119],v[212:215],v[52:55]\ns_add_u32 m0,s64,147456\ns_add_u32 s69,s68,3670016\nM v[112:115],a[104:107],v[208:211],v[112:115]\ns_waitcnt vmcnt(15) lgkmcnt(1)\ns_barrier\nM v[112:115],a[120:123],v[212:215],v[112:115]\n" \
"ds_read_b64_tr_b16 v[192:193],v241 offset:0\nds_read_b64_tr_b16 v[194:195],v241 offset:256\nM v[116:119],a[108:111],v[208:211],v[116:119]\nds_read_b64_tr_b16 v[196:197],v241 offset:8192\nds_read_b64_tr_b16 v[198:199],v241 offset:8448\nM v[116:119],a[124:127],v[212:215],v[116:119]\nds_read_b64_tr_b16 v[200:201],v242 offset:0\nds_read_b64_tr_b16 v[202:203],v242 offset:256\n" \
"M v[56:59],a[96:99],v[216:219],v[56:59]\nds_read_b64_tr_b16 v[204:205],v242 offset:8192\nds_read_b64_tr_b16 v[206:207],v242 offset:8448\nM v[56:59],a[112:115],v[220:223],v[56:59]\nds_read_b64_tr_b16 v[208:209],v241 offset:512\nds_read_b64_tr_b16 v[210:211],v241 offset:768\nM v[60:63],a[100:103],v[216:219],v[60:63]\nds_read_b64_tr_b16 v[212:213],v241 offset:8704\n" \
"ds_read_b64_tr_b16 v[214:215],v241 offset:8960\nM v[60:63],a[116:119],v[220:223],v[60:63]\nbuffer_load_dwordx4 v243,s[40:43],s69 offen offset:0 lds\nM v[120:123],a[104:107],v[216:219],v[120:123]\nds_write_b64 v250,v[158:159] offset:7176\ns_lshl_b32 s68,s60,19\nM v[120:123],a[120:123],v[220:223],v[120:123]\ns_lshl2_add_u32 s68,s64,s68\n" \
"s_add_u32 s68,s68,s70\nM v[124:127],a[108:111],v[216:219],v[124:127]\nds_read_b64_tr_b16 v[216:217],v242 offset:512\nds_read_b64_tr_b16 v[218:219],v242 offset:768\nM v[124:127],a[124:127],v[220:223],v[124:127]\nds_read_b64_tr_b16 v[220:221],v242 offset:8704\ns_waitcnt lgkmcnt(14)\nM v[0:3],a[128:131],v[192:195],v[0:3]\n" \
"ds_read_b64_tr_b16 v[222:223],v242 offset:8960\ns_waitcnt lgkmcnt(13)\nM v[0:3],a[144:147],v[196:199],v[0:3]\nbuffer_load_dwordx4 v244,s[40:43],s69 offen offset:1024 lds\nM v[4:7],a[132:135],v[192:195],v[4:7]\nds_write_b64 v250,v[160:161] offset:8192\nM v[4:7],a[148:151],v[196:199],v[4:7]\nM v[64:67],a[136:139],v[192:195],v[64:67]\n" \
"M v[64:67],a[152:155],v[196:199],v[64:67]\nbuffer_store_dwordx4 v[168:171],v240,s[52:55],s74 offen offset:2048 sc1\nM v[68:71],a[140:143],v[192:195],v[68:71]\nds_read_b64_tr_b16 v[192:193],v241 offset:1024\nds_read_b64_tr_b16 v[194:195],v241 offset:1280\nM v[68:71],a[156:159],v[196:199],v[68:71]\nds_read_b64_tr_b16 v[196:197],v241 offset:9216\ns_waitcnt lgkmcnt(9)\n" \
"M v[8:11],a[128:131],v[200:203],v[8:11]\nds_read_b64_tr_b16 v[198:199],v241 offset:9472\nM v[8:11],a[144:147],v[204:207],v[8:11]\nbuffer_load_dwordx4 v245,s[40:43],s69 offen offset:2048 lds\nM v[12:15],a[132:135],v[200:203],v[12:15]\nds_write_b64 v250,v[162:163] offset:8200\nM v[12:15],a[148:151],v[204:207],v[12:15]\nM v[72:75],a[136:139],v[200:203],v[72:75]\n" \
"M v[72:75],a[152:155],v[204:207],v[72:75]\nbuffer_load_dwordx4 v246,s[40:43],s69 offen offset:3072 lds\nM v[76:79],a[140:143],v[200:203],v[76:79]\nds_read_b64_tr_b16 v[200:201],v242 offset:1024\nds_read_b64_tr_b16 v[202:203],v242 offset:1280\nM v[76:79],a[156:159],v[204:207],v[76:79]\nds_read_b64_tr_b16 v[204:205],v242 offset:9216\nds_read_b64_tr_b16 v[206:207],v242 offset:9472\n" \
"M v[16:19],a[128:131],v[208:211],v[16:19]\nM v[16:19],a[144:147],v[212:215],v[16:19]\nM v[20:23],a[132:135],v[208:211],v[20:23]\nM v[20:23],a[148:151],v[212:215],v[20:23]\nM v[80:83],a[136:139],v[208:211],v[80:83]\nM v[80:83],a[152:155],v[212:215],v[80:83]\nM v[84:87],a[140:143],v[208:211],v[84:87]\nbuffer_load_dword v248,v228,s[52:55],s80 offen sc1\n" \
"M v[84:87],a[156:159],v[212:215],v[84:87]\nds_read_b64_tr_b16 v[208:209],v241 offset:1536\ns_waitcnt lgkmcnt(6)\nM v[24:27],a[128:131],v[216:219],v[24:27]\nbuffer_store_dwordx4 v[172:175],v240,s[52:55],s74 offen offset:3072 sc1\nM v[24:27],a[144:147],v[220:223],v[24:27]\nds_read_b64_tr_b16 v[210:211],v241 offset:1792\nds_read_b64_tr_b16 v[212:213],v241 offset:9728\n" \
"M v[28:31],a[132:135],v[216:219],v[28:31]\nds_read_b64_tr_b16 v[214:215],v241 offset:9984\ns_add_u32 s74,s74,4096\nM v[28:31],a[148:151],v[220:223],v[28:31]\nds_write_b64 v250,v[164:165] offset:9216\nM v[88:91],a[136:139],v[216:219],v[88:91]\nM v[88:91],a[152:155],v[220:223],v[88:91]\nM v[92:95],a[140:143],v[216:219],v[92:95]\n" \
"ds_read_b64_tr_b16 v[216:217],v242 offset:1536\nds_read_b64_tr_b16 v[218:219],v242 offset:1792\nM v[92:95],a[156:159],v[220:223],v[92:95]\nds_read_b64_tr_b16 v[220:221],v242 offset:9728\nds_read_b64_tr_b16 v[222:223],v242 offset:9984\nM v[32:35],a[128:131],v[192:195],v[32:35]\nM v[32:35],a[144:147],v[196:199],v[32:35]\nM v[36:39],a[132:135],v[192:195],v[36:39]\n" \
"ds_write_b64 v250,v[166:167] offset:9224\nM v[36:39],a[148:151],v[196:199],v[36:39]\nM v[96:99],a[136:139],v[192:195],v[96:99]\nM v[96:99],a[152:155],v[196:199],v[96:99]\nM v[100:103],a[140:143],v[192:195],v[100:103]\nM v[100:103],a[156:159],v[196:199],v[100:103]\ns_waitcnt lgkmcnt(9)\nM v[40:43],a[128:131],v[200:203],v[40:43]\n" \
"buffer_store_dwordx4 v[176:179],v240,s[52:55],s74 offen offset:0 sc1\nM v[40:43],a[144:147],v[204:207],v[40:43]\nM v[44:47],a[132:135],v[200:203],v[44:47]\nM v[44:47],a[148:151],v[204:207],v[44:47]\nM v[104:107],a[136:139],v[200:203],v[104:107]\nM v[104:107],a[152:155],v[204:207],v[104:107]\nM v[108:111],a[140:143],v[200:203],v[108:111]\nds_write_b64 v250,v[168:169] offset:10240\n" \
"M v[108:111],a[156:159],v[204:207],v[108:111]\ns_waitcnt lgkmcnt(4)\nM v[48:51],a[128:131],v[208:211],v[48:51]\nM v[48:51],a[144:147],v[212:215],v[48:51]\nM v[52:55],a[132:135],v[208:211],v[52:55]\ns_sub_u32 s40,s12,0x1000\ns_subb_u32 s41,s13,0\nM v[52:55],a[148:151],v[212:215],v[52:55]\n" \
"s_add_u32 m0,s64,98304\ns_xor_b32 s69,s99,0\nM v[112:115],a[136:139],v[208:211],v[112:115]\ns_waitcnt vmcnt(18) lgkmcnt(1)\ns_barrier\nM v[112:115],a[152:155],v[212:215],v[112:115]\nds_read_b64_tr_b16 v[192:193],v241 offset:16384\nds_read_b64_tr_b16 v[194:195],v241 offset:16640\n" \
"M v[116:119],a[140:143],v[208:211],v[116:119]\nds_read_b64_tr_b16 v[196:197],v241 offset:24576\nds_read_b64_tr_b16 v[198:199],v241 offset:24832\nM v[116:119],a[156:159],v[212:215],v[116:119]\nds_read_b64_tr_b16 v[200:201],v242 offset:16384\nds_read_b64_tr_b16 v[202:203],v242 offset:16640\nM v[56:59],a[128:131],v[216:219],v[56:59]\nds_read_b64_tr_b16 v[204:205],v242 offset:24576\n" \
"ds_read_b64_tr_b16 v[206:207],v242 offset:24832\nM v[56:59],a[144:147],v[220:223],v[56:59]\nds_read_b64_tr_b16 v[208:209],v241 offset:16896\nds_read_b64_tr_b16 v[210:211],v241 offset:17152\nM v[60:63],a[132:135],v[216:219],v[60:63]\nbuffer_store_dwordx4 v[180:183],v240,s[52:55],s74 offen offset:1024 sc1\nM v[60:63],a[148:151],v[220:223],v[60:63]\nds_read_b64_tr_b16 v[212:213],v241 offset:25088\n" \
"ds_read_b64_tr_b16 v[214:215],v241 offset:25344\nM v[120:123],a[136:139],v[216:219],v[120:123]\nds_write_b64 v250,v[170:171] offset:10248\ns_lshl_b32 s12,s61,13\nM v[120:123],a[152:155],v[220:223],v[120:123]\nM v[124:127],a[140:143],v[216:219],v[124:127]\nds_read_b64_tr_b16 v[216:217],v242 offset:16896\nds_read_b64_tr_b16 v[218:219],v242 offset:17152\n" \
"M v[124:127],a[156:159],v[220:223],v[124:127]\nds_read_b64_tr_b16 v[220:221],v242 offset:25088\ns_waitcnt lgkmcnt(14)\nM v[0:3],a[160:163],v[192:195],v[0:3]\nds_read_b64_tr_b16 v[222:223],v242 offset:25344\ns_waitcnt lgkmcnt(13)\nM v[0:3],a[176:179],v[196:199],v[0:3]\nbuffer_load_dwordx4 v243,s[40:43],s69 offen offset:0 lds\n" \
"M v[4:7],a[164:167],v[192:195],v[4:7]\nM v[4:7],a[180:183],v[196:199],v[4:7]\nM v[64:67],a[168:171],v[192:195],v[64:67]\nM v[64:67],a[184:187],v[196:199],v[64:67]\nbuffer_load_dwordx4 v244,s[40:43],s69 offen offset:1024 lds\nM v[68:71],a[172:175],v[192:195],v[68:71]\nds_read_b64_tr_b16 v[192:193],v241 offset:17408\nds_read_b64_tr_b16 v[194:195],v241 offset:17664\n" \
"M v[68:71],a[188:191],v[196:199],v[68:71]\nds_read_b64_tr_b16 v[196:197],v241 offset:25600\ns_waitcnt lgkmcnt(9)\nM v[8:11],a[160:163],v[200:203],v[8:11]\nds_write_b64 v250,v[172:173] offset:11264\nds_read_b64_tr_b16 v[198:199],v241 offset:25856\nM v[8:11],a[176:179],v[204:207],v[8:11]\nbuffer_load_dwordx4 v245,s[40:43],s69 offen offset:2048 lds\n" \
"M v[12:15],a[164:167],v[200:203],v[12:15]\nM v[12:15],a[180:183],v[204:207],v[12:15]\nM v[72:75],a[168:171],v[200:203],v[72:75]\nM v[72:75],a[184:187],v[204:207],v[72:75]\nbuffer_store_dwordx4 v[184:187],v240,s[52:55],s74 offen offset:2048 sc1\nM v[76:79],a[172:175],v[200:203],v[76:79]\ns_waitcnt vmcnt(7)\nv_cmp_eq_u32 vcc,0,v248\n" \
"M v[76:79],a[188:191],v[204:207],v[76:79]\ns_cbranch_vccnz .Lps0\n.Lpb0:\nds_read_b64_tr_b16 v[200:201],v242 offset:17408\nM v[16:19],a[160:163],v[208:211],v[16:19]\nds_read_b64_tr_b16 v[202:203],v242 offset:17664\ns_waitcnt lgkmcnt(7)\nM v[16:19],a[176:179],v[212:215],v[16:19]\n" \
"ds_read_b64_tr_b16 v[204:205],v242 offset:25600\nds_read_b64_tr_b16 v[206:207],v242 offset:25856\nM v[20:23],a[164:167],v[208:211],v[20:23]\nbuffer_load_dwordx4 a[0:3],v240,s[52:55],s76 offen offset:0 sc1\nM v[20:23],a[180:183],v[212:215],v[20:23]\nds_write_b64 v250,v[174:175] offset:11272\nv_and_b32 v228,31,v249\nM v[80:83],a[168:171],v[208:211],v[80:83]\n" \
"s_lshl_b32 s80,s63,7\nv_lshlrev_b32 v228,2,v228\nM v[80:83],a[184:187],v[212:215],v[80:83]\ns_add_u32 s80,s80,81920\nv_mov_b32 v251,v228\nM v[84:87],a[172:175],v[208:211],v[84:87]\nds_read_b64_tr_b16 v[208:209],v241 offset:17920\nds_read_b64_tr_b16 v[210:211],v241 offset:18176\n" \
"M v[84:87],a[188:191],v[212:215],v[84:87]\nds_read_b64_tr_b16 v[212:213],v241 offset:26112\nds_read_b64_tr_b16 v[214:215],v241 offset:26368\nM v[24:27],a[160:163],v[216:219],v[24:27]\nbuffer_load_dwordx4 a[16:19],v240,s[52:55],s76 offen offset:2048 sc1\nM v[24:27],a[176:179],v[220:223],v[24:27]\nM v[28:31],a[164:167],v[216:219],v[28:31]\nM v[28:31],a[180:183],v[220:223],v[28:31]\n" \
"M v[88:91],a[168:171],v[216:219],v[88:91]\nbuffer_load_dwordx4 a[4:7],v240,s[52:55],s76 offen offset:1024 sc1\nM v[88:91],a[184:187],v[220:223],v[88:91]\nM v[92:95],a[172:175],v[216:219],v[92:95]\nds_read_b64_tr_b16 v[216:217],v242 offset:17920\nds_read_b64_tr_b16 v[218:219],v242 offset:18176\nM v[92:95],a[188:191],v[220:223],v[92:95]\nds_read_b64_tr_b16 v[220:221],v242 offset:26112\n" \
"s_waitcnt lgkmcnt(7)\nM v[32:35],a[160:163],v[192:195],v[32:35]\nbuffer_store_dwordx4 v[188:191],v240,s[52:55],s74 offen offset:3072 sc1\nM v[32:35],a[176:179],v[196:199],v[32:35]\nds_read_b64_tr_b16 v[222:223],v242 offset:26368\ns_add_u32 s74,s74,16764928\nM v[36:39],a[164:167],v[192:195],v[36:39]\nds_write_b64 v250,v[176:177] offset:12288\n" \
"M v[36:39],a[180:183],v[196:199],v[36:39]\nM v[96:99],a[168:171],v[192:195],v[96:99]\nbuffer_load_dwordx4 a[20:23],v240,s[52:55],s76 offen offset:3072 sc1\nM v[96:99],a[184:187],v[196:199],v[96:99]\ns_xor_b32 s76,s76,4096\nM v[100:103],a[172:175],v[192:195],v[100:103]\nM v[100:103],a[188:191],v[196:199],v[100:103]\nM v[40:43],a[160:163],v[200:203],v[40:43]\n" \
"buffer_load_dwordx4 a[8:11],v240,s[52:55],s77 offen offset:0 sc1\nM v[40:43],a[176:179],v[204:207],v[40:43]\nM v[44:47],a[164:167],v[200:203],v[44:47]\nds_write_b64 v250,v[178:179] offset:12296\nM v[44:47],a[180:183],v[204:207],v[44:47]\nM v[104:107],a[168:171],v[200:203],v[104:107]\nbuffer_load_dwordx4 a[24:27],v240,s[52:55],s77 offen offset:2048 sc1\nM v[104:107],a[184:187],v[204:207],v[104:107]\n" \
"M v[108:111],a[172:175],v[200:203],v[108:111]\nM v[108:111],a[188:191],v[204:207],v[108:111]\ns_waitcnt lgkmcnt(3)\nM v[48:51],a[160:163],v[208:211],v[48:51]\nbuffer_load_dwordx4 a[12:15],v240,s[52:55],s77 offen offset:1024 sc1\nM v[48:51],a[176:179],v[212:215],v[48:51]\nM v[52:55],a[164:167],v[208:211],v[52:55]\nM v[52:55],a[180:183],v[212:215],v[52:55]\n" \
"M v[112:115],a[168:171],v[208:211],v[112:115]\ns_waitcnt lgkmcnt(1)\ns_barrier\nM v[112:115],a[184:187],v[212:215],v[112:115]\nds_read_b64_tr_b16 v[192:193],v241 offset:32768\nds_read_b64_tr_b16 v[194:195],v241 offset:33024\nM v[116:119],a[172:175],v[208:211],v[116:119]\nds_read_b64_tr_b16 v[196:197],v241 offset:40960\n" \
"ds_read_b64_tr_b16 v[198:199],v241 offset:41216\nM v[116:119],a[188:191],v[212:215],v[116:119]\nds_read_b64_tr_b16 v[200:201],v242 offset:32768\nds_read_b64_tr_b16 v[202:203],v242 offset:33024\nM v[56:59],a[160:163],v[216:219],v[56:59]\nds_read_b64_tr_b16 v[204:205],v242 offset:40960\nds_read_b64_tr_b16 v[206:207],v242 offset:41216\nM v[56:59],a[176:179],v[220:223],v[56:59]\n" \
"ds_read_b64_tr_b16 v[208:209],v241 offset:33280\nds_read_b64_tr_b16 v[210:211],v241 offset:33536\nM v[60:63],a[164:167],v[216:219],v[60:63]\nds_read_b64_tr_b16 v[212:213],v241 offset:41472\nds_read_b64_tr_b16 v[214:215],v241 offset:41728\nM v[60:63],a[180:183],v[220:223],v[60:63]\nbuffer_load_dwordx4 a[28:31],v240,s[52:55],s77 offen offset:3072 sc1\nM v[120:123],a[168:171],v[216:219],v[120:123]\n" \
"ds_write_b64 v250,v[180:181] offset:13312\ns_xor_b32 s77,s77,4096\nM v[120:123],a[184:187],v[220:223],v[120:123]\nM v[124:127],a[172:175],v[216:219],v[124:127]\nds_read_b64_tr_b16 v[216:217],v242 offset:33280\nds_read_b64_tr_b16 v[218:219],v242 offset:33536\nM v[124:127],a[188:191],v[220:223],v[124:127]\nds_read_b64_tr_b16 v[220:221],v242 offset:41472\n" \
"s_waitcnt lgkmcnt(14)\nM v[0:3],a[192:195],v[192:195],v[0:3]\nds_read_b64_tr_b16 v[222:223],v242 offset:41728\ns_waitcnt lgkmcnt(13)\nM v[0:3],a[208:211],v[196:199],v[0:3]\nbuffer_load_dwordx4 v246,s[40:43],s69 offen offset:3072 lds\nM v[4:7],a[196:199],v[192:195],v[4:7]\ns_add_u32 m0,s64,114688\n" \
"s_xor_b32 s69,s99,524288\nM v[4:7],a[212:215],v[196:199],v[4:7]\nds_write_b64 v250,v[182:183] offset:13320\nM v[64:67],a[200:203],v[192:195],v[64:67]\nM v[64:67],a[216:219],v[196:199],v[64:67]\nbuffer_load_dwordx4 a[32:35],v240,s[52:55],s76 offen offset:0 sc1\nM v[68:71],a[204:207],v[192:195],v[68:71]\nds_read_b64_tr_b16 v[192:193],v241 offset:33792\n" \
"ds_read_b64_tr_b16 v[194:195],v241 offset:34048\nM v[68:71],a[220:223],v[196:199],v[68:71]\nds_read_b64_tr_b16 v[196:197],v241 offset:41984\ns_waitcnt lgkmcnt(9)\nM v[8:11],a[192:195],v[200:203],v[8:11]\nds_read_b64_tr_b16 v[198:199],v241 offset:42240\nM v[8:11],a[208:211],v[204:207],v[8:11]\nbuffer_load_dwordx4 a[48:51],v240,s[52:55],s76 offen offset:2048 sc1\n" \
"M v[12:15],a[196:199],v[200:203],v[12:15]\nM v[12:15],a[212:215],v[204:207],v[12:15]\nM v[72:75],a[200:203],v[200:203],v[72:75]\nM v[72:75],a[216:219],v[204:207],v[72:75]\nbuffer_load_dwordx4 a[36:39],v240,s[52:55],s76 offen offset:1024 sc1\nM v[76:79],a[204:207],v[200:203],v[76:79]\nds_read_b64_tr_b16 v[200:201],v242 offset:33792\nds_read_b64_tr_b16 v[202:203],v242 offset:34048\n" \
"M v[76:79],a[220:223],v[204:207],v[76:79]\nds_read_b64_tr_b16 v[204:205],v242 offset:41984\nds_read_b64_tr_b16 v[206:207],v242 offset:42240\nM v[16:19],a[192:195],v[208:211],v[16:19]\nds_write_b64 v250,v[184:185] offset:14336\nM v[16:19],a[208:211],v[212:215],v[16:19]\nbuffer_load_dwordx4 a[52:55],v240,s[52:55],s76 offen offset:3072 sc1\nM v[20:23],a[196:199],v[208:211],v[20:23]\n" \
"s_xor_b32 s76,s76,12288\nM v[20:23],a[212:215],v[212:215],v[20:23]\nM v[80:83],a[200:203],v[208:211],v[80:83]\nM v[80:83],a[216:219],v[212:215],v[80:83]\nbuffer_load_dwordx4 a[40:43],v240,s[52:55],s77 offen offset:0 sc1\nM v[84:87],a[204:207],v[208:211],v[84:87]\nds_read_b64_tr_b16 v[208:209],v241 offset:34304\nds_read_b64_tr_b16 v[210:211],v241 offset:34560\n" \
"M v[84:87],a[220:223],v[212:215],v[84:87]\nds_read_b64_tr_b16 v[212:213],v241 offset:42496\ns_waitcnt lgkmcnt(8)\nM v[24:27],a[192:195],v[216:219],v[24:27]\nds_read_b64_tr_b16 v[214:215],v241 offset:42752\nM v[24:27],a[208:211],v[220:223],v[24:27]\nbuffer_load_dwordx4 a[56:59],v240,s[52:55],s77 offen offset:2048 sc1\nM v[28:31],a[196:199],v[216:219],v[28:31]\n" \
"ds_write_b64 v250,v[186:187] offset:14344\nM v[28:31],a[212:215],v[220:223],v[28:31]\nM v[88:91],a[200:203],v[216:219],v[88:91]\nM v[88:91],a[216:219],v[220:223],v[88:91]\nbuffer_load_dwordx4 a[44:47],v240,s[52:55],s77 offen offset:1024 sc1\nM v[92:95],a[204:207],v[216:219],v[92:95]\nds_read_b64_tr_b16 v[216:217],v242 offset:34304\nds_read_b64_tr_b16 v[218:219],v242 offset:34560\n" \
"M v[92:95],a[220:223],v[220:223],v[92:95]\nds_read_b64_tr_b16 v[220:221],v242 offset:42496\nds_read_b64_tr_b16 v[222:223],v242 offset:42752\nM v[32:35],a[192:195],v[192:195],v[32:35]\nM v[32:35],a[208:211],v[196:199],v[32:35]\nbuffer_load_dwordx4 a[60:63],v240,s[52:55],s77 offen offset:3072 sc1\nM v[36:39],a[196:199],v[192:195],v[36:39]\ns_xor_b32 s77,s77,12288\n" \
"M v[36:39],a[212:215],v[196:199],v[36:39]\nM v[96:99],a[200:203],v[192:195],v[96:99]\nM v[96:99],a[216:219],v[196:199],v[96:99]\nbuffer_load_dwordx4 v243,s[40:43],s69 offen offset:0 lds\nM v[100:103],a[204:207],v[192:195],v[100:103]\nds_write_b64 v250,v[188:189] offset:15360\nM v[100:103],a[220:223],v[196:199],v[100:103]\ns_waitcnt lgkmcnt(6)\n" \
"M v[40:43],a[192:195],v[200:203],v[40:43]\nM v[40:43],a[208:211],v[204:207],v[40:43]\nbuffer_load_dwordx4 v244,s[40:43],s69 offen offset:1024 lds\nM v[44:47],a[196:199],v[200:203],v[44:47]\nM v[44:47],a[212:215],v[204:207],v[44:47]\nM v[104:107],a[200:203],v[200:203],v[104:107]\nM v[104:107],a[216:219],v[204:207],v[104:107]\nbuffer_load_dwordx4 v245,s[40:43],s69 offen offset:2048 lds\n" \
"M v[108:111],a[204:207],v[200:203],v[108:111]\nM v[108:111],a[220:223],v[204:207],v[108:111]\nM v[48:51],a[192:195],v[208:211],v[48:51]\nM v[48:51],a[208:211],v[212:215],v[48:51]\nbuffer_load_dwordx4 v246,s[40:43],s69 offen offset:3072 lds\nM v[52:55],a[196:199],v[208:211],v[52:55]\ns_add_u32 m0,s64,131072\ns_xor_b32 s69,s99,1048576\n" \
"M v[52:55],a[212:215],v[212:215],v[52:55]\nds_write_b64 v250,v[190:191] offset:15368\nv_add_u32 v250,65536,v250\nM v[112:115],a[200:203],v[208:211],v[112:115]\ns_waitcnt lgkmcnt(2)\ns_barrier\nM v[112:115],a[216:219],v[212:215],v[112:115]\nds_read_b64_tr_b16 v[192:193],v241 offset:49152\n" \
"ds_read_b64_tr_b16 v[194:195],v241 offset:49408\nM v[116:119],a[204:207],v[208:211],v[116:119]\nds_read_b64_tr_b16 v[196:197],v241 offset:57344\nds_read_b64_tr_b16 v[198:199],v241 offset:57600\nM v[116:119],a[220:223],v[212:215],v[116:119]\nds_read_b64_tr_b16 v[200:201],v242 offset:49152\nds_read_b64_tr_b16 v[202:203],v242 offset:49408\nM v[56:59],a[192:195],v[216:219],v[56:59]\n" \
"ds_read_b64_tr_b16 v[204:205],v242 offset:57344\nds_read_b64_tr_b16 v[206:207],v242 offset:57600\nM v[56:59],a[208:211],v[220:223],v[56:59]\nds_read_b64_tr_b16 v[208:209],v241 offset:49664\nds_read_b64_tr_b16 v[210:211],v241 offset:49920\nM v[60:63],a[196:199],v[216:219],v[60:63]\nds_read_b64_tr_b16 v[212:213],v241 offset:57856\nds_read_b64_tr_b16 v[214:215],v241 offset:58112\n" \
"M v[60:63],a[212:215],v[220:223],v[60:63]\nbuffer_load_dwordx4 a[64:67],v240,s[52:55],s76 offen offset:0 sc1\nM v[120:123],a[200:203],v[216:219],v[120:123]\nM v[120:123],a[216:219],v[220:223],v[120:123]\nM v[124:127],a[204:207],v[216:219],v[124:127]\nds_read_b64_tr_b16 v[216:217],v242 offset:49664\nds_read_b64_tr_b16 v[218:219],v242 offset:49920\nM v[124:127],a[220:223],v[220:223],v[124:127]\n" \
"ds_read_b64_tr_b16 v[220:221],v242 offset:57856\ns_waitcnt lgkmcnt(13)\nM v[0:3],a[224:227],v[192:195],v[0:3]\nds_read_b64_tr_b16 v[222:223],v242 offset:58112\ns_waitcnt lgkmcnt(12)\nM v[0:3],a[240:243],v[196:199],v[0:3]\nbuffer_load_dwordx4 a[68:71],v240,s[52:55],s76 offen offset:1024 sc1\nM v[4:7],a[228:231],v[192:195],v[4:7]\n" \
"M v[4:7],a[244:247],v[196:199],v[4:7]\nM v[64:67],a[232:235],v[192:195],v[64:67]\nM v[64:67],a[248:251],v[196:199],v[64:67]\nbuffer_load_dwordx4 a[80:83],v240,s[52:55],s76 offen offset:2048 sc1\nM v[68:71],a[236:239],v[192:195],v[68:71]\nds_read_b64_tr_b16 v[192:193],v241 offset:50176\nds_read_b64_tr_b16 v[194:195],v241 offset:50432\nM v[68:71],a[252:255],v[196:199],v[68:71]\n" \
"ds_read_b64_tr_b16 v[196:197],v241 offset:58368\ns_waitcnt lgkmcnt(7)\nM v[8:11],a[224:227],v[200:203],v[8:11]\nds_read_b64_tr_b16 v[198:199],v241 offset:58624\nv_cvt_pk_bf16_f32 v128,v0,v1\nM v[8:11],a[240:243],v[204:207],v[8:11]\nv_cvt_pk_bf16_f32 v129,v2,v3\nv_cvt_pk_bf16_f32 v130,v4,v5\n" \
"M v[12:15],a[228:231],v[200:203],v[12:15]\nv_cvt_pk_bf16_f32 v131,v6,v7\nv_cvt_pk_bf16_f32 v160,v64,v65\nM v[12:15],a[244:247],v[204:207],v[12:15]\nv_cvt_pk_bf16_f32 v161,v66,v67\nv_cvt_pk_bf16_f32 v162,v68,v69\nM v[72:75],a[232:235],v[200:203],v[72:75]\nv_cvt_pk_bf16_f32 v163,v70,v71\n" \
"v_cvt_pk_bf16_f32 v132,v8,v9\nM v[72:75],a[248:251],v[204:207],v[72:75]\nbuffer_load_dwordx4 a[84:87],v240,s[52:55],s76 offen offset:3072 sc1\nM v[76:79],a[236:239],v[200:203],v[76:79]\nds_read_b64_tr_b16 v[200:201],v242 offset:50176\nds_read_b64_tr_b16 v[202:203],v242 offset:50432\nM v[76:79],a[252:255],v[204:207],v[76:79]\nds_read_b64_tr_b16 v[204:205],v242 offset:58368\n" \
"ds_read_b64_tr_b16 v[206:207],v242 offset:58624\nM v[16:19],a[224:227],v[208:211],v[16:19]\nv_cvt_pk_bf16_f32 v133,v10,v11\nv_cvt_pk_bf16_f32 v134,v12,v13\nM v[16:19],a[240:243],v[212:215],v[16:19]\nv_cvt_pk_bf16_f32 v135,v14,v15\nv_cvt_pk_bf16_f32 v164,v72,v73\nM v[20:23],a[228:231],v[208:211],v[20:23]\n" \
"v_cvt_pk_bf16_f32 v165,v74,v75\nv_cvt_pk_bf16_f32 v166,v76,v77\nM v[20:23],a[244:247],v[212:215],v[20:23]\nv_cvt_pk_bf16_f32 v167,v78,v79\ns_xor_b32 s76,s76,4096\nM v[80:83],a[232:235],v[208:211],v[80:83]\nv_cvt_pk_bf16_f32 v136,v16,v17\nv_cvt_pk_bf16_f32 v137,v18,v19\n" \
"M v[80:83],a[248:251],v[212:215],v[80:83]\nbuffer_load_dwordx4 a[72:75],v240,s[52:55],s77 offen offset:0 sc1\nM v[84:87],a[236:239],v[208:211],v[84:87]\nds_read_b64_tr_b16 v[208:209],v241 offset:50688\nds_read_b64_tr_b16 v[210:211],v241 offset:50944\nM v[84:87],a[252:255],v[212:215],v[84:87]\nds_read_b64_tr_b16 v[212:213],v241 offset:58880\ns_waitcnt lgkmcnt(7)\n" \
"M v[24:27],a[224:227],v[216:219],v[24:27]\nds_read_b64_tr_b16 v[214:215],v241 offset:59136\nv_cvt_pk_bf16_f32 v138,v20,v21\nM v[24:27],a[240:243],v[220:223],v[24:27]\nv_cvt_pk_bf16_f32 v139,v22,v23\nv_cvt_pk_bf16_f32 v168,v80,v81\nM v[28:31],a[228:231],v[216:219],v[28:31]\nv_cvt_pk_bf16_f32 v169,v82,v83\n" \
"v_cvt_pk_bf16_f32 v170,v84,v85\nM v[28:31],a[244:247],v[220:223],v[28:31]\nbuffer_load_dwordx4 a[88:91],v240,s[52:55],s77 offen offset:2048 sc1\nM v[88:91],a[232:235],v[216:219],v[88:91]\nv_cvt_pk_bf16_f32 v171,v86,v87\nv_cvt_pk_bf16_f32 v140,v24,v25\nM v[88:91],a[248:251],v[220:223],v[88:91]\nv_cvt_pk_bf16_f32 v141,v26,v27\n" \
"M v[92:95],a[236:239],v[216:219],v[92:95]\nds_read_b64_tr_b16 v[216:217],v242 offset:50688\nds_read_b64_tr_b16 v[218:219],v242 offset:50944\nM v[92:95],a[252:255],v[220:223],v[92:95]\nds_read_b64_tr_b16 v[220:221],v242 offset:58880\nds_read_b64_tr_b16 v[222:223],v242 offset:59136\nM v[32:35],a[224:227],v[192:195],v[32:35]\nv_cvt_pk_bf16_f32 v142,v28,v29\n" \
"v_cvt_pk_bf16_f32 v143,v30,v31\nM v[32:35],a[240:243],v[196:199],v[32:35]\nv_cvt_pk_bf16_f32 v172,v88,v89\nv_cvt_pk_bf16_f32 v173,v90,v91\nM v[36:39],a[228:231],v[192:195],v[36:39]\nv_cvt_pk_bf16_f32 v174,v92,v93\nv_cvt_pk_bf16_f32 v175,v94,v95\nM v[36:39],a[244:247],v[196:199],v[36:39]\n" \
"buffer_load_dwordx4 a[76:79],v240,s[52:55],s77 offen offset:1024 sc1\nM v[96:99],a[232:235],v[192:195],v[96:99]\nv_cvt_pk_bf16_f32 v144,v32,v33\nv_cvt_pk_bf16_f32 v145,v34,v35\nM v[96:99],a[248:251],v[196:199],v[96:99]\nM v[100:103],a[236:239],v[192:195],v[100:103]\nM v[100:103],a[252:255],v[196:199],v[100:103]\ns_waitcnt vmcnt(20)\n" \
"buffer_store_dword v252,off,s[52:55],s29 sc1\ns_waitcnt lgkmcnt(4)\nM v[40:43],a[224:227],v[200:203],v[40:43]\nv_cvt_pk_bf16_f32 v146,v36,v37\nv_cvt_pk_bf16_f32 v147,v38,v39\nM v[40:43],a[240:243],v[204:207],v[40:43]\nv_cvt_pk_bf16_f32 v176,v96,v97\nv_cvt_pk_bf16_f32 v177,v98,v99\n" \
"M v[44:47],a[228:231],v[200:203],v[44:47]\nv_cvt_pk_bf16_f32 v178,v100,v101\nv_cvt_pk_bf16_f32 v179,v102,v103\nM v[44:47],a[244:247],v[204:207],v[44:47]\nbuffer_load_dwordx4 a[92:95],v240,s[52:55],s77 offen offset:3072 sc1\nM v[104:107],a[232:235],v[200:203],v[104:107]\nv_cvt_pk_bf16_f32 v148,v40,v41\nv_cvt_pk_bf16_f32 v149,v42,v43\n" \
"M v[104:107],a[248:251],v[204:207],v[104:107]\ns_xor_b32 s77,s77,4096\ns_add_u32 s29,s29,4096\nM v[108:111],a[236:239],v[200:203],v[108:111]\nv_cvt_pk_bf16_f32 v150,v44,v45\nv_cvt_pk_bf16_f32 v151,v46,v47\nM v[108:111],a[252:255],v[204:207],v[108:111]\nbuffer_load_dwordx4 v243,s[40:43],s69 offen offset:0 lds\n" \
"M v[48:51],a[224:227],v[208:211],v[48:51]\nv_cvt_pk_bf16_f32 v180,v104,v105\nv_cvt_pk_bf16_f32 v181,v106,v107\nM v[48:51],a[240:243],v[212:215],v[48:51]\nM v[52:55],a[228:231],v[208:211],v[52:55]\nM v[52:55],a[244:247],v[212:215],v[52:55]\nbuffer_load_dwordx4 v244,s[40:43],s69 offen offset:1024 lds\nM v[112:115],a[232:235],v[208:211],v[112:115]\n" \
"s_waitcnt vmcnt(15) lgkmcnt(0)\ns_barrier\nM v[112:115],a[248:251],v[212:215],v[112:115]\nds_read_b64_tr_b16 v[192:193],v241 offset:0\nds_read_b64_tr_b16 v[194:195],v241 offset:256\nM v[116:119],a[236:239],v[208:211],v[116:119]\nds_read_b64_tr_b16 v[196:197],v241 offset:8192\nds_read_b64_tr_b16 v[198:199],v241 offset:8448\n" \
"M v[116:119],a[252:255],v[212:215],v[116:119]\nds_read_b64_tr_b16 v[200:201],v242 offset:0\nds_read_b64_tr_b16 v[202:203],v242 offset:256\nM v[56:59],a[224:227],v[216:219],v[56:59]\nds_read_b64_tr_b16 v[204:205],v242 offset:8192\nds_read_b64_tr_b16 v[206:207],v242 offset:8448\nM v[56:59],a[240:243],v[220:223],v[56:59]\nds_read_b64_tr_b16 v[208:209],v241 offset:512\n" \
"ds_read_b64_tr_b16 v[210:211],v241 offset:768\nM v[60:63],a[228:231],v[216:219],v[60:63]\nds_read_b64_tr_b16 v[212:213],v241 offset:8704\nds_read_b64_tr_b16 v[214:215],v241 offset:8960\nM v[60:63],a[244:247],v[220:223],v[60:63]\nv_cvt_pk_bf16_f32 v182,v108,v109\nv_cvt_pk_bf16_f32 v183,v110,v111\nM v[120:123],a[232:235],v[216:219],v[120:123]\n" \
"v_cvt_pk_bf16_f32 v152,v48,v49\nv_cvt_pk_bf16_f32 v153,v50,v51\nM v[120:123],a[248:251],v[220:223],v[120:123]\nv_cvt_pk_bf16_f32 v154,v52,v53\nv_cvt_pk_bf16_f32 v155,v54,v55\nM v[124:127],a[236:239],v[216:219],v[124:127]\nds_read_b64_tr_b16 v[216:217],v242 offset:512\nds_read_b64_tr_b16 v[218:219],v242 offset:768\n" \
"M v[124:127],a[252:255],v[220:223],v[124:127]\nds_read_b64_tr_b16 v[220:221],v242 offset:8704\ns_waitcnt lgkmcnt(13)\nM v[0:3],v[192:195],a[0:3],0\nds_read_b64_tr_b16 v[222:223],v242 offset:8960\ns_waitcnt lgkmcnt(12)\nM v[0:3],v[196:199],a[16:19],v[0:3]\nv_cvt_pk_bf16_f32 v184,v112,v113\n" \
"v_cvt_pk_bf16_f32 v185,v114,v115\nM v[4:7],v[192:195],a[4:7],0\nv_cvt_pk_bf16_f32 v186,v116,v117\nv_cvt_pk_bf16_f32 v187,v118,v119\nM v[4:7],v[196:199],a[20:23],v[4:7]\nv_cvt_pk_bf16_f32 v156,v56,v57\nv_cvt_pk_bf16_f32 v157,v58,v59\nM v[64:67],v[192:195],a[8:11],0\n" \
"v_cvt_pk_bf16_f32 v158,v60,v61\nv_cvt_pk_bf16_f32 v159,v62,v63\nM v[64:67],v[196:199],a[24:27],v[64:67]\nv_cvt_pk_bf16_f32 v188,v120,v121\nv_cvt_pk_bf16_f32 v189,v122,v123\nM v[68:71],v[192:195],a[12:15],0\nds_read_b64_tr_b16 v[192:193],v241 offset:1024\nds_read_b64_tr_b16 v[194:195],v241 offset:1280\n" \
"M v[68:71],v[196:199],a[28:31],v[68:71]\nds_read_b64_tr_b16 v[196:197],v241 offset:9216\ns_waitcnt lgkmcnt(7)\nM v[8:11],v[200:203],a[0:3],0\nds_read_b64_tr_b16 v[198:199],v241 offset:9472\nv_cvt_pk_bf16_f32 v190,v124,v125\nM v[8:11],v[204:207],a[16:19],v[8:11]\nbuffer_load_dwordx4 v245,s[40:43],s69 offen offset:2048 lds\n" \
"M v[12:15],v[200:203],a[4:7],0\nv_cvt_pk_bf16_f32 v191,v126,v127\nM v[12:15],v[204:207],a[20:23],v[12:15]\nM v[72:75],v[200:203],a[8:11],0\nM v[72:75],v[204:207],a[24:27],v[72:75]\nbuffer_load_dwordx4 v246,s[40:43],s69 offen offset:3072 lds\nM v[76:79],v[200:203],a[12:15],0\nds_read_b64_tr_b16 v[200:201],v242 offset:1024\n" \
"ds_read_b64_tr_b16 v[202:203],v242 offset:1280\nM v[76:79],v[204:207],a[28:31],v[76:79]\nds_read_b64_tr_b16 v[204:205],v242 offset:9216\nds_read_b64_tr_b16 v[206:207],v242 offset:9472\nM v[16:19],v[208:211],a[0:3],0\ns_add_u32 m0,s64,147456\ns_xor_b32 s69,s99,1572864\nM v[16:19],v[212:215],a[16:19],v[16:19]\n" \
"buffer_load_dwordx4 a[96:99],v240,s[52:55],s76 offen offset:0 sc1\nM v[20:23],v[208:211],a[4:7],0\nM v[20:23],v[212:215],a[20:23],v[20:23]\nM v[80:83],v[208:211],a[8:11],0\nM v[80:83],v[212:215],a[24:27],v[80:83]\nbuffer_store_dwordx4 v[128:131],v240,s[52:55],s74 offen offset:0 sc1\nM v[84:87],v[208:211],a[12:15],0\nds_read_b64_tr_b16 v[208:209],v241 offset:1536\n" \
"ds_read_b64_tr_b16 v[210:211],v241 offset:1792\nM v[84:87],v[212:215],a[28:31],v[84:87]\nds_read_b64_tr_b16 v[212:213],v241 offset:9728\ns_waitcnt lgkmcnt(7)\nM v[24:27],v[216:219],a[0:3],0\nds_read_b64_tr_b16 v[214:215],v241 offset:9984\nM v[24:27],v[220:223],a[16:19],v[24:27]\nbuffer_load_dwordx4 a[112:115],v240,s[52:55],s76 offen offset:2048 sc1\n" \
"M v[28:31],v[216:219],a[4:7],0\nM v[28:31],v[220:223],a[20:23],v[28:31]\nM v[88:91],v[216:219],a[8:11],0\nM v[88:91],v[220:223],a[24:27],v[88:91]\nbuffer_load_dwordx4 a[100:103],v240,s[52:55],s76 offen offset:1024 sc1\nM v[92:95],v[216:219],a[12:15],0\nds_read_b64_tr_b16 v[216:217],v242 offset:1536\nds_read_b64_tr_b16 v[218:219],v242 offset:1792\n" \
"M v[92:95],v[220:223],a[28:31],v[92:95]\nds_read_b64_tr_b16 v[220:221],v242 offset:9728\nds_read_b64_tr_b16 v[222:223],v242 offset:9984\nM v[32:35],v[192:195],a[0:3],0\nM v[32:35],v[196:199],a[16:19],v[32:35]\nbuffer_load_dwordx4 a[116:119],v240,s[52:55],s76 offen offset:3072 sc1\nM v[36:39],v[192:195],a[4:7],0\ns_xor_b32 s76,s76,28672\n" \
"M v[36:39],v[196:199],a[20:23],v[36:39]\nM v[96:99],v[192:195],a[8:11],0\nM v[96:99],v[196:199],a[24:27],v[96:99]\nbuffer_load_dwordx4 a[104:107],v240,s[52:55],s77 offen offset:0 sc1\nM v[100:103],v[192:195],a[12:15],0\nM v[100:103],v[196:199],a[28:31],v[100:103]\ns_waitcnt lgkmcnt(4)\nM v[40:43],v[200:203],a[0:3],0\n" \
"M v[40:43],v[204:207],a[16:19],v[40:43]\nbuffer_load_dwordx4 a[120:123],v240,s[52:55],s77 offen offset:2048 sc1\nM v[44:47],v[200:203],a[4:7],0\nM v[44:47],v[204:207],a[20:23],v[44:47]\nM v[104:107],v[200:203],a[8:11],0\nM v[104:107],v[204:207],a[24:27],v[104:107]\nbuffer_load_dwordx4 a[108:111],v240,s[52:55],s77 offen offset:1024 sc1\nM v[108:111],v[200:203],a[12:15],0\n" \
"M v[108:111],v[204:207],a[28:31],v[108:111]\nM v[48:51],v[208:211],a[0:3],0\nM v[48:51],v[212:215],a[16:19],v[48:51]\nbuffer_load_dwordx4 a[124:127],v240,s[52:55],s77 offen offset:3072 sc1\nM v[52:55],v[208:211],a[4:7],0\ns_xor_b32 s77,s77,28672\nM v[52:55],v[212:215],a[20:23],v[52:55]\nM v[112:115],v[208:211],a[8:11],0\n" \
"s_waitcnt vmcnt(15) lgkmcnt(0)\ns_barrier\nM v[112:115],v[212:215],a[24:27],v[112:115]\nds_read_b64_tr_b16 v[192:193],v241 offset:16384\nds_read_b64_tr_b16 v[194:195],v241 offset:16640\nM v[116:119],v[208:211],a[12:15],0\nds_read_b64_tr_b16 v[196:197],v241 offset:24576\nds_read_b64_tr_b16 v[198:199],v241 offset:24832\n" \
"buffer_store_dwordx4 v[132:135],v240,s[52:55],s74 offen offset:1024 sc1\nM v[116:119],v[212:215],a[28:31],v[116:119]\nds_read_b64_tr_b16 v[200:201],v242 offset:16384\nds_read_b64_tr_b16 v[202:203],v242 offset:16640\nM v[56:59],v[216:219],a[0:3],0\nds_read_b64_tr_b16 v[204:205],v242 offset:24576\nds_read_b64_tr_b16 v[206:207],v242 offset:24832\nM v[56:59],v[220:223],a[16:19],v[56:59]\n" \
"ds_read_b64_tr_b16 v[208:209],v241 offset:16896\nds_read_b64_tr_b16 v[210:211],v241 offset:17152\nM v[60:63],v[216:219],a[4:7],0\nds_read_b64_tr_b16 v[212:213],v241 offset:25088\nds_read_b64_tr_b16 v[214:215],v241 offset:25344\nM v[60:63],v[220:223],a[20:23],v[60:63]\nbuffer_load_dwordx4 v243,s[40:43],s69 offen offset:0 lds\nM v[120:123],v[216:219],a[8:11],0\n" \
"M v[120:123],v[220:223],a[24:27],v[120:123]\nM v[124:127],v[216:219],a[12:15],0\nds_read_b64_tr_b16 v[216:217],v242 offset:16896\nds_read_b64_tr_b16 v[218:219],v242 offset:17152\nM v[124:127],v[220:223],a[28:31],v[124:127]\nds_read_b64_tr_b16 v[220:221],v242 offset:25088\ns_waitcnt lgkmcnt(13)\nM v[0:3],v[192:195],a[32:35],v[0:3]\n" \
"ds_read_b64_tr_b16 v[222:223],v242 offset:25344\ns_waitcnt lgkmcnt(12)\nM v[0:3],v[196:199],a[48:51],v[0:3]\nbuffer_load_dwordx4 v244,s[40:43],s69 offen offset:1024 lds\nM v[4:7],v[192:195],a[36:39],v[4:7]\nM v[4:7],v[196:199],a[52:55],v[4:7]\nM v[64:67],v[192:195],a[40:43],v[64:67]\nM v[64:67],v[196:199],a[56:59],v[64:67]\n" \
"buffer_load_dwordx4 v245,s[40:43],s69 offen offset:2048 lds\nM v[68:71],v[192:195],a[44:47],v[68:71]\nds_read_b64_tr_b16 v[192:193],v241 offset:17408\nds_read_b64_tr_b16 v[194:195],v241 offset:17664\nM v[68:71],v[196:199],a[60:63],v[68:71]\nds_read_b64_tr_b16 v[196:197],v241 offset:25600\ns_waitcnt lgkmcnt(7)\nM v[8:11],v[200:203],a[32:35],v[8:11]\n" \
"ds_read_b64_tr_b16 v[198:199],v241 offset:25856\nM v[8:11],v[204:207],a[48:51],v[8:11]\nbuffer_load_dwordx4 v246,s[40:43],s69 offen offset:3072 lds\nM v[12:15],v[200:203],a[36:39],v[12:15]\ns_add_u32 m0,s64,98304\ns_xor_b32 s69,s99,2097152\nM v[12:15],v[204:207],a[52:55],v[12:15]\nM v[72:75],v[200:203],a[40:43],v[72:75]\n" \
"M v[72:75],v[204:207],a[56:59],v[72:75]\nbuffer_load_dwordx4 a[128:131],v240,s[52:55],s76 offen offset:0 sc1\nM v[76:79],v[200:203],a[44:47],v[76:79]\nds_read_b64_tr_b16 v[200:201],v242 offset:17408\nds_read_b64_tr_b16 v[202:203],v242 offset:17664\nM v[76:79],v[204:207],a[60:63],v[76:79]\nds_read_b64_tr_b16 v[204:205],v242 offset:25600\nds_read_b64_tr_b16 v[206:207],v242 offset:25856\n" \
"M v[16:19],v[208:211],a[32:35],v[16:19]\nM v[16:19],v[212:215],a[48:51],v[16:19]\nbuffer_store_dwordx4 v[136:139],v240,s[52:55],s74 offen offset:2048 sc1\nM v[20:23],v[208:211],a[36:39],v[20:23]\nM v[20:23],v[212:215],a[52:55],v[20:23]\nM v[80:83],v[208:211],a[40:43],v[80:83]\nM v[80:83],v[212:215],a[56:59],v[80:83]\nbuffer_load_dwordx4 a[144:147],v240,s[52:55],s76 offen offset:2048 sc1\n" \
"M v[84:87],v[208:211],a[44:47],v[84:87]\nds_read_b64_tr_b16 v[208:209],v241 offset:17920\nds_read_b64_tr_b16 v[210:211],v241 offset:18176\nM v[84:87],v[212:215],a[60:63],v[84:87]\nds_read_b64_tr_b16 v[212:213],v241 offset:26112\ns_waitcnt lgkmcnt(7)\nM v[24:27],v[216:219],a[32:35],v[24:27]\nds_read_b64_tr_b16 v[214:215],v241 offset:26368\n" \
"M v[24:27],v[220:223],a[48:51],v[24:27]\nbuffer_load_dwordx4 a[132:135],v240,s[52:55],s76 offen offset:1024 sc1\nM v[28:31],v[216:219],a[36:39],v[28:31]\nM v[28:31],v[220:223],a[52:55],v[28:31]\nM v[88:91],v[216:219],a[40:43],v[88:91]\nM v[88:91],v[220:223],a[56:59],v[88:91]\nbuffer_load_dwordx4 a[148:151],v240,s[52:55],s76 offen offset:3072 sc1\nM v[92:95],v[216:219],a[44:47],v[92:95]\n" \
"ds_read_b64_tr_b16 v[216:217],v242 offset:17920\nds_read_b64_tr_b16 v[218:219],v242 offset:18176\nM v[92:95],v[220:223],a[60:63],v[92:95]\nds_read_b64_tr_b16 v[220:221],v242 offset:26112\nds_read_b64_tr_b16 v[222:223],v242 offset:26368\nM v[32:35],v[192:195],a[32:35],v[32:35]\ns_xor_b32 s76,s76,4096\nM v[32:35],v[196:199],a[48:51],v[32:35]\n" \
"buffer_load_dwordx4 a[136:139],v240,s[52:55],s77 offen offset:0 sc1\nM v[36:39],v[192:195],a[36:39],v[36:39]\nM v[36:39],v[196:199],a[52:55],v[36:39]\nM v[96:99],v[192:195],a[40:43],v[96:99]\nM v[96:99],v[196:199],a[56:59],v[96:99]\nbuffer_load_dwordx4 a[152:155],v240,s[52:55],s77 offen offset:2048 sc1\nM v[100:103],v[192:195],a[44:47],v[100:103]\nM v[100:103],v[196:199],a[60:63],v[100:103]\n" \
"s_waitcnt lgkmcnt(4)\nM v[40:43],v[200:203],a[32:35],v[40:43]\nM v[40:43],v[204:207],a[48:51],v[40:43]\nbuffer_load_dwordx4 a[140:143],v240,s[52:55],s77 offen offset:1024 sc1\nM v[44:47],v[200:203],a[36:39],v[44:47]\nM v[44:47],v[204:207],a[52:55],v[44:47]\nM v[104:107],v[200:203],a[40:43],v[104:107]\nM v[104:107],v[204:207],a[56:59],v[104:107]\n" \
"buffer_load_dwordx4 a[156:159],v240,s[52:55],s77 offen offset:3072 sc1\nM v[108:111],v[200:203],a[44:47],v[108:111]\ns_xor_b32 s77,s77,4096\nM v[108:111],v[204:207],a[60:63],v[108:111]\nM v[48:51],v[208:211],a[32:35],v[48:51]\nM v[48:51],v[212:215],a[48:51],v[48:51]\nbuffer_store_dwordx4 v[140:143],v240,s[52:55],s74 offen offset:3072 sc1\nM v[52:55],v[208:211],a[36:39],v[52:55]\n" \
"s_add_u32 s74,s74,4096\nM v[52:55],v[212:215],a[52:55],v[52:55]\nM v[112:115],v[208:211],a[40:43],v[112:115]\ns_waitcnt vmcnt(23) lgkmcnt(0)\ns_barrier\nM v[112:115],v[212:215],a[56:59],v[112:115]\nds_read_b64_tr_b16 v[192:193],v241 offset:32768\nds_read_b64_tr_b16 v[194:195],v241 offset:33024\n" \
"M v[116:119],v[208:211],a[44:47],v[116:119]\nds_read_b64_tr_b16 v[196:197],v241 offset:40960\nds_read_b64_tr_b16 v[198:199],v241 offset:41216\nM v[116:119],v[212:215],a[60:63],v[116:119]\nds_read_b64_tr_b16 v[200:201],v242 offset:32768\nds_read_b64_tr_b16 v[202:203],v242 offset:33024\nM v[56:59],v[216:219],a[32:35],v[56:59]\nds_read_b64_tr_b16 v[204:205],v242 offset:40960\n" \
"ds_read_b64_tr_b16 v[206:207],v242 offset:41216\nM v[56:59],v[220:223],a[48:51],v[56:59]\nds_read_b64_tr_b16 v[208:209],v241 offset:33280\nds_read_b64_tr_b16 v[210:211],v241 offset:33536\nM v[60:63],v[216:219],a[36:39],v[60:63]\nds_read_b64_tr_b16 v[212:213],v241 offset:41472\nds_read_b64_tr_b16 v[214:215],v241 offset:41728\nM v[60:63],v[220:223],a[52:55],v[60:63]\n" \
"buffer_load_dwordx4 v243,s[40:43],s69 offen offset:0 lds\nM v[120:123],v[216:219],a[40:43],v[120:123]\nM v[120:123],v[220:223],a[56:59],v[120:123]\nM v[124:127],v[216:219],a[44:47],v[124:127]\nds_read_b64_tr_b16 v[216:217],v242 offset:33280\nds_read_b64_tr_b16 v[218:219],v242 offset:33536\nM v[124:127],v[220:223],a[60:63],v[124:127]\nds_read_b64_tr_b16 v[220:221],v242 offset:41472\n" \
"s_waitcnt lgkmcnt(13)\nM v[0:3],v[192:195],a[64:67],v[0:3]\nds_read_b64_tr_b16 v[222:223],v242 offset:41728\ns_waitcnt lgkmcnt(12)\nM v[0:3],v[196:199],a[80:83],v[0:3]\nbuffer_load_dwordx4 v244,s[40:43],s69 offen offset:1024 lds\nM v[4:7],v[192:195],a[68:71],v[4:7]\nM v[4:7],v[196:199],a[84:87],v[4:7]\n" \
"M v[64:67],v[192:195],a[72:75],v[64:67]\nM v[64:67],v[196:199],a[88:91],v[64:67]\nbuffer_load_dwordx4 v245,s[40:43],s69 offen offset:2048 lds\nM v[68:71],v[192:195],a[76:79],v[68:71]\nds_read_b64_tr_b16 v[192:193],v241 offset:33792\nds_read_b64_tr_b16 v[194:195],v241 offset:34048\nM v[68:71],v[196:199],a[92:95],v[68:71]\nds_read_b64_tr_b16 v[196:197],v241 offset:41984\n" \
"s_waitcnt lgkmcnt(7)\nM v[8:11],v[200:203],a[64:67],v[8:11]\nds_read_b64_tr_b16 v[198:199],v241 offset:42240\nM v[8:11],v[204:207],a[80:83],v[8:11]\nbuffer_load_dwordx4 v246,s[40:43],s69 offen offset:3072 lds\nM v[12:15],v[200:203],a[68:71],v[12:15]\ns_add_u32 m0,s64,114688\ns_xor_b32 s69,s99,2621440\n" \
"M v[12:15],v[204:207],a[84:87],v[12:15]\nM v[72:75],v[200:203],a[72:75],v[72:75]\nM v[72:75],v[204:207],a[88:91],v[72:75]\nbuffer_store_dwordx4 v[144:147],v240,s[52:55],s74 offen offset:0 sc1\nM v[76:79],v[200:203],a[76:79],v[76:79]\nds_read_b64_tr_b16 v[200:201],v242 offset:33792\nds_read_b64_tr_b16 v[202:203],v242 offset:34048\nM v[76:79],v[204:207],a[92:95],v[76:79]\n" \
"ds_read_b64_tr_b16 v[204:205],v242 offset:41984\nds_read_b64_tr_b16 v[206:207],v242 offset:42240\nM v[16:19],v[208:211],a[64:67],v[16:19]\nM v[16:19],v[212:215],a[80:83],v[16:19]\nbuffer_load_dwordx4 a[160:163],v240,s[52:55],s76 offen offset:0 sc1\nM v[20:23],v[208:211],a[68:71],v[20:23]\nM v[20:23],v[212:215],a[84:87],v[20:23]\nM v[80:83],v[208:211],a[72:75],v[80:83]\n" \
"M v[80:83],v[212:215],a[88:91],v[80:83]\nbuffer_load_dwordx4 a[176:179],v240,s[52:55],s76 offen offset:2048 sc1\nM v[84:87],v[208:211],a[76:79],v[84:87]\nds_read_b64_tr_b16 v[208:209],v241 offset:34304\nds_read_b64_tr_b16 v[210:211],v241 offset:34560\nM v[84:87],v[212:215],a[92:95],v[84:87]\nds_read_b64_tr_b16 v[212:213],v241 offset:42496\ns_waitcnt lgkmcnt(7)\n" \
"M v[24:27],v[216:219],a[64:67],v[24:27]\nds_read_b64_tr_b16 v[214:215],v241 offset:42752\nM v[24:27],v[220:223],a[80:83],v[24:27]\nbuffer_load_dwordx4 a[164:167],v240,s[52:55],s76 offen offset:1024 sc1\nM v[28:31],v[216:219],a[68:71],v[28:31]\nM v[28:31],v[220:223],a[84:87],v[28:31]\nM v[88:91],v[216:219],a[72:75],v[88:91]\nM v[88:91],v[220:223],a[88:91],v[88:91]\n" \
"buffer_load_dwordx4 a[180:183],v240,s[52:55],s76 offen offset:3072 sc1\nM v[92:95],v[216:219],a[76:79],v[92:95]\nds_read_b64_tr_b16 v[216:217],v242 offset:34304\nds_read_b64_tr_b16 v[218:219],v242 offset:34560\nM v[92:95],v[220:223],a[92:95],v[92:95]\nds_read_b64_tr_b16 v[220:221],v242 offset:42496\nds_read_b64_tr_b16 v[222:223],v242 offset:42752\nM v[32:35],v[192:195],a[64:67],v[32:35]\n" \
"s_xor_b32 s76,s76,12288\nM v[32:35],v[196:199],a[80:83],v[32:35]\nbuffer_load_dwordx4 a[168:171],v240,s[52:55],s77 offen offset:0 sc1\nM v[36:39],v[192:195],a[68:71],v[36:39]\nM v[36:39],v[196:199],a[84:87],v[36:39]\nM v[96:99],v[192:195],a[72:75],v[96:99]\nM v[96:99],v[196:199],a[88:91],v[96:99]\nbuffer_load_dwordx4 a[184:187],v240,s[52:55],s77 offen offset:2048 sc1\n" \
"M v[100:103],v[192:195],a[76:79],v[100:103]\nM v[100:103],v[196:199],a[92:95],v[100:103]\ns_waitcnt lgkmcnt(4)\nM v[40:43],v[200:203],a[64:67],v[40:43]\nM v[40:43],v[204:207],a[80:83],v[40:43]\nbuffer_load_dwordx4 a[172:175],v240,s[52:55],s77 offen offset:1024 sc1\nM v[44:47],v[200:203],a[68:71],v[44:47]\nM v[44:47],v[204:207],a[84:87],v[44:47]\n" \
"M v[104:107],v[200:203],a[72:75],v[104:107]\nM v[104:107],v[204:207],a[88:91],v[104:107]\nbuffer_store_dwordx4 v[148:151],v240,s[52:55],s74 offen offset:1024 sc1\nM v[108:111],v[200:203],a[76:79],v[108:111]\nM v[108:111],v[204:207],a[92:95],v[108:111]\nM v[48:51],v[208:211],a[64:67],v[48:51]\nM v[48:51],v[212:215],a[80:83],v[48:51]\nbuffer_load_dwordx4 a[188:191],v240,s[52:55],s77 offen offset:3072 sc1\n" \
"M v[52:55],v[208:211],a[68:71],v[52:55]\ns_xor_b32 s77,s77,12288\nM v[52:55],v[212:215],a[84:87],v[52:55]\nM v[112:115],v[208:211],a[72:75],v[112:115]\ns_waitcnt vmcnt(23) lgkmcnt(0)\ns_barrier\nM v[112:115],v[212:215],a[88:91],v[112:115]\nds_read_b64_tr_b16 v[192:193],v241 offset:49152\n" \
"ds_read_b64_tr_b16 v[194:195],v241 offset:49408\nM v[116:119],v[208:211],a[76:79],v[116:119]\nds_read_b64_tr_b16 v[196:197],v241 offset:57344\nds_read_b64_tr_b16 v[198:199],v241 offset:57600\nM v[116:119],v[212:215],a[92:95],v[116:119]\nds_read_b64_tr_b16 v[200:201],v242 offset:49152\nds_read_b64_tr_b16 v[202:203],v242 offset:49408\nM v[56:59],v[216:219],a[64:67],v[56:59]\n" \
"ds_read_b64_tr_b16 v[204:205],v242 offset:57344\nds_read_b64_tr_b16 v[206:207],v242 offset:57600\nM v[56:59],v[220:223],a[80:83],v[56:59]\nds_read_b64_tr_b16 v[208:209],v241 offset:49664\nds_read_b64_tr_b16 v[210:211],v241 offset:49920\nM v[60:63],v[216:219],a[68:71],v[60:63]\nds_read_b64_tr_b16 v[212:213],v241 offset:57856\nds_read_b64_tr_b16 v[214:215],v241 offset:58112\n" \
"M v[60:63],v[220:223],a[84:87],v[60:63]\nbuffer_load_dwordx4 v243,s[40:43],s69 offen offset:0 lds\nM v[120:123],v[216:219],a[72:75],v[120:123]\nM v[120:123],v[220:223],a[88:91],v[120:123]\nM v[124:127],v[216:219],a[76:79],v[124:127]\nds_read_b64_tr_b16 v[216:217],v242 offset:49664\nds_read_b64_tr_b16 v[218:219],v242 offset:49920\nM v[124:127],v[220:223],a[92:95],v[124:127]\n" \
"ds_read_b64_tr_b16 v[220:221],v242 offset:57856\ns_waitcnt lgkmcnt(13)\nM v[0:3],v[192:195],a[96:99],v[0:3]\nds_read_b64_tr_b16 v[222:223],v242 offset:58112\ns_waitcnt lgkmcnt(12)\nM v[0:3],v[196:199],a[112:115],v[0:3]\nbuffer_load_dwordx4 v244,s[40:43],s69 offen offset:1024 lds\nM v[4:7],v[192:195],a[100:103],v[4:7]\n" \
"M v[4:7],v[196:199],a[116:119],v[4:7]\nM v[64:67],v[192:195],a[104:107],v[64:67]\nM v[64:67],v[196:199],a[120:123],v[64:67]\nbuffer_load_dwordx4 v245,s[40:43],s69 offen offset:2048 lds\nM v[68:71],v[192:195],a[108:111],v[68:71]\nds_read_b64_tr_b16 v[192:193],v241 offset:50176\nds_read_b64_tr_b16 v[194:195],v241 offset:50432\nM v[68:71],v[196:199],a[124:127],v[68:71]\n" \
"ds_read_b64_tr_b16 v[196:197],v241 offset:58368\ns_waitcnt lgkmcnt(7)\nM v[8:11],v[200:203],a[96:99],v[8:11]\nds_read_b64_tr_b16 v[198:199],v241 offset:58624\nM v[8:11],v[204:207],a[112:115],v[8:11]\nbuffer_store_dwordx4 v[152:155],v240,s[52:55],s74 offen offset:2048 sc1\nM v[12:15],v[200:203],a[100:103],v[12:15]\nM v[12:15],v[204:207],a[116:119],v[12:15]\n" \
"M v[72:75],v[200:203],a[104:107],v[72:75]\nM v[72:75],v[204:207],a[120:123],v[72:75]\nbuffer_load_dwordx4 v246,s[40:43],s69 offen offset:3072 lds\nM v[76:79],v[200:203],a[108:111],v[76:79]\nds_read_b64_tr_b16 v[200:201],v242 offset:50176\nds_read_b64_tr_b16 v[202:203],v242 offset:50432\nM v[76:79],v[204:207],a[124:127],v[76:79]\nds_read_b64_tr_b16 v[204:205],v242 offset:58368\n" \
"ds_read_b64_tr_b16 v[206:207],v242 offset:58624\nM v[16:19],v[208:211],a[96:99],v[16:19]\ns_add_u32 m0,s64,131072\ns_xor_b32 s69,s99,3145728\nM v[16:19],v[212:215],a[112:115],v[16:19]\nbuffer_load_dwordx4 a[192:195],v240,s[52:55],s76 offen offset:0 sc1\nM v[20:23],v[208:211],a[100:103],v[20:23]\nM v[20:23],v[212:215],a[116:119],v[20:23]\n" \
"M v[80:83],v[208:211],a[104:107],v[80:83]\nM v[80:83],v[212:215],a[120:123],v[80:83]\nbuffer_load_dwordx4 a[208:211],v240,s[52:55],s76 offen offset:2048 sc1\nM v[84:87],v[208:211],a[108:111],v[84:87]\nds_read_b64_tr_b16 v[208:209],v241 offset:50688\nds_read_b64_tr_b16 v[210:211],v241 offset:50944\nM v[84:87],v[212:215],a[124:127],v[84:87]\nds_read_b64_tr_b16 v[212:213],v241 offset:58880\n" \
"s_waitcnt lgkmcnt(7)\nM v[24:27],v[216:219],a[96:99],v[24:27]\nds_read_b64_tr_b16 v[214:215],v241 offset:59136\nM v[24:27],v[220:223],a[112:115],v[24:27]\nbuffer_load_dwordx4 a[196:199],v240,s[52:55],s76 offen offset:1024 sc1\nM v[28:31],v[216:219],a[100:103],v[28:31]\nM v[28:31],v[220:223],a[116:119],v[28:31]\nM v[88:91],v[216:219],a[104:107],v[88:91]\n" \
"M v[88:91],v[220:223],a[120:123],v[88:91]\nbuffer_load_dwordx4 a[212:215],v240,s[52:55],s76 offen offset:3072 sc1\nM v[92:95],v[216:219],a[108:111],v[92:95]\nds_read_b64_tr_b16 v[216:217],v242 offset:50688\nds_read_b64_tr_b16 v[218:219],v242 offset:50944\nM v[92:95],v[220:223],a[124:127],v[92:95]\nds_read_b64_tr_b16 v[220:221],v242 offset:58880\nds_read_b64_tr_b16 v[222:223],v242 offset:59136\n" \
"M v[32:35],v[192:195],a[96:99],v[32:35]\ns_xor_b32 s76,s76,4096\nM v[32:35],v[196:199],a[112:115],v[32:35]\nbuffer_load_dwordx4 a[200:203],v240,s[52:55],s77 offen offset:0 sc1\nM v[36:39],v[192:195],a[100:103],v[36:39]\nM v[36:39],v[196:199],a[116:119],v[36:39]\nM v[96:99],v[192:195],a[104:107],v[96:99]\nM v[96:99],v[196:199],a[120:123],v[96:99]\n" \
"buffer_load_dwordx4 a[216:219],v240,s[52:55],s77 offen offset:2048 sc1\nM v[100:103],v[192:195],a[108:111],v[100:103]\nM v[100:103],v[196:199],a[124:127],v[100:103]\ns_waitcnt lgkmcnt(4)\nM v[40:43],v[200:203],a[96:99],v[40:43]\nM v[40:43],v[204:207],a[112:115],v[40:43]\nbuffer_store_dwordx4 v[156:159],v240,s[52:55],s74 offen offset:3072 sc1\nM v[44:47],v[200:203],a[100:103],v[44:47]\n" \
"s_add_u32 s74,s74,4096\nM v[44:47],v[204:207],a[116:119],v[44:47]\nM v[104:107],v[200:203],a[104:107],v[104:107]\nM v[104:107],v[204:207],a[120:123],v[104:107]\nbuffer_load_dwordx4 a[204:207],v240,s[52:55],s77 offen offset:1024 sc1\nM v[108:111],v[200:203],a[108:111],v[108:111]\nM v[108:111],v[204:207],a[124:127],v[108:111]\nM v[48:51],v[208:211],a[96:99],v[48:51]\n" \
"M v[48:51],v[212:215],a[112:115],v[48:51]\nbuffer_load_dwordx4 a[220:223],v240,s[52:55],s77 offen offset:3072 sc1\nM v[52:55],v[208:211],a[100:103],v[52:55]\ns_xor_b32 s77,s77,4096\nM v[52:55],v[212:215],a[116:119],v[52:55]\nM v[112:115],v[208:211],a[104:107],v[112:115]\ns_waitcnt vmcnt(24) lgkmcnt(0)\ns_barrier\n" \
"M v[112:115],v[212:215],a[120:123],v[112:115]\nds_read_b64_tr_b16 v[192:193],v241 offset:0\nds_read_b64_tr_b16 v[194:195],v241 offset:256\nM v[116:119],v[208:211],a[108:111],v[116:119]\nds_read_b64_tr_b16 v[196:197],v241 offset:8192\nds_read_b64_tr_b16 v[198:199],v241 offset:8448\nM v[116:119],v[212:215],a[124:127],v[116:119]\nds_read_b64_tr_b16 v[200:201],v242 offset:0\n" \
"ds_read_b64_tr_b16 v[202:203],v242 offset:256\nM v[56:59],v[216:219],a[96:99],v[56:59]\nds_read_b64_tr_b16 v[204:205],v242 offset:8192\nds_read_b64_tr_b16 v[206:207],v242 offset:8448\nM v[56:59],v[220:223],a[112:115],v[56:59]\nds_read_b64_tr_b16 v[208:209],v241 offset:512\nds_read_b64_tr_b16 v[210:211],v241 offset:768\nM v[60:63],v[216:219],a[100:103],v[60:63]\n" \
"ds_read_b64_tr_b16 v[212:213],v241 offset:8704\nds_read_b64_tr_b16 v[214:215],v241 offset:8960\nM v[60:63],v[220:223],a[116:119],v[60:63]\nbuffer_load_dwordx4 v243,s[40:43],s69 offen offset:0 lds\nM v[120:123],v[216:219],a[104:107],v[120:123]\nM v[120:123],v[220:223],a[120:123],v[120:123]\nM v[124:127],v[216:219],a[108:111],v[124:127]\nds_read_b64_tr_b16 v[216:217],v242 offset:512\n" \
"ds_read_b64_tr_b16 v[218:219],v242 offset:768\nM v[124:127],v[220:223],a[124:127],v[124:127]\nds_read_b64_tr_b16 v[220:221],v242 offset:8704\ns_waitcnt lgkmcnt(13)\nM v[0:3],v[192:195],a[128:131],v[0:3]\nds_read_b64_tr_b16 v[222:223],v242 offset:8960\ns_waitcnt lgkmcnt(12)\nM v[0:3],v[196:199],a[144:147],v[0:3]\n" \
"buffer_load_dwordx4 v244,s[40:43],s69 offen offset:1024 lds\nM v[4:7],v[192:195],a[132:135],v[4:7]\nM v[4:7],v[196:199],a[148:151],v[4:7]\nM v[64:67],v[192:195],a[136:139],v[64:67]\nM v[64:67],v[196:199],a[152:155],v[64:67]\nbuffer_store_dwordx4 v[160:163],v240,s[52:55],s74 offen offset:0 sc1\nM v[68:71],v[192:195],a[140:143],v[68:71]\nds_read_b64_tr_b16 v[192:193],v241 offset:1024\n" \
"ds_read_b64_tr_b16 v[194:195],v241 offset:1280\nM v[68:71],v[196:199],a[156:159],v[68:71]\nds_read_b64_tr_b16 v[196:197],v241 offset:9216\ns_waitcnt lgkmcnt(7)\nM v[8:11],v[200:203],a[128:131],v[8:11]\nds_read_b64_tr_b16 v[198:199],v241 offset:9472\nM v[8:11],v[204:207],a[144:147],v[8:11]\nbuffer_load_dwordx4 v245,s[40:43],s69 offen offset:2048 lds\n" \
"M v[12:15],v[200:203],a[132:135],v[12:15]\nM v[12:15],v[204:207],a[148:151],v[12:15]\nM v[72:75],v[200:203],a[136:139],v[72:75]\nM v[72:75],v[204:207],a[152:155],v[72:75]\nbuffer_load_dwordx4 v246,s[40:43],s69 offen offset:3072 lds\nM v[76:79],v[200:203],a[140:143],v[76:79]\nds_read_b64_tr_b16 v[200:201],v242 offset:1024\nds_read_b64_tr_b16 v[202:203],v242 offset:1280\n" \
"M v[76:79],v[204:207],a[156:159],v[76:79]\nds_read_b64_tr_b16 v[204:205],v242 offset:9216\nds_read_b64_tr_b16 v[206:207],v242 offset:9472\nM v[16:19],v[208:211],a[128:131],v[16:19]\ns_add_u32 m0,s64,147456\ns_xor_b32 s69,s99,3670016\nM v[16:19],v[212:215],a[144:147],v[16:19]\nbuffer_load_dwordx4 a[224:227],v240,s[52:55],s76 offen offset:0 sc1\n" \
"M v[20:23],v[208:211],a[132:135],v[20:23]\ns_mov_b32 s99,0\nM v[20:23],v[212:215],a[148:151],v[20:23]\nM v[80:83],v[208:211],a[136:139],v[80:83]\nds_read_b128 a[0:3],v240 offset:0\nM v[80:83],v[212:215],a[152:155],v[80:83]\nbuffer_load_dword v248,v228,s[52:55],s80 offen sc1\nM v[84:87],v[208:211],a[140:143],v[84:87]\n" \
"ds_read_b64_tr_b16 v[208:209],v241 offset:1536\nds_read_b64_tr_b16 v[210:211],v241 offset:1792\nM v[84:87],v[212:215],a[156:159],v[84:87]\nds_read_b64_tr_b16 v[212:213],v241 offset:9728\ns_waitcnt lgkmcnt(8)\nM v[24:27],v[216:219],a[128:131],v[24:27]\nbuffer_load_dwordx4 a[240:243],v240,s[52:55],s76 offen offset:2048 sc1\nM v[24:27],v[220:223],a[144:147],v[24:27]\n" \
"ds_read_b64_tr_b16 v[214:215],v241 offset:9984\nM v[28:31],v[216:219],a[132:135],v[28:31]\nds_read_b128 a[4:7],v240 offset:1024\nM v[28:31],v[220:223],a[148:151],v[28:31]\nM v[88:91],v[216:219],a[136:139],v[88:91]\nbuffer_load_dwordx4 a[228:231],v240,s[52:55],s76 offen offset:1024 sc1\nM v[88:91],v[220:223],a[152:155],v[88:91]\nM v[92:95],v[216:219],a[140:143],v[92:95]\n" \
"ds_read_b64_tr_b16 v[216:217],v242 offset:1536\nds_read_b64_tr_b16 v[218:219],v242 offset:1792\nM v[92:95],v[220:223],a[156:159],v[92:95]\nds_read_b64_tr_b16 v[220:221],v242 offset:9728\nds_read_b64_tr_b16 v[222:223],v242 offset:9984\nM v[32:35],v[192:195],a[128:131],v[32:35]\nbuffer_load_dwordx4 a[244:247],v240,s[52:55],s76 offen offset:3072 sc1\nM v[32:35],v[196:199],a[144:147],v[32:35]\n" \
"ds_read_b128 a[8:11],v240 offset:2048\nM v[36:39],v[192:195],a[132:135],v[36:39]\nM v[36:39],v[196:199],a[148:151],v[36:39]\nM v[96:99],v[192:195],a[136:139],v[96:99]\nbuffer_store_dwordx4 v[164:167],v240,s[52:55],s74 offen offset:1024 sc1\nM v[96:99],v[196:199],a[152:155],v[96:99]\nM v[100:103],v[192:195],a[140:143],v[100:103]\nds_read_b128 a[12:15],v240 offset:3072\n" \
"M v[100:103],v[196:199],a[156:159],v[100:103]\ns_waitcnt lgkmcnt(7)\nM v[40:43],v[200:203],a[128:131],v[40:43]\nbuffer_load_dwordx4 a[232:235],v240,s[52:55],s77 offen offset:0 sc1\nM v[40:43],v[204:207],a[144:147],v[40:43]\nM v[44:47],v[200:203],a[132:135],v[44:47]\nM v[44:47],v[204:207],a[148:151],v[44:47]\nM v[104:107],v[200:203],a[136:139],v[104:107]\n" \
"buffer_load_dwordx4 a[248:251],v240,s[52:55],s77 offen offset:2048 sc1\nM v[104:107],v[204:207],a[152:155],v[104:107]\nds_read_b128 a[16:19],v240 offset:4096\nM v[108:111],v[200:203],a[140:143],v[108:111]\nM v[108:111],v[204:207],a[156:159],v[108:111]\nM v[48:51],v[208:211],a[128:131],v[48:51]\nbuffer_load_dwordx4 a[236:239],v240,s[52:55],s77 offen offset:1024 sc1\nM v[48:51],v[212:215],a[144:147],v[48:51]\n" \
"M v[52:55],v[208:211],a[132:135],v[52:55]\nds_read_b128 a[20:23],v240 offset:5120\nM v[52:55],v[212:215],a[148:151],v[52:55]\nM v[112:115],v[208:211],a[136:139],v[112:115]\ns_waitcnt vmcnt(17) lgkmcnt(3)\ns_barrier\nM v[112:115],v[212:215],a[152:155],v[112:115]\nds_read_b64_tr_b16 v[192:193],v241 offset:16384\n" \
"ds_read_b64_tr_b16 v[194:195],v241 offset:16640\nM v[116:119],v[208:211],a[140:143],v[116:119]\nds_read_b64_tr_b16 v[196:197],v241 offset:24576\nds_read_b64_tr_b16 v[198:199],v241 offset:24832\nM v[116:119],v[212:215],a[156:159],v[116:119]\nds_read_b64_tr_b16 v[200:201],v242 offset:16384\nds_read_b64_tr_b16 v[202:203],v242 offset:16640\nM v[56:59],v[216:219],a[128:131],v[56:59]\n" \
"ds_read_b64_tr_b16 v[204:205],v242 offset:24576\nds_read_b64_tr_b16 v[206:207],v242 offset:24832\nM v[56:59],v[220:223],a[144:147],v[56:59]\nds_read_b64_tr_b16 v[208:209],v241 offset:16896\nds_read_b64_tr_b16 v[210:211],v241 offset:17152\nM v[60:63],v[216:219],a[132:135],v[60:63]\nds_read_b64_tr_b16 v[212:213],v241 offset:25088\nds_read_b64_tr_b16 v[214:215],v241 offset:25344\n" \
"M v[60:63],v[220:223],a[148:151],v[60:63]\nbuffer_load_dwordx4 a[252:255],v240,s[52:55],s77 offen offset:3072 sc1\nM v[120:123],v[216:219],a[136:139],v[120:123]\nds_read_b128 a[24:27],v240 offset:6144\nM v[120:123],v[220:223],a[152:155],v[120:123]\nM v[124:127],v[216:219],a[140:143],v[124:127]\nds_read_b64_tr_b16 v[216:217],v242 offset:16896\nds_read_b64_tr_b16 v[218:219],v242 offset:17152\n" \
"M v[124:127],v[220:223],a[156:159],v[124:127]\nds_read_b64_tr_b16 v[220:221],v242 offset:25088\ns_waitcnt lgkmcnt(14)\nM v[0:3],v[192:195],a[160:163],v[0:3]\nds_read_b64_tr_b16 v[222:223],v242 offset:25344\ns_waitcnt lgkmcnt(13)\nM v[0:3],v[196:199],a[176:179],v[0:3]\nbuffer_store_dwordx4 v[168:171],v240,s[52:55],s74 offen offset:2048 sc1\n" \
"M v[4:7],v[192:195],a[164:167],v[4:7]\nds_read_b128 a[28:31],v240 offset:7168\nM v[4:7],v[196:199],a[180:183],v[4:7]\nM v[64:67],v[192:195],a[168:171],v[64:67]\nds_read_b128 a[32:35],v240 offset:8192\nM v[64:67],v[196:199],a[184:187],v[64:67]\nbuffer_load_dwordx4 v243,s[40:43],s69 offen offset:0 lds\nM v[68:71],v[192:195],a[172:175],v[68:71]\n" \
"ds_read_b64_tr_b16 v[192:193],v241 offset:17408\nds_read_b64_tr_b16 v[194:195],v241 offset:17664\nM v[68:71],v[196:199],a[188:191],v[68:71]\nds_read_b64_tr_b16 v[196:197],v241 offset:25600\ns_waitcnt lgkmcnt(10)\nM v[8:11],v[200:203],a[160:163],v[8:11]\nds_read_b64_tr_b16 v[198:199],v241 offset:25856\nM v[8:11],v[204:207],a[176:179],v[8:11]\n" \
"buffer_load_dwordx4 v244,s[40:43],s69 offen offset:1024 lds\nM v[12:15],v[200:203],a[164:167],v[12:15]\nds_read_b128 a[36:39],v240 offset:9216\nM v[12:15],v[204:207],a[180:183],v[12:15]\nM v[72:75],v[200:203],a[168:171],v[72:75]\nM v[72:75],v[204:207],a[184:187],v[72:75]\nbuffer_load_dwordx4 v245,s[40:43],s69 offen offset:2048 lds\nM v[76:79],v[200:203],a[172:175],v[76:79]\n" \
"ds_read_b64_tr_b16 v[200:201],v242 offset:17408\nds_read_b64_tr_b16 v[202:203],v242 offset:17664\nM v[76:79],v[204:207],a[188:191],v[76:79]\nds_read_b64_tr_b16 v[204:205],v242 offset:25600\nds_read_b64_tr_b16 v[206:207],v242 offset:25856\nM v[16:19],v[208:211],a[160:163],v[16:19]\nds_read_b128 a[40:43],v240 offset:10240\nM v[16:19],v[212:215],a[176:179],v[16:19]\n" \
"buffer_load_dwordx4 v246,s[40:43],s69 offen offset:3072 lds\nM v[20:23],v[208:211],a[164:167],v[20:23]\ns_sub_u32 s40,s18,0x1000\ns_subb_u32 s41,s19,0\nM v[20:23],v[212:215],a[180:183],v[20:23]\ns_waitcnt vmcnt(13)\nv_cmp_eq_u32 vcc,0,v248\nM v[80:83],v[208:211],a[168:171],v[80:83]\n" \
"s_cbranch_vccnz .Lps1\n.Lpb1:\ns_add_u32 s80,s80,4096\nM v[80:83],v[212:215],a[184:187],v[80:83]\ns_cmp_ge_u32 s62,6\ns_cselect_b64 vcc,-1,0\nM v[84:87],v[208:211],a[172:175],v[84:87]\nds_read_b64_tr_b16 v[208:209],v241 offset:17920\n" \
"ds_read_b64_tr_b16 v[210:211],v241 offset:18176\nM v[84:87],v[212:215],a[188:191],v[84:87]\nds_read_b64_tr_b16 v[212:213],v241 offset:26112\ns_waitcnt lgkmcnt(8)\nM v[24:27],v[216:219],a[160:163],v[24:27]\nds_read_b64_tr_b16 v[214:215],v241 offset:26368\nv_cndmask_b32 v254,v231,v229,vcc\nM v[24:27],v[220:223],a[176:179],v[24:27]\n" \
"s_cmp_eq_u32 s62,3\ns_cselect_b64 vcc,-1,0\nM v[28:31],v[216:219],a[164:167],v[28:31]\nv_cndmask_b32 v254,v254,v230,vcc\nv_bfe_u32 v230,v249,2,3\nM v[28:31],v[220:223],a[180:183],v[28:31]\nv_and_b32 v231,0x70,v243\nv_lshl_or_b32 v230,v230,10,v231\n" \
"M v[88:91],v[216:219],a[168:171],v[88:91]\nv_and_b32 v231,16,v249\nv_lshlrev_b32 v231,1,v231\nM v[88:91],v[220:223],a[184:187],v[88:91]\nv_xor_b32 v230,v230,v231\nv_add_u32 v243,4096,v230\nM v[92:95],v[216:219],a[172:175],v[92:95]\nbuffer_store_dwordx4 v[172:175],v240,s[52:55],s74 offen offset:3072 sc1\n" \
"M v[92:95],v[220:223],a[188:191],v[92:95]\nds_read_b64_tr_b16 v[216:217],v242 offset:17920\nds_read_b64_tr_b16 v[218:219],v242 offset:18176\nM v[32:35],v[192:195],a[160:163],v[32:35]\nds_read_b64_tr_b16 v[220:221],v242 offset:26112\nds_read_b64_tr_b16 v[222:223],v242 offset:26368\nM v[32:35],v[196:199],a[176:179],v[32:35]\ns_add_u32 s74,s74,4096\n" \
"v_add_u32 v244,3200,v230\nM v[36:39],v[192:195],a[164:167],v[36:39]\nv_add_u32 v245,10240,v230\nv_add_u32 v246,9344,v230\nM v[36:39],v[196:199],a[180:183],v[36:39]\nv_and_b32 v229,3,v249\nv_cmp_eq_u32 vcc,0,v229\nM v[96:99],v[192:195],a[168:171],v[96:99]\n" \
"v_mov_b32 v230,0x3f803f80\nv_cndmask_b32 v229,0,v230,vcc\nM v[96:99],v[196:199],a[184:187],v[96:99]\nv_and_b32 v230,0xff,v241\nv_and_b32 v231,16,v249\nM v[100:103],v[192:195],a[172:175],v[100:103]\nv_lshl_or_b32 v230,v231,4,v230\nv_and_b32 v231,32,v249\n" \
"M v[100:103],v[196:199],a[188:191],v[100:103]\nds_read_b128 a[44:47],v240 offset:11264\ns_waitcnt lgkmcnt(5)\nM v[40:43],v[200:203],a[160:163],v[40:43]\nds_read_b128 a[48:51],v240 offset:12288\nds_read_b128 a[52:55],v240 offset:13312\nM v[40:43],v[204:207],a[176:179],v[40:43]\nds_read_b128 a[56:59],v240 offset:14336\n" \
"M v[44:47],v[200:203],a[164:167],v[44:47]\nM v[44:47],v[204:207],a[180:183],v[44:47]\nM v[104:107],v[200:203],a[168:171],v[104:107]\nM v[104:107],v[204:207],a[184:187],v[104:107]\nM v[108:111],v[200:203],a[172:175],v[108:111]\nds_read_b128 a[60:63],v240 offset:15360\nM v[108:111],v[204:207],a[188:191],v[108:111]\nM v[48:51],v[208:211],a[160:163],v[48:51]\n" \
"M v[48:51],v[212:215],a[176:179],v[48:51]\nM v[52:55],v[208:211],a[164:167],v[52:55]\nM v[52:55],v[212:215],a[180:183],v[52:55]\nM v[112:115],v[208:211],a[168:171],v[112:115]\ns_waitcnt lgkmcnt(5)\ns_barrier\nM v[112:115],v[212:215],a[184:187],v[112:115]\nds_read_b64_tr_b16 v[192:193],v241 offset:32768\n" \
"ds_read_b64_tr_b16 v[194:195],v241 offset:33024\nM v[116:119],v[208:211],a[172:175],v[116:119]\nds_read_b64_tr_b16 v[196:197],v241 offset:40960\nds_read_b64_tr_b16 v[198:199],v241 offset:41216\nM v[116:119],v[212:215],a[188:191],v[116:119]\nds_read_b64_tr_b16 v[200:201],v242 offset:32768\nds_read_b64_tr_b16 v[202:203],v242 offset:33024\nM v[56:59],v[216:219],a[160:163],v[56:59]\n" \
"ds_read_b64_tr_b16 v[204:205],v242 offset:40960\nds_read_b64_tr_b16 v[206:207],v242 offset:41216\nM v[56:59],v[220:223],a[176:179],v[56:59]\nds_read_b64_tr_b16 v[208:209],v241 offset:33280\nds_read_b64_tr_b16 v[210:211],v241 offset:33536\nM v[60:63],v[216:219],a[164:167],v[60:63]\nds_read_b64_tr_b16 v[212:213],v241 offset:41472\nds_read_b64_tr_b16 v[214:215],v241 offset:41728\n" \
"M v[60:63],v[220:223],a[180:183],v[60:63]\nds_write_b64 v250,v[128:129] offset:0\nM v[120:123],v[216:219],a[168:171],v[120:123]\nbuffer_store_dwordx4 v[176:179],v240,s[52:55],s74 offen offset:0 sc1\nM v[120:123],v[220:223],a[184:187],v[120:123]\nds_write_b64 v250,v[130:131] offset:8\nM v[124:127],v[216:219],a[172:175],v[124:127]\nds_read_b64_tr_b16 v[216:217],v242 offset:33280\n" \
"ds_read_b64_tr_b16 v[218:219],v242 offset:33536\nM v[124:127],v[220:223],a[188:191],v[124:127]\nds_read_b64_tr_b16 v[220:221],v242 offset:41472\ns_waitcnt lgkmcnt(15)\nM v[0:3],v[192:195],a[192:195],v[0:3]\nds_read_b64_tr_b16 v[222:223],v242 offset:41728\ns_waitcnt lgkmcnt(14)\nM v[0:3],v[196:199],a[208:211],v[0:3]\n" \
"ds_write_b64 v250,v[132:133] offset:1024\nM v[4:7],v[192:195],a[196:199],v[4:7]\nds_write_b64 v250,v[134:135] offset:1032\nM v[4:7],v[196:199],a[212:215],v[4:7]\nds_write_b64 v250,v[136:137] offset:2048\nM v[64:67],v[192:195],a[200:203],v[64:67]\nM v[64:67],v[196:199],a[216:219],v[64:67]\nds_write_b64 v250,v[138:139] offset:2056\n" \
"M v[68:71],v[192:195],a[204:207],v[68:71]\nds_read_b64_tr_b16 v[192:193],v241 offset:33792\nds_read_b64_tr_b16 v[194:195],v241 offset:34048\nM v[68:71],v[196:199],a[220:223],v[68:71]\nds_read_b64_tr_b16 v[196:197],v241 offset:41984\ns_waitcnt lgkmcnt(12)\nM v[8:11],v[200:203],a[192:195],v[8:11]\nds_write_b64 v250,v[140:141] offset:3072\n" \
"ds_read_b64_tr_b16 v[198:199],v241 offset:42240\nM v[8:11],v[204:207],a[208:211],v[8:11]\nM v[12:15],v[200:203],a[196:199],v[12:15]\nM v[12:15],v[204:207],a[212:215],v[12:15]\nds_write_b64 v250,v[142:143] offset:3080\nM v[72:75],v[200:203],a[200:203],v[72:75]\nM v[72:75],v[204:207],a[216:219],v[72:75]\nM v[76:79],v[200:203],a[204:207],v[76:79]\n" \
"ds_read_b64_tr_b16 v[200:201],v242 offset:33792\nds_read_b64_tr_b16 v[202:203],v242 offset:34048\nM v[76:79],v[204:207],a[220:223],v[76:79]\nds_read_b64_tr_b16 v[204:205],v242 offset:41984\nds_read_b64_tr_b16 v[206:207],v242 offset:42240\nM v[16:19],v[208:211],a[192:195],v[16:19]\nds_write_b64 v250,v[144:145] offset:4096\nM v[16:19],v[212:215],a[208:211],v[16:19]\n" \
"ds_write_b64 v250,v[146:147] offset:4104\nM v[20:23],v[208:211],a[196:199],v[20:23]\nM v[20:23],v[212:215],a[212:215],v[20:23]\nM v[80:83],v[208:211],a[200:203],v[80:83]\nds_write_b64 v250,v[148:149] offset:5120\nM v[80:83],v[212:215],a[216:219],v[80:83]\nM v[84:87],v[208:211],a[204:207],v[84:87]\nds_read_b64_tr_b16 v[208:209],v241 offset:34304\n" \
"ds_read_b64_tr_b16 v[210:211],v241 offset:34560\nM v[84:87],v[212:215],a[220:223],v[84:87]\nds_read_b64_tr_b16 v[212:213],v241 offset:42496\ns_waitcnt lgkmcnt(11)\nM v[24:27],v[216:219],a[192:195],v[24:27]\nds_write_b64 v250,v[150:151] offset:5128\nds_read_b64_tr_b16 v[214:215],v241 offset:42752\nM v[24:27],v[220:223],a[208:211],v[24:27]\n" \
"M v[28:31],v[216:219],a[196:199],v[28:31]\nbuffer_store_dwordx4 v[180:183],v240,s[52:55],s74 offen offset:1024 sc1\nM v[28:31],v[220:223],a[212:215],v[28:31]\nds_write_b64 v250,v[152:153] offset:6144\nM v[88:91],v[216:219],a[200:203],v[88:91]\nM v[88:91],v[220:223],a[216:219],v[88:91]\nds_write_b64 v250,v[154:155] offset:6152\nM v[92:95],v[216:219],a[204:207],v[92:95]\n" \
"ds_read_b64_tr_b16 v[216:217],v242 offset:34304\nds_read_b64_tr_b16 v[218:219],v242 offset:34560\nM v[92:95],v[220:223],a[220:223],v[92:95]\nds_read_b64_tr_b16 v[220:221],v242 offset:42496\nds_read_b64_tr_b16 v[222:223],v242 offset:42752\nM v[32:35],v[192:195],a[192:195],v[32:35]\nds_write_b64 v250,v[156:157] offset:7168\nM v[32:35],v[196:199],a[208:211],v[32:35]\n" \
"M v[36:39],v[192:195],a[196:199],v[36:39]\nM v[36:39],v[196:199],a[212:215],v[36:39]\nds_write_b64 v250,v[158:159] offset:7176\nM v[96:99],v[192:195],a[200:203],v[96:99]\nM v[96:99],v[196:199],a[216:219],v[96:99]\nM v[100:103],v[192:195],a[204:207],v[100:103]\nds_write_b64 v250,v[160:161] offset:8192\nM v[100:103],v[196:199],a[220:223],v[100:103]\n" \
"s_waitcnt lgkmcnt(9)\nM v[40:43],v[200:203],a[192:195],v[40:43]\nM v[40:43],v[204:207],a[208:211],v[40:43]\nds_write_b64 v250,v[162:163] offset:8200\nM v[44:47],v[200:203],a[196:199],v[44:47]\nM v[44:47],v[204:207],a[212:215],v[44:47]\nM v[104:107],v[200:203],a[200:203],v[104:107]\nds_write_b64 v250,v[164:165] offset:9216\n" \
"M v[104:107],v[204:207],a[216:219],v[104:107]\nM v[108:111],v[200:203],a[204:207],v[108:111]\nM v[108:111],v[204:207],a[220:223],v[108:111]\nds_write_b64 v250,v[166:167] offset:9224\nM v[48:51],v[208:211],a[192:195],v[48:51]\nM v[48:51],v[212:215],a[208:211],v[48:51]\nM v[52:55],v[208:211],a[196:199],v[52:55]\nds_write_b64 v250,v[168:169] offset:10240\n" \
"M v[52:55],v[212:215],a[212:215],v[52:55]\nM v[112:115],v[208:211],a[200:203],v[112:115]\ns_waitcnt vmcnt(3) lgkmcnt(5)\ns_barrier\nM v[112:115],v[212:215],a[216:219],v[112:115]\nds_read_b64_tr_b16 v[192:193],v241 offset:49152\nds_read_b64_tr_b16 v[194:195],v241 offset:49408\nM v[116:119],v[208:211],a[204:207],v[116:119]\n" \
"ds_read_b64_tr_b16 v[196:197],v241 offset:57344\nds_read_b64_tr_b16 v[198:199],v241 offset:57600\nM v[116:119],v[212:215],a[220:223],v[116:119]\nds_read_b64_tr_b16 v[200:201],v242 offset:49152\nds_read_b64_tr_b16 v[202:203],v242 offset:49408\nM v[56:59],v[216:219],a[192:195],v[56:59]\nbuffer_store_dwordx4 v[184:187],v240,s[52:55],s74 offen offset:2048 sc1\nM v[56:59],v[220:223],a[208:211],v[56:59]\n" \
"ds_read_b64_tr_b16 v[204:205],v242 offset:57344\nds_read_b64_tr_b16 v[206:207],v242 offset:57600\nM v[60:63],v[216:219],a[196:199],v[60:63]\nds_read_b64_tr_b16 v[208:209],v241 offset:49664\nds_read_b64_tr_b16 v[210:211],v241 offset:49920\nM v[60:63],v[220:223],a[212:215],v[60:63]\nds_read_b64_tr_b16 v[212:213],v241 offset:57856\nds_read_b64_tr_b16 v[214:215],v241 offset:58112\n" \
"M v[120:123],v[216:219],a[200:203],v[120:123]\nds_write_b64 v250,v[170:171] offset:10248\nv_accvgpr_write_b32 a208,v229\nM v[120:123],v[220:223],a[216:219],v[120:123]\nds_write_b64 v250,v[172:173] offset:11264\nv_accvgpr_write_b32 a209,v229\nM v[124:127],v[216:219],a[204:207],v[124:127]\nds_read_b64_tr_b16 v[216:217],v242 offset:49664\n" \
"ds_read_b64_tr_b16 v[218:219],v242 offset:49920\nM v[124:127],v[220:223],a[220:223],v[124:127]\nds_read_b64_tr_b16 v[220:221],v242 offset:57856\ns_waitcnt lgkmcnt(15)\nM v[0:3],v[192:195],a[224:227],v[0:3]\nds_read_b64_tr_b16 v[222:223],v242 offset:58112\ns_waitcnt lgkmcnt(14)\nM v[0:3],v[196:199],a[240:243],v[0:3]\n" \
"ds_write_b64 v250,v[174:175] offset:11272\nv_accvgpr_write_b32 a210,v229\nM v[4:7],v[192:195],a[228:231],v[4:7]\nds_write_b64 v250,v[176:177] offset:12288\nv_accvgpr_write_b32 a211,v229\nM v[4:7],v[196:199],a[244:247],v[4:7]\nds_write_b64 v250,v[178:179] offset:12296\nM v[64:67],v[192:195],a[232:235],v[64:67]\n" \
"v_cvt_pk_bf16_f32 v0,v0,v1\nv_cvt_pk_bf16_f32 v1,v2,v3\nM v[64:67],v[196:199],a[248:251],v[64:67]\nds_write_b64 v250,v[180:181] offset:13312\nv_accvgpr_write_b32 a128,v0\nM v[68:71],v[192:195],a[236:239],v[68:71]\nds_read_b64_tr_b16 v[192:193],v241 offset:50176\nds_read_b64_tr_b16 v[194:195],v241 offset:50432\n" \
"M v[68:71],v[196:199],a[252:255],v[68:71]\nds_read_b64_tr_b16 v[196:197],v241 offset:58368\ns_waitcnt lgkmcnt(12)\nM v[8:11],v[200:203],a[224:227],v[8:11]\nds_read_b64_tr_b16 v[198:199],v241 offset:58624\nv_cvt_pk_bf16_f32 v4,v4,v5\nM v[8:11],v[204:207],a[240:243],v[8:11]\nv_cvt_pk_bf16_f32 v64,v64,v65\n" \
"v_cvt_pk_bf16_f32 v5,v6,v7\nM v[12:15],v[200:203],a[228:231],v[12:15]\nv_cvt_pk_bf16_f32 v68,v68,v69\nv_cvt_pk_bf16_f32 v65,v66,v67\nM v[12:15],v[204:207],a[244:247],v[12:15]\nds_write_b64 v250,v[182:183] offset:13320\nv_cvt_pk_bf16_f32 v69,v70,v71\nM v[72:75],v[200:203],a[232:235],v[72:75]\n" \
"ds_write_b64 v250,v[184:185] offset:14336\nv_cvt_pk_bf16_f32 v2,v8,v9\nM v[72:75],v[204:207],a[248:251],v[72:75]\nds_write_b64 v250,v[186:187] offset:14344\nv_cvt_pk_bf16_f32 v3,v10,v11\nM v[76:79],v[200:203],a[236:239],v[76:79]\nds_read_b64_tr_b16 v[200:201],v242 offset:50176\nds_read_b64_tr_b16 v[202:203],v242 offset:50432\n" \
"M v[76:79],v[204:207],a[252:255],v[76:79]\nds_read_b64_tr_b16 v[204:205],v242 offset:58368\nds_read_b64_tr_b16 v[206:207],v242 offset:58624\nM v[16:19],v[208:211],a[224:227],v[16:19]\nds_write_b64 v250,v[188:189] offset:15360\nv_cvt_pk_bf16_f32 v6,v12,v13\nM v[16:19],v[212:215],a[240:243],v[16:19]\nv_cvt_pk_bf16_f32 v7,v14,v15\n" \
"v_cvt_pk_bf16_f32 v66,v72,v73\nM v[20:23],v[208:211],a[228:231],v[20:23]\nv_cvt_pk_bf16_f32 v67,v74,v75\nv_cvt_pk_bf16_f32 v70,v76,v77\nM v[20:23],v[212:215],a[244:247],v[20:23]\nds_write_b64 v250,v[190:191] offset:15368\nv_cvt_pk_bf16_f32 v71,v78,v79\nM v[80:83],v[208:211],a[232:235],v[80:83]\n" \
"v_cvt_pk_bf16_f32 v16,v16,v17\nv_cvt_pk_bf16_f32 v17,v18,v19\nM v[80:83],v[212:215],a[248:251],v[80:83]\nv_add_u32 v250,114688,v240\nv_accvgpr_write_b32 a129,v1\nM v[84:87],v[208:211],a[236:239],v[84:87]\nbuffer_store_dwordx4 v[188:191],v240,s[52:55],s74 offen offset:3072 sc1\nM v[84:87],v[212:215],a[252:255],v[84:87]\n" \
"ds_read_b64_tr_b16 v[208:209],v241 offset:50688\ns_waitcnt lgkmcnt(7)\nM v[24:27],v[216:219],a[224:227],v[24:27]\nds_read_b64_tr_b16 v[210:211],v241 offset:50944\nds_read_b64_tr_b16 v[212:213],v241 offset:58880\nM v[24:27],v[220:223],a[240:243],v[24:27]\nds_read_b64_tr_b16 v[214:215],v241 offset:59136\nv_cvt_pk_bf16_f32 v20,v20,v21\n" \
"M v[28:31],v[216:219],a[228:231],v[28:31]\nv_cvt_pk_bf16_f32 v80,v80,v81\nv_cvt_pk_bf16_f32 v84,v84,v85\nM v[28:31],v[220:223],a[244:247],v[28:31]\nv_cvt_pk_bf16_f32 v21,v22,v23\nv_cvt_pk_bf16_f32 v81,v82,v83\nM v[88:91],v[216:219],a[232:235],v[88:91]\nv_cvt_pk_bf16_f32 v85,v86,v87\n" \
"v_cvt_pk_bf16_f32 v18,v24,v25\nM v[88:91],v[220:223],a[248:251],v[88:91]\nv_cvt_pk_bf16_f32 v19,v26,v27\nv_add_u32 v241,65536,v240\nM v[92:95],v[216:219],a[236:239],v[92:95]\nds_read_b64_tr_b16 v[216:217],v242 offset:50688\nds_read_b64_tr_b16 v[218:219],v242 offset:50944\nM v[92:95],v[220:223],a[252:255],v[92:95]\n" \
"ds_read_b64_tr_b16 v[220:221],v242 offset:58880\nds_read_b64_tr_b16 v[222:223],v242 offset:59136\nM v[32:35],v[192:195],a[224:227],v[32:35]\nv_cvt_pk_bf16_f32 v22,v28,v29\nv_cvt_pk_bf16_f32 v23,v30,v31\nM v[32:35],v[196:199],a[240:243],v[32:35]\nv_cvt_pk_bf16_f32 v82,v88,v89\nv_cvt_pk_bf16_f32 v83,v90,v91\n" \
"M v[36:39],v[192:195],a[228:231],v[36:39]\nv_cvt_pk_bf16_f32 v86,v92,v93\nv_cvt_pk_bf16_f32 v87,v94,v95\nM v[36:39],v[196:199],a[244:247],v[36:39]\nv_lshl_or_b32 v242,v231,6,v230\nv_accvgpr_write_b32 a130,v2\nM v[96:99],v[192:195],a[232:235],v[96:99]\nv_cvt_pk_bf16_f32 v32,v32,v33\n" \
"v_cvt_pk_bf16_f32 v33,v34,v35\nM v[96:99],v[196:199],a[248:251],v[96:99]\nv_accvgpr_write_b32 a131,v3\nv_accvgpr_write_b32 a144,v4\nM v[100:103],v[192:195],a[236:239],v[100:103]\nv_cvt_pk_bf16_f32 v36,v36,v37\nv_cvt_pk_bf16_f32 v37,v38,v39\nM v[100:103],v[196:199],a[252:255],v[100:103]\n" \
"v_accvgpr_write_b32 a145,v5\ns_waitcnt lgkmcnt(4)\nM v[40:43],v[200:203],a[224:227],v[40:43]\nv_cvt_pk_bf16_f32 v96,v96,v97\nv_cvt_pk_bf16_f32 v97,v98,v99\nM v[40:43],v[204:207],a[240:243],v[40:43]\nv_accvgpr_write_b32 a146,v6\nv_accvgpr_write_b32 a147,v7\n" \
"M v[44:47],v[200:203],a[228:231],v[44:47]\nv_cvt_pk_bf16_f32 v100,v100,v101\nv_cvt_pk_bf16_f32 v101,v102,v103\nM v[44:47],v[204:207],a[244:247],v[44:47]\nv_accvgpr_write_b32 a160,v64\nv_accvgpr_write_b32 a161,v65\nM v[104:107],v[200:203],a[232:235],v[104:107]\nv_cvt_pk_bf16_f32 v34,v40,v41\n" \
"v_cvt_pk_bf16_f32 v35,v42,v43\nM v[104:107],v[204:207],a[248:251],v[104:107]\nv_accvgpr_write_b32 a162,v66\nv_accvgpr_write_b32 a163,v67\nM v[108:111],v[200:203],a[236:239],v[108:111]\nv_cvt_pk_bf16_f32 v38,v44,v45\nv_cvt_pk_bf16_f32 v39,v46,v47\nM v[108:111],v[204:207],a[252:255],v[108:111]\n" \
"v_accvgpr_write_b32 a176,v68\nv_accvgpr_write_b32 a177,v69\nM v[48:51],v[208:211],a[224:227],v[48:51]\nv_cvt_pk_bf16_f32 v98,v104,v105\nv_cvt_pk_bf16_f32 v99,v106,v107\nM v[48:51],v[212:215],a[240:243],v[48:51]\nv_accvgpr_write_b32 a178,v70\nv_accvgpr_write_b32 a179,v71\n" \
"M v[52:55],v[208:211],a[228:231],v[52:55]\nv_cvt_pk_bf16_f32 v102,v108,v109\nv_cvt_pk_bf16_f32 v103,v110,v111\nM v[52:55],v[212:215],a[244:247],v[52:55]\nv_accvgpr_write_b32 a132,v16\nv_accvgpr_write_b32 a133,v17\nM v[112:115],v[208:211],a[232:235],v[112:115]\nv_cvt_pk_bf16_f32 v48,v48,v49\n" \
"v_cvt_pk_bf16_f32 v49,v50,v51\nM v[112:115],v[212:215],a[248:251],v[112:115]\nv_accvgpr_write_b32 a134,v18\nv_accvgpr_write_b32 a135,v19\nM v[116:119],v[208:211],a[236:239],v[116:119]\nv_cvt_pk_bf16_f32 v52,v52,v53\nv_cvt_pk_bf16_f32 v53,v54,v55\nM v[116:119],v[212:215],a[252:255],v[116:119]\n" \
"v_accvgpr_write_b32 a148,v20\ns_waitcnt lgkmcnt(0)\nM v[56:59],v[216:219],a[224:227],v[56:59]\nv_cvt_pk_bf16_f32 v112,v112,v113\nv_cvt_pk_bf16_f32 v113,v114,v115\nM v[56:59],v[220:223],a[240:243],v[56:59]\nv_accvgpr_write_b32 a149,v21\nv_accvgpr_write_b32 a150,v22\n" \
"M v[60:63],v[216:219],a[228:231],v[60:63]\nv_cvt_pk_bf16_f32 v116,v116,v117\nv_cvt_pk_bf16_f32 v117,v118,v119\nM v[60:63],v[220:223],a[244:247],v[60:63]\nv_accvgpr_write_b32 a244,0\nv_accvgpr_write_b32 a245,0\nM v[120:123],v[216:219],a[232:235],v[120:123]\nv_cvt_pk_bf16_f32 v50,v56,v57\n" \
"v_cvt_pk_bf16_f32 v51,v58,v59\nM v[120:123],v[220:223],a[248:251],v[120:123]\nv_accvgpr_write_b32 a248,-1\nv_accvgpr_write_b32 a249,-1\nM v[124:127],v[216:219],a[236:239],v[124:127]\nv_cvt_pk_bf16_f32 v54,v60,v61\nv_cvt_pk_bf16_f32 v55,v62,v63\nM v[124:127],v[220:223],a[252:255],v[124:127]\n" \
"s_waitcnt vmcnt(0)\nbuffer_store_dword v252,off,s[52:55],s29 sc1\nv_cvt_pk_bf16_f32 v114,v120,v121\nv_cvt_pk_bf16_f32 v115,v122,v123\nv_accvgpr_write_b32 a246,0\nv_accvgpr_write_b32 a250,-1\nv_accvgpr_write_b32 a247,0\nv_accvgpr_write_b32 a251,-1\n" \
"v_cvt_pk_bf16_f32 v118,v124,v125\nv_cvt_pk_bf16_f32 v119,v126,v127\nv_accvgpr_write_b32 a151,v23\nv_accvgpr_write_b32 a164,v80\nv_accvgpr_write_b32 a165,v81\nv_accvgpr_write_b32 a166,v82\nv_accvgpr_write_b32 a167,v83\nv_accvgpr_write_b32 a180,v84\n" \
"v_accvgpr_write_b32 a181,v85\nv_accvgpr_write_b32 a182,v86\nv_accvgpr_write_b32 a183,v87\nv_accvgpr_write_b32 a136,v32\nv_accvgpr_write_b32 a137,v33\nv_accvgpr_write_b32 a138,v34\nv_accvgpr_write_b32 a139,v35\nv_accvgpr_write_b32 a152,v36\n" \
"v_accvgpr_write_b32 a153,v37\nv_accvgpr_write_b32 a154,v38\nv_accvgpr_write_b32 a155,v39\nv_accvgpr_write_b32 a168,v96\nv_accvgpr_write_b32 a169,v97\nv_accvgpr_write_b32 a170,v98\nv_accvgpr_write_b32 a171,v99\nv_accvgpr_write_b32 a184,v100\n" \
"v_accvgpr_write_b32 a185,v101\nv_accvgpr_write_b32 a186,v102\nv_accvgpr_write_b32 a187,v103\nv_accvgpr_write_b32 a140,v48\nv_accvgpr_write_b32 a141,v49\nv_accvgpr_write_b32 a142,v50\nv_accvgpr_write_b32 a143,v51\nv_accvgpr_write_b32 a156,v52\n" \
"v_accvgpr_write_b32 a157,v53\nv_accvgpr_write_b32 a158,v54\nv_accvgpr_write_b32 a159,v55\nv_accvgpr_write_b32 a172,v112\nv_accvgpr_write_b32 a173,v113\nv_accvgpr_write_b32 a174,v114\nv_accvgpr_write_b32 a175,v115\nv_accvgpr_write_b32 a188,v116\n" \
"v_accvgpr_write_b32 a189,v117\nv_accvgpr_write_b32 a190,v118\nv_accvgpr_write_b32 a191,v119\ns_waitcnt lgkmcnt(0)\n.Lattn:\ns_cmp_lt_u32 s7,2\ns_cbranch_scc0 .Lp23\ns_cmp_eq_u32 s7,0\n" \
"s_cbranch_scc0 .Lpro1\n.Lpro0:\ns_barrier\nM v[64:67],a[0:3],a[128:131],0\nds_read_b128 a[32:35],v240 offset:40960\nds_read_b128 a[36:39],v240 offset:41984\nM v[64:67],a[4:7],a[132:135],v[64:67]\nds_read_b128 a[40:43],v240 offset:43008\n" \
"s_mov_b32 m0,s64\nv_mov_b32 v218,0\nM v[64:67],a[8:11],a[136:139],v[64:67]\nds_read_b128 a[44:47],v240 offset:44032\nv_mov_b32 v219,0\nM v[64:67],a[12:15],a[140:143],v[64:67]\nds_read_b128 a[48:51],v240 offset:45056\nbuffer_load_dwordx4 v240,s[44:47],s36 offen offset:0 sc1 lds\n" \
"M v[68:71],a[12:15],a[156:159],0\nds_read_b128 a[12:15],v240 offset:35840\nM v[68:71],a[8:11],a[152:155],v[68:71]\nds_read_b128 a[8:11],v240 offset:34816\nM v[68:71],a[4:7],a[148:151],v[68:71]\nds_read_b128 a[4:7],v240 offset:33792\nv_max3_f32 v225,v64,v65,v66\nM v[68:71],a[0:3],a[144:147],v[68:71]\n" \
"ds_read_b128 a[0:3],v240 offset:32768\nv_max_f32 v225,v225,v67\nv_cndmask_b32 v225,v224,v225,s[90:91]\nM v[76:79],a[28:31],a[156:159],0\nds_read_b128 a[28:31],v240 offset:39936\nv_cndmask_b32 v64,v64,v252,s[82:83]\nM v[76:79],a[24:27],a[152:155],v[76:79]\nds_read_b128 a[24:27],v240 offset:38912\n" \
"v_max3_f32 v229,v68,v69,v70\nv_cndmask_b32 v65,v65,v252,s[84:85]\nM v[76:79],a[20:23],a[148:151],v[76:79]\nds_read_b128 a[20:23],v240 offset:37888\nv_cndmask_b32 v66,v66,v252,s[86:87]\nM v[76:79],a[16:19],a[144:147],v[76:79]\nds_read_b128 a[16:19],v240 offset:36864\nv_cndmask_b32 v67,v67,v252,s[88:89]\n" \
"s_waitcnt lgkmcnt(4)\nM v[136:139],a[0:3],a[160:163],0\nbuffer_load_dwordx4 v240,s[44:47],s36 offen offset:1024 sc1 lds\nM v[136:139],a[4:7],a[164:167],v[136:139]\nds_read_b128 a[52:55],v240 offset:46080\nds_read_b128 a[56:59],v240 offset:47104\nv_max3_f32 v229,v229,v71,v76\nM v[136:139],a[8:11],a[168:171],v[136:139]\n" \
"v_max3_f32 v229,v229,v77,v78\nv_max_f32 v229,v229,v79\nv_cndmask_b32 v229,v228,v229,s[90:91]\nM v[136:139],a[12:15],a[172:175],v[136:139]\nds_read_b128 a[60:63],v240 offset:48128\nv_permlane16_swap_b32 v225,v229\nM v[140:143],a[12:15],a[188:191],0\nv_max_f32 v225,v225,v229\n" \
"v_mov_b32 v229,v225\nds_read_b128 a[64:67],v241 offset:0\nM v[140:143],a[8:11],a[184:187],v[140:143]\nv_permlane32_swap_b32 v225,v229\nv_max_f32 v225,v225,v229\nM v[140:143],a[4:7],a[180:183],v[140:143]\nv_mov_b32 v229,v225\nv_max3_f32 v233,v136,v137,v138\n" \
"ds_read_b128 a[68:71],v241 offset:1024\nM v[140:143],a[0:3],a[176:179],v[140:143]\nv_permlane16_swap_b32 v225,v229\ns_waitcnt lgkmcnt(5)\nM v[152:155],a[16:19],a[160:163],0\nv_sub_f32 v224,0,v225\nv_sub_f32 v226,0,v225\nv_sub_f32 v227,0,v225\n" \
"M v[152:155],a[20:23],a[164:167],v[152:155]\nv_sub_f32 v225,0,v225\nv_sub_f32 v228,0,v229\nM v[152:155],a[24:27],a[168:171],v[152:155]\nv_sub_f32 v230,0,v229\nv_sub_f32 v231,0,v229\nv_sub_f32 v229,0,v229\nM v[152:155],a[28:31],a[172:175],v[152:155]\n" \
"v_max3_f32 v237,v140,v141,v142\nds_read_b128 a[72:75],v241 offset:2048\nM v[156:159],a[28:31],a[188:191],0\nbuffer_load_dwordx4 v240,s[44:47],s36 offen offset:2048 sc1 lds\nds_read_b128 a[76:79],v241 offset:3072\nM v[156:159],a[24:27],a[184:187],v[156:159]\nds_read_b128 a[80:83],v241 offset:4096\nds_read_b128 a[84:87],v241 offset:5120\n" \
"M v[156:159],a[20:23],a[180:183],v[156:159]\nv_max3_f32 v233,v233,v139,v152\nv_max3_f32 v233,v233,v153,v154\nv_max_f32 v233,v233,v155\nM v[156:159],a[16:19],a[176:179],v[156:159]\nv_cndmask_b32 v233,v232,v233,s[90:91]\nds_read_b128 a[88:91],v241 offset:6144\nv_add_f32 v64,v64,v224\n" \
"M v[128:131],a[0:3],a[128:131],v[224:227]\nbuffer_load_dwordx4 v240,s[44:47],s36 offen offset:3072 sc1 lds\nM v[128:131],a[4:7],a[132:135],v[128:131]\nv_add_f32 v65,v65,v224\nv_add_f32 v66,v66,v224\nv_max3_f32 v237,v237,v143,v156\nM v[128:131],a[8:11],a[136:139],v[128:131]\nv_max3_f32 v237,v237,v157,v158\n" \
"v_max_f32 v237,v237,v159\nM v[128:131],a[12:15],a[140:143],v[128:131]\nv_cndmask_b32 v237,v236,v237,s[90:91]\nv_add_f32 v67,v67,v224\nds_read_b128 a[92:95],v241 offset:7168\nM v[132:135],a[12:15],a[156:159],v[228:231]\nv_permlane16_swap_b32 v233,v237\nv_max_f32 v233,v233,v237\n" \
"M v[132:135],a[8:11],a[152:155],v[132:135]\nv_mov_b32 v237,v233\nv_exp_f32 v64,v64\nM v[132:135],a[4:7],a[148:151],v[132:135]\nv_permlane32_swap_b32 v233,v237\nv_max_f32 v233,v233,v237\nM v[132:135],a[0:3],a[144:147],v[132:135]\nv_mov_b32 v237,v233\n" \
"v_exp_f32 v65,v65\nM v[144:147],a[16:19],a[128:131],v[224:227]\nv_permlane16_swap_b32 v233,v237\nv_sub_f32 v232,0,v233\nM v[144:147],a[20:23],a[132:135],v[144:147]\nv_sub_f32 v234,0,v233\nv_sub_f32 v235,0,v233\nv_sub_f32 v233,0,v233\n" \
"M v[144:147],a[24:27],a[136:139],v[144:147]\nv_sub_f32 v236,0,v237\nv_sub_f32 v238,0,v237\nM v[144:147],a[28:31],a[140:143],v[144:147]\nv_sub_f32 v239,0,v237\nv_sub_f32 v237,0,v237\nv_cvt_pk_bf16_f32 v64,v64,v65\nM v[148:151],a[28:31],a[156:159],v[228:231]\n" \
"v_exp_f32 v66,v66\nv_add_f32 v68,v68,v228\nM v[148:151],a[24:27],a[152:155],v[148:151]\nv_exp_f32 v67,v67\nM v[148:151],a[20:23],a[148:151],v[148:151]\nv_cvt_pk_bf16_f32 v65,v66,v67\nv_mov_b32 v66,0\nv_mov_b32 v67,0\n" \
"M v[148:151],a[16:19],a[144:147],v[148:151]\nv_add_f32 v69,v69,v228\nv_cndmask_b32 v76,v76,v252,s[82:83]\nM v[160:163],a[32:35],a[128:131],v[224:227]\nv_cndmask_b32 v77,v77,v252,s[84:85]\nv_cndmask_b32 v78,v78,v252,s[86:87]\nv_cndmask_b32 v79,v79,v252,s[88:89]\nM v[160:163],a[36:39],a[132:135],v[160:163]\n" \
"v_add_f32 v70,v70,v228\nv_add_f32 v71,v71,v228\nM v[160:163],a[40:43],a[136:139],v[160:163]\nv_exp_f32 v68,v68\nv_add_f32 v76,v76,v228\nM v[160:163],a[44:47],a[140:143],v[160:163]\nv_exp_f32 v69,v69\nM v[164:167],a[44:47],a[156:159],v[228:231]\n" \
"v_exp_f32 v70,v70\nv_add_f32 v77,v77,v228\nM v[164:167],a[40:43],a[152:155],v[164:167]\nv_exp_f32 v71,v71\nM v[164:167],a[36:39],a[148:151],v[164:167]\nv_add_f32 v78,v78,v228\nv_add_f32 v79,v79,v228\nv_cvt_pk_bf16_f32 v68,v68,v69\n" \
"M v[164:167],a[32:35],a[144:147],v[164:167]\nv_exp_f32 v76,v76\nM v[176:179],a[48:51],a[128:131],v[224:227]\nv_exp_f32 v77,v77\ns_waitcnt lgkmcnt(0)\nM v[176:179],a[52:55],a[132:135],v[176:179]\nv_exp_f32 v78,v78\nv_cvt_pk_bf16_f32 v69,v70,v71\n" \
"M v[176:179],a[56:59],a[136:139],v[176:179]\nv_exp_f32 v79,v79\nM v[176:179],a[60:63],a[140:143],v[176:179]\nv_cvt_pk_bf16_f32 v70,v76,v77\nv_cvt_pk_bf16_f32 v71,v78,v79\nds_read_b128 a[0:3],v240 offset:49152\nM v[180:183],a[60:63],a[156:159],v[228:231]\nds_read_b128 a[4:7],v240 offset:50176\n" \
"ds_read_b128 a[8:11],v240 offset:51200\nM v[180:183],a[56:59],a[152:155],v[180:183]\nds_read_b128 a[12:15],v240 offset:52224\nv_exp_f32 v128,v128\nM v[180:183],a[52:55],a[148:151],v[180:183]\nds_read_b128 a[16:19],v240 offset:53248\nv_add_f32 v136,v136,v232\nM v[180:183],a[48:51],a[144:147],v[180:183]\n" \
"v_exp_f32 v129,v129\nds_read_b128 a[20:23],v240 offset:54272\nM v[168:171],a[32:35],a[160:163],v[232:235]\nv_exp_f32 v130,v130\nM v[168:171],a[36:39],a[164:167],v[168:171]\nv_exp_f32 v131,v131\nv_cvt_pk_bf16_f32 v192,v128,v129\nM v[168:171],a[40:43],a[168:171],v[168:171]\n" \
"v_cvt_pk_bf16_f32 v193,v130,v131\nds_read_b128 a[24:27],v240 offset:55296\nM v[168:171],a[44:47],a[172:175],v[168:171]\nds_read_b128 a[28:31],v240 offset:56320\nv_exp_f32 v132,v132\nM v[172:175],a[44:47],a[188:191],v[236:239]\nv_exp_f32 v133,v133\nM v[172:175],a[40:43],a[184:187],v[172:175]\n" \
"v_exp_f32 v134,v134\nv_cvt_pk_bf16_f32 v196,v132,v133\nM v[172:175],a[36:39],a[180:183],v[172:175]\nbuffer_load_dword v248,v251,s[52:55],s80 offen sc1\nv_add_f32 v137,v137,v232\nM v[172:175],a[32:35],a[176:179],v[172:175]\nv_exp_f32 v135,v135\nM v[188:191],a[60:63],a[188:191],v[236:239]\n" \
"v_cvt_pk_bf16_f32 v197,v134,v135\nv_exp_f32 v144,v144\nM v[188:191],a[56:59],a[184:187],v[188:191]\nv_exp_f32 v145,v145\nM v[188:191],a[52:55],a[180:183],v[188:191]\nv_exp_f32 v146,v146\nv_cvt_pk_bf16_f32 v194,v144,v145\nM v[188:191],a[48:51],a[176:179],v[188:191]\n" \
"v_exp_f32 v147,v147\nM v[0:3],a[64:67],v[64:67],0\nv_cvt_pk_bf16_f32 v195,v146,v147\nv_exp_f32 v148,v148\nM v[8:11],a[68:71],v[64:67],0\nv_exp_f32 v149,v149\nM v[16:19],a[72:75],v[64:67],0\nv_exp_f32 v150,v150\n" \
"v_cvt_pk_bf16_f32 v198,v148,v149\nM v[24:27],a[76:79],v[64:67],0\nv_exp_f32 v151,v151\nM v[32:35],a[80:83],v[64:67],0\nv_cvt_pk_bf16_f32 v199,v150,v151\nv_add_f32 v138,v138,v232\nv_add_f32 v139,v139,v232\nM v[40:43],a[84:87],v[64:67],0\n" \
"v_exp_f32 v136,v136\nM v[48:51],a[88:91],v[64:67],0\nv_exp_f32 v137,v137\nv_add_f32 v140,v140,v236\nM v[56:59],a[92:95],v[64:67],0\nv_exp_f32 v138,v138\nv_cvt_pk_bf16_f32 v200,v136,v137\nM a[192:195],a[208:211],v[64:67],0\n" \
"v_exp_f32 v139,v139\nM v[4:7],a[64:67],v[68:71],0\nv_cvt_pk_bf16_f32 v201,v138,v139\nv_add_f32 v141,v141,v236\nv_add_f32 v142,v142,v236\nM v[12:15],a[68:71],v[68:71],0\nv_add_f32 v143,v143,v236\nds_read_b128 a[32:35],v240 offset:57344\n" \
"M v[20:23],a[72:75],v[68:71],0\nv_exp_f32 v140,v140\nv_add_f32 v152,v152,v232\nM v[28:31],a[76:79],v[68:71],0\nv_exp_f32 v141,v141\nM v[36:39],a[80:83],v[68:71],0\nv_exp_f32 v142,v142\nv_cvt_pk_bf16_f32 v204,v140,v141\n" \
"M v[44:47],a[84:87],v[68:71],0\nv_exp_f32 v143,v143\nM v[52:55],a[88:91],v[68:71],0\nv_cvt_pk_bf16_f32 v205,v142,v143\nv_add_f32 v153,v153,v232\nv_add_f32 v154,v154,v232\nM v[60:63],a[92:95],v[68:71],0\nv_add_f32 v155,v155,v232\n" \
"ds_read_b128 a[36:39],v240 offset:58368\nM a[196:199],a[208:211],v[68:71],0\nv_exp_f32 v152,v152\ns_waitcnt lgkmcnt(2)\nM v[128:131],a[0:3],a[128:131],v[224:227]\nv_exp_f32 v153,v153\nM v[128:131],a[4:7],a[132:135],v[128:131]\nv_exp_f32 v154,v154\n" \
"v_cvt_pk_bf16_f32 v202,v152,v153\nM v[128:131],a[8:11],a[136:139],v[128:131]\nv_exp_f32 v155,v155\nM v[128:131],a[12:15],a[140:143],v[128:131]\nv_cvt_pk_bf16_f32 v203,v154,v155\nds_read_b128 a[40:43],v240 offset:59392\nds_read_b128 a[44:47],v240 offset:60416\nM v[132:135],a[12:15],a[156:159],v[228:231]\n" \
"v_add_f32 v156,v156,v236\nv_add_f32 v157,v157,v236\nv_add_f32 v158,v158,v236\nM v[132:135],a[8:11],a[152:155],v[132:135]\nv_add_f32 v159,v159,v236\nds_read_b128 a[48:51],v240 offset:61440\nM v[132:135],a[4:7],a[148:151],v[132:135]\nv_exp_f32 v156,v156\n" \
"ds_read_b128 a[52:55],v240 offset:62464\nM v[132:135],a[0:3],a[144:147],v[132:135]\nv_exp_f32 v157,v157\nM v[144:147],a[16:19],a[128:131],v[224:227]\nv_exp_f32 v158,v158\nv_cvt_pk_bf16_f32 v206,v156,v157\nM v[144:147],a[20:23],a[132:135],v[144:147]\nv_exp_f32 v159,v159\n" \
"M v[144:147],a[24:27],a[136:139],v[144:147]\nv_cvt_pk_bf16_f32 v207,v158,v159\nv_exp_f32 v160,v160\nM v[144:147],a[28:31],a[140:143],v[144:147]\nv_exp_f32 v161,v161\nM v[148:151],a[28:31],a[156:159],v[228:231]\nv_exp_f32 v162,v162\nv_cvt_pk_bf16_f32 v208,v160,v161\n" \
"M v[148:151],a[24:27],a[152:155],v[148:151]\nv_exp_f32 v163,v163\nM v[148:151],a[20:23],a[148:151],v[148:151]\nv_cvt_pk_bf16_f32 v209,v162,v163\nds_read_b128 a[56:59],v240 offset:63488\nds_read_b128 a[60:63],v240 offset:64512\nM v[148:151],a[16:19],a[144:147],v[148:151]\nv_exp_f32 v164,v164\n" \
"M v[136:139],a[0:3],a[160:163],v[232:235]\nv_exp_f32 v165,v165\nv_cndmask_b32 v168,v168,v252,s[82:83]\nM v[136:139],a[4:7],a[164:167],v[136:139]\nv_exp_f32 v166,v166\nv_cvt_pk_bf16_f32 v212,v164,v165\nM v[136:139],a[8:11],a[168:171],v[136:139]\nv_exp_f32 v167,v167\n" \
"M v[136:139],a[12:15],a[172:175],v[136:139]\nv_cvt_pk_bf16_f32 v213,v166,v167\nv_exp_f32 v176,v176\nM v[140:143],a[12:15],a[188:191],v[236:239]\nv_exp_f32 v177,v177\nM v[140:143],a[8:11],a[184:187],v[140:143]\nv_exp_f32 v178,v178\nv_cvt_pk_bf16_f32 v210,v176,v177\n" \
"M v[140:143],a[4:7],a[180:183],v[140:143]\nv_exp_f32 v179,v179\nM v[140:143],a[0:3],a[176:179],v[140:143]\nv_cvt_pk_bf16_f32 v211,v178,v179\nv_exp_f32 v180,v180\nM v[152:155],a[16:19],a[160:163],v[232:235]\nv_exp_f32 v181,v181\nM v[152:155],a[20:23],a[164:167],v[152:155]\n" \
"v_exp_f32 v182,v182\nv_cvt_pk_bf16_f32 v214,v180,v181\nM v[152:155],a[24:27],a[168:171],v[152:155]\ns_waitcnt vmcnt(1)\ns_barrier\nM v[152:155],a[28:31],a[172:175],v[152:155]\nv_exp_f32 v183,v183\nv_cndmask_b32 v169,v169,v252,s[84:85]\n" \
"M v[156:159],a[28:31],a[188:191],v[236:239]\nv_cvt_pk_bf16_f32 v215,v182,v183\nv_cndmask_b32 v170,v170,v252,s[86:87]\nM v[156:159],a[24:27],a[184:187],v[156:159]\nv_cndmask_b32 v171,v171,v252,s[88:89]\nv_exp_f32 v168,v168\nM v[156:159],a[20:23],a[180:183],v[156:159]\nv_exp_f32 v169,v169\n" \
"ds_read_b128 a[64:67],v241 offset:32768\nM v[156:159],a[16:19],a[176:179],v[156:159]\nv_cvt_pk_bf16_f32 v216,v168,v169\ns_waitcnt lgkmcnt(1)\nM v[160:163],a[32:35],a[128:131],v[224:227]\nv_exp_f32 v170,v170\nds_read_b128 a[96:99],v241 offset:40960\nM v[160:163],a[36:39],a[132:135],v[160:163]\n" \
"v_exp_f32 v171,v171\nM v[160:163],a[40:43],a[136:139],v[160:163]\nv_cvt_pk_bf16_f32 v217,v170,v171\nv_exp_f32 v172,v172\nM v[160:163],a[44:47],a[140:143],v[160:163]\nv_exp_f32 v173,v173\nM v[164:167],a[44:47],a[156:159],v[228:231]\nv_exp_f32 v174,v174\n" \
"v_cvt_pk_bf16_f32 v220,v172,v173\nM v[164:167],a[40:43],a[152:155],v[164:167]\nv_exp_f32 v175,v175\nM v[164:167],a[36:39],a[148:151],v[164:167]\nv_cvt_pk_bf16_f32 v221,v174,v175\nds_read_b128 a[68:71],v241 offset:33792\nds_read_b128 a[100:103],v241 offset:41984\nM v[164:167],a[32:35],a[144:147],v[164:167]\n" \
"ds_read_b128 a[72:75],v241 offset:34816\nv_cndmask_b32 v188,v188,v252,s[82:83]\nM v[176:179],a[48:51],a[128:131],v[224:227]\nv_cndmask_b32 v189,v189,v252,s[84:85]\nv_cndmask_b32 v190,v190,v252,s[86:87]\nv_cndmask_b32 v191,v191,v252,s[88:89]\nM v[176:179],a[52:55],a[132:135],v[176:179]\nv_exp_f32 v188,v188\n" \
"M v[176:179],a[56:59],a[136:139],v[176:179]\nv_exp_f32 v189,v189\nds_read_b128 a[104:107],v241 offset:43008\nM v[176:179],a[60:63],a[140:143],v[176:179]\nv_exp_f32 v190,v190\nv_cvt_pk_bf16_f32 v222,v188,v189\nM v[180:183],a[60:63],a[156:159],v[228:231]\nv_exp_f32 v191,v191\n" \
"M v[180:183],a[56:59],a[152:155],v[180:183]\nv_cvt_pk_bf16_f32 v223,v190,v191\nds_read_b128 a[76:79],v241 offset:35840\nds_read_b128 a[108:111],v241 offset:44032\nM v[180:183],a[52:55],a[148:151],v[180:183]\nds_read_b128 a[80:83],v241 offset:36864\ns_waitcnt vmcnt(0)\nv_cmp_eq_u32 vcc,0,v248\n" \
"M v[180:183],a[48:51],a[144:147],v[180:183]\nds_read_b128 a[112:115],v241 offset:45056\ns_cbranch_vccnz .Lps2\n.Lpb2:\ns_add_u32 m0,s64,65536\nM v[168:171],a[32:35],a[160:163],v[232:235]\nbuffer_load_dwordx4 v240,s[48:51],s36 offen offset:0 sc1 lds\nM v[168:171],a[36:39],a[164:167],v[168:171]\n" \
"ds_read_b128 a[84:87],v241 offset:37888\nv_exp_f32 v128,v128\nM v[168:171],a[40:43],a[168:171],v[168:171]\nds_read_b128 a[116:119],v241 offset:46080\nds_read_b128 a[88:91],v241 offset:38912\nM v[168:171],a[44:47],a[172:175],v[168:171]\nds_read_b128 a[120:123],v241 offset:47104\nv_exp_f32 v129,v129\n" \
"M v[172:175],a[44:47],a[188:191],v[236:239]\nbuffer_load_dwordx4 v240,s[48:51],s36 offen offset:1024 sc1 lds\nM v[172:175],a[40:43],a[184:187],v[172:175]\nds_read_b128 a[92:95],v241 offset:39936\nv_exp_f32 v130,v130\nM v[172:175],a[36:39],a[180:183],v[172:175]\nds_read_b128 a[124:127],v241 offset:48128\nds_read_b128 a[0:3],v240 offset:0\n" \
"M v[172:175],a[32:35],a[176:179],v[172:175]\nds_read_b128 a[4:7],v240 offset:1024\nv_exp_f32 v131,v131\nM v[184:187],a[48:51],a[160:163],v[232:235]\nds_read_b128 a[8:11],v240 offset:2048\nbuffer_load_dwordx4 v240,s[48:51],s36 offen offset:2048 sc1 lds\nM v[184:187],a[52:55],a[164:167],v[184:187]\nv_exp_f32 v132,v132\n" \
"M v[184:187],a[56:59],a[168:171],v[184:187]\nds_read_b128 a[12:15],v240 offset:3072\nv_exp_f32 v133,v133\nM v[184:187],a[60:63],a[172:175],v[184:187]\nds_read_b128 a[16:19],v240 offset:4096\nds_read_b128 a[20:23],v240 offset:5120\nM v[188:191],a[60:63],a[188:191],v[236:239]\nds_read_b128 a[24:27],v240 offset:6144\n" \
"buffer_load_dwordx4 v240,s[48:51],s36 offen offset:3072 sc1 lds\nM v[188:191],a[56:59],a[184:187],v[188:191]\nds_read_b128 a[28:31],v240 offset:7168\ns_add_u32 m0,s64,16384\nM v[188:191],a[52:55],a[180:183],v[188:191]\nds_read_b128 a[32:35],v240 offset:8192\nv_exp_f32 v134,v134\nM v[188:191],a[48:51],a[176:179],v[188:191]\n" \
"ds_read_b128 a[36:39],v240 offset:9216\ns_waitcnt lgkmcnt(9)\nM v[0:3],a[64:67],v[192:195],v[0:3]\nds_read_b128 a[40:43],v240 offset:10240\nbuffer_load_dwordx4 v240,s[44:47],s65 offen offset:0 sc1 lds\nM v[0:3],a[96:99],v[208:211],v[0:3]\nv_exp_f32 v135,v135\nM v[8:11],a[68:71],v[192:195],v[8:11]\n" \
"ds_read_b128 a[44:47],v240 offset:11264\nv_exp_f32 v144,v144\nM v[8:11],a[100:103],v[208:211],v[8:11]\nds_read_b128 a[48:51],v240 offset:12288\nds_read_b128 a[52:55],v240 offset:13312\nM v[16:19],a[72:75],v[192:195],v[16:19]\nbuffer_load_dwordx4 v240,s[44:47],s65 offen offset:1024 sc1 lds\nds_read_b128 a[56:59],v240 offset:14336\n" \
"M v[16:19],a[104:107],v[208:211],v[16:19]\nds_read_b128 a[60:63],v240 offset:15360\nv_exp_f32 v145,v145\nM v[24:27],a[76:79],v[192:195],v[24:27]\nv_exp_f32 v146,v146\nM v[24:27],a[108:111],v[208:211],v[24:27]\nv_exp_f32 v147,v147\nM v[32:35],a[80:83],v[192:195],v[32:35]\n" \
"buffer_load_dwordx4 v240,s[44:47],s65 offen offset:2048 sc1 lds\nM v[32:35],a[112:115],v[208:211],v[32:35]\nv_exp_f32 v148,v148\nM v[40:43],a[84:87],v[192:195],v[40:43]\nv_exp_f32 v149,v149\nM v[40:43],a[116:119],v[208:211],v[40:43]\nv_exp_f32 v150,v150\nM v[48:51],a[88:91],v[192:195],v[48:51]\n" \
"buffer_load_dwordx4 v240,s[44:47],s65 offen offset:3072 sc1 lds\nM v[48:51],a[120:123],v[208:211],v[48:51]\ns_add_u32 m0,s64,81920\nv_exp_f32 v151,v151\nM v[56:59],a[92:95],v[192:195],v[56:59]\nv_exp_f32 v136,v136\nM v[56:59],a[124:127],v[208:211],v[56:59]\nv_exp_f32 v137,v137\n" \
"M a[192:195],a[208:211],v[192:195],a[192:195]\nbuffer_load_dwordx4 v240,s[48:51],s65 offen offset:0 sc1 lds\nM a[192:195],a[208:211],v[208:211],a[192:195]\nv_exp_f32 v138,v138\nv_cvt_pk_bf16_f32 v192,v128,v129\nM v[4:7],a[64:67],v[196:199],v[4:7]\nv_exp_f32 v139,v139\nM v[4:7],a[96:99],v[212:215],v[4:7]\n" \
"v_exp_f32 v152,v152\nv_cvt_pk_bf16_f32 v193,v130,v131\nM v[12:15],a[68:71],v[196:199],v[12:15]\nbuffer_load_dwordx4 v240,s[48:51],s65 offen offset:1024 sc1 lds\nv_cvt_pk_bf16_f32 v194,v144,v145\nM v[12:15],a[100:103],v[212:215],v[12:15]\nv_exp_f32 v153,v153\nM v[20:23],a[72:75],v[196:199],v[20:23]\n" \
"v_exp_f32 v154,v154\nv_cvt_pk_bf16_f32 v195,v146,v147\nM v[20:23],a[104:107],v[212:215],v[20:23]\nv_exp_f32 v155,v155\nM v[28:31],a[76:79],v[196:199],v[28:31]\nbuffer_load_dwordx4 v240,s[48:51],s65 offen offset:2048 sc1 lds\nM v[28:31],a[108:111],v[212:215],v[28:31]\nv_exp_f32 v160,v160\n" \
"M v[36:39],a[80:83],v[196:199],v[36:39]\ns_barrier\nv_exp_f32 v161,v161\nM v[36:39],a[112:115],v[212:215],v[36:39]\nv_exp_f32 v162,v162\nM v[44:47],a[84:87],v[196:199],v[44:47]\nbuffer_load_dwordx4 v240,s[48:51],s65 offen offset:3072 sc1 lds\ns_add_u32 m0,s64,32768\n" \
"M v[44:47],a[116:119],v[212:215],v[44:47]\nv_exp_f32 v163,v163\nM v[52:55],a[88:91],v[196:199],v[52:55]\nv_exp_f32 v176,v176\nv_cvt_pk_bf16_f32 v208,v160,v161\nM v[52:55],a[120:123],v[212:215],v[52:55]\nv_exp_f32 v177,v177\nM v[60:63],a[92:95],v[196:199],v[60:63]\n" \
"buffer_load_dwordx4 v240,s[44:47],s66 offen offset:0 sc1 lds\nv_cvt_pk_bf16_f32 v209,v162,v163\nM v[60:63],a[124:127],v[212:215],v[60:63]\nv_exp_f32 v178,v178\nv_cvt_pk_bf16_f32 v210,v176,v177\nM a[196:199],a[208:211],v[196:199],a[196:199]\nv_exp_f32 v179,v179\nM a[196:199],a[208:211],v[212:215],a[196:199]\n" \
"v_exp_f32 v140,v140\nv_cvt_pk_bf16_f32 v196,v132,v133\nM v[64:67],a[64:67],v[200:203],0\nbuffer_load_dwordx4 v240,s[44:47],s66 offen offset:1024 sc1 lds\nM v[64:67],a[96:99],v[216:219],v[64:67]\nv_exp_f32 v141,v141\nv_cvt_pk_bf16_f32 v197,v134,v135\nM v[72:75],a[68:71],v[200:203],0\n" \
"v_exp_f32 v142,v142\nM v[72:75],a[100:103],v[216:219],v[72:75]\nv_exp_f32 v143,v143\nv_cvt_pk_bf16_f32 v198,v148,v149\nM v[80:83],a[72:75],v[200:203],0\nbuffer_load_dwordx4 v240,s[44:47],s66 offen offset:2048 sc1 lds\nM v[80:83],a[104:107],v[216:219],v[80:83]\nv_exp_f32 v156,v156\n" \
"v_cvt_pk_bf16_f32 v199,v150,v151\nM v[88:91],a[76:79],v[200:203],0\nv_exp_f32 v157,v157\nM v[88:91],a[108:111],v[216:219],v[88:91]\nv_exp_f32 v158,v158\nv_cvt_pk_bf16_f32 v211,v178,v179\nM v[96:99],a[80:83],v[200:203],0\nbuffer_load_dwordx4 v240,s[44:47],s66 offen offset:3072 sc1 lds\n" \
"M v[96:99],a[112:115],v[216:219],v[96:99]\ns_add_u32 m0,s64,98304\nv_exp_f32 v159,v159\nM v[104:107],a[84:87],v[200:203],0\nv_exp_f32 v164,v164\nM v[104:107],a[116:119],v[216:219],v[104:107]\nv_exp_f32 v165,v165\nM v[112:115],a[88:91],v[200:203],0\n" \
"v_exp_f32 v166,v166\nM v[112:115],a[120:123],v[216:219],v[112:115]\nv_exp_f32 v167,v167\nM v[120:123],a[92:95],v[200:203],0\nv_exp_f32 v180,v180\nM v[120:123],a[124:127],v[216:219],v[120:123]\nv_exp_f32 v181,v181\nM a[200:203],a[208:211],v[200:203],0\n" \
"v_exp_f32 v182,v182\nv_cvt_pk_bf16_f32 v200,v136,v137\nM a[200:203],a[208:211],v[216:219],a[200:203]\nv_exp_f32 v183,v183\nM v[68:71],a[64:67],v[204:207],0\nv_exp_f32 v168,v168\nv_cvt_pk_bf16_f32 v201,v138,v139\nM v[68:71],a[96:99],v[220:223],v[68:71]\n" \
"v_exp_f32 v169,v169\nM v[76:79],a[68:71],v[204:207],0\nv_exp_f32 v170,v170\nv_cvt_pk_bf16_f32 v202,v152,v153\nM v[76:79],a[100:103],v[220:223],v[76:79]\nv_exp_f32 v171,v171\nM v[84:87],a[72:75],v[204:207],0\nv_cvt_pk_bf16_f32 v203,v154,v155\n" \
"buffer_load_dwordx4 v240,s[48:51],s66 offen offset:0 sc1 lds\nM v[84:87],a[104:107],v[220:223],v[84:87]\nM v[92:95],a[76:79],v[204:207],0\nM v[92:95],a[108:111],v[220:223],v[92:95]\nM v[100:103],a[80:83],v[204:207],0\nbuffer_load_dwordx4 v240,s[48:51],s66 offen offset:1024 sc1 lds\nM v[100:103],a[112:115],v[220:223],v[100:103]\nM v[108:111],a[84:87],v[204:207],0\n" \
"M v[108:111],a[116:119],v[220:223],v[108:111]\nM v[116:119],a[88:91],v[204:207],0\nbuffer_load_dwordx4 v240,s[48:51],s66 offen offset:2048 sc1 lds\nM v[116:119],a[120:123],v[220:223],v[116:119]\nM v[124:127],a[92:95],v[204:207],0\nM v[124:127],a[124:127],v[220:223],v[124:127]\nM a[204:207],a[208:211],v[204:207],0\nbuffer_load_dwordx4 v240,s[48:51],s66 offen offset:3072 sc1 lds\n" \
"M a[204:207],a[208:211],v[220:223],a[204:207]\ns_cmp_eq_u32 s62,0\ns_cbranch_scc1 .Ltrans\ns_branch .Ldual\n.Lpro1:\ns_barrier\nM v[64:67],a[0:3],a[128:131],0\nds_read_b128 a[64:67],v241 offset:0\n" \
"ds_read_b128 a[96:99],v241 offset:8192\nM v[64:67],a[4:7],a[132:135],v[64:67]\nds_read_b128 a[68:71],v241 offset:1024\ns_mov_b32 m0,s64\nv_mov_b32 v202,0\nM v[64:67],a[8:11],a[136:139],v[64:67]\nbuffer_load_dwordx4 v240,s[44:47],s36 offen offset:0 sc1 lds\nM v[64:67],a[12:15],a[140:143],v[64:67]\n" \
"ds_read_b128 a[100:103],v241 offset:9216\nds_read_b128 a[72:75],v241 offset:2048\nv_mov_b32 v203,0\nM v[68:71],a[12:15],a[156:159],0\nds_read_b128 a[12:15],v240 offset:35840\nM v[68:71],a[8:11],a[152:155],v[68:71]\nds_read_b128 a[8:11],v240 offset:34816\nM v[68:71],a[4:7],a[148:151],v[68:71]\n" \
"ds_read_b128 a[4:7],v240 offset:33792\nbuffer_load_dwordx4 v240,s[44:47],s36 offen offset:1024 sc1 lds\nM v[68:71],a[0:3],a[144:147],v[68:71]\nds_read_b128 a[0:3],v240 offset:32768\nv_max3_f32 v225,v64,v65,v66\nM v[72:75],a[16:19],a[128:131],0\nds_read_b128 a[104:107],v241 offset:10240\nM v[72:75],a[20:23],a[132:135],v[72:75]\n" \
"ds_read_b128 a[76:79],v241 offset:3072\nM v[72:75],a[24:27],a[136:139],v[72:75]\nbuffer_load_dwordx4 v240,s[44:47],s36 offen offset:2048 sc1 lds\nM v[72:75],a[28:31],a[140:143],v[72:75]\nv_max3_f32 v229,v68,v69,v70\nds_read_b128 a[108:111],v241 offset:11264\nds_read_b128 a[80:83],v241 offset:4096\nM v[76:79],a[28:31],a[156:159],0\n" \
"ds_read_b128 a[28:31],v240 offset:39936\nM v[76:79],a[24:27],a[152:155],v[76:79]\nds_read_b128 a[24:27],v240 offset:38912\nM v[76:79],a[20:23],a[148:151],v[76:79]\nds_read_b128 a[20:23],v240 offset:37888\nv_max3_f32 v225,v225,v67,v72\nv_max3_f32 v225,v225,v73,v74\nM v[76:79],a[16:19],a[144:147],v[76:79]\n" \
"ds_read_b128 a[16:19],v240 offset:36864\nv_max_f32 v225,v225,v75\nv_cndmask_b32 v225,v224,v225,s[90:91]\nM v[80:83],a[32:35],a[128:131],0\nbuffer_load_dwordx4 v240,s[44:47],s36 offen offset:3072 sc1 lds\nM v[80:83],a[36:39],a[132:135],v[80:83]\nds_read_b128 a[112:115],v241 offset:12288\nds_read_b128 a[84:87],v241 offset:5120\n" \
"v_max3_f32 v229,v229,v71,v76\nM v[80:83],a[40:43],a[136:139],v[80:83]\nv_max3_f32 v229,v229,v77,v78\nv_max_f32 v229,v229,v79\nv_cndmask_b32 v229,v228,v229,s[90:91]\nM v[80:83],a[44:47],a[140:143],v[80:83]\nds_read_b128 a[116:119],v241 offset:13312\nv_permlane16_swap_b32 v225,v229\n" \
"M v[84:87],a[44:47],a[156:159],0\nv_max_f32 v225,v225,v229\nv_mov_b32 v229,v225\nds_read_b128 a[44:47],v240 offset:44032\nM v[84:87],a[40:43],a[152:155],v[84:87]\nv_permlane32_swap_b32 v225,v229\nv_max_f32 v225,v225,v229\nM v[84:87],a[36:39],a[148:151],v[84:87]\n" \
"v_mov_b32 v229,v225\nds_read_b128 a[36:39],v240 offset:41984\nds_read_b128 a[40:43],v240 offset:43008\nM v[84:87],a[32:35],a[144:147],v[84:87]\nv_permlane16_swap_b32 v225,v229\nv_sub_f32 v224,0,v225\nv_sub_f32 v226,0,v225\nM v[92:95],a[60:63],a[156:159],0\n" \
"v_sub_f32 v227,0,v225\nv_sub_f32 v225,0,v225\nM v[92:95],a[56:59],a[152:155],v[92:95]\nv_sub_f32 v228,0,v229\nv_sub_f32 v230,0,v229\nv_sub_f32 v231,0,v229\nM v[92:95],a[52:55],a[148:151],v[92:95]\nv_sub_f32 v229,0,v229\n" \
"ds_read_b128 a[32:35],v240 offset:40960\nds_read_b128 a[52:55],v240 offset:46080\nM v[92:95],a[48:51],a[144:147],v[92:95]\nds_read_b128 a[48:51],v240 offset:45056\ns_waitcnt lgkmcnt(7)\nM v[136:139],a[0:3],a[160:163],0\nds_read_b128 a[56:59],v240 offset:47104\nv_add_f32 v64,v64,v224\n" \
"v_add_f32 v65,v65,v224\nM v[136:139],a[4:7],a[164:167],v[136:139]\nds_read_b128 a[60:63],v240 offset:48128\nv_add_f32 v66,v66,v224\nM v[136:139],a[8:11],a[168:171],v[136:139]\nv_add_f32 v67,v67,v224\nv_exp_f32 v64,v64\nM v[136:139],a[12:15],a[172:175],v[136:139]\n" \
"v_exp_f32 v65,v65\nv_cndmask_b32 v80,v80,v252,s[82:83]\nM v[140:143],a[12:15],a[188:191],0\nv_cndmask_b32 v81,v81,v252,s[84:85]\nv_add_f32 v72,v72,v224\nM v[140:143],a[8:11],a[184:187],v[140:143]\nv_exp_f32 v66,v66\nv_add_f32 v73,v73,v224\n" \
"M v[140:143],a[4:7],a[180:183],v[140:143]\nv_exp_f32 v67,v67\nv_add_f32 v74,v74,v224\nM v[140:143],a[0:3],a[176:179],v[140:143]\nv_add_f32 v75,v75,v224\nv_cndmask_b32 v82,v82,v252,s[86:87]\nM v[156:159],a[28:31],a[188:191],0\nv_cndmask_b32 v83,v83,v252,s[88:89]\n" \
"v_add_f32 v80,v80,v224\nv_add_f32 v81,v81,v224\nM v[156:159],a[24:27],a[184:187],v[156:159]\nv_exp_f32 v72,v72\nv_add_f32 v82,v82,v224\nM v[156:159],a[20:23],a[180:183],v[156:159]\nv_exp_f32 v73,v73\nM v[156:159],a[16:19],a[176:179],v[156:159]\n" \
"v_exp_f32 v74,v74\nv_add_f32 v83,v83,v224\nM v[128:131],a[0:3],a[128:131],v[224:227]\nv_exp_f32 v75,v75\nM v[128:131],a[4:7],a[132:135],v[128:131]\nv_exp_f32 v80,v80\nv_cvt_pk_bf16_f32 v64,v64,v65\nM v[128:131],a[8:11],a[136:139],v[128:131]\n" \
"v_exp_f32 v81,v81\nv_cvt_pk_bf16_f32 v65,v66,v67\nM v[128:131],a[12:15],a[140:143],v[128:131]\nv_exp_f32 v82,v82\nM v[132:135],a[12:15],a[156:159],v[228:231]\nv_exp_f32 v83,v83\nv_cvt_pk_bf16_f32 v66,v72,v73\nM v[132:135],a[8:11],a[152:155],v[132:135]\n" \
"v_cvt_pk_bf16_f32 v67,v74,v75\nv_cvt_pk_bf16_f32 v80,v80,v81\nv_cvt_pk_bf16_f32 v81,v82,v83\nM v[132:135],a[4:7],a[148:151],v[132:135]\nv_mov_b32 v82,0\nv_mov_b32 v83,0\nM v[132:135],a[0:3],a[144:147],v[132:135]\nds_read_b128 a[88:91],v241 offset:6144\n" \
"ds_read_b128 a[120:123],v241 offset:14336\nv_add_f32 v68,v68,v228\nM v[144:147],a[16:19],a[128:131],v[224:227]\nds_read_b128 a[92:95],v241 offset:7168\nv_add_f32 v69,v69,v228\nv_add_f32 v70,v70,v228\nM v[144:147],a[20:23],a[132:135],v[144:147]\nds_read_b128 a[124:127],v241 offset:15360\n" \
"v_add_f32 v71,v71,v228\nM v[144:147],a[24:27],a[136:139],v[144:147]\nv_exp_f32 v68,v68\nv_add_f32 v76,v76,v228\nM v[144:147],a[28:31],a[140:143],v[144:147]\nv_exp_f32 v69,v69\nM v[148:151],a[28:31],a[156:159],v[228:231]\nv_exp_f32 v70,v70\n" \
"v_add_f32 v77,v77,v228\nM v[148:151],a[24:27],a[152:155],v[148:151]\nv_exp_f32 v71,v71\nv_add_f32 v78,v78,v228\nM v[148:151],a[20:23],a[148:151],v[148:151]\nv_add_f32 v79,v79,v228\nv_add_f32 v84,v84,v228\nM v[148:151],a[16:19],a[144:147],v[148:151]\n" \
"v_add_f32 v85,v85,v228\nv_cndmask_b32 v92,v92,v252,s[82:83]\ns_waitcnt lgkmcnt(4)\nM v[160:163],a[32:35],a[128:131],v[224:227]\nv_exp_f32 v76,v76\nv_cndmask_b32 v93,v93,v252,s[84:85]\nM v[160:163],a[36:39],a[132:135],v[160:163]\nv_exp_f32 v77,v77\n" \
"M v[160:163],a[40:43],a[136:139],v[160:163]\nv_exp_f32 v78,v78\nv_cndmask_b32 v94,v94,v252,s[86:87]\nM v[160:163],a[44:47],a[140:143],v[160:163]\nv_exp_f32 v79,v79\nM v[164:167],a[44:47],a[156:159],v[228:231]\nv_cndmask_b32 v95,v95,v252,s[88:89]\nv_add_f32 v86,v86,v228\n" \
"v_add_f32 v87,v87,v228\nM v[164:167],a[40:43],a[152:155],v[164:167]\nv_exp_f32 v84,v84\nv_cvt_pk_bf16_f32 v68,v68,v69\nM v[164:167],a[36:39],a[148:151],v[164:167]\nv_exp_f32 v85,v85\nM v[164:167],a[32:35],a[144:147],v[164:167]\nv_exp_f32 v86,v86\n" \
"v_add_f32 v92,v92,v228\nM v[176:179],a[48:51],a[128:131],v[224:227]\nv_exp_f32 v87,v87\nv_add_f32 v93,v93,v228\nM v[176:179],a[52:55],a[132:135],v[176:179]\nv_add_f32 v94,v94,v228\nv_add_f32 v95,v95,v228\nM v[176:179],a[56:59],a[136:139],v[176:179]\n" \
"buffer_load_dword v248,v251,s[52:55],s80 offen sc1\nv_cvt_pk_bf16_f32 v69,v70,v71\nM v[176:179],a[60:63],a[140:143],v[176:179]\nv_exp_f32 v92,v92\nv_cvt_pk_bf16_f32 v70,v76,v77\nM v[180:183],a[60:63],a[156:159],v[228:231]\nv_exp_f32 v93,v93\nM v[180:183],a[56:59],a[152:155],v[180:183]\n" \
"v_exp_f32 v94,v94\nv_cvt_pk_bf16_f32 v71,v78,v79\nM v[180:183],a[52:55],a[148:151],v[180:183]\nv_exp_f32 v95,v95\nM v[180:183],a[48:51],a[144:147],v[180:183]\nv_cvt_pk_bf16_f32 v84,v84,v85\nv_cvt_pk_bf16_f32 v85,v86,v87\nv_cvt_pk_bf16_f32 v86,v92,v93\n" \
"M v[0:3],a[64:67],v[64:67],0\nv_cvt_pk_bf16_f32 v87,v94,v95\nds_read_b128 a[0:3],v240 offset:49152\nds_read_b128 a[4:7],v240 offset:50176\nM v[0:3],a[96:99],v[80:83],v[0:3]\nds_read_b128 a[8:11],v240 offset:51200\nv_max3_f32 v237,v140,v141,v142\nM v[8:11],a[68:71],v[64:67],0\n" \
"ds_read_b128 a[12:15],v240 offset:52224\nv_exp_f32 v128,v128\nM v[8:11],a[100:103],v[80:83],v[8:11]\nds_read_b128 a[16:19],v240 offset:53248\nv_exp_f32 v129,v129\nM v[16:19],a[72:75],v[64:67],0\nv_exp_f32 v130,v130\nM v[16:19],a[104:107],v[80:83],v[16:19]\n" \
"v_exp_f32 v131,v131\nds_read_b128 a[20:23],v240 offset:54272\nM v[24:27],a[76:79],v[64:67],0\nv_cvt_pk_bf16_f32 v192,v128,v129\nv_cvt_pk_bf16_f32 v193,v130,v131\nds_read_b128 a[24:27],v240 offset:55296\nM v[24:27],a[108:111],v[80:83],v[24:27]\nds_read_b128 a[28:31],v240 offset:56320\n" \
"v_max3_f32 v237,v237,v143,v156\nM v[32:35],a[80:83],v[64:67],0\nv_exp_f32 v132,v132\nv_max3_f32 v233,v136,v137,v138\nM v[32:35],a[112:115],v[80:83],v[32:35]\nv_exp_f32 v133,v133\nM v[40:43],a[84:87],v[64:67],0\nv_exp_f32 v134,v134\n" \
"v_cvt_pk_bf16_f32 v196,v132,v133\nM v[40:43],a[116:119],v[80:83],v[40:43]\nv_exp_f32 v135,v135\ns_waitcnt lgkmcnt(4)\nM v[48:51],a[88:91],v[64:67],0\nv_cvt_pk_bf16_f32 v197,v134,v135\nv_max3_f32 v237,v237,v157,v158\nM v[48:51],a[120:123],v[80:83],v[48:51]\n" \
"v_exp_f32 v144,v144\nv_max_f32 v233,v233,v139\nM v[56:59],a[92:95],v[64:67],0\nv_exp_f32 v145,v145\nv_max_f32 v237,v237,v159\nM v[56:59],a[124:127],v[80:83],v[56:59]\nv_exp_f32 v146,v146\nM a[192:195],a[208:211],v[64:67],0\n" \
"v_exp_f32 v147,v147\nv_cvt_pk_bf16_f32 v194,v144,v145\nM a[192:195],a[208:211],v[80:83],a[192:195]\nv_cvt_pk_bf16_f32 v195,v146,v147\nv_cndmask_b32 v233,v232,v233,s[90:91]\nM v[4:7],a[64:67],v[68:71],0\nv_cndmask_b32 v237,v236,v237,s[90:91]\nv_exp_f32 v148,v148\n" \
"M v[4:7],a[96:99],v[84:87],v[4:7]\nv_permlane16_swap_b32 v233,v237\nv_max_f32 v233,v233,v237\nv_mov_b32 v237,v233\nM v[12:15],a[68:71],v[68:71],0\nv_exp_f32 v149,v149\nM v[12:15],a[100:103],v[84:87],v[12:15]\nv_exp_f32 v150,v150\n" \
"v_permlane32_swap_b32 v233,v237\nM v[20:23],a[72:75],v[68:71],0\nv_exp_f32 v151,v151\nv_cvt_pk_bf16_f32 v198,v148,v149\nM v[20:23],a[104:107],v[84:87],v[20:23]\nv_cvt_pk_bf16_f32 v199,v150,v151\nv_max_f32 v233,v233,v237\nM v[28:31],a[76:79],v[68:71],0\n" \
"v_mov_b32 v237,v233\nv_cndmask_b32 v136,v136,v252,s[82:83]\nv_cndmask_b32 v137,v137,v252,s[84:85]\nM v[28:31],a[108:111],v[84:87],v[28:31]\nv_permlane16_swap_b32 v233,v237\nv_cndmask_b32 v138,v138,v252,s[86:87]\nv_cndmask_b32 v139,v139,v252,s[88:89]\nM v[36:39],a[80:83],v[68:71],0\n" \
"v_sub_f32 v232,0,v233\nv_sub_f32 v234,0,v233\nM v[36:39],a[112:115],v[84:87],v[36:39]\nv_sub_f32 v235,0,v233\nv_add_f32 v136,v136,v232\nv_add_f32 v137,v137,v232\nM v[44:47],a[84:87],v[68:71],0\nv_add_f32 v138,v138,v232\n" \
"v_add_f32 v139,v139,v232\nM v[44:47],a[116:119],v[84:87],v[44:47]\nv_sub_f32 v233,0,v233\nv_exp_f32 v136,v136\nM v[52:55],a[88:91],v[68:71],0\nv_exp_f32 v137,v137\nv_sub_f32 v236,0,v237\nM v[52:55],a[120:123],v[84:87],v[52:55]\n" \
"v_exp_f32 v138,v138\nM v[60:63],a[92:95],v[68:71],0\nv_exp_f32 v139,v139\nv_cvt_pk_bf16_f32 v200,v136,v137\nM v[60:63],a[124:127],v[84:87],v[60:63]\nv_cvt_pk_bf16_f32 v201,v138,v139\nv_sub_f32 v238,0,v237\nv_sub_f32 v239,0,v237\n" \
"M a[196:199],a[208:211],v[68:71],0\nv_add_f32 v140,v140,v236\nv_add_f32 v141,v141,v236\nM a[196:199],a[208:211],v[84:87],a[196:199]\nv_add_f32 v142,v142,v236\nv_add_f32 v143,v143,v236\nv_sub_f32 v237,0,v237\nM v[128:131],a[0:3],a[128:131],v[224:227]\n" \
"v_exp_f32 v140,v140\nds_read_b128 a[32:35],v240 offset:57344\nM v[128:131],a[4:7],a[132:135],v[128:131]\nv_exp_f32 v141,v141\nM v[128:131],a[8:11],a[136:139],v[128:131]\nv_exp_f32 v142,v142\nv_cvt_pk_bf16_f32 v204,v140,v141\nM v[128:131],a[12:15],a[140:143],v[128:131]\n" \
"v_exp_f32 v143,v143\nM v[132:135],a[12:15],a[156:159],v[228:231]\nv_cvt_pk_bf16_f32 v205,v142,v143\nds_read_b128 a[36:39],v240 offset:58368\nds_read_b128 a[40:43],v240 offset:59392\nM v[132:135],a[8:11],a[152:155],v[132:135]\nds_read_b128 a[44:47],v240 offset:60416\nv_cndmask_b32 v156,v156,v252,s[82:83]\n" \
"v_cndmask_b32 v157,v157,v252,s[84:85]\nM v[132:135],a[4:7],a[148:151],v[132:135]\nv_cndmask_b32 v158,v158,v252,s[86:87]\nv_cndmask_b32 v159,v159,v252,s[88:89]\nM v[132:135],a[0:3],a[144:147],v[132:135]\nv_add_f32 v156,v156,v236\nv_add_f32 v157,v157,v236\ns_waitcnt lgkmcnt(4)\n" \
"M v[144:147],a[16:19],a[128:131],v[224:227]\ns_waitcnt vmcnt(1)\ns_barrier\nv_add_f32 v158,v158,v236\nM v[144:147],a[20:23],a[132:135],v[144:147]\nv_add_f32 v159,v159,v236\nds_read_b128 a[48:51],v240 offset:61440\nM v[144:147],a[24:27],a[136:139],v[144:147]\n" \
"v_exp_f32 v156,v156\nds_read_b128 a[52:55],v240 offset:62464\nM v[144:147],a[28:31],a[140:143],v[144:147]\nv_exp_f32 v157,v157\nM v[148:151],a[28:31],a[156:159],v[228:231]\nv_exp_f32 v158,v158\nv_cvt_pk_bf16_f32 v206,v156,v157\nM v[148:151],a[24:27],a[152:155],v[148:151]\n" \
"v_exp_f32 v159,v159\nds_read_b128 a[56:59],v240 offset:63488\nM v[148:151],a[20:23],a[148:151],v[148:151]\nv_cvt_pk_bf16_f32 v207,v158,v159\nds_read_b128 a[60:63],v240 offset:64512\nM v[148:151],a[16:19],a[144:147],v[148:151]\nv_exp_f32 v160,v160\nds_read_b128 a[64:67],v241 offset:32768\n" \
"M v[136:139],a[0:3],a[160:163],v[232:235]\nv_exp_f32 v161,v161\nds_read_b128 a[96:99],v241 offset:40960\nM v[136:139],a[4:7],a[164:167],v[136:139]\nv_exp_f32 v162,v162\nM v[136:139],a[8:11],a[168:171],v[136:139]\nv_exp_f32 v163,v163\nv_cvt_pk_bf16_f32 v208,v160,v161\n" \
"M v[136:139],a[12:15],a[172:175],v[136:139]\nv_cvt_pk_bf16_f32 v209,v162,v163\nv_exp_f32 v164,v164\nM v[140:143],a[12:15],a[188:191],v[236:239]\nv_exp_f32 v165,v165\nM v[140:143],a[8:11],a[184:187],v[140:143]\nv_exp_f32 v166,v166\nv_cvt_pk_bf16_f32 v212,v164,v165\n" \
"M v[140:143],a[4:7],a[180:183],v[140:143]\nv_exp_f32 v167,v167\nM v[140:143],a[0:3],a[176:179],v[140:143]\nv_cvt_pk_bf16_f32 v213,v166,v167\nv_exp_f32 v176,v176\nM v[152:155],a[16:19],a[160:163],v[232:235]\nv_exp_f32 v177,v177\nds_read_b128 a[68:71],v241 offset:33792\n" \
"M v[152:155],a[20:23],a[164:167],v[152:155]\nv_exp_f32 v178,v178\nM v[152:155],a[24:27],a[168:171],v[152:155]\nv_exp_f32 v179,v179\nv_cvt_pk_bf16_f32 v210,v176,v177\nM v[152:155],a[28:31],a[172:175],v[152:155]\nv_cvt_pk_bf16_f32 v211,v178,v179\nv_exp_f32 v180,v180\n" \
"M v[156:159],a[28:31],a[188:191],v[236:239]\nv_exp_f32 v181,v181\nM v[156:159],a[24:27],a[184:187],v[156:159]\nv_exp_f32 v182,v182\nv_cvt_pk_bf16_f32 v214,v180,v181\nM v[156:159],a[20:23],a[180:183],v[156:159]\nv_exp_f32 v183,v183\nds_read_b128 a[100:103],v241 offset:41984\n" \
"M v[156:159],a[16:19],a[176:179],v[156:159]\nv_cvt_pk_bf16_f32 v215,v182,v183\ns_waitcnt lgkmcnt(2)\nM v[160:163],a[32:35],a[128:131],v[224:227]\nds_read_b128 a[72:75],v241 offset:34816\nds_read_b128 a[104:107],v241 offset:43008\ns_waitcnt vmcnt(0)\nv_cmp_eq_u32 vcc,0,v248\n" \
"M v[160:163],a[36:39],a[132:135],v[160:163]\nds_read_b128 a[76:79],v241 offset:35840\ns_cbranch_vccnz .Lps3\n.Lpb3:\nM v[160:163],a[40:43],a[136:139],v[160:163]\nds_read_b128 a[108:111],v241 offset:44032\nv_exp_f32 v128,v128\nM v[160:163],a[44:47],a[140:143],v[160:163]\n" \
"ds_read_b128 a[80:83],v241 offset:36864\nv_exp_f32 v129,v129\nM v[164:167],a[44:47],a[156:159],v[228:231]\nds_read_b128 a[112:115],v241 offset:45056\ns_add_u32 m0,s64,65536\nM v[164:167],a[40:43],a[152:155],v[164:167]\nds_read_b128 a[84:87],v241 offset:37888\nbuffer_load_dwordx4 v240,s[48:51],s36 offen offset:0 sc1 lds\n" \
"M v[164:167],a[36:39],a[148:151],v[164:167]\nds_read_b128 a[116:119],v241 offset:46080\nv_exp_f32 v130,v130\nM v[164:167],a[32:35],a[144:147],v[164:167]\nv_exp_f32 v131,v131\nM v[176:179],a[48:51],a[128:131],v[224:227]\nds_read_b128 a[88:91],v241 offset:38912\nv_exp_f32 v132,v132\n" \
"M v[176:179],a[52:55],a[132:135],v[176:179]\nds_read_b128 a[120:123],v241 offset:47104\nds_read_b128 a[92:95],v241 offset:39936\nM v[176:179],a[56:59],a[136:139],v[176:179]\nds_read_b128 a[124:127],v241 offset:48128\nbuffer_load_dwordx4 v240,s[48:51],s36 offen offset:1024 sc1 lds\nM v[176:179],a[60:63],a[140:143],v[176:179]\nds_read_b128 a[0:3],v240 offset:0\n" \
"v_exp_f32 v133,v133\nM v[180:183],a[60:63],a[156:159],v[228:231]\nv_exp_f32 v134,v134\nM v[180:183],a[56:59],a[152:155],v[180:183]\nds_read_b128 a[4:7],v240 offset:1024\nv_exp_f32 v135,v135\nM v[180:183],a[52:55],a[148:151],v[180:183]\nds_read_b128 a[8:11],v240 offset:2048\n" \
"buffer_load_dwordx4 v240,s[48:51],s36 offen offset:2048 sc1 lds\nM v[180:183],a[48:51],a[144:147],v[180:183]\nds_read_b128 a[12:15],v240 offset:3072\nds_read_b128 a[16:19],v240 offset:4096\nM v[168:171],a[32:35],a[160:163],v[232:235]\nds_read_b128 a[20:23],v240 offset:5120\nv_exp_f32 v144,v144\nM v[168:171],a[36:39],a[164:167],v[168:171]\n" \
"ds_read_b128 a[24:27],v240 offset:6144\nv_exp_f32 v145,v145\nM v[168:171],a[40:43],a[168:171],v[168:171]\nbuffer_load_dwordx4 v240,s[48:51],s36 offen offset:3072 sc1 lds\nM v[168:171],a[44:47],a[172:175],v[168:171]\nds_read_b128 a[28:31],v240 offset:7168\nv_exp_f32 v146,v146\nM v[172:175],a[44:47],a[188:191],v[236:239]\n" \
"v_exp_f32 v147,v147\nM v[172:175],a[40:43],a[184:187],v[172:175]\ns_add_u32 m0,s64,16384\nv_exp_f32 v148,v148\nM v[172:175],a[36:39],a[180:183],v[172:175]\nbuffer_load_dwordx4 v240,s[44:47],s65 offen offset:0 sc1 lds\nM v[172:175],a[32:35],a[176:179],v[172:175]\nds_read_b128 a[32:35],v240 offset:8192\n" \
"ds_read_b128 a[36:39],v240 offset:9216\nM v[184:187],a[48:51],a[160:163],v[232:235]\nds_read_b128 a[40:43],v240 offset:10240\nv_exp_f32 v149,v149\nM v[184:187],a[52:55],a[164:167],v[184:187]\nds_read_b128 a[44:47],v240 offset:11264\nv_exp_f32 v150,v150\nM v[184:187],a[56:59],a[168:171],v[184:187]\n" \
"buffer_load_dwordx4 v240,s[44:47],s65 offen offset:1024 sc1 lds\nM v[184:187],a[60:63],a[172:175],v[184:187]\nv_exp_f32 v151,v151\nM v[188:191],a[60:63],a[188:191],v[236:239]\nv_exp_f32 v136,v136\nM v[188:191],a[56:59],a[184:187],v[188:191]\nv_exp_f32 v137,v137\nM v[188:191],a[52:55],a[180:183],v[188:191]\n" \
"buffer_load_dwordx4 v240,s[44:47],s65 offen offset:2048 sc1 lds\nM v[188:191],a[48:51],a[176:179],v[188:191]\nds_read_b128 a[48:51],v240 offset:12288\nds_read_b128 a[52:55],v240 offset:13312\nM v[0:3],a[64:67],v[192:195],v[0:3]\nds_read_b128 a[56:59],v240 offset:14336\nv_exp_f32 v138,v138\nM v[0:3],a[96:99],v[208:211],v[0:3]\n" \
"v_exp_f32 v139,v139\ns_waitcnt lgkmcnt(4)\nM v[8:11],a[68:71],v[192:195],v[8:11]\nbuffer_load_dwordx4 v240,s[44:47],s65 offen offset:3072 sc1 lds\nM v[8:11],a[100:103],v[208:211],v[8:11]\ns_add_u32 m0,s64,81920\nv_exp_f32 v152,v152\nM v[16:19],a[72:75],v[192:195],v[16:19]\n" \
"ds_read_b128 a[60:63],v240 offset:15360\nv_exp_f32 v153,v153\nM v[16:19],a[104:107],v[208:211],v[16:19]\nv_exp_f32 v154,v154\nM v[24:27],a[76:79],v[192:195],v[24:27]\nbuffer_load_dwordx4 v240,s[48:51],s65 offen offset:0 sc1 lds\nM v[24:27],a[108:111],v[208:211],v[24:27]\nv_exp_f32 v155,v155\n" \
"M v[32:35],a[80:83],v[192:195],v[32:35]\ns_barrier\nv_exp_f32 v160,v160\nM v[32:35],a[112:115],v[208:211],v[32:35]\nv_exp_f32 v161,v161\nM v[40:43],a[84:87],v[192:195],v[40:43]\nbuffer_load_dwordx4 v240,s[48:51],s65 offen offset:1024 sc1 lds\nM v[40:43],a[116:119],v[208:211],v[40:43]\n" \
"v_exp_f32 v162,v162\nM v[48:51],a[88:91],v[192:195],v[48:51]\nv_exp_f32 v163,v163\nM v[48:51],a[120:123],v[208:211],v[48:51]\nv_exp_f32 v176,v176\nM v[56:59],a[92:95],v[192:195],v[56:59]\nbuffer_load_dwordx4 v240,s[48:51],s65 offen offset:2048 sc1 lds\nM v[56:59],a[124:127],v[208:211],v[56:59]\n" \
"v_exp_f32 v177,v177\nM a[192:195],a[208:211],v[192:195],a[192:195]\nv_exp_f32 v178,v178\nM a[192:195],a[208:211],v[208:211],a[192:195]\nv_exp_f32 v179,v179\nv_cvt_pk_bf16_f32 v192,v128,v129\nM v[4:7],a[64:67],v[196:199],v[4:7]\nbuffer_load_dwordx4 v240,s[48:51],s65 offen offset:3072 sc1 lds\n" \
"M v[4:7],a[96:99],v[212:215],v[4:7]\ns_add_u32 m0,s64,32768\nv_exp_f32 v140,v140\nM v[12:15],a[68:71],v[196:199],v[12:15]\nv_exp_f32 v141,v141\nv_cvt_pk_bf16_f32 v193,v130,v131\nM v[12:15],a[100:103],v[212:215],v[12:15]\nv_exp_f32 v142,v142\n" \
"M v[20:23],a[72:75],v[196:199],v[20:23]\nbuffer_load_dwordx4 v240,s[44:47],s66 offen offset:0 sc1 lds\nv_cvt_pk_bf16_f32 v194,v144,v145\nM v[20:23],a[104:107],v[212:215],v[20:23]\nv_exp_f32 v143,v143\nv_cvt_pk_bf16_f32 v195,v146,v147\nM v[28:31],a[76:79],v[196:199],v[28:31]\nv_exp_f32 v156,v156\n" \
"M v[28:31],a[108:111],v[212:215],v[28:31]\nv_exp_f32 v157,v157\nv_cvt_pk_bf16_f32 v208,v160,v161\nM v[36:39],a[80:83],v[196:199],v[36:39]\nbuffer_load_dwordx4 v240,s[44:47],s66 offen offset:1024 sc1 lds\nM v[36:39],a[112:115],v[212:215],v[36:39]\nv_exp_f32 v158,v158\nv_cvt_pk_bf16_f32 v209,v162,v163\n" \
"M v[44:47],a[84:87],v[196:199],v[44:47]\nv_exp_f32 v159,v159\nv_cvt_pk_bf16_f32 v210,v176,v177\nM v[44:47],a[116:119],v[212:215],v[44:47]\nv_exp_f32 v164,v164\nM v[52:55],a[88:91],v[196:199],v[52:55]\nbuffer_load_dwordx4 v240,s[44:47],s66 offen offset:2048 sc1 lds\nv_cvt_pk_bf16_f32 v211,v178,v179\n" \
"M v[52:55],a[120:123],v[212:215],v[52:55]\nv_exp_f32 v165,v165\nM v[60:63],a[92:95],v[196:199],v[60:63]\nv_exp_f32 v166,v166\nM v[60:63],a[124:127],v[212:215],v[60:63]\nv_exp_f32 v167,v167\nM a[196:199],a[208:211],v[196:199],a[196:199]\nbuffer_load_dwordx4 v240,s[44:47],s66 offen offset:3072 sc1 lds\n" \
"s_add_u32 m0,s64,98304\nM a[196:199],a[208:211],v[212:215],a[196:199]\nv_exp_f32 v180,v180\nM v[64:67],a[64:67],v[200:203],0\nv_exp_f32 v181,v181\nv_cvt_pk_bf16_f32 v196,v132,v133\nM v[72:75],a[68:71],v[200:203],0\nv_exp_f32 v182,v182\n" \
"M v[80:83],a[72:75],v[200:203],0\nv_exp_f32 v183,v183\nv_cvt_pk_bf16_f32 v197,v134,v135\nM v[88:91],a[76:79],v[200:203],0\nv_exp_f32 v168,v168\nv_cvt_pk_bf16_f32 v198,v148,v149\nM v[96:99],a[80:83],v[200:203],0\nv_exp_f32 v169,v169\n" \
"M v[104:107],a[84:87],v[200:203],0\nv_exp_f32 v170,v170\nv_cvt_pk_bf16_f32 v199,v150,v151\nM v[112:115],a[88:91],v[200:203],0\nv_exp_f32 v171,v171\nM v[120:123],a[92:95],v[200:203],0\nbuffer_load_dwordx4 v240,s[48:51],s66 offen offset:0 sc1 lds\nM a[200:203],a[208:211],v[200:203],0\n" \
"v_cvt_pk_bf16_f32 v200,v136,v137\nv_cvt_pk_bf16_f32 v201,v138,v139\nv_cvt_pk_bf16_f32 v202,v152,v153\nM v[68:71],a[64:67],v[204:207],0\nv_cvt_pk_bf16_f32 v203,v154,v155\nM v[76:79],a[68:71],v[204:207],0\nM v[84:87],a[72:75],v[204:207],0\nbuffer_load_dwordx4 v240,s[48:51],s66 offen offset:1024 sc1 lds\n" \
"M v[92:95],a[76:79],v[204:207],0\nM v[100:103],a[80:83],v[204:207],0\nM v[108:111],a[84:87],v[204:207],0\nM v[116:119],a[88:91],v[204:207],0\nbuffer_load_dwordx4 v240,s[48:51],s66 offen offset:2048 sc1 lds\nM v[124:127],a[92:95],v[204:207],0\nbuffer_load_dwordx4 v240,s[48:51],s66 offen offset:3072 sc1 lds\nM a[204:207],a[208:211],v[204:207],0\n" \
"s_cmp_eq_u32 s62,0\ns_cbranch_scc1 .Ltrans\ns_branch .Ldual\n.Lp23:\ns_cmp_eq_u32 s7,2\ns_cbranch_scc0 .Lpro3\n.Lpro2:\ns_barrier\n" \
"M v[64:67],a[0:3],a[128:131],0\nds_read_b128 a[64:67],v241 offset:0\nds_read_b128 a[96:99],v241 offset:8192\nM v[64:67],a[4:7],a[132:135],v[64:67]\nds_read_b128 a[68:71],v241 offset:1024\ns_mov_b32 m0,s64\nv_mov_b32 v250,139264\nM v[64:67],a[8:11],a[136:139],v[64:67]\n" \
"ds_read_b128 a[100:103],v241 offset:9216\nbuffer_load_dwordx4 v240,s[44:47],s36 offen offset:0 sc1 lds\nM v[64:67],a[12:15],a[140:143],v[64:67]\nds_read_b128 a[72:75],v241 offset:2048\nv_mov_b32 v184,0xff800000\nv_mov_b32 v185,0xff800000\nM v[68:71],a[12:15],a[156:159],0\nds_read_b128 a[12:15],v240 offset:19456\n" \
"v_mov_b32 v186,0xff800000\nv_mov_b32 v187,0xff800000\nM v[68:71],a[8:11],a[152:155],v[68:71]\nds_read_b128 a[8:11],v240 offset:18432\nv_max3_f32 v225,v64,v65,v66\nM v[68:71],a[4:7],a[148:151],v[68:71]\nds_read_b128 a[4:7],v240 offset:17408\nbuffer_load_dwordx4 v240,s[44:47],s36 offen offset:1024 sc1 lds\n" \
"M v[68:71],a[0:3],a[144:147],v[68:71]\nds_read_b128 a[0:3],v240 offset:16384\nM v[72:75],a[16:19],a[128:131],0\nds_read_b128 a[104:107],v241 offset:10240\nM v[72:75],a[20:23],a[132:135],v[72:75]\nds_read_b128 a[76:79],v241 offset:3072\nM v[72:75],a[24:27],a[136:139],v[72:75]\nds_read_b128 a[108:111],v241 offset:11264\n" \
"buffer_load_dwordx4 v240,s[44:47],s36 offen offset:2048 sc1 lds\nM v[72:75],a[28:31],a[140:143],v[72:75]\nv_max3_f32 v229,v68,v69,v70\nds_write_b128 v250,a[244:247]\nM v[76:79],a[28:31],a[156:159],0\nds_read_b128 a[28:31],v240 offset:23552\nM v[76:79],a[24:27],a[152:155],v[76:79]\nds_read_b128 a[24:27],v240 offset:22528\n" \
"M v[76:79],a[20:23],a[148:151],v[76:79]\nds_read_b128 a[20:23],v240 offset:21504\nv_max3_f32 v225,v225,v67,v72\nv_max3_f32 v225,v225,v73,v74\nM v[76:79],a[16:19],a[144:147],v[76:79]\nv_max_f32 v225,v225,v75\nv_cndmask_b32 v225,v224,v225,s[90:91]\nds_read_b128 a[16:19],v240 offset:20480\n" \
"M v[80:83],a[32:35],a[128:131],0\nds_read_b128 a[80:83],v241 offset:4096\nbuffer_load_dwordx4 v240,s[44:47],s36 offen offset:3072 sc1 lds\nM v[80:83],a[36:39],a[132:135],v[80:83]\nds_read_b128 a[112:115],v241 offset:12288\nv_max3_f32 v229,v229,v71,v76\nv_max3_f32 v229,v229,v77,v78\nM v[80:83],a[40:43],a[136:139],v[80:83]\n" \
"v_max_f32 v229,v229,v79\nv_cndmask_b32 v229,v228,v229,s[90:91]\nds_read_b128 a[84:87],v241 offset:5120\nM v[80:83],a[44:47],a[140:143],v[80:83]\nv_permlane16_swap_b32 v225,v229\nv_max_f32 v225,v225,v229\nv_mov_b32 v229,v225\nM v[84:87],a[44:47],a[156:159],0\n" \
"ds_read_b128 a[116:119],v241 offset:13312\nv_permlane32_swap_b32 v225,v229\nv_max_f32 v225,v225,v229\nM v[84:87],a[40:43],a[152:155],v[84:87]\nv_mov_b32 v229,v225\nds_read_b128 a[88:91],v241 offset:6144\nds_read_b128 a[120:123],v241 offset:14336\nM v[84:87],a[36:39],a[148:151],v[84:87]\n" \
"v_permlane16_swap_b32 v225,v229\nv_sub_f32 v224,0,v225\nv_sub_f32 v226,0,v225\nM v[84:87],a[32:35],a[144:147],v[84:87]\nv_sub_f32 v227,0,v225\nv_sub_f32 v225,0,v225\nv_sub_f32 v228,0,v229\nM v[88:91],a[48:51],a[128:131],0\n" \
"v_sub_f32 v230,0,v229\nv_sub_f32 v231,0,v229\nM v[88:91],a[52:55],a[132:135],v[88:91]\nv_sub_f32 v229,0,v229\nv_add_f32 v64,v64,v224\nv_add_f32 v65,v65,v224\nM v[88:91],a[56:59],a[136:139],v[88:91]\nv_add_f32 v66,v66,v224\n" \
"v_add_f32 v67,v67,v224\nv_add_f32 v72,v72,v224\nM v[88:91],a[60:63],a[140:143],v[88:91]\nv_exp_f32 v64,v64\nv_add_f32 v73,v73,v224\nM v[92:95],a[60:63],a[156:159],0\nv_exp_f32 v65,v65\nv_add_f32 v74,v74,v224\n" \
"M v[92:95],a[56:59],a[152:155],v[92:95]\nv_exp_f32 v66,v66\nv_add_f32 v75,v75,v224\nM v[92:95],a[52:55],a[148:151],v[92:95]\nv_exp_f32 v67,v67\nv_add_f32 v80,v80,v224\nM v[92:95],a[48:51],a[144:147],v[92:95]\nv_add_f32 v81,v81,v224\n" \
"v_add_f32 v82,v82,v224\ns_waitcnt lgkmcnt(0)\nM v[96:99],a[0:3],a[128:131],v[224:227]\nv_exp_f32 v72,v72\nv_add_f32 v83,v83,v224\nM v[96:99],a[4:7],a[132:135],v[96:99]\nv_exp_f32 v73,v73\nv_cvt_pk_bf16_f32 v64,v64,v65\n" \
"M v[96:99],a[8:11],a[136:139],v[96:99]\nv_exp_f32 v74,v74\nv_add_f32 v88,v88,v224\nM v[96:99],a[12:15],a[140:143],v[96:99]\nv_exp_f32 v75,v75\nv_add_f32 v89,v89,v224\nM v[100:103],a[12:15],a[156:159],v[228:231]\nv_exp_f32 v80,v80\n" \
"v_add_f32 v90,v90,v224\nM v[100:103],a[8:11],a[152:155],v[100:103]\nv_exp_f32 v81,v81\nM v[100:103],a[4:7],a[148:151],v[100:103]\nv_exp_f32 v82,v82\nv_add_f32 v91,v91,v224\nM v[100:103],a[0:3],a[144:147],v[100:103]\nv_exp_f32 v83,v83\n" \
"v_cvt_pk_bf16_f32 v65,v66,v67\nM v[108:111],a[28:31],a[156:159],v[228:231]\nv_exp_f32 v88,v88\nv_cvt_pk_bf16_f32 v66,v72,v73\nM v[108:111],a[24:27],a[152:155],v[108:111]\nv_exp_f32 v89,v89\nv_cvt_pk_bf16_f32 v67,v74,v75\nM v[108:111],a[20:23],a[148:151],v[108:111]\n" \
"v_exp_f32 v90,v90\nv_exp_f32 v91,v91\nv_cvt_pk_bf16_f32 v80,v80,v81\nM v[108:111],a[16:19],a[144:147],v[108:111]\nv_cvt_pk_bf16_f32 v81,v82,v83\nv_cvt_pk_bf16_f32 v82,v88,v89\nv_cvt_pk_bf16_f32 v83,v90,v91\nM v[0:3],a[64:67],v[64:67],0\n" \
"ds_read_b128 a[92:95],v241 offset:7168\nds_read_b128 a[124:127],v241 offset:15360\nv_add_f32 v68,v68,v228\nM v[0:3],a[96:99],v[80:83],v[0:3]\nv_add_f32 v69,v69,v228\nv_add_f32 v70,v70,v228\nv_add_f32 v71,v71,v228\nM v[8:11],a[68:71],v[64:67],0\n" \
"v_exp_f32 v68,v68\nv_add_f32 v76,v76,v228\nM v[8:11],a[100:103],v[80:83],v[8:11]\nv_exp_f32 v69,v69\nv_add_f32 v77,v77,v228\nM v[16:19],a[72:75],v[64:67],0\nv_exp_f32 v70,v70\nv_add_f32 v78,v78,v228\n" \
"M v[16:19],a[104:107],v[80:83],v[16:19]\nv_exp_f32 v71,v71\nM v[24:27],a[76:79],v[64:67],0\nv_add_f32 v79,v79,v228\nv_add_f32 v84,v84,v228\nv_add_f32 v85,v85,v228\nM v[24:27],a[108:111],v[80:83],v[24:27]\nv_exp_f32 v76,v76\n" \
"v_add_f32 v86,v86,v228\nM v[32:35],a[80:83],v[64:67],0\nv_exp_f32 v77,v77\nv_add_f32 v87,v87,v228\nM v[32:35],a[112:115],v[80:83],v[32:35]\nv_exp_f32 v78,v78\nv_cvt_pk_bf16_f32 v68,v68,v69\nM v[40:43],a[84:87],v[64:67],0\n" \
"v_exp_f32 v79,v79\nv_add_f32 v92,v92,v228\nM v[40:43],a[116:119],v[80:83],v[40:43]\nv_exp_f32 v84,v84\nv_add_f32 v93,v93,v228\nM v[48:51],a[88:91],v[64:67],0\nv_exp_f32 v85,v85\nv_add_f32 v94,v94,v228\n" \
"M v[48:51],a[120:123],v[80:83],v[48:51]\nv_exp_f32 v86,v86\ns_waitcnt lgkmcnt(0)\nM v[56:59],a[92:95],v[64:67],0\nv_exp_f32 v87,v87\nv_add_f32 v95,v95,v228\nM v[56:59],a[124:127],v[80:83],v[56:59]\nv_cvt_pk_bf16_f32 v69,v70,v71\n" \
"v_exp_f32 v92,v92\nM a[192:195],a[208:211],v[64:67],0\nv_exp_f32 v93,v93\nv_exp_f32 v94,v94\nv_exp_f32 v95,v95\nv_cvt_pk_bf16_f32 v70,v76,v77\nv_cvt_pk_bf16_f32 v71,v78,v79\nv_cvt_pk_bf16_f32 v84,v84,v85\n" \
"M a[192:195],a[208:211],v[80:83],a[192:195]\nv_cvt_pk_bf16_f32 v85,v86,v87\nv_cvt_pk_bf16_f32 v86,v92,v93\nv_cvt_pk_bf16_f32 v87,v94,v95\nM v[4:7],a[64:67],v[68:71],0\nds_read_b128 a[0:3],v240 offset:32768\nds_read_b128 a[4:7],v240 offset:33792\nM v[4:7],a[96:99],v[84:87],v[4:7]\n" \
"ds_read_b128 a[8:11],v240 offset:34816\nv_cndmask_b32 v96,v96,v252,s[82:83]\nv_cndmask_b32 v97,v97,v252,s[84:85]\nM v[12:15],a[68:71],v[68:71],0\nds_read_b128 a[12:15],v240 offset:35840\nv_cndmask_b32 v98,v98,v252,s[86:87]\nv_cndmask_b32 v99,v99,v252,s[88:89]\nM v[12:15],a[100:103],v[84:87],v[12:15]\n" \
"ds_read_b128 a[16:19],v240 offset:36864\nv_exp_f32 v96,v96\nM v[20:23],a[72:75],v[68:71],0\nds_read_b128 a[20:23],v240 offset:37888\nv_exp_f32 v97,v97\nM v[20:23],a[104:107],v[84:87],v[20:23]\nds_read_b128 a[24:27],v240 offset:38912\nv_exp_f32 v98,v98\n" \
"M v[28:31],a[76:79],v[68:71],0\nbuffer_load_dword v248,v251,s[52:55],s80 offen sc1\nds_read_b128 a[28:31],v240 offset:39936\nM v[28:31],a[108:111],v[84:87],v[28:31]\nds_read_b128 a[32:35],v240 offset:40960\nv_exp_f32 v99,v99\nM v[36:39],a[80:83],v[68:71],0\nds_read_b128 a[36:39],v240 offset:41984\n" \
"v_cvt_pk_bf16_f32 v96,v96,v97\nv_cvt_pk_bf16_f32 v97,v98,v99\nM v[36:39],a[112:115],v[84:87],v[36:39]\nds_read_b128 a[40:43],v240 offset:43008\nv_mov_b32 v98,0\nv_mov_b32 v99,0\nM v[44:47],a[84:87],v[68:71],0\nds_read_b128 a[44:47],v240 offset:44032\n" \
"v_exp_f32 v100,v100\nM v[44:47],a[116:119],v[84:87],v[44:47]\nds_read_b128 a[48:51],v240 offset:45056\nv_exp_f32 v101,v101\nM v[52:55],a[88:91],v[68:71],0\nds_read_b128 a[52:55],v240 offset:46080\nv_cndmask_b32 v108,v108,v252,s[82:83]\nM v[52:55],a[120:123],v[84:87],v[52:55]\n" \
"ds_read_b128 a[56:59],v240 offset:47104\nv_cndmask_b32 v109,v109,v252,s[84:85]\nv_cndmask_b32 v110,v110,v252,s[86:87]\nM v[60:63],a[92:95],v[68:71],0\nds_read_b128 a[60:63],v240 offset:48128\nv_cndmask_b32 v111,v111,v252,s[88:89]\nv_cvt_pk_bf16_f32 v100,v100,v101\nM v[60:63],a[124:127],v[84:87],v[60:63]\n" \
"ds_read_b128 a[64:67],v241 offset:16384\nv_exp_f32 v102,v102\nM a[196:199],a[208:211],v[68:71],0\nds_read_b128 a[68:71],v241 offset:17408\nv_exp_f32 v103,v103\nM a[196:199],a[208:211],v[84:87],a[196:199]\nds_read_b128 a[72:75],v241 offset:18432\nv_cvt_pk_bf16_f32 v101,v102,v103\n" \
"s_waitcnt lgkmcnt(8)\nM v[128:131],a[0:3],a[128:131],v[224:227]\nds_read_b128 a[76:79],v241 offset:19456\nv_exp_f32 v108,v108\nM v[128:131],a[4:7],a[132:135],v[128:131]\nds_read_b128 a[80:83],v241 offset:20480\nv_exp_f32 v109,v109\nM v[128:131],a[8:11],a[136:139],v[128:131]\n" \
"ds_read_b128 a[84:87],v241 offset:21504\nv_exp_f32 v110,v110\nM v[128:131],a[12:15],a[140:143],v[128:131]\nds_read_b128 a[88:91],v241 offset:22528\nv_exp_f32 v111,v111\nM v[132:135],a[12:15],a[156:159],v[228:231]\nds_read_b128 a[92:95],v241 offset:23552\nv_cvt_pk_bf16_f32 v102,v108,v109\n" \
"v_cvt_pk_bf16_f32 v103,v110,v111\nM v[132:135],a[8:11],a[152:155],v[132:135]\nds_read_b128 a[8:11],v240 offset:51200\nv_exp_f32 v128,v128\nM v[132:135],a[4:7],a[148:151],v[132:135]\nds_read_b128 a[4:7],v240 offset:50176\nv_exp_f32 v129,v129\nM v[132:135],a[0:3],a[144:147],v[132:135]\n" \
"ds_read_b128 a[0:3],v240 offset:49152\nv_cvt_pk_bf16_f32 v192,v128,v129\nM v[144:147],a[16:19],a[128:131],v[224:227]\nds_read_b128 a[12:15],v240 offset:52224\nv_exp_f32 v130,v130\nM v[144:147],a[20:23],a[132:135],v[144:147]\nv_exp_f32 v131,v131\nds_read_b128 a[96:99],v241 offset:40960\n" \
"M v[144:147],a[24:27],a[136:139],v[144:147]\nv_cvt_pk_bf16_f32 v193,v130,v131\nv_exp_f32 v132,v132\nM v[144:147],a[28:31],a[140:143],v[144:147]\nv_exp_f32 v133,v133\nds_read_b128 v[64:67],v250\nM v[148:151],a[28:31],a[156:159],v[228:231]\nds_read_b128 a[28:31],v240 offset:56320\n" \
"v_exp_f32 v134,v134\nM v[148:151],a[24:27],a[152:155],v[148:151]\nds_read_b128 a[24:27],v240 offset:55296\nv_exp_f32 v135,v135\nM v[148:151],a[20:23],a[148:151],v[148:151]\nds_read_b128 a[20:23],v240 offset:54272\nv_cvt_pk_bf16_f32 v196,v132,v133\nv_cvt_pk_bf16_f32 v197,v134,v135\n" \
"M v[148:151],a[16:19],a[144:147],v[148:151]\nds_read_b128 a[16:19],v240 offset:53248\nv_exp_f32 v144,v144\nM v[160:163],a[32:35],a[128:131],v[224:227]\nv_exp_f32 v145,v145\nds_read_b128 v[68:71],v250\nM v[160:163],a[36:39],a[132:135],v[160:163]\nv_exp_f32 v146,v146\n" \
"v_cvt_pk_bf16_f32 v194,v144,v145\nM v[160:163],a[40:43],a[136:139],v[160:163]\nv_exp_f32 v147,v147\ns_waitcnt lgkmcnt(6)\nM v[160:163],a[44:47],a[140:143],v[160:163]\nv_cvt_pk_bf16_f32 v195,v146,v147\nds_read_b128 v[72:75],v250\nM v[164:167],a[44:47],a[156:159],v[228:231]\n" \
"ds_read_b128 a[44:47],v240 offset:60416\nv_exp_f32 v148,v148\nM v[164:167],a[40:43],a[152:155],v[164:167]\nds_read_b128 a[40:43],v240 offset:59392\nv_exp_f32 v149,v149\nM v[164:167],a[36:39],a[148:151],v[164:167]\nds_read_b128 a[36:39],v240 offset:58368\nv_exp_f32 v150,v150\n" \
"M v[164:167],a[32:35],a[144:147],v[164:167]\nds_read_b128 a[32:35],v240 offset:57344\nv_exp_f32 v151,v151\nM v[176:179],a[48:51],a[128:131],v[224:227]\nv_cvt_pk_bf16_f32 v198,v148,v149\nv_cvt_pk_bf16_f32 v199,v150,v151\nds_read_b128 v[76:79],v250\nM v[176:179],a[52:55],a[132:135],v[176:179]\n" \
"v_exp_f32 v160,v160\nds_read_b128 v[80:83],v250\nM v[176:179],a[56:59],a[136:139],v[176:179]\nv_exp_f32 v161,v161\nds_read_b128 v[84:87],v250\nM v[176:179],a[60:63],a[140:143],v[176:179]\nv_exp_f32 v162,v162\nv_cvt_pk_bf16_f32 v208,v160,v161\n" \
"M v[180:183],a[60:63],a[156:159],v[228:231]\nv_exp_f32 v163,v163\nds_read_b128 a[60:63],v240 offset:64512\nM v[180:183],a[56:59],a[152:155],v[180:183]\nv_cvt_pk_bf16_f32 v209,v162,v163\nds_read_b128 a[56:59],v240 offset:63488\nds_read_b128 v[88:91],v250\nM v[180:183],a[52:55],a[148:151],v[180:183]\n" \
"ds_read_b128 a[52:55],v240 offset:62464\nv_exp_f32 v164,v164\nM v[180:183],a[48:51],a[144:147],v[180:183]\nds_read_b128 a[48:51],v240 offset:61440\nv_exp_f32 v165,v165\nM v[0:3],a[64:67],v[96:99],v[0:3]\nv_exp_f32 v166,v166\nM v[8:11],a[68:71],v[96:99],v[8:11]\n" \
"v_exp_f32 v167,v167\nv_cvt_pk_bf16_f32 v212,v164,v165\nM v[16:19],a[72:75],v[96:99],v[16:19]\nv_cvt_pk_bf16_f32 v213,v166,v167\nv_exp_f32 v176,v176\nM v[24:27],a[76:79],v[96:99],v[24:27]\nv_exp_f32 v177,v177\nds_read_b128 v[92:95],v250\n" \
"M v[32:35],a[80:83],v[96:99],v[32:35]\nv_exp_f32 v178,v178\nv_cvt_pk_bf16_f32 v210,v176,v177\nM v[40:43],a[84:87],v[96:99],v[40:43]\nv_exp_f32 v179,v179\nds_read_b128 v[104:107],v250\nM v[48:51],a[88:91],v[96:99],v[48:51]\ns_waitcnt vmcnt(0)\n" \
"s_barrier\nv_cvt_pk_bf16_f32 v211,v178,v179\nM v[56:59],a[92:95],v[96:99],v[56:59]\nv_exp_f32 v180,v180\nds_read_b128 v[108:111],v250\nM a[192:195],a[208:211],v[96:99],a[192:195]\nv_exp_f32 v181,v181\nds_read_b128 v[96:99],v250\n" \
"M v[4:7],a[64:67],v[100:103],v[4:7]\nv_exp_f32 v182,v182\nv_cvt_pk_bf16_f32 v214,v180,v181\nM v[12:15],a[68:71],v[100:103],v[12:15]\nv_exp_f32 v183,v183\nds_read_b128 a[64:67],v241 offset:32768\nM v[20:23],a[72:75],v[100:103],v[20:23]\nv_cvt_pk_bf16_f32 v215,v182,v183\n" \
"ds_read_b128 a[68:71],v241 offset:33792\nds_read_b128 v[112:115],v250\nM v[28:31],a[76:79],v[100:103],v[28:31]\nds_read_b128 v[116:119],v250\nv_cmp_eq_u32 vcc,0,v248\nM v[36:39],a[80:83],v[100:103],v[36:39]\nds_read_b128 v[120:123],v250\ns_cbranch_vccnz .Lps4\n" \
".Lpb4:\ns_add_u32 m0,s64,65536\nM v[44:47],a[84:87],v[100:103],v[44:47]\nds_read_b128 v[124:127],v250\nbuffer_load_dwordx4 v240,s[48:51],s36 offen offset:0 sc1 lds\nM v[52:55],a[88:91],v[100:103],v[52:55]\nds_read_b128 a[200:203],v250\nM v[60:63],a[92:95],v[100:103],v[60:63]\n" \
"ds_read_b128 a[204:207],v250\nM a[196:199],a[208:211],v[100:103],a[196:199]\nds_read_b128 v[100:103],v250\nv_add_u32 v250,0xc000,v241\nM v[136:139],a[0:3],a[160:163],0\nds_read_b128 a[100:103],v241 offset:41984\nbuffer_load_dwordx4 v240,s[48:51],s36 offen offset:1024 sc1 lds\nM v[136:139],a[4:7],a[164:167],v[136:139]\n" \
"ds_read_b128 a[72:75],v241 offset:34816\nM v[136:139],a[8:11],a[168:171],v[136:139]\nds_read_b128 a[104:107],v241 offset:43008\nM v[136:139],a[12:15],a[172:175],v[136:139]\nds_read_b128 a[76:79],v241 offset:35840\nM v[140:143],a[12:15],a[188:191],0\nds_read_b128 a[108:111],v241 offset:44032\nbuffer_load_dwordx4 v240,s[48:51],s36 offen offset:2048 sc1 lds\n" \
"M v[140:143],a[8:11],a[184:187],v[140:143]\nds_read_b128 a[80:83],v241 offset:36864\nM v[140:143],a[4:7],a[180:183],v[140:143]\nds_read_b128 a[112:115],v241 offset:45056\nv_max3_f32 v233,v136,v137,v138\nM v[140:143],a[0:3],a[176:179],v[140:143]\nds_read_b128 a[84:87],v241 offset:37888\ns_waitcnt lgkmcnt(11)\n" \
"M v[152:155],a[16:19],a[160:163],0\nds_read_b128 a[116:119],v241 offset:46080\nbuffer_load_dwordx4 v240,s[48:51],s36 offen offset:3072 sc1 lds\nM v[152:155],a[20:23],a[164:167],v[152:155]\nds_read_b128 a[88:91],v241 offset:38912\ns_add_u32 m0,s64,16384\nv_max3_f32 v237,v140,v141,v142\nM v[152:155],a[24:27],a[168:171],v[152:155]\n" \
"ds_read_b128 a[120:123],v241 offset:47104\nM v[152:155],a[28:31],a[172:175],v[152:155]\nds_read_b128 a[92:95],v241 offset:39936\nM v[156:159],a[28:31],a[188:191],0\nds_read_b128 a[124:127],v241 offset:48128\nbuffer_load_dwordx4 v240,s[44:47],s65 offen offset:0 sc1 lds\nM v[156:159],a[24:27],a[184:187],v[156:159]\nM v[156:159],a[20:23],a[180:183],v[156:159]\n" \
"M v[156:159],a[16:19],a[176:179],v[156:159]\nM v[128:131],a[0:3],a[128:131],v[224:227]\nv_max3_f32 v233,v233,v139,v152\nv_max3_f32 v233,v233,v153,v154\nv_max_f32 v233,v233,v155\nM v[128:131],a[4:7],a[132:135],v[128:131]\nv_cndmask_b32 v233,v232,v233,s[90:91]\nbuffer_load_dwordx4 v240,s[44:47],s65 offen offset:1024 sc1 lds\n" \
"M v[128:131],a[8:11],a[136:139],v[128:131]\nv_max3_f32 v237,v237,v143,v156\nv_max3_f32 v237,v237,v157,v158\nv_max_f32 v237,v237,v159\nM v[128:131],a[12:15],a[140:143],v[128:131]\nv_cndmask_b32 v237,v236,v237,s[90:91]\nM v[132:135],a[12:15],a[156:159],v[228:231]\nM v[132:135],a[8:11],a[152:155],v[132:135]\n" \
"v_permlane16_swap_b32 v233,v237\nv_max_f32 v233,v233,v237\nv_mov_b32 v237,v233\nM v[132:135],a[4:7],a[148:151],v[132:135]\nbuffer_load_dwordx4 v240,s[44:47],s65 offen offset:2048 sc1 lds\nv_permlane32_swap_b32 v233,v237\nM v[132:135],a[0:3],a[144:147],v[132:135]\nv_max_f32 v233,v233,v237\n" \
"v_mov_b32 v237,v233\nds_read_b128 a[0:3],v240 offset:0\nM v[144:147],a[16:19],a[128:131],v[224:227]\nv_permlane16_swap_b32 v233,v237\nv_sub_f32 v232,0,v233\nv_sub_f32 v234,0,v233\nM v[144:147],a[20:23],a[132:135],v[144:147]\nv_sub_f32 v235,0,v233\n" \
"v_sub_f32 v233,0,v233\nv_sub_f32 v236,0,v237\nM v[144:147],a[24:27],a[136:139],v[144:147]\nv_sub_f32 v238,0,v237\nv_sub_f32 v239,0,v237\nv_sub_f32 v237,0,v237\nM v[144:147],a[28:31],a[140:143],v[144:147]\nds_read_b128 a[4:7],v240 offset:1024\n" \
"ds_read_b128 a[8:11],v240 offset:2048\nv_add_f32 v136,v136,v232\nM v[148:151],a[28:31],a[156:159],v[228:231]\nds_read_b128 a[12:15],v240 offset:3072\nbuffer_load_dwordx4 v240,s[44:47],s65 offen offset:3072 sc1 lds\nM v[148:151],a[24:27],a[152:155],v[148:151]\ns_add_u32 m0,s64,81920\nv_add_f32 v137,v137,v232\n" \
"v_add_f32 v138,v138,v232\nM v[148:151],a[20:23],a[148:151],v[148:151]\nv_add_f32 v139,v139,v232\nv_add_f32 v152,v152,v232\nv_add_f32 v153,v153,v232\nM v[148:151],a[16:19],a[144:147],v[148:151]\nds_read_b128 a[16:19],v240 offset:4096\nds_read_b128 a[20:23],v240 offset:5120\n" \
"v_add_f32 v154,v154,v232\nM v[160:163],a[32:35],a[128:131],v[224:227]\nds_read_b128 a[24:27],v240 offset:6144\nv_add_f32 v155,v155,v232\nM v[160:163],a[36:39],a[132:135],v[160:163]\nds_read_b128 a[28:31],v240 offset:7168\nbuffer_load_dwordx4 v240,s[48:51],s65 offen offset:0 sc1 lds\nM v[160:163],a[40:43],a[136:139],v[160:163]\n" \
"v_exp_f32 v136,v136\nv_add_f32 v140,v140,v236\nM v[160:163],a[44:47],a[140:143],v[160:163]\nv_exp_f32 v137,v137\nv_add_f32 v141,v141,v236\nM v[164:167],a[44:47],a[156:159],v[228:231]\nv_exp_f32 v138,v138\nv_add_f32 v142,v142,v236\n" \
"M v[164:167],a[40:43],a[152:155],v[164:167]\nbuffer_load_dwordx4 v240,s[48:51],s65 offen offset:1024 sc1 lds\nv_add_f32 v143,v143,v236\nM v[164:167],a[36:39],a[148:151],v[164:167]\nv_exp_f32 v139,v139\nv_add_f32 v156,v156,v236\nM v[164:167],a[32:35],a[144:147],v[164:167]\nv_exp_f32 v152,v152\n" \
"v_add_f32 v157,v157,v236\nM v[176:179],a[48:51],a[128:131],v[224:227]\nv_exp_f32 v153,v153\nv_add_f32 v158,v158,v236\nM v[176:179],a[52:55],a[132:135],v[176:179]\nbuffer_load_dwordx4 v240,s[48:51],s65 offen offset:2048 sc1 lds\nv_add_f32 v159,v159,v236\nM v[176:179],a[56:59],a[136:139],v[176:179]\n" \
"v_exp_f32 v154,v154\nv_cvt_pk_bf16_f32 v200,v136,v137\nM v[176:179],a[60:63],a[140:143],v[176:179]\nv_exp_f32 v155,v155\nv_cvt_pk_bf16_f32 v201,v138,v139\nM v[180:183],a[60:63],a[156:159],v[228:231]\nv_exp_f32 v128,v128\nM v[180:183],a[56:59],a[152:155],v[180:183]\n" \
"buffer_load_dwordx4 v240,s[48:51],s65 offen offset:3072 sc1 lds\ns_add_u32 m0,s64,32768\nM v[180:183],a[52:55],a[148:151],v[180:183]\nv_exp_f32 v129,v129\nv_cvt_pk_bf16_f32 v202,v152,v153\nM v[180:183],a[48:51],a[144:147],v[180:183]\nv_exp_f32 v130,v130\nv_cvt_pk_bf16_f32 v203,v154,v155\n" \
"M v[168:171],a[32:35],a[160:163],v[232:235]\nv_exp_f32 v131,v131\nM v[168:171],a[36:39],a[164:167],v[168:171]\nbuffer_load_dwordx4 v240,s[44:47],s66 offen offset:0 sc1 lds\nM v[168:171],a[40:43],a[168:171],v[168:171]\nv_exp_f32 v132,v132\nM v[168:171],a[44:47],a[172:175],v[168:171]\nv_exp_f32 v133,v133\n" \
"M v[172:175],a[44:47],a[188:191],v[236:239]\nv_exp_f32 v134,v134\nM v[172:175],a[40:43],a[184:187],v[172:175]\nbuffer_load_dwordx4 v240,s[44:47],s66 offen offset:1024 sc1 lds\nM v[172:175],a[36:39],a[180:183],v[172:175]\ns_waitcnt lgkmcnt(0)\ns_barrier\nv_cndmask_b32 v168,v168,v252,s[82:83]\n" \
"M v[172:175],a[32:35],a[176:179],v[172:175]\nds_read_b128 a[32:35],v240 offset:8192\nds_read_b128 a[36:39],v240 offset:9216\nv_cndmask_b32 v169,v169,v252,s[84:85]\nM v[188:191],a[60:63],a[188:191],v[236:239]\nds_read_b128 a[40:43],v240 offset:10240\nv_exp_f32 v135,v135\nM v[188:191],a[56:59],a[184:187],v[188:191]\n" \
"ds_read_b128 a[44:47],v240 offset:11264\nv_cndmask_b32 v170,v170,v252,s[86:87]\nM v[188:191],a[52:55],a[180:183],v[188:191]\nbuffer_load_dwordx4 v240,s[44:47],s66 offen offset:2048 sc1 lds\nv_cndmask_b32 v171,v171,v252,s[88:89]\nM v[188:191],a[48:51],a[176:179],v[188:191]\nds_read_b128 a[48:51],v240 offset:12288\nv_exp_f32 v144,v144\n" \
"M v[0:3],a[64:67],v[192:195],v[0:3]\nds_read_b128 a[52:55],v240 offset:13312\nv_exp_f32 v145,v145\nM v[0:3],a[96:99],v[208:211],v[0:3]\nds_read_b128 a[56:59],v240 offset:14336\nv_exp_f32 v146,v146\nM v[8:11],a[68:71],v[192:195],v[8:11]\nds_read_b128 a[60:63],v240 offset:15360\n" \
"buffer_load_dwordx4 v240,s[44:47],s66 offen offset:3072 sc1 lds\nM v[8:11],a[100:103],v[208:211],v[8:11]\nv_exp_f32 v147,v147\ns_add_u32 m0,s64,98304\nM v[16:19],a[72:75],v[192:195],v[16:19]\nv_exp_f32 v148,v148\nv_cndmask_b32 v188,v188,v252,s[82:83]\nM v[16:19],a[104:107],v[208:211],v[16:19]\n" \
"v_exp_f32 v149,v149\nv_cndmask_b32 v189,v189,v252,s[84:85]\nM v[24:27],a[76:79],v[192:195],v[24:27]\nv_exp_f32 v150,v150\nv_cndmask_b32 v190,v190,v252,s[86:87]\nM v[24:27],a[108:111],v[208:211],v[24:27]\nv_exp_f32 v151,v151\nv_cndmask_b32 v191,v191,v252,s[88:89]\n" \
"M v[32:35],a[80:83],v[192:195],v[32:35]\nv_exp_f32 v160,v160\nM v[32:35],a[112:115],v[208:211],v[32:35]\nv_exp_f32 v161,v161\nM v[40:43],a[84:87],v[192:195],v[40:43]\nv_exp_f32 v162,v162\nM v[40:43],a[116:119],v[208:211],v[40:43]\nv_exp_f32 v163,v163\n" \
"M v[48:51],a[88:91],v[192:195],v[48:51]\nv_exp_f32 v176,v176\nM v[48:51],a[120:123],v[208:211],v[48:51]\nv_exp_f32 v177,v177\nM v[56:59],a[92:95],v[192:195],v[56:59]\nv_exp_f32 v178,v178\nM v[56:59],a[124:127],v[208:211],v[56:59]\nv_exp_f32 v179,v179\n" \
"M a[192:195],a[208:211],v[192:195],a[192:195]\nv_exp_f32 v140,v140\nv_cvt_pk_bf16_f32 v192,v128,v129\nM a[192:195],a[208:211],v[208:211],a[192:195]\nv_exp_f32 v141,v141\nv_cvt_pk_bf16_f32 v193,v130,v131\nM v[4:7],a[64:67],v[196:199],v[4:7]\nv_exp_f32 v142,v142\n" \
"v_cvt_pk_bf16_f32 v194,v144,v145\nM v[4:7],a[96:99],v[212:215],v[4:7]\nv_exp_f32 v143,v143\nv_cvt_pk_bf16_f32 v195,v146,v147\nM v[12:15],a[68:71],v[196:199],v[12:15]\nv_exp_f32 v156,v156\nv_cvt_pk_bf16_f32 v208,v160,v161\nM v[12:15],a[100:103],v[212:215],v[12:15]\n" \
"v_exp_f32 v157,v157\nv_cvt_pk_bf16_f32 v209,v162,v163\nM v[20:23],a[72:75],v[196:199],v[20:23]\nv_exp_f32 v158,v158\nM v[20:23],a[104:107],v[212:215],v[20:23]\nv_exp_f32 v159,v159\nv_cvt_pk_bf16_f32 v210,v176,v177\nM v[28:31],a[76:79],v[196:199],v[28:31]\n" \
"v_exp_f32 v164,v164\nv_cvt_pk_bf16_f32 v211,v178,v179\nM v[28:31],a[108:111],v[212:215],v[28:31]\nv_exp_f32 v165,v165\nM v[36:39],a[80:83],v[196:199],v[36:39]\nv_exp_f32 v166,v166\nM v[36:39],a[112:115],v[212:215],v[36:39]\nv_exp_f32 v167,v167\n" \
"M v[44:47],a[84:87],v[196:199],v[44:47]\nv_exp_f32 v180,v180\nM v[44:47],a[116:119],v[212:215],v[44:47]\nv_exp_f32 v181,v181\nM v[52:55],a[88:91],v[196:199],v[52:55]\nv_exp_f32 v182,v182\nM v[52:55],a[120:123],v[212:215],v[52:55]\nv_exp_f32 v183,v183\n" \
"M v[60:63],a[92:95],v[196:199],v[60:63]\nv_exp_f32 v168,v168\nM v[60:63],a[124:127],v[212:215],v[60:63]\nv_exp_f32 v169,v169\nM a[196:199],a[208:211],v[196:199],a[196:199]\nv_exp_f32 v170,v170\nv_exp_f32 v171,v171\nv_cvt_pk_bf16_f32 v196,v132,v133\n" \
"v_cvt_pk_bf16_f32 v197,v134,v135\nv_cvt_pk_bf16_f32 v198,v148,v149\nv_cvt_pk_bf16_f32 v199,v150,v151\nbuffer_load_dwordx4 v240,s[48:51],s66 offen offset:0 sc1 lds\nbuffer_load_dwordx4 v240,s[48:51],s66 offen offset:1024 sc1 lds\nbuffer_load_dwordx4 v240,s[48:51],s66 offen offset:2048 sc1 lds\nbuffer_load_dwordx4 v240,s[48:51],s66 offen offset:3072 sc1 lds\nM a[196:199],a[208:211],v[212:215],a[196:199]\n" \
"s_cmp_eq_u32 s62,0\ns_cbranch_scc1 .Ltrans\ns_branch .Ldual\n.Lpro3:\ns_barrier\nM v[64:67],a[0:3],a[128:131],0\nds_read_b128 a[64:67],v241 offset:0\nds_read_b128 a[96:99],v241 offset:8192\n" \
"M v[64:67],a[4:7],a[132:135],v[64:67]\nds_read_b128 a[68:71],v241 offset:1024\ns_mov_b32 m0,s64\nv_mov_b32 v250,143360\nM v[64:67],a[8:11],a[136:139],v[64:67]\nds_read_b128 a[100:103],v241 offset:9216\nbuffer_load_dwordx4 v240,s[44:47],s36 offen offset:0 sc1 lds\nM v[64:67],a[12:15],a[140:143],v[64:67]\n" \
"ds_read_b128 a[72:75],v241 offset:2048\nv_mov_b32 v168,0\nv_mov_b32 v169,0\nM v[68:71],a[12:15],a[156:159],0\nds_read_b128 a[12:15],v240 offset:19456\nv_mov_b32 v170,0\nv_mov_b32 v171,0\nM v[68:71],a[8:11],a[152:155],v[68:71]\n" \
"ds_read_b128 a[8:11],v240 offset:18432\nv_max3_f32 v225,v64,v65,v66\nv_mov_b32 v172,0xff800000\nM v[68:71],a[4:7],a[148:151],v[68:71]\nds_read_b128 a[4:7],v240 offset:17408\nbuffer_load_dwordx4 v240,s[44:47],s36 offen offset:1024 sc1 lds\nM v[68:71],a[0:3],a[144:147],v[68:71]\nds_read_b128 a[0:3],v240 offset:16384\n" \
"v_mov_b32 v173,0xff800000\nv_mov_b32 v174,0xff800000\nM v[72:75],a[16:19],a[128:131],0\nds_read_b128 a[104:107],v241 offset:10240\nv_mov_b32 v175,0xff800000\nv_mov_b32 v184,0xff800000\nM v[72:75],a[20:23],a[132:135],v[72:75]\nv_max3_f32 v229,v68,v69,v70\n" \
"ds_read_b128 a[76:79],v241 offset:3072\nv_mov_b32 v185,0xff800000\nM v[72:75],a[24:27],a[136:139],v[72:75]\nds_read_b128 a[108:111],v241 offset:11264\nv_mov_b32 v186,0xff800000\nM v[72:75],a[28:31],a[140:143],v[72:75]\nds_read_b128 a[80:83],v241 offset:4096\nbuffer_load_dwordx4 v240,s[44:47],s36 offen offset:2048 sc1 lds\n" \
"M v[76:79],a[28:31],a[156:159],0\nds_read_b128 a[28:31],v240 offset:23552\nv_mov_b32 v187,0xff800000\nv_mov_b32 v188,0xff800000\nM v[76:79],a[24:27],a[152:155],v[76:79]\nds_read_b128 a[24:27],v240 offset:22528\nv_max3_f32 v225,v225,v67,v72\nv_max3_f32 v225,v225,v73,v74\n" \
"M v[76:79],a[20:23],a[148:151],v[76:79]\nv_max_f32 v225,v225,v75\nv_cndmask_b32 v225,v224,v225,s[90:91]\nds_read_b128 a[20:23],v240 offset:21504\nM v[76:79],a[16:19],a[144:147],v[76:79]\nds_read_b128 a[16:19],v240 offset:20480\nbuffer_load_dwordx4 v240,s[44:47],s36 offen offset:3072 sc1 lds\nM v[80:83],a[32:35],a[128:131],0\n" \
"ds_read_b128 a[112:115],v241 offset:12288\nv_mov_b32 v189,0xff800000\nv_mov_b32 v190,0xff800000\nM v[80:83],a[36:39],a[132:135],v[80:83]\nds_read_b128 a[84:87],v241 offset:5120\nv_max3_f32 v229,v229,v71,v76\nv_max3_f32 v229,v229,v77,v78\nM v[80:83],a[40:43],a[136:139],v[80:83]\n" \
"v_max_f32 v229,v229,v79\nv_cndmask_b32 v229,v228,v229,s[90:91]\nds_read_b128 a[116:119],v241 offset:13312\nM v[80:83],a[44:47],a[140:143],v[80:83]\nv_permlane16_swap_b32 v225,v229\nv_max_f32 v225,v225,v229\nv_mov_b32 v229,v225\nM v[84:87],a[44:47],a[156:159],0\n" \
"ds_read_b128 a[44:47],v240 offset:27648\nv_permlane32_swap_b32 v225,v229\nv_max_f32 v225,v225,v229\nM v[84:87],a[40:43],a[152:155],v[84:87]\nv_mov_b32 v229,v225\nds_read_b128 a[40:43],v240 offset:26624\nM v[84:87],a[36:39],a[148:151],v[84:87]\nv_permlane16_swap_b32 v225,v229\n" \
"v_sub_f32 v224,0,v225\nv_sub_f32 v226,0,v225\nM v[84:87],a[32:35],a[144:147],v[84:87]\nv_sub_f32 v227,0,v225\nv_sub_f32 v225,0,v225\nv_sub_f32 v228,0,v229\nM v[88:91],a[48:51],a[128:131],0\nv_sub_f32 v230,0,v229\n" \
"v_sub_f32 v231,0,v229\nv_sub_f32 v229,0,v229\nM v[88:91],a[52:55],a[132:135],v[88:91]\nds_read_b128 a[32:35],v240 offset:24576\nds_read_b128 a[36:39],v240 offset:25600\nv_add_f32 v64,v64,v224\nM v[88:91],a[56:59],a[136:139],v[88:91]\nv_add_f32 v65,v65,v224\n" \
"v_add_f32 v66,v66,v224\nv_add_f32 v67,v67,v224\nM v[88:91],a[60:63],a[140:143],v[88:91]\nv_exp_f32 v64,v64\nv_add_f32 v72,v72,v224\nM v[92:95],a[60:63],a[156:159],0\nds_read_b128 a[60:63],v240 offset:31744\nv_exp_f32 v65,v65\n" \
"M v[92:95],a[56:59],a[152:155],v[92:95]\nds_read_b128 a[56:59],v240 offset:30720\nv_exp_f32 v66,v66\nM v[92:95],a[52:55],a[148:151],v[92:95]\nds_read_b128 a[52:55],v240 offset:29696\nv_exp_f32 v67,v67\nM v[92:95],a[48:51],a[144:147],v[92:95]\nds_read_b128 a[48:51],v240 offset:28672\n" \
"v_add_f32 v73,v73,v224\ns_waitcnt lgkmcnt(6)\nM v[96:99],a[0:3],a[128:131],v[224:227]\nv_add_f32 v74,v74,v224\nv_add_f32 v75,v75,v224\nM v[96:99],a[4:7],a[132:135],v[96:99]\nv_add_f32 v80,v80,v224\nv_add_f32 v81,v81,v224\n" \
"v_add_f32 v82,v82,v224\nM v[96:99],a[8:11],a[136:139],v[96:99]\nv_exp_f32 v72,v72\nv_add_f32 v83,v83,v224\nM v[96:99],a[12:15],a[140:143],v[96:99]\nv_exp_f32 v73,v73\nv_cvt_pk_bf16_f32 v64,v64,v65\nM v[100:103],a[12:15],a[156:159],v[228:231]\n" \
"v_exp_f32 v74,v74\nv_add_f32 v88,v88,v224\nM v[100:103],a[8:11],a[152:155],v[100:103]\nv_exp_f32 v75,v75\nv_add_f32 v89,v89,v224\nM v[100:103],a[4:7],a[148:151],v[100:103]\nv_exp_f32 v80,v80\nv_add_f32 v90,v90,v224\n" \
"M v[100:103],a[0:3],a[144:147],v[100:103]\nv_exp_f32 v81,v81\nv_add_f32 v91,v91,v224\nM v[104:107],a[16:19],a[128:131],v[224:227]\nv_exp_f32 v82,v82\nv_cvt_pk_bf16_f32 v65,v66,v67\nM v[104:107],a[20:23],a[132:135],v[104:107]\nv_exp_f32 v83,v83\n" \
"v_cvt_pk_bf16_f32 v66,v72,v73\nM v[104:107],a[24:27],a[136:139],v[104:107]\nv_exp_f32 v88,v88\nv_cvt_pk_bf16_f32 v67,v74,v75\nM v[104:107],a[28:31],a[140:143],v[104:107]\nv_exp_f32 v89,v89\nM v[108:111],a[28:31],a[156:159],v[228:231]\nv_exp_f32 v90,v90\n" \
"v_cvt_pk_bf16_f32 v80,v80,v81\nM v[108:111],a[24:27],a[152:155],v[108:111]\nv_exp_f32 v91,v91\nv_cvt_pk_bf16_f32 v81,v82,v83\nM v[108:111],a[20:23],a[148:151],v[108:111]\nv_cvt_pk_bf16_f32 v82,v88,v89\nv_cvt_pk_bf16_f32 v83,v90,v91\nds_read_b128 a[88:91],v241 offset:6144\n" \
"M v[108:111],a[16:19],a[144:147],v[108:111]\nds_read_b128 a[120:123],v241 offset:14336\nds_read_b128 a[92:95],v241 offset:7168\ns_waitcnt lgkmcnt(3)\nM v[112:115],a[32:35],a[128:131],v[224:227]\nds_read_b128 a[124:127],v241 offset:15360\nv_add_f32 v68,v68,v228\nv_add_f32 v69,v69,v228\n" \
"M v[112:115],a[36:39],a[132:135],v[112:115]\nv_add_f32 v70,v70,v228\nv_add_f32 v71,v71,v228\nv_add_f32 v76,v76,v228\nM v[112:115],a[40:43],a[136:139],v[112:115]\nv_exp_f32 v68,v68\nv_add_f32 v77,v77,v228\nM v[112:115],a[44:47],a[140:143],v[112:115]\n" \
"v_exp_f32 v69,v69\nv_add_f32 v78,v78,v228\nM v[116:119],a[44:47],a[156:159],v[228:231]\nv_exp_f32 v70,v70\nv_add_f32 v79,v79,v228\nM v[116:119],a[40:43],a[152:155],v[116:119]\nv_exp_f32 v71,v71\nv_add_f32 v84,v84,v228\n" \
"M v[116:119],a[36:39],a[148:151],v[116:119]\nv_add_f32 v85,v85,v228\nv_add_f32 v86,v86,v228\nM v[116:119],a[32:35],a[144:147],v[116:119]\nv_exp_f32 v76,v76\nv_add_f32 v87,v87,v228\nM v[124:127],a[60:63],a[156:159],v[228:231]\nv_exp_f32 v77,v77\n" \
"v_cvt_pk_bf16_f32 v68,v68,v69\nM v[124:127],a[56:59],a[152:155],v[124:127]\nv_exp_f32 v78,v78\nv_add_f32 v92,v92,v228\nM v[124:127],a[52:55],a[148:151],v[124:127]\nv_exp_f32 v79,v79\nv_add_f32 v93,v93,v228\nM v[124:127],a[48:51],a[144:147],v[124:127]\n" \
"v_exp_f32 v84,v84\nv_add_f32 v94,v94,v228\nM v[0:3],a[64:67],v[64:67],0\nv_exp_f32 v85,v85\nv_add_f32 v95,v95,v228\nM v[0:3],a[96:99],v[80:83],v[0:3]\nv_exp_f32 v86,v86\nv_cvt_pk_bf16_f32 v69,v70,v71\n" \
"M v[8:11],a[68:71],v[64:67],0\nv_exp_f32 v87,v87\nv_cvt_pk_bf16_f32 v70,v76,v77\nM v[8:11],a[100:103],v[80:83],v[8:11]\nv_exp_f32 v92,v92\nv_cvt_pk_bf16_f32 v71,v78,v79\nM v[16:19],a[72:75],v[64:67],0\nv_exp_f32 v93,v93\n" \
"v_cvt_pk_bf16_f32 v84,v84,v85\nM v[16:19],a[104:107],v[80:83],v[16:19]\nv_exp_f32 v94,v94\nM v[24:27],a[76:79],v[64:67],0\nv_exp_f32 v95,v95\nv_cvt_pk_bf16_f32 v85,v86,v87\nM v[24:27],a[108:111],v[80:83],v[24:27]\nv_cvt_pk_bf16_f32 v86,v92,v93\n" \
"v_cvt_pk_bf16_f32 v87,v94,v95\nds_read_b128 a[0:3],v240 offset:32768\nM v[32:35],a[80:83],v[64:67],0\nds_read_b128 a[4:7],v240 offset:33792\nds_read_b128 a[8:11],v240 offset:34816\nv_cndmask_b32 v112,v112,v252,s[82:83]\nM v[32:35],a[112:115],v[80:83],v[32:35]\nds_read_b128 a[12:15],v240 offset:35840\n" \
"v_exp_f32 v96,v96\nM v[40:43],a[84:87],v[64:67],0\nds_read_b128 a[16:19],v240 offset:36864\nv_exp_f32 v97,v97\nM v[40:43],a[116:119],v[80:83],v[40:43]\nds_read_b128 a[20:23],v240 offset:37888\nv_cndmask_b32 v113,v113,v252,s[84:85]\ns_waitcnt lgkmcnt(6)\n" \
"M v[48:51],a[88:91],v[64:67],0\nds_read_b128 a[24:27],v240 offset:38912\nv_exp_f32 v98,v98\nM v[48:51],a[120:123],v[80:83],v[48:51]\nds_read_b128 a[28:31],v240 offset:39936\nv_exp_f32 v99,v99\nM v[56:59],a[92:95],v[64:67],0\nbuffer_load_dword v248,v251,s[52:55],s80 offen sc1\n" \
"ds_read_b128 a[32:35],v240 offset:40960\nM v[56:59],a[124:127],v[80:83],v[56:59]\nds_read_b128 a[36:39],v240 offset:41984\nv_exp_f32 v104,v104\nM a[192:195],a[208:211],v[64:67],0\nds_read_b128 a[40:43],v240 offset:43008\nv_cndmask_b32 v114,v114,v252,s[86:87]\nM a[192:195],a[208:211],v[80:83],a[192:195]\n" \
"ds_read_b128 a[44:47],v240 offset:44032\nv_exp_f32 v105,v105\nM v[4:7],a[64:67],v[68:71],0\nds_read_b128 a[48:51],v240 offset:45056\nv_exp_f32 v106,v106\nM v[4:7],a[96:99],v[84:87],v[4:7]\nds_read_b128 a[52:55],v240 offset:46080\nv_exp_f32 v107,v107\n" \
"M v[12:15],a[68:71],v[68:71],0\nds_read_b128 a[56:59],v240 offset:47104\nv_cndmask_b32 v115,v115,v252,s[88:89]\nv_cvt_pk_bf16_f32 v96,v96,v97\nM v[12:15],a[100:103],v[84:87],v[12:15]\nds_read_b128 a[60:63],v240 offset:48128\nv_exp_f32 v112,v112\nM v[20:23],a[72:75],v[68:71],0\n" \
"ds_read_b128 a[64:67],v241 offset:16384\nv_exp_f32 v113,v113\nM v[20:23],a[104:107],v[84:87],v[20:23]\nds_read_b128 a[96:99],v241 offset:24576\nv_exp_f32 v114,v114\nM v[28:31],a[76:79],v[68:71],0\nds_read_b128 a[68:71],v241 offset:17408\nv_exp_f32 v115,v115\n" \
"M v[28:31],a[108:111],v[84:87],v[28:31]\nds_read_b128 a[100:103],v241 offset:25600\nv_cvt_pk_bf16_f32 v97,v98,v99\nv_cvt_pk_bf16_f32 v98,v104,v105\nM v[36:39],a[80:83],v[68:71],0\nds_read_b128 a[72:75],v241 offset:18432\nv_cvt_pk_bf16_f32 v99,v106,v107\nM v[36:39],a[112:115],v[84:87],v[36:39]\n" \
"ds_read_b128 a[104:107],v241 offset:26624\nv_cvt_pk_bf16_f32 v112,v112,v113\nv_cvt_pk_bf16_f32 v113,v114,v115\nM v[44:47],a[84:87],v[68:71],0\nds_read_b128 a[76:79],v241 offset:19456\nv_mov_b32 v114,0\nv_mov_b32 v115,0\nM v[44:47],a[116:119],v[84:87],v[44:47]\n" \
"ds_read_b128 a[108:111],v241 offset:27648\nv_exp_f32 v100,v100\nM v[52:55],a[88:91],v[68:71],0\nds_read_b128 a[80:83],v241 offset:20480\nv_exp_f32 v101,v101\nM v[52:55],a[120:123],v[84:87],v[52:55]\nds_read_b128 a[112:115],v241 offset:28672\nv_exp_f32 v102,v102\n" \
"M v[60:63],a[92:95],v[68:71],0\nds_read_b128 a[84:87],v241 offset:21504\nv_exp_f32 v103,v103\nM v[60:63],a[124:127],v[84:87],v[60:63]\nds_read_b128 a[116:119],v241 offset:29696\nv_exp_f32 v108,v108\nM a[196:199],a[208:211],v[68:71],0\nds_read_b128 a[88:91],v241 offset:22528\n" \
"v_exp_f32 v109,v109\nM a[196:199],a[208:211],v[84:87],a[196:199]\nds_read_b128 a[120:123],v241 offset:30720\nv_cvt_pk_bf16_f32 v100,v100,v101\ns_waitcnt lgkmcnt(8)\nM v[128:131],a[0:3],a[128:131],v[224:227]\nds_read_b128 a[92:95],v241 offset:23552\nv_exp_f32 v110,v110\n" \
"M v[128:131],a[4:7],a[132:135],v[128:131]\nds_read_b128 a[124:127],v241 offset:31744\nv_cndmask_b32 v124,v124,v252,s[82:83]\nM v[128:131],a[8:11],a[136:139],v[128:131]\nv_exp_f32 v111,v111\nv_cndmask_b32 v125,v125,v252,s[84:85]\nM v[128:131],a[12:15],a[140:143],v[128:131]\nv_exp_f32 v116,v116\n" \
"v_cndmask_b32 v126,v126,v252,s[86:87]\nM v[132:135],a[12:15],a[156:159],v[228:231]\nv_exp_f32 v117,v117\nv_cndmask_b32 v127,v127,v252,s[88:89]\nM v[132:135],a[8:11],a[152:155],v[132:135]\nv_exp_f32 v118,v118\nv_cvt_pk_bf16_f32 v101,v102,v103\nM v[132:135],a[4:7],a[148:151],v[132:135]\n" \
"v_exp_f32 v119,v119\nv_cvt_pk_bf16_f32 v102,v108,v109\nM v[132:135],a[0:3],a[144:147],v[132:135]\nv_exp_f32 v124,v124\nv_cvt_pk_bf16_f32 v103,v110,v111\nM v[144:147],a[16:19],a[128:131],v[224:227]\nv_exp_f32 v125,v125\nv_cvt_pk_bf16_f32 v116,v116,v117\n" \
"M v[144:147],a[20:23],a[132:135],v[144:147]\nv_exp_f32 v126,v126\nv_cvt_pk_bf16_f32 v117,v118,v119\nM v[144:147],a[24:27],a[136:139],v[144:147]\nv_exp_f32 v127,v127\nv_cvt_pk_bf16_f32 v118,v124,v125\nM v[144:147],a[28:31],a[140:143],v[144:147]\nv_cvt_pk_bf16_f32 v119,v126,v127\n" \
"ds_read_b128 a[0:3],v240 offset:49152\nds_read_b128 a[4:7],v240 offset:50176\nM v[148:151],a[28:31],a[156:159],v[228:231]\nds_read_b128 a[8:11],v240 offset:51200\nv_mov_b32 v191,0xff800000\nM v[148:151],a[24:27],a[152:155],v[148:151]\nds_read_b128 a[12:15],v240 offset:52224\nv_exp_f32 v128,v128\n" \
"M v[148:151],a[20:23],a[148:151],v[148:151]\nds_read_b128 a[28:31],v240 offset:56320\nv_exp_f32 v129,v129\nM v[148:151],a[16:19],a[144:147],v[148:151]\nds_read_b128 a[24:27],v240 offset:55296\nv_exp_f32 v130,v130\nM v[160:163],a[32:35],a[128:131],v[224:227]\nds_read_b128 a[20:23],v240 offset:54272\n" \
"v_exp_f32 v131,v131\nM v[160:163],a[36:39],a[132:135],v[160:163]\nds_read_b128 a[16:19],v240 offset:53248\nv_cvt_pk_bf16_f32 v192,v128,v129\nv_cvt_pk_bf16_f32 v193,v130,v131\nM v[160:163],a[40:43],a[136:139],v[160:163]\nv_exp_f32 v132,v132\nv_mov_b32 v202,0\n" \
"M v[160:163],a[44:47],a[140:143],v[160:163]\nv_exp_f32 v133,v133\nv_mov_b32 v203,0\nM v[164:167],a[44:47],a[156:159],v[228:231]\nv_exp_f32 v134,v134\nv_cvt_pk_bf16_f32 v196,v132,v133\nM v[164:167],a[40:43],a[152:155],v[164:167]\nv_exp_f32 v135,v135\n" \
"ds_read_b128 a[40:43],v240 offset:59392\nM v[164:167],a[36:39],a[148:151],v[164:167]\nv_cvt_pk_bf16_f32 v197,v134,v135\nv_exp_f32 v144,v144\nM v[164:167],a[32:35],a[144:147],v[164:167]\nds_read_b128 a[32:35],v240 offset:57344\nds_read_b128 a[36:39],v240 offset:58368\nM v[176:179],a[48:51],a[128:131],v[224:227]\n" \
"v_exp_f32 v145,v145\nds_read_b128 a[44:47],v240 offset:60416\nM v[176:179],a[52:55],a[132:135],v[176:179]\nv_exp_f32 v146,v146\nv_cvt_pk_bf16_f32 v194,v144,v145\nM v[176:179],a[56:59],a[136:139],v[176:179]\nv_exp_f32 v147,v147\ns_waitcnt vmcnt(0)\n" \
"v_cmp_eq_u32 vcc,0,v248\nM v[176:179],a[60:63],a[140:143],v[176:179]\nv_cvt_pk_bf16_f32 v195,v146,v147\nv_exp_f32 v148,v148\nM v[180:183],a[60:63],a[156:159],v[228:231]\ns_waitcnt lgkmcnt(4)\ns_barrier\nds_read_b128 a[60:63],v240 offset:64512\n" \
"M v[180:183],a[56:59],a[152:155],v[180:183]\nv_exp_f32 v149,v149\nds_read_b128 a[56:59],v240 offset:63488\nM v[180:183],a[52:55],a[148:151],v[180:183]\nv_exp_f32 v150,v150\nv_cvt_pk_bf16_f32 v198,v148,v149\nM v[180:183],a[48:51],a[144:147],v[180:183]\nv_exp_f32 v151,v151\n" \
"ds_read_b128 a[48:51],v240 offset:61440\nM v[0:3],a[64:67],v[96:99],v[0:3]\nv_cvt_pk_bf16_f32 v199,v150,v151\nv_exp_f32 v160,v160\nM v[0:3],a[96:99],v[112:115],v[0:3]\nv_exp_f32 v161,v161\nds_read_b128 a[52:55],v240 offset:62464\nM v[8:11],a[68:71],v[96:99],v[8:11]\n" \
"v_exp_f32 v162,v162\nM v[8:11],a[100:103],v[112:115],v[8:11]\nv_exp_f32 v163,v163\nv_cvt_pk_bf16_f32 v208,v160,v161\nM v[16:19],a[72:75],v[96:99],v[16:19]\nv_cvt_pk_bf16_f32 v209,v162,v163\nds_write_b128 v250,a[244:247]\nM v[16:19],a[104:107],v[112:115],v[16:19]\n" \
"v_exp_f32 v164,v164\nds_read_b128 v[64:67],v250\nM v[24:27],a[76:79],v[96:99],v[24:27]\nv_exp_f32 v165,v165\nds_read_b128 v[68:71],v250\nM v[24:27],a[108:111],v[112:115],v[24:27]\nv_exp_f32 v166,v166\nds_read_b128 v[72:75],v250\n" \
"M v[32:35],a[80:83],v[96:99],v[32:35]\nv_exp_f32 v167,v167\nds_read_b128 v[76:79],v250\nM v[32:35],a[112:115],v[112:115],v[32:35]\nds_read_b128 v[80:83],v250\nds_read_b128 v[84:87],v250\nv_cvt_pk_bf16_f32 v212,v164,v165\nM v[40:43],a[84:87],v[96:99],v[40:43]\n" \
"ds_read_b128 v[88:91],v250\nv_cvt_pk_bf16_f32 v213,v166,v167\ns_cbranch_vccnz .Lps5\n.Lpb5:\nM v[40:43],a[116:119],v[112:115],v[40:43]\nds_read_b128 v[92:95],v250\nv_exp_f32 v176,v176\nM v[48:51],a[88:91],v[96:99],v[48:51]\n" \
"ds_read_b128 v[104:107],v250\nv_exp_f32 v177,v177\nM v[48:51],a[120:123],v[112:115],v[48:51]\nds_read_b128 v[108:111],v250\nv_cvt_pk_bf16_f32 v210,v176,v177\nM v[56:59],a[92:95],v[96:99],v[56:59]\nds_read_b128 v[120:123],v250\nv_exp_f32 v178,v178\n" \
"M v[56:59],a[124:127],v[112:115],v[56:59]\nds_read_b128 v[124:127],v250\nv_exp_f32 v179,v179\nM a[192:195],a[208:211],v[96:99],a[192:195]\nds_read_b128 v[96:99],v250\nv_cvt_pk_bf16_f32 v211,v178,v179\ns_add_u32 m0,s64,65536\nM a[192:195],a[208:211],v[112:115],a[192:195]\n" \
"ds_read_b128 v[112:115],v250\nv_exp_f32 v180,v180\nM v[4:7],a[64:67],v[100:103],v[4:7]\nds_read_b128 a[200:203],v250\nv_exp_f32 v181,v181\nM v[4:7],a[96:99],v[116:119],v[4:7]\nds_read_b128 a[204:207],v250\nv_exp_f32 v182,v182\n" \
"M v[12:15],a[68:71],v[100:103],v[12:15]\nds_read_b128 a[64:67],v241 offset:32768\nv_exp_f32 v183,v183\nM v[12:15],a[100:103],v[116:119],v[12:15]\nds_read_b128 a[96:99],v241 offset:40960\nv_cvt_pk_bf16_f32 v214,v180,v181\nv_cvt_pk_bf16_f32 v215,v182,v183\nM v[20:23],a[72:75],v[100:103],v[20:23]\n" \
"ds_read_b128 a[68:71],v241 offset:33792\nbuffer_load_dwordx4 v240,s[48:51],s36 offen offset:0 sc1 lds\nM v[20:23],a[104:107],v[116:119],v[20:23]\nds_read_b128 a[100:103],v241 offset:41984\nM v[28:31],a[76:79],v[100:103],v[28:31]\nds_read_b128 a[72:75],v241 offset:34816\nM v[28:31],a[108:111],v[116:119],v[28:31]\nds_read_b128 a[104:107],v241 offset:43008\n" \
"M v[36:39],a[80:83],v[100:103],v[36:39]\nds_read_b128 a[76:79],v241 offset:35840\nbuffer_load_dwordx4 v240,s[48:51],s36 offen offset:1024 sc1 lds\nM v[36:39],a[112:115],v[116:119],v[36:39]\nds_read_b128 a[108:111],v241 offset:44032\nM v[44:47],a[84:87],v[100:103],v[44:47]\nds_read_b128 a[80:83],v241 offset:36864\nM v[44:47],a[116:119],v[116:119],v[44:47]\n" \
"ds_read_b128 a[112:115],v241 offset:45056\nM v[52:55],a[88:91],v[100:103],v[52:55]\nds_read_b128 a[84:87],v241 offset:37888\nbuffer_load_dwordx4 v240,s[48:51],s36 offen offset:2048 sc1 lds\nM v[52:55],a[120:123],v[116:119],v[52:55]\nds_read_b128 a[116:119],v241 offset:46080\nM v[60:63],a[92:95],v[100:103],v[60:63]\nds_read_b128 a[88:91],v241 offset:38912\n" \
"M v[60:63],a[124:127],v[116:119],v[60:63]\nds_read_b128 a[120:123],v241 offset:47104\nM a[196:199],a[208:211],v[100:103],a[196:199]\nds_read_b128 v[100:103],v250\nbuffer_load_dwordx4 v240,s[48:51],s36 offen offset:3072 sc1 lds\nM a[196:199],a[208:211],v[116:119],a[196:199]\nds_read_b128 v[116:119],v250\ns_add_u32 m0,s64,16384\n" \
"M v[136:139],a[0:3],a[160:163],0\nds_read_b128 a[92:95],v241 offset:39936\nv_add_u32 v250,0xc000,v241\nM v[136:139],a[4:7],a[164:167],v[136:139]\nds_read_b128 a[124:127],v241 offset:48128\nM v[136:139],a[8:11],a[168:171],v[136:139]\nbuffer_load_dwordx4 v240,s[44:47],s65 offen offset:0 sc1 lds\nM v[136:139],a[12:15],a[172:175],v[136:139]\n" \
"M v[140:143],a[12:15],a[188:191],0\nM v[140:143],a[8:11],a[184:187],v[140:143]\nM v[140:143],a[4:7],a[180:183],v[140:143]\nbuffer_load_dwordx4 v240,s[44:47],s65 offen offset:1024 sc1 lds\nM v[140:143],a[0:3],a[176:179],v[140:143]\nM v[156:159],a[28:31],a[188:191],0\nM v[156:159],a[24:27],a[184:187],v[156:159]\nM v[156:159],a[20:23],a[180:183],v[156:159]\n" \
"v_max3_f32 v233,v136,v137,v138\nv_max_f32 v233,v233,v139\nv_cndmask_b32 v233,v232,v233,s[90:91]\nM v[156:159],a[16:19],a[176:179],v[156:159]\nbuffer_load_dwordx4 v240,s[44:47],s65 offen offset:2048 sc1 lds\nv_max3_f32 v237,v140,v141,v142\nM v[128:131],a[0:3],a[128:131],v[224:227]\nv_cndmask_b32 v136,v136,v252,s[82:83]\n" \
"v_cndmask_b32 v137,v137,v252,s[84:85]\nv_cndmask_b32 v138,v138,v252,s[86:87]\nM v[128:131],a[4:7],a[132:135],v[128:131]\nv_cndmask_b32 v139,v139,v252,s[88:89]\nv_max3_f32 v237,v237,v143,v156\nv_max3_f32 v237,v237,v157,v158\nM v[128:131],a[8:11],a[136:139],v[128:131]\nv_max_f32 v237,v237,v159\n" \
"v_cndmask_b32 v237,v236,v237,s[90:91]\nv_cndmask_b32 v156,v156,v252,s[82:83]\nM v[128:131],a[12:15],a[140:143],v[128:131]\nv_permlane16_swap_b32 v233,v237\nv_max_f32 v233,v233,v237\nv_mov_b32 v237,v233\nM v[132:135],a[12:15],a[156:159],v[228:231]\nbuffer_load_dwordx4 v240,s[44:47],s65 offen offset:3072 sc1 lds\n" \
"v_permlane32_swap_b32 v233,v237\nM v[132:135],a[8:11],a[152:155],v[132:135]\nv_max_f32 v233,v233,v237\ns_add_u32 m0,s64,81920\nv_mov_b32 v237,v233\nM v[132:135],a[4:7],a[148:151],v[132:135]\nv_cndmask_b32 v157,v157,v252,s[84:85]\nv_permlane16_swap_b32 v233,v237\n" \
"v_sub_f32 v232,0,v233\nM v[132:135],a[0:3],a[144:147],v[132:135]\nds_read_b128 a[0:3],v240 offset:0\nds_read_b128 a[4:7],v240 offset:1024\nv_add_f32 v136,v136,v232\nM v[144:147],a[16:19],a[128:131],v[224:227]\nds_read_b128 a[8:11],v240 offset:2048\nv_add_f32 v137,v137,v232\n" \
"M v[144:147],a[20:23],a[132:135],v[144:147]\nds_read_b128 a[12:15],v240 offset:3072\nbuffer_load_dwordx4 v240,s[48:51],s65 offen offset:0 sc1 lds\nM v[144:147],a[24:27],a[136:139],v[144:147]\nv_add_f32 v138,v138,v232\nv_add_f32 v139,v139,v232\nv_cndmask_b32 v158,v158,v252,s[86:87]\nM v[144:147],a[28:31],a[140:143],v[144:147]\n" \
"v_cndmask_b32 v159,v159,v252,s[88:89]\nv_sub_f32 v236,0,v237\nv_sub_f32 v234,0,v233\nM v[148:151],a[28:31],a[156:159],v[228:231]\nv_exp_f32 v136,v136\nv_sub_f32 v235,0,v233\nM v[148:151],a[24:27],a[152:155],v[148:151]\nbuffer_load_dwordx4 v240,s[48:51],s65 offen offset:1024 sc1 lds\n" \
"v_sub_f32 v238,0,v237\nM v[148:151],a[20:23],a[148:151],v[148:151]\nv_exp_f32 v137,v137\nv_sub_f32 v239,0,v237\nM v[148:151],a[16:19],a[144:147],v[148:151]\nds_read_b128 a[16:19],v240 offset:4096\nds_read_b128 a[20:23],v240 offset:5120\ns_waitcnt lgkmcnt(5)\n" \
"M v[160:163],a[32:35],a[128:131],v[224:227]\nds_read_b128 a[24:27],v240 offset:6144\nv_exp_f32 v138,v138\nM v[160:163],a[36:39],a[132:135],v[160:163]\nds_read_b128 a[28:31],v240 offset:7168\nbuffer_load_dwordx4 v240,s[48:51],s65 offen offset:2048 sc1 lds\nM v[160:163],a[40:43],a[136:139],v[160:163]\nv_exp_f32 v139,v139\n" \
"v_add_f32 v140,v140,v236\nM v[160:163],a[44:47],a[140:143],v[160:163]\nv_exp_f32 v128,v128\nM v[164:167],a[44:47],a[156:159],v[228:231]\ns_barrier\nv_exp_f32 v129,v129\nM v[164:167],a[40:43],a[152:155],v[164:167]\nbuffer_load_dwordx4 v240,s[48:51],s65 offen offset:3072 sc1 lds\n" \
"s_add_u32 m0,s64,32768\nM v[164:167],a[36:39],a[148:151],v[164:167]\nv_exp_f32 v130,v130\nv_add_f32 v141,v141,v236\nM v[164:167],a[32:35],a[144:147],v[164:167]\nds_read_b128 a[32:35],v240 offset:8192\nds_read_b128 a[36:39],v240 offset:9216\nv_add_f32 v142,v142,v236\n" \
"M v[176:179],a[48:51],a[128:131],v[224:227]\nds_read_b128 a[40:43],v240 offset:10240\nv_exp_f32 v131,v131\nM v[176:179],a[52:55],a[132:135],v[176:179]\nds_read_b128 a[44:47],v240 offset:11264\nbuffer_load_dwordx4 v240,s[44:47],s66 offen offset:0 sc1 lds\nM v[176:179],a[56:59],a[136:139],v[176:179]\nv_exp_f32 v132,v132\n" \
"v_add_f32 v143,v143,v236\nM v[176:179],a[60:63],a[140:143],v[176:179]\nv_exp_f32 v133,v133\nv_add_f32 v156,v156,v236\nM v[180:183],a[60:63],a[156:159],v[228:231]\nv_exp_f32 v134,v134\nv_add_f32 v157,v157,v236\nM v[180:183],a[56:59],a[152:155],v[180:183]\n" \
"buffer_load_dwordx4 v240,s[44:47],s66 offen offset:1024 sc1 lds\nv_add_f32 v158,v158,v236\nM v[180:183],a[52:55],a[148:151],v[180:183]\nv_exp_f32 v135,v135\nM v[180:183],a[48:51],a[144:147],v[180:183]\nds_read_b128 a[48:51],v240 offset:12288\nds_read_b128 a[52:55],v240 offset:13312\nv_add_f32 v159,v159,v236\n" \
"M v[0:3],a[64:67],v[192:195],v[0:3]\nds_read_b128 a[56:59],v240 offset:14336\nv_exp_f32 v144,v144\nM v[0:3],a[96:99],v[208:211],v[0:3]\nds_read_b128 a[60:63],v240 offset:15360\nbuffer_load_dwordx4 v240,s[44:47],s66 offen offset:2048 sc1 lds\nM v[8:11],a[68:71],v[192:195],v[8:11]\nv_exp_f32 v145,v145\n" \
"v_sub_f32 v233,0,v233\nM v[8:11],a[100:103],v[208:211],v[8:11]\nv_exp_f32 v146,v146\nv_sub_f32 v237,0,v237\nM v[16:19],a[72:75],v[192:195],v[16:19]\nv_exp_f32 v147,v147\nv_cvt_pk_bf16_f32 v200,v136,v137\nM v[16:19],a[104:107],v[208:211],v[16:19]\n" \
"buffer_load_dwordx4 v240,s[44:47],s66 offen offset:3072 sc1 lds\ns_add_u32 m0,s64,98304\nM v[24:27],a[76:79],v[192:195],v[24:27]\nv_exp_f32 v148,v148\nv_cvt_pk_bf16_f32 v201,v138,v139\nM v[24:27],a[108:111],v[208:211],v[24:27]\nv_exp_f32 v149,v149\nM v[32:35],a[80:83],v[192:195],v[32:35]\n" \
"v_exp_f32 v150,v150\nM v[32:35],a[112:115],v[208:211],v[32:35]\nv_exp_f32 v151,v151\nM v[40:43],a[84:87],v[192:195],v[40:43]\nv_exp_f32 v160,v160\nM v[40:43],a[116:119],v[208:211],v[40:43]\nv_exp_f32 v161,v161\nM v[48:51],a[88:91],v[192:195],v[48:51]\n" \
"v_exp_f32 v162,v162\nM v[48:51],a[120:123],v[208:211],v[48:51]\nv_exp_f32 v163,v163\nM v[56:59],a[92:95],v[192:195],v[56:59]\nv_exp_f32 v176,v176\nM v[56:59],a[124:127],v[208:211],v[56:59]\nv_exp_f32 v177,v177\nM a[192:195],a[208:211],v[192:195],a[192:195]\n" \
"v_exp_f32 v178,v178\nv_cvt_pk_bf16_f32 v192,v128,v129\nM a[192:195],a[208:211],v[208:211],a[192:195]\nv_exp_f32 v179,v179\nv_cvt_pk_bf16_f32 v193,v130,v131\nM v[4:7],a[64:67],v[196:199],v[4:7]\nv_exp_f32 v140,v140\nv_cvt_pk_bf16_f32 v194,v144,v145\n" \
"M v[4:7],a[96:99],v[212:215],v[4:7]\nv_exp_f32 v141,v141\nv_cvt_pk_bf16_f32 v195,v146,v147\nM v[12:15],a[68:71],v[196:199],v[12:15]\nv_exp_f32 v142,v142\nM v[12:15],a[100:103],v[212:215],v[12:15]\nv_exp_f32 v143,v143\nv_cvt_pk_bf16_f32 v208,v160,v161\n" \
"M v[20:23],a[72:75],v[196:199],v[20:23]\nv_exp_f32 v156,v156\nv_cvt_pk_bf16_f32 v209,v162,v163\nM v[20:23],a[104:107],v[212:215],v[20:23]\nv_exp_f32 v157,v157\nv_cvt_pk_bf16_f32 v210,v176,v177\nM v[28:31],a[76:79],v[196:199],v[28:31]\nv_exp_f32 v158,v158\n" \
"v_cvt_pk_bf16_f32 v211,v178,v179\nM v[28:31],a[108:111],v[212:215],v[28:31]\nv_exp_f32 v159,v159\nM v[36:39],a[80:83],v[196:199],v[36:39]\nv_exp_f32 v164,v164\nM v[36:39],a[112:115],v[212:215],v[36:39]\nv_exp_f32 v165,v165\nM v[44:47],a[84:87],v[196:199],v[44:47]\n" \
"v_exp_f32 v166,v166\nM v[44:47],a[116:119],v[212:215],v[44:47]\nv_exp_f32 v167,v167\nM v[52:55],a[88:91],v[196:199],v[52:55]\nv_exp_f32 v180,v180\nM v[52:55],a[120:123],v[212:215],v[52:55]\nv_exp_f32 v181,v181\nM v[60:63],a[92:95],v[196:199],v[60:63]\n" \
"v_exp_f32 v182,v182\nM v[60:63],a[124:127],v[212:215],v[60:63]\nv_exp_f32 v183,v183\nM a[196:199],a[208:211],v[196:199],a[196:199]\nv_cvt_pk_bf16_f32 v196,v132,v133\nv_cvt_pk_bf16_f32 v197,v134,v135\nv_cvt_pk_bf16_f32 v198,v148,v149\nv_cvt_pk_bf16_f32 v199,v150,v151\n" \
"buffer_load_dwordx4 v240,s[48:51],s66 offen offset:0 sc1 lds\nbuffer_load_dwordx4 v240,s[48:51],s66 offen offset:1024 sc1 lds\nbuffer_load_dwordx4 v240,s[48:51],s66 offen offset:2048 sc1 lds\nbuffer_load_dwordx4 v240,s[48:51],s66 offen offset:3072 sc1 lds\nM a[196:199],a[208:211],v[212:215],a[196:199]\ns_cmp_eq_u32 s62,0\ns_cbranch_scc1 .Ltrans\n.Ldual:\n" \
"s_waitcnt lgkmcnt(4)\nM v[128:131],a[0:3],a[128:131],v[224:227]\ns_add_u32 m0,s94,s64\nv_cvt_pk_bf16_f32 v204,v140,v141\nM v[128:131],a[4:7],a[132:135],v[128:131]\nds_read_b128 a[64:67],v250 offset:0\nds_read_b128 a[96:99],v250 offset:8192\nM v[128:131],a[8:11],a[136:139],v[128:131]\n" \
"v_cvt_pk_bf16_f32 v205,v142,v143\nv_cvt_pk_bf16_f32 v206,v156,v157\nM v[128:131],a[12:15],a[140:143],v[128:131]\nv_cvt_pk_bf16_f32 v207,v158,v159\nv_cvt_pk_bf16_f32 v212,v164,v165\nM v[132:135],a[12:15],a[156:159],v[228:231]\nbuffer_load_dwordx4 v240,s[44:47],s95 offen offset:0 sc1 lds\nM v[132:135],a[8:11],a[152:155],v[132:135]\n" \
"v_cvt_pk_bf16_f32 v213,v166,v167\nv_cvt_pk_bf16_f32 v216,v168,v169\nM v[132:135],a[4:7],a[148:151],v[132:135]\nv_cvt_pk_bf16_f32 v217,v170,v171\nv_cvt_pk_bf16_f32 v214,v180,v181\nM v[132:135],a[0:3],a[144:147],v[132:135]\nds_read_b128 a[68:71],v250 offset:1024\nds_read_b128 a[100:103],v250 offset:9216\n" \
"M v[136:139],a[0:3],a[160:163],v[232:235]\nv_exp_f32 v172,v172\nM v[136:139],a[4:7],a[164:167],v[136:139]\nv_exp_f32 v173,v173\nM v[136:139],a[8:11],a[168:171],v[136:139]\nv_exp_f32 v174,v174\nM v[136:139],a[12:15],a[172:175],v[136:139]\nv_exp_f32 v175,v175\n" \
"M v[140:143],a[12:15],a[188:191],v[236:239]\nv_cvt_pk_bf16_f32 v220,v172,v173\nv_cvt_pk_bf16_f32 v221,v174,v175\nM v[140:143],a[8:11],a[184:187],v[140:143]\nds_read_b128 a[72:75],v250 offset:2048\nds_read_b128 a[104:107],v250 offset:10240\nM v[140:143],a[4:7],a[180:183],v[140:143]\nv_cvt_pk_bf16_f32 v215,v182,v183\n" \
"s_add_u32 s94,s94,0x4000\nM v[140:143],a[0:3],a[176:179],v[140:143]\nv_exp_f32 v184,v184\nM v[144:147],a[16:19],a[128:131],v[224:227]\nv_exp_f32 v185,v185\nM v[144:147],a[20:23],a[132:135],v[144:147]\nv_exp_f32 v186,v186\nM v[144:147],a[24:27],a[136:139],v[144:147]\n" \
"v_exp_f32 v187,v187\nM v[144:147],a[28:31],a[140:143],v[144:147]\nds_read_b128 a[76:79],v250 offset:3072\nds_read_b128 a[108:111],v250 offset:11264\nM v[148:151],a[28:31],a[156:159],v[228:231]\nv_cvt_pk_bf16_f32 v218,v184,v185\nv_cvt_pk_bf16_f32 v219,v186,v187\nM v[148:151],a[24:27],a[152:155],v[148:151]\n" \
"v_exp_f32 v188,v188\nM v[148:151],a[20:23],a[148:151],v[148:151]\nbuffer_load_dwordx4 v240,s[44:47],s95 offen offset:1024 sc1 lds\nM v[148:151],a[16:19],a[144:147],v[148:151]\nv_exp_f32 v189,v189\nM v[152:155],a[16:19],a[160:163],v[232:235]\nv_exp_f32 v190,v190\nM v[152:155],a[20:23],a[164:167],v[152:155]\n" \
"ds_read_b128 a[80:83],v250 offset:4096\nds_read_b128 a[112:115],v250 offset:12288\nM v[152:155],a[24:27],a[168:171],v[152:155]\nv_exp_f32 v191,v191\nM v[152:155],a[28:31],a[172:175],v[152:155]\nv_cvt_pk_bf16_f32 v222,v188,v189\nv_cvt_pk_bf16_f32 v223,v190,v191\nM v[156:159],a[28:31],a[188:191],v[236:239]\n" \
"v_exp_f32 v128,v128\nM v[156:159],a[24:27],a[184:187],v[156:159]\nv_exp_f32 v129,v129\nM v[156:159],a[20:23],a[180:183],v[156:159]\nv_exp_f32 v130,v130\nM v[156:159],a[16:19],a[176:179],v[156:159]\nds_read_b128 a[84:87],v250 offset:5120\nds_read_b128 a[116:119],v250 offset:13312\n" \
"M v[160:163],a[32:35],a[128:131],v[224:227]\nv_exp_f32 v131,v131\nM v[160:163],a[36:39],a[132:135],v[160:163]\nv_exp_f32 v144,v144\nM v[160:163],a[40:43],a[136:139],v[160:163]\nv_exp_f32 v145,v145\nM v[160:163],a[44:47],a[140:143],v[160:163]\nv_exp_f32 v146,v146\n" \
"M v[164:167],a[44:47],a[156:159],v[228:231]\nv_exp_f32 v147,v147\nM v[164:167],a[40:43],a[152:155],v[164:167]\nds_read_b128 a[88:91],v250 offset:6144\nds_read_b128 a[120:123],v250 offset:14336\nM v[164:167],a[36:39],a[148:151],v[164:167]\nv_exp_f32 v132,v132\nM v[164:167],a[32:35],a[144:147],v[164:167]\n" \
"v_exp_f32 v133,v133\nM v[168:171],a[32:35],a[160:163],v[232:235]\nbuffer_load_dwordx4 v240,s[44:47],s95 offen offset:2048 sc1 lds\nM v[168:171],a[36:39],a[164:167],v[168:171]\nv_exp_f32 v134,v134\nM v[168:171],a[40:43],a[168:171],v[168:171]\nv_exp_f32 v135,v135\nM v[168:171],a[44:47],a[172:175],v[168:171]\n" \
"ds_read_b128 a[92:95],v250 offset:7168\nds_read_b128 a[124:127],v250 offset:15360\nM v[172:175],a[44:47],a[188:191],v[236:239]\nv_xor_b32 v250,0x18000,v250\ns_and_b32 s94,s94,0xffff\nM v[172:175],a[40:43],a[184:187],v[172:175]\nv_exp_f32 v148,v148\nM v[172:175],a[36:39],a[180:183],v[172:175]\n" \
"v_exp_f32 v149,v149\nM v[172:175],a[32:35],a[176:179],v[172:175]\ns_waitcnt lgkmcnt(4)\nM v[176:179],a[48:51],a[128:131],v[224:227]\nv_exp_f32 v150,v150\nM v[176:179],a[52:55],a[132:135],v[176:179]\nv_exp_f32 v151,v151\nM v[176:179],a[56:59],a[136:139],v[176:179]\n" \
"v_exp_f32 v136,v136\nM v[176:179],a[60:63],a[140:143],v[176:179]\nv_exp_f32 v137,v137\nM v[180:183],a[60:63],a[156:159],v[228:231]\nv_exp_f32 v138,v138\nM v[180:183],a[56:59],a[152:155],v[180:183]\nv_exp_f32 v139,v139\nM v[180:183],a[52:55],a[148:151],v[180:183]\n" \
"v_exp_f32 v152,v152\nM v[180:183],a[48:51],a[144:147],v[180:183]\nv_exp_f32 v153,v153\nM v[184:187],a[48:51],a[160:163],v[232:235]\nv_exp_f32 v154,v154\nM v[184:187],a[52:55],a[164:167],v[184:187]\nv_exp_f32 v155,v155\nM v[184:187],a[56:59],a[168:171],v[184:187]\n" \
"buffer_load_dwordx4 v240,s[44:47],s95 offen offset:3072 sc1 lds\nM v[184:187],a[60:63],a[172:175],v[184:187]\nv_exp_f32 v160,v160\nM v[188:191],a[60:63],a[188:191],v[236:239]\nv_exp_f32 v161,v161\nM v[188:191],a[56:59],a[184:187],v[188:191]\nv_exp_f32 v162,v162\nM v[188:191],a[52:55],a[180:183],v[188:191]\n" \
"v_exp_f32 v163,v163\nM v[188:191],a[48:51],a[176:179],v[188:191]\nv_exp_f32 v176,v176\nM v[0:3],a[64:67],v[192:195],v[0:3]\ns_waitcnt vmcnt(16) lgkmcnt(0)\ns_barrier\nM v[0:3],a[96:99],v[208:211],v[0:3]\nds_read_b128 a[0:3],v250 offset:0\n" \
"ds_read_b128 a[4:7],v250 offset:1024\nM v[8:11],a[68:71],v[192:195],v[8:11]\nv_exp_f32 v177,v177\nM v[8:11],a[100:103],v[208:211],v[8:11]\nv_exp_f32 v178,v178\nM v[16:19],a[72:75],v[192:195],v[16:19]\ns_add_u32 m0,m0,0x10000\nM v[16:19],a[104:107],v[208:211],v[16:19]\n" \
"buffer_load_dwordx4 v240,s[48:51],s95 offen offset:0 sc1 lds\nM v[24:27],a[76:79],v[192:195],v[24:27]\nv_exp_f32 v179,v179\nM v[24:27],a[108:111],v[208:211],v[24:27]\nv_exp_f32 v140,v140\nM v[32:35],a[80:83],v[192:195],v[32:35]\nv_exp_f32 v141,v141\nM v[32:35],a[112:115],v[208:211],v[32:35]\n" \
"ds_read_b128 a[8:11],v250 offset:2048\nds_read_b128 a[12:15],v250 offset:3072\nM v[40:43],a[84:87],v[192:195],v[40:43]\nv_exp_f32 v142,v142\nM v[40:43],a[116:119],v[208:211],v[40:43]\nv_exp_f32 v143,v143\nM v[48:51],a[88:91],v[192:195],v[48:51]\nv_exp_f32 v156,v156\n" \
"M v[48:51],a[120:123],v[208:211],v[48:51]\nv_exp_f32 v157,v157\nM v[56:59],a[92:95],v[192:195],v[56:59]\nv_exp_f32 v158,v158\nM v[56:59],a[124:127],v[208:211],v[56:59]\nv_exp_f32 v159,v159\nM a[192:195],a[208:211],v[192:195],a[192:195]\nv_exp_f32 v164,v164\n" \
"M a[192:195],a[208:211],v[208:211],a[192:195]\nds_read_b128 a[16:19],v250 offset:4096\nds_read_b128 a[20:23],v250 offset:5120\nM v[4:7],a[64:67],v[196:199],v[4:7]\nv_exp_f32 v165,v165\nM v[4:7],a[96:99],v[212:215],v[4:7]\nv_exp_f32 v166,v166\nM v[12:15],a[68:71],v[196:199],v[12:15]\n" \
"v_exp_f32 v167,v167\nM v[12:15],a[100:103],v[212:215],v[12:15]\nbuffer_load_dwordx4 v240,s[48:51],s95 offen offset:1024 sc1 lds\nM v[20:23],a[72:75],v[196:199],v[20:23]\nv_exp_f32 v180,v180\nM v[20:23],a[104:107],v[212:215],v[20:23]\nv_exp_f32 v181,v181\nM v[28:31],a[76:79],v[196:199],v[28:31]\n" \
"v_exp_f32 v182,v182\nM v[28:31],a[108:111],v[212:215],v[28:31]\nds_read_b128 a[24:27],v250 offset:6144\nds_read_b128 a[28:31],v250 offset:7168\nM v[36:39],a[80:83],v[196:199],v[36:39]\nv_exp_f32 v183,v183\nM v[36:39],a[112:115],v[212:215],v[36:39]\nv_exp_f32 v168,v168\n" \
"M v[44:47],a[84:87],v[196:199],v[44:47]\nv_exp_f32 v169,v169\nM v[44:47],a[116:119],v[212:215],v[44:47]\nv_exp_f32 v170,v170\nM v[52:55],a[88:91],v[196:199],v[52:55]\nv_exp_f32 v171,v171\nM v[52:55],a[120:123],v[212:215],v[52:55]\nv_cvt_pk_bf16_f32 v192,v128,v129\n" \
"v_cvt_pk_bf16_f32 v193,v130,v131\nM v[60:63],a[92:95],v[196:199],v[60:63]\nv_cvt_pk_bf16_f32 v194,v144,v145\nv_cvt_pk_bf16_f32 v195,v146,v147\nM v[60:63],a[124:127],v[212:215],v[60:63]\nds_read_b128 a[32:35],v250 offset:8192\nds_read_b128 a[36:39],v250 offset:9216\nM a[196:199],a[208:211],v[196:199],a[196:199]\n" \
"v_cvt_pk_bf16_f32 v208,v160,v161\nv_cvt_pk_bf16_f32 v209,v162,v163\nM a[196:199],a[208:211],v[212:215],a[196:199]\nv_cvt_pk_bf16_f32 v210,v176,v177\nv_cvt_pk_bf16_f32 v211,v178,v179\nM v[64:67],a[64:67],v[200:203],v[64:67]\nv_cvt_pk_bf16_f32 v196,v132,v133\nv_cvt_pk_bf16_f32 v197,v134,v135\n" \
"M v[64:67],a[96:99],v[216:219],v[64:67]\nbuffer_load_dwordx4 v240,s[48:51],s95 offen offset:2048 sc1 lds\nM v[72:75],a[68:71],v[200:203],v[72:75]\nv_cvt_pk_bf16_f32 v198,v148,v149\nv_cvt_pk_bf16_f32 v199,v150,v151\nM v[72:75],a[100:103],v[216:219],v[72:75]\nM v[80:83],a[72:75],v[200:203],v[80:83]\nM v[80:83],a[104:107],v[216:219],v[80:83]\n" \
"ds_read_b128 a[40:43],v250 offset:10240\nds_read_b128 a[44:47],v250 offset:11264\nM v[88:91],a[76:79],v[200:203],v[88:91]\nM v[88:91],a[108:111],v[216:219],v[88:91]\nM v[96:99],a[80:83],v[200:203],v[96:99]\nM v[96:99],a[112:115],v[216:219],v[96:99]\nM v[104:107],a[84:87],v[200:203],v[104:107]\nM v[104:107],a[116:119],v[216:219],v[104:107]\n" \
"M v[112:115],a[88:91],v[200:203],v[112:115]\nM v[112:115],a[120:123],v[216:219],v[112:115]\nds_read_b128 a[48:51],v250 offset:12288\nds_read_b128 a[52:55],v250 offset:13312\nM v[120:123],a[92:95],v[200:203],v[120:123]\nM v[120:123],a[124:127],v[216:219],v[120:123]\nM a[200:203],a[208:211],v[200:203],a[200:203]\nv_cvt_pk_bf16_f32 v200,v136,v137\n" \
"v_cvt_pk_bf16_f32 v201,v138,v139\nM a[200:203],a[208:211],v[216:219],a[200:203]\nbuffer_load_dwordx4 v240,s[48:51],s95 offen offset:3072 sc1 lds\nM v[68:71],a[64:67],v[204:207],v[68:71]\nv_cvt_pk_bf16_f32 v202,v152,v153\nv_cvt_pk_bf16_f32 v203,v154,v155\nM v[68:71],a[96:99],v[220:223],v[68:71]\ns_cmp_eq_u32 s95,s96\n" \
"s_cselect_b32 s72,0xc000,s98\ns_add_u32 s95,s95,s72\nM v[76:79],a[68:71],v[204:207],v[76:79]\nM v[76:79],a[100:103],v[220:223],v[76:79]\nds_read_b128 a[56:59],v250 offset:14336\nds_read_b128 a[60:63],v250 offset:15360\nM v[84:87],a[72:75],v[204:207],v[84:87]\nv_add_u32 v250,s94,v241\n" \
"M v[84:87],a[104:107],v[220:223],v[84:87]\nM v[92:95],a[76:79],v[204:207],v[92:95]\nM v[92:95],a[108:111],v[220:223],v[92:95]\nM v[100:103],a[80:83],v[204:207],v[100:103]\nM v[100:103],a[112:115],v[220:223],v[100:103]\nM v[108:111],a[84:87],v[204:207],v[108:111]\nM v[108:111],a[116:119],v[220:223],v[108:111]\nM v[116:119],a[88:91],v[204:207],v[116:119]\n" \
"buffer_load_dword v247,v254,s[56:59],s1 offen\nM v[116:119],a[120:123],v[220:223],v[116:119]\ns_add_u32 s1,s1,s0\nM v[124:127],a[92:95],v[204:207],v[124:127]\nM v[124:127],a[124:127],v[220:223],v[124:127]\nM a[204:207],a[208:211],v[204:207],a[204:207]\nM a[204:207],a[208:211],v[220:223],a[204:207]\ns_cmp_lg_u32 s95,s97\n" \
"s_cbranch_scc1 .Ldual\ns_cmp_lt_u32 s62,4\ns_cbranch_scc1 .Ltrans\n.Ldf:\ns_waitcnt lgkmcnt(4)\nM v[128:131],a[0:3],a[128:131],v[224:227]\ns_add_u32 m0,s94,s64\nv_cvt_pk_bf16_f32 v204,v140,v141\n" \
"M v[128:131],a[4:7],a[132:135],v[128:131]\nds_read_b128 a[64:67],v250 offset:0\nds_read_b128 a[96:99],v250 offset:8192\nM v[128:131],a[8:11],a[136:139],v[128:131]\nv_cvt_pk_bf16_f32 v205,v142,v143\nv_lshrrev_b32 v248,2,v249\nM v[128:131],a[12:15],a[140:143],v[128:131]\nv_mul_u32_u24 v248,3584,v248\n" \
"v_and_b32 v251,3,v249\nM v[132:135],a[12:15],a[156:159],v[228:231]\nbuffer_load_dwordx4 v240,s[44:47],s95 offen offset:0 sc1 lds\nM v[132:135],a[8:11],a[152:155],v[132:135]\ns_mul_i32 s70,s7,458752\nv_lshl_add_u32 v248,v251,3,v248\nM v[132:135],a[4:7],a[148:151],v[132:135]\ns_add_u32 s70,s70,s2\n" \
"v_add_u32 v248,s70,v248\nM v[132:135],a[0:3],a[144:147],v[132:135]\nds_read_b128 a[68:71],v250 offset:1024\nds_read_b128 a[100:103],v250 offset:9216\nM v[136:139],a[0:3],a[160:163],v[232:235]\nv_and_b32 v248,s3,v248\nv_cvt_pk_bf16_f32 v206,v156,v157\nM v[136:139],a[4:7],a[164:167],v[136:139]\n" \
"v_cvt_pk_bf16_f32 v207,v158,v159\nv_cvt_pk_bf16_f32 v212,v164,v165\nM v[136:139],a[8:11],a[168:171],v[136:139]\nv_cvt_pk_bf16_f32 v213,v166,v167\nv_cvt_pk_bf16_f32 v216,v168,v169\nM v[136:139],a[12:15],a[172:175],v[136:139]\nv_cvt_pk_bf16_f32 v217,v170,v171\ns_and_b32 s71,s3,57344\n" \
"M v[140:143],a[12:15],a[188:191],v[236:239]\nv_exp_f32 v172,v172\nM v[140:143],a[8:11],a[184:187],v[140:143]\nds_read_b128 a[72:75],v250 offset:2048\nds_read_b128 a[104:107],v250 offset:10240\nM v[140:143],a[4:7],a[180:183],v[140:143]\nglobal_load_dwordx2 a[212:213],v248,s[22:23]\nM v[140:143],a[0:3],a[176:179],v[140:143]\n" \
"v_exp_f32 v173,v173\nM v[144:147],a[16:19],a[128:131],v[224:227]\nv_exp_f32 v174,v174\nM v[144:147],a[20:23],a[132:135],v[144:147]\nv_exp_f32 v175,v175\nM v[144:147],a[24:27],a[136:139],v[144:147]\nv_cvt_pk_bf16_f32 v220,v172,v173\nv_cvt_pk_bf16_f32 v221,v174,v175\n" \
"M v[144:147],a[28:31],a[140:143],v[144:147]\nds_read_b128 a[76:79],v250 offset:3072\nds_read_b128 a[108:111],v250 offset:11264\nM v[148:151],a[28:31],a[156:159],v[228:231]\nv_cvt_pk_bf16_f32 v214,v180,v181\nv_cvt_pk_bf16_f32 v215,v182,v183\nM v[148:151],a[24:27],a[152:155],v[148:151]\nv_exp_f32 v184,v184\n" \
"M v[148:151],a[20:23],a[148:151],v[148:151]\nbuffer_load_dwordx4 v240,s[44:47],s95 offen offset:1024 sc1 lds\nM v[148:151],a[16:19],a[144:147],v[148:151]\nv_exp_f32 v185,v185\nM v[152:155],a[16:19],a[160:163],v[232:235]\nv_exp_f32 v186,v186\nM v[152:155],a[20:23],a[164:167],v[152:155]\nds_read_b128 a[80:83],v250 offset:4096\n" \
"ds_read_b128 a[112:115],v250 offset:12288\nM v[152:155],a[24:27],a[168:171],v[152:155]\nv_exp_f32 v187,v187\nM v[152:155],a[28:31],a[172:175],v[152:155]\nv_cvt_pk_bf16_f32 v218,v184,v185\nv_cvt_pk_bf16_f32 v219,v186,v187\nM v[156:159],a[28:31],a[188:191],v[236:239]\nv_exp_f32 v188,v188\n" \
"M v[156:159],a[24:27],a[184:187],v[156:159]\nv_exp_f32 v189,v189\nM v[156:159],a[20:23],a[180:183],v[156:159]\nv_exp_f32 v190,v190\nM v[156:159],a[16:19],a[176:179],v[156:159]\nds_read_b128 a[84:87],v250 offset:5120\nds_read_b128 a[116:119],v250 offset:13312\nM v[160:163],a[32:35],a[128:131],v[224:227]\n" \
"global_load_dwordx2 a[228:229],v248,s[24:25]\nM v[160:163],a[36:39],a[132:135],v[160:163]\nv_add_u32 v248,s71,v248\nv_cvt_pk_bf16_f32 v222,v188,v189\nM v[160:163],a[40:43],a[136:139],v[160:163]\nv_exp_f32 v191,v191\nM v[160:163],a[44:47],a[140:143],v[160:163]\nv_cvt_pk_bf16_f32 v223,v190,v191\n" \
"s_add_u32 s94,s94,0x4000\nM v[164:167],a[44:47],a[156:159],v[228:231]\nv_exp_f32 v128,v128\nM v[164:167],a[40:43],a[152:155],v[164:167]\nds_read_b128 a[88:91],v250 offset:6144\nds_read_b128 a[120:123],v250 offset:14336\nM v[164:167],a[36:39],a[148:151],v[164:167]\nv_exp_f32 v129,v129\n" \
"M v[164:167],a[32:35],a[144:147],v[164:167]\nv_exp_f32 v130,v130\nM v[168:171],a[32:35],a[160:163],v[232:235]\nbuffer_load_dwordx4 v240,s[44:47],s95 offen offset:2048 sc1 lds\nM v[168:171],a[36:39],a[164:167],v[168:171]\nv_exp_f32 v131,v131\nM v[168:171],a[40:43],a[168:171],v[168:171]\nv_exp_f32 v144,v144\n" \
"M v[168:171],a[44:47],a[172:175],v[168:171]\nds_read_b128 a[92:95],v250 offset:7168\nds_read_b128 a[124:127],v250 offset:15360\nM v[172:175],a[44:47],a[188:191],v[236:239]\nv_xor_b32 v250,0x18000,v250\ns_and_b32 s94,s94,0xffff\nM v[172:175],a[40:43],a[184:187],v[172:175]\nv_exp_f32 v145,v145\n" \
"M v[172:175],a[36:39],a[180:183],v[172:175]\nv_exp_f32 v146,v146\nM v[172:175],a[32:35],a[176:179],v[172:175]\ns_waitcnt lgkmcnt(4)\nM v[176:179],a[48:51],a[128:131],v[224:227]\nv_exp_f32 v147,v147\nM v[176:179],a[52:55],a[132:135],v[176:179]\nv_exp_f32 v132,v132\n" \
"M v[176:179],a[56:59],a[136:139],v[176:179]\nglobal_load_dwordx2 a[214:215],v248,s[22:23]\nM v[176:179],a[60:63],a[140:143],v[176:179]\nv_exp_f32 v133,v133\nM v[180:183],a[60:63],a[156:159],v[228:231]\nv_exp_f32 v134,v134\nM v[180:183],a[56:59],a[152:155],v[180:183]\nv_exp_f32 v135,v135\n" \
"M v[180:183],a[52:55],a[148:151],v[180:183]\nv_exp_f32 v148,v148\nM v[180:183],a[48:51],a[144:147],v[180:183]\nv_exp_f32 v149,v149\nM v[184:187],a[48:51],a[160:163],v[232:235]\nv_exp_f32 v150,v150\nM v[184:187],a[52:55],a[164:167],v[184:187]\nv_exp_f32 v151,v151\n" \
"M v[184:187],a[56:59],a[168:171],v[184:187]\nbuffer_load_dwordx4 v240,s[44:47],s95 offen offset:3072 sc1 lds\nM v[184:187],a[60:63],a[172:175],v[184:187]\nv_exp_f32 v136,v136\nM v[188:191],a[60:63],a[188:191],v[236:239]\nv_exp_f32 v137,v137\nM v[188:191],a[56:59],a[184:187],v[188:191]\nv_exp_f32 v138,v138\n" \
"M v[188:191],a[52:55],a[180:183],v[188:191]\nglobal_load_dwordx2 a[230:231],v248,s[24:25]\nM v[188:191],a[48:51],a[176:179],v[188:191]\ns_add_u32 m0,m0,0x10000\nv_add_u32 v248,s71,v248\nM v[0:3],a[64:67],v[192:195],v[0:3]\ns_waitcnt vmcnt(16) lgkmcnt(0)\ns_barrier\n" \
"M v[0:3],a[96:99],v[208:211],v[0:3]\nds_read_b128 a[0:3],v250 offset:0\nds_read_b128 a[4:7],v250 offset:1024\nM v[8:11],a[68:71],v[192:195],v[8:11]\nv_exp_f32 v139,v139\nM v[8:11],a[100:103],v[208:211],v[8:11]\nv_exp_f32 v152,v152\nM v[16:19],a[72:75],v[192:195],v[16:19]\n" \
"v_exp_f32 v153,v153\nM v[16:19],a[104:107],v[208:211],v[16:19]\nbuffer_load_dwordx4 v240,s[48:51],s95 offen offset:0 sc1 lds\nM v[24:27],a[76:79],v[192:195],v[24:27]\nv_exp_f32 v154,v154\nM v[24:27],a[108:111],v[208:211],v[24:27]\nv_exp_f32 v155,v155\nM v[32:35],a[80:83],v[192:195],v[32:35]\n" \
"v_exp_f32 v160,v160\nM v[32:35],a[112:115],v[208:211],v[32:35]\nds_read_b128 a[8:11],v250 offset:2048\nds_read_b128 a[12:15],v250 offset:3072\nM v[40:43],a[84:87],v[192:195],v[40:43]\nv_exp_f32 v161,v161\nM v[40:43],a[116:119],v[208:211],v[40:43]\nv_exp_f32 v162,v162\n" \
"M v[48:51],a[88:91],v[192:195],v[48:51]\nv_exp_f32 v163,v163\nM v[48:51],a[120:123],v[208:211],v[48:51]\nglobal_load_dwordx2 a[216:217],v248,s[22:23]\nM v[56:59],a[92:95],v[192:195],v[56:59]\nv_exp_f32 v176,v176\nM v[56:59],a[124:127],v[208:211],v[56:59]\nv_exp_f32 v177,v177\n" \
"M a[192:195],a[208:211],v[192:195],a[192:195]\nv_exp_f32 v178,v178\nM a[192:195],a[208:211],v[208:211],a[192:195]\nds_read_b128 a[16:19],v250 offset:4096\nds_read_b128 a[20:23],v250 offset:5120\nM v[4:7],a[64:67],v[196:199],v[4:7]\nv_exp_f32 v179,v179\nM v[4:7],a[96:99],v[212:215],v[4:7]\n" \
"v_exp_f32 v140,v140\nM v[12:15],a[68:71],v[196:199],v[12:15]\nv_exp_f32 v141,v141\nM v[12:15],a[100:103],v[212:215],v[12:15]\nbuffer_load_dwordx4 v240,s[48:51],s95 offen offset:1024 sc1 lds\nM v[20:23],a[72:75],v[196:199],v[20:23]\nv_exp_f32 v142,v142\nM v[20:23],a[104:107],v[212:215],v[20:23]\n" \
"v_exp_f32 v143,v143\nM v[28:31],a[76:79],v[196:199],v[28:31]\nv_exp_f32 v156,v156\nM v[28:31],a[108:111],v[212:215],v[28:31]\nds_read_b128 a[24:27],v250 offset:6144\nds_read_b128 a[28:31],v250 offset:7168\nM v[36:39],a[80:83],v[196:199],v[36:39]\nv_exp_f32 v157,v157\n" \
"M v[36:39],a[112:115],v[212:215],v[36:39]\nv_exp_f32 v158,v158\nM v[44:47],a[84:87],v[196:199],v[44:47]\nv_exp_f32 v159,v159\nM v[44:47],a[116:119],v[212:215],v[44:47]\nglobal_load_dwordx2 a[232:233],v248,s[24:25]\nM v[52:55],a[88:91],v[196:199],v[52:55]\nv_add_u32 v248,s71,v248\n" \
"v_cvt_pk_bf16_f32 v192,v128,v129\nM v[52:55],a[120:123],v[212:215],v[52:55]\nv_exp_f32 v164,v164\nM v[60:63],a[92:95],v[196:199],v[60:63]\nv_exp_f32 v165,v165\nM v[60:63],a[124:127],v[212:215],v[60:63]\nds_read_b128 a[32:35],v250 offset:8192\nds_read_b128 a[36:39],v250 offset:9216\n" \
"M a[196:199],a[208:211],v[196:199],a[196:199]\nv_exp_f32 v166,v166\nM a[196:199],a[208:211],v[212:215],a[196:199]\nv_exp_f32 v167,v167\nM v[64:67],a[64:67],v[200:203],v[64:67]\nv_exp_f32 v180,v180\nM v[64:67],a[96:99],v[216:219],v[64:67]\nbuffer_load_dwordx4 v240,s[48:51],s95 offen offset:2048 sc1 lds\n" \
"M v[72:75],a[68:71],v[200:203],v[72:75]\nv_exp_f32 v181,v181\nM v[72:75],a[100:103],v[216:219],v[72:75]\nv_exp_f32 v182,v182\nM v[80:83],a[72:75],v[200:203],v[80:83]\nv_exp_f32 v183,v183\nM v[80:83],a[104:107],v[216:219],v[80:83]\nds_read_b128 a[40:43],v250 offset:10240\n" \
"ds_read_b128 a[44:47],v250 offset:11264\nM v[88:91],a[76:79],v[200:203],v[88:91]\nv_exp_f32 v168,v168\nM v[88:91],a[108:111],v[216:219],v[88:91]\nv_exp_f32 v169,v169\nM v[96:99],a[80:83],v[200:203],v[96:99]\nv_exp_f32 v170,v170\nM v[96:99],a[112:115],v[216:219],v[96:99]\n" \
"global_load_dwordx2 a[218:219],v248,s[22:23]\nM v[104:107],a[84:87],v[200:203],v[104:107]\nv_exp_f32 v171,v171\nM v[104:107],a[116:119],v[216:219],v[104:107]\nv_cvt_pk_bf16_f32 v193,v130,v131\nv_cvt_pk_bf16_f32 v194,v144,v145\nM v[112:115],a[88:91],v[200:203],v[112:115]\nv_cvt_pk_bf16_f32 v195,v146,v147\n" \
"v_cvt_pk_bf16_f32 v208,v160,v161\nM v[112:115],a[120:123],v[216:219],v[112:115]\nds_read_b128 a[48:51],v250 offset:12288\nds_read_b128 a[52:55],v250 offset:13312\nM v[120:123],a[92:95],v[200:203],v[120:123]\nv_cvt_pk_bf16_f32 v209,v162,v163\nv_cvt_pk_bf16_f32 v210,v176,v177\nM v[120:123],a[124:127],v[216:219],v[120:123]\n" \
"v_cvt_pk_bf16_f32 v211,v178,v179\nv_cvt_pk_bf16_f32 v196,v132,v133\nM a[200:203],a[208:211],v[200:203],a[200:203]\nv_cvt_pk_bf16_f32 v197,v134,v135\nv_cvt_pk_bf16_f32 v198,v148,v149\nM a[200:203],a[208:211],v[216:219],a[200:203]\nbuffer_load_dwordx4 v240,s[48:51],s95 offen offset:3072 sc1 lds\nM v[68:71],a[64:67],v[204:207],v[68:71]\n" \
"v_cvt_pk_bf16_f32 v199,v150,v151\nv_cvt_pk_bf16_f32 v200,v136,v137\nM v[68:71],a[96:99],v[220:223],v[68:71]\nv_cvt_pk_bf16_f32 v201,v138,v139\nv_cvt_pk_bf16_f32 v202,v152,v153\nM v[76:79],a[68:71],v[204:207],v[76:79]\nv_cvt_pk_bf16_f32 v203,v154,v155\ns_add_u32 s95,s95,0x4000\n" \
"M v[76:79],a[100:103],v[220:223],v[76:79]\nds_read_b128 a[56:59],v250 offset:14336\nds_read_b128 a[60:63],v250 offset:15360\nM v[84:87],a[72:75],v[204:207],v[84:87]\nv_add_u32 v250,s94,v241\nM v[84:87],a[104:107],v[220:223],v[84:87]\nM v[92:95],a[76:79],v[204:207],v[92:95]\nM v[92:95],a[108:111],v[220:223],v[92:95]\n" \
"global_load_dwordx2 a[234:235],v248,s[24:25]\nM v[100:103],a[80:83],v[204:207],v[100:103]\nv_add_u32 v248,s71,v248\nM v[100:103],a[112:115],v[220:223],v[100:103]\nM v[108:111],a[84:87],v[204:207],v[108:111]\nM v[108:111],a[116:119],v[220:223],v[108:111]\nM v[116:119],a[88:91],v[204:207],v[116:119]\nM v[116:119],a[120:123],v[220:223],v[116:119]\n" \
"M v[124:127],a[92:95],v[204:207],v[124:127]\nM v[124:127],a[124:127],v[220:223],v[124:127]\nM a[204:207],a[208:211],v[204:207],a[204:207]\nM a[204:207],a[208:211],v[220:223],a[204:207]\ns_waitcnt lgkmcnt(4)\nM v[128:131],a[0:3],a[128:131],v[224:227]\ns_add_u32 m0,s94,s64\nv_cvt_pk_bf16_f32 v204,v140,v141\n" \
"M v[128:131],a[4:7],a[132:135],v[128:131]\nds_read_b128 a[64:67],v250 offset:0\nds_read_b128 a[96:99],v250 offset:8192\nM v[128:131],a[8:11],a[136:139],v[128:131]\nv_cvt_pk_bf16_f32 v205,v142,v143\nv_cvt_pk_bf16_f32 v206,v156,v157\nM v[128:131],a[12:15],a[140:143],v[128:131]\nv_cvt_pk_bf16_f32 v207,v158,v159\n" \
"v_cvt_pk_bf16_f32 v212,v164,v165\nM v[132:135],a[12:15],a[156:159],v[228:231]\nbuffer_load_dwordx4 v240,s[44:47],s95 offen offset:0 sc1 lds\nM v[132:135],a[8:11],a[152:155],v[132:135]\nv_cvt_pk_bf16_f32 v213,v166,v167\nv_cvt_pk_bf16_f32 v216,v168,v169\nM v[132:135],a[4:7],a[148:151],v[132:135]\nv_cvt_pk_bf16_f32 v217,v170,v171\n" \
"v_cvt_pk_bf16_f32 v214,v180,v181\nM v[132:135],a[0:3],a[144:147],v[132:135]\nds_read_b128 a[68:71],v250 offset:1024\nds_read_b128 a[100:103],v250 offset:9216\nM v[136:139],a[0:3],a[160:163],v[232:235]\nv_exp_f32 v172,v172\nM v[136:139],a[4:7],a[164:167],v[136:139]\nv_exp_f32 v173,v173\n" \
"M v[136:139],a[8:11],a[168:171],v[136:139]\nv_exp_f32 v174,v174\nM v[136:139],a[12:15],a[172:175],v[136:139]\nv_exp_f32 v175,v175\nM v[140:143],a[12:15],a[188:191],v[236:239]\nv_cvt_pk_bf16_f32 v220,v172,v173\nv_cvt_pk_bf16_f32 v221,v174,v175\nM v[140:143],a[8:11],a[184:187],v[140:143]\n" \
"ds_read_b128 a[72:75],v250 offset:2048\nds_read_b128 a[104:107],v250 offset:10240\nM v[140:143],a[4:7],a[180:183],v[140:143]\nglobal_load_dwordx2 a[220:221],v248,s[22:23]\nM v[140:143],a[0:3],a[176:179],v[140:143]\nv_cvt_pk_bf16_f32 v215,v182,v183\ns_add_u32 s94,s94,0x4000\nM v[144:147],a[16:19],a[128:131],v[224:227]\n" \
"v_exp_f32 v184,v184\nM v[144:147],a[20:23],a[132:135],v[144:147]\nv_exp_f32 v185,v185\nM v[144:147],a[24:27],a[136:139],v[144:147]\nv_exp_f32 v186,v186\nM v[144:147],a[28:31],a[140:143],v[144:147]\nds_read_b128 a[76:79],v250 offset:3072\nds_read_b128 a[108:111],v250 offset:11264\n" \
"M v[148:151],a[28:31],a[156:159],v[228:231]\nv_exp_f32 v187,v187\nM v[148:151],a[24:27],a[152:155],v[148:151]\nv_cvt_pk_bf16_f32 v218,v184,v185\nv_cvt_pk_bf16_f32 v219,v186,v187\nM v[148:151],a[20:23],a[148:151],v[148:151]\nbuffer_load_dwordx4 v240,s[44:47],s95 offen offset:1024 sc1 lds\nM v[148:151],a[16:19],a[144:147],v[148:151]\n" \
"v_exp_f32 v188,v188\nM v[152:155],a[16:19],a[160:163],v[232:235]\nv_exp_f32 v189,v189\nM v[152:155],a[20:23],a[164:167],v[152:155]\nds_read_b128 a[80:83],v250 offset:4096\nds_read_b128 a[112:115],v250 offset:12288\nM v[152:155],a[24:27],a[168:171],v[152:155]\nv_exp_f32 v190,v190\n" \
"M v[152:155],a[28:31],a[172:175],v[152:155]\nv_exp_f32 v191,v191\nM v[156:159],a[28:31],a[188:191],v[236:239]\nv_cvt_pk_bf16_f32 v222,v188,v189\nv_cvt_pk_bf16_f32 v223,v190,v191\nM v[156:159],a[24:27],a[184:187],v[156:159]\nv_exp_f32 v128,v128\nM v[156:159],a[20:23],a[180:183],v[156:159]\n" \
"v_exp_f32 v129,v129\nM v[156:159],a[16:19],a[176:179],v[156:159]\nds_read_b128 a[84:87],v250 offset:5120\nds_read_b128 a[116:119],v250 offset:13312\nM v[160:163],a[32:35],a[128:131],v[224:227]\nglobal_load_dwordx2 a[236:237],v248,s[24:25]\nM v[160:163],a[36:39],a[132:135],v[160:163]\nv_add_u32 v248,s71,v248\n" \
"s_and_b32 s94,s94,0xffff\nM v[160:163],a[40:43],a[136:139],v[160:163]\nv_exp_f32 v130,v130\nM v[160:163],a[44:47],a[140:143],v[160:163]\nv_exp_f32 v131,v131\nM v[164:167],a[44:47],a[156:159],v[228:231]\nv_exp_f32 v144,v144\nM v[164:167],a[40:43],a[152:155],v[164:167]\n" \
"ds_read_b128 a[88:91],v250 offset:6144\nds_read_b128 a[120:123],v250 offset:14336\nM v[164:167],a[36:39],a[148:151],v[164:167]\nv_exp_f32 v145,v145\nM v[164:167],a[32:35],a[144:147],v[164:167]\nv_exp_f32 v146,v146\nM v[168:171],a[32:35],a[160:163],v[232:235]\nbuffer_load_dwordx4 v240,s[44:47],s95 offen offset:2048 sc1 lds\n" \
"M v[168:171],a[36:39],a[164:167],v[168:171]\nv_exp_f32 v147,v147\nM v[168:171],a[40:43],a[168:171],v[168:171]\nv_exp_f32 v132,v132\nM v[168:171],a[44:47],a[172:175],v[168:171]\nds_read_b128 a[92:95],v250 offset:7168\nds_read_b128 a[124:127],v250 offset:15360\nM v[172:175],a[44:47],a[188:191],v[236:239]\n" \
"v_exp_f32 v133,v133\nM v[172:175],a[40:43],a[184:187],v[172:175]\nv_exp_f32 v134,v134\nM v[172:175],a[36:39],a[180:183],v[172:175]\nv_exp_f32 v135,v135\nM v[172:175],a[32:35],a[176:179],v[172:175]\nv_xor_b32 v250,0x18000,v250\ns_waitcnt lgkmcnt(3)\n" \
"M v[176:179],a[48:51],a[128:131],v[224:227]\nv_exp_f32 v148,v148\nM v[176:179],a[52:55],a[132:135],v[176:179]\nv_exp_f32 v149,v149\nM v[176:179],a[56:59],a[136:139],v[176:179]\nglobal_load_dwordx2 a[222:223],v248,s[22:23]\nM v[176:179],a[60:63],a[140:143],v[176:179]\nv_exp_f32 v150,v150\n" \
"M v[180:183],a[60:63],a[156:159],v[228:231]\nv_exp_f32 v151,v151\nM v[180:183],a[56:59],a[152:155],v[180:183]\nv_exp_f32 v136,v136\nM v[180:183],a[52:55],a[148:151],v[180:183]\nv_exp_f32 v137,v137\nM v[180:183],a[48:51],a[144:147],v[180:183]\nv_exp_f32 v138,v138\n" \
"M v[184:187],a[48:51],a[160:163],v[232:235]\nv_exp_f32 v139,v139\nM v[184:187],a[52:55],a[164:167],v[184:187]\nv_exp_f32 v152,v152\nM v[184:187],a[56:59],a[168:171],v[184:187]\nbuffer_load_dwordx4 v240,s[44:47],s95 offen offset:3072 sc1 lds\nM v[184:187],a[60:63],a[172:175],v[184:187]\nv_exp_f32 v153,v153\n" \
"M v[188:191],a[60:63],a[188:191],v[236:239]\nv_exp_f32 v154,v154\nM v[188:191],a[56:59],a[184:187],v[188:191]\nv_exp_f32 v155,v155\nM v[188:191],a[52:55],a[180:183],v[188:191]\nglobal_load_dwordx2 a[238:239],v248,s[24:25]\nM v[188:191],a[48:51],a[176:179],v[188:191]\ns_add_u32 m0,m0,0x10000\n" \
"v_add_u32 v248,s71,v248\nM v[0:3],a[64:67],v[192:195],v[0:3]\ns_waitcnt vmcnt(16) lgkmcnt(0)\ns_barrier\nM v[0:3],a[96:99],v[208:211],v[0:3]\nds_read_b128 a[0:3],v250 offset:0\nds_read_b128 a[4:7],v250 offset:1024\nM v[8:11],a[68:71],v[192:195],v[8:11]\n" \
"v_exp_f32 v160,v160\nM v[8:11],a[100:103],v[208:211],v[8:11]\nv_exp_f32 v161,v161\nM v[16:19],a[72:75],v[192:195],v[16:19]\nv_exp_f32 v162,v162\nM v[16:19],a[104:107],v[208:211],v[16:19]\nbuffer_load_dwordx4 v240,s[48:51],s95 offen offset:0 sc1 lds\nM v[24:27],a[76:79],v[192:195],v[24:27]\n" \
"v_exp_f32 v163,v163\nM v[24:27],a[108:111],v[208:211],v[24:27]\nv_exp_f32 v176,v176\nM v[32:35],a[80:83],v[192:195],v[32:35]\nv_exp_f32 v177,v177\nM v[32:35],a[112:115],v[208:211],v[32:35]\nds_read_b128 a[8:11],v250 offset:2048\nds_read_b128 a[12:15],v250 offset:3072\n" \
"M v[40:43],a[84:87],v[192:195],v[40:43]\nv_exp_f32 v178,v178\nM v[40:43],a[116:119],v[208:211],v[40:43]\nv_exp_f32 v179,v179\nM v[48:51],a[88:91],v[192:195],v[48:51]\nv_exp_f32 v140,v140\nM v[48:51],a[120:123],v[208:211],v[48:51]\nglobal_load_dwordx2 a[224:225],v248,s[22:23]\n" \
"M v[56:59],a[92:95],v[192:195],v[56:59]\nv_exp_f32 v141,v141\nM v[56:59],a[124:127],v[208:211],v[56:59]\nv_exp_f32 v142,v142\nM a[192:195],a[208:211],v[192:195],a[192:195]\nv_exp_f32 v143,v143\nM a[192:195],a[208:211],v[208:211],a[192:195]\nds_read_b128 a[16:19],v250 offset:4096\n" \
"ds_read_b128 a[20:23],v250 offset:5120\nM v[4:7],a[64:67],v[196:199],v[4:7]\nv_exp_f32 v156,v156\nM v[4:7],a[96:99],v[212:215],v[4:7]\nv_exp_f32 v157,v157\nM v[12:15],a[68:71],v[196:199],v[12:15]\nv_exp_f32 v158,v158\nM v[12:15],a[100:103],v[212:215],v[12:15]\n" \
"buffer_load_dwordx4 v240,s[48:51],s95 offen offset:1024 sc1 lds\nM v[20:23],a[72:75],v[196:199],v[20:23]\nv_exp_f32 v159,v159\nM v[20:23],a[104:107],v[212:215],v[20:23]\nv_exp_f32 v164,v164\nM v[28:31],a[76:79],v[196:199],v[28:31]\nv_exp_f32 v165,v165\nM v[28:31],a[108:111],v[212:215],v[28:31]\n" \
"ds_read_b128 a[24:27],v250 offset:6144\nds_read_b128 a[28:31],v250 offset:7168\nM v[36:39],a[80:83],v[196:199],v[36:39]\nv_exp_f32 v166,v166\nM v[36:39],a[112:115],v[212:215],v[36:39]\nv_exp_f32 v167,v167\nM v[44:47],a[84:87],v[196:199],v[44:47]\nv_exp_f32 v180,v180\n" \
"M v[44:47],a[116:119],v[212:215],v[44:47]\nglobal_load_dwordx2 a[240:241],v248,s[24:25]\nM v[52:55],a[88:91],v[196:199],v[52:55]\nv_add_u32 v248,s71,v248\nv_cvt_pk_bf16_f32 v192,v128,v129\nM v[52:55],a[120:123],v[212:215],v[52:55]\nv_exp_f32 v181,v181\nM v[60:63],a[92:95],v[196:199],v[60:63]\n" \
"v_exp_f32 v182,v182\nM v[60:63],a[124:127],v[212:215],v[60:63]\nds_read_b128 a[32:35],v250 offset:8192\nds_read_b128 a[36:39],v250 offset:9216\nM a[196:199],a[208:211],v[196:199],a[196:199]\nv_exp_f32 v183,v183\nM a[196:199],a[208:211],v[212:215],a[196:199]\nv_exp_f32 v168,v168\n" \
"M v[64:67],a[64:67],v[200:203],v[64:67]\nv_exp_f32 v169,v169\nM v[64:67],a[96:99],v[216:219],v[64:67]\nbuffer_load_dwordx4 v240,s[48:51],s95 offen offset:2048 sc1 lds\nM v[72:75],a[68:71],v[200:203],v[72:75]\nv_exp_f32 v170,v170\nM v[72:75],a[100:103],v[216:219],v[72:75]\nv_exp_f32 v171,v171\n" \
"M v[80:83],a[72:75],v[200:203],v[80:83]\nv_cvt_pk_bf16_f32 v193,v130,v131\nv_cvt_pk_bf16_f32 v194,v144,v145\nM v[80:83],a[104:107],v[216:219],v[80:83]\nds_read_b128 a[40:43],v250 offset:10240\nds_read_b128 a[44:47],v250 offset:11264\nM v[88:91],a[76:79],v[200:203],v[88:91]\nv_cvt_pk_bf16_f32 v195,v146,v147\n" \
"v_cvt_pk_bf16_f32 v208,v160,v161\nM v[88:91],a[108:111],v[216:219],v[88:91]\nv_cvt_pk_bf16_f32 v209,v162,v163\nv_cvt_pk_bf16_f32 v210,v176,v177\nM v[96:99],a[80:83],v[200:203],v[96:99]\nv_cvt_pk_bf16_f32 v211,v178,v179\nv_cvt_pk_bf16_f32 v196,v132,v133\nM v[96:99],a[112:115],v[216:219],v[96:99]\n" \
"global_load_dwordx2 a[226:227],v248,s[22:23]\nM v[104:107],a[84:87],v[200:203],v[104:107]\nv_cvt_pk_bf16_f32 v197,v134,v135\nv_cvt_pk_bf16_f32 v198,v148,v149\nM v[104:107],a[116:119],v[216:219],v[104:107]\nv_cvt_pk_bf16_f32 v199,v150,v151\nM v[112:115],a[88:91],v[200:203],v[112:115]\nM v[112:115],a[120:123],v[216:219],v[112:115]\n" \
"ds_read_b128 a[48:51],v250 offset:12288\nds_read_b128 a[52:55],v250 offset:13312\nM v[120:123],a[92:95],v[200:203],v[120:123]\nM v[120:123],a[124:127],v[216:219],v[120:123]\nM a[200:203],a[208:211],v[200:203],a[200:203]\nv_cvt_pk_bf16_f32 v200,v136,v137\nv_cvt_pk_bf16_f32 v201,v138,v139\nM a[200:203],a[208:211],v[216:219],a[200:203]\n" \
"buffer_load_dwordx4 v240,s[48:51],s95 offen offset:3072 sc1 lds\nM v[68:71],a[64:67],v[204:207],v[68:71]\nv_cvt_pk_bf16_f32 v202,v152,v153\nv_cvt_pk_bf16_f32 v203,v154,v155\nM v[68:71],a[96:99],v[220:223],v[68:71]\ns_add_u32 s95,s95,0x4000\nM v[76:79],a[68:71],v[204:207],v[76:79]\nM v[76:79],a[100:103],v[220:223],v[76:79]\n" \
"ds_read_b128 a[56:59],v250 offset:14336\nds_read_b128 a[60:63],v250 offset:15360\nM v[84:87],a[72:75],v[204:207],v[84:87]\nv_add_u32 v250,s94,v241\nM v[84:87],a[104:107],v[220:223],v[84:87]\nM v[92:95],a[76:79],v[204:207],v[92:95]\nM v[92:95],a[108:111],v[220:223],v[92:95]\nglobal_load_dwordx2 a[242:243],v248,s[24:25]\n" \
"M v[100:103],a[80:83],v[204:207],v[100:103]\nv_add_u32 v248,s71,v248\nM v[100:103],a[112:115],v[220:223],v[100:103]\nM v[108:111],a[84:87],v[204:207],v[108:111]\nM v[108:111],a[116:119],v[220:223],v[108:111]\nM v[116:119],a[88:91],v[204:207],v[116:119]\nM v[116:119],a[120:123],v[220:223],v[116:119]\nM v[124:127],a[92:95],v[204:207],v[124:127]\n" \
"M v[124:127],a[124:127],v[220:223],v[124:127]\nM a[204:207],a[208:211],v[204:207],a[204:207]\nM a[204:207],a[208:211],v[220:223],a[204:207]\ns_cmp_eq_u32 s62,7\ns_cbranch_scc0 .Ltrans\ns_waitcnt lgkmcnt(4)\nM v[136:139],a[0:3],a[160:163],v[232:235]\ns_add_u32 m0,s94,s64\n" \
"v_cvt_pk_bf16_f32 v204,v140,v141\nM v[136:139],a[4:7],a[164:167],v[136:139]\nds_read_b128 a[64:67],v250 offset:0\nds_read_b128 a[96:99],v250 offset:8192\nM v[136:139],a[8:11],a[168:171],v[136:139]\nbuffer_load_dwordx4 v240,s[44:47],s95 offen offset:0 sc1 lds\nM v[136:139],a[12:15],a[172:175],v[136:139]\nv_cvt_pk_bf16_f32 v205,v142,v143\n" \
"v_cvt_pk_bf16_f32 v206,v156,v157\nM v[140:143],a[12:15],a[188:191],v[236:239]\nds_read_b128 a[68:71],v250 offset:1024\nds_read_b128 a[100:103],v250 offset:9216\nM v[140:143],a[8:11],a[184:187],v[140:143]\nbuffer_store_dwordx4 a[248:251],v253,s[52:55],s33 offen offset:0 sc1\nM v[140:143],a[4:7],a[180:183],v[140:143]\nv_cvt_pk_bf16_f32 v207,v158,v159\n" \
"v_cvt_pk_bf16_f32 v216,v168,v169\nM v[140:143],a[0:3],a[176:179],v[140:143]\nds_read_b128 a[72:75],v250 offset:2048\nds_read_b128 a[104:107],v250 offset:10240\nM v[152:155],a[16:19],a[160:163],v[232:235]\nv_cvt_pk_bf16_f32 v217,v170,v171\ns_add_u32 s71,s33,16384\nM v[152:155],a[20:23],a[164:167],v[152:155]\n" \
"v_exp_f32 v172,v172\nM v[152:155],a[24:27],a[168:171],v[152:155]\nds_read_b128 a[76:79],v250 offset:3072\nds_read_b128 a[108:111],v250 offset:11264\nM v[152:155],a[28:31],a[172:175],v[152:155]\nbuffer_load_dwordx4 v240,s[44:47],s95 offen offset:1024 sc1 lds\nM v[156:159],a[28:31],a[188:191],v[236:239]\nv_exp_f32 v173,v173\n" \
"M v[156:159],a[24:27],a[184:187],v[156:159]\nds_read_b128 a[80:83],v250 offset:4096\nds_read_b128 a[112:115],v250 offset:12288\nM v[156:159],a[20:23],a[180:183],v[156:159]\nbuffer_store_dwordx4 a[248:251],v253,s[52:55],s33 offen offset:1024 sc1\nM v[156:159],a[16:19],a[176:179],v[156:159]\nv_exp_f32 v174,v174\nM v[168:171],a[32:35],a[160:163],v[232:235]\n" \
"ds_read_b128 a[84:87],v250 offset:5120\nds_read_b128 a[116:119],v250 offset:13312\nM v[168:171],a[36:39],a[164:167],v[168:171]\nv_exp_f32 v175,v175\nM v[168:171],a[40:43],a[168:171],v[168:171]\nv_cvt_pk_bf16_f32 v220,v172,v173\nv_cvt_pk_bf16_f32 v221,v174,v175\nM v[168:171],a[44:47],a[172:175],v[168:171]\n" \
"ds_read_b128 a[88:91],v250 offset:6144\nds_read_b128 a[120:123],v250 offset:14336\nM v[172:175],a[44:47],a[188:191],v[236:239]\nbuffer_load_dwordx4 v240,s[44:47],s95 offen offset:2048 sc1 lds\nM v[172:175],a[40:43],a[184:187],v[172:175]\nv_exp_f32 v184,v184\nM v[172:175],a[36:39],a[180:183],v[172:175]\nv_exp_f32 v185,v185\n" \
"v_exp_f32 v186,v186\nv_exp_f32 v187,v187\nds_read_b128 a[92:95],v250 offset:7168\nds_read_b128 a[124:127],v250 offset:15360\nM v[172:175],a[32:35],a[176:179],v[172:175]\nbuffer_store_dwordx4 a[248:251],v253,s[52:55],s33 offen offset:2048 sc1\nv_cvt_pk_bf16_f32 v218,v184,v185\nv_cvt_pk_bf16_f32 v219,v186,v187\n" \
"s_waitcnt lgkmcnt(6)\nM v[184:187],a[48:51],a[160:163],v[232:235]\nv_exp_f32 v188,v188\nM v[184:187],a[52:55],a[164:167],v[184:187]\nv_exp_f32 v189,v189\nM v[184:187],a[56:59],a[168:171],v[184:187]\nv_exp_f32 v190,v190\nv_exp_f32 v191,v191\n" \
"M v[184:187],a[60:63],a[172:175],v[184:187]\nv_cvt_pk_bf16_f32 v222,v188,v189\nv_cvt_pk_bf16_f32 v223,v190,v191\nM v[188:191],a[60:63],a[188:191],v[236:239]\nv_xor_b32 v250,0x18000,v250\ns_add_u32 s70,s78,16384\nM v[188:191],a[56:59],a[184:187],v[188:191]\nbuffer_load_dwordx4 v240,s[44:47],s95 offen offset:3072 sc1 lds\n" \
"M v[188:191],a[52:55],a[180:183],v[188:191]\ns_add_u32 m0,m0,0x10000\nv_cvt_pk_bf16_f32 v212,v164,v165\nM v[188:191],a[48:51],a[176:179],v[188:191]\nv_cvt_pk_bf16_f32 v213,v166,v167\nv_cvt_pk_bf16_f32 v214,v180,v181\nM v[64:67],a[64:67],v[200:203],v[64:67]\nbuffer_store_dwordx4 a[248:251],v253,s[52:55],s33 offen offset:3072 sc1\n" \
"M v[64:67],a[96:99],v[216:219],v[64:67]\nv_cvt_pk_bf16_f32 v215,v182,v183\ns_add_u32 s94,s94,0x4000\nM v[72:75],a[68:71],v[200:203],v[72:75]\ns_waitcnt vmcnt(16) lgkmcnt(0)\ns_barrier\nM v[72:75],a[100:103],v[216:219],v[72:75]\nds_read_b128 a[0:3],v250 offset:0\n" \
"ds_read_b128 a[4:7],v250 offset:1024\nM v[80:83],a[72:75],v[200:203],v[80:83]\nv_exp_f32 v136,v136\nM v[80:83],a[104:107],v[216:219],v[80:83]\nbuffer_store_dwordx4 a[248:251],v253,s[52:55],s71 offen offset:0 sc1\nM v[88:91],a[76:79],v[200:203],v[88:91]\nv_exp_f32 v137,v137\nM v[88:91],a[108:111],v[216:219],v[88:91]\n" \
"buffer_load_dwordx4 v240,s[48:51],s95 offen offset:0 sc1 lds\nM v[96:99],a[80:83],v[200:203],v[96:99]\nv_exp_f32 v138,v138\nM v[96:99],a[112:115],v[216:219],v[96:99]\nbuffer_store_dwordx4 a[248:251],v253,s[52:55],s71 offen offset:1024 sc1\nM v[104:107],a[84:87],v[200:203],v[104:107]\nv_exp_f32 v139,v139\nM v[104:107],a[116:119],v[216:219],v[104:107]\n" \
"ds_read_b128 a[8:11],v250 offset:2048\nds_read_b128 a[12:15],v250 offset:3072\nM v[112:115],a[88:91],v[200:203],v[112:115]\nv_exp_f32 v152,v152\nM v[112:115],a[120:123],v[216:219],v[112:115]\nbuffer_store_dwordx4 a[248:251],v253,s[52:55],s71 offen offset:2048 sc1\nM v[120:123],a[92:95],v[200:203],v[120:123]\nv_exp_f32 v153,v153\n" \
"M v[120:123],a[124:127],v[216:219],v[120:123]\nv_exp_f32 v154,v154\nM a[200:203],a[208:211],v[200:203],a[200:203]\nv_exp_f32 v155,v155\nM a[200:203],a[208:211],v[216:219],a[200:203]\nbuffer_store_dwordx4 a[248:251],v253,s[52:55],s71 offen offset:3072 sc1\nM v[68:71],a[64:67],v[204:207],v[68:71]\nv_exp_f32 v168,v168\n" \
"M v[68:71],a[96:99],v[220:223],v[68:71]\nds_read_b128 a[16:19],v250 offset:4096\nds_read_b128 a[20:23],v250 offset:5120\nM v[76:79],a[68:71],v[204:207],v[76:79]\nv_exp_f32 v169,v169\nM v[76:79],a[100:103],v[220:223],v[76:79]\nv_exp_f32 v170,v170\nM v[84:87],a[72:75],v[204:207],v[84:87]\n" \
"v_exp_f32 v171,v171\nM v[84:87],a[104:107],v[220:223],v[84:87]\nbuffer_load_dwordx4 v240,s[48:51],s95 offen offset:1024 sc1 lds\nM v[92:95],a[76:79],v[204:207],v[92:95]\nv_exp_f32 v184,v184\nM v[92:95],a[108:111],v[220:223],v[92:95]\nbuffer_store_dwordx4 a[248:251],v253,s[52:55],s78 offen offset:0 sc1\nM v[100:103],a[80:83],v[204:207],v[100:103]\n" \
"v_exp_f32 v185,v185\nM v[100:103],a[112:115],v[220:223],v[100:103]\nds_read_b128 a[24:27],v250 offset:6144\nds_read_b128 a[28:31],v250 offset:7168\nM v[108:111],a[84:87],v[204:207],v[108:111]\nv_exp_f32 v186,v186\nM v[108:111],a[116:119],v[220:223],v[108:111]\nbuffer_store_dwordx4 a[248:251],v253,s[52:55],s78 offen offset:1024 sc1\n" \
"M v[116:119],a[88:91],v[204:207],v[116:119]\nv_exp_f32 v187,v187\nM v[116:119],a[120:123],v[220:223],v[116:119]\ns_and_b32 s94,s94,0xffff\nv_cvt_pk_bf16_f32 v200,v136,v137\nM v[124:127],a[92:95],v[204:207],v[124:127]\nv_exp_f32 v140,v140\nM v[124:127],a[124:127],v[220:223],v[124:127]\n" \
"buffer_store_dwordx4 a[248:251],v253,s[52:55],s78 offen offset:2048 sc1\nM a[204:207],a[208:211],v[204:207],a[204:207]\nv_exp_f32 v141,v141\nM a[204:207],a[208:211],v[220:223],a[204:207]\nds_read_b128 a[32:35],v250 offset:8192\nds_read_b128 a[36:39],v250 offset:9216\nM v[0:3],a[64:67],v[192:195],v[0:3]\nv_exp_f32 v142,v142\n" \
"M v[0:3],a[96:99],v[208:211],v[0:3]\nv_exp_f32 v143,v143\nM v[8:11],a[68:71],v[192:195],v[8:11]\nv_exp_f32 v156,v156\nM v[8:11],a[100:103],v[208:211],v[8:11]\nbuffer_load_dwordx4 v240,s[48:51],s95 offen offset:2048 sc1 lds\nM v[16:19],a[72:75],v[192:195],v[16:19]\nv_exp_f32 v157,v157\n" \
"M v[16:19],a[104:107],v[208:211],v[16:19]\nbuffer_store_dwordx4 a[248:251],v253,s[52:55],s78 offen offset:3072 sc1\nM v[24:27],a[76:79],v[192:195],v[24:27]\nv_exp_f32 v158,v158\nM v[24:27],a[108:111],v[208:211],v[24:27]\nds_read_b128 a[40:43],v250 offset:10240\nds_read_b128 a[44:47],v250 offset:11264\nM v[32:35],a[80:83],v[192:195],v[32:35]\n" \
"v_exp_f32 v159,v159\nM v[32:35],a[112:115],v[208:211],v[32:35]\nbuffer_store_dwordx4 a[248:251],v253,s[52:55],s70 offen offset:0 sc1\nM v[40:43],a[84:87],v[192:195],v[40:43]\nv_cvt_pk_bf16_f32 v201,v138,v139\nv_cvt_pk_bf16_f32 v202,v152,v153\nM v[40:43],a[116:119],v[208:211],v[40:43]\nv_cvt_pk_bf16_f32 v203,v154,v155\n" \
"v_cvt_pk_bf16_f32 v216,v168,v169\nM v[48:51],a[88:91],v[192:195],v[48:51]\nv_cvt_pk_bf16_f32 v217,v170,v171\nv_cvt_pk_bf16_f32 v218,v184,v185\nM v[48:51],a[120:123],v[208:211],v[48:51]\nbuffer_store_dwordx4 a[248:251],v253,s[52:55],s70 offen offset:1024 sc1\nM v[56:59],a[92:95],v[192:195],v[56:59]\nv_cvt_pk_bf16_f32 v219,v186,v187\n" \
"M v[56:59],a[124:127],v[208:211],v[56:59]\nds_read_b128 a[48:51],v250 offset:12288\nds_read_b128 a[52:55],v250 offset:13312\nM a[192:195],a[208:211],v[192:195],a[192:195]\nM a[192:195],a[208:211],v[208:211],a[192:195]\nM v[4:7],a[64:67],v[196:199],v[4:7]\nM v[4:7],a[96:99],v[212:215],v[4:7]\nbuffer_load_dwordx4 v240,s[48:51],s95 offen offset:3072 sc1 lds\n" \
"M v[12:15],a[68:71],v[196:199],v[12:15]\ns_add_u32 s95,s95,0xffff4000\nM v[12:15],a[100:103],v[212:215],v[12:15]\nbuffer_store_dwordx4 a[248:251],v253,s[52:55],s70 offen offset:2048 sc1\nM v[20:23],a[72:75],v[196:199],v[20:23]\nM v[20:23],a[104:107],v[212:215],v[20:23]\nds_read_b128 a[56:59],v250 offset:14336\nds_read_b128 a[60:63],v250 offset:15360\n" \
"M v[28:31],a[76:79],v[196:199],v[28:31]\nv_add_u32 v250,s94,v241\nM v[28:31],a[108:111],v[212:215],v[28:31]\nbuffer_store_dwordx4 a[248:251],v253,s[52:55],s70 offen offset:3072 sc1\nM v[36:39],a[80:83],v[196:199],v[36:39]\nM v[36:39],a[112:115],v[212:215],v[36:39]\nM v[44:47],a[84:87],v[196:199],v[44:47]\nM v[44:47],a[116:119],v[212:215],v[44:47]\n" \
"M v[52:55],a[88:91],v[196:199],v[52:55]\nM v[52:55],a[120:123],v[212:215],v[52:55]\nM v[60:63],a[92:95],v[196:199],v[60:63]\nM v[60:63],a[124:127],v[212:215],v[60:63]\nM a[196:199],a[208:211],v[196:199],a[196:199]\nM a[196:199],a[208:211],v[212:215],a[196:199]\n.Lsl:\ns_waitcnt lgkmcnt(4)\n" \
"M v[136:139],a[0:3],a[160:163],v[232:235]\ns_add_u32 m0,s94,s64\nv_cvt_pk_bf16_f32 v204,v140,v141\nM v[136:139],a[4:7],a[164:167],v[136:139]\nds_read_b128 a[64:67],v250 offset:0\nds_read_b128 a[96:99],v250 offset:8192\nM v[136:139],a[8:11],a[168:171],v[136:139]\nbuffer_load_dwordx4 v240,s[44:47],s95 offen offset:0 sc1 lds\n" \
"M v[136:139],a[12:15],a[172:175],v[136:139]\nv_cvt_pk_bf16_f32 v205,v142,v143\nv_cvt_pk_bf16_f32 v206,v156,v157\nM v[140:143],a[12:15],a[188:191],v[236:239]\nds_read_b128 a[68:71],v250 offset:1024\nds_read_b128 a[100:103],v250 offset:9216\nM v[140:143],a[8:11],a[184:187],v[140:143]\nv_cvt_pk_bf16_f32 v207,v158,v159\n" \
"s_add_u32 s94,s94,0x4000\nM v[140:143],a[4:7],a[180:183],v[140:143]\nv_exp_f32 v172,v172\nM v[140:143],a[0:3],a[176:179],v[140:143]\nds_read_b128 a[72:75],v250 offset:2048\nds_read_b128 a[104:107],v250 offset:10240\nM v[152:155],a[16:19],a[160:163],v[232:235]\nv_exp_f32 v173,v173\n" \
"M v[152:155],a[20:23],a[164:167],v[152:155]\nv_exp_f32 v174,v174\nM v[152:155],a[24:27],a[168:171],v[152:155]\nds_read_b128 a[76:79],v250 offset:3072\nds_read_b128 a[108:111],v250 offset:11264\nM v[152:155],a[28:31],a[172:175],v[152:155]\nbuffer_load_dwordx4 v240,s[44:47],s95 offen offset:1024 sc1 lds\nM v[156:159],a[28:31],a[188:191],v[236:239]\n" \
"v_exp_f32 v175,v175\nM v[156:159],a[24:27],a[184:187],v[156:159]\nds_read_b128 a[80:83],v250 offset:4096\nds_read_b128 a[112:115],v250 offset:12288\nM v[156:159],a[20:23],a[180:183],v[156:159]\nv_cvt_pk_bf16_f32 v220,v172,v173\nv_cvt_pk_bf16_f32 v221,v174,v175\nM v[156:159],a[16:19],a[176:179],v[156:159]\n" \
"v_exp_f32 v188,v188\nM v[168:171],a[32:35],a[160:163],v[232:235]\nds_read_b128 a[84:87],v250 offset:5120\nds_read_b128 a[116:119],v250 offset:13312\nM v[168:171],a[36:39],a[164:167],v[168:171]\nv_exp_f32 v189,v189\nM v[168:171],a[40:43],a[168:171],v[168:171]\nv_exp_f32 v190,v190\n" \
"M v[168:171],a[44:47],a[172:175],v[168:171]\nds_read_b128 a[88:91],v250 offset:6144\nds_read_b128 a[120:123],v250 offset:14336\nM v[172:175],a[44:47],a[188:191],v[236:239]\nbuffer_load_dwordx4 v240,s[44:47],s95 offen offset:2048 sc1 lds\nM v[172:175],a[40:43],a[184:187],v[172:175]\nv_exp_f32 v191,v191\nM v[172:175],a[36:39],a[180:183],v[172:175]\n" \
"ds_read_b128 a[92:95],v250 offset:7168\nds_read_b128 a[124:127],v250 offset:15360\nM v[172:175],a[32:35],a[176:179],v[172:175]\nv_cvt_pk_bf16_f32 v222,v188,v189\ns_waitcnt lgkmcnt(7)\nM v[184:187],a[48:51],a[160:163],v[232:235]\nv_cvt_pk_bf16_f32 v223,v190,v191\nv_xor_b32 v250,0x18000,v250\n" \
"M v[184:187],a[52:55],a[164:167],v[184:187]\nv_exp_f32 v136,v136\nM v[184:187],a[56:59],a[168:171],v[184:187]\nv_exp_f32 v137,v137\nM v[184:187],a[60:63],a[172:175],v[184:187]\nv_exp_f32 v138,v138\nM v[188:191],a[60:63],a[188:191],v[236:239]\nv_exp_f32 v139,v139\n" \
"M v[188:191],a[56:59],a[184:187],v[188:191]\nbuffer_load_dwordx4 v240,s[44:47],s95 offen offset:3072 sc1 lds\nM v[188:191],a[52:55],a[180:183],v[188:191]\ns_add_u32 m0,m0,0x10000\ns_and_b32 s94,s94,0xffff\nM v[188:191],a[48:51],a[176:179],v[188:191]\nv_exp_f32 v152,v152\nM v[64:67],a[64:67],v[200:203],v[64:67]\n" \
"v_exp_f32 v153,v153\nM v[64:67],a[96:99],v[216:219],v[64:67]\nv_exp_f32 v154,v154\nM v[72:75],a[68:71],v[200:203],v[72:75]\ns_waitcnt vmcnt(16) lgkmcnt(0)\ns_barrier\nM v[72:75],a[100:103],v[216:219],v[72:75]\nds_read_b128 a[0:3],v250 offset:0\n" \
"ds_read_b128 a[4:7],v250 offset:1024\nM v[80:83],a[72:75],v[200:203],v[80:83]\nv_exp_f32 v155,v155\nM v[80:83],a[104:107],v[216:219],v[80:83]\nbuffer_load_dwordx4 v240,s[48:51],s95 offen offset:0 sc1 lds\nM v[88:91],a[76:79],v[200:203],v[88:91]\nv_exp_f32 v168,v168\nM v[88:91],a[108:111],v[216:219],v[88:91]\n" \
"ds_read_b128 a[8:11],v250 offset:2048\nds_read_b128 a[12:15],v250 offset:3072\nM v[96:99],a[80:83],v[200:203],v[96:99]\nv_exp_f32 v169,v169\nM v[96:99],a[112:115],v[216:219],v[96:99]\nv_exp_f32 v170,v170\nM v[104:107],a[84:87],v[200:203],v[104:107]\nv_exp_f32 v171,v171\n" \
"M v[104:107],a[116:119],v[216:219],v[104:107]\nds_read_b128 a[16:19],v250 offset:4096\nds_read_b128 a[20:23],v250 offset:5120\nM v[112:115],a[88:91],v[200:203],v[112:115]\nv_exp_f32 v184,v184\nM v[112:115],a[120:123],v[216:219],v[112:115]\nbuffer_load_dwordx4 v240,s[48:51],s95 offen offset:1024 sc1 lds\nM v[120:123],a[92:95],v[200:203],v[120:123]\n" \
"v_exp_f32 v185,v185\nM v[120:123],a[124:127],v[216:219],v[120:123]\nds_read_b128 a[24:27],v250 offset:6144\nds_read_b128 a[28:31],v250 offset:7168\nM a[200:203],a[208:211],v[200:203],a[200:203]\nv_exp_f32 v186,v186\nM a[200:203],a[208:211],v[216:219],a[200:203]\nv_exp_f32 v187,v187\n" \
"M v[68:71],a[64:67],v[204:207],v[68:71]\nv_exp_f32 v140,v140\nM v[68:71],a[96:99],v[220:223],v[68:71]\nds_read_b128 a[32:35],v250 offset:8192\nds_read_b128 a[36:39],v250 offset:9216\nM v[76:79],a[68:71],v[204:207],v[76:79]\nv_exp_f32 v141,v141\nM v[76:79],a[100:103],v[220:223],v[76:79]\n" \
"buffer_load_dwordx4 v240,s[48:51],s95 offen offset:2048 sc1 lds\nM v[84:87],a[72:75],v[204:207],v[84:87]\nv_exp_f32 v142,v142\nM v[84:87],a[104:107],v[220:223],v[84:87]\nds_read_b128 a[40:43],v250 offset:10240\nds_read_b128 a[44:47],v250 offset:11264\nM v[92:95],a[76:79],v[204:207],v[92:95]\nv_exp_f32 v143,v143\n" \
"M v[92:95],a[108:111],v[220:223],v[92:95]\nv_exp_f32 v156,v156\nM v[100:103],a[80:83],v[204:207],v[100:103]\nv_exp_f32 v157,v157\nM v[100:103],a[112:115],v[220:223],v[100:103]\nds_read_b128 a[48:51],v250 offset:12288\nds_read_b128 a[52:55],v250 offset:13312\nM v[108:111],a[84:87],v[204:207],v[108:111]\n" \
"v_exp_f32 v158,v158\nM v[108:111],a[116:119],v[220:223],v[108:111]\nbuffer_load_dwordx4 v240,s[48:51],s95 offen offset:3072 sc1 lds\nM v[116:119],a[88:91],v[204:207],v[116:119]\nv_exp_f32 v159,v159\nM v[116:119],a[120:123],v[220:223],v[116:119]\nds_read_b128 a[56:59],v250 offset:14336\nds_read_b128 a[60:63],v250 offset:15360\n" \
"M v[124:127],a[92:95],v[204:207],v[124:127]\nv_cvt_pk_bf16_f32 v200,v136,v137\nv_cvt_pk_bf16_f32 v201,v138,v139\nM v[124:127],a[124:127],v[220:223],v[124:127]\nv_cvt_pk_bf16_f32 v202,v152,v153\nv_cvt_pk_bf16_f32 v203,v154,v155\nM a[204:207],a[208:211],v[204:207],a[204:207]\nv_cvt_pk_bf16_f32 v216,v168,v169\n" \
"v_cvt_pk_bf16_f32 v217,v170,v171\nv_cvt_pk_bf16_f32 v218,v184,v185\nv_cvt_pk_bf16_f32 v219,v186,v187\nv_add_u32 v250,s94,v241\ns_add_u32 s95,s95,0x4000\nM a[204:207],a[208:211],v[220:223],a[204:207]\ns_cmp_lg_u32 s95,s96\ns_cbranch_scc1 .Lsl\n" \
"s_waitcnt lgkmcnt(4)\nM v[128:131],a[0:3],a[128:131],v[224:227]\ns_add_u32 m0,s94,s64\nv_cvt_pk_bf16_f32 v204,v140,v141\nM v[128:131],a[4:7],a[132:135],v[128:131]\nds_read_b128 a[64:67],v250 offset:0\nds_read_b128 a[96:99],v250 offset:8192\nM v[128:131],a[8:11],a[136:139],v[128:131]\n" \
"buffer_load_dwordx4 v240,s[44:47],s95 offen offset:0 sc1 lds\nM v[128:131],a[12:15],a[140:143],v[128:131]\nv_exp_f32 v188,v188\nM v[132:135],a[12:15],a[156:159],v[228:231]\nds_read_b128 a[68:71],v250 offset:1024\nds_read_b128 a[100:103],v250 offset:9216\nM v[132:135],a[8:11],a[152:155],v[132:135]\nv_exp_f32 v189,v189\n" \
"M v[132:135],a[4:7],a[148:151],v[132:135]\nv_exp_f32 v190,v190\nM v[132:135],a[0:3],a[144:147],v[132:135]\nds_read_b128 a[72:75],v250 offset:2048\nds_read_b128 a[104:107],v250 offset:10240\nM v[144:147],a[16:19],a[128:131],v[224:227]\nv_exp_f32 v191,v191\nM v[144:147],a[20:23],a[132:135],v[144:147]\n" \
"v_exp_f32 v172,v172\nM v[144:147],a[24:27],a[136:139],v[144:147]\nds_read_b128 a[76:79],v250 offset:3072\nds_read_b128 a[108:111],v250 offset:11264\nM v[144:147],a[28:31],a[140:143],v[144:147]\nbuffer_load_dwordx4 v240,s[44:47],s95 offen offset:1024 sc1 lds\nM v[148:151],a[28:31],a[156:159],v[228:231]\nv_exp_f32 v173,v173\n" \
"M v[148:151],a[24:27],a[152:155],v[148:151]\nds_read_b128 a[80:83],v250 offset:4096\nds_read_b128 a[112:115],v250 offset:12288\nM v[148:151],a[20:23],a[148:151],v[148:151]\nv_exp_f32 v174,v174\nM v[148:151],a[16:19],a[144:147],v[148:151]\nv_exp_f32 v175,v175\nM v[160:163],a[32:35],a[128:131],v[224:227]\n" \
"ds_read_b128 a[84:87],v250 offset:5120\nds_read_b128 a[116:119],v250 offset:13312\nM v[160:163],a[36:39],a[132:135],v[160:163]\nv_cvt_pk_bf16_f32 v205,v142,v143\nv_cvt_pk_bf16_f32 v206,v156,v157\nM v[160:163],a[40:43],a[136:139],v[160:163]\nv_cvt_pk_bf16_f32 v207,v158,v159\nv_cvt_pk_bf16_f32 v220,v172,v173\n" \
"M v[160:163],a[44:47],a[140:143],v[160:163]\nds_read_b128 a[88:91],v250 offset:6144\nds_read_b128 a[120:123],v250 offset:14336\nM v[164:167],a[44:47],a[156:159],v[228:231]\nbuffer_load_dwordx4 v240,s[44:47],s95 offen offset:2048 sc1 lds\nM v[164:167],a[40:43],a[152:155],v[164:167]\nv_cvt_pk_bf16_f32 v221,v174,v175\nv_cvt_pk_bf16_f32 v222,v188,v189\n" \
"M v[164:167],a[36:39],a[148:151],v[164:167]\nds_read_b128 a[92:95],v250 offset:7168\nds_read_b128 a[124:127],v250 offset:15360\nM v[164:167],a[32:35],a[144:147],v[164:167]\nv_xor_b32 v250,0x18000,v250\ns_waitcnt lgkmcnt(7)\nM v[176:179],a[48:51],a[128:131],v[224:227]\nv_cvt_pk_bf16_f32 v223,v190,v191\n" \
"s_add_u32 s94,s94,0x4000\nM v[176:179],a[52:55],a[132:135],v[176:179]\nv_exp_f32 v128,v128\nM v[176:179],a[56:59],a[136:139],v[176:179]\nv_exp_f32 v129,v129\nM v[176:179],a[60:63],a[140:143],v[176:179]\nv_exp_f32 v130,v130\nM v[180:183],a[60:63],a[156:159],v[228:231]\n" \
"v_exp_f32 v131,v131\nM v[180:183],a[56:59],a[152:155],v[180:183]\nbuffer_load_dwordx4 v240,s[44:47],s95 offen offset:3072 sc1 lds\nM v[180:183],a[52:55],a[148:151],v[180:183]\ns_add_u32 m0,m0,0x10000\ns_and_b32 s94,s94,0xffff\nM v[180:183],a[48:51],a[144:147],v[180:183]\nv_exp_f32 v144,v144\n" \
"M v[64:67],a[64:67],v[200:203],v[64:67]\nv_exp_f32 v145,v145\nM v[64:67],a[96:99],v[216:219],v[64:67]\nv_exp_f32 v146,v146\nM v[72:75],a[68:71],v[200:203],v[72:75]\ns_waitcnt vmcnt(16) lgkmcnt(0)\ns_barrier\nM v[72:75],a[100:103],v[216:219],v[72:75]\n" \
"ds_read_b128 a[0:3],v250 offset:0\nds_read_b128 a[4:7],v250 offset:1024\nM v[80:83],a[72:75],v[200:203],v[80:83]\nv_exp_f32 v147,v147\nM v[80:83],a[104:107],v[216:219],v[80:83]\nbuffer_load_dwordx4 v240,s[48:51],s95 offen offset:0 sc1 lds\nM v[88:91],a[76:79],v[200:203],v[88:91]\nv_exp_f32 v160,v160\n" \
"M v[88:91],a[108:111],v[216:219],v[88:91]\nds_read_b128 a[8:11],v250 offset:2048\nds_read_b128 a[12:15],v250 offset:3072\nM v[96:99],a[80:83],v[200:203],v[96:99]\nv_exp_f32 v161,v161\nM v[96:99],a[112:115],v[216:219],v[96:99]\nv_exp_f32 v162,v162\nM v[104:107],a[84:87],v[200:203],v[104:107]\n" \
"v_exp_f32 v163,v163\nM v[104:107],a[116:119],v[216:219],v[104:107]\nds_read_b128 a[16:19],v250 offset:4096\nds_read_b128 a[20:23],v250 offset:5120\nM v[112:115],a[88:91],v[200:203],v[112:115]\nv_exp_f32 v176,v176\nM v[112:115],a[120:123],v[216:219],v[112:115]\nbuffer_load_dwordx4 v240,s[48:51],s95 offen offset:1024 sc1 lds\n" \
"M v[120:123],a[92:95],v[200:203],v[120:123]\nv_exp_f32 v177,v177\nM v[120:123],a[124:127],v[216:219],v[120:123]\nds_read_b128 a[24:27],v250 offset:6144\nds_read_b128 a[28:31],v250 offset:7168\nM a[200:203],a[208:211],v[200:203],a[200:203]\nv_exp_f32 v178,v178\nM a[200:203],a[208:211],v[216:219],a[200:203]\n" \
"v_exp_f32 v179,v179\nM v[68:71],a[64:67],v[204:207],v[68:71]\nv_exp_f32 v132,v132\nM v[68:71],a[96:99],v[220:223],v[68:71]\nds_read_b128 a[32:35],v250 offset:8192\nds_read_b128 a[36:39],v250 offset:9216\nM v[76:79],a[68:71],v[204:207],v[76:79]\nv_exp_f32 v133,v133\n" \
"M v[76:79],a[100:103],v[220:223],v[76:79]\nbuffer_load_dwordx4 v240,s[48:51],s95 offen offset:2048 sc1 lds\nM v[84:87],a[72:75],v[204:207],v[84:87]\nv_exp_f32 v134,v134\nM v[84:87],a[104:107],v[220:223],v[84:87]\nds_read_b128 a[40:43],v250 offset:10240\nds_read_b128 a[44:47],v250 offset:11264\nM v[92:95],a[76:79],v[204:207],v[92:95]\n" \
"v_exp_f32 v135,v135\nM v[92:95],a[108:111],v[220:223],v[92:95]\nv_exp_f32 v148,v148\nM v[100:103],a[80:83],v[204:207],v[100:103]\nv_exp_f32 v149,v149\nM v[100:103],a[112:115],v[220:223],v[100:103]\nds_read_b128 a[48:51],v250 offset:12288\nds_read_b128 a[52:55],v250 offset:13312\n" \
"M v[108:111],a[84:87],v[204:207],v[108:111]\nv_exp_f32 v150,v150\nM v[108:111],a[116:119],v[220:223],v[108:111]\nbuffer_load_dwordx4 v240,s[48:51],s95 offen offset:3072 sc1 lds\nM v[116:119],a[88:91],v[204:207],v[116:119]\nv_exp_f32 v151,v151\nM v[116:119],a[120:123],v[220:223],v[116:119]\nds_read_b128 a[56:59],v250 offset:14336\n" \
"ds_read_b128 a[60:63],v250 offset:15360\nM v[124:127],a[92:95],v[204:207],v[124:127]\nv_cvt_pk_bf16_f32 v192,v128,v129\nv_cvt_pk_bf16_f32 v193,v130,v131\nM v[124:127],a[124:127],v[220:223],v[124:127]\nv_cvt_pk_bf16_f32 v194,v144,v145\nv_cvt_pk_bf16_f32 v195,v146,v147\nM a[204:207],a[208:211],v[204:207],a[204:207]\n" \
"v_cvt_pk_bf16_f32 v208,v160,v161\nv_cvt_pk_bf16_f32 v209,v162,v163\nv_cvt_pk_bf16_f32 v210,v176,v177\nv_cvt_pk_bf16_f32 v211,v178,v179\nv_add_u32 v250,s94,v241\ns_add_u32 s95,s95,0x4000\nM a[204:207],a[208:211],v[220:223],a[204:207]\ns_branch .Lse\n" \
".Ltrans:\ns_waitcnt lgkmcnt(4)\nM v[128:131],a[0:3],a[128:131],v[224:227]\ns_add_u32 m0,s94,s64\nv_cvt_pk_bf16_f32 v212,v164,v165\nM v[128:131],a[4:7],a[132:135],v[128:131]\nds_read_b128 a[64:67],v250 offset:0\nds_read_b128 a[96:99],v250 offset:8192\n" \
"M v[128:131],a[8:11],a[136:139],v[128:131]\nbuffer_load_dwordx4 v240,s[44:47],s95 offen offset:0 sc1 lds\nM v[128:131],a[12:15],a[140:143],v[128:131]\nv_cvt_pk_bf16_f32 v213,v166,v167\nv_cvt_pk_bf16_f32 v214,v180,v181\nM v[132:135],a[12:15],a[156:159],v[228:231]\nds_read_b128 a[68:71],v250 offset:1024\nds_read_b128 a[100:103],v250 offset:9216\n" \
"M v[132:135],a[8:11],a[152:155],v[132:135]\nbuffer_store_dwordx4 a[248:251],v253,s[52:55],s33 offen offset:0 sc1\nM v[132:135],a[4:7],a[148:151],v[132:135]\nv_cvt_pk_bf16_f32 v215,v182,v183\ns_add_u32 s71,s33,16384\nM v[132:135],a[0:3],a[144:147],v[132:135]\nds_read_b128 a[72:75],v250 offset:2048\nds_read_b128 a[104:107],v250 offset:10240\n" \
"M v[144:147],a[16:19],a[128:131],v[224:227]\nv_exp_f32 v184,v184\nM v[144:147],a[20:23],a[132:135],v[144:147]\nv_exp_f32 v185,v185\nM v[144:147],a[24:27],a[136:139],v[144:147]\nds_read_b128 a[76:79],v250 offset:3072\nds_read_b128 a[108:111],v250 offset:11264\nM v[144:147],a[28:31],a[140:143],v[144:147]\n" \
"buffer_load_dwordx4 v240,s[44:47],s95 offen offset:1024 sc1 lds\nM v[148:151],a[28:31],a[156:159],v[228:231]\nv_exp_f32 v186,v186\nM v[148:151],a[24:27],a[152:155],v[148:151]\nds_read_b128 a[80:83],v250 offset:4096\nds_read_b128 a[112:115],v250 offset:12288\nM v[148:151],a[20:23],a[148:151],v[148:151]\nbuffer_store_dwordx4 a[248:251],v253,s[52:55],s33 offen offset:1024 sc1\n" \
"M v[148:151],a[16:19],a[144:147],v[148:151]\nv_exp_f32 v187,v187\nM v[160:163],a[32:35],a[128:131],v[224:227]\nds_read_b128 a[84:87],v250 offset:5120\nds_read_b128 a[116:119],v250 offset:13312\nM v[160:163],a[36:39],a[132:135],v[160:163]\nv_cvt_pk_bf16_f32 v216,v168,v169\nv_cvt_pk_bf16_f32 v217,v170,v171\n" \
"M v[160:163],a[40:43],a[136:139],v[160:163]\nv_cvt_pk_bf16_f32 v218,v184,v185\nv_cvt_pk_bf16_f32 v219,v186,v187\nM v[160:163],a[44:47],a[140:143],v[160:163]\nds_read_b128 a[88:91],v250 offset:6144\nds_read_b128 a[120:123],v250 offset:14336\nM v[164:167],a[44:47],a[156:159],v[228:231]\nbuffer_load_dwordx4 v240,s[44:47],s95 offen offset:2048 sc1 lds\n" \
"M v[164:167],a[40:43],a[152:155],v[164:167]\ns_add_u32 s70,s78,16384\nv_cvt_pk_bf16_f32 v204,v140,v141\nM v[164:167],a[36:39],a[148:151],v[164:167]\nds_read_b128 a[92:95],v250 offset:7168\nds_read_b128 a[124:127],v250 offset:15360\nM v[164:167],a[32:35],a[144:147],v[164:167]\nbuffer_store_dwordx4 a[248:251],v253,s[52:55],s33 offen offset:2048 sc1\n" \
"s_waitcnt lgkmcnt(6)\nM v[176:179],a[48:51],a[128:131],v[224:227]\nv_xor_b32 v250,0x18000,v250\nv_cvt_pk_bf16_f32 v205,v142,v143\nM v[176:179],a[52:55],a[132:135],v[176:179]\nv_exp_f32 v172,v172\nM v[176:179],a[56:59],a[136:139],v[176:179]\nv_exp_f32 v173,v173\n" \
"M v[176:179],a[60:63],a[140:143],v[176:179]\nv_exp_f32 v174,v174\nM v[180:183],a[60:63],a[156:159],v[228:231]\nv_exp_f32 v175,v175\nM v[180:183],a[56:59],a[152:155],v[180:183]\nbuffer_load_dwordx4 v240,s[44:47],s95 offen offset:3072 sc1 lds\nM v[180:183],a[52:55],a[148:151],v[180:183]\ns_add_u32 m0,m0,0x10000\n" \
"v_cvt_pk_bf16_f32 v206,v156,v157\nM v[180:183],a[48:51],a[144:147],v[180:183]\nv_exp_f32 v188,v188\nM v[0:3],a[64:67],v[192:195],v[0:3]\nbuffer_store_dwordx4 a[248:251],v253,s[52:55],s33 offen offset:3072 sc1\nM v[0:3],a[96:99],v[208:211],v[0:3]\nv_exp_f32 v189,v189\nM v[8:11],a[68:71],v[192:195],v[8:11]\n" \
"s_waitcnt vmcnt(16) lgkmcnt(0)\ns_barrier\nM v[8:11],a[100:103],v[208:211],v[8:11]\nds_read_b128 a[0:3],v250 offset:0\nds_read_b128 a[4:7],v250 offset:1024\nM v[16:19],a[72:75],v[192:195],v[16:19]\nv_exp_f32 v190,v190\nM v[16:19],a[104:107],v[208:211],v[16:19]\n" \
"buffer_store_dwordx4 a[248:251],v253,s[52:55],s71 offen offset:0 sc1\nM v[24:27],a[76:79],v[192:195],v[24:27]\nv_exp_f32 v191,v191\nM v[24:27],a[108:111],v[208:211],v[24:27]\nbuffer_load_dwordx4 v240,s[48:51],s95 offen offset:0 sc1 lds\nM v[32:35],a[80:83],v[192:195],v[32:35]\nv_cvt_pk_bf16_f32 v207,v158,v159\nv_cvt_pk_bf16_f32 v220,v172,v173\n" \
"M v[32:35],a[112:115],v[208:211],v[32:35]\nbuffer_store_dwordx4 a[248:251],v253,s[52:55],s71 offen offset:1024 sc1\nM v[40:43],a[84:87],v[192:195],v[40:43]\nv_cvt_pk_bf16_f32 v221,v174,v175\nv_cvt_pk_bf16_f32 v222,v188,v189\nM v[40:43],a[116:119],v[208:211],v[40:43]\nds_read_b128 a[8:11],v250 offset:2048\nds_read_b128 a[12:15],v250 offset:3072\n" \
"M v[48:51],a[88:91],v[192:195],v[48:51]\nv_cvt_pk_bf16_f32 v223,v190,v191\ns_add_u32 s94,s94,0x4000\nM v[48:51],a[120:123],v[208:211],v[48:51]\nbuffer_store_dwordx4 a[248:251],v253,s[52:55],s71 offen offset:2048 sc1\nM v[56:59],a[92:95],v[192:195],v[56:59]\nv_exp_f32 v128,v128\nM v[56:59],a[124:127],v[208:211],v[56:59]\n" \
"v_exp_f32 v129,v129\nM a[192:195],a[208:211],v[192:195],a[192:195]\nv_exp_f32 v130,v130\nM a[192:195],a[208:211],v[208:211],a[192:195]\nbuffer_store_dwordx4 a[248:251],v253,s[52:55],s71 offen offset:3072 sc1\nM v[4:7],a[64:67],v[196:199],v[4:7]\nv_exp_f32 v131,v131\nM v[4:7],a[96:99],v[212:215],v[4:7]\n" \
"ds_read_b128 a[16:19],v250 offset:4096\nds_read_b128 a[20:23],v250 offset:5120\nM v[12:15],a[68:71],v[196:199],v[12:15]\nv_exp_f32 v144,v144\nM v[12:15],a[100:103],v[212:215],v[12:15]\nv_exp_f32 v145,v145\nM v[20:23],a[72:75],v[196:199],v[20:23]\nv_exp_f32 v146,v146\n" \
"M v[20:23],a[104:107],v[212:215],v[20:23]\nbuffer_load_dwordx4 v240,s[48:51],s95 offen offset:1024 sc1 lds\nM v[28:31],a[76:79],v[196:199],v[28:31]\nv_exp_f32 v147,v147\nM v[28:31],a[108:111],v[212:215],v[28:31]\nbuffer_store_dwordx4 a[248:251],v253,s[52:55],s78 offen offset:0 sc1\nM v[36:39],a[80:83],v[196:199],v[36:39]\nv_exp_f32 v160,v160\n" \
"M v[36:39],a[112:115],v[212:215],v[36:39]\nds_read_b128 a[24:27],v250 offset:6144\nds_read_b128 a[28:31],v250 offset:7168\nM v[44:47],a[84:87],v[196:199],v[44:47]\nv_exp_f32 v161,v161\nM v[44:47],a[116:119],v[212:215],v[44:47]\nbuffer_store_dwordx4 a[248:251],v253,s[52:55],s78 offen offset:1024 sc1\nM v[52:55],a[88:91],v[196:199],v[52:55]\n" \
"v_exp_f32 v162,v162\nM v[52:55],a[120:123],v[212:215],v[52:55]\nv_exp_f32 v163,v163\nM v[60:63],a[92:95],v[196:199],v[60:63]\nv_exp_f32 v176,v176\nM v[60:63],a[124:127],v[212:215],v[60:63]\nbuffer_store_dwordx4 a[248:251],v253,s[52:55],s78 offen offset:2048 sc1\nM a[196:199],a[208:211],v[196:199],a[196:199]\n" \
"v_exp_f32 v177,v177\nM a[196:199],a[208:211],v[212:215],a[196:199]\nds_read_b128 a[32:35],v250 offset:8192\nds_read_b128 a[36:39],v250 offset:9216\nM v[64:67],a[64:67],v[200:203],v[64:67]\nv_exp_f32 v178,v178\nM v[64:67],a[96:99],v[216:219],v[64:67]\nv_exp_f32 v179,v179\n" \
"M v[72:75],a[68:71],v[200:203],v[72:75]\ns_and_b32 s94,s94,0xffff\nv_cvt_pk_bf16_f32 v192,v128,v129\nM v[72:75],a[100:103],v[216:219],v[72:75]\nbuffer_load_dwordx4 v240,s[48:51],s95 offen offset:2048 sc1 lds\nM v[80:83],a[72:75],v[200:203],v[80:83]\nv_exp_f32 v132,v132\nM v[80:83],a[104:107],v[216:219],v[80:83]\n" \
"buffer_store_dwordx4 a[248:251],v253,s[52:55],s78 offen offset:3072 sc1\nM v[88:91],a[76:79],v[200:203],v[88:91]\nv_exp_f32 v133,v133\nM v[88:91],a[108:111],v[216:219],v[88:91]\nds_read_b128 a[40:43],v250 offset:10240\nds_read_b128 a[44:47],v250 offset:11264\nM v[96:99],a[80:83],v[200:203],v[96:99]\nv_exp_f32 v134,v134\n" \
"M v[96:99],a[112:115],v[216:219],v[96:99]\nbuffer_store_dwordx4 a[248:251],v253,s[52:55],s70 offen offset:0 sc1\nM v[104:107],a[84:87],v[200:203],v[104:107]\nv_exp_f32 v135,v135\nM v[104:107],a[116:119],v[216:219],v[104:107]\nv_exp_f32 v148,v148\nM v[112:115],a[88:91],v[200:203],v[112:115]\nv_exp_f32 v149,v149\n" \
"M v[112:115],a[120:123],v[216:219],v[112:115]\nbuffer_store_dwordx4 a[248:251],v253,s[52:55],s70 offen offset:1024 sc1\nM v[120:123],a[92:95],v[200:203],v[120:123]\nv_exp_f32 v150,v150\nM v[120:123],a[124:127],v[216:219],v[120:123]\nds_read_b128 a[48:51],v250 offset:12288\nds_read_b128 a[52:55],v250 offset:13312\nM a[200:203],a[208:211],v[200:203],a[200:203]\n" \
"v_exp_f32 v151,v151\nM a[200:203],a[208:211],v[216:219],a[200:203]\nv_cvt_pk_bf16_f32 v193,v130,v131\nv_cvt_pk_bf16_f32 v194,v144,v145\nM v[68:71],a[64:67],v[204:207],v[68:71]\nv_cvt_pk_bf16_f32 v195,v146,v147\nv_cvt_pk_bf16_f32 v208,v160,v161\nM v[68:71],a[96:99],v[220:223],v[68:71]\n" \
"buffer_load_dwordx4 v240,s[48:51],s95 offen offset:3072 sc1 lds\nM v[76:79],a[68:71],v[204:207],v[76:79]\nv_cvt_pk_bf16_f32 v209,v162,v163\nv_cvt_pk_bf16_f32 v210,v176,v177\nM v[76:79],a[100:103],v[220:223],v[76:79]\nbuffer_store_dwordx4 a[248:251],v253,s[52:55],s70 offen offset:2048 sc1\nM v[84:87],a[72:75],v[204:207],v[84:87]\nv_cvt_pk_bf16_f32 v211,v178,v179\n" \
"s_add_u32 s95,s95,0x4000\nM v[84:87],a[104:107],v[220:223],v[84:87]\nds_read_b128 a[56:59],v250 offset:14336\nds_read_b128 a[60:63],v250 offset:15360\nM v[92:95],a[76:79],v[204:207],v[92:95]\nv_add_u32 v250,s94,v241\nM v[92:95],a[108:111],v[220:223],v[92:95]\nbuffer_store_dwordx4 a[248:251],v253,s[52:55],s70 offen offset:3072 sc1\n" \
"M v[100:103],a[80:83],v[204:207],v[100:103]\nM v[100:103],a[112:115],v[220:223],v[100:103]\nM v[108:111],a[84:87],v[204:207],v[108:111]\nM v[108:111],a[116:119],v[220:223],v[108:111]\nM v[116:119],a[88:91],v[204:207],v[116:119]\nM v[116:119],a[120:123],v[220:223],v[116:119]\nM v[124:127],a[92:95],v[204:207],v[124:127]\nM v[124:127],a[124:127],v[220:223],v[124:127]\n" \
"M a[204:207],a[208:211],v[204:207],a[204:207]\nM a[204:207],a[208:211],v[220:223],a[204:207]\n.Lse:\ns_waitcnt lgkmcnt(4)\nM v[128:131],a[0:3],a[128:131],v[224:227]\ns_add_u32 m0,s94,s64\nv_accvgpr_read_b32 v232,a200\nv_cvt_pk_bf16_f32 v196,v132,v133\n" \
"v_cvt_pk_bf16_f32 v197,v134,v135\nM v[128:131],a[4:7],a[132:135],v[128:131]\nds_read_b128 a[64:67],v250 offset:0\nds_read_b128 a[96:99],v250 offset:8192\nv_accvgpr_read_b32 v236,a204\nv_add_f32 v233,v232,v236\nM v[128:131],a[8:11],a[136:139],v[128:131]\nbuffer_load_dwordx4 v240,s[44:47],s95 offen offset:0 sc1 lds\n" \
"v_rcp_f32 v232,v232\nM v[128:131],a[12:15],a[140:143],v[128:131]\nv_rcp_f32 v236,v236\nv_add_f32 v233,v233,v232\nv_add_f32 v233,v233,v236\nM v[132:135],a[12:15],a[156:159],v[228:231]\nds_read_b128 a[68:71],v250 offset:1024\nds_read_b128 a[100:103],v250 offset:9216\n" \
"v_cmp_ngt_f32 vcc,s28,v233\nv_mul_f32 v64,v64,v232\nM v[132:135],a[8:11],a[152:155],v[132:135]\ns_and_b64 vcc,vcc,s[90:91]\ns_cselect_b32 s39,0x8000000,s16\ns_cselect_b32 s16,s16,0x8000000\ns_cselect_b32 s71,2,1\ns_addc_u32 s99,s99,0\n" \
"M v[132:135],a[4:7],a[148:151],v[132:135]\nv_mul_f32 v65,v65,v232\ns_add_u32 s37,s12,s67\nv_mul_f32 v66,v66,v232\nv_mul_f32 v67,v67,v232\nM v[132:135],a[0:3],a[144:147],v[132:135]\nds_read_b128 a[72:75],v250 offset:2048\nds_read_b128 a[104:107],v250 offset:10240\n" \
"v_cvt_pk_bf16_f32 v64,v64,v65\ns_add_u32 s37,s37,1048576\nM v[144:147],a[16:19],a[128:131],v[224:227]\nv_mul_f32 v72,v72,v232\nv_mul_f32 v73,v73,v232\nv_mul_f32 v74,v74,v232\nv_mul_f32 v75,v75,v232\nv_cvt_pk_bf16_f32 v65,v66,v67\n" \
"M v[144:147],a[20:23],a[132:135],v[144:147]\ns_or_b32 s37,s37,s39\nv_cvt_pk_bf16_f32 v66,v72,v73\nv_cvt_pk_bf16_f32 v67,v74,v75\nbuffer_store_dwordx4 v[64:67],v240,s[52:55],s37 offen offset:0 sc1\nM v[144:147],a[24:27],a[136:139],v[144:147]\nds_read_b128 a[76:79],v250 offset:3072\nds_read_b128 a[108:111],v250 offset:11264\n" \
"v_cvt_pk_bf16_f32 v198,v148,v149\nv_cvt_pk_bf16_f32 v199,v150,v151\nv_mul_f32 v68,v68,v236\nM v[144:147],a[28:31],a[140:143],v[144:147]\nbuffer_load_dwordx4 v240,s[44:47],s95 offen offset:1024 sc1 lds\nv_mul_f32 v69,v69,v236\nv_mul_f32 v70,v70,v236\nM v[148:151],a[28:31],a[156:159],v[228:231]\n" \
"v_mul_f32 v71,v71,v236\nv_cvt_pk_bf16_f32 v68,v68,v69\nv_mul_f32 v76,v76,v236\nv_mul_f32 v77,v77,v236\nM v[148:151],a[24:27],a[152:155],v[148:151]\nds_read_b128 a[80:83],v250 offset:4096\nds_read_b128 a[112:115],v250 offset:12288\nv_mul_f32 v78,v78,v236\n" \
"v_mul_f32 v79,v79,v236\nM v[148:151],a[20:23],a[148:151],v[148:151]\nv_cvt_pk_bf16_f32 v69,v70,v71\nv_cvt_pk_bf16_f32 v70,v76,v77\nv_cvt_pk_bf16_f32 v71,v78,v79\nv_mul_f32 v80,v80,v232\nM v[148:151],a[16:19],a[144:147],v[148:151]\nv_exp_f32 v164,v164\n" \
"v_exp_f32 v165,v165\nM v[160:163],a[32:35],a[128:131],v[224:227]\nds_read_b128 a[84:87],v250 offset:5120\nds_read_b128 a[116:119],v250 offset:13312\nv_exp_f32 v166,v166\nv_cvt_pk_bf16_f32 v212,v164,v165\nM v[160:163],a[36:39],a[132:135],v[160:163]\nbuffer_store_dwordx4 v[68:71],v240,s[52:55],s37 offen offset:1024 sc1\n" \
"v_exp_f32 v167,v167\nM v[160:163],a[40:43],a[136:139],v[160:163]\nv_cvt_pk_bf16_f32 v213,v166,v167\nv_mul_f32 v81,v81,v232\nv_mul_f32 v82,v82,v232\nv_mul_f32 v83,v83,v232\nM v[160:163],a[44:47],a[140:143],v[160:163]\nds_read_b128 a[88:91],v250 offset:6144\n" \
"ds_read_b128 a[120:123],v250 offset:14336\nv_cvt_pk_bf16_f32 v80,v80,v81\nv_mul_f32 v88,v88,v232\nM v[164:167],a[44:47],a[156:159],v[228:231]\nbuffer_load_dwordx4 v240,s[44:47],s95 offen offset:2048 sc1 lds\nv_mul_f32 v89,v89,v232\nv_mul_f32 v90,v90,v232\nM v[164:167],a[40:43],a[152:155],v[164:167]\n" \
"v_mul_f32 v91,v91,v232\nv_cvt_pk_bf16_f32 v81,v82,v83\nv_cvt_pk_bf16_f32 v82,v88,v89\nv_cvt_pk_bf16_f32 v83,v90,v91\nv_mul_f32 v84,v84,v236\nM v[164:167],a[36:39],a[148:151],v[164:167]\nds_read_b128 a[92:95],v250 offset:7168\nds_read_b128 a[124:127],v250 offset:15360\n" \
"v_exp_f32 v180,v180\nM v[164:167],a[32:35],a[144:147],v[164:167]\nv_exp_f32 v181,v181\nv_mul_f32 v85,v85,v236\ns_waitcnt lgkmcnt(4)\nM v[176:179],a[48:51],a[128:131],v[224:227]\nv_exp_f32 v182,v182\nv_exp_f32 v183,v183\n" \
"M v[176:179],a[52:55],a[132:135],v[176:179]\nbuffer_store_dwordx4 v[80:83],v240,s[52:55],s37 offen offset:2048 sc1\nv_cvt_pk_bf16_f32 v214,v180,v181\nv_cvt_pk_bf16_f32 v215,v182,v183\nM v[176:179],a[56:59],a[136:139],v[176:179]\nv_mul_f32 v86,v86,v236\nv_mul_f32 v87,v87,v236\nv_cvt_pk_bf16_f32 v84,v84,v85\n" \
"v_mul_f32 v92,v92,v236\nM v[176:179],a[60:63],a[140:143],v[176:179]\nv_mul_f32 v93,v93,v236\nv_mul_f32 v94,v94,v236\nv_mul_f32 v95,v95,v236\nv_cvt_pk_bf16_f32 v85,v86,v87\nv_cvt_pk_bf16_f32 v86,v92,v93\nM v[180:183],a[60:63],a[156:159],v[228:231]\n" \
"v_cvt_pk_bf16_f32 v87,v94,v95\nv_xor_b32 v250,0x18000,v250\nv_mul_f32 v96,v96,v232\nv_mul_f32 v97,v97,v232\nM v[180:183],a[56:59],a[152:155],v[180:183]\nbuffer_load_dwordx4 v240,s[44:47],s95 offen offset:3072 sc1 lds\ns_add_u32 m0,m0,0x10000\nv_mul_f32 v98,v98,v232\n" \
"M v[180:183],a[52:55],a[148:151],v[180:183]\nv_mul_f32 v99,v99,v232\nv_cvt_pk_bf16_f32 v96,v96,v97\nv_mul_f32 v104,v104,v232\nv_mul_f32 v105,v105,v232\nM v[180:183],a[48:51],a[144:147],v[180:183]\nv_mul_f32 v106,v106,v232\nv_mul_f32 v107,v107,v232\n" \
"v_cvt_pk_bf16_f32 v97,v98,v99\ns_add_u32 s38,s37,4096\nM v[0:3],a[64:67],v[192:195],v[0:3]\nv_cvt_pk_bf16_f32 v98,v104,v105\nv_cvt_pk_bf16_f32 v99,v106,v107\nv_mul_f32 v100,v100,v236\nv_mul_f32 v101,v101,v236\nv_mul_f32 v102,v102,v236\n" \
"M v[0:3],a[96:99],v[208:211],v[0:3]\nbuffer_store_dwordx4 v[84:87],v240,s[52:55],s37 offen offset:3072 sc1\nv_mul_f32 v103,v103,v236\nv_cvt_pk_bf16_f32 v100,v100,v101\nM v[8:11],a[68:71],v[192:195],v[8:11]\ns_waitcnt vmcnt(16) lgkmcnt(0)\ns_barrier\nv_mul_f32 v108,v108,v236\n" \
"v_mul_f32 v109,v109,v236\nM v[8:11],a[100:103],v[208:211],v[8:11]\nds_read_b128 a[0:3],v250 offset:0\nds_read_b128 a[4:7],v250 offset:1024\nv_mul_f32 v110,v110,v236\nv_mul_f32 v111,v111,v236\nM v[16:19],a[72:75],v[192:195],v[16:19]\nv_cvt_pk_bf16_f32 v101,v102,v103\n" \
"v_cvt_pk_bf16_f32 v102,v108,v109\nv_cvt_pk_bf16_f32 v103,v110,v111\nv_mul_f32 v112,v112,v232\nM v[16:19],a[104:107],v[208:211],v[16:19]\nbuffer_load_dwordx4 v240,s[48:51],s95 offen offset:0 sc1 lds\nv_mul_f32 v113,v113,v232\nv_mul_f32 v114,v114,v232\nv_mul_f32 v115,v115,v232\n" \
"M v[24:27],a[76:79],v[192:195],v[24:27]\nv_cvt_pk_bf16_f32 v112,v112,v113\nv_mul_f32 v120,v120,v232\nv_mul_f32 v121,v121,v232\nv_mul_f32 v122,v122,v232\nM v[24:27],a[108:111],v[208:211],v[24:27]\nds_read_b128 a[8:11],v250 offset:2048\nds_read_b128 a[12:15],v250 offset:3072\n" \
"v_mul_f32 v123,v123,v232\nv_cvt_pk_bf16_f32 v113,v114,v115\nM v[32:35],a[80:83],v[192:195],v[32:35]\nv_cvt_pk_bf16_f32 v114,v120,v121\nv_cvt_pk_bf16_f32 v115,v122,v123\nv_mul_f32 v116,v116,v236\nv_mul_f32 v117,v117,v236\nM v[32:35],a[112:115],v[208:211],v[32:35]\n" \
"buffer_store_dwordx4 v[96:99],v240,s[52:55],s38 offen offset:0 sc1\nv_mul_f32 v118,v118,v236\nv_mul_f32 v119,v119,v236\nM v[40:43],a[84:87],v[192:195],v[40:43]\nv_cvt_pk_bf16_f32 v116,v116,v117\nv_mul_f32 v124,v124,v236\nv_mul_f32 v125,v125,v236\nv_mul_f32 v126,v126,v236\n" \
"M v[40:43],a[116:119],v[208:211],v[40:43]\nds_read_b128 a[16:19],v250 offset:4096\nds_read_b128 a[20:23],v250 offset:5120\nv_mul_f32 v127,v127,v236\nv_cvt_pk_bf16_f32 v117,v118,v119\nv_cvt_pk_bf16_f32 v118,v124,v125\nM v[48:51],a[88:91],v[192:195],v[48:51]\nv_cvt_pk_bf16_f32 v119,v126,v127\n" \
"v_exp_f32 v128,v128\ns_add_u32 s94,s94,0x4000\nM v[48:51],a[120:123],v[208:211],v[48:51]\nbuffer_load_dwordx4 v240,s[48:51],s95 offen offset:1024 sc1 lds\nv_exp_f32 v129,v129\nM v[56:59],a[92:95],v[192:195],v[56:59]\nv_exp_f32 v130,v130\nv_exp_f32 v131,v131\n" \
"M v[56:59],a[124:127],v[208:211],v[56:59]\nds_read_b128 a[24:27],v250 offset:6144\nds_read_b128 a[28:31],v250 offset:7168\nv_exp_f32 v144,v144\nM a[192:195],a[208:211],v[192:195],a[192:195]\nv_exp_f32 v145,v145\nv_exp_f32 v146,v146\ns_lshl2_add_u32 s77,s61,s79\n" \
"M a[192:195],a[208:211],v[208:211],a[192:195]\nbuffer_store_dwordx4 v[100:103],v240,s[52:55],s38 offen offset:1024 sc1\nv_exp_f32 v147,v147\nM v[4:7],a[64:67],v[196:199],v[4:7]\nv_exp_f32 v160,v160\nv_exp_f32 v161,v161\nM v[4:7],a[96:99],v[212:215],v[4:7]\nds_read_b128 a[32:35],v250 offset:8192\n" \
"ds_read_b128 a[36:39],v250 offset:9216\nv_exp_f32 v162,v162\nM v[12:15],a[68:71],v[196:199],v[12:15]\nv_exp_f32 v163,v163\nv_exp_f32 v176,v176\nM v[12:15],a[100:103],v[212:215],v[12:15]\nbuffer_load_dwordx4 v240,s[48:51],s95 offen offset:2048 sc1 lds\nv_exp_f32 v177,v177\n" \
"M v[20:23],a[72:75],v[196:199],v[20:23]\nv_exp_f32 v178,v178\nv_exp_f32 v179,v179\ns_and_b32 s94,s94,0xffff\nM v[20:23],a[104:107],v[212:215],v[20:23]\nds_read_b128 a[40:43],v250 offset:10240\nds_read_b128 a[44:47],v250 offset:11264\ns_add_u32 s77,s77,4096\n" \
"v_mov_b32 v233,s71\nM v[28:31],a[76:79],v[196:199],v[28:31]\nv_exp_f32 v132,v132\nv_exp_f32 v133,v133\nM v[28:31],a[108:111],v[212:215],v[28:31]\nbuffer_store_dwordx4 v[112:115],v240,s[52:55],s38 offen offset:2048 sc1\nv_exp_f32 v134,v134\nM v[36:39],a[80:83],v[196:199],v[36:39]\n" \
"v_exp_f32 v135,v135\nv_exp_f32 v148,v148\nM v[36:39],a[112:115],v[212:215],v[36:39]\nds_read_b128 a[48:51],v250 offset:12288\nds_read_b128 a[52:55],v250 offset:13312\nv_exp_f32 v149,v149\nv_cvt_pk_bf16_f32 v192,v128,v129\nM v[44:47],a[84:87],v[196:199],v[44:47]\n" \
"v_exp_f32 v150,v150\nv_exp_f32 v151,v151\nM v[44:47],a[116:119],v[212:215],v[44:47]\nbuffer_load_dwordx4 v240,s[48:51],s95 offen offset:3072 sc1 lds\nv_cvt_pk_bf16_f32 v193,v130,v131\nv_cvt_pk_bf16_f32 v194,v144,v145\nM v[52:55],a[88:91],v[196:199],v[52:55]\nv_cvt_pk_bf16_f32 v195,v146,v147\n" \
"v_cvt_pk_bf16_f32 v208,v160,v161\nv_cvt_pk_bf16_f32 v209,v162,v163\nv_cvt_pk_bf16_f32 v210,v176,v177\nM v[52:55],a[120:123],v[212:215],v[52:55]\nds_read_b128 a[56:59],v250 offset:14336\nds_read_b128 a[60:63],v250 offset:15360\nv_cvt_pk_bf16_f32 v211,v178,v179\nv_add_u32 v250,s94,v241\n" \
"M v[60:63],a[92:95],v[196:199],v[60:63]\ns_add_u32 s95,s95,0x4000\nM v[60:63],a[124:127],v[212:215],v[60:63]\nbuffer_store_dwordx4 v[116:119],v240,s[52:55],s38 offen offset:3072 sc1\nM a[196:199],a[208:211],v[196:199],a[196:199]\nM a[196:199],a[208:211],v[212:215],a[196:199]\ns_cmp_ge_u32 s95,s15\ns_cbranch_scc1 .Lsx\n" \
".Lsingle:\ns_waitcnt lgkmcnt(4)\nM v[128:131],a[0:3],a[128:131],v[224:227]\ns_xor_b32 m0,m0,0x14000\nv_cvt_pk_bf16_f32 v196,v132,v133\nM v[128:131],a[4:7],a[132:135],v[128:131]\nds_read_b128 a[64:67],v250 offset:0\nds_read_b128 a[96:99],v250 offset:8192\n" \
"M v[128:131],a[8:11],a[136:139],v[128:131]\nbuffer_load_dwordx4 v240,s[44:47],s95 offen offset:0 sc1 lds\nM v[128:131],a[12:15],a[140:143],v[128:131]\nv_cvt_pk_bf16_f32 v197,v134,v135\nv_cvt_pk_bf16_f32 v198,v148,v149\nM v[132:135],a[12:15],a[156:159],v[228:231]\nds_read_b128 a[68:71],v250 offset:1024\nds_read_b128 a[100:103],v250 offset:9216\n" \
"M v[132:135],a[8:11],a[152:155],v[132:135]\nv_exp_f32 v164,v164\nM v[132:135],a[4:7],a[148:151],v[132:135]\nv_exp_f32 v165,v165\nM v[132:135],a[0:3],a[144:147],v[132:135]\nds_read_b128 a[72:75],v250 offset:2048\nds_read_b128 a[104:107],v250 offset:10240\nM v[144:147],a[16:19],a[128:131],v[224:227]\n" \
"v_cvt_pk_bf16_f32 v199,v150,v151\nv_cvt_pk_bf16_f32 v212,v164,v165\nM v[144:147],a[20:23],a[132:135],v[144:147]\nv_exp_f32 v166,v166\nM v[144:147],a[24:27],a[136:139],v[144:147]\nds_read_b128 a[76:79],v250 offset:3072\nds_read_b128 a[108:111],v250 offset:11264\nM v[144:147],a[28:31],a[140:143],v[144:147]\n" \
"buffer_load_dwordx4 v240,s[44:47],s95 offen offset:1024 sc1 lds\nM v[148:151],a[28:31],a[156:159],v[228:231]\nv_exp_f32 v167,v167\nM v[148:151],a[24:27],a[152:155],v[148:151]\nds_read_b128 a[80:83],v250 offset:4096\nds_read_b128 a[112:115],v250 offset:12288\nM v[148:151],a[20:23],a[148:151],v[148:151]\nv_exp_f32 v180,v180\n" \
"M v[148:151],a[16:19],a[144:147],v[148:151]\nv_exp_f32 v181,v181\nM v[160:163],a[32:35],a[128:131],v[224:227]\nds_read_b128 a[84:87],v250 offset:5120\nds_read_b128 a[116:119],v250 offset:13312\nM v[160:163],a[36:39],a[132:135],v[160:163]\nv_cvt_pk_bf16_f32 v213,v166,v167\nv_cvt_pk_bf16_f32 v214,v180,v181\n" \
"M v[160:163],a[40:43],a[136:139],v[160:163]\nv_exp_f32 v182,v182\nM v[160:163],a[44:47],a[140:143],v[160:163]\nds_read_b128 a[88:91],v250 offset:6144\nds_read_b128 a[120:123],v250 offset:14336\nM v[164:167],a[44:47],a[156:159],v[228:231]\nbuffer_load_dwordx4 v240,s[44:47],s95 offen offset:2048 sc1 lds\nM v[164:167],a[40:43],a[152:155],v[164:167]\n" \
"v_exp_f32 v183,v183\nM v[164:167],a[36:39],a[148:151],v[164:167]\nds_read_b128 a[92:95],v250 offset:7168\nds_read_b128 a[124:127],v250 offset:15360\nM v[164:167],a[32:35],a[144:147],v[164:167]\nv_cvt_pk_bf16_f32 v215,v182,v183\ns_waitcnt lgkmcnt(7)\nM v[176:179],a[48:51],a[128:131],v[224:227]\n" \
"v_exp_f32 v128,v128\nM v[176:179],a[52:55],a[132:135],v[176:179]\nv_exp_f32 v129,v129\nM v[176:179],a[56:59],a[136:139],v[176:179]\nv_exp_f32 v130,v130\nM v[176:179],a[60:63],a[140:143],v[176:179]\nv_exp_f32 v131,v131\nM v[180:183],a[60:63],a[156:159],v[228:231]\n" \
"v_exp_f32 v132,v132\nM v[180:183],a[56:59],a[152:155],v[180:183]\nbuffer_load_dwordx4 v240,s[44:47],s95 offen offset:3072 sc1 lds\nM v[180:183],a[52:55],a[148:151],v[180:183]\nv_xor_b32 v250,0x18000,v250\ns_xor_b32 m0,m0,0x10000\nM v[180:183],a[48:51],a[144:147],v[180:183]\nv_exp_f32 v133,v133\n" \
"M v[0:3],a[64:67],v[192:195],v[0:3]\nv_exp_f32 v134,v134\nM v[0:3],a[96:99],v[208:211],v[0:3]\nv_exp_f32 v135,v135\nM v[8:11],a[68:71],v[192:195],v[8:11]\ns_waitcnt vmcnt(16) lgkmcnt(0)\ns_barrier\nM v[8:11],a[100:103],v[208:211],v[8:11]\n" \
"ds_read_b128 a[0:3],v250 offset:0\nds_read_b128 a[4:7],v250 offset:1024\nM v[16:19],a[72:75],v[192:195],v[16:19]\nv_exp_f32 v144,v144\nM v[16:19],a[104:107],v[208:211],v[16:19]\nbuffer_load_dwordx4 v240,s[48:51],s95 offen offset:0 sc1 lds\nM v[24:27],a[76:79],v[192:195],v[24:27]\nv_exp_f32 v145,v145\n" \
"M v[24:27],a[108:111],v[208:211],v[24:27]\nds_read_b128 a[8:11],v250 offset:2048\nds_read_b128 a[12:15],v250 offset:3072\nM v[32:35],a[80:83],v[192:195],v[32:35]\nv_exp_f32 v146,v146\nM v[32:35],a[112:115],v[208:211],v[32:35]\nv_exp_f32 v147,v147\nM v[40:43],a[84:87],v[192:195],v[40:43]\n" \
"v_exp_f32 v148,v148\nM v[40:43],a[116:119],v[208:211],v[40:43]\nds_read_b128 a[16:19],v250 offset:4096\nds_read_b128 a[20:23],v250 offset:5120\nM v[48:51],a[88:91],v[192:195],v[48:51]\nv_exp_f32 v149,v149\nM v[48:51],a[120:123],v[208:211],v[48:51]\nbuffer_load_dwordx4 v240,s[48:51],s95 offen offset:1024 sc1 lds\n" \
"M v[56:59],a[92:95],v[192:195],v[56:59]\nv_exp_f32 v150,v150\nM v[56:59],a[124:127],v[208:211],v[56:59]\nds_read_b128 a[24:27],v250 offset:6144\nds_read_b128 a[28:31],v250 offset:7168\nM a[192:195],a[208:211],v[192:195],a[192:195]\nv_cvt_pk_bf16_f32 v192,v128,v129\nv_cvt_pk_bf16_f32 v193,v130,v131\n" \
"M a[192:195],a[208:211],v[208:211],a[192:195]\nv_cvt_pk_bf16_f32 v194,v144,v145\nv_cvt_pk_bf16_f32 v195,v146,v147\nM v[4:7],a[64:67],v[196:199],v[4:7]\nv_exp_f32 v151,v151\nM v[4:7],a[96:99],v[212:215],v[4:7]\nds_read_b128 a[32:35],v250 offset:8192\nds_read_b128 a[36:39],v250 offset:9216\n" \
"M v[12:15],a[68:71],v[196:199],v[12:15]\nv_exp_f32 v160,v160\nM v[12:15],a[100:103],v[212:215],v[12:15]\nbuffer_load_dwordx4 v240,s[48:51],s95 offen offset:2048 sc1 lds\nM v[20:23],a[72:75],v[196:199],v[20:23]\nv_exp_f32 v161,v161\nM v[20:23],a[104:107],v[212:215],v[20:23]\nds_read_b128 a[40:43],v250 offset:10240\n" \
"ds_read_b128 a[44:47],v250 offset:11264\nM v[28:31],a[76:79],v[196:199],v[28:31]\nv_exp_f32 v162,v162\nM v[28:31],a[108:111],v[212:215],v[28:31]\nv_exp_f32 v163,v163\nM v[36:39],a[80:83],v[196:199],v[36:39]\nv_cvt_pk_bf16_f32 v208,v160,v161\nv_cvt_pk_bf16_f32 v209,v162,v163\n" \
"M v[36:39],a[112:115],v[212:215],v[36:39]\nds_read_b128 a[48:51],v250 offset:12288\nds_read_b128 a[52:55],v250 offset:13312\nM v[44:47],a[84:87],v[196:199],v[44:47]\nv_exp_f32 v164,v164\nM v[44:47],a[116:119],v[212:215],v[44:47]\nbuffer_load_dwordx4 v240,s[48:51],s95 offen offset:3072 sc1 lds\nM v[52:55],a[88:91],v[196:199],v[52:55]\n" \
"s_add_u32 s95,s95,0x4000\ns_xor_b32 m0,m0,0x1c000\nM v[52:55],a[120:123],v[212:215],v[52:55]\nds_read_b128 a[56:59],v250 offset:14336\nds_read_b128 a[60:63],v250 offset:15360\nM v[60:63],a[92:95],v[196:199],v[60:63]\nv_exp_f32 v165,v165\nM v[60:63],a[124:127],v[212:215],v[60:63]\n" \
"v_exp_f32 v166,v166\nM a[196:199],a[208:211],v[196:199],a[196:199]\nv_xor_b32 v250,0x14000,v250\nv_cvt_pk_bf16_f32 v196,v132,v133\nM a[196:199],a[208:211],v[212:215],a[196:199]\nv_cvt_pk_bf16_f32 v197,v134,v135\ns_waitcnt lgkmcnt(4)\nM v[128:131],a[0:3],a[128:131],v[224:227]\n" \
"v_cvt_pk_bf16_f32 v198,v148,v149\nv_cvt_pk_bf16_f32 v199,v150,v151\nM v[128:131],a[4:7],a[132:135],v[128:131]\nds_read_b128 a[64:67],v250 offset:0\nds_read_b128 a[96:99],v250 offset:8192\nM v[128:131],a[8:11],a[136:139],v[128:131]\nbuffer_load_dwordx4 v240,s[44:47],s95 offen offset:0 sc1 lds\nM v[128:131],a[12:15],a[140:143],v[128:131]\n" \
"v_exp_f32 v167,v167\nM v[132:135],a[12:15],a[156:159],v[228:231]\nds_read_b128 a[68:71],v250 offset:1024\nds_read_b128 a[100:103],v250 offset:9216\nM v[132:135],a[8:11],a[152:155],v[132:135]\nv_cvt_pk_bf16_f32 v212,v164,v165\nv_cvt_pk_bf16_f32 v213,v166,v167\nM v[132:135],a[4:7],a[148:151],v[132:135]\n" \
"v_exp_f32 v176,v176\nM v[132:135],a[0:3],a[144:147],v[132:135]\nds_read_b128 a[72:75],v250 offset:2048\nds_read_b128 a[104:107],v250 offset:10240\nM v[144:147],a[16:19],a[128:131],v[224:227]\nv_exp_f32 v177,v177\nM v[144:147],a[20:23],a[132:135],v[144:147]\nv_exp_f32 v178,v178\n" \
"M v[144:147],a[24:27],a[136:139],v[144:147]\nds_read_b128 a[76:79],v250 offset:3072\nds_read_b128 a[108:111],v250 offset:11264\nM v[144:147],a[28:31],a[140:143],v[144:147]\nbuffer_load_dwordx4 v240,s[44:47],s95 offen offset:1024 sc1 lds\nM v[148:151],a[28:31],a[156:159],v[228:231]\nv_exp_f32 v179,v179\nM v[148:151],a[24:27],a[152:155],v[148:151]\n" \
"ds_read_b128 a[80:83],v250 offset:4096\nds_read_b128 a[112:115],v250 offset:12288\nM v[148:151],a[20:23],a[148:151],v[148:151]\nv_cvt_pk_bf16_f32 v210,v176,v177\nv_cvt_pk_bf16_f32 v211,v178,v179\nM v[148:151],a[16:19],a[144:147],v[148:151]\nv_exp_f32 v180,v180\nM v[160:163],a[32:35],a[128:131],v[224:227]\n" \
"ds_read_b128 a[84:87],v250 offset:5120\nds_read_b128 a[116:119],v250 offset:13312\nM v[160:163],a[36:39],a[132:135],v[160:163]\nv_exp_f32 v181,v181\nM v[160:163],a[40:43],a[136:139],v[160:163]\nv_exp_f32 v182,v182\nM v[160:163],a[44:47],a[140:143],v[160:163]\nds_read_b128 a[88:91],v250 offset:6144\n" \
"ds_read_b128 a[120:123],v250 offset:14336\nM v[164:167],a[44:47],a[156:159],v[228:231]\nbuffer_load_dwordx4 v240,s[44:47],s95 offen offset:2048 sc1 lds\nM v[164:167],a[40:43],a[152:155],v[164:167]\nv_exp_f32 v183,v183\nM v[164:167],a[36:39],a[148:151],v[164:167]\nds_read_b128 a[92:95],v250 offset:7168\nds_read_b128 a[124:127],v250 offset:15360\n" \
"M v[164:167],a[32:35],a[144:147],v[164:167]\nv_cvt_pk_bf16_f32 v214,v180,v181\ns_waitcnt lgkmcnt(7)\nM v[176:179],a[48:51],a[128:131],v[224:227]\nv_cvt_pk_bf16_f32 v215,v182,v183\nv_xor_b32 v250,0x18000,v250\nM v[176:179],a[52:55],a[132:135],v[176:179]\nv_exp_f32 v128,v128\n" \
"M v[176:179],a[56:59],a[136:139],v[176:179]\nv_exp_f32 v129,v129\nM v[176:179],a[60:63],a[140:143],v[176:179]\nv_exp_f32 v130,v130\nM v[180:183],a[60:63],a[156:159],v[228:231]\nv_exp_f32 v131,v131\nM v[180:183],a[56:59],a[152:155],v[180:183]\nbuffer_load_dwordx4 v240,s[44:47],s95 offen offset:3072 sc1 lds\n" \
"M v[180:183],a[52:55],a[148:151],v[180:183]\nv_exp_f32 v144,v144\nM v[180:183],a[48:51],a[144:147],v[180:183]\nv_exp_f32 v145,v145\nM v[0:3],a[64:67],v[192:195],v[0:3]\nv_exp_f32 v146,v146\nM v[0:3],a[96:99],v[208:211],v[0:3]\nv_exp_f32 v147,v147\n" \
"M v[8:11],a[68:71],v[192:195],v[8:11]\ns_waitcnt vmcnt(16) lgkmcnt(0)\ns_barrier\nM v[8:11],a[100:103],v[208:211],v[8:11]\nds_read_b128 a[0:3],v250 offset:0\nds_read_b128 a[4:7],v250 offset:1024\nM v[16:19],a[72:75],v[192:195],v[16:19]\ns_xor_b32 m0,m0,0x10000\n" \
"M v[16:19],a[104:107],v[208:211],v[16:19]\nbuffer_load_dwordx4 v240,s[48:51],s95 offen offset:0 sc1 lds\nM v[24:27],a[76:79],v[192:195],v[24:27]\nv_exp_f32 v160,v160\nM v[24:27],a[108:111],v[208:211],v[24:27]\nds_read_b128 a[8:11],v250 offset:2048\nds_read_b128 a[12:15],v250 offset:3072\nM v[32:35],a[80:83],v[192:195],v[32:35]\n" \
"v_exp_f32 v161,v161\nM v[32:35],a[112:115],v[208:211],v[32:35]\nv_exp_f32 v162,v162\nM v[40:43],a[84:87],v[192:195],v[40:43]\nv_exp_f32 v163,v163\nM v[40:43],a[116:119],v[208:211],v[40:43]\nds_read_b128 a[16:19],v250 offset:4096\nds_read_b128 a[20:23],v250 offset:5120\n" \
"M v[48:51],a[88:91],v[192:195],v[48:51]\nv_exp_f32 v176,v176\nM v[48:51],a[120:123],v[208:211],v[48:51]\nbuffer_load_dwordx4 v240,s[48:51],s95 offen offset:1024 sc1 lds\nM v[56:59],a[92:95],v[192:195],v[56:59]\nv_exp_f32 v177,v177\nM v[56:59],a[124:127],v[208:211],v[56:59]\nds_read_b128 a[24:27],v250 offset:6144\n" \
"ds_read_b128 a[28:31],v250 offset:7168\nM a[192:195],a[208:211],v[192:195],a[192:195]\nv_exp_f32 v178,v178\nM a[192:195],a[208:211],v[208:211],a[192:195]\nv_exp_f32 v179,v179\nM v[4:7],a[64:67],v[196:199],v[4:7]\nv_exp_f32 v132,v132\nM v[4:7],a[96:99],v[212:215],v[4:7]\n" \
"ds_read_b128 a[32:35],v250 offset:8192\nds_read_b128 a[36:39],v250 offset:9216\nM v[12:15],a[68:71],v[196:199],v[12:15]\nv_exp_f32 v133,v133\nM v[12:15],a[100:103],v[212:215],v[12:15]\nbuffer_load_dwordx4 v240,s[48:51],s95 offen offset:2048 sc1 lds\nM v[20:23],a[72:75],v[196:199],v[20:23]\nv_exp_f32 v134,v134\n" \
"M v[20:23],a[104:107],v[212:215],v[20:23]\nds_read_b128 a[40:43],v250 offset:10240\nds_read_b128 a[44:47],v250 offset:11264\nM v[28:31],a[76:79],v[196:199],v[28:31]\nv_exp_f32 v135,v135\nM v[28:31],a[108:111],v[212:215],v[28:31]\nv_exp_f32 v148,v148\nM v[36:39],a[80:83],v[196:199],v[36:39]\n" \
"v_exp_f32 v149,v149\nM v[36:39],a[112:115],v[212:215],v[36:39]\nds_read_b128 a[48:51],v250 offset:12288\nds_read_b128 a[52:55],v250 offset:13312\nM v[44:47],a[84:87],v[196:199],v[44:47]\nv_exp_f32 v150,v150\nM v[44:47],a[116:119],v[212:215],v[44:47]\nbuffer_load_dwordx4 v240,s[48:51],s95 offen offset:3072 sc1 lds\n" \
"M v[52:55],a[88:91],v[196:199],v[52:55]\nv_exp_f32 v151,v151\nM v[52:55],a[120:123],v[212:215],v[52:55]\nds_read_b128 a[56:59],v250 offset:14336\nds_read_b128 a[60:63],v250 offset:15360\nM v[60:63],a[92:95],v[196:199],v[60:63]\nv_cvt_pk_bf16_f32 v192,v128,v129\nv_cvt_pk_bf16_f32 v193,v130,v131\n" \
"M v[60:63],a[124:127],v[212:215],v[60:63]\nv_cvt_pk_bf16_f32 v194,v144,v145\nv_cvt_pk_bf16_f32 v195,v146,v147\nM a[196:199],a[208:211],v[196:199],a[196:199]\nv_cvt_pk_bf16_f32 v208,v160,v161\nv_cvt_pk_bf16_f32 v209,v162,v163\nv_cvt_pk_bf16_f32 v210,v176,v177\nv_cvt_pk_bf16_f32 v211,v178,v179\n" \
"v_xor_b32 v250,0x1c000,v250\ns_add_u32 s95,s95,0x4000\nM a[196:199],a[208:211],v[212:215],a[196:199]\ns_cmp_lg_u32 s95,s15\ns_cbranch_scc1 .Lsingle\ns_and_b32 s94,m0,0x8000\ns_or_b32 s94,s94,0x4000\n.Lsx:\n" \
".Lsw:\ns_waitcnt lgkmcnt(4)\nM v[128:131],a[0:3],a[128:131],v[224:227]\ns_lshl4_add_u32 s69,s12,s68\ns_add_u32 m0,s64,131072\nM v[128:131],a[4:7],a[132:135],v[128:131]\nds_read_b128 a[64:67],v250 offset:0\nds_read_b128 a[96:99],v250 offset:8192\n" \
"v_cvt_pk_bf16_f32 v196,v132,v133\nM v[128:131],a[8:11],a[136:139],v[128:131]\nbuffer_load_dwordx4 v243,s[40:43],s69 offen offset:0 lds\nM v[128:131],a[12:15],a[140:143],v[128:131]\nv_cvt_pk_bf16_f32 v197,v134,v135\nv_cvt_pk_bf16_f32 v198,v148,v149\nv_cvt_pk_bf16_f32 v199,v150,v151\nM v[132:135],a[12:15],a[156:159],v[228:231]\n" \
"ds_read_b128 a[68:71],v250 offset:1024\nds_read_b128 a[100:103],v250 offset:9216\nM v[132:135],a[8:11],a[152:155],v[132:135]\nbuffer_load_dwordx4 v244,s[40:43],s69 offen offset:1024 lds\ns_add_u32 s94,s94,0x4000\nM v[132:135],a[4:7],a[148:151],v[132:135]\nv_exp_f32 v164,v164\nM v[132:135],a[0:3],a[144:147],v[132:135]\n" \
"ds_read_b128 a[72:75],v250 offset:2048\nds_read_b128 a[104:107],v250 offset:10240\ns_and_b32 s94,s94,0xffff\nM v[144:147],a[16:19],a[128:131],v[224:227]\nbuffer_load_dwordx4 v245,s[40:43],s69 offen offset:2048 lds\nM v[144:147],a[20:23],a[132:135],v[144:147]\nv_exp_f32 v165,v165\ns_add_u32 s95,s95,0x4000\n" \
"M v[144:147],a[24:27],a[136:139],v[144:147]\nds_read_b128 a[76:79],v250 offset:3072\nds_read_b128 a[108:111],v250 offset:11264\nM v[144:147],a[28:31],a[140:143],v[144:147]\nbuffer_load_dwordx4 v246,s[40:43],s69 offen offset:3072 lds\ns_lshl4_add_u32 s69,s12,s68\nM v[148:151],a[28:31],a[156:159],v[228:231]\ns_add_u32 s69,s69,0x10000\n" \
"s_add_u32 m0,s64,147456\nM v[148:151],a[24:27],a[152:155],v[148:151]\nds_read_b128 a[80:83],v250 offset:4096\nds_read_b128 a[112:115],v250 offset:12288\nv_cvt_pk_bf16_f32 v212,v164,v165\nM v[148:151],a[20:23],a[148:151],v[148:151]\nv_exp_f32 v166,v166\nM v[148:151],a[16:19],a[144:147],v[148:151]\n" \
"v_exp_f32 v167,v167\nM v[160:163],a[32:35],a[128:131],v[224:227]\nds_read_b128 a[84:87],v250 offset:5120\nds_read_b128 a[116:119],v250 offset:13312\nv_cvt_pk_bf16_f32 v213,v166,v167\nM v[160:163],a[36:39],a[132:135],v[160:163]\nbuffer_load_dwordx4 v243,s[40:43],s69 offen offset:0 lds\nM v[160:163],a[40:43],a[136:139],v[160:163]\n" \
"v_exp_f32 v180,v180\nM v[160:163],a[44:47],a[140:143],v[160:163]\nds_read_b128 a[88:91],v250 offset:6144\nds_read_b128 a[120:123],v250 offset:14336\nM v[164:167],a[44:47],a[156:159],v[228:231]\nv_exp_f32 v181,v181\nM v[164:167],a[40:43],a[152:155],v[164:167]\nbuffer_load_dwordx4 v244,s[40:43],s69 offen offset:1024 lds\n" \
"M v[164:167],a[36:39],a[148:151],v[164:167]\nds_read_b128 a[92:95],v250 offset:7168\nds_read_b128 a[124:127],v250 offset:15360\nv_cvt_pk_bf16_f32 v214,v180,v181\nM v[164:167],a[32:35],a[144:147],v[164:167]\nv_xor_b32 v250,0x18000,v250\ns_waitcnt lgkmcnt(6)\nM v[176:179],a[48:51],a[128:131],v[224:227]\n" \
"s_waitcnt vmcnt(6)\nbuffer_store_dword v233,off,s[52:55],s77 sc1\nM v[176:179],a[52:55],a[132:135],v[176:179]\nbuffer_load_dwordx4 v245,s[40:43],s69 offen offset:2048 lds\nM v[176:179],a[56:59],a[136:139],v[176:179]\nv_exp_f32 v182,v182\nv_exp_f32 v183,v183\nM v[176:179],a[60:63],a[140:143],v[176:179]\n" \
"v_cvt_pk_bf16_f32 v215,v182,v183\nM v[180:183],a[60:63],a[156:159],v[228:231]\nv_exp_f32 v128,v128\nM v[180:183],a[56:59],a[152:155],v[180:183]\nbuffer_load_dwordx4 v246,s[40:43],s69 offen offset:3072 lds\nM v[180:183],a[52:55],a[148:151],v[180:183]\nv_exp_f32 v129,v129\nM v[180:183],a[48:51],a[144:147],v[180:183]\n" \
"s_lshl4_add_u32 s69,s10,s68\nv_exp_f32 v130,v130\nM v[0:3],a[64:67],v[192:195],v[0:3]\nv_exp_f32 v131,v131\nM v[0:3],a[96:99],v[208:211],v[0:3]\ns_add_u32 m0,s64,0\nv_exp_f32 v144,v144\nM v[8:11],a[68:71],v[192:195],v[8:11]\n" \
"s_waitcnt vmcnt(16) lgkmcnt(0)\ns_barrier\nM v[8:11],a[100:103],v[208:211],v[8:11]\nds_read_b128 a[0:3],v250 offset:0\nds_read_b128 a[4:7],v250 offset:1024\nM v[16:19],a[72:75],v[192:195],v[16:19]\nv_exp_f32 v145,v145\nM v[16:19],a[104:107],v[208:211],v[16:19]\n" \
"buffer_load_dwordx4 v243,s[40:43],s69 offen offset:0 lds\nM v[24:27],a[76:79],v[192:195],v[24:27]\nv_exp_f32 v146,v146\nM v[24:27],a[108:111],v[208:211],v[24:27]\nds_read_b128 a[8:11],v250 offset:2048\nds_read_b128 a[12:15],v250 offset:3072\nM v[32:35],a[80:83],v[192:195],v[32:35]\nv_exp_f32 v147,v147\n" \
"M v[32:35],a[112:115],v[208:211],v[32:35]\nbuffer_load_dwordx4 v244,s[40:43],s69 offen offset:1024 lds\nM v[40:43],a[84:87],v[192:195],v[40:43]\nv_exp_f32 v160,v160\nM v[40:43],a[116:119],v[208:211],v[40:43]\nds_read_b128 a[16:19],v250 offset:4096\nds_read_b128 a[20:23],v250 offset:5120\nM v[48:51],a[88:91],v[192:195],v[48:51]\n" \
"v_exp_f32 v161,v161\nM v[48:51],a[120:123],v[208:211],v[48:51]\nbuffer_load_dwordx4 v245,s[40:43],s69 offen offset:2048 lds\nM v[56:59],a[92:95],v[192:195],v[56:59]\nv_exp_f32 v162,v162\nM v[56:59],a[124:127],v[208:211],v[56:59]\nds_read_b128 a[24:27],v250 offset:6144\nds_read_b128 a[28:31],v250 offset:7168\n" \
"M a[192:195],a[208:211],v[192:195],a[192:195]\nv_exp_f32 v163,v163\nv_cvt_pk_bf16_f32 v192,v128,v129\nM a[192:195],a[208:211],v[208:211],a[192:195]\nbuffer_load_dwordx4 v246,s[40:43],s69 offen offset:3072 lds\nM v[4:7],a[64:67],v[196:199],v[4:7]\ns_lshl4_add_u32 s69,s10,s68\ns_add_u32 s69,s69,0x10000\n" \
"s_add_u32 m0,s64,32768\nM v[4:7],a[96:99],v[212:215],v[4:7]\nds_read_b128 a[32:35],v250 offset:8192\nds_read_b128 a[36:39],v250 offset:9216\nM v[12:15],a[68:71],v[196:199],v[12:15]\nv_exp_f32 v176,v176\nv_cvt_pk_bf16_f32 v193,v130,v131\nM v[12:15],a[100:103],v[212:215],v[12:15]\n" \
"buffer_load_dwordx4 v243,s[40:43],s69 offen offset:0 lds\nM v[20:23],a[72:75],v[196:199],v[20:23]\nv_exp_f32 v177,v177\nv_cvt_pk_bf16_f32 v194,v144,v145\nM v[20:23],a[104:107],v[212:215],v[20:23]\nds_read_b128 a[40:43],v250 offset:10240\nds_read_b128 a[44:47],v250 offset:11264\nM v[28:31],a[76:79],v[196:199],v[28:31]\n" \
"v_exp_f32 v178,v178\nv_cvt_pk_bf16_f32 v195,v146,v147\nM v[28:31],a[108:111],v[212:215],v[28:31]\nbuffer_load_dwordx4 v244,s[40:43],s69 offen offset:1024 lds\nM v[36:39],a[80:83],v[196:199],v[36:39]\nv_exp_f32 v179,v179\nv_cvt_pk_bf16_f32 v208,v160,v161\nM v[36:39],a[112:115],v[212:215],v[36:39]\n" \
"ds_read_b128 a[48:51],v250 offset:12288\nds_read_b128 a[52:55],v250 offset:13312\nM v[44:47],a[84:87],v[196:199],v[44:47]\nv_exp_f32 v132,v132\nv_cvt_pk_bf16_f32 v209,v162,v163\nM v[44:47],a[116:119],v[212:215],v[44:47]\nbuffer_load_dwordx4 v245,s[40:43],s69 offen offset:2048 lds\nv_cvt_pk_bf16_f32 v210,v176,v177\n" \
"M v[52:55],a[88:91],v[196:199],v[52:55]\nv_exp_f32 v133,v133\nM v[52:55],a[120:123],v[212:215],v[52:55]\nds_read_b128 a[56:59],v250 offset:14336\nds_read_b128 a[60:63],v250 offset:15360\nv_cvt_pk_bf16_f32 v211,v178,v179\nM v[60:63],a[92:95],v[196:199],v[60:63]\nv_exp_f32 v134,v134\n" \
"M v[60:63],a[124:127],v[212:215],v[60:63]\nbuffer_load_dwordx4 v246,s[40:43],s69 offen offset:3072 lds\nv_add_u32 v250,s94,v241\nM a[196:199],a[208:211],v[196:199],a[196:199]\nv_exp_f32 v135,v135\nM a[196:199],a[208:211],v[212:215],a[196:199]\nv_exp_f32 v148,v148\nv_exp_f32 v149,v149\n" \
"v_exp_f32 v150,v150\nv_exp_f32 v151,v151\n.Lod:\ns_waitcnt lgkmcnt(2)\nM v[128:131],a[0:3],a[128:131],v[224:227]\nv_cvt_pk_bf16_f32 v196,v132,v133\nv_cvt_pk_bf16_f32 v197,v134,v135\nM v[128:131],a[4:7],a[132:135],v[128:131]\n" \
"ds_read_b128 a[64:67],v250 offset:0\nds_read_b128 a[96:99],v250 offset:8192\nM v[128:131],a[8:11],a[136:139],v[128:131]\ns_lshl2_add_u32 s39,s62,s7\ns_lshl_b32 s39,s39,5\nM v[128:131],a[12:15],a[140:143],v[128:131]\ns_add_u32 s39,s39,s63\nv_cvt_pk_bf16_f32 v198,v148,v149\n" \
"v_cvt_pk_bf16_f32 v199,v150,v151\nM v[132:135],a[12:15],a[156:159],v[228:231]\nds_read_b128 a[68:71],v250 offset:1024\nds_read_b128 a[100:103],v250 offset:9216\nM v[132:135],a[8:11],a[152:155],v[132:135]\ns_lshl2_add_u32 s39,s39,73728\nv_and_b32 v251,3,v249\nM v[132:135],a[4:7],a[148:151],v[132:135]\n" \
"s_waitcnt vmcnt(16)\nbuffer_store_dword v252,off,s[52:55],s39 sc1\nM v[132:135],a[0:3],a[144:147],v[132:135]\nds_read_b128 a[72:75],v250 offset:2048\nds_read_b128 a[104:107],v250 offset:10240\nv_lshlrev_b32 v251,2,v251\nM v[144:147],a[16:19],a[128:131],v[224:227]\nv_exp_f32 v164,v164\n" \
"M v[144:147],a[20:23],a[132:135],v[144:147]\nv_exp_f32 v165,v165\nM v[144:147],a[24:27],a[136:139],v[144:147]\nds_read_b128 a[76:79],v250 offset:3072\nds_read_b128 a[108:111],v250 offset:11264\nM v[144:147],a[28:31],a[140:143],v[144:147]\nv_exp_f32 v166,v166\nv_cvt_pk_bf16_f32 v212,v164,v165\n" \
"M v[148:151],a[28:31],a[156:159],v[228:231]\nv_exp_f32 v167,v167\nM v[148:151],a[24:27],a[152:155],v[148:151]\nds_read_b128 a[80:83],v250 offset:4096\nds_read_b128 a[112:115],v250 offset:12288\nM v[148:151],a[20:23],a[148:151],v[148:151]\nv_cvt_pk_bf16_f32 v213,v166,v167\ns_add_u32 s80,s79,4096\n" \
"M v[148:151],a[16:19],a[144:147],v[148:151]\nv_exp_f32 v180,v180\ns_add_u32 s94,s94,0x4000\nM v[160:163],a[32:35],a[128:131],v[224:227]\nds_read_b128 a[84:87],v250 offset:5120\nds_read_b128 a[116:119],v250 offset:13312\nM v[160:163],a[36:39],a[132:135],v[160:163]\nv_exp_f32 v181,v181\n" \
"M v[160:163],a[40:43],a[136:139],v[160:163]\nv_exp_f32 v182,v182\nM v[160:163],a[44:47],a[140:143],v[160:163]\nds_read_b128 a[88:91],v250 offset:6144\nds_read_b128 a[120:123],v250 offset:14336\nv_cvt_pk_bf16_f32 v214,v180,v181\nM v[164:167],a[44:47],a[156:159],v[228:231]\nbuffer_load_dword v248,v251,s[52:55],s80 offen sc1\n" \
"M v[164:167],a[40:43],a[152:155],v[164:167]\nv_exp_f32 v183,v183\nM v[164:167],a[36:39],a[148:151],v[164:167]\nds_read_b128 a[92:95],v250 offset:7168\nds_read_b128 a[124:127],v250 offset:15360\nM v[164:167],a[32:35],a[144:147],v[164:167]\nv_cvt_pk_bf16_f32 v215,v182,v183\ns_and_b32 s94,s94,0xffff\n" \
"v_add_u32 v250,s94,v241\nM v[176:179],a[48:51],a[128:131],v[224:227]\nv_exp_f32 v128,v128\nM v[176:179],a[52:55],a[132:135],v[176:179]\ns_add_u32 s65,s10,s67\ns_waitcnt lgkmcnt(4)\nM v[176:179],a[56:59],a[136:139],v[176:179]\nv_exp_f32 v129,v129\n" \
"M v[176:179],a[60:63],a[140:143],v[176:179]\nv_exp_f32 v130,v130\ns_add_u32 s65,s65,1048576\nM v[180:183],a[60:63],a[156:159],v[228:231]\nv_exp_f32 v131,v131\nM v[180:183],a[56:59],a[152:155],v[180:183]\nv_exp_f32 v144,v144\nM v[180:183],a[52:55],a[148:151],v[180:183]\n" \
"v_exp_f32 v145,v145\nM v[180:183],a[48:51],a[144:147],v[180:183]\nv_exp_f32 v146,v146\nv_add_u32 v226,131072,v242\nM v[0:3],a[64:67],v[192:195],v[0:3]\nv_exp_f32 v147,v147\nM v[0:3],a[96:99],v[208:211],v[0:3]\nv_exp_f32 v160,v160\n" \
"M v[8:11],a[68:71],v[192:195],v[8:11]\ns_waitcnt lgkmcnt(0)\ns_barrier\nM v[8:11],a[100:103],v[208:211],v[8:11]\ns_lshl4_add_u32 s69,s11,s68\ns_add_u32 m0,s64,16384\nv_xor_b32 v227,32,v226\nM v[16:19],a[72:75],v[192:195],v[16:19]\n" \
"v_exp_f32 v161,v161\nM v[16:19],a[104:107],v[208:211],v[16:19]\nv_exp_f32 v162,v162\nM v[24:27],a[76:79],v[192:195],v[24:27]\nv_exp_f32 v163,v163\nM v[24:27],a[108:111],v[208:211],v[24:27]\nv_exp_f32 v176,v176\ns_add_u32 s37,s12,s67\n" \
"M v[32:35],a[80:83],v[192:195],v[32:35]\nbuffer_load_dwordx4 v243,s[40:43],s69 offen offset:0 lds\nM v[32:35],a[112:115],v[208:211],v[32:35]\nv_exp_f32 v177,v177\nM v[40:43],a[84:87],v[192:195],v[40:43]\nv_exp_f32 v178,v178\nM v[40:43],a[116:119],v[208:211],v[40:43]\nv_exp_f32 v179,v179\n" \
"s_add_u32 s38,s37,4096\nM v[48:51],a[88:91],v[192:195],v[48:51]\nv_exp_f32 v132,v132\nM v[48:51],a[120:123],v[208:211],v[48:51]\nv_exp_f32 v133,v133\nM v[56:59],a[92:95],v[192:195],v[56:59]\nv_exp_f32 v134,v134\nM v[56:59],a[124:127],v[208:211],v[56:59]\n" \
"v_exp_f32 v135,v135\ns_lshl2_add_u32 s74,s63,s7\nM a[192:195],a[208:211],v[192:195],a[192:195]\nbuffer_load_dwordx4 v244,s[40:43],s69 offen offset:1024 lds\nM a[192:195],a[208:211],v[208:211],a[192:195]\nv_cvt_pk_bf16_f32 v192,v128,v129\nv_cvt_pk_bf16_f32 v193,v130,v131\nM v[4:7],a[64:67],v[196:199],v[4:7]\n" \
"ds_read_b128 a[64:67],v250 offset:0\nv_cvt_pk_bf16_f32 v194,v144,v145\nM v[4:7],a[96:99],v[212:215],v[4:7]\nds_read_b128 a[96:99],v250 offset:8192\nv_cvt_pk_bf16_f32 v195,v146,v147\nv_cvt_pk_bf16_f32 v208,v160,v161\nM v[12:15],a[68:71],v[196:199],v[12:15]\nds_read_b128 a[68:71],v250 offset:1024\n" \
"v_cvt_pk_bf16_f32 v209,v162,v163\nM v[12:15],a[100:103],v[212:215],v[12:15]\nds_read_b128 a[100:103],v250 offset:9216\nv_cvt_pk_bf16_f32 v210,v176,v177\nM v[20:23],a[72:75],v[196:199],v[20:23]\nds_read_b128 a[72:75],v250 offset:2048\nv_cvt_pk_bf16_f32 v211,v178,v179\nM v[20:23],a[104:107],v[212:215],v[20:23]\n" \
"ds_read_b128 a[104:107],v250 offset:10240\nv_exp_f32 v148,v148\nM v[28:31],a[76:79],v[196:199],v[28:31]\nbuffer_load_dwordx4 v245,s[40:43],s69 offen offset:2048 lds\nM v[28:31],a[108:111],v[212:215],v[28:31]\nds_read_b128 a[76:79],v250 offset:3072\nds_read_b128 a[108:111],v250 offset:11264\nM v[36:39],a[80:83],v[196:199],v[36:39]\n" \
"ds_read_b128 a[80:83],v250 offset:4096\ns_lshl_b32 s72,s62,7\nM v[36:39],a[112:115],v[212:215],v[36:39]\nds_read_b128 a[112:115],v250 offset:12288\nv_exp_f32 v149,v149\nM v[44:47],a[84:87],v[196:199],v[44:47]\nds_read_b128 a[84:87],v250 offset:5120\ns_add_u32 s95,s74,s72\n" \
"M v[44:47],a[116:119],v[212:215],v[44:47]\nds_read_b128 a[116:119],v250 offset:13312\ns_lshl2_add_u32 s76,s61,s79\nM v[52:55],a[88:91],v[196:199],v[52:55]\nds_read_b128 a[88:91],v250 offset:6144\ns_sub_u32 s74,s74,s72\nM v[52:55],a[120:123],v[212:215],v[52:55]\nds_read_b128 a[120:123],v250 offset:14336\n" \
"v_exp_f32 v150,v150\nM v[60:63],a[92:95],v[196:199],v[60:63]\nbuffer_load_dwordx4 v246,s[40:43],s69 offen offset:3072 lds\nM v[60:63],a[124:127],v[212:215],v[60:63]\ns_waitcnt vmcnt(4)\nv_and_b32 v248,s17,v248\nM a[196:199],a[208:211],v[196:199],a[196:199]\nv_cmp_eq_u32 vcc,0,v248\n" \
"ds_read_b128 a[92:95],v250 offset:7168\nM a[196:199],a[208:211],v[212:215],a[196:199]\ns_cbranch_vccnz .Lps6\n.Lpb6:\ns_lshl4_add_u32 s69,s11,s68\ns_waitcnt lgkmcnt(4)\nM v[0:3],a[64:67],v[192:195],v[0:3]\nbuffer_load_dwordx4 v[72:75],v240,s[52:55],s65 offen offset:0 sc1\n" \
"M v[0:3],a[96:99],v[208:211],v[0:3]\ns_add_u32 s69,s69,0x10000\ns_add_u32 m0,s64,49152\nM v[8:11],a[68:71],v[192:195],v[8:11]\nds_read_b128 a[124:127],v250 offset:15360\nv_cvt_pk_bf16_f32 v196,v132,v133\nM v[8:11],a[100:103],v[208:211],v[8:11]\nv_exp_f32 v151,v151\n" \
"v_cvt_pk_bf16_f32 v197,v134,v135\nM v[16:19],a[72:75],v[192:195],v[16:19]\nbuffer_load_dwordx4 v243,s[40:43],s69 offen offset:0 lds\nM v[16:19],a[104:107],v[208:211],v[16:19]\nv_exp_f32 v164,v164\nM v[24:27],a[76:79],v[192:195],v[24:27]\nv_exp_f32 v165,v165\nM v[24:27],a[108:111],v[208:211],v[24:27]\n" \
"v_exp_f32 v166,v166\nv_cvt_pk_bf16_f32 v198,v148,v149\nM v[32:35],a[80:83],v[192:195],v[32:35]\nbuffer_load_dwordx4 v[76:79],v240,s[52:55],s65 offen offset:1024 sc1\nM v[32:35],a[112:115],v[208:211],v[32:35]\nv_exp_f32 v167,v167\nM v[40:43],a[84:87],v[192:195],v[40:43]\nv_cvt_pk_bf16_f32 v199,v150,v151\n" \
"s_waitcnt lgkmcnt(0)\nM v[40:43],a[116:119],v[208:211],v[40:43]\nv_exp_f32 v180,v180\nv_cvt_pk_bf16_f32 v212,v164,v165\nM v[48:51],a[88:91],v[192:195],v[48:51]\nbuffer_load_dwordx4 v244,s[40:43],s69 offen offset:1024 lds\nM v[48:51],a[120:123],v[208:211],v[48:51]\nv_exp_f32 v181,v181\n" \
"M v[56:59],a[92:95],v[192:195],v[56:59]\nv_exp_f32 v182,v182\nM v[56:59],a[124:127],v[208:211],v[56:59]\nv_exp_f32 v183,v183\nv_cvt_pk_bf16_f32 v213,v166,v167\nM a[192:195],a[208:211],v[192:195],a[192:195]\nbuffer_load_dwordx4 v[88:91],v240,s[52:55],s65 offen offset:2048 sc1\nM a[192:195],a[208:211],v[208:211],a[192:195]\n" \
"v_cvt_pk_bf16_f32 v214,v180,v181\nv_cvt_pk_bf16_f32 v215,v182,v183\nM v[4:7],a[64:67],v[196:199],v[4:7]\ns_barrier\nds_read_b64_tr_b16 v[200:201],v226 offset:0\nM v[4:7],a[96:99],v[212:215],v[4:7]\nds_read_b64_tr_b16 v[202:203],v226 offset:4096\nds_read_b64_tr_b16 v[204:205],v226 offset:8192\n" \
"ds_read_b64_tr_b16 v[206:207],v226 offset:12288\nM v[12:15],a[68:71],v[196:199],v[12:15]\nbuffer_load_dwordx4 v245,s[40:43],s69 offen offset:2048 lds\nM v[12:15],a[100:103],v[212:215],v[12:15]\nds_read_b64_tr_b16 v[216:217],v227 offset:0\nds_read_b64_tr_b16 v[218:219],v227 offset:4096\nM v[20:23],a[72:75],v[196:199],v[20:23]\nds_read_b64_tr_b16 v[220:221],v227 offset:8192\n" \
"ds_read_b64_tr_b16 v[222:223],v227 offset:12288\nM v[20:23],a[104:107],v[212:215],v[20:23]\nds_read_b64_tr_b16 v[192:193],v226 offset:512\nds_read_b64_tr_b16 v[194:195],v226 offset:4608\nds_read_b64_tr_b16 v[208:209],v226 offset:8704\nM v[28:31],a[76:79],v[196:199],v[28:31]\nbuffer_load_dwordx4 v[92:95],v240,s[52:55],s65 offen offset:3072 sc1\nM v[28:31],a[108:111],v[212:215],v[28:31]\n" \
"s_add_u32 s65,s65,4096\nds_read_b64_tr_b16 v[210:211],v226 offset:12800\nM v[36:39],a[80:83],v[196:199],v[36:39]\nv_accvgpr_read_b32 v224,a192\nv_cmp_eq_u32 vcc,2,v248\nM v[36:39],a[112:115],v[212:215],v[36:39]\ns_cmp_lg_u64 vcc,0\ns_addc_u32 s99,s99,0\n" \
"v_xor_b32 v250,0x8000,v250\nM v[44:47],a[84:87],v[196:199],v[44:47]\nbuffer_load_dwordx4 v246,s[40:43],s69 offen offset:3072 lds\nM v[44:47],a[116:119],v[212:215],v[44:47]\nv_add_u32 v229,s64,v250\ns_lshl_b32 s72,s7,9\nM v[52:55],a[88:91],v[196:199],v[52:55]\nv_lshl_add_u32 v239,v249,3,s72\n" \
"s_lshr_b32 s72,s64,1\nM v[52:55],a[120:123],v[212:215],v[52:55]\nv_and_b32 v254,32,v249\ns_lshr_b32 s70,s60,1\nv_lshlrev_b32 v254,3,v254\nM v[60:63],a[92:95],v[196:199],v[60:63]\nbuffer_load_dwordx4 v[104:107],v240,s[52:55],s65 offen offset:0 sc1\nM v[60:63],a[124:127],v[212:215],v[60:63]\n" \
"s_lshl_b32 s70,s70,7\nv_lshl_add_u32 v254,v249,2,v254\nM a[196:199],a[208:211],v[196:199],a[196:199]\nds_read_b64_tr_b16 v[196:197],v227 offset:512\nds_read_b64_tr_b16 v[198:199],v227 offset:4608\nM a[196:199],a[208:211],v[212:215],a[196:199]\nds_read_b64_tr_b16 v[212:213],v227 offset:8704\nds_read_b64_tr_b16 v[214:215],v227 offset:12800\n" \
"s_waitcnt lgkmcnt(4)\nM v[128:131],v[64:67],v[200:203],0\ns_lshl_b32 s80,s62,9\nv_xor_b32 v254,s70,v254\ns_add_u32 s80,s80,73728\nv_and_b32 v236,15,v249\nM v[128:131],v[80:83],v[204:207],v[128:131]\nv_accvgpr_read_b32 v228,a196\n" \
"v_add_f32 v225,v224,v228\nv_rcp_f32 v224,v224\nv_lshrrev_b32 v235,4,v249\nM v[132:135],v[68:71],v[200:203],0\nv_mul_f32 v0,v0,v224\nv_mul_f32 v1,v1,v224\nv_mul_f32 v2,v2,v224\nv_mul_f32 v3,v3,v224\n" \
"M v[132:135],v[84:87],v[204:207],v[132:135]\nv_cvt_pk_bf16_f32 v0,v0,v1\nv_mul_f32 v8,v8,v224\nv_mul_f32 v9,v9,v224\nv_mul_f32 v10,v10,v224\nv_mul_f32 v11,v11,v224\nM v[136:139],v[64:67],v[216:219],0\nbuffer_load_dwordx4 v[108:111],v240,s[52:55],s65 offen offset:1024 sc1\n" \
"v_cvt_pk_bf16_f32 v1,v2,v3\nds_read_b64_tr_b16 v[200:201],v226 offset:1024\nM v[136:139],v[80:83],v[220:223],v[136:139]\nds_read_b64_tr_b16 v[202:203],v226 offset:5120\nv_cvt_pk_bf16_f32 v2,v8,v9\nv_cvt_pk_bf16_f32 v3,v10,v11\nds_read_b64_tr_b16 v[204:205],v226 offset:9216\nds_read_b64_tr_b16 v[206:207],v226 offset:13312\n" \
"M v[140:143],v[68:71],v[216:219],0\nbuffer_store_dwordx4 v[0:3],v240,s[52:55],s37 offen offset:0 sc1\nv_rcp_f32 v228,v228\nM v[140:143],v[84:87],v[220:223],v[140:143]\nv_mul_f32 v4,v4,v228\nv_mul_f32 v5,v5,v228\nds_read_b64_tr_b16 v[216:217],v227 offset:1024\nds_read_b64_tr_b16 v[218:219],v227 offset:5120\n" \
"v_mul_f32 v6,v6,v228\nM v[144:147],v[64:67],v[192:195],0\nv_mul_f32 v7,v7,v228\nv_cvt_pk_bf16_f32 v4,v4,v5\nv_mul_f32 v12,v12,v228\nv_mul_f32 v13,v13,v228\nM v[144:147],v[80:83],v[208:211],v[144:147]\nv_mul_f32 v14,v14,v228\n" \
"v_mul_f32 v15,v15,v228\nv_cvt_pk_bf16_f32 v5,v6,v7\nds_read_b64_tr_b16 v[220:221],v227 offset:9216\nds_read_b64_tr_b16 v[222:223],v227 offset:13312\nM v[148:151],v[68:71],v[192:195],0\nv_cvt_pk_bf16_f32 v6,v12,v13\nv_cvt_pk_bf16_f32 v7,v14,v15\nds_read_b64_tr_b16 v[192:193],v226 offset:1536\n" \
"ds_read_b64_tr_b16 v[194:195],v226 offset:5632\nM v[148:151],v[84:87],v[208:211],v[148:151]\nbuffer_store_dwordx4 v[4:7],v240,s[52:55],s37 offen offset:1024 sc1\nv_mul_f32 v16,v16,v224\nv_mul_f32 v17,v17,v224\ns_waitcnt lgkmcnt(6)\nM v[152:155],v[64:67],v[196:199],0\nbuffer_load_dwordx4 v[120:123],v240,s[52:55],s65 offen offset:2048 sc1\n" \
"ds_read_b64_tr_b16 v[208:209],v226 offset:9728\nds_read_b64_tr_b16 v[210:211],v226 offset:13824\nM v[152:155],v[80:83],v[212:215],v[152:155]\nv_mul_f32 v18,v18,v224\nv_mul_f32 v19,v19,v224\nv_cvt_pk_bf16_f32 v16,v16,v17\nv_mul_f32 v24,v24,v224\nv_mul_f32 v25,v25,v224\n" \
"M v[156:159],v[68:71],v[196:199],0\nv_mul_f32 v26,v26,v224\nv_mul_f32 v27,v27,v224\nv_cvt_pk_bf16_f32 v17,v18,v19\nv_cvt_pk_bf16_f32 v18,v24,v25\nM v[156:159],v[84:87],v[212:215],v[156:159]\nv_cvt_pk_bf16_f32 v19,v26,v27\nds_read_b64_tr_b16 v[196:197],v227 offset:1536\n" \
"ds_read_b64_tr_b16 v[198:199],v227 offset:5632\nds_read_b64_tr_b16 v[212:213],v227 offset:9728\nv_mul_f32 v20,v20,v228\nM v[160:163],v[64:67],v[200:203],0\nbuffer_store_dwordx4 v[16:19],v240,s[52:55],s37 offen offset:2048 sc1\nds_read_b64_tr_b16 v[214:215],v227 offset:13824\nv_mul_f32 v21,v21,v228\nM v[160:163],v[80:83],v[204:207],v[160:163]\n" \
"v_mul_f32 v22,v22,v228\nv_mul_f32 v23,v23,v228\nv_cvt_pk_bf16_f32 v20,v20,v21\nv_mul_f32 v28,v28,v228\nv_mul_f32 v29,v29,v228\nM v[164:167],v[68:71],v[200:203],0\nv_mul_f32 v30,v30,v228\nv_mul_f32 v31,v31,v228\n" \
"v_cvt_pk_bf16_f32 v21,v22,v23\nv_cvt_pk_bf16_f32 v22,v28,v29\nM v[164:167],v[84:87],v[204:207],v[164:167]\nv_cvt_pk_bf16_f32 v23,v30,v31\nds_read_b64_tr_b16 v[200:201],v226 offset:16384\nds_read_b64_tr_b16 v[202:203],v226 offset:20480\nds_read_b64_tr_b16 v[204:205],v226 offset:24576\ns_waitcnt lgkmcnt(6)\n" \
"M v[168:171],v[64:67],v[216:219],0\nbuffer_load_dwordx4 v[124:127],v240,s[52:55],s65 offen offset:3072 sc1\nds_read_b64_tr_b16 v[206:207],v226 offset:28672\nv_mul_f32 v32,v32,v224\nM v[168:171],v[80:83],v[220:223],v[168:171]\nbuffer_store_dwordx4 v[20:23],v240,s[52:55],s37 offen offset:3072 sc1\nv_mul_f32 v33,v33,v224\nv_mul_f32 v34,v34,v224\n" \
"v_mul_f32 v35,v35,v224\nM v[172:175],v[68:71],v[216:219],0\nv_cvt_pk_bf16_f32 v32,v32,v33\nv_mul_f32 v40,v40,v224\nv_mul_f32 v41,v41,v224\nv_mul_f32 v42,v42,v224\nM v[172:175],v[84:87],v[220:223],v[172:175]\nv_mul_f32 v43,v43,v224\n" \
"v_cvt_pk_bf16_f32 v33,v34,v35\nds_read_b64_tr_b16 v[216:217],v227 offset:16384\nds_read_b64_tr_b16 v[218:219],v227 offset:20480\nv_cvt_pk_bf16_f32 v34,v40,v41\nM v[176:179],v[64:67],v[192:195],0\nv_cvt_pk_bf16_f32 v35,v42,v43\nds_read_b64_tr_b16 v[220:221],v227 offset:24576\nds_read_b64_tr_b16 v[222:223],v227 offset:28672\n" \
"s_add_u32 s65,s11,s67\nM v[176:179],v[80:83],v[208:211],v[176:179]\ns_add_u32 s65,s65,1048576\nv_mul_f32 v36,v36,v228\nv_mul_f32 v37,v37,v228\nv_mul_f32 v38,v38,v228\nv_mul_f32 v39,v39,v228\nM v[180:183],v[68:71],v[192:195],0\n" \
"buffer_store_dwordx4 v[32:35],v240,s[52:55],s38 offen offset:0 sc1\nds_read_b64_tr_b16 v[192:193],v226 offset:16896\nds_read_b64_tr_b16 v[194:195],v226 offset:20992\nM v[180:183],v[84:87],v[208:211],v[180:183]\nv_cvt_pk_bf16_f32 v36,v36,v37\nv_mul_f32 v44,v44,v228\nv_mul_f32 v45,v45,v228\nv_mul_f32 v46,v46,v228\n" \
"s_waitcnt lgkmcnt(6)\nM v[184:187],v[64:67],v[196:199],0\nbuffer_load_dwordx4 a[0:3],v240,s[52:55],s65 offen offset:0 sc1\nv_mul_f32 v47,v47,v228\nv_cvt_pk_bf16_f32 v37,v38,v39\nM v[184:187],v[80:83],v[212:215],v[184:187]\nds_read_b64_tr_b16 v[208:209],v226 offset:25088\nds_read_b64_tr_b16 v[210:211],v226 offset:29184\n" \
"v_cvt_pk_bf16_f32 v38,v44,v45\nv_cvt_pk_bf16_f32 v39,v46,v47\nv_add_f32 v225,v225,v224\nM v[188:191],v[68:71],v[196:199],0\nds_read_b64_tr_b16 v[196:197],v227 offset:16896\nds_read_b64_tr_b16 v[198:199],v227 offset:20992\nv_add_f32 v225,v225,v228\nv_cmp_ngt_f32 vcc,s28,v225\n" \
"M v[188:191],v[84:87],v[212:215],v[188:191]\nbuffer_store_dwordx4 v[36:39],v240,s[52:55],s38 offen offset:1024 sc1\nv_mul_f32 v48,v48,v224\nv_mul_f32 v49,v49,v224\nds_read_b64_tr_b16 v[212:213],v227 offset:25088\nM v[128:131],v[96:99],v[200:203],v[128:131]\nds_read_b64_tr_b16 v[214:215],v227 offset:29184\ns_and_b64 vcc,vcc,s[90:91]\n" \
"s_addc_u32 s99,s99,0\nv_mul_f32 v50,v50,v224\nM v[128:131],v[112:115],v[204:207],v[128:131]\nv_mul_f32 v51,v51,v224\nv_cvt_pk_bf16_f32 v48,v48,v49\nv_mul_f32 v56,v56,v224\nv_mul_f32 v57,v57,v224\nv_mul_f32 v58,v58,v224\n" \
"M v[132:135],v[100:103],v[200:203],v[132:135]\nv_mul_f32 v59,v59,v224\nv_cvt_pk_bf16_f32 v49,v50,v51\nv_cvt_pk_bf16_f32 v50,v56,v57\nv_cvt_pk_bf16_f32 v51,v58,v59\nM v[132:135],v[116:119],v[204:207],v[132:135]\nv_mov_b32 v230,s99\nds_write_b32 v229,v230\n" \
"ds_read_b64_tr_b16 v[200:201],v226 offset:17408\nds_read_b64_tr_b16 v[202:203],v226 offset:21504\ns_waitcnt lgkmcnt(5)\nM v[136:139],v[96:99],v[216:219],v[136:139]\nbuffer_store_dwordx4 v[48:51],v240,s[52:55],s38 offen offset:2048 sc1\nbuffer_load_dwordx4 a[4:7],v240,s[52:55],s65 offen offset:1024 sc1\nM v[136:139],v[112:115],v[220:223],v[136:139]\nds_read_b64_tr_b16 v[204:205],v226 offset:25600\n" \
"ds_read_b64_tr_b16 v[206:207],v226 offset:29696\nv_mul_f32 v52,v52,v228\nv_mul_f32 v53,v53,v228\nv_mul_f32 v54,v54,v228\nM v[140:143],v[100:103],v[216:219],v[140:143]\nv_mul_f32 v55,v55,v228\nv_cvt_pk_bf16_f32 v52,v52,v53\nv_mul_f32 v60,v60,v228\n" \
"v_mul_f32 v61,v61,v228\nM v[140:143],v[116:119],v[220:223],v[140:143]\nv_mul_f32 v62,v62,v228\nv_mul_f32 v63,v63,v228\nv_cvt_pk_bf16_f32 v53,v54,v55\nv_cvt_pk_bf16_f32 v54,v60,v61\nv_cvt_pk_bf16_f32 v55,v62,v63\nM v[144:147],v[96:99],v[192:195],v[144:147]\n" \
"ds_read_b64_tr_b16 v[216:217],v227 offset:17408\nds_read_b64_tr_b16 v[218:219],v227 offset:21504\nds_read_b64_tr_b16 v[220:221],v227 offset:25600\ns_lshl4_add_u32 s69,s14,s68\nM v[144:147],v[112:115],v[208:211],v[144:147]\nbuffer_store_dwordx4 v[52:55],v240,s[52:55],s38 offen offset:3072 sc1\nds_read_b64_tr_b16 v[222:223],v227 offset:29696\ns_add_u32 m0,s64,65536\n" \
"v_lshl_add_u32 v229,v251,10,v250\nM v[148:151],v[100:103],v[192:195],v[148:151]\nds_read_b64_tr_b16 v[192:193],v226 offset:17920\nds_read_b64_tr_b16 v[194:195],v226 offset:22016\nv_lshlrev_b32 v236,1,v236\nv_lshl_or_b32 v235,v235,8,v236\nM v[148:151],v[116:119],v[208:211],v[148:151]\nds_read_b64_tr_b16 v[208:209],v226 offset:26112\n" \
"ds_read_b64_tr_b16 v[210:211],v226 offset:30208\nv_add_u32 v226,0,v242\ns_lshl_b32 s95,s95,10\nv_add_u32 v236,s95,v235\nM v[152:155],v[96:99],v[196:199],v[152:155]\nbuffer_load_dwordx4 a[8:11],v240,s[52:55],s65 offen offset:2048 sc1\ns_add_u32 s74,s74,1920\ns_waitcnt lgkmcnt(7)\n" \
"M v[152:155],v[112:115],v[212:215],v[152:155]\ns_lshl_b32 s74,s74,10\nv_add_u32 v237,s74,v235\ns_add_u32 s38,s33,16384\nv_lshlrev_b32 v238,1,v240\nv_add_u32 v238,83968,v238\nM v[156:159],v[100:103],v[196:199],v[156:159]\ns_barrier\n" \
"buffer_load_dwordx4 v243,s[40:43],s69 offen offset:0 lds\nds_read_b64_tr_b16 v[196:197],v227 offset:17920\nM v[156:159],v[116:119],v[212:215],v[156:159]\nds_read_b64_tr_b16 v[198:199],v227 offset:22016\nds_read_b32 v230,v229\nds_read_b64_tr_b16 v[212:213],v227 offset:26112\nv_add_u32 v239,83968,v239\nv_bfe_u32 v228,v249,2,2\n" \
"M v[160:163],v[96:99],v[200:203],v[160:163]\nds_read_b64_tr_b16 v[214:215],v227 offset:30208\nv_xor_b32 v227,32,v226\nv_lshrrev_b32 v229,4,v249\ns_mov_b32 s97,4096\nM v[160:163],v[112:115],v[204:207],v[160:163]\ns_add_u32 s95,s95,54525952\ns_add_u32 s74,s74,54525952\n" \
"M v[164:167],v[100:103],v[200:203],v[164:167]\nds_read_b64_tr_b16 v[200:201],v226 offset:0\nds_read_b64_tr_b16 v[202:203],v226 offset:4096\nM v[164:167],v[116:119],v[204:207],v[164:167]\nds_read_b64_tr_b16 v[204:205],v226 offset:8192\ns_waitcnt lgkmcnt(8)\nM v[168:171],v[96:99],v[216:219],v[168:171]\nbuffer_load_dwordx4 a[12:15],v240,s[52:55],s65 offen offset:3072 sc1\n" \
"M v[168:171],v[112:115],v[220:223],v[168:171]\nds_read_b64_tr_b16 v[206:207],v226 offset:12288\ns_add_u32 s65,s65,4096\nM v[172:175],v[100:103],v[216:219],v[172:175]\nbuffer_load_dwordx4 v244,s[40:43],s69 offen offset:1024 lds\nM v[172:175],v[116:119],v[220:223],v[172:175]\nds_read_b64_tr_b16 v[216:217],v227 offset:0\nds_read_b64_tr_b16 v[218:219],v227 offset:4096\n" \
"M v[176:179],v[96:99],v[192:195],v[176:179]\nds_read_b64_tr_b16 v[220:221],v227 offset:8192\nds_read_b64_tr_b16 v[222:223],v227 offset:12288\nM v[176:179],v[112:115],v[208:211],v[176:179]\ns_waitcnt lgkmcnt(8)\nv_cmp_ne_u32 vcc,0,v230\nM v[180:183],v[100:103],v[192:195],v[180:183]\ns_cbranch_vccnz .Lredo\n" \
"ds_read_b64_tr_b16 v[192:193],v226 offset:512\nM v[180:183],v[116:119],v[208:211],v[180:183]\nds_read_b64_tr_b16 v[194:195],v226 offset:4608\nds_read_b64_tr_b16 v[208:209],v226 offset:8704\nM v[184:187],v[96:99],v[196:199],v[184:187]\nbuffer_load_dwordx4 a[16:19],v240,s[52:55],s65 offen offset:0 sc1\nM v[184:187],v[112:115],v[212:215],v[184:187]\nds_read_b64_tr_b16 v[210:211],v226 offset:12800\n" \
"s_and_b32 s58,s3,16384\nM v[188:191],v[100:103],v[196:199],v[188:191]\nbuffer_load_dwordx4 v245,s[40:43],s69 offen offset:2048 lds\nM v[188:191],v[116:119],v[212:215],v[188:191]\nds_read_b64_tr_b16 v[196:197],v227 offset:512\ns_waitcnt vmcnt(27) lgkmcnt(6)\nM v[128:131],v[72:75],v[200:203],v[128:131]\nds_read_b64_tr_b16 v[198:199],v227 offset:4608\n" \
"s_waitcnt vmcnt(23)\nM v[128:131],v[88:91],v[204:207],v[128:131]\nds_read_b64_tr_b16 v[212:213],v227 offset:8704\nds_read_b64_tr_b16 v[214:215],v227 offset:12800\nM v[132:135],v[76:79],v[200:203],v[132:135]\nds_read_b64_tr_b16 v[200:201],v226 offset:1024\ns_waitcnt vmcnt(21)\nM v[132:135],v[92:95],v[204:207],v[132:135]\n" \
"ds_read_b64_tr_b16 v[202:203],v226 offset:5120\nds_read_b64_tr_b16 v[204:205],v226 offset:9216\nM v[136:139],v[72:75],v[216:219],v[136:139]\nbuffer_load_dwordx4 a[20:23],v240,s[52:55],s65 offen offset:1024 sc1\ns_waitcnt lgkmcnt(7)\nM v[136:139],v[88:91],v[220:223],v[136:139]\nds_read_b64_tr_b16 v[206:207],v226 offset:13312\nv_mov_b32 v250,82944\n" \
"M v[140:143],v[76:79],v[216:219],v[140:143]\nbuffer_load_dwordx4 v246,s[40:43],s69 offen offset:3072 lds\nM v[140:143],v[92:95],v[220:223],v[140:143]\nds_read_b64_tr_b16 v[216:217],v227 offset:1024\nds_read_b64_tr_b16 v[218:219],v227 offset:5120\nM v[144:147],v[72:75],v[192:195],v[144:147]\nds_read_b64_tr_b16 v[220:221],v227 offset:9216\nds_read_b64_tr_b16 v[222:223],v227 offset:13312\n" \
"M v[144:147],v[88:91],v[208:211],v[144:147]\ns_lshl4_add_u32 s69,s14,s68\ns_add_u32 s69,s69,0x10000\nM v[148:151],v[76:79],v[192:195],v[148:151]\nds_read_b64_tr_b16 v[192:193],v226 offset:1536\nds_read_b64_tr_b16 v[194:195],v226 offset:5632\nM v[148:151],v[92:95],v[208:211],v[148:151]\ns_add_u32 m0,s64,98304\n" \
"s_waitcnt lgkmcnt(7)\nM v[152:155],v[72:75],v[196:199],v[152:155]\nbuffer_load_dwordx4 a[24:27],v240,s[52:55],s65 offen offset:2048 sc1\nM v[152:155],v[88:91],v[212:215],v[152:155]\nds_read_b64_tr_b16 v[208:209],v226 offset:9728\nds_read_b64_tr_b16 v[210:211],v226 offset:13824\nM v[156:159],v[76:79],v[196:199],v[156:159]\nbuffer_load_dwordx4 v243,s[40:43],s69 offen offset:0 lds\n" \
"M v[156:159],v[92:95],v[212:215],v[156:159]\nds_read_b64_tr_b16 v[196:197],v227 offset:1536\nds_read_b64_tr_b16 v[198:199],v227 offset:5632\nM v[160:163],v[72:75],v[200:203],v[160:163]\nds_read_b64_tr_b16 v[212:213],v227 offset:9728\ns_waitcnt lgkmcnt(7)\nM v[160:163],v[88:91],v[204:207],v[160:163]\nds_read_b64_tr_b16 v[214:215],v227 offset:13824\n" \
"s_lshl_b32 s70,s2,9\nM v[164:167],v[76:79],v[200:203],v[164:167]\nds_read_b64_tr_b16 v[200:201],v226 offset:32768\nds_read_b64_tr_b16 v[202:203],v226 offset:36864\nM v[164:167],v[92:95],v[204:207],v[164:167]\nds_read_b64_tr_b16 v[204:205],v226 offset:40960\nds_read_b64_tr_b16 v[206:207],v226 offset:45056\nM v[168:171],v[72:75],v[216:219],v[168:171]\n" \
"buffer_load_dwordx4 a[28:31],v240,s[52:55],s65 offen offset:3072 sc1\nM v[168:171],v[88:91],v[220:223],v[168:171]\ns_add_u32 s65,s14,s67\ns_add_u32 s65,s65,1048576\nM v[172:175],v[76:79],v[216:219],v[172:175]\nbuffer_load_dwordx4 v244,s[40:43],s69 offen offset:1024 lds\nM v[172:175],v[92:95],v[220:223],v[172:175]\nds_read_b64_tr_b16 v[216:217],v227 offset:32768\n" \
"s_waitcnt lgkmcnt(6)\nM v[176:179],v[72:75],v[192:195],v[176:179]\nds_read_b64_tr_b16 v[218:219],v227 offset:36864\nds_read_b64_tr_b16 v[220:221],v227 offset:40960\nM v[176:179],v[88:91],v[208:211],v[176:179]\nds_read_b64_tr_b16 v[222:223],v227 offset:45056\ns_lshl_b32 s71,s7,8\nM v[180:183],v[76:79],v[192:195],v[180:183]\n" \
"ds_read_b64_tr_b16 v[192:193],v226 offset:33280\nds_read_b64_tr_b16 v[194:195],v226 offset:37376\nM v[180:183],v[92:95],v[208:211],v[180:183]\nds_read_b64_tr_b16 v[208:209],v226 offset:41472\nds_read_b64_tr_b16 v[210:211],v226 offset:45568\nM v[184:187],v[72:75],v[196:199],v[184:187]\nbuffer_load_dwordx4 a[32:35],v240,s[52:55],s65 offen offset:0 sc1\ns_waitcnt lgkmcnt(8)\n" \
"M v[184:187],v[88:91],v[212:215],v[184:187]\nds_write_b128 v250,a[244:247]\nM v[188:191],v[76:79],v[196:199],v[188:191]\nbuffer_load_dwordx4 v245,s[40:43],s69 offen offset:2048 lds\nM v[188:191],v[92:95],v[212:215],v[188:191]\nds_read_b64_tr_b16 v[196:197],v227 offset:33280\ns_waitcnt vmcnt(26)\nM v[128:131],v[104:107],v[200:203],v[128:131]\n" \
"ds_read_b64_tr_b16 v[198:199],v227 offset:37376\ns_waitcnt vmcnt(23)\nM v[128:131],v[120:123],v[204:207],v[128:131]\nds_read_b64_tr_b16 v[212:213],v227 offset:41472\nds_read_b64_tr_b16 v[214:215],v227 offset:45568\nM v[132:135],v[108:111],v[200:203],v[132:135]\nds_read_b64_tr_b16 v[200:201],v226 offset:33792\ns_waitcnt vmcnt(21)\n" \
"M v[132:135],v[124:127],v[204:207],v[132:135]\nds_read_b64_tr_b16 v[202:203],v226 offset:37888\ns_waitcnt lgkmcnt(7)\nM v[136:139],v[104:107],v[216:219],v[136:139]\nbuffer_load_dwordx4 a[36:39],v240,s[52:55],s65 offen offset:1024 sc1\nM v[136:139],v[120:123],v[220:223],v[136:139]\nds_read_b64_tr_b16 v[204:205],v226 offset:41984\nds_read_b64_tr_b16 v[206:207],v226 offset:46080\n" \
"M v[140:143],v[108:111],v[216:219],v[140:143]\nbuffer_load_dwordx4 v246,s[40:43],s69 offen offset:3072 lds\nM v[140:143],v[124:127],v[220:223],v[140:143]\nds_read_b64_tr_b16 v[216:217],v227 offset:33792\nds_read_b64_tr_b16 v[218:219],v227 offset:37888\nM v[144:147],v[104:107],v[192:195],v[144:147]\nds_read_b64_tr_b16 v[220:221],v227 offset:41984\nds_read_b64_tr_b16 v[222:223],v227 offset:46080\n" \
"M v[144:147],v[120:123],v[208:211],v[144:147]\nv_lshl_add_u32 v241,v249,2,s71\ns_add_u32 s70,s70,4096\nM v[148:151],v[108:111],v[192:195],v[148:151]\nds_read_b64_tr_b16 v[192:193],v226 offset:34304\nds_read_b64_tr_b16 v[194:195],v226 offset:38400\nM v[148:151],v[124:127],v[208:211],v[148:151]\nds_read_b64_tr_b16 v[208:209],v226 offset:42496\n" \
"s_waitcnt lgkmcnt(9)\nM v[152:155],v[104:107],v[196:199],v[152:155]\nbuffer_load_dwordx4 a[40:43],v240,s[52:55],s65 offen offset:2048 sc1\nM v[152:155],v[120:123],v[212:215],v[152:155]\nds_read_b64_tr_b16 v[210:211],v226 offset:46592\ns_add_u32 m0,s64,83968\nM v[156:159],v[108:111],v[196:199],v[156:159]\ns_waitcnt vmcnt(16)\n" \
"buffer_store_dword v252,off,s[52:55],s76 sc1\ns_barrier\nM v[156:159],v[124:127],v[212:215],v[156:159]\nds_read_b64_tr_b16 v[196:197],v227 offset:34304\nds_read_b64_tr_b16 v[198:199],v227 offset:38400\nM v[160:163],v[104:107],v[200:203],v[160:163]\nbuffer_load_dwordx4 v240,s[52:55],s72 offen offset:0 sc1 lds\ns_waitcnt lgkmcnt(6)\n" \
"M v[160:163],v[120:123],v[204:207],v[160:163]\nds_read_b64_tr_b16 v[212:213],v227 offset:42496\nds_read_b64_tr_b16 v[214:215],v227 offset:46592\nM v[164:167],v[108:111],v[200:203],v[164:167]\nds_read_b64_tr_b16 v[200:201],v226 offset:16384\nds_read_b64_tr_b16 v[202:203],v226 offset:20480\nM v[164:167],v[124:127],v[204:207],v[164:167]\nds_read_b64_tr_b16 v[204:205],v226 offset:24576\n" \
"ds_read_b64_tr_b16 v[206:207],v226 offset:28672\nM v[168:171],v[104:107],v[216:219],v[168:171]\nbuffer_load_dwordx4 v240,s[52:55],s72 offen offset:1024 sc1 lds\ns_lshl_b32 s72,s2,9\ns_add_u32 s56,s26,s72\ns_addc_u32 s57,s27,0\ns_add_u32 m0,s64,114688\nbuffer_load_dwordx4 a[44:47],v240,s[52:55],s65 offen offset:3072 sc1\n" \
"M v[168:171],v[120:123],v[220:223],v[168:171]\ns_add_u32 s65,s65,4096\nv_add_u32 v230,s70,v241\nM v[172:175],v[108:111],v[216:219],v[172:175]\nds_read_b64_tr_b16 v[216:217],v227 offset:16384\nds_read_b64_tr_b16 v[218:219],v227 offset:20480\nM v[172:175],v[124:127],v[220:223],v[172:175]\nds_read_b64_tr_b16 v[220:221],v227 offset:24576\n" \
"s_waitcnt lgkmcnt(7)\nM v[176:179],v[104:107],v[192:195],v[176:179]\nbuffer_load_dwordx4 v240,s[56:59],s64 offen offset:0 lds\nM v[176:179],v[120:123],v[208:211],v[176:179]\nds_read_b64_tr_b16 v[222:223],v227 offset:28672\nv_add_u32 v231,8192,v230\nM v[180:183],v[108:111],v[192:195],v[180:183]\nds_read_b64_tr_b16 v[192:193],v226 offset:16896\n" \
"ds_read_b64_tr_b16 v[194:195],v226 offset:20992\nM v[180:183],v[124:127],v[208:211],v[180:183]\nds_read_b64_tr_b16 v[208:209],v226 offset:25088\nds_read_b64_tr_b16 v[210:211],v226 offset:29184\nM v[184:187],v[104:107],v[196:199],v[184:187]\nbuffer_load_dwordx4 v240,s[56:59],s64 offen offset:1024 lds\nbuffer_load_dwordx4 a[48:51],v240,s[52:55],s65 offen offset:0 sc1\nM v[184:187],v[120:123],v[212:215],v[184:187]\n" \
"v_accvgpr_write_b32 a194,0\nv_accvgpr_write_b32 a195,0\nM v[188:191],v[108:111],v[196:199],v[188:191]\nds_read_b64_tr_b16 v[196:197],v227 offset:16896\nds_read_b64_tr_b16 v[198:199],v227 offset:20992\nM v[188:191],v[124:127],v[212:215],v[188:191]\nds_read_b64_tr_b16 v[212:213],v227 offset:25088\ns_waitcnt lgkmcnt(8)\n" \
"M v[128:131],a[0:3],v[200:203],v[128:131]\nbuffer_load_dwordx4 v240,s[56:59],s64 offen offset:2048 lds\nM v[128:131],a[8:11],v[204:207],v[128:131]\nds_read_b64_tr_b16 v[214:215],v227 offset:29184\nv_accvgpr_write_b32 a196,0\nM v[132:135],a[4:7],v[200:203],v[132:135]\nds_read_b64_tr_b16 v[200:201],v226 offset:17408\ns_waitcnt vmcnt(22)\n" \
"M v[132:135],a[12:15],v[204:207],v[132:135]\nds_read_b64_tr_b16 v[202:203],v226 offset:21504\nds_read_b64_tr_b16 v[204:205],v226 offset:25600\nM v[136:139],a[0:3],v[216:219],v[136:139]\nbuffer_load_dwordx4 v240,s[56:59],s64 offen offset:3072 lds\nbuffer_load_dwordx4 a[52:55],v240,s[52:55],s65 offen offset:1024 sc1\ns_waitcnt lgkmcnt(7)\nM v[136:139],a[8:11],v[220:223],v[136:139]\n" \
"ds_read_b64_tr_b16 v[206:207],v226 offset:29696\nv_accvgpr_write_b32 a197,0\nM v[140:143],a[4:7],v[216:219],v[140:143]\nds_read_b64_tr_b16 v[216:217],v227 offset:17408\nds_read_b64_tr_b16 v[218:219],v227 offset:21504\nM v[140:143],a[12:15],v[220:223],v[140:143]\nds_read_b64_tr_b16 v[220:221],v227 offset:25600\nds_read_b64_tr_b16 v[222:223],v227 offset:29696\n" \
"M v[144:147],a[0:3],v[192:195],v[144:147]\ns_load_dwordx2 s[44:45],s[4:5],0x68\ns_mov_b32 s46,2097152\nM v[144:147],a[8:11],v[208:211],v[144:147]\nM v[148:151],a[4:7],v[192:195],v[148:151]\nds_read_b64_tr_b16 v[192:193],v226 offset:17920\nds_read_b64_tr_b16 v[194:195],v226 offset:22016\nM v[148:151],a[12:15],v[208:211],v[148:151]\n" \
"ds_read_b64_tr_b16 v[208:209],v226 offset:26112\ns_waitcnt lgkmcnt(8)\nM v[152:155],a[0:3],v[196:199],v[152:155]\nbuffer_load_dwordx4 a[56:59],v240,s[52:55],s65 offen offset:2048 sc1\nM v[152:155],a[8:11],v[212:215],v[152:155]\nds_read_b64_tr_b16 v[210:211],v226 offset:30208\nM v[156:159],a[4:7],v[196:199],v[156:159]\nds_read_b64_tr_b16 v[196:197],v227 offset:17920\n" \
"ds_read_b64_tr_b16 v[198:199],v227 offset:22016\nM v[156:159],a[12:15],v[212:215],v[156:159]\nds_read_b64_tr_b16 v[212:213],v227 offset:26112\nds_read_b64_tr_b16 v[214:215],v227 offset:30208\nM v[160:163],a[0:3],v[200:203],v[160:163]\ns_waitcnt lgkmcnt(8)\nM v[160:163],a[8:11],v[204:207],v[160:163]\nM v[164:167],a[4:7],v[200:203],v[164:167]\n" \
"ds_read_b64_tr_b16 v[200:201],v226 offset:49152\nds_read_b64_tr_b16 v[202:203],v226 offset:53248\nM v[164:167],a[12:15],v[204:207],v[164:167]\nds_read_b64_tr_b16 v[204:205],v226 offset:57344\nds_read_b64_tr_b16 v[206:207],v226 offset:61440\nM v[168:171],a[0:3],v[216:219],v[168:171]\nbuffer_load_dwordx4 a[60:63],v240,s[52:55],s65 offen offset:3072 sc1\nM v[168:171],a[8:11],v[220:223],v[168:171]\n" \
"s_add_u32 s65,s10,s67\nM v[172:175],a[4:7],v[216:219],v[172:175]\nds_read_b64_tr_b16 v[216:217],v227 offset:49152\nds_read_b64_tr_b16 v[218:219],v227 offset:53248\nM v[172:175],a[12:15],v[220:223],v[172:175]\nds_read_b64_tr_b16 v[220:221],v227 offset:57344\ns_waitcnt lgkmcnt(8)\nM v[176:179],a[0:3],v[192:195],v[176:179]\n" \
"ds_read_b64_tr_b16 v[222:223],v227 offset:61440\nM v[176:179],a[8:11],v[208:211],v[176:179]\nM v[180:183],a[4:7],v[192:195],v[180:183]\nds_read_b64_tr_b16 v[192:193],v226 offset:49664\nds_read_b64_tr_b16 v[194:195],v226 offset:53760\nM v[180:183],a[12:15],v[208:211],v[180:183]\nds_read_b64_tr_b16 v[208:209],v226 offset:57856\nds_read_b64_tr_b16 v[210:211],v226 offset:61952\n" \
"M v[184:187],a[0:3],v[196:199],v[184:187]\ns_waitcnt lgkmcnt(8)\nM v[184:187],a[8:11],v[212:215],v[184:187]\nM v[188:191],a[4:7],v[196:199],v[188:191]\nds_read_b64_tr_b16 v[196:197],v227 offset:49664\nds_read_b64_tr_b16 v[198:199],v227 offset:53760\nM v[188:191],a[12:15],v[212:215],v[188:191]\nds_read_b64_tr_b16 v[212:213],v227 offset:57856\n" \
"s_waitcnt vmcnt(24)\nM v[128:131],a[16:19],v[200:203],v[128:131]\nds_read_b64_tr_b16 v[214:215],v227 offset:61952\ns_waitcnt vmcnt(20)\nM v[128:131],a[24:27],v[204:207],v[128:131]\nM v[132:135],a[20:23],v[200:203],v[132:135]\nds_read_b64_tr_b16 v[200:201],v226 offset:50176\ns_waitcnt vmcnt(18)\n" \
"M v[132:135],a[28:31],v[204:207],v[132:135]\nds_read_b64_tr_b16 v[202:203],v226 offset:54272\ns_waitcnt lgkmcnt(8)\nM v[136:139],a[16:19],v[216:219],v[136:139]\nds_read_b64_tr_b16 v[204:205],v226 offset:58368\nds_read_b64_tr_b16 v[206:207],v226 offset:62464\nM v[136:139],a[24:27],v[220:223],v[136:139]\nM v[140:143],a[20:23],v[216:219],v[140:143]\n" \
"ds_read_b64_tr_b16 v[216:217],v227 offset:50176\nds_read_b64_tr_b16 v[218:219],v227 offset:54272\nM v[140:143],a[28:31],v[220:223],v[140:143]\nds_read_b64_tr_b16 v[220:221],v227 offset:58368\nds_read_b64_tr_b16 v[222:223],v227 offset:62464\nM v[144:147],a[16:19],v[192:195],v[144:147]\nbuffer_load_dword v248,v254,s[52:55],s80 offen sc1\ns_waitcnt lgkmcnt(8)\n" \
"M v[144:147],a[24:27],v[208:211],v[144:147]\nM v[148:151],a[20:23],v[192:195],v[148:151]\nglobal_load_ushort v64,v236,s[8:9] offset:0\nM v[148:151],a[28:31],v[208:211],v[148:151]\nds_read_b64_tr_b16 v[192:193],v226 offset:50688\nds_read_b64_tr_b16 v[194:195],v226 offset:54784\nM v[152:155],a[16:19],v[196:199],v[152:155]\nglobal_load_ushort v80,v237,s[8:9] offset:0\n" \
"M v[152:155],a[24:27],v[212:215],v[152:155]\nds_read_b64_tr_b16 v[208:209],v226 offset:58880\nds_read_b64_tr_b16 v[210:211],v226 offset:62976\nM v[156:159],a[20:23],v[196:199],v[156:159]\ns_waitcnt vmcnt(6)\ns_barrier\nM v[156:159],a[28:31],v[212:215],v[156:159]\nglobal_load_ushort v96,v235,s[20:21] offset:0\n" \
"s_waitcnt lgkmcnt(4)\nM v[160:163],a[16:19],v[200:203],v[160:163]\nds_read_b64_tr_b16 v[196:197],v227 offset:50688\nds_read_b64_tr_b16 v[198:199],v227 offset:54784\nM v[160:163],a[24:27],v[204:207],v[160:163]\nglobal_load_ushort v65,v236,s[8:9] offset:32\nM v[164:167],a[20:23],v[200:203],v[164:167]\nds_read_b64_tr_b16 v[212:213],v227 offset:58880\n" \
"ds_read_b64_tr_b16 v[214:215],v227 offset:62976\nM v[164:167],a[28:31],v[204:207],v[164:167]\nglobal_load_ushort v81,v237,s[8:9] offset:32\nM v[168:171],a[16:19],v[216:219],v[168:171]\nv_add_u32 v226,65536,v242\nds_read_b64_tr_b16 v[200:201],v226 offset:0\nM v[168:171],a[24:27],v[220:223],v[168:171]\nds_read_b64_tr_b16 v[202:203],v226 offset:4096\n" \
"global_load_ushort v97,v235,s[20:21] offset:32\nM v[172:175],a[20:23],v[216:219],v[172:175]\nds_read_b64_tr_b16 v[204:205],v226 offset:8192\nds_read_b64_tr_b16 v[206:207],v226 offset:12288\nM v[172:175],a[28:31],v[220:223],v[172:175]\nglobal_load_ushort v66,v236,s[8:9] offset:64\ns_waitcnt lgkmcnt(6)\nM v[176:179],a[16:19],v[192:195],v[176:179]\n" \
"v_xor_b32 v227,32,v226\nds_read_b64_tr_b16 v[216:217],v227 offset:0\nM v[176:179],a[24:27],v[208:211],v[176:179]\nds_read_b64_tr_b16 v[218:219],v227 offset:4096\nglobal_load_ushort v82,v237,s[8:9] offset:64\nM v[180:183],a[20:23],v[192:195],v[180:183]\nds_read_b64_tr_b16 v[220:221],v227 offset:8192\nds_read_b64_tr_b16 v[222:223],v227 offset:12288\n" \
"M v[180:183],a[28:31],v[208:211],v[180:183]\nglobal_load_ushort v98,v235,s[20:21] offset:64\nM v[184:187],a[16:19],v[196:199],v[184:187]\nds_read_b64_tr_b16 v[192:193],v226 offset:512\ns_waitcnt lgkmcnt(7)\nM v[184:187],a[24:27],v[212:215],v[184:187]\nds_read_b64_tr_b16 v[194:195],v226 offset:4608\nglobal_load_ushort v67,v236,s[8:9] offset:96\n" \
"M v[188:191],a[20:23],v[196:199],v[188:191]\nds_read_b64_tr_b16 v[208:209],v226 offset:8704\nds_read_b64_tr_b16 v[210:211],v226 offset:12800\nM v[188:191],a[28:31],v[212:215],v[188:191]\nglobal_load_ushort v83,v237,s[8:9] offset:96\nM v[128:131],a[32:35],v[200:203],v[128:131]\nds_read_b64_tr_b16 v[196:197],v227 offset:512\ns_waitcnt lgkmcnt(7)\n" \
"M v[128:131],a[40:43],v[204:207],v[128:131]\nds_read_b64_tr_b16 v[198:199],v227 offset:4608\nglobal_load_ushort v99,v235,s[20:21] offset:96\nM v[132:135],a[36:39],v[200:203],v[132:135]\nds_read_b64_tr_b16 v[212:213],v227 offset:8704\nds_read_b64_tr_b16 v[214:215],v227 offset:12800\nM v[132:135],a[44:47],v[204:207],v[132:135]\nglobal_load_ushort v68,v236,s[8:9] offset:128\n" \
"M v[136:139],a[32:35],v[216:219],v[136:139]\nds_read_b64_tr_b16 v[200:201],v226 offset:1024\ns_waitcnt lgkmcnt(7)\nM v[136:139],a[40:43],v[220:223],v[136:139]\nds_read_b64_tr_b16 v[202:203],v226 offset:5120\nglobal_load_ushort v84,v237,s[8:9] offset:128\nM v[140:143],a[36:39],v[216:219],v[140:143]\nds_read_b64_tr_b16 v[204:205],v226 offset:9216\n" \
"ds_read_b64_tr_b16 v[206:207],v226 offset:13312\nM v[140:143],a[44:47],v[220:223],v[140:143]\nglobal_load_ushort v100,v235,s[20:21] offset:128\nM v[144:147],a[32:35],v[192:195],v[144:147]\nds_read_b64_tr_b16 v[216:217],v227 offset:1024\ns_waitcnt lgkmcnt(7)\nM v[144:147],a[40:43],v[208:211],v[144:147]\nds_read_b64_tr_b16 v[218:219],v227 offset:5120\n" \
"global_load_ushort v69,v236,s[8:9] offset:160\nM v[148:151],a[36:39],v[192:195],v[148:151]\nds_read_b64_tr_b16 v[220:221],v227 offset:9216\nds_read_b64_tr_b16 v[222:223],v227 offset:13312\nM v[148:151],a[44:47],v[208:211],v[148:151]\nglobal_load_ushort v85,v237,s[8:9] offset:160\nM v[152:155],a[32:35],v[196:199],v[152:155]\nds_read_b64_tr_b16 v[192:193],v226 offset:1536\n" \
"s_waitcnt lgkmcnt(7)\nM v[152:155],a[40:43],v[212:215],v[152:155]\nds_read_b64_tr_b16 v[194:195],v226 offset:5632\nglobal_load_ushort v101,v235,s[20:21] offset:160\nM v[156:159],a[36:39],v[196:199],v[156:159]\nds_read_b64_tr_b16 v[208:209],v226 offset:9728\nds_read_b64_tr_b16 v[210:211],v226 offset:13824\nM v[156:159],a[44:47],v[212:215],v[156:159]\n" \
"global_load_ushort v70,v236,s[8:9] offset:192\nM v[160:163],a[32:35],v[200:203],v[160:163]\nds_read_b64_tr_b16 v[196:197],v227 offset:1536\ns_waitcnt lgkmcnt(7)\nM v[160:163],a[40:43],v[204:207],v[160:163]\nds_read_b64_tr_b16 v[198:199],v227 offset:5632\nglobal_load_ushort v86,v237,s[8:9] offset:192\nM v[164:167],a[36:39],v[200:203],v[164:167]\n" \
"ds_read_b64_tr_b16 v[212:213],v227 offset:9728\nds_read_b64_tr_b16 v[214:215],v227 offset:13824\nM v[164:167],a[44:47],v[204:207],v[164:167]\ns_waitcnt vmcnt(20)\nv_cmp_eq_u32 vcc,0,v248\nglobal_load_ushort v102,v235,s[20:21] offset:192\nM v[168:171],a[32:35],v[216:219],v[168:171]\ns_cbranch_vccnz .Lps7\n" \
".Lpb7:\nbuffer_load_dword v248,v251,s[52:55],s79 offen sc1\ns_waitcnt lgkmcnt(4)\nM v[168:171],a[40:43],v[220:223],v[168:171]\nds_read_b64_tr_b16 v[200:201],v226 offset:32768\nds_read_b64_tr_b16 v[202:203],v226 offset:36864\nglobal_load_ushort v71,v236,s[8:9] offset:224\nM v[172:175],a[36:39],v[216:219],v[172:175]\n" \
"ds_read_b64_tr_b16 v[204:205],v226 offset:40960\nds_read_b64_tr_b16 v[206:207],v226 offset:45056\nM v[172:175],a[44:47],v[220:223],v[172:175]\nglobal_load_ushort v87,v237,s[8:9] offset:224\nM v[176:179],a[32:35],v[192:195],v[176:179]\nds_read_b64_tr_b16 v[216:217],v227 offset:32768\nds_read_b64_tr_b16 v[218:219],v227 offset:36864\nM v[176:179],a[40:43],v[208:211],v[176:179]\n" \
"global_load_ushort v103,v235,s[20:21] offset:224\nM v[180:183],a[36:39],v[192:195],v[180:183]\nds_read_b64_tr_b16 v[220:221],v227 offset:40960\nds_read_b64_tr_b16 v[222:223],v227 offset:45056\nM v[180:183],a[44:47],v[208:211],v[180:183]\nds_read_b64_tr_b16 v[192:193],v226 offset:33280\ns_waitcnt lgkmcnt(9)\nM v[184:187],a[32:35],v[196:199],v[184:187]\n" \
"ds_read_b64_tr_b16 v[194:195],v226 offset:37376\nds_read_b64_tr_b16 v[208:209],v226 offset:41472\nM v[184:187],a[40:43],v[212:215],v[184:187]\nds_read_b64_tr_b16 v[210:211],v226 offset:45568\nds_read_b64 v[112:113],v239 offset:0\nM v[188:191],a[36:39],v[196:199],v[188:191]\nds_read_b64_tr_b16 v[196:197],v227 offset:33280\nds_read_b64_tr_b16 v[198:199],v227 offset:37376\n" \
"M v[188:191],a[44:47],v[212:215],v[188:191]\nds_read_b64_tr_b16 v[212:213],v227 offset:41472\ns_waitcnt lgkmcnt(11)\nM v[128:131],a[48:51],v[200:203],v[128:131]\nds_read_b64_tr_b16 v[214:215],v227 offset:45568\nds_read_b64 v[114:115],v239 offset:4096\nM v[128:131],a[56:59],v[204:207],v[128:131]\nds_read_b64 v[116:117],v239 offset:8192\n" \
"ds_read_b64 v[118:119],v239 offset:12288\nM v[132:135],a[52:55],v[200:203],v[132:135]\nds_read_b64_tr_b16 v[200:201],v226 offset:33792\nds_read_b64_tr_b16 v[202:203],v226 offset:37888\nM v[132:135],a[60:63],v[204:207],v[132:135]\nds_read_b64_tr_b16 v[204:205],v226 offset:41984\ns_waitcnt lgkmcnt(13)\nM v[136:139],a[48:51],v[216:219],v[136:139]\n" \
"ds_read_b64_tr_b16 v[206:207],v226 offset:46080\nM v[136:139],a[56:59],v[220:223],v[136:139]\nM v[140:143],a[52:55],v[216:219],v[140:143]\nds_read_b64_tr_b16 v[216:217],v227 offset:33792\nds_read_b64_tr_b16 v[218:219],v227 offset:37888\nM v[140:143],a[60:63],v[220:223],v[140:143]\nds_read_b64_tr_b16 v[220:221],v227 offset:41984\nds_read_b64_tr_b16 v[222:223],v227 offset:46080\n" \
"M v[144:147],a[48:51],v[192:195],v[144:147]\ns_waitcnt lgkmcnt(11)\nM v[144:147],a[56:59],v[208:211],v[144:147]\nv_cvt_pk_bf16_f32 v128,v128,v136\nv_cvt_pk_bf16_f32 v129,v129,v137\nM v[148:151],a[52:55],v[192:195],v[148:151]\nds_read_b64_tr_b16 v[192:193],v226 offset:34304\nds_read_b64_tr_b16 v[194:195],v226 offset:38400\n" \
"M v[148:151],a[60:63],v[208:211],v[148:151]\nds_read_b64_tr_b16 v[208:209],v226 offset:42496\nds_read_b64_tr_b16 v[210:211],v226 offset:46592\nM v[152:155],a[48:51],v[196:199],v[152:155]\nv_add_u32 v226,131072,v242\nv_cvt_pk_bf16_f32 v130,v130,v138\nM v[152:155],a[56:59],v[212:215],v[152:155]\nv_cvt_pk_bf16_f32 v131,v131,v139\n" \
"v_min_u32 v136,-2,v128\nM v[156:159],a[52:55],v[196:199],v[156:159]\nds_read_b64_tr_b16 v[196:197],v227 offset:34304\nds_read_b64_tr_b16 v[198:199],v227 offset:38400\nM v[156:159],a[60:63],v[212:215],v[156:159]\nds_read_b64_tr_b16 v[212:213],v227 offset:42496\ns_waitcnt lgkmcnt(7)\nM v[160:163],a[48:51],v[200:203],v[160:163]\n" \
"ds_read_b64_tr_b16 v[214:215],v227 offset:46592\nv_xor_b32 v227,32,v226\nM v[160:163],a[56:59],v[204:207],v[160:163]\nv_cvt_pk_bf16_f32 v144,v144,v152\nv_min_u32 v137,-2,v144\nM v[164:167],a[52:55],v[200:203],v[164:167]\nds_read_b64_tr_b16 v[200:201],v226 offset:0\nds_read_b64_tr_b16 v[202:203],v226 offset:4096\n" \
"M v[164:167],a[60:63],v[204:207],v[164:167]\nds_read_b64_tr_b16 v[204:205],v226 offset:8192\nds_read_b64_tr_b16 v[206:207],v226 offset:12288\nM v[168:171],a[48:51],v[216:219],v[168:171]\nv_cvt_pk_bf16_f32 v145,v145,v153\nv_cvt_pk_bf16_f32 v146,v146,v154\nM v[168:171],a[56:59],v[220:223],v[168:171]\nv_cvt_pk_bf16_f32 v147,v147,v155\n" \
"v_min_u32 v152,-2,v129\nM v[172:175],a[52:55],v[216:219],v[172:175]\ns_waitcnt vmcnt(3)\nv_cmp_eq_u32 vcc,0,v248\nM v[172:175],a[60:63],v[220:223],v[172:175]\ns_cbranch_vccnz .Lps8\n.Lpb8:\ns_waitcnt lgkmcnt(4)\n" \
"M v[176:179],a[48:51],v[192:195],v[176:179]\nbuffer_load_dwordx4 a[64:67],v240,s[52:55],s65 offen offset:0 sc1\nM v[176:179],a[56:59],v[208:211],v[176:179]\nds_read_b64_tr_b16 v[216:217],v227 offset:0\nds_read_b64_tr_b16 v[218:219],v227 offset:4096\nM v[180:183],a[52:55],v[192:195],v[180:183]\nds_read_b64_tr_b16 v[220:221],v227 offset:8192\nds_read_b64_tr_b16 v[222:223],v227 offset:12288\n" \
"M v[180:183],a[60:63],v[208:211],v[180:183]\nds_read_b64_tr_b16 v[192:193],v226 offset:512\nds_read_b64_tr_b16 v[194:195],v226 offset:4608\nM v[184:187],a[48:51],v[196:199],v[184:187]\nbuffer_load_dwordx4 a[68:71],v240,s[52:55],s65 offen offset:1024 sc1\nM v[184:187],a[56:59],v[212:215],v[184:187]\nds_read_b64_tr_b16 v[208:209],v226 offset:8704\nds_read_b64_tr_b16 v[210:211],v226 offset:12800\n" \
"M v[188:191],a[52:55],v[196:199],v[188:191]\nds_read_b64_tr_b16 v[196:197],v227 offset:512\nds_read_b64_tr_b16 v[198:199],v227 offset:4608\nM v[188:191],a[60:63],v[212:215],v[188:191]\nds_read_b64_tr_b16 v[212:213],v227 offset:8704\ns_waitcnt lgkmcnt(11)\nM v[8:11],v[0:3],v[200:203],0\nbuffer_load_dwordx4 a[72:75],v240,s[52:55],s65 offen offset:2048 sc1\n" \
"M v[8:11],v[16:19],v[204:207],v[8:11]\nds_read_b64_tr_b16 v[214:215],v227 offset:12800\nv_cvt_pk_bf16_f32 v160,v160,v168\nM v[12:15],v[4:7],v[200:203],0\nds_read_b64_tr_b16 v[200:201],v226 offset:1024\nds_read_b64_tr_b16 v[202:203],v226 offset:5120\nM v[12:15],v[20:23],v[204:207],v[12:15]\nds_read_b64_tr_b16 v[204:205],v226 offset:9216\n" \
"s_waitcnt lgkmcnt(9)\nM v[24:27],v[0:3],v[216:219],0\nbuffer_load_dwordx4 a[76:79],v240,s[52:55],s65 offen offset:3072 sc1\nM v[24:27],v[16:19],v[220:223],v[24:27]\nds_read_b64_tr_b16 v[206:207],v226 offset:13312\ns_add_u32 s65,s65,4096\nM v[28:31],v[4:7],v[216:219],0\nds_read_b64_tr_b16 v[216:217],v227 offset:1024\n" \
"ds_read_b64_tr_b16 v[218:219],v227 offset:5120\nM v[28:31],v[20:23],v[220:223],v[28:31]\nds_read_b64_tr_b16 v[220:221],v227 offset:9216\nds_read_b64_tr_b16 v[222:223],v227 offset:13312\nM v[40:43],v[0:3],v[192:195],0\nbuffer_load_dwordx4 a[80:83],v240,s[52:55],s65 offen offset:0 sc1\ns_waitcnt lgkmcnt(8)\nM v[40:43],v[16:19],v[208:211],v[40:43]\n" \
"v_cvt_pk_bf16_f32 v176,v176,v184\nv_min_u32 v138,-2,v160\nM v[44:47],v[4:7],v[192:195],0\nds_read_b64_tr_b16 v[192:193],v226 offset:1536\nds_read_b64_tr_b16 v[194:195],v226 offset:5632\nM v[44:47],v[20:23],v[208:211],v[44:47]\nv_min_u32 v139,-2,v176\nds_read_b64_tr_b16 v[208:209],v226 offset:9728\n" \
"M v[56:59],v[0:3],v[196:199],0\nbuffer_load_dwordx4 a[84:87],v240,s[52:55],s65 offen offset:1024 sc1\nM v[56:59],v[16:19],v[212:215],v[56:59]\nds_read_b64_tr_b16 v[210:211],v226 offset:13824\nv_cvt_pk_bf16_f32 v161,v161,v169\nM v[60:63],v[4:7],v[196:199],0\nbuffer_store_dwordx4 v[136:139],v253,s[52:55],s33 offen offset:0 sc1\nM v[60:63],v[20:23],v[212:215],v[60:63]\n" \
"ds_read_b64_tr_b16 v[196:197],v227 offset:1536\ns_waitcnt lgkmcnt(5)\nM v[72:75],v[0:3],v[200:203],0\nbuffer_load_dwordx4 a[88:91],v240,s[52:55],s65 offen offset:2048 sc1\nM v[72:75],v[16:19],v[204:207],v[72:75]\nds_read_b64_tr_b16 v[198:199],v227 offset:5632\nds_read_b64_tr_b16 v[212:213],v227 offset:9728\nM v[76:79],v[4:7],v[200:203],0\n" \
"ds_read_b64_tr_b16 v[214:215],v227 offset:13824\nds_read_b64_tr_b16 v[200:201],v226 offset:16384\nM v[76:79],v[20:23],v[204:207],v[76:79]\nds_read_b64_tr_b16 v[202:203],v226 offset:20480\nds_read_b64_tr_b16 v[204:205],v226 offset:24576\nM v[88:91],v[0:3],v[216:219],0\nbuffer_load_dwordx4 a[92:95],v240,s[52:55],s65 offen offset:3072 sc1\nM v[88:91],v[16:19],v[220:223],v[88:91]\n" \
"ds_read_b64_tr_b16 v[206:207],v226 offset:28672\ns_add_u32 s65,s11,s67\nM v[92:95],v[4:7],v[216:219],0\nds_read_b64_tr_b16 v[216:217],v227 offset:16384\nds_read_b64_tr_b16 v[218:219],v227 offset:20480\nM v[92:95],v[20:23],v[220:223],v[92:95]\nds_read_b64_tr_b16 v[220:221],v227 offset:24576\ns_waitcnt lgkmcnt(10)\n" \
"M v[104:107],v[0:3],v[192:195],0\nbuffer_load_dwordx4 a[96:99],v240,s[52:55],s65 offen offset:0 sc1\nM v[104:107],v[16:19],v[208:211],v[104:107]\nds_read_b64_tr_b16 v[222:223],v227 offset:28672\nv_cvt_pk_bf16_f32 v177,v177,v185\nM v[108:111],v[4:7],v[192:195],0\nds_read_b64_tr_b16 v[192:193],v226 offset:16896\nds_read_b64_tr_b16 v[194:195],v226 offset:20992\n" \
"M v[108:111],v[20:23],v[208:211],v[108:111]\nv_min_u32 v153,-2,v145\ns_waitcnt lgkmcnt(7)\nM v[120:123],v[0:3],v[196:199],0\nbuffer_load_dwordx4 a[100:103],v240,s[52:55],s65 offen offset:1024 sc1\nM v[120:123],v[16:19],v[212:215],v[120:123]\nv_min_u32 v154,-2,v161\nv_min_u32 v155,-2,v177\n" \
"M v[124:127],v[4:7],v[196:199],0\nds_read_b64_tr_b16 v[208:209],v226 offset:25088\nds_read_b64_tr_b16 v[210:211],v226 offset:29184\nbuffer_store_dwordx4 v[152:155],v253,s[52:55],s33 offen offset:1024 sc1\nM v[124:127],v[20:23],v[212:215],v[124:127]\nds_read_b64_tr_b16 v[196:197],v227 offset:16896\nds_read_b64_tr_b16 v[198:199],v227 offset:20992\nM v[8:11],v[32:35],v[200:203],v[8:11]\n" \
"buffer_load_dwordx4 a[104:107],v240,s[52:55],s65 offen offset:2048 sc1\ns_waitcnt lgkmcnt(6)\nM v[8:11],v[48:51],v[204:207],v[8:11]\nds_read_b64_tr_b16 v[212:213],v227 offset:25088\nds_read_b64_tr_b16 v[214:215],v227 offset:29184\nM v[12:15],v[36:39],v[200:203],v[12:15]\nds_read_b64_tr_b16 v[200:201],v226 offset:17408\nds_read_b64_tr_b16 v[202:203],v226 offset:21504\n" \
"M v[12:15],v[52:55],v[204:207],v[12:15]\nds_read_b64_tr_b16 v[204:205],v226 offset:25600\nds_read_b64_tr_b16 v[206:207],v226 offset:29696\nM v[24:27],v[32:35],v[216:219],v[24:27]\nbuffer_load_dwordx4 a[108:111],v240,s[52:55],s65 offen offset:3072 sc1\nM v[24:27],v[48:51],v[220:223],v[24:27]\ns_add_u32 s65,s65,4096\nv_cvt_pk_bf16_f32 v162,v162,v170\n" \
"M v[28:31],v[36:39],v[216:219],v[28:31]\nds_read_b64_tr_b16 v[216:217],v227 offset:17408\nds_read_b64_tr_b16 v[218:219],v227 offset:21504\nM v[28:31],v[52:55],v[220:223],v[28:31]\nds_read_b64_tr_b16 v[220:221],v227 offset:25600\ns_waitcnt lgkmcnt(9)\nM v[40:43],v[32:35],v[192:195],v[40:43]\nbuffer_load_dwordx4 a[112:115],v240,s[52:55],s65 offen offset:0 sc1\n" \
"M v[40:43],v[48:51],v[208:211],v[40:43]\nds_read_b64_tr_b16 v[222:223],v227 offset:29696\nv_cvt_pk_bf16_f32 v163,v163,v171\nM v[44:47],v[36:39],v[192:195],v[44:47]\nds_read_b64_tr_b16 v[192:193],v226 offset:17920\nds_read_b64_tr_b16 v[194:195],v226 offset:22016\nM v[44:47],v[52:55],v[208:211],v[44:47]\nv_cvt_pk_bf16_f32 v178,v178,v186\n" \
"v_min_u32 v168,-2,v130\nM v[56:59],v[32:35],v[196:199],v[56:59]\nbuffer_load_dwordx4 a[116:119],v240,s[52:55],s65 offen offset:1024 sc1\ns_waitcnt lgkmcnt(6)\nM v[56:59],v[48:51],v[212:215],v[56:59]\nv_min_u32 v169,-2,v146\nv_min_u32 v170,-2,v162\nM v[60:63],v[36:39],v[196:199],v[60:63]\n" \
"v_min_u32 v171,-2,v178\nds_read_b64_tr_b16 v[208:209],v226 offset:26112\nds_read_b64_tr_b16 v[210:211],v226 offset:30208\nbuffer_store_dwordx4 v[168:171],v253,s[52:55],s33 offen offset:2048 sc1\nM v[60:63],v[52:55],v[212:215],v[60:63]\nds_read_b64_tr_b16 v[196:197],v227 offset:17920\nds_read_b64_tr_b16 v[198:199],v227 offset:22016\nM v[72:75],v[32:35],v[200:203],v[72:75]\n" \
"buffer_load_dwordx4 a[120:123],v240,s[52:55],s65 offen offset:2048 sc1\nM v[72:75],v[48:51],v[204:207],v[72:75]\nds_read_b64_tr_b16 v[212:213],v227 offset:26112\nds_read_b64_tr_b16 v[214:215],v227 offset:30208\nM v[76:79],v[36:39],v[200:203],v[76:79]\nv_add_u32 v226,0,v242\nds_read_b64_tr_b16 v[200:201],v226 offset:0\nM v[76:79],v[52:55],v[204:207],v[76:79]\n" \
"ds_read_b64_tr_b16 v[202:203],v226 offset:4096\ns_waitcnt lgkmcnt(8)\nM v[88:91],v[32:35],v[216:219],v[88:91]\nbuffer_load_dwordx4 a[124:127],v240,s[52:55],s65 offen offset:3072 sc1\nM v[88:91],v[48:51],v[220:223],v[88:91]\nds_read_b64_tr_b16 v[204:205],v226 offset:8192\nds_read_b64_tr_b16 v[206:207],v226 offset:12288\nM v[92:95],v[36:39],v[216:219],v[92:95]\n" \
"s_add_u32 s65,s14,s67\nv_xor_b32 v227,32,v226\nM v[92:95],v[52:55],v[220:223],v[92:95]\nds_read_b64_tr_b16 v[216:217],v227 offset:0\nds_read_b64_tr_b16 v[218:219],v227 offset:4096\nM v[104:107],v[32:35],v[192:195],v[104:107]\nbuffer_load_dwordx4 a[128:131],v240,s[52:55],s65 offen offset:0 sc1\ns_waitcnt lgkmcnt(6)\n" \
"M v[104:107],v[48:51],v[208:211],v[104:107]\nds_read_b64_tr_b16 v[220:221],v227 offset:8192\nds_read_b64_tr_b16 v[222:223],v227 offset:12288\nM v[108:111],v[36:39],v[192:195],v[108:111]\nv_cvt_pk_bf16_f32 v179,v179,v187\nds_read_b64_tr_b16 v[192:193],v226 offset:512\nM v[108:111],v[52:55],v[208:211],v[108:111]\nds_read_b64_tr_b16 v[194:195],v226 offset:4608\n" \
"v_min_u32 v184,-2,v131\nM v[120:123],v[32:35],v[196:199],v[120:123]\nbuffer_load_dwordx4 a[132:135],v240,s[52:55],s65 offen offset:1024 sc1\nM v[120:123],v[48:51],v[212:215],v[120:123]\nv_min_u32 v185,-2,v147\nv_min_u32 v186,-2,v163\nM v[124:127],v[36:39],v[196:199],v[124:127]\nv_min_u32 v187,-2,v179\n" \
"buffer_store_dwordx4 v[184:187],v253,s[52:55],s33 offen offset:3072 sc1\nds_read_b64_tr_b16 v[208:209],v226 offset:8704\nds_read_b64_tr_b16 v[210:211],v226 offset:12800\nM v[124:127],v[52:55],v[212:215],v[124:127]\nds_read_b64_tr_b16 v[196:197],v227 offset:512\ns_waitcnt vmcnt(18) lgkmcnt(7)\nM v[8:11],a[64:67],v[200:203],v[8:11]\nbuffer_load_dwordx4 a[136:139],v240,s[52:55],s65 offen offset:2048 sc1\n" \
"M v[8:11],a[72:75],v[204:207],v[8:11]\nds_read_b64_tr_b16 v[198:199],v227 offset:4608\nds_read_b64_tr_b16 v[212:213],v227 offset:8704\nM v[12:15],a[68:71],v[200:203],v[12:15]\nds_read_b64_tr_b16 v[214:215],v227 offset:12800\nds_read_b64_tr_b16 v[200:201],v226 offset:1024\nM v[12:15],a[76:79],v[204:207],v[12:15]\nds_read_b64_tr_b16 v[202:203],v226 offset:5120\n" \
"ds_read_b64_tr_b16 v[204:205],v226 offset:9216\nM v[24:27],a[64:67],v[216:219],v[24:27]\nbuffer_load_dwordx4 a[140:143],v240,s[52:55],s65 offen offset:3072 sc1\ns_waitcnt lgkmcnt(9)\nM v[24:27],a[72:75],v[220:223],v[24:27]\nds_read_b64_tr_b16 v[206:207],v226 offset:13312\ns_add_u32 s65,s65,4096\nM v[28:31],a[68:71],v[216:219],v[28:31]\n" \
"ds_read_b64_tr_b16 v[216:217],v227 offset:1024\nds_read_b64_tr_b16 v[218:219],v227 offset:5120\nM v[28:31],a[76:79],v[220:223],v[28:31]\nds_read_b64_tr_b16 v[220:221],v227 offset:9216\nds_read_b64_tr_b16 v[222:223],v227 offset:13312\nM v[40:43],a[64:67],v[192:195],v[40:43]\nbuffer_load_dwordx4 a[144:147],v240,s[52:55],s65 offen offset:0 sc1\ns_waitcnt lgkmcnt(9)\n" \
"M v[40:43],a[72:75],v[208:211],v[40:43]\nv_cvt_pk_bf16_f32 v132,v132,v140\nv_cvt_pk_bf16_f32 v133,v133,v141\nM v[44:47],a[68:71],v[192:195],v[44:47]\nv_cvt_pk_bf16_f32 v134,v134,v142\nv_cvt_pk_bf16_f32 v135,v135,v143\nM v[44:47],a[76:79],v[208:211],v[44:47]\nv_cvt_pk_bf16_f32 v148,v148,v156\n" \
"v_cvt_pk_bf16_f32 v164,v164,v172\nM v[56:59],a[64:67],v[196:199],v[56:59]\nbuffer_load_dwordx4 a[148:151],v240,s[52:55],s65 offen offset:1024 sc1\ns_waitcnt lgkmcnt(4)\nM v[56:59],a[72:75],v[212:215],v[56:59]\nv_cvt_pk_bf16_f32 v180,v180,v188\nds_read_b64_tr_b16 v[192:193],v226 offset:1536\nds_read_b64_tr_b16 v[194:195],v226 offset:5632\n" \
"M v[60:63],a[68:71],v[196:199],v[60:63]\nv_min_u32 v140,-2,v132\nv_min_u32 v141,-2,v148\nv_min_u32 v142,-2,v164\nv_min_u32 v143,-2,v180\nbuffer_store_dwordx4 v[140:143],v253,s[52:55],s38 offen offset:0 sc1\nds_read_b64_tr_b16 v[208:209],v226 offset:9728\nds_read_b64_tr_b16 v[210:211],v226 offset:13824\n" \
"M v[60:63],a[76:79],v[212:215],v[60:63]\nds_read_b64_tr_b16 v[196:197],v227 offset:1536\nds_read_b64_tr_b16 v[198:199],v227 offset:5632\nM v[72:75],a[64:67],v[200:203],v[72:75]\nbuffer_load_dwordx4 a[152:155],v240,s[52:55],s65 offen offset:2048 sc1\nM v[72:75],a[72:75],v[204:207],v[72:75]\nds_read_b64_tr_b16 v[212:213],v227 offset:9728\nds_read_b64_tr_b16 v[214:215],v227 offset:13824\n" \
"M v[76:79],a[68:71],v[200:203],v[76:79]\nds_read_b64_tr_b16 v[200:201],v226 offset:32768\nds_read_b64_tr_b16 v[202:203],v226 offset:36864\nM v[76:79],a[76:79],v[204:207],v[76:79]\nds_read_b64_tr_b16 v[204:205],v226 offset:40960\ns_waitcnt lgkmcnt(9)\nM v[88:91],a[64:67],v[216:219],v[88:91]\nbuffer_load_dwordx4 a[156:159],v240,s[52:55],s65 offen offset:3072 sc1\n" \
"M v[88:91],a[72:75],v[220:223],v[88:91]\nds_read_b64_tr_b16 v[206:207],v226 offset:45056\nv_cvt_pk_bf16_f32 v149,v149,v157\nM v[92:95],a[68:71],v[216:219],v[92:95]\nds_read_b64_tr_b16 v[216:217],v227 offset:32768\nds_read_b64_tr_b16 v[218:219],v227 offset:36864\nM v[92:95],a[76:79],v[220:223],v[92:95]\nds_read_b64_tr_b16 v[220:221],v227 offset:40960\n" \
"ds_read_b64_tr_b16 v[222:223],v227 offset:45056\nM v[104:107],a[64:67],v[192:195],v[104:107]\nv_cvt_pk_bf16_f32 v150,v150,v158\ns_waitcnt lgkmcnt(9)\nM v[104:107],a[72:75],v[208:211],v[104:107]\nv_cvt_pk_bf16_f32 v151,v151,v159\nv_cvt_pk_bf16_f32 v165,v165,v173\nM v[108:111],a[68:71],v[192:195],v[108:111]\n" \
"v_cvt_pk_bf16_f32 v181,v181,v189\nds_read_b64_tr_b16 v[192:193],v226 offset:33280\nM v[108:111],a[76:79],v[208:211],v[108:111]\nds_read_b64_tr_b16 v[194:195],v226 offset:37376\nv_min_u32 v156,-2,v133\nM v[120:123],a[64:67],v[196:199],v[120:123]\nv_min_u32 v157,-2,v149\ns_waitcnt lgkmcnt(6)\n" \
"M v[120:123],a[72:75],v[212:215],v[120:123]\nv_min_u32 v158,-2,v165\nv_min_u32 v159,-2,v181\nM v[124:127],a[68:71],v[196:199],v[124:127]\nbuffer_store_dwordx4 v[156:159],v253,s[52:55],s38 offen offset:1024 sc1\nds_read_b64_tr_b16 v[208:209],v226 offset:41472\nds_read_b64_tr_b16 v[210:211],v226 offset:45568\nM v[124:127],a[76:79],v[212:215],v[124:127]\n" \
"ds_read_b64_tr_b16 v[196:197],v227 offset:33280\ns_waitcnt vmcnt(24)\nM v[8:11],a[80:83],v[200:203],v[8:11]\nds_read_b64_tr_b16 v[198:199],v227 offset:37376\ns_waitcnt vmcnt(19)\nM v[8:11],a[88:91],v[204:207],v[8:11]\nds_read_b64_tr_b16 v[212:213],v227 offset:41472\nds_read_b64_tr_b16 v[214:215],v227 offset:45568\n" \
"M v[12:15],a[84:87],v[200:203],v[12:15]\nds_read_b64_tr_b16 v[200:201],v226 offset:33792\nds_read_b64_tr_b16 v[202:203],v226 offset:37888\nM v[12:15],a[92:95],v[204:207],v[12:15]\nds_read_b64_tr_b16 v[204:205],v226 offset:41984\ns_waitcnt lgkmcnt(9)\nM v[24:27],a[80:83],v[216:219],v[24:27]\nds_read_b64_tr_b16 v[206:207],v226 offset:46080\n" \
"v_cvt_pk_bf16_f32 v166,v166,v174\nM v[24:27],a[88:91],v[220:223],v[24:27]\nv_cvt_pk_bf16_f32 v167,v167,v175\nv_cvt_pk_bf16_f32 v182,v182,v190\nM v[28:31],a[84:87],v[216:219],v[28:31]\nds_read_b64_tr_b16 v[216:217],v227 offset:33792\nds_read_b64_tr_b16 v[218:219],v227 offset:37888\nM v[28:31],a[92:95],v[220:223],v[28:31]\n" \
"ds_read_b64_tr_b16 v[220:221],v227 offset:41984\nds_read_b64_tr_b16 v[222:223],v227 offset:46080\nM v[40:43],a[80:83],v[192:195],v[40:43]\nv_min_u32 v172,-2,v134\ns_waitcnt lgkmcnt(9)\nM v[40:43],a[88:91],v[208:211],v[40:43]\nv_min_u32 v173,-2,v150\nv_min_u32 v174,-2,v166\n" \
"M v[44:47],a[84:87],v[192:195],v[44:47]\nds_read_b64_tr_b16 v[192:193],v226 offset:34304\nds_read_b64_tr_b16 v[194:195],v226 offset:38400\nM v[44:47],a[92:95],v[208:211],v[44:47]\nv_min_u32 v175,-2,v182\nds_read_b64_tr_b16 v[208:209],v226 offset:42496\nM v[56:59],a[80:83],v[196:199],v[56:59]\nds_read_b64_tr_b16 v[210:211],v226 offset:46592\n" \
"s_waitcnt lgkmcnt(8)\nM v[56:59],a[88:91],v[212:215],v[56:59]\nv_cvt_pk_bf16_f32 v183,v183,v191\nv_min_u32 v188,-2,v135\nM v[60:63],a[84:87],v[196:199],v[60:63]\nbuffer_store_dwordx4 v[172:175],v253,s[52:55],s38 offen offset:2048 sc1\nM v[60:63],a[92:95],v[212:215],v[60:63]\nds_read_b64_tr_b16 v[196:197],v227 offset:34304\n" \
"ds_read_b64_tr_b16 v[198:199],v227 offset:38400\nM v[72:75],a[80:83],v[200:203],v[72:75]\nds_read_b64_tr_b16 v[212:213],v227 offset:42496\nds_read_b64_tr_b16 v[214:215],v227 offset:46592\nM v[72:75],a[88:91],v[204:207],v[72:75]\nv_min_u32 v189,-2,v151\nv_min_u32 v190,-2,v167\nM v[76:79],a[84:87],v[200:203],v[76:79]\n" \
"ds_read_b64_tr_b16 v[200:201],v226 offset:16384\nds_read_b64_tr_b16 v[202:203],v226 offset:20480\nM v[76:79],a[92:95],v[204:207],v[76:79]\nds_read_b64_tr_b16 v[204:205],v226 offset:24576\ns_waitcnt lgkmcnt(7)\nM v[88:91],a[80:83],v[216:219],v[88:91]\nds_read_b64_tr_b16 v[206:207],v226 offset:28672\nv_min_u32 v191,-2,v183\n" \
"M v[88:91],a[88:91],v[220:223],v[88:91]\nds_read_b128 v[0:3],v238 offset:0\nds_read_b128 v[4:7],v238 offset:16\nM v[92:95],a[84:87],v[216:219],v[92:95]\nds_read_b64_tr_b16 v[216:217],v227 offset:16384\nv_cmp_eq_u32 vcc,v228,v229\nM v[92:95],a[92:95],v[220:223],v[92:95]\nds_read_b64_tr_b16 v[218:219],v227 offset:20480\n" \
"ds_read_b64_tr_b16 v[220:221],v227 offset:24576\nM v[104:107],a[80:83],v[192:195],v[104:107]\nds_read_b64_tr_b16 v[222:223],v227 offset:28672\nds_read_b128 v[16:19],v238 offset:4096\nM v[104:107],a[88:91],v[208:211],v[104:107]\nds_read_b128 v[20:23],v238 offset:4112\nv_and_b32 v228,1,v249\nM v[108:111],a[84:87],v[192:195],v[108:111]\n" \
"ds_read_b64_tr_b16 v[192:193],v226 offset:16896\nv_lshlrev_b32 v228,4,v228\nM v[108:111],a[92:95],v[208:211],v[108:111]\nds_read_b64_tr_b16 v[194:195],v226 offset:20992\ns_waitcnt lgkmcnt(10)\nM v[120:123],a[80:83],v[196:199],v[120:123]\nds_read_b64_tr_b16 v[208:209],v226 offset:25088\nds_read_b64_tr_b16 v[210:211],v226 offset:29184\n" \
"M v[120:123],a[88:91],v[212:215],v[120:123]\nds_read_b128 v[32:35],v238 offset:8192\nds_read_b128 v[36:39],v238 offset:8208\nM v[124:127],a[84:87],v[196:199],v[124:127]\nbuffer_store_dwordx4 v[188:191],v253,s[52:55],s38 offen offset:3072 sc1\nM v[124:127],a[92:95],v[212:215],v[124:127]\nds_read_b64_tr_b16 v[196:197],v227 offset:16896\nds_read_b64_tr_b16 v[198:199],v227 offset:20992\n" \
"M v[8:11],a[96:99],v[200:203],v[8:11]\nds_read_b64_tr_b16 v[212:213],v227 offset:25088\ns_waitcnt vmcnt(16)\nM v[8:11],a[104:107],v[204:207],v[8:11]\nds_read_b64_tr_b16 v[214:215],v227 offset:29184\nds_read_b128 v[48:51],v238 offset:12288\nM v[12:15],a[100:103],v[200:203],v[12:15]\nds_read_b64_tr_b16 v[200:201],v226 offset:17408\n" \
"ds_read_b64_tr_b16 v[202:203],v226 offset:21504\nM v[12:15],a[108:111],v[204:207],v[12:15]\nds_read_b64_tr_b16 v[204:205],v226 offset:25600\ns_waitcnt lgkmcnt(11)\nM v[24:27],a[96:99],v[216:219],v[24:27]\nds_read_b64_tr_b16 v[206:207],v226 offset:29696\nds_read_b128 v[52:55],v238 offset:12304\nM v[24:27],a[104:107],v[220:223],v[24:27]\n" \
"v_mov_b32 v229,0x3f80\nv_lshlrev_b32 v228,v228,v229\nM v[28:31],a[100:103],v[216:219],v[28:31]\nds_read_b64_tr_b16 v[216:217],v227 offset:17408\nds_read_b64_tr_b16 v[218:219],v227 offset:21504\nM v[28:31],a[108:111],v[220:223],v[28:31]\nds_read_b64_tr_b16 v[220:221],v227 offset:25600\nds_read_b64_tr_b16 v[222:223],v227 offset:29696\n" \
"M v[40:43],a[96:99],v[192:195],v[40:43]\nv_cndmask_b32 v228,0,v228,vcc\ns_waitcnt lgkmcnt(10)\nM v[40:43],a[104:107],v[208:211],v[40:43]\nv_and_b32 v229,2,v249\nv_cmp_eq_u32 vcc,0,v229\nM v[44:47],a[100:103],v[192:195],v[44:47]\nds_read_b64_tr_b16 v[192:193],v226 offset:17920\n" \
"ds_read_b64_tr_b16 v[194:195],v226 offset:22016\nM v[44:47],a[108:111],v[208:211],v[44:47]\nds_read_b64_tr_b16 v[208:209],v226 offset:26112\nds_read_b64_tr_b16 v[210:211],v226 offset:30208\nM v[56:59],a[96:99],v[196:199],v[56:59]\nv_cndmask_b32 v229,0,v228,vcc\nv_and_b32 v244,15,v249\nM v[56:59],a[104:107],v[212:215],v[56:59]\n" \
"v_and_b32 v245,0x300,v240\nv_lshlrev_b32 v246,11,v249\nM v[60:63],a[100:103],v[196:199],v[60:63]\nds_read_b64_tr_b16 v[196:197],v227 offset:17920\nds_read_b64_tr_b16 v[198:199],v227 offset:22016\nM v[60:63],a[108:111],v[212:215],v[60:63]\nds_read_b64_tr_b16 v[212:213],v227 offset:26112\ns_waitcnt lgkmcnt(7)\n" \
"M v[72:75],a[96:99],v[200:203],v[72:75]\nds_read_b64_tr_b16 v[214:215],v227 offset:30208\nv_cmp_lt_u32 vcc,7,v249\nM v[72:75],a[104:107],v[204:207],v[72:75]\nv_mov_b32 v230,0x40000000\nv_lshl_or_b32 v244,v244,1,v245\nM v[76:79],a[100:103],v[200:203],v[76:79]\nds_read_b64_tr_b16 v[200:201],v226 offset:49152\n" \
"ds_read_b64_tr_b16 v[202:203],v226 offset:53248\nM v[76:79],a[108:111],v[204:207],v[76:79]\nds_read_b64_tr_b16 v[204:205],v226 offset:57344\nds_read_b64_tr_b16 v[206:207],v226 offset:61440\nM v[88:91],a[96:99],v[216:219],v[88:91]\ns_add_u32 s70,s64,114688\nv_cndmask_b32 v246,v246,v230,vcc\nM v[88:91],a[104:107],v[220:223],v[88:91]\n" \
"v_add_u32 v244,s70,v244\nv_add_u32 v245,s70,v240\nM v[92:95],a[100:103],v[216:219],v[92:95]\nds_read_b64_tr_b16 v[216:217],v227 offset:49152\nds_read_b64_tr_b16 v[218:219],v227 offset:53248\nM v[92:95],a[108:111],v[220:223],v[92:95]\nds_read_b64_tr_b16 v[220:221],v227 offset:57344\ns_waitcnt lgkmcnt(7)\n" \
"M v[104:107],a[96:99],v[192:195],v[104:107]\nds_read_b64_tr_b16 v[222:223],v227 offset:61440\ns_lshl_b32 s80,s60,11\nM v[104:107],a[104:107],v[208:211],v[104:107]\nv_and_b32 v230,3,v249\nv_lshrrev_b32 v231,2,v249\nM v[108:111],a[100:103],v[192:195],v[108:111]\nds_read_b64_tr_b16 v[192:193],v226 offset:49664\n" \
"ds_read_b64_tr_b16 v[194:195],v226 offset:53760\nM v[108:111],a[108:111],v[208:211],v[108:111]\nds_read_b64_tr_b16 v[208:209],v226 offset:57856\nds_read_b64_tr_b16 v[210:211],v226 offset:61952\nM v[120:123],a[96:99],v[196:199],v[120:123]\ns_lshl3_add_u32 s37,s6,s75\ns_add_u32 s80,s80,557056\nM v[120:123],a[104:107],v[212:215],v[120:123]\n" \
"v_lshl_add_u32 v231,v230,5,v231\ns_lshl_b32 s70,s7,8\nM v[124:127],a[100:103],v[196:199],v[124:127]\nds_read_b64_tr_b16 v[196:197],v227 offset:49664\nds_read_b64_tr_b16 v[198:199],v227 offset:53760\nM v[124:127],a[108:111],v[212:215],v[124:127]\nds_read_b64_tr_b16 v[212:213],v227 offset:57856\ns_waitcnt lgkmcnt(7)\n" \
"M v[8:11],a[112:115],v[200:203],v[8:11]\nds_read_b64_tr_b16 v[214:215],v227 offset:61952\ns_waitcnt vmcnt(11)\nM v[8:11],a[120:123],v[204:207],v[8:11]\nv_add_f32 v32,1.0,v32\nv_add_f32 v33,1.0,v33\nM v[12:15],a[116:119],v[200:203],v[12:15]\nds_read_b64_tr_b16 v[200:201],v226 offset:50176\n" \
"ds_read_b64_tr_b16 v[202:203],v226 offset:54272\nM v[12:15],a[124:127],v[204:207],v[12:15]\nds_read_b64_tr_b16 v[204:205],v226 offset:58368\nds_read_b64_tr_b16 v[206:207],v226 offset:62464\nM v[24:27],a[112:115],v[216:219],v[24:27]\nv_add_f32 v34,1.0,v34\nv_add_f32 v35,1.0,v35\nM v[24:27],a[120:123],v[220:223],v[24:27]\n" \
"v_add_f32 v36,1.0,v36\nv_add_f32 v37,1.0,v37\nM v[28:31],a[116:119],v[216:219],v[28:31]\nds_read_b64_tr_b16 v[216:217],v227 offset:50176\nds_read_b64_tr_b16 v[218:219],v227 offset:54272\nM v[28:31],a[124:127],v[220:223],v[28:31]\nds_read_b64_tr_b16 v[220:221],v227 offset:58368\ns_waitcnt lgkmcnt(7)\n" \
"M v[40:43],a[112:115],v[192:195],v[40:43]\nds_read_b64_tr_b16 v[222:223],v227 offset:62464\nv_add_f32 v38,1.0,v38\nM v[40:43],a[120:123],v[208:211],v[40:43]\nv_add_f32 v39,1.0,v39\nv_add_f32 v116,1.0,v116\nM v[44:47],a[116:119],v[192:195],v[44:47]\nds_read_b64_tr_b16 v[192:193],v226 offset:50688\n" \
"ds_read_b64_tr_b16 v[194:195],v226 offset:54784\nM v[44:47],a[124:127],v[208:211],v[44:47]\nds_read_b64_tr_b16 v[208:209],v226 offset:58880\nds_read_b64_tr_b16 v[210:211],v226 offset:62976\nM v[56:59],a[112:115],v[196:199],v[56:59]\nv_add_u32 v226,65536,v242\nv_add_f32 v117,1.0,v117\nM v[56:59],a[120:123],v[212:215],v[56:59]\n" \
"s_add_u32 s37,s37,557056\nv_lshlrev_b32 v251,5,v249\nM v[60:63],a[116:119],v[196:199],v[60:63]\nds_read_b64_tr_b16 v[196:197],v227 offset:50688\nds_read_b64_tr_b16 v[198:199],v227 offset:54784\nM v[60:63],a[124:127],v[212:215],v[60:63]\nds_read_b64_tr_b16 v[212:213],v227 offset:58880\ns_waitcnt lgkmcnt(7)\n" \
"M v[72:75],a[112:115],v[200:203],v[72:75]\nds_read_b64_tr_b16 v[214:215],v227 offset:62976\nv_xor_b32 v227,32,v226\nM v[72:75],a[120:123],v[204:207],v[72:75]\ns_or_b32 s80,s80,s75\nv_lshl_add_u32 v248,v231,1,s70\nM v[76:79],a[116:119],v[200:203],v[76:79]\nds_read_b64_tr_b16 v[200:201],v226 offset:0\n" \
"ds_read_b64_tr_b16 v[202:203],v226 offset:4096\nM v[76:79],a[124:127],v[204:207],v[76:79]\nds_read_b64_tr_b16 v[204:205],v226 offset:8192\nds_read_b64_tr_b16 v[206:207],v226 offset:12288\nM v[88:91],a[112:115],v[216:219],v[88:91]\nv_lshlrev_b32 v64,16,v64\nv_lshlrev_b32 v65,16,v65\nM v[88:91],a[120:123],v[220:223],v[88:91]\n" \
"v_lshlrev_b32 v66,16,v66\nv_lshlrev_b32 v67,16,v67\nM v[92:95],a[116:119],v[216:219],v[92:95]\nds_read_b64_tr_b16 v[216:217],v227 offset:0\nds_read_b64_tr_b16 v[218:219],v227 offset:4096\nM v[92:95],a[124:127],v[220:223],v[92:95]\nds_read_b64_tr_b16 v[220:221],v227 offset:8192\ns_waitcnt lgkmcnt(7)\n" \
"M v[104:107],a[112:115],v[192:195],v[104:107]\nds_read_b64_tr_b16 v[222:223],v227 offset:12288\nv_lshlrev_b32 v68,16,v68\nM v[104:107],a[120:123],v[208:211],v[104:107]\nv_lshlrev_b32 v69,16,v69\nv_lshlrev_b32 v70,16,v70\nM v[108:111],a[116:119],v[192:195],v[108:111]\nds_read_b64_tr_b16 v[192:193],v226 offset:512\n" \
"ds_read_b64_tr_b16 v[194:195],v226 offset:4608\nM v[108:111],a[124:127],v[208:211],v[108:111]\nds_read_b64_tr_b16 v[208:209],v226 offset:8704\nds_read_b64_tr_b16 v[210:211],v226 offset:12800\nM v[120:123],a[112:115],v[196:199],v[120:123]\nv_lshlrev_b32 v71,16,v71\nv_lshlrev_b32 v80,16,v80\nM v[120:123],a[120:123],v[212:215],v[120:123]\n" \
"v_lshlrev_b32 v81,16,v81\nv_lshlrev_b32 v82,16,v82\nM v[124:127],a[116:119],v[196:199],v[124:127]\nds_read_b64_tr_b16 v[196:197],v227 offset:512\nds_read_b64_tr_b16 v[198:199],v227 offset:4608\nM v[124:127],a[124:127],v[212:215],v[124:127]\nds_read_b64_tr_b16 v[212:213],v227 offset:8704\ns_waitcnt lgkmcnt(7)\n" \
"M v[8:11],a[128:131],v[200:203],v[8:11]\nds_read_b64_tr_b16 v[214:215],v227 offset:12800\ns_waitcnt vmcnt(6)\nM v[8:11],a[136:139],v[204:207],v[8:11]\nv_lshlrev_b32 v83,16,v83\nv_lshlrev_b32 v84,16,v84\nM v[12:15],a[132:135],v[200:203],v[12:15]\nds_read_b64_tr_b16 v[200:201],v226 offset:1024\n" \
"ds_read_b64_tr_b16 v[202:203],v226 offset:5120\nM v[12:15],a[140:143],v[204:207],v[12:15]\nds_read_b64_tr_b16 v[204:205],v226 offset:9216\nds_read_b64_tr_b16 v[206:207],v226 offset:13312\nM v[24:27],a[128:131],v[216:219],v[24:27]\nv_lshlrev_b32 v85,16,v85\nv_lshlrev_b32 v86,16,v86\nM v[24:27],a[136:139],v[220:223],v[24:27]\n" \
"v_lshlrev_b32 v87,16,v87\nv_lshlrev_b32 v96,16,v96\nM v[28:31],a[132:135],v[216:219],v[28:31]\nds_read_b64_tr_b16 v[216:217],v227 offset:1024\nds_read_b64_tr_b16 v[218:219],v227 offset:5120\nM v[28:31],a[140:143],v[220:223],v[28:31]\nds_read_b64_tr_b16 v[220:221],v227 offset:9216\ns_waitcnt lgkmcnt(7)\n" \
"M v[40:43],a[128:131],v[192:195],v[40:43]\nds_read_b64_tr_b16 v[222:223],v227 offset:13312\nv_lshlrev_b32 v97,16,v97\nM v[40:43],a[136:139],v[208:211],v[40:43]\nv_lshlrev_b32 v98,16,v98\nv_lshlrev_b32 v99,16,v99\nM v[44:47],a[132:135],v[192:195],v[44:47]\nds_read_b64_tr_b16 v[192:193],v226 offset:1536\n" \
"ds_read_b64_tr_b16 v[194:195],v226 offset:5632\nM v[44:47],a[140:143],v[208:211],v[44:47]\nds_read_b64_tr_b16 v[208:209],v226 offset:9728\nds_read_b64_tr_b16 v[210:211],v226 offset:13824\nM v[56:59],a[128:131],v[196:199],v[56:59]\nv_lshlrev_b32 v100,16,v100\nv_lshlrev_b32 v101,16,v101\nM v[56:59],a[136:139],v[212:215],v[56:59]\n" \
"v_lshlrev_b32 v102,16,v102\nv_lshlrev_b32 v103,16,v103\nM v[60:63],a[132:135],v[196:199],v[60:63]\nds_read_b64_tr_b16 v[196:197],v227 offset:1536\nds_read_b64_tr_b16 v[198:199],v227 offset:5632\nM v[60:63],a[140:143],v[212:215],v[60:63]\nds_read_b64_tr_b16 v[212:213],v227 offset:9728\ns_waitcnt lgkmcnt(7)\n" \
"M v[72:75],a[128:131],v[200:203],v[72:75]\nds_read_b64_tr_b16 v[214:215],v227 offset:13824\nv_cvt_pk_bf16_f32 v32,0,v32\nM v[72:75],a[136:139],v[204:207],v[72:75]\nv_cvt_pk_bf16_f32 v33,0,v33\nv_cvt_pk_bf16_f32 v34,0,v34\nM v[76:79],a[132:135],v[200:203],v[76:79]\nds_read_b64_tr_b16 v[200:201],v226 offset:32768\n" \
"ds_read_b64_tr_b16 v[202:203],v226 offset:36864\nM v[76:79],a[140:143],v[204:207],v[76:79]\nds_read_b64_tr_b16 v[204:205],v226 offset:40960\nds_read_b64_tr_b16 v[206:207],v226 offset:45056\nM v[88:91],a[128:131],v[216:219],v[88:91]\nv_cvt_pk_bf16_f32 v35,0,v35\nv_cvt_pk_bf16_f32 v36,0,v36\nM v[88:91],a[136:139],v[220:223],v[88:91]\n" \
"v_cvt_pk_bf16_f32 v37,0,v37\nv_cvt_pk_bf16_f32 v38,0,v38\nM v[92:95],a[132:135],v[216:219],v[92:95]\nds_read_b64_tr_b16 v[216:217],v227 offset:32768\nds_read_b64_tr_b16 v[218:219],v227 offset:36864\nM v[92:95],a[140:143],v[220:223],v[92:95]\nds_read_b64_tr_b16 v[220:221],v227 offset:40960\ns_waitcnt lgkmcnt(7)\n" \
"M v[104:107],a[128:131],v[192:195],v[104:107]\nds_read_b64_tr_b16 v[222:223],v227 offset:45056\nv_cvt_pk_bf16_f32 v39,0,v39\nM v[104:107],a[136:139],v[208:211],v[104:107]\nv_cvt_pk_bf16_f32 v116,0,v116\nv_cvt_pk_bf16_f32 v117,0,v117\nM v[108:111],a[132:135],v[192:195],v[108:111]\nds_read_b64_tr_b16 v[192:193],v226 offset:33280\n" \
"ds_read_b64_tr_b16 v[194:195],v226 offset:37376\nM v[108:111],a[140:143],v[208:211],v[108:111]\nds_read_b64_tr_b16 v[208:209],v226 offset:41472\nds_read_b64_tr_b16 v[210:211],v226 offset:45568\nM v[120:123],a[128:131],v[196:199],v[120:123]\nv_xor_b32 v228,v228,v229\ns_add_u32 s38,s78,16384\nM v[120:123],a[136:139],v[212:215],v[120:123]\n" \
"v_accvgpr_write_b32 a192,v229\nv_accvgpr_write_b32 a193,v228\nM v[124:127],a[132:135],v[196:199],v[124:127]\nds_read_b64_tr_b16 v[196:197],v227 offset:33280\nds_read_b64_tr_b16 v[198:199],v227 offset:37376\nM v[124:127],a[140:143],v[212:215],v[124:127]\nds_read_b64_tr_b16 v[212:213],v227 offset:41472\ns_waitcnt lgkmcnt(7)\n" \
"M v[8:11],a[144:147],v[200:203],v[8:11]\nds_read_b64_tr_b16 v[214:215],v227 offset:45568\ns_waitcnt vmcnt(3)\nM v[8:11],a[152:155],v[204:207],v[8:11]\nv_accvgpr_write_b32 a198,v229\nv_accvgpr_write_b32 a199,v228\nM v[12:15],a[148:151],v[200:203],v[12:15]\nds_read_b64_tr_b16 v[200:201],v226 offset:33792\n" \
"ds_read_b64_tr_b16 v[202:203],v226 offset:37888\nM v[12:15],a[156:159],v[204:207],v[12:15]\nds_read_b64_tr_b16 v[204:205],v226 offset:41984\nds_read_b64_tr_b16 v[206:207],v226 offset:46080\nM v[24:27],a[144:147],v[216:219],v[24:27]\nM v[24:27],a[152:155],v[220:223],v[24:27]\nM v[28:31],a[148:151],v[216:219],v[28:31]\nds_read_b64_tr_b16 v[216:217],v227 offset:33792\n" \
"ds_read_b64_tr_b16 v[218:219],v227 offset:37888\nM v[28:31],a[156:159],v[220:223],v[28:31]\nds_read_b64_tr_b16 v[220:221],v227 offset:41984\ns_waitcnt lgkmcnt(8)\nM v[40:43],a[144:147],v[192:195],v[40:43]\nds_read_b64_tr_b16 v[222:223],v227 offset:46080\nv_cvt_pk_bf16_f32 v8,v8,v24\nM v[40:43],a[152:155],v[208:211],v[40:43]\n" \
"v_cvt_pk_bf16_f32 v9,v9,v25\nv_cvt_pk_bf16_f32 v10,v10,v26\nM v[44:47],a[148:151],v[192:195],v[44:47]\nds_read_b64_tr_b16 v[192:193],v226 offset:34304\nds_read_b64_tr_b16 v[194:195],v226 offset:38400\nM v[44:47],a[156:159],v[208:211],v[44:47]\nds_read_b64_tr_b16 v[208:209],v226 offset:42496\nds_read_b64_tr_b16 v[210:211],v226 offset:46592\n" \
"M v[56:59],a[144:147],v[196:199],v[56:59]\nv_cvt_pk_bf16_f32 v11,v11,v27\ns_waitcnt lgkmcnt(8)\nM v[56:59],a[152:155],v[212:215],v[56:59]\nv_cvt_pk_bf16_f32 v12,v12,v28\nv_cvt_pk_bf16_f32 v13,v13,v29\nM v[60:63],a[148:151],v[196:199],v[60:63]\nds_read_b64_tr_b16 v[196:197],v227 offset:34304\n" \
"ds_read_b64_tr_b16 v[198:199],v227 offset:38400\nM v[60:63],a[156:159],v[212:215],v[60:63]\nds_read_b64_tr_b16 v[212:213],v227 offset:42496\nds_read_b64_tr_b16 v[214:215],v227 offset:46592\nM v[72:75],a[144:147],v[200:203],v[72:75]\nv_cvt_pk_bf16_f32 v40,v40,v56\nv_cvt_pk_bf16_f32 v41,v41,v57\nM v[72:75],a[152:155],v[204:207],v[72:75]\n" \
"v_cvt_pk_bf16_f32 v42,v42,v58\nv_cvt_pk_bf16_f32 v43,v43,v59\nM v[76:79],a[148:151],v[200:203],v[76:79]\nv_cvt_pk_bf16_f32 v14,v14,v30\nv_cvt_pk_bf16_f32 v15,v15,v31\nM v[76:79],a[156:159],v[204:207],v[76:79]\nv_cvt_pk_bf16_f32 v44,v44,v60\ns_waitcnt lgkmcnt(4)\n" \
"M v[88:91],a[144:147],v[216:219],v[88:91]\nv_cvt_pk_bf16_f32 v45,v45,v61\nv_cvt_pk_bf16_f32 v46,v46,v62\nM v[88:91],a[152:155],v[220:223],v[88:91]\nv_cvt_pk_bf16_f32 v47,v47,v63\nv_min_u32 v24,-2,v8\nM v[92:95],a[148:151],v[216:219],v[92:95]\nv_min_u32 v25,-2,v40\n" \
"v_min_u32 v56,-2,v9\nM v[92:95],a[156:159],v[220:223],v[92:95]\nv_min_u32 v57,-2,v41\nv_min_u32 v28,-2,v12\nM v[104:107],a[144:147],v[192:195],v[104:107]\nv_cvt_pk_bf16_f32 v72,v72,v88\nv_cvt_pk_bf16_f32 v73,v73,v89\nM v[104:107],a[152:155],v[208:211],v[104:107]\n" \
"v_cvt_pk_bf16_f32 v74,v74,v90\nv_cvt_pk_bf16_f32 v75,v75,v91\nM v[108:111],a[148:151],v[192:195],v[108:111]\nv_cvt_pk_bf16_f32 v76,v76,v92\nv_cvt_pk_bf16_f32 v77,v77,v93\nM v[108:111],a[156:159],v[208:211],v[108:111]\nv_cvt_pk_bf16_f32 v78,v78,v94\ns_waitcnt lgkmcnt(0)\n" \
"M v[120:123],a[144:147],v[196:199],v[120:123]\nv_cvt_pk_bf16_f32 v79,v79,v95\nv_min_u32 v26,-2,v72\nM v[120:123],a[152:155],v[212:215],v[120:123]\nv_min_u32 v58,-2,v73\nv_min_u32 v88,-2,v10\nM v[124:127],a[148:151],v[196:199],v[124:127]\nv_min_u32 v89,-2,v42\n" \
"v_min_u32 v90,-2,v74\nM v[124:127],a[156:159],v[212:215],v[124:127]\nv_min_u32 v29,-2,v44\nv_min_u32 v30,-2,v76\nv_cvt_pk_bf16_f32 v104,v104,v120\nv_cvt_pk_bf16_f32 v105,v105,v121\nv_cvt_pk_bf16_f32 v106,v106,v122\nv_cvt_pk_bf16_f32 v107,v107,v123\n" \
"v_min_u32 v27,-2,v104\nv_min_u32 v59,-2,v105\nv_cvt_pk_bf16_f32 v108,v108,v124\nv_cvt_pk_bf16_f32 v109,v109,v125\nv_cvt_pk_bf16_f32 v110,v110,v126\nv_cvt_pk_bf16_f32 v111,v111,v127\nv_min_u32 v91,-2,v106\nv_min_u32 v120,-2,v11\n" \
"v_min_u32 v121,-2,v43\nv_min_u32 v122,-2,v75\nv_min_u32 v123,-2,v107\nv_min_u32 v31,-2,v108\nv_min_u32 v60,-2,v13\nv_min_u32 v61,-2,v45\nv_min_u32 v62,-2,v77\nv_min_u32 v63,-2,v109\n" \
"v_min_u32 v92,-2,v14\nv_min_u32 v93,-2,v46\nv_min_u32 v94,-2,v78\nv_min_u32 v95,-2,v110\nv_min_u32 v124,-2,v15\nv_min_u32 v125,-2,v47\nv_min_u32 v126,-2,v79\nv_min_u32 v127,-2,v111\n" \
"buffer_store_dwordx4 v[24:27],v253,s[52:55],s78 offen offset:0 sc1\nbuffer_store_dwordx4 v[56:59],v253,s[52:55],s78 offen offset:1024 sc1\nbuffer_store_dwordx4 v[88:91],v253,s[52:55],s78 offen offset:2048 sc1\nbuffer_store_dwordx4 v[120:123],v253,s[52:55],s78 offen offset:3072 sc1\nbuffer_store_dwordx4 v[28:31],v253,s[52:55],s38 offen offset:0 sc1\nbuffer_store_dwordx4 v[60:63],v253,s[52:55],s38 offen offset:1024 sc1\nbuffer_store_dwordx4 v[92:95],v253,s[52:55],s38 offen offset:2048 sc1\nbuffer_store_dwordx4 v[124:127],v253,s[52:55],s38 offen offset:3072 sc1\n" \
".Ltail:\ns_add_u32 s72,s95,0\nbuffer_load_dwordx4 v[160:163],v240,s[52:55],s72 offen sc1\ns_add_u32 s72,s95,2097152\nbuffer_load_dwordx4 v[164:167],v240,s[52:55],s72 offen sc1\ns_add_u32 s72,s95,4194304\nbuffer_load_dwordx4 v[168:171],v240,s[52:55],s72 offen sc1\ns_add_u32 s72,s95,6291456\n" \
"buffer_load_dwordx4 v[172:175],v240,s[52:55],s72 offen sc1\ns_add_u32 s72,s95,8388608\nbuffer_load_dwordx4 v[176:179],v240,s[52:55],s72 offen sc1\ns_add_u32 s72,s95,10485760\nbuffer_load_dwordx4 v[180:183],v240,s[52:55],s72 offen sc1\ns_add_u32 s72,s95,12582912\nbuffer_load_dwordx4 v[184:187],v240,s[52:55],s72 offen sc1\ns_add_u32 s72,s95,14680064\n" \
"buffer_load_dwordx4 v[188:191],v240,s[52:55],s72 offen sc1\n.Ltail2:\ns_add_u32 s72,s74,0\nbuffer_load_dwordx4 v[128:131],v240,s[52:55],s72 offen sc1\ns_add_u32 s72,s74,2097152\nbuffer_load_dwordx4 v[132:135],v240,s[52:55],s72 offen sc1\ns_add_u32 s72,s74,4194304\nbuffer_load_dwordx4 v[136:139],v240,s[52:55],s72 offen sc1\n" \
"s_add_u32 s72,s74,6291456\nbuffer_load_dwordx4 v[140:143],v240,s[52:55],s72 offen sc1\ns_add_u32 s72,s74,8388608\nbuffer_load_dwordx4 v[144:147],v240,s[52:55],s72 offen sc1\ns_add_u32 s72,s74,10485760\nbuffer_load_dwordx4 v[148:151],v240,s[52:55],s72 offen sc1\ns_add_u32 s72,s74,12582912\nbuffer_load_dwordx4 v[152:155],v240,s[52:55],s72 offen sc1\n" \
"s_add_u32 s72,s74,14680064\nbuffer_load_dwordx4 v[156:159],v240,s[52:55],s72 offen sc1\ns_mov_b32 s94,0\ns_mov_b32 s29,1\ns_cmp_eq_u32 s3,0\ns_cbranch_scc1 .Lnp\ns_mov_b32 s70,0x05040100\ns_mov_b32 s71,0x07060302\n" \
"v_accvgpr_read_b32 v228,a212\nv_accvgpr_read_b32 v229,a213\nv_accvgpr_read_b32 v230,a214\nv_accvgpr_read_b32 v231,a215\nv_perm_b32 v192,v230,v228,s70\nv_perm_b32 v193,v230,v228,s71\nv_perm_b32 v194,v231,v229,s70\nv_perm_b32 v195,v231,v229,s71\n" \
"v_accvgpr_read_b32 v228,a216\nv_accvgpr_read_b32 v229,a217\nv_accvgpr_read_b32 v230,a218\nv_accvgpr_read_b32 v231,a219\nv_perm_b32 v196,v230,v228,s70\nv_perm_b32 v197,v230,v228,s71\nv_perm_b32 v198,v231,v229,s70\nv_perm_b32 v199,v231,v229,s71\n" \
"v_accvgpr_read_b32 v228,a220\nv_accvgpr_read_b32 v229,a221\nv_accvgpr_read_b32 v230,a222\nv_accvgpr_read_b32 v231,a223\nv_perm_b32 v200,v230,v228,s70\nv_perm_b32 v201,v230,v228,s71\nv_perm_b32 v202,v231,v229,s70\nv_perm_b32 v203,v231,v229,s71\n" \
"v_accvgpr_read_b32 v228,a224\nv_accvgpr_read_b32 v229,a225\nv_accvgpr_read_b32 v230,a226\nv_accvgpr_read_b32 v231,a227\nv_perm_b32 v204,v230,v228,s70\nv_perm_b32 v205,v230,v228,s71\nv_perm_b32 v206,v231,v229,s70\nv_perm_b32 v207,v231,v229,s71\n" \
"v_accvgpr_read_b32 v228,a228\nv_accvgpr_read_b32 v229,a229\nv_accvgpr_read_b32 v230,a230\nv_accvgpr_read_b32 v231,a231\nv_perm_b32 v208,v230,v228,s70\nv_perm_b32 v209,v230,v228,s71\nv_perm_b32 v210,v231,v229,s70\nv_perm_b32 v211,v231,v229,s71\n" \
"v_accvgpr_read_b32 v228,a232\nv_accvgpr_read_b32 v229,a233\nv_accvgpr_read_b32 v230,a234\nv_accvgpr_read_b32 v231,a235\nv_perm_b32 v212,v230,v228,s70\nv_perm_b32 v213,v230,v228,s71\nv_perm_b32 v214,v231,v229,s70\nv_perm_b32 v215,v231,v229,s71\n" \
"v_accvgpr_read_b32 v228,a236\nv_accvgpr_read_b32 v229,a237\nv_accvgpr_read_b32 v230,a238\nv_accvgpr_read_b32 v231,a239\nv_perm_b32 v216,v230,v228,s70\nv_perm_b32 v217,v230,v228,s71\nv_perm_b32 v218,v231,v229,s70\nv_perm_b32 v219,v231,v229,s71\n" \
"v_accvgpr_read_b32 v228,a240\nv_accvgpr_read_b32 v229,a241\nv_accvgpr_read_b32 v230,a242\nv_accvgpr_read_b32 v231,a243\nv_perm_b32 v220,v230,v228,s70\nv_perm_b32 v221,v230,v228,s71\nv_perm_b32 v222,v231,v229,s70\nv_perm_b32 v223,v231,v229,s71\n" \
"v_and_b32 v228,3,v249\nv_lshrrev_b32 v229,4,v249\nv_and_b32 v230,1,v229\nv_lshrrev_b32 v229,1,v229\nv_lshlrev_b32 v230,5,v230\nv_lshl_or_b32 v230,v228,6,v230\nv_lshl_or_b32 v230,v229,8,v230\ns_lshl2_add_u32 s70,s7,83008\n" \
"v_add_u32 v252,s70,v230\nv_and_b32 v230,0xf0,v240\nv_add_u32 v253,83008,v230\n.Lnp:\n.Lrow1:\ns_waitcnt vmcnt(15)\nM v[72:75],a[192:195],v[160:163],0\nM v[76:79],a[196:199],v[160:163],0\n" \
"s_waitcnt vmcnt(14)\nM v[72:75],a[192:195],v[164:167],v[72:75]\nM v[76:79],a[196:199],v[164:167],v[76:79]\ns_waitcnt vmcnt(13)\nM v[72:75],a[192:195],v[168:171],v[72:75]\nM v[76:79],a[196:199],v[168:171],v[76:79]\ns_waitcnt vmcnt(12)\nM v[72:75],a[192:195],v[172:175],v[72:75]\n" \
"M v[76:79],a[196:199],v[172:175],v[76:79]\ns_waitcnt vmcnt(11)\nM v[72:75],a[192:195],v[176:179],v[72:75]\nM v[76:79],a[196:199],v[176:179],v[76:79]\ns_waitcnt vmcnt(10)\nM v[72:75],a[192:195],v[180:183],v[72:75]\nM v[76:79],a[196:199],v[180:183],v[76:79]\ns_waitcnt vmcnt(9)\n" \
"M v[72:75],a[192:195],v[184:187],v[72:75]\nM v[76:79],a[196:199],v[184:187],v[76:79]\ns_waitcnt vmcnt(8)\nM v[72:75],a[192:195],v[188:191],v[72:75]\nM v[76:79],a[196:199],v[188:191],v[76:79]\ns_sub_u32 s38,56622112,s95\ns_lshr_b32 s38,s38,5\ns_nop 4\n" \
"v_cvt_pk_bf16_f32 v72,0,v72\nv_cvt_pk_bf16_f32 v73,0,v73\nv_cvt_pk_bf16_f32 v74,0,v74\nv_cvt_pk_bf16_f32 v75,0,v75\nv_cvt_pk_bf16_f32 v76,0,v76\nv_cvt_pk_bf16_f32 v77,0,v77\nv_cvt_pk_bf16_f32 v78,0,v78\nv_cvt_pk_bf16_f32 v79,0,v79\n" \
"v_pk_mul_f32 v[72:73],v[72:73],v[0:1]\nv_pk_mul_f32 v[74:75],v[74:75],v[2:3]\nv_pk_mul_f32 v[76:77],v[76:77],v[4:5]\nv_pk_mul_f32 v[78:79],v[78:79],v[6:7]\nv_cvt_pk_bf16_f32 v72,0,v72\nv_cvt_pk_bf16_f32 v73,0,v73\nv_cvt_pk_bf16_f32 v74,0,v74\nv_cvt_pk_bf16_f32 v75,0,v75\n" \
"v_cvt_pk_bf16_f32 v76,0,v76\nv_cvt_pk_bf16_f32 v77,0,v77\nv_cvt_pk_bf16_f32 v78,0,v78\nv_cvt_pk_bf16_f32 v79,0,v79\nv_pk_add_f32 v[72:73],v[72:73],v[64:65]\nv_pk_add_f32 v[74:75],v[74:75],v[66:67]\nv_pk_add_f32 v[76:77],v[76:77],v[68:69]\nv_pk_add_f32 v[78:79],v[78:79],v[70:71]\n" \
"v_cvt_pk_bf16_f32 v72,0,v72\nv_cvt_pk_bf16_f32 v73,0,v73\nv_cvt_pk_bf16_f32 v74,0,v74\nv_cvt_pk_bf16_f32 v75,0,v75\nv_cvt_pk_bf16_f32 v76,0,v76\nv_cvt_pk_bf16_f32 v77,0,v77\nv_cvt_pk_bf16_f32 v78,0,v78\nv_cvt_pk_bf16_f32 v79,0,v79\n" \
"v_readfirstlane_b32 s70,v72\ns_nop 1\nv_subrev_f32 v104,s70,v72\nv_subrev_f32 v105,s70,v73\nv_subrev_f32 v106,s70,v74\nv_subrev_f32 v107,s70,v75\nv_subrev_f32 v108,s70,v76\nv_subrev_f32 v109,s70,v77\n" \
"v_subrev_f32 v110,s70,v78\nv_subrev_f32 v111,s70,v79\nv_pk_mul_f32 v[124:125],v[104:105],v[104:105]\nv_pk_add_f32 v[120:121],v[104:105],v[106:107]\nv_pk_fma_f32 v[124:125],v[106:107],v[106:107],v[124:125]\nv_pk_fma_f32 v[124:125],v[108:109],v[108:109],v[124:125]\nv_pk_fma_f32 v[124:125],v[110:111],v[110:111],v[124:125]\nv_pk_add_f32 v[108:109],v[108:109],v[110:111]\n" \
"v_pk_add_f32 v[120:121],v[120:121],v[108:109]\nv_add_f32 v120,v120,v121\nv_add_f32 v121,v124,v125\ns_nop 1\nv_permlane16_swap_b32 v120,v121\nv_add_f32 v120,v120,v121\nv_mov_b32 v121,v120\ns_nop 1\n" \
"v_permlane32_swap_b32 v120,v121\nv_add_f32 v120,v120,v121\ns_nop 1\nv_add_f32_dpp v120,v120,v120 quad_perm:[1,0,3,2] row_mask:0xf bank_mask:0xf\ns_nop 1\nv_add_f32_dpp v120,v120,v120 quad_perm:[2,3,0,1] row_mask:0xf bank_mask:0xf\ns_nop 1\nv_add_f32_dpp v120,v120,v120 row_half_mirror row_mask:0xf bank_mask:0xf\n" \
"s_nop 1\nv_add_f32_dpp v120,v120,v120 row_mirror row_mask:0xf bank_mask:0xf\nv_mul_f32 v120,0x3b000000,v120\nv_mov_b32 v121,v120\nv_cmp_u_f32 vcc,v120,v120\ns_nop 0\nv_permlane16_swap_b32 v120,v121\ns_cbranch_vccnz .Lrs1\n" \
".Lrb1:\nv_fma_f32 v121,-v120,v120,v121\nv_add_f32 v122,s70,v120\nv_max_f32 v121,0,v121\nv_cvt_pk_bf16_f32 v122,0,v122\nv_cvt_pk_bf16_f32 v121,0,v121\nv_add_f32 v121,0x3727c5ac,v121\nv_cvt_pk_bf16_f32 v121,0,v121\n" \
"v_sqrt_f32 v121,v121\nv_pk_add_f32 v[88:89],v[72:73],v[122:123] op_sel_hi:[1,0] neg_lo:[0,1] neg_hi:[0,1]\nv_cvt_pk_bf16_f32 v121,0,v121\nv_rcp_f32 v123,v121\nv_cvt_pk_bf16_f32 v88,0,v88\nv_pk_add_f32 v[90:91],v[74:75],v[122:123] op_sel_hi:[1,0] neg_lo:[0,1] neg_hi:[0,1]\nv_pk_add_f32 v[92:93],v[76:77],v[122:123] op_sel_hi:[1,0] neg_lo:[0,1] neg_hi:[0,1]\nv_pk_add_f32 v[94:95],v[78:79],v[122:123] op_sel_hi:[1,0] neg_lo:[0,1] neg_hi:[0,1]\n" \
"v_cvt_pk_bf16_f32 v89,0,v89\nv_pk_mul_f32 v[88:89],v[88:89],v[122:123] op_sel:[0,1]\nv_cvt_pk_bf16_f32 v88,0,v88\nv_cvt_pk_bf16_f32 v89,0,v89\nv_pk_mul_f32 v[88:89],v[88:89],v[32:33]\nv_cvt_pk_bf16_f32 v88,0,v88\nv_cvt_pk_bf16_f32 v89,0,v89\nv_pk_add_f32 v[88:89],v[88:89],v[16:17]\n" \
"v_cvt_pk_bf16_f32 v88,0,v88\nv_cvt_pk_bf16_f32 v89,0,v89\nv_pk_mul_f32 v[124:125],v[88:89],v[96:97]\nv_cvt_pk_bf16_f32 v90,0,v90\nv_cvt_pk_bf16_f32 v91,0,v91\nv_pk_mul_f32 v[90:91],v[90:91],v[122:123] op_sel:[0,1]\nv_cvt_pk_bf16_f32 v90,0,v90\nv_cvt_pk_bf16_f32 v91,0,v91\n" \
"v_pk_mul_f32 v[90:91],v[90:91],v[34:35]\nv_cvt_pk_bf16_f32 v90,0,v90\nv_cvt_pk_bf16_f32 v91,0,v91\nv_pk_add_f32 v[90:91],v[90:91],v[18:19]\nv_cvt_pk_bf16_f32 v90,0,v90\nv_cvt_pk_bf16_f32 v91,0,v91\nv_pk_fma_f32 v[124:125],v[90:91],v[98:99],v[124:125]\nv_cvt_pk_bf16_f32 v92,0,v92\n" \
"v_cvt_pk_bf16_f32 v93,0,v93\nv_pk_mul_f32 v[92:93],v[92:93],v[122:123] op_sel:[0,1]\nv_cvt_pk_bf16_f32 v92,0,v92\nv_cvt_pk_bf16_f32 v93,0,v93\nv_pk_mul_f32 v[92:93],v[92:93],v[36:37]\nv_cvt_pk_bf16_f32 v92,0,v92\nv_cvt_pk_bf16_f32 v93,0,v93\nv_pk_add_f32 v[92:93],v[92:93],v[20:21]\n" \
"v_cvt_pk_bf16_f32 v92,0,v92\nv_cvt_pk_bf16_f32 v93,0,v93\nv_pk_fma_f32 v[124:125],v[92:93],v[100:101],v[124:125]\nv_cvt_pk_bf16_f32 v94,0,v94\nv_cvt_pk_bf16_f32 v95,0,v95\nv_pk_mul_f32 v[94:95],v[94:95],v[122:123] op_sel:[0,1]\nv_cvt_pk_bf16_f32 v94,0,v94\nv_cvt_pk_bf16_f32 v95,0,v95\n" \
"v_pk_mul_f32 v[94:95],v[94:95],v[38:39]\nv_cvt_pk_bf16_f32 v94,0,v94\nv_cvt_pk_bf16_f32 v95,0,v95\nv_pk_add_f32 v[94:95],v[94:95],v[22:23]\nv_cvt_pk_bf16_f32 v94,0,v94\nv_cvt_pk_bf16_f32 v95,0,v95\nv_pk_fma_f32 v[124:125],v[94:95],v[102:103],v[124:125]\nv_add_f32 v124,v124,v125\n" \
"v_mov_b32 v125,v124\nv_lshrrev_b32 v104,16,v121\nv_or_b32 v104,v104,v122\nv_permlane16_swap_b32 v124,v125\nv_add_f32 v124,v124,v125\nv_mov_b32 v125,v124\ns_nop 1\nv_permlane32_swap_b32 v124,v125\n" \
"v_add_f32 v124,v124,v125\ns_nop 1\nv_add_f32_dpp v124,v124,v124 quad_perm:[1,0,3,2] row_mask:0xf bank_mask:0xf\ns_nop 1\nv_add_f32_dpp v124,v124,v124 quad_perm:[2,3,0,1] row_mask:0xf bank_mask:0xf\ns_nop 1\nv_add_f32_dpp v124,v124,v124 row_half_mirror row_mask:0xf bank_mask:0xf\ns_nop 1\n" \
"v_add_f32_dpp v124,v124,v124 row_mirror row_mask:0xf bank_mask:0xf\nv_cvt_pk_bf16_f32 v124,0,v124\ns_nop 0\nv_readfirstlane_b32 s77,v124\ns_nop 0\ns_ashr_i32 s71,s77,31\ns_and_b32 s72,s77,0x7fffffff\ns_or_b32 s71,s71,0x80000000\n" \
"s_xor_b32 s77,s77,s71\ns_and_b32 s77,s77,0xffff0000\ns_cmp_gt_u32 s72,0x7f800000\ns_cselect_b32 s77,0,s77\ns_or_b32 s77,s77,s38\nv_mov_b32 v105,s77\nds_max_u64 v250,v[104:105]\n.Lrow0:\n" \
"s_waitcnt vmcnt(7)\nM v[8:11],a[192:195],v[128:131],0\nM v[12:15],a[196:199],v[128:131],0\ns_waitcnt vmcnt(6)\nM v[8:11],a[192:195],v[132:135],v[8:11]\nM v[12:15],a[196:199],v[132:135],v[12:15]\ns_waitcnt vmcnt(5)\nM v[8:11],a[192:195],v[136:139],v[8:11]\n" \
"M v[12:15],a[196:199],v[136:139],v[12:15]\ns_waitcnt vmcnt(4)\nM v[8:11],a[192:195],v[140:143],v[8:11]\nM v[12:15],a[196:199],v[140:143],v[12:15]\ns_waitcnt vmcnt(3)\nM v[8:11],a[192:195],v[144:147],v[8:11]\nM v[12:15],a[196:199],v[144:147],v[12:15]\ns_waitcnt vmcnt(2)\n" \
"M v[8:11],a[192:195],v[148:151],v[8:11]\nM v[12:15],a[196:199],v[148:151],v[12:15]\ns_waitcnt vmcnt(1)\nM v[8:11],a[192:195],v[152:155],v[8:11]\nM v[12:15],a[196:199],v[152:155],v[12:15]\ns_waitcnt vmcnt(0)\nM v[8:11],a[192:195],v[156:159],v[8:11]\nM v[12:15],a[196:199],v[156:159],v[12:15]\n" \
"s_sub_u32 s39,56622112,s74\ns_lshr_b32 s39,s39,5\ns_nop 4\nv_cvt_pk_bf16_f32 v8,0,v8\nv_cvt_pk_bf16_f32 v9,0,v9\nv_cvt_pk_bf16_f32 v10,0,v10\nv_cvt_pk_bf16_f32 v11,0,v11\nv_cvt_pk_bf16_f32 v12,0,v12\n" \
"v_cvt_pk_bf16_f32 v13,0,v13\nv_cvt_pk_bf16_f32 v14,0,v14\nv_cvt_pk_bf16_f32 v15,0,v15\nv_pk_mul_f32 v[8:9],v[8:9],v[0:1]\nv_pk_mul_f32 v[10:11],v[10:11],v[2:3]\nv_pk_mul_f32 v[12:13],v[12:13],v[4:5]\nv_pk_mul_f32 v[14:15],v[14:15],v[6:7]\nv_cvt_pk_bf16_f32 v8,0,v8\n" \
"v_cvt_pk_bf16_f32 v9,0,v9\nv_cvt_pk_bf16_f32 v10,0,v10\nv_cvt_pk_bf16_f32 v11,0,v11\nv_cvt_pk_bf16_f32 v12,0,v12\nv_cvt_pk_bf16_f32 v13,0,v13\nv_cvt_pk_bf16_f32 v14,0,v14\nv_cvt_pk_bf16_f32 v15,0,v15\nv_pk_add_f32 v[8:9],v[8:9],v[80:81]\n" \
"v_pk_add_f32 v[10:11],v[10:11],v[82:83]\nv_pk_add_f32 v[12:13],v[12:13],v[84:85]\nv_pk_add_f32 v[14:15],v[14:15],v[86:87]\nv_cvt_pk_bf16_f32 v8,0,v8\nv_cvt_pk_bf16_f32 v9,0,v9\nv_cvt_pk_bf16_f32 v10,0,v10\nv_cvt_pk_bf16_f32 v11,0,v11\nv_cvt_pk_bf16_f32 v12,0,v12\n" \
"v_cvt_pk_bf16_f32 v13,0,v13\nv_cvt_pk_bf16_f32 v14,0,v14\nv_cvt_pk_bf16_f32 v15,0,v15\nv_readfirstlane_b32 s70,v8\ns_nop 1\nv_subrev_f32 v40,s70,v8\nv_subrev_f32 v41,s70,v9\nv_subrev_f32 v42,s70,v10\n" \
"v_subrev_f32 v43,s70,v11\nv_subrev_f32 v44,s70,v12\nv_subrev_f32 v45,s70,v13\nv_subrev_f32 v46,s70,v14\nv_subrev_f32 v47,s70,v15\nv_pk_mul_f32 v[60:61],v[40:41],v[40:41]\nv_pk_add_f32 v[56:57],v[40:41],v[42:43]\nv_pk_fma_f32 v[60:61],v[42:43],v[42:43],v[60:61]\n" \
"v_pk_fma_f32 v[60:61],v[44:45],v[44:45],v[60:61]\nv_pk_fma_f32 v[60:61],v[46:47],v[46:47],v[60:61]\nv_pk_add_f32 v[44:45],v[44:45],v[46:47]\nv_pk_add_f32 v[56:57],v[56:57],v[44:45]\nv_add_f32 v56,v56,v57\nv_add_f32 v57,v60,v61\ns_nop 1\nv_permlane16_swap_b32 v56,v57\n" \
"v_add_f32 v56,v56,v57\nv_mov_b32 v57,v56\ns_nop 1\nv_permlane32_swap_b32 v56,v57\nv_add_f32 v56,v56,v57\ns_nop 1\nv_add_f32_dpp v56,v56,v56 quad_perm:[1,0,3,2] row_mask:0xf bank_mask:0xf\ns_nop 1\n" \
"v_add_f32_dpp v56,v56,v56 quad_perm:[2,3,0,1] row_mask:0xf bank_mask:0xf\ns_nop 1\nv_add_f32_dpp v56,v56,v56 row_half_mirror row_mask:0xf bank_mask:0xf\ns_nop 1\nv_add_f32_dpp v56,v56,v56 row_mirror row_mask:0xf bank_mask:0xf\nv_mul_f32 v56,0x3b000000,v56\nv_mov_b32 v57,v56\nv_cmp_u_f32 vcc,v56,v56\n" \
"s_nop 0\nv_permlane16_swap_b32 v56,v57\ns_cbranch_vccnz .Lrs0\n.Lrb0:\nbuffer_load_dwordx4 v[232:235],v251,s[52:55],s80 offen sc1\nbuffer_load_dwordx4 v[236:239],v251,s[52:55],s80 offen offset:16 sc1\nv_fma_f32 v57,-v56,v56,v57\nv_add_f32 v58,s70,v56\n" \
"v_max_f32 v57,0,v57\nv_cvt_pk_bf16_f32 v58,0,v58\nv_cvt_pk_bf16_f32 v57,0,v57\nv_add_f32 v57,0x3727c5ac,v57\nv_cvt_pk_bf16_f32 v57,0,v57\nv_sqrt_f32 v57,v57\nv_pk_add_f32 v[24:25],v[8:9],v[58:59] op_sel_hi:[1,0] neg_lo:[0,1] neg_hi:[0,1]\nv_cvt_pk_bf16_f32 v57,0,v57\n" \
"v_rcp_f32 v59,v57\nv_cvt_pk_bf16_f32 v24,0,v24\nv_pk_add_f32 v[26:27],v[10:11],v[58:59] op_sel_hi:[1,0] neg_lo:[0,1] neg_hi:[0,1]\nv_pk_add_f32 v[28:29],v[12:13],v[58:59] op_sel_hi:[1,0] neg_lo:[0,1] neg_hi:[0,1]\nv_pk_add_f32 v[30:31],v[14:15],v[58:59] op_sel_hi:[1,0] neg_lo:[0,1] neg_hi:[0,1]\nv_cvt_pk_bf16_f32 v25,0,v25\nv_pk_mul_f32 v[24:25],v[24:25],v[58:59] op_sel:[0,1]\nv_cvt_pk_bf16_f32 v24,0,v24\n" \
"v_cvt_pk_bf16_f32 v25,0,v25\nv_pk_mul_f32 v[24:25],v[24:25],v[32:33]\nv_cvt_pk_bf16_f32 v24,0,v24\nv_cvt_pk_bf16_f32 v25,0,v25\nv_pk_add_f32 v[24:25],v[24:25],v[16:17]\nv_cvt_pk_bf16_f32 v24,0,v24\nv_cvt_pk_bf16_f32 v25,0,v25\nv_pk_mul_f32 v[60:61],v[24:25],v[96:97]\n" \
"v_cvt_pk_bf16_f32 v26,0,v26\nv_cvt_pk_bf16_f32 v27,0,v27\nv_pk_mul_f32 v[26:27],v[26:27],v[58:59] op_sel:[0,1]\nv_cvt_pk_bf16_f32 v26,0,v26\nv_cvt_pk_bf16_f32 v27,0,v27\nv_pk_mul_f32 v[26:27],v[26:27],v[34:35]\nv_cvt_pk_bf16_f32 v26,0,v26\nv_cvt_pk_bf16_f32 v27,0,v27\n" \
"v_pk_add_f32 v[26:27],v[26:27],v[18:19]\nv_cvt_pk_bf16_f32 v26,0,v26\nv_cvt_pk_bf16_f32 v27,0,v27\nv_pk_fma_f32 v[60:61],v[26:27],v[98:99],v[60:61]\nv_cvt_pk_bf16_f32 v28,0,v28\nv_cvt_pk_bf16_f32 v29,0,v29\nv_pk_mul_f32 v[28:29],v[28:29],v[58:59] op_sel:[0,1]\nv_cvt_pk_bf16_f32 v28,0,v28\n" \
"v_cvt_pk_bf16_f32 v29,0,v29\nv_pk_mul_f32 v[28:29],v[28:29],v[36:37]\nv_cvt_pk_bf16_f32 v28,0,v28\nv_cvt_pk_bf16_f32 v29,0,v29\nv_pk_add_f32 v[28:29],v[28:29],v[20:21]\nv_cvt_pk_bf16_f32 v28,0,v28\nv_cvt_pk_bf16_f32 v29,0,v29\nv_pk_fma_f32 v[60:61],v[28:29],v[100:101],v[60:61]\n" \
"v_cvt_pk_bf16_f32 v30,0,v30\nv_cvt_pk_bf16_f32 v31,0,v31\nv_pk_mul_f32 v[30:31],v[30:31],v[58:59] op_sel:[0,1]\nv_cvt_pk_bf16_f32 v30,0,v30\nv_cvt_pk_bf16_f32 v31,0,v31\nv_pk_mul_f32 v[30:31],v[30:31],v[38:39]\nv_cvt_pk_bf16_f32 v30,0,v30\nv_cvt_pk_bf16_f32 v31,0,v31\n" \
"v_pk_add_f32 v[30:31],v[30:31],v[22:23]\nv_cvt_pk_bf16_f32 v30,0,v30\nv_cvt_pk_bf16_f32 v31,0,v31\nv_pk_fma_f32 v[60:61],v[30:31],v[102:103],v[60:61]\nv_add_f32 v60,v60,v61\nv_mov_b32 v61,v60\nv_lshrrev_b32 v40,16,v57\nv_or_b32 v40,v40,v58\n" \
"v_permlane16_swap_b32 v60,v61\nv_add_f32 v60,v60,v61\nv_mov_b32 v61,v60\ns_nop 1\nv_permlane32_swap_b32 v60,v61\nv_add_f32 v60,v60,v61\ns_nop 1\nv_add_f32_dpp v60,v60,v60 quad_perm:[1,0,3,2] row_mask:0xf bank_mask:0xf\n" \
"s_nop 1\nv_add_f32_dpp v60,v60,v60 quad_perm:[2,3,0,1] row_mask:0xf bank_mask:0xf\ns_nop 1\nv_add_f32_dpp v60,v60,v60 row_half_mirror row_mask:0xf bank_mask:0xf\ns_nop 1\nv_add_f32_dpp v60,v60,v60 row_mirror row_mask:0xf bank_mask:0xf\nv_cvt_pk_bf16_f32 v60,0,v60\ns_nop 0\n" \
"v_readfirstlane_b32 s76,v60\ns_nop 0\ns_ashr_i32 s71,s76,31\ns_and_b32 s72,s76,0x7fffffff\ns_or_b32 s71,s71,0x80000000\ns_xor_b32 s76,s76,s71\ns_and_b32 s76,s76,0xffff0000\ns_cmp_gt_u32 s72,0x7f800000\n" \
"s_cselect_b32 s76,0,s76\ns_or_b32 s76,s76,s39\nv_mov_b32 v41,s76\nds_max_u64 v250,v[40:41]\ns_waitcnt lgkmcnt(0)\ns_barrier\nds_read_b64 v[224:225],v250\ns_waitcnt lgkmcnt(0)\n" \
"buffer_store_dwordx2 v[224:225],v246,s[52:55],s37 offen sc1\nv_readfirstlane_b32 s97,v225\ns_sub_u32 s95,s95,54525952\ns_sub_u32 s74,s74,54525952\ns_mov_b32 s29,0\ns_cmp_eq_u32 s97,s77\ns_cselect_b32 s65,s95,0x40000000\ns_cselect_b32 s95,0x40000000,s95\n" \
"s_cmp_eq_u32 s97,s76\ns_cselect_b32 s66,s74,0x40000000\ns_cselect_b32 s74,0x40000000,s74\ns_lshr_b32 s70,s6,7\ns_or_b32 s38,s7,s70\ns_cmp_lg_u32 s3,0\ns_waitcnt vmcnt(1)\ns_cbranch_scc1 .Lkp\n" \
"v_pk_mul_f32 v[88:89],v[88:89],v[48:49]\nv_pk_mul_f32 v[90:91],v[90:91],v[50:51]\nv_pk_mul_f32 v[92:93],v[92:93],v[52:53]\nv_pk_mul_f32 v[94:95],v[94:95],v[54:55]\nv_cvt_pk_bf16_f32 v88,0,v88\nv_cvt_pk_bf16_f32 v89,0,v89\nv_cvt_pk_bf16_f32 v90,0,v90\nv_cvt_pk_bf16_f32 v91,0,v91\n" \
"v_cvt_pk_bf16_f32 v92,0,v92\nv_cvt_pk_bf16_f32 v93,0,v93\nv_cvt_pk_bf16_f32 v94,0,v94\nv_cvt_pk_bf16_f32 v95,0,v95\nv_pk_add_f32 v[88:89],v[88:89],v[72:73]\nv_pk_add_f32 v[90:91],v[90:91],v[74:75]\nv_pk_add_f32 v[92:93],v[92:93],v[76:77]\nv_pk_add_f32 v[94:95],v[94:95],v[78:79]\n" \
"v_cvt_pk_bf16_f32 v88,0,v88\nds_write_b16_d16_hi v244,v88 offset:0\nv_cvt_pk_bf16_f32 v89,0,v89\nds_write_b16_d16_hi v244,v89 offset:32\nv_cvt_pk_bf16_f32 v90,0,v90\nds_write_b16_d16_hi v244,v90 offset:64\nv_cvt_pk_bf16_f32 v91,0,v91\nds_write_b16_d16_hi v244,v91 offset:96\n" \
"v_cvt_pk_bf16_f32 v92,0,v92\nds_write_b16_d16_hi v244,v92 offset:128\nv_cvt_pk_bf16_f32 v93,0,v93\nds_write_b16_d16_hi v244,v93 offset:160\nv_cvt_pk_bf16_f32 v94,0,v94\nds_write_b16_d16_hi v244,v94 offset:192\nv_cvt_pk_bf16_f32 v95,0,v95\nds_write_b16_d16_hi v244,v95 offset:224\n" \
"ds_read_b128 v[64:67],v245 offset:0\nv_pk_mul_f32 v[24:25],v[24:25],v[48:49]\nv_pk_mul_f32 v[26:27],v[26:27],v[50:51]\nv_pk_mul_f32 v[28:29],v[28:29],v[52:53]\nv_pk_mul_f32 v[30:31],v[30:31],v[54:55]\nv_cvt_pk_bf16_f32 v24,0,v24\nv_cvt_pk_bf16_f32 v25,0,v25\nv_cvt_pk_bf16_f32 v26,0,v26\n" \
"v_cvt_pk_bf16_f32 v27,0,v27\nv_cvt_pk_bf16_f32 v28,0,v28\nv_cvt_pk_bf16_f32 v29,0,v29\nv_cvt_pk_bf16_f32 v30,0,v30\nv_cvt_pk_bf16_f32 v31,0,v31\nv_pk_add_f32 v[24:25],v[24:25],v[8:9]\nv_pk_add_f32 v[26:27],v[26:27],v[10:11]\nv_pk_add_f32 v[28:29],v[28:29],v[12:13]\n" \
"v_pk_add_f32 v[30:31],v[30:31],v[14:15]\nv_cvt_pk_bf16_f32 v24,0,v24\nds_write_b16_d16_hi v244,v24 offset:1024\nv_cvt_pk_bf16_f32 v25,0,v25\nds_write_b16_d16_hi v244,v25 offset:1056\nv_cvt_pk_bf16_f32 v26,0,v26\nds_write_b16_d16_hi v244,v26 offset:1088\nv_cvt_pk_bf16_f32 v27,0,v27\n" \
"ds_write_b16_d16_hi v244,v27 offset:1120\nv_cvt_pk_bf16_f32 v28,0,v28\nds_write_b16_d16_hi v244,v28 offset:1152\nv_cvt_pk_bf16_f32 v29,0,v29\nds_write_b16_d16_hi v244,v29 offset:1184\nv_cvt_pk_bf16_f32 v30,0,v30\nds_write_b16_d16_hi v244,v30 offset:1216\nv_cvt_pk_bf16_f32 v31,0,v31\n" \
"ds_write_b16_d16_hi v244,v31 offset:1248\nds_read_b128 v[68:71],v245 offset:1024\ns_waitcnt lgkmcnt(0)\nbuffer_store_dwordx4 v[64:67],v240,s[44:47],s95 offen sc1\nbuffer_store_dwordx4 v[68:71],v240,s[44:47],s74 offen sc1\ns_cmp_eq_u32 s7,0\ns_cselect_b32 s70,1,0\ns_xor_b32 s71,s65,s66\n" \
"s_or_b32 s70,s70,s71\ns_cbranch_scc0 .Lfin\ns_lshr_b32 s70,s6,3\ns_and_b32 s70,s70,15\ns_lshl4_add_u32 s98,s60,s70\nv_lshrrev_b32 v242,4,v249\ns_lshl_b32 s71,s98,2\nv_add_u32 v242,s71,v242\n" \
"v_and_b32 v244,15,v242\nv_bfe_u32 v245,v242,4,3\nv_lshl_add_u32 v244,v244,3,v245\nv_and_b32 v245,0x180,v242\nv_add_lshl_u32 v243,v244,v245,1\nv_lshlrev_b32 v244,1,v243\ns_mov_b32 s70,4096\nv_lshlrev_b32 v242,1,v242\n" \
"buffer_load_dword v62,v244,s[52:55],0 offen offset:0 sc1\nbuffer_load_dword v63,v244,s[52:55],0 offen offset:2048 sc1\nbuffer_load_dword v126,v244,s[52:55],s70 offen offset:0 sc1\nbuffer_load_dword v127,v244,s[52:55],s70 offen offset:2048 sc1\n.Lkp:\ns_cmp_lg_u32 s7,0\ns_cbranch_scc1 .Lkl\nv_max3_u32 v228,v233,v235,v237\n" \
"v_min3_u32 v229,v233,v235,v237\nv_max_u32 v228,v228,v239\nv_min_u32 v229,v229,v239\nv_cmp_lt_u32 vcc,s94,v228\nv_cmp_eq_u32 s[90:91],0,v229\ns_cbranch_vccnz .Lnew\ns_cmp_eq_u64 s[90:91],0\ns_cbranch_scc1 .Lv0\n" \
"s_sub_u32 s73,s73,1\ns_cselect_b32 s73,0,s73\ns_cbranch_scc1 .Lv0\ns_sleep 3\nbuffer_load_dwordx4 v[232:235],v251,s[52:55],s80 offen sc1\nbuffer_load_dwordx4 v[236:239],v251,s[52:55],s80 offen offset:16 sc1\ns_waitcnt vmcnt(0)\ns_branch .Lkp\n" \
".Lv0:\nv_mov_b32 v230,s96\ns_or_b32 s72,s94,2\nv_mov_b32 v231,s72\nds_write_b64 v250,v[230:231] offset:8\ns_branch .Lver\n.Lkl:\nds_read_b64 v[230:231],v250 offset:8\n" \
"s_mov_b32 s91,0\ns_waitcnt lgkmcnt(0)\nv_readfirstlane_b32 s72,v231\nv_readfirstlane_b32 s96,v230\ns_and_b32 s90,s72,2\ns_xor_b32 s72,s72,s90\ns_xor_b32 s90,s90,2\ns_cmp_lg_u32 s72,s94\n" \
"s_mov_b32 s94,s72\ns_cbranch_scc1 .Lsp\ns_cmp_eq_u32 s90,0\ns_cbranch_scc1 .Lver\ns_sub_u32 s73,s73,1\ns_cselect_b32 s73,0,s73\ns_cbranch_scc1 .Lver\ns_sleep 1\n" \
".Lyield:\ns_branch .Lkl\n.Lnew:\nv_mov_b32 v230,v228\nv_cmp_eq_u32 vcc,v228,v233\ns_cmp_eq_u64 s[90:91],0\ns_cselect_b32 s71,2,0\nv_max_u32_dpp v230,v230,v230 quad_perm:[1,0,3,2] row_mask:0xf bank_mask:0xf\n" \
"v_cndmask_b32 v229,v238,v232,vcc\nv_cmp_eq_u32 vcc,v228,v235\nv_max_u32_dpp v230,v230,v230 quad_perm:[2,3,0,1] row_mask:0xf bank_mask:0xf\ns_nop 1\nv_max_u32_dpp v230,v230,v230 row_half_mirror row_mask:0xf bank_mask:0xf\nv_cndmask_b32 v229,v229,v234,vcc\nv_cmp_eq_u32 vcc,v228,v237\nv_max_u32_dpp v230,v230,v230 row_mirror row_mask:0xf bank_mask:0xf\n" \
"v_mov_b32 v231,v230\nv_cndmask_b32 v229,v229,v236,vcc\ns_nop 0\nv_permlane16_swap_b32 v230,v231\nv_max_u32 v230,v230,v231\nv_mov_b32 v231,v230\ns_nop 1\nv_permlane32_swap_b32 v230,v231\n" \
"v_max_u32 v230,v230,v231\ns_nop 0\nv_readfirstlane_b32 s94,v230\ns_nop 1\nv_cmp_eq_u32 vcc,s94,v228\ns_nop 0\ns_ff1_i32_b64 s72,vcc\nv_readlane_b32 s96,v229,s72\n" \
"s_nop 1\nv_mov_b32 v230,s96\ns_or_b32 s72,s94,s71\nv_mov_b32 v231,s72\nds_write_b64 v250,v[230:231] offset:8\n.Lsp:\ns_not_b32 s15,s94\ns_bfe_u32 s15,s15,0xb0005\n" \
"s_lshl_b32 s15,s15,10\ns_cmp_eq_u32 s3,0\ns_cbranch_scc1 .Lkp\nv_add_u32 v70,114688,v241\ns_add_u32 s72,s15,54525952\nbuffer_load_dword v41,v241,s[52:55],s72 offen sc1\ns_add_u32 s72,s15,56623104\nbuffer_load_dword v43,v241,s[52:55],s72 offen sc1\n" \
"s_add_u32 s72,s15,58720256\nbuffer_load_dword v45,v241,s[52:55],s72 offen sc1\ns_add_u32 s72,s15,60817408\nbuffer_load_dword v47,v241,s[52:55],s72 offen sc1\ns_add_u32 s72,s15,62914560\nbuffer_load_dword v105,v241,s[52:55],s72 offen sc1\ns_add_u32 s72,s15,65011712\nbuffer_load_dword v107,v241,s[52:55],s72 offen sc1\n" \
"s_add_u32 s72,s15,67108864\nbuffer_load_dword v109,v241,s[52:55],s72 offen sc1\ns_add_u32 s72,s15,69206016\nbuffer_load_dword v111,v241,s[52:55],s72 offen sc1\nv_add_u32 v66,s15,v248\nglobal_load_ushort v64,v66,s[8:9]\nglobal_load_ushort v65,v66,s[8:9] offset:32\nbuffer_load_dwordx4 v[232:235],v251,s[52:55],s80 offen sc1\n" \
"buffer_load_dwordx4 v[236:239],v251,s[52:55],s80 offen offset:16 sc1\nds_read_b32 v161,v70 offset:0\nds_read_b32 v163,v70 offset:1024\nds_read_b32 v165,v70 offset:2048\nds_read_b32 v167,v70 offset:3072\nds_read_b32 v169,v70 offset:4096\nds_read_b32 v171,v70 offset:5120\nds_read_b32 v173,v70 offset:6144\n" \
"ds_read_b32 v175,v70 offset:7168\nds_read_b32 v177,v70 offset:8192\nds_read_b32 v179,v70 offset:9216\nds_read_b32 v181,v70 offset:10240\nds_read_b32 v183,v70 offset:11264\nds_read_b32 v185,v70 offset:12288\nds_read_b32 v187,v70 offset:13312\nds_read_b32 v189,v70 offset:14336\n" \
"ds_read_b32 v191,v70 offset:15360\ns_waitcnt lgkmcnt(15)\nv_lshlrev_b32 v160,16,v161\nv_and_b32 v161,0xffff0000,v161\ns_waitcnt lgkmcnt(14)\nv_lshlrev_b32 v162,16,v163\nv_and_b32 v163,0xffff0000,v163\ns_waitcnt lgkmcnt(13)\n" \
"v_lshlrev_b32 v164,16,v165\nv_and_b32 v165,0xffff0000,v165\ns_waitcnt lgkmcnt(12)\nv_lshlrev_b32 v166,16,v167\nv_and_b32 v167,0xffff0000,v167\ns_waitcnt lgkmcnt(11)\nv_lshlrev_b32 v168,16,v169\nv_and_b32 v169,0xffff0000,v169\n" \
"s_waitcnt lgkmcnt(10)\nv_lshlrev_b32 v170,16,v171\nv_and_b32 v171,0xffff0000,v171\ns_waitcnt lgkmcnt(9)\nv_lshlrev_b32 v172,16,v173\nv_and_b32 v173,0xffff0000,v173\ns_waitcnt lgkmcnt(8)\nv_lshlrev_b32 v174,16,v175\n" \
"v_and_b32 v175,0xffff0000,v175\ns_waitcnt lgkmcnt(7)\nv_lshlrev_b32 v176,16,v177\nv_and_b32 v177,0xffff0000,v177\ns_waitcnt lgkmcnt(6)\nv_lshlrev_b32 v178,16,v179\nv_and_b32 v179,0xffff0000,v179\ns_waitcnt lgkmcnt(5)\n" \
"v_lshlrev_b32 v180,16,v181\nv_and_b32 v181,0xffff0000,v181\ns_waitcnt lgkmcnt(4)\nv_lshlrev_b32 v182,16,v183\nv_and_b32 v183,0xffff0000,v183\ns_waitcnt lgkmcnt(3)\nv_lshlrev_b32 v184,16,v185\nv_and_b32 v185,0xffff0000,v185\n" \
"s_waitcnt lgkmcnt(1)\nv_lshlrev_b32 v186,16,v187\nv_and_b32 v187,0xffff0000,v187\nv_lshlrev_b32 v188,16,v189\nv_and_b32 v189,0xffff0000,v189\ns_waitcnt lgkmcnt(0)\nv_lshlrev_b32 v190,16,v191\nv_and_b32 v191,0xffff0000,v191\n" \
"s_and_b32 s82,s96,0xffff0000\ns_lshl_b32 s83,s96,16\nv_rcp_f32 v68,s83\ns_waitcnt vmcnt(11)\nv_lshlrev_b32 v40,16,v41\nv_and_b32 v41,0xffff0000,v41\ns_waitcnt vmcnt(10)\nv_lshlrev_b32 v42,16,v43\n" \
"v_and_b32 v43,0xffff0000,v43\nv_pk_add_f32 v[40:41],v[40:41],v[42:43]\ns_waitcnt vmcnt(9)\nv_lshlrev_b32 v44,16,v45\nv_and_b32 v45,0xffff0000,v45\ns_waitcnt vmcnt(8)\nv_lshlrev_b32 v46,16,v47\nv_and_b32 v47,0xffff0000,v47\n" \
"v_pk_add_f32 v[44:45],v[44:45],v[46:47]\nv_pk_add_f32 v[40:41],v[40:41],v[44:45]\ns_waitcnt vmcnt(7)\nv_lshlrev_b32 v104,16,v105\nv_and_b32 v105,0xffff0000,v105\ns_waitcnt vmcnt(6)\nv_lshlrev_b32 v106,16,v107\nv_and_b32 v107,0xffff0000,v107\n" \
"v_pk_add_f32 v[104:105],v[104:105],v[106:107]\ns_waitcnt vmcnt(5)\nv_lshlrev_b32 v108,16,v109\nv_and_b32 v109,0xffff0000,v109\ns_waitcnt vmcnt(4)\nv_lshlrev_b32 v110,16,v111\nv_and_b32 v111,0xffff0000,v111\nv_pk_add_f32 v[108:109],v[108:109],v[110:111]\n" \
"v_pk_add_f32 v[104:105],v[104:105],v[108:109]\nv_pk_add_f32 v[40:41],v[40:41],v[104:105]\nv_cvt_pk_bf16_f32 v40,0,v40\nv_cvt_pk_bf16_f32 v41,0,v41\nv_pk_mul_f32 v[40:41],v[40:41],v[112:113]\nv_cvt_pk_bf16_f32 v40,0,v40\nv_cvt_pk_bf16_f32 v41,0,v41\ns_waitcnt vmcnt(3)\n" \
"v_lshlrev_b32 v64,16,v64\ns_waitcnt vmcnt(2)\nv_lshlrev_b32 v65,16,v65\nv_pk_add_f32 v[40:41],v[40:41],v[64:65]\nv_cvt_pk_bf16_f32 v40,0,v40\nv_cvt_pk_bf16_f32 v41,0,v41\nv_pk_add_f32 v[40:41],v[40:41],s[82:83] op_sel_hi:[1,0] neg_lo:[0,1] neg_hi:[0,1]\nv_cvt_pk_bf16_f32 v40,0,v40\n" \
"v_cvt_pk_bf16_f32 v41,0,v41\nv_pk_mul_f32 v[40:41],v[40:41],v[68:69] op_sel_hi:[1,0]\nv_cvt_pk_bf16_f32 v40,0,v40\nv_cvt_pk_bf16_f32 v41,0,v41\nv_pk_mul_f32 v[40:41],v[40:41],v[116:117]\nv_cvt_pk_bf16_f32 v40,0,v40\nv_cvt_pk_bf16_f32 v41,0,v41\nv_pk_add_f32 v[40:41],v[40:41],v[114:115]\n" \
"v_cvt_pk_bf16_f32 v40,v40,v41\ns_nop 1\nv_mov_b32_dpp v44,v40 quad_perm:[0,0,0,0] row_mask:0xf bank_mask:0xf\nv_mov_b32_dpp v45,v40 quad_perm:[1,1,1,1] row_mask:0xf bank_mask:0xf\nv_mov_b32_dpp v46,v40 quad_perm:[2,2,2,2] row_mask:0xf bank_mask:0xf\nv_mov_b32_dpp v47,v40 quad_perm:[3,3,3,3] row_mask:0xf bank_mask:0xf\nv_dot2_f32_bf16 v120,v192,v44,0\nv_dot2_f32_bf16 v121,v193,v44,0\n" \
"v_dot2_f32_bf16 v122,v194,v44,0\nv_dot2_f32_bf16 v123,v195,v44,0\nv_dot2_f32_bf16 v124,v208,v44,0\nv_dot2_f32_bf16 v125,v209,v44,0\nv_dot2_f32_bf16 v126,v210,v44,0\nv_dot2_f32_bf16 v127,v211,v44,0\nv_dot2_f32_bf16 v120,v196,v45,v120\nv_dot2_f32_bf16 v121,v197,v45,v121\n" \
"v_dot2_f32_bf16 v122,v198,v45,v122\nv_dot2_f32_bf16 v123,v199,v45,v123\nv_dot2_f32_bf16 v124,v212,v45,v124\nv_dot2_f32_bf16 v125,v213,v45,v125\nv_dot2_f32_bf16 v126,v214,v45,v126\nv_dot2_f32_bf16 v127,v215,v45,v127\nv_dot2_f32_bf16 v120,v200,v46,v120\nv_dot2_f32_bf16 v121,v201,v46,v121\n" \
"v_dot2_f32_bf16 v122,v202,v46,v122\nv_dot2_f32_bf16 v123,v203,v46,v123\nv_dot2_f32_bf16 v124,v216,v46,v124\nv_dot2_f32_bf16 v125,v217,v46,v125\nv_dot2_f32_bf16 v126,v218,v46,v126\nv_dot2_f32_bf16 v127,v219,v46,v127\nv_dot2_f32_bf16 v120,v204,v47,v120\nv_dot2_f32_bf16 v121,v205,v47,v121\n" \
"v_dot2_f32_bf16 v122,v206,v47,v122\nv_dot2_f32_bf16 v123,v207,v47,v123\nv_dot2_f32_bf16 v124,v220,v47,v124\nv_dot2_f32_bf16 v125,v221,v47,v125\nv_dot2_f32_bf16 v126,v222,v47,v126\nv_dot2_f32_bf16 v127,v223,v47,v127\nv_permlane32_swap_b32 v120,v124\nv_add_f32 v120,v120,v124\n" \
"v_permlane32_swap_b32 v121,v125\nv_add_f32 v121,v121,v125\nv_permlane32_swap_b32 v122,v126\nv_add_f32 v122,v122,v126\nv_permlane32_swap_b32 v123,v127\nv_add_f32 v123,v123,v127\nv_permlane16_swap_b32 v120,v122\nv_add_f32 v120,v120,v122\n" \
"v_permlane16_swap_b32 v121,v123\nv_add_f32 v121,v121,v123\nv_add_f32_dpp v120,v120,v120 row_ror:4 row_mask:0xf bank_mask:0xf\ns_nop 1\nv_add_f32_dpp v120,v120,v120 row_ror:8 row_mask:0xf bank_mask:0xf\nv_add_f32_dpp v121,v121,v121 row_ror:4 row_mask:0xf bank_mask:0xf\ns_nop 1\nv_add_f32_dpp v121,v121,v121 row_ror:8 row_mask:0xf bank_mask:0xf\n" \
"s_cmp_eq_u64 s[90:91],0\ns_cbranch_scc1 .Lver\ns_waitcnt vmcnt(0)\ns_branch .Lkp\n.Lver:\ns_cmp_eq_u32 s3,0\ns_cbranch_scc1 .Lnf\nds_write_b32 v252,v120\n" \
"ds_write_b32 v252,v121 offset:16\ns_lshr_b32 s70,s2,1\nv_and_b32 v64,4,v241\nv_lshrrev_b32 v241,3,v241\nv_lshlrev_b32 v241,11,v241\nv_lshl_or_b32 v241,v64,1,v241\ns_add_u32 s70,s70,98304\ns_waitcnt lgkmcnt(0)\n" \
"s_barrier\nds_read_b128 v[40:43],v253\nds_read_b128 v[44:47],v253 offset:256\ns_waitcnt lgkmcnt(1)\nv_pk_add_f32 v[40:41],v[40:41],v[42:43]\nv_add_f32 v40,v40,v41\nv_cvt_pk_bf16_f32 v40,0,v40\nv_mul_f32 v41,0xbfb8aa3b,v40\n" \
"v_exp_f32 v41,v41\ns_waitcnt lgkmcnt(0)\nv_pk_add_f32 v[44:45],v[44:45],v[46:47]\nv_add_f32 v41,1.0,v41\nv_rcp_f32 v41,v41\nv_add_f32 v44,v44,v45\nv_cvt_pk_bf16_f32 v44,0,v44\nv_mul_f32 v40,v40,v41\n" \
"v_cvt_pk_bf16_f32 v40,0,v40\nv_mul_f32 v40,v40,v44\nv_cvt_pk_bf16_f32 v40,0,v40\ns_nop 0\nv_readlane_b32 s8,v40,0\nv_readlane_b32 s9,v40,1\nv_readlane_b32 s10,v40,2\nv_readlane_b32 s11,v40,3\n" \
"v_readlane_b32 s12,v40,4\nv_readlane_b32 s13,v40,5\nv_readlane_b32 s14,v40,6\nv_readlane_b32 s15,v40,7\nv_readlane_b32 s16,v40,8\nv_readlane_b32 s17,v40,9\nv_readlane_b32 s18,v40,10\nv_readlane_b32 s19,v40,11\n" \
"v_readlane_b32 s20,v40,12\nv_readlane_b32 s21,v40,13\nv_readlane_b32 s22,v40,14\nv_readlane_b32 s23,v40,15\nv_pk_mul_f32 v[120:121],v[160:161],s[8:9] op_sel_hi:[1,0]\nv_pk_mul_f32 v[122:123],v[162:163],s[8:9] op_sel:[0,1]\nv_pk_fma_f32 v[120:121],v[164:165],s[10:11],v[120:121] op_sel_hi:[1,0,1]\nv_pk_fma_f32 v[122:123],v[166:167],s[10:11],v[122:123] op_sel:[0,1,0]\n" \
"v_pk_fma_f32 v[120:121],v[168:169],s[12:13],v[120:121] op_sel_hi:[1,0,1]\nv_pk_fma_f32 v[122:123],v[170:171],s[12:13],v[122:123] op_sel:[0,1,0]\nv_pk_fma_f32 v[120:121],v[172:173],s[14:15],v[120:121] op_sel_hi:[1,0,1]\nv_pk_fma_f32 v[122:123],v[174:175],s[14:15],v[122:123] op_sel:[0,1,0]\nv_pk_fma_f32 v[120:121],v[176:177],s[16:17],v[120:121] op_sel_hi:[1,0,1]\nv_pk_fma_f32 v[122:123],v[178:179],s[16:17],v[122:123] op_sel:[0,1,0]\nv_pk_fma_f32 v[120:121],v[180:181],s[18:19],v[120:121] op_sel_hi:[1,0,1]\nv_pk_fma_f32 v[122:123],v[182:183],s[18:19],v[122:123] op_sel:[0,1,0]\n" \
"v_pk_fma_f32 v[120:121],v[184:185],s[20:21],v[120:121] op_sel_hi:[1,0,1]\nv_pk_fma_f32 v[122:123],v[186:187],s[20:21],v[122:123] op_sel:[0,1,0]\nv_pk_fma_f32 v[120:121],v[188:189],s[22:23],v[120:121] op_sel_hi:[1,0,1]\nv_pk_fma_f32 v[122:123],v[190:191],s[22:23],v[122:123] op_sel:[0,1,0]\nv_pk_add_f32 v[120:121],v[120:121],v[122:123]\nv_min_u32 v120,-2,v120\nv_min_u32 v121,-2,v121\nbuffer_store_dwordx2 v[120:121],v241,s[52:55],s70 offen sc1\n" \
"v_pk_mul_f32 v[88:89],v[88:89],v[48:49]\nv_pk_mul_f32 v[90:91],v[90:91],v[50:51]\nv_pk_mul_f32 v[92:93],v[92:93],v[52:53]\nv_pk_mul_f32 v[94:95],v[94:95],v[54:55]\nv_cvt_pk_bf16_f32 v88,0,v88\nv_cvt_pk_bf16_f32 v89,0,v89\nv_cvt_pk_bf16_f32 v90,0,v90\nv_cvt_pk_bf16_f32 v91,0,v91\n" \
"v_cvt_pk_bf16_f32 v92,0,v92\nv_cvt_pk_bf16_f32 v93,0,v93\nv_cvt_pk_bf16_f32 v94,0,v94\nv_cvt_pk_bf16_f32 v95,0,v95\nv_pk_add_f32 v[88:89],v[88:89],v[72:73]\nv_pk_add_f32 v[90:91],v[90:91],v[74:75]\nv_pk_add_f32 v[92:93],v[92:93],v[76:77]\nv_pk_add_f32 v[94:95],v[94:95],v[78:79]\n" \
"v_cvt_pk_bf16_f32 v88,0,v88\nds_write_b16_d16_hi v244,v88 offset:0\nv_cvt_pk_bf16_f32 v89,0,v89\nds_write_b16_d16_hi v244,v89 offset:32\nv_cvt_pk_bf16_f32 v90,0,v90\nds_write_b16_d16_hi v244,v90 offset:64\nv_cvt_pk_bf16_f32 v91,0,v91\nds_write_b16_d16_hi v244,v91 offset:96\n" \
"v_cvt_pk_bf16_f32 v92,0,v92\nds_write_b16_d16_hi v244,v92 offset:128\nv_cvt_pk_bf16_f32 v93,0,v93\nds_write_b16_d16_hi v244,v93 offset:160\nv_cvt_pk_bf16_f32 v94,0,v94\nds_write_b16_d16_hi v244,v94 offset:192\nv_cvt_pk_bf16_f32 v95,0,v95\nds_write_b16_d16_hi v244,v95 offset:224\n" \
"ds_read_b128 v[64:67],v245 offset:0\nv_pk_mul_f32 v[24:25],v[24:25],v[48:49]\nv_pk_mul_f32 v[26:27],v[26:27],v[50:51]\nv_pk_mul_f32 v[28:29],v[28:29],v[52:53]\nv_pk_mul_f32 v[30:31],v[30:31],v[54:55]\nv_cvt_pk_bf16_f32 v24,0,v24\nv_cvt_pk_bf16_f32 v25,0,v25\nv_cvt_pk_bf16_f32 v26,0,v26\n" \
"v_cvt_pk_bf16_f32 v27,0,v27\nv_cvt_pk_bf16_f32 v28,0,v28\nv_cvt_pk_bf16_f32 v29,0,v29\nv_cvt_pk_bf16_f32 v30,0,v30\nv_cvt_pk_bf16_f32 v31,0,v31\nv_pk_add_f32 v[24:25],v[24:25],v[8:9]\nv_pk_add_f32 v[26:27],v[26:27],v[10:11]\nv_pk_add_f32 v[28:29],v[28:29],v[12:13]\n" \
"v_pk_add_f32 v[30:31],v[30:31],v[14:15]\nv_cvt_pk_bf16_f32 v24,0,v24\nds_write_b16_d16_hi v244,v24 offset:1024\nv_cvt_pk_bf16_f32 v25,0,v25\nds_write_b16_d16_hi v244,v25 offset:1056\nv_cvt_pk_bf16_f32 v26,0,v26\nds_write_b16_d16_hi v244,v26 offset:1088\nv_cvt_pk_bf16_f32 v27,0,v27\n" \
"ds_write_b16_d16_hi v244,v27 offset:1120\nv_cvt_pk_bf16_f32 v28,0,v28\nds_write_b16_d16_hi v244,v28 offset:1152\nv_cvt_pk_bf16_f32 v29,0,v29\nds_write_b16_d16_hi v244,v29 offset:1184\nv_cvt_pk_bf16_f32 v30,0,v30\nds_write_b16_d16_hi v244,v30 offset:1216\nv_cvt_pk_bf16_f32 v31,0,v31\n" \
"ds_write_b16_d16_hi v244,v31 offset:1248\nds_read_b128 v[68:71],v245 offset:1024\ns_waitcnt lgkmcnt(0)\nbuffer_store_dwordx4 v[64:67],v240,s[44:47],s95 offen sc1\nbuffer_store_dwordx4 v[68:71],v240,s[44:47],s74 offen sc1\n.Lnf:\ns_cmp_eq_u32 s94,s97\ns_cbranch_scc1 .Lwin\n" \
"buffer_store_dwordx4 v[64:67],v240,s[44:47],s65 offen sc1\nbuffer_store_dwordx4 v[68:71],v240,s[44:47],s66 offen sc1\n.Lwin:\ns_cmp_lg_u32 s38,0\ns_cbranch_scc1 .Lfin\ns_add_u32 s72,s15,54525952\nbuffer_load_ushort v128,v243,s[52:55],s72 offen sc1\ns_add_u32 s72,s15,56623104\n" \
"buffer_load_ushort v129,v243,s[52:55],s72 offen sc1\ns_add_u32 s72,s15,58720256\nbuffer_load_ushort v130,v243,s[52:55],s72 offen sc1\ns_add_u32 s72,s15,60817408\nbuffer_load_ushort v131,v243,s[52:55],s72 offen sc1\ns_add_u32 s72,s15,62914560\nbuffer_load_ushort v132,v243,s[52:55],s72 offen sc1\ns_add_u32 s72,s15,65011712\n" \
"buffer_load_ushort v133,v243,s[52:55],s72 offen sc1\ns_add_u32 s72,s15,67108864\nbuffer_load_ushort v134,v243,s[52:55],s72 offen sc1\ns_add_u32 s72,s15,69206016\nbuffer_load_ushort v135,v243,s[52:55],s72 offen sc1\nv_add_u32 v242,s15,v242\nglobal_load_ushort v144,v242,s[8:9]\nv_min_u32 v168,55,v249\n" \
"v_lshlrev_b32 v168,4,v168\ns_lshl_b32 s70,s98,11\ns_add_u32 s70,s70,98304\ns_add_u32 s71,s70,896\ns_and_b32 s82,s96,0xffff0000\ns_lshl_b32 s83,s96,16\nv_rcp_f32 v148,s83\ns_ashr_i32 s37,s94,31\n" \
"s_not_b32 s37,s37\ns_or_b32 s37,s37,0x80000000\ns_xor_b32 s37,s37,s94\ns_and_b32 s37,s37,0xffff0000\nv_mov_b32 v149,0xbfb8aa3b\nv_mul_f32 v149,s37,v149\nv_exp_f32 v149,v149\nv_and_b32 v150,15,v249\n" \
"v_add_f32 v149,1.0,v149\nv_rcp_f32 v149,v149\nv_cmp_eq_u32 vcc,0,v150\nv_cvt_pk_bf16_f32 v149,0,v149\nv_mov_b32 v150,0x40000000\nv_cndmask_b32 v150,v150,v242,vcc\nv_cmp_gt_u32 s[90:91],56,v249\ns_waitcnt vmcnt(8)\n" \
"v_lshlrev_b32 v128,16,v128\ns_waitcnt vmcnt(7)\nv_lshlrev_b32 v129,16,v129\nv_add_f32 v128,v128,v129\ns_waitcnt vmcnt(6)\nv_lshlrev_b32 v130,16,v130\ns_waitcnt vmcnt(5)\nv_lshlrev_b32 v131,16,v131\n" \
"v_add_f32 v130,v130,v131\nv_add_f32 v128,v128,v130\ns_waitcnt vmcnt(4)\nv_lshlrev_b32 v132,16,v132\ns_waitcnt vmcnt(3)\nv_lshlrev_b32 v133,16,v133\nv_add_f32 v132,v132,v133\ns_waitcnt vmcnt(2)\n" \
"v_lshlrev_b32 v134,16,v134\ns_waitcnt vmcnt(1)\nv_lshlrev_b32 v135,16,v135\nv_add_f32 v134,v134,v135\nv_add_f32 v132,v132,v134\nv_add_f32 v128,v128,v132\nv_cvt_pk_bf16_f32 v128,0,v128\nv_mul_f32 v128,v128,v62\n" \
"v_cvt_pk_bf16_f32 v128,0,v128\ns_waitcnt vmcnt(0)\nv_add_f32 v126,1.0,v126\nv_cvt_pk_bf16_f32 v126,0,v126\nv_lshlrev_b32 v144,16,v144\nv_add_f32 v128,v128,v144\nv_cvt_pk_bf16_f32 v128,0,v128\nv_subrev_f32 v129,s82,v128\n" \
"v_cvt_pk_bf16_f32 v129,0,v129\nv_mul_f32 v129,v129,v148\nv_cvt_pk_bf16_f32 v129,0,v129\nv_mul_f32 v129,v129,v126\nv_cvt_pk_bf16_f32 v129,0,v129\nv_add_f32 v129,v129,v63\nv_cvt_pk_bf16_f32 v129,0,v129\n.Lp2:\n" \
"buffer_load_dwordx4 v[160:163],v168,s[52:55],s70 offen sc1\nbuffer_load_dwordx4 v[164:167],v168,s[52:55],s71 offen sc1\ns_waitcnt vmcnt(0)\nv_max3_u32 v228,v160,v161,v162\nv_max3_u32 v229,v163,v164,v165\nv_max3_u32 v228,v228,v166,v167\nv_max_u32 v228,v228,v229\nv_pk_add_f32 v[160:161],v[160:161],v[164:165]\n" \
"v_cmp_eq_u32 vcc,-1,v228\nv_pk_add_f32 v[162:163],v[162:163],v[166:167]\ns_sub_u32 s73,s73,1\ns_cselect_b32 s73,0,s73\ns_cselect_b64 vcc,0,vcc\ns_cmp_lg_u64 vcc,0\ns_cselect_b32 s72,0x8000000,0\nv_cndmask_b32 v160,0,v160,s[90:91]\n" \
"v_cndmask_b32 v161,0,v161,s[90:91]\nv_cndmask_b32 v162,0,v162,s[90:91]\nv_cndmask_b32 v163,0,v163,s[90:91]\ns_nop 0\nv_permlane32_swap_b32 v160,v162\nv_permlane32_swap_b32 v161,v163\nv_pk_add_f32 v[160:161],v[160:161],v[162:163]\ns_nop 1\n" \
"v_permlane16_swap_b32 v160,v161\nv_add_f32 v160,v160,v161\ns_nop 1\nv_add_f32_dpp v160,v160,v160 quad_perm:[1,0,3,2] row_mask:0xf bank_mask:0xf\ns_nop 1\nv_add_f32_dpp v160,v160,v160 quad_perm:[2,3,0,1] row_mask:0xf bank_mask:0xf\ns_nop 1\nv_add_f32_dpp v160,v160,v160 row_half_mirror row_mask:0xf bank_mask:0xf\n" \
"s_nop 1\nv_add_f32_dpp v160,v160,v160 row_mirror row_mask:0xf bank_mask:0xf\nv_cvt_pk_bf16_f32 v160,0,v160\nv_mul_f32 v160,v160,v149\nv_cvt_pk_bf16_f32 v160,0,v160\nv_add_f32 v160,v160,v129\nv_cvt_pk_bf16_f32 v160,0,v160\nv_mul_f32 v160,v160,v127\n" \
"v_cvt_pk_bf16_f32 v160,0,v160\nv_add_f32 v160,v160,v128\nv_cvt_pk_bf16_f32 v160,0,v160\nbuffer_store_short_d16_hi v160,v150,s[44:47],s72 offen sc1\n.Lp2s:\ns_cbranch_scc1 .Lp2\ns_branch .Lfin\n.Lhs:\n" \
"s_sub_u32 s73,s73,1\ns_cselect_b32 s73,0,s73\ns_cbranch_scc1 .Lhb\ns_sleep 2\nbuffer_load_dwordx2 v[166:167],v165,s[52:55],s80 offen sc1\ns_waitcnt vmcnt(0)\nv_xor_b32 v166,v166,v230\nv_xor_b32 v167,v167,v231\n" \
"v_or_b32 v166,v166,v167\nv_cmp_ne_u32 vcc,0,v166\ns_nop 0\ns_cbranch_vccnz .Lhs\ns_branch .Lhb\n.Lps0:\ns_sub_u32 s73,s73,1\ns_cselect_b32 s73,0,s73\n" \
"s_cbranch_scc1 .Lpb0\ns_sleep 1\nbuffer_load_dword v248,v228,s[52:55],s80 offen sc1\ns_waitcnt vmcnt(0)\nv_cmp_eq_u32 vcc,0,v248\ns_nop 0\ns_cbranch_vccnz .Lps0\ns_branch .Lpb0\n" \
".Lps1:\ns_sub_u32 s73,s73,1\ns_cselect_b32 s73,0,s73\ns_cbranch_scc1 .Lpb1\ns_sleep 1\nbuffer_load_dword v248,v228,s[52:55],s80 offen sc1\ns_waitcnt vmcnt(0)\nv_cmp_eq_u32 vcc,0,v248\n" \
"s_nop 0\ns_cbranch_vccnz .Lps1\ns_branch .Lpb1\n.Lps2:\ns_sub_u32 s73,s73,1\ns_cselect_b32 s73,0,s73\ns_cbranch_scc1 .Lpb2\ns_sleep 1\n" \
"buffer_load_dword v248,v251,s[52:55],s80 offen sc1\ns_waitcnt vmcnt(0)\nv_cmp_eq_u32 vcc,0,v248\ns_nop 0\ns_cbranch_vccnz .Lps2\ns_branch .Lpb2\n.Lps3:\ns_sub_u32 s73,s73,1\n" \
"s_cselect_b32 s73,0,s73\ns_cbranch_scc1 .Lpb3\ns_sleep 1\nbuffer_load_dword v248,v251,s[52:55],s80 offen sc1\ns_waitcnt vmcnt(0)\nv_cmp_eq_u32 vcc,0,v248\ns_nop 0\ns_cbranch_vccnz .Lps3\n" \
"s_branch .Lpb3\n.Lps4:\ns_sub_u32 s73,s73,1\ns_cselect_b32 s73,0,s73\ns_cbranch_scc1 .Lpb4\ns_sleep 1\nbuffer_load_dword v248,v251,s[52:55],s80 offen sc1\ns_waitcnt vmcnt(0)\n" \
"v_cmp_eq_u32 vcc,0,v248\ns_nop 0\ns_cbranch_vccnz .Lps4\ns_branch .Lpb4\n.Lps5:\ns_sub_u32 s73,s73,1\ns_cselect_b32 s73,0,s73\ns_cbranch_scc1 .Lpb5\n" \
"s_sleep 1\nbuffer_load_dword v248,v251,s[52:55],s80 offen sc1\ns_waitcnt vmcnt(0)\nv_cmp_eq_u32 vcc,0,v248\ns_nop 0\ns_cbranch_vccnz .Lps5\ns_branch .Lpb5\n.Lps6:\n" \
"s_sub_u32 s73,s73,1\ns_cselect_b32 s73,0,s73\ns_cbranch_scc1 .Lpb6\ns_sleep 1\nbuffer_load_dword v248,v251,s[52:55],s80 offen sc1\ns_waitcnt vmcnt(0)\nv_and_b32 v248,s17,v248\nv_cmp_eq_u32 vcc,0,v248\n" \
"s_nop 0\ns_cbranch_vccnz .Lps6\ns_branch .Lpb6\n.Lps7:\ns_sub_u32 s73,s73,1\ns_cselect_b32 s73,0,s73\ns_cbranch_scc1 .Lpb7\ns_sleep 1\n" \
"buffer_load_dword v248,v254,s[52:55],s80 offen sc1\ns_waitcnt vmcnt(0)\nv_cmp_eq_u32 vcc,0,v248\ns_nop 0\ns_cbranch_vccnz .Lps7\ns_branch .Lpb7\n.Lps8:\ns_sub_u32 s73,s73,1\n" \
"s_cselect_b32 s73,0,s73\ns_cbranch_scc1 .Lpb8\ns_sleep 1\nbuffer_load_dword v248,v251,s[52:55],s79 offen sc1\ns_waitcnt vmcnt(0)\nv_cmp_eq_u32 vcc,0,v248\ns_nop 0\ns_cbranch_vccnz .Lps8\n" \
"s_branch .Lpb8\n.Lrs1:\ns_mov_b32 s71,0\n.Lrl1:\ns_mov_b32 s97,0\nv_max3_u32 v124,v160,v161,v162\nv_max3_u32 v124,v124,v163,v163\nv_cmp_eq_u32 vcc,-1,v124\n" \
"s_add_u32 s72,s95,0\ns_cbranch_vccz .Lrz10\ns_mov_b64 exec,vcc\ns_mov_b32 s97,1\nbuffer_load_dwordx4 v[160:163],v240,s[52:55],s72 offen sc1\ns_mov_b64 exec,-1\n.Lrz10:\nv_max3_u32 v124,v164,v165,v166\n" \
"v_max3_u32 v124,v124,v167,v167\nv_cmp_eq_u32 vcc,-1,v124\ns_add_u32 s72,s95,2097152\ns_cbranch_vccz .Lrz11\ns_mov_b64 exec,vcc\ns_mov_b32 s97,1\nbuffer_load_dwordx4 v[164:167],v240,s[52:55],s72 offen sc1\ns_mov_b64 exec,-1\n" \
".Lrz11:\nv_max3_u32 v124,v168,v169,v170\nv_max3_u32 v124,v124,v171,v171\nv_cmp_eq_u32 vcc,-1,v124\ns_add_u32 s72,s95,4194304\ns_cbranch_vccz .Lrz12\ns_mov_b64 exec,vcc\ns_mov_b32 s97,1\n" \
"buffer_load_dwordx4 v[168:171],v240,s[52:55],s72 offen sc1\ns_mov_b64 exec,-1\n.Lrz12:\nv_max3_u32 v124,v172,v173,v174\nv_max3_u32 v124,v124,v175,v175\nv_cmp_eq_u32 vcc,-1,v124\ns_add_u32 s72,s95,6291456\ns_cbranch_vccz .Lrz13\n" \
"s_mov_b64 exec,vcc\ns_mov_b32 s97,1\nbuffer_load_dwordx4 v[172:175],v240,s[52:55],s72 offen sc1\ns_mov_b64 exec,-1\n.Lrz13:\nv_max3_u32 v124,v176,v177,v178\nv_max3_u32 v124,v124,v179,v179\nv_cmp_eq_u32 vcc,-1,v124\n" \
"s_add_u32 s72,s95,8388608\ns_cbranch_vccz .Lrz14\ns_mov_b64 exec,vcc\ns_mov_b32 s97,1\nbuffer_load_dwordx4 v[176:179],v240,s[52:55],s72 offen sc1\ns_mov_b64 exec,-1\n.Lrz14:\nv_max3_u32 v124,v180,v181,v182\n" \
"v_max3_u32 v124,v124,v183,v183\nv_cmp_eq_u32 vcc,-1,v124\ns_add_u32 s72,s95,10485760\ns_cbranch_vccz .Lrz15\ns_mov_b64 exec,vcc\ns_mov_b32 s97,1\nbuffer_load_dwordx4 v[180:183],v240,s[52:55],s72 offen sc1\ns_mov_b64 exec,-1\n" \
".Lrz15:\nv_max3_u32 v124,v184,v185,v186\nv_max3_u32 v124,v124,v187,v187\nv_cmp_eq_u32 vcc,-1,v124\ns_add_u32 s72,s95,12582912\ns_cbranch_vccz .Lrz16\ns_mov_b64 exec,vcc\ns_mov_b32 s97,1\n" \
"buffer_load_dwordx4 v[184:187],v240,s[52:55],s72 offen sc1\ns_mov_b64 exec,-1\n.Lrz16:\nv_max3_u32 v124,v188,v189,v190\nv_max3_u32 v124,v124,v191,v191\nv_cmp_eq_u32 vcc,-1,v124\ns_add_u32 s72,s95,14680064\ns_cbranch_vccz .Lrz17\n" \
"s_mov_b64 exec,vcc\ns_mov_b32 s97,1\nbuffer_load_dwordx4 v[188:191],v240,s[52:55],s72 offen sc1\ns_mov_b64 exec,-1\n.Lrz17:\ns_cmp_lg_u32 s97,0\ns_cbranch_scc1 .Lrm1\ns_cmp_eq_u32 s71,0\n" \
"s_cbranch_scc1 .Lrb1\ns_branch .Lrow1\n.Lrm1:\ns_mov_b32 s71,1\ns_waitcnt vmcnt(0)\ns_sub_u32 s73,s73,1\ns_cselect_b32 s73,0,s73\ns_cbranch_scc1 .Lrb1\n" \
"s_sleep 2\ns_branch .Lrl1\n.Lrs0:\ns_mov_b32 s71,0\n.Lrl0:\ns_mov_b32 s97,0\nv_max3_u32 v60,v128,v129,v130\nv_max3_u32 v60,v60,v131,v131\n" \
"v_cmp_eq_u32 vcc,-1,v60\ns_add_u32 s72,s74,0\ns_cbranch_vccz .Lrz00\ns_mov_b64 exec,vcc\ns_mov_b32 s97,1\nbuffer_load_dwordx4 v[128:131],v240,s[52:55],s72 offen sc1\ns_mov_b64 exec,-1\n.Lrz00:\n" \
"v_max3_u32 v60,v132,v133,v134\nv_max3_u32 v60,v60,v135,v135\nv_cmp_eq_u32 vcc,-1,v60\ns_add_u32 s72,s74,2097152\ns_cbranch_vccz .Lrz01\ns_mov_b64 exec,vcc\ns_mov_b32 s97,1\nbuffer_load_dwordx4 v[132:135],v240,s[52:55],s72 offen sc1\n" \
"s_mov_b64 exec,-1\n.Lrz01:\nv_max3_u32 v60,v136,v137,v138\nv_max3_u32 v60,v60,v139,v139\nv_cmp_eq_u32 vcc,-1,v60\ns_add_u32 s72,s74,4194304\ns_cbranch_vccz .Lrz02\ns_mov_b64 exec,vcc\n" \
"s_mov_b32 s97,1\nbuffer_load_dwordx4 v[136:139],v240,s[52:55],s72 offen sc1\ns_mov_b64 exec,-1\n.Lrz02:\nv_max3_u32 v60,v140,v141,v142\nv_max3_u32 v60,v60,v143,v143\nv_cmp_eq_u32 vcc,-1,v60\ns_add_u32 s72,s74,6291456\n" \
"s_cbranch_vccz .Lrz03\ns_mov_b64 exec,vcc\ns_mov_b32 s97,1\nbuffer_load_dwordx4 v[140:143],v240,s[52:55],s72 offen sc1\ns_mov_b64 exec,-1\n.Lrz03:\nv_max3_u32 v60,v144,v145,v146\nv_max3_u32 v60,v60,v147,v147\n" \
"v_cmp_eq_u32 vcc,-1,v60\ns_add_u32 s72,s74,8388608\ns_cbranch_vccz .Lrz04\ns_mov_b64 exec,vcc\ns_mov_b32 s97,1\nbuffer_load_dwordx4 v[144:147],v240,s[52:55],s72 offen sc1\ns_mov_b64 exec,-1\n.Lrz04:\n" \
"v_max3_u32 v60,v148,v149,v150\nv_max3_u32 v60,v60,v151,v151\nv_cmp_eq_u32 vcc,-1,v60\ns_add_u32 s72,s74,10485760\ns_cbranch_vccz .Lrz05\ns_mov_b64 exec,vcc\ns_mov_b32 s97,1\nbuffer_load_dwordx4 v[148:151],v240,s[52:55],s72 offen sc1\n" \
"s_mov_b64 exec,-1\n.Lrz05:\nv_max3_u32 v60,v152,v153,v154\nv_max3_u32 v60,v60,v155,v155\nv_cmp_eq_u32 vcc,-1,v60\ns_add_u32 s72,s74,12582912\ns_cbranch_vccz .Lrz06\ns_mov_b64 exec,vcc\n" \
"s_mov_b32 s97,1\nbuffer_load_dwordx4 v[152:155],v240,s[52:55],s72 offen sc1\ns_mov_b64 exec,-1\n.Lrz06:\nv_max3_u32 v60,v156,v157,v158\nv_max3_u32 v60,v60,v159,v159\nv_cmp_eq_u32 vcc,-1,v60\ns_add_u32 s72,s74,14680064\n" \
"s_cbranch_vccz .Lrz07\ns_mov_b64 exec,vcc\ns_mov_b32 s97,1\nbuffer_load_dwordx4 v[156:159],v240,s[52:55],s72 offen sc1\ns_mov_b64 exec,-1\n.Lrz07:\ns_cmp_lg_u32 s97,0\ns_cbranch_scc1 .Lrm0\n" \
"s_cmp_eq_u32 s71,0\ns_cbranch_scc1 .Lrb0\ns_branch .Lrow0\n.Lrm0:\ns_mov_b32 s71,1\ns_waitcnt vmcnt(0)\ns_sub_u32 s73,s73,1\ns_cselect_b32 s73,0,s73\n" \
"s_cbranch_scc1 .Lrb0\ns_sleep 2\ns_branch .Lrl0\n.Lredo:\ns_waitcnt vmcnt(0) lgkmcnt(0)\ns_barrier\ns_mov_b64 s[90:91],0\ns_mov_b32 s99,0\n" \
"s_mov_b32 s17,1\ns_lshl_b32 s70,s63,19\ns_add_u32 s37,s70,3145728\ns_lshl_b32 s71,s62,15\ns_add_u32 s72,s37,s64\ns_add_u32 s72,s72,491520\ns_sub_u32 s72,s72,s71\ns_add_u32 m0,s64,0\n" \
"s_nop 0\nbuffer_load_dwordx4 v240,s[52:55],s72 offen offset:0 sc1 lds\nbuffer_load_dwordx4 v240,s[52:55],s72 offen offset:1024 sc1 lds\nbuffer_load_dwordx4 v240,s[52:55],s72 offen offset:2048 sc1 lds\nbuffer_load_dwordx4 v240,s[52:55],s72 offen offset:3072 sc1 lds\ns_add_u32 m0,s64,65536\ns_add_u32 s72,s72,16777216\nbuffer_load_dwordx4 v240,s[52:55],s72 offen offset:0 sc1 lds\n" \
"buffer_load_dwordx4 v240,s[52:55],s72 offen offset:1024 sc1 lds\nbuffer_load_dwordx4 v240,s[52:55],s72 offen offset:2048 sc1 lds\nbuffer_load_dwordx4 v240,s[52:55],s72 offen offset:3072 sc1 lds\ns_add_u32 s72,s37,s64\ns_add_u32 s72,s72,507904\ns_sub_u32 s72,s72,s71\ns_add_u32 m0,s64,16384\ns_nop 0\n" \
"buffer_load_dwordx4 v240,s[52:55],s72 offen offset:0 sc1 lds\nbuffer_load_dwordx4 v240,s[52:55],s72 offen offset:1024 sc1 lds\nbuffer_load_dwordx4 v240,s[52:55],s72 offen offset:2048 sc1 lds\nbuffer_load_dwordx4 v240,s[52:55],s72 offen offset:3072 sc1 lds\ns_add_u32 m0,s64,81920\ns_add_u32 s72,s72,16777216\nbuffer_load_dwordx4 v240,s[52:55],s72 offen offset:0 sc1 lds\nbuffer_load_dwordx4 v240,s[52:55],s72 offen offset:1024 sc1 lds\n" \
"buffer_load_dwordx4 v240,s[52:55],s72 offen offset:2048 sc1 lds\nbuffer_load_dwordx4 v240,s[52:55],s72 offen offset:3072 sc1 lds\ns_add_u32 s72,s37,s64\ns_add_u32 s72,s72,16384\ns_add_u32 s72,s72,s71\ns_add_u32 m0,s64,32768\ns_nop 0\nbuffer_load_dwordx4 v240,s[52:55],s72 offen offset:0 sc1 lds\n" \
"buffer_load_dwordx4 v240,s[52:55],s72 offen offset:1024 sc1 lds\nbuffer_load_dwordx4 v240,s[52:55],s72 offen offset:2048 sc1 lds\nbuffer_load_dwordx4 v240,s[52:55],s72 offen offset:3072 sc1 lds\ns_add_u32 m0,s64,98304\ns_add_u32 s72,s72,16777216\nbuffer_load_dwordx4 v240,s[52:55],s72 offen offset:0 sc1 lds\nbuffer_load_dwordx4 v240,s[52:55],s72 offen offset:1024 sc1 lds\nbuffer_load_dwordx4 v240,s[52:55],s72 offen offset:2048 sc1 lds\n" \
"buffer_load_dwordx4 v240,s[52:55],s72 offen offset:3072 sc1 lds\ns_add_u32 s72,s37,s64\ns_add_u32 s72,s72,0\ns_add_u32 s72,s72,s71\ns_add_u32 m0,s64,49152\ns_nop 0\nbuffer_load_dwordx4 v240,s[52:55],s72 offen offset:0 sc1 lds\nbuffer_load_dwordx4 v240,s[52:55],s72 offen offset:1024 sc1 lds\n" \
"buffer_load_dwordx4 v240,s[52:55],s72 offen offset:2048 sc1 lds\nbuffer_load_dwordx4 v240,s[52:55],s72 offen offset:3072 sc1 lds\ns_add_u32 m0,s64,114688\ns_add_u32 s72,s72,16777216\nbuffer_load_dwordx4 v240,s[52:55],s72 offen offset:0 sc1 lds\nbuffer_load_dwordx4 v240,s[52:55],s72 offen offset:1024 sc1 lds\nbuffer_load_dwordx4 v240,s[52:55],s72 offen offset:2048 sc1 lds\nbuffer_load_dwordx4 v240,s[52:55],s72 offen offset:3072 sc1 lds\n" \
"v_and_b32 v251,15,v249\nv_lshrrev_b32 v250,4,v249\nv_lshlrev_b32 v250,2,v250\nv_sub_u32 v251,v251,v250\ns_lshl_b32 s70,s62,7\ns_lshl_b32 s72,s7,5\ns_sub_u32 s38,s72,s70\ns_add_u32 s38,s38,1920\n" \
"s_sub_u32 s39,s70,s72\ns_add_u32 s39,s39,96\nv_add_u32 v225,s38,v251\nv_add_u32 v225,0,v225\nv_mov_b32 v224,0xff800000\nv_add_u32 v229,s38,v251\nv_add_u32 v229,16,v229\nv_mov_b32 v228,0xff800000\n" \
"v_add_u32 v233,s39,v251\nv_add_u32 v233,0,v233\nv_mov_b32 v232,0xff800000\nv_add_u32 v237,s39,v251\nv_add_u32 v237,16,v237\nv_mov_b32 v236,0xff800000\ns_sub_u32 s38,524288,s71\ns_add_u32 s38,s38,s37\n" \
".Lrl:\nbuffer_load_dwordx4 a[0:3],v240,s[52:55],s37 offen offset:0 sc1\nbuffer_load_dwordx4 a[4:7],v240,s[52:55],s37 offen offset:1024 sc1\nbuffer_load_dwordx4 a[8:11],v240,s[52:55],s37 offen offset:2048 sc1\nbuffer_load_dwordx4 a[12:15],v240,s[52:55],s37 offen offset:3072 sc1\ns_add_u32 s37,s37,4096\nbuffer_load_dwordx4 a[16:19],v240,s[52:55],s37 offen offset:0 sc1\nbuffer_load_dwordx4 a[20:23],v240,s[52:55],s37 offen offset:1024 sc1\n" \
"buffer_load_dwordx4 a[24:27],v240,s[52:55],s37 offen offset:2048 sc1\nbuffer_load_dwordx4 a[28:31],v240,s[52:55],s37 offen offset:3072 sc1\ns_add_u32 s37,s37,4096\nbuffer_load_dwordx4 a[32:35],v240,s[52:55],s37 offen offset:0 sc1\nbuffer_load_dwordx4 a[36:39],v240,s[52:55],s37 offen offset:1024 sc1\nbuffer_load_dwordx4 a[40:43],v240,s[52:55],s37 offen offset:2048 sc1\nbuffer_load_dwordx4 a[44:47],v240,s[52:55],s37 offen offset:3072 sc1\ns_add_u32 s37,s37,4096\n" \
"buffer_load_dwordx4 a[48:51],v240,s[52:55],s37 offen offset:0 sc1\nbuffer_load_dwordx4 a[52:55],v240,s[52:55],s37 offen offset:1024 sc1\nbuffer_load_dwordx4 a[56:59],v240,s[52:55],s37 offen offset:2048 sc1\nbuffer_load_dwordx4 a[60:63],v240,s[52:55],s37 offen offset:3072 sc1\ns_add_u32 s37,s37,4096\ns_waitcnt vmcnt(15)\nM v[128:131],a[0:3],a[128:131],0\ns_waitcnt vmcnt(14)\n" \
"M v[128:131],a[4:7],a[132:135],v[128:131]\ns_waitcnt vmcnt(13)\nM v[128:131],a[8:11],a[136:139],v[128:131]\ns_waitcnt vmcnt(12)\nM v[128:131],a[12:15],a[140:143],v[128:131]\nM v[132:135],a[0:3],a[144:147],0\nM v[132:135],a[4:7],a[148:151],v[132:135]\nM v[132:135],a[8:11],a[152:155],v[132:135]\n" \
"M v[132:135],a[12:15],a[156:159],v[132:135]\nM v[136:139],a[0:3],a[160:163],0\nM v[136:139],a[4:7],a[164:167],v[136:139]\nM v[136:139],a[8:11],a[168:171],v[136:139]\nM v[136:139],a[12:15],a[172:175],v[136:139]\nM v[140:143],a[0:3],a[176:179],0\nM v[140:143],a[4:7],a[180:183],v[140:143]\nM v[140:143],a[8:11],a[184:187],v[140:143]\n" \
"M v[140:143],a[12:15],a[188:191],v[140:143]\ns_waitcnt vmcnt(11)\nM v[144:147],a[16:19],a[128:131],0\ns_waitcnt vmcnt(10)\nM v[144:147],a[20:23],a[132:135],v[144:147]\ns_waitcnt vmcnt(9)\nM v[144:147],a[24:27],a[136:139],v[144:147]\ns_waitcnt vmcnt(8)\n" \
"M v[144:147],a[28:31],a[140:143],v[144:147]\nM v[148:151],a[16:19],a[144:147],0\nM v[148:151],a[20:23],a[148:151],v[148:151]\nM v[148:151],a[24:27],a[152:155],v[148:151]\nM v[148:151],a[28:31],a[156:159],v[148:151]\nM v[152:155],a[16:19],a[160:163],0\nM v[152:155],a[20:23],a[164:167],v[152:155]\nM v[152:155],a[24:27],a[168:171],v[152:155]\n" \
"M v[152:155],a[28:31],a[172:175],v[152:155]\nM v[156:159],a[16:19],a[176:179],0\nM v[156:159],a[20:23],a[180:183],v[156:159]\nM v[156:159],a[24:27],a[184:187],v[156:159]\nM v[156:159],a[28:31],a[188:191],v[156:159]\ns_waitcnt vmcnt(7)\nM v[160:163],a[32:35],a[128:131],0\ns_waitcnt vmcnt(6)\n" \
"M v[160:163],a[36:39],a[132:135],v[160:163]\ns_waitcnt vmcnt(5)\nM v[160:163],a[40:43],a[136:139],v[160:163]\ns_waitcnt vmcnt(4)\nM v[160:163],a[44:47],a[140:143],v[160:163]\nM v[164:167],a[32:35],a[144:147],0\nM v[164:167],a[36:39],a[148:151],v[164:167]\nM v[164:167],a[40:43],a[152:155],v[164:167]\n" \
"M v[164:167],a[44:47],a[156:159],v[164:167]\nM v[168:171],a[32:35],a[160:163],0\nM v[168:171],a[36:39],a[164:167],v[168:171]\nM v[168:171],a[40:43],a[168:171],v[168:171]\nM v[168:171],a[44:47],a[172:175],v[168:171]\nM v[172:175],a[32:35],a[176:179],0\nM v[172:175],a[36:39],a[180:183],v[172:175]\nM v[172:175],a[40:43],a[184:187],v[172:175]\n" \
"M v[172:175],a[44:47],a[188:191],v[172:175]\ns_waitcnt vmcnt(3)\nM v[176:179],a[48:51],a[128:131],0\ns_waitcnt vmcnt(2)\nM v[176:179],a[52:55],a[132:135],v[176:179]\ns_waitcnt vmcnt(1)\nM v[176:179],a[56:59],a[136:139],v[176:179]\ns_waitcnt vmcnt(0)\n" \
"M v[176:179],a[60:63],a[140:143],v[176:179]\nM v[180:183],a[48:51],a[144:147],0\nM v[180:183],a[52:55],a[148:151],v[180:183]\nM v[180:183],a[56:59],a[152:155],v[180:183]\nM v[180:183],a[60:63],a[156:159],v[180:183]\nM v[184:187],a[48:51],a[160:163],0\nM v[184:187],a[52:55],a[164:167],v[184:187]\nM v[184:187],a[56:59],a[168:171],v[184:187]\n" \
"M v[184:187],a[60:63],a[172:175],v[184:187]\nM v[188:191],a[48:51],a[176:179],0\nM v[188:191],a[52:55],a[180:183],v[188:191]\nM v[188:191],a[56:59],a[184:187],v[188:191]\nM v[188:191],a[60:63],a[188:191],v[188:191]\nv_cmp_le_i32 vcc,0,v225\ns_nop 1\nv_cndmask_b32 v128,v252,v128,vcc\n" \
"v_max_f32 v224,v224,v128\nv_cmp_le_i32 vcc,1,v225\ns_nop 1\nv_cndmask_b32 v129,v252,v129,vcc\nv_max_f32 v224,v224,v129\nv_cmp_le_i32 vcc,2,v225\ns_nop 1\nv_cndmask_b32 v130,v252,v130,vcc\n" \
"v_max_f32 v224,v224,v130\nv_cmp_le_i32 vcc,3,v225\ns_nop 1\nv_cndmask_b32 v131,v252,v131,vcc\nv_max_f32 v224,v224,v131\nv_cmp_le_i32 vcc,0,v229\ns_nop 1\nv_cndmask_b32 v132,v252,v132,vcc\n" \
"v_max_f32 v228,v228,v132\nv_cmp_le_i32 vcc,1,v229\ns_nop 1\nv_cndmask_b32 v133,v252,v133,vcc\nv_max_f32 v228,v228,v133\nv_cmp_le_i32 vcc,2,v229\ns_nop 1\nv_cndmask_b32 v134,v252,v134,vcc\n" \
"v_max_f32 v228,v228,v134\nv_cmp_le_i32 vcc,3,v229\ns_nop 1\nv_cndmask_b32 v135,v252,v135,vcc\nv_max_f32 v228,v228,v135\nv_cmp_le_i32 vcc,0,v233\ns_nop 1\nv_cndmask_b32 v136,v252,v136,vcc\n" \
"v_max_f32 v232,v232,v136\nv_cmp_le_i32 vcc,1,v233\ns_nop 1\nv_cndmask_b32 v137,v252,v137,vcc\nv_max_f32 v232,v232,v137\nv_cmp_le_i32 vcc,2,v233\ns_nop 1\nv_cndmask_b32 v138,v252,v138,vcc\n" \
"v_max_f32 v232,v232,v138\nv_cmp_le_i32 vcc,3,v233\ns_nop 1\nv_cndmask_b32 v139,v252,v139,vcc\nv_max_f32 v232,v232,v139\nv_cmp_le_i32 vcc,0,v237\ns_nop 1\nv_cndmask_b32 v140,v252,v140,vcc\n" \
"v_max_f32 v236,v236,v140\nv_cmp_le_i32 vcc,1,v237\ns_nop 1\nv_cndmask_b32 v141,v252,v141,vcc\nv_max_f32 v236,v236,v141\nv_cmp_le_i32 vcc,2,v237\ns_nop 1\nv_cndmask_b32 v142,v252,v142,vcc\n" \
"v_max_f32 v236,v236,v142\nv_cmp_le_i32 vcc,3,v237\ns_nop 1\nv_cndmask_b32 v143,v252,v143,vcc\nv_max_f32 v236,v236,v143\nv_cmp_le_i32 vcc,16,v225\ns_nop 1\nv_cndmask_b32 v144,v252,v144,vcc\n" \
"v_max_f32 v224,v224,v144\nv_cmp_le_i32 vcc,17,v225\ns_nop 1\nv_cndmask_b32 v145,v252,v145,vcc\nv_max_f32 v224,v224,v145\nv_cmp_le_i32 vcc,18,v225\ns_nop 1\nv_cndmask_b32 v146,v252,v146,vcc\n" \
"v_max_f32 v224,v224,v146\nv_cmp_le_i32 vcc,19,v225\ns_nop 1\nv_cndmask_b32 v147,v252,v147,vcc\nv_max_f32 v224,v224,v147\nv_cmp_le_i32 vcc,16,v229\ns_nop 1\nv_cndmask_b32 v148,v252,v148,vcc\n" \
"v_max_f32 v228,v228,v148\nv_cmp_le_i32 vcc,17,v229\ns_nop 1\nv_cndmask_b32 v149,v252,v149,vcc\nv_max_f32 v228,v228,v149\nv_cmp_le_i32 vcc,18,v229\ns_nop 1\nv_cndmask_b32 v150,v252,v150,vcc\n" \
"v_max_f32 v228,v228,v150\nv_cmp_le_i32 vcc,19,v229\ns_nop 1\nv_cndmask_b32 v151,v252,v151,vcc\nv_max_f32 v228,v228,v151\nv_cmp_le_i32 vcc,16,v233\ns_nop 1\nv_cndmask_b32 v152,v252,v152,vcc\n" \
"v_max_f32 v232,v232,v152\nv_cmp_le_i32 vcc,17,v233\ns_nop 1\nv_cndmask_b32 v153,v252,v153,vcc\nv_max_f32 v232,v232,v153\nv_cmp_le_i32 vcc,18,v233\ns_nop 1\nv_cndmask_b32 v154,v252,v154,vcc\n" \
"v_max_f32 v232,v232,v154\nv_cmp_le_i32 vcc,19,v233\ns_nop 1\nv_cndmask_b32 v155,v252,v155,vcc\nv_max_f32 v232,v232,v155\nv_cmp_le_i32 vcc,16,v237\ns_nop 1\nv_cndmask_b32 v156,v252,v156,vcc\n" \
"v_max_f32 v236,v236,v156\nv_cmp_le_i32 vcc,17,v237\ns_nop 1\nv_cndmask_b32 v157,v252,v157,vcc\nv_max_f32 v236,v236,v157\nv_cmp_le_i32 vcc,18,v237\ns_nop 1\nv_cndmask_b32 v158,v252,v158,vcc\n" \
"v_max_f32 v236,v236,v158\nv_cmp_le_i32 vcc,19,v237\ns_nop 1\nv_cndmask_b32 v159,v252,v159,vcc\nv_max_f32 v236,v236,v159\nv_cmp_le_i32 vcc,32,v225\ns_nop 1\nv_cndmask_b32 v160,v252,v160,vcc\n" \
"v_max_f32 v224,v224,v160\nv_cmp_le_i32 vcc,33,v225\ns_nop 1\nv_cndmask_b32 v161,v252,v161,vcc\nv_max_f32 v224,v224,v161\nv_cmp_le_i32 vcc,34,v225\ns_nop 1\nv_cndmask_b32 v162,v252,v162,vcc\n" \
"v_max_f32 v224,v224,v162\nv_cmp_le_i32 vcc,35,v225\ns_nop 1\nv_cndmask_b32 v163,v252,v163,vcc\nv_max_f32 v224,v224,v163\nv_cmp_le_i32 vcc,32,v229\ns_nop 1\nv_cndmask_b32 v164,v252,v164,vcc\n" \
"v_max_f32 v228,v228,v164\nv_cmp_le_i32 vcc,33,v229\ns_nop 1\nv_cndmask_b32 v165,v252,v165,vcc\nv_max_f32 v228,v228,v165\nv_cmp_le_i32 vcc,34,v229\ns_nop 1\nv_cndmask_b32 v166,v252,v166,vcc\n" \
"v_max_f32 v228,v228,v166\nv_cmp_le_i32 vcc,35,v229\ns_nop 1\nv_cndmask_b32 v167,v252,v167,vcc\nv_max_f32 v228,v228,v167\nv_cmp_le_i32 vcc,32,v233\ns_nop 1\nv_cndmask_b32 v168,v252,v168,vcc\n" \
"v_max_f32 v232,v232,v168\nv_cmp_le_i32 vcc,33,v233\ns_nop 1\nv_cndmask_b32 v169,v252,v169,vcc\nv_max_f32 v232,v232,v169\nv_cmp_le_i32 vcc,34,v233\ns_nop 1\nv_cndmask_b32 v170,v252,v170,vcc\n" \
"v_max_f32 v232,v232,v170\nv_cmp_le_i32 vcc,35,v233\ns_nop 1\nv_cndmask_b32 v171,v252,v171,vcc\nv_max_f32 v232,v232,v171\nv_cmp_le_i32 vcc,32,v237\ns_nop 1\nv_cndmask_b32 v172,v252,v172,vcc\n" \
"v_max_f32 v236,v236,v172\nv_cmp_le_i32 vcc,33,v237\ns_nop 1\nv_cndmask_b32 v173,v252,v173,vcc\nv_max_f32 v236,v236,v173\nv_cmp_le_i32 vcc,34,v237\ns_nop 1\nv_cndmask_b32 v174,v252,v174,vcc\n" \
"v_max_f32 v236,v236,v174\nv_cmp_le_i32 vcc,35,v237\ns_nop 1\nv_cndmask_b32 v175,v252,v175,vcc\nv_max_f32 v236,v236,v175\nv_cmp_le_i32 vcc,48,v225\ns_nop 1\nv_cndmask_b32 v176,v252,v176,vcc\n" \
"v_max_f32 v224,v224,v176\nv_cmp_le_i32 vcc,49,v225\ns_nop 1\nv_cndmask_b32 v177,v252,v177,vcc\nv_max_f32 v224,v224,v177\nv_cmp_le_i32 vcc,50,v225\ns_nop 1\nv_cndmask_b32 v178,v252,v178,vcc\n" \
"v_max_f32 v224,v224,v178\nv_cmp_le_i32 vcc,51,v225\ns_nop 1\nv_cndmask_b32 v179,v252,v179,vcc\nv_max_f32 v224,v224,v179\nv_cmp_le_i32 vcc,48,v229\ns_nop 1\nv_cndmask_b32 v180,v252,v180,vcc\n" \
"v_max_f32 v228,v228,v180\nv_cmp_le_i32 vcc,49,v229\ns_nop 1\nv_cndmask_b32 v181,v252,v181,vcc\nv_max_f32 v228,v228,v181\nv_cmp_le_i32 vcc,50,v229\ns_nop 1\nv_cndmask_b32 v182,v252,v182,vcc\n" \
"v_max_f32 v228,v228,v182\nv_cmp_le_i32 vcc,51,v229\ns_nop 1\nv_cndmask_b32 v183,v252,v183,vcc\nv_max_f32 v228,v228,v183\nv_cmp_le_i32 vcc,48,v233\ns_nop 1\nv_cndmask_b32 v184,v252,v184,vcc\n" \
"v_max_f32 v232,v232,v184\nv_cmp_le_i32 vcc,49,v233\ns_nop 1\nv_cndmask_b32 v185,v252,v185,vcc\nv_max_f32 v232,v232,v185\nv_cmp_le_i32 vcc,50,v233\ns_nop 1\nv_cndmask_b32 v186,v252,v186,vcc\n" \
"v_max_f32 v232,v232,v186\nv_cmp_le_i32 vcc,51,v233\ns_nop 1\nv_cndmask_b32 v187,v252,v187,vcc\nv_max_f32 v232,v232,v187\nv_cmp_le_i32 vcc,48,v237\ns_nop 1\nv_cndmask_b32 v188,v252,v188,vcc\n" \
"v_max_f32 v236,v236,v188\nv_cmp_le_i32 vcc,49,v237\ns_nop 1\nv_cndmask_b32 v189,v252,v189,vcc\nv_max_f32 v236,v236,v189\nv_cmp_le_i32 vcc,50,v237\ns_nop 1\nv_cndmask_b32 v190,v252,v190,vcc\n" \
"v_max_f32 v236,v236,v190\nv_cmp_le_i32 vcc,51,v237\ns_nop 1\nv_cndmask_b32 v191,v252,v191,vcc\nv_max_f32 v236,v236,v191\nv_subrev_u32 v225,64,v225\nv_subrev_u32 v229,64,v229\nv_subrev_u32 v233,64,v233\n" \
"v_subrev_u32 v237,64,v237\ns_cmp_lt_u32 s37,s38\ns_cbranch_scc1 .Lrl\ns_waitcnt vmcnt(0)\ns_barrier\nds_read_b128 a[0:3],v240 offset:0\nds_read_b128 a[4:7],v240 offset:1024\nds_read_b128 a[8:11],v240 offset:2048\n" \
"ds_read_b128 a[12:15],v240 offset:3072\nds_read_b128 a[16:19],v240 offset:4096\nds_read_b128 a[20:23],v240 offset:5120\nds_read_b128 a[24:27],v240 offset:6144\nds_read_b128 a[28:31],v240 offset:7168\nds_read_b128 a[32:35],v240 offset:8192\nds_read_b128 a[36:39],v240 offset:9216\nds_read_b128 a[40:43],v240 offset:10240\n" \
"ds_read_b128 a[44:47],v240 offset:11264\nds_read_b128 a[48:51],v240 offset:12288\nds_read_b128 a[52:55],v240 offset:13312\nds_read_b128 a[56:59],v240 offset:14336\nds_read_b128 a[60:63],v240 offset:15360\ns_cmp_eq_u32 s62,0\ns_cselect_b32 s36,0x8000,0\ns_add_u32 s36,s36,s64\n" \
"s_cmp_eq_u32 s36,s96\ns_cselect_b32 s72,0xc000,s98\ns_add_u32 s65,s36,s72\ns_cmp_eq_u32 s65,s96\ns_cselect_b32 s72,0xc000,s98\ns_add_u32 s66,s65,s72\ns_cmp_eq_u32 s66,s96\ns_cselect_b32 s72,0xc000,s98\n" \
"s_add_u32 s95,s66,s72\ns_add_u32 s97,s96,65536\ns_add_u32 s15,s64,s46\ns_cmp_gt_i32 s62,-1\ns_cselect_b32 s72,s98,0xfffe4000\ns_add_u32 s15,s15,s72\ns_mov_b32 s94,0xc000\ns_cmp_eq_u32 s62,7\n" \
"s_cselect_b32 s72,98304,0\ns_sub_u32 s97,s97,s72\ns_cmp_lt_u32 s62,4\ns_cselect_b32 s72,32768,0\ns_add_u32 s97,s97,s72\nv_and_b32 v251,31,v249\nv_lshlrev_b32 v251,2,v251\ns_lshl_b32 s80,s63,7\n" \
"s_add_u32 s80,s80,86016\nv_add_u32 v250,0xc000,v241\ns_waitcnt lgkmcnt(0)\ns_branch .Lattn\n.Lfin:\n" \
""
constexpr size_t WS_DBG = (size_t)51 << 20;
constexpr unsigned LDS_BYTES = 163840;
struct Args {
    const u16 *x, *ckv, *w_q, *w_uk, *w_uv, *w_o, *w_r, *wg, *wu, *wd, *cvec, *w_mod;
    char* ws; u16* out;
};
typedef const __attribute__((address_space(4))) Args* kargs_t;
#define STAMPD(i) do {} while (0)
#define C8(p, a) p #a "0", p #a "1", p #a "2", p #a "3", p #a "4", p #a "5", p #a "6", p #a "7", p #a "8", p #a "9"
#define C100(p, a) C8(p, a##0), C8(p, a##1), C8(p, a##2), C8(p, a##3), C8(p, a##4), C8(p, a##5), C8(p, a##6), C8(p, a##7), C8(p, a##8), C8(p, a##9)
#define C10_99(p) C8(p, 1), C8(p, 2), C8(p, 3), C8(p, 4), C8(p, 5), C8(p, 6), C8(p, 7), C8(p, 8), C8(p, 9)
#define C200_249(p) C8(p, 20), C8(p, 21), C8(p, 22), C8(p, 23), C8(p, 24)
#define BODY_CLOB "memory", "vcc", "scc", "s0", "s1", "s2", "s3", "s8", "s9", C8("s", 1), C8("s", 2), "s30", "s31", "s33", "s34", "s35", "s36", "s37", "s38", "s39", C8("s", 4), C8("s", 5), C8("s", 6), C8("s", 7), C8("s", 8), C8("s", 9), \
    C8("v", ), C10_99("v"), C100("v", 1), C200_249("v"), "v250", "v251", "v252", "v253", "v254", C8("a", ), C10_99("a"), C100("a", 1), C200_249("a"), "a250", "a251", "a252", "a253", "a254", "a255"

__global__ __launch_bounds__(256, 1) void k_all(const Args a_) {
    kargs_t ap = (kargs_t)__builtin_amdgcn_kernarg_segment_ptr(); unsigned bid = blockIdx.x, wid = rfl(threadIdx.x >> 6), sv_;
    asm volatile(BODY_ASM : "+{s[4:5]}"(ap), "+{s6}"(bid), "+{s7}"(wid), "={v255}"(sv_) :: BODY_CLOB);
    asm volatile(".Lcode_e:");
}
extern "C" void kernel_launch(void* const* d_in, const int* in_sizes, int n_in, void* d_out, int out_size, void* d_ws, size_t ws_size, hipStream_t stream) {
    Args a;
    a.x = (const u16*)d_in[0]; a.ckv = (const u16*)d_in[1]; a.w_q = (const u16*)d_in[2]; a.w_uk = (const u16*)d_in[3]; a.w_uv = (const u16*)d_in[4]; a.w_o = (const u16*)d_in[5];
    a.w_r = (const u16*)d_in[7]; a.wg = (const u16*)d_in[8]; a.wu = (const u16*)d_in[9]; a.wd = (const u16*)d_in[10]; a.cvec = (const u16*)d_in[11]; a.w_mod = (const u16*)d_in[12];
    a.ws = (char*)d_ws;
    a.out = (u16*)d_out;
    (void)hipFuncSetAttribute((const void*)k_all, hipFuncAttributeMaxDynamicSharedMemorySize, LDS_BYTES);
    k_all<<<256, 256, LDS_BYTES, stream>>>(a);
}
```

```cpp
#include <hip/hip_runtime.h>
#include <hip/hip_bf16.h>
#include <stdint.h>
typedef unsigned short u16;
#define DI __device__ __forceinline__
DI unsigned lane_id() { unsigned l; asm volatile("v_mbcnt_lo_u32_b32 %0, -1, 0\n\tv_mbcnt_hi_u32_b32 %0, -1, %0" : "=v"(l)); return l; }
DI unsigned rfl(unsigned x) { return __builtin_amdgcn_readfirstlane(x); }
#define BODY_ASM ".macro M a,b,c,d\nv_mfma_f32_16x16x32_bf16 \\a,\\b,\\c,\\d\n.endm\n" \
".Lcode_b:\ns_load_dwordx16 s[8:23],s[4:5],0x0\ns_load_dwordx8 s[24:31],s[4:5],0x40\ns_load_dwordx2 s[34:35],s[4:5],0x60\nv_mbcnt_lo_u32_b32 v249,-1,0\nv_mbcnt_hi_u32_b32 v249,-1,v249\ns_bfe_u32 s61,s6,0x20003\ns_lshl_b32 s64,s7,12\n" \
"s_and_b32 s60,s6,7\ns_lshr_b32 s62,s6,5\ns_lshl2_add_u32 s63,s60,s61\nv_lshlrev_b32 v240,4,v249\nv_lshrrev_b32 v0,3,v249\nv_and_b32 v2,7,v249\nv_bfe_u32 v6,v249,2,3\nv_lshrrev_b32 v7,5,v249\n" \
"v_and_b32 v8,48,v240\nv_lshlrev_b32 v6,13,v6\nv_lshl_or_b32 v7,v7,6,v8\nv_or_b32 v6,0x1000,v6\nv_or_b32 v243,v6,v7\nv_add_u32 v244,0xfffffc80,v243\nv_xor_b32 v6,32,v243\nv_add_u32 v245,63488,v6\n" \
"v_add_u32 v246,62592,v6\ns_lshl_b32 s68,s7,17\ns_lshl_b32 s70,s63,8\ns_add_u32 s68,s68,s70\ns_mov_b32 s42,4198400\ns_mov_b32 s43,0x20000\nv_lshrrev_b32 v6,4,v249\nv_xor_b32 v7,v6,v2\n" \
"v_lshlrev_b32 v7,4,v7\nv_lshl_or_b32 v224,v0,10,v7\nv_xor_b32 v7,64,v224\nv_add_u32 v225,7168,v7\nv_add_u32 v226,14336,v224\nv_add_u32 v227,21504,v7\ns_lshl_b32 s70,s62,1\ns_sub_u32 s71,s7,s70\n" \
"s_add_u32 s71,s71,30\ns_sub_u32 s72,s70,s7\ns_add_u32 s72,s72,3\ns_cmp_lt_u32 s7,2\ns_cselect_b32 s74,s71,s72\ns_lshl_b32 s66,s74,16\ns_mul_i32 s65,s7,24576\ns_mov_b32 s38,2097152\n" \
"s_mov_b32 s39,0x20000\ns_xor_b32 s94,s61,s62\ns_and_b32 s94,s94,1\ns_lshl_b32 s99,s94,19\ns_or_b32 s99,s99,s68\ns_waitcnt lgkmcnt(0)\ns_sub_u32 s40,s14,0x1000\ns_subb_u32 s41,s15,0\n" \
"s_mov_b64 s[36:37],s[10:11]\ns_add_u32 m0,s64,147456\ns_add_u32 s69,s68,0\nbuffer_load_dwordx4 v243,s[40:43],s69 offen offset:0 lds\nbuffer_load_dwordx4 v244,s[40:43],s69 offen offset:1024 lds\nbuffer_load_dwordx4 v245,s[40:43],s69 offen offset:2048 lds\nbuffer_load_dwordx4 v246,s[40:43],s69 offen offset:3072 lds\ns_add_u32 m0,s65,0\n" \
"s_xor_b32 s67,s66,0\nbuffer_load_dwordx4 v224,s[36:39],s67 offen offset:0 lds\nbuffer_load_dwordx4 v225,s[36:39],s67 offen offset:1024 lds\nbuffer_load_dwordx4 v226,s[36:39],s67 offen offset:2048 lds\nbuffer_load_dwordx4 v227,s[36:39],s67 offen offset:3072 lds\ns_add_u32 m0,s65,4096\ns_xor_b32 s67,s66,32768\nbuffer_load_dwordx4 v224,s[36:39],s67 offen offset:0 lds\n" \
"buffer_load_dwordx4 v225,s[36:39],s67 offen offset:1024 lds\nbuffer_load_dwordx4 v226,s[36:39],s67 offen offset:2048 lds\nbuffer_load_dwordx4 v227,s[36:39],s67 offen offset:3072 lds\ns_cmp_lt_u32 s6,192\ns_cselect_b32 s70,s6,0\ns_mul_i32 s71,s70,171\ns_lshr_b32 s71,s71,13\ns_mul_i32 s72,s71,48\n" \
"s_sub_u32 s70,s70,s72\ns_lshl2_add_u32 s72,s71,s7\ns_lshl_b32 s72,s72,5\nv_lshl_add_u32 v1,v0,2,s72\nv_mul_u32_u24 v3,6144,v1\nv_lshl_add_u32 v3,v2,4,v3\ns_lshl_b32 s72,s70,7\ns_add_u32 s72,s72,3072\n" \
"v_add_u32 v3,s72,v3\nv_add_u32 v4,12288,v3\nv_lshlrev_b32 v1,1,v1\nglobal_load_dwordx2 v[168:169],v1,s[28:29]\nglobal_load_dwordx4 v[152:155],v3,s[30:31] offset:-3072\nglobal_load_dwordx4 v[156:159],v3,s[30:31] offset:3072\nglobal_load_dwordx4 v[160:163],v4,s[30:31] offset:-3072\nglobal_load_dwordx4 v[164:167],v4,s[30:31] offset:3072\n" \
"s_mul_i32 s71,s71,12288\ns_lshl_b32 s70,s70,8\ns_add_u32 s79,s70,s71\ns_add_u32 s79,s79,786432\ns_cmp_eq_u32 s7,0\ns_cselect_b32 s75,0,0x8000000\ns_cmp_lt_u32 s6,192\ns_cselect_b32 s70,s75,0x8000000\n" \
"s_or_b32 s79,s79,s70\ns_mov_b64 s[52:53],s[34:35]\ns_mov_b32 s54,71303168\ns_mov_b32 s55,0x20000\ns_mov_b32 s70,94208\nbuffer_load_dwordx2 v[230:231],off,s[52:55],s70 sc1\ns_getpc_b64 s[56:57]\n.Lpc0:\n" \
"s_and_b32 s57,s57,0xffff\ns_mov_b32 s58,.Lcode_e-.Lpc0\ns_mov_b32 s59,0x20000\ns_lshr_b32 s70,s6,3\nv_and_b32 v251,1,v249\ns_lshl2_add_u32 s78,s70,s7\nv_lshlrev_b32 v251,14,v251\ns_lshl_b32 s78,s78,7\n" \
"buffer_load_dword v247,v251,s[56:59],s78 offen\nv_and_b32 v34,15,v249\nv_lshrrev_b32 v35,1,v34\nv_xor_b32 v35,v35,v6\nv_lshlrev_b32 v35,4,v35\nv_lshl_or_b32 v35,v34,7,v35\nv_add_u32 v228,s65,v35\nv_xor_b32 v229,64,v228\n" \
"v_bfe_u32 v34,v249,2,2\nv_and_b32 v35,3,v249\nv_lshlrev_b32 v35,3,v35\nv_lshl_or_b32 v35,v34,6,v35\nv_lshl_or_b32 v35,v6,11,v35\nv_and_b32 v34,16,v249\nv_lshl_or_b32 v35,v34,1,v35\nv_add_u32 v241,98304,v35\n" \
"v_xor_b32 v242,32,v241\ns_mov_b32 s73,0x400000\nv_mov_b32 v252,0xff800000\ns_mov_b32 s82,0x05040100\ns_mov_b32 s83,0x07060302\nv_lshlrev_b32 v190,5,v2\nv_lshl_or_b32 v190,v6,3,v190\ns_lshl_b32 s72,s7,8\n" \
"s_add_u32 s72,s72,114688\nv_add_u32 v190,s72,v190\ns_lshl_b32 s72,s7,3\ns_add_u32 s72,s72,115712\nv_lshl_add_u32 v191,v2,5,s72\nv_lshlrev_b32 v251,5,v2\nv_add_u32 v251,115712,v251\nv_lshlrev_b32 v250,2,v249\n" \
"v_add_u32 v250,114688,v250\ns_lshl_b32 s70,s62,7\ns_xor_b32 s71,s63,12\ns_lshl3_add_u32 s71,s71,s70\ns_lshl_b32 s72,s63,3\ns_sub_u32 s72,s72,s70\ns_add_u32 s72,s72,1792\ns_cmp_lt_u32 s63,16\n" \
"s_cselect_b32 s71,s71,s72\ns_lshl_b32 s71,s71,10\ns_lshl_b32 s72,s7,7\ns_add_u32 s71,s71,s72\nv_lshlrev_b32 v5,10,v2\nv_lshl_add_u32 v5,v0,4,v5\nv_add_u32 v5,s71,v5\ns_lshl_b32 s70,s6,11\n" \
"s_cmp_lt_u32 s6,128\ns_cselect_b32 s70,s70,0x8000000\ns_lshl_b32 s71,s7,9\ns_add_u32 s70,s70,s71\ns_add_u32 s80,s70,98304\nv_mov_b32 v28,-1\nv_mov_b32 v29,-1\nv_lshlrev_b32 v30,3,v249\n" \
"s_lshl_b32 s70,s6,7\ns_cmp_lt_u32 s6,224\ns_cselect_b32 s71,65536,0x40000000\ns_cmp_lt_u32 s6,128\ns_cselect_b32 s72,556928,0x40000000\nv_mov_b32 v31,s71\nv_mov_b32 v32,s72\nv_cmp_lt_u32 vcc,31,v249\n" \
"s_or_b32 s70,s70,s75\nv_mov_b32 v33,0\nv_cndmask_b32 v31,v31,v32,vcc\nv_lshl_add_u32 v31,v249,2,v31\ns_add_u32 m0,s64,98304\ns_add_u32 s69,s68,524288\nbuffer_load_dwordx4 v243,s[40:43],s69 offen offset:0 lds\nbuffer_load_dwordx4 v244,s[40:43],s69 offen offset:1024 lds\n" \
"buffer_load_dwordx4 v245,s[40:43],s69 offen offset:2048 lds\nbuffer_load_dwordx4 v246,s[40:43],s69 offen offset:3072 lds\ns_add_u32 m0,s65,8192\ns_xor_b32 s67,s66,128\nbuffer_load_dwordx4 v224,s[36:39],s67 offen offset:0 lds\nbuffer_load_dwordx4 v225,s[36:39],s67 offen offset:1024 lds\nbuffer_load_dwordx4 v226,s[36:39],s67 offen offset:2048 lds\nbuffer_load_dwordx4 v227,s[36:39],s67 offen offset:3072 lds\n" \
"s_add_u32 m0,s65,12288\ns_xor_b32 s67,s66,32896\nbuffer_load_dwordx4 v224,s[36:39],s67 offen offset:0 lds\nbuffer_load_dwordx4 v225,s[36:39],s67 offen offset:1024 lds\nbuffer_load_dwordx4 v226,s[36:39],s67 offen offset:2048 lds\nbuffer_load_dwordx4 v227,s[36:39],s67 offen offset:3072 lds\nglobal_load_dwordx4 v[128:131],v5,s[8:9]\nglobal_load_dwordx4 v[132:135],v5,s[8:9] offset:512\n" \
"buffer_store_dwordx2 v[28:29],v30,s[52:55],s80 offen sc1\nbuffer_store_dword v33,v31,s[52:55],s70 offen sc1\ns_waitcnt vmcnt(23)\ns_barrier\ns_add_u32 m0,s64,131072\ns_add_u32 s69,s68,1048576\nds_read_b128 a[0:3],v228 offset:0\nds_read_b128 a[4:7],v228 offset:2048\n" \
"ds_read_b128 a[8:11],v228 offset:4096\nds_read_b128 a[12:15],v228 offset:6144\nds_read_b128 a[16:19],v229 offset:0\nds_read_b128 a[20:23],v229 offset:2048\nds_read_b128 a[24:27],v229 offset:4096\nds_read_b128 a[28:31],v229 offset:6144\nds_read_b64_tr_b16 v[192:193],v241 offset:49152\nds_read_b64_tr_b16 v[194:195],v241 offset:49408\n" \
"ds_read_b64_tr_b16 v[196:197],v241 offset:57344\nds_read_b64_tr_b16 v[198:199],v241 offset:57600\nds_read_b64_tr_b16 v[200:201],v242 offset:49152\nds_read_b64_tr_b16 v[202:203],v242 offset:49408\nds_read_b64_tr_b16 v[204:205],v242 offset:57344\nds_read_b64_tr_b16 v[206:207],v242 offset:57600\nds_read_b64_tr_b16 v[208:209],v241 offset:49664\nds_read_b64_tr_b16 v[210:211],v241 offset:49920\n" \
"ds_read_b64_tr_b16 v[212:213],v241 offset:57856\nds_read_b64_tr_b16 v[214:215],v241 offset:58112\nds_read_b64_tr_b16 v[216:217],v242 offset:49664\nds_read_b64_tr_b16 v[218:219],v242 offset:49920\nds_read_b64_tr_b16 v[220:221],v242 offset:57856\nds_read_b64_tr_b16 v[222:223],v242 offset:58112\ns_waitcnt vmcnt(22)\nv_lshlrev_b32 v170,16,v168\n" \
"v_and_b32 v171,0xffff0000,v168\nv_lshlrev_b32 v172,16,v169\nv_and_b32 v173,0xffff0000,v169\nv_mul_f32 v184,0xbfb8aa3b,v170\nv_mul_f32 v185,0xbfb8aa3b,v171\nv_mul_f32 v186,0xbfb8aa3b,v172\nv_mul_f32 v187,0xbfb8aa3b,v173\nv_exp_f32 v184,v184\n" \
"v_exp_f32 v185,v185\nv_exp_f32 v186,v186\nv_exp_f32 v187,v187\nv_add_f32 v184,1.0,v184\nv_add_f32 v185,1.0,v185\nv_add_f32 v186,1.0,v186\nv_add_f32 v187,1.0,v187\nv_rcp_f32 v184,v184\n" \
"v_rcp_f32 v185,v185\nv_rcp_f32 v186,v186\nv_rcp_f32 v187,v187\nv_mul_f32 v170,v170,v184\nv_mul_f32 v171,v171,v185\nv_mul_f32 v172,v172,v186\nv_mul_f32 v173,v173,v187\nv_cvt_pk_bf16_f32 v170,v170,v171\n" \
"v_cvt_pk_bf16_f32 v171,v172,v173\ns_waitcnt lgkmcnt(5)\nM v[0:3],v[192:195],a[0:3],0\nbuffer_load_dwordx4 v243,s[40:43],s69 offen offset:0 lds\nM v[0:3],v[196:199],a[16:19],v[0:3]\nv_bfe_u32 v253,v249,1,2\ns_lshl2_add_u32 s76,s62,s7\nv_and_b32 v248,1,v249\n" \
"v_lshlrev_b32 v253,13,v253\ns_mul_i32 s76,s76,32768\ns_lshl_b32 s77,s63,8\nM v[4:7],v[192:195],a[4:7],0\nv_lshl_or_b32 v253,v248,7,v253\ns_add_u32 s76,s76,s77\nv_and_b32 v254,7,v249\nv_add_u32 v253,s76,v253\n" \
"M v[4:7],v[196:199],a[20:23],v[4:7]\nv_and_b32 v234,7,v249\ns_lshl3_add_u32 s72,s6,s75\nM v[64:67],v[192:195],a[8:11],0\nv_lshlrev_b32 v234,11,v234\ns_add_u32 s72,s72,573440\nM v[64:67],v[196:199],a[24:27],v[64:67]\nbuffer_load_dwordx4 v244,s[40:43],s69 offen offset:1024 lds\n" \
"M v[68:71],v[192:195],a[12:15],0\ns_waitcnt vmcnt(22)\nv_perm_b32 v182,v156,v152,s82\nv_perm_b32 v183,v156,v152,s83\nv_perm_b32 v184,v157,v153,s82\nv_perm_b32 v185,v157,v153,s83\nv_perm_b32 v186,v158,v154,s82\nv_perm_b32 v187,v158,v154,s83\n" \
"v_perm_b32 v188,v159,v155,s82\nv_perm_b32 v189,v159,v155,s83\nv_dot2_f32_bf16 v174,v182,v170,0\nv_dot2_f32_bf16 v175,v183,v170,0\nv_dot2_f32_bf16 v176,v184,v170,0\nv_dot2_f32_bf16 v177,v185,v170,0\nv_dot2_f32_bf16 v178,v186,v170,0\nv_dot2_f32_bf16 v179,v187,v170,0\n" \
"v_dot2_f32_bf16 v180,v188,v170,0\nv_dot2_f32_bf16 v181,v189,v170,0\ns_waitcnt vmcnt(20)\nv_perm_b32 v182,v164,v160,s82\nv_perm_b32 v183,v164,v160,s83\nv_perm_b32 v184,v165,v161,s82\nv_perm_b32 v185,v165,v161,s83\nv_perm_b32 v186,v166,v162,s82\n" \
"v_perm_b32 v187,v166,v162,s83\nv_perm_b32 v188,v167,v163,s82\nv_perm_b32 v189,v167,v163,s83\nv_dot2_f32_bf16 v174,v182,v171,v174\nv_dot2_f32_bf16 v175,v183,v171,v175\nv_dot2_f32_bf16 v176,v184,v171,v176\nv_dot2_f32_bf16 v177,v185,v171,v177\nv_dot2_f32_bf16 v178,v186,v171,v178\n" \
"v_dot2_f32_bf16 v179,v187,v171,v179\nv_dot2_f32_bf16 v180,v188,v171,v180\nv_dot2_f32_bf16 v181,v189,v171,v181\nv_permlane32_swap_b32 v174,v178\nv_permlane32_swap_b32 v175,v179\nv_permlane32_swap_b32 v176,v180\nv_permlane32_swap_b32 v177,v181\nv_add_f32 v174,v174,v178\n" \
"v_add_f32 v175,v175,v179\nv_add_f32 v176,v176,v180\nv_add_f32 v177,v177,v181\nds_read_b64_tr_b16 v[192:193],v241 offset:50176\nv_permlane16_swap_b32 v174,v176\nv_permlane16_swap_b32 v175,v177\nv_add_f32 v174,v174,v176\nv_add_f32 v175,v175,v177\n" \
"ds_read_b64_tr_b16 v[194:195],v241 offset:50432\nv_add_f32_dpp v174,v174,v174 row_ror:8 row_mask:0xf bank_mask:0xf\nv_add_f32_dpp v175,v175,v175 row_ror:8 row_mask:0xf bank_mask:0xf\nds_write_b64 v190,v[174:175]\nM v[68:71],v[196:199],a[28:31],v[68:71]\nds_read_b64_tr_b16 v[196:197],v241 offset:58368\nds_read_b64_tr_b16 v[198:199],v241 offset:58624\nM v[8:11],v[200:203],a[0:3],0\n" \
"s_waitcnt vmcnt(18)\nv_readfirstlane_b32 s92,v230\nv_readfirstlane_b32 s93,v231\nM v[8:11],v[204:207],a[16:19],v[8:11]\ns_add_u32 s70,s92,1\ns_addc_u32 s71,s93,0\nM v[12:15],v[200:203],a[4:7],0\nbuffer_load_dwordx4 v245,s[40:43],s69 offen offset:2048 lds\n" \
"M v[12:15],v[204:207],a[20:23],v[12:15]\ns_add_u32 s92,s92,0x7f4a7c15\ns_addc_u32 s93,s93,0x9e3779b9\nM v[72:75],v[200:203],a[8:11],0\nv_mov_b32 v230,s92\nv_mov_b32 v231,s93\nM v[72:75],v[204:207],a[24:27],v[72:75]\ns_lshl2_add_u32 s80,s60,s7\n" \
"s_lshl_b32 s80,s80,9\nM v[76:79],v[200:203],a[12:15],0\nds_read_b64_tr_b16 v[200:201],v242 offset:50176\nds_read_b64_tr_b16 v[202:203],v242 offset:50432\nM v[76:79],v[204:207],a[28:31],v[76:79]\nbuffer_load_dwordx4 v246,s[40:43],s69 offen offset:3072 lds\nds_read_b64_tr_b16 v[204:205],v242 offset:58368\nds_read_b64_tr_b16 v[206:207],v242 offset:58624\n" \
"M v[16:19],v[208:211],a[0:3],0\ns_add_u32 s80,s80,573440\ns_waitcnt lgkmcnt(6)\nM v[16:19],v[212:215],a[16:19],v[16:19]\ns_add_u32 m0,s65,16384\ns_xor_b32 s67,s66,256\nM v[20:23],v[208:211],a[4:7],0\nv_mov_b32 v232,s70\n" \
"v_mov_b32 v233,s71\nM v[20:23],v[212:215],a[20:23],v[20:23]\ns_lshr_b32 s70,s64,2\ns_add_u32 s71,s70,786432\nM v[80:83],v[208:211],a[8:11],0\nbuffer_load_dwordx4 v224,s[36:39],s67 offen offset:0 lds\nM v[80:83],v[212:215],a[24:27],v[80:83]\nv_lshlrev_b32 v254,14,v254\n" \
"s_add_u32 s78,s78,32768\nM v[84:87],v[208:211],a[12:15],0\nds_read_b64_tr_b16 v[208:209],v241 offset:50688\nds_read_b64_tr_b16 v[210:211],v241 offset:50944\nM v[84:87],v[212:215],a[28:31],v[84:87]\nds_read_b64_tr_b16 v[212:213],v241 offset:58880\nds_read_b64_tr_b16 v[214:215],v241 offset:59136\nM v[24:27],v[216:219],a[0:3],0\n" \
"s_mov_b32 s98,0x4000\ns_mov_b32 s0,28672\nM v[24:27],v[220:223],a[16:19],v[24:27]\nbuffer_load_dwordx4 v225,s[36:39],s67 offen offset:1024 lds\nM v[28:31],v[216:219],a[4:7],0\ns_add_u32 s10,s61,1\ns_add_u32 s11,s61,2\nM v[28:31],v[220:223],a[20:23],v[28:31]\n" \
"s_add_u32 s14,s61,3\ns_and_b32 s10,s10,3\nM v[88:91],v[216:219],a[8:11],0\ns_and_b32 s11,s11,3\ns_and_b32 s14,s14,3\nM v[88:91],v[220:223],a[24:27],v[88:91]\ns_mov_b32 s47,0x20000\ns_mov_b32 s51,0x20000\n" \
"M v[92:95],v[216:219],a[12:15],0\nbuffer_load_dwordx4 v226,s[36:39],s67 offen offset:2048 lds\nds_read_b64_tr_b16 v[216:217],v242 offset:50688\nds_read_b64_tr_b16 v[218:219],v242 offset:50944\nM v[92:95],v[220:223],a[28:31],v[92:95]\nds_read_b64_tr_b16 v[220:221],v242 offset:58880\nds_read_b64_tr_b16 v[222:223],v242 offset:59136\nM v[32:35],v[192:195],a[0:3],0\n" \
"s_lshl_b32 s10,s10,13\ns_waitcnt lgkmcnt(6)\nM v[32:35],v[196:199],a[16:19],v[32:35]\ns_lshl_b32 s11,s11,13\ns_lshl_b32 s14,s14,13\nM v[36:39],v[192:195],a[4:7],0\ns_mov_b32 s82,0xf0000\ns_mov_b32 s83,0xfff00ff\n" \
"M v[36:39],v[196:199],a[20:23],v[36:39]\nbuffer_load_dwordx4 v227,s[36:39],s67 offen offset:3072 lds\nM v[96:99],v[192:195],a[8:11],0\ns_mov_b32 s84,0x1f0001\ns_mov_b32 s85,0x1fff01ff\nM v[96:99],v[196:199],a[24:27],v[96:99]\ns_add_u32 m0,s65,20480\ns_xor_b32 s67,s66,33024\n" \
"M v[100:103],v[192:195],a[12:15],0\ns_mov_b32 s86,0x3f0003\ns_mov_b32 s87,0x3fff03ff\nM v[100:103],v[196:199],a[28:31],v[100:103]\ns_mov_b32 s88,0x7f0007\ns_mov_b32 s89,0x7fff07ff\nM v[40:43],v[200:203],a[0:3],0\nbuffer_load_dwordx4 v224,s[36:39],s67 offen offset:0 lds\n" \
"s_waitcnt vmcnt(11)\nv_lshlrev_b32 v136,16,v128\nv_and_b32 v137,0xffff0000,v128\nv_lshlrev_b32 v138,16,v129\nv_and_b32 v139,0xffff0000,v129\nv_lshlrev_b32 v140,16,v130\nv_and_b32 v141,0xffff0000,v130\nv_lshlrev_b32 v142,16,v131\n" \
"v_and_b32 v143,0xffff0000,v131\nv_lshlrev_b32 v144,16,v132\nv_and_b32 v145,0xffff0000,v132\nv_lshlrev_b32 v146,16,v133\nv_and_b32 v147,0xffff0000,v133\nv_lshlrev_b32 v148,16,v134\nv_and_b32 v149,0xffff0000,v134\nv_lshlrev_b32 v150,16,v135\n" \
"v_and_b32 v151,0xffff0000,v135\nv_add_f32 v168,v136,v140\nv_add_f32 v128,v144,v148\nv_add_f32 v169,v137,v141\nv_add_f32 v129,v145,v149\nv_add_f32 v170,v138,v142\nv_add_f32 v130,v146,v150\nv_add_f32 v171,v139,v143\n" \
"v_add_f32 v131,v147,v151\nv_add_f32 v168,v168,v128\nv_add_f32 v169,v169,v129\nv_add_f32 v170,v170,v130\nv_add_f32 v171,v171,v131\nv_add_f32 v168,v168,v169\nv_add_f32 v170,v170,v171\nv_add_f32 v168,v168,v170\n" \
"M v[40:43],v[204:207],a[16:19],v[40:43]\ns_mov_b64 s[90:91],-1\nv_add_f32_dpp v168,v168,v168 row_ror:8 row_mask:0xf bank_mask:0xf\nv_mov_b32 v170,v168\nM v[44:47],v[200:203],a[4:7],0\ns_mov_b32 s28,0x5f800000\nv_permlane16_swap_b32 v168,v170\nv_add_f32 v168,v168,v170\n" \
"v_mov_b32 v170,v168\ns_nop 1\nv_permlane32_swap_b32 v168,v170\nv_add_f32 v168,v168,v170\nv_mul_f32 v168,0x3c000000,v168\nv_sub_f32 v152,v136,v168\nv_sub_f32 v153,v137,v168\nv_sub_f32 v154,v138,v168\n" \
"v_sub_f32 v155,v139,v168\nv_sub_f32 v156,v140,v168\nv_sub_f32 v157,v141,v168\nv_sub_f32 v158,v142,v168\nv_sub_f32 v159,v143,v168\nv_mul_f32 v128,v152,v152\nv_mul_f32 v129,v153,v153\nv_mul_f32 v130,v154,v154\n" \
"v_mul_f32 v131,v155,v155\nv_sub_f32 v160,v144,v168\nv_sub_f32 v161,v145,v168\nv_sub_f32 v162,v146,v168\nv_sub_f32 v163,v147,v168\nv_fmac_f32 v128,v156,v156\nv_fmac_f32 v129,v157,v157\nv_fmac_f32 v130,v158,v158\n" \
"v_fmac_f32 v131,v159,v159\nv_sub_f32 v164,v148,v168\nv_sub_f32 v165,v149,v168\nv_sub_f32 v166,v150,v168\nv_sub_f32 v167,v151,v168\nv_fmac_f32 v128,v160,v160\nv_fmac_f32 v129,v161,v161\nv_fmac_f32 v130,v162,v162\n" \
"v_fmac_f32 v131,v163,v163\nv_fmac_f32 v128,v164,v164\nv_fmac_f32 v129,v165,v165\nv_fmac_f32 v130,v166,v166\nv_fmac_f32 v131,v167,v167\nv_add_f32 v128,v128,v129\nv_add_f32 v130,v130,v131\nv_add_f32 v169,v128,v130\n" \
"s_nop 1\nv_add_f32_dpp v169,v169,v169 row_ror:8 row_mask:0xf bank_mask:0xf\nv_mov_b32 v170,v169\ns_nop 1\nv_permlane16_swap_b32 v169,v170\nv_add_f32 v169,v169,v170\nv_mov_b32 v170,v169\ns_nop 1\n" \
"v_permlane32_swap_b32 v169,v170\nv_add_f32 v169,v169,v170\nds_write_b64 v191,v[168:169]\nM v[44:47],v[204:207],a[20:23],v[44:47]\nv_and_b32 v169,3,v249\nv_mul_u32_u24 v169,12288,v169\nM v[104:107],v[200:203],a[8:11],0\nv_and_b32 v170,28,v249\n" \
"v_add_u32 v169,v169,v170\nM v[104:107],v[204:207],a[24:27],v[104:107]\nbuffer_load_dwordx4 v225,s[36:39],s67 offen offset:1024 lds\nM v[108:111],v[200:203],a[12:15],0\nv_lshlrev_b32 v170,3,v170\nM v[108:111],v[204:207],a[28:31],v[108:111]\nM v[48:51],v[208:211],a[0:3],0\ns_waitcnt lgkmcnt(1)\n" \
"M v[48:51],v[212:215],a[16:19],v[48:51]\nM v[52:55],v[208:211],a[4:7],0\nbuffer_load_dwordx4 v226,s[36:39],s67 offen offset:2048 lds\nM v[52:55],v[212:215],a[20:23],v[52:55]\nM v[112:115],v[208:211],a[8:11],0\ns_barrier\nds_read_b128 a[32:35],v228 offset:8192\nds_read_b128 a[36:39],v228 offset:10240\n" \
"ds_read_b128 a[40:43],v228 offset:12288\nds_read_b128 a[44:47],v228 offset:14336\nds_read_b128 a[48:51],v229 offset:8192\nds_read_b128 a[52:55],v229 offset:10240\nds_read_b128 a[56:59],v229 offset:12288\nds_read_b128 a[60:63],v229 offset:14336\nds_read_b32 v184,v250 offset:0\nds_read_b32 v185,v250 offset:256\n" \
"ds_read_b32 v186,v250 offset:512\nds_read_b32 v187,v250 offset:768\ns_waitcnt lgkmcnt(2)\nv_add_f32 v184,v184,v185\ns_waitcnt lgkmcnt(0)\nv_add_f32 v186,v186,v187\nv_lshlrev_b32 v185,2,v249\nv_add_f32 v184,v184,v186\n" \
"buffer_store_dword v184,v185,s[52:55],s79 offen sc1\nds_read_b64_tr_b16 v[192:193],v241 offset:0\nds_read_b64_tr_b16 v[194:195],v241 offset:256\nds_read_b64_tr_b16 v[196:197],v241 offset:8192\nds_read_b64_tr_b16 v[198:199],v241 offset:8448\nds_read_b64_tr_b16 v[200:201],v242 offset:0\nds_read_b64_tr_b16 v[202:203],v242 offset:256\nds_read_b64_tr_b16 v[204:205],v242 offset:8192\n" \
"ds_read_b64_tr_b16 v[206:207],v242 offset:8448\nM v[112:115],v[212:215],a[24:27],v[112:115]\nv_bfe_u32 v250,v249,3,2\nM v[116:119],v[208:211],a[12:15],0\nds_read_b64_tr_b16 v[208:209],v241 offset:512\nds_read_b64_tr_b16 v[210:211],v241 offset:768\nM v[116:119],v[212:215],a[28:31],v[116:119]\nbuffer_load_dwordx4 v227,s[36:39],s67 offen offset:3072 lds\n" \
"ds_read_b64_tr_b16 v[212:213],v241 offset:8704\nds_read_b64_tr_b16 v[214:215],v241 offset:8960\nM v[56:59],v[216:219],a[0:3],0\nM v[56:59],v[220:223],a[16:19],v[56:59]\ns_add_u32 m0,s65,0\ns_xor_b32 s67,s66,384\nM v[60:63],v[216:219],a[4:7],0\nM v[60:63],v[220:223],a[20:23],v[60:63]\n" \
"M v[120:123],v[216:219],a[8:11],0\nbuffer_load_dwordx4 v224,s[36:39],s67 offen offset:0 lds\nM v[120:123],v[220:223],a[24:27],v[120:123]\nM v[124:127],v[216:219],a[12:15],0\nds_read_b64_tr_b16 v[216:217],v242 offset:512\nds_read_b64_tr_b16 v[218:219],v242 offset:768\nM v[124:127],v[220:223],a[28:31],v[124:127]\nds_read_b64_tr_b16 v[220:221],v242 offset:8704\n" \
"ds_read_b64_tr_b16 v[222:223],v242 offset:8960\ns_waitcnt lgkmcnt(11)\nM v[0:3],v[192:195],a[32:35],v[0:3]\nM v[0:3],v[196:199],a[48:51],v[0:3]\nbuffer_load_dwordx4 v225,s[36:39],s67 offen offset:1024 lds\nM v[4:7],v[192:195],a[36:39],v[4:7]\nM v[4:7],v[196:199],a[52:55],v[4:7]\nM v[64:67],v[192:195],a[40:43],v[64:67]\n" \
"M v[64:67],v[196:199],a[56:59],v[64:67]\nM v[68:71],v[192:195],a[44:47],v[68:71]\nbuffer_load_dwordx4 v226,s[36:39],s67 offen offset:2048 lds\nds_read_b64_tr_b16 v[192:193],v241 offset:1024\nds_read_b64_tr_b16 v[194:195],v241 offset:1280\nM v[68:71],v[196:199],a[60:63],v[68:71]\nds_read_b64_tr_b16 v[196:197],v241 offset:9216\nds_read_b64_tr_b16 v[198:199],v241 offset:9472\n" \
"s_waitcnt lgkmcnt(8)\nM v[8:11],v[200:203],a[32:35],v[8:11]\nM v[8:11],v[204:207],a[48:51],v[8:11]\nM v[12:15],v[200:203],a[36:39],v[12:15]\nM v[12:15],v[204:207],a[52:55],v[12:15]\nbuffer_load_dwordx4 v227,s[36:39],s67 offen offset:3072 lds\nM v[72:75],v[200:203],a[40:43],v[72:75]\nM v[72:75],v[204:207],a[56:59],v[72:75]\n" \
"s_add_u32 m0,s65,4096\ns_xor_b32 s67,s66,33152\nM v[76:79],v[200:203],a[44:47],v[76:79]\nds_read_b64_tr_b16 v[200:201],v242 offset:1024\nds_read_b64_tr_b16 v[202:203],v242 offset:1280\nM v[76:79],v[204:207],a[60:63],v[76:79]\nds_read_b64_tr_b16 v[204:205],v242 offset:9216\nds_read_b64_tr_b16 v[206:207],v242 offset:9472\n" \
"M v[16:19],v[208:211],a[32:35],v[16:19]\nbuffer_load_dwordx4 v224,s[36:39],s67 offen offset:0 lds\nM v[16:19],v[212:215],a[48:51],v[16:19]\nM v[20:23],v[208:211],a[36:39],v[20:23]\nM v[20:23],v[212:215],a[52:55],v[20:23]\nM v[80:83],v[208:211],a[40:43],v[80:83]\nM v[80:83],v[212:215],a[56:59],v[80:83]\nbuffer_load_dwordx4 v225,s[36:39],s67 offen offset:1024 lds\n" \
"M v[84:87],v[208:211],a[44:47],v[84:87]\nds_read_b64_tr_b16 v[208:209],v241 offset:1536\nds_read_b64_tr_b16 v[210:211],v241 offset:1792\nM v[84:87],v[212:215],a[60:63],v[84:87]\nds_read_b64_tr_b16 v[212:213],v241 offset:9728\nds_read_b64_tr_b16 v[214:215],v241 offset:9984\ns_waitcnt lgkmcnt(8)\nM v[24:27],v[216:219],a[32:35],v[24:27]\n" \
"M v[24:27],v[220:223],a[48:51],v[24:27]\nM v[28:31],v[216:219],a[36:39],v[28:31]\nbuffer_load_dwordx4 v226,s[36:39],s67 offen offset:2048 lds\nM v[28:31],v[220:223],a[52:55],v[28:31]\nM v[88:91],v[216:219],a[40:43],v[88:91]\nM v[88:91],v[220:223],a[56:59],v[88:91]\nM v[92:95],v[216:219],a[44:47],v[92:95]\nds_read_b64_tr_b16 v[216:217],v242 offset:1536\n" \
"ds_read_b64_tr_b16 v[218:219],v242 offset:1792\nM v[92:95],v[220:223],a[60:63],v[92:95]\nbuffer_load_dwordx4 v227,s[36:39],s67 offen offset:3072 lds\nds_read_b64_tr_b16 v[220:221],v242 offset:9728\nds_read_b64_tr_b16 v[222:223],v242 offset:9984\nM v[32:35],v[192:195],a[32:35],v[32:35]\nM v[32:35],v[196:199],a[48:51],v[32:35]\ns_add_u32 m0,s64,147456\n" \
"s_add_u32 s69,s68,1572864\nM v[36:39],v[192:195],a[36:39],v[36:39]\nM v[36:39],v[196:199],a[52:55],v[36:39]\nM v[96:99],v[192:195],a[40:43],v[96:99]\nbuffer_load_dwordx4 v243,s[40:43],s69 offen offset:0 lds\nM v[96:99],v[196:199],a[56:59],v[96:99]\nM v[100:103],v[192:195],a[44:47],v[100:103]\nM v[100:103],v[196:199],a[60:63],v[100:103]\n" \
"s_waitcnt lgkmcnt(4)\nM v[40:43],v[200:203],a[32:35],v[40:43]\nM v[40:43],v[204:207],a[48:51],v[40:43]\nbuffer_load_dwordx4 v244,s[40:43],s69 offen offset:1024 lds\nM v[44:47],v[200:203],a[36:39],v[44:47]\nM v[44:47],v[204:207],a[52:55],v[44:47]\nM v[104:107],v[200:203],a[40:43],v[104:107]\nM v[104:107],v[204:207],a[56:59],v[104:107]\n" \
"M v[108:111],v[200:203],a[44:47],v[108:111]\nbuffer_load_dwordx4 v245,s[40:43],s69 offen offset:2048 lds\nM v[108:111],v[204:207],a[60:63],v[108:111]\nM v[48:51],v[208:211],a[32:35],v[48:51]\nM v[48:51],v[212:215],a[48:51],v[48:51]\nM v[52:55],v[208:211],a[36:39],v[52:55]\nM v[52:55],v[212:215],a[52:55],v[52:55]\nbuffer_load_dwordx4 v246,s[40:43],s69 offen offset:3072 lds\n" \
"M v[112:115],v[208:211],a[40:43],v[112:115]\ns_waitcnt vmcnt(12) lgkmcnt(0)\ns_barrier\nds_read_b128 a[64:67],v228 offset:16384\nds_read_b128 a[68:71],v228 offset:18432\nds_read_b128 a[72:75],v228 offset:20480\nds_read_b128 a[76:79],v228 offset:22528\nds_read_b128 a[80:83],v229 offset:16384\n" \
"ds_read_b128 a[84:87],v229 offset:18432\nds_read_b128 a[88:91],v229 offset:20480\nds_read_b128 a[92:95],v229 offset:22528\nM v[112:115],v[212:215],a[56:59],v[112:115]\ns_add_u32 m0,s65,8192\ns_xor_b32 s67,s66,512\nds_read_b64_tr_b16 v[192:193],v241 offset:32768\nds_read_b64_tr_b16 v[194:195],v241 offset:33024\n" \
"M v[116:119],v[208:211],a[44:47],v[116:119]\nbuffer_store_dwordx2 v[230:231],v234,s[52:55],s72 offen sc1\nds_read_b64_tr_b16 v[196:197],v241 offset:40960\nds_read_b64_tr_b16 v[198:199],v241 offset:41216\nM v[116:119],v[212:215],a[60:63],v[116:119]\ns_cmp_eq_u32 s6,0\ns_cselect_b32 s72,s75,0x8000000\nM v[56:59],v[216:219],a[32:35],v[56:59]\n" \
"buffer_load_dwordx4 v224,s[36:39],s67 offen offset:0 lds\nM v[56:59],v[220:223],a[48:51],v[56:59]\ns_add_u32 s72,s72,94208\nM v[60:63],v[216:219],a[36:39],v[60:63]\nM v[60:63],v[220:223],a[52:55],v[60:63]\nds_read_b64_tr_b16 v[200:201],v242 offset:32768\nds_read_b64_tr_b16 v[202:203],v242 offset:33024\nds_read_b64_tr_b16 v[204:205],v242 offset:40960\n" \
"ds_read_b64_tr_b16 v[206:207],v242 offset:41216\nds_read_b64_tr_b16 v[208:209],v241 offset:33280\nds_read_b64_tr_b16 v[210:211],v241 offset:33536\nds_read_b64_tr_b16 v[212:213],v241 offset:41472\nds_read_b64_tr_b16 v[214:215],v241 offset:41728\nM v[120:123],v[216:219],a[40:43],v[120:123]\nM v[120:123],v[220:223],a[56:59],v[120:123]\nbuffer_load_dwordx4 v225,s[36:39],s67 offen offset:1024 lds\n" \
"M v[124:127],v[216:219],a[44:47],v[124:127]\nds_read_b64_tr_b16 v[216:217],v242 offset:33280\nds_read_b64_tr_b16 v[218:219],v242 offset:33536\nM v[124:127],v[220:223],a[60:63],v[124:127]\nds_read_b64_tr_b16 v[220:221],v242 offset:41472\nds_read_b64_tr_b16 v[222:223],v242 offset:41728\ns_waitcnt lgkmcnt(14)\nM v[0:3],v[192:195],a[64:67],v[0:3]\n" \
"s_waitcnt lgkmcnt(12)\nM v[0:3],v[196:199],a[80:83],v[0:3]\nM v[4:7],v[192:195],a[68:71],v[4:7]\nbuffer_load_dwordx4 v226,s[36:39],s67 offen offset:2048 lds\nM v[4:7],v[196:199],a[84:87],v[4:7]\nds_read_b128 v[156:159],v251\nM v[64:67],v[192:195],a[72:75],v[64:67]\nds_read_b128 v[160:163],v251 offset:16\n" \
"v_lshrrev_b32 v251,3,v249\nM v[64:67],v[196:199],a[88:91],v[64:67]\nM v[68:71],v[192:195],a[76:79],v[68:71]\nds_read_b64_tr_b16 v[192:193],v241 offset:33792\nds_read_b64_tr_b16 v[194:195],v241 offset:34048\nM v[68:71],v[196:199],a[92:95],v[68:71]\nbuffer_load_dwordx4 v227,s[36:39],s67 offen offset:3072 lds\nds_read_b64_tr_b16 v[196:197],v241 offset:41984\n" \
"ds_read_b64_tr_b16 v[198:199],v241 offset:42240\ns_waitcnt lgkmcnt(10)\nM v[8:11],v[200:203],a[64:67],v[8:11]\nM v[8:11],v[204:207],a[80:83],v[8:11]\ns_add_u32 m0,s65,12288\ns_xor_b32 s67,s66,33280\nM v[12:15],v[200:203],a[68:71],v[12:15]\nM v[12:15],v[204:207],a[84:87],v[12:15]\n" \
"M v[72:75],v[200:203],a[72:75],v[72:75]\nbuffer_load_dwordx4 v224,s[36:39],s67 offen offset:0 lds\nM v[72:75],v[204:207],a[88:91],v[72:75]\ns_waitcnt lgkmcnt(5)\nv_add_f32 v164,v156,v158\nM v[76:79],v[200:203],a[76:79],v[76:79]\nds_read_b64_tr_b16 v[200:201],v242 offset:33792\nds_read_b64_tr_b16 v[202:203],v242 offset:34048\n" \
"M v[76:79],v[204:207],a[92:95],v[76:79]\nds_read_b64_tr_b16 v[204:205],v242 offset:41984\nds_read_b64_tr_b16 v[206:207],v242 offset:42240\nM v[16:19],v[208:211],a[64:67],v[16:19]\ns_waitcnt lgkmcnt(8)\nv_add_f32 v165,v160,v162\nM v[16:19],v[212:215],a[80:83],v[16:19]\nbuffer_load_dwordx4 v225,s[36:39],s67 offen offset:1024 lds\n" \
"M v[20:23],v[208:211],a[68:71],v[20:23]\nv_add_f32 v164,v164,v165\nv_lshlrev_b32 v165,3,v249\nM v[20:23],v[212:215],a[84:87],v[20:23]\nv_mul_f32 v164,0x3e800000,v164\nv_sub_f32 v156,v156,v164\nM v[80:83],v[208:211],a[72:75],v[80:83]\nv_sub_f32 v158,v158,v164\n" \
"v_mul_f32 v156,v156,v156\nM v[80:83],v[212:215],a[88:91],v[80:83]\nv_sub_f32 v160,v160,v164\nv_fmac_f32 v156,v158,v158\nM v[84:87],v[208:211],a[76:79],v[84:87]\nbuffer_load_dwordx4 v226,s[36:39],s67 offen offset:2048 lds\nds_read_b64_tr_b16 v[208:209],v241 offset:34304\nds_read_b64_tr_b16 v[210:211],v241 offset:34560\n" \
"M v[84:87],v[212:215],a[92:95],v[84:87]\nds_read_b64_tr_b16 v[212:213],v241 offset:42496\nds_read_b64_tr_b16 v[214:215],v241 offset:42752\nM v[24:27],v[216:219],a[64:67],v[24:27]\nv_add_f32 v157,v157,v159\nv_add_f32 v161,v161,v163\nM v[24:27],v[220:223],a[80:83],v[24:27]\nv_sub_f32 v162,v162,v164\n" \
"v_fmac_f32 v156,v160,v160\nM v[28:31],v[216:219],a[68:71],v[28:31]\nglobal_load_dword v247,v253,s[16:17]\nM v[28:31],v[220:223],a[84:87],v[28:31]\nbuffer_load_dwordx4 v227,s[36:39],s67 offen offset:3072 lds\nM v[88:91],v[216:219],a[72:75],v[88:91]\nv_add_u32 v253,0x100000,v253\nv_add_f32 v157,v157,v161\n" \
"M v[88:91],v[220:223],a[88:91],v[88:91]\ns_add_u32 m0,s64,98304\ns_add_u32 s69,s68,2097152\nM v[92:95],v[216:219],a[76:79],v[92:95]\nds_read_b64_tr_b16 v[216:217],v242 offset:34304\nds_read_b64_tr_b16 v[218:219],v242 offset:34560\nM v[92:95],v[220:223],a[92:95],v[92:95]\nds_read_b64_tr_b16 v[220:221],v242 offset:42496\n" \
"ds_read_b64_tr_b16 v[222:223],v242 offset:42752\ns_waitcnt lgkmcnt(8)\nM v[32:35],v[192:195],a[64:67],v[32:35]\nbuffer_load_dwordx4 v243,s[40:43],s69 offen offset:0 lds\nM v[32:35],v[196:199],a[80:83],v[32:35]\nv_fmac_f32 v156,v162,v162\nv_fmac_f32 v157,0x43000000,v156\nM v[36:39],v[192:195],a[68:71],v[36:39]\n" \
"v_mul_f32 v157,0x3b000000,v157\nv_cvt_pk_bf16_f32 v157,0,v157\nM v[36:39],v[196:199],a[84:87],v[36:39]\nv_add_f32 v157,0x3727c5ac,v157\nv_cvt_pk_bf16_f32 v157,0,v157\nM v[96:99],v[192:195],a[72:75],v[96:99]\nv_sqrt_f32 v157,v157\nM v[96:99],v[196:199],a[88:91],v[96:99]\n" \
"buffer_load_dwordx4 v244,s[40:43],s69 offen offset:1024 lds\nM v[100:103],v[192:195],a[76:79],v[100:103]\nv_cvt_pk_bf16_f32 v164,0,v164\nv_cvt_pk_bf16_f32 v157,0,v157\nM v[100:103],v[196:199],a[92:95],v[100:103]\nv_rcp_f32 v157,v157\nM v[40:43],v[200:203],a[64:67],v[40:43]\nv_sub_f32 v136,v136,v164\n" \
"v_sub_f32 v137,v137,v164\nM v[40:43],v[204:207],a[80:83],v[40:43]\nv_sub_f32 v138,v138,v164\nv_sub_f32 v139,v139,v164\nM v[44:47],v[200:203],a[68:71],v[44:47]\nbuffer_load_dwordx4 v245,s[40:43],s69 offen offset:2048 lds\nM v[44:47],v[204:207],a[84:87],v[44:47]\nv_sub_f32 v140,v140,v164\n" \
"v_sub_f32 v141,v141,v164\nM v[104:107],v[200:203],a[72:75],v[104:107]\nv_sub_f32 v142,v142,v164\nv_sub_f32 v143,v143,v164\nM v[104:107],v[204:207],a[88:91],v[104:107]\nv_sub_f32 v144,v144,v164\nv_sub_f32 v145,v145,v164\nM v[108:111],v[200:203],a[76:79],v[108:111]\n" \
"v_sub_f32 v146,v146,v164\nv_sub_f32 v147,v147,v164\nM v[108:111],v[204:207],a[92:95],v[108:111]\nbuffer_load_dwordx4 v246,s[40:43],s69 offen offset:3072 lds\ns_waitcnt lgkmcnt(0)\nM v[48:51],v[208:211],a[64:67],v[48:51]\nv_sub_f32 v148,v148,v164\nv_sub_f32 v149,v149,v164\n" \
"M v[48:51],v[212:215],a[80:83],v[48:51]\ns_add_u32 m0,s65,16384\ns_xor_b32 s67,s66,640\nM v[52:55],v[208:211],a[68:71],v[52:55]\nv_sub_f32 v150,v150,v164\nv_sub_f32 v151,v151,v164\nM v[52:55],v[212:215],a[84:87],v[52:55]\nv_cvt_pk_bf16_f32 v136,0,v136\n" \
"v_cvt_pk_bf16_f32 v137,0,v137\nM v[112:115],v[208:211],a[72:75],v[112:115]\nbuffer_load_dwordx4 v224,s[36:39],s67 offen offset:0 lds\ns_waitcnt vmcnt(15)\ns_barrier\nds_read_b128 a[96:99],v228 offset:0\nds_read_b128 a[100:103],v228 offset:2048\nds_read_b128 a[104:107],v228 offset:4096\n" \
"ds_read_b128 a[108:111],v228 offset:6144\nds_read_b128 a[112:115],v229 offset:0\nds_read_b128 a[116:119],v229 offset:2048\nds_read_b128 a[120:123],v229 offset:4096\nds_read_b128 a[124:127],v229 offset:6144\nM v[112:115],v[212:215],a[88:91],v[112:115]\nds_read_b64_tr_b16 v[192:193],v241 offset:49152\nds_read_b64_tr_b16 v[194:195],v241 offset:49408\n" \
"M v[116:119],v[208:211],a[76:79],v[116:119]\nds_read_b64_tr_b16 v[196:197],v241 offset:57344\nds_read_b64_tr_b16 v[198:199],v241 offset:57600\nM v[116:119],v[212:215],a[92:95],v[116:119]\nv_cvt_pk_bf16_f32 v138,0,v138\nv_cvt_pk_bf16_f32 v139,0,v139\nM v[56:59],v[216:219],a[64:67],v[56:59]\nv_cvt_pk_bf16_f32 v140,0,v140\n" \
"v_cvt_pk_bf16_f32 v141,0,v141\nM v[56:59],v[220:223],a[80:83],v[56:59]\nbuffer_load_dwordx4 v225,s[36:39],s67 offen offset:1024 lds\nM v[60:63],v[216:219],a[68:71],v[60:63]\nv_cvt_pk_bf16_f32 v142,0,v142\nv_cvt_pk_bf16_f32 v143,0,v143\nM v[60:63],v[220:223],a[84:87],v[60:63]\nv_cvt_pk_bf16_f32 v144,0,v144\n" \
"v_cvt_pk_bf16_f32 v145,0,v145\nM v[120:123],v[216:219],a[72:75],v[120:123]\nv_cvt_pk_bf16_f32 v146,0,v146\nv_cvt_pk_bf16_f32 v147,0,v147\nM v[120:123],v[220:223],a[88:91],v[120:123]\nds_read_b64_tr_b16 v[200:201],v242 offset:49152\nds_read_b64_tr_b16 v[202:203],v242 offset:49408\nM v[124:127],v[216:219],a[76:79],v[124:127]\n" \
"buffer_load_dwordx4 v226,s[36:39],s67 offen offset:2048 lds\nds_read_b64_tr_b16 v[204:205],v242 offset:57344\nds_read_b64_tr_b16 v[206:207],v242 offset:57600\nM v[124:127],v[220:223],a[92:95],v[124:127]\nds_read_b64_tr_b16 v[208:209],v241 offset:49664\ns_waitcnt lgkmcnt(7)\nM v[0:3],v[192:195],a[96:99],v[0:3]\nds_read_b64_tr_b16 v[210:211],v241 offset:49920\n" \
"s_waitcnt lgkmcnt(6)\nM v[0:3],v[196:199],a[112:115],v[0:3]\nds_read_b64_tr_b16 v[212:213],v241 offset:57856\nds_read_b64_tr_b16 v[214:215],v241 offset:58112\nM v[4:7],v[192:195],a[100:103],v[4:7]\nglobal_load_dword v247,v253,s[16:17]\nM v[4:7],v[196:199],a[116:119],v[4:7]\nbuffer_load_dwordx4 v227,s[36:39],s67 offen offset:3072 lds\n" \
"M v[64:67],v[192:195],a[104:107],v[64:67]\nds_read_b64_tr_b16 v[216:217],v242 offset:49664\nds_read_b64_tr_b16 v[218:219],v242 offset:49920\nM v[64:67],v[196:199],a[120:123],v[64:67]\ns_add_u32 m0,s65,20480\ns_xor_b32 s67,s66,33408\nM v[68:71],v[192:195],a[108:111],v[68:71]\nds_read_b64_tr_b16 v[220:221],v242 offset:57856\n" \
"ds_read_b64_tr_b16 v[222:223],v242 offset:58112\nM v[68:71],v[196:199],a[124:127],v[68:71]\nds_read_b64_tr_b16 v[192:193],v241 offset:50176\ns_waitcnt lgkmcnt(11)\nM v[8:11],v[200:203],a[96:99],v[8:11]\nbuffer_load_dwordx4 v224,s[36:39],s67 offen offset:0 lds\ns_waitcnt lgkmcnt(9)\nM v[8:11],v[204:207],a[112:115],v[8:11]\n" \
"ds_read_b64_tr_b16 v[194:195],v241 offset:50432\nds_read_b64_tr_b16 v[196:197],v241 offset:58368\nM v[12:15],v[200:203],a[100:103],v[12:15]\nds_read_b64_tr_b16 v[198:199],v241 offset:58624\nv_add_u32 v253,0x100000,v253\nM v[12:15],v[204:207],a[116:119],v[12:15]\nv_cvt_pk_bf16_f32 v148,0,v148\nv_cvt_pk_bf16_f32 v149,0,v149\n" \
"M v[72:75],v[200:203],a[104:107],v[72:75]\nv_cvt_pk_bf16_f32 v150,0,v150\nv_cvt_pk_bf16_f32 v151,0,v151\nM v[72:75],v[204:207],a[120:123],v[72:75]\nbuffer_load_dwordx4 v225,s[36:39],s67 offen offset:1024 lds\nM v[76:79],v[200:203],a[108:111],v[76:79]\nds_read_b64_tr_b16 v[200:201],v242 offset:50176\nds_read_b64_tr_b16 v[202:203],v242 offset:50432\n" \
"M v[76:79],v[204:207],a[124:127],v[76:79]\nds_read_b64_tr_b16 v[204:205],v242 offset:58368\ns_waitcnt lgkmcnt(9)\nM v[16:19],v[208:211],a[96:99],v[16:19]\nds_read_b64_tr_b16 v[206:207],v242 offset:58624\nv_mul_f32 v136,v136,v157\nM v[16:19],v[212:215],a[112:115],v[16:19]\nv_mul_f32 v137,v137,v157\n" \
"v_mul_f32 v138,v138,v157\nM v[20:23],v[208:211],a[100:103],v[20:23]\nbuffer_load_dwordx4 v226,s[36:39],s67 offen offset:2048 lds\nM v[20:23],v[212:215],a[116:119],v[20:23]\nv_mul_f32 v139,v139,v157\nv_mul_f32 v140,v140,v157\nM v[80:83],v[208:211],a[104:107],v[80:83]\nv_mul_f32 v141,v141,v157\n" \
"v_mul_f32 v142,v142,v157\nM v[80:83],v[212:215],a[120:123],v[80:83]\nv_mul_f32 v143,v143,v157\nv_mul_f32 v144,v144,v157\nM v[84:87],v[208:211],a[108:111],v[84:87]\nds_read_b64_tr_b16 v[208:209],v241 offset:50688\nds_read_b64_tr_b16 v[210:211],v241 offset:50944\nM v[84:87],v[212:215],a[124:127],v[84:87]\n" \
"buffer_load_dwordx4 v227,s[36:39],s67 offen offset:3072 lds\nM v[24:27],v[216:219],a[96:99],v[24:27]\nds_read_b64_tr_b16 v[212:213],v241 offset:58880\ns_waitcnt lgkmcnt(6)\nM v[24:27],v[220:223],a[112:115],v[24:27]\ns_add_u32 m0,s64,114688\ns_add_u32 s69,s68,2621440\nM v[28:31],v[216:219],a[100:103],v[28:31]\n" \
"ds_read_b64_tr_b16 v[214:215],v241 offset:59136\nv_mul_f32 v145,v145,v157\nM v[28:31],v[220:223],a[116:119],v[28:31]\nv_mul_f32 v146,v146,v157\nv_mul_f32 v147,v147,v157\nM v[88:91],v[216:219],a[104:107],v[88:91]\nbuffer_load_dwordx4 v243,s[40:43],s69 offen offset:0 lds\nM v[88:91],v[220:223],a[120:123],v[88:91]\n" \
"v_mul_f32 v148,v148,v157\nv_mul_f32 v149,v149,v157\nM v[92:95],v[216:219],a[108:111],v[92:95]\nds_read_b64_tr_b16 v[216:217],v242 offset:50688\nds_read_b64_tr_b16 v[218:219],v242 offset:50944\nM v[92:95],v[220:223],a[124:127],v[92:95]\nds_read_b64_tr_b16 v[220:221],v242 offset:58880\nds_read_b64_tr_b16 v[222:223],v242 offset:59136\n" \
"M v[32:35],v[192:195],a[96:99],v[32:35]\nbuffer_load_dwordx2 v[166:167],v165,s[52:55],s80 offen sc1\nM v[32:35],v[196:199],a[112:115],v[32:35]\nbuffer_load_dwordx4 v244,s[40:43],s69 offen offset:1024 lds\nM v[36:39],v[192:195],a[100:103],v[36:39]\nv_mul_f32 v150,v150,v157\nv_mul_f32 v151,v151,v157\nM v[36:39],v[196:199],a[116:119],v[36:39]\n" \
"v_cvt_pk_bf16_f32 v144,0,v144\nv_cvt_pk_bf16_f32 v145,0,v145\nM v[96:99],v[192:195],a[104:107],v[96:99]\nv_cvt_pk_bf16_f32 v136,0,v136\nv_cvt_pk_bf16_f32 v137,0,v137\nM v[96:99],v[196:199],a[120:123],v[96:99]\nv_cvt_pk_bf16_f32 v138,0,v138\nv_cvt_pk_bf16_f32 v139,0,v139\n" \
"M v[100:103],v[192:195],a[108:111],v[100:103]\nbuffer_load_dwordx4 v245,s[40:43],s69 offen offset:2048 lds\nM v[100:103],v[196:199],a[124:127],v[100:103]\nv_cvt_pk_bf16_f32 v140,0,v140\ns_waitcnt lgkmcnt(4)\nM v[40:43],v[200:203],a[96:99],v[40:43]\nv_cvt_pk_bf16_f32 v141,0,v141\nv_cvt_pk_bf16_f32 v142,0,v142\n" \
"M v[40:43],v[204:207],a[112:115],v[40:43]\nv_cvt_pk_bf16_f32 v143,0,v143\nv_cvt_pk_bf16_f32 v146,0,v146\nM v[44:47],v[200:203],a[100:103],v[44:47]\nglobal_load_dword v247,v253,s[16:17]\nM v[44:47],v[204:207],a[116:119],v[44:47]\nbuffer_load_dwordx4 v246,s[40:43],s69 offen offset:3072 lds\nM v[104:107],v[200:203],a[104:107],v[104:107]\n" \
"v_add_u32 v253,0x100000,v253\nv_cvt_pk_bf16_f32 v147,0,v147\nM v[104:107],v[204:207],a[120:123],v[104:107]\ns_add_u32 m0,s65,0\ns_xor_b32 s67,s66,768\nM v[108:111],v[200:203],a[108:111],v[108:111]\nv_cvt_pk_bf16_f32 v148,0,v148\nv_cvt_pk_bf16_f32 v149,0,v149\n" \
"M v[108:111],v[204:207],a[124:127],v[108:111]\nv_cvt_pk_bf16_f32 v150,0,v150\nv_cvt_pk_bf16_f32 v151,0,v151\nM v[48:51],v[208:211],a[96:99],v[48:51]\nbuffer_load_dwordx4 v224,s[36:39],s67 offen offset:0 lds\nM v[48:51],v[212:215],a[112:115],v[48:51]\nM v[52:55],v[208:211],a[100:103],v[52:55]\nM v[52:55],v[212:215],a[116:119],v[52:55]\n" \
"M v[112:115],v[208:211],a[104:107],v[112:115]\ns_waitcnt vmcnt(16) lgkmcnt(0)\ns_barrier\nM v[112:115],v[212:215],a[120:123],v[112:115]\nbuffer_load_dwordx4 v225,s[36:39],s67 offen offset:1024 lds\nds_read_b128 a[128:131],v228 offset:8192\nds_read_b64_tr_b16 v[192:193],v241 offset:0\nds_read_b64_tr_b16 v[194:195],v241 offset:256\n" \
"M v[116:119],v[208:211],a[108:111],v[116:119]\nds_read_b128 a[144:147],v229 offset:8192\nds_read_b64_tr_b16 v[196:197],v241 offset:8192\nds_read_b64_tr_b16 v[198:199],v241 offset:8448\nM v[116:119],v[212:215],a[124:127],v[116:119]\nds_read_b128 a[132:135],v228 offset:10240\nM v[56:59],v[216:219],a[96:99],v[56:59]\nds_read_b128 a[148:151],v229 offset:10240\n" \
"M v[56:59],v[220:223],a[112:115],v[56:59]\nds_read_b128 a[136:139],v228 offset:12288\nM v[60:63],v[216:219],a[100:103],v[60:63]\nbuffer_load_dwordx4 v226,s[36:39],s67 offen offset:2048 lds\nds_read_b128 a[152:155],v229 offset:12288\nM v[60:63],v[220:223],a[116:119],v[60:63]\nds_read_b128 a[140:143],v228 offset:14336\nM v[120:123],v[216:219],a[104:107],v[120:123]\n" \
"ds_read_b128 a[156:159],v229 offset:14336\nM v[120:123],v[220:223],a[120:123],v[120:123]\nds_read_b64_tr_b16 v[200:201],v242 offset:0\nds_read_b64_tr_b16 v[202:203],v242 offset:256\nM v[124:127],v[216:219],a[108:111],v[124:127]\nds_read_b64_tr_b16 v[204:205],v242 offset:8192\nds_read_b64_tr_b16 v[206:207],v242 offset:8448\nM v[124:127],v[220:223],a[124:127],v[124:127]\n" \
"buffer_load_dwordx4 v227,s[36:39],s67 offen offset:3072 lds\ns_waitcnt lgkmcnt(13)\nM v[0:3],v[192:195],a[128:131],v[0:3]\nds_read_b64_tr_b16 v[208:209],v241 offset:512\ns_waitcnt lgkmcnt(11)\nM v[0:3],v[196:199],a[144:147],v[0:3]\ns_add_u32 m0,s65,4096\ns_xor_b32 s67,s66,33536\n" \
"s_waitcnt lgkmcnt(10)\nM v[4:7],v[192:195],a[132:135],v[4:7]\nds_read_b64_tr_b16 v[210:211],v241 offset:768\ns_waitcnt lgkmcnt(10)\nM v[4:7],v[196:199],a[148:151],v[4:7]\nds_read_b64_tr_b16 v[212:213],v241 offset:8704\ns_waitcnt lgkmcnt(10)\nM v[64:67],v[192:195],a[136:139],v[64:67]\n" \
"buffer_load_dwordx4 v224,s[36:39],s67 offen offset:0 lds\ns_waitcnt lgkmcnt(9)\nM v[64:67],v[196:199],a[152:155],v[64:67]\nds_read_b64_tr_b16 v[214:215],v241 offset:8960\ns_waitcnt lgkmcnt(9)\nM v[68:71],v[192:195],a[140:143],v[68:71]\nds_read_b64_tr_b16 v[216:217],v242 offset:512\ns_waitcnt lgkmcnt(9)\n" \
"M v[68:71],v[196:199],a[156:159],v[68:71]\nds_read_b64_tr_b16 v[218:219],v242 offset:768\ns_waitcnt lgkmcnt(8)\nM v[8:11],v[200:203],a[128:131],v[8:11]\nds_read_b64_tr_b16 v[220:221],v242 offset:8704\ns_waitcnt lgkmcnt(7)\nM v[8:11],v[204:207],a[144:147],v[8:11]\nbuffer_load_dwordx4 v225,s[36:39],s67 offen offset:1024 lds\n" \
"M v[12:15],v[200:203],a[132:135],v[12:15]\nds_read_b64_tr_b16 v[222:223],v242 offset:8960\nds_read_b64_tr_b16 v[192:193],v241 offset:1024\nM v[12:15],v[204:207],a[148:151],v[12:15]\nds_read_b64_tr_b16 v[194:195],v241 offset:1280\nds_read_b64_tr_b16 v[196:197],v241 offset:9216\nM v[72:75],v[200:203],a[136:139],v[72:75]\nds_read_b64_tr_b16 v[198:199],v241 offset:9472\n" \
"M v[72:75],v[204:207],a[152:155],v[72:75]\nM v[76:79],v[200:203],a[140:143],v[76:79]\nbuffer_load_dwordx4 v226,s[36:39],s67 offen offset:2048 lds\nM v[76:79],v[204:207],a[156:159],v[76:79]\nds_read_b64_tr_b16 v[200:201],v242 offset:1024\ns_waitcnt lgkmcnt(10)\nM v[16:19],v[208:211],a[128:131],v[16:19]\nds_read_b64_tr_b16 v[202:203],v242 offset:1280\n" \
"s_waitcnt lgkmcnt(10)\nM v[16:19],v[212:215],a[144:147],v[16:19]\nds_read_b64_tr_b16 v[204:205],v242 offset:9216\nds_read_b64_tr_b16 v[206:207],v242 offset:9472\nM v[20:23],v[208:211],a[132:135],v[20:23]\nglobal_load_dword v247,v253,s[16:17]\nM v[20:23],v[212:215],a[148:151],v[20:23]\nbuffer_load_dwordx4 v227,s[36:39],s67 offen offset:3072 lds\n" \
"M v[80:83],v[208:211],a[136:139],v[80:83]\nv_add_u32 v253,0xffd00000,v253\nM v[80:83],v[212:215],a[152:155],v[80:83]\ns_add_u32 m0,s64,131072\ns_add_u32 s69,s68,3145728\nM v[84:87],v[208:211],a[140:143],v[84:87]\nds_read_b64_tr_b16 v[208:209],v241 offset:1536\nds_read_b64_tr_b16 v[210:211],v241 offset:1792\n" \
"M v[84:87],v[212:215],a[156:159],v[84:87]\nds_read_b64_tr_b16 v[212:213],v241 offset:9728\ns_waitcnt lgkmcnt(7)\nM v[24:27],v[216:219],a[128:131],v[24:27]\nbuffer_load_dwordx4 v243,s[40:43],s69 offen offset:0 lds\nM v[24:27],v[220:223],a[144:147],v[24:27]\nds_read_b64_tr_b16 v[214:215],v241 offset:9984\nM v[28:31],v[216:219],a[132:135],v[28:31]\n" \
"M v[28:31],v[220:223],a[148:151],v[28:31]\nM v[88:91],v[216:219],a[136:139],v[88:91]\nM v[88:91],v[220:223],a[152:155],v[88:91]\nbuffer_load_dwordx4 v244,s[40:43],s69 offen offset:1024 lds\nM v[92:95],v[216:219],a[140:143],v[92:95]\nds_read_b64_tr_b16 v[216:217],v242 offset:1536\nds_read_b64_tr_b16 v[218:219],v242 offset:1792\nM v[92:95],v[220:223],a[156:159],v[92:95]\n" \
"ds_read_b64_tr_b16 v[220:221],v242 offset:9728\nds_read_b64_tr_b16 v[222:223],v242 offset:9984\nM v[32:35],v[192:195],a[128:131],v[32:35]\nM v[32:35],v[196:199],a[144:147],v[32:35]\nM v[36:39],v[192:195],a[132:135],v[36:39]\nbuffer_load_dwordx4 v245,s[40:43],s69 offen offset:2048 lds\nM v[36:39],v[196:199],a[148:151],v[36:39]\nM v[96:99],v[192:195],a[136:139],v[96:99]\n" \
"s_waitcnt vmcnt(16)\nv_xor_b32 v166,v166,v230\nM v[96:99],v[196:199],a[152:155],v[96:99]\nv_xor_b32 v167,v167,v231\nv_or_b32 v166,v166,v167\nM v[100:103],v[192:195],a[140:143],v[100:103]\nv_cmp_ne_u32 vcc,0,v166\nM v[100:103],v[196:199],a[156:159],v[100:103]\n" \
"buffer_load_dwordx4 v246,s[40:43],s69 offen offset:3072 lds\ns_waitcnt lgkmcnt(4)\nM v[40:43],v[200:203],a[128:131],v[40:43]\ns_cbranch_vccnz .Lhs\n.Lhb:\nv_bfe_u32 v231,v249,2,3\nM v[40:43],v[204:207],a[144:147],v[40:43]\ns_add_u32 m0,s65,8192\n" \
"s_xor_b32 s67,s66,896\nM v[44:47],v[200:203],a[132:135],v[44:47]\nv_mul_u32_u24 v231,3584,v231\nv_and_b32 v230,31,v249\nM v[44:47],v[204:207],a[148:151],v[44:47]\nv_lshlrev_b32 v230,7,v230\nM v[104:107],v[200:203],a[136:139],v[104:107]\nbuffer_load_dwordx4 v224,s[36:39],s67 offen offset:0 lds\n" \
"M v[104:107],v[204:207],a[152:155],v[104:107]\nM v[108:111],v[200:203],a[140:143],v[108:111]\nM v[108:111],v[204:207],a[156:159],v[108:111]\nM v[48:51],v[208:211],a[128:131],v[48:51]\nM v[48:51],v[212:215],a[144:147],v[48:51]\nbuffer_load_dwordx4 v225,s[36:39],s67 offen offset:1024 lds\nM v[52:55],v[208:211],a[132:135],v[52:55]\nM v[52:55],v[212:215],a[148:151],v[52:55]\n" \
"M v[112:115],v[208:211],a[136:139],v[112:115]\ns_waitcnt vmcnt(15) lgkmcnt(0)\ns_barrier\nM v[112:115],v[212:215],a[152:155],v[112:115]\nds_read_b128 a[160:163],v228 offset:16384\nds_read_b64_tr_b16 v[192:193],v241 offset:16384\nds_read_b64_tr_b16 v[194:195],v241 offset:16640\nM v[116:119],v[208:211],a[140:143],v[116:119]\n" \
"ds_read_b128 a[176:179],v229 offset:16384\nds_read_b64_tr_b16 v[196:197],v241 offset:24576\nds_read_b64_tr_b16 v[198:199],v241 offset:24832\nM v[116:119],v[212:215],a[156:159],v[116:119]\nds_read_b128 a[164:167],v228 offset:18432\nM v[56:59],v[216:219],a[128:131],v[56:59]\nbuffer_load_dwordx4 v[152:155],v240,s[52:55],s71 offen sc1\nds_read_b128 a[180:183],v229 offset:18432\n" \
"M v[56:59],v[220:223],a[144:147],v[56:59]\nbuffer_load_dwordx4 v226,s[36:39],s67 offen offset:2048 lds\ns_add_u32 s71,s70,798720\nds_read_b128 a[168:171],v228 offset:20480\nbuffer_load_dwordx4 v[156:159],v240,s[52:55],s71 offen sc1\nM v[60:63],v[216:219],a[132:135],v[60:63]\nds_read_b128 a[184:187],v229 offset:20480\ns_add_u32 s71,s70,811008\n" \
"M v[60:63],v[220:223],a[148:151],v[60:63]\nbuffer_load_dwordx4 v227,s[36:39],s67 offen offset:3072 lds\nbuffer_load_dwordx4 v[160:163],v240,s[52:55],s71 offen sc1\nds_read_b128 a[172:175],v228 offset:22528\nM v[120:123],v[216:219],a[136:139],v[120:123]\ns_add_u32 s71,s70,823296\nds_read_b128 a[188:191],v229 offset:22528\nbuffer_load_dwordx4 v[164:167],v240,s[52:55],s71 offen sc1\n" \
"M v[120:123],v[220:223],a[152:155],v[120:123]\ns_add_u32 m0,s65,12288\ns_xor_b32 s67,s66,33664\nds_read_b64_tr_b16 v[200:201],v242 offset:16384\nds_read_b64_tr_b16 v[202:203],v242 offset:16640\nM v[124:127],v[216:219],a[140:143],v[124:127]\nds_read_b64_tr_b16 v[204:205],v242 offset:24576\nds_read_b64_tr_b16 v[206:207],v242 offset:24832\n" \
"M v[124:127],v[220:223],a[156:159],v[124:127]\ns_lshl_b32 s71,s6,5\ns_waitcnt lgkmcnt(9)\nM v[0:3],v[192:195],a[160:163],v[0:3]\nds_read_b64_tr_b16 v[208:209],v241 offset:16896\ns_add_u32 s71,s71,790528\nM v[0:3],v[196:199],a[176:179],v[0:3]\nds_read_b64_tr_b16 v[210:211],v241 offset:17152\n" \
"ds_read_b64_tr_b16 v[212:213],v241 offset:25088\nM v[4:7],v[192:195],a[164:167],v[4:7]\nds_read_b64_tr_b16 v[214:215],v241 offset:25344\ns_waitcnt lgkmcnt(11)\nM v[4:7],v[196:199],a[180:183],v[4:7]\nds_read_b64_tr_b16 v[216:217],v242 offset:16896\nds_read_b64_tr_b16 v[218:219],v242 offset:17152\nM v[64:67],v[192:195],a[168:171],v[64:67]\n" \
"ds_read_b64_tr_b16 v[220:221],v242 offset:25088\ns_waitcnt lgkmcnt(13)\nM v[64:67],v[196:199],a[184:187],v[64:67]\nds_read_b64_tr_b16 v[222:223],v242 offset:25344\ns_waitcnt lgkmcnt(13)\nM v[68:71],v[192:195],a[172:175],v[68:71]\nds_read_b64_tr_b16 v[192:193],v241 offset:17408\ns_waitcnt lgkmcnt(13)\n" \
"M v[68:71],v[196:199],a[188:191],v[68:71]\nds_read_b64_tr_b16 v[194:195],v241 offset:17664\ns_waitcnt lgkmcnt(12)\nM v[8:11],v[200:203],a[160:163],v[8:11]\nds_read_b64_tr_b16 v[196:197],v241 offset:25600\ns_waitcnt lgkmcnt(11)\nM v[8:11],v[204:207],a[176:179],v[8:11]\ns_cmp_lt_u32 s7,2\n" \
"s_cselect_b32 s70,0,1.0\nM v[12:15],v[200:203],a[164:167],v[12:15]\nbuffer_load_dword v247,v254,s[56:59],s78 offen\nM v[12:15],v[204:207],a[180:183],v[12:15]\nds_read_b64_tr_b16 v[198:199],v241 offset:25856\nv_and_b32 v254,3,v249\nM v[72:75],v[200:203],a[168:171],v[72:75]\ns_cmp_eq_u32 s62,5\n" \
"s_cselect_b64 s[56:57],s[22:23],s[24:25]\nM v[72:75],v[204:207],a[184:187],v[72:75]\nv_lshl_add_u32 v231,v254,7,v231\nM v[76:79],v[200:203],a[172:175],v[76:79]\nglobal_load_dword v247,v253,s[12:13]\nM v[76:79],v[204:207],a[188:191],v[76:79]\nds_read_b64_tr_b16 v[200:201],v242 offset:17408\ns_waitcnt lgkmcnt(6)\n" \
"M v[16:19],v[208:211],a[160:163],v[16:19]\nds_read_b64_tr_b16 v[202:203],v242 offset:17664\nds_read_b64_tr_b16 v[204:205],v242 offset:25600\nM v[16:19],v[212:215],a[176:179],v[16:19]\nds_read_b64_tr_b16 v[206:207],v242 offset:25856\ns_add_u32 s69,s68,3670016\nM v[20:23],v[208:211],a[164:167],v[20:23]\nbuffer_load_dwordx4 v224,s[36:39],s67 offen offset:0 lds\n" \
"M v[20:23],v[212:215],a[180:183],v[20:23]\nv_add_u32 v253,0x100000,v253\nM v[80:83],v[208:211],a[168:171],v[80:83]\nM v[80:83],v[212:215],a[184:187],v[80:83]\nM v[84:87],v[208:211],a[172:175],v[84:87]\nbuffer_load_dwordx4 v225,s[36:39],s67 offen offset:1024 lds\nM v[84:87],v[212:215],a[188:191],v[84:87]\nds_read_b64_tr_b16 v[208:209],v241 offset:17920\n" \
"ds_read_b64_tr_b16 v[210:211],v241 offset:18176\nM v[24:27],v[216:219],a[160:163],v[24:27]\nds_read_b64_tr_b16 v[212:213],v241 offset:26112\ns_waitcnt lgkmcnt(7)\nM v[24:27],v[220:223],a[176:179],v[24:27]\nds_read_b64_tr_b16 v[214:215],v241 offset:26368\nM v[28:31],v[216:219],a[164:167],v[28:31]\nbuffer_load_dwordx4 v226,s[36:39],s67 offen offset:2048 lds\n" \
"M v[28:31],v[220:223],a[180:183],v[28:31]\nM v[88:91],v[216:219],a[168:171],v[88:91]\nM v[88:91],v[220:223],a[184:187],v[88:91]\nM v[92:95],v[216:219],a[172:175],v[92:95]\nbuffer_load_dwordx4 v227,s[36:39],s67 offen offset:3072 lds\nM v[92:95],v[220:223],a[188:191],v[92:95]\ns_add_u32 m0,s64,147456\nds_read_b64_tr_b16 v[216:217],v242 offset:17920\n" \
"M v[32:35],v[192:195],a[160:163],v[32:35]\nds_read_b64_tr_b16 v[218:219],v242 offset:18176\nds_read_b64_tr_b16 v[220:221],v242 offset:26112\nM v[32:35],v[196:199],a[176:179],v[32:35]\ns_cmp_eq_u32 s62,0\ns_cselect_b32 s36,0x8000,0\nM v[36:39],v[192:195],a[164:167],v[36:39]\nds_read_b64_tr_b16 v[222:223],v242 offset:26368\n" \
"s_add_u32 s36,s36,s64\nM v[36:39],v[196:199],a[180:183],v[36:39]\nbuffer_load_dwordx4 v243,s[40:43],s69 offen offset:0 lds\nM v[96:99],v[192:195],a[168:171],v[96:99]\nM v[96:99],v[196:199],a[184:187],v[96:99]\nM v[100:103],v[192:195],a[172:175],v[100:103]\nM v[100:103],v[196:199],a[188:191],v[100:103]\ns_waitcnt lgkmcnt(4)\n" \
"M v[40:43],v[200:203],a[160:163],v[40:43]\nbuffer_load_dwordx4 v244,s[40:43],s69 offen offset:1024 lds\nM v[40:43],v[204:207],a[176:179],v[40:43]\nM v[44:47],v[200:203],a[164:167],v[44:47]\nM v[44:47],v[204:207],a[180:183],v[44:47]\nM v[104:107],v[200:203],a[168:171],v[104:107]\nbuffer_load_dwordx4 v245,s[40:43],s69 offen offset:2048 lds\nM v[104:107],v[204:207],a[184:187],v[104:107]\n" \
"M v[108:111],v[200:203],a[172:175],v[108:111]\nM v[108:111],v[204:207],a[188:191],v[108:111]\nM v[48:51],v[208:211],a[160:163],v[48:51]\nbuffer_load_dwordx4 v246,s[40:43],s69 offen offset:3072 lds\nM v[48:51],v[212:215],a[176:179],v[48:51]\ns_sub_u32 s40,s16,0x1000\ns_subb_u32 s41,s17,0\nM v[52:55],v[208:211],a[164:167],v[52:55]\n" \
"s_add_u32 m0,s64,98304\ns_add_u32 s69,s68,0\nM v[52:55],v[212:215],a[180:183],v[52:55]\ns_mov_b32 s16,0\ns_mov_b32 s17,3\nM v[112:115],v[208:211],a[168:171],v[112:115]\ns_waitcnt vmcnt(18) lgkmcnt(0)\ns_barrier\n" \
"M v[112:115],v[212:215],a[184:187],v[112:115]\nds_read_b128 a[192:195],v228 offset:0\nds_read_b64_tr_b16 v[192:193],v241 offset:32768\nds_read_b64_tr_b16 v[194:195],v241 offset:33024\nM v[116:119],v[208:211],a[172:175],v[116:119]\nglobal_load_dword v247,v253,s[12:13]\nds_read_b128 a[208:211],v229 offset:0\nds_read_b64_tr_b16 v[196:197],v241 offset:40960\n" \
"ds_read_b64_tr_b16 v[198:199],v241 offset:41216\nM v[116:119],v[212:215],a[188:191],v[116:119]\nds_read_b128 a[196:199],v228 offset:2048\nv_add_u32 v253,0x100000,v253\nM v[56:59],v[216:219],a[160:163],v[56:59]\nds_read_b128 a[212:215],v229 offset:2048\nM v[56:59],v[220:223],a[176:179],v[56:59]\nds_read_b128 a[200:203],v228 offset:4096\n" \
"M v[60:63],v[216:219],a[164:167],v[60:63]\nds_read_b128 a[216:219],v229 offset:4096\nM v[60:63],v[220:223],a[180:183],v[60:63]\nds_read_b128 a[204:207],v228 offset:6144\nM v[120:123],v[216:219],a[168:171],v[120:123]\nds_read_b128 a[220:223],v229 offset:6144\nM v[120:123],v[220:223],a[184:187],v[120:123]\nds_read_b64_tr_b16 v[200:201],v242 offset:32768\n" \
"ds_read_b64_tr_b16 v[202:203],v242 offset:33024\nM v[124:127],v[216:219],a[172:175],v[124:127]\nds_read_b64_tr_b16 v[204:205],v242 offset:40960\nds_read_b64_tr_b16 v[206:207],v242 offset:41216\nM v[124:127],v[220:223],a[188:191],v[124:127]\ns_waitcnt vmcnt(14)\nv_add_f32 v152,v152,v156\ns_waitcnt lgkmcnt(13)\n" \
"M v[0:3],v[192:195],a[192:195],v[0:3]\nds_read_b64_tr_b16 v[208:209],v241 offset:33280\ns_waitcnt lgkmcnt(11)\nM v[0:3],v[196:199],a[208:211],v[0:3]\nds_read_b64_tr_b16 v[210:211],v241 offset:33536\ns_waitcnt lgkmcnt(11)\nM v[4:7],v[192:195],a[196:199],v[4:7]\nds_read_b64_tr_b16 v[212:213],v241 offset:41472\n" \
"s_waitcnt lgkmcnt(11)\nM v[4:7],v[196:199],a[212:215],v[4:7]\nds_read_b64_tr_b16 v[214:215],v241 offset:41728\ns_waitcnt lgkmcnt(11)\nM v[64:67],v[192:195],a[200:203],v[64:67]\nds_read_b64_tr_b16 v[216:217],v242 offset:33280\ns_waitcnt lgkmcnt(11)\nM v[64:67],v[196:199],a[216:219],v[64:67]\n" \
"ds_read_b64_tr_b16 v[218:219],v242 offset:33536\ns_waitcnt lgkmcnt(11)\nM v[68:71],v[192:195],a[204:207],v[68:71]\nds_read_b64_tr_b16 v[220:221],v242 offset:41472\ns_waitcnt lgkmcnt(11)\nM v[68:71],v[196:199],a[220:223],v[68:71]\nds_read_b64_tr_b16 v[222:223],v242 offset:41728\ns_waitcnt lgkmcnt(10)\n" \
"M v[8:11],v[200:203],a[192:195],v[8:11]\nds_read_b64_tr_b16 v[192:193],v241 offset:33792\ns_waitcnt lgkmcnt(9)\nM v[8:11],v[204:207],a[208:211],v[8:11]\nds_read_b64_tr_b16 v[194:195],v241 offset:34048\nds_read_b64_tr_b16 v[196:197],v241 offset:41984\nM v[12:15],v[200:203],a[196:199],v[12:15]\nds_read_b64_tr_b16 v[198:199],v241 offset:42240\n" \
"s_waitcnt vmcnt(11)\nv_add_f32 v160,v160,v164\nM v[12:15],v[204:207],a[212:215],v[12:15]\nv_add_f32 v153,v153,v157\nv_add_f32 v161,v161,v165\nM v[72:75],v[200:203],a[200:203],v[72:75]\nv_add_f32 v154,v154,v158\nv_add_f32 v162,v162,v166\n" \
"M v[72:75],v[204:207],a[216:219],v[72:75]\nv_add_f32 v155,v155,v159\nv_add_f32 v163,v163,v167\nM v[76:79],v[200:203],a[204:207],v[76:79]\nds_read_b64_tr_b16 v[200:201],v242 offset:33792\nds_read_b64_tr_b16 v[202:203],v242 offset:34048\nM v[76:79],v[204:207],a[220:223],v[76:79]\nds_read_b64_tr_b16 v[204:205],v242 offset:41984\n" \
"s_waitcnt lgkmcnt(9)\nM v[16:19],v[208:211],a[192:195],v[16:19]\nds_read_b64_tr_b16 v[206:207],v242 offset:42240\nv_add_f32 v152,v152,v160\nM v[16:19],v[212:215],a[208:211],v[16:19]\nbuffer_load_dword v168,v169,s[52:55],s71 offen sc1\nM v[20:23],v[208:211],a[196:199],v[20:23]\nv_add_f32 v153,v153,v161\n" \
"v_add_f32 v154,v154,v162\nM v[20:23],v[212:215],a[212:215],v[20:23]\nv_add_f32 v155,v155,v163\nv_cvt_pk_bf16_f32 v152,0,v152\nM v[80:83],v[208:211],a[200:203],v[80:83]\nv_cvt_pk_bf16_f32 v153,0,v153\nv_cvt_pk_bf16_f32 v154,0,v154\nM v[80:83],v[212:215],a[216:219],v[80:83]\n" \
"v_cvt_pk_bf16_f32 v155,0,v155\nv_add_f32 v152,s70,v152\nM v[84:87],v[208:211],a[204:207],v[84:87]\nds_read_b64_tr_b16 v[208:209],v241 offset:34304\nds_read_b64_tr_b16 v[210:211],v241 offset:34560\nM v[84:87],v[212:215],a[220:223],v[84:87]\nds_read_b64_tr_b16 v[212:213],v241 offset:42496\nds_read_b64_tr_b16 v[214:215],v241 offset:42752\n" \
"M v[24:27],v[216:219],a[192:195],v[24:27]\nv_add_f32 v153,s70,v153\ns_waitcnt lgkmcnt(7)\nM v[24:27],v[220:223],a[208:211],v[24:27]\nv_add_f32 v154,s70,v154\nv_add_f32 v155,s70,v155\nM v[28:31],v[216:219],a[196:199],v[28:31]\nglobal_load_dword v247,v253,s[12:13]\n" \
"M v[28:31],v[220:223],a[212:215],v[28:31]\ns_lshr_b32 s71,s64,2\nv_cvt_pk_bf16_f32 v152,0,v152\nM v[88:91],v[216:219],a[200:203],v[88:91]\nv_cvt_pk_bf16_f32 v153,0,v153\nv_cvt_pk_bf16_f32 v154,0,v154\nM v[88:91],v[220:223],a[216:219],v[88:91]\nv_cvt_pk_bf16_f32 v155,0,v155\n" \
"v_add_u32 v171,s71,v240\nM v[92:95],v[216:219],a[204:207],v[92:95]\nds_write_b128 v171,v[152:155] offset:16384\nM v[92:95],v[220:223],a[220:223],v[92:95]\nds_read_b64_tr_b16 v[216:217],v242 offset:34304\nds_read_b64_tr_b16 v[218:219],v242 offset:34560\nM v[32:35],v[192:195],a[192:195],v[32:35]\nds_read_b64_tr_b16 v[220:221],v242 offset:42496\n" \
"ds_read_b64_tr_b16 v[222:223],v242 offset:42752\nM v[32:35],v[196:199],a[208:211],v[32:35]\nv_add_u32 v253,0x100000,v253\ns_lshl_b32 s70,s7,8\nM v[36:39],v[192:195],a[196:199],v[36:39]\nbuffer_load_dwordx4 v243,s[40:43],s69 offen offset:0 lds\nM v[36:39],v[196:199],a[212:215],v[36:39]\nv_lshl_add_u32 v251,v251,5,s70\n" \
"s_lshl_b32 s71,s6,5\nM v[96:99],v[192:195],a[200:203],v[96:99]\ns_and_b32 s71,s71,0xfffffe00\ns_xor_b32 s70,s63,16\nM v[96:99],v[196:199],a[216:219],v[96:99]\ns_lshr_b32 s70,s70,2\ns_lshl3_add_u32 s70,s62,s70\nM v[100:103],v[192:195],a[204:207],v[100:103]\n" \
"buffer_load_dwordx4 v244,s[40:43],s69 offen offset:1024 lds\nM v[100:103],v[196:199],a[220:223],v[100:103]\ns_lshl_b32 s70,s70,15\ns_waitcnt lgkmcnt(5)\nM v[40:43],v[200:203],a[192:195],v[40:43]\ns_add_u32 s70,s70,s64\nM v[40:43],v[204:207],a[208:211],v[40:43]\nM v[44:47],v[200:203],a[196:199],v[44:47]\n" \
"buffer_load_dwordx4 v245,s[40:43],s69 offen offset:2048 lds\nM v[44:47],v[204:207],a[212:215],v[44:47]\nM v[104:107],v[200:203],a[200:203],v[104:107]\nM v[104:107],v[204:207],a[216:219],v[104:107]\nM v[108:111],v[200:203],a[204:207],v[108:111]\nbuffer_load_dwordx4 v246,s[40:43],s69 offen offset:3072 lds\nM v[108:111],v[204:207],a[220:223],v[108:111]\ns_add_u32 m0,s64,114688\n" \
"s_add_u32 s69,s68,524288\nM v[48:51],v[208:211],a[192:195],v[48:51]\nM v[48:51],v[212:215],a[208:211],v[48:51]\nM v[52:55],v[208:211],a[196:199],v[52:55]\nbuffer_store_dwordx2 v[232:233],off,s[52:55],s72 sc1\nM v[52:55],v[212:215],a[212:215],v[52:55]\ns_and_b32 s72,s6,1\ns_lshl_b32 s72,s72,8\n" \
"M v[112:115],v[208:211],a[200:203],v[112:115]\ns_waitcnt vmcnt(8) lgkmcnt(0)\ns_barrier\nM v[112:115],v[212:215],a[216:219],v[112:115]\nds_read_b128 a[224:227],v228 offset:8192\nds_read_b64_tr_b16 v[192:193],v241 offset:49152\nds_read_b64_tr_b16 v[194:195],v241 offset:49408\nM v[116:119],v[208:211],a[204:207],v[116:119]\n" \
"ds_read_b128 a[240:243],v229 offset:8192\nds_read_b64_tr_b16 v[196:197],v241 offset:57344\nds_read_b64_tr_b16 v[198:199],v241 offset:57600\nM v[116:119],v[212:215],a[220:223],v[116:119]\nds_read_b128 a[228:231],v228 offset:10240\ns_add_u32 s71,s71,s72\nM v[56:59],v[216:219],a[192:195],v[56:59]\nds_read_b128 a[244:247],v229 offset:10240\n" \
"s_and_b32 s72,s6,14\nM v[56:59],v[220:223],a[208:211],v[56:59]\nds_read_b128 a[232:235],v228 offset:12288\ns_lshl1_add_u32 s71,s72,s71\nM v[60:63],v[216:219],a[196:199],v[60:63]\nds_read_b128 a[248:251],v229 offset:12288\ns_or_b32 s71,s71,s75\nM v[60:63],v[220:223],a[212:215],v[60:63]\n" \
"ds_read_b128 a[236:239],v228 offset:14336\ns_mov_b32 s72,0x3e0293ee\nM v[120:123],v[216:219],a[200:203],v[120:123]\nds_read_b128 a[252:255],v229 offset:14336\nv_and_b32 v228,15,v249\nM v[120:123],v[220:223],a[216:219],v[120:123]\nds_read_b64_tr_b16 v[200:201],v242 offset:49152\nds_read_b64_tr_b16 v[202:203],v242 offset:49408\n" \
"M v[124:127],v[216:219],a[204:207],v[124:127]\nds_read_b64_tr_b16 v[204:205],v242 offset:57344\nds_read_b64_tr_b16 v[206:207],v242 offset:57600\nM v[124:127],v[220:223],a[220:223],v[124:127]\nv_and_b32 v229,16,v249\ns_waitcnt lgkmcnt(13)\nM v[0:3],v[192:195],a[224:227],v[0:3]\nds_read_b64_tr_b16 v[208:209],v241 offset:49664\n" \
"s_waitcnt lgkmcnt(11)\nM v[0:3],v[196:199],a[240:243],v[0:3]\nds_read_b64_tr_b16 v[210:211],v241 offset:49920\ns_waitcnt lgkmcnt(11)\nM v[4:7],v[192:195],a[228:231],v[4:7]\nds_read_b64_tr_b16 v[212:213],v241 offset:57856\ns_waitcnt lgkmcnt(11)\nM v[4:7],v[196:199],a[244:247],v[4:7]\n" \
"ds_read_b64_tr_b16 v[214:215],v241 offset:58112\ns_waitcnt lgkmcnt(11)\nM v[64:67],v[192:195],a[232:235],v[64:67]\nds_read_b64_tr_b16 v[216:217],v242 offset:49664\ns_waitcnt lgkmcnt(11)\nM v[64:67],v[196:199],a[248:251],v[64:67]\nds_read_b64_tr_b16 v[218:219],v242 offset:49920\ns_waitcnt lgkmcnt(11)\n" \
"M v[68:71],v[192:195],a[236:239],v[68:71]\nglobal_load_dword v247,v253,s[12:13]\ns_waitcnt lgkmcnt(10)\nM v[68:71],v[196:199],a[252:255],v[68:71]\nds_read_b64_tr_b16 v[220:221],v242 offset:57856\ns_waitcnt lgkmcnt(9)\nM v[8:11],v[200:203],a[224:227],v[8:11]\nds_read_b64_tr_b16 v[222:223],v242 offset:58112\n" \
"s_waitcnt lgkmcnt(8)\nM v[8:11],v[204:207],a[240:243],v[8:11]\nds_read_b64_tr_b16 v[192:193],v241 offset:50176\nds_read_b64_tr_b16 v[194:195],v241 offset:50432\nM v[12:15],v[200:203],a[228:231],v[12:15]\nds_read_b64_tr_b16 v[196:197],v241 offset:58368\nds_read_b64_tr_b16 v[198:199],v241 offset:58624\nM v[12:15],v[204:207],a[244:247],v[12:15]\n" \
"ds_read_b128 v[176:179],v251 offset:19456\nds_read_b128 v[160:163],v251 offset:17408\nM v[72:75],v[200:203],a[232:235],v[72:75]\nds_read_b128 v[172:175],v251 offset:18448\nv_cvt_pk_bf16_f32 v128,v0,v1\nM v[72:75],v[204:207],a[248:251],v[72:75]\nds_read_b128 v[152:155],v251 offset:16384\nv_cvt_pk_bf16_f32 v129,v2,v3\n" \
"M v[76:79],v[200:203],a[236:239],v[76:79]\nds_read_b64_tr_b16 v[200:201],v242 offset:50176\nv_cvt_pk_bf16_f32 v130,v8,v9\nM v[76:79],v[204:207],a[252:255],v[76:79]\nds_read_b64_tr_b16 v[202:203],v242 offset:50432\ns_waitcnt lgkmcnt(12)\nM v[16:19],v[208:211],a[224:227],v[16:19]\nds_read_b64_tr_b16 v[204:205],v242 offset:58368\n" \
"ds_read_b64_tr_b16 v[206:207],v242 offset:58624\nM v[16:19],v[212:215],a[240:243],v[16:19]\nds_read_b128 v[156:159],v251 offset:16400\nds_read_b128 v[180:183],v251 offset:19472\nM v[20:23],v[208:211],a[228:231],v[20:23]\nds_read_b128 v[164:167],v251 offset:17424\nv_cvt_pk_bf16_f32 v131,v10,v11\nM v[20:23],v[212:215],a[244:247],v[20:23]\n" \
"buffer_load_dwordx4 v243,s[40:43],s69 offen offset:0 lds\nM v[80:83],v[208:211],a[232:235],v[80:83]\nv_cvt_pk_bf16_f32 v132,v16,v17\nv_cvt_pk_bf16_f32 v133,v18,v19\nM v[80:83],v[212:215],a[248:251],v[80:83]\ns_waitcnt lgkmcnt(10)\nv_mul_f32 v144,v144,v176\nM v[84:87],v[208:211],a[236:239],v[84:87]\n" \
"ds_read_b64_tr_b16 v[208:209],v241 offset:50688\nds_read_b64_tr_b16 v[210:211],v241 offset:50944\nM v[84:87],v[212:215],a[252:255],v[84:87]\nds_read_b64_tr_b16 v[212:213],v241 offset:58880\nds_read_b64_tr_b16 v[214:215],v241 offset:59136\nM v[24:27],v[216:219],a[224:227],v[24:27]\ns_waitcnt vmcnt(8)\nv_add_f32_dpp v168,v168,v168 quad_perm:[1,0,3,2] row_mask:0xf bank_mask:0xf\n" \
"M v[24:27],v[220:223],a[240:243],v[24:27]\nv_mul_f32 v145,v145,v177\nv_add_f32_dpp v168,v168,v168 quad_perm:[2,3,0,1] row_mask:0xf bank_mask:0xf\nM v[28:31],v[216:219],a[228:231],v[28:31]\nv_cvt_pk_bf16_f32 v168,0,v168\nv_cvt_pk_bf16_f32 v144,0,v144\nM v[28:31],v[220:223],a[244:247],v[28:31]\nbuffer_store_dword v168,v170,s[52:55],s71 offen sc1\n" \
"M v[88:91],v[216:219],a[232:235],v[88:91]\nds_read_b128 v[168:171],v251 offset:18432\nv_cvt_pk_bf16_f32 v145,0,v145\nM v[88:91],v[220:223],a[248:251],v[88:91]\ns_waitcnt lgkmcnt(5)\nv_add_f32 v144,v144,v160\nM v[92:95],v[216:219],a[236:239],v[92:95]\nds_read_b64_tr_b16 v[216:217],v242 offset:50688\n" \
"ds_read_b64_tr_b16 v[218:219],v242 offset:50944\nM v[92:95],v[220:223],a[252:255],v[92:95]\nds_read_b64_tr_b16 v[220:221],v242 offset:58880\nds_read_b64_tr_b16 v[222:223],v242 offset:59136\nM v[32:35],v[192:195],a[224:227],v[32:35]\nv_add_f32 v145,v145,v161\nv_cvt_pk_bf16_f32 v144,0,v144\nM v[32:35],v[196:199],a[240:243],v[32:35]\n" \
"v_cvt_pk_bf16_f32 v145,0,v145\nv_mul_f32 v144,s72,v144\nM v[36:39],v[192:195],a[228:231],v[36:39]\nv_mul_f32 v145,s72,v145\nv_cvt_pk_bf16_f32 v236,v144,v145\nM v[36:39],v[196:199],a[244:247],v[36:39]\nv_cvt_pk_bf16_f32 v144,v4,v5\nv_cvt_pk_bf16_f32 v145,v6,v7\n" \
"M v[96:99],v[192:195],a[232:235],v[96:99]\nv_cvt_pk_bf16_f32 v160,v64,v65\nv_cvt_pk_bf16_f32 v161,v66,v67\nM v[96:99],v[196:199],a[248:251],v[96:99]\ns_and_b32 s71,s63,2\nv_mul_f32 v140,v140,v172\nM v[100:103],v[192:195],a[236:239],v[100:103]\ns_waitcnt lgkmcnt(4)\n" \
"v_mul_f32 v136,v136,v168\nM v[100:103],v[196:199],a[252:255],v[100:103]\nv_mul_f32 v137,v137,v169\nv_mul_f32 v138,v138,v170\nM v[40:43],v[200:203],a[224:227],v[40:43]\nv_mul_f32 v139,v139,v171\nv_mul_f32 v141,v141,v173\nM v[40:43],v[204:207],a[240:243],v[40:43]\n" \
"v_mul_f32 v142,v142,v174\nv_mul_f32 v143,v143,v175\nM v[44:47],v[200:203],a[228:231],v[44:47]\ns_lshl_b32 s71,s71,9\nv_cvt_pk_bf16_f32 v176,v68,v69\nM v[44:47],v[204:207],a[244:247],v[44:47]\nv_cvt_pk_bf16_f32 v177,v70,v71\nv_cvt_pk_bf16_f32 v136,0,v136\n" \
"M v[104:107],v[200:203],a[232:235],v[104:107]\nv_cvt_pk_bf16_f32 v137,0,v137\nv_cvt_pk_bf16_f32 v138,0,v138\nM v[104:107],v[204:207],a[248:251],v[104:107]\nv_cvt_pk_bf16_f32 v139,0,v139\nv_cvt_pk_bf16_f32 v140,0,v140\nM v[108:111],v[200:203],a[236:239],v[108:111]\nv_cvt_pk_bf16_f32 v141,0,v141\n" \
"v_cvt_pk_bf16_f32 v142,0,v142\nM v[108:111],v[204:207],a[252:255],v[108:111]\nv_cvt_pk_bf16_f32 v143,0,v143\nv_lshrrev_b32 v251,5,v249\nM v[48:51],v[208:211],a[224:227],v[48:51]\ns_add_u32 s70,s70,s71\nv_add_f32 v136,v136,v152\nM v[48:51],v[212:215],a[240:243],v[48:51]\n" \
"v_add_f32 v137,v137,v153\nv_add_f32 v138,v138,v154\nM v[52:55],v[208:211],a[228:231],v[52:55]\nv_add_f32 v139,v139,v155\nv_add_f32 v140,v140,v156\nM v[52:55],v[212:215],a[244:247],v[52:55]\nv_add_f32 v141,v141,v157\nv_add_f32 v142,v142,v158\n" \
"M v[112:115],v[208:211],a[232:235],v[112:115]\ns_waitcnt vmcnt(4) lgkmcnt(0)\ns_barrier\nM v[112:115],v[212:215],a[248:251],v[112:115]\nds_read_b64_tr_b16 v[192:193],v241 offset:0\nds_read_b64_tr_b16 v[194:195],v241 offset:256\nM v[116:119],v[208:211],a[236:239],v[116:119]\nds_read_b64_tr_b16 v[196:197],v241 offset:8192\n" \
"ds_read_b64_tr_b16 v[198:199],v241 offset:8448\nM v[116:119],v[212:215],a[252:255],v[116:119]\nds_read_b64_tr_b16 v[200:201],v242 offset:0\nds_read_b64_tr_b16 v[202:203],v242 offset:256\nM v[56:59],v[216:219],a[224:227],v[56:59]\nds_read_b64_tr_b16 v[204:205],v242 offset:8192\nds_read_b64_tr_b16 v[206:207],v242 offset:8448\nM v[56:59],v[220:223],a[240:243],v[56:59]\n" \
"ds_read_b64_tr_b16 v[208:209],v241 offset:512\nds_read_b64_tr_b16 v[210:211],v241 offset:768\nM v[60:63],v[216:219],a[228:231],v[60:63]\nv_add_f32 v143,v143,v159\nv_mul_f32 v146,v146,v178\nM v[60:63],v[220:223],a[244:247],v[60:63]\nv_mul_f32 v147,v147,v179\nv_lshlrev_b32 v251,11,v251\n" \
"M v[120:123],v[216:219],a[232:235],v[120:123]\ns_and_b32 s71,s63,1\nds_read_b64_tr_b16 v[212:213],v241 offset:8704\nM v[120:123],v[220:223],a[248:251],v[120:123]\nds_read_b64_tr_b16 v[214:215],v241 offset:8960\nv_cvt_pk_bf16_f32 v136,0,v136\nM v[124:127],v[216:219],a[236:239],v[124:127]\nv_cvt_pk_bf16_f32 v137,0,v137\n" \
"v_cvt_pk_bf16_f32 v138,0,v138\nM v[124:127],v[220:223],a[252:255],v[124:127]\nv_cvt_pk_bf16_f32 v139,0,v139\ns_waitcnt lgkmcnt(10)\nM v[0:3],a[0:3],v[192:195],0\nv_cvt_pk_bf16_f32 v140,0,v140\ns_waitcnt lgkmcnt(8)\nM v[0:3],a[16:19],v[196:199],v[0:3]\n" \
"v_cvt_pk_bf16_f32 v141,0,v141\nv_cvt_pk_bf16_f32 v142,0,v142\nM v[4:7],a[4:7],v[192:195],0\nv_cvt_pk_bf16_f32 v143,0,v143\nv_cvt_pk_bf16_f32 v146,0,v146\nM v[4:7],a[20:23],v[196:199],v[4:7]\nv_cvt_pk_bf16_f32 v147,0,v147\nv_lshl_or_b32 v251,v250,8,v251\n" \
"M v[64:67],a[8:11],v[192:195],0\ns_lshl_b32 s71,s71,7\nv_mul_f32 v136,s72,v136\nM v[64:67],a[24:27],v[196:199],v[64:67]\nv_mul_f32 v137,s72,v137\nv_mul_f32 v138,s72,v138\nM v[68:71],a[12:15],v[192:195],0\nv_mul_f32 v139,s72,v139\n" \
"v_mul_f32 v140,s72,v140\nM v[68:71],a[28:31],v[196:199],v[68:71]\nv_mul_f32 v141,s72,v141\nv_mul_f32 v142,s72,v142\nv_mul_f32 v143,s72,v143\nv_add_f32 v146,v146,v162\nv_add_f32 v147,v147,v163\nv_and_b32 v250,0x70,v240\n" \
"s_add_u32 s70,s70,s71\ns_waitcnt lgkmcnt(0)\nM v[8:11],a[0:3],v[200:203],0\nv_cvt_pk_bf16_f32 v232,v136,v137\nv_cvt_pk_bf16_f32 v233,v138,v139\nv_cvt_pk_bf16_f32 v234,v140,v141\nv_cvt_pk_bf16_f32 v235,v142,v143\nv_cvt_pk_bf16_f32 v146,0,v146\n" \
"v_cvt_pk_bf16_f32 v147,0,v147\nv_or_b32 v251,v251,v250\ns_add_u32 s70,s70,1048576\nv_mul_f32 v146,s72,v146\nv_mul_f32 v147,s72,v147\nbuffer_store_dwordx4 v[232:235],v251,s[52:55],s70 offen sc1\nM v[8:11],a[16:19],v[204:207],v[8:11]\nv_cvt_pk_bf16_f32 v237,v146,v147\n" \
"v_mul_f32 v148,v148,v180\nv_mul_f32 v149,v149,v181\nv_mul_f32 v150,v150,v182\nv_mul_f32 v151,v151,v183\nv_cvt_pk_bf16_f32 v146,v12,v13\nv_cvt_pk_bf16_f32 v147,v14,v15\nM v[12:15],a[4:7],v[200:203],0\nv_cvt_pk_bf16_f32 v148,0,v148\n" \
"v_cvt_pk_bf16_f32 v149,0,v149\nv_cvt_pk_bf16_f32 v150,0,v150\nv_cvt_pk_bf16_f32 v151,0,v151\nv_add_f32 v148,v148,v164\nv_add_f32 v149,v149,v165\nv_add_f32 v150,v150,v166\nv_add_f32 v151,v151,v167\nM v[12:15],a[20:23],v[204:207],v[12:15]\n" \
"v_cvt_pk_bf16_f32 v148,0,v148\nv_cvt_pk_bf16_f32 v149,0,v149\nv_cvt_pk_bf16_f32 v150,0,v150\nv_cvt_pk_bf16_f32 v151,0,v151\nv_mul_f32 v148,s72,v148\nv_mul_f32 v149,s72,v149\nv_mul_f32 v150,s72,v150\nv_mul_f32 v151,s72,v151\n" \
"v_cvt_pk_bf16_f32 v162,v72,v73\nv_cvt_pk_bf16_f32 v163,v74,v75\nM v[72:75],a[8:11],v[200:203],0\nv_cvt_pk_bf16_f32 v238,v148,v149\nv_cvt_pk_bf16_f32 v239,v150,v151\ns_add_u32 s71,s70,16384\nbuffer_load_dwordx4 v244,s[40:43],s69 offen offset:1024 lds\nbuffer_load_dwordx4 v245,s[40:43],s69 offen offset:2048 lds\n" \
"buffer_load_dwordx4 v246,s[40:43],s69 offen offset:3072 lds\nbuffer_store_dwordx4 v[236:239],v251,s[52:55],s71 offen sc1\nM v[72:75],a[24:27],v[204:207],v[72:75]\nv_cvt_pk_bf16_f32 v178,v76,v77\nv_cvt_pk_bf16_f32 v179,v78,v79\nds_read_b64_tr_b16 v[216:217],v242 offset:512\nds_read_b64_tr_b16 v[218:219],v242 offset:768\nM v[76:79],a[12:15],v[200:203],0\n" \
"ds_read_b64_tr_b16 v[220:221],v242 offset:8704\nds_read_b64_tr_b16 v[222:223],v242 offset:8960\nM v[76:79],a[28:31],v[204:207],v[76:79]\nv_cvt_pk_bf16_f32 v148,v20,v21\nv_cvt_pk_bf16_f32 v149,v22,v23\nM v[16:19],a[0:3],v[208:211],0\nv_cvt_pk_bf16_f32 v164,v80,v81\nv_cvt_pk_bf16_f32 v165,v82,v83\n" \
"M v[16:19],a[16:19],v[212:215],v[16:19]\nv_cvt_pk_bf16_f32 v180,v84,v85\nv_cvt_pk_bf16_f32 v181,v86,v87\nM v[20:23],a[4:7],v[208:211],0\nds_read_b64_tr_b16 v[192:193],v241 offset:1024\nds_read_b64_tr_b16 v[194:195],v241 offset:1280\nM v[20:23],a[20:23],v[212:215],v[20:23]\nds_read_b64_tr_b16 v[196:197],v241 offset:9216\n" \
"ds_read_b64_tr_b16 v[198:199],v241 offset:9472\nM v[80:83],a[8:11],v[208:211],0\nv_cvt_pk_bf16_f32 v134,v24,v25\nv_cvt_pk_bf16_f32 v135,v26,v27\nM v[80:83],a[24:27],v[212:215],v[80:83]\nv_cvt_pk_bf16_f32 v150,v28,v29\nv_cvt_pk_bf16_f32 v151,v30,v31\nM v[84:87],a[12:15],v[208:211],0\n" \
"v_cvt_pk_bf16_f32 v166,v88,v89\nv_cvt_pk_bf16_f32 v167,v90,v91\nM v[84:87],a[28:31],v[212:215],v[84:87]\nv_cvt_pk_bf16_f32 v182,v92,v93\ns_waitcnt lgkmcnt(6)\nM v[24:27],a[0:3],v[216:219],0\nv_cvt_pk_bf16_f32 v183,v94,v95\ns_waitcnt lgkmcnt(4)\n" \
"M v[24:27],a[16:19],v[220:223],v[24:27]\nds_read_b64_tr_b16 v[200:201],v242 offset:1024\nds_read_b64_tr_b16 v[202:203],v242 offset:1280\nM v[28:31],a[4:7],v[216:219],0\nds_read_b64_tr_b16 v[204:205],v242 offset:9216\nds_read_b64_tr_b16 v[206:207],v242 offset:9472\nM v[28:31],a[20:23],v[220:223],v[28:31]\nv_cvt_pk_bf16_f32 v136,v32,v33\n" \
"v_cvt_pk_bf16_f32 v137,v34,v35\nM v[88:91],a[8:11],v[216:219],0\nv_cvt_pk_bf16_f32 v152,v36,v37\nv_cvt_pk_bf16_f32 v153,v38,v39\nM v[88:91],a[24:27],v[220:223],v[88:91]\nv_cvt_pk_bf16_f32 v168,v96,v97\nv_cvt_pk_bf16_f32 v169,v98,v99\nM v[92:95],a[12:15],v[216:219],0\n" \
"s_add_u32 m0,s64,131072\ns_add_u32 s69,s68,1048576\nM v[92:95],a[28:31],v[220:223],v[92:95]\nv_cvt_pk_bf16_f32 v184,v100,v101\ns_waitcnt lgkmcnt(4)\nM v[32:35],a[0:3],v[192:195],0\nv_cvt_pk_bf16_f32 v185,v102,v103\nds_read_b64_tr_b16 v[208:209],v241 offset:1536\n" \
"M v[32:35],a[16:19],v[196:199],v[32:35]\nds_read_b64_tr_b16 v[210:211],v241 offset:1792\nds_read_b64_tr_b16 v[212:213],v241 offset:9728\nM v[36:39],a[4:7],v[192:195],0\nds_read_b64_tr_b16 v[214:215],v241 offset:9984\nv_cvt_pk_bf16_f32 v138,v40,v41\nM v[36:39],a[20:23],v[196:199],v[36:39]\nv_cvt_pk_bf16_f32 v139,v42,v43\n" \
"v_cvt_pk_bf16_f32 v154,v44,v45\nM v[96:99],a[8:11],v[192:195],0\nv_cvt_pk_bf16_f32 v155,v46,v47\nds_read_b64_tr_b16 v[216:217],v242 offset:1536\nM v[96:99],a[24:27],v[196:199],v[96:99]\nds_read_b64_tr_b16 v[218:219],v242 offset:1792\nds_read_b64_tr_b16 v[220:221],v242 offset:9728\nM v[100:103],a[12:15],v[192:195],0\n" \
"ds_read_b64_tr_b16 v[222:223],v242 offset:9984\nv_cvt_pk_bf16_f32 v170,v104,v105\nM v[100:103],a[28:31],v[196:199],v[100:103]\nv_cvt_pk_bf16_f32 v171,v106,v107\ns_waitcnt lgkmcnt(8)\nM v[40:43],a[0:3],v[200:203],0\nv_cvt_pk_bf16_f32 v186,v108,v109\nv_cvt_pk_bf16_f32 v187,v110,v111\n" \
"M v[40:43],a[16:19],v[204:207],v[40:43]\nv_cvt_pk_bf16_f32 v140,v48,v49\nv_cvt_pk_bf16_f32 v141,v50,v51\nM v[44:47],a[4:7],v[200:203],0\nv_cvt_pk_bf16_f32 v156,v52,v53\nv_cvt_pk_bf16_f32 v157,v54,v55\nM v[44:47],a[20:23],v[204:207],v[44:47]\nv_cvt_pk_bf16_f32 v172,v112,v113\n" \
"v_cvt_pk_bf16_f32 v173,v114,v115\nM v[104:107],a[8:11],v[200:203],0\nv_cvt_pk_bf16_f32 v188,v116,v117\nv_cvt_pk_bf16_f32 v189,v118,v119\nM v[104:107],a[24:27],v[204:207],v[104:107]\nv_cvt_pk_bf16_f32 v142,v56,v57\nv_cvt_pk_bf16_f32 v143,v58,v59\nM v[108:111],a[12:15],v[200:203],0\n" \
"v_cvt_pk_bf16_f32 v158,v60,v61\nv_cvt_pk_bf16_f32 v159,v62,v63\nM v[108:111],a[28:31],v[204:207],v[108:111]\nv_cvt_pk_bf16_f32 v174,v120,v121\ns_waitcnt lgkmcnt(2)\nM v[48:51],a[0:3],v[208:211],0\nv_cvt_pk_bf16_f32 v175,v122,v123\nv_cvt_pk_bf16_f32 v190,v124,v125\n" \
"M v[48:51],a[16:19],v[212:215],v[48:51]\nv_cvt_pk_bf16_f32 v191,v126,v127\ns_lshl_b32 s70,s62,5\nM v[52:55],a[4:7],v[208:211],0\ns_add_u32 s70,s70,s63\ns_lshl2_add_u32 s70,s70,s7\nM v[52:55],a[20:23],v[212:215],v[52:55]\ns_lshl2_add_u32 s79,s70,90112\n" \
"s_lshl_b32 s70,s60,4\nM v[112:115],a[8:11],v[208:211],0\ns_waitcnt vmcnt(1) lgkmcnt(0)\ns_barrier\nM v[112:115],a[24:27],v[212:215],v[112:115]\nds_read_b64_tr_b16 v[192:193],v241 offset:16384\nds_read_b64_tr_b16 v[194:195],v241 offset:16640\nM v[116:119],a[12:15],v[208:211],0\n" \
"ds_read_b64_tr_b16 v[196:197],v241 offset:24576\nds_read_b64_tr_b16 v[198:199],v241 offset:24832\nM v[116:119],a[28:31],v[212:215],v[116:119]\nds_read_b64_tr_b16 v[200:201],v242 offset:16384\nds_read_b64_tr_b16 v[202:203],v242 offset:16640\nM v[56:59],a[0:3],v[216:219],0\nds_read_b64_tr_b16 v[204:205],v242 offset:24576\nds_read_b64_tr_b16 v[206:207],v242 offset:24832\n" \
"M v[56:59],a[16:19],v[220:223],v[56:59]\nds_read_b64_tr_b16 v[208:209],v241 offset:16896\nds_read_b64_tr_b16 v[210:211],v241 offset:17152\nM v[60:63],a[4:7],v[216:219],0\nds_read_b64_tr_b16 v[212:213],v241 offset:25088\nds_read_b64_tr_b16 v[214:215],v241 offset:25344\nM v[60:63],a[20:23],v[220:223],v[60:63]\nbuffer_load_dwordx4 v243,s[40:43],s69 offen offset:0 lds\n" \
"M v[120:123],a[8:11],v[216:219],0\ns_sub_u32 s70,s70,s62\ns_add_u32 s70,s70,15\nM v[120:123],a[24:27],v[220:223],v[120:123]\ns_lshl2_add_u32 s70,s70,s7\ns_lshl_b32 s70,s70,15\nM v[124:127],a[12:15],v[216:219],0\nds_read_b64_tr_b16 v[216:217],v242 offset:16896\n" \
"ds_read_b64_tr_b16 v[218:219],v242 offset:17152\nM v[124:127],a[28:31],v[220:223],v[124:127]\ns_lshl_b32 s71,s61,8\ns_waitcnt lgkmcnt(12)\nM v[0:3],a[32:35],v[192:195],v[0:3]\nds_read_b64_tr_b16 v[220:221],v242 offset:25088\ns_waitcnt lgkmcnt(11)\nM v[0:3],a[48:51],v[196:199],v[0:3]\n" \
"buffer_load_dwordx4 v244,s[40:43],s69 offen offset:1024 lds\nM v[4:7],a[36:39],v[192:195],v[4:7]\nds_read_b64_tr_b16 v[222:223],v242 offset:25344\ns_add_u32 s70,s70,s71\nM v[4:7],a[52:55],v[196:199],v[4:7]\ns_add_u32 s78,s70,54525952\ns_lshl3_add_u32 s70,s62,s7\nM v[64:67],a[40:43],v[192:195],v[64:67]\n" \
"s_lshl_b32 s76,s70,15\ns_lshl_b32 s80,s70,6\nM v[64:67],a[56:59],v[196:199],v[64:67]\nbuffer_load_dwordx4 v245,s[40:43],s69 offen offset:2048 lds\nM v[68:71],a[44:47],v[192:195],v[68:71]\ns_add_u32 s76,s76,1048576\ns_add_u32 s77,s76,131072\nM v[68:71],a[60:63],v[196:199],v[68:71]\n" \
"s_lshl_b32 s70,s94,12\ns_waitcnt lgkmcnt(4)\nM v[8:11],a[32:35],v[200:203],v[8:11]\ns_or_b32 s76,s76,s70\ns_or_b32 s77,s77,s70\nM v[8:11],a[48:51],v[204:207],v[8:11]\nbuffer_load_dwordx4 v246,s[40:43],s69 offen offset:3072 lds\nM v[12:15],a[36:39],v[200:203],v[12:15]\n" \
"s_lshl_b32 s70,s63,5\ns_add_u32 s70,s70,s74\nM v[12:15],a[52:55],v[204:207],v[12:15]\ns_lshl_b32 s74,s70,14\ns_add_u32 s74,s74,3145728\nM v[72:75],a[40:43],v[200:203],v[72:75]\nds_read_b64_tr_b16 v[192:193],v241 offset:17408\nds_read_b64_tr_b16 v[194:195],v241 offset:17664\n" \
"M v[72:75],a[56:59],v[204:207],v[72:75]\nds_read_b64_tr_b16 v[196:197],v241 offset:25600\nds_read_b64_tr_b16 v[198:199],v241 offset:25856\nM v[76:79],a[44:47],v[200:203],v[76:79]\nds_read_b64_tr_b16 v[200:201],v242 offset:17408\nds_read_b64_tr_b16 v[202:203],v242 offset:17664\nM v[76:79],a[60:63],v[204:207],v[76:79]\nds_read_b64_tr_b16 v[204:205],v242 offset:25600\n" \
"ds_read_b64_tr_b16 v[206:207],v242 offset:25856\nM v[16:19],a[32:35],v[208:211],v[16:19]\nbuffer_store_dwordx4 v[128:131],v240,s[52:55],s74 offen offset:0 sc1\nM v[16:19],a[48:51],v[212:215],v[16:19]\ns_add_u32 m0,s64,147456\ns_add_u32 s69,s68,1572864\nM v[20:23],a[36:39],v[208:211],v[20:23]\nv_lshlrev_b32 v228,2,v228\n" \
"v_lshl_or_b32 v228,v229,4,v228\nM v[20:23],a[52:55],v[212:215],v[20:23]\ns_add_u32 s80,s80,90112\ns_lshl2_add_u32 s29,s70,81920\nM v[80:83],a[40:43],v[208:211],v[80:83]\nbuffer_load_dwordx4 v243,s[40:43],s69 offen offset:0 lds\nM v[80:83],a[56:59],v[212:215],v[80:83]\ns_lshl_b32 s70,s7,14\n" \
"v_add_u32 v250,s70,v240\nM v[84:87],a[44:47],v[208:211],v[84:87]\nds_read_b64_tr_b16 v[208:209],v241 offset:17920\nds_read_b64_tr_b16 v[210:211],v241 offset:18176\nM v[84:87],a[60:63],v[212:215],v[84:87]\nds_read_b64_tr_b16 v[212:213],v241 offset:26112\ns_waitcnt lgkmcnt(8)\nM v[24:27],a[32:35],v[216:219],v[24:27]\n" \
"ds_read_b64_tr_b16 v[214:215],v241 offset:26368\ns_lshl_b32 s70,s63,19\nM v[24:27],a[48:51],v[220:223],v[24:27]\nbuffer_load_dwordx4 v244,s[40:43],s69 offen offset:1024 lds\nM v[28:31],a[36:39],v[216:219],v[28:31]\nds_write_b64 v250,v[128:129] offset:0\ns_add_u32 s72,s78,16384\nM v[28:31],a[52:55],v[220:223],v[28:31]\n" \
"ds_write_b64 v250,v[130:131] offset:8\ns_mul_i32 s71,s60,14\nM v[88:91],a[40:43],v[216:219],v[88:91]\nds_write_b64 v250,v[132:133] offset:1024\nv_and_b32 v229,15,v249\nM v[88:91],a[56:59],v[220:223],v[88:91]\nbuffer_load_dwordx4 v245,s[40:43],s69 offen offset:2048 lds\nM v[92:95],a[44:47],v[216:219],v[92:95]\n" \
"ds_read_b64_tr_b16 v[216:217],v242 offset:17920\nds_read_b64_tr_b16 v[218:219],v242 offset:18176\nM v[92:95],a[60:63],v[220:223],v[92:95]\nds_read_b64_tr_b16 v[220:221],v242 offset:26112\nds_read_b64_tr_b16 v[222:223],v242 offset:26368\nM v[32:35],a[32:35],v[192:195],v[32:35]\nv_lshlrev_b32 v229,7,v229\ns_waitcnt lgkmcnt(10)\n" \
"M v[32:35],a[48:51],v[196:199],v[32:35]\nbuffer_load_dwordx4 v246,s[40:43],s69 offen offset:3072 lds\nM v[36:39],a[36:39],v[192:195],v[36:39]\ns_add_u32 m0,s64,98304\ns_add_u32 s69,s68,2097152\nM v[36:39],a[52:55],v[196:199],v[36:39]\nds_write_b64 v250,v[134:135] offset:1032\nv_add_u32 v253,0xffd00000,v253\n" \
"M v[96:99],a[40:43],v[192:195],v[96:99]\ns_add_u32 s44,s34,s70\ns_addc_u32 s45,s35,0\nM v[96:99],a[56:59],v[196:199],v[96:99]\nbuffer_store_dwordx4 v[132:135],v240,s[52:55],s74 offen offset:1024 sc1\nM v[100:103],a[44:47],v[192:195],v[100:103]\ns_lshl_b32 s70,s62,15\ns_add_u32 s96,s70,s64\n" \
"M v[100:103],a[60:63],v[196:199],v[100:103]\ns_sub_u32 s96,s96,16384\ns_sub_u32 s46,491520,s70\nM v[40:43],a[32:35],v[200:203],v[40:43]\ns_cmp_eq_u32 s36,s96\ns_cselect_b32 s72,0xc000,s98\ns_add_u32 s65,s36,s72\nM v[40:43],a[48:51],v[204:207],v[40:43]\n" \
"buffer_load_dwordx4 v243,s[40:43],s69 offen offset:0 lds\nM v[44:47],a[36:39],v[200:203],v[44:47]\ns_cmp_eq_u32 s65,s96\ns_cselect_b32 s72,0xc000,s98\ns_add_u32 s66,s65,s72\nM v[44:47],a[52:55],v[204:207],v[44:47]\ns_cmp_eq_u32 s66,s96\ns_cselect_b32 s72,0xc000,s98\n" \
"s_add_u32 s95,s66,s72\nM v[104:107],a[40:43],v[200:203],v[104:107]\ns_lshl2_add_u32 s70,s62,0\ns_sub_u32 s70,s70,s7\nM v[104:107],a[56:59],v[204:207],v[104:107]\nbuffer_load_dwordx4 v244,s[40:43],s69 offen offset:1024 lds\nM v[108:111],a[44:47],v[200:203],v[108:111]\nds_write_b64 v250,v[136:137] offset:2048\n" \
"s_lshl_b32 s70,s70,16\nM v[108:111],a[60:63],v[204:207],v[108:111]\ns_add_u32 s33,s78,s70\ns_waitcnt lgkmcnt(1)\nM v[48:51],a[32:35],v[208:211],v[48:51]\ns_cmp_eq_u32 s62,7\ns_cselect_b32 s46,229376,s46\nM v[48:51],a[48:51],v[212:215],v[48:51]\n" \
"s_waitcnt vmcnt(12)\nbuffer_store_dword v252,off,s[52:55],s79 sc1\nM v[52:55],a[36:39],v[208:211],v[52:55]\nbuffer_load_dwordx4 v245,s[40:43],s69 offen offset:2048 lds\nM v[52:55],a[52:55],v[212:215],v[52:55]\ns_cmp_gt_i32 s62,-1\ns_cselect_b32 s72,s98,0xfffe4000\nM v[112:115],a[40:43],v[208:211],v[112:115]\n" \
"s_waitcnt vmcnt(10)\ns_barrier\nM v[112:115],a[56:59],v[212:215],v[112:115]\nds_read_b64_tr_b16 v[192:193],v241 offset:32768\nds_read_b64_tr_b16 v[194:195],v241 offset:33024\nM v[116:119],a[44:47],v[208:211],v[116:119]\nds_read_b64_tr_b16 v[196:197],v241 offset:40960\nds_read_b64_tr_b16 v[198:199],v241 offset:41216\n" \
"M v[116:119],a[60:63],v[212:215],v[116:119]\nbuffer_store_dwordx4 v[136:139],v240,s[52:55],s74 offen offset:2048 sc1\nM v[56:59],a[32:35],v[216:219],v[56:59]\nds_read_b64_tr_b16 v[200:201],v242 offset:32768\nds_read_b64_tr_b16 v[202:203],v242 offset:33024\nM v[56:59],a[48:51],v[220:223],v[56:59]\nds_read_b64_tr_b16 v[204:205],v242 offset:40960\nds_read_b64_tr_b16 v[206:207],v242 offset:41216\n" \
"M v[60:63],a[36:39],v[216:219],v[60:63]\nds_read_b64_tr_b16 v[208:209],v241 offset:33280\nds_read_b64_tr_b16 v[210:211],v241 offset:33536\nM v[60:63],a[52:55],v[220:223],v[60:63]\nds_read_b64_tr_b16 v[212:213],v241 offset:41472\nds_read_b64_tr_b16 v[214:215],v241 offset:41728\nM v[120:123],a[40:43],v[216:219],v[120:123]\nbuffer_load_dwordx4 v246,s[40:43],s69 offen offset:3072 lds\n" \
"M v[120:123],a[56:59],v[220:223],v[120:123]\ns_add_u32 m0,s64,114688\ns_add_u32 s69,s68,2621440\nM v[124:127],a[44:47],v[216:219],v[124:127]\nds_read_b64_tr_b16 v[216:217],v242 offset:33280\nds_read_b64_tr_b16 v[218:219],v242 offset:33536\nM v[124:127],a[60:63],v[220:223],v[124:127]\nds_read_b64_tr_b16 v[220:221],v242 offset:41472\n" \
"s_waitcnt lgkmcnt(13)\nM v[0:3],a[64:67],v[192:195],v[0:3]\nds_read_b64_tr_b16 v[222:223],v242 offset:41728\ns_waitcnt lgkmcnt(12)\nM v[0:3],a[80:83],v[196:199],v[0:3]\nbuffer_load_dwordx4 v243,s[40:43],s69 offen offset:0 lds\nM v[4:7],a[68:71],v[192:195],v[4:7]\nds_write_b64 v250,v[138:139] offset:2056\n" \
"s_add_u32 s15,s64,s46\nM v[4:7],a[84:87],v[196:199],v[4:7]\ns_lshl3_add_u32 s70,s62,s7\ns_add_u32 s15,s15,s72\nM v[64:67],a[72:75],v[192:195],v[64:67]\ns_lshl3_add_u32 s70,s70,s60\ns_lshl_b32 s67,s70,15\nM v[64:67],a[88:91],v[196:199],v[64:67]\n" \
"buffer_load_dwordx4 v244,s[40:43],s69 offen offset:1024 lds\nM v[68:71],a[76:79],v[192:195],v[68:71]\nds_read_b64_tr_b16 v[192:193],v241 offset:33792\nds_read_b64_tr_b16 v[194:195],v241 offset:34048\nM v[68:71],a[92:95],v[196:199],v[68:71]\nds_read_b64_tr_b16 v[196:197],v241 offset:41984\ns_waitcnt lgkmcnt(8)\nM v[8:11],a[64:67],v[200:203],v[8:11]\n" \
"ds_read_b64_tr_b16 v[198:199],v241 offset:42240\ns_add_u32 s97,s96,65536\nM v[8:11],a[80:83],v[204:207],v[8:11]\nbuffer_store_dwordx4 v[140:143],v240,s[52:55],s74 offen offset:3072 sc1\nM v[12:15],a[68:71],v[200:203],v[12:15]\ns_add_u32 s74,s74,4096\ns_lshl3_add_u32 s70,s62,s60\nM v[12:15],a[84:87],v[204:207],v[12:15]\n" \
"ds_write_b64 v250,v[140:141] offset:3072\ns_lshl2_add_u32 s70,s70,s7\nM v[72:75],a[72:75],v[200:203],v[72:75]\ns_cmp_eq_u32 s62,7\ns_cselect_b32 s72,98304,0\nM v[72:75],a[88:91],v[204:207],v[72:75]\nbuffer_load_dwordx4 v245,s[40:43],s69 offen offset:2048 lds\nM v[76:79],a[76:79],v[200:203],v[76:79]\n" \
"ds_read_b64_tr_b16 v[200:201],v242 offset:33792\nds_read_b64_tr_b16 v[202:203],v242 offset:34048\nM v[76:79],a[92:95],v[204:207],v[76:79]\nds_read_b64_tr_b16 v[204:205],v242 offset:41984\nds_read_b64_tr_b16 v[206:207],v242 offset:42240\nM v[16:19],a[64:67],v[208:211],v[16:19]\ns_sub_u32 s97,s97,s72\ns_lshl4_add_u32 s79,s70,65536\n" \
"M v[16:19],a[80:83],v[212:215],v[16:19]\nbuffer_load_dwordx4 v246,s[40:43],s69 offen offset:3072 lds\nM v[20:23],a[68:71],v[208:211],v[20:23]\nds_write_b64 v250,v[142:143] offset:3080\ns_lshr_b32 s70,s6,3\nM v[20:23],a[84:87],v[212:215],v[20:23]\ns_cmp_lt_u32 s62,4\ns_cselect_b32 s72,32768,0\n" \
"M v[80:83],a[72:75],v[208:211],v[80:83]\ns_add_u32 s97,s97,s72\ns_sub_u32 s70,s70,16\nM v[80:83],a[88:91],v[212:215],v[80:83]\ns_add_u32 s2,s70,s71\ns_lshl_b32 s72,s60,1\nM v[84:87],a[76:79],v[208:211],v[84:87]\nds_read_b64_tr_b16 v[208:209],v241 offset:34304\n" \
"ds_read_b64_tr_b16 v[210:211],v241 offset:34560\nM v[84:87],a[92:95],v[212:215],v[84:87]\nds_read_b64_tr_b16 v[212:213],v241 offset:42496\ns_waitcnt lgkmcnt(8)\nM v[24:27],a[64:67],v[216:219],v[24:27]\nbuffer_store_dwordx4 v[144:147],v240,s[52:55],s74 offen offset:0 sc1\nM v[24:27],a[80:83],v[220:223],v[24:27]\nds_read_b64_tr_b16 v[214:215],v241 offset:42752\n" \
"s_mul_i32 s71,s60,448\nM v[28:31],a[68:71],v[216:219],v[28:31]\ns_add_u32 s72,s72,s62\ns_and_b32 s71,s71,0xffffff80\nM v[28:31],a[84:87],v[220:223],v[28:31]\ns_sub_u32 s72,s72,6\ns_sub_u32 s58,1835008,s71\nM v[88:91],a[72:75],v[216:219],v[88:91]\n" \
"s_lshl_b32 s72,s72,18\ns_lshl_b32 s2,s2,5\nM v[88:91],a[88:91],v[220:223],v[88:91]\ns_cmp_ge_u32 s62,6\ns_cselect_b64 s[56:57],s[18:19],s[56:57]\ns_cselect_b32 s71,s72,s71\ns_cselect_b32 s58,0x40000,s58\ns_cselect_b32 s0,2048,s0\n" \
"M v[92:95],a[76:79],v[216:219],v[92:95]\nds_read_b64_tr_b16 v[216:217],v242 offset:34304\nds_read_b64_tr_b16 v[218:219],v242 offset:34560\nM v[92:95],a[92:95],v[220:223],v[92:95]\nds_read_b64_tr_b16 v[220:221],v242 offset:42496\nds_read_b64_tr_b16 v[222:223],v242 offset:42752\nM v[32:35],a[64:67],v[192:195],v[32:35]\nds_write_b64 v250,v[144:145] offset:4096\n" \
"s_mul_i32 s72,s60,229376\nM v[32:35],a[80:83],v[196:199],v[32:35]\ns_cmp_eq_u32 s62,3\ns_cselect_b64 s[56:57],s[26:27],s[56:57]\ns_cselect_b32 s71,s72,s71\ns_cselect_b32 s58,229376,s58\ns_cselect_b32 s0,4096,s0\nM v[36:39],a[68:71],v[192:195],v[36:39]\n" \
"s_cmp_lt_u32 s70,14\ns_cselect_b32 s2,s2,0\ns_cselect_b32 s3,-1,0\nM v[36:39],a[84:87],v[196:199],v[36:39]\ns_lshl2_add_u32 s70,s61,s7\nv_lshrrev_b32 v253,4,v249\nM v[96:99],a[72:75],v[192:195],v[96:99]\nv_and_b32 v251,0xf0,v240\n" \
"s_mul_i32 s1,s70,s0\nM v[96:99],a[88:91],v[196:199],v[96:99]\ns_add_u32 s48,s44,19922944\ns_addc_u32 s49,s45,0\nM v[100:103],a[76:79],v[192:195],v[100:103]\nv_lshl_or_b32 v253,v253,12,v251\ns_lshl_b32 s70,s61,8\nM v[100:103],a[92:95],v[196:199],v[100:103]\n" \
"s_mov_b32 s50,s46\ns_waitcnt lgkmcnt(4)\nM v[40:43],a[64:67],v[200:203],v[40:43]\ns_add_u32 s44,s44,3145728\ns_addc_u32 s45,s45,0\nM v[40:43],a[80:83],v[204:207],v[40:43]\ns_mov_b32 s94,0xc000\ns_sub_u32 s33,s33,1867776\n" \
"M v[44:47],a[68:71],v[200:203],v[44:47]\nbuffer_store_dwordx4 v[148:151],v240,s[52:55],s74 offen offset:1024 sc1\nM v[44:47],a[84:87],v[204:207],v[44:47]\nds_write_b64 v250,v[146:147] offset:4104\ns_add_u32 s67,s67,36700160\nM v[104:107],a[72:75],v[200:203],v[104:107]\ns_cmp_lt_u32 s62,3\ns_cselect_b32 s58,0,s58\n" \
"M v[104:107],a[88:91],v[204:207],v[104:107]\ns_add_u32 s56,s56,s71\ns_addc_u32 s57,s57,0\nM v[108:111],a[76:79],v[200:203],v[108:111]\ns_lshl_b32 s0,s0,4\nM v[108:111],a[92:95],v[204:207],v[108:111]\nM v[48:51],a[64:67],v[208:211],v[48:51]\nM v[48:51],a[80:83],v[212:215],v[48:51]\n" \
"M v[52:55],a[68:71],v[208:211],v[52:55]\ns_add_u32 m0,s64,131072\ns_add_u32 s69,s68,3145728\nM v[52:55],a[84:87],v[212:215],v[52:55]\nM v[112:115],a[72:75],v[208:211],v[112:115]\ns_waitcnt vmcnt(14) lgkmcnt(1)\ns_barrier\nM v[112:115],a[88:91],v[212:215],v[112:115]\n" \
"ds_read_b64_tr_b16 v[192:193],v241 offset:49152\nds_read_b64_tr_b16 v[194:195],v241 offset:49408\nM v[116:119],a[76:79],v[208:211],v[116:119]\nds_read_b64_tr_b16 v[196:197],v241 offset:57344\nds_read_b64_tr_b16 v[198:199],v241 offset:57600\nM v[116:119],a[92:95],v[212:215],v[116:119]\nds_read_b64_tr_b16 v[200:201],v242 offset:49152\nds_read_b64_tr_b16 v[202:203],v242 offset:49408\n" \
"M v[56:59],a[64:67],v[216:219],v[56:59]\nds_read_b64_tr_b16 v[204:205],v242 offset:57344\nds_read_b64_tr_b16 v[206:207],v242 offset:57600\nM v[56:59],a[80:83],v[220:223],v[56:59]\nds_read_b64_tr_b16 v[208:209],v241 offset:49664\nds_read_b64_tr_b16 v[210:211],v241 offset:49920\nM v[60:63],a[68:71],v[216:219],v[60:63]\nds_read_b64_tr_b16 v[212:213],v241 offset:57856\n" \
"ds_read_b64_tr_b16 v[214:215],v241 offset:58112\nM v[60:63],a[84:87],v[220:223],v[60:63]\nbuffer_load_dwordx4 v243,s[40:43],s69 offen offset:0 lds\nM v[120:123],a[72:75],v[216:219],v[120:123]\nds_write_b64 v250,v[148:149] offset:5120\nM v[120:123],a[88:91],v[220:223],v[120:123]\nM v[124:127],a[76:79],v[216:219],v[124:127]\nds_read_b64_tr_b16 v[216:217],v242 offset:49664\n" \
"ds_read_b64_tr_b16 v[218:219],v242 offset:49920\nM v[124:127],a[92:95],v[220:223],v[124:127]\nds_read_b64_tr_b16 v[220:221],v242 offset:57856\ns_waitcnt lgkmcnt(14)\nM v[0:3],a[96:99],v[192:195],v[0:3]\nbuffer_store_dwordx4 v[152:155],v240,s[52:55],s74 offen offset:2048 sc1\ns_waitcnt lgkmcnt(12)\nM v[0:3],a[112:115],v[196:199],v[0:3]\n" \
"ds_read_b64_tr_b16 v[222:223],v242 offset:58112\nM v[4:7],a[100:103],v[192:195],v[4:7]\nds_write_b64 v250,v[150:151] offset:5128\nM v[4:7],a[116:119],v[196:199],v[4:7]\nM v[64:67],a[104:107],v[192:195],v[64:67]\nbuffer_load_dwordx4 v244,s[40:43],s69 offen offset:1024 lds\nM v[64:67],a[120:123],v[196:199],v[64:67]\nM v[68:71],a[108:111],v[192:195],v[68:71]\n" \
"ds_read_b64_tr_b16 v[192:193],v241 offset:50176\nds_read_b64_tr_b16 v[194:195],v241 offset:50432\nM v[68:71],a[124:127],v[196:199],v[68:71]\nds_read_b64_tr_b16 v[196:197],v241 offset:58368\ns_waitcnt lgkmcnt(9)\nM v[8:11],a[96:99],v[200:203],v[8:11]\nbuffer_load_dwordx4 v245,s[40:43],s69 offen offset:2048 lds\nM v[8:11],a[112:115],v[204:207],v[8:11]\n" \
"ds_read_b64_tr_b16 v[198:199],v241 offset:58624\nM v[12:15],a[100:103],v[200:203],v[12:15]\nM v[12:15],a[116:119],v[204:207],v[12:15]\nM v[72:75],a[104:107],v[200:203],v[72:75]\nbuffer_load_dwordx4 v246,s[40:43],s69 offen offset:3072 lds\nM v[72:75],a[120:123],v[204:207],v[72:75]\nM v[76:79],a[108:111],v[200:203],v[76:79]\nds_read_b64_tr_b16 v[200:201],v242 offset:50176\n" \
"ds_read_b64_tr_b16 v[202:203],v242 offset:50432\nM v[76:79],a[124:127],v[204:207],v[76:79]\nds_read_b64_tr_b16 v[204:205],v242 offset:58368\nds_read_b64_tr_b16 v[206:207],v242 offset:58624\nM v[16:19],a[96:99],v[208:211],v[16:19]\nbuffer_store_dwordx4 v[156:159],v240,s[52:55],s74 offen offset:3072 sc1\nM v[16:19],a[112:115],v[212:215],v[16:19]\nds_write_b64 v250,v[152:153] offset:6144\n" \
"s_add_u32 s74,s74,4096\nM v[20:23],a[100:103],v[208:211],v[20:23]\nM v[20:23],a[116:119],v[212:215],v[20:23]\nM v[80:83],a[104:107],v[208:211],v[80:83]\nM v[80:83],a[120:123],v[212:215],v[80:83]\nM v[84:87],a[108:111],v[208:211],v[84:87]\nds_read_b64_tr_b16 v[208:209],v241 offset:50688\nds_read_b64_tr_b16 v[210:211],v241 offset:50944\n" \
"M v[84:87],a[124:127],v[212:215],v[84:87]\nds_read_b64_tr_b16 v[212:213],v241 offset:58880\ns_waitcnt lgkmcnt(8)\nM v[24:27],a[96:99],v[216:219],v[24:27]\nds_read_b64_tr_b16 v[214:215],v241 offset:59136\nM v[24:27],a[112:115],v[220:223],v[24:27]\nM v[28:31],a[100:103],v[216:219],v[28:31]\nds_write_b64 v250,v[154:155] offset:6152\n" \
"M v[28:31],a[116:119],v[220:223],v[28:31]\nM v[88:91],a[104:107],v[216:219],v[88:91]\nM v[88:91],a[120:123],v[220:223],v[88:91]\nM v[92:95],a[108:111],v[216:219],v[92:95]\nds_read_b64_tr_b16 v[216:217],v242 offset:50688\nds_read_b64_tr_b16 v[218:219],v242 offset:50944\nM v[92:95],a[124:127],v[220:223],v[92:95]\nds_read_b64_tr_b16 v[220:221],v242 offset:58880\n" \
"ds_read_b64_tr_b16 v[222:223],v242 offset:59136\nM v[32:35],a[96:99],v[192:195],v[32:35]\nbuffer_store_dwordx4 v[160:163],v240,s[52:55],s74 offen offset:0 sc1\nM v[32:35],a[112:115],v[196:199],v[32:35]\nM v[36:39],a[100:103],v[192:195],v[36:39]\nM v[36:39],a[116:119],v[196:199],v[36:39]\nM v[96:99],a[104:107],v[192:195],v[96:99]\nM v[96:99],a[120:123],v[196:199],v[96:99]\n" \
"M v[100:103],a[108:111],v[192:195],v[100:103]\nds_write_b64 v250,v[156:157] offset:7168\nM v[100:103],a[124:127],v[196:199],v[100:103]\ns_waitcnt lgkmcnt(7)\nM v[40:43],a[96:99],v[200:203],v[40:43]\nM v[40:43],a[112:115],v[204:207],v[40:43]\nM v[44:47],a[100:103],v[200:203],v[44:47]\nM v[44:47],a[116:119],v[204:207],v[44:47]\n" \
"M v[104:107],a[104:107],v[200:203],v[104:107]\nM v[104:107],a[120:123],v[204:207],v[104:107]\nM v[108:111],a[108:111],v[200:203],v[108:111]\nM v[108:111],a[124:127],v[204:207],v[108:111]\nM v[48:51],a[96:99],v[208:211],v[48:51]\ns_waitcnt lgkmcnt(3)\nM v[48:51],a[112:115],v[212:215],v[48:51]\nM v[52:55],a[100:103],v[208:211],v[52:55]\n" \
"buffer_store_dwordx4 v[164:167],v240,s[52:55],s74 offen offset:1024 sc1\nM v[52:55],a[116:119],v[212:215],v[52:55]\ns_add_u32 m0,s64,147456\ns_add_u32 s69,s68,3670016\nM v[112:115],a[104:107],v[208:211],v[112:115]\ns_waitcnt vmcnt(15) lgkmcnt(1)\ns_barrier\nM v[112:115],a[120:123],v[212:215],v[112:115]\n" \
"ds_read_b64_tr_b16 v[192:193],v241 offset:0\nds_read_b64_tr_b16 v[194:195],v241 offset:256\nM v[116:119],a[108:111],v[208:211],v[116:119]\nds_read_b64_tr_b16 v[196:197],v241 offset:8192\nds_read_b64_tr_b16 v[198:199],v241 offset:8448\nM v[116:119],a[124:127],v[212:215],v[116:119]\nds_read_b64_tr_b16 v[200:201],v242 offset:0\nds_read_b64_tr_b16 v[202:203],v242 offset:256\n" \
"M v[56:59],a[96:99],v[216:219],v[56:59]\nds_read_b64_tr_b16 v[204:205],v242 offset:8192\nds_read_b64_tr_b16 v[206:207],v242 offset:8448\nM v[56:59],a[112:115],v[220:223],v[56:59]\nds_read_b64_tr_b16 v[208:209],v241 offset:512\nds_read_b64_tr_b16 v[210:211],v241 offset:768\nM v[60:63],a[100:103],v[216:219],v[60:63]\nds_read_b64_tr_b16 v[212:213],v241 offset:8704\n" \
"ds_read_b64_tr_b16 v[214:215],v241 offset:8960\nM v[60:63],a[116:119],v[220:223],v[60:63]\nbuffer_load_dwordx4 v243,s[40:43],s69 offen offset:0 lds\nM v[120:123],a[104:107],v[216:219],v[120:123]\nds_write_b64 v250,v[158:159] offset:7176\ns_lshl_b32 s68,s60,19\nM v[120:123],a[120:123],v[220:223],v[120:123]\ns_lshl2_add_u32 s68,s64,s68\n" \
"s_add_u32 s68,s68,s70\nM v[124:127],a[108:111],v[216:219],v[124:127]\nds_read_b64_tr_b16 v[216:217],v242 offset:512\nds_read_b64_tr_b16 v[218:219],v242 offset:768\nM v[124:127],a[124:127],v[220:223],v[124:127]\nds_read_b64_tr_b16 v[220:221],v242 offset:8704\ns_waitcnt lgkmcnt(14)\nM v[0:3],a[128:131],v[192:195],v[0:3]\n" \
"ds_read_b64_tr_b16 v[222:223],v242 offset:8960\ns_waitcnt lgkmcnt(13)\nM v[0:3],a[144:147],v[196:199],v[0:3]\nbuffer_load_dwordx4 v244,s[40:43],s69 offen offset:1024 lds\nM v[4:7],a[132:135],v[192:195],v[4:7]\nds_write_b64 v250,v[160:161] offset:8192\nM v[4:7],a[148:151],v[196:199],v[4:7]\nM v[64:67],a[136:139],v[192:195],v[64:67]\n" \
"M v[64:67],a[152:155],v[196:199],v[64:67]\nbuffer_store_dwordx4 v[168:171],v240,s[52:55],s74 offen offset:2048 sc1\nM v[68:71],a[140:143],v[192:195],v[68:71]\nds_read_b64_tr_b16 v[192:193],v241 offset:1024\nds_read_b64_tr_b16 v[194:195],v241 offset:1280\nM v[68:71],a[156:159],v[196:199],v[68:71]\nds_read_b64_tr_b16 v[196:197],v241 offset:9216\ns_waitcnt lgkmcnt(9)\n" \
"M v[8:11],a[128:131],v[200:203],v[8:11]\nds_read_b64_tr_b16 v[198:199],v241 offset:9472\nM v[8:11],a[144:147],v[204:207],v[8:11]\nbuffer_load_dwordx4 v245,s[40:43],s69 offen offset:2048 lds\nM v[12:15],a[132:135],v[200:203],v[12:15]\nds_write_b64 v250,v[162:163] offset:8200\nM v[12:15],a[148:151],v[204:207],v[12:15]\nM v[72:75],a[136:139],v[200:203],v[72:75]\n" \
"M v[72:75],a[152:155],v[204:207],v[72:75]\nbuffer_load_dwordx4 v246,s[40:43],s69 offen offset:3072 lds\nM v[76:79],a[140:143],v[200:203],v[76:79]\nds_read_b64_tr_b16 v[200:201],v242 offset:1024\nds_read_b64_tr_b16 v[202:203],v242 offset:1280\nM v[76:79],a[156:159],v[204:207],v[76:79]\nds_read_b64_tr_b16 v[204:205],v242 offset:9216\nds_read_b64_tr_b16 v[206:207],v242 offset:9472\n" \
"M v[16:19],a[128:131],v[208:211],v[16:19]\nM v[16:19],a[144:147],v[212:215],v[16:19]\nM v[20:23],a[132:135],v[208:211],v[20:23]\nM v[20:23],a[148:151],v[212:215],v[20:23]\nM v[80:83],a[136:139],v[208:211],v[80:83]\nM v[80:83],a[152:155],v[212:215],v[80:83]\nM v[84:87],a[140:143],v[208:211],v[84:87]\nbuffer_load_dword v248,v228,s[52:55],s80 offen sc1\n" \
"M v[84:87],a[156:159],v[212:215],v[84:87]\nds_read_b64_tr_b16 v[208:209],v241 offset:1536\ns_waitcnt lgkmcnt(6)\nM v[24:27],a[128:131],v[216:219],v[24:27]\nbuffer_store_dwordx4 v[172:175],v240,s[52:55],s74 offen offset:3072 sc1\nM v[24:27],a[144:147],v[220:223],v[24:27]\nds_read_b64_tr_b16 v[210:211],v241 offset:1792\nds_read_b64_tr_b16 v[212:213],v241 offset:9728\n" \
"M v[28:31],a[132:135],v[216:219],v[28:31]\nds_read_b64_tr_b16 v[214:215],v241 offset:9984\ns_add_u32 s74,s74,4096\nM v[28:31],a[148:151],v[220:223],v[28:31]\nds_write_b64 v250,v[164:165] offset:9216\nM v[88:91],a[136:139],v[216:219],v[88:91]\nM v[88:91],a[152:155],v[220:223],v[88:91]\nM v[92:95],a[140:143],v[216:219],v[92:95]\n" \
"ds_read_b64_tr_b16 v[216:217],v242 offset:1536\nds_read_b64_tr_b16 v[218:219],v242 offset:1792\nM v[92:95],a[156:159],v[220:223],v[92:95]\nds_read_b64_tr_b16 v[220:221],v242 offset:9728\nds_read_b64_tr_b16 v[222:223],v242 offset:9984\nM v[32:35],a[128:131],v[192:195],v[32:35]\nM v[32:35],a[144:147],v[196:199],v[32:35]\nM v[36:39],a[132:135],v[192:195],v[36:39]\n" \
"ds_write_b64 v250,v[166:167] offset:9224\nM v[36:39],a[148:151],v[196:199],v[36:39]\nM v[96:99],a[136:139],v[192:195],v[96:99]\nM v[96:99],a[152:155],v[196:199],v[96:99]\nM v[100:103],a[140:143],v[192:195],v[100:103]\nM v[100:103],a[156:159],v[196:199],v[100:103]\ns_waitcnt lgkmcnt(9)\nM v[40:43],a[128:131],v[200:203],v[40:43]\n" \
"buffer_store_dwordx4 v[176:179],v240,s[52:55],s74 offen offset:0 sc1\nM v[40:43],a[144:147],v[204:207],v[40:43]\nM v[44:47],a[132:135],v[200:203],v[44:47]\nM v[44:47],a[148:151],v[204:207],v[44:47]\nM v[104:107],a[136:139],v[200:203],v[104:107]\nM v[104:107],a[152:155],v[204:207],v[104:107]\nM v[108:111],a[140:143],v[200:203],v[108:111]\nds_write_b64 v250,v[168:169] offset:10240\n" \
"M v[108:111],a[156:159],v[204:207],v[108:111]\ns_waitcnt lgkmcnt(4)\nM v[48:51],a[128:131],v[208:211],v[48:51]\nM v[48:51],a[144:147],v[212:215],v[48:51]\nM v[52:55],a[132:135],v[208:211],v[52:55]\ns_sub_u32 s40,s12,0x1000\ns_subb_u32 s41,s13,0\nM v[52:55],a[148:151],v[212:215],v[52:55]\n" \
"s_add_u32 m0,s64,98304\ns_xor_b32 s69,s99,0\nM v[112:115],a[136:139],v[208:211],v[112:115]\ns_waitcnt vmcnt(18) lgkmcnt(1)\ns_barrier\nM v[112:115],a[152:155],v[212:215],v[112:115]\nds_read_b64_tr_b16 v[192:193],v241 offset:16384\nds_read_b64_tr_b16 v[194:195],v241 offset:16640\n" \
"M v[116:119],a[140:143],v[208:211],v[116:119]\nds_read_b64_tr_b16 v[196:197],v241 offset:24576\nds_read_b64_tr_b16 v[198:199],v241 offset:24832\nM v[116:119],a[156:159],v[212:215],v[116:119]\nds_read_b64_tr_b16 v[200:201],v242 offset:16384\nds_read_b64_tr_b16 v[202:203],v242 offset:16640\nM v[56:59],a[128:131],v[216:219],v[56:59]\nds_read_b64_tr_b16 v[204:205],v242 offset:24576\n" \
"ds_read_b64_tr_b16 v[206:207],v242 offset:24832\nM v[56:59],a[144:147],v[220:223],v[56:59]\nds_read_b64_tr_b16 v[208:209],v241 offset:16896\nds_read_b64_tr_b16 v[210:211],v241 offset:17152\nM v[60:63],a[132:135],v[216:219],v[60:63]\nbuffer_store_dwordx4 v[180:183],v240,s[52:55],s74 offen offset:1024 sc1\nM v[60:63],a[148:151],v[220:223],v[60:63]\nds_read_b64_tr_b16 v[212:213],v241 offset:25088\n" \
"ds_read_b64_tr_b16 v[214:215],v241 offset:25344\nM v[120:123],a[136:139],v[216:219],v[120:123]\nds_write_b64 v250,v[170:171] offset:10248\ns_lshl_b32 s12,s61,13\nM v[120:123],a[152:155],v[220:223],v[120:123]\nM v[124:127],a[140:143],v[216:219],v[124:127]\nds_read_b64_tr_b16 v[216:217],v242 offset:16896\nds_read_b64_tr_b16 v[218:219],v242 offset:17152\n" \
"M v[124:127],a[156:159],v[220:223],v[124:127]\nds_read_b64_tr_b16 v[220:221],v242 offset:25088\ns_waitcnt lgkmcnt(14)\nM v[0:3],a[160:163],v[192:195],v[0:3]\nds_read_b64_tr_b16 v[222:223],v242 offset:25344\ns_waitcnt lgkmcnt(13)\nM v[0:3],a[176:179],v[196:199],v[0:3]\nbuffer_load_dwordx4 v243,s[40:43],s69 offen offset:0 lds\n" \
"M v[4:7],a[164:167],v[192:195],v[4:7]\nM v[4:7],a[180:183],v[196:199],v[4:7]\nM v[64:67],a[168:171],v[192:195],v[64:67]\nM v[64:67],a[184:187],v[196:199],v[64:67]\nbuffer_load_dwordx4 v244,s[40:43],s69 offen offset:1024 lds\nM v[68:71],a[172:175],v[192:195],v[68:71]\nds_read_b64_tr_b16 v[192:193],v241 offset:17408\nds_read_b64_tr_b16 v[194:195],v241 offset:17664\n" \
"M v[68:71],a[188:191],v[196:199],v[68:71]\nds_read_b64_tr_b16 v[196:197],v241 offset:25600\ns_waitcnt lgkmcnt(9)\nM v[8:11],a[160:163],v[200:203],v[8:11]\nds_write_b64 v250,v[172:173] offset:11264\nds_read_b64_tr_b16 v[198:199],v241 offset:25856\nM v[8:11],a[176:179],v[204:207],v[8:11]\nbuffer_load_dwordx4 v245,s[40:43],s69 offen offset:2048 lds\n" \
"M v[12:15],a[164:167],v[200:203],v[12:15]\nM v[12:15],a[180:183],v[204:207],v[12:15]\nM v[72:75],a[168:171],v[200:203],v[72:75]\nM v[72:75],a[184:187],v[204:207],v[72:75]\nbuffer_store_dwordx4 v[184:187],v240,s[52:55],s74 offen offset:2048 sc1\nM v[76:79],a[172:175],v[200:203],v[76:79]\ns_waitcnt vmcnt(7)\nv_cmp_eq_u32 vcc,0,v248\n" \
"M v[76:79],a[188:191],v[204:207],v[76:79]\ns_cbranch_vccnz .Lps0\n.Lpb0:\nds_read_b64_tr_b16 v[200:201],v242 offset:17408\nM v[16:19],a[160:163],v[208:211],v[16:19]\nds_read_b64_tr_b16 v[202:203],v242 offset:17664\ns_waitcnt lgkmcnt(7)\nM v[16:19],a[176:179],v[212:215],v[16:19]\n" \
"ds_read_b64_tr_b16 v[204:205],v242 offset:25600\nds_read_b64_tr_b16 v[206:207],v242 offset:25856\nM v[20:23],a[164:167],v[208:211],v[20:23]\nbuffer_load_dwordx4 a[0:3],v240,s[52:55],s76 offen offset:0 sc1\nM v[20:23],a[180:183],v[212:215],v[20:23]\nds_write_b64 v250,v[174:175] offset:11272\nv_and_b32 v228,31,v249\nM v[80:83],a[168:171],v[208:211],v[80:83]\n" \
"s_lshl_b32 s80,s63,7\nv_lshlrev_b32 v228,2,v228\nM v[80:83],a[184:187],v[212:215],v[80:83]\ns_add_u32 s80,s80,81920\nv_mov_b32 v251,v228\nM v[84:87],a[172:175],v[208:211],v[84:87]\nds_read_b64_tr_b16 v[208:209],v241 offset:17920\nds_read_b64_tr_b16 v[210:211],v241 offset:18176\n" \
"M v[84:87],a[188:191],v[212:215],v[84:87]\nds_read_b64_tr_b16 v[212:213],v241 offset:26112\nds_read_b64_tr_b16 v[214:215],v241 offset:26368\nM v[24:27],a[160:163],v[216:219],v[24:27]\nbuffer_load_dwordx4 a[16:19],v240,s[52:55],s76 offen offset:2048 sc1\nM v[24:27],a[176:179],v[220:223],v[24:27]\nM v[28:31],a[164:167],v[216:219],v[28:31]\nM v[28:31],a[180:183],v[220:223],v[28:31]\n" \
"M v[88:91],a[168:171],v[216:219],v[88:91]\nbuffer_load_dwordx4 a[4:7],v240,s[52:55],s76 offen offset:1024 sc1\nM v[88:91],a[184:187],v[220:223],v[88:91]\nM v[92:95],a[172:175],v[216:219],v[92:95]\nds_read_b64_tr_b16 v[216:217],v242 offset:17920\nds_read_b64_tr_b16 v[218:219],v242 offset:18176\nM v[92:95],a[188:191],v[220:223],v[92:95]\nds_read_b64_tr_b16 v[220:221],v242 offset:26112\n" \
"s_waitcnt lgkmcnt(7)\nM v[32:35],a[160:163],v[192:195],v[32:35]\nbuffer_store_dwordx4 v[188:191],v240,s[52:55],s74 offen offset:3072 sc1\nM v[32:35],a[176:179],v[196:199],v[32:35]\nds_read_b64_tr_b16 v[222:223],v242 offset:26368\ns_add_u32 s74,s74,16764928\nM v[36:39],a[164:167],v[192:195],v[36:39]\nds_write_b64 v250,v[176:177] offset:12288\n" \
"M v[36:39],a[180:183],v[196:199],v[36:39]\nM v[96:99],a[168:171],v[192:195],v[96:99]\nbuffer_load_dwordx4 a[20:23],v240,s[52:55],s76 offen offset:3072 sc1\nM v[96:99],a[184:187],v[196:199],v[96:99]\ns_xor_b32 s76,s76,4096\nM v[100:103],a[172:175],v[192:195],v[100:103]\nM v[100:103],a[188:191],v[196:199],v[100:103]\nM v[40:43],a[160:163],v[200:203],v[40:43]\n" \
"buffer_load_dwordx4 a[8:11],v240,s[52:55],s77 offen offset:0 sc1\nM v[40:43],a[176:179],v[204:207],v[40:43]\nM v[44:47],a[164:167],v[200:203],v[44:47]\nds_write_b64 v250,v[178:179] offset:12296\nM v[44:47],a[180:183],v[204:207],v[44:47]\nM v[104:107],a[168:171],v[200:203],v[104:107]\nbuffer_load_dwordx4 a[24:27],v240,s[52:55],s77 offen offset:2048 sc1\nM v[104:107],a[184:187],v[204:207],v[104:107]\n" \
"M v[108:111],a[172:175],v[200:203],v[108:111]\nM v[108:111],a[188:191],v[204:207],v[108:111]\ns_waitcnt lgkmcnt(3)\nM v[48:51],a[160:163],v[208:211],v[48:51]\nbuffer_load_dwordx4 a[12:15],v240,s[52:55],s77 offen offset:1024 sc1\nM v[48:51],a[176:179],v[212:215],v[48:51]\nM v[52:55],a[164:167],v[208:211],v[52:55]\nM v[52:55],a[180:183],v[212:215],v[52:55]\n" \
"M v[112:115],a[168:171],v[208:211],v[112:115]\ns_waitcnt lgkmcnt(1)\ns_barrier\nM v[112:115],a[184:187],v[212:215],v[112:115]\nds_read_b64_tr_b16 v[192:193],v241 offset:32768\nds_read_b64_tr_b16 v[194:195],v241 offset:33024\nM v[116:119],a[172:175],v[208:211],v[116:119]\nds_read_b64_tr_b16 v[196:197],v241 offset:40960\n" \
"ds_read_b64_tr_b16 v[198:199],v241 offset:41216\nM v[116:119],a[188:191],v[212:215],v[116:119]\nds_read_b64_tr_b16 v[200:201],v242 offset:32768\nds_read_b64_tr_b16 v[202:203],v242 offset:33024\nM v[56:59],a[160:163],v[216:219],v[56:59]\nds_read_b64_tr_b16 v[204:205],v242 offset:40960\nds_read_b64_tr_b16 v[206:207],v242 offset:41216\nM v[56:59],a[176:179],v[220:223],v[56:59]\n" \
"ds_read_b64_tr_b16 v[208:209],v241 offset:33280\nds_read_b64_tr_b16 v[210:211],v241 offset:33536\nM v[60:63],a[164:167],v[216:219],v[60:63]\nds_read_b64_tr_b16 v[212:213],v241 offset:41472\nds_read_b64_tr_b16 v[214:215],v241 offset:41728\nM v[60:63],a[180:183],v[220:223],v[60:63]\nbuffer_load_dwordx4 a[28:31],v240,s[52:55],s77 offen offset:3072 sc1\nM v[120:123],a[168:171],v[216:219],v[120:123]\n" \
"ds_write_b64 v250,v[180:181] offset:13312\ns_xor_b32 s77,s77,4096\nM v[120:123],a[184:187],v[220:223],v[120:123]\nM v[124:127],a[172:175],v[216:219],v[124:127]\nds_read_b64_tr_b16 v[216:217],v242 offset:33280\nds_read_b64_tr_b16 v[218:219],v242 offset:33536\nM v[124:127],a[188:191],v[220:223],v[124:127]\nds_read_b64_tr_b16 v[220:221],v242 offset:41472\n" \
"s_waitcnt lgkmcnt(14)\nM v[0:3],a[192:195],v[192:195],v[0:3]\nds_read_b64_tr_b16 v[222:223],v242 offset:41728\ns_waitcnt lgkmcnt(13)\nM v[0:3],a[208:211],v[196:199],v[0:3]\nbuffer_load_dwordx4 v246,s[40:43],s69 offen offset:3072 lds\nM v[4:7],a[196:199],v[192:195],v[4:7]\ns_add_u32 m0,s64,114688\n" \
"s_xor_b32 s69,s99,524288\nM v[4:7],a[212:215],v[196:199],v[4:7]\nds_write_b64 v250,v[182:183] offset:13320\nM v[64:67],a[200:203],v[192:195],v[64:67]\nM v[64:67],a[216:219],v[196:199],v[64:67]\nbuffer_load_dwordx4 a[32:35],v240,s[52:55],s76 offen offset:0 sc1\nM v[68:71],a[204:207],v[192:195],v[68:71]\nds_read_b64_tr_b16 v[192:193],v241 offset:33792\n" \
"ds_read_b64_tr_b16 v[194:195],v241 offset:34048\nM v[68:71],a[220:223],v[196:199],v[68:71]\nds_read_b64_tr_b16 v[196:197],v241 offset:41984\ns_waitcnt lgkmcnt(9)\nM v[8:11],a[192:195],v[200:203],v[8:11]\nds_read_b64_tr_b16 v[198:199],v241 offset:42240\nM v[8:11],a[208:211],v[204:207],v[8:11]\nbuffer_load_dwordx4 a[48:51],v240,s[52:55],s76 offen offset:2048 sc1\n" \
"M v[12:15],a[196:199],v[200:203],v[12:15]\nM v[12:15],a[212:215],v[204:207],v[12:15]\nM v[72:75],a[200:203],v[200:203],v[72:75]\nM v[72:75],a[216:219],v[204:207],v[72:75]\nbuffer_load_dwordx4 a[36:39],v240,s[52:55],s76 offen offset:1024 sc1\nM v[76:79],a[204:207],v[200:203],v[76:79]\nds_read_b64_tr_b16 v[200:201],v242 offset:33792\nds_read_b64_tr_b16 v[202:203],v242 offset:34048\n" \
"M v[76:79],a[220:223],v[204:207],v[76:79]\nds_read_b64_tr_b16 v[204:205],v242 offset:41984\nds_read_b64_tr_b16 v[206:207],v242 offset:42240\nM v[16:19],a[192:195],v[208:211],v[16:19]\nds_write_b64 v250,v[184:185] offset:14336\nM v[16:19],a[208:211],v[212:215],v[16:19]\nbuffer_load_dwordx4 a[52:55],v240,s[52:55],s76 offen offset:3072 sc1\nM v[20:23],a[196:199],v[208:211],v[20:23]\n" \
"s_xor_b32 s76,s76,12288\nM v[20:23],a[212:215],v[212:215],v[20:23]\nM v[80:83],a[200:203],v[208:211],v[80:83]\nM v[80:83],a[216:219],v[212:215],v[80:83]\nbuffer_load_dwordx4 a[40:43],v240,s[52:55],s77 offen offset:0 sc1\nM v[84:87],a[204:207],v[208:211],v[84:87]\nds_read_b64_tr_b16 v[208:209],v241 offset:34304\nds_read_b64_tr_b16 v[210:211],v241 offset:34560\n" \
"M v[84:87],a[220:223],v[212:215],v[84:87]\nds_read_b64_tr_b16 v[212:213],v241 offset:42496\ns_waitcnt lgkmcnt(8)\nM v[24:27],a[192:195],v[216:219],v[24:27]\nds_read_b64_tr_b16 v[214:215],v241 offset:42752\nM v[24:27],a[208:211],v[220:223],v[24:27]\nbuffer_load_dwordx4 a[56:59],v240,s[52:55],s77 offen offset:2048 sc1\nM v[28:31],a[196:199],v[216:219],v[28:31]\n" \
"ds_write_b64 v250,v[186:187] offset:14344\nM v[28:31],a[212:215],v[220:223],v[28:31]\nM v[88:91],a[200:203],v[216:219],v[88:91]\nM v[88:91],a[216:219],v[220:223],v[88:91]\nbuffer_load_dwordx4 a[44:47],v240,s[52:55],s77 offen offset:1024 sc1\nM v[92:95],a[204:207],v[216:219],v[92:95]\nds_read_b64_tr_b16 v[216:217],v242 offset:34304\nds_read_b64_tr_b16 v[218:219],v242 offset:34560\n" \
"M v[92:95],a[220:223],v[220:223],v[92:95]\nds_read_b64_tr_b16 v[220:221],v242 offset:42496\nds_read_b64_tr_b16 v[222:223],v242 offset:42752\nM v[32:35],a[192:195],v[192:195],v[32:35]\nM v[32:35],a[208:211],v[196:199],v[32:35]\nbuffer_load_dwordx4 a[60:63],v240,s[52:55],s77 offen offset:3072 sc1\nM v[36:39],a[196:199],v[192:195],v[36:39]\ns_xor_b32 s77,s77,12288\n" \
"M v[36:39],a[212:215],v[196:199],v[36:39]\nM v[96:99],a[200:203],v[192:195],v[96:99]\nM v[96:99],a[216:219],v[196:199],v[96:99]\nbuffer_load_dwordx4 v243,s[40:43],s69 offen offset:0 lds\nM v[100:103],a[204:207],v[192:195],v[100:103]\nds_write_b64 v250,v[188:189] offset:15360\nM v[100:103],a[220:223],v[196:199],v[100:103]\ns_waitcnt lgkmcnt(6)\n" \
"M v[40:43],a[192:195],v[200:203],v[40:43]\nM v[40:43],a[208:211],v[204:207],v[40:43]\nbuffer_load_dwordx4 v244,s[40:43],s69 offen offset:1024 lds\nM v[44:47],a[196:199],v[200:203],v[44:47]\nM v[44:47],a[212:215],v[204:207],v[44:47]\nM v[104:107],a[200:203],v[200:203],v[104:107]\nM v[104:107],a[216:219],v[204:207],v[104:107]\nbuffer_load_dwordx4 v245,s[40:43],s69 offen offset:2048 lds\n" \
"M v[108:111],a[204:207],v[200:203],v[108:111]\nM v[108:111],a[220:223],v[204:207],v[108:111]\nM v[48:51],a[192:195],v[208:211],v[48:51]\nM v[48:51],a[208:211],v[212:215],v[48:51]\nbuffer_load_dwordx4 v246,s[40:43],s69 offen offset:3072 lds\nM v[52:55],a[196:199],v[208:211],v[52:55]\ns_add_u32 m0,s64,131072\ns_xor_b32 s69,s99,1048576\n" \
"M v[52:55],a[212:215],v[212:215],v[52:55]\nds_write_b64 v250,v[190:191] offset:15368\nv_add_u32 v250,65536,v250\nM v[112:115],a[200:203],v[208:211],v[112:115]\ns_waitcnt lgkmcnt(2)\ns_barrier\nM v[112:115],a[216:219],v[212:215],v[112:115]\nds_read_b64_tr_b16 v[192:193],v241 offset:49152\n" \
"ds_read_b64_tr_b16 v[194:195],v241 offset:49408\nM v[116:119],a[204:207],v[208:211],v[116:119]\nds_read_b64_tr_b16 v[196:197],v241 offset:57344\nds_read_b64_tr_b16 v[198:199],v241 offset:57600\nM v[116:119],a[220:223],v[212:215],v[116:119]\nds_read_b64_tr_b16 v[200:201],v242 offset:49152\nds_read_b64_tr_b16 v[202:203],v242 offset:49408\nM v[56:59],a[192:195],v[216:219],v[56:59]\n" \
"ds_read_b64_tr_b16 v[204:205],v242 offset:57344\nds_read_b64_tr_b16 v[206:207],v242 offset:57600\nM v[56:59],a[208:211],v[220:223],v[56:59]\nds_read_b64_tr_b16 v[208:209],v241 offset:49664\nds_read_b64_tr_b16 v[210:211],v241 offset:49920\nM v[60:63],a[196:199],v[216:219],v[60:63]\nds_read_b64_tr_b16 v[212:213],v241 offset:57856\nds_read_b64_tr_b16 v[214:215],v241 offset:58112\n" \
"M v[60:63],a[212:215],v[220:223],v[60:63]\nbuffer_load_dwordx4 a[64:67],v240,s[52:55],s76 offen offset:0 sc1\nM v[120:123],a[200:203],v[216:219],v[120:123]\nM v[120:123],a[216:219],v[220:223],v[120:123]\nM v[124:127],a[204:207],v[216:219],v[124:127]\nds_read_b64_tr_b16 v[216:217],v242 offset:49664\nds_read_b64_tr_b16 v[218:219],v242 offset:49920\nM v[124:127],a[220:223],v[220:223],v[124:127]\n" \
"ds_read_b64_tr_b16 v[220:221],v242 offset:57856\ns_waitcnt lgkmcnt(13)\nM v[0:3],a[224:227],v[192:195],v[0:3]\nds_read_b64_tr_b16 v[222:223],v242 offset:58112\ns_waitcnt lgkmcnt(12)\nM v[0:3],a[240:243],v[196:199],v[0:3]\nbuffer_load_dwordx4 a[68:71],v240,s[52:55],s76 offen offset:1024 sc1\nM v[4:7],a[228:231],v[192:195],v[4:7]\n" \
"M v[4:7],a[244:247],v[196:199],v[4:7]\nM v[64:67],a[232:235],v[192:195],v[64:67]\nM v[64:67],a[248:251],v[196:199],v[64:67]\nbuffer_load_dwordx4 a[80:83],v240,s[52:55],s76 offen offset:2048 sc1\nM v[68:71],a[236:239],v[192:195],v[68:71]\nds_read_b64_tr_b16 v[192:193],v241 offset:50176\nds_read_b64_tr_b16 v[194:195],v241 offset:50432\nM v[68:71],a[252:255],v[196:199],v[68:71]\n" \
"ds_read_b64_tr_b16 v[196:197],v241 offset:58368\ns_waitcnt lgkmcnt(7)\nM v[8:11],a[224:227],v[200:203],v[8:11]\nds_read_b64_tr_b16 v[198:199],v241 offset:58624\nv_cvt_pk_bf16_f32 v128,v0,v1\nM v[8:11],a[240:243],v[204:207],v[8:11]\nv_cvt_pk_bf16_f32 v129,v2,v3\nv_cvt_pk_bf16_f32 v130,v4,v5\n" \
"M v[12:15],a[228:231],v[200:203],v[12:15]\nv_cvt_pk_bf16_f32 v131,v6,v7\nv_cvt_pk_bf16_f32 v160,v64,v65\nM v[12:15],a[244:247],v[204:207],v[12:15]\nv_cvt_pk_bf16_f32 v161,v66,v67\nv_cvt_pk_bf16_f32 v162,v68,v69\nM v[72:75],a[232:235],v[200:203],v[72:75]\nv_cvt_pk_bf16_f32 v163,v70,v71\n" \
"v_cvt_pk_bf16_f32 v132,v8,v9\nM v[72:75],a[248:251],v[204:207],v[72:75]\nbuffer_load_dwordx4 a[84:87],v240,s[52:55],s76 offen offset:3072 sc1\nM v[76:79],a[236:239],v[200:203],v[76:79]\nds_read_b64_tr_b16 v[200:201],v242 offset:50176\nds_read_b64_tr_b16 v[202:203],v242 offset:50432\nM v[76:79],a[252:255],v[204:207],v[76:79]\nds_read_b64_tr_b16 v[204:205],v242 offset:58368\n" \
"ds_read_b64_tr_b16 v[206:207],v242 offset:58624\nM v[16:19],a[224:227],v[208:211],v[16:19]\nv_cvt_pk_bf16_f32 v133,v10,v11\nv_cvt_pk_bf16_f32 v134,v12,v13\nM v[16:19],a[240:243],v[212:215],v[16:19]\nv_cvt_pk_bf16_f32 v135,v14,v15\nv_cvt_pk_bf16_f32 v164,v72,v73\nM v[20:23],a[228:231],v[208:211],v[20:23]\n" \
"v_cvt_pk_bf16_f32 v165,v74,v75\nv_cvt_pk_bf16_f32 v166,v76,v77\nM v[20:23],a[244:247],v[212:215],v[20:23]\nv_cvt_pk_bf16_f32 v167,v78,v79\ns_xor_b32 s76,s76,4096\nM v[80:83],a[232:235],v[208:211],v[80:83]\nv_cvt_pk_bf16_f32 v136,v16,v17\nv_cvt_pk_bf16_f32 v137,v18,v19\n" \
"M v[80:83],a[248:251],v[212:215],v[80:83]\nbuffer_load_dwordx4 a[72:75],v240,s[52:55],s77 offen offset:0 sc1\nM v[84:87],a[236:239],v[208:211],v[84:87]\nds_read_b64_tr_b16 v[208:209],v241 offset:50688\nds_read_b64_tr_b16 v[210:211],v241 offset:50944\nM v[84:87],a[252:255],v[212:215],v[84:87]\nds_read_b64_tr_b16 v[212:213],v241 offset:58880\ns_waitcnt lgkmcnt(7)\n" \
"M v[24:27],a[224:227],v[216:219],v[24:27]\nds_read_b64_tr_b16 v[214:215],v241 offset:59136\nv_cvt_pk_bf16_f32 v138,v20,v21\nM v[24:27],a[240:243],v[220:223],v[24:27]\nv_cvt_pk_bf16_f32 v139,v22,v23\nv_cvt_pk_bf16_f32 v168,v80,v81\nM v[28:31],a[228:231],v[216:219],v[28:31]\nv_cvt_pk_bf16_f32 v169,v82,v83\n" \
"v_cvt_pk_bf16_f32 v170,v84,v85\nM v[28:31],a[244:247],v[220:223],v[28:31]\nbuffer_load_dwordx4 a[88:91],v240,s[52:55],s77 offen offset:2048 sc1\nM v[88:91],a[232:235],v[216:219],v[88:91]\nv_cvt_pk_bf16_f32 v171,v86,v87\nv_cvt_pk_bf16_f32 v140,v24,v25\nM v[88:91],a[248:251],v[220:223],v[88:91]\nv_cvt_pk_bf16_f32 v141,v26,v27\n" \
"M v[92:95],a[236:239],v[216:219],v[92:95]\nds_read_b64_tr_b16 v[216:217],v242 offset:50688\nds_read_b64_tr_b16 v[218:219],v242 offset:50944\nM v[92:95],a[252:255],v[220:223],v[92:95]\nds_read_b64_tr_b16 v[220:221],v242 offset:58880\nds_read_b64_tr_b16 v[222:223],v242 offset:59136\nM v[32:35],a[224:227],v[192:195],v[32:35]\nv_cvt_pk_bf16_f32 v142,v28,v29\n" \
"v_cvt_pk_bf16_f32 v143,v30,v31\nM v[32:35],a[240:243],v[196:199],v[32:35]\nv_cvt_pk_bf16_f32 v172,v88,v89\nv_cvt_pk_bf16_f32 v173,v90,v91\nM v[36:39],a[228:231],v[192:195],v[36:39]\nv_cvt_pk_bf16_f32 v174,v92,v93\nv_cvt_pk_bf16_f32 v175,v94,v95\nM v[36:39],a[244:247],v[196:199],v[36:39]\n" \
"buffer_load_dwordx4 a[76:79],v240,s[52:55],s77 offen offset:1024 sc1\nM v[96:99],a[232:235],v[192:195],v[96:99]\nv_cvt_pk_bf16_f32 v144,v32,v33\nv_cvt_pk_bf16_f32 v145,v34,v35\nM v[96:99],a[248:251],v[196:199],v[96:99]\nM v[100:103],a[236:239],v[192:195],v[100:103]\nM v[100:103],a[252:255],v[196:199],v[100:103]\ns_waitcnt vmcnt(20)\n" \
"buffer_store_dword v252,off,s[52:55],s29 sc1\ns_waitcnt lgkmcnt(4)\nM v[40:43],a[224:227],v[200:203],v[40:43]\nv_cvt_pk_bf16_f32 v146,v36,v37\nv_cvt_pk_bf16_f32 v147,v38,v39\nM v[40:43],a[240:243],v[204:207],v[40:43]\nv_cvt_pk_bf16_f32 v176,v96,v97\nv_cvt_pk_bf16_f32 v177,v98,v99\n" \
"M v[44:47],a[228:231],v[200:203],v[44:47]\nv_cvt_pk_bf16_f32 v178,v100,v101\nv_cvt_pk_bf16_f32 v179,v102,v103\nM v[44:47],a[244:247],v[204:207],v[44:47]\nbuffer_load_dwordx4 a[92:95],v240,s[52:55],s77 offen offset:3072 sc1\nM v[104:107],a[232:235],v[200:203],v[104:107]\nv_cvt_pk_bf16_f32 v148,v40,v41\nv_cvt_pk_bf16_f32 v149,v42,v43\n" \
"M v[104:107],a[248:251],v[204:207],v[104:107]\ns_xor_b32 s77,s77,4096\ns_add_u32 s29,s29,4096\nM v[108:111],a[236:239],v[200:203],v[108:111]\nv_cvt_pk_bf16_f32 v150,v44,v45\nv_cvt_pk_bf16_f32 v151,v46,v47\nM v[108:111],a[252:255],v[204:207],v[108:111]\nbuffer_load_dwordx4 v243,s[40:43],s69 offen offset:0 lds\n" \
"M v[48:51],a[224:227],v[208:211],v[48:51]\nv_cvt_pk_bf16_f32 v180,v104,v105\nv_cvt_pk_bf16_f32 v181,v106,v107\nM v[48:51],a[240:243],v[212:215],v[48:51]\nM v[52:55],a[228:231],v[208:211],v[52:55]\nM v[52:55],a[244:247],v[212:215],v[52:55]\nbuffer_load_dwordx4 v244,s[40:43],s69 offen offset:1024 lds\nM v[112:115],a[232:235],v[208:211],v[112:115]\n" \
"s_waitcnt vmcnt(15) lgkmcnt(0)\ns_barrier\nM v[112:115],a[248:251],v[212:215],v[112:115]\nds_read_b64_tr_b16 v[192:193],v241 offset:0\nds_read_b64_tr_b16 v[194:195],v241 offset:256\nM v[116:119],a[236:239],v[208:211],v[116:119]\nds_read_b64_tr_b16 v[196:197],v241 offset:8192\nds_read_b64_tr_b16 v[198:199],v241 offset:8448\n" \
"M v[116:119],a[252:255],v[212:215],v[116:119]\nds_read_b64_tr_b16 v[200:201],v242 offset:0\nds_read_b64_tr_b16 v[202:203],v242 offset:256\nM v[56:59],a[224:227],v[216:219],v[56:59]\nds_read_b64_tr_b16 v[204:205],v242 offset:8192\nds_read_b64_tr_b16 v[206:207],v242 offset:8448\nM v[56:59],a[240:243],v[220:223],v[56:59]\nds_read_b64_tr_b16 v[208:209],v241 offset:512\n" \
"ds_read_b64_tr_b16 v[210:211],v241 offset:768\nM v[60:63],a[228:231],v[216:219],v[60:63]\nds_read_b64_tr_b16 v[212:213],v241 offset:8704\nds_read_b64_tr_b16 v[214:215],v241 offset:8960\nM v[60:63],a[244:247],v[220:223],v[60:63]\nv_cvt_pk_bf16_f32 v182,v108,v109\nv_cvt_pk_bf16_f32 v183,v110,v111\nM v[120:123],a[232:235],v[216:219],v[120:123]\n" \
"v_cvt_pk_bf16_f32 v152,v48,v49\nv_cvt_pk_bf16_f32 v153,v50,v51\nM v[120:123],a[248:251],v[220:223],v[120:123]\nv_cvt_pk_bf16_f32 v154,v52,v53\nv_cvt_pk_bf16_f32 v155,v54,v55\nM v[124:127],a[236:239],v[216:219],v[124:127]\nds_read_b64_tr_b16 v[216:217],v242 offset:512\nds_read_b64_tr_b16 v[218:219],v242 offset:768\n" \
"M v[124:127],a[252:255],v[220:223],v[124:127]\nds_read_b64_tr_b16 v[220:221],v242 offset:8704\ns_waitcnt lgkmcnt(13)\nM v[0:3],v[192:195],a[0:3],0\nds_read_b64_tr_b16 v[222:223],v242 offset:8960\ns_waitcnt lgkmcnt(12)\nM v[0:3],v[196:199],a[16:19],v[0:3]\nv_cvt_pk_bf16_f32 v184,v112,v113\n" \
"v_cvt_pk_bf16_f32 v185,v114,v115\nM v[4:7],v[192:195],a[4:7],0\nv_cvt_pk_bf16_f32 v186,v116,v117\nv_cvt_pk_bf16_f32 v187,v118,v119\nM v[4:7],v[196:199],a[20:23],v[4:7]\nv_cvt_pk_bf16_f32 v156,v56,v57\nv_cvt_pk_bf16_f32 v157,v58,v59\nM v[64:67],v[192:195],a[8:11],0\n" \
"v_cvt_pk_bf16_f32 v158,v60,v61\nv_cvt_pk_bf16_f32 v159,v62,v63\nM v[64:67],v[196:199],a[24:27],v[64:67]\nv_cvt_pk_bf16_f32 v188,v120,v121\nv_cvt_pk_bf16_f32 v189,v122,v123\nM v[68:71],v[192:195],a[12:15],0\nds_read_b64_tr_b16 v[192:193],v241 offset:1024\nds_read_b64_tr_b16 v[194:195],v241 offset:1280\n" \
"M v[68:71],v[196:199],a[28:31],v[68:71]\nds_read_b64_tr_b16 v[196:197],v241 offset:9216\ns_waitcnt lgkmcnt(7)\nM v[8:11],v[200:203],a[0:3],0\nds_read_b64_tr_b16 v[198:199],v241 offset:9472\nv_cvt_pk_bf16_f32 v190,v124,v125\nM v[8:11],v[204:207],a[16:19],v[8:11]\nbuffer_load_dwordx4 v245,s[40:43],s69 offen offset:2048 lds\n" \
"M v[12:15],v[200:203],a[4:7],0\nv_cvt_pk_bf16_f32 v191,v126,v127\nM v[12:15],v[204:207],a[20:23],v[12:15]\nM v[72:75],v[200:203],a[8:11],0\nM v[72:75],v[204:207],a[24:27],v[72:75]\nbuffer_load_dwordx4 v246,s[40:43],s69 offen offset:3072 lds\nM v[76:79],v[200:203],a[12:15],0\nds_read_b64_tr_b16 v[200:201],v242 offset:1024\n" \
"ds_read_b64_tr_b16 v[202:203],v242 offset:1280\nM v[76:79],v[204:207],a[28:31],v[76:79]\nds_read_b64_tr_b16 v[204:205],v242 offset:9216\nds_read_b64_tr_b16 v[206:207],v242 offset:9472\nM v[16:19],v[208:211],a[0:3],0\ns_add_u32 m0,s64,147456\ns_xor_b32 s69,s99,1572864\nM v[16:19],v[212:215],a[16:19],v[16:19]\n" \
"buffer_load_dwordx4 a[96:99],v240,s[52:55],s76 offen offset:0 sc1\nM v[20:23],v[208:211],a[4:7],0\nM v[20:23],v[212:215],a[20:23],v[20:23]\nM v[80:83],v[208:211],a[8:11],0\nM v[80:83],v[212:215],a[24:27],v[80:83]\nbuffer_store_dwordx4 v[128:131],v240,s[52:55],s74 offen offset:0 sc1\nM v[84:87],v[208:211],a[12:15],0\nds_read_b64_tr_b16 v[208:209],v241 offset:1536\n" \
"ds_read_b64_tr_b16 v[210:211],v241 offset:1792\nM v[84:87],v[212:215],a[28:31],v[84:87]\nds_read_b64_tr_b16 v[212:213],v241 offset:9728\ns_waitcnt lgkmcnt(7)\nM v[24:27],v[216:219],a[0:3],0\nds_read_b64_tr_b16 v[214:215],v241 offset:9984\nM v[24:27],v[220:223],a[16:19],v[24:27]\nbuffer_load_dwordx4 a[112:115],v240,s[52:55],s76 offen offset:2048 sc1\n" \
"M v[28:31],v[216:219],a[4:7],0\nM v[28:31],v[220:223],a[20:23],v[28:31]\nM v[88:91],v[216:219],a[8:11],0\nM v[88:91],v[220:223],a[24:27],v[88:91]\nbuffer_load_dwordx4 a[100:103],v240,s[52:55],s76 offen offset:1024 sc1\nM v[92:95],v[216:219],a[12:15],0\nds_read_b64_tr_b16 v[216:217],v242 offset:1536\nds_read_b64_tr_b16 v[218:219],v242 offset:1792\n" \
"M v[92:95],v[220:223],a[28:31],v[92:95]\nds_read_b64_tr_b16 v[220:221],v242 offset:9728\nds_read_b64_tr_b16 v[222:223],v242 offset:9984\nM v[32:35],v[192:195],a[0:3],0\nM v[32:35],v[196:199],a[16:19],v[32:35]\nbuffer_load_dwordx4 a[116:119],v240,s[52:55],s76 offen offset:3072 sc1\nM v[36:39],v[192:195],a[4:7],0\ns_xor_b32 s76,s76,28672\n" \
"M v[36:39],v[196:199],a[20:23],v[36:39]\nM v[96:99],v[192:195],a[8:11],0\nM v[96:99],v[196:199],a[24:27],v[96:99]\nbuffer_load_dwordx4 a[104:107],v240,s[52:55],s77 offen offset:0 sc1\nM v[100:103],v[192:195],a[12:15],0\nM v[100:103],v[196:199],a[28:31],v[100:103]\ns_waitcnt lgkmcnt(4)\nM v[40:43],v[200:203],a[0:3],0\n" \
"M v[40:43],v[204:207],a[16:19],v[40:43]\nbuffer_load_dwordx4 a[120:123],v240,s[52:55],s77 offen offset:2048 sc1\nM v[44:47],v[200:203],a[4:7],0\nM v[44:47],v[204:207],a[20:23],v[44:47]\nM v[104:107],v[200:203],a[8:11],0\nM v[104:107],v[204:207],a[24:27],v[104:107]\nbuffer_load_dwordx4 a[108:111],v240,s[52:55],s77 offen offset:1024 sc1\nM v[108:111],v[200:203],a[12:15],0\n" \
"M v[108:111],v[204:207],a[28:31],v[108:111]\nM v[48:51],v[208:211],a[0:3],0\nM v[48:51],v[212:215],a[16:19],v[48:51]\nbuffer_load_dwordx4 a[124:127],v240,s[52:55],s77 offen offset:3072 sc1\nM v[52:55],v[208:211],a[4:7],0\ns_xor_b32 s77,s77,28672\nM v[52:55],v[212:215],a[20:23],v[52:55]\nM v[112:115],v[208:211],a[8:11],0\n" \
"s_waitcnt vmcnt(15) lgkmcnt(0)\ns_barrier\nM v[112:115],v[212:215],a[24:27],v[112:115]\nds_read_b64_tr_b16 v[192:193],v241 offset:16384\nds_read_b64_tr_b16 v[194:195],v241 offset:16640\nM v[116:119],v[208:211],a[12:15],0\nds_read_b64_tr_b16 v[196:197],v241 offset:24576\nds_read_b64_tr_b16 v[198:199],v241 offset:24832\n" \
"buffer_store_dwordx4 v[132:135],v240,s[52:55],s74 offen offset:1024 sc1\nM v[116:119],v[212:215],a[28:31],v[116:119]\nds_read_b64_tr_b16 v[200:201],v242 offset:16384\nds_read_b64_tr_b16 v[202:203],v242 offset:16640\nM v[56:59],v[216:219],a[0:3],0\nds_read_b64_tr_b16 v[204:205],v242 offset:24576\nds_read_b64_tr_b16 v[206:207],v242 offset:24832\nM v[56:59],v[220:223],a[16:19],v[56:59]\n" \
"ds_read_b64_tr_b16 v[208:209],v241 offset:16896\nds_read_b64_tr_b16 v[210:211],v241 offset:17152\nM v[60:63],v[216:219],a[4:7],0\nds_read_b64_tr_b16 v[212:213],v241 offset:25088\nds_read_b64_tr_b16 v[214:215],v241 offset:25344\nM v[60:63],v[220:223],a[20:23],v[60:63]\nbuffer_load_dwordx4 v243,s[40:43],s69 offen offset:0 lds\nM v[120:123],v[216:219],a[8:11],0\n" \
"M v[120:123],v[220:223],a[24:27],v[120:123]\nM v[124:127],v[216:219],a[12:15],0\nds_read_b64_tr_b16 v[216:217],v242 offset:16896\nds_read_b64_tr_b16 v[218:219],v242 offset:17152\nM v[124:127],v[220:223],a[28:31],v[124:127]\nds_read_b64_tr_b16 v[220:221],v242 offset:25088\ns_waitcnt lgkmcnt(13)\nM v[0:3],v[192:195],a[32:35],v[0:3]\n" \
"ds_read_b64_tr_b16 v[222:223],v242 offset:25344\ns_waitcnt lgkmcnt(12)\nM v[0:3],v[196:199],a[48:51],v[0:3]\nbuffer_load_dwordx4 v244,s[40:43],s69 offen offset:1024 lds\nM v[4:7],v[192:195],a[36:39],v[4:7]\nM v[4:7],v[196:199],a[52:55],v[4:7]\nM v[64:67],v[192:195],a[40:43],v[64:67]\nM v[64:67],v[196:199],a[56:59],v[64:67]\n" \
"buffer_load_dwordx4 v245,s[40:43],s69 offen offset:2048 lds\nM v[68:71],v[192:195],a[44:47],v[68:71]\nds_read_b64_tr_b16 v[192:193],v241 offset:17408\nds_read_b64_tr_b16 v[194:195],v241 offset:17664\nM v[68:71],v[196:199],a[60:63],v[68:71]\nds_read_b64_tr_b16 v[196:197],v241 offset:25600\ns_waitcnt lgkmcnt(7)\nM v[8:11],v[200:203],a[32:35],v[8:11]\n" \
"ds_read_b64_tr_b16 v[198:199],v241 offset:25856\nM v[8:11],v[204:207],a[48:51],v[8:11]\nbuffer_load_dwordx4 v246,s[40:43],s69 offen offset:3072 lds\nM v[12:15],v[200:203],a[36:39],v[12:15]\ns_add_u32 m0,s64,98304\ns_xor_b32 s69,s99,2097152\nM v[12:15],v[204:207],a[52:55],v[12:15]\nM v[72:75],v[200:203],a[40:43],v[72:75]\n" \
"M v[72:75],v[204:207],a[56:59],v[72:75]\nbuffer_load_dwordx4 a[128:131],v240,s[52:55],s76 offen offset:0 sc1\nM v[76:79],v[200:203],a[44:47],v[76:79]\nds_read_b64_tr_b16 v[200:201],v242 offset:17408\nds_read_b64_tr_b16 v[202:203],v242 offset:17664\nM v[76:79],v[204:207],a[60:63],v[76:79]\nds_read_b64_tr_b16 v[204:205],v242 offset:25600\nds_read_b64_tr_b16 v[206:207],v242 offset:25856\n" \
"M v[16:19],v[208:211],a[32:35],v[16:19]\nM v[16:19],v[212:215],a[48:51],v[16:19]\nbuffer_store_dwordx4 v[136:139],v240,s[52:55],s74 offen offset:2048 sc1\nM v[20:23],v[208:211],a[36:39],v[20:23]\nM v[20:23],v[212:215],a[52:55],v[20:23]\nM v[80:83],v[208:211],a[40:43],v[80:83]\nM v[80:83],v[212:215],a[56:59],v[80:83]\nbuffer_load_dwordx4 a[144:147],v240,s[52:55],s76 offen offset:2048 sc1\n" \
"M v[84:87],v[208:211],a[44:47],v[84:87]\nds_read_b64_tr_b16 v[208:209],v241 offset:17920\nds_read_b64_tr_b16 v[210:211],v241 offset:18176\nM v[84:87],v[212:215],a[60:63],v[84:87]\nds_read_b64_tr_b16 v[212:213],v241 offset:26112\ns_waitcnt lgkmcnt(7)\nM v[24:27],v[216:219],a[32:35],v[24:27]\nds_read_b64_tr_b16 v[214:215],v241 offset:26368\n" \
"M v[24:27],v[220:223],a[48:51],v[24:27]\nbuffer_load_dwordx4 a[132:135],v240,s[52:55],s76 offen offset:1024 sc1\nM v[28:31],v[216:219],a[36:39],v[28:31]\nM v[28:31],v[220:223],a[52:55],v[28:31]\nM v[88:91],v[216:219],a[40:43],v[88:91]\nM v[88:91],v[220:223],a[56:59],v[88:91]\nbuffer_load_dwordx4 a[148:151],v240,s[52:55],s76 offen offset:3072 sc1\nM v[92:95],v[216:219],a[44:47],v[92:95]\n" \
"ds_read_b64_tr_b16 v[216:217],v242 offset:17920\nds_read_b64_tr_b16 v[218:219],v242 offset:18176\nM v[92:95],v[220:223],a[60:63],v[92:95]\nds_read_b64_tr_b16 v[220:221],v242 offset:26112\nds_read_b64_tr_b16 v[222:223],v242 offset:26368\nM v[32:35],v[192:195],a[32:35],v[32:35]\ns_xor_b32 s76,s76,4096\nM v[32:35],v[196:199],a[48:51],v[32:35]\n" \
"buffer_load_dwordx4 a[136:139],v240,s[52:55],s77 offen offset:0 sc1\nM v[36:39],v[192:195],a[36:39],v[36:39]\nM v[36:39],v[196:199],a[52:55],v[36:39]\nM v[96:99],v[192:195],a[40:43],v[96:99]\nM v[96:99],v[196:199],a[56:59],v[96:99]\nbuffer_load_dwordx4 a[152:155],v240,s[52:55],s77 offen offset:2048 sc1\nM v[100:103],v[192:195],a[44:47],v[100:103]\nM v[100:103],v[196:199],a[60:63],v[100:103]\n" \
"s_waitcnt lgkmcnt(4)\nM v[40:43],v[200:203],a[32:35],v[40:43]\nM v[40:43],v[204:207],a[48:51],v[40:43]\nbuffer_load_dwordx4 a[140:143],v240,s[52:55],s77 offen offset:1024 sc1\nM v[44:47],v[200:203],a[36:39],v[44:47]\nM v[44:47],v[204:207],a[52:55],v[44:47]\nM v[104:107],v[200:203],a[40:43],v[104:107]\nM v[104:107],v[204:207],a[56:59],v[104:107]\n" \
"buffer_load_dwordx4 a[156:159],v240,s[52:55],s77 offen offset:3072 sc1\nM v[108:111],v[200:203],a[44:47],v[108:111]\ns_xor_b32 s77,s77,4096\nM v[108:111],v[204:207],a[60:63],v[108:111]\nM v[48:51],v[208:211],a[32:35],v[48:51]\nM v[48:51],v[212:215],a[48:51],v[48:51]\nbuffer_store_dwordx4 v[140:143],v240,s[52:55],s74 offen offset:3072 sc1\nM v[52:55],v[208:211],a[36:39],v[52:55]\n" \
"s_add_u32 s74,s74,4096\nM v[52:55],v[212:215],a[52:55],v[52:55]\nM v[112:115],v[208:211],a[40:43],v[112:115]\ns_waitcnt vmcnt(23) lgkmcnt(0)\ns_barrier\nM v[112:115],v[212:215],a[56:59],v[112:115]\nds_read_b64_tr_b16 v[192:193],v241 offset:32768\nds_read_b64_tr_b16 v[194:195],v241 offset:33024\n" \
"M v[116:119],v[208:211],a[44:47],v[116:119]\nds_read_b64_tr_b16 v[196:197],v241 offset:40960\nds_read_b64_tr_b16 v[198:199],v241 offset:41216\nM v[116:119],v[212:215],a[60:63],v[116:119]\nds_read_b64_tr_b16 v[200:201],v242 offset:32768\nds_read_b64_tr_b16 v[202:203],v242 offset:33024\nM v[56:59],v[216:219],a[32:35],v[56:59]\nds_read_b64_tr_b16 v[204:205],v242 offset:40960\n" \
"ds_read_b64_tr_b16 v[206:207],v242 offset:41216\nM v[56:59],v[220:223],a[48:51],v[56:59]\nds_read_b64_tr_b16 v[208:209],v241 offset:33280\nds_read_b64_tr_b16 v[210:211],v241 offset:33536\nM v[60:63],v[216:219],a[36:39],v[60:63]\nds_read_b64_tr_b16 v[212:213],v241 offset:41472\nds_read_b64_tr_b16 v[214:215],v241 offset:41728\nM v[60:63],v[220:223],a[52:55],v[60:63]\n" \
"buffer_load_dwordx4 v243,s[40:43],s69 offen offset:0 lds\nM v[120:123],v[216:219],a[40:43],v[120:123]\nM v[120:123],v[220:223],a[56:59],v[120:123]\nM v[124:127],v[216:219],a[44:47],v[124:127]\nds_read_b64_tr_b16 v[216:217],v242 offset:33280\nds_read_b64_tr_b16 v[218:219],v242 offset:33536\nM v[124:127],v[220:223],a[60:63],v[124:127]\nds_read_b64_tr_b16 v[220:221],v242 offset:41472\n" \
"s_waitcnt lgkmcnt(13)\nM v[0:3],v[192:195],a[64:67],v[0:3]\nds_read_b64_tr_b16 v[222:223],v242 offset:41728\ns_waitcnt lgkmcnt(12)\nM v[0:3],v[196:199],a[80:83],v[0:3]\nbuffer_load_dwordx4 v244,s[40:43],s69 offen offset:1024 lds\nM v[4:7],v[192:195],a[68:71],v[4:7]\nM v[4:7],v[196:199],a[84:87],v[4:7]\n" \
"M v[64:67],v[192:195],a[72:75],v[64:67]\nM v[64:67],v[196:199],a[88:91],v[64:67]\nbuffer_load_dwordx4 v245,s[40:43],s69 offen offset:2048 lds\nM v[68:71],v[192:195],a[76:79],v[68:71]\nds_read_b64_tr_b16 v[192:193],v241 offset:33792\nds_read_b64_tr_b16 v[194:195],v241 offset:34048\nM v[68:71],v[196:199],a[92:95],v[68:71]\nds_read_b64_tr_b16 v[196:197],v241 offset:41984\n" \
"s_waitcnt lgkmcnt(7)\nM v[8:11],v[200:203],a[64:67],v[8:11]\nds_read_b64_tr_b16 v[198:199],v241 offset:42240\nM v[8:11],v[204:207],a[80:83],v[8:11]\nbuffer_load_dwordx4 v246,s[40:43],s69 offen offset:3072 lds\nM v[12:15],v[200:203],a[68:71],v[12:15]\ns_add_u32 m0,s64,114688\ns_xor_b32 s69,s99,2621440\n" \
"M v[12:15],v[204:207],a[84:87],v[12:15]\nM v[72:75],v[200:203],a[72:75],v[72:75]\nM v[72:75],v[204:207],a[88:91],v[72:75]\nbuffer_store_dwordx4 v[144:147],v240,s[52:55],s74 offen offset:0 sc1\nM v[76:79],v[200:203],a[76:79],v[76:79]\nds_read_b64_tr_b16 v[200:201],v242 offset:33792\nds_read_b64_tr_b16 v[202:203],v242 offset:34048\nM v[76:79],v[204:207],a[92:95],v[76:79]\n" \
"ds_read_b64_tr_b16 v[204:205],v242 offset:41984\nds_read_b64_tr_b16 v[206:207],v242 offset:42240\nM v[16:19],v[208:211],a[64:67],v[16:19]\nM v[16:19],v[212:215],a[80:83],v[16:19]\nbuffer_load_dwordx4 a[160:163],v240,s[52:55],s76 offen offset:0 sc1\nM v[20:23],v[208:211],a[68:71],v[20:23]\nM v[20:23],v[212:215],a[84:87],v[20:23]\nM v[80:83],v[208:211],a[72:75],v[80:83]\n" \
"M v[80:83],v[212:215],a[88:91],v[80:83]\nbuffer_load_dwordx4 a[176:179],v240,s[52:55],s76 offen offset:2048 sc1\nM v[84:87],v[208:211],a[76:79],v[84:87]\nds_read_b64_tr_b16 v[208:209],v241 offset:34304\nds_read_b64_tr_b16 v[210:211],v241 offset:34560\nM v[84:87],v[212:215],a[92:95],v[84:87]\nds_read_b64_tr_b16 v[212:213],v241 offset:42496\ns_waitcnt lgkmcnt(7)\n" \
"M v[24:27],v[216:219],a[64:67],v[24:27]\nds_read_b64_tr_b16 v[214:215],v241 offset:42752\nM v[24:27],v[220:223],a[80:83],v[24:27]\nbuffer_load_dwordx4 a[164:167],v240,s[52:55],s76 offen offset:1024 sc1\nM v[28:31],v[216:219],a[68:71],v[28:31]\nM v[28:31],v[220:223],a[84:87],v[28:31]\nM v[88:91],v[216:219],a[72:75],v[88:91]\nM v[88:91],v[220:223],a[88:91],v[88:91]\n" \
"buffer_load_dwordx4 a[180:183],v240,s[52:55],s76 offen offset:3072 sc1\nM v[92:95],v[216:219],a[76:79],v[92:95]\nds_read_b64_tr_b16 v[216:217],v242 offset:34304\nds_read_b64_tr_b16 v[218:219],v242 offset:34560\nM v[92:95],v[220:223],a[92:95],v[92:95]\nds_read_b64_tr_b16 v[220:221],v242 offset:42496\nds_read_b64_tr_b16 v[222:223],v242 offset:42752\nM v[32:35],v[192:195],a[64:67],v[32:35]\n" \
"s_xor_b32 s76,s76,12288\nM v[32:35],v[196:199],a[80:83],v[32:35]\nbuffer_load_dwordx4 a[168:171],v240,s[52:55],s77 offen offset:0 sc1\nM v[36:39],v[192:195],a[68:71],v[36:39]\nM v[36:39],v[196:199],a[84:87],v[36:39]\nM v[96:99],v[192:195],a[72:75],v[96:99]\nM v[96:99],v[196:199],a[88:91],v[96:99]\nbuffer_load_dwordx4 a[184:187],v240,s[52:55],s77 offen offset:2048 sc1\n" \
"M v[100:103],v[192:195],a[76:79],v[100:103]\nM v[100:103],v[196:199],a[92:95],v[100:103]\ns_waitcnt lgkmcnt(4)\nM v[40:43],v[200:203],a[64:67],v[40:43]\nM v[40:43],v[204:207],a[80:83],v[40:43]\nbuffer_load_dwordx4 a[172:175],v240,s[52:55],s77 offen offset:1024 sc1\nM v[44:47],v[200:203],a[68:71],v[44:47]\nM v[44:47],v[204:207],a[84:87],v[44:47]\n" \
"M v[104:107],v[200:203],a[72:75],v[104:107]\nM v[104:107],v[204:207],a[88:91],v[104:107]\nbuffer_store_dwordx4 v[148:151],v240,s[52:55],s74 offen offset:1024 sc1\nM v[108:111],v[200:203],a[76:79],v[108:111]\nM v[108:111],v[204:207],a[92:95],v[108:111]\nM v[48:51],v[208:211],a[64:67],v[48:51]\nM v[48:51],v[212:215],a[80:83],v[48:51]\nbuffer_load_dwordx4 a[188:191],v240,s[52:55],s77 offen offset:3072 sc1\n" \
"M v[52:55],v[208:211],a[68:71],v[52:55]\ns_xor_b32 s77,s77,12288\nM v[52:55],v[212:215],a[84:87],v[52:55]\nM v[112:115],v[208:211],a[72:75],v[112:115]\ns_waitcnt vmcnt(23) lgkmcnt(0)\ns_barrier\nM v[112:115],v[212:215],a[88:91],v[112:115]\nds_read_b64_tr_b16 v[192:193],v241 offset:49152\n" \
"ds_read_b64_tr_b16 v[194:195],v241 offset:49408\nM v[116:119],v[208:211],a[76:79],v[116:119]\nds_read_b64_tr_b16 v[196:197],v241 offset:57344\nds_read_b64_tr_b16 v[198:199],v241 offset:57600\nM v[116:119],v[212:215],a[92:95],v[116:119]\nds_read_b64_tr_b16 v[200:201],v242 offset:49152\nds_read_b64_tr_b16 v[202:203],v242 offset:49408\nM v[56:59],v[216:219],a[64:67],v[56:59]\n" \
"ds_read_b64_tr_b16 v[204:205],v242 offset:57344\nds_read_b64_tr_b16 v[206:207],v242 offset:57600\nM v[56:59],v[220:223],a[80:83],v[56:59]\nds_read_b64_tr_b16 v[208:209],v241 offset:49664\nds_read_b64_tr_b16 v[210:211],v241 offset:49920\nM v[60:63],v[216:219],a[68:71],v[60:63]\nds_read_b64_tr_b16 v[212:213],v241 offset:57856\nds_read_b64_tr_b16 v[214:215],v241 offset:58112\n" \
"M v[60:63],v[220:223],a[84:87],v[60:63]\nbuffer_load_dwordx4 v243,s[40:43],s69 offen offset:0 lds\nM v[120:123],v[216:219],a[72:75],v[120:123]\nM v[120:123],v[220:223],a[88:91],v[120:123]\nM v[124:127],v[216:219],a[76:79],v[124:127]\nds_read_b64_tr_b16 v[216:217],v242 offset:49664\nds_read_b64_tr_b16 v[218:219],v242 offset:49920\nM v[124:127],v[220:223],a[92:95],v[124:127]\n" \
"ds_read_b64_tr_b16 v[220:221],v242 offset:57856\ns_waitcnt lgkmcnt(13)\nM v[0:3],v[192:195],a[96:99],v[0:3]\nds_read_b64_tr_b16 v[222:223],v242 offset:58112\ns_waitcnt lgkmcnt(12)\nM v[0:3],v[196:199],a[112:115],v[0:3]\nbuffer_load_dwordx4 v244,s[40:43],s69 offen offset:1024 lds\nM v[4:7],v[192:195],a[100:103],v[4:7]\n" \
"M v[4:7],v[196:199],a[116:119],v[4:7]\nM v[64:67],v[192:195],a[104:107],v[64:67]\nM v[64:67],v[196:199],a[120:123],v[64:67]\nbuffer_load_dwordx4 v245,s[40:43],s69 offen offset:2048 lds\nM v[68:71],v[192:195],a[108:111],v[68:71]\nds_read_b64_tr_b16 v[192:193],v241 offset:50176\nds_read_b64_tr_b16 v[194:195],v241 offset:50432\nM v[68:71],v[196:199],a[124:127],v[68:71]\n" \
"ds_read_b64_tr_b16 v[196:197],v241 offset:58368\ns_waitcnt lgkmcnt(7)\nM v[8:11],v[200:203],a[96:99],v[8:11]\nds_read_b64_tr_b16 v[198:199],v241 offset:58624\nM v[8:11],v[204:207],a[112:115],v[8:11]\nbuffer_store_dwordx4 v[152:155],v240,s[52:55],s74 offen offset:2048 sc1\nM v[12:15],v[200:203],a[100:103],v[12:15]\nM v[12:15],v[204:207],a[116:119],v[12:15]\n" \
"M v[72:75],v[200:203],a[104:107],v[72:75]\nM v[72:75],v[204:207],a[120:123],v[72:75]\nbuffer_load_dwordx4 v246,s[40:43],s69 offen offset:3072 lds\nM v[76:79],v[200:203],a[108:111],v[76:79]\nds_read_b64_tr_b16 v[200:201],v242 offset:50176\nds_read_b64_tr_b16 v[202:203],v242 offset:50432\nM v[76:79],v[204:207],a[124:127],v[76:79]\nds_read_b64_tr_b16 v[204:205],v242 offset:58368\n" \
"ds_read_b64_tr_b16 v[206:207],v242 offset:58624\nM v[16:19],v[208:211],a[96:99],v[16:19]\ns_add_u32 m0,s64,131072\ns_xor_b32 s69,s99,3145728\nM v[16:19],v[212:215],a[112:115],v[16:19]\nbuffer_load_dwordx4 a[192:195],v240,s[52:55],s76 offen offset:0 sc1\nM v[20:23],v[208:211],a[100:103],v[20:23]\nM v[20:23],v[212:215],a[116:119],v[20:23]\n" \
"M v[80:83],v[208:211],a[104:107],v[80:83]\nM v[80:83],v[212:215],a[120:123],v[80:83]\nbuffer_load_dwordx4 a[208:211],v240,s[52:55],s76 offen offset:2048 sc1\nM v[84:87],v[208:211],a[108:111],v[84:87]\nds_read_b64_tr_b16 v[208:209],v241 offset:50688\nds_read_b64_tr_b16 v[210:211],v241 offset:50944\nM v[84:87],v[212:215],a[124:127],v[84:87]\nds_read_b64_tr_b16 v[212:213],v241 offset:58880\n" \
"s_waitcnt lgkmcnt(7)\nM v[24:27],v[216:219],a[96:99],v[24:27]\nds_read_b64_tr_b16 v[214:215],v241 offset:59136\nM v[24:27],v[220:223],a[112:115],v[24:27]\nbuffer_load_dwordx4 a[196:199],v240,s[52:55],s76 offen offset:1024 sc1\nM v[28:31],v[216:219],a[100:103],v[28:31]\nM v[28:31],v[220:223],a[116:119],v[28:31]\nM v[88:91],v[216:219],a[104:107],v[88:91]\n" \
"M v[88:91],v[220:223],a[120:123],v[88:91]\nbuffer_load_dwordx4 a[212:215],v240,s[52:55],s76 offen offset:3072 sc1\nM v[92:95],v[216:219],a[108:111],v[92:95]\nds_read_b64_tr_b16 v[216:217],v242 offset:50688\nds_read_b64_tr_b16 v[218:219],v242 offset:50944\nM v[92:95],v[220:223],a[124:127],v[92:95]\nds_read_b64_tr_b16 v[220:221],v242 offset:58880\nds_read_b64_tr_b16 v[222:223],v242 offset:59136\n" \
"M v[32:35],v[192:195],a[96:99],v[32:35]\ns_xor_b32 s76,s76,4096\nM v[32:35],v[196:199],a[112:115],v[32:35]\nbuffer_load_dwordx4 a[200:203],v240,s[52:55],s77 offen offset:0 sc1\nM v[36:39],v[192:195],a[100:103],v[36:39]\nM v[36:39],v[196:199],a[116:119],v[36:39]\nM v[96:99],v[192:195],a[104:107],v[96:99]\nM v[96:99],v[196:199],a[120:123],v[96:99]\n" \
"buffer_load_dwordx4 a[216:219],v240,s[52:55],s77 offen offset:2048 sc1\nM v[100:103],v[192:195],a[108:111],v[100:103]\nM v[100:103],v[196:199],a[124:127],v[100:103]\ns_waitcnt lgkmcnt(4)\nM v[40:43],v[200:203],a[96:99],v[40:43]\nM v[40:43],v[204:207],a[112:115],v[40:43]\nbuffer_store_dwordx4 v[156:159],v240,s[52:55],s74 offen offset:3072 sc1\nM v[44:47],v[200:203],a[100:103],v[44:47]\n" \
"s_add_u32 s74,s74,4096\nM v[44:47],v[204:207],a[116:119],v[44:47]\nM v[104:107],v[200:203],a[104:107],v[104:107]\nM v[104:107],v[204:207],a[120:123],v[104:107]\nbuffer_load_dwordx4 a[204:207],v240,s[52:55],s77 offen offset:1024 sc1\nM v[108:111],v[200:203],a[108:111],v[108:111]\nM v[108:111],v[204:207],a[124:127],v[108:111]\nM v[48:51],v[208:211],a[96:99],v[48:51]\n" \
"M v[48:51],v[212:215],a[112:115],v[48:51]\nbuffer_load_dwordx4 a[220:223],v240,s[52:55],s77 offen offset:3072 sc1\nM v[52:55],v[208:211],a[100:103],v[52:55]\ns_xor_b32 s77,s77,4096\nM v[52:55],v[212:215],a[116:119],v[52:55]\nM v[112:115],v[208:211],a[104:107],v[112:115]\ns_waitcnt vmcnt(24) lgkmcnt(0)\ns_barrier\n" \
"M v[112:115],v[212:215],a[120:123],v[112:115]\nds_read_b64_tr_b16 v[192:193],v241 offset:0\nds_read_b64_tr_b16 v[194:195],v241 offset:256\nM v[116:119],v[208:211],a[108:111],v[116:119]\nds_read_b64_tr_b16 v[196:197],v241 offset:8192\nds_read_b64_tr_b16 v[198:199],v241 offset:8448\nM v[116:119],v[212:215],a[124:127],v[116:119]\nds_read_b64_tr_b16 v[200:201],v242 offset:0\n" \
"ds_read_b64_tr_b16 v[202:203],v242 offset:256\nM v[56:59],v[216:219],a[96:99],v[56:59]\nds_read_b64_tr_b16 v[204:205],v242 offset:8192\nds_read_b64_tr_b16 v[206:207],v242 offset:8448\nM v[56:59],v[220:223],a[112:115],v[56:59]\nds_read_b64_tr_b16 v[208:209],v241 offset:512\nds_read_b64_tr_b16 v[210:211],v241 offset:768\nM v[60:63],v[216:219],a[100:103],v[60:63]\n" \
"ds_read_b64_tr_b16 v[212:213],v241 offset:8704\nds_read_b64_tr_b16 v[214:215],v241 offset:8960\nM v[60:63],v[220:223],a[116:119],v[60:63]\nbuffer_load_dwordx4 v243,s[40:43],s69 offen offset:0 lds\nM v[120:123],v[216:219],a[104:107],v[120:123]\nM v[120:123],v[220:223],a[120:123],v[120:123]\nM v[124:127],v[216:219],a[108:111],v[124:127]\nds_read_b64_tr_b16 v[216:217],v242 offset:512\n" \
"ds_read_b64_tr_b16 v[218:219],v242 offset:768\nM v[124:127],v[220:223],a[124:127],v[124:127]\nds_read_b64_tr_b16 v[220:221],v242 offset:8704\ns_waitcnt lgkmcnt(13)\nM v[0:3],v[192:195],a[128:131],v[0:3]\nds_read_b64_tr_b16 v[222:223],v242 offset:8960\ns_waitcnt lgkmcnt(12)\nM v[0:3],v[196:199],a[144:147],v[0:3]\n" \
"buffer_load_dwordx4 v244,s[40:43],s69 offen offset:1024 lds\nM v[4:7],v[192:195],a[132:135],v[4:7]\nM v[4:7],v[196:199],a[148:151],v[4:7]\nM v[64:67],v[192:195],a[136:139],v[64:67]\nM v[64:67],v[196:199],a[152:155],v[64:67]\nbuffer_store_dwordx4 v[160:163],v240,s[52:55],s74 offen offset:0 sc1\nM v[68:71],v[192:195],a[140:143],v[68:71]\nds_read_b64_tr_b16 v[192:193],v241 offset:1024\n" \
"ds_read_b64_tr_b16 v[194:195],v241 offset:1280\nM v[68:71],v[196:199],a[156:159],v[68:71]\nds_read_b64_tr_b16 v[196:197],v241 offset:9216\ns_waitcnt lgkmcnt(7)\nM v[8:11],v[200:203],a[128:131],v[8:11]\nds_read_b64_tr_b16 v[198:199],v241 offset:9472\nM v[8:11],v[204:207],a[144:147],v[8:11]\nbuffer_load_dwordx4 v245,s[40:43],s69 offen offset:2048 lds\n" \
"M v[12:15],v[200:203],a[132:135],v[12:15]\nM v[12:15],v[204:207],a[148:151],v[12:15]\nM v[72:75],v[200:203],a[136:139],v[72:75]\nM v[72:75],v[204:207],a[152:155],v[72:75]\nbuffer_load_dwordx4 v246,s[40:43],s69 offen offset:3072 lds\nM v[76:79],v[200:203],a[140:143],v[76:79]\nds_read_b64_tr_b16 v[200:201],v242 offset:1024\nds_read_b64_tr_b16 v[202:203],v242 offset:1280\n" \
"M v[76:79],v[204:207],a[156:159],v[76:79]\nds_read_b64_tr_b16 v[204:205],v242 offset:9216\nds_read_b64_tr_b16 v[206:207],v242 offset:9472\nM v[16:19],v[208:211],a[128:131],v[16:19]\ns_add_u32 m0,s64,147456\ns_xor_b32 s69,s99,3670016\nM v[16:19],v[212:215],a[144:147],v[16:19]\nbuffer_load_dwordx4 a[224:227],v240,s[52:55],s76 offen offset:0 sc1\n" \
"M v[20:23],v[208:211],a[132:135],v[20:23]\ns_mov_b32 s99,0\nM v[20:23],v[212:215],a[148:151],v[20:23]\nM v[80:83],v[208:211],a[136:139],v[80:83]\nds_read_b128 a[0:3],v240 offset:0\nM v[80:83],v[212:215],a[152:155],v[80:83]\nbuffer_load_dword v248,v228,s[52:55],s80 offen sc1\nM v[84:87],v[208:211],a[140:143],v[84:87]\n" \
"ds_read_b64_tr_b16 v[208:209],v241 offset:1536\nds_read_b64_tr_b16 v[210:211],v241 offset:1792\nM v[84:87],v[212:215],a[156:159],v[84:87]\nds_read_b64_tr_b16 v[212:213],v241 offset:9728\ns_waitcnt lgkmcnt(8)\nM v[24:27],v[216:219],a[128:131],v[24:27]\nbuffer_load_dwordx4 a[240:243],v240,s[52:55],s76 offen offset:2048 sc1\nM v[24:27],v[220:223],a[144:147],v[24:27]\n" \
"ds_read_b64_tr_b16 v[214:215],v241 offset:9984\nM v[28:31],v[216:219],a[132:135],v[28:31]\nds_read_b128 a[4:7],v240 offset:1024\nM v[28:31],v[220:223],a[148:151],v[28:31]\nM v[88:91],v[216:219],a[136:139],v[88:91]\nbuffer_load_dwordx4 a[228:231],v240,s[52:55],s76 offen offset:1024 sc1\nM v[88:91],v[220:223],a[152:155],v[88:91]\nM v[92:95],v[216:219],a[140:143],v[92:95]\n" \
"ds_read_b64_tr_b16 v[216:217],v242 offset:1536\nds_read_b64_tr_b16 v[218:219],v242 offset:1792\nM v[92:95],v[220:223],a[156:159],v[92:95]\nds_read_b64_tr_b16 v[220:221],v242 offset:9728\nds_read_b64_tr_b16 v[222:223],v242 offset:9984\nM v[32:35],v[192:195],a[128:131],v[32:35]\nbuffer_load_dwordx4 a[244:247],v240,s[52:55],s76 offen offset:3072 sc1\nM v[32:35],v[196:199],a[144:147],v[32:35]\n" \
"ds_read_b128 a[8:11],v240 offset:2048\nM v[36:39],v[192:195],a[132:135],v[36:39]\nM v[36:39],v[196:199],a[148:151],v[36:39]\nM v[96:99],v[192:195],a[136:139],v[96:99]\nbuffer_store_dwordx4 v[164:167],v240,s[52:55],s74 offen offset:1024 sc1\nM v[96:99],v[196:199],a[152:155],v[96:99]\nM v[100:103],v[192:195],a[140:143],v[100:103]\nds_read_b128 a[12:15],v240 offset:3072\n" \
"M v[100:103],v[196:199],a[156:159],v[100:103]\ns_waitcnt lgkmcnt(7)\nM v[40:43],v[200:203],a[128:131],v[40:43]\nbuffer_load_dwordx4 a[232:235],v240,s[52:55],s77 offen offset:0 sc1\nM v[40:43],v[204:207],a[144:147],v[40:43]\nM v[44:47],v[200:203],a[132:135],v[44:47]\nM v[44:47],v[204:207],a[148:151],v[44:47]\nM v[104:107],v[200:203],a[136:139],v[104:107]\n" \
"buffer_load_dwordx4 a[248:251],v240,s[52:55],s77 offen offset:2048 sc1\nM v[104:107],v[204:207],a[152:155],v[104:107]\nds_read_b128 a[16:19],v240 offset:4096\nM v[108:111],v[200:203],a[140:143],v[108:111]\nM v[108:111],v[204:207],a[156:159],v[108:111]\nM v[48:51],v[208:211],a[128:131],v[48:51]\nbuffer_load_dwordx4 a[236:239],v240,s[52:55],s77 offen offset:1024 sc1\nM v[48:51],v[212:215],a[144:147],v[48:51]\n" \
"M v[52:55],v[208:211],a[132:135],v[52:55]\nds_read_b128 a[20:23],v240 offset:5120\nM v[52:55],v[212:215],a[148:151],v[52:55]\nM v[112:115],v[208:211],a[136:139],v[112:115]\ns_waitcnt vmcnt(17) lgkmcnt(3)\ns_barrier\nM v[112:115],v[212:215],a[152:155],v[112:115]\nds_read_b64_tr_b16 v[192:193],v241 offset:16384\n" \
"ds_read_b64_tr_b16 v[194:195],v241 offset:16640\nM v[116:119],v[208:211],a[140:143],v[116:119]\nds_read_b64_tr_b16 v[196:197],v241 offset:24576\nds_read_b64_tr_b16 v[198:199],v241 offset:24832\nM v[116:119],v[212:215],a[156:159],v[116:119]\nds_read_b64_tr_b16 v[200:201],v242 offset:16384\nds_read_b64_tr_b16 v[202:203],v242 offset:16640\nM v[56:59],v[216:219],a[128:131],v[56:59]\n" \
"ds_read_b64_tr_b16 v[204:205],v242 offset:24576\nds_read_b64_tr_b16 v[206:207],v242 offset:24832\nM v[56:59],v[220:223],a[144:147],v[56:59]\nds_read_b64_tr_b16 v[208:209],v241 offset:16896\nds_read_b64_tr_b16 v[210:211],v241 offset:17152\nM v[60:63],v[216:219],a[132:135],v[60:63]\nds_read_b64_tr_b16 v[212:213],v241 offset:25088\nds_read_b64_tr_b16 v[214:215],v241 offset:25344\n" \
"M v[60:63],v[220:223],a[148:151],v[60:63]\nbuffer_load_dwordx4 a[252:255],v240,s[52:55],s77 offen offset:3072 sc1\nM v[120:123],v[216:219],a[136:139],v[120:123]\nds_read_b128 a[24:27],v240 offset:6144\nM v[120:123],v[220:223],a[152:155],v[120:123]\nM v[124:127],v[216:219],a[140:143],v[124:127]\nds_read_b64_tr_b16 v[216:217],v242 offset:16896\nds_read_b64_tr_b16 v[218:219],v242 offset:17152\n" \
"M v[124:127],v[220:223],a[156:159],v[124:127]\nds_read_b64_tr_b16 v[220:221],v242 offset:25088\ns_waitcnt lgkmcnt(14)\nM v[0:3],v[192:195],a[160:163],v[0:3]\nds_read_b64_tr_b16 v[222:223],v242 offset:25344\ns_waitcnt lgkmcnt(13)\nM v[0:3],v[196:199],a[176:179],v[0:3]\nbuffer_store_dwordx4 v[168:171],v240,s[52:55],s74 offen offset:2048 sc1\n" \
"M v[4:7],v[192:195],a[164:167],v[4:7]\nds_read_b128 a[28:31],v240 offset:7168\nM v[4:7],v[196:199],a[180:183],v[4:7]\nM v[64:67],v[192:195],a[168:171],v[64:67]\nds_read_b128 a[32:35],v240 offset:8192\nM v[64:67],v[196:199],a[184:187],v[64:67]\nbuffer_load_dwordx4 v243,s[40:43],s69 offen offset:0 lds\nM v[68:71],v[192:195],a[172:175],v[68:71]\n" \
"ds_read_b64_tr_b16 v[192:193],v241 offset:17408\nds_read_b64_tr_b16 v[194:195],v241 offset:17664\nM v[68:71],v[196:199],a[188:191],v[68:71]\nds_read_b64_tr_b16 v[196:197],v241 offset:25600\ns_waitcnt lgkmcnt(10)\nM v[8:11],v[200:203],a[160:163],v[8:11]\nds_read_b64_tr_b16 v[198:199],v241 offset:25856\nM v[8:11],v[204:207],a[176:179],v[8:11]\n" \
"buffer_load_dwordx4 v244,s[40:43],s69 offen offset:1024 lds\nM v[12:15],v[200:203],a[164:167],v[12:15]\nds_read_b128 a[36:39],v240 offset:9216\nM v[12:15],v[204:207],a[180:183],v[12:15]\nM v[72:75],v[200:203],a[168:171],v[72:75]\nM v[72:75],v[204:207],a[184:187],v[72:75]\nbuffer_load_dwordx4 v245,s[40:43],s69 offen offset:2048 lds\nM v[76:79],v[200:203],a[172:175],v[76:79]\n" \
"ds_read_b64_tr_b16 v[200:201],v242 offset:17408\nds_read_b64_tr_b16 v[202:203],v242 offset:17664\nM v[76:79],v[204:207],a[188:191],v[76:79]\nds_read_b64_tr_b16 v[204:205],v242 offset:25600\nds_read_b64_tr_b16 v[206:207],v242 offset:25856\nM v[16:19],v[208:211],a[160:163],v[16:19]\nds_read_b128 a[40:43],v240 offset:10240\nM v[16:19],v[212:215],a[176:179],v[16:19]\n" \
"buffer_load_dwordx4 v246,s[40:43],s69 offen offset:3072 lds\nM v[20:23],v[208:211],a[164:167],v[20:23]\ns_sub_u32 s40,s18,0x1000\ns_subb_u32 s41,s19,0\nM v[20:23],v[212:215],a[180:183],v[20:23]\ns_waitcnt vmcnt(13)\nv_cmp_eq_u32 vcc,0,v248\nM v[80:83],v[208:211],a[168:171],v[80:83]\n" \
"s_cbranch_vccnz .Lps1\n.Lpb1:\ns_add_u32 s80,s80,4096\nM v[80:83],v[212:215],a[184:187],v[80:83]\ns_cmp_ge_u32 s62,6\ns_cselect_b64 vcc,-1,0\nM v[84:87],v[208:211],a[172:175],v[84:87]\nds_read_b64_tr_b16 v[208:209],v241 offset:17920\n" \
"ds_read_b64_tr_b16 v[210:211],v241 offset:18176\nM v[84:87],v[212:215],a[188:191],v[84:87]\nds_read_b64_tr_b16 v[212:213],v241 offset:26112\ns_waitcnt lgkmcnt(8)\nM v[24:27],v[216:219],a[160:163],v[24:27]\nds_read_b64_tr_b16 v[214:215],v241 offset:26368\nv_cndmask_b32 v254,v231,v229,vcc\nM v[24:27],v[220:223],a[176:179],v[24:27]\n" \
"s_cmp_eq_u32 s62,3\ns_cselect_b64 vcc,-1,0\nM v[28:31],v[216:219],a[164:167],v[28:31]\nv_cndmask_b32 v254,v254,v230,vcc\nv_bfe_u32 v230,v249,2,3\nM v[28:31],v[220:223],a[180:183],v[28:31]\nv_and_b32 v231,0x70,v243\nv_lshl_or_b32 v230,v230,10,v231\n" \
"M v[88:91],v[216:219],a[168:171],v[88:91]\nv_and_b32 v231,16,v249\nv_lshlrev_b32 v231,1,v231\nM v[88:91],v[220:223],a[184:187],v[88:91]\nv_xor_b32 v230,v230,v231\nv_add_u32 v243,4096,v230\nM v[92:95],v[216:219],a[172:175],v[92:95]\nbuffer_store_dwordx4 v[172:175],v240,s[52:55],s74 offen offset:3072 sc1\n" \
"M v[92:95],v[220:223],a[188:191],v[92:95]\nds_read_b64_tr_b16 v[216:217],v242 offset:17920\nds_read_b64_tr_b16 v[218:219],v242 offset:18176\nM v[32:35],v[192:195],a[160:163],v[32:35]\nds_read_b64_tr_b16 v[220:221],v242 offset:26112\nds_read_b64_tr_b16 v[222:223],v242 offset:26368\nM v[32:35],v[196:199],a[176:179],v[32:35]\ns_add_u32 s74,s74,4096\n" \
"v_add_u32 v244,3200,v230\nM v[36:39],v[192:195],a[164:167],v[36:39]\nv_add_u32 v245,10240,v230\nv_add_u32 v246,9344,v230\nM v[36:39],v[196:199],a[180:183],v[36:39]\nv_and_b32 v229,3,v249\nv_cmp_eq_u32 vcc,0,v229\nM v[96:99],v[192:195],a[168:171],v[96:99]\n" \
"v_mov_b32 v230,0x3f803f80\nv_cndmask_b32 v229,0,v230,vcc\nM v[96:99],v[196:199],a[184:187],v[96:99]\nv_and_b32 v230,0xff,v241\nv_and_b32 v231,16,v249\nM v[100:103],v[192:195],a[172:175],v[100:103]\nv_lshl_or_b32 v230,v231,4,v230\nv_and_b32 v231,32,v249\n" \
"M v[100:103],v[196:199],a[188:191],v[100:103]\nds_read_b128 a[44:47],v240 offset:11264\ns_waitcnt lgkmcnt(5)\nM v[40:43],v[200:203],a[160:163],v[40:43]\nds_read_b128 a[48:51],v240 offset:12288\nds_read_b128 a[52:55],v240 offset:13312\nM v[40:43],v[204:207],a[176:179],v[40:43]\nds_read_b128 a[56:59],v240 offset:14336\n" \
"M v[44:47],v[200:203],a[164:167],v[44:47]\nM v[44:47],v[204:207],a[180:183],v[44:47]\nM v[104:107],v[200:203],a[168:171],v[104:107]\nM v[104:107],v[204:207],a[184:187],v[104:107]\nM v[108:111],v[200:203],a[172:175],v[108:111]\nds_read_b128 a[60:63],v240 offset:15360\nM v[108:111],v[204:207],a[188:191],v[108:111]\nM v[48:51],v[208:211],a[160:163],v[48:51]\n" \
"M v[48:51],v[212:215],a[176:179],v[48:51]\nM v[52:55],v[208:211],a[164:167],v[52:55]\nM v[52:55],v[212:215],a[180:183],v[52:55]\nM v[112:115],v[208:211],a[168:171],v[112:115]\ns_waitcnt lgkmcnt(5)\ns_barrier\nM v[112:115],v[212:215],a[184:187],v[112:115]\nds_read_b64_tr_b16 v[192:193],v241 offset:32768\n" \
"ds_read_b64_tr_b16 v[194:195],v241 offset:33024\nM v[116:119],v[208:211],a[172:175],v[116:119]\nds_read_b64_tr_b16 v[196:197],v241 offset:40960\nds_read_b64_tr_b16 v[198:199],v241 offset:41216\nM v[116:119],v[212:215],a[188:191],v[116:119]\nds_read_b64_tr_b16 v[200:201],v242 offset:32768\nds_read_b64_tr_b16 v[202:203],v242 offset:33024\nM v[56:59],v[216:219],a[160:163],v[56:59]\n" \
"ds_read_b64_tr_b16 v[204:205],v242 offset:40960\nds_read_b64_tr_b16 v[206:207],v242 offset:41216\nM v[56:59],v[220:223],a[176:179],v[56:59]\nds_read_b64_tr_b16 v[208:209],v241 offset:33280\nds_read_b64_tr_b16 v[210:211],v241 offset:33536\nM v[60:63],v[216:219],a[164:167],v[60:63]\nds_read_b64_tr_b16 v[212:213],v241 offset:41472\nds_read_b64_tr_b16 v[214:215],v241 offset:41728\n" \
"M v[60:63],v[220:223],a[180:183],v[60:63]\nds_write_b64 v250,v[128:129] offset:0\nM v[120:123],v[216:219],a[168:171],v[120:123]\nbuffer_store_dwordx4 v[176:179],v240,s[52:55],s74 offen offset:0 sc1\nM v[120:123],v[220:223],a[184:187],v[120:123]\nds_write_b64 v250,v[130:131] offset:8\nM v[124:127],v[216:219],a[172:175],v[124:127]\nds_read_b64_tr_b16 v[216:217],v242 offset:33280\n" \
"ds_read_b64_tr_b16 v[218:219],v242 offset:33536\nM v[124:127],v[220:223],a[188:191],v[124:127]\nds_read_b64_tr_b16 v[220:221],v242 offset:41472\ns_waitcnt lgkmcnt(15)\nM v[0:3],v[192:195],a[192:195],v[0:3]\nds_read_b64_tr_b16 v[222:223],v242 offset:41728\ns_waitcnt lgkmcnt(14)\nM v[0:3],v[196:199],a[208:211],v[0:3]\n" \
"ds_write_b64 v250,v[132:133] offset:1024\nM v[4:7],v[192:195],a[196:199],v[4:7]\nds_write_b64 v250,v[134:135] offset:1032\nM v[4:7],v[196:199],a[212:215],v[4:7]\nds_write_b64 v250,v[136:137] offset:2048\nM v[64:67],v[192:195],a[200:203],v[64:67]\nM v[64:67],v[196:199],a[216:219],v[64:67]\nds_write_b64 v250,v[138:139] offset:2056\n" \
"M v[68:71],v[192:195],a[204:207],v[68:71]\nds_read_b64_tr_b16 v[192:193],v241 offset:33792\nds_read_b64_tr_b16 v[194:195],v241 offset:34048\nM v[68:71],v[196:199],a[220:223],v[68:71]\nds_read_b64_tr_b16 v[196:197],v241 offset:41984\ns_waitcnt lgkmcnt(12)\nM v[8:11],v[200:203],a[192:195],v[8:11]\nds_write_b64 v250,v[140:141] offset:3072\n" \
"ds_read_b64_tr_b16 v[198:199],v241 offset:42240\nM v[8:11],v[204:207],a[208:211],v[8:11]\nM v[12:15],v[200:203],a[196:199],v[12:15]\nM v[12:15],v[204:207],a[212:215],v[12:15]\nds_write_b64 v250,v[142:143] offset:3080\nM v[72:75],v[200:203],a[200:203],v[72:75]\nM v[72:75],v[204:207],a[216:219],v[72:75]\nM v[76:79],v[200:203],a[204:207],v[76:79]\n" \
"ds_read_b64_tr_b16 v[200:201],v242 offset:33792\nds_read_b64_tr_b16 v[202:203],v242 offset:34048\nM v[76:79],v[204:207],a[220:223],v[76:79]\nds_read_b64_tr_b16 v[204:205],v242 offset:41984\nds_read_b64_tr_b16 v[206:207],v242 offset:42240\nM v[16:19],v[208:211],a[192:195],v[16:19]\nds_write_b64 v250,v[144:145] offset:4096\nM v[16:19],v[212:215],a[208:211],v[16:19]\n" \
"ds_write_b64 v250,v[146:147] offset:4104\nM v[20:23],v[208:211],a[196:199],v[20:23]\nM v[20:23],v[212:215],a[212:215],v[20:23]\nM v[80:83],v[208:211],a[200:203],v[80:83]\nds_write_b64 v250,v[148:149] offset:5120\nM v[80:83],v[212:215],a[216:219],v[80:83]\nM v[84:87],v[208:211],a[204:207],v[84:87]\nds_read_b64_tr_b16 v[208:209],v241 offset:34304\n" \
"ds_read_b64_tr_b16 v[210:211],v241 offset:34560\nM v[84:87],v[212:215],a[220:223],v[84:87]\nds_read_b64_tr_b16 v[212:213],v241 offset:42496\ns_waitcnt lgkmcnt(11)\nM v[24:27],v[216:219],a[192:195],v[24:27]\nds_write_b64 v250,v[150:151] offset:5128\nds_read_b64_tr_b16 v[214:215],v241 offset:42752\nM v[24:27],v[220:223],a[208:211],v[24:27]\n" \
"M v[28:31],v[216:219],a[196:199],v[28:31]\nbuffer_store_dwordx4 v[180:183],v240,s[52:55],s74 offen offset:1024 sc1\nM v[28:31],v[220:223],a[212:215],v[28:31]\nds_write_b64 v250,v[152:153] offset:6144\nM v[88:91],v[216:219],a[200:203],v[88:91]\nM v[88:91],v[220:223],a[216:219],v[88:91]\nds_write_b64 v250,v[154:155] offset:6152\nM v[92:95],v[216:219],a[204:207],v[92:95]\n" \
"ds_read_b64_tr_b16 v[216:217],v242 offset:34304\nds_read_b64_tr_b16 v[218:219],v242 offset:34560\nM v[92:95],v[220:223],a[220:223],v[92:95]\nds_read_b64_tr_b16 v[220:221],v242 offset:42496\nds_read_b64_tr_b16 v[222:223],v242 offset:42752\nM v[32:35],v[192:195],a[192:195],v[32:35]\nds_write_b64 v250,v[156:157] offset:7168\nM v[32:35],v[196:199],a[208:211],v[32:35]\n" \
"M v[36:39],v[192:195],a[196:199],v[36:39]\nM v[36:39],v[196:199],a[212:215],v[36:39]\nds_write_b64 v250,v[158:159] offset:7176\nM v[96:99],v[192:195],a[200:203],v[96:99]\nM v[96:99],v[196:199],a[216:219],v[96:99]\nM v[100:103],v[192:195],a[204:207],v[100:103]\nds_write_b64 v250,v[160:161] offset:8192\nM v[100:103],v[196:199],a[220:223],v[100:103]\n" \
"s_waitcnt lgkmcnt(9)\nM v[40:43],v[200:203],a[192:195],v[40:43]\nM v[40:43],v[204:207],a[208:211],v[40:43]\nds_write_b64 v250,v[162:163] offset:8200\nM v[44:47],v[200:203],a[196:199],v[44:47]\nM v[44:47],v[204:207],a[212:215],v[44:47]\nM v[104:107],v[200:203],a[200:203],v[104:107]\nds_write_b64 v250,v[164:165] offset:9216\n" \
"M v[104:107],v[204:207],a[216:219],v[104:107]\nM v[108:111],v[200:203],a[204:207],v[108:111]\nM v[108:111],v[204:207],a[220:223],v[108:111]\nds_write_b64 v250,v[166:167] offset:9224\nM v[48:51],v[208:211],a[192:195],v[48:51]\nM v[48:51],v[212:215],a[208:211],v[48:51]\nM v[52:55],v[208:211],a[196:199],v[52:55]\nds_write_b64 v250,v[168:169] offset:10240\n" \
"M v[52:55],v[212:215],a[212:215],v[52:55]\nM v[112:115],v[208:211],a[200:203],v[112:115]\ns_waitcnt vmcnt(3) lgkmcnt(5)\ns_barrier\nM v[112:115],v[212:215],a[216:219],v[112:115]\nds_read_b64_tr_b16 v[192:193],v241 offset:49152\nds_read_b64_tr_b16 v[194:195],v241 offset:49408\nM v[116:119],v[208:211],a[204:207],v[116:119]\n" \
"ds_read_b64_tr_b16 v[196:197],v241 offset:57344\nds_read_b64_tr_b16 v[198:199],v241 offset:57600\nM v[116:119],v[212:215],a[220:223],v[116:119]\nds_read_b64_tr_b16 v[200:201],v242 offset:49152\nds_read_b64_tr_b16 v[202:203],v242 offset:49408\nM v[56:59],v[216:219],a[192:195],v[56:59]\nbuffer_store_dwordx4 v[184:187],v240,s[52:55],s74 offen offset:2048 sc1\nM v[56:59],v[220:223],a[208:211],v[56:59]\n" \
"ds_read_b64_tr_b16 v[204:205],v242 offset:57344\nds_read_b64_tr_b16 v[206:207],v242 offset:57600\nM v[60:63],v[216:219],a[196:199],v[60:63]\nds_read_b64_tr_b16 v[208:209],v241 offset:49664\nds_read_b64_tr_b16 v[210:211],v241 offset:49920\nM v[60:63],v[220:223],a[212:215],v[60:63]\nds_read_b64_tr_b16 v[212:213],v241 offset:57856\nds_read_b64_tr_b16 v[214:215],v241 offset:58112\n" \
"M v[120:123],v[216:219],a[200:203],v[120:123]\nds_write_b64 v250,v[170:171] offset:10248\nv_accvgpr_write_b32 a208,v229\nM v[120:123],v[220:223],a[216:219],v[120:123]\nds_write_b64 v250,v[172:173] offset:11264\nv_accvgpr_write_b32 a209,v229\nM v[124:127],v[216:219],a[204:207],v[124:127]\nds_read_b64_tr_b16 v[216:217],v242 offset:49664\n" \
"ds_read_b64_tr_b16 v[218:219],v242 offset:49920\nM v[124:127],v[220:223],a[220:223],v[124:127]\nds_read_b64_tr_b16 v[220:221],v242 offset:57856\ns_waitcnt lgkmcnt(15)\nM v[0:3],v[192:195],a[224:227],v[0:3]\nds_read_b64_tr_b16 v[222:223],v242 offset:58112\ns_waitcnt lgkmcnt(14)\nM v[0:3],v[196:199],a[240:243],v[0:3]\n" \
"ds_write_b64 v250,v[174:175] offset:11272\nv_accvgpr_write_b32 a210,v229\nM v[4:7],v[192:195],a[228:231],v[4:7]\nds_write_b64 v250,v[176:177] offset:12288\nv_accvgpr_write_b32 a211,v229\nM v[4:7],v[196:199],a[244:247],v[4:7]\nds_write_b64 v250,v[178:179] offset:12296\nM v[64:67],v[192:195],a[232:235],v[64:67]\n" \
"v_cvt_pk_bf16_f32 v0,v0,v1\nv_cvt_pk_bf16_f32 v1,v2,v3\nM v[64:67],v[196:199],a[248:251],v[64:67]\nds_write_b64 v250,v[180:181] offset:13312\nv_accvgpr_write_b32 a128,v0\nM v[68:71],v[192:195],a[236:239],v[68:71]\nds_read_b64_tr_b16 v[192:193],v241 offset:50176\nds_read_b64_tr_b16 v[194:195],v241 offset:50432\n" \
"M v[68:71],v[196:199],a[252:255],v[68:71]\nds_read_b64_tr_b16 v[196:197],v241 offset:58368\ns_waitcnt lgkmcnt(12)\nM v[8:11],v[200:203],a[224:227],v[8:11]\nds_read_b64_tr_b16 v[198:199],v241 offset:58624\nv_cvt_pk_bf16_f32 v4,v4,v5\nM v[8:11],v[204:207],a[240:243],v[8:11]\nv_cvt_pk_bf16_f32 v64,v64,v65\n" \
"v_cvt_pk_bf16_f32 v5,v6,v7\nM v[12:15],v[200:203],a[228:231],v[12:15]\nv_cvt_pk_bf16_f32 v68,v68,v69\nv_cvt_pk_bf16_f32 v65,v66,v67\nM v[12:15],v[204:207],a[244:247],v[12:15]\nds_write_b64 v250,v[182:183] offset:13320\nv_cvt_pk_bf16_f32 v69,v70,v71\nM v[72:75],v[200:203],a[232:235],v[72:75]\n" \
"ds_write_b64 v250,v[184:185] offset:14336\nv_cvt_pk_bf16_f32 v2,v8,v9\nM v[72:75],v[204:207],a[248:251],v[72:75]\nds_write_b64 v250,v[186:187] offset:14344\nv_cvt_pk_bf16_f32 v3,v10,v11\nM v[76:79],v[200:203],a[236:239],v[76:79]\nds_read_b64_tr_b16 v[200:201],v242 offset:50176\nds_read_b64_tr_b16 v[202:203],v242 offset:50432\n" \
"M v[76:79],v[204:207],a[252:255],v[76:79]\nds_read_b64_tr_b16 v[204:205],v242 offset:58368\nds_read_b64_tr_b16 v[206:207],v242 offset:58624\nM v[16:19],v[208:211],a[224:227],v[16:19]\nds_write_b64 v250,v[188:189] offset:15360\nv_cvt_pk_bf16_f32 v6,v12,v13\nM v[16:19],v[212:215],a[240:243],v[16:19]\nv_cvt_pk_bf16_f32 v7,v14,v15\n" \
"v_cvt_pk_bf16_f32 v66,v72,v73\nM v[20:23],v[208:211],a[228:231],v[20:23]\nv_cvt_pk_bf16_f32 v67,v74,v75\nv_cvt_pk_bf16_f32 v70,v76,v77\nM v[20:23],v[212:215],a[244:247],v[20:23]\nds_write_b64 v250,v[190:191] offset:15368\nv_cvt_pk_bf16_f32 v71,v78,v79\nM v[80:83],v[208:211],a[232:235],v[80:83]\n" \
"v_cvt_pk_bf16_f32 v16,v16,v17\nv_cvt_pk_bf16_f32 v17,v18,v19\nM v[80:83],v[212:215],a[248:251],v[80:83]\nv_add_u32 v250,114688,v240\nv_accvgpr_write_b32 a129,v1\nM v[84:87],v[208:211],a[236:239],v[84:87]\nbuffer_store_dwordx4 v[188:191],v240,s[52:55],s74 offen offset:3072 sc1\nM v[84:87],v[212:215],a[252:255],v[84:87]\n" \
"ds_read_b64_tr_b16 v[208:209],v241 offset:50688\ns_waitcnt lgkmcnt(7)\nM v[24:27],v[216:219],a[224:227],v[24:27]\nds_read_b64_tr_b16 v[210:211],v241 offset:50944\nds_read_b64_tr_b16 v[212:213],v241 offset:58880\nM v[24:27],v[220:223],a[240:243],v[24:27]\nds_read_b64_tr_b16 v[214:215],v241 offset:59136\nv_cvt_pk_bf16_f32 v20,v20,v21\n" \
"M v[28:31],v[216:219],a[228:231],v[28:31]\nv_cvt_pk_bf16_f32 v80,v80,v81\nv_cvt_pk_bf16_f32 v84,v84,v85\nM v[28:31],v[220:223],a[244:247],v[28:31]\nv_cvt_pk_bf16_f32 v21,v22,v23\nv_cvt_pk_bf16_f32 v81,v82,v83\nM v[88:91],v[216:219],a[232:235],v[88:91]\nv_cvt_pk_bf16_f32 v85,v86,v87\n" \
"v_cvt_pk_bf16_f32 v18,v24,v25\nM v[88:91],v[220:223],a[248:251],v[88:91]\nv_cvt_pk_bf16_f32 v19,v26,v27\nv_add_u32 v241,65536,v240\nM v[92:95],v[216:219],a[236:239],v[92:95]\nds_read_b64_tr_b16 v[216:217],v242 offset:50688\nds_read_b64_tr_b16 v[218:219],v242 offset:50944\nM v[92:95],v[220:223],a[252:255],v[92:95]\n" \
"ds_read_b64_tr_b16 v[220:221],v242 offset:58880\nds_read_b64_tr_b16 v[222:223],v242 offset:59136\nM v[32:35],v[192:195],a[224:227],v[32:35]\nv_cvt_pk_bf16_f32 v22,v28,v29\nv_cvt_pk_bf16_f32 v23,v30,v31\nM v[32:35],v[196:199],a[240:243],v[32:35]\nv_cvt_pk_bf16_f32 v82,v88,v89\nv_cvt_pk_bf16_f32 v83,v90,v91\n" \
"M v[36:39],v[192:195],a[228:231],v[36:39]\nv_cvt_pk_bf16_f32 v86,v92,v93\nv_cvt_pk_bf16_f32 v87,v94,v95\nM v[36:39],v[196:199],a[244:247],v[36:39]\nv_lshl_or_b32 v242,v231,6,v230\nv_accvgpr_write_b32 a130,v2\nM v[96:99],v[192:195],a[232:235],v[96:99]\nv_cvt_pk_bf16_f32 v32,v32,v33\n" \
"v_cvt_pk_bf16_f32 v33,v34,v35\nM v[96:99],v[196:199],a[248:251],v[96:99]\nv_accvgpr_write_b32 a131,v3\nv_accvgpr_write_b32 a144,v4\nM v[100:103],v[192:195],a[236:239],v[100:103]\nv_cvt_pk_bf16_f32 v36,v36,v37\nv_cvt_pk_bf16_f32 v37,v38,v39\nM v[100:103],v[196:199],a[252:255],v[100:103]\n" \
"v_accvgpr_write_b32 a145,v5\ns_waitcnt lgkmcnt(4)\nM v[40:43],v[200:203],a[224:227],v[40:43]\nv_cvt_pk_bf16_f32 v96,v96,v97\nv_cvt_pk_bf16_f32 v97,v98,v99\nM v[40:43],v[204:207],a[240:243],v[40:43]\nv_accvgpr_write_b32 a146,v6\nv_accvgpr_write_b32 a147,v7\n" \
"M v[44:47],v[200:203],a[228:231],v[44:47]\nv_cvt_pk_bf16_f32 v100,v100,v101\nv_cvt_pk_bf16_f32 v101,v102,v103\nM v[44:47],v[204:207],a[244:247],v[44:47]\nv_accvgpr_write_b32 a160,v64\nv_accvgpr_write_b32 a161,v65\nM v[104:107],v[200:203],a[232:235],v[104:107]\nv_cvt_pk_bf16_f32 v34,v40,v41\n" \
"v_cvt_pk_bf16_f32 v35,v42,v43\nM v[104:107],v[204:207],a[248:251],v[104:107]\nv_accvgpr_write_b32 a162,v66\nv_accvgpr_write_b32 a163,v67\nM v[108:111],v[200:203],a[236:239],v[108:111]\nv_cvt_pk_bf16_f32 v38,v44,v45\nv_cvt_pk_bf16_f32 v39,v46,v47\nM v[108:111],v[204:207],a[252:255],v[108:111]\n" \
"v_accvgpr_write_b32 a176,v68\nv_accvgpr_write_b32 a177,v69\nM v[48:51],v[208:211],a[224:227],v[48:51]\nv_cvt_pk_bf16_f32 v98,v104,v105\nv_cvt_pk_bf16_f32 v99,v106,v107\nM v[48:51],v[212:215],a[240:243],v[48:51]\nv_accvgpr_write_b32 a178,v70\nv_accvgpr_write_b32 a179,v71\n" \
"M v[52:55],v[208:211],a[228:231],v[52:55]\nv_cvt_pk_bf16_f32 v102,v108,v109\nv_cvt_pk_bf16_f32 v103,v110,v111\nM v[52:55],v[212:215],a[244:247],v[52:55]\nv_accvgpr_write_b32 a132,v16\nv_accvgpr_write_b32 a133,v17\nM v[112:115],v[208:211],a[232:235],v[112:115]\nv_cvt_pk_bf16_f32 v48,v48,v49\n" \
"v_cvt_pk_bf16_f32 v49,v50,v51\nM v[112:115],v[212:215],a[248:251],v[112:115]\nv_accvgpr_write_b32 a134,v18\nv_accvgpr_write_b32 a135,v19\nM v[116:119],v[208:211],a[236:239],v[116:119]\nv_cvt_pk_bf16_f32 v52,v52,v53\nv_cvt_pk_bf16_f32 v53,v54,v55\nM v[116:119],v[212:215],a[252:255],v[116:119]\n" \
"v_accvgpr_write_b32 a148,v20\ns_waitcnt lgkmcnt(0)\nM v[56:59],v[216:219],a[224:227],v[56:59]\nv_cvt_pk_bf16_f32 v112,v112,v113\nv_cvt_pk_bf16_f32 v113,v114,v115\nM v[56:59],v[220:223],a[240:243],v[56:59]\nv_accvgpr_write_b32 a149,v21\nv_accvgpr_write_b32 a150,v22\n" \
"M v[60:63],v[216:219],a[228:231],v[60:63]\nv_cvt_pk_bf16_f32 v116,v116,v117\nv_cvt_pk_bf16_f32 v117,v118,v119\nM v[60:63],v[220:223],a[244:247],v[60:63]\nv_accvgpr_write_b32 a244,0\nv_accvgpr_write_b32 a245,0\nM v[120:123],v[216:219],a[232:235],v[120:123]\nv_cvt_pk_bf16_f32 v50,v56,v57\n" \
"v_cvt_pk_bf16_f32 v51,v58,v59\nM v[120:123],v[220:223],a[248:251],v[120:123]\nv_accvgpr_write_b32 a248,-1\nv_accvgpr_write_b32 a249,-1\nM v[124:127],v[216:219],a[236:239],v[124:127]\nv_cvt_pk_bf16_f32 v54,v60,v61\nv_cvt_pk_bf16_f32 v55,v62,v63\nM v[124:127],v[220:223],a[252:255],v[124:127]\n" \
"s_waitcnt vmcnt(0)\nbuffer_store_dword v252,off,s[52:55],s29 sc1\nv_cvt_pk_bf16_f32 v114,v120,v121\nv_cvt_pk_bf16_f32 v115,v122,v123\nv_accvgpr_write_b32 a246,0\nv_accvgpr_write_b32 a250,-1\nv_accvgpr_write_b32 a247,0\nv_accvgpr_write_b32 a251,-1\n" \
"v_cvt_pk_bf16_f32 v118,v124,v125\nv_cvt_pk_bf16_f32 v119,v126,v127\nv_accvgpr_write_b32 a151,v23\nv_accvgpr_write_b32 a164,v80\nv_accvgpr_write_b32 a165,v81\nv_accvgpr_write_b32 a166,v82\nv_accvgpr_write_b32 a167,v83\nv_accvgpr_write_b32 a180,v84\n" \
"v_accvgpr_write_b32 a181,v85\nv_accvgpr_write_b32 a182,v86\nv_accvgpr_write_b32 a183,v87\nv_accvgpr_write_b32 a136,v32\nv_accvgpr_write_b32 a137,v33\nv_accvgpr_write_b32 a138,v34\nv_accvgpr_write_b32 a139,v35\nv_accvgpr_write_b32 a152,v36\n" \
"v_accvgpr_write_b32 a153,v37\nv_accvgpr_write_b32 a154,v38\nv_accvgpr_write_b32 a155,v39\nv_accvgpr_write_b32 a168,v96\nv_accvgpr_write_b32 a169,v97\nv_accvgpr_write_b32 a170,v98\nv_accvgpr_write_b32 a171,v99\nv_accvgpr_write_b32 a184,v100\n" \
"v_accvgpr_write_b32 a185,v101\nv_accvgpr_write_b32 a186,v102\nv_accvgpr_write_b32 a187,v103\nv_accvgpr_write_b32 a140,v48\nv_accvgpr_write_b32 a141,v49\nv_accvgpr_write_b32 a142,v50\nv_accvgpr_write_b32 a143,v51\nv_accvgpr_write_b32 a156,v52\n" \
"v_accvgpr_write_b32 a157,v53\nv_accvgpr_write_b32 a158,v54\nv_accvgpr_write_b32 a159,v55\nv_accvgpr_write_b32 a172,v112\nv_accvgpr_write_b32 a173,v113\nv_accvgpr_write_b32 a174,v114\nv_accvgpr_write_b32 a175,v115\nv_accvgpr_write_b32 a188,v116\n" \
"v_accvgpr_write_b32 a189,v117\nv_accvgpr_write_b32 a190,v118\nv_accvgpr_write_b32 a191,v119\ns_waitcnt lgkmcnt(0)\n.Lattn:\ns_cmp_lt_u32 s7,2\ns_cbranch_scc0 .Lp23\ns_cmp_eq_u32 s7,0\n" \
"s_cbranch_scc0 .Lpro1\n.Lpro0:\ns_barrier\nM v[64:67],a[0:3],a[128:131],0\nds_read_b128 a[32:35],v240 offset:40960\nds_read_b128 a[36:39],v240 offset:41984\nM v[64:67],a[4:7],a[132:135],v[64:67]\nds_read_b128 a[40:43],v240 offset:43008\n" \
"s_mov_b32 m0,s64\nv_mov_b32 v218,0\nM v[64:67],a[8:11],a[136:139],v[64:67]\nds_read_b128 a[44:47],v240 offset:44032\nv_mov_b32 v219,0\nM v[64:67],a[12:15],a[140:143],v[64:67]\nds_read_b128 a[48:51],v240 offset:45056\nbuffer_load_dwordx4 v240,s[44:47],s36 offen offset:0 sc1 lds\n" \
"M v[68:71],a[12:15],a[156:159],0\nds_read_b128 a[12:15],v240 offset:35840\nM v[68:71],a[8:11],a[152:155],v[68:71]\nds_read_b128 a[8:11],v240 offset:34816\nM v[68:71],a[4:7],a[148:151],v[68:71]\nds_read_b128 a[4:7],v240 offset:33792\nv_max3_f32 v225,v64,v65,v66\nM v[68:71],a[0:3],a[144:147],v[68:71]\n" \
"ds_read_b128 a[0:3],v240 offset:32768\nv_max_f32 v225,v225,v67\nv_cndmask_b32 v225,v224,v225,s[90:91]\nM v[76:79],a[28:31],a[156:159],0\nds_read_b128 a[28:31],v240 offset:39936\nv_cndmask_b32 v64,v64,v252,s[82:83]\nM v[76:79],a[24:27],a[152:155],v[76:79]\nds_read_b128 a[24:27],v240 offset:38912\n" \
"v_max3_f32 v229,v68,v69,v70\nv_cndmask_b32 v65,v65,v252,s[84:85]\nM v[76:79],a[20:23],a[148:151],v[76:79]\nds_read_b128 a[20:23],v240 offset:37888\nv_cndmask_b32 v66,v66,v252,s[86:87]\nM v[76:79],a[16:19],a[144:147],v[76:79]\nds_read_b128 a[16:19],v240 offset:36864\nv_cndmask_b32 v67,v67,v252,s[88:89]\n" \
"s_waitcnt lgkmcnt(4)\nM v[136:139],a[0:3],a[160:163],0\nbuffer_load_dwordx4 v240,s[44:47],s36 offen offset:1024 sc1 lds\nM v[136:139],a[4:7],a[164:167],v[136:139]\nds_read_b128 a[52:55],v240 offset:46080\nds_read_b128 a[56:59],v240 offset:47104\nv_max3_f32 v229,v229,v71,v76\nM v[136:139],a[8:11],a[168:171],v[136:139]\n" \
"v_max3_f32 v229,v229,v77,v78\nv_max_f32 v229,v229,v79\nv_cndmask_b32 v229,v228,v229,s[90:91]\nM v[136:139],a[12:15],a[172:175],v[136:139]\nds_read_b128 a[60:63],v240 offset:48128\nv_permlane16_swap_b32 v225,v229\nM v[140:143],a[12:15],a[188:191],0\nv_max_f32 v225,v225,v229\n" \
"v_mov_b32 v229,v225\nds_read_b128 a[64:67],v241 offset:0\nM v[140:143],a[8:11],a[184:187],v[140:143]\nv_permlane32_swap_b32 v225,v229\nv_max_f32 v225,v225,v229\nM v[140:143],a[4:7],a[180:183],v[140:143]\nv_mov_b32 v229,v225\nv_max3_f32 v233,v136,v137,v138\n" \
"ds_read_b128 a[68:71],v241 offset:1024\nM v[140:143],a[0:3],a[176:179],v[140:143]\nv_permlane16_swap_b32 v225,v229\ns_waitcnt lgkmcnt(5)\nM v[152:155],a[16:19],a[160:163],0\nv_sub_f32 v224,0,v225\nv_sub_f32 v226,0,v225\nv_sub_f32 v227,0,v225\n" \
"M v[152:155],a[20:23],a[164:167],v[152:155]\nv_sub_f32 v225,0,v225\nv_sub_f32 v228,0,v229\nM v[152:155],a[24:27],a[168:171],v[152:155]\nv_sub_f32 v230,0,v229\nv_sub_f32 v231,0,v229\nv_sub_f32 v229,0,v229\nM v[152:155],a[28:31],a[172:175],v[152:155]\n" \
"v_max3_f32 v237,v140,v141,v142\nds_read_b128 a[72:75],v241 offset:2048\nM v[156:159],a[28:31],a[188:191],0\nbuffer_load_dwordx4 v240,s[44:47],s36 offen offset:2048 sc1 lds\nds_read_b128 a[76:79],v241 offset:3072\nM v[156:159],a[24:27],a[184:187],v[156:159]\nds_read_b128 a[80:83],v241 offset:4096\nds_read_b128 a[84:87],v241 offset:5120\n" \
"M v[156:159],a[20:23],a[180:183],v[156:159]\nv_max3_f32 v233,v233,v139,v152\nv_max3_f32 v233,v233,v153,v154\nv_max_f32 v233,v233,v155\nM v[156:159],a[16:19],a[176:179],v[156:159]\nv_cndmask_b32 v233,v232,v233,s[90:91]\nds_read_b128 a[88:91],v241 offset:6144\nv_add_f32 v64,v64,v224\n" \
"M v[128:131],a[0:3],a[128:131],v[224:227]\nbuffer_load_dwordx4 v240,s[44:47],s36 offen offset:3072 sc1 lds\nM v[128:131],a[4:7],a[132:135],v[128:131]\nv_add_f32 v65,v65,v224\nv_add_f32 v66,v66,v224\nv_max3_f32 v237,v237,v143,v156\nM v[128:131],a[8:11],a[136:139],v[128:131]\nv_max3_f32 v237,v237,v157,v158\n" \
"v_max_f32 v237,v237,v159\nM v[128:131],a[12:15],a[140:143],v[128:131]\nv_cndmask_b32 v237,v236,v237,s[90:91]\nv_add_f32 v67,v67,v224\nds_read_b128 a[92:95],v241 offset:7168\nM v[132:135],a[12:15],a[156:159],v[228:231]\nv_permlane16_swap_b32 v233,v237\nv_max_f32 v233,v233,v237\n" \
"M v[132:135],a[8:11],a[152:155],v[132:135]\nv_mov_b32 v237,v233\nv_exp_f32 v64,v64\nM v[132:135],a[4:7],a[148:151],v[132:135]\nv_permlane32_swap_b32 v233,v237\nv_max_f32 v233,v233,v237\nM v[132:135],a[0:3],a[144:147],v[132:135]\nv_mov_b32 v237,v233\n" \
"v_exp_f32 v65,v65\nM v[144:147],a[16:19],a[128:131],v[224:227]\nv_permlane16_swap_b32 v233,v237\nv_sub_f32 v232,0,v233\nM v[144:147],a[20:23],a[132:135],v[144:147]\nv_sub_f32 v234,0,v233\nv_sub_f32 v235,0,v233\nv_sub_f32 v233,0,v233\n" \
"M v[144:147],a[24:27],a[136:139],v[144:147]\nv_sub_f32 v236,0,v237\nv_sub_f32 v238,0,v237\nM v[144:147],a[28:31],a[140:143],v[144:147]\nv_sub_f32 v239,0,v237\nv_sub_f32 v237,0,v237\nv_cvt_pk_bf16_f32 v64,v64,v65\nM v[148:151],a[28:31],a[156:159],v[228:231]\n" \
"v_exp_f32 v66,v66\nv_add_f32 v68,v68,v228\nM v[148:151],a[24:27],a[152:155],v[148:151]\nv_exp_f32 v67,v67\nM v[148:151],a[20:23],a[148:151],v[148:151]\nv_cvt_pk_bf16_f32 v65,v66,v67\nv_mov_b32 v66,0\nv_mov_b32 v67,0\n" \
"M v[148:151],a[16:19],a[144:147],v[148:151]\nv_add_f32 v69,v69,v228\nv_cndmask_b32 v76,v76,v252,s[82:83]\nM v[160:163],a[32:35],a[128:131],v[224:227]\nv_cndmask_b32 v77,v77,v252,s[84:85]\nv_cndmask_b32 v78,v78,v252,s[86:87]\nv_cndmask_b32 v79,v79,v252,s[88:89]\nM v[160:163],a[36:39],a[132:135],v[160:163]\n" \
"v_add_f32 v70,v70,v228\nv_add_f32 v71,v71,v228\nM v[160:163],a[40:43],a[136:139],v[160:163]\nv_exp_f32 v68,v68\nv_add_f32 v76,v76,v228\nM v[160:163],a[44:47],a[140:143],v[160:163]\nv_exp_f32 v69,v69\nM v[164:167],a[44:47],a[156:159],v[228:231]\n" \
"v_exp_f32 v70,v70\nv_add_f32 v77,v77,v228\nM v[164:167],a[40:43],a[152:155],v[164:167]\nv_exp_f32 v71,v71\nM v[164:167],a[36:39],a[148:151],v[164:167]\nv_add_f32 v78,v78,v228\nv_add_f32 v79,v79,v228\nv_cvt_pk_bf16_f32 v68,v68,v69\n" \
"M v[164:167],a[32:35],a[144:147],v[164:167]\nv_exp_f32 v76,v76\nM v[176:179],a[48:51],a[128:131],v[224:227]\nv_exp_f32 v77,v77\ns_waitcnt lgkmcnt(0)\nM v[176:179],a[52:55],a[132:135],v[176:179]\nv_exp_f32 v78,v78\nv_cvt_pk_bf16_f32 v69,v70,v71\n" \
"M v[176:179],a[56:59],a[136:139],v[176:179]\nv_exp_f32 v79,v79\nM v[176:179],a[60:63],a[140:143],v[176:179]\nv_cvt_pk_bf16_f32 v70,v76,v77\nv_cvt_pk_bf16_f32 v71,v78,v79\nds_read_b128 a[0:3],v240 offset:49152\nM v[180:183],a[60:63],a[156:159],v[228:231]\nds_read_b128 a[4:7],v240 offset:50176\n" \
"ds_read_b128 a[8:11],v240 offset:51200\nM v[180:183],a[56:59],a[152:155],v[180:183]\nds_read_b128 a[12:15],v240 offset:52224\nv_exp_f32 v128,v128\nM v[180:183],a[52:55],a[148:151],v[180:183]\nds_read_b128 a[16:19],v240 offset:53248\nv_add_f32 v136,v136,v232\nM v[180:183],a[48:51],a[144:147],v[180:183]\n" \
"v_exp_f32 v129,v129\nds_read_b128 a[20:23],v240 offset:54272\nM v[168:171],a[32:35],a[160:163],v[232:235]\nv_exp_f32 v130,v130\nM v[168:171],a[36:39],a[164:167],v[168:171]\nv_exp_f32 v131,v131\nv_cvt_pk_bf16_f32 v192,v128,v129\nM v[168:171],a[40:43],a[168:171],v[168:171]\n" \
"v_cvt_pk_bf16_f32 v193,v130,v131\nds_read_b128 a[24:27],v240 offset:55296\nM v[168:171],a[44:47],a[172:175],v[168:171]\nds_read_b128 a[28:31],v240 offset:56320\nv_exp_f32 v132,v132\nM v[172:175],a[44:47],a[188:191],v[236:239]\nv_exp_f32 v133,v133\nM v[172:175],a[40:43],a[184:187],v[172:175]\n" \
"v_exp_f32 v134,v134\nv_cvt_pk_bf16_f32 v196,v132,v133\nM v[172:175],a[36:39],a[180:183],v[172:175]\nbuffer_load_dword v248,v251,s[52:55],s80 offen sc1\nv_add_f32 v137,v137,v232\nM v[172:175],a[32:35],a[176:179],v[172:175]\nv_exp_f32 v135,v135\nM v[188:191],a[60:63],a[188:191],v[236:239]\n" \
"v_cvt_pk_bf16_f32 v197,v134,v135\nv_exp_f32 v144,v144\nM v[188:191],a[56:59],a[184:187],v[188:191]\nv_exp_f32 v145,v145\nM v[188:191],a[52:55],a[180:183],v[188:191]\nv_exp_f32 v146,v146\nv_cvt_pk_bf16_f32 v194,v144,v145\nM v[188:191],a[48:51],a[176:179],v[188:191]\n" \
"v_exp_f32 v147,v147\nM v[0:3],a[64:67],v[64:67],0\nv_cvt_pk_bf16_f32 v195,v146,v147\nv_exp_f32 v148,v148\nM v[8:11],a[68:71],v[64:67],0\nv_exp_f32 v149,v149\nM v[16:19],a[72:75],v[64:67],0\nv_exp_f32 v150,v150\n" \
"v_cvt_pk_bf16_f32 v198,v148,v149\nM v[24:27],a[76:79],v[64:67],0\nv_exp_f32 v151,v151\nM v[32:35],a[80:83],v[64:67],0\nv_cvt_pk_bf16_f32 v199,v150,v151\nv_add_f32 v138,v138,v232\nv_add_f32 v139,v139,v232\nM v[40:43],a[84:87],v[64:67],0\n" \
"v_exp_f32 v136,v136\nM v[48:51],a[88:91],v[64:67],0\nv_exp_f32 v137,v137\nv_add_f32 v140,v140,v236\nM v[56:59],a[92:95],v[64:67],0\nv_exp_f32 v138,v138\nv_cvt_pk_bf16_f32 v200,v136,v137\nM a[192:195],a[208:211],v[64:67],0\n" \
"v_exp_f32 v139,v139\nM v[4:7],a[64:67],v[68:71],0\nv_cvt_pk_bf16_f32 v201,v138,v139\nv_add_f32 v141,v141,v236\nv_add_f32 v142,v142,v236\nM v[12:15],a[68:71],v[68:71],0\nv_add_f32 v143,v143,v236\nds_read_b128 a[32:35],v240 offset:57344\n" \
"M v[20:23],a[72:75],v[68:71],0\nv_exp_f32 v140,v140\nv_add_f32 v152,v152,v232\nM v[28:31],a[76:79],v[68:71],0\nv_exp_f32 v141,v141\nM v[36:39],a[80:83],v[68:71],0\nv_exp_f32 v142,v142\nv_cvt_pk_bf16_f32 v204,v140,v141\n" \
"M v[44:47],a[84:87],v[68:71],0\nv_exp_f32 v143,v143\nM v[52:55],a[88:91],v[68:71],0\nv_cvt_pk_bf16_f32 v205,v142,v143\nv_add_f32 v153,v153,v232\nv_add_f32 v154,v154,v232\nM v[60:63],a[92:95],v[68:71],0\nv_add_f32 v155,v155,v232\n" \
"ds_read_b128 a[36:39],v240 offset:58368\nM a[196:199],a[208:211],v[68:71],0\nv_exp_f32 v152,v152\ns_waitcnt lgkmcnt(2)\nM v[128:131],a[0:3],a[128:131],v[224:227]\nv_exp_f32 v153,v153\nM v[128:131],a[4:7],a[132:135],v[128:131]\nv_exp_f32 v154,v154\n" \
"v_cvt_pk_bf16_f32 v202,v152,v153\nM v[128:131],a[8:11],a[136:139],v[128:131]\nv_exp_f32 v155,v155\nM v[128:131],a[12:15],a[140:143],v[128:131]\nv_cvt_pk_bf16_f32 v203,v154,v155\nds_read_b128 a[40:43],v240 offset:59392\nds_read_b128 a[44:47],v240 offset:60416\nM v[132:135],a[12:15],a[156:159],v[228:231]\n" \
"v_add_f32 v156,v156,v236\nv_add_f32 v157,v157,v236\nv_add_f32 v158,v158,v236\nM v[132:135],a[8:11],a[152:155],v[132:135]\nv_add_f32 v159,v159,v236\nds_read_b128 a[48:51],v240 offset:61440\nM v[132:135],a[4:7],a[148:151],v[132:135]\nv_exp_f32 v156,v156\n" \
"ds_read_b128 a[52:55],v240 offset:62464\nM v[132:135],a[0:3],a[144:147],v[132:135]\nv_exp_f32 v157,v157\nM v[144:147],a[16:19],a[128:131],v[224:227]\nv_exp_f32 v158,v158\nv_cvt_pk_bf16_f32 v206,v156,v157\nM v[144:147],a[20:23],a[132:135],v[144:147]\nv_exp_f32 v159,v159\n" \
"M v[144:147],a[24:27],a[136:139],v[144:147]\nv_cvt_pk_bf16_f32 v207,v158,v159\nv_exp_f32 v160,v160\nM v[144:147],a[28:31],a[140:143],v[144:147]\nv_exp_f32 v161,v161\nM v[148:151],a[28:31],a[156:159],v[228:231]\nv_exp_f32 v162,v162\nv_cvt_pk_bf16_f32 v208,v160,v161\n" \
"M v[148:151],a[24:27],a[152:155],v[148:151]\nv_exp_f32 v163,v163\nM v[148:151],a[20:23],a[148:151],v[148:151]\nv_cvt_pk_bf16_f32 v209,v162,v163\nds_read_b128 a[56:59],v240 offset:63488\nds_read_b128 a[60:63],v240 offset:64512\nM v[148:151],a[16:19],a[144:147],v[148:151]\nv_exp_f32 v164,v164\n" \
"M v[136:139],a[0:3],a[160:163],v[232:235]\nv_exp_f32 v165,v165\nv_cndmask_b32 v168,v168,v252,s[82:83]\nM v[136:139],a[4:7],a[164:167],v[136:139]\nv_exp_f32 v166,v166\nv_cvt_pk_bf16_f32 v212,v164,v165\nM v[136:139],a[8:11],a[168:171],v[136:139]\nv_exp_f32 v167,v167\n" \
"M v[136:139],a[12:15],a[172:175],v[136:139]\nv_cvt_pk_bf16_f32 v213,v166,v167\nv_exp_f32 v176,v176\nM v[140:143],a[12:15],a[188:191],v[236:239]\nv_exp_f32 v177,v177\nM v[140:143],a[8:11],a[184:187],v[140:143]\nv_exp_f32 v178,v178\nv_cvt_pk_bf16_f32 v210,v176,v177\n" \
"M v[140:143],a[4:7],a[180:183],v[140:143]\nv_exp_f32 v179,v179\nM v[140:143],a[0:3],a[176:179],v[140:143]\nv_cvt_pk_bf16_f32 v211,v178,v179\nv_exp_f32 v180,v180\nM v[152:155],a[16:19],a[160:163],v[232:235]\nv_exp_f32 v181,v181\nM v[152:155],a[20:23],a[164:167],v[152:155]\n" \
"v_exp_f32 v182,v182\nv_cvt_pk_bf16_f32 v214,v180,v181\nM v[152:155],a[24:27],a[168:171],v[152:155]\ns_waitcnt vmcnt(1)\ns_barrier\nM v[152:155],a[28:31],a[172:175],v[152:155]\nv_exp_f32 v183,v183\nv_cndmask_b32 v169,v169,v252,s[84:85]\n" \
"M v[156:159],a[28:31],a[188:191],v[236:239]\nv_cvt_pk_bf16_f32 v215,v182,v183\nv_cndmask_b32 v170,v170,v252,s[86:87]\nM v[156:159],a[24:27],a[184:187],v[156:159]\nv_cndmask_b32 v171,v171,v252,s[88:89]\nv_exp_f32 v168,v168\nM v[156:159],a[20:23],a[180:183],v[156:159]\nv_exp_f32 v169,v169\n" \
"ds_read_b128 a[64:67],v241 offset:32768\nM v[156:159],a[16:19],a[176:179],v[156:159]\nv_cvt_pk_bf16_f32 v216,v168,v169\ns_waitcnt lgkmcnt(1)\nM v[160:163],a[32:35],a[128:131],v[224:227]\nv_exp_f32 v170,v170\nds_read_b128 a[96:99],v241 offset:40960\nM v[160:163],a[36:39],a[132:135],v[160:163]\n" \
"v_exp_f32 v171,v171\nM v[160:163],a[40:43],a[136:139],v[160:163]\nv_cvt_pk_bf16_f32 v217,v170,v171\nv_exp_f32 v172,v172\nM v[160:163],a[44:47],a[140:143],v[160:163]\nv_exp_f32 v173,v173\nM v[164:167],a[44:47],a[156:159],v[228:231]\nv_exp_f32 v174,v174\n" \
"v_cvt_pk_bf16_f32 v220,v172,v173\nM v[164:167],a[40:43],a[152:155],v[164:167]\nv_exp_f32 v175,v175\nM v[164:167],a[36:39],a[148:151],v[164:167]\nv_cvt_pk_bf16_f32 v221,v174,v175\nds_read_b128 a[68:71],v241 offset:33792\nds_read_b128 a[100:103],v241 offset:41984\nM v[164:167],a[32:35],a[144:147],v[164:167]\n" \
"ds_read_b128 a[72:75],v241 offset:34816\nv_cndmask_b32 v188,v188,v252,s[82:83]\nM v[176:179],a[48:51],a[128:131],v[224:227]\nv_cndmask_b32 v189,v189,v252,s[84:85]\nv_cndmask_b32 v190,v190,v252,s[86:87]\nv_cndmask_b32 v191,v191,v252,s[88:89]\nM v[176:179],a[52:55],a[132:135],v[176:179]\nv_exp_f32 v188,v188\n" \
"M v[176:179],a[56:59],a[136:139],v[176:179]\nv_exp_f32 v189,v189\nds_read_b128 a[104:107],v241 offset:43008\nM v[176:179],a[60:63],a[140:143],v[176:179]\nv_exp_f32 v190,v190\nv_cvt_pk_bf16_f32 v222,v188,v189\nM v[180:183],a[60:63],a[156:159],v[228:231]\nv_exp_f32 v191,v191\n" \
"M v[180:183],a[56:59],a[152:155],v[180:183]\nv_cvt_pk_bf16_f32 v223,v190,v191\nds_read_b128 a[76:79],v241 offset:35840\nds_read_b128 a[108:111],v241 offset:44032\nM v[180:183],a[52:55],a[148:151],v[180:183]\nds_read_b128 a[80:83],v241 offset:36864\ns_waitcnt vmcnt(0)\nv_cmp_eq_u32 vcc,0,v248\n" \
"M v[180:183],a[48:51],a[144:147],v[180:183]\nds_read_b128 a[112:115],v241 offset:45056\ns_cbranch_vccnz .Lps2\n.Lpb2:\ns_add_u32 m0,s64,65536\nM v[168:171],a[32:35],a[160:163],v[232:235]\nbuffer_load_dwordx4 v240,s[48:51],s36 offen offset:0 sc1 lds\nM v[168:171],a[36:39],a[164:167],v[168:171]\n" \
"ds_read_b128 a[84:87],v241 offset:37888\nv_exp_f32 v128,v128\nM v[168:171],a[40:43],a[168:171],v[168:171]\nds_read_b128 a[116:119],v241 offset:46080\nds_read_b128 a[88:91],v241 offset:38912\nM v[168:171],a[44:47],a[172:175],v[168:171]\nds_read_b128 a[120:123],v241 offset:47104\nv_exp_f32 v129,v129\n" \
"M v[172:175],a[44:47],a[188:191],v[236:239]\nbuffer_load_dwordx4 v240,s[48:51],s36 offen offset:1024 sc1 lds\nM v[172:175],a[40:43],a[184:187],v[172:175]\nds_read_b128 a[92:95],v241 offset:39936\nv_exp_f32 v130,v130\nM v[172:175],a[36:39],a[180:183],v[172:175]\nds_read_b128 a[124:127],v241 offset:48128\nds_read_b128 a[0:3],v240 offset:0\n" \
"M v[172:175],a[32:35],a[176:179],v[172:175]\nds_read_b128 a[4:7],v240 offset:1024\nv_exp_f32 v131,v131\nM v[184:187],a[48:51],a[160:163],v[232:235]\nds_read_b128 a[8:11],v240 offset:2048\nbuffer_load_dwordx4 v240,s[48:51],s36 offen offset:2048 sc1 lds\nM v[184:187],a[52:55],a[164:167],v[184:187]\nv_exp_f32 v132,v132\n" \
"M v[184:187],a[56:59],a[168:171],v[184:187]\nds_read_b128 a[12:15],v240 offset:3072\nv_exp_f32 v133,v133\nM v[184:187],a[60:63],a[172:175],v[184:187]\nds_read_b128 a[16:19],v240 offset:4096\nds_read_b128 a[20:23],v240 offset:5120\nM v[188:191],a[60:63],a[188:191],v[236:239]\nds_read_b128 a[24:27],v240 offset:6144\n" \
"buffer_load_dwordx4 v240,s[48:51],s36 offen offset:3072 sc1 lds\nM v[188:191],a[56:59],a[184:187],v[188:191]\nds_read_b128 a[28:31],v240 offset:7168\ns_add_u32 m0,s64,16384\nM v[188:191],a[52:55],a[180:183],v[188:191]\nds_read_b128 a[32:35],v240 offset:8192\nv_exp_f32 v134,v134\nM v[188:191],a[48:51],a[176:179],v[188:191]\n" \
"ds_read_b128 a[36:39],v240 offset:9216\ns_waitcnt lgkmcnt(9)\nM v[0:3],a[64:67],v[192:195],v[0:3]\nds_read_b128 a[40:43],v240 offset:10240\nbuffer_load_dwordx4 v240,s[44:47],s65 offen offset:0 sc1 lds\nM v[0:3],a[96:99],v[208:211],v[0:3]\nv_exp_f32 v135,v135\nM v[8:11],a[68:71],v[192:195],v[8:11]\n" \
"ds_read_b128 a[44:47],v240 offset:11264\nv_exp_f32 v144,v144\nM v[8:11],a[100:103],v[208:211],v[8:11]\nds_read_b128 a[48:51],v240 offset:12288\nds_read_b128 a[52:55],v240 offset:13312\nM v[16:19],a[72:75],v[192:195],v[16:19]\nbuffer_load_dwordx4 v240,s[44:47],s65 offen offset:1024 sc1 lds\nds_read_b128 a[56:59],v240 offset:14336\n" \
"M v[16:19],a[104:107],v[208:211],v[16:19]\nds_read_b128 a[60:63],v240 offset:15360\nv_exp_f32 v145,v145\nM v[24:27],a[76:79],v[192:195],v[24:27]\nv_exp_f32 v146,v146\nM v[24:27],a[108:111],v[208:211],v[24:27]\nv_exp_f32 v147,v147\nM v[32:35],a[80:83],v[192:195],v[32:35]\n" \
"buffer_load_dwordx4 v240,s[44:47],s65 offen offset:2048 sc1 lds\nM v[32:35],a[112:115],v[208:211],v[32:35]\nv_exp_f32 v148,v148\nM v[40:43],a[84:87],v[192:195],v[40:43]\nv_exp_f32 v149,v149\nM v[40:43],a[116:119],v[208:211],v[40:43]\nv_exp_f32 v150,v150\nM v[48:51],a[88:91],v[192:195],v[48:51]\n" \
"buffer_load_dwordx4 v240,s[44:47],s65 offen offset:3072 sc1 lds\nM v[48:51],a[120:123],v[208:211],v[48:51]\ns_add_u32 m0,s64,81920\nv_exp_f32 v151,v151\nM v[56:59],a[92:95],v[192:195],v[56:59]\nv_exp_f32 v136,v136\nM v[56:59],a[124:127],v[208:211],v[56:59]\nv_exp_f32 v137,v137\n" \
"M a[192:195],a[208:211],v[192:195],a[192:195]\nbuffer_load_dwordx4 v240,s[48:51],s65 offen offset:0 sc1 lds\nM a[192:195],a[208:211],v[208:211],a[192:195]\nv_exp_f32 v138,v138\nv_cvt_pk_bf16_f32 v192,v128,v129\nM v[4:7],a[64:67],v[196:199],v[4:7]\nv_exp_f32 v139,v139\nM v[4:7],a[96:99],v[212:215],v[4:7]\n" \
"v_exp_f32 v152,v152\nv_cvt_pk_bf16_f32 v193,v130,v131\nM v[12:15],a[68:71],v[196:199],v[12:15]\nbuffer_load_dwordx4 v240,s[48:51],s65 offen offset:1024 sc1 lds\nv_cvt_pk_bf16_f32 v194,v144,v145\nM v[12:15],a[100:103],v[212:215],v[12:15]\nv_exp_f32 v153,v153\nM v[20:23],a[72:75],v[196:199],v[20:23]\n" \
"v_exp_f32 v154,v154\nv_cvt_pk_bf16_f32 v195,v146,v147\nM v[20:23],a[104:107],v[212:215],v[20:23]\nv_exp_f32 v155,v155\nM v[28:31],a[76:79],v[196:199],v[28:31]\nbuffer_load_dwordx4 v240,s[48:51],s65 offen offset:2048 sc1 lds\nM v[28:31],a[108:111],v[212:215],v[28:31]\nv_exp_f32 v160,v160\n" \
"M v[36:39],a[80:83],v[196:199],v[36:39]\ns_barrier\nv_exp_f32 v161,v161\nM v[36:39],a[112:115],v[212:215],v[36:39]\nv_exp_f32 v162,v162\nM v[44:47],a[84:87],v[196:199],v[44:47]\nbuffer_load_dwordx4 v240,s[48:51],s65 offen offset:3072 sc1 lds\ns_add_u32 m0,s64,32768\n" \
"M v[44:47],a[116:119],v[212:215],v[44:47]\nv_exp_f32 v163,v163\nM v[52:55],a[88:91],v[196:199],v[52:55]\nv_exp_f32 v176,v176\nv_cvt_pk_bf16_f32 v208,v160,v161\nM v[52:55],a[120:123],v[212:215],v[52:55]\nv_exp_f32 v177,v177\nM v[60:63],a[92:95],v[196:199],v[60:63]\n" \
"buffer_load_dwordx4 v240,s[44:47],s66 offen offset:0 sc1 lds\nv_cvt_pk_bf16_f32 v209,v162,v163\nM v[60:63],a[124:127],v[212:215],v[60:63]\nv_exp_f32 v178,v178\nv_cvt_pk_bf16_f32 v210,v176,v177\nM a[196:199],a[208:211],v[196:199],a[196:199]\nv_exp_f32 v179,v179\nM a[196:199],a[208:211],v[212:215],a[196:199]\n" \
"v_exp_f32 v140,v140\nv_cvt_pk_bf16_f32 v196,v132,v133\nM v[64:67],a[64:67],v[200:203],0\nbuffer_load_dwordx4 v240,s[44:47],s66 offen offset:1024 sc1 lds\nM v[64:67],a[96:99],v[216:219],v[64:67]\nv_exp_f32 v141,v141\nv_cvt_pk_bf16_f32 v197,v134,v135\nM v[72:75],a[68:71],v[200:203],0\n" \
"v_exp_f32 v142,v142\nM v[72:75],a[100:103],v[216:219],v[72:75]\nv_exp_f32 v143,v143\nv_cvt_pk_bf16_f32 v198,v148,v149\nM v[80:83],a[72:75],v[200:203],0\nbuffer_load_dwordx4 v240,s[44:47],s66 offen offset:2048 sc1 lds\nM v[80:83],a[104:107],v[216:219],v[80:83]\nv_exp_f32 v156,v156\n" \
"v_cvt_pk_bf16_f32 v199,v150,v151\nM v[88:91],a[76:79],v[200:203],0\nv_exp_f32 v157,v157\nM v[88:91],a[108:111],v[216:219],v[88:91]\nv_exp_f32 v158,v158\nv_cvt_pk_bf16_f32 v211,v178,v179\nM v[96:99],a[80:83],v[200:203],0\nbuffer_load_dwordx4 v240,s[44:47],s66 offen offset:3072 sc1 lds\n" \
"M v[96:99],a[112:115],v[216:219],v[96:99]\ns_add_u32 m0,s64,98304\nv_exp_f32 v159,v159\nM v[104:107],a[84:87],v[200:203],0\nv_exp_f32 v164,v164\nM v[104:107],a[116:119],v[216:219],v[104:107]\nv_exp_f32 v165,v165\nM v[112:115],a[88:91],v[200:203],0\n" \
"v_exp_f32 v166,v166\nM v[112:115],a[120:123],v[216:219],v[112:115]\nv_exp_f32 v167,v167\nM v[120:123],a[92:95],v[200:203],0\nv_exp_f32 v180,v180\nM v[120:123],a[124:127],v[216:219],v[120:123]\nv_exp_f32 v181,v181\nM a[200:203],a[208:211],v[200:203],0\n" \
"v_exp_f32 v182,v182\nv_cvt_pk_bf16_f32 v200,v136,v137\nM a[200:203],a[208:211],v[216:219],a[200:203]\nv_exp_f32 v183,v183\nM v[68:71],a[64:67],v[204:207],0\nv_exp_f32 v168,v168\nv_cvt_pk_bf16_f32 v201,v138,v139\nM v[68:71],a[96:99],v[220:223],v[68:71]\n" \
"v_exp_f32 v169,v169\nM v[76:79],a[68:71],v[204:207],0\nv_exp_f32 v170,v170\nv_cvt_pk_bf16_f32 v202,v152,v153\nM v[76:79],a[100:103],v[220:223],v[76:79]\nv_exp_f32 v171,v171\nM v[84:87],a[72:75],v[204:207],0\nv_cvt_pk_bf16_f32 v203,v154,v155\n" \
"buffer_load_dwordx4 v240,s[48:51],s66 offen offset:0 sc1 lds\nM v[84:87],a[104:107],v[220:223],v[84:87]\nM v[92:95],a[76:79],v[204:207],0\nM v[92:95],a[108:111],v[220:223],v[92:95]\nM v[100:103],a[80:83],v[204:207],0\nbuffer_load_dwordx4 v240,s[48:51],s66 offen offset:1024 sc1 lds\nM v[100:103],a[112:115],v[220:223],v[100:103]\nM v[108:111],a[84:87],v[204:207],0\n" \
"M v[108:111],a[116:119],v[220:223],v[108:111]\nM v[116:119],a[88:91],v[204:207],0\nbuffer_load_dwordx4 v240,s[48:51],s66 offen offset:2048 sc1 lds\nM v[116:119],a[120:123],v[220:223],v[116:119]\nM v[124:127],a[92:95],v[204:207],0\nM v[124:127],a[124:127],v[220:223],v[124:127]\nM a[204:207],a[208:211],v[204:207],0\nbuffer_load_dwordx4 v240,s[48:51],s66 offen offset:3072 sc1 lds\n" \
"M a[204:207],a[208:211],v[220:223],a[204:207]\ns_cmp_eq_u32 s62,0\ns_cbranch_scc1 .Ltrans\ns_branch .Ldual\n.Lpro1:\ns_barrier\nM v[64:67],a[0:3],a[128:131],0\nds_read_b128 a[64:67],v241 offset:0\n" \
"ds_read_b128 a[96:99],v241 offset:8192\nM v[64:67],a[4:7],a[132:135],v[64:67]\nds_read_b128 a[68:71],v241 offset:1024\ns_mov_b32 m0,s64\nv_mov_b32 v202,0\nM v[64:67],a[8:11],a[136:139],v[64:67]\nbuffer_load_dwordx4 v240,s[44:47],s36 offen offset:0 sc1 lds\nM v[64:67],a[12:15],a[140:143],v[64:67]\n" \
"ds_read_b128 a[100:103],v241 offset:9216\nds_read_b128 a[72:75],v241 offset:2048\nv_mov_b32 v203,0\nM v[68:71],a[12:15],a[156:159],0\nds_read_b128 a[12:15],v240 offset:35840\nM v[68:71],a[8:11],a[152:155],v[68:71]\nds_read_b128 a[8:11],v240 offset:34816\nM v[68:71],a[4:7],a[148:151],v[68:71]\n" \
"ds_read_b128 a[4:7],v240 offset:33792\nbuffer_load_dwordx4 v240,s[44:47],s36 offen offset:1024 sc1 lds\nM v[68:71],a[0:3],a[144:147],v[68:71]\nds_read_b128 a[0:3],v240 offset:32768\nv_max3_f32 v225,v64,v65,v66\nM v[72:75],a[16:19],a[128:131],0\nds_read_b128 a[104:107],v241 offset:10240\nM v[72:75],a[20:23],a[132:135],v[72:75]\n" \
"ds_read_b128 a[76:79],v241 offset:3072\nM v[72:75],a[24:27],a[136:139],v[72:75]\nbuffer_load_dwordx4 v240,s[44:47],s36 offen offset:2048 sc1 lds\nM v[72:75],a[28:31],a[140:143],v[72:75]\nv_max3_f32 v229,v68,v69,v70\nds_read_b128 a[108:111],v241 offset:11264\nds_read_b128 a[80:83],v241 offset:4096\nM v[76:79],a[28:31],a[156:159],0\n" \
"ds_read_b128 a[28:31],v240 offset:39936\nM v[76:79],a[24:27],a[152:155],v[76:79]\nds_read_b128 a[24:27],v240 offset:38912\nM v[76:79],a[20:23],a[148:151],v[76:79]\nds_read_b128 a[20:23],v240 offset:37888\nv_max3_f32 v225,v225,v67,v72\nv_max3_f32 v225,v225,v73,v74\nM v[76:79],a[16:19],a[144:147],v[76:79]\n" \
"ds_read_b128 a[16:19],v240 offset:36864\nv_max_f32 v225,v225,v75\nv_cndmask_b32 v225,v224,v225,s[90:91]\nM v[80:83],a[32:35],a[128:131],0\nbuffer_load_dwordx4 v240,s[44:47],s36 offen offset:3072 sc1 lds\nM v[80:83],a[36:39],a[132:135],v[80:83]\nds_read_b128 a[112:115],v241 offset:12288\nds_read_b128 a[84:87],v241 offset:5120\n" \
"v_max3_f32 v229,v229,v71,v76\nM v[80:83],a[40:43],a[136:139],v[80:83]\nv_max3_f32 v229,v229,v77,v78\nv_max_f32 v229,v229,v79\nv_cndmask_b32 v229,v228,v229,s[90:91]\nM v[80:83],a[44:47],a[140:143],v[80:83]\nds_read_b128 a[116:119],v241 offset:13312\nv_permlane16_swap_b32 v225,v229\n" \
"M v[84:87],a[44:47],a[156:159],0\nv_max_f32 v225,v225,v229\nv_mov_b32 v229,v225\nds_read_b128 a[44:47],v240 offset:44032\nM v[84:87],a[40:43],a[152:155],v[84:87]\nv_permlane32_swap_b32 v225,v229\nv_max_f32 v225,v225,v229\nM v[84:87],a[36:39],a[148:151],v[84:87]\n" \
"v_mov_b32 v229,v225\nds_read_b128 a[36:39],v240 offset:41984\nds_read_b128 a[40:43],v240 offset:43008\nM v[84:87],a[32:35],a[144:147],v[84:87]\nv_permlane16_swap_b32 v225,v229\nv_sub_f32 v224,0,v225\nv_sub_f32 v226,0,v225\nM v[92:95],a[60:63],a[156:159],0\n" \
"v_sub_f32 v227,0,v225\nv_sub_f32 v225,0,v225\nM v[92:95],a[56:59],a[152:155],v[92:95]\nv_sub_f32 v228,0,v229\nv_sub_f32 v230,0,v229\nv_sub_f32 v231,0,v229\nM v[92:95],a[52:55],a[148:151],v[92:95]\nv_sub_f32 v229,0,v229\n" \
"ds_read_b128 a[32:35],v240 offset:40960\nds_read_b128 a[52:55],v240 offset:46080\nM v[92:95],a[48:51],a[144:147],v[92:95]\nds_read_b128 a[48:51],v240 offset:45056\ns_waitcnt lgkmcnt(7)\nM v[136:139],a[0:3],a[160:163],0\nds_read_b128 a[56:59],v240 offset:47104\nv_add_f32 v64,v64,v224\n" \
"v_add_f32 v65,v65,v224\nM v[136:139],a[4:7],a[164:167],v[136:139]\nds_read_b128 a[60:63],v240 offset:48128\nv_add_f32 v66,v66,v224\nM v[136:139],a[8:11],a[168:171],v[136:139]\nv_add_f32 v67,v67,v224\nv_exp_f32 v64,v64\nM v[136:139],a[12:15],a[172:175],v[136:139]\n" \
"v_exp_f32 v65,v65\nv_cndmask_b32 v80,v80,v252,s[82:83]\nM v[140:143],a[12:15],a[188:191],0\nv_cndmask_b32 v81,v81,v252,s[84:85]\nv_add_f32 v72,v72,v224\nM v[140:143],a[8:11],a[184:187],v[140:143]\nv_exp_f32 v66,v66\nv_add_f32 v73,v73,v224\n" \
"M v[140:143],a[4:7],a[180:183],v[140:143]\nv_exp_f32 v67,v67\nv_add_f32 v74,v74,v224\nM v[140:143],a[0:3],a[176:179],v[140:143]\nv_add_f32 v75,v75,v224\nv_cndmask_b32 v82,v82,v252,s[86:87]\nM v[156:159],a[28:31],a[188:191],0\nv_cndmask_b32 v83,v83,v252,s[88:89]\n" \
"v_add_f32 v80,v80,v224\nv_add_f32 v81,v81,v224\nM v[156:159],a[24:27],a[184:187],v[156:159]\nv_exp_f32 v72,v72\nv_add_f32 v82,v82,v224\nM v[156:159],a[20:23],a[180:183],v[156:159]\nv_exp_f32 v73,v73\nM v[156:159],a[16:19],a[176:179],v[156:159]\n" \
"v_exp_f32 v74,v74\nv_add_f32 v83,v83,v224\nM v[128:131],a[0:3],a[128:131],v[224:227]\nv_exp_f32 v75,v75\nM v[128:131],a[4:7],a[132:135],v[128:131]\nv_exp_f32 v80,v80\nv_cvt_pk_bf16_f32 v64,v64,v65\nM v[128:131],a[8:11],a[136:139],v[128:131]\n" \
"v_exp_f32 v81,v81\nv_cvt_pk_bf16_f32 v65,v66,v67\nM v[128:131],a[12:15],a[140:143],v[128:131]\nv_exp_f32 v82,v82\nM v[132:135],a[12:15],a[156:159],v[228:231]\nv_exp_f32 v83,v83\nv_cvt_pk_bf16_f32 v66,v72,v73\nM v[132:135],a[8:11],a[152:155],v[132:135]\n" \
"v_cvt_pk_bf16_f32 v67,v74,v75\nv_cvt_pk_bf16_f32 v80,v80,v81\nv_cvt_pk_bf16_f32 v81,v82,v83\nM v[132:135],a[4:7],a[148:151],v[132:135]\nv_mov_b32 v82,0\nv_mov_b32 v83,0\nM v[132:135],a[0:3],a[144:147],v[132:135]\nds_read_b128 a[88:91],v241 offset:6144\n" \
"ds_read_b128 a[120:123],v241 offset:14336\nv_add_f32 v68,v68,v228\nM v[144:147],a[16:19],a[128:131],v[224:227]\nds_read_b128 a[92:95],v241 offset:7168\nv_add_f32 v69,v69,v228\nv_add_f32 v70,v70,v228\nM v[144:147],a[20:23],a[132:135],v[144:147]\nds_read_b128 a[124:127],v241 offset:15360\n" \
"v_add_f32 v71,v71,v228\nM v[144:147],a[24:27],a[136:139],v[144:147]\nv_exp_f32 v68,v68\nv_add_f32 v76,v76,v228\nM v[144:147],a[28:31],a[140:143],v[144:147]\nv_exp_f32 v69,v69\nM v[148:151],a[28:31],a[156:159],v[228:231]\nv_exp_f32 v70,v70\n" \
"v_add_f32 v77,v77,v228\nM v[148:151],a[24:27],a[152:155],v[148:151]\nv_exp_f32 v71,v71\nv_add_f32 v78,v78,v228\nM v[148:151],a[20:23],a[148:151],v[148:151]\nv_add_f32 v79,v79,v228\nv_add_f32 v84,v84,v228\nM v[148:151],a[16:19],a[144:147],v[148:151]\n" \
"v_add_f32 v85,v85,v228\nv_cndmask_b32 v92,v92,v252,s[82:83]\ns_waitcnt lgkmcnt(4)\nM v[160:163],a[32:35],a[128:131],v[224:227]\nv_exp_f32 v76,v76\nv_cndmask_b32 v93,v93,v252,s[84:85]\nM v[160:163],a[36:39],a[132:135],v[160:163]\nv_exp_f32 v77,v77\n" \
"M v[160:163],a[40:43],a[136:139],v[160:163]\nv_exp_f32 v78,v78\nv_cndmask_b32 v94,v94,v252,s[86:87]\nM v[160:163],a[44:47],a[140:143],v[160:163]\nv_exp_f32 v79,v79\nM v[164:167],a[44:47],a[156:159],v[228:231]\nv_cndmask_b32 v95,v95,v252,s[88:89]\nv_add_f32 v86,v86,v228\n" \
"v_add_f32 v87,v87,v228\nM v[164:167],a[40:43],a[152:155],v[164:167]\nv_exp_f32 v84,v84\nv_cvt_pk_bf16_f32 v68,v68,v69\nM v[164:167],a[36:39],a[148:151],v[164:167]\nv_exp_f32 v85,v85\nM v[164:167],a[32:35],a[144:147],v[164:167]\nv_exp_f32 v86,v86\n" \
"v_add_f32 v92,v92,v228\nM v[176:179],a[48:51],a[128:131],v[224:227]\nv_exp_f32 v87,v87\nv_add_f32 v93,v93,v228\nM v[176:179],a[52:55],a[132:135],v[176:179]\nv_add_f32 v94,v94,v228\nv_add_f32 v95,v95,v228\nM v[176:179],a[56:59],a[136:139],v[176:179]\n" \
"buffer_load_dword v248,v251,s[52:55],s80 offen sc1\nv_cvt_pk_bf16_f32 v69,v70,v71\nM v[176:179],a[60:63],a[140:143],v[176:179]\nv_exp_f32 v92,v92\nv_cvt_pk_bf16_f32 v70,v76,v77\nM v[180:183],a[60:63],a[156:159],v[228:231]\nv_exp_f32 v93,v93\nM v[180:183],a[56:59],a[152:155],v[180:183]\n" \
"v_exp_f32 v94,v94\nv_cvt_pk_bf16_f32 v71,v78,v79\nM v[180:183],a[52:55],a[148:151],v[180:183]\nv_exp_f32 v95,v95\nM v[180:183],a[48:51],a[144:147],v[180:183]\nv_cvt_pk_bf16_f32 v84,v84,v85\nv_cvt_pk_bf16_f32 v85,v86,v87\nv_cvt_pk_bf16_f32 v86,v92,v93\n" \
"M v[0:3],a[64:67],v[64:67],0\nv_cvt_pk_bf16_f32 v87,v94,v95\nds_read_b128 a[0:3],v240 offset:49152\nds_read_b128 a[4:7],v240 offset:50176\nM v[0:3],a[96:99],v[80:83],v[0:3]\nds_read_b128 a[8:11],v240 offset:51200\nv_max3_f32 v237,v140,v141,v142\nM v[8:11],a[68:71],v[64:67],0\n" \
"ds_read_b128 a[12:15],v240 offset:52224\nv_exp_f32 v128,v128\nM v[8:11],a[100:103],v[80:83],v[8:11]\nds_read_b128 a[16:19],v240 offset:53248\nv_exp_f32 v129,v129\nM v[16:19],a[72:75],v[64:67],0\nv_exp_f32 v130,v130\nM v[16:19],a[104:107],v[80:83],v[16:19]\n" \
"v_exp_f32 v131,v131\nds_read_b128 a[20:23],v240 offset:54272\nM v[24:27],a[76:79],v[64:67],0\nv_cvt_pk_bf16_f32 v192,v128,v129\nv_cvt_pk_bf16_f32 v193,v130,v131\nds_read_b128 a[24:27],v240 offset:55296\nM v[24:27],a[108:111],v[80:83],v[24:27]\nds_read_b128 a[28:31],v240 offset:56320\n" \
"v_max3_f32 v237,v237,v143,v156\nM v[32:35],a[80:83],v[64:67],0\nv_exp_f32 v132,v132\nv_max3_f32 v233,v136,v137,v138\nM v[32:35],a[112:115],v[80:83],v[32:35]\nv_exp_f32 v133,v133\nM v[40:43],a[84:87],v[64:67],0\nv_exp_f32 v134,v134\n" \
"v_cvt_pk_bf16_f32 v196,v132,v133\nM v[40:43],a[116:119],v[80:83],v[40:43]\nv_exp_f32 v135,v135\ns_waitcnt lgkmcnt(4)\nM v[48:51],a[88:91],v[64:67],0\nv_cvt_pk_bf16_f32 v197,v134,v135\nv_max3_f32 v237,v237,v157,v158\nM v[48:51],a[120:123],v[80:83],v[48:51]\n" \
"v_exp_f32 v144,v144\nv_max_f32 v233,v233,v139\nM v[56:59],a[92:95],v[64:67],0\nv_exp_f32 v145,v145\nv_max_f32 v237,v237,v159\nM v[56:59],a[124:127],v[80:83],v[56:59]\nv_exp_f32 v146,v146\nM a[192:195],a[208:211],v[64:67],0\n" \
"v_exp_f32 v147,v147\nv_cvt_pk_bf16_f32 v194,v144,v145\nM a[192:195],a[208:211],v[80:83],a[192:195]\nv_cvt_pk_bf16_f32 v195,v146,v147\nv_cndmask_b32 v233,v232,v233,s[90:91]\nM v[4:7],a[64:67],v[68:71],0\nv_cndmask_b32 v237,v236,v237,s[90:91]\nv_exp_f32 v148,v148\n" \
"M v[4:7],a[96:99],v[84:87],v[4:7]\nv_permlane16_swap_b32 v233,v237\nv_max_f32 v233,v233,v237\nv_mov_b32 v237,v233\nM v[12:15],a[68:71],v[68:71],0\nv_exp_f32 v149,v149\nM v[12:15],a[100:103],v[84:87],v[12:15]\nv_exp_f32 v150,v150\n" \
"v_permlane32_swap_b32 v233,v237\nM v[20:23],a[72:75],v[68:71],0\nv_exp_f32 v151,v151\nv_cvt_pk_bf16_f32 v198,v148,v149\nM v[20:23],a[104:107],v[84:87],v[20:23]\nv_cvt_pk_bf16_f32 v199,v150,v151\nv_max_f32 v233,v233,v237\nM v[28:31],a[76:79],v[68:71],0\n" \
"v_mov_b32 v237,v233\nv_cndmask_b32 v136,v136,v252,s[82:83]\nv_cndmask_b32 v137,v137,v252,s[84:85]\nM v[28:31],a[108:111],v[84:87],v[28:31]\nv_permlane16_swap_b32 v233,v237\nv_cndmask_b32 v138,v138,v252,s[86:87]\nv_cndmask_b32 v139,v139,v252,s[88:89]\nM v[36:39],a[80:83],v[68:71],0\n" \
"v_sub_f32 v232,0,v233\nv_sub_f32 v234,0,v233\nM v[36:39],a[112:115],v[84:87],v[36:39]\nv_sub_f32 v235,0,v233\nv_add_f32 v136,v136,v232\nv_add_f32 v137,v137,v232\nM v[44:47],a[84:87],v[68:71],0\nv_add_f32 v138,v138,v232\n" \
"v_add_f32 v139,v139,v232\nM v[44:47],a[116:119],v[84:87],v[44:47]\nv_sub_f32 v233,0,v233\nv_exp_f32 v136,v136\nM v[52:55],a[88:91],v[68:71],0\nv_exp_f32 v137,v137\nv_sub_f32 v236,0,v237\nM v[52:55],a[120:123],v[84:87],v[52:55]\n" \
"v_exp_f32 v138,v138\nM v[60:63],a[92:95],v[68:71],0\nv_exp_f32 v139,v139\nv_cvt_pk_bf16_f32 v200,v136,v137\nM v[60:63],a[124:127],v[84:87],v[60:63]\nv_cvt_pk_bf16_f32 v201,v138,v139\nv_sub_f32 v238,0,v237\nv_sub_f32 v239,0,v237\n" \
"M a[196:199],a[208:211],v[68:71],0\nv_add_f32 v140,v140,v236\nv_add_f32 v141,v141,v236\nM a[196:199],a[208:211],v[84:87],a[196:199]\nv_add_f32 v142,v142,v236\nv_add_f32 v143,v143,v236\nv_sub_f32 v237,0,v237\nM v[128:131],a[0:3],a[128:131],v[224:227]\n" \
"v_exp_f32 v140,v140\nds_read_b128 a[32:35],v240 offset:57344\nM v[128:131],a[4:7],a[132:135],v[128:131]\nv_exp_f32 v141,v141\nM v[128:131],a[8:11],a[136:139],v[128:131]\nv_exp_f32 v142,v142\nv_cvt_pk_bf16_f32 v204,v140,v141\nM v[128:131],a[12:15],a[140:143],v[128:131]\n" \
"v_exp_f32 v143,v143\nM v[132:135],a[12:15],a[156:159],v[228:231]\nv_cvt_pk_bf16_f32 v205,v142,v143\nds_read_b128 a[36:39],v240 offset:58368\nds_read_b128 a[40:43],v240 offset:59392\nM v[132:135],a[8:11],a[152:155],v[132:135]\nds_read_b128 a[44:47],v240 offset:60416\nv_cndmask_b32 v156,v156,v252,s[82:83]\n" \
"v_cndmask_b32 v157,v157,v252,s[84:85]\nM v[132:135],a[4:7],a[148:151],v[132:135]\nv_cndmask_b32 v158,v158,v252,s[86:87]\nv_cndmask_b32 v159,v159,v252,s[88:89]\nM v[132:135],a[0:3],a[144:147],v[132:135]\nv_add_f32 v156,v156,v236\nv_add_f32 v157,v157,v236\ns_waitcnt lgkmcnt(4)\n" \
"M v[144:147],a[16:19],a[128:131],v[224:227]\ns_waitcnt vmcnt(1)\ns_barrier\nv_add_f32 v158,v158,v236\nM v[144:147],a[20:23],a[132:135],v[144:147]\nv_add_f32 v159,v159,v236\nds_read_b128 a[48:51],v240 offset:61440\nM v[144:147],a[24:27],a[136:139],v[144:147]\n" \
"v_exp_f32 v156,v156\nds_read_b128 a[52:55],v240 offset:62464\nM v[144:147],a[28:31],a[140:143],v[144:147]\nv_exp_f32 v157,v157\nM v[148:151],a[28:31],a[156:159],v[228:231]\nv_exp_f32 v158,v158\nv_cvt_pk_bf16_f32 v206,v156,v157\nM v[148:151],a[24:27],a[152:155],v[148:151]\n" \
"v_exp_f32 v159,v159\nds_read_b128 a[56:59],v240 offset:63488\nM v[148:151],a[20:23],a[148:151],v[148:151]\nv_cvt_pk_bf16_f32 v207,v158,v159\nds_read_b128 a[60:63],v240 offset:64512\nM v[148:151],a[16:19],a[144:147],v[148:151]\nv_exp_f32 v160,v160\nds_read_b128 a[64:67],v241 offset:32768\n" \
"M v[136:139],a[0:3],a[160:163],v[232:235]\nv_exp_f32 v161,v161\nds_read_b128 a[96:99],v241 offset:40960\nM v[136:139],a[4:7],a[164:167],v[136:139]\nv_exp_f32 v162,v162\nM v[136:139],a[8:11],a[168:171],v[136:139]\nv_exp_f32 v163,v163\nv_cvt_pk_bf16_f32 v208,v160,v161\n" \
"M v[136:139],a[12:15],a[172:175],v[136:139]\nv_cvt_pk_bf16_f32 v209,v162,v163\nv_exp_f32 v164,v164\nM v[140:143],a[12:15],a[188:191],v[236:239]\nv_exp_f32 v165,v165\nM v[140:143],a[8:11],a[184:187],v[140:143]\nv_exp_f32 v166,v166\nv_cvt_pk_bf16_f32 v212,v164,v165\n" \
"M v[140:143],a[4:7],a[180:183],v[140:143]\nv_exp_f32 v167,v167\nM v[140:143],a[0:3],a[176:179],v[140:143]\nv_cvt_pk_bf16_f32 v213,v166,v167\nv_exp_f32 v176,v176\nM v[152:155],a[16:19],a[160:163],v[232:235]\nv_exp_f32 v177,v177\nds_read_b128 a[68:71],v241 offset:33792\n" \
"M v[152:155],a[20:23],a[164:167],v[152:155]\nv_exp_f32 v178,v178\nM v[152:155],a[24:27],a[168:171],v[152:155]\nv_exp_f32 v179,v179\nv_cvt_pk_bf16_f32 v210,v176,v177\nM v[152:155],a[28:31],a[172:175],v[152:155]\nv_cvt_pk_bf16_f32 v211,v178,v179\nv_exp_f32 v180,v180\n" \
"M v[156:159],a[28:31],a[188:191],v[236:239]\nv_exp_f32 v181,v181\nM v[156:159],a[24:27],a[184:187],v[156:159]\nv_exp_f32 v182,v182\nv_cvt_pk_bf16_f32 v214,v180,v181\nM v[156:159],a[20:23],a[180:183],v[156:159]\nv_exp_f32 v183,v183\nds_read_b128 a[100:103],v241 offset:41984\n" \
"M v[156:159],a[16:19],a[176:179],v[156:159]\nv_cvt_pk_bf16_f32 v215,v182,v183\ns_waitcnt lgkmcnt(2)\nM v[160:163],a[32:35],a[128:131],v[224:227]\nds_read_b128 a[72:75],v241 offset:34816\nds_read_b128 a[104:107],v241 offset:43008\ns_waitcnt vmcnt(0)\nv_cmp_eq_u32 vcc,0,v248\n" \
"M v[160:163],a[36:39],a[132:135],v[160:163]\nds_read_b128 a[76:79],v241 offset:35840\ns_cbranch_vccnz .Lps3\n.Lpb3:\nM v[160:163],a[40:43],a[136:139],v[160:163]\nds_read_b128 a[108:111],v241 offset:44032\nv_exp_f32 v128,v128\nM v[160:163],a[44:47],a[140:143],v[160:163]\n" \
"ds_read_b128 a[80:83],v241 offset:36864\nv_exp_f32 v129,v129\nM v[164:167],a[44:47],a[156:159],v[228:231]\nds_read_b128 a[112:115],v241 offset:45056\ns_add_u32 m0,s64,65536\nM v[164:167],a[40:43],a[152:155],v[164:167]\nds_read_b128 a[84:87],v241 offset:37888\nbuffer_load_dwordx4 v240,s[48:51],s36 offen offset:0 sc1 lds\n" \
"M v[164:167],a[36:39],a[148:151],v[164:167]\nds_read_b128 a[116:119],v241 offset:46080\nv_exp_f32 v130,v130\nM v[164:167],a[32:35],a[144:147],v[164:167]\nv_exp_f32 v131,v131\nM v[176:179],a[48:51],a[128:131],v[224:227]\nds_read_b128 a[88:91],v241 offset:38912\nv_exp_f32 v132,v132\n" \
"M v[176:179],a[52:55],a[132:135],v[176:179]\nds_read_b128 a[120:123],v241 offset:47104\nds_read_b128 a[92:95],v241 offset:39936\nM v[176:179],a[56:59],a[136:139],v[176:179]\nds_read_b128 a[124:127],v241 offset:48128\nbuffer_load_dwordx4 v240,s[48:51],s36 offen offset:1024 sc1 lds\nM v[176:179],a[60:63],a[140:143],v[176:179]\nds_read_b128 a[0:3],v240 offset:0\n" \
"v_exp_f32 v133,v133\nM v[180:183],a[60:63],a[156:159],v[228:231]\nv_exp_f32 v134,v134\nM v[180:183],a[56:59],a[152:155],v[180:183]\nds_read_b128 a[4:7],v240 offset:1024\nv_exp_f32 v135,v135\nM v[180:183],a[52:55],a[148:151],v[180:183]\nds_read_b128 a[8:11],v240 offset:2048\n" \
"buffer_load_dwordx4 v240,s[48:51],s36 offen offset:2048 sc1 lds\nM v[180:183],a[48:51],a[144:147],v[180:183]\nds_read_b128 a[12:15],v240 offset:3072\nds_read_b128 a[16:19],v240 offset:4096\nM v[168:171],a[32:35],a[160:163],v[232:235]\nds_read_b128 a[20:23],v240 offset:5120\nv_exp_f32 v144,v144\nM v[168:171],a[36:39],a[164:167],v[168:171]\n" \
"ds_read_b128 a[24:27],v240 offset:6144\nv_exp_f32 v145,v145\nM v[168:171],a[40:43],a[168:171],v[168:171]\nbuffer_load_dwordx4 v240,s[48:51],s36 offen offset:3072 sc1 lds\nM v[168:171],a[44:47],a[172:175],v[168:171]\nds_read_b128 a[28:31],v240 offset:7168\nv_exp_f32 v146,v146\nM v[172:175],a[44:47],a[188:191],v[236:239]\n" \
"v_exp_f32 v147,v147\nM v[172:175],a[40:43],a[184:187],v[172:175]\ns_add_u32 m0,s64,16384\nv_exp_f32 v148,v148\nM v[172:175],a[36:39],a[180:183],v[172:175]\nbuffer_load_dwordx4 v240,s[44:47],s65 offen offset:0 sc1 lds\nM v[172:175],a[32:35],a[176:179],v[172:175]\nds_read_b128 a[32:35],v240 offset:8192\n" \
"ds_read_b128 a[36:39],v240 offset:9216\nM v[184:187],a[48:51],a[160:163],v[232:235]\nds_read_b128 a[40:43],v240 offset:10240\nv_exp_f32 v149,v149\nM v[184:187],a[52:55],a[164:167],v[184:187]\nds_read_b128 a[44:47],v240 offset:11264\nv_exp_f32 v150,v150\nM v[184:187],a[56:59],a[168:171],v[184:187]\n" \
"buffer_load_dwordx4 v240,s[44:47],s65 offen offset:1024 sc1 lds\nM v[184:187],a[60:63],a[172:175],v[184:187]\nv_exp_f32 v151,v151\nM v[188:191],a[60:63],a[188:191],v[236:239]\nv_exp_f32 v136,v136\nM v[188:191],a[56:59],a[184:187],v[188:191]\nv_exp_f32 v137,v137\nM v[188:191],a[52:55],a[180:183],v[188:191]\n" \
"buffer_load_dwordx4 v240,s[44:47],s65 offen offset:2048 sc1 lds\nM v[188:191],a[48:51],a[176:179],v[188:191]\nds_read_b128 a[48:51],v240 offset:12288\nds_read_b128 a[52:55],v240 offset:13312\nM v[0:3],a[64:67],v[192:195],v[0:3]\nds_read_b128 a[56:59],v240 offset:14336\nv_exp_f32 v138,v138\nM v[0:3],a[96:99],v[208:211],v[0:3]\n" \
"v_exp_f32 v139,v139\ns_waitcnt lgkmcnt(4)\nM v[8:11],a[68:71],v[192:195],v[8:11]\nbuffer_load_dwordx4 v240,s[44:47],s65 offen offset:3072 sc1 lds\nM v[8:11],a[100:103],v[208:211],v[8:11]\ns_add_u32 m0,s64,81920\nv_exp_f32 v152,v152\nM v[16:19],a[72:75],v[192:195],v[16:19]\n" \
"ds_read_b128 a[60:63],v240 offset:15360\nv_exp_f32 v153,v153\nM v[16:19],a[104:107],v[208:211],v[16:19]\nv_exp_f32 v154,v154\nM v[24:27],a[76:79],v[192:195],v[24:27]\nbuffer_load_dwordx4 v240,s[48:51],s65 offen offset:0 sc1 lds\nM v[24:27],a[108:111],v[208:211],v[24:27]\nv_exp_f32 v155,v155\n" \
"M v[32:35],a[80:83],v[192:195],v[32:35]\ns_barrier\nv_exp_f32 v160,v160\nM v[32:35],a[112:115],v[208:211],v[32:35]\nv_exp_f32 v161,v161\nM v[40:43],a[84:87],v[192:195],v[40:43]\nbuffer_load_dwordx4 v240,s[48:51],s65 offen offset:1024 sc1 lds\nM v[40:43],a[116:119],v[208:211],v[40:43]\n" \
"v_exp_f32 v162,v162\nM v[48:51],a[88:91],v[192:195],v[48:51]\nv_exp_f32 v163,v163\nM v[48:51],a[120:123],v[208:211],v[48:51]\nv_exp_f32 v176,v176\nM v[56:59],a[92:95],v[192:195],v[56:59]\nbuffer_load_dwordx4 v240,s[48:51],s65 offen offset:2048 sc1 lds\nM v[56:59],a[124:127],v[208:211],v[56:59]\n" \
"v_exp_f32 v177,v177\nM a[192:195],a[208:211],v[192:195],a[192:195]\nv_exp_f32 v178,v178\nM a[192:195],a[208:211],v[208:211],a[192:195]\nv_exp_f32 v179,v179\nv_cvt_pk_bf16_f32 v192,v128,v129\nM v[4:7],a[64:67],v[196:199],v[4:7]\nbuffer_load_dwordx4 v240,s[48:51],s65 offen offset:3072 sc1 lds\n" \
"M v[4:7],a[96:99],v[212:215],v[4:7]\ns_add_u32 m0,s64,32768\nv_exp_f32 v140,v140\nM v[12:15],a[68:71],v[196:199],v[12:15]\nv_exp_f32 v141,v141\nv_cvt_pk_bf16_f32 v193,v130,v131\nM v[12:15],a[100:103],v[212:215],v[12:15]\nv_exp_f32 v142,v142\n" \
"M v[20:23],a[72:75],v[196:199],v[20:23]\nbuffer_load_dwordx4 v240,s[44:47],s66 offen offset:0 sc1 lds\nv_cvt_pk_bf16_f32 v194,v144,v145\nM v[20:23],a[104:107],v[212:215],v[20:23]\nv_exp_f32 v143,v143\nv_cvt_pk_bf16_f32 v195,v146,v147\nM v[28:31],a[76:79],v[196:199],v[28:31]\nv_exp_f32 v156,v156\n" \
"M v[28:31],a[108:111],v[212:215],v[28:31]\nv_exp_f32 v157,v157\nv_cvt_pk_bf16_f32 v208,v160,v161\nM v[36:39],a[80:83],v[196:199],v[36:39]\nbuffer_load_dwordx4 v240,s[44:47],s66 offen offset:1024 sc1 lds\nM v[36:39],a[112:115],v[212:215],v[36:39]\nv_exp_f32 v158,v158\nv_cvt_pk_bf16_f32 v209,v162,v163\n" \
"M v[44:47],a[84:87],v[196:199],v[44:47]\nv_exp_f32 v159,v159\nv_cvt_pk_bf16_f32 v210,v176,v177\nM v[44:47],a[116:119],v[212:215],v[44:47]\nv_exp_f32 v164,v164\nM v[52:55],a[88:91],v[196:199],v[52:55]\nbuffer_load_dwordx4 v240,s[44:47],s66 offen offset:2048 sc1 lds\nv_cvt_pk_bf16_f32 v211,v178,v179\n" \
"M v[52:55],a[120:123],v[212:215],v[52:55]\nv_exp_f32 v165,v165\nM v[60:63],a[92:95],v[196:199],v[60:63]\nv_exp_f32 v166,v166\nM v[60:63],a[124:127],v[212:215],v[60:63]\nv_exp_f32 v167,v167\nM a[196:199],a[208:211],v[196:199],a[196:199]\nbuffer_load_dwordx4 v240,s[44:47],s66 offen offset:3072 sc1 lds\n" \
"s_add_u32 m0,s64,98304\nM a[196:199],a[208:211],v[212:215],a[196:199]\nv_exp_f32 v180,v180\nM v[64:67],a[64:67],v[200:203],0\nv_exp_f32 v181,v181\nv_cvt_pk_bf16_f32 v196,v132,v133\nM v[72:75],a[68:71],v[200:203],0\nv_exp_f32 v182,v182\n" \
"M v[80:83],a[72:75],v[200:203],0\nv_exp_f32 v183,v183\nv_cvt_pk_bf16_f32 v197,v134,v135\nM v[88:91],a[76:79],v[200:203],0\nv_exp_f32 v168,v168\nv_cvt_pk_bf16_f32 v198,v148,v149\nM v[96:99],a[80:83],v[200:203],0\nv_exp_f32 v169,v169\n" \
"M v[104:107],a[84:87],v[200:203],0\nv_exp_f32 v170,v170\nv_cvt_pk_bf16_f32 v199,v150,v151\nM v[112:115],a[88:91],v[200:203],0\nv_exp_f32 v171,v171\nM v[120:123],a[92:95],v[200:203],0\nbuffer_load_dwordx4 v240,s[48:51],s66 offen offset:0 sc1 lds\nM a[200:203],a[208:211],v[200:203],0\n" \
"v_cvt_pk_bf16_f32 v200,v136,v137\nv_cvt_pk_bf16_f32 v201,v138,v139\nv_cvt_pk_bf16_f32 v202,v152,v153\nM v[68:71],a[64:67],v[204:207],0\nv_cvt_pk_bf16_f32 v203,v154,v155\nM v[76:79],a[68:71],v[204:207],0\nM v[84:87],a[72:75],v[204:207],0\nbuffer_load_dwordx4 v240,s[48:51],s66 offen offset:1024 sc1 lds\n" \
"M v[92:95],a[76:79],v[204:207],0\nM v[100:103],a[80:83],v[204:207],0\nM v[108:111],a[84:87],v[204:207],0\nM v[116:119],a[88:91],v[204:207],0\nbuffer_load_dwordx4 v240,s[48:51],s66 offen offset:2048 sc1 lds\nM v[124:127],a[92:95],v[204:207],0\nbuffer_load_dwordx4 v240,s[48:51],s66 offen offset:3072 sc1 lds\nM a[204:207],a[208:211],v[204:207],0\n" \
"s_cmp_eq_u32 s62,0\ns_cbranch_scc1 .Ltrans\ns_branch .Ldual\n.Lp23:\ns_cmp_eq_u32 s7,2\ns_cbranch_scc0 .Lpro3\n.Lpro2:\ns_barrier\n" \
"M v[64:67],a[0:3],a[128:131],0\nds_read_b128 a[64:67],v241 offset:0\nds_read_b128 a[96:99],v241 offset:8192\nM v[64:67],a[4:7],a[132:135],v[64:67]\nds_read_b128 a[68:71],v241 offset:1024\ns_mov_b32 m0,s64\nv_mov_b32 v250,139264\nM v[64:67],a[8:11],a[136:139],v[64:67]\n" \
"ds_read_b128 a[100:103],v241 offset:9216\nbuffer_load_dwordx4 v240,s[44:47],s36 offen offset:0 sc1 lds\nM v[64:67],a[12:15],a[140:143],v[64:67]\nds_read_b128 a[72:75],v241 offset:2048\nv_mov_b32 v184,0xff800000\nv_mov_b32 v185,0xff800000\nM v[68:71],a[12:15],a[156:159],0\nds_read_b128 a[12:15],v240 offset:19456\n" \
"v_mov_b32 v186,0xff800000\nv_mov_b32 v187,0xff800000\nM v[68:71],a[8:11],a[152:155],v[68:71]\nds_read_b128 a[8:11],v240 offset:18432\nv_max3_f32 v225,v64,v65,v66\nM v[68:71],a[4:7],a[148:151],v[68:71]\nds_read_b128 a[4:7],v240 offset:17408\nbuffer_load_dwordx4 v240,s[44:47],s36 offen offset:1024 sc1 lds\n" \
"M v[68:71],a[0:3],a[144:147],v[68:71]\nds_read_b128 a[0:3],v240 offset:16384\nM v[72:75],a[16:19],a[128:131],0\nds_read_b128 a[104:107],v241 offset:10240\nM v[72:75],a[20:23],a[132:135],v[72:75]\nds_read_b128 a[76:79],v241 offset:3072\nM v[72:75],a[24:27],a[136:139],v[72:75]\nds_read_b128 a[108:111],v241 offset:11264\n" \
"buffer_load_dwordx4 v240,s[44:47],s36 offen offset:2048 sc1 lds\nM v[72:75],a[28:31],a[140:143],v[72:75]\nv_max3_f32 v229,v68,v69,v70\nds_write_b128 v250,a[244:247]\nM v[76:79],a[28:31],a[156:159],0\nds_read_b128 a[28:31],v240 offset:23552\nM v[76:79],a[24:27],a[152:155],v[76:79]\nds_read_b128 a[24:27],v240 offset:22528\n" \
"M v[76:79],a[20:23],a[148:151],v[76:79]\nds_read_b128 a[20:23],v240 offset:21504\nv_max3_f32 v225,v225,v67,v72\nv_max3_f32 v225,v225,v73,v74\nM v[76:79],a[16:19],a[144:147],v[76:79]\nv_max_f32 v225,v225,v75\nv_cndmask_b32 v225,v224,v225,s[90:91]\nds_read_b128 a[16:19],v240 offset:20480\n" \
"M v[80:83],a[32:35],a[128:131],0\nds_read_b128 a[80:83],v241 offset:4096\nbuffer_load_dwordx4 v240,s[44:47],s36 offen offset:3072 sc1 lds\nM v[80:83],a[36:39],a[132:135],v[80:83]\nds_read_b128 a[112:115],v241 offset:12288\nv_max3_f32 v229,v229,v71,v76\nv_max3_f32 v229,v229,v77,v78\nM v[80:83],a[40:43],a[136:139],v[80:83]\n" \
"v_max_f32 v229,v229,v79\nv_cndmask_b32 v229,v228,v229,s[90:91]\nds_read_b128 a[84:87],v241 offset:5120\nM v[80:83],a[44:47],a[140:143],v[80:83]\nv_permlane16_swap_b32 v225,v229\nv_max_f32 v225,v225,v229\nv_mov_b32 v229,v225\nM v[84:87],a[44:47],a[156:159],0\n" \
"ds_read_b128 a[116:119],v241 offset:13312\nv_permlane32_swap_b32 v225,v229\nv_max_f32 v225,v225,v229\nM v[84:87],a[40:43],a[152:155],v[84:87]\nv_mov_b32 v229,v225\nds_read_b128 a[88:91],v241 offset:6144\nds_read_b128 a[120:123],v241 offset:14336\nM v[84:87],a[36:39],a[148:151],v[84:87]\n" \
"v_permlane16_swap_b32 v225,v229\nv_sub_f32 v224,0,v225\nv_sub_f32 v226,0,v225\nM v[84:87],a[32:35],a[144:147],v[84:87]\nv_sub_f32 v227,0,v225\nv_sub_f32 v225,0,v225\nv_sub_f32 v228,0,v229\nM v[88:91],a[48:51],a[128:131],0\n" \
"v_sub_f32 v230,0,v229\nv_sub_f32 v231,0,v229\nM v[88:91],a[52:55],a[132:135],v[88:91]\nv_sub_f32 v229,0,v229\nv_add_f32 v64,v64,v224\nv_add_f32 v65,v65,v224\nM v[88:91],a[56:59],a[136:139],v[88:91]\nv_add_f32 v66,v66,v224\n" \
"v_add_f32 v67,v67,v224\nv_add_f32 v72,v72,v224\nM v[88:91],a[60:63],a[140:143],v[88:91]\nv_exp_f32 v64,v64\nv_add_f32 v73,v73,v224\nM v[92:95],a[60:63],a[156:159],0\nv_exp_f32 v65,v65\nv_add_f32 v74,v74,v224\n" \
"M v[92:95],a[56:59],a[152:155],v[92:95]\nv_exp_f32 v66,v66\nv_add_f32 v75,v75,v224\nM v[92:95],a[52:55],a[148:151],v[92:95]\nv_exp_f32 v67,v67\nv_add_f32 v80,v80,v224\nM v[92:95],a[48:51],a[144:147],v[92:95]\nv_add_f32 v81,v81,v224\n" \
"v_add_f32 v82,v82,v224\ns_waitcnt lgkmcnt(0)\nM v[96:99],a[0:3],a[128:131],v[224:227]\nv_exp_f32 v72,v72\nv_add_f32 v83,v83,v224\nM v[96:99],a[4:7],a[132:135],v[96:99]\nv_exp_f32 v73,v73\nv_cvt_pk_bf16_f32 v64,v64,v65\n" \
"M v[96:99],a[8:11],a[136:139],v[96:99]\nv_exp_f32 v74,v74\nv_add_f32 v88,v88,v224\nM v[96:99],a[12:15],a[140:143],v[96:99]\nv_exp_f32 v75,v75\nv_add_f32 v89,v89,v224\nM v[100:103],a[12:15],a[156:159],v[228:231]\nv_exp_f32 v80,v80\n" \
"v_add_f32 v90,v90,v224\nM v[100:103],a[8:11],a[152:155],v[100:103]\nv_exp_f32 v81,v81\nM v[100:103],a[4:7],a[148:151],v[100:103]\nv_exp_f32 v82,v82\nv_add_f32 v91,v91,v224\nM v[100:103],a[0:3],a[144:147],v[100:103]\nv_exp_f32 v83,v83\n" \
"v_cvt_pk_bf16_f32 v65,v66,v67\nM v[108:111],a[28:31],a[156:159],v[228:231]\nv_exp_f32 v88,v88\nv_cvt_pk_bf16_f32 v66,v72,v73\nM v[108:111],a[24:27],a[152:155],v[108:111]\nv_exp_f32 v89,v89\nv_cvt_pk_bf16_f32 v67,v74,v75\nM v[108:111],a[20:23],a[148:151],v[108:111]\n" \
"v_exp_f32 v90,v90\nv_exp_f32 v91,v91\nv_cvt_pk_bf16_f32 v80,v80,v81\nM v[108:111],a[16:19],a[144:147],v[108:111]\nv_cvt_pk_bf16_f32 v81,v82,v83\nv_cvt_pk_bf16_f32 v82,v88,v89\nv_cvt_pk_bf16_f32 v83,v90,v91\nM v[0:3],a[64:67],v[64:67],0\n" \
"ds_read_b128 a[92:95],v241 offset:7168\nds_read_b128 a[124:127],v241 offset:15360\nv_add_f32 v68,v68,v228\nM v[0:3],a[96:99],v[80:83],v[0:3]\nv_add_f32 v69,v69,v228\nv_add_f32 v70,v70,v228\nv_add_f32 v71,v71,v228\nM v[8:11],a[68:71],v[64:67],0\n" \
"v_exp_f32 v68,v68\nv_add_f32 v76,v76,v228\nM v[8:11],a[100:103],v[80:83],v[8:11]\nv_exp_f32 v69,v69\nv_add_f32 v77,v77,v228\nM v[16:19],a[72:75],v[64:67],0\nv_exp_f32 v70,v70\nv_add_f32 v78,v78,v228\n" \
"M v[16:19],a[104:107],v[80:83],v[16:19]\nv_exp_f32 v71,v71\nM v[24:27],a[76:79],v[64:67],0\nv_add_f32 v79,v79,v228\nv_add_f32 v84,v84,v228\nv_add_f32 v85,v85,v228\nM v[24:27],a[108:111],v[80:83],v[24:27]\nv_exp_f32 v76,v76\n" \
"v_add_f32 v86,v86,v228\nM v[32:35],a[80:83],v[64:67],0\nv_exp_f32 v77,v77\nv_add_f32 v87,v87,v228\nM v[32:35],a[112:115],v[80:83],v[32:35]\nv_exp_f32 v78,v78\nv_cvt_pk_bf16_f32 v68,v68,v69\nM v[40:43],a[84:87],v[64:67],0\n" \
"v_exp_f32 v79,v79\nv_add_f32 v92,v92,v228\nM v[40:43],a[116:119],v[80:83],v[40:43]\nv_exp_f32 v84,v84\nv_add_f32 v93,v93,v228\nM v[48:51],a[88:91],v[64:67],0\nv_exp_f32 v85,v85\nv_add_f32 v94,v94,v228\n" \
"M v[48:51],a[120:123],v[80:83],v[48:51]\nv_exp_f32 v86,v86\ns_waitcnt lgkmcnt(0)\nM v[56:59],a[92:95],v[64:67],0\nv_exp_f32 v87,v87\nv_add_f32 v95,v95,v228\nM v[56:59],a[124:127],v[80:83],v[56:59]\nv_cvt_pk_bf16_f32 v69,v70,v71\n" \
"v_exp_f32 v92,v92\nM a[192:195],a[208:211],v[64:67],0\nv_exp_f32 v93,v93\nv_exp_f32 v94,v94\nv_exp_f32 v95,v95\nv_cvt_pk_bf16_f32 v70,v76,v77\nv_cvt_pk_bf16_f32 v71,v78,v79\nv_cvt_pk_bf16_f32 v84,v84,v85\n" \
"M a[192:195],a[208:211],v[80:83],a[192:195]\nv_cvt_pk_bf16_f32 v85,v86,v87\nv_cvt_pk_bf16_f32 v86,v92,v93\nv_cvt_pk_bf16_f32 v87,v94,v95\nM v[4:7],a[64:67],v[68:71],0\nds_read_b128 a[0:3],v240 offset:32768\nds_read_b128 a[4:7],v240 offset:33792\nM v[4:7],a[96:99],v[84:87],v[4:7]\n" \
"ds_read_b128 a[8:11],v240 offset:34816\nv_cndmask_b32 v96,v96,v252,s[82:83]\nv_cndmask_b32 v97,v97,v252,s[84:85]\nM v[12:15],a[68:71],v[68:71],0\nds_read_b128 a[12:15],v240 offset:35840\nv_cndmask_b32 v98,v98,v252,s[86:87]\nv_cndmask_b32 v99,v99,v252,s[88:89]\nM v[12:15],a[100:103],v[84:87],v[12:15]\n" \
"ds_read_b128 a[16:19],v240 offset:36864\nv_exp_f32 v96,v96\nM v[20:23],a[72:75],v[68:71],0\nds_read_b128 a[20:23],v240 offset:37888\nv_exp_f32 v97,v97\nM v[20:23],a[104:107],v[84:87],v[20:23]\nds_read_b128 a[24:27],v240 offset:38912\nv_exp_f32 v98,v98\n" \
"M v[28:31],a[76:79],v[68:71],0\nbuffer_load_dword v248,v251,s[52:55],s80 offen sc1\nds_read_b128 a[28:31],v240 offset:39936\nM v[28:31],a[108:111],v[84:87],v[28:31]\nds_read_b128 a[32:35],v240 offset:40960\nv_exp_f32 v99,v99\nM v[36:39],a[80:83],v[68:71],0\nds_read_b128 a[36:39],v240 offset:41984\n" \
"v_cvt_pk_bf16_f32 v96,v96,v97\nv_cvt_pk_bf16_f32 v97,v98,v99\nM v[36:39],a[112:115],v[84:87],v[36:39]\nds_read_b128 a[40:43],v240 offset:43008\nv_mov_b32 v98,0\nv_mov_b32 v99,0\nM v[44:47],a[84:87],v[68:71],0\nds_read_b128 a[44:47],v240 offset:44032\n" \
"v_exp_f32 v100,v100\nM v[44:47],a[116:119],v[84:87],v[44:47]\nds_read_b128 a[48:51],v240 offset:45056\nv_exp_f32 v101,v101\nM v[52:55],a[88:91],v[68:71],0\nds_read_b128 a[52:55],v240 offset:46080\nv_cndmask_b32 v108,v108,v252,s[82:83]\nM v[52:55],a[120:123],v[84:87],v[52:55]\n" \
"ds_read_b128 a[56:59],v240 offset:47104\nv_cndmask_b32 v109,v109,v252,s[84:85]\nv_cndmask_b32 v110,v110,v252,s[86:87]\nM v[60:63],a[92:95],v[68:71],0\nds_read_b128 a[60:63],v240 offset:48128\nv_cndmask_b32 v111,v111,v252,s[88:89]\nv_cvt_pk_bf16_f32 v100,v100,v101\nM v[60:63],a[124:127],v[84:87],v[60:63]\n" \
"ds_read_b128 a[64:67],v241 offset:16384\nv_exp_f32 v102,v102\nM a[196:199],a[208:211],v[68:71],0\nds_read_b128 a[68:71],v241 offset:17408\nv_exp_f32 v103,v103\nM a[196:199],a[208:211],v[84:87],a[196:199]\nds_read_b128 a[72:75],v241 offset:18432\nv_cvt_pk_bf16_f32 v101,v102,v103\n" \
"s_waitcnt lgkmcnt(8)\nM v[128:131],a[0:3],a[128:131],v[224:227]\nds_read_b128 a[76:79],v241 offset:19456\nv_exp_f32 v108,v108\nM v[128:131],a[4:7],a[132:135],v[128:131]\nds_read_b128 a[80:83],v241 offset:20480\nv_exp_f32 v109,v109\nM v[128:131],a[8:11],a[136:139],v[128:131]\n" \
"ds_read_b128 a[84:87],v241 offset:21504\nv_exp_f32 v110,v110\nM v[128:131],a[12:15],a[140:143],v[128:131]\nds_read_b128 a[88:91],v241 offset:22528\nv_exp_f32 v111,v111\nM v[132:135],a[12:15],a[156:159],v[228:231]\nds_read_b128 a[92:95],v241 offset:23552\nv_cvt_pk_bf16_f32 v102,v108,v109\n" \
"v_cvt_pk_bf16_f32 v103,v110,v111\nM v[132:135],a[8:11],a[152:155],v[132:135]\nds_read_b128 a[8:11],v240 offset:51200\nv_exp_f32 v128,v128\nM v[132:135],a[4:7],a[148:151],v[132:135]\nds_read_b128 a[4:7],v240 offset:50176\nv_exp_f32 v129,v129\nM v[132:135],a[0:3],a[144:147],v[132:135]\n" \
"ds_read_b128 a[0:3],v240 offset:49152\nv_cvt_pk_bf16_f32 v192,v128,v129\nM v[144:147],a[16:19],a[128:131],v[224:227]\nds_read_b128 a[12:15],v240 offset:52224\nv_exp_f32 v130,v130\nM v[144:147],a[20:23],a[132:135],v[144:147]\nv_exp_f32 v131,v131\nds_read_b128 a[96:99],v241 offset:40960\n" \
"M v[144:147],a[24:27],a[136:139],v[144:147]\nv_cvt_pk_bf16_f32 v193,v130,v131\nv_exp_f32 v132,v132\nM v[144:147],a[28:31],a[140:143],v[144:147]\nv_exp_f32 v133,v133\nds_read_b128 v[64:67],v250\nM v[148:151],a[28:31],a[156:159],v[228:231]\nds_read_b128 a[28:31],v240 offset:56320\n" \
"v_exp_f32 v134,v134\nM v[148:151],a[24:27],a[152:155],v[148:151]\nds_read_b128 a[24:27],v240 offset:55296\nv_exp_f32 v135,v135\nM v[148:151],a[20:23],a[148:151],v[148:151]\nds_read_b128 a[20:23],v240 offset:54272\nv_cvt_pk_bf16_f32 v196,v132,v133\nv_cvt_pk_bf16_f32 v197,v134,v135\n" \
"M v[148:151],a[16:19],a[144:147],v[148:151]\nds_read_b128 a[16:19],v240 offset:53248\nv_exp_f32 v144,v144\nM v[160:163],a[32:35],a[128:131],v[224:227]\nv_exp_f32 v145,v145\nds_read_b128 v[68:71],v250\nM v[160:163],a[36:39],a[132:135],v[160:163]\nv_exp_f32 v146,v146\n" \
"v_cvt_pk_bf16_f32 v194,v144,v145\nM v[160:163],a[40:43],a[136:139],v[160:163]\nv_exp_f32 v147,v147\ns_waitcnt lgkmcnt(6)\nM v[160:163],a[44:47],a[140:143],v[160:163]\nv_cvt_pk_bf16_f32 v195,v146,v147\nds_read_b128 v[72:75],v250\nM v[164:167],a[44:47],a[156:159],v[228:231]\n" \
"ds_read_b128 a[44:47],v240 offset:60416\nv_exp_f32 v148,v148\nM v[164:167],a[40:43],a[152:155],v[164:167]\nds_read_b128 a[40:43],v240 offset:59392\nv_exp_f32 v149,v149\nM v[164:167],a[36:39],a[148:151],v[164:167]\nds_read_b128 a[36:39],v240 offset:58368\nv_exp_f32 v150,v150\n" \
"M v[164:167],a[32:35],a[144:147],v[164:167]\nds_read_b128 a[32:35],v240 offset:57344\nv_exp_f32 v151,v151\nM v[176:179],a[48:51],a[128:131],v[224:227]\nv_cvt_pk_bf16_f32 v198,v148,v149\nv_cvt_pk_bf16_f32 v199,v150,v151\nds_read_b128 v[76:79],v250\nM v[176:179],a[52:55],a[132:135],v[176:179]\n" \
"v_exp_f32 v160,v160\nds_read_b128 v[80:83],v250\nM v[176:179],a[56:59],a[136:139],v[176:179]\nv_exp_f32 v161,v161\nds_read_b128 v[84:87],v250\nM v[176:179],a[60:63],a[140:143],v[176:179]\nv_exp_f32 v162,v162\nv_cvt_pk_bf16_f32 v208,v160,v161\n" \
"M v[180:183],a[60:63],a[156:159],v[228:231]\nv_exp_f32 v163,v163\nds_read_b128 a[60:63],v240 offset:64512\nM v[180:183],a[56:59],a[152:155],v[180:183]\nv_cvt_pk_bf16_f32 v209,v162,v163\nds_read_b128 a[56:59],v240 offset:63488\nds_read_b128 v[88:91],v250\nM v[180:183],a[52:55],a[148:151],v[180:183]\n" \
"ds_read_b128 a[52:55],v240 offset:62464\nv_exp_f32 v164,v164\nM v[180:183],a[48:51],a[144:147],v[180:183]\nds_read_b128 a[48:51],v240 offset:61440\nv_exp_f32 v165,v165\nM v[0:3],a[64:67],v[96:99],v[0:3]\nv_exp_f32 v166,v166\nM v[8:11],a[68:71],v[96:99],v[8:11]\n" \
"v_exp_f32 v167,v167\nv_cvt_pk_bf16_f32 v212,v164,v165\nM v[16:19],a[72:75],v[96:99],v[16:19]\nv_cvt_pk_bf16_f32 v213,v166,v167\nv_exp_f32 v176,v176\nM v[24:27],a[76:79],v[96:99],v[24:27]\nv_exp_f32 v177,v177\nds_read_b128 v[92:95],v250\n" \
"M v[32:35],a[80:83],v[96:99],v[32:35]\nv_exp_f32 v178,v178\nv_cvt_pk_bf16_f32 v210,v176,v177\nM v[40:43],a[84:87],v[96:99],v[40:43]\nv_exp_f32 v179,v179\nds_read_b128 v[104:107],v250\nM v[48:51],a[88:91],v[96:99],v[48:51]\ns_waitcnt vmcnt(0)\n" \
"s_barrier\nv_cvt_pk_bf16_f32 v211,v178,v179\nM v[56:59],a[92:95],v[96:99],v[56:59]\nv_exp_f32 v180,v180\nds_read_b128 v[108:111],v250\nM a[192:195],a[208:211],v[96:99],a[192:195]\nv_exp_f32 v181,v181\nds_read_b128 v[96:99],v250\n" \
"M v[4:7],a[64:67],v[100:103],v[4:7]\nv_exp_f32 v182,v182\nv_cvt_pk_bf16_f32 v214,v180,v181\nM v[12:15],a[68:71],v[100:103],v[12:15]\nv_exp_f32 v183,v183\nds_read_b128 a[64:67],v241 offset:32768\nM v[20:23],a[72:75],v[100:103],v[20:23]\nv_cvt_pk_bf16_f32 v215,v182,v183\n" \
"ds_read_b128 a[68:71],v241 offset:33792\nds_read_b128 v[112:115],v250\nM v[28:31],a[76:79],v[100:103],v[28:31]\nds_read_b128 v[116:119],v250\nv_cmp_eq_u32 vcc,0,v248\nM v[36:39],a[80:83],v[100:103],v[36:39]\nds_read_b128 v[120:123],v250\ns_cbranch_vccnz .Lps4\n" \
".Lpb4:\ns_add_u32 m0,s64,65536\nM v[44:47],a[84:87],v[100:103],v[44:47]\nds_read_b128 v[124:127],v250\nbuffer_load_dwordx4 v240,s[48:51],s36 offen offset:0 sc1 lds\nM v[52:55],a[88:91],v[100:103],v[52:55]\nds_read_b128 a[200:203],v250\nM v[60:63],a[92:95],v[100:103],v[60:63]\n" \
"ds_read_b128 a[204:207],v250\nM a[196:199],a[208:211],v[100:103],a[196:199]\nds_read_b128 v[100:103],v250\nv_add_u32 v250,0xc000,v241\nM v[136:139],a[0:3],a[160:163],0\nds_read_b128 a[100:103],v241 offset:41984\nbuffer_load_dwordx4 v240,s[48:51],s36 offen offset:1024 sc1 lds\nM v[136:139],a[4:7],a[164:167],v[136:139]\n" \
"ds_read_b128 a[72:75],v241 offset:34816\nM v[136:139],a[8:11],a[168:171],v[136:139]\nds_read_b128 a[104:107],v241 offset:43008\nM v[136:139],a[12:15],a[172:175],v[136:139]\nds_read_b128 a[76:79],v241 offset:35840\nM v[140:143],a[12:15],a[188:191],0\nds_read_b128 a[108:111],v241 offset:44032\nbuffer_load_dwordx4 v240,s[48:51],s36 offen offset:2048 sc1 lds\n" \
"M v[140:143],a[8:11],a[184:187],v[140:143]\nds_read_b128 a[80:83],v241 offset:36864\nM v[140:143],a[4:7],a[180:183],v[140:143]\nds_read_b128 a[112:115],v241 offset:45056\nv_max3_f32 v233,v136,v137,v138\nM v[140:143],a[0:3],a[176:179],v[140:143]\nds_read_b128 a[84:87],v241 offset:37888\ns_waitcnt lgkmcnt(11)\n" \
"M v[152:155],a[16:19],a[160:163],0\nds_read_b128 a[116:119],v241 offset:46080\nbuffer_load_dwordx4 v240,s[48:51],s36 offen offset:3072 sc1 lds\nM v[152:155],a[20:23],a[164:167],v[152:155]\nds_read_b128 a[88:91],v241 offset:38912\ns_add_u32 m0,s64,16384\nv_max3_f32 v237,v140,v141,v142\nM v[152:155],a[24:27],a[168:171],v[152:155]\n" \
"ds_read_b128 a[120:123],v241 offset:47104\nM v[152:155],a[28:31],a[172:175],v[152:155]\nds_read_b128 a[92:95],v241 offset:39936\nM v[156:159],a[28:31],a[188:191],0\nds_read_b128 a[124:127],v241 offset:48128\nbuffer_load_dwordx4 v240,s[44:47],s65 offen offset:0 sc1 lds\nM v[156:159],a[24:27],a[184:187],v[156:159]\nM v[156:159],a[20:23],a[180:183],v[156:159]\n" \
"M v[156:159],a[16:19],a[176:179],v[156:159]\nM v[128:131],a[0:3],a[128:131],v[224:227]\nv_max3_f32 v233,v233,v139,v152\nv_max3_f32 v233,v233,v153,v154\nv_max_f32 v233,v233,v155\nM v[128:131],a[4:7],a[132:135],v[128:131]\nv_cndmask_b32 v233,v232,v233,s[90:91]\nbuffer_load_dwordx4 v240,s[44:47],s65 offen offset:1024 sc1 lds\n" \
"M v[128:131],a[8:11],a[136:139],v[128:131]\nv_max3_f32 v237,v237,v143,v156\nv_max3_f32 v237,v237,v157,v158\nv_max_f32 v237,v237,v159\nM v[128:131],a[12:15],a[140:143],v[128:131]\nv_cndmask_b32 v237,v236,v237,s[90:91]\nM v[132:135],a[12:15],a[156:159],v[228:231]\nM v[132:135],a[8:11],a[152:155],v[132:135]\n" \
"v_permlane16_swap_b32 v233,v237\nv_max_f32 v233,v233,v237\nv_mov_b32 v237,v233\nM v[132:135],a[4:7],a[148:151],v[132:135]\nbuffer_load_dwordx4 v240,s[44:47],s65 offen offset:2048 sc1 lds\nv_permlane32_swap_b32 v233,v237\nM v[132:135],a[0:3],a[144:147],v[132:135]\nv_max_f32 v233,v233,v237\n" \
"v_mov_b32 v237,v233\nds_read_b128 a[0:3],v240 offset:0\nM v[144:147],a[16:19],a[128:131],v[224:227]\nv_permlane16_swap_b32 v233,v237\nv_sub_f32 v232,0,v233\nv_sub_f32 v234,0,v233\nM v[144:147],a[20:23],a[132:135],v[144:147]\nv_sub_f32 v235,0,v233\n" \
"v_sub_f32 v233,0,v233\nv_sub_f32 v236,0,v237\nM v[144:147],a[24:27],a[136:139],v[144:147]\nv_sub_f32 v238,0,v237\nv_sub_f32 v239,0,v237\nv_sub_f32 v237,0,v237\nM v[144:147],a[28:31],a[140:143],v[144:147]\nds_read_b128 a[4:7],v240 offset:1024\n" \
"ds_read_b128 a[8:11],v240 offset:2048\nv_add_f32 v136,v136,v232\nM v[148:151],a[28:31],a[156:159],v[228:231]\nds_read_b128 a[12:15],v240 offset:3072\nbuffer_load_dwordx4 v240,s[44:47],s65 offen offset:3072 sc1 lds\nM v[148:151],a[24:27],a[152:155],v[148:151]\ns_add_u32 m0,s64,81920\nv_add_f32 v137,v137,v232\n" \
"v_add_f32 v138,v138,v232\nM v[148:151],a[20:23],a[148:151],v[148:151]\nv_add_f32 v139,v139,v232\nv_add_f32 v152,v152,v232\nv_add_f32 v153,v153,v232\nM v[148:151],a[16:19],a[144:147],v[148:151]\nds_read_b128 a[16:19],v240 offset:4096\nds_read_b128 a[20:23],v240 offset:5120\n" \
"v_add_f32 v154,v154,v232\nM v[160:163],a[32:35],a[128:131],v[224:227]\nds_read_b128 a[24:27],v240 offset:6144\nv_add_f32 v155,v155,v232\nM v[160:163],a[36:39],a[132:135],v[160:163]\nds_read_b128 a[28:31],v240 offset:7168\nbuffer_load_dwordx4 v240,s[48:51],s65 offen offset:0 sc1 lds\nM v[160:163],a[40:43],a[136:139],v[160:163]\n" \
"v_exp_f32 v136,v136\nv_add_f32 v140,v140,v236\nM v[160:163],a[44:47],a[140:143],v[160:163]\nv_exp_f32 v137,v137\nv_add_f32 v141,v141,v236\nM v[164:167],a[44:47],a[156:159],v[228:231]\nv_exp_f32 v138,v138\nv_add_f32 v142,v142,v236\n" \
"M v[164:167],a[40:43],a[152:155],v[164:167]\nbuffer_load_dwordx4 v240,s[48:51],s65 offen offset:1024 sc1 lds\nv_add_f32 v143,v143,v236\nM v[164:167],a[36:39],a[148:151],v[164:167]\nv_exp_f32 v139,v139\nv_add_f32 v156,v156,v236\nM v[164:167],a[32:35],a[144:147],v[164:167]\nv_exp_f32 v152,v152\n" \
"v_add_f32 v157,v157,v236\nM v[176:179],a[48:51],a[128:131],v[224:227]\nv_exp_f32 v153,v153\nv_add_f32 v158,v158,v236\nM v[176:179],a[52:55],a[132:135],v[176:179]\nbuffer_load_dwordx4 v240,s[48:51],s65 offen offset:2048 sc1 lds\nv_add_f32 v159,v159,v236\nM v[176:179],a[56:59],a[136:139],v[176:179]\n" \
"v_exp_f32 v154,v154\nv_cvt_pk_bf16_f32 v200,v136,v137\nM v[176:179],a[60:63],a[140:143],v[176:179]\nv_exp_f32 v155,v155\nv_cvt_pk_bf16_f32 v201,v138,v139\nM v[180:183],a[60:63],a[156:159],v[228:231]\nv_exp_f32 v128,v128\nM v[180:183],a[56:59],a[152:155],v[180:183]\n" \
"buffer_load_dwordx4 v240,s[48:51],s65 offen offset:3072 sc1 lds\ns_add_u32 m0,s64,32768\nM v[180:183],a[52:55],a[148:151],v[180:183]\nv_exp_f32 v129,v129\nv_cvt_pk_bf16_f32 v202,v152,v153\nM v[180:183],a[48:51],a[144:147],v[180:183]\nv_exp_f32 v130,v130\nv_cvt_pk_bf16_f32 v203,v154,v155\n" \
"M v[168:171],a[32:35],a[160:163],v[232:235]\nv_exp_f32 v131,v131\nM v[168:171],a[36:39],a[164:167],v[168:171]\nbuffer_load_dwordx4 v240,s[44:47],s66 offen offset:0 sc1 lds\nM v[168:171],a[40:43],a[168:171],v[168:171]\nv_exp_f32 v132,v132\nM v[168:171],a[44:47],a[172:175],v[168:171]\nv_exp_f32 v133,v133\n" \
"M v[172:175],a[44:47],a[188:191],v[236:239]\nv_exp_f32 v134,v134\nM v[172:175],a[40:43],a[184:187],v[172:175]\nbuffer_load_dwordx4 v240,s[44:47],s66 offen offset:1024 sc1 lds\nM v[172:175],a[36:39],a[180:183],v[172:175]\ns_waitcnt lgkmcnt(0)\ns_barrier\nv_cndmask_b32 v168,v168,v252,s[82:83]\n" \
"M v[172:175],a[32:35],a[176:179],v[172:175]\nds_read_b128 a[32:35],v240 offset:8192\nds_read_b128 a[36:39],v240 offset:9216\nv_cndmask_b32 v169,v169,v252,s[84:85]\nM v[188:191],a[60:63],a[188:191],v[236:239]\nds_read_b128 a[40:43],v240 offset:10240\nv_exp_f32 v135,v135\nM v[188:191],a[56:59],a[184:187],v[188:191]\n" \
"ds_read_b128 a[44:47],v240 offset:11264\nv_cndmask_b32 v170,v170,v252,s[86:87]\nM v[188:191],a[52:55],a[180:183],v[188:191]\nbuffer_load_dwordx4 v240,s[44:47],s66 offen offset:2048 sc1 lds\nv_cndmask_b32 v171,v171,v252,s[88:89]\nM v[188:191],a[48:51],a[176:179],v[188:191]\nds_read_b128 a[48:51],v240 offset:12288\nv_exp_f32 v144,v144\n" \
"M v[0:3],a[64:67],v[192:195],v[0:3]\nds_read_b128 a[52:55],v240 offset:13312\nv_exp_f32 v145,v145\nM v[0:3],a[96:99],v[208:211],v[0:3]\nds_read_b128 a[56:59],v240 offset:14336\nv_exp_f32 v146,v146\nM v[8:11],a[68:71],v[192:195],v[8:11]\nds_read_b128 a[60:63],v240 offset:15360\n" \
"buffer_load_dwordx4 v240,s[44:47],s66 offen offset:3072 sc1 lds\nM v[8:11],a[100:103],v[208:211],v[8:11]\nv_exp_f32 v147,v147\ns_add_u32 m0,s64,98304\nM v[16:19],a[72:75],v[192:195],v[16:19]\nv_exp_f32 v148,v148\nv_cndmask_b32 v188,v188,v252,s[82:83]\nM v[16:19],a[104:107],v[208:211],v[16:19]\n" \
"v_exp_f32 v149,v149\nv_cndmask_b32 v189,v189,v252,s[84:85]\nM v[24:27],a[76:79],v[192:195],v[24:27]\nv_exp_f32 v150,v150\nv_cndmask_b32 v190,v190,v252,s[86:87]\nM v[24:27],a[108:111],v[208:211],v[24:27]\nv_exp_f32 v151,v151\nv_cndmask_b32 v191,v191,v252,s[88:89]\n" \
"M v[32:35],a[80:83],v[192:195],v[32:35]\nv_exp_f32 v160,v160\nM v[32:35],a[112:115],v[208:211],v[32:35]\nv_exp_f32 v161,v161\nM v[40:43],a[84:87],v[192:195],v[40:43]\nv_exp_f32 v162,v162\nM v[40:43],a[116:119],v[208:211],v[40:43]\nv_exp_f32 v163,v163\n" \
"M v[48:51],a[88:91],v[192:195],v[48:51]\nv_exp_f32 v176,v176\nM v[48:51],a[120:123],v[208:211],v[48:51]\nv_exp_f32 v177,v177\nM v[56:59],a[92:95],v[192:195],v[56:59]\nv_exp_f32 v178,v178\nM v[56:59],a[124:127],v[208:211],v[56:59]\nv_exp_f32 v179,v179\n" \
"M a[192:195],a[208:211],v[192:195],a[192:195]\nv_exp_f32 v140,v140\nv_cvt_pk_bf16_f32 v192,v128,v129\nM a[192:195],a[208:211],v[208:211],a[192:195]\nv_exp_f32 v141,v141\nv_cvt_pk_bf16_f32 v193,v130,v131\nM v[4:7],a[64:67],v[196:199],v[4:7]\nv_exp_f32 v142,v142\n" \
"v_cvt_pk_bf16_f32 v194,v144,v145\nM v[4:7],a[96:99],v[212:215],v[4:7]\nv_exp_f32 v143,v143\nv_cvt_pk_bf16_f32 v195,v146,v147\nM v[12:15],a[68:71],v[196:199],v[12:15]\nv_exp_f32 v156,v156\nv_cvt_pk_bf16_f32 v208,v160,v161\nM v[12:15],a[100:103],v[212:215],v[12:15]\n" \
"v_exp_f32 v157,v157\nv_cvt_pk_bf16_f32 v209,v162,v163\nM v[20:23],a[72:75],v[196:199],v[20:23]\nv_exp_f32 v158,v158\nM v[20:23],a[104:107],v[212:215],v[20:23]\nv_exp_f32 v159,v159\nv_cvt_pk_bf16_f32 v210,v176,v177\nM v[28:31],a[76:79],v[196:199],v[28:31]\n" \
"v_exp_f32 v164,v164\nv_cvt_pk_bf16_f32 v211,v178,v179\nM v[28:31],a[108:111],v[212:215],v[28:31]\nv_exp_f32 v165,v165\nM v[36:39],a[80:83],v[196:199],v[36:39]\nv_exp_f32 v166,v166\nM v[36:39],a[112:115],v[212:215],v[36:39]\nv_exp_f32 v167,v167\n" \
"M v[44:47],a[84:87],v[196:199],v[44:47]\nv_exp_f32 v180,v180\nM v[44:47],a[116:119],v[212:215],v[44:47]\nv_exp_f32 v181,v181\nM v[52:55],a[88:91],v[196:199],v[52:55]\nv_exp_f32 v182,v182\nM v[52:55],a[120:123],v[212:215],v[52:55]\nv_exp_f32 v183,v183\n" \
"M v[60:63],a[92:95],v[196:199],v[60:63]\nv_exp_f32 v168,v168\nM v[60:63],a[124:127],v[212:215],v[60:63]\nv_exp_f32 v169,v169\nM a[196:199],a[208:211],v[196:199],a[196:199]\nv_exp_f32 v170,v170\nv_exp_f32 v171,v171\nv_cvt_pk_bf16_f32 v196,v132,v133\n" \
"v_cvt_pk_bf16_f32 v197,v134,v135\nv_cvt_pk_bf16_f32 v198,v148,v149\nv_cvt_pk_bf16_f32 v199,v150,v151\nbuffer_load_dwordx4 v240,s[48:51],s66 offen offset:0 sc1 lds\nbuffer_load_dwordx4 v240,s[48:51],s66 offen offset:1024 sc1 lds\nbuffer_load_dwordx4 v240,s[48:51],s66 offen offset:2048 sc1 lds\nbuffer_load_dwordx4 v240,s[48:51],s66 offen offset:3072 sc1 lds\nM a[196:199],a[208:211],v[212:215],a[196:199]\n" \
"s_cmp_eq_u32 s62,0\ns_cbranch_scc1 .Ltrans\ns_branch .Ldual\n.Lpro3:\ns_barrier\nM v[64:67],a[0:3],a[128:131],0\nds_read_b128 a[64:67],v241 offset:0\nds_read_b128 a[96:99],v241 offset:8192\n" \
"M v[64:67],a[4:7],a[132:135],v[64:67]\nds_read_b128 a[68:71],v241 offset:1024\ns_mov_b32 m0,s64\nv_mov_b32 v250,143360\nM v[64:67],a[8:11],a[136:139],v[64:67]\nds_read_b128 a[100:103],v241 offset:9216\nbuffer_load_dwordx4 v240,s[44:47],s36 offen offset:0 sc1 lds\nM v[64:67],a[12:15],a[140:143],v[64:67]\n" \
"ds_read_b128 a[72:75],v241 offset:2048\nv_mov_b32 v168,0\nv_mov_b32 v169,0\nM v[68:71],a[12:15],a[156:159],0\nds_read_b128 a[12:15],v240 offset:19456\nv_mov_b32 v170,0\nv_mov_b32 v171,0\nM v[68:71],a[8:11],a[152:155],v[68:71]\n" \
"ds_read_b128 a[8:11],v240 offset:18432\nv_max3_f32 v225,v64,v65,v66\nv_mov_b32 v172,0xff800000\nM v[68:71],a[4:7],a[148:151],v[68:71]\nds_read_b128 a[4:7],v240 offset:17408\nbuffer_load_dwordx4 v240,s[44:47],s36 offen offset:1024 sc1 lds\nM v[68:71],a[0:3],a[144:147],v[68:71]\nds_read_b128 a[0:3],v240 offset:16384\n" \
"v_mov_b32 v173,0xff800000\nv_mov_b32 v174,0xff800000\nM v[72:75],a[16:19],a[128:131],0\nds_read_b128 a[104:107],v241 offset:10240\nv_mov_b32 v175,0xff800000\nv_mov_b32 v184,0xff800000\nM v[72:75],a[20:23],a[132:135],v[72:75]\nv_max3_f32 v229,v68,v69,v70\n" \
"ds_read_b128 a[76:79],v241 offset:3072\nv_mov_b32 v185,0xff800000\nM v[72:75],a[24:27],a[136:139],v[72:75]\nds_read_b128 a[108:111],v241 offset:11264\nv_mov_b32 v186,0xff800000\nM v[72:75],a[28:31],a[140:143],v[72:75]\nds_read_b128 a[80:83],v241 offset:4096\nbuffer_load_dwordx4 v240,s[44:47],s36 offen offset:2048 sc1 lds\n" \
"M v[76:79],a[28:31],a[156:159],0\nds_read_b128 a[28:31],v240 offset:23552\nv_mov_b32 v187,0xff800000\nv_mov_b32 v188,0xff800000\nM v[76:79],a[24:27],a[152:155],v[76:79]\nds_read_b128 a[24:27],v240 offset:22528\nv_max3_f32 v225,v225,v67,v72\nv_max3_f32 v225,v225,v73,v74\n" \
"M v[76:79],a[20:23],a[148:151],v[76:79]\nv_max_f32 v225,v225,v75\nv_cndmask_b32 v225,v224,v225,s[90:91]\nds_read_b128 a[20:23],v240 offset:21504\nM v[76:79],a[16:19],a[144:147],v[76:79]\nds_read_b128 a[16:19],v240 offset:20480\nbuffer_load_dwordx4 v240,s[44:47],s36 offen offset:3072 sc1 lds\nM v[80:83],a[32:35],a[128:131],0\n" \
"ds_read_b128 a[112:115],v241 offset:12288\nv_mov_b32 v189,0xff800000\nv_mov_b32 v190,0xff800000\nM v[80:83],a[36:39],a[132:135],v[80:83]\nds_read_b128 a[84:87],v241 offset:5120\nv_max3_f32 v229,v229,v71,v76\nv_max3_f32 v229,v229,v77,v78\nM v[80:83],a[40:43],a[136:139],v[80:83]\n" \
"v_max_f32 v229,v229,v79\nv_cndmask_b32 v229,v228,v229,s[90:91]\nds_read_b128 a[116:119],v241 offset:13312\nM v[80:83],a[44:47],a[140:143],v[80:83]\nv_permlane16_swap_b32 v225,v229\nv_max_f32 v225,v225,v229\nv_mov_b32 v229,v225\nM v[84:87],a[44:47],a[156:159],0\n" \
"ds_read_b128 a[44:47],v240 offset:27648\nv_permlane32_swap_b32 v225,v229\nv_max_f32 v225,v225,v229\nM v[84:87],a[40:43],a[152:155],v[84:87]\nv_mov_b32 v229,v225\nds_read_b128 a[40:43],v240 offset:26624\nM v[84:87],a[36:39],a[148:151],v[84:87]\nv_permlane16_swap_b32 v225,v229\n" \
"v_sub_f32 v224,0,v225\nv_sub_f32 v226,0,v225\nM v[84:87],a[32:35],a[144:147],v[84:87]\nv_sub_f32 v227,0,v225\nv_sub_f32 v225,0,v225\nv_sub_f32 v228,0,v229\nM v[88:91],a[48:51],a[128:131],0\nv_sub_f32 v230,0,v229\n" \
"v_sub_f32 v231,0,v229\nv_sub_f32 v229,0,v229\nM v[88:91],a[52:55],a[132:135],v[88:91]\nds_read_b128 a[32:35],v240 offset:24576\nds_read_b128 a[36:39],v240 offset:25600\nv_add_f32 v64,v64,v224\nM v[88:91],a[56:59],a[136:139],v[88:91]\nv_add_f32 v65,v65,v224\n" \
"v_add_f32 v66,v66,v224\nv_add_f32 v67,v67,v224\nM v[88:91],a[60:63],a[140:143],v[88:91]\nv_exp_f32 v64,v64\nv_add_f32 v72,v72,v224\nM v[92:95],a[60:63],a[156:159],0\nds_read_b128 a[60:63],v240 offset:31744\nv_exp_f32 v65,v65\n" \
"M v[92:95],a[56:59],a[152:155],v[92:95]\nds_read_b128 a[56:59],v240 offset:30720\nv_exp_f32 v66,v66\nM v[92:95],a[52:55],a[148:151],v[92:95]\nds_read_b128 a[52:55],v240 offset:29696\nv_exp_f32 v67,v67\nM v[92:95],a[48:51],a[144:147],v[92:95]\nds_read_b128 a[48:51],v240 offset:28672\n" \
"v_add_f32 v73,v73,v224\ns_waitcnt lgkmcnt(6)\nM v[96:99],a[0:3],a[128:131],v[224:227]\nv_add_f32 v74,v74,v224\nv_add_f32 v75,v75,v224\nM v[96:99],a[4:7],a[132:135],v[96:99]\nv_add_f32 v80,v80,v224\nv_add_f32 v81,v81,v224\n" \
"v_add_f32 v82,v82,v224\nM v[96:99],a[8:11],a[136:139],v[96:99]\nv_exp_f32 v72,v72\nv_add_f32 v83,v83,v224\nM v[96:99],a[12:15],a[140:143],v[96:99]\nv_exp_f32 v73,v73\nv_cvt_pk_bf16_f32 v64,v64,v65\nM v[100:103],a[12:15],a[156:159],v[228:231]\n" \
"v_exp_f32 v74,v74\nv_add_f32 v88,v88,v224\nM v[100:103],a[8:11],a[152:155],v[100:103]\nv_exp_f32 v75,v75\nv_add_f32 v89,v89,v224\nM v[100:103],a[4:7],a[148:151],v[100:103]\nv_exp_f32 v80,v80\nv_add_f32 v90,v90,v224\n" \
"M v[100:103],a[0:3],a[144:147],v[100:103]\nv_exp_f32 v81,v81\nv_add_f32 v91,v91,v224\nM v[104:107],a[16:19],a[128:131],v[224:227]\nv_exp_f32 v82,v82\nv_cvt_pk_bf16_f32 v65,v66,v67\nM v[104:107],a[20:23],a[132:135],v[104:107]\nv_exp_f32 v83,v83\n" \
"v_cvt_pk_bf16_f32 v66,v72,v73\nM v[104:107],a[24:27],a[136:139],v[104:107]\nv_exp_f32 v88,v88\nv_cvt_pk_bf16_f32 v67,v74,v75\nM v[104:107],a[28:31],a[140:143],v[104:107]\nv_exp_f32 v89,v89\nM v[108:111],a[28:31],a[156:159],v[228:231]\nv_exp_f32 v90,v90\n" \
"v_cvt_pk_bf16_f32 v80,v80,v81\nM v[108:111],a[24:27],a[152:155],v[108:111]\nv_exp_f32 v91,v91\nv_cvt_pk_bf16_f32 v81,v82,v83\nM v[108:111],a[20:23],a[148:151],v[108:111]\nv_cvt_pk_bf16_f32 v82,v88,v89\nv_cvt_pk_bf16_f32 v83,v90,v91\nds_read_b128 a[88:91],v241 offset:6144\n" \
"M v[108:111],a[16:19],a[144:147],v[108:111]\nds_read_b128 a[120:123],v241 offset:14336\nds_read_b128 a[92:95],v241 offset:7168\ns_waitcnt lgkmcnt(3)\nM v[112:115],a[32:35],a[128:131],v[224:227]\nds_read_b128 a[124:127],v241 offset:15360\nv_add_f32 v68,v68,v228\nv_add_f32 v69,v69,v228\n" \
"M v[112:115],a[36:39],a[132:135],v[112:115]\nv_add_f32 v70,v70,v228\nv_add_f32 v71,v71,v228\nv_add_f32 v76,v76,v228\nM v[112:115],a[40:43],a[136:139],v[112:115]\nv_exp_f32 v68,v68\nv_add_f32 v77,v77,v228\nM v[112:115],a[44:47],a[140:143],v[112:115]\n" \
"v_exp_f32 v69,v69\nv_add_f32 v78,v78,v228\nM v[116:119],a[44:47],a[156:159],v[228:231]\nv_exp_f32 v70,v70\nv_add_f32 v79,v79,v228\nM v[116:119],a[40:43],a[152:155],v[116:119]\nv_exp_f32 v71,v71\nv_add_f32 v84,v84,v228\n" \
"M v[116:119],a[36:39],a[148:151],v[116:119]\nv_add_f32 v85,v85,v228\nv_add_f32 v86,v86,v228\nM v[116:119],a[32:35],a[144:147],v[116:119]\nv_exp_f32 v76,v76\nv_add_f32 v87,v87,v228\nM v[124:127],a[60:63],a[156:159],v[228:231]\nv_exp_f32 v77,v77\n" \
"v_cvt_pk_bf16_f32 v68,v68,v69\nM v[124:127],a[56:59],a[152:155],v[124:127]\nv_exp_f32 v78,v78\nv_add_f32 v92,v92,v228\nM v[124:127],a[52:55],a[148:151],v[124:127]\nv_exp_f32 v79,v79\nv_add_f32 v93,v93,v228\nM v[124:127],a[48:51],a[144:147],v[124:127]\n" \
"v_exp_f32 v84,v84\nv_add_f32 v94,v94,v228\nM v[0:3],a[64:67],v[64:67],0\nv_exp_f32 v85,v85\nv_add_f32 v95,v95,v228\nM v[0:3],a[96:99],v[80:83],v[0:3]\nv_exp_f32 v86,v86\nv_cvt_pk_bf16_f32 v69,v70,v71\n" \
"M v[8:11],a[68:71],v[64:67],0\nv_exp_f32 v87,v87\nv_cvt_pk_bf16_f32 v70,v76,v77\nM v[8:11],a[100:103],v[80:83],v[8:11]\nv_exp_f32 v92,v92\nv_cvt_pk_bf16_f32 v71,v78,v79\nM v[16:19],a[72:75],v[64:67],0\nv_exp_f32 v93,v93\n" \
"v_cvt_pk_bf16_f32 v84,v84,v85\nM v[16:19],a[104:107],v[80:83],v[16:19]\nv_exp_f32 v94,v94\nM v[24:27],a[76:79],v[64:67],0\nv_exp_f32 v95,v95\nv_cvt_pk_bf16_f32 v85,v86,v87\nM v[24:27],a[108:111],v[80:83],v[24:27]\nv_cvt_pk_bf16_f32 v86,v92,v93\n" \
"v_cvt_pk_bf16_f32 v87,v94,v95\nds_read_b128 a[0:3],v240 offset:32768\nM v[32:35],a[80:83],v[64:67],0\nds_read_b128 a[4:7],v240 offset:33792\nds_read_b128 a[8:11],v240 offset:34816\nv_cndmask_b32 v112,v112,v252,s[82:83]\nM v[32:35],a[112:115],v[80:83],v[32:35]\nds_read_b128 a[12:15],v240 offset:35840\n" \
"v_exp_f32 v96,v96\nM v[40:43],a[84:87],v[64:67],0\nds_read_b128 a[16:19],v240 offset:36864\nv_exp_f32 v97,v97\nM v[40:43],a[116:119],v[80:83],v[40:43]\nds_read_b128 a[20:23],v240 offset:37888\nv_cndmask_b32 v113,v113,v252,s[84:85]\ns_waitcnt lgkmcnt(6)\n" \
"M v[48:51],a[88:91],v[64:67],0\nds_read_b128 a[24:27],v240 offset:38912\nv_exp_f32 v98,v98\nM v[48:51],a[120:123],v[80:83],v[48:51]\nds_read_b128 a[28:31],v240 offset:39936\nv_exp_f32 v99,v99\nM v[56:59],a[92:95],v[64:67],0\nbuffer_load_dword v248,v251,s[52:55],s80 offen sc1\n" \
"ds_read_b128 a[32:35],v240 offset:40960\nM v[56:59],a[124:127],v[80:83],v[56:59]\nds_read_b128 a[36:39],v240 offset:41984\nv_exp_f32 v104,v104\nM a[192:195],a[208:211],v[64:67],0\nds_read_b128 a[40:43],v240 offset:43008\nv_cndmask_b32 v114,v114,v252,s[86:87]\nM a[192:195],a[208:211],v[80:83],a[192:195]\n" \
"ds_read_b128 a[44:47],v240 offset:44032\nv_exp_f32 v105,v105\nM v[4:7],a[64:67],v[68:71],0\nds_read_b128 a[48:51],v240 offset:45056\nv_exp_f32 v106,v106\nM v[4:7],a[96:99],v[84:87],v[4:7]\nds_read_b128 a[52:55],v240 offset:46080\nv_exp_f32 v107,v107\n" \
"M v[12:15],a[68:71],v[68:71],0\nds_read_b128 a[56:59],v240 offset:47104\nv_cndmask_b32 v115,v115,v252,s[88:89]\nv_cvt_pk_bf16_f32 v96,v96,v97\nM v[12:15],a[100:103],v[84:87],v[12:15]\nds_read_b128 a[60:63],v240 offset:48128\nv_exp_f32 v112,v112\nM v[20:23],a[72:75],v[68:71],0\n" \
"ds_read_b128 a[64:67],v241 offset:16384\nv_exp_f32 v113,v113\nM v[20:23],a[104:107],v[84:87],v[20:23]\nds_read_b128 a[96:99],v241 offset:24576\nv_exp_f32 v114,v114\nM v[28:31],a[76:79],v[68:71],0\nds_read_b128 a[68:71],v241 offset:17408\nv_exp_f32 v115,v115\n" \
"M v[28:31],a[108:111],v[84:87],v[28:31]\nds_read_b128 a[100:103],v241 offset:25600\nv_cvt_pk_bf16_f32 v97,v98,v99\nv_cvt_pk_bf16_f32 v98,v104,v105\nM v[36:39],a[80:83],v[68:71],0\nds_read_b128 a[72:75],v241 offset:18432\nv_cvt_pk_bf16_f32 v99,v106,v107\nM v[36:39],a[112:115],v[84:87],v[36:39]\n" \
"ds_read_b128 a[104:107],v241 offset:26624\nv_cvt_pk_bf16_f32 v112,v112,v113\nv_cvt_pk_bf16_f32 v113,v114,v115\nM v[44:47],a[84:87],v[68:71],0\nds_read_b128 a[76:79],v241 offset:19456\nv_mov_b32 v114,0\nv_mov_b32 v115,0\nM v[44:47],a[116:119],v[84:87],v[44:47]\n" \
"ds_read_b128 a[108:111],v241 offset:27648\nv_exp_f32 v100,v100\nM v[52:55],a[88:91],v[68:71],0\nds_read_b128 a[80:83],v241 offset:20480\nv_exp_f32 v101,v101\nM v[52:55],a[120:123],v[84:87],v[52:55]\nds_read_b128 a[112:115],v241 offset:28672\nv_exp_f32 v102,v102\n" \
"M v[60:63],a[92:95],v[68:71],0\nds_read_b128 a[84:87],v241 offset:21504\nv_exp_f32 v103,v103\nM v[60:63],a[124:127],v[84:87],v[60:63]\nds_read_b128 a[116:119],v241 offset:29696\nv_exp_f32 v108,v108\nM a[196:199],a[208:211],v[68:71],0\nds_read_b128 a[88:91],v241 offset:22528\n" \
"v_exp_f32 v109,v109\nM a[196:199],a[208:211],v[84:87],a[196:199]\nds_read_b128 a[120:123],v241 offset:30720\nv_cvt_pk_bf16_f32 v100,v100,v101\ns_waitcnt lgkmcnt(8)\nM v[128:131],a[0:3],a[128:131],v[224:227]\nds_read_b128 a[92:95],v241 offset:23552\nv_exp_f32 v110,v110\n" \
"M v[128:131],a[4:7],a[132:135],v[128:131]\nds_read_b128 a[124:127],v241 offset:31744\nv_cndmask_b32 v124,v124,v252,s[82:83]\nM v[128:131],a[8:11],a[136:139],v[128:131]\nv_exp_f32 v111,v111\nv_cndmask_b32 v125,v125,v252,s[84:85]\nM v[128:131],a[12:15],a[140:143],v[128:131]\nv_exp_f32 v116,v116\n" \
"v_cndmask_b32 v126,v126,v252,s[86:87]\nM v[132:135],a[12:15],a[156:159],v[228:231]\nv_exp_f32 v117,v117\nv_cndmask_b32 v127,v127,v252,s[88:89]\nM v[132:135],a[8:11],a[152:155],v[132:135]\nv_exp_f32 v118,v118\nv_cvt_pk_bf16_f32 v101,v102,v103\nM v[132:135],a[4:7],a[148:151],v[132:135]\n" \
"v_exp_f32 v119,v119\nv_cvt_pk_bf16_f32 v102,v108,v109\nM v[132:135],a[0:3],a[144:147],v[132:135]\nv_exp_f32 v124,v124\nv_cvt_pk_bf16_f32 v103,v110,v111\nM v[144:147],a[16:19],a[128:131],v[224:227]\nv_exp_f32 v125,v125\nv_cvt_pk_bf16_f32 v116,v116,v117\n" \
"M v[144:147],a[20:23],a[132:135],v[144:147]\nv_exp_f32 v126,v126\nv_cvt_pk_bf16_f32 v117,v118,v119\nM v[144:147],a[24:27],a[136:139],v[144:147]\nv_exp_f32 v127,v127\nv_cvt_pk_bf16_f32 v118,v124,v125\nM v[144:147],a[28:31],a[140:143],v[144:147]\nv_cvt_pk_bf16_f32 v119,v126,v127\n" \
"ds_read_b128 a[0:3],v240 offset:49152\nds_read_b128 a[4:7],v240 offset:50176\nM v[148:151],a[28:31],a[156:159],v[228:231]\nds_read_b128 a[8:11],v240 offset:51200\nv_mov_b32 v191,0xff800000\nM v[148:151],a[24:27],a[152:155],v[148:151]\nds_read_b128 a[12:15],v240 offset:52224\nv_exp_f32 v128,v128\n" \
"M v[148:151],a[20:23],a[148:151],v[148:151]\nds_read_b128 a[28:31],v240 offset:56320\nv_exp_f32 v129,v129\nM v[148:151],a[16:19],a[144:147],v[148:151]\nds_read_b128 a[24:27],v240 offset:55296\nv_exp_f32 v130,v130\nM v[160:163],a[32:35],a[128:131],v[224:227]\nds_read_b128 a[20:23],v240 offset:54272\n" \
"v_exp_f32 v131,v131\nM v[160:163],a[36:39],a[132:135],v[160:163]\nds_read_b128 a[16:19],v240 offset:53248\nv_cvt_pk_bf16_f32 v192,v128,v129\nv_cvt_pk_bf16_f32 v193,v130,v131\nM v[160:163],a[40:43],a[136:139],v[160:163]\nv_exp_f32 v132,v132\nv_mov_b32 v202,0\n" \
"M v[160:163],a[44:47],a[140:143],v[160:163]\nv_exp_f32 v133,v133\nv_mov_b32 v203,0\nM v[164:167],a[44:47],a[156:159],v[228:231]\nv_exp_f32 v134,v134\nv_cvt_pk_bf16_f32 v196,v132,v133\nM v[164:167],a[40:43],a[152:155],v[164:167]\nv_exp_f32 v135,v135\n" \
"ds_read_b128 a[40:43],v240 offset:59392\nM v[164:167],a[36:39],a[148:151],v[164:167]\nv_cvt_pk_bf16_f32 v197,v134,v135\nv_exp_f32 v144,v144\nM v[164:167],a[32:35],a[144:147],v[164:167]\nds_read_b128 a[32:35],v240 offset:57344\nds_read_b128 a[36:39],v240 offset:58368\nM v[176:179],a[48:51],a[128:131],v[224:227]\n" \
"v_exp_f32 v145,v145\nds_read_b128 a[44:47],v240 offset:60416\nM v[176:179],a[52:55],a[132:135],v[176:179]\nv_exp_f32 v146,v146\nv_cvt_pk_bf16_f32 v194,v144,v145\nM v[176:179],a[56:59],a[136:139],v[176:179]\nv_exp_f32 v147,v147\ns_waitcnt vmcnt(0)\n" \
"v_cmp_eq_u32 vcc,0,v248\nM v[176:179],a[60:63],a[140:143],v[176:179]\nv_cvt_pk_bf16_f32 v195,v146,v147\nv_exp_f32 v148,v148\nM v[180:183],a[60:63],a[156:159],v[228:231]\ns_waitcnt lgkmcnt(4)\ns_barrier\nds_read_b128 a[60:63],v240 offset:64512\n" \
"M v[180:183],a[56:59],a[152:155],v[180:183]\nv_exp_f32 v149,v149\nds_read_b128 a[56:59],v240 offset:63488\nM v[180:183],a[52:55],a[148:151],v[180:183]\nv_exp_f32 v150,v150\nv_cvt_pk_bf16_f32 v198,v148,v149\nM v[180:183],a[48:51],a[144:147],v[180:183]\nv_exp_f32 v151,v151\n" \
"ds_read_b128 a[48:51],v240 offset:61440\nM v[0:3],a[64:67],v[96:99],v[0:3]\nv_cvt_pk_bf16_f32 v199,v150,v151\nv_exp_f32 v160,v160\nM v[0:3],a[96:99],v[112:115],v[0:3]\nv_exp_f32 v161,v161\nds_read_b128 a[52:55],v240 offset:62464\nM v[8:11],a[68:71],v[96:99],v[8:11]\n" \
"v_exp_f32 v162,v162\nM v[8:11],a[100:103],v[112:115],v[8:11]\nv_exp_f32 v163,v163\nv_cvt_pk_bf16_f32 v208,v160,v161\nM v[16:19],a[72:75],v[96:99],v[16:19]\nv_cvt_pk_bf16_f32 v209,v162,v163\nds_write_b128 v250,a[244:247]\nM v[16:19],a[104:107],v[112:115],v[16:19]\n" \
"v_exp_f32 v164,v164\nds_read_b128 v[64:67],v250\nM v[24:27],a[76:79],v[96:99],v[24:27]\nv_exp_f32 v165,v165\nds_read_b128 v[68:71],v250\nM v[24:27],a[108:111],v[112:115],v[24:27]\nv_exp_f32 v166,v166\nds_read_b128 v[72:75],v250\n" \
"M v[32:35],a[80:83],v[96:99],v[32:35]\nv_exp_f32 v167,v167\nds_read_b128 v[76:79],v250\nM v[32:35],a[112:115],v[112:115],v[32:35]\nds_read_b128 v[80:83],v250\nds_read_b128 v[84:87],v250\nv_cvt_pk_bf16_f32 v212,v164,v165\nM v[40:43],a[84:87],v[96:99],v[40:43]\n" \
"ds_read_b128 v[88:91],v250\nv_cvt_pk_bf16_f32 v213,v166,v167\ns_cbranch_vccnz .Lps5\n.Lpb5:\nM v[40:43],a[116:119],v[112:115],v[40:43]\nds_read_b128 v[92:95],v250\nv_exp_f32 v176,v176\nM v[48:51],a[88:91],v[96:99],v[48:51]\n" \
"ds_read_b128 v[104:107],v250\nv_exp_f32 v177,v177\nM v[48:51],a[120:123],v[112:115],v[48:51]\nds_read_b128 v[108:111],v250\nv_cvt_pk_bf16_f32 v210,v176,v177\nM v[56:59],a[92:95],v[96:99],v[56:59]\nds_read_b128 v[120:123],v250\nv_exp_f32 v178,v178\n" \
"M v[56:59],a[124:127],v[112:115],v[56:59]\nds_read_b128 v[124:127],v250\nv_exp_f32 v179,v179\nM a[192:195],a[208:211],v[96:99],a[192:195]\nds_read_b128 v[96:99],v250\nv_cvt_pk_bf16_f32 v211,v178,v179\ns_add_u32 m0,s64,65536\nM a[192:195],a[208:211],v[112:115],a[192:195]\n" \
"ds_read_b128 v[112:115],v250\nv_exp_f32 v180,v180\nM v[4:7],a[64:67],v[100:103],v[4:7]\nds_read_b128 a[200:203],v250\nv_exp_f32 v181,v181\nM v[4:7],a[96:99],v[116:119],v[4:7]\nds_read_b128 a[204:207],v250\nv_exp_f32 v182,v182\n" \
"M v[12:15],a[68:71],v[100:103],v[12:15]\nds_read_b128 a[64:67],v241 offset:32768\nv_exp_f32 v183,v183\nM v[12:15],a[100:103],v[116:119],v[12:15]\nds_read_b128 a[96:99],v241 offset:40960\nv_cvt_pk_bf16_f32 v214,v180,v181\nv_cvt_pk_bf16_f32 v215,v182,v183\nM v[20:23],a[72:75],v[100:103],v[20:23]\n" \
"ds_read_b128 a[68:71],v241 offset:33792\nbuffer_load_dwordx4 v240,s[48:51],s36 offen offset:0 sc1 lds\nM v[20:23],a[104:107],v[116:119],v[20:23]\nds_read_b128 a[100:103],v241 offset:41984\nM v[28:31],a[76:79],v[100:103],v[28:31]\nds_read_b128 a[72:75],v241 offset:34816\nM v[28:31],a[108:111],v[116:119],v[28:31]\nds_read_b128 a[104:107],v241 offset:43008\n" \
"M v[36:39],a[80:83],v[100:103],v[36:39]\nds_read_b128 a[76:79],v241 offset:35840\nbuffer_load_dwordx4 v240,s[48:51],s36 offen offset:1024 sc1 lds\nM v[36:39],a[112:115],v[116:119],v[36:39]\nds_read_b128 a[108:111],v241 offset:44032\nM v[44:47],a[84:87],v[100:103],v[44:47]\nds_read_b128 a[80:83],v241 offset:36864\nM v[44:47],a[116:119],v[116:119],v[44:47]\n" \
"ds_read_b128 a[112:115],v241 offset:45056\nM v[52:55],a[88:91],v[100:103],v[52:55]\nds_read_b128 a[84:87],v241 offset:37888\nbuffer_load_dwordx4 v240,s[48:51],s36 offen offset:2048 sc1 lds\nM v[52:55],a[120:123],v[116:119],v[52:55]\nds_read_b128 a[116:119],v241 offset:46080\nM v[60:63],a[92:95],v[100:103],v[60:63]\nds_read_b128 a[88:91],v241 offset:38912\n" \
"M v[60:63],a[124:127],v[116:119],v[60:63]\nds_read_b128 a[120:123],v241 offset:47104\nM a[196:199],a[208:211],v[100:103],a[196:199]\nds_read_b128 v[100:103],v250\nbuffer_load_dwordx4 v240,s[48:51],s36 offen offset:3072 sc1 lds\nM a[196:199],a[208:211],v[116:119],a[196:199]\nds_read_b128 v[116:119],v250\ns_add_u32 m0,s64,16384\n" \
"M v[136:139],a[0:3],a[160:163],0\nds_read_b128 a[92:95],v241 offset:39936\nv_add_u32 v250,0xc000,v241\nM v[136:139],a[4:7],a[164:167],v[136:139]\nds_read_b128 a[124:127],v241 offset:48128\nM v[136:139],a[8:11],a[168:171],v[136:139]\nbuffer_load_dwordx4 v240,s[44:47],s65 offen offset:0 sc1 lds\nM v[136:139],a[12:15],a[172:175],v[136:139]\n" \
"M v[140:143],a[12:15],a[188:191],0\nM v[140:143],a[8:11],a[184:187],v[140:143]\nM v[140:143],a[4:7],a[180:183],v[140:143]\nbuffer_load_dwordx4 v240,s[44:47],s65 offen offset:1024 sc1 lds\nM v[140:143],a[0:3],a[176:179],v[140:143]\nM v[156:159],a[28:31],a[188:191],0\nM v[156:159],a[24:27],a[184:187],v[156:159]\nM v[156:159],a[20:23],a[180:183],v[156:159]\n" \
"v_max3_f32 v233,v136,v137,v138\nv_max_f32 v233,v233,v139\nv_cndmask_b32 v233,v232,v233,s[90:91]\nM v[156:159],a[16:19],a[176:179],v[156:159]\nbuffer_load_dwordx4 v240,s[44:47],s65 offen offset:2048 sc1 lds\nv_max3_f32 v237,v140,v141,v142\nM v[128:131],a[0:3],a[128:131],v[224:227]\nv_cndmask_b32 v136,v136,v252,s[82:83]\n" \
"v_cndmask_b32 v137,v137,v252,s[84:85]\nv_cndmask_b32 v138,v138,v252,s[86:87]\nM v[128:131],a[4:7],a[132:135],v[128:131]\nv_cndmask_b32 v139,v139,v252,s[88:89]\nv_max3_f32 v237,v237,v143,v156\nv_max3_f32 v237,v237,v157,v158\nM v[128:131],a[8:11],a[136:139],v[128:131]\nv_max_f32 v237,v237,v159\n" \
"v_cndmask_b32 v237,v236,v237,s[90:91]\nv_cndmask_b32 v156,v156,v252,s[82:83]\nM v[128:131],a[12:15],a[140:143],v[128:131]\nv_permlane16_swap_b32 v233,v237\nv_max_f32 v233,v233,v237\nv_mov_b32 v237,v233\nM v[132:135],a[12:15],a[156:159],v[228:231]\nbuffer_load_dwordx4 v240,s[44:47],s65 offen offset:3072 sc1 lds\n" \
"v_permlane32_swap_b32 v233,v237\nM v[132:135],a[8:11],a[152:155],v[132:135]\nv_max_f32 v233,v233,v237\ns_add_u32 m0,s64,81920\nv_mov_b32 v237,v233\nM v[132:135],a[4:7],a[148:151],v[132:135]\nv_cndmask_b32 v157,v157,v252,s[84:85]\nv_permlane16_swap_b32 v233,v237\n" \
"v_sub_f32 v232,0,v233\nM v[132:135],a[0:3],a[144:147],v[132:135]\nds_read_b128 a[0:3],v240 offset:0\nds_read_b128 a[4:7],v240 offset:1024\nv_add_f32 v136,v136,v232\nM v[144:147],a[16:19],a[128:131],v[224:227]\nds_read_b128 a[8:11],v240 offset:2048\nv_add_f32 v137,v137,v232\n" \
"M v[144:147],a[20:23],a[132:135],v[144:147]\nds_read_b128 a[12:15],v240 offset:3072\nbuffer_load_dwordx4 v240,s[48:51],s65 offen offset:0 sc1 lds\nM v[144:147],a[24:27],a[136:139],v[144:147]\nv_add_f32 v138,v138,v232\nv_add_f32 v139,v139,v232\nv_cndmask_b32 v158,v158,v252,s[86:87]\nM v[144:147],a[28:31],a[140:143],v[144:147]\n" \
"v_cndmask_b32 v159,v159,v252,s[88:89]\nv_sub_f32 v236,0,v237\nv_sub_f32 v234,0,v233\nM v[148:151],a[28:31],a[156:159],v[228:231]\nv_exp_f32 v136,v136\nv_sub_f32 v235,0,v233\nM v[148:151],a[24:27],a[152:155],v[148:151]\nbuffer_load_dwordx4 v240,s[48:51],s65 offen offset:1024 sc1 lds\n" \
"v_sub_f32 v238,0,v237\nM v[148:151],a[20:23],a[148:151],v[148:151]\nv_exp_f32 v137,v137\nv_sub_f32 v239,0,v237\nM v[148:151],a[16:19],a[144:147],v[148:151]\nds_read_b128 a[16:19],v240 offset:4096\nds_read_b128 a[20:23],v240 offset:5120\ns_waitcnt lgkmcnt(5)\n" \
"M v[160:163],a[32:35],a[128:131],v[224:227]\nds_read_b128 a[24:27],v240 offset:6144\nv_exp_f32 v138,v138\nM v[160:163],a[36:39],a[132:135],v[160:163]\nds_read_b128 a[28:31],v240 offset:7168\nbuffer_load_dwordx4 v240,s[48:51],s65 offen offset:2048 sc1 lds\nM v[160:163],a[40:43],a[136:139],v[160:163]\nv_exp_f32 v139,v139\n" \
"v_add_f32 v140,v140,v236\nM v[160:163],a[44:47],a[140:143],v[160:163]\nv_exp_f32 v128,v128\nM v[164:167],a[44:47],a[156:159],v[228:231]\ns_barrier\nv_exp_f32 v129,v129\nM v[164:167],a[40:43],a[152:155],v[164:167]\nbuffer_load_dwordx4 v240,s[48:51],s65 offen offset:3072 sc1 lds\n" \
"s_add_u32 m0,s64,32768\nM v[164:167],a[36:39],a[148:151],v[164:167]\nv_exp_f32 v130,v130\nv_add_f32 v141,v141,v236\nM v[164:167],a[32:35],a[144:147],v[164:167]\nds_read_b128 a[32:35],v240 offset:8192\nds_read_b128 a[36:39],v240 offset:9216\nv_add_f32 v142,v142,v236\n" \
"M v[176:179],a[48:51],a[128:131],v[224:227]\nds_read_b128 a[40:43],v240 offset:10240\nv_exp_f32 v131,v131\nM v[176:179],a[52:55],a[132:135],v[176:179]\nds_read_b128 a[44:47],v240 offset:11264\nbuffer_load_dwordx4 v240,s[44:47],s66 offen offset:0 sc1 lds\nM v[176:179],a[56:59],a[136:139],v[176:179]\nv_exp_f32 v132,v132\n" \
"v_add_f32 v143,v143,v236\nM v[176:179],a[60:63],a[140:143],v[176:179]\nv_exp_f32 v133,v133\nv_add_f32 v156,v156,v236\nM v[180:183],a[60:63],a[156:159],v[228:231]\nv_exp_f32 v134,v134\nv_add_f32 v157,v157,v236\nM v[180:183],a[56:59],a[152:155],v[180:183]\n" \
"buffer_load_dwordx4 v240,s[44:47],s66 offen offset:1024 sc1 lds\nv_add_f32 v158,v158,v236\nM v[180:183],a[52:55],a[148:151],v[180:183]\nv_exp_f32 v135,v135\nM v[180:183],a[48:51],a[144:147],v[180:183]\nds_read_b128 a[48:51],v240 offset:12288\nds_read_b128 a[52:55],v240 offset:13312\nv_add_f32 v159,v159,v236\n" \
"M v[0:3],a[64:67],v[192:195],v[0:3]\nds_read_b128 a[56:59],v240 offset:14336\nv_exp_f32 v144,v144\nM v[0:3],a[96:99],v[208:211],v[0:3]\nds_read_b128 a[60:63],v240 offset:15360\nbuffer_load_dwordx4 v240,s[44:47],s66 offen offset:2048 sc1 lds\nM v[8:11],a[68:71],v[192:195],v[8:11]\nv_exp_f32 v145,v145\n" \
"v_sub_f32 v233,0,v233\nM v[8:11],a[100:103],v[208:211],v[8:11]\nv_exp_f32 v146,v146\nv_sub_f32 v237,0,v237\nM v[16:19],a[72:75],v[192:195],v[16:19]\nv_exp_f32 v147,v147\nv_cvt_pk_bf16_f32 v200,v136,v137\nM v[16:19],a[104:107],v[208:211],v[16:19]\n" \
"buffer_load_dwordx4 v240,s[44:47],s66 offen offset:3072 sc1 lds\ns_add_u32 m0,s64,98304\nM v[24:27],a[76:79],v[192:195],v[24:27]\nv_exp_f32 v148,v148\nv_cvt_pk_bf16_f32 v201,v138,v139\nM v[24:27],a[108:111],v[208:211],v[24:27]\nv_exp_f32 v149,v149\nM v[32:35],a[80:83],v[192:195],v[32:35]\n" \
"v_exp_f32 v150,v150\nM v[32:35],a[112:115],v[208:211],v[32:35]\nv_exp_f32 v151,v151\nM v[40:43],a[84:87],v[192:195],v[40:43]\nv_exp_f32 v160,v160\nM v[40:43],a[116:119],v[208:211],v[40:43]\nv_exp_f32 v161,v161\nM v[48:51],a[88:91],v[192:195],v[48:51]\n" \
"v_exp_f32 v162,v162\nM v[48:51],a[120:123],v[208:211],v[48:51]\nv_exp_f32 v163,v163\nM v[56:59],a[92:95],v[192:195],v[56:59]\nv_exp_f32 v176,v176\nM v[56:59],a[124:127],v[208:211],v[56:59]\nv_exp_f32 v177,v177\nM a[192:195],a[208:211],v[192:195],a[192:195]\n" \
"v_exp_f32 v178,v178\nv_cvt_pk_bf16_f32 v192,v128,v129\nM a[192:195],a[208:211],v[208:211],a[192:195]\nv_exp_f32 v179,v179\nv_cvt_pk_bf16_f32 v193,v130,v131\nM v[4:7],a[64:67],v[196:199],v[4:7]\nv_exp_f32 v140,v140\nv_cvt_pk_bf16_f32 v194,v144,v145\n" \
"M v[4:7],a[96:99],v[212:215],v[4:7]\nv_exp_f32 v141,v141\nv_cvt_pk_bf16_f32 v195,v146,v147\nM v[12:15],a[68:71],v[196:199],v[12:15]\nv_exp_f32 v142,v142\nM v[12:15],a[100:103],v[212:215],v[12:15]\nv_exp_f32 v143,v143\nv_cvt_pk_bf16_f32 v208,v160,v161\n" \
"M v[20:23],a[72:75],v[196:199],v[20:23]\nv_exp_f32 v156,v156\nv_cvt_pk_bf16_f32 v209,v162,v163\nM v[20:23],a[104:107],v[212:215],v[20:23]\nv_exp_f32 v157,v157\nv_cvt_pk_bf16_f32 v210,v176,v177\nM v[28:31],a[76:79],v[196:199],v[28:31]\nv_exp_f32 v158,v158\n" \
"v_cvt_pk_bf16_f32 v211,v178,v179\nM v[28:31],a[108:111],v[212:215],v[28:31]\nv_exp_f32 v159,v159\nM v[36:39],a[80:83],v[196:199],v[36:39]\nv_exp_f32 v164,v164\nM v[36:39],a[112:115],v[212:215],v[36:39]\nv_exp_f32 v165,v165\nM v[44:47],a[84:87],v[196:199],v[44:47]\n" \
"v_exp_f32 v166,v166\nM v[44:47],a[116:119],v[212:215],v[44:47]\nv_exp_f32 v167,v167\nM v[52:55],a[88:91],v[196:199],v[52:55]\nv_exp_f32 v180,v180\nM v[52:55],a[120:123],v[212:215],v[52:55]\nv_exp_f32 v181,v181\nM v[60:63],a[92:95],v[196:199],v[60:63]\n" \
"v_exp_f32 v182,v182\nM v[60:63],a[124:127],v[212:215],v[60:63]\nv_exp_f32 v183,v183\nM a[196:199],a[208:211],v[196:199],a[196:199]\nv_cvt_pk_bf16_f32 v196,v132,v133\nv_cvt_pk_bf16_f32 v197,v134,v135\nv_cvt_pk_bf16_f32 v198,v148,v149\nv_cvt_pk_bf16_f32 v199,v150,v151\n" \
"buffer_load_dwordx4 v240,s[48:51],s66 offen offset:0 sc1 lds\nbuffer_load_dwordx4 v240,s[48:51],s66 offen offset:1024 sc1 lds\nbuffer_load_dwordx4 v240,s[48:51],s66 offen offset:2048 sc1 lds\nbuffer_load_dwordx4 v240,s[48:51],s66 offen offset:3072 sc1 lds\nM a[196:199],a[208:211],v[212:215],a[196:199]\ns_cmp_eq_u32 s62,0\ns_cbranch_scc1 .Ltrans\n.Ldual:\n" \
"s_waitcnt lgkmcnt(4)\nM v[128:131],a[0:3],a[128:131],v[224:227]\ns_add_u32 m0,s94,s64\nv_cvt_pk_bf16_f32 v204,v140,v141\nM v[128:131],a[4:7],a[132:135],v[128:131]\nds_read_b128 a[64:67],v250 offset:0\nds_read_b128 a[96:99],v250 offset:8192\nM v[128:131],a[8:11],a[136:139],v[128:131]\n" \
"v_cvt_pk_bf16_f32 v205,v142,v143\nv_cvt_pk_bf16_f32 v206,v156,v157\nM v[128:131],a[12:15],a[140:143],v[128:131]\nv_cvt_pk_bf16_f32 v207,v158,v159\nv_cvt_pk_bf16_f32 v212,v164,v165\nM v[132:135],a[12:15],a[156:159],v[228:231]\nbuffer_load_dwordx4 v240,s[44:47],s95 offen offset:0 sc1 lds\nM v[132:135],a[8:11],a[152:155],v[132:135]\n" \
"v_cvt_pk_bf16_f32 v213,v166,v167\nv_cvt_pk_bf16_f32 v216,v168,v169\nM v[132:135],a[4:7],a[148:151],v[132:135]\nv_cvt_pk_bf16_f32 v217,v170,v171\nv_cvt_pk_bf16_f32 v214,v180,v181\nM v[132:135],a[0:3],a[144:147],v[132:135]\nds_read_b128 a[68:71],v250 offset:1024\nds_read_b128 a[100:103],v250 offset:9216\n" \
"M v[136:139],a[0:3],a[160:163],v[232:235]\nv_exp_f32 v172,v172\nM v[136:139],a[4:7],a[164:167],v[136:139]\nv_exp_f32 v173,v173\nM v[136:139],a[8:11],a[168:171],v[136:139]\nv_exp_f32 v174,v174\nM v[136:139],a[12:15],a[172:175],v[136:139]\nv_exp_f32 v175,v175\n" \
"M v[140:143],a[12:15],a[188:191],v[236:239]\nv_cvt_pk_bf16_f32 v220,v172,v173\nv_cvt_pk_bf16_f32 v221,v174,v175\nM v[140:143],a[8:11],a[184:187],v[140:143]\nds_read_b128 a[72:75],v250 offset:2048\nds_read_b128 a[104:107],v250 offset:10240\nM v[140:143],a[4:7],a[180:183],v[140:143]\nv_cvt_pk_bf16_f32 v215,v182,v183\n" \
"s_add_u32 s94,s94,0x4000\nM v[140:143],a[0:3],a[176:179],v[140:143]\nv_exp_f32 v184,v184\nM v[144:147],a[16:19],a[128:131],v[224:227]\nv_exp_f32 v185,v185\nM v[144:147],a[20:23],a[132:135],v[144:147]\nv_exp_f32 v186,v186\nM v[144:147],a[24:27],a[136:139],v[144:147]\n" \
"v_exp_f32 v187,v187\nM v[144:147],a[28:31],a[140:143],v[144:147]\nds_read_b128 a[76:79],v250 offset:3072\nds_read_b128 a[108:111],v250 offset:11264\nM v[148:151],a[28:31],a[156:159],v[228:231]\nv_cvt_pk_bf16_f32 v218,v184,v185\nv_cvt_pk_bf16_f32 v219,v186,v187\nM v[148:151],a[24:27],a[152:155],v[148:151]\n" \
"v_exp_f32 v188,v188\nM v[148:151],a[20:23],a[148:151],v[148:151]\nbuffer_load_dwordx4 v240,s[44:47],s95 offen offset:1024 sc1 lds\nM v[148:151],a[16:19],a[144:147],v[148:151]\nv_exp_f32 v189,v189\nM v[152:155],a[16:19],a[160:163],v[232:235]\nv_exp_f32 v190,v190\nM v[152:155],a[20:23],a[164:167],v[152:155]\n" \
"ds_read_b128 a[80:83],v250 offset:4096\nds_read_b128 a[112:115],v250 offset:12288\nM v[152:155],a[24:27],a[168:171],v[152:155]\nv_exp_f32 v191,v191\nM v[152:155],a[28:31],a[172:175],v[152:155]\nv_cvt_pk_bf16_f32 v222,v188,v189\nv_cvt_pk_bf16_f32 v223,v190,v191\nM v[156:159],a[28:31],a[188:191],v[236:239]\n" \
"v_exp_f32 v128,v128\nM v[156:159],a[24:27],a[184:187],v[156:159]\nv_exp_f32 v129,v129\nM v[156:159],a[20:23],a[180:183],v[156:159]\nv_exp_f32 v130,v130\nM v[156:159],a[16:19],a[176:179],v[156:159]\nds_read_b128 a[84:87],v250 offset:5120\nds_read_b128 a[116:119],v250 offset:13312\n" \
"M v[160:163],a[32:35],a[128:131],v[224:227]\nv_exp_f32 v131,v131\nM v[160:163],a[36:39],a[132:135],v[160:163]\nv_exp_f32 v144,v144\nM v[160:163],a[40:43],a[136:139],v[160:163]\nv_exp_f32 v145,v145\nM v[160:163],a[44:47],a[140:143],v[160:163]\nv_exp_f32 v146,v146\n" \
"M v[164:167],a[44:47],a[156:159],v[228:231]\nv_exp_f32 v147,v147\nM v[164:167],a[40:43],a[152:155],v[164:167]\nds_read_b128 a[88:91],v250 offset:6144\nds_read_b128 a[120:123],v250 offset:14336\nM v[164:167],a[36:39],a[148:151],v[164:167]\nv_exp_f32 v132,v132\nM v[164:167],a[32:35],a[144:147],v[164:167]\n" \
"v_exp_f32 v133,v133\nM v[168:171],a[32:35],a[160:163],v[232:235]\nbuffer_load_dwordx4 v240,s[44:47],s95 offen offset:2048 sc1 lds\nM v[168:171],a[36:39],a[164:167],v[168:171]\nv_exp_f32 v134,v134\nM v[168:171],a[40:43],a[168:171],v[168:171]\nv_exp_f32 v135,v135\nM v[168:171],a[44:47],a[172:175],v[168:171]\n" \
"ds_read_b128 a[92:95],v250 offset:7168\nds_read_b128 a[124:127],v250 offset:15360\nM v[172:175],a[44:47],a[188:191],v[236:239]\nv_xor_b32 v250,0x18000,v250\ns_and_b32 s94,s94,0xffff\nM v[172:175],a[40:43],a[184:187],v[172:175]\nv_exp_f32 v148,v148\nM v[172:175],a[36:39],a[180:183],v[172:175]\n" \
"v_exp_f32 v149,v149\nM v[172:175],a[32:35],a[176:179],v[172:175]\ns_waitcnt lgkmcnt(4)\nM v[176:179],a[48:51],a[128:131],v[224:227]\nv_exp_f32 v150,v150\nM v[176:179],a[52:55],a[132:135],v[176:179]\nv_exp_f32 v151,v151\nM v[176:179],a[56:59],a[136:139],v[176:179]\n" \
"v_exp_f32 v136,v136\nM v[176:179],a[60:63],a[140:143],v[176:179]\nv_exp_f32 v137,v137\nM v[180:183],a[60:63],a[156:159],v[228:231]\nv_exp_f32 v138,v138\nM v[180:183],a[56:59],a[152:155],v[180:183]\nv_exp_f32 v139,v139\nM v[180:183],a[52:55],a[148:151],v[180:183]\n" \
"v_exp_f32 v152,v152\nM v[180:183],a[48:51],a[144:147],v[180:183]\nv_exp_f32 v153,v153\nM v[184:187],a[48:51],a[160:163],v[232:235]\nv_exp_f32 v154,v154\nM v[184:187],a[52:55],a[164:167],v[184:187]\nv_exp_f32 v155,v155\nM v[184:187],a[56:59],a[168:171],v[184:187]\n" \
"buffer_load_dwordx4 v240,s[44:47],s95 offen offset:3072 sc1 lds\nM v[184:187],a[60:63],a[172:175],v[184:187]\nv_exp_f32 v160,v160\nM v[188:191],a[60:63],a[188:191],v[236:239]\nv_exp_f32 v161,v161\nM v[188:191],a[56:59],a[184:187],v[188:191]\nv_exp_f32 v162,v162\nM v[188:191],a[52:55],a[180:183],v[188:191]\n" \
"v_exp_f32 v163,v163\nM v[188:191],a[48:51],a[176:179],v[188:191]\nv_exp_f32 v176,v176\nM v[0:3],a[64:67],v[192:195],v[0:3]\ns_waitcnt vmcnt(16) lgkmcnt(0)\ns_barrier\nM v[0:3],a[96:99],v[208:211],v[0:3]\nds_read_b128 a[0:3],v250 offset:0\n" \
"ds_read_b128 a[4:7],v250 offset:1024\nM v[8:11],a[68:71],v[192:195],v[8:11]\nv_exp_f32 v177,v177\nM v[8:11],a[100:103],v[208:211],v[8:11]\nv_exp_f32 v178,v178\nM v[16:19],a[72:75],v[192:195],v[16:19]\ns_add_u32 m0,m0,0x10000\nM v[16:19],a[104:107],v[208:211],v[16:19]\n" \
"buffer_load_dwordx4 v240,s[48:51],s95 offen offset:0 sc1 lds\nM v[24:27],a[76:79],v[192:195],v[24:27]\nv_exp_f32 v179,v179\nM v[24:27],a[108:111],v[208:211],v[24:27]\nv_exp_f32 v140,v140\nM v[32:35],a[80:83],v[192:195],v[32:35]\nv_exp_f32 v141,v141\nM v[32:35],a[112:115],v[208:211],v[32:35]\n" \
"ds_read_b128 a[8:11],v250 offset:2048\nds_read_b128 a[12:15],v250 offset:3072\nM v[40:43],a[84:87],v[192:195],v[40:43]\nv_exp_f32 v142,v142\nM v[40:43],a[116:119],v[208:211],v[40:43]\nv_exp_f32 v143,v143\nM v[48:51],a[88:91],v[192:195],v[48:51]\nv_exp_f32 v156,v156\n" \
"M v[48:51],a[120:123],v[208:211],v[48:51]\nv_exp_f32 v157,v157\nM v[56:59],a[92:95],v[192:195],v[56:59]\nv_exp_f32 v158,v158\nM v[56:59],a[124:127],v[208:211],v[56:59]\nv_exp_f32 v159,v159\nM a[192:195],a[208:211],v[192:195],a[192:195]\nv_exp_f32 v164,v164\n" \
"M a[192:195],a[208:211],v[208:211],a[192:195]\nds_read_b128 a[16:19],v250 offset:4096\nds_read_b128 a[20:23],v250 offset:5120\nM v[4:7],a[64:67],v[196:199],v[4:7]\nv_exp_f32 v165,v165\nM v[4:7],a[96:99],v[212:215],v[4:7]\nv_exp_f32 v166,v166\nM v[12:15],a[68:71],v[196:199],v[12:15]\n" \
"v_exp_f32 v167,v167\nM v[12:15],a[100:103],v[212:215],v[12:15]\nbuffer_load_dwordx4 v240,s[48:51],s95 offen offset:1024 sc1 lds\nM v[20:23],a[72:75],v[196:199],v[20:23]\nv_exp_f32 v180,v180\nM v[20:23],a[104:107],v[212:215],v[20:23]\nv_exp_f32 v181,v181\nM v[28:31],a[76:79],v[196:199],v[28:31]\n" \
"v_exp_f32 v182,v182\nM v[28:31],a[108:111],v[212:215],v[28:31]\nds_read_b128 a[24:27],v250 offset:6144\nds_read_b128 a[28:31],v250 offset:7168\nM v[36:39],a[80:83],v[196:199],v[36:39]\nv_exp_f32 v183,v183\nM v[36:39],a[112:115],v[212:215],v[36:39]\nv_exp_f32 v168,v168\n" \
"M v[44:47],a[84:87],v[196:199],v[44:47]\nv_exp_f32 v169,v169\nM v[44:47],a[116:119],v[212:215],v[44:47]\nv_exp_f32 v170,v170\nM v[52:55],a[88:91],v[196:199],v[52:55]\nv_exp_f32 v171,v171\nM v[52:55],a[120:123],v[212:215],v[52:55]\nv_cvt_pk_bf16_f32 v192,v128,v129\n" \
"v_cvt_pk_bf16_f32 v193,v130,v131\nM v[60:63],a[92:95],v[196:199],v[60:63]\nv_cvt_pk_bf16_f32 v194,v144,v145\nv_cvt_pk_bf16_f32 v195,v146,v147\nM v[60:63],a[124:127],v[212:215],v[60:63]\nds_read_b128 a[32:35],v250 offset:8192\nds_read_b128 a[36:39],v250 offset:9216\nM a[196:199],a[208:211],v[196:199],a[196:199]\n" \
"v_cvt_pk_bf16_f32 v208,v160,v161\nv_cvt_pk_bf16_f32 v209,v162,v163\nM a[196:199],a[208:211],v[212:215],a[196:199]\nv_cvt_pk_bf16_f32 v210,v176,v177\nv_cvt_pk_bf16_f32 v211,v178,v179\nM v[64:67],a[64:67],v[200:203],v[64:67]\nv_cvt_pk_bf16_f32 v196,v132,v133\nv_cvt_pk_bf16_f32 v197,v134,v135\n" \
"M v[64:67],a[96:99],v[216:219],v[64:67]\nbuffer_load_dwordx4 v240,s[48:51],s95 offen offset:2048 sc1 lds\nM v[72:75],a[68:71],v[200:203],v[72:75]\nv_cvt_pk_bf16_f32 v198,v148,v149\nv_cvt_pk_bf16_f32 v199,v150,v151\nM v[72:75],a[100:103],v[216:219],v[72:75]\nM v[80:83],a[72:75],v[200:203],v[80:83]\nM v[80:83],a[104:107],v[216:219],v[80:83]\n" \
"ds_read_b128 a[40:43],v250 offset:10240\nds_read_b128 a[44:47],v250 offset:11264\nM v[88:91],a[76:79],v[200:203],v[88:91]\nM v[88:91],a[108:111],v[216:219],v[88:91]\nM v[96:99],a[80:83],v[200:203],v[96:99]\nM v[96:99],a[112:115],v[216:219],v[96:99]\nM v[104:107],a[84:87],v[200:203],v[104:107]\nM v[104:107],a[116:119],v[216:219],v[104:107]\n" \
"M v[112:115],a[88:91],v[200:203],v[112:115]\nM v[112:115],a[120:123],v[216:219],v[112:115]\nds_read_b128 a[48:51],v250 offset:12288\nds_read_b128 a[52:55],v250 offset:13312\nM v[120:123],a[92:95],v[200:203],v[120:123]\nM v[120:123],a[124:127],v[216:219],v[120:123]\nM a[200:203],a[208:211],v[200:203],a[200:203]\nv_cvt_pk_bf16_f32 v200,v136,v137\n" \
"v_cvt_pk_bf16_f32 v201,v138,v139\nM a[200:203],a[208:211],v[216:219],a[200:203]\nbuffer_load_dwordx4 v240,s[48:51],s95 offen offset:3072 sc1 lds\nM v[68:71],a[64:67],v[204:207],v[68:71]\nv_cvt_pk_bf16_f32 v202,v152,v153\nv_cvt_pk_bf16_f32 v203,v154,v155\nM v[68:71],a[96:99],v[220:223],v[68:71]\ns_cmp_eq_u32 s95,s96\n" \
"s_cselect_b32 s72,0xc000,s98\ns_add_u32 s95,s95,s72\nM v[76:79],a[68:71],v[204:207],v[76:79]\nM v[76:79],a[100:103],v[220:223],v[76:79]\nds_read_b128 a[56:59],v250 offset:14336\nds_read_b128 a[60:63],v250 offset:15360\nM v[84:87],a[72:75],v[204:207],v[84:87]\nv_add_u32 v250,s94,v241\n" \
"M v[84:87],a[104:107],v[220:223],v[84:87]\nM v[92:95],a[76:79],v[204:207],v[92:95]\nM v[92:95],a[108:111],v[220:223],v[92:95]\nM v[100:103],a[80:83],v[204:207],v[100:103]\nM v[100:103],a[112:115],v[220:223],v[100:103]\nM v[108:111],a[84:87],v[204:207],v[108:111]\nM v[108:111],a[116:119],v[220:223],v[108:111]\nM v[116:119],a[88:91],v[204:207],v[116:119]\n" \
"buffer_load_dword v247,v254,s[56:59],s1 offen\nM v[116:119],a[120:123],v[220:223],v[116:119]\ns_add_u32 s1,s1,s0\nM v[124:127],a[92:95],v[204:207],v[124:127]\nM v[124:127],a[124:127],v[220:223],v[124:127]\nM a[204:207],a[208:211],v[204:207],a[204:207]\nM a[204:207],a[208:211],v[220:223],a[204:207]\ns_cmp_lg_u32 s95,s97\n" \
"s_cbranch_scc1 .Ldual\ns_cmp_lt_u32 s62,4\ns_cbranch_scc1 .Ltrans\n.Ldf:\ns_waitcnt lgkmcnt(4)\nM v[128:131],a[0:3],a[128:131],v[224:227]\ns_add_u32 m0,s94,s64\nv_cvt_pk_bf16_f32 v204,v140,v141\n" \
"M v[128:131],a[4:7],a[132:135],v[128:131]\nds_read_b128 a[64:67],v250 offset:0\nds_read_b128 a[96:99],v250 offset:8192\nM v[128:131],a[8:11],a[136:139],v[128:131]\nv_cvt_pk_bf16_f32 v205,v142,v143\nv_lshrrev_b32 v248,2,v249\nM v[128:131],a[12:15],a[140:143],v[128:131]\nv_mul_u32_u24 v248,3584,v248\n" \
"v_and_b32 v251,3,v249\nM v[132:135],a[12:15],a[156:159],v[228:231]\nbuffer_load_dwordx4 v240,s[44:47],s95 offen offset:0 sc1 lds\nM v[132:135],a[8:11],a[152:155],v[132:135]\ns_mul_i32 s70,s7,458752\nv_lshl_add_u32 v248,v251,3,v248\nM v[132:135],a[4:7],a[148:151],v[132:135]\ns_add_u32 s70,s70,s2\n" \
"v_add_u32 v248,s70,v248\nM v[132:135],a[0:3],a[144:147],v[132:135]\nds_read_b128 a[68:71],v250 offset:1024\nds_read_b128 a[100:103],v250 offset:9216\nM v[136:139],a[0:3],a[160:163],v[232:235]\nv_and_b32 v248,s3,v248\nv_cvt_pk_bf16_f32 v206,v156,v157\nM v[136:139],a[4:7],a[164:167],v[136:139]\n" \
"v_cvt_pk_bf16_f32 v207,v158,v159\nv_cvt_pk_bf16_f32 v212,v164,v165\nM v[136:139],a[8:11],a[168:171],v[136:139]\nv_cvt_pk_bf16_f32 v213,v166,v167\nv_cvt_pk_bf16_f32 v216,v168,v169\nM v[136:139],a[12:15],a[172:175],v[136:139]\nv_cvt_pk_bf16_f32 v217,v170,v171\ns_and_b32 s71,s3,57344\n" \
"M v[140:143],a[12:15],a[188:191],v[236:239]\nv_exp_f32 v172,v172\nM v[140:143],a[8:11],a[184:187],v[140:143]\nds_read_b128 a[72:75],v250 offset:2048\nds_read_b128 a[104:107],v250 offset:10240\nM v[140:143],a[4:7],a[180:183],v[140:143]\nglobal_load_dwordx2 a[212:213],v248,s[22:23]\nM v[140:143],a[0:3],a[176:179],v[140:143]\n" \
"v_exp_f32 v173,v173\nM v[144:147],a[16:19],a[128:131],v[224:227]\nv_exp_f32 v174,v174\nM v[144:147],a[20:23],a[132:135],v[144:147]\nv_exp_f32 v175,v175\nM v[144:147],a[24:27],a[136:139],v[144:147]\nv_cvt_pk_bf16_f32 v220,v172,v173\nv_cvt_pk_bf16_f32 v221,v174,v175\n" \
"M v[144:147],a[28:31],a[140:143],v[144:147]\nds_read_b128 a[76:79],v250 offset:3072\nds_read_b128 a[108:111],v250 offset:11264\nM v[148:151],a[28:31],a[156:159],v[228:231]\nv_cvt_pk_bf16_f32 v214,v180,v181\nv_cvt_pk_bf16_f32 v215,v182,v183\nM v[148:151],a[24:27],a[152:155],v[148:151]\nv_exp_f32 v184,v184\n" \
"M v[148:151],a[20:23],a[148:151],v[148:151]\nbuffer_load_dwordx4 v240,s[44:47],s95 offen offset:1024 sc1 lds\nM v[148:151],a[16:19],a[144:147],v[148:151]\nv_exp_f32 v185,v185\nM v[152:155],a[16:19],a[160:163],v[232:235]\nv_exp_f32 v186,v186\nM v[152:155],a[20:23],a[164:167],v[152:155]\nds_read_b128 a[80:83],v250 offset:4096\n" \
"ds_read_b128 a[112:115],v250 offset:12288\nM v[152:155],a[24:27],a[168:171],v[152:155]\nv_exp_f32 v187,v187\nM v[152:155],a[28:31],a[172:175],v[152:155]\nv_cvt_pk_bf16_f32 v218,v184,v185\nv_cvt_pk_bf16_f32 v219,v186,v187\nM v[156:159],a[28:31],a[188:191],v[236:239]\nv_exp_f32 v188,v188\n" \
"M v[156:159],a[24:27],a[184:187],v[156:159]\nv_exp_f32 v189,v189\nM v[156:159],a[20:23],a[180:183],v[156:159]\nv_exp_f32 v190,v190\nM v[156:159],a[16:19],a[176:179],v[156:159]\nds_read_b128 a[84:87],v250 offset:5120\nds_read_b128 a[116:119],v250 offset:13312\nM v[160:163],a[32:35],a[128:131],v[224:227]\n" \
"global_load_dwordx2 a[228:229],v248,s[24:25]\nM v[160:163],a[36:39],a[132:135],v[160:163]\nv_add_u32 v248,s71,v248\nv_cvt_pk_bf16_f32 v222,v188,v189\nM v[160:163],a[40:43],a[136:139],v[160:163]\nv_exp_f32 v191,v191\nM v[160:163],a[44:47],a[140:143],v[160:163]\nv_cvt_pk_bf16_f32 v223,v190,v191\n" \
"s_add_u32 s94,s94,0x4000\nM v[164:167],a[44:47],a[156:159],v[228:231]\nv_exp_f32 v128,v128\nM v[164:167],a[40:43],a[152:155],v[164:167]\nds_read_b128 a[88:91],v250 offset:6144\nds_read_b128 a[120:123],v250 offset:14336\nM v[164:167],a[36:39],a[148:151],v[164:167]\nv_exp_f32 v129,v129\n" \
"M v[164:167],a[32:35],a[144:147],v[164:167]\nv_exp_f32 v130,v130\nM v[168:171],a[32:35],a[160:163],v[232:235]\nbuffer_load_dwordx4 v240,s[44:47],s95 offen offset:2048 sc1 lds\nM v[168:171],a[36:39],a[164:167],v[168:171]\nv_exp_f32 v131,v131\nM v[168:171],a[40:43],a[168:171],v[168:171]\nv_exp_f32 v144,v144\n" \
"M v[168:171],a[44:47],a[172:175],v[168:171]\nds_read_b128 a[92:95],v250 offset:7168\nds_read_b128 a[124:127],v250 offset:15360\nM v[172:175],a[44:47],a[188:191],v[236:239]\nv_xor_b32 v250,0x18000,v250\ns_and_b32 s94,s94,0xffff\nM v[172:175],a[40:43],a[184:187],v[172:175]\nv_exp_f32 v145,v145\n" \
"M v[172:175],a[36:39],a[180:183],v[172:175]\nv_exp_f32 v146,v146\nM v[172:175],a[32:35],a[176:179],v[172:175]\ns_waitcnt lgkmcnt(4)\nM v[176:179],a[48:51],a[128:131],v[224:227]\nv_exp_f32 v147,v147\nM v[176:179],a[52:55],a[132:135],v[176:179]\nv_exp_f32 v132,v132\n" \
"M v[176:179],a[56:59],a[136:139],v[176:179]\nglobal_load_dwordx2 a[214:215],v248,s[22:23]\nM v[176:179],a[60:63],a[140:143],v[176:179]\nv_exp_f32 v133,v133\nM v[180:183],a[60:63],a[156:159],v[228:231]\nv_exp_f32 v134,v134\nM v[180:183],a[56:59],a[152:155],v[180:183]\nv_exp_f32 v135,v135\n" \
"M v[180:183],a[52:55],a[148:151],v[180:183]\nv_exp_f32 v148,v148\nM v[180:183],a[48:51],a[144:147],v[180:183]\nv_exp_f32 v149,v149\nM v[184:187],a[48:51],a[160:163],v[232:235]\nv_exp_f32 v150,v150\nM v[184:187],a[52:55],a[164:167],v[184:187]\nv_exp_f32 v151,v151\n" \
"M v[184:187],a[56:59],a[168:171],v[184:187]\nbuffer_load_dwordx4 v240,s[44:47],s95 offen offset:3072 sc1 lds\nM v[184:187],a[60:63],a[172:175],v[184:187]\nv_exp_f32 v136,v136\nM v[188:191],a[60:63],a[188:191],v[236:239]\nv_exp_f32 v137,v137\nM v[188:191],a[56:59],a[184:187],v[188:191]\nv_exp_f32 v138,v138\n" \
"M v[188:191],a[52:55],a[180:183],v[188:191]\nglobal_load_dwordx2 a[230:231],v248,s[24:25]\nM v[188:191],a[48:51],a[176:179],v[188:191]\ns_add_u32 m0,m0,0x10000\nv_add_u32 v248,s71,v248\nM v[0:3],a[64:67],v[192:195],v[0:3]\ns_waitcnt vmcnt(16) lgkmcnt(0)\ns_barrier\n" \
"M v[0:3],a[96:99],v[208:211],v[0:3]\nds_read_b128 a[0:3],v250 offset:0\nds_read_b128 a[4:7],v250 offset:1024\nM v[8:11],a[68:71],v[192:195],v[8:11]\nv_exp_f32 v139,v139\nM v[8:11],a[100:103],v[208:211],v[8:11]\nv_exp_f32 v152,v152\nM v[16:19],a[72:75],v[192:195],v[16:19]\n" \
"v_exp_f32 v153,v153\nM v[16:19],a[104:107],v[208:211],v[16:19]\nbuffer_load_dwordx4 v240,s[48:51],s95 offen offset:0 sc1 lds\nM v[24:27],a[76:79],v[192:195],v[24:27]\nv_exp_f32 v154,v154\nM v[24:27],a[108:111],v[208:211],v[24:27]\nv_exp_f32 v155,v155\nM v[32:35],a[80:83],v[192:195],v[32:35]\n" \
"v_exp_f32 v160,v160\nM v[32:35],a[112:115],v[208:211],v[32:35]\nds_read_b128 a[8:11],v250 offset:2048\nds_read_b128 a[12:15],v250 offset:3072\nM v[40:43],a[84:87],v[192:195],v[40:43]\nv_exp_f32 v161,v161\nM v[40:43],a[116:119],v[208:211],v[40:43]\nv_exp_f32 v162,v162\n" \
"M v[48:51],a[88:91],v[192:195],v[48:51]\nv_exp_f32 v163,v163\nM v[48:51],a[120:123],v[208:211],v[48:51]\nglobal_load_dwordx2 a[216:217],v248,s[22:23]\nM v[56:59],a[92:95],v[192:195],v[56:59]\nv_exp_f32 v176,v176\nM v[56:59],a[124:127],v[208:211],v[56:59]\nv_exp_f32 v177,v177\n" \
"M a[192:195],a[208:211],v[192:195],a[192:195]\nv_exp_f32 v178,v178\nM a[192:195],a[208:211],v[208:211],a[192:195]\nds_read_b128 a[16:19],v250 offset:4096\nds_read_b128 a[20:23],v250 offset:5120\nM v[4:7],a[64:67],v[196:199],v[4:7]\nv_exp_f32 v179,v179\nM v[4:7],a[96:99],v[212:215],v[4:7]\n" \
"v_exp_f32 v140,v140\nM v[12:15],a[68:71],v[196:199],v[12:15]\nv_exp_f32 v141,v141\nM v[12:15],a[100:103],v[212:215],v[12:15]\nbuffer_load_dwordx4 v240,s[48:51],s95 offen offset:1024 sc1 lds\nM v[20:23],a[72:75],v[196:199],v[20:23]\nv_exp_f32 v142,v142\nM v[20:23],a[104:107],v[212:215],v[20:23]\n" \
"v_exp_f32 v143,v143\nM v[28:31],a[76:79],v[196:199],v[28:31]\nv_exp_f32 v156,v156\nM v[28:31],a[108:111],v[212:215],v[28:31]\nds_read_b128 a[24:27],v250 offset:6144\nds_read_b128 a[28:31],v250 offset:7168\nM v[36:39],a[80:83],v[196:199],v[36:39]\nv_exp_f32 v157,v157\n" \
"M v[36:39],a[112:115],v[212:215],v[36:39]\nv_exp_f32 v158,v158\nM v[44:47],a[84:87],v[196:199],v[44:47]\nv_exp_f32 v159,v159\nM v[44:47],a[116:119],v[212:215],v[44:47]\nglobal_load_dwordx2 a[232:233],v248,s[24:25]\nM v[52:55],a[88:91],v[196:199],v[52:55]\nv_add_u32 v248,s71,v248\n" \
"v_cvt_pk_bf16_f32 v192,v128,v129\nM v[52:55],a[120:123],v[212:215],v[52:55]\nv_exp_f32 v164,v164\nM v[60:63],a[92:95],v[196:199],v[60:63]\nv_exp_f32 v165,v165\nM v[60:63],a[124:127],v[212:215],v[60:63]\nds_read_b128 a[32:35],v250 offset:8192\nds_read_b128 a[36:39],v250 offset:9216\n" \
"M a[196:199],a[208:211],v[196:199],a[196:199]\nv_exp_f32 v166,v166\nM a[196:199],a[208:211],v[212:215],a[196:199]\nv_exp_f32 v167,v167\nM v[64:67],a[64:67],v[200:203],v[64:67]\nv_exp_f32 v180,v180\nM v[64:67],a[96:99],v[216:219],v[64:67]\nbuffer_load_dwordx4 v240,s[48:51],s95 offen offset:2048 sc1 lds\n" \
"M v[72:75],a[68:71],v[200:203],v[72:75]\nv_exp_f32 v181,v181\nM v[72:75],a[100:103],v[216:219],v[72:75]\nv_exp_f32 v182,v182\nM v[80:83],a[72:75],v[200:203],v[80:83]\nv_exp_f32 v183,v183\nM v[80:83],a[104:107],v[216:219],v[80:83]\nds_read_b128 a[40:43],v250 offset:10240\n" \
"ds_read_b128 a[44:47],v250 offset:11264\nM v[88:91],a[76:79],v[200:203],v[88:91]\nv_exp_f32 v168,v168\nM v[88:91],a[108:111],v[216:219],v[88:91]\nv_exp_f32 v169,v169\nM v[96:99],a[80:83],v[200:203],v[96:99]\nv_exp_f32 v170,v170\nM v[96:99],a[112:115],v[216:219],v[96:99]\n" \
"global_load_dwordx2 a[218:219],v248,s[22:23]\nM v[104:107],a[84:87],v[200:203],v[104:107]\nv_exp_f32 v171,v171\nM v[104:107],a[116:119],v[216:219],v[104:107]\nv_cvt_pk_bf16_f32 v193,v130,v131\nv_cvt_pk_bf16_f32 v194,v144,v145\nM v[112:115],a[88:91],v[200:203],v[112:115]\nv_cvt_pk_bf16_f32 v195,v146,v147\n" \
"v_cvt_pk_bf16_f32 v208,v160,v161\nM v[112:115],a[120:123],v[216:219],v[112:115]\nds_read_b128 a[48:51],v250 offset:12288\nds_read_b128 a[52:55],v250 offset:13312\nM v[120:123],a[92:95],v[200:203],v[120:123]\nv_cvt_pk_bf16_f32 v209,v162,v163\nv_cvt_pk_bf16_f32 v210,v176,v177\nM v[120:123],a[124:127],v[216:219],v[120:123]\n" \
"v_cvt_pk_bf16_f32 v211,v178,v179\nv_cvt_pk_bf16_f32 v196,v132,v133\nM a[200:203],a[208:211],v[200:203],a[200:203]\nv_cvt_pk_bf16_f32 v197,v134,v135\nv_cvt_pk_bf16_f32 v198,v148,v149\nM a[200:203],a[208:211],v[216:219],a[200:203]\nbuffer_load_dwordx4 v240,s[48:51],s95 offen offset:3072 sc1 lds\nM v[68:71],a[64:67],v[204:207],v[68:71]\n" \
"v_cvt_pk_bf16_f32 v199,v150,v151\nv_cvt_pk_bf16_f32 v200,v136,v137\nM v[68:71],a[96:99],v[220:223],v[68:71]\nv_cvt_pk_bf16_f32 v201,v138,v139\nv_cvt_pk_bf16_f32 v202,v152,v153\nM v[76:79],a[68:71],v[204:207],v[76:79]\nv_cvt_pk_bf16_f32 v203,v154,v155\ns_add_u32 s95,s95,0x4000\n" \
"M v[76:79],a[100:103],v[220:223],v[76:79]\nds_read_b128 a[56:59],v250 offset:14336\nds_read_b128 a[60:63],v250 offset:15360\nM v[84:87],a[72:75],v[204:207],v[84:87]\nv_add_u32 v250,s94,v241\nM v[84:87],a[104:107],v[220:223],v[84:87]\nM v[92:95],a[76:79],v[204:207],v[92:95]\nM v[92:95],a[108:111],v[220:223],v[92:95]\n" \
"global_load_dwordx2 a[234:235],v248,s[24:25]\nM v[100:103],a[80:83],v[204:207],v[100:103]\nv_add_u32 v248,s71,v248\nM v[100:103],a[112:115],v[220:223],v[100:103]\nM v[108:111],a[84:87],v[204:207],v[108:111]\nM v[108:111],a[116:119],v[220:223],v[108:111]\nM v[116:119],a[88:91],v[204:207],v[116:119]\nM v[116:119],a[120:123],v[220:223],v[116:119]\n" \
"M v[124:127],a[92:95],v[204:207],v[124:127]\nM v[124:127],a[124:127],v[220:223],v[124:127]\nM a[204:207],a[208:211],v[204:207],a[204:207]\nM a[204:207],a[208:211],v[220:223],a[204:207]\ns_waitcnt lgkmcnt(4)\nM v[128:131],a[0:3],a[128:131],v[224:227]\ns_add_u32 m0,s94,s64\nv_cvt_pk_bf16_f32 v204,v140,v141\n" \
"M v[128:131],a[4:7],a[132:135],v[128:131]\nds_read_b128 a[64:67],v250 offset:0\nds_read_b128 a[96:99],v250 offset:8192\nM v[128:131],a[8:11],a[136:139],v[128:131]\nv_cvt_pk_bf16_f32 v205,v142,v143\nv_cvt_pk_bf16_f32 v206,v156,v157\nM v[128:131],a[12:15],a[140:143],v[128:131]\nv_cvt_pk_bf16_f32 v207,v158,v159\n" \
"v_cvt_pk_bf16_f32 v212,v164,v165\nM v[132:135],a[12:15],a[156:159],v[228:231]\nbuffer_load_dwordx4 v240,s[44:47],s95 offen offset:0 sc1 lds\nM v[132:135],a[8:11],a[152:155],v[132:135]\nv_cvt_pk_bf16_f32 v213,v166,v167\nv_cvt_pk_bf16_f32 v216,v168,v169\nM v[132:135],a[4:7],a[148:151],v[132:135]\nv_cvt_pk_bf16_f32 v217,v170,v171\n" \
"v_cvt_pk_bf16_f32 v214,v180,v181\nM v[132:135],a[0:3],a[144:147],v[132:135]\nds_read_b128 a[68:71],v250 offset:1024\nds_read_b128 a[100:103],v250 offset:9216\nM v[136:139],a[0:3],a[160:163],v[232:235]\nv_exp_f32 v172,v172\nM v[136:139],a[4:7],a[164:167],v[136:139]\nv_exp_f32 v173,v173\n" \
"M v[136:139],a[8:11],a[168:171],v[136:139]\nv_exp_f32 v174,v174\nM v[136:139],a[12:15],a[172:175],v[136:139]\nv_exp_f32 v175,v175\nM v[140:143],a[12:15],a[188:191],v[236:239]\nv_cvt_pk_bf16_f32 v220,v172,v173\nv_cvt_pk_bf16_f32 v221,v174,v175\nM v[140:143],a[8:11],a[184:187],v[140:143]\n" \
"ds_read_b128 a[72:75],v250 offset:2048\nds_read_b128 a[104:107],v250 offset:10240\nM v[140:143],a[4:7],a[180:183],v[140:143]\nglobal_load_dwordx2 a[220:221],v248,s[22:23]\nM v[140:143],a[0:3],a[176:179],v[140:143]\nv_cvt_pk_bf16_f32 v215,v182,v183\ns_add_u32 s94,s94,0x4000\nM v[144:147],a[16:19],a[128:131],v[224:227]\n" \
"v_exp_f32 v184,v184\nM v[144:147],a[20:23],a[132:135],v[144:147]\nv_exp_f32 v185,v185\nM v[144:147],a[24:27],a[136:139],v[144:147]\nv_exp_f32 v186,v186\nM v[144:147],a[28:31],a[140:143],v[144:147]\nds_read_b128 a[76:79],v250 offset:3072\nds_read_b128 a[108:111],v250 offset:11264\n" \
"M v[148:151],a[28:31],a[156:159],v[228:231]\nv_exp_f32 v187,v187\nM v[148:151],a[24:27],a[152:155],v[148:151]\nv_cvt_pk_bf16_f32 v218,v184,v185\nv_cvt_pk_bf16_f32 v219,v186,v187\nM v[148:151],a[20:23],a[148:151],v[148:151]\nbuffer_load_dwordx4 v240,s[44:47],s95 offen offset:1024 sc1 lds\nM v[148:151],a[16:19],a[144:147],v[148:151]\n" \
"v_exp_f32 v188,v188\nM v[152:155],a[16:19],a[160:163],v[232:235]\nv_exp_f32 v189,v189\nM v[152:155],a[20:23],a[164:167],v[152:155]\nds_read_b128 a[80:83],v250 offset:4096\nds_read_b128 a[112:115],v250 offset:12288\nM v[152:155],a[24:27],a[168:171],v[152:155]\nv_exp_f32 v190,v190\n" \
"M v[152:155],a[28:31],a[172:175],v[152:155]\nv_exp_f32 v191,v191\nM v[156:159],a[28:31],a[188:191],v[236:239]\nv_cvt_pk_bf16_f32 v222,v188,v189\nv_cvt_pk_bf16_f32 v223,v190,v191\nM v[156:159],a[24:27],a[184:187],v[156:159]\nv_exp_f32 v128,v128\nM v[156:159],a[20:23],a[180:183],v[156:159]\n" \
"v_exp_f32 v129,v129\nM v[156:159],a[16:19],a[176:179],v[156:159]\nds_read_b128 a[84:87],v250 offset:5120\nds_read_b128 a[116:119],v250 offset:13312\nM v[160:163],a[32:35],a[128:131],v[224:227]\nglobal_load_dwordx2 a[236:237],v248,s[24:25]\nM v[160:163],a[36:39],a[132:135],v[160:163]\nv_add_u32 v248,s71,v248\n" \
"s_and_b32 s94,s94,0xffff\nM v[160:163],a[40:43],a[136:139],v[160:163]\nv_exp_f32 v130,v130\nM v[160:163],a[44:47],a[140:143],v[160:163]\nv_exp_f32 v131,v131\nM v[164:167],a[44:47],a[156:159],v[228:231]\nv_exp_f32 v144,v144\nM v[164:167],a[40:43],a[152:155],v[164:167]\n" \
"ds_read_b128 a[88:91],v250 offset:6144\nds_read_b128 a[120:123],v250 offset:14336\nM v[164:167],a[36:39],a[148:151],v[164:167]\nv_exp_f32 v145,v145\nM v[164:167],a[32:35],a[144:147],v[164:167]\nv_exp_f32 v146,v146\nM v[168:171],a[32:35],a[160:163],v[232:235]\nbuffer_load_dwordx4 v240,s[44:47],s95 offen offset:2048 sc1 lds\n" \
"M v[168:171],a[36:39],a[164:167],v[168:171]\nv_exp_f32 v147,v147\nM v[168:171],a[40:43],a[168:171],v[168:171]\nv_exp_f32 v132,v132\nM v[168:171],a[44:47],a[172:175],v[168:171]\nds_read_b128 a[92:95],v250 offset:7168\nds_read_b128 a[124:127],v250 offset:15360\nM v[172:175],a[44:47],a[188:191],v[236:239]\n" \
"v_exp_f32 v133,v133\nM v[172:175],a[40:43],a[184:187],v[172:175]\nv_exp_f32 v134,v134\nM v[172:175],a[36:39],a[180:183],v[172:175]\nv_exp_f32 v135,v135\nM v[172:175],a[32:35],a[176:179],v[172:175]\nv_xor_b32 v250,0x18000,v250\ns_waitcnt lgkmcnt(3)\n" \
"M v[176:179],a[48:51],a[128:131],v[224:227]\nv_exp_f32 v148,v148\nM v[176:179],a[52:55],a[132:135],v[176:179]\nv_exp_f32 v149,v149\nM v[176:179],a[56:59],a[136:139],v[176:179]\nglobal_load_dwordx2 a[222:223],v248,s[22:23]\nM v[176:179],a[60:63],a[140:143],v[176:179]\nv_exp_f32 v150,v150\n" \
"M v[180:183],a[60:63],a[156:159],v[228:231]\nv_exp_f32 v151,v151\nM v[180:183],a[56:59],a[152:155],v[180:183]\nv_exp_f32 v136,v136\nM v[180:183],a[52:55],a[148:151],v[180:183]\nv_exp_f32 v137,v137\nM v[180:183],a[48:51],a[144:147],v[180:183]\nv_exp_f32 v138,v138\n" \
"M v[184:187],a[48:51],a[160:163],v[232:235]\nv_exp_f32 v139,v139\nM v[184:187],a[52:55],a[164:167],v[184:187]\nv_exp_f32 v152,v152\nM v[184:187],a[56:59],a[168:171],v[184:187]\nbuffer_load_dwordx4 v240,s[44:47],s95 offen offset:3072 sc1 lds\nM v[184:187],a[60:63],a[172:175],v[184:187]\nv_exp_f32 v153,v153\n" \
"M v[188:191],a[60:63],a[188:191],v[236:239]\nv_exp_f32 v154,v154\nM v[188:191],a[56:59],a[184:187],v[188:191]\nv_exp_f32 v155,v155\nM v[188:191],a[52:55],a[180:183],v[188:191]\nglobal_load_dwordx2 a[238:239],v248,s[24:25]\nM v[188:191],a[48:51],a[176:179],v[188:191]\ns_add_u32 m0,m0,0x10000\n" \
"v_add_u32 v248,s71,v248\nM v[0:3],a[64:67],v[192:195],v[0:3]\ns_waitcnt vmcnt(16) lgkmcnt(0)\ns_barrier\nM v[0:3],a[96:99],v[208:211],v[0:3]\nds_read_b128 a[0:3],v250 offset:0\nds_read_b128 a[4:7],v250 offset:1024\nM v[8:11],a[68:71],v[192:195],v[8:11]\n" \
"v_exp_f32 v160,v160\nM v[8:11],a[100:103],v[208:211],v[8:11]\nv_exp_f32 v161,v161\nM v[16:19],a[72:75],v[192:195],v[16:19]\nv_exp_f32 v162,v162\nM v[16:19],a[104:107],v[208:211],v[16:19]\nbuffer_load_dwordx4 v240,s[48:51],s95 offen offset:0 sc1 lds\nM v[24:27],a[76:79],v[192:195],v[24:27]\n" \
"v_exp_f32 v163,v163\nM v[24:27],a[108:111],v[208:211],v[24:27]\nv_exp_f32 v176,v176\nM v[32:35],a[80:83],v[192:195],v[32:35]\nv_exp_f32 v177,v177\nM v[32:35],a[112:115],v[208:211],v[32:35]\nds_read_b128 a[8:11],v250 offset:2048\nds_read_b128 a[12:15],v250 offset:3072\n" \
"M v[40:43],a[84:87],v[192:195],v[40:43]\nv_exp_f32 v178,v178\nM v[40:43],a[116:119],v[208:211],v[40:43]\nv_exp_f32 v179,v179\nM v[48:51],a[88:91],v[192:195],v[48:51]\nv_exp_f32 v140,v140\nM v[48:51],a[120:123],v[208:211],v[48:51]\nglobal_load_dwordx2 a[224:225],v248,s[22:23]\n" \
"M v[56:59],a[92:95],v[192:195],v[56:59]\nv_exp_f32 v141,v141\nM v[56:59],a[124:127],v[208:211],v[56:59]\nv_exp_f32 v142,v142\nM a[192:195],a[208:211],v[192:195],a[192:195]\nv_exp_f32 v143,v143\nM a[192:195],a[208:211],v[208:211],a[192:195]\nds_read_b128 a[16:19],v250 offset:4096\n" \
"ds_read_b128 a[20:23],v250 offset:5120\nM v[4:7],a[64:67],v[196:199],v[4:7]\nv_exp_f32 v156,v156\nM v[4:7],a[96:99],v[212:215],v[4:7]\nv_exp_f32 v157,v157\nM v[12:15],a[68:71],v[196:199],v[12:15]\nv_exp_f32 v158,v158\nM v[12:15],a[100:103],v[212:215],v[12:15]\n" \
"buffer_load_dwordx4 v240,s[48:51],s95 offen offset:1024 sc1 lds\nM v[20:23],a[72:75],v[196:199],v[20:23]\nv_exp_f32 v159,v159\nM v[20:23],a[104:107],v[212:215],v[20:23]\nv_exp_f32 v164,v164\nM v[28:31],a[76:79],v[196:199],v[28:31]\nv_exp_f32 v165,v165\nM v[28:31],a[108:111],v[212:215],v[28:31]\n" \
"ds_read_b128 a[24:27],v250 offset:6144\nds_read_b128 a[28:31],v250 offset:7168\nM v[36:39],a[80:83],v[196:199],v[36:39]\nv_exp_f32 v166,v166\nM v[36:39],a[112:115],v[212:215],v[36:39]\nv_exp_f32 v167,v167\nM v[44:47],a[84:87],v[196:199],v[44:47]\nv_exp_f32 v180,v180\n" \
"M v[44:47],a[116:119],v[212:215],v[44:47]\nglobal_load_dwordx2 a[240:241],v248,s[24:25]\nM v[52:55],a[88:91],v[196:199],v[52:55]\nv_add_u32 v248,s71,v248\nv_cvt_pk_bf16_f32 v192,v128,v129\nM v[52:55],a[120:123],v[212:215],v[52:55]\nv_exp_f32 v181,v181\nM v[60:63],a[92:95],v[196:199],v[60:63]\n" \
"v_exp_f32 v182,v182\nM v[60:63],a[124:127],v[212:215],v[60:63]\nds_read_b128 a[32:35],v250 offset:8192\nds_read_b128 a[36:39],v250 offset:9216\nM a[196:199],a[208:211],v[196:199],a[196:199]\nv_exp_f32 v183,v183\nM a[196:199],a[208:211],v[212:215],a[196:199]\nv_exp_f32 v168,v168\n" \
"M v[64:67],a[64:67],v[200:203],v[64:67]\nv_exp_f32 v169,v169\nM v[64:67],a[96:99],v[216:219],v[64:67]\nbuffer_load_dwordx4 v240,s[48:51],s95 offen offset:2048 sc1 lds\nM v[72:75],a[68:71],v[200:203],v[72:75]\nv_exp_f32 v170,v170\nM v[72:75],a[100:103],v[216:219],v[72:75]\nv_exp_f32 v171,v171\n" \
"M v[80:83],a[72:75],v[200:203],v[80:83]\nv_cvt_pk_bf16_f32 v193,v130,v131\nv_cvt_pk_bf16_f32 v194,v144,v145\nM v[80:83],a[104:107],v[216:219],v[80:83]\nds_read_b128 a[40:43],v250 offset:10240\nds_read_b128 a[44:47],v250 offset:11264\nM v[88:91],a[76:79],v[200:203],v[88:91]\nv_cvt_pk_bf16_f32 v195,v146,v147\n" \
"v_cvt_pk_bf16_f32 v208,v160,v161\nM v[88:91],a[108:111],v[216:219],v[88:91]\nv_cvt_pk_bf16_f32 v209,v162,v163\nv_cvt_pk_bf16_f32 v210,v176,v177\nM v[96:99],a[80:83],v[200:203],v[96:99]\nv_cvt_pk_bf16_f32 v211,v178,v179\nv_cvt_pk_bf16_f32 v196,v132,v133\nM v[96:99],a[112:115],v[216:219],v[96:99]\n" \
"global_load_dwordx2 a[226:227],v248,s[22:23]\nM v[104:107],a[84:87],v[200:203],v[104:107]\nv_cvt_pk_bf16_f32 v197,v134,v135\nv_cvt_pk_bf16_f32 v198,v148,v149\nM v[104:107],a[116:119],v[216:219],v[104:107]\nv_cvt_pk_bf16_f32 v199,v150,v151\nM v[112:115],a[88:91],v[200:203],v[112:115]\nM v[112:115],a[120:123],v[216:219],v[112:115]\n" \
"ds_read_b128 a[48:51],v250 offset:12288\nds_read_b128 a[52:55],v250 offset:13312\nM v[120:123],a[92:95],v[200:203],v[120:123]\nM v[120:123],a[124:127],v[216:219],v[120:123]\nM a[200:203],a[208:211],v[200:203],a[200:203]\nv_cvt_pk_bf16_f32 v200,v136,v137\nv_cvt_pk_bf16_f32 v201,v138,v139\nM a[200:203],a[208:211],v[216:219],a[200:203]\n" \
"buffer_load_dwordx4 v240,s[48:51],s95 offen offset:3072 sc1 lds\nM v[68:71],a[64:67],v[204:207],v[68:71]\nv_cvt_pk_bf16_f32 v202,v152,v153\nv_cvt_pk_bf16_f32 v203,v154,v155\nM v[68:71],a[96:99],v[220:223],v[68:71]\ns_add_u32 s95,s95,0x4000\nM v[76:79],a[68:71],v[204:207],v[76:79]\nM v[76:79],a[100:103],v[220:223],v[76:79]\n" \
"ds_read_b128 a[56:59],v250 offset:14336\nds_read_b128 a[60:63],v250 offset:15360\nM v[84:87],a[72:75],v[204:207],v[84:87]\nv_add_u32 v250,s94,v241\nM v[84:87],a[104:107],v[220:223],v[84:87]\nM v[92:95],a[76:79],v[204:207],v[92:95]\nM v[92:95],a[108:111],v[220:223],v[92:95]\nglobal_load_dwordx2 a[242:243],v248,s[24:25]\n" \
"M v[100:103],a[80:83],v[204:207],v[100:103]\nv_add_u32 v248,s71,v248\nM v[100:103],a[112:115],v[220:223],v[100:103]\nM v[108:111],a[84:87],v[204:207],v[108:111]\nM v[108:111],a[116:119],v[220:223],v[108:111]\nM v[116:119],a[88:91],v[204:207],v[116:119]\nM v[116:119],a[120:123],v[220:223],v[116:119]\nM v[124:127],a[92:95],v[204:207],v[124:127]\n" \
"M v[124:127],a[124:127],v[220:223],v[124:127]\nM a[204:207],a[208:211],v[204:207],a[204:207]\nM a[204:207],a[208:211],v[220:223],a[204:207]\ns_cmp_eq_u32 s62,7\ns_cbranch_scc0 .Ltrans\ns_waitcnt lgkmcnt(4)\nM v[136:139],a[0:3],a[160:163],v[232:235]\ns_add_u32 m0,s94,s64\n" \
"v_cvt_pk_bf16_f32 v204,v140,v141\nM v[136:139],a[4:7],a[164:167],v[136:139]\nds_read_b128 a[64:67],v250 offset:0\nds_read_b128 a[96:99],v250 offset:8192\nM v[136:139],a[8:11],a[168:171],v[136:139]\nbuffer_load_dwordx4 v240,s[44:47],s95 offen offset:0 sc1 lds\nM v[136:139],a[12:15],a[172:175],v[136:139]\nv_cvt_pk_bf16_f32 v205,v142,v143\n" \
"v_cvt_pk_bf16_f32 v206,v156,v157\nM v[140:143],a[12:15],a[188:191],v[236:239]\nds_read_b128 a[68:71],v250 offset:1024\nds_read_b128 a[100:103],v250 offset:9216\nM v[140:143],a[8:11],a[184:187],v[140:143]\nbuffer_store_dwordx4 a[248:251],v253,s[52:55],s33 offen offset:0 sc1\nM v[140:143],a[4:7],a[180:183],v[140:143]\nv_cvt_pk_bf16_f32 v207,v158,v159\n" \
"v_cvt_pk_bf16_f32 v216,v168,v169\nM v[140:143],a[0:3],a[176:179],v[140:143]\nds_read_b128 a[72:75],v250 offset:2048\nds_read_b128 a[104:107],v250 offset:10240\nM v[152:155],a[16:19],a[160:163],v[232:235]\nv_cvt_pk_bf16_f32 v217,v170,v171\ns_add_u32 s71,s33,16384\nM v[152:155],a[20:23],a[164:167],v[152:155]\n" \
"v_exp_f32 v172,v172\nM v[152:155],a[24:27],a[168:171],v[152:155]\nds_read_b128 a[76:79],v250 offset:3072\nds_read_b128 a[108:111],v250 offset:11264\nM v[152:155],a[28:31],a[172:175],v[152:155]\nbuffer_load_dwordx4 v240,s[44:47],s95 offen offset:1024 sc1 lds\nM v[156:159],a[28:31],a[188:191],v[236:239]\nv_exp_f32 v173,v173\n" \
"M v[156:159],a[24:27],a[184:187],v[156:159]\nds_read_b128 a[80:83],v250 offset:4096\nds_read_b128 a[112:115],v250 offset:12288\nM v[156:159],a[20:23],a[180:183],v[156:159]\nbuffer_store_dwordx4 a[248:251],v253,s[52:55],s33 offen offset:1024 sc1\nM v[156:159],a[16:19],a[176:179],v[156:159]\nv_exp_f32 v174,v174\nM v[168:171],a[32:35],a[160:163],v[232:235]\n" \
"ds_read_b128 a[84:87],v250 offset:5120\nds_read_b128 a[116:119],v250 offset:13312\nM v[168:171],a[36:39],a[164:167],v[168:171]\nv_exp_f32 v175,v175\nM v[168:171],a[40:43],a[168:171],v[168:171]\nv_cvt_pk_bf16_f32 v220,v172,v173\nv_cvt_pk_bf16_f32 v221,v174,v175\nM v[168:171],a[44:47],a[172:175],v[168:171]\n" \
"ds_read_b128 a[88:91],v250 offset:6144\nds_read_b128 a[120:123],v250 offset:14336\nM v[172:175],a[44:47],a[188:191],v[236:239]\nbuffer_load_dwordx4 v240,s[44:47],s95 offen offset:2048 sc1 lds\nM v[172:175],a[40:43],a[184:187],v[172:175]\nv_exp_f32 v184,v184\nM v[172:175],a[36:39],a[180:183],v[172:175]\nv_exp_f32 v185,v185\n" \
"v_exp_f32 v186,v186\nv_exp_f32 v187,v187\nds_read_b128 a[92:95],v250 offset:7168\nds_read_b128 a[124:127],v250 offset:15360\nM v[172:175],a[32:35],a[176:179],v[172:175]\nbuffer_store_dwordx4 a[248:251],v253,s[52:55],s33 offen offset:2048 sc1\nv_cvt_pk_bf16_f32 v218,v184,v185\nv_cvt_pk_bf16_f32 v219,v186,v187\n" \
"s_waitcnt lgkmcnt(6)\nM v[184:187],a[48:51],a[160:163],v[232:235]\nv_exp_f32 v188,v188\nM v[184:187],a[52:55],a[164:167],v[184:187]\nv_exp_f32 v189,v189\nM v[184:187],a[56:59],a[168:171],v[184:187]\nv_exp_f32 v190,v190\nv_exp_f32 v191,v191\n" \
"M v[184:187],a[60:63],a[172:175],v[184:187]\nv_cvt_pk_bf16_f32 v222,v188,v189\nv_cvt_pk_bf16_f32 v223,v190,v191\nM v[188:191],a[60:63],a[188:191],v[236:239]\nv_xor_b32 v250,0x18000,v250\ns_add_u32 s70,s78,16384\nM v[188:191],a[56:59],a[184:187],v[188:191]\nbuffer_load_dwordx4 v240,s[44:47],s95 offen offset:3072 sc1 lds\n" \
"M v[188:191],a[52:55],a[180:183],v[188:191]\ns_add_u32 m0,m0,0x10000\nv_cvt_pk_bf16_f32 v212,v164,v165\nM v[188:191],a[48:51],a[176:179],v[188:191]\nv_cvt_pk_bf16_f32 v213,v166,v167\nv_cvt_pk_bf16_f32 v214,v180,v181\nM v[64:67],a[64:67],v[200:203],v[64:67]\nbuffer_store_dwordx4 a[248:251],v253,s[52:55],s33 offen offset:3072 sc1\n" \
"M v[64:67],a[96:99],v[216:219],v[64:67]\nv_cvt_pk_bf16_f32 v215,v182,v183\ns_add_u32 s94,s94,0x4000\nM v[72:75],a[68:71],v[200:203],v[72:75]\ns_waitcnt vmcnt(16) lgkmcnt(0)\ns_barrier\nM v[72:75],a[100:103],v[216:219],v[72:75]\nds_read_b128 a[0:3],v250 offset:0\n" \
"ds_read_b128 a[4:7],v250 offset:1024\nM v[80:83],a[72:75],v[200:203],v[80:83]\nv_exp_f32 v136,v136\nM v[80:83],a[104:107],v[216:219],v[80:83]\nbuffer_store_dwordx4 a[248:251],v253,s[52:55],s71 offen offset:0 sc1\nM v[88:91],a[76:79],v[200:203],v[88:91]\nv_exp_f32 v137,v137\nM v[88:91],a[108:111],v[216:219],v[88:91]\n" \
"buffer_load_dwordx4 v240,s[48:51],s95 offen offset:0 sc1 lds\nM v[96:99],a[80:83],v[200:203],v[96:99]\nv_exp_f32 v138,v138\nM v[96:99],a[112:115],v[216:219],v[96:99]\nbuffer_store_dwordx4 a[248:251],v253,s[52:55],s71 offen offset:1024 sc1\nM v[104:107],a[84:87],v[200:203],v[104:107]\nv_exp_f32 v139,v139\nM v[104:107],a[116:119],v[216:219],v[104:107]\n" \
"ds_read_b128 a[8:11],v250 offset:2048\nds_read_b128 a[12:15],v250 offset:3072\nM v[112:115],a[88:91],v[200:203],v[112:115]\nv_exp_f32 v152,v152\nM v[112:115],a[120:123],v[216:219],v[112:115]\nbuffer_store_dwordx4 a[248:251],v253,s[52:55],s71 offen offset:2048 sc1\nM v[120:123],a[92:95],v[200:203],v[120:123]\nv_exp_f32 v153,v153\n" \
"M v[120:123],a[124:127],v[216:219],v[120:123]\nv_exp_f32 v154,v154\nM a[200:203],a[208:211],v[200:203],a[200:203]\nv_exp_f32 v155,v155\nM a[200:203],a[208:211],v[216:219],a[200:203]\nbuffer_store_dwordx4 a[248:251],v253,s[52:55],s71 offen offset:3072 sc1\nM v[68:71],a[64:67],v[204:207],v[68:71]\nv_exp_f32 v168,v168\n" \
"M v[68:71],a[96:99],v[220:223],v[68:71]\nds_read_b128 a[16:19],v250 offset:4096\nds_read_b128 a[20:23],v250 offset:5120\nM v[76:79],a[68:71],v[204:207],v[76:79]\nv_exp_f32 v169,v169\nM v[76:79],a[100:103],v[220:223],v[76:79]\nv_exp_f32 v170,v170\nM v[84:87],a[72:75],v[204:207],v[84:87]\n" \
"v_exp_f32 v171,v171\nM v[84:87],a[104:107],v[220:223],v[84:87]\nbuffer_load_dwordx4 v240,s[48:51],s95 offen offset:1024 sc1 lds\nM v[92:95],a[76:79],v[204:207],v[92:95]\nv_exp_f32 v184,v184\nM v[92:95],a[108:111],v[220:223],v[92:95]\nbuffer_store_dwordx4 a[248:251],v253,s[52:55],s78 offen offset:0 sc1\nM v[100:103],a[80:83],v[204:207],v[100:103]\n" \
"v_exp_f32 v185,v185\nM v[100:103],a[112:115],v[220:223],v[100:103]\nds_read_b128 a[24:27],v250 offset:6144\nds_read_b128 a[28:31],v250 offset:7168\nM v[108:111],a[84:87],v[204:207],v[108:111]\nv_exp_f32 v186,v186\nM v[108:111],a[116:119],v[220:223],v[108:111]\nbuffer_store_dwordx4 a[248:251],v253,s[52:55],s78 offen offset:1024 sc1\n" \
"M v[116:119],a[88:91],v[204:207],v[116:119]\nv_exp_f32 v187,v187\nM v[116:119],a[120:123],v[220:223],v[116:119]\ns_and_b32 s94,s94,0xffff\nv_cvt_pk_bf16_f32 v200,v136,v137\nM v[124:127],a[92:95],v[204:207],v[124:127]\nv_exp_f32 v140,v140\nM v[124:127],a[124:127],v[220:223],v[124:127]\n" \
"buffer_store_dwordx4 a[248:251],v253,s[52:55],s78 offen offset:2048 sc1\nM a[204:207],a[208:211],v[204:207],a[204:207]\nv_exp_f32 v141,v141\nM a[204:207],a[208:211],v[220:223],a[204:207]\nds_read_b128 a[32:35],v250 offset:8192\nds_read_b128 a[36:39],v250 offset:9216\nM v[0:3],a[64:67],v[192:195],v[0:3]\nv_exp_f32 v142,v142\n" \
"M v[0:3],a[96:99],v[208:211],v[0:3]\nv_exp_f32 v143,v143\nM v[8:11],a[68:71],v[192:195],v[8:11]\nv_exp_f32 v156,v156\nM v[8:11],a[100:103],v[208:211],v[8:11]\nbuffer_load_dwordx4 v240,s[48:51],s95 offen offset:2048 sc1 lds\nM v[16:19],a[72:75],v[192:195],v[16:19]\nv_exp_f32 v157,v157\n" \
"M v[16:19],a[104:107],v[208:211],v[16:19]\nbuffer_store_dwordx4 a[248:251],v253,s[52:55],s78 offen offset:3072 sc1\nM v[24:27],a[76:79],v[192:195],v[24:27]\nv_exp_f32 v158,v158\nM v[24:27],a[108:111],v[208:211],v[24:27]\nds_read_b128 a[40:43],v250 offset:10240\nds_read_b128 a[44:47],v250 offset:11264\nM v[32:35],a[80:83],v[192:195],v[32:35]\n" \
"v_exp_f32 v159,v159\nM v[32:35],a[112:115],v[208:211],v[32:35]\nbuffer_store_dwordx4 a[248:251],v253,s[52:55],s70 offen offset:0 sc1\nM v[40:43],a[84:87],v[192:195],v[40:43]\nv_cvt_pk_bf16_f32 v201,v138,v139\nv_cvt_pk_bf16_f32 v202,v152,v153\nM v[40:43],a[116:119],v[208:211],v[40:43]\nv_cvt_pk_bf16_f32 v203,v154,v155\n" \
"v_cvt_pk_bf16_f32 v216,v168,v169\nM v[48:51],a[88:91],v[192:195],v[48:51]\nv_cvt_pk_bf16_f32 v217,v170,v171\nv_cvt_pk_bf16_f32 v218,v184,v185\nM v[48:51],a[120:123],v[208:211],v[48:51]\nbuffer_store_dwordx4 a[248:251],v253,s[52:55],s70 offen offset:1024 sc1\nM v[56:59],a[92:95],v[192:195],v[56:59]\nv_cvt_pk_bf16_f32 v219,v186,v187\n" \
"M v[56:59],a[124:127],v[208:211],v[56:59]\nds_read_b128 a[48:51],v250 offset:12288\nds_read_b128 a[52:55],v250 offset:13312\nM a[192:195],a[208:211],v[192:195],a[192:195]\nM a[192:195],a[208:211],v[208:211],a[192:195]\nM v[4:7],a[64:67],v[196:199],v[4:7]\nM v[4:7],a[96:99],v[212:215],v[4:7]\nbuffer_load_dwordx4 v240,s[48:51],s95 offen offset:3072 sc1 lds\n" \
"M v[12:15],a[68:71],v[196:199],v[12:15]\ns_add_u32 s95,s95,0xffff4000\nM v[12:15],a[100:103],v[212:215],v[12:15]\nbuffer_store_dwordx4 a[248:251],v253,s[52:55],s70 offen offset:2048 sc1\nM v[20:23],a[72:75],v[196:199],v[20:23]\nM v[20:23],a[104:107],v[212:215],v[20:23]\nds_read_b128 a[56:59],v250 offset:14336\nds_read_b128 a[60:63],v250 offset:15360\n" \
"M v[28:31],a[76:79],v[196:199],v[28:31]\nv_add_u32 v250,s94,v241\nM v[28:31],a[108:111],v[212:215],v[28:31]\nbuffer_store_dwordx4 a[248:251],v253,s[52:55],s70 offen offset:3072 sc1\nM v[36:39],a[80:83],v[196:199],v[36:39]\nM v[36:39],a[112:115],v[212:215],v[36:39]\nM v[44:47],a[84:87],v[196:199],v[44:47]\nM v[44:47],a[116:119],v[212:215],v[44:47]\n" \
"M v[52:55],a[88:91],v[196:199],v[52:55]\nM v[52:55],a[120:123],v[212:215],v[52:55]\nM v[60:63],a[92:95],v[196:199],v[60:63]\nM v[60:63],a[124:127],v[212:215],v[60:63]\nM a[196:199],a[208:211],v[196:199],a[196:199]\nM a[196:199],a[208:211],v[212:215],a[196:199]\n.Lsl:\ns_waitcnt lgkmcnt(4)\n" \
"M v[136:139],a[0:3],a[160:163],v[232:235]\ns_add_u32 m0,s94,s64\nv_cvt_pk_bf16_f32 v204,v140,v141\nM v[136:139],a[4:7],a[164:167],v[136:139]\nds_read_b128 a[64:67],v250 offset:0\nds_read_b128 a[96:99],v250 offset:8192\nM v[136:139],a[8:11],a[168:171],v[136:139]\nbuffer_load_dwordx4 v240,s[44:47],s95 offen offset:0 sc1 lds\n" \
"M v[136:139],a[12:15],a[172:175],v[136:139]\nv_cvt_pk_bf16_f32 v205,v142,v143\nv_cvt_pk_bf16_f32 v206,v156,v157\nM v[140:143],a[12:15],a[188:191],v[236:239]\nds_read_b128 a[68:71],v250 offset:1024\nds_read_b128 a[100:103],v250 offset:9216\nM v[140:143],a[8:11],a[184:187],v[140:143]\nv_cvt_pk_bf16_f32 v207,v158,v159\n" \
"s_add_u32 s94,s94,0x4000\nM v[140:143],a[4:7],a[180:183],v[140:143]\nv_exp_f32 v172,v172\nM v[140:143],a[0:3],a[176:179],v[140:143]\nds_read_b128 a[72:75],v250 offset:2048\nds_read_b128 a[104:107],v250 offset:10240\nM v[152:155],a[16:19],a[160:163],v[232:235]\nv_exp_f32 v173,v173\n" \
"M v[152:155],a[20:23],a[164:167],v[152:155]\nv_exp_f32 v174,v174\nM v[152:155],a[24:27],a[168:171],v[152:155]\nds_read_b128 a[76:79],v250 offset:3072\nds_read_b128 a[108:111],v250 offset:11264\nM v[152:155],a[28:31],a[172:175],v[152:155]\nbuffer_load_dwordx4 v240,s[44:47],s95 offen offset:1024 sc1 lds\nM v[156:159],a[28:31],a[188:191],v[236:239]\n" \
"v_exp_f32 v175,v175\nM v[156:159],a[24:27],a[184:187],v[156:159]\nds_read_b128 a[80:83],v250 offset:4096\nds_read_b128 a[112:115],v250 offset:12288\nM v[156:159],a[20:23],a[180:183],v[156:159]\nv_cvt_pk_bf16_f32 v220,v172,v173\nv_cvt_pk_bf16_f32 v221,v174,v175\nM v[156:159],a[16:19],a[176:179],v[156:159]\n" \
"v_exp_f32 v188,v188\nM v[168:171],a[32:35],a[160:163],v[232:235]\nds_read_b128 a[84:87],v250 offset:5120\nds_read_b128 a[116:119],v250 offset:13312\nM v[168:171],a[36:39],a[164:167],v[168:171]\nv_exp_f32 v189,v189\nM v[168:171],a[40:43],a[168:171],v[168:171]\nv_exp_f32 v190,v190\n" \
"M v[168:171],a[44:47],a[172:175],v[168:171]\nds_read_b128 a[88:91],v250 offset:6144\nds_read_b128 a[120:123],v250 offset:14336\nM v[172:175],a[44:47],a[188:191],v[236:239]\nbuffer_load_dwordx4 v240,s[44:47],s95 offen offset:2048 sc1 lds\nM v[172:175],a[40:43],a[184:187],v[172:175]\nv_exp_f32 v191,v191\nM v[172:175],a[36:39],a[180:183],v[172:175]\n" \
"ds_read_b128 a[92:95],v250 offset:7168\nds_read_b128 a[124:127],v250 offset:15360\nM v[172:175],a[32:35],a[176:179],v[172:175]\nv_cvt_pk_bf16_f32 v222,v188,v189\ns_waitcnt lgkmcnt(7)\nM v[184:187],a[48:51],a[160:163],v[232:235]\nv_cvt_pk_bf16_f32 v223,v190,v191\nv_xor_b32 v250,0x18000,v250\n" \
"M v[184:187],a[52:55],a[164:167],v[184:187]\nv_exp_f32 v136,v136\nM v[184:187],a[56:59],a[168:171],v[184:187]\nv_exp_f32 v137,v137\nM v[184:187],a[60:63],a[172:175],v[184:187]\nv_exp_f32 v138,v138\nM v[188:191],a[60:63],a[188:191],v[236:239]\nv_exp_f32 v139,v139\n" \
"M v[188:191],a[56:59],a[184:187],v[188:191]\nbuffer_load_dwordx4 v240,s[44:47],s95 offen offset:3072 sc1 lds\nM v[188:191],a[52:55],a[180:183],v[188:191]\ns_add_u32 m0,m0,0x10000\ns_and_b32 s94,s94,0xffff\nM v[188:191],a[48:51],a[176:179],v[188:191]\nv_exp_f32 v152,v152\nM v[64:67],a[64:67],v[200:203],v[64:67]\n" \
"v_exp_f32 v153,v153\nM v[64:67],a[96:99],v[216:219],v[64:67]\nv_exp_f32 v154,v154\nM v[72:75],a[68:71],v[200:203],v[72:75]\ns_waitcnt vmcnt(16) lgkmcnt(0)\ns_barrier\nM v[72:75],a[100:103],v[216:219],v[72:75]\nds_read_b128 a[0:3],v250 offset:0\n" \
"ds_read_b128 a[4:7],v250 offset:1024\nM v[80:83],a[72:75],v[200:203],v[80:83]\nv_exp_f32 v155,v155\nM v[80:83],a[104:107],v[216:219],v[80:83]\nbuffer_load_dwordx4 v240,s[48:51],s95 offen offset:0 sc1 lds\nM v[88:91],a[76:79],v[200:203],v[88:91]\nv_exp_f32 v168,v168\nM v[88:91],a[108:111],v[216:219],v[88:91]\n" \
"ds_read_b128 a[8:11],v250 offset:2048\nds_read_b128 a[12:15],v250 offset:3072\nM v[96:99],a[80:83],v[200:203],v[96:99]\nv_exp_f32 v169,v169\nM v[96:99],a[112:115],v[216:219],v[96:99]\nv_exp_f32 v170,v170\nM v[104:107],a[84:87],v[200:203],v[104:107]\nv_exp_f32 v171,v171\n" \
"M v[104:107],a[116:119],v[216:219],v[104:107]\nds_read_b128 a[16:19],v250 offset:4096\nds_read_b128 a[20:23],v250 offset:5120\nM v[112:115],a[88:91],v[200:203],v[112:115]\nv_exp_f32 v184,v184\nM v[112:115],a[120:123],v[216:219],v[112:115]\nbuffer_load_dwordx4 v240,s[48:51],s95 offen offset:1024 sc1 lds\nM v[120:123],a[92:95],v[200:203],v[120:123]\n" \
"v_exp_f32 v185,v185\nM v[120:123],a[124:127],v[216:219],v[120:123]\nds_read_b128 a[24:27],v250 offset:6144\nds_read_b128 a[28:31],v250 offset:7168\nM a[200:203],a[208:211],v[200:203],a[200:203]\nv_exp_f32 v186,v186\nM a[200:203],a[208:211],v[216:219],a[200:203]\nv_exp_f32 v187,v187\n" \
"M v[68:71],a[64:67],v[204:207],v[68:71]\nv_exp_f32 v140,v140\nM v[68:71],a[96:99],v[220:223],v[68:71]\nds_read_b128 a[32:35],v250 offset:8192\nds_read_b128 a[36:39],v250 offset:9216\nM v[76:79],a[68:71],v[204:207],v[76:79]\nv_exp_f32 v141,v141\nM v[76:79],a[100:103],v[220:223],v[76:79]\n" \
"buffer_load_dwordx4 v240,s[48:51],s95 offen offset:2048 sc1 lds\nM v[84:87],a[72:75],v[204:207],v[84:87]\nv_exp_f32 v142,v142\nM v[84:87],a[104:107],v[220:223],v[84:87]\nds_read_b128 a[40:43],v250 offset:10240\nds_read_b128 a[44:47],v250 offset:11264\nM v[92:95],a[76:79],v[204:207],v[92:95]\nv_exp_f32 v143,v143\n" \
"M v[92:95],a[108:111],v[220:223],v[92:95]\nv_exp_f32 v156,v156\nM v[100:103],a[80:83],v[204:207],v[100:103]\nv_exp_f32 v157,v157\nM v[100:103],a[112:115],v[220:223],v[100:103]\nds_read_b128 a[48:51],v250 offset:12288\nds_read_b128 a[52:55],v250 offset:13312\nM v[108:111],a[84:87],v[204:207],v[108:111]\n" \
"v_exp_f32 v158,v158\nM v[108:111],a[116:119],v[220:223],v[108:111]\nbuffer_load_dwordx4 v240,s[48:51],s95 offen offset:3072 sc1 lds\nM v[116:119],a[88:91],v[204:207],v[116:119]\nv_exp_f32 v159,v159\nM v[116:119],a[120:123],v[220:223],v[116:119]\nds_read_b128 a[56:59],v250 offset:14336\nds_read_b128 a[60:63],v250 offset:15360\n" \
"M v[124:127],a[92:95],v[204:207],v[124:127]\nv_cvt_pk_bf16_f32 v200,v136,v137\nv_cvt_pk_bf16_f32 v201,v138,v139\nM v[124:127],a[124:127],v[220:223],v[124:127]\nv_cvt_pk_bf16_f32 v202,v152,v153\nv_cvt_pk_bf16_f32 v203,v154,v155\nM a[204:207],a[208:211],v[204:207],a[204:207]\nv_cvt_pk_bf16_f32 v216,v168,v169\n" \
"v_cvt_pk_bf16_f32 v217,v170,v171\nv_cvt_pk_bf16_f32 v218,v184,v185\nv_cvt_pk_bf16_f32 v219,v186,v187\nv_add_u32 v250,s94,v241\ns_add_u32 s95,s95,0x4000\nM a[204:207],a[208:211],v[220:223],a[204:207]\ns_cmp_lg_u32 s95,s96\ns_cbranch_scc1 .Lsl\n" \
"s_waitcnt lgkmcnt(4)\nM v[128:131],a[0:3],a[128:131],v[224:227]\ns_add_u32 m0,s94,s64\nv_cvt_pk_bf16_f32 v204,v140,v141\nM v[128:131],a[4:7],a[132:135],v[128:131]\nds_read_b128 a[64:67],v250 offset:0\nds_read_b128 a[96:99],v250 offset:8192\nM v[128:131],a[8:11],a[136:139],v[128:131]\n" \
"buffer_load_dwordx4 v240,s[44:47],s95 offen offset:0 sc1 lds\nM v[128:131],a[12:15],a[140:143],v[128:131]\nv_exp_f32 v188,v188\nM v[132:135],a[12:15],a[156:159],v[228:231]\nds_read_b128 a[68:71],v250 offset:1024\nds_read_b128 a[100:103],v250 offset:9216\nM v[132:135],a[8:11],a[152:155],v[132:135]\nv_exp_f32 v189,v189\n" \
"M v[132:135],a[4:7],a[148:151],v[132:135]\nv_exp_f32 v190,v190\nM v[132:135],a[0:3],a[144:147],v[132:135]\nds_read_b128 a[72:75],v250 offset:2048\nds_read_b128 a[104:107],v250 offset:10240\nM v[144:147],a[16:19],a[128:131],v[224:227]\nv_exp_f32 v191,v191\nM v[144:147],a[20:23],a[132:135],v[144:147]\n" \
"v_exp_f32 v172,v172\nM v[144:147],a[24:27],a[136:139],v[144:147]\nds_read_b128 a[76:79],v250 offset:3072\nds_read_b128 a[108:111],v250 offset:11264\nM v[144:147],a[28:31],a[140:143],v[144:147]\nbuffer_load_dwordx4 v240,s[44:47],s95 offen offset:1024 sc1 lds\nM v[148:151],a[28:31],a[156:159],v[228:231]\nv_exp_f32 v173,v173\n" \
"M v[148:151],a[24:27],a[152:155],v[148:151]\nds_read_b128 a[80:83],v250 offset:4096\nds_read_b128 a[112:115],v250 offset:12288\nM v[148:151],a[20:23],a[148:151],v[148:151]\nv_exp_f32 v174,v174\nM v[148:151],a[16:19],a[144:147],v[148:151]\nv_exp_f32 v175,v175\nM v[160:163],a[32:35],a[128:131],v[224:227]\n" \
"ds_read_b128 a[84:87],v250 offset:5120\nds_read_b128 a[116:119],v250 offset:13312\nM v[160:163],a[36:39],a[132:135],v[160:163]\nv_cvt_pk_bf16_f32 v205,v142,v143\nv_cvt_pk_bf16_f32 v206,v156,v157\nM v[160:163],a[40:43],a[136:139],v[160:163]\nv_cvt_pk_bf16_f32 v207,v158,v159\nv_cvt_pk_bf16_f32 v220,v172,v173\n" \
"M v[160:163],a[44:47],a[140:143],v[160:163]\nds_read_b128 a[88:91],v250 offset:6144\nds_read_b128 a[120:123],v250 offset:14336\nM v[164:167],a[44:47],a[156:159],v[228:231]\nbuffer_load_dwordx4 v240,s[44:47],s95 offen offset:2048 sc1 lds\nM v[164:167],a[40:43],a[152:155],v[164:167]\nv_cvt_pk_bf16_f32 v221,v174,v175\nv_cvt_pk_bf16_f32 v222,v188,v189\n" \
"M v[164:167],a[36:39],a[148:151],v[164:167]\nds_read_b128 a[92:95],v250 offset:7168\nds_read_b128 a[124:127],v250 offset:15360\nM v[164:167],a[32:35],a[144:147],v[164:167]\nv_xor_b32 v250,0x18000,v250\ns_waitcnt lgkmcnt(7)\nM v[176:179],a[48:51],a[128:131],v[224:227]\nv_cvt_pk_bf16_f32 v223,v190,v191\n" \
"s_add_u32 s94,s94,0x4000\nM v[176:179],a[52:55],a[132:135],v[176:179]\nv_exp_f32 v128,v128\nM v[176:179],a[56:59],a[136:139],v[176:179]\nv_exp_f32 v129,v129\nM v[176:179],a[60:63],a[140:143],v[176:179]\nv_exp_f32 v130,v130\nM v[180:183],a[60:63],a[156:159],v[228:231]\n" \
"v_exp_f32 v131,v131\nM v[180:183],a[56:59],a[152:155],v[180:183]\nbuffer_load_dwordx4 v240,s[44:47],s95 offen offset:3072 sc1 lds\nM v[180:183],a[52:55],a[148:151],v[180:183]\ns_add_u32 m0,m0,0x10000\ns_and_b32 s94,s94,0xffff\nM v[180:183],a[48:51],a[144:147],v[180:183]\nv_exp_f32 v144,v144\n" \
"M v[64:67],a[64:67],v[200:203],v[64:67]\nv_exp_f32 v145,v145\nM v[64:67],a[96:99],v[216:219],v[64:67]\nv_exp_f32 v146,v146\nM v[72:75],a[68:71],v[200:203],v[72:75]\ns_waitcnt vmcnt(16) lgkmcnt(0)\ns_barrier\nM v[72:75],a[100:103],v[216:219],v[72:75]\n" \
"ds_read_b128 a[0:3],v250 offset:0\nds_read_b128 a[4:7],v250 offset:1024\nM v[80:83],a[72:75],v[200:203],v[80:83]\nv_exp_f32 v147,v147\nM v[80:83],a[104:107],v[216:219],v[80:83]\nbuffer_load_dwordx4 v240,s[48:51],s95 offen offset:0 sc1 lds\nM v[88:91],a[76:79],v[200:203],v[88:91]\nv_exp_f32 v160,v160\n" \
"M v[88:91],a[108:111],v[216:219],v[88:91]\nds_read_b128 a[8:11],v250 offset:2048\nds_read_b128 a[12:15],v250 offset:3072\nM v[96:99],a[80:83],v[200:203],v[96:99]\nv_exp_f32 v161,v161\nM v[96:99],a[112:115],v[216:219],v[96:99]\nv_exp_f32 v162,v162\nM v[104:107],a[84:87],v[200:203],v[104:107]\n" \
"v_exp_f32 v163,v163\nM v[104:107],a[116:119],v[216:219],v[104:107]\nds_read_b128 a[16:19],v250 offset:4096\nds_read_b128 a[20:23],v250 offset:5120\nM v[112:115],a[88:91],v[200:203],v[112:115]\nv_exp_f32 v176,v176\nM v[112:115],a[120:123],v[216:219],v[112:115]\nbuffer_load_dwordx4 v240,s[48:51],s95 offen offset:1024 sc1 lds\n" \
"M v[120:123],a[92:95],v[200:203],v[120:123]\nv_exp_f32 v177,v177\nM v[120:123],a[124:127],v[216:219],v[120:123]\nds_read_b128 a[24:27],v250 offset:6144\nds_read_b128 a[28:31],v250 offset:7168\nM a[200:203],a[208:211],v[200:203],a[200:203]\nv_exp_f32 v178,v178\nM a[200:203],a[208:211],v[216:219],a[200:203]\n" \
"v_exp_f32 v179,v179\nM v[68:71],a[64:67],v[204:207],v[68:71]\nv_exp_f32 v132,v132\nM v[68:71],a[96:99],v[220:223],v[68:71]\nds_read_b128 a[32:35],v250 offset:8192\nds_read_b128 a[36:39],v250 offset:9216\nM v[76:79],a[68:71],v[204:207],v[76:79]\nv_exp_f32 v133,v133\n" \
"M v[76:79],a[100:103],v[220:223],v[76:79]\nbuffer_load_dwordx4 v240,s[48:51],s95 offen offset:2048 sc1 lds\nM v[84:87],a[72:75],v[204:207],v[84:87]\nv_exp_f32 v134,v134\nM v[84:87],a[104:107],v[220:223],v[84:87]\nds_read_b128 a[40:43],v250 offset:10240\nds_read_b128 a[44:47],v250 offset:11264\nM v[92:95],a[76:79],v[204:207],v[92:95]\n" \
"v_exp_f32 v135,v135\nM v[92:95],a[108:111],v[220:223],v[92:95]\nv_exp_f32 v148,v148\nM v[100:103],a[80:83],v[204:207],v[100:103]\nv_exp_f32 v149,v149\nM v[100:103],a[112:115],v[220:223],v[100:103]\nds_read_b128 a[48:51],v250 offset:12288\nds_read_b128 a[52:55],v250 offset:13312\n" \
"M v[108:111],a[84:87],v[204:207],v[108:111]\nv_exp_f32 v150,v150\nM v[108:111],a[116:119],v[220:223],v[108:111]\nbuffer_load_dwordx4 v240,s[48:51],s95 offen offset:3072 sc1 lds\nM v[116:119],a[88:91],v[204:207],v[116:119]\nv_exp_f32 v151,v151\nM v[116:119],a[120:123],v[220:223],v[116:119]\nds_read_b128 a[56:59],v250 offset:14336\n" \
"ds_read_b128 a[60:63],v250 offset:15360\nM v[124:127],a[92:95],v[204:207],v[124:127]\nv_cvt_pk_bf16_f32 v192,v128,v129\nv_cvt_pk_bf16_f32 v193,v130,v131\nM v[124:127],a[124:127],v[220:223],v[124:127]\nv_cvt_pk_bf16_f32 v194,v144,v145\nv_cvt_pk_bf16_f32 v195,v146,v147\nM a[204:207],a[208:211],v[204:207],a[204:207]\n" \
"v_cvt_pk_bf16_f32 v208,v160,v161\nv_cvt_pk_bf16_f32 v209,v162,v163\nv_cvt_pk_bf16_f32 v210,v176,v177\nv_cvt_pk_bf16_f32 v211,v178,v179\nv_add_u32 v250,s94,v241\ns_add_u32 s95,s95,0x4000\nM a[204:207],a[208:211],v[220:223],a[204:207]\ns_branch .Lse\n" \
".Ltrans:\ns_waitcnt lgkmcnt(4)\nM v[128:131],a[0:3],a[128:131],v[224:227]\ns_add_u32 m0,s94,s64\nv_cvt_pk_bf16_f32 v212,v164,v165\nM v[128:131],a[4:7],a[132:135],v[128:131]\nds_read_b128 a[64:67],v250 offset:0\nds_read_b128 a[96:99],v250 offset:8192\n" \
"M v[128:131],a[8:11],a[136:139],v[128:131]\nbuffer_load_dwordx4 v240,s[44:47],s95 offen offset:0 sc1 lds\nM v[128:131],a[12:15],a[140:143],v[128:131]\nv_cvt_pk_bf16_f32 v213,v166,v167\nv_cvt_pk_bf16_f32 v214,v180,v181\nM v[132:135],a[12:15],a[156:159],v[228:231]\nds_read_b128 a[68:71],v250 offset:1024\nds_read_b128 a[100:103],v250 offset:9216\n" \
"M v[132:135],a[8:11],a[152:155],v[132:135]\nbuffer_store_dwordx4 a[248:251],v253,s[52:55],s33 offen offset:0 sc1\nM v[132:135],a[4:7],a[148:151],v[132:135]\nv_cvt_pk_bf16_f32 v215,v182,v183\ns_add_u32 s71,s33,16384\nM v[132:135],a[0:3],a[144:147],v[132:135]\nds_read_b128 a[72:75],v250 offset:2048\nds_read_b128 a[104:107],v250 offset:10240\n" \
"M v[144:147],a[16:19],a[128:131],v[224:227]\nv_exp_f32 v184,v184\nM v[144:147],a[20:23],a[132:135],v[144:147]\nv_exp_f32 v185,v185\nM v[144:147],a[24:27],a[136:139],v[144:147]\nds_read_b128 a[76:79],v250 offset:3072\nds_read_b128 a[108:111],v250 offset:11264\nM v[144:147],a[28:31],a[140:143],v[144:147]\n" \
"buffer_load_dwordx4 v240,s[44:47],s95 offen offset:1024 sc1 lds\nM v[148:151],a[28:31],a[156:159],v[228:231]\nv_exp_f32 v186,v186\nM v[148:151],a[24:27],a[152:155],v[148:151]\nds_read_b128 a[80:83],v250 offset:4096\nds_read_b128 a[112:115],v250 offset:12288\nM v[148:151],a[20:23],a[148:151],v[148:151]\nbuffer_store_dwordx4 a[248:251],v253,s[52:55],s33 offen offset:1024 sc1\n" \
"M v[148:151],a[16:19],a[144:147],v[148:151]\nv_exp_f32 v187,v187\nM v[160:163],a[32:35],a[128:131],v[224:227]\nds_read_b128 a[84:87],v250 offset:5120\nds_read_b128 a[116:119],v250 offset:13312\nM v[160:163],a[36:39],a[132:135],v[160:163]\nv_cvt_pk_bf16_f32 v216,v168,v169\nv_cvt_pk_bf16_f32 v217,v170,v171\n" \
"M v[160:163],a[40:43],a[136:139],v[160:163]\nv_cvt_pk_bf16_f32 v218,v184,v185\nv_cvt_pk_bf16_f32 v219,v186,v187\nM v[160:163],a[44:47],a[140:143],v[160:163]\nds_read_b128 a[88:91],v250 offset:6144\nds_read_b128 a[120:123],v250 offset:14336\nM v[164:167],a[44:47],a[156:159],v[228:231]\nbuffer_load_dwordx4 v240,s[44:47],s95 offen offset:2048 sc1 lds\n" \
"M v[164:167],a[40:43],a[152:155],v[164:167]\ns_add_u32 s70,s78,16384\nv_cvt_pk_bf16_f32 v204,v140,v141\nM v[164:167],a[36:39],a[148:151],v[164:167]\nds_read_b128 a[92:95],v250 offset:7168\nds_read_b128 a[124:127],v250 offset:15360\nM v[164:167],a[32:35],a[144:147],v[164:167]\nbuffer_store_dwordx4 a[248:251],v253,s[52:55],s33 offen offset:2048 sc1\n" \
"s_waitcnt lgkmcnt(6)\nM v[176:179],a[48:51],a[128:131],v[224:227]\nv_xor_b32 v250,0x18000,v250\nv_cvt_pk_bf16_f32 v205,v142,v143\nM v[176:179],a[52:55],a[132:135],v[176:179]\nv_exp_f32 v172,v172\nM v[176:179],a[56:59],a[136:139],v[176:179]\nv_exp_f32 v173,v173\n" \
"M v[176:179],a[60:63],a[140:143],v[176:179]\nv_exp_f32 v174,v174\nM v[180:183],a[60:63],a[156:159],v[228:231]\nv_exp_f32 v175,v175\nM v[180:183],a[56:59],a[152:155],v[180:183]\nbuffer_load_dwordx4 v240,s[44:47],s95 offen offset:3072 sc1 lds\nM v[180:183],a[52:55],a[148:151],v[180:183]\ns_add_u32 m0,m0,0x10000\n" \
"v_cvt_pk_bf16_f32 v206,v156,v157\nM v[180:183],a[48:51],a[144:147],v[180:183]\nv_exp_f32 v188,v188\nM v[0:3],a[64:67],v[192:195],v[0:3]\nbuffer_store_dwordx4 a[248:251],v253,s[52:55],s33 offen offset:3072 sc1\nM v[0:3],a[96:99],v[208:211],v[0:3]\nv_exp_f32 v189,v189\nM v[8:11],a[68:71],v[192:195],v[8:11]\n" \
"s_waitcnt vmcnt(16) lgkmcnt(0)\ns_barrier\nM v[8:11],a[100:103],v[208:211],v[8:11]\nds_read_b128 a[0:3],v250 offset:0\nds_read_b128 a[4:7],v250 offset:1024\nM v[16:19],a[72:75],v[192:195],v[16:19]\nv_exp_f32 v190,v190\nM v[16:19],a[104:107],v[208:211],v[16:19]\n" \
"buffer_store_dwordx4 a[248:251],v253,s[52:55],s71 offen offset:0 sc1\nM v[24:27],a[76:79],v[192:195],v[24:27]\nv_exp_f32 v191,v191\nM v[24:27],a[108:111],v[208:211],v[24:27]\nbuffer_load_dwordx4 v240,s[48:51],s95 offen offset:0 sc1 lds\nM v[32:35],a[80:83],v[192:195],v[32:35]\nv_cvt_pk_bf16_f32 v207,v158,v159\nv_cvt_pk_bf16_f32 v220,v172,v173\n" \
"M v[32:35],a[112:115],v[208:211],v[32:35]\nbuffer_store_dwordx4 a[248:251],v253,s[52:55],s71 offen offset:1024 sc1\nM v[40:43],a[84:87],v[192:195],v[40:43]\nv_cvt_pk_bf16_f32 v221,v174,v175\nv_cvt_pk_bf16_f32 v222,v188,v189\nM v[40:43],a[116:119],v[208:211],v[40:43]\nds_read_b128 a[8:11],v250 offset:2048\nds_read_b128 a[12:15],v250 offset:3072\n" \
"M v[48:51],a[88:91],v[192:195],v[48:51]\nv_cvt_pk_bf16_f32 v223,v190,v191\ns_add_u32 s94,s94,0x4000\nM v[48:51],a[120:123],v[208:211],v[48:51]\nbuffer_store_dwordx4 a[248:251],v253,s[52:55],s71 offen offset:2048 sc1\nM v[56:59],a[92:95],v[192:195],v[56:59]\nv_exp_f32 v128,v128\nM v[56:59],a[124:127],v[208:211],v[56:59]\n" \
"v_exp_f32 v129,v129\nM a[192:195],a[208:211],v[192:195],a[192:195]\nv_exp_f32 v130,v130\nM a[192:195],a[208:211],v[208:211],a[192:195]\nbuffer_store_dwordx4 a[248:251],v253,s[52:55],s71 offen offset:3072 sc1\nM v[4:7],a[64:67],v[196:199],v[4:7]\nv_exp_f32 v131,v131\nM v[4:7],a[96:99],v[212:215],v[4:7]\n" \
"ds_read_b128 a[16:19],v250 offset:4096\nds_read_b128 a[20:23],v250 offset:5120\nM v[12:15],a[68:71],v[196:199],v[12:15]\nv_exp_f32 v144,v144\nM v[12:15],a[100:103],v[212:215],v[12:15]\nv_exp_f32 v145,v145\nM v[20:23],a[72:75],v[196:199],v[20:23]\nv_exp_f32 v146,v146\n" \
"M v[20:23],a[104:107],v[212:215],v[20:23]\nbuffer_load_dwordx4 v240,s[48:51],s95 offen offset:1024 sc1 lds\nM v[28:31],a[76:79],v[196:199],v[28:31]\nv_exp_f32 v147,v147\nM v[28:31],a[108:111],v[212:215],v[28:31]\nbuffer_store_dwordx4 a[248:251],v253,s[52:55],s78 offen offset:0 sc1\nM v[36:39],a[80:83],v[196:199],v[36:39]\nv_exp_f32 v160,v160\n" \
"M v[36:39],a[112:115],v[212:215],v[36:39]\nds_read_b128 a[24:27],v250 offset:6144\nds_read_b128 a[28:31],v250 offset:7168\nM v[44:47],a[84:87],v[196:199],v[44:47]\nv_exp_f32 v161,v161\nM v[44:47],a[116:119],v[212:215],v[44:47]\nbuffer_store_dwordx4 a[248:251],v253,s[52:55],s78 offen offset:1024 sc1\nM v[52:55],a[88:91],v[196:199],v[52:55]\n" \
"v_exp_f32 v162,v162\nM v[52:55],a[120:123],v[212:215],v[52:55]\nv_exp_f32 v163,v163\nM v[60:63],a[92:95],v[196:199],v[60:63]\nv_exp_f32 v176,v176\nM v[60:63],a[124:127],v[212:215],v[60:63]\nbuffer_store_dwordx4 a[248:251],v253,s[52:55],s78 offen offset:2048 sc1\nM a[196:199],a[208:211],v[196:199],a[196:199]\n" \
"v_exp_f32 v177,v177\nM a[196:199],a[208:211],v[212:215],a[196:199]\nds_read_b128 a[32:35],v250 offset:8192\nds_read_b128 a[36:39],v250 offset:9216\nM v[64:67],a[64:67],v[200:203],v[64:67]\nv_exp_f32 v178,v178\nM v[64:67],a[96:99],v[216:219],v[64:67]\nv_exp_f32 v179,v179\n" \
"M v[72:75],a[68:71],v[200:203],v[72:75]\ns_and_b32 s94,s94,0xffff\nv_cvt_pk_bf16_f32 v192,v128,v129\nM v[72:75],a[100:103],v[216:219],v[72:75]\nbuffer_load_dwordx4 v240,s[48:51],s95 offen offset:2048 sc1 lds\nM v[80:83],a[72:75],v[200:203],v[80:83]\nv_exp_f32 v132,v132\nM v[80:83],a[104:107],v[216:219],v[80:83]\n" \
"buffer_store_dwordx4 a[248:251],v253,s[52:55],s78 offen offset:3072 sc1\nM v[88:91],a[76:79],v[200:203],v[88:91]\nv_exp_f32 v133,v133\nM v[88:91],a[108:111],v[216:219],v[88:91]\nds_read_b128 a[40:43],v250 offset:10240\nds_read_b128 a[44:47],v250 offset:11264\nM v[96:99],a[80:83],v[200:203],v[96:99]\nv_exp_f32 v134,v134\n" \
"M v[96:99],a[112:115],v[216:219],v[96:99]\nbuffer_store_dwordx4 a[248:251],v253,s[52:55],s70 offen offset:0 sc1\nM v[104:107],a[84:87],v[200:203],v[104:107]\nv_exp_f32 v135,v135\nM v[104:107],a[116:119],v[216:219],v[104:107]\nv_exp_f32 v148,v148\nM v[112:115],a[88:91],v[200:203],v[112:115]\nv_exp_f32 v149,v149\n" \
"M v[112:115],a[120:123],v[216:219],v[112:115]\nbuffer_store_dwordx4 a[248:251],v253,s[52:55],s70 offen offset:1024 sc1\nM v[120:123],a[92:95],v[200:203],v[120:123]\nv_exp_f32 v150,v150\nM v[120:123],a[124:127],v[216:219],v[120:123]\nds_read_b128 a[48:51],v250 offset:12288\nds_read_b128 a[52:55],v250 offset:13312\nM a[200:203],a[208:211],v[200:203],a[200:203]\n" \
"v_exp_f32 v151,v151\nM a[200:203],a[208:211],v[216:219],a[200:203]\nv_cvt_pk_bf16_f32 v193,v130,v131\nv_cvt_pk_bf16_f32 v194,v144,v145\nM v[68:71],a[64:67],v[204:207],v[68:71]\nv_cvt_pk_bf16_f32 v195,v146,v147\nv_cvt_pk_bf16_f32 v208,v160,v161\nM v[68:71],a[96:99],v[220:223],v[68:71]\n" \
"buffer_load_dwordx4 v240,s[48:51],s95 offen offset:3072 sc1 lds\nM v[76:79],a[68:71],v[204:207],v[76:79]\nv_cvt_pk_bf16_f32 v209,v162,v163\nv_cvt_pk_bf16_f32 v210,v176,v177\nM v[76:79],a[100:103],v[220:223],v[76:79]\nbuffer_store_dwordx4 a[248:251],v253,s[52:55],s70 offen offset:2048 sc1\nM v[84:87],a[72:75],v[204:207],v[84:87]\nv_cvt_pk_bf16_f32 v211,v178,v179\n" \
"s_add_u32 s95,s95,0x4000\nM v[84:87],a[104:107],v[220:223],v[84:87]\nds_read_b128 a[56:59],v250 offset:14336\nds_read_b128 a[60:63],v250 offset:15360\nM v[92:95],a[76:79],v[204:207],v[92:95]\nv_add_u32 v250,s94,v241\nM v[92:95],a[108:111],v[220:223],v[92:95]\nbuffer_store_dwordx4 a[248:251],v253,s[52:55],s70 offen offset:3072 sc1\n" \
"M v[100:103],a[80:83],v[204:207],v[100:103]\nM v[100:103],a[112:115],v[220:223],v[100:103]\nM v[108:111],a[84:87],v[204:207],v[108:111]\nM v[108:111],a[116:119],v[220:223],v[108:111]\nM v[116:119],a[88:91],v[204:207],v[116:119]\nM v[116:119],a[120:123],v[220:223],v[116:119]\nM v[124:127],a[92:95],v[204:207],v[124:127]\nM v[124:127],a[124:127],v[220:223],v[124:127]\n" \
"M a[204:207],a[208:211],v[204:207],a[204:207]\nM a[204:207],a[208:211],v[220:223],a[204:207]\n.Lse:\ns_waitcnt lgkmcnt(4)\nM v[128:131],a[0:3],a[128:131],v[224:227]\ns_add_u32 m0,s94,s64\nv_accvgpr_read_b32 v232,a200\nv_cvt_pk_bf16_f32 v196,v132,v133\n" \
"v_cvt_pk_bf16_f32 v197,v134,v135\nM v[128:131],a[4:7],a[132:135],v[128:131]\nds_read_b128 a[64:67],v250 offset:0\nds_read_b128 a[96:99],v250 offset:8192\nv_accvgpr_read_b32 v236,a204\nv_add_f32 v233,v232,v236\nM v[128:131],a[8:11],a[136:139],v[128:131]\nbuffer_load_dwordx4 v240,s[44:47],s95 offen offset:0 sc1 lds\n" \
"v_rcp_f32 v232,v232\nM v[128:131],a[12:15],a[140:143],v[128:131]\nv_rcp_f32 v236,v236\nv_add_f32 v233,v233,v232\nv_add_f32 v233,v233,v236\nM v[132:135],a[12:15],a[156:159],v[228:231]\nds_read_b128 a[68:71],v250 offset:1024\nds_read_b128 a[100:103],v250 offset:9216\n" \
"v_cmp_ngt_f32 vcc,s28,v233\nv_mul_f32 v64,v64,v232\nM v[132:135],a[8:11],a[152:155],v[132:135]\ns_and_b64 vcc,vcc,s[90:91]\ns_cselect_b32 s39,0x8000000,s16\ns_cselect_b32 s16,s16,0x8000000\ns_cselect_b32 s71,2,1\ns_addc_u32 s99,s99,0\n" \
"M v[132:135],a[4:7],a[148:151],v[132:135]\nv_mul_f32 v65,v65,v232\ns_add_u32 s37,s12,s67\nv_mul_f32 v66,v66,v232\nv_mul_f32 v67,v67,v232\nM v[132:135],a[0:3],a[144:147],v[132:135]\nds_read_b128 a[72:75],v250 offset:2048\nds_read_b128 a[104:107],v250 offset:10240\n" \
"v_cvt_pk_bf16_f32 v64,v64,v65\ns_add_u32 s37,s37,1048576\nM v[144:147],a[16:19],a[128:131],v[224:227]\nv_mul_f32 v72,v72,v232\nv_mul_f32 v73,v73,v232\nv_mul_f32 v74,v74,v232\nv_mul_f32 v75,v75,v232\nv_cvt_pk_bf16_f32 v65,v66,v67\n" \
"M v[144:147],a[20:23],a[132:135],v[144:147]\ns_or_b32 s37,s37,s39\nv_cvt_pk_bf16_f32 v66,v72,v73\nv_cvt_pk_bf16_f32 v67,v74,v75\nbuffer_store_dwordx4 v[64:67],v240,s[52:55],s37 offen offset:0 sc1\nM v[144:147],a[24:27],a[136:139],v[144:147]\nds_read_b128 a[76:79],v250 offset:3072\nds_read_b128 a[108:111],v250 offset:11264\n" \
"v_cvt_pk_bf16_f32 v198,v148,v149\nv_cvt_pk_bf16_f32 v199,v150,v151\nv_mul_f32 v68,v68,v236\nM v[144:147],a[28:31],a[140:143],v[144:147]\nbuffer_load_dwordx4 v240,s[44:47],s95 offen offset:1024 sc1 lds\nv_mul_f32 v69,v69,v236\nv_mul_f32 v70,v70,v236\nM v[148:151],a[28:31],a[156:159],v[228:231]\n" \
"v_mul_f32 v71,v71,v236\nv_cvt_pk_bf16_f32 v68,v68,v69\nv_mul_f32 v76,v76,v236\nv_mul_f32 v77,v77,v236\nM v[148:151],a[24:27],a[152:155],v[148:151]\nds_read_b128 a[80:83],v250 offset:4096\nds_read_b128 a[112:115],v250 offset:12288\nv_mul_f32 v78,v78,v236\n" \
"v_mul_f32 v79,v79,v236\nM v[148:151],a[20:23],a[148:151],v[148:151]\nv_cvt_pk_bf16_f32 v69,v70,v71\nv_cvt_pk_bf16_f32 v70,v76,v77\nv_cvt_pk_bf16_f32 v71,v78,v79\nv_mul_f32 v80,v80,v232\nM v[148:151],a[16:19],a[144:147],v[148:151]\nv_exp_f32 v164,v164\n" \
"v_exp_f32 v165,v165\nM v[160:163],a[32:35],a[128:131],v[224:227]\nds_read_b128 a[84:87],v250 offset:5120\nds_read_b128 a[116:119],v250 offset:13312\nv_exp_f32 v166,v166\nv_cvt_pk_bf16_f32 v212,v164,v165\nM v[160:163],a[36:39],a[132:135],v[160:163]\nbuffer_store_dwordx4 v[68:71],v240,s[52:55],s37 offen offset:1024 sc1\n" \
"v_exp_f32 v167,v167\nM v[160:163],a[40:43],a[136:139],v[160:163]\nv_cvt_pk_bf16_f32 v213,v166,v167\nv_mul_f32 v81,v81,v232\nv_mul_f32 v82,v82,v232\nv_mul_f32 v83,v83,v232\nM v[160:163],a[44:47],a[140:143],v[160:163]\nds_read_b128 a[88:91],v250 offset:6144\n" \
"ds_read_b128 a[120:123],v250 offset:14336\nv_cvt_pk_bf16_f32 v80,v80,v81\nv_mul_f32 v88,v88,v232\nM v[164:167],a[44:47],a[156:159],v[228:231]\nbuffer_load_dwordx4 v240,s[44:47],s95 offen offset:2048 sc1 lds\nv_mul_f32 v89,v89,v232\nv_mul_f32 v90,v90,v232\nM v[164:167],a[40:43],a[152:155],v[164:167]\n" \
"v_mul_f32 v91,v91,v232\nv_cvt_pk_bf16_f32 v81,v82,v83\nv_cvt_pk_bf16_f32 v82,v88,v89\nv_cvt_pk_bf16_f32 v83,v90,v91\nv_mul_f32 v84,v84,v236\nM v[164:167],a[36:39],a[148:151],v[164:167]\nds_read_b128 a[92:95],v250 offset:7168\nds_read_b128 a[124:127],v250 offset:15360\n" \
"v_exp_f32 v180,v180\nM v[164:167],a[32:35],a[144:147],v[164:167]\nv_exp_f32 v181,v181\nv_mul_f32 v85,v85,v236\ns_waitcnt lgkmcnt(4)\nM v[176:179],a[48:51],a[128:131],v[224:227]\nv_exp_f32 v182,v182\nv_exp_f32 v183,v183\n" \
"M v[176:179],a[52:55],a[132:135],v[176:179]\nbuffer_store_dwordx4 v[80:83],v240,s[52:55],s37 offen offset:2048 sc1\nv_cvt_pk_bf16_f32 v214,v180,v181\nv_cvt_pk_bf16_f32 v215,v182,v183\nM v[176:179],a[56:59],a[136:139],v[176:179]\nv_mul_f32 v86,v86,v236\nv_mul_f32 v87,v87,v236\nv_cvt_pk_bf16_f32 v84,v84,v85\n" \
"v_mul_f32 v92,v92,v236\nM v[176:179],a[60:63],a[140:143],v[176:179]\nv_mul_f32 v93,v93,v236\nv_mul_f32 v94,v94,v236\nv_mul_f32 v95,v95,v236\nv_cvt_pk_bf16_f32 v85,v86,v87\nv_cvt_pk_bf16_f32 v86,v92,v93\nM v[180:183],a[60:63],a[156:159],v[228:231]\n" \
"v_cvt_pk_bf16_f32 v87,v94,v95\nv_xor_b32 v250,0x18000,v250\nv_mul_f32 v96,v96,v232\nv_mul_f32 v97,v97,v232\nM v[180:183],a[56:59],a[152:155],v[180:183]\nbuffer_load_dwordx4 v240,s[44:47],s95 offen offset:3072 sc1 lds\ns_add_u32 m0,m0,0x10000\nv_mul_f32 v98,v98,v232\n" \
"M v[180:183],a[52:55],a[148:151],v[180:183]\nv_mul_f32 v99,v99,v232\nv_cvt_pk_bf16_f32 v96,v96,v97\nv_mul_f32 v104,v104,v232\nv_mul_f32 v105,v105,v232\nM v[180:183],a[48:51],a[144:147],v[180:183]\nv_mul_f32 v106,v106,v232\nv_mul_f32 v107,v107,v232\n" \
"v_cvt_pk_bf16_f32 v97,v98,v99\ns_add_u32 s38,s37,4096\nM v[0:3],a[64:67],v[192:195],v[0:3]\nv_cvt_pk_bf16_f32 v98,v104,v105\nv_cvt_pk_bf16_f32 v99,v106,v107\nv_mul_f32 v100,v100,v236\nv_mul_f32 v101,v101,v236\nv_mul_f32 v102,v102,v236\n" \
"M v[0:3],a[96:99],v[208:211],v[0:3]\nbuffer_store_dwordx4 v[84:87],v240,s[52:55],s37 offen offset:3072 sc1\nv_mul_f32 v103,v103,v236\nv_cvt_pk_bf16_f32 v100,v100,v101\nM v[8:11],a[68:71],v[192:195],v[8:11]\ns_waitcnt vmcnt(16) lgkmcnt(0)\ns_barrier\nv_mul_f32 v108,v108,v236\n" \
"v_mul_f32 v109,v109,v236\nM v[8:11],a[100:103],v[208:211],v[8:11]\nds_read_b128 a[0:3],v250 offset:0\nds_read_b128 a[4:7],v250 offset:1024\nv_mul_f32 v110,v110,v236\nv_mul_f32 v111,v111,v236\nM v[16:19],a[72:75],v[192:195],v[16:19]\nv_cvt_pk_bf16_f32 v101,v102,v103\n" \
"v_cvt_pk_bf16_f32 v102,v108,v109\nv_cvt_pk_bf16_f32 v103,v110,v111\nv_mul_f32 v112,v112,v232\nM v[16:19],a[104:107],v[208:211],v[16:19]\nbuffer_load_dwordx4 v240,s[48:51],s95 offen offset:0 sc1 lds\nv_mul_f32 v113,v113,v232\nv_mul_f32 v114,v114,v232\nv_mul_f32 v115,v115,v232\n" \
"M v[24:27],a[76:79],v[192:195],v[24:27]\nv_cvt_pk_bf16_f32 v112,v112,v113\nv_mul_f32 v120,v120,v232\nv_mul_f32 v121,v121,v232\nv_mul_f32 v122,v122,v232\nM v[24:27],a[108:111],v[208:211],v[24:27]\nds_read_b128 a[8:11],v250 offset:2048\nds_read_b128 a[12:15],v250 offset:3072\n" \
"v_mul_f32 v123,v123,v232\nv_cvt_pk_bf16_f32 v113,v114,v115\nM v[32:35],a[80:83],v[192:195],v[32:35]\nv_cvt_pk_bf16_f32 v114,v120,v121\nv_cvt_pk_bf16_f32 v115,v122,v123\nv_mul_f32 v116,v116,v236\nv_mul_f32 v117,v117,v236\nM v[32:35],a[112:115],v[208:211],v[32:35]\n" \
"buffer_store_dwordx4 v[96:99],v240,s[52:55],s38 offen offset:0 sc1\nv_mul_f32 v118,v118,v236\nv_mul_f32 v119,v119,v236\nM v[40:43],a[84:87],v[192:195],v[40:43]\nv_cvt_pk_bf16_f32 v116,v116,v117\nv_mul_f32 v124,v124,v236\nv_mul_f32 v125,v125,v236\nv_mul_f32 v126,v126,v236\n" \
"M v[40:43],a[116:119],v[208:211],v[40:43]\nds_read_b128 a[16:19],v250 offset:4096\nds_read_b128 a[20:23],v250 offset:5120\nv_mul_f32 v127,v127,v236\nv_cvt_pk_bf16_f32 v117,v118,v119\nv_cvt_pk_bf16_f32 v118,v124,v125\nM v[48:51],a[88:91],v[192:195],v[48:51]\nv_cvt_pk_bf16_f32 v119,v126,v127\n" \
"v_exp_f32 v128,v128\ns_add_u32 s94,s94,0x4000\nM v[48:51],a[120:123],v[208:211],v[48:51]\nbuffer_load_dwordx4 v240,s[48:51],s95 offen offset:1024 sc1 lds\nv_exp_f32 v129,v129\nM v[56:59],a[92:95],v[192:195],v[56:59]\nv_exp_f32 v130,v130\nv_exp_f32 v131,v131\n" \
"M v[56:59],a[124:127],v[208:211],v[56:59]\nds_read_b128 a[24:27],v250 offset:6144\nds_read_b128 a[28:31],v250 offset:7168\nv_exp_f32 v144,v144\nM a[192:195],a[208:211],v[192:195],a[192:195]\nv_exp_f32 v145,v145\nv_exp_f32 v146,v146\ns_lshl2_add_u32 s77,s61,s79\n" \
"M a[192:195],a[208:211],v[208:211],a[192:195]\nbuffer_store_dwordx4 v[100:103],v240,s[52:55],s38 offen offset:1024 sc1\nv_exp_f32 v147,v147\nM v[4:7],a[64:67],v[196:199],v[4:7]\nv_exp_f32 v160,v160\nv_exp_f32 v161,v161\nM v[4:7],a[96:99],v[212:215],v[4:7]\nds_read_b128 a[32:35],v250 offset:8192\n" \
"ds_read_b128 a[36:39],v250 offset:9216\nv_exp_f32 v162,v162\nM v[12:15],a[68:71],v[196:199],v[12:15]\nv_exp_f32 v163,v163\nv_exp_f32 v176,v176\nM v[12:15],a[100:103],v[212:215],v[12:15]\nbuffer_load_dwordx4 v240,s[48:51],s95 offen offset:2048 sc1 lds\nv_exp_f32 v177,v177\n" \
"M v[20:23],a[72:75],v[196:199],v[20:23]\nv_exp_f32 v178,v178\nv_exp_f32 v179,v179\ns_and_b32 s94,s94,0xffff\nM v[20:23],a[104:107],v[212:215],v[20:23]\nds_read_b128 a[40:43],v250 offset:10240\nds_read_b128 a[44:47],v250 offset:11264\ns_add_u32 s77,s77,4096\n" \
"v_mov_b32 v233,s71\nM v[28:31],a[76:79],v[196:199],v[28:31]\nv_exp_f32 v132,v132\nv_exp_f32 v133,v133\nM v[28:31],a[108:111],v[212:215],v[28:31]\nbuffer_store_dwordx4 v[112:115],v240,s[52:55],s38 offen offset:2048 sc1\nv_exp_f32 v134,v134\nM v[36:39],a[80:83],v[196:199],v[36:39]\n" \
"v_exp_f32 v135,v135\nv_exp_f32 v148,v148\nM v[36:39],a[112:115],v[212:215],v[36:39]\nds_read_b128 a[48:51],v250 offset:12288\nds_read_b128 a[52:55],v250 offset:13312\nv_exp_f32 v149,v149\nv_cvt_pk_bf16_f32 v192,v128,v129\nM v[44:47],a[84:87],v[196:199],v[44:47]\n" \
"v_exp_f32 v150,v150\nv_exp_f32 v151,v151\nM v[44:47],a[116:119],v[212:215],v[44:47]\nbuffer_load_dwordx4 v240,s[48:51],s95 offen offset:3072 sc1 lds\nv_cvt_pk_bf16_f32 v193,v130,v131\nv_cvt_pk_bf16_f32 v194,v144,v145\nM v[52:55],a[88:91],v[196:199],v[52:55]\nv_cvt_pk_bf16_f32 v195,v146,v147\n" \
"v_cvt_pk_bf16_f32 v208,v160,v161\nv_cvt_pk_bf16_f32 v209,v162,v163\nv_cvt_pk_bf16_f32 v210,v176,v177\nM v[52:55],a[120:123],v[212:215],v[52:55]\nds_read_b128 a[56:59],v250 offset:14336\nds_read_b128 a[60:63],v250 offset:15360\nv_cvt_pk_bf16_f32 v211,v178,v179\nv_add_u32 v250,s94,v241\n" \
"M v[60:63],a[92:95],v[196:199],v[60:63]\ns_add_u32 s95,s95,0x4000\nM v[60:63],a[124:127],v[212:215],v[60:63]\nbuffer_store_dwordx4 v[116:119],v240,s[52:55],s38 offen offset:3072 sc1\nM a[196:199],a[208:211],v[196:199],a[196:199]\nM a[196:199],a[208:211],v[212:215],a[196:199]\ns_cmp_ge_u32 s95,s15\ns_cbranch_scc1 .Lsx\n" \
".Lsingle:\ns_waitcnt lgkmcnt(4)\nM v[128:131],a[0:3],a[128:131],v[224:227]\ns_xor_b32 m0,m0,0x14000\nv_cvt_pk_bf16_f32 v196,v132,v133\nM v[128:131],a[4:7],a[132:135],v[128:131]\nds_read_b128 a[64:67],v250 offset:0\nds_read_b128 a[96:99],v250 offset:8192\n" \
"M v[128:131],a[8:11],a[136:139],v[128:131]\nbuffer_load_dwordx4 v240,s[44:47],s95 offen offset:0 sc1 lds\nM v[128:131],a[12:15],a[140:143],v[128:131]\nv_cvt_pk_bf16_f32 v197,v134,v135\nv_cvt_pk_bf16_f32 v198,v148,v149\nM v[132:135],a[12:15],a[156:159],v[228:231]\nds_read_b128 a[68:71],v250 offset:1024\nds_read_b128 a[100:103],v250 offset:9216\n" \
"M v[132:135],a[8:11],a[152:155],v[132:135]\nv_exp_f32 v164,v164\nM v[132:135],a[4:7],a[148:151],v[132:135]\nv_exp_f32 v165,v165\nM v[132:135],a[0:3],a[144:147],v[132:135]\nds_read_b128 a[72:75],v250 offset:2048\nds_read_b128 a[104:107],v250 offset:10240\nM v[144:147],a[16:19],a[128:131],v[224:227]\n" \
"v_cvt_pk_bf16_f32 v199,v150,v151\nv_cvt_pk_bf16_f32 v212,v164,v165\nM v[144:147],a[20:23],a[132:135],v[144:147]\nv_exp_f32 v166,v166\nM v[144:147],a[24:27],a[136:139],v[144:147]\nds_read_b128 a[76:79],v250 offset:3072\nds_read_b128 a[108:111],v250 offset:11264\nM v[144:147],a[28:31],a[140:143],v[144:147]\n" \
"buffer_load_dwordx4 v240,s[44:47],s95 offen offset:1024 sc1 lds\nM v[148:151],a[28:31],a[156:159],v[228:231]\nv_exp_f32 v167,v167\nM v[148:151],a[24:27],a[152:155],v[148:151]\nds_read_b128 a[80:83],v250 offset:4096\nds_read_b128 a[112:115],v250 offset:12288\nM v[148:151],a[20:23],a[148:151],v[148:151]\nv_exp_f32 v180,v180\n" \
"M v[148:151],a[16:19],a[144:147],v[148:151]\nv_exp_f32 v181,v181\nM v[160:163],a[32:35],a[128:131],v[224:227]\nds_read_b128 a[84:87],v250 offset:5120\nds_read_b128 a[116:119],v250 offset:13312\nM v[160:163],a[36:39],a[132:135],v[160:163]\nv_cvt_pk_bf16_f32 v213,v166,v167\nv_cvt_pk_bf16_f32 v214,v180,v181\n" \
"M v[160:163],a[40:43],a[136:139],v[160:163]\nv_exp_f32 v182,v182\nM v[160:163],a[44:47],a[140:143],v[160:163]\nds_read_b128 a[88:91],v250 offset:6144\nds_read_b128 a[120:123],v250 offset:14336\nM v[164:167],a[44:47],a[156:159],v[228:231]\nbuffer_load_dwordx4 v240,s[44:47],s95 offen offset:2048 sc1 lds\nM v[164:167],a[40:43],a[152:155],v[164:167]\n" \
"v_exp_f32 v183,v183\nM v[164:167],a[36:39],a[148:151],v[164:167]\nds_read_b128 a[92:95],v250 offset:7168\nds_read_b128 a[124:127],v250 offset:15360\nM v[164:167],a[32:35],a[144:147],v[164:167]\nv_cvt_pk_bf16_f32 v215,v182,v183\ns_waitcnt lgkmcnt(7)\nM v[176:179],a[48:51],a[128:131],v[224:227]\n" \
"v_exp_f32 v128,v128\nM v[176:179],a[52:55],a[132:135],v[176:179]\nv_exp_f32 v129,v129\nM v[176:179],a[56:59],a[136:139],v[176:179]\nv_exp_f32 v130,v130\nM v[176:179],a[60:63],a[140:143],v[176:179]\nv_exp_f32 v131,v131\nM v[180:183],a[60:63],a[156:159],v[228:231]\n" \
"v_exp_f32 v132,v132\nM v[180:183],a[56:59],a[152:155],v[180:183]\nbuffer_load_dwordx4 v240,s[44:47],s95 offen offset:3072 sc1 lds\nM v[180:183],a[52:55],a[148:151],v[180:183]\nv_xor_b32 v250,0x18000,v250\ns_xor_b32 m0,m0,0x10000\nM v[180:183],a[48:51],a[144:147],v[180:183]\nv_exp_f32 v133,v133\n" \
"M v[0:3],a[64:67],v[192:195],v[0:3]\nv_exp_f32 v134,v134\nM v[0:3],a[96:99],v[208:211],v[0:3]\nv_exp_f32 v135,v135\nM v[8:11],a[68:71],v[192:195],v[8:11]\ns_waitcnt vmcnt(16) lgkmcnt(0)\ns_barrier\nM v[8:11],a[100:103],v[208:211],v[8:11]\n" \
"ds_read_b128 a[0:3],v250 offset:0\nds_read_b128 a[4:7],v250 offset:1024\nM v[16:19],a[72:75],v[192:195],v[16:19]\nv_exp_f32 v144,v144\nM v[16:19],a[104:107],v[208:211],v[16:19]\nbuffer_load_dwordx4 v240,s[48:51],s95 offen offset:0 sc1 lds\nM v[24:27],a[76:79],v[192:195],v[24:27]\nv_exp_f32 v145,v145\n" \
"M v[24:27],a[108:111],v[208:211],v[24:27]\nds_read_b128 a[8:11],v250 offset:2048\nds_read_b128 a[12:15],v250 offset:3072\nM v[32:35],a[80:83],v[192:195],v[32:35]\nv_exp_f32 v146,v146\nM v[32:35],a[112:115],v[208:211],v[32:35]\nv_exp_f32 v147,v147\nM v[40:43],a[84:87],v[192:195],v[40:43]\n" \
"v_exp_f32 v148,v148\nM v[40:43],a[116:119],v[208:211],v[40:43]\nds_read_b128 a[16:19],v250 offset:4096\nds_read_b128 a[20:23],v250 offset:5120\nM v[48:51],a[88:91],v[192:195],v[48:51]\nv_exp_f32 v149,v149\nM v[48:51],a[120:123],v[208:211],v[48:51]\nbuffer_load_dwordx4 v240,s[48:51],s95 offen offset:1024 sc1 lds\n" \
"M v[56:59],a[92:95],v[192:195],v[56:59]\nv_exp_f32 v150,v150\nM v[56:59],a[124:127],v[208:211],v[56:59]\nds_read_b128 a[24:27],v250 offset:6144\nds_read_b128 a[28:31],v250 offset:7168\nM a[192:195],a[208:211],v[192:195],a[192:195]\nv_cvt_pk_bf16_f32 v192,v128,v129\nv_cvt_pk_bf16_f32 v193,v130,v131\n" \
"M a[192:195],a[208:211],v[208:211],a[192:195]\nv_cvt_pk_bf16_f32 v194,v144,v145\nv_cvt_pk_bf16_f32 v195,v146,v147\nM v[4:7],a[64:67],v[196:199],v[4:7]\nv_exp_f32 v151,v151\nM v[4:7],a[96:99],v[212:215],v[4:7]\nds_read_b128 a[32:35],v250 offset:8192\nds_read_b128 a[36:39],v250 offset:9216\n" \
"M v[12:15],a[68:71],v[196:199],v[12:15]\nv_exp_f32 v160,v160\nM v[12:15],a[100:103],v[212:215],v[12:15]\nbuffer_load_dwordx4 v240,s[48:51],s95 offen offset:2048 sc1 lds\nM v[20:23],a[72:75],v[196:199],v[20:23]\nv_exp_f32 v161,v161\nM v[20:23],a[104:107],v[212:215],v[20:23]\nds_read_b128 a[40:43],v250 offset:10240\n" \
"ds_read_b128 a[44:47],v250 offset:11264\nM v[28:31],a[76:79],v[196:199],v[28:31]\nv_exp_f32 v162,v162\nM v[28:31],a[108:111],v[212:215],v[28:31]\nv_exp_f32 v163,v163\nM v[36:39],a[80:83],v[196:199],v[36:39]\nv_cvt_pk_bf16_f32 v208,v160,v161\nv_cvt_pk_bf16_f32 v209,v162,v163\n" \
"M v[36:39],a[112:115],v[212:215],v[36:39]\nds_read_b128 a[48:51],v250 offset:12288\nds_read_b128 a[52:55],v250 offset:13312\nM v[44:47],a[84:87],v[196:199],v[44:47]\nv_exp_f32 v164,v164\nM v[44:47],a[116:119],v[212:215],v[44:47]\nbuffer_load_dwordx4 v240,s[48:51],s95 offen offset:3072 sc1 lds\nM v[52:55],a[88:91],v[196:199],v[52:55]\n" \
"s_add_u32 s95,s95,0x4000\ns_xor_b32 m0,m0,0x1c000\nM v[52:55],a[120:123],v[212:215],v[52:55]\nds_read_b128 a[56:59],v250 offset:14336\nds_read_b128 a[60:63],v250 offset:15360\nM v[60:63],a[92:95],v[196:199],v[60:63]\nv_exp_f32 v165,v165\nM v[60:63],a[124:127],v[212:215],v[60:63]\n" \
"v_exp_f32 v166,v166\nM a[196:199],a[208:211],v[196:199],a[196:199]\nv_xor_b32 v250,0x14000,v250\nv_cvt_pk_bf16_f32 v196,v132,v133\nM a[196:199],a[208:211],v[212:215],a[196:199]\nv_cvt_pk_bf16_f32 v197,v134,v135\ns_waitcnt lgkmcnt(4)\nM v[128:131],a[0:3],a[128:131],v[224:227]\n" \
"v_cvt_pk_bf16_f32 v198,v148,v149\nv_cvt_pk_bf16_f32 v199,v150,v151\nM v[128:131],a[4:7],a[132:135],v[128:131]\nds_read_b128 a[64:67],v250 offset:0\nds_read_b128 a[96:99],v250 offset:8192\nM v[128:131],a[8:11],a[136:139],v[128:131]\nbuffer_load_dwordx4 v240,s[44:47],s95 offen offset:0 sc1 lds\nM v[128:131],a[12:15],a[140:143],v[128:131]\n" \
"v_exp_f32 v167,v167\nM v[132:135],a[12:15],a[156:159],v[228:231]\nds_read_b128 a[68:71],v250 offset:1024\nds_read_b128 a[100:103],v250 offset:9216\nM v[132:135],a[8:11],a[152:155],v[132:135]\nv_cvt_pk_bf16_f32 v212,v164,v165\nv_cvt_pk_bf16_f32 v213,v166,v167\nM v[132:135],a[4:7],a[148:151],v[132:135]\n" \
"v_exp_f32 v176,v176\nM v[132:135],a[0:3],a[144:147],v[132:135]\nds_read_b128 a[72:75],v250 offset:2048\nds_read_b128 a[104:107],v250 offset:10240\nM v[144:147],a[16:19],a[128:131],v[224:227]\nv_exp_f32 v177,v177\nM v[144:147],a[20:23],a[132:135],v[144:147]\nv_exp_f32 v178,v178\n" \
"M v[144:147],a[24:27],a[136:139],v[144:147]\nds_read_b128 a[76:79],v250 offset:3072\nds_read_b128 a[108:111],v250 offset:11264\nM v[144:147],a[28:31],a[140:143],v[144:147]\nbuffer_load_dwordx4 v240,s[44:47],s95 offen offset:1024 sc1 lds\nM v[148:151],a[28:31],a[156:159],v[228:231]\nv_exp_f32 v179,v179\nM v[148:151],a[24:27],a[152:155],v[148:151]\n" \
"ds_read_b128 a[80:83],v250 offset:4096\nds_read_b128 a[112:115],v250 offset:12288\nM v[148:151],a[20:23],a[148:151],v[148:151]\nv_cvt_pk_bf16_f32 v210,v176,v177\nv_cvt_pk_bf16_f32 v211,v178,v179\nM v[148:151],a[16:19],a[144:147],v[148:151]\nv_exp_f32 v180,v180\nM v[160:163],a[32:35],a[128:131],v[224:227]\n" \
"ds_read_b128 a[84:87],v250 offset:5120\nds_read_b128 a[116:119],v250 offset:13312\nM v[160:163],a[36:39],a[132:135],v[160:163]\nv_exp_f32 v181,v181\nM v[160:163],a[40:43],a[136:139],v[160:163]\nv_exp_f32 v182,v182\nM v[160:163],a[44:47],a[140:143],v[160:163]\nds_read_b128 a[88:91],v250 offset:6144\n" \
"ds_read_b128 a[120:123],v250 offset:14336\nM v[164:167],a[44:47],a[156:159],v[228:231]\nbuffer_load_dwordx4 v240,s[44:47],s95 offen offset:2048 sc1 lds\nM v[164:167],a[40:43],a[152:155],v[164:167]\nv_exp_f32 v183,v183\nM v[164:167],a[36:39],a[148:151],v[164:167]\nds_read_b128 a[92:95],v250 offset:7168\nds_read_b128 a[124:127],v250 offset:15360\n" \
"M v[164:167],a[32:35],a[144:147],v[164:167]\nv_cvt_pk_bf16_f32 v214,v180,v181\ns_waitcnt lgkmcnt(7)\nM v[176:179],a[48:51],a[128:131],v[224:227]\nv_cvt_pk_bf16_f32 v215,v182,v183\nv_xor_b32 v250,0x18000,v250\nM v[176:179],a[52:55],a[132:135],v[176:179]\nv_exp_f32 v128,v128\n" \
"M v[176:179],a[56:59],a[136:139],v[176:179]\nv_exp_f32 v129,v129\nM v[176:179],a[60:63],a[140:143],v[176:179]\nv_exp_f32 v130,v130\nM v[180:183],a[60:63],a[156:159],v[228:231]\nv_exp_f32 v131,v131\nM v[180:183],a[56:59],a[152:155],v[180:183]\nbuffer_load_dwordx4 v240,s[44:47],s95 offen offset:3072 sc1 lds\n" \
"M v[180:183],a[52:55],a[148:151],v[180:183]\nv_exp_f32 v144,v144\nM v[180:183],a[48:51],a[144:147],v[180:183]\nv_exp_f32 v145,v145\nM v[0:3],a[64:67],v[192:195],v[0:3]\nv_exp_f32 v146,v146\nM v[0:3],a[96:99],v[208:211],v[0:3]\nv_exp_f32 v147,v147\n" \
"M v[8:11],a[68:71],v[192:195],v[8:11]\ns_waitcnt vmcnt(16) lgkmcnt(0)\ns_barrier\nM v[8:11],a[100:103],v[208:211],v[8:11]\nds_read_b128 a[0:3],v250 offset:0\nds_read_b128 a[4:7],v250 offset:1024\nM v[16:19],a[72:75],v[192:195],v[16:19]\ns_xor_b32 m0,m0,0x10000\n" \
"M v[16:19],a[104:107],v[208:211],v[16:19]\nbuffer_load_dwordx4 v240,s[48:51],s95 offen offset:0 sc1 lds\nM v[24:27],a[76:79],v[192:195],v[24:27]\nv_exp_f32 v160,v160\nM v[24:27],a[108:111],v[208:211],v[24:27]\nds_read_b128 a[8:11],v250 offset:2048\nds_read_b128 a[12:15],v250 offset:3072\nM v[32:35],a[80:83],v[192:195],v[32:35]\n" \
"v_exp_f32 v161,v161\nM v[32:35],a[112:115],v[208:211],v[32:35]\nv_exp_f32 v162,v162\nM v[40:43],a[84:87],v[192:195],v[40:43]\nv_exp_f32 v163,v163\nM v[40:43],a[116:119],v[208:211],v[40:43]\nds_read_b128 a[16:19],v250 offset:4096\nds_read_b128 a[20:23],v250 offset:5120\n" \
"M v[48:51],a[88:91],v[192:195],v[48:51]\nv_exp_f32 v176,v176\nM v[48:51],a[120:123],v[208:211],v[48:51]\nbuffer_load_dwordx4 v240,s[48:51],s95 offen offset:1024 sc1 lds\nM v[56:59],a[92:95],v[192:195],v[56:59]\nv_exp_f32 v177,v177\nM v[56:59],a[124:127],v[208:211],v[56:59]\nds_read_b128 a[24:27],v250 offset:6144\n" \
"ds_read_b128 a[28:31],v250 offset:7168\nM a[192:195],a[208:211],v[192:195],a[192:195]\nv_exp_f32 v178,v178\nM a[192:195],a[208:211],v[208:211],a[192:195]\nv_exp_f32 v179,v179\nM v[4:7],a[64:67],v[196:199],v[4:7]\nv_exp_f32 v132,v132\nM v[4:7],a[96:99],v[212:215],v[4:7]\n" \
"ds_read_b128 a[32:35],v250 offset:8192\nds_read_b128 a[36:39],v250 offset:9216\nM v[12:15],a[68:71],v[196:199],v[12:15]\nv_exp_f32 v133,v133\nM v[12:15],a[100:103],v[212:215],v[12:15]\nbuffer_load_dwordx4 v240,s[48:51],s95 offen offset:2048 sc1 lds\nM v[20:23],a[72:75],v[196:199],v[20:23]\nv_exp_f32 v134,v134\n" \
"M v[20:23],a[104:107],v[212:215],v[20:23]\nds_read_b128 a[40:43],v250 offset:10240\nds_read_b128 a[44:47],v250 offset:11264\nM v[28:31],a[76:79],v[196:199],v[28:31]\nv_exp_f32 v135,v135\nM v[28:31],a[108:111],v[212:215],v[28:31]\nv_exp_f32 v148,v148\nM v[36:39],a[80:83],v[196:199],v[36:39]\n" \
"v_exp_f32 v149,v149\nM v[36:39],a[112:115],v[212:215],v[36:39]\nds_read_b128 a[48:51],v250 offset:12288\nds_read_b128 a[52:55],v250 offset:13312\nM v[44:47],a[84:87],v[196:199],v[44:47]\nv_exp_f32 v150,v150\nM v[44:47],a[116:119],v[212:215],v[44:47]\nbuffer_load_dwordx4 v240,s[48:51],s95 offen offset:3072 sc1 lds\n" \
"M v[52:55],a[88:91],v[196:199],v[52:55]\nv_exp_f32 v151,v151\nM v[52:55],a[120:123],v[212:215],v[52:55]\nds_read_b128 a[56:59],v250 offset:14336\nds_read_b128 a[60:63],v250 offset:15360\nM v[60:63],a[92:95],v[196:199],v[60:63]\nv_cvt_pk_bf16_f32 v192,v128,v129\nv_cvt_pk_bf16_f32 v193,v130,v131\n" \
"M v[60:63],a[124:127],v[212:215],v[60:63]\nv_cvt_pk_bf16_f32 v194,v144,v145\nv_cvt_pk_bf16_f32 v195,v146,v147\nM a[196:199],a[208:211],v[196:199],a[196:199]\nv_cvt_pk_bf16_f32 v208,v160,v161\nv_cvt_pk_bf16_f32 v209,v162,v163\nv_cvt_pk_bf16_f32 v210,v176,v177\nv_cvt_pk_bf16_f32 v211,v178,v179\n" \
"v_xor_b32 v250,0x1c000,v250\ns_add_u32 s95,s95,0x4000\nM a[196:199],a[208:211],v[212:215],a[196:199]\ns_cmp_lg_u32 s95,s15\ns_cbranch_scc1 .Lsingle\ns_and_b32 s94,m0,0x8000\ns_or_b32 s94,s94,0x4000\n.Lsx:\n" \
".Lsw:\ns_waitcnt lgkmcnt(4)\nM v[128:131],a[0:3],a[128:131],v[224:227]\ns_lshl4_add_u32 s69,s12,s68\ns_add_u32 m0,s64,131072\nM v[128:131],a[4:7],a[132:135],v[128:131]\nds_read_b128 a[64:67],v250 offset:0\nds_read_b128 a[96:99],v250 offset:8192\n" \
"v_cvt_pk_bf16_f32 v196,v132,v133\nM v[128:131],a[8:11],a[136:139],v[128:131]\nbuffer_load_dwordx4 v243,s[40:43],s69 offen offset:0 lds\nM v[128:131],a[12:15],a[140:143],v[128:131]\nv_cvt_pk_bf16_f32 v197,v134,v135\nv_cvt_pk_bf16_f32 v198,v148,v149\nv_cvt_pk_bf16_f32 v199,v150,v151\nM v[132:135],a[12:15],a[156:159],v[228:231]\n" \
"ds_read_b128 a[68:71],v250 offset:1024\nds_read_b128 a[100:103],v250 offset:9216\nM v[132:135],a[8:11],a[152:155],v[132:135]\nbuffer_load_dwordx4 v244,s[40:43],s69 offen offset:1024 lds\ns_add_u32 s94,s94,0x4000\nM v[132:135],a[4:7],a[148:151],v[132:135]\nv_exp_f32 v164,v164\nM v[132:135],a[0:3],a[144:147],v[132:135]\n" \
"ds_read_b128 a[72:75],v250 offset:2048\nds_read_b128 a[104:107],v250 offset:10240\ns_and_b32 s94,s94,0xffff\nM v[144:147],a[16:19],a[128:131],v[224:227]\nbuffer_load_dwordx4 v245,s[40:43],s69 offen offset:2048 lds\nM v[144:147],a[20:23],a[132:135],v[144:147]\nv_exp_f32 v165,v165\ns_add_u32 s95,s95,0x4000\n" \
"M v[144:147],a[24:27],a[136:139],v[144:147]\nds_read_b128 a[76:79],v250 offset:3072\nds_read_b128 a[108:111],v250 offset:11264\nM v[144:147],a[28:31],a[140:143],v[144:147]\nbuffer_load_dwordx4 v246,s[40:43],s69 offen offset:3072 lds\ns_lshl4_add_u32 s69,s12,s68\nM v[148:151],a[28:31],a[156:159],v[228:231]\ns_add_u32 s69,s69,0x10000\n" \
"s_add_u32 m0,s64,147456\nM v[148:151],a[24:27],a[152:155],v[148:151]\nds_read_b128 a[80:83],v250 offset:4096\nds_read_b128 a[112:115],v250 offset:12288\nv_cvt_pk_bf16_f32 v212,v164,v165\nM v[148:151],a[20:23],a[148:151],v[148:151]\nv_exp_f32 v166,v166\nM v[148:151],a[16:19],a[144:147],v[148:151]\n" \
"v_exp_f32 v167,v167\nM v[160:163],a[32:35],a[128:131],v[224:227]\nds_read_b128 a[84:87],v250 offset:5120\nds_read_b128 a[116:119],v250 offset:13312\nv_cvt_pk_bf16_f32 v213,v166,v167\nM v[160:163],a[36:39],a[132:135],v[160:163]\nbuffer_load_dwordx4 v243,s[40:43],s69 offen offset:0 lds\nM v[160:163],a[40:43],a[136:139],v[160:163]\n" \
"v_exp_f32 v180,v180\nM v[160:163],a[44:47],a[140:143],v[160:163]\nds_read_b128 a[88:91],v250 offset:6144\nds_read_b128 a[120:123],v250 offset:14336\nM v[164:167],a[44:47],a[156:159],v[228:231]\nv_exp_f32 v181,v181\nM v[164:167],a[40:43],a[152:155],v[164:167]\nbuffer_load_dwordx4 v244,s[40:43],s69 offen offset:1024 lds\n" \
"M v[164:167],a[36:39],a[148:151],v[164:167]\nds_read_b128 a[92:95],v250 offset:7168\nds_read_b128 a[124:127],v250 offset:15360\nv_cvt_pk_bf16_f32 v214,v180,v181\nM v[164:167],a[32:35],a[144:147],v[164:167]\nv_xor_b32 v250,0x18000,v250\ns_waitcnt lgkmcnt(6)\nM v[176:179],a[48:51],a[128:131],v[224:227]\n" \
"s_waitcnt vmcnt(6)\nbuffer_store_dword v233,off,s[52:55],s77 sc1\nM v[176:179],a[52:55],a[132:135],v[176:179]\nbuffer_load_dwordx4 v245,s[40:43],s69 offen offset:2048 lds\nM v[176:179],a[56:59],a[136:139],v[176:179]\nv_exp_f32 v182,v182\nv_exp_f32 v183,v183\nM v[176:179],a[60:63],a[140:143],v[176:179]\n" \
"v_cvt_pk_bf16_f32 v215,v182,v183\nM v[180:183],a[60:63],a[156:159],v[228:231]\nv_exp_f32 v128,v128\nM v[180:183],a[56:59],a[152:155],v[180:183]\nbuffer_load_dwordx4 v246,s[40:43],s69 offen offset:3072 lds\nM v[180:183],a[52:55],a[148:151],v[180:183]\nv_exp_f32 v129,v129\nM v[180:183],a[48:51],a[144:147],v[180:183]\n" \
"s_lshl4_add_u32 s69,s10,s68\nv_exp_f32 v130,v130\nM v[0:3],a[64:67],v[192:195],v[0:3]\nv_exp_f32 v131,v131\nM v[0:3],a[96:99],v[208:211],v[0:3]\ns_add_u32 m0,s64,0\nv_exp_f32 v144,v144\nM v[8:11],a[68:71],v[192:195],v[8:11]\n" \
"s_waitcnt vmcnt(16) lgkmcnt(0)\ns_barrier\nM v[8:11],a[100:103],v[208:211],v[8:11]\nds_read_b128 a[0:3],v250 offset:0\nds_read_b128 a[4:7],v250 offset:1024\nM v[16:19],a[72:75],v[192:195],v[16:19]\nv_exp_f32 v145,v145\nM v[16:19],a[104:107],v[208:211],v[16:19]\n" \
"buffer_load_dwordx4 v243,s[40:43],s69 offen offset:0 lds\nM v[24:27],a[76:79],v[192:195],v[24:27]\nv_exp_f32 v146,v146\nM v[24:27],a[108:111],v[208:211],v[24:27]\nds_read_b128 a[8:11],v250 offset:2048\nds_read_b128 a[12:15],v250 offset:3072\nM v[32:35],a[80:83],v[192:195],v[32:35]\nv_exp_f32 v147,v147\n" \
"M v[32:35],a[112:115],v[208:211],v[32:35]\nbuffer_load_dwordx4 v244,s[40:43],s69 offen offset:1024 lds\nM v[40:43],a[84:87],v[192:195],v[40:43]\nv_exp_f32 v160,v160\nM v[40:43],a[116:119],v[208:211],v[40:43]\nds_read_b128 a[16:19],v250 offset:4096\nds_read_b128 a[20:23],v250 offset:5120\nM v[48:51],a[88:91],v[192:195],v[48:51]\n" \
"v_exp_f32 v161,v161\nM v[48:51],a[120:123],v[208:211],v[48:51]\nbuffer_load_dwordx4 v245,s[40:43],s69 offen offset:2048 lds\nM v[56:59],a[92:95],v[192:195],v[56:59]\nv_exp_f32 v162,v162\nM v[56:59],a[124:127],v[208:211],v[56:59]\nds_read_b128 a[24:27],v250 offset:6144\nds_read_b128 a[28:31],v250 offset:7168\n" \
"M a[192:195],a[208:211],v[192:195],a[192:195]\nv_exp_f32 v163,v163\nv_cvt_pk_bf16_f32 v192,v128,v129\nM a[192:195],a[208:211],v[208:211],a[192:195]\nbuffer_load_dwordx4 v246,s[40:43],s69 offen offset:3072 lds\nM v[4:7],a[64:67],v[196:199],v[4:7]\ns_lshl4_add_u32 s69,s10,s68\ns_add_u32 s69,s69,0x10000\n" \
"s_add_u32 m0,s64,32768\nM v[4:7],a[96:99],v[212:215],v[4:7]\nds_read_b128 a[32:35],v250 offset:8192\nds_read_b128 a[36:39],v250 offset:9216\nM v[12:15],a[68:71],v[196:199],v[12:15]\nv_exp_f32 v176,v176\nv_cvt_pk_bf16_f32 v193,v130,v131\nM v[12:15],a[100:103],v[212:215],v[12:15]\n" \
"buffer_load_dwordx4 v243,s[40:43],s69 offen offset:0 lds\nM v[20:23],a[72:75],v[196:199],v[20:23]\nv_exp_f32 v177,v177\nv_cvt_pk_bf16_f32 v194,v144,v145\nM v[20:23],a[104:107],v[212:215],v[20:23]\nds_read_b128 a[40:43],v250 offset:10240\nds_read_b128 a[44:47],v250 offset:11264\nM v[28:31],a[76:79],v[196:199],v[28:31]\n" \
"v_exp_f32 v178,v178\nv_cvt_pk_bf16_f32 v195,v146,v147\nM v[28:31],a[108:111],v[212:215],v[28:31]\nbuffer_load_dwordx4 v244,s[40:43],s69 offen offset:1024 lds\nM v[36:39],a[80:83],v[196:199],v[36:39]\nv_exp_f32 v179,v179\nv_cvt_pk_bf16_f32 v208,v160,v161\nM v[36:39],a[112:115],v[212:215],v[36:39]\n" \
"ds_read_b128 a[48:51],v250 offset:12288\nds_read_b128 a[52:55],v250 offset:13312\nM v[44:47],a[84:87],v[196:199],v[44:47]\nv_exp_f32 v132,v132\nv_cvt_pk_bf16_f32 v209,v162,v163\nM v[44:47],a[116:119],v[212:215],v[44:47]\nbuffer_load_dwordx4 v245,s[40:43],s69 offen offset:2048 lds\nv_cvt_pk_bf16_f32 v210,v176,v177\n" \
"M v[52:55],a[88:91],v[196:199],v[52:55]\nv_exp_f32 v133,v133\nM v[52:55],a[120:123],v[212:215],v[52:55]\nds_read_b128 a[56:59],v250 offset:14336\nds_read_b128 a[60:63],v250 offset:15360\nv_cvt_pk_bf16_f32 v211,v178,v179\nM v[60:63],a[92:95],v[196:199],v[60:63]\nv_exp_f32 v134,v134\n" \
"M v[60:63],a[124:127],v[212:215],v[60:63]\nbuffer_load_dwordx4 v246,s[40:43],s69 offen offset:3072 lds\nv_add_u32 v250,s94,v241\nM a[196:199],a[208:211],v[196:199],a[196:199]\nv_exp_f32 v135,v135\nM a[196:199],a[208:211],v[212:215],a[196:199]\nv_exp_f32 v148,v148\nv_exp_f32 v149,v149\n" \
"v_exp_f32 v150,v150\nv_exp_f32 v151,v151\n.Lod:\ns_waitcnt lgkmcnt(2)\nM v[128:131],a[0:3],a[128:131],v[224:227]\nv_cvt_pk_bf16_f32 v196,v132,v133\nv_cvt_pk_bf16_f32 v197,v134,v135\nM v[128:131],a[4:7],a[132:135],v[128:131]\n" \
"ds_read_b128 a[64:67],v250 offset:0\nds_read_b128 a[96:99],v250 offset:8192\nM v[128:131],a[8:11],a[136:139],v[128:131]\ns_lshl2_add_u32 s39,s62,s7\ns_lshl_b32 s39,s39,5\nM v[128:131],a[12:15],a[140:143],v[128:131]\ns_add_u32 s39,s39,s63\nv_cvt_pk_bf16_f32 v198,v148,v149\n" \
"v_cvt_pk_bf16_f32 v199,v150,v151\nM v[132:135],a[12:15],a[156:159],v[228:231]\nds_read_b128 a[68:71],v250 offset:1024\nds_read_b128 a[100:103],v250 offset:9216\nM v[132:135],a[8:11],a[152:155],v[132:135]\ns_lshl2_add_u32 s39,s39,73728\nv_and_b32 v251,3,v249\nM v[132:135],a[4:7],a[148:151],v[132:135]\n" \
"s_waitcnt vmcnt(16)\nbuffer_store_dword v252,off,s[52:55],s39 sc1\nM v[132:135],a[0:3],a[144:147],v[132:135]\nds_read_b128 a[72:75],v250 offset:2048\nds_read_b128 a[104:107],v250 offset:10240\nv_lshlrev_b32 v251,2,v251\nM v[144:147],a[16:19],a[128:131],v[224:227]\nv_exp_f32 v164,v164\n" \
"M v[144:147],a[20:23],a[132:135],v[144:147]\nv_exp_f32 v165,v165\nM v[144:147],a[24:27],a[136:139],v[144:147]\nds_read_b128 a[76:79],v250 offset:3072\nds_read_b128 a[108:111],v250 offset:11264\nM v[144:147],a[28:31],a[140:143],v[144:147]\nv_exp_f32 v166,v166\nv_cvt_pk_bf16_f32 v212,v164,v165\n" \
"M v[148:151],a[28:31],a[156:159],v[228:231]\nv_exp_f32 v167,v167\nM v[148:151],a[24:27],a[152:155],v[148:151]\nds_read_b128 a[80:83],v250 offset:4096\nds_read_b128 a[112:115],v250 offset:12288\nM v[148:151],a[20:23],a[148:151],v[148:151]\nv_cvt_pk_bf16_f32 v213,v166,v167\ns_add_u32 s80,s79,4096\n" \
"M v[148:151],a[16:19],a[144:147],v[148:151]\nv_exp_f32 v180,v180\ns_add_u32 s94,s94,0x4000\nM v[160:163],a[32:35],a[128:131],v[224:227]\nds_read_b128 a[84:87],v250 offset:5120\nds_read_b128 a[116:119],v250 offset:13312\nM v[160:163],a[36:39],a[132:135],v[160:163]\nv_exp_f32 v181,v181\n" \
"M v[160:163],a[40:43],a[136:139],v[160:163]\nv_exp_f32 v182,v182\nM v[160:163],a[44:47],a[140:143],v[160:163]\nds_read_b128 a[88:91],v250 offset:6144\nds_read_b128 a[120:123],v250 offset:14336\nv_cvt_pk_bf16_f32 v214,v180,v181\nM v[164:167],a[44:47],a[156:159],v[228:231]\nbuffer_load_dword v248,v251,s[52:55],s80 offen sc1\n" \
"M v[164:167],a[40:43],a[152:155],v[164:167]\nv_exp_f32 v183,v183\nM v[164:167],a[36:39],a[148:151],v[164:167]\nds_read_b128 a[92:95],v250 offset:7168\nds_read_b128 a[124:127],v250 offset:15360\nM v[164:167],a[32:35],a[144:147],v[164:167]\nv_cvt_pk_bf16_f32 v215,v182,v183\ns_and_b32 s94,s94,0xffff\n" \
"v_add_u32 v250,s94,v241\nM v[176:179],a[48:51],a[128:131],v[224:227]\nv_exp_f32 v128,v128\nM v[176:179],a[52:55],a[132:135],v[176:179]\ns_add_u32 s65,s10,s67\ns_waitcnt lgkmcnt(4)\nM v[176:179],a[56:59],a[136:139],v[176:179]\nv_exp_f32 v129,v129\n" \
"M v[176:179],a[60:63],a[140:143],v[176:179]\nv_exp_f32 v130,v130\ns_add_u32 s65,s65,1048576\nM v[180:183],a[60:63],a[156:159],v[228:231]\nv_exp_f32 v131,v131\nM v[180:183],a[56:59],a[152:155],v[180:183]\nv_exp_f32 v144,v144\nM v[180:183],a[52:55],a[148:151],v[180:183]\n" \
"v_exp_f32 v145,v145\nM v[180:183],a[48:51],a[144:147],v[180:183]\nv_exp_f32 v146,v146\nv_add_u32 v226,131072,v242\nM v[0:3],a[64:67],v[192:195],v[0:3]\nv_exp_f32 v147,v147\nM v[0:3],a[96:99],v[208:211],v[0:3]\nv_exp_f32 v160,v160\n" \
"M v[8:11],a[68:71],v[192:195],v[8:11]\ns_waitcnt lgkmcnt(0)\ns_barrier\nM v[8:11],a[100:103],v[208:211],v[8:11]\ns_lshl4_add_u32 s69,s11,s68\ns_add_u32 m0,s64,16384\nv_xor_b32 v227,32,v226\nM v[16:19],a[72:75],v[192:195],v[16:19]\n" \
"v_exp_f32 v161,v161\nM v[16:19],a[104:107],v[208:211],v[16:19]\nv_exp_f32 v162,v162\nM v[24:27],a[76:79],v[192:195],v[24:27]\nv_exp_f32 v163,v163\nM v[24:27],a[108:111],v[208:211],v[24:27]\nv_exp_f32 v176,v176\ns_add_u32 s37,s12,s67\n" \
"M v[32:35],a[80:83],v[192:195],v[32:35]\nbuffer_load_dwordx4 v243,s[40:43],s69 offen offset:0 lds\nM v[32:35],a[112:115],v[208:211],v[32:35]\nv_exp_f32 v177,v177\nM v[40:43],a[84:87],v[192:195],v[40:43]\nv_exp_f32 v178,v178\nM v[40:43],a[116:119],v[208:211],v[40:43]\nv_exp_f32 v179,v179\n" \
"s_add_u32 s38,s37,4096\nM v[48:51],a[88:91],v[192:195],v[48:51]\nv_exp_f32 v132,v132\nM v[48:51],a[120:123],v[208:211],v[48:51]\nv_exp_f32 v133,v133\nM v[56:59],a[92:95],v[192:195],v[56:59]\nv_exp_f32 v134,v134\nM v[56:59],a[124:127],v[208:211],v[56:59]\n" \
"v_exp_f32 v135,v135\ns_lshl2_add_u32 s74,s63,s7\nM a[192:195],a[208:211],v[192:195],a[192:195]\nbuffer_load_dwordx4 v244,s[40:43],s69 offen offset:1024 lds\nM a[192:195],a[208:211],v[208:211],a[192:195]\nv_cvt_pk_bf16_f32 v192,v128,v129\nv_cvt_pk_bf16_f32 v193,v130,v131\nM v[4:7],a[64:67],v[196:199],v[4:7]\n" \
"ds_read_b128 a[64:67],v250 offset:0\nv_cvt_pk_bf16_f32 v194,v144,v145\nM v[4:7],a[96:99],v[212:215],v[4:7]\nds_read_b128 a[96:99],v250 offset:8192\nv_cvt_pk_bf16_f32 v195,v146,v147\nv_cvt_pk_bf16_f32 v208,v160,v161\nM v[12:15],a[68:71],v[196:199],v[12:15]\nds_read_b128 a[68:71],v250 offset:1024\n" \
"v_cvt_pk_bf16_f32 v209,v162,v163\nM v[12:15],a[100:103],v[212:215],v[12:15]\nds_read_b128 a[100:103],v250 offset:9216\nv_cvt_pk_bf16_f32 v210,v176,v177\nM v[20:23],a[72:75],v[196:199],v[20:23]\nds_read_b128 a[72:75],v250 offset:2048\nv_cvt_pk_bf16_f32 v211,v178,v179\nM v[20:23],a[104:107],v[212:215],v[20:23]\n" \
"ds_read_b128 a[104:107],v250 offset:10240\nv_exp_f32 v148,v148\nM v[28:31],a[76:79],v[196:199],v[28:31]\nbuffer_load_dwordx4 v245,s[40:43],s69 offen offset:2048 lds\nM v[28:31],a[108:111],v[212:215],v[28:31]\nds_read_b128 a[76:79],v250 offset:3072\nds_read_b128 a[108:111],v250 offset:11264\nM v[36:39],a[80:83],v[196:199],v[36:39]\n" \
"ds_read_b128 a[80:83],v250 offset:4096\ns_lshl_b32 s72,s62,7\nM v[36:39],a[112:115],v[212:215],v[36:39]\nds_read_b128 a[112:115],v250 offset:12288\nv_exp_f32 v149,v149\nM v[44:47],a[84:87],v[196:199],v[44:47]\nds_read_b128 a[84:87],v250 offset:5120\ns_add_u32 s95,s74,s72\n" \
"M v[44:47],a[116:119],v[212:215],v[44:47]\nds_read_b128 a[116:119],v250 offset:13312\ns_lshl2_add_u32 s76,s61,s79\nM v[52:55],a[88:91],v[196:199],v[52:55]\nds_read_b128 a[88:91],v250 offset:6144\ns_sub_u32 s74,s74,s72\nM v[52:55],a[120:123],v[212:215],v[52:55]\nds_read_b128 a[120:123],v250 offset:14336\n" \
"v_exp_f32 v150,v150\nM v[60:63],a[92:95],v[196:199],v[60:63]\nbuffer_load_dwordx4 v246,s[40:43],s69 offen offset:3072 lds\nM v[60:63],a[124:127],v[212:215],v[60:63]\ns_waitcnt vmcnt(4)\nv_and_b32 v248,s17,v248\nM a[196:199],a[208:211],v[196:199],a[196:199]\nv_cmp_eq_u32 vcc,0,v248\n" \
"ds_read_b128 a[92:95],v250 offset:7168\nM a[196:199],a[208:211],v[212:215],a[196:199]\ns_cbranch_vccnz .Lps6\n.Lpb6:\ns_lshl4_add_u32 s69,s11,s68\ns_waitcnt lgkmcnt(4)\nM v[0:3],a[64:67],v[192:195],v[0:3]\nbuffer_load_dwordx4 v[72:75],v240,s[52:55],s65 offen offset:0 sc1\n" \
"M v[0:3],a[96:99],v[208:211],v[0:3]\ns_add_u32 s69,s69,0x10000\ns_add_u32 m0,s64,49152\nM v[8:11],a[68:71],v[192:195],v[8:11]\nds_read_b128 a[124:127],v250 offset:15360\nv_cvt_pk_bf16_f32 v196,v132,v133\nM v[8:11],a[100:103],v[208:211],v[8:11]\nv_exp_f32 v151,v151\n" \
"v_cvt_pk_bf16_f32 v197,v134,v135\nM v[16:19],a[72:75],v[192:195],v[16:19]\nbuffer_load_dwordx4 v243,s[40:43],s69 offen offset:0 lds\nM v[16:19],a[104:107],v[208:211],v[16:19]\nv_exp_f32 v164,v164\nM v[24:27],a[76:79],v[192:195],v[24:27]\nv_exp_f32 v165,v165\nM v[24:27],a[108:111],v[208:211],v[24:27]\n" \
"v_exp_f32 v166,v166\nv_cvt_pk_bf16_f32 v198,v148,v149\nM v[32:35],a[80:83],v[192:195],v[32:35]\nbuffer_load_dwordx4 v[76:79],v240,s[52:55],s65 offen offset:1024 sc1\nM v[32:35],a[112:115],v[208:211],v[32:35]\nv_exp_f32 v167,v167\nM v[40:43],a[84:87],v[192:195],v[40:43]\nv_cvt_pk_bf16_f32 v199,v150,v151\n" \
"s_waitcnt lgkmcnt(0)\nM v[40:43],a[116:119],v[208:211],v[40:43]\nv_exp_f32 v180,v180\nv_cvt_pk_bf16_f32 v212,v164,v165\nM v[48:51],a[88:91],v[192:195],v[48:51]\nbuffer_load_dwordx4 v244,s[40:43],s69 offen offset:1024 lds\nM v[48:51],a[120:123],v[208:211],v[48:51]\nv_exp_f32 v181,v181\n" \
"M v[56:59],a[92:95],v[192:195],v[56:59]\nv_exp_f32 v182,v182\nM v[56:59],a[124:127],v[208:211],v[56:59]\nv_exp_f32 v183,v183\nv_cvt_pk_bf16_f32 v213,v166,v167\nM a[192:195],a[208:211],v[192:195],a[192:195]\nbuffer_load_dwordx4 v[88:91],v240,s[52:55],s65 offen offset:2048 sc1\nM a[192:195],a[208:211],v[208:211],a[192:195]\n" \
"v_cvt_pk_bf16_f32 v214,v180,v181\nv_cvt_pk_bf16_f32 v215,v182,v183\nM v[4:7],a[64:67],v[196:199],v[4:7]\ns_barrier\nds_read_b64_tr_b16 v[200:201],v226 offset:0\nM v[4:7],a[96:99],v[212:215],v[4:7]\nds_read_b64_tr_b16 v[202:203],v226 offset:4096\nds_read_b64_tr_b16 v[204:205],v226 offset:8192\n" \
"ds_read_b64_tr_b16 v[206:207],v226 offset:12288\nM v[12:15],a[68:71],v[196:199],v[12:15]\nbuffer_load_dwordx4 v245,s[40:43],s69 offen offset:2048 lds\nM v[12:15],a[100:103],v[212:215],v[12:15]\nds_read_b64_tr_b16 v[216:217],v227 offset:0\nds_read_b64_tr_b16 v[218:219],v227 offset:4096\nM v[20:23],a[72:75],v[196:199],v[20:23]\nds_read_b64_tr_b16 v[220:221],v227 offset:8192\n" \
"ds_read_b64_tr_b16 v[222:223],v227 offset:12288\nM v[20:23],a[104:107],v[212:215],v[20:23]\nds_read_b64_tr_b16 v[192:193],v226 offset:512\nds_read_b64_tr_b16 v[194:195],v226 offset:4608\nds_read_b64_tr_b16 v[208:209],v226 offset:8704\nM v[28:31],a[76:79],v[196:199],v[28:31]\nbuffer_load_dwordx4 v[92:95],v240,s[52:55],s65 offen offset:3072 sc1\nM v[28:31],a[108:111],v[212:215],v[28:31]\n" \
"s_add_u32 s65,s65,4096\nds_read_b64_tr_b16 v[210:211],v226 offset:12800\nM v[36:39],a[80:83],v[196:199],v[36:39]\nv_accvgpr_read_b32 v224,a192\nv_cmp_eq_u32 vcc,2,v248\nM v[36:39],a[112:115],v[212:215],v[36:39]\ns_cmp_lg_u64 vcc,0\ns_addc_u32 s99,s99,0\n" \
"v_xor_b32 v250,0x8000,v250\nM v[44:47],a[84:87],v[196:199],v[44:47]\nbuffer_load_dwordx4 v246,s[40:43],s69 offen offset:3072 lds\nM v[44:47],a[116:119],v[212:215],v[44:47]\nv_add_u32 v229,s64,v250\ns_lshl_b32 s72,s7,9\nM v[52:55],a[88:91],v[196:199],v[52:55]\nv_lshl_add_u32 v239,v249,3,s72\n" \
"s_lshr_b32 s72,s64,1\nM v[52:55],a[120:123],v[212:215],v[52:55]\nv_and_b32 v254,32,v249\ns_lshr_b32 s70,s60,1\nv_lshlrev_b32 v254,3,v254\nM v[60:63],a[92:95],v[196:199],v[60:63]\nbuffer_load_dwordx4 v[104:107],v240,s[52:55],s65 offen offset:0 sc1\nM v[60:63],a[124:127],v[212:215],v[60:63]\n" \
"s_lshl_b32 s70,s70,7\nv_lshl_add_u32 v254,v249,2,v254\nM a[196:199],a[208:211],v[196:199],a[196:199]\nds_read_b64_tr_b16 v[196:197],v227 offset:512\nds_read_b64_tr_b16 v[198:199],v227 offset:4608\nM a[196:199],a[208:211],v[212:215],a[196:199]\nds_read_b64_tr_b16 v[212:213],v227 offset:8704\nds_read_b64_tr_b16 v[214:215],v227 offset:12800\n" \
"s_waitcnt lgkmcnt(4)\nM v[128:131],v[64:67],v[200:203],0\ns_lshl_b32 s80,s62,9\nv_xor_b32 v254,s70,v254\ns_add_u32 s80,s80,73728\nv_and_b32 v236,15,v249\nM v[128:131],v[80:83],v[204:207],v[128:131]\nv_accvgpr_read_b32 v228,a196\n" \
"v_add_f32 v225,v224,v228\nv_rcp_f32 v224,v224\nv_lshrrev_b32 v235,4,v249\nM v[132:135],v[68:71],v[200:203],0\nv_mul_f32 v0,v0,v224\nv_mul_f32 v1,v1,v224\nv_mul_f32 v2,v2,v224\nv_mul_f32 v3,v3,v224\n" \
"M v[132:135],v[84:87],v[204:207],v[132:135]\nv_cvt_pk_bf16_f32 v0,v0,v1\nv_mul_f32 v8,v8,v224\nv_mul_f32 v9,v9,v224\nv_mul_f32 v10,v10,v224\nv_mul_f32 v11,v11,v224\nM v[136:139],v[64:67],v[216:219],0\nbuffer_load_dwordx4 v[108:111],v240,s[52:55],s65 offen offset:1024 sc1\n" \
"v_cvt_pk_bf16_f32 v1,v2,v3\nds_read_b64_tr_b16 v[200:201],v226 offset:1024\nM v[136:139],v[80:83],v[220:223],v[136:139]\nds_read_b64_tr_b16 v[202:203],v226 offset:5120\nv_cvt_pk_bf16_f32 v2,v8,v9\nv_cvt_pk_bf16_f32 v3,v10,v11\nds_read_b64_tr_b16 v[204:205],v226 offset:9216\nds_read_b64_tr_b16 v[206:207],v226 offset:13312\n" \
"M v[140:143],v[68:71],v[216:219],0\nbuffer_store_dwordx4 v[0:3],v240,s[52:55],s37 offen offset:0 sc1\nv_rcp_f32 v228,v228\nM v[140:143],v[84:87],v[220:223],v[140:143]\nv_mul_f32 v4,v4,v228\nv_mul_f32 v5,v5,v228\nds_read_b64_tr_b16 v[216:217],v227 offset:1024\nds_read_b64_tr_b16 v[218:219],v227 offset:5120\n" \
"v_mul_f32 v6,v6,v228\nM v[144:147],v[64:67],v[192:195],0\nv_mul_f32 v7,v7,v228\nv_cvt_pk_bf16_f32 v4,v4,v5\nv_mul_f32 v12,v12,v228\nv_mul_f32 v13,v13,v228\nM v[144:147],v[80:83],v[208:211],v[144:147]\nv_mul_f32 v14,v14,v228\n" \
"v_mul_f32 v15,v15,v228\nv_cvt_pk_bf16_f32 v5,v6,v7\nds_read_b64_tr_b16 v[220:221],v227 offset:9216\nds_read_b64_tr_b16 v[222:223],v227 offset:13312\nM v[148:151],v[68:71],v[192:195],0\nv_cvt_pk_bf16_f32 v6,v12,v13\nv_cvt_pk_bf16_f32 v7,v14,v15\nds_read_b64_tr_b16 v[192:193],v226 offset:1536\n" \
"ds_read_b64_tr_b16 v[194:195],v226 offset:5632\nM v[148:151],v[84:87],v[208:211],v[148:151]\nbuffer_store_dwordx4 v[4:7],v240,s[52:55],s37 offen offset:1024 sc1\nv_mul_f32 v16,v16,v224\nv_mul_f32 v17,v17,v224\ns_waitcnt lgkmcnt(6)\nM v[152:155],v[64:67],v[196:199],0\nbuffer_load_dwordx4 v[120:123],v240,s[52:55],s65 offen offset:2048 sc1\n" \
"ds_read_b64_tr_b16 v[208:209],v226 offset:9728\nds_read_b64_tr_b16 v[210:211],v226 offset:13824\nM v[152:155],v[80:83],v[212:215],v[152:155]\nv_mul_f32 v18,v18,v224\nv_mul_f32 v19,v19,v224\nv_cvt_pk_bf16_f32 v16,v16,v17\nv_mul_f32 v24,v24,v224\nv_mul_f32 v25,v25,v224\n" \
"M v[156:159],v[68:71],v[196:199],0\nv_mul_f32 v26,v26,v224\nv_mul_f32 v27,v27,v224\nv_cvt_pk_bf16_f32 v17,v18,v19\nv_cvt_pk_bf16_f32 v18,v24,v25\nM v[156:159],v[84:87],v[212:215],v[156:159]\nv_cvt_pk_bf16_f32 v19,v26,v27\nds_read_b64_tr_b16 v[196:197],v227 offset:1536\n" \
"ds_read_b64_tr_b16 v[198:199],v227 offset:5632\nds_read_b64_tr_b16 v[212:213],v227 offset:9728\nv_mul_f32 v20,v20,v228\nM v[160:163],v[64:67],v[200:203],0\nbuffer_store_dwordx4 v[16:19],v240,s[52:55],s37 offen offset:2048 sc1\nds_read_b64_tr_b16 v[214:215],v227 offset:13824\nv_mul_f32 v21,v21,v228\nM v[160:163],v[80:83],v[204:207],v[160:163]\n" \
"v_mul_f32 v22,v22,v228\nv_mul_f32 v23,v23,v228\nv_cvt_pk_bf16_f32 v20,v20,v21\nv_mul_f32 v28,v28,v228\nv_mul_f32 v29,v29,v228\nM v[164:167],v[68:71],v[200:203],0\nv_mul_f32 v30,v30,v228\nv_mul_f32 v31,v31,v228\n" \
"v_cvt_pk_bf16_f32 v21,v22,v23\nv_cvt_pk_bf16_f32 v22,v28,v29\nM v[164:167],v[84:87],v[204:207],v[164:167]\nv_cvt_pk_bf16_f32 v23,v30,v31\nds_read_b64_tr_b16 v[200:201],v226 offset:16384\nds_read_b64_tr_b16 v[202:203],v226 offset:20480\nds_read_b64_tr_b16 v[204:205],v226 offset:24576\ns_waitcnt lgkmcnt(6)\n" \
"M v[168:171],v[64:67],v[216:219],0\nbuffer_load_dwordx4 v[124:127],v240,s[52:55],s65 offen offset:3072 sc1\nds_read_b64_tr_b16 v[206:207],v226 offset:28672\nv_mul_f32 v32,v32,v224\nM v[168:171],v[80:83],v[220:223],v[168:171]\nbuffer_store_dwordx4 v[20:23],v240,s[52:55],s37 offen offset:3072 sc1\nv_mul_f32 v33,v33,v224\nv_mul_f32 v34,v34,v224\n" \
"v_mul_f32 v35,v35,v224\nM v[172:175],v[68:71],v[216:219],0\nv_cvt_pk_bf16_f32 v32,v32,v33\nv_mul_f32 v40,v40,v224\nv_mul_f32 v41,v41,v224\nv_mul_f32 v42,v42,v224\nM v[172:175],v[84:87],v[220:223],v[172:175]\nv_mul_f32 v43,v43,v224\n" \
"v_cvt_pk_bf16_f32 v33,v34,v35\nds_read_b64_tr_b16 v[216:217],v227 offset:16384\nds_read_b64_tr_b16 v[218:219],v227 offset:20480\nv_cvt_pk_bf16_f32 v34,v40,v41\nM v[176:179],v[64:67],v[192:195],0\nv_cvt_pk_bf16_f32 v35,v42,v43\nds_read_b64_tr_b16 v[220:221],v227 offset:24576\nds_read_b64_tr_b16 v[222:223],v227 offset:28672\n" \
"s_add_u32 s65,s11,s67\nM v[176:179],v[80:83],v[208:211],v[176:179]\ns_add_u32 s65,s65,1048576\nv_mul_f32 v36,v36,v228\nv_mul_f32 v37,v37,v228\nv_mul_f32 v38,v38,v228\nv_mul_f32 v39,v39,v228\nM v[180:183],v[68:71],v[192:195],0\n" \
"buffer_store_dwordx4 v[32:35],v240,s[52:55],s38 offen offset:0 sc1\nds_read_b64_tr_b16 v[192:193],v226 offset:16896\nds_read_b64_tr_b16 v[194:195],v226 offset:20992\nM v[180:183],v[84:87],v[208:211],v[180:183]\nv_cvt_pk_bf16_f32 v36,v36,v37\nv_mul_f32 v44,v44,v228\nv_mul_f32 v45,v45,v228\nv_mul_f32 v46,v46,v228\n" \
"s_waitcnt lgkmcnt(6)\nM v[184:187],v[64:67],v[196:199],0\nbuffer_load_dwordx4 a[0:3],v240,s[52:55],s65 offen offset:0 sc1\nv_mul_f32 v47,v47,v228\nv_cvt_pk_bf16_f32 v37,v38,v39\nM v[184:187],v[80:83],v[212:215],v[184:187]\nds_read_b64_tr_b16 v[208:209],v226 offset:25088\nds_read_b64_tr_b16 v[210:211],v226 offset:29184\n" \
"v_cvt_pk_bf16_f32 v38,v44,v45\nv_cvt_pk_bf16_f32 v39,v46,v47\nv_add_f32 v225,v225,v224\nM v[188:191],v[68:71],v[196:199],0\nds_read_b64_tr_b16 v[196:197],v227 offset:16896\nds_read_b64_tr_b16 v[198:199],v227 offset:20992\nv_add_f32 v225,v225,v228\nv_cmp_ngt_f32 vcc,s28,v225\n" \
"M v[188:191],v[84:87],v[212:215],v[188:191]\nbuffer_store_dwordx4 v[36:39],v240,s[52:55],s38 offen offset:1024 sc1\nv_mul_f32 v48,v48,v224\nv_mul_f32 v49,v49,v224\nds_read_b64_tr_b16 v[212:213],v227 offset:25088\nM v[128:131],v[96:99],v[200:203],v[128:131]\nds_read_b64_tr_b16 v[214:215],v227 offset:29184\ns_and_b64 vcc,vcc,s[90:91]\n" \
"s_addc_u32 s99,s99,0\nv_mul_f32 v50,v50,v224\nM v[128:131],v[112:115],v[204:207],v[128:131]\nv_mul_f32 v51,v51,v224\nv_cvt_pk_bf16_f32 v48,v48,v49\nv_mul_f32 v56,v56,v224\nv_mul_f32 v57,v57,v224\nv_mul_f32 v58,v58,v224\n" \
"M v[132:135],v[100:103],v[200:203],v[132:135]\nv_mul_f32 v59,v59,v224\nv_cvt_pk_bf16_f32 v49,v50,v51\nv_cvt_pk_bf16_f32 v50,v56,v57\nv_cvt_pk_bf16_f32 v51,v58,v59\nM v[132:135],v[116:119],v[204:207],v[132:135]\nv_mov_b32 v230,s99\nds_write_b32 v229,v230\n" \
"ds_read_b64_tr_b16 v[200:201],v226 offset:17408\nds_read_b64_tr_b16 v[202:203],v226 offset:21504\ns_waitcnt lgkmcnt(5)\nM v[136:139],v[96:99],v[216:219],v[136:139]\nbuffer_store_dwordx4 v[48:51],v240,s[52:55],s38 offen offset:2048 sc1\nbuffer_load_dwordx4 a[4:7],v240,s[52:55],s65 offen offset:1024 sc1\nM v[136:139],v[112:115],v[220:223],v[136:139]\nds_read_b64_tr_b16 v[204:205],v226 offset:25600\n" \
"ds_read_b64_tr_b16 v[206:207],v226 offset:29696\nv_mul_f32 v52,v52,v228\nv_mul_f32 v53,v53,v228\nv_mul_f32 v54,v54,v228\nM v[140:143],v[100:103],v[216:219],v[140:143]\nv_mul_f32 v55,v55,v228\nv_cvt_pk_bf16_f32 v52,v52,v53\nv_mul_f32 v60,v60,v228\n" \
"v_mul_f32 v61,v61,v228\nM v[140:143],v[116:119],v[220:223],v[140:143]\nv_mul_f32 v62,v62,v228\nv_mul_f32 v63,v63,v228\nv_cvt_pk_bf16_f32 v53,v54,v55\nv_cvt_pk_bf16_f32 v54,v60,v61\nv_cvt_pk_bf16_f32 v55,v62,v63\nM v[144:147],v[96:99],v[192:195],v[144:147]\n" \
"ds_read_b64_tr_b16 v[216:217],v227 offset:17408\nds_read_b64_tr_b16 v[218:219],v227 offset:21504\nds_read_b64_tr_b16 v[220:221],v227 offset:25600\ns_lshl4_add_u32 s69,s14,s68\nM v[144:147],v[112:115],v[208:211],v[144:147]\nbuffer_store_dwordx4 v[52:55],v240,s[52:55],s38 offen offset:3072 sc1\nds_read_b64_tr_b16 v[222:223],v227 offset:29696\ns_add_u32 m0,s64,65536\n" \
"v_lshl_add_u32 v229,v251,10,v250\nM v[148:151],v[100:103],v[192:195],v[148:151]\nds_read_b64_tr_b16 v[192:193],v226 offset:17920\nds_read_b64_tr_b16 v[194:195],v226 offset:22016\nv_lshlrev_b32 v236,1,v236\nv_lshl_or_b32 v235,v235,8,v236\nM v[148:151],v[116:119],v[208:211],v[148:151]\nds_read_b64_tr_b16 v[208:209],v226 offset:26112\n" \
"ds_read_b64_tr_b16 v[210:211],v226 offset:30208\nv_add_u32 v226,0,v242\ns_lshl_b32 s95,s95,10\nv_add_u32 v236,s95,v235\nM v[152:155],v[96:99],v[196:199],v[152:155]\nbuffer_load_dwordx4 a[8:11],v240,s[52:55],s65 offen offset:2048 sc1\ns_add_u32 s74,s74,1920\ns_waitcnt lgkmcnt(7)\n" \
"M v[152:155],v[112:115],v[212:215],v[152:155]\ns_lshl_b32 s74,s74,10\nv_add_u32 v237,s74,v235\ns_add_u32 s38,s33,16384\nv_lshlrev_b32 v238,1,v240\nv_add_u32 v238,83968,v238\nM v[156:159],v[100:103],v[196:199],v[156:159]\ns_barrier\n" \
"buffer_load_dwordx4 v243,s[40:43],s69 offen offset:0 lds\nds_read_b64_tr_b16 v[196:197],v227 offset:17920\nM v[156:159],v[116:119],v[212:215],v[156:159]\nds_read_b64_tr_b16 v[198:199],v227 offset:22016\nds_read_b32 v230,v229\nds_read_b64_tr_b16 v[212:213],v227 offset:26112\nv_add_u32 v239,83968,v239\nv_bfe_u32 v228,v249,2,2\n" \
"M v[160:163],v[96:99],v[200:203],v[160:163]\nds_read_b64_tr_b16 v[214:215],v227 offset:30208\nv_xor_b32 v227,32,v226\nv_lshrrev_b32 v229,4,v249\ns_mov_b32 s97,4096\nM v[160:163],v[112:115],v[204:207],v[160:163]\ns_add_u32 s95,s95,54525952\ns_add_u32 s74,s74,54525952\n" \
"M v[164:167],v[100:103],v[200:203],v[164:167]\nds_read_b64_tr_b16 v[200:201],v226 offset:0\nds_read_b64_tr_b16 v[202:203],v226 offset:4096\nM v[164:167],v[116:119],v[204:207],v[164:167]\nds_read_b64_tr_b16 v[204:205],v226 offset:8192\ns_waitcnt lgkmcnt(8)\nM v[168:171],v[96:99],v[216:219],v[168:171]\nbuffer_load_dwordx4 a[12:15],v240,s[52:55],s65 offen offset:3072 sc1\n" \
"M v[168:171],v[112:115],v[220:223],v[168:171]\nds_read_b64_tr_b16 v[206:207],v226 offset:12288\ns_add_u32 s65,s65,4096\nM v[172:175],v[100:103],v[216:219],v[172:175]\nbuffer_load_dwordx4 v244,s[40:43],s69 offen offset:1024 lds\nM v[172:175],v[116:119],v[220:223],v[172:175]\nds_read_b64_tr_b16 v[216:217],v227 offset:0\nds_read_b64_tr_b16 v[218:219],v227 offset:4096\n" \
"M v[176:179],v[96:99],v[192:195],v[176:179]\nds_read_b64_tr_b16 v[220:221],v227 offset:8192\nds_read_b64_tr_b16 v[222:223],v227 offset:12288\nM v[176:179],v[112:115],v[208:211],v[176:179]\ns_waitcnt lgkmcnt(8)\nv_cmp_ne_u32 vcc,0,v230\nM v[180:183],v[100:103],v[192:195],v[180:183]\ns_cbranch_vccnz .Lredo\n" \
"ds_read_b64_tr_b16 v[192:193],v226 offset:512\nM v[180:183],v[116:119],v[208:211],v[180:183]\nds_read_b64_tr_b16 v[194:195],v226 offset:4608\nds_read_b64_tr_b16 v[208:209],v226 offset:8704\nM v[184:187],v[96:99],v[196:199],v[184:187]\nbuffer_load_dwordx4 a[16:19],v240,s[52:55],s65 offen offset:0 sc1\nM v[184:187],v[112:115],v[212:215],v[184:187]\nds_read_b64_tr_b16 v[210:211],v226 offset:12800\n" \
"s_and_b32 s58,s3,16384\nM v[188:191],v[100:103],v[196:199],v[188:191]\nbuffer_load_dwordx4 v245,s[40:43],s69 offen offset:2048 lds\nM v[188:191],v[116:119],v[212:215],v[188:191]\nds_read_b64_tr_b16 v[196:197],v227 offset:512\ns_waitcnt vmcnt(27) lgkmcnt(6)\nM v[128:131],v[72:75],v[200:203],v[128:131]\nds_read_b64_tr_b16 v[198:199],v227 offset:4608\n" \
"s_waitcnt vmcnt(23)\nM v[128:131],v[88:91],v[204:207],v[128:131]\nds_read_b64_tr_b16 v[212:213],v227 offset:8704\nds_read_b64_tr_b16 v[214:215],v227 offset:12800\nM v[132:135],v[76:79],v[200:203],v[132:135]\nds_read_b64_tr_b16 v[200:201],v226 offset:1024\ns_waitcnt vmcnt(21)\nM v[132:135],v[92:95],v[204:207],v[132:135]\n" \
"ds_read_b64_tr_b16 v[202:203],v226 offset:5120\nds_read_b64_tr_b16 v[204:205],v226 offset:9216\nM v[136:139],v[72:75],v[216:219],v[136:139]\nbuffer_load_dwordx4 a[20:23],v240,s[52:55],s65 offen offset:1024 sc1\ns_waitcnt lgkmcnt(7)\nM v[136:139],v[88:91],v[220:223],v[136:139]\nds_read_b64_tr_b16 v[206:207],v226 offset:13312\nv_mov_b32 v250,82944\n" \
"M v[140:143],v[76:79],v[216:219],v[140:143]\nbuffer_load_dwordx4 v246,s[40:43],s69 offen offset:3072 lds\nM v[140:143],v[92:95],v[220:223],v[140:143]\nds_read_b64_tr_b16 v[216:217],v227 offset:1024\nds_read_b64_tr_b16 v[218:219],v227 offset:5120\nM v[144:147],v[72:75],v[192:195],v[144:147]\nds_read_b64_tr_b16 v[220:221],v227 offset:9216\nds_read_b64_tr_b16 v[222:223],v227 offset:13312\n" \
"M v[144:147],v[88:91],v[208:211],v[144:147]\ns_lshl4_add_u32 s69,s14,s68\ns_add_u32 s69,s69,0x10000\nM v[148:151],v[76:79],v[192:195],v[148:151]\nds_read_b64_tr_b16 v[192:193],v226 offset:1536\nds_read_b64_tr_b16 v[194:195],v226 offset:5632\nM v[148:151],v[92:95],v[208:211],v[148:151]\ns_add_u32 m0,s64,98304\n" \
"s_waitcnt lgkmcnt(7)\nM v[152:155],v[72:75],v[196:199],v[152:155]\nbuffer_load_dwordx4 a[24:27],v240,s[52:55],s65 offen offset:2048 sc1\nM v[152:155],v[88:91],v[212:215],v[152:155]\nds_read_b64_tr_b16 v[208:209],v226 offset:9728\nds_read_b64_tr_b16 v[210:211],v226 offset:13824\nM v[156:159],v[76:79],v[196:199],v[156:159]\nbuffer_load_dwordx4 v243,s[40:43],s69 offen offset:0 lds\n" \
"M v[156:159],v[92:95],v[212:215],v[156:159]\nds_read_b64_tr_b16 v[196:197],v227 offset:1536\nds_read_b64_tr_b16 v[198:199],v227 offset:5632\nM v[160:163],v[72:75],v[200:203],v[160:163]\nds_read_b64_tr_b16 v[212:213],v227 offset:9728\ns_waitcnt lgkmcnt(7)\nM v[160:163],v[88:91],v[204:207],v[160:163]\nds_read_b64_tr_b16 v[214:215],v227 offset:13824\n" \
"s_lshl_b32 s70,s2,9\nM v[164:167],v[76:79],v[200:203],v[164:167]\nds_read_b64_tr_b16 v[200:201],v226 offset:32768\nds_read_b64_tr_b16 v[202:203],v226 offset:36864\nM v[164:167],v[92:95],v[204:207],v[164:167]\nds_read_b64_tr_b16 v[204:205],v226 offset:40960\nds_read_b64_tr_b16 v[206:207],v226 offset:45056\nM v[168:171],v[72:75],v[216:219],v[168:171]\n" \
"buffer_load_dwordx4 a[28:31],v240,s[52:55],s65 offen offset:3072 sc1\nM v[168:171],v[88:91],v[220:223],v[168:171]\ns_add_u32 s65,s14,s67\ns_add_u32 s65,s65,1048576\nM v[172:175],v[76:79],v[216:219],v[172:175]\nbuffer_load_dwordx4 v244,s[40:43],s69 offen offset:1024 lds\nM v[172:175],v[92:95],v[220:223],v[172:175]\nds_read_b64_tr_b16 v[216:217],v227 offset:32768\n" \
"s_waitcnt lgkmcnt(6)\nM v[176:179],v[72:75],v[192:195],v[176:179]\nds_read_b64_tr_b16 v[218:219],v227 offset:36864\nds_read_b64_tr_b16 v[220:221],v227 offset:40960\nM v[176:179],v[88:91],v[208:211],v[176:179]\nds_read_b64_tr_b16 v[222:223],v227 offset:45056\ns_lshl_b32 s71,s7,8\nM v[180:183],v[76:79],v[192:195],v[180:183]\n" \
"ds_read_b64_tr_b16 v[192:193],v226 offset:33280\nds_read_b64_tr_b16 v[194:195],v226 offset:37376\nM v[180:183],v[92:95],v[208:211],v[180:183]\nds_read_b64_tr_b16 v[208:209],v226 offset:41472\nds_read_b64_tr_b16 v[210:211],v226 offset:45568\nM v[184:187],v[72:75],v[196:199],v[184:187]\nbuffer_load_dwordx4 a[32:35],v240,s[52:55],s65 offen offset:0 sc1\ns_waitcnt lgkmcnt(8)\n" \
"M v[184:187],v[88:91],v[212:215],v[184:187]\nds_write_b128 v250,a[244:247]\nM v[188:191],v[76:79],v[196:199],v[188:191]\nbuffer_load_dwordx4 v245,s[40:43],s69 offen offset:2048 lds\nM v[188:191],v[92:95],v[212:215],v[188:191]\nds_read_b64_tr_b16 v[196:197],v227 offset:33280\ns_waitcnt vmcnt(26)\nM v[128:131],v[104:107],v[200:203],v[128:131]\n" \
"ds_read_b64_tr_b16 v[198:199],v227 offset:37376\ns_waitcnt vmcnt(23)\nM v[128:131],v[120:123],v[204:207],v[128:131]\nds_read_b64_tr_b16 v[212:213],v227 offset:41472\nds_read_b64_tr_b16 v[214:215],v227 offset:45568\nM v[132:135],v[108:111],v[200:203],v[132:135]\nds_read_b64_tr_b16 v[200:201],v226 offset:33792\ns_waitcnt vmcnt(21)\n" \
"M v[132:135],v[124:127],v[204:207],v[132:135]\nds_read_b64_tr_b16 v[202:203],v226 offset:37888\ns_waitcnt lgkmcnt(7)\nM v[136:139],v[104:107],v[216:219],v[136:139]\nbuffer_load_dwordx4 a[36:39],v240,s[52:55],s65 offen offset:1024 sc1\nM v[136:139],v[120:123],v[220:223],v[136:139]\nds_read_b64_tr_b16 v[204:205],v226 offset:41984\nds_read_b64_tr_b16 v[206:207],v226 offset:46080\n" \
"M v[140:143],v[108:111],v[216:219],v[140:143]\nbuffer_load_dwordx4 v246,s[40:43],s69 offen offset:3072 lds\nM v[140:143],v[124:127],v[220:223],v[140:143]\nds_read_b64_tr_b16 v[216:217],v227 offset:33792\nds_read_b64_tr_b16 v[218:219],v227 offset:37888\nM v[144:147],v[104:107],v[192:195],v[144:147]\nds_read_b64_tr_b16 v[220:221],v227 offset:41984\nds_read_b64_tr_b16 v[222:223],v227 offset:46080\n" \
"M v[144:147],v[120:123],v[208:211],v[144:147]\nv_lshl_add_u32 v241,v249,2,s71\ns_add_u32 s70,s70,4096\nM v[148:151],v[108:111],v[192:195],v[148:151]\nds_read_b64_tr_b16 v[192:193],v226 offset:34304\nds_read_b64_tr_b16 v[194:195],v226 offset:38400\nM v[148:151],v[124:127],v[208:211],v[148:151]\nds_read_b64_tr_b16 v[208:209],v226 offset:42496\n" \
"s_waitcnt lgkmcnt(9)\nM v[152:155],v[104:107],v[196:199],v[152:155]\nbuffer_load_dwordx4 a[40:43],v240,s[52:55],s65 offen offset:2048 sc1\nM v[152:155],v[120:123],v[212:215],v[152:155]\nds_read_b64_tr_b16 v[210:211],v226 offset:46592\ns_add_u32 m0,s64,83968\nM v[156:159],v[108:111],v[196:199],v[156:159]\ns_waitcnt vmcnt(16)\n" \
"buffer_store_dword v252,off,s[52:55],s76 sc1\ns_barrier\nM v[156:159],v[124:127],v[212:215],v[156:159]\nds_read_b64_tr_b16 v[196:197],v227 offset:34304\nds_read_b64_tr_b16 v[198:199],v227 offset:38400\nM v[160:163],v[104:107],v[200:203],v[160:163]\nbuffer_load_dwordx4 v240,s[52:55],s72 offen offset:0 sc1 lds\ns_waitcnt lgkmcnt(6)\n" \
"M v[160:163],v[120:123],v[204:207],v[160:163]\nds_read_b64_tr_b16 v[212:213],v227 offset:42496\nds_read_b64_tr_b16 v[214:215],v227 offset:46592\nM v[164:167],v[108:111],v[200:203],v[164:167]\nds_read_b64_tr_b16 v[200:201],v226 offset:16384\nds_read_b64_tr_b16 v[202:203],v226 offset:20480\nM v[164:167],v[124:127],v[204:207],v[164:167]\nds_read_b64_tr_b16 v[204:205],v226 offset:24576\n" \
"ds_read_b64_tr_b16 v[206:207],v226 offset:28672\nM v[168:171],v[104:107],v[216:219],v[168:171]\nbuffer_load_dwordx4 v240,s[52:55],s72 offen offset:1024 sc1 lds\ns_lshl_b32 s72,s2,9\ns_add_u32 s56,s26,s72\ns_addc_u32 s57,s27,0\ns_add_u32 m0,s64,114688\nbuffer_load_dwordx4 a[44:47],v240,s[52:55],s65 offen offset:3072 sc1\n" \
"M v[168:171],v[120:123],v[220:223],v[168:171]\ns_add_u32 s65,s65,4096\nv_add_u32 v230,s70,v241\nM v[172:175],v[108:111],v[216:219],v[172:175]\nds_read_b64_tr_b16 v[216:217],v227 offset:16384\nds_read_b64_tr_b16 v[218:219],v227 offset:20480\nM v[172:175],v[124:127],v[220:223],v[172:175]\nds_read_b64_tr_b16 v[220:221],v227 offset:24576\n" \
"s_waitcnt lgkmcnt(7)\nM v[176:179],v[104:107],v[192:195],v[176:179]\nbuffer_load_dwordx4 v240,s[56:59],s64 offen offset:0 lds\nM v[176:179],v[120:123],v[208:211],v[176:179]\nds_read_b64_tr_b16 v[222:223],v227 offset:28672\nv_add_u32 v231,8192,v230\nM v[180:183],v[108:111],v[192:195],v[180:183]\nds_read_b64_tr_b16 v[192:193],v226 offset:16896\n" \
"ds_read_b64_tr_b16 v[194:195],v226 offset:20992\nM v[180:183],v[124:127],v[208:211],v[180:183]\nds_read_b64_tr_b16 v[208:209],v226 offset:25088\nds_read_b64_tr_b16 v[210:211],v226 offset:29184\nM v[184:187],v[104:107],v[196:199],v[184:187]\nbuffer_load_dwordx4 v240,s[56:59],s64 offen offset:1024 lds\nbuffer_load_dwordx4 a[48:51],v240,s[52:55],s65 offen offset:0 sc1\nM v[184:187],v[120:123],v[212:215],v[184:187]\n" \
"v_accvgpr_write_b32 a194,0\nv_accvgpr_write_b32 a195,0\nM v[188:191],v[108:111],v[196:199],v[188:191]\nds_read_b64_tr_b16 v[196:197],v227 offset:16896\nds_read_b64_tr_b16 v[198:199],v227 offset:20992\nM v[188:191],v[124:127],v[212:215],v[188:191]\nds_read_b64_tr_b16 v[212:213],v227 offset:25088\ns_waitcnt lgkmcnt(8)\n" \
"M v[128:131],a[0:3],v[200:203],v[128:131]\nbuffer_load_dwordx4 v240,s[56:59],s64 offen offset:2048 lds\nM v[128:131],a[8:11],v[204:207],v[128:131]\nds_read_b64_tr_b16 v[214:215],v227 offset:29184\nv_accvgpr_write_b32 a196,0\nM v[132:135],a[4:7],v[200:203],v[132:135]\nds_read_b64_tr_b16 v[200:201],v226 offset:17408\ns_waitcnt vmcnt(22)\n" \
"M v[132:135],a[12:15],v[204:207],v[132:135]\nds_read_b64_tr_b16 v[202:203],v226 offset:21504\nds_read_b64_tr_b16 v[204:205],v226 offset:25600\nM v[136:139],a[0:3],v[216:219],v[136:139]\nbuffer_load_dwordx4 v240,s[56:59],s64 offen offset:3072 lds\nbuffer_load_dwordx4 a[52:55],v240,s[52:55],s65 offen offset:1024 sc1\ns_waitcnt lgkmcnt(7)\nM v[136:139],a[8:11],v[220:223],v[136:139]\n" \
"ds_read_b64_tr_b16 v[206:207],v226 offset:29696\nv_accvgpr_write_b32 a197,0\nM v[140:143],a[4:7],v[216:219],v[140:143]\nds_read_b64_tr_b16 v[216:217],v227 offset:17408\nds_read_b64_tr_b16 v[218:219],v227 offset:21504\nM v[140:143],a[12:15],v[220:223],v[140:143]\nds_read_b64_tr_b16 v[220:221],v227 offset:25600\nds_read_b64_tr_b16 v[222:223],v227 offset:29696\n" \
"M v[144:147],a[0:3],v[192:195],v[144:147]\ns_load_dwordx2 s[44:45],s[4:5],0x68\ns_mov_b32 s46,2097152\nM v[144:147],a[8:11],v[208:211],v[144:147]\nM v[148:151],a[4:7],v[192:195],v[148:151]\nds_read_b64_tr_b16 v[192:193],v226 offset:17920\nds_read_b64_tr_b16 v[194:195],v226 offset:22016\nM v[148:151],a[12:15],v[208:211],v[148:151]\n" \
"ds_read_b64_tr_b16 v[208:209],v226 offset:26112\ns_waitcnt lgkmcnt(8)\nM v[152:155],a[0:3],v[196:199],v[152:155]\nbuffer_load_dwordx4 a[56:59],v240,s[52:55],s65 offen offset:2048 sc1\nM v[152:155],a[8:11],v[212:215],v[152:155]\nds_read_b64_tr_b16 v[210:211],v226 offset:30208\nM v[156:159],a[4:7],v[196:199],v[156:159]\nds_read_b64_tr_b16 v[196:197],v227 offset:17920\n" \
"ds_read_b64_tr_b16 v[198:199],v227 offset:22016\nM v[156:159],a[12:15],v[212:215],v[156:159]\nds_read_b64_tr_b16 v[212:213],v227 offset:26112\nds_read_b64_tr_b16 v[214:215],v227 offset:30208\nM v[160:163],a[0:3],v[200:203],v[160:163]\ns_waitcnt lgkmcnt(8)\nM v[160:163],a[8:11],v[204:207],v[160:163]\nM v[164:167],a[4:7],v[200:203],v[164:167]\n" \
"ds_read_b64_tr_b16 v[200:201],v226 offset:49152\nds_read_b64_tr_b16 v[202:203],v226 offset:53248\nM v[164:167],a[12:15],v[204:207],v[164:167]\nds_read_b64_tr_b16 v[204:205],v226 offset:57344\nds_read_b64_tr_b16 v[206:207],v226 offset:61440\nM v[168:171],a[0:3],v[216:219],v[168:171]\nbuffer_load_dwordx4 a[60:63],v240,s[52:55],s65 offen offset:3072 sc1\nM v[168:171],a[8:11],v[220:223],v[168:171]\n" \
"s_add_u32 s65,s10,s67\nM v[172:175],a[4:7],v[216:219],v[172:175]\nds_read_b64_tr_b16 v[216:217],v227 offset:49152\nds_read_b64_tr_b16 v[218:219],v227 offset:53248\nM v[172:175],a[12:15],v[220:223],v[172:175]\nds_read_b64_tr_b16 v[220:221],v227 offset:57344\ns_waitcnt lgkmcnt(8)\nM v[176:179],a[0:3],v[192:195],v[176:179]\n" \
"ds_read_b64_tr_b16 v[222:223],v227 offset:61440\nM v[176:179],a[8:11],v[208:211],v[176:179]\nM v[180:183],a[4:7],v[192:195],v[180:183]\nds_read_b64_tr_b16 v[192:193],v226 offset:49664\nds_read_b64_tr_b16 v[194:195],v226 offset:53760\nM v[180:183],a[12:15],v[208:211],v[180:183]\nds_read_b64_tr_b16 v[208:209],v226 offset:57856\nds_read_b64_tr_b16 v[210:211],v226 offset:61952\n" \
"M v[184:187],a[0:3],v[196:199],v[184:187]\ns_waitcnt lgkmcnt(8)\nM v[184:187],a[8:11],v[212:215],v[184:187]\nM v[188:191],a[4:7],v[196:199],v[188:191]\nds_read_b64_tr_b16 v[196:197],v227 offset:49664\nds_read_b64_tr_b16 v[198:199],v227 offset:53760\nM v[188:191],a[12:15],v[212:215],v[188:191]\nds_read_b64_tr_b16 v[212:213],v227 offset:57856\n" \
"s_waitcnt vmcnt(24)\nM v[128:131],a[16:19],v[200:203],v[128:131]\nds_read_b64_tr_b16 v[214:215],v227 offset:61952\ns_waitcnt vmcnt(20)\nM v[128:131],a[24:27],v[204:207],v[128:131]\nM v[132:135],a[20:23],v[200:203],v[132:135]\nds_read_b64_tr_b16 v[200:201],v226 offset:50176\ns_waitcnt vmcnt(18)\n" \
"M v[132:135],a[28:31],v[204:207],v[132:135]\nds_read_b64_tr_b16 v[202:203],v226 offset:54272\ns_waitcnt lgkmcnt(8)\nM v[136:139],a[16:19],v[216:219],v[136:139]\nds_read_b64_tr_b16 v[204:205],v226 offset:58368\nds_read_b64_tr_b16 v[206:207],v226 offset:62464\nM v[136:139],a[24:27],v[220:223],v[136:139]\nM v[140:143],a[20:23],v[216:219],v[140:143]\n" \
"ds_read_b64_tr_b16 v[216:217],v227 offset:50176\nds_read_b64_tr_b16 v[218:219],v227 offset:54272\nM v[140:143],a[28:31],v[220:223],v[140:143]\nds_read_b64_tr_b16 v[220:221],v227 offset:58368\nds_read_b64_tr_b16 v[222:223],v227 offset:62464\nM v[144:147],a[16:19],v[192:195],v[144:147]\nbuffer_load_dword v248,v254,s[52:55],s80 offen sc1\ns_waitcnt lgkmcnt(8)\n" \
"M v[144:147],a[24:27],v[208:211],v[144:147]\nM v[148:151],a[20:23],v[192:195],v[148:151]\nglobal_load_ushort v64,v236,s[8:9] offset:0\nM v[148:151],a[28:31],v[208:211],v[148:151]\nds_read_b64_tr_b16 v[192:193],v226 offset:50688\nds_read_b64_tr_b16 v[194:195],v226 offset:54784\nM v[152:155],a[16:19],v[196:199],v[152:155]\nglobal_load_ushort v80,v237,s[8:9] offset:0\n" \
"M v[152:155],a[24:27],v[212:215],v[152:155]\nds_read_b64_tr_b16 v[208:209],v226 offset:58880\nds_read_b64_tr_b16 v[210:211],v226 offset:62976\nM v[156:159],a[20:23],v[196:199],v[156:159]\ns_waitcnt vmcnt(6)\ns_barrier\nM v[156:159],a[28:31],v[212:215],v[156:159]\nglobal_load_ushort v96,v235,s[20:21] offset:0\n" \
"s_waitcnt lgkmcnt(4)\nM v[160:163],a[16:19],v[200:203],v[160:163]\nds_read_b64_tr_b16 v[196:197],v227 offset:50688\nds_read_b64_tr_b16 v[198:199],v227 offset:54784\nM v[160:163],a[24:27],v[204:207],v[160:163]\nglobal_load_ushort v65,v236,s[8:9] offset:32\nM v[164:167],a[20:23],v[200:203],v[164:167]\nds_read_b64_tr_b16 v[212:213],v227 offset:58880\n" \
"ds_read_b64_tr_b16 v[214:215],v227 offset:62976\nM v[164:167],a[28:31],v[204:207],v[164:167]\nglobal_load_ushort v81,v237,s[8:9] offset:32\nM v[168:171],a[16:19],v[216:219],v[168:171]\nv_add_u32 v226,65536,v242\nds_read_b64_tr_b16 v[200:201],v226 offset:0\nM v[168:171],a[24:27],v[220:223],v[168:171]\nds_read_b64_tr_b16 v[202:203],v226 offset:4096\n" \
"global_load_ushort v97,v235,s[20:21] offset:32\nM v[172:175],a[20:23],v[216:219],v[172:175]\nds_read_b64_tr_b16 v[204:205],v226 offset:8192\nds_read_b64_tr_b16 v[206:207],v226 offset:12288\nM v[172:175],a[28:31],v[220:223],v[172:175]\nglobal_load_ushort v66,v236,s[8:9] offset:64\ns_waitcnt lgkmcnt(6)\nM v[176:179],a[16:19],v[192:195],v[176:179]\n" \
"v_xor_b32 v227,32,v226\nds_read_b64_tr_b16 v[216:217],v227 offset:0\nM v[176:179],a[24:27],v[208:211],v[176:179]\nds_read_b64_tr_b16 v[218:219],v227 offset:4096\nglobal_load_ushort v82,v237,s[8:9] offset:64\nM v[180:183],a[20:23],v[192:195],v[180:183]\nds_read_b64_tr_b16 v[220:221],v227 offset:8192\nds_read_b64_tr_b16 v[222:223],v227 offset:12288\n" \
"M v[180:183],a[28:31],v[208:211],v[180:183]\nglobal_load_ushort v98,v235,s[20:21] offset:64\nM v[184:187],a[16:19],v[196:199],v[184:187]\nds_read_b64_tr_b16 v[192:193],v226 offset:512\ns_waitcnt lgkmcnt(7)\nM v[184:187],a[24:27],v[212:215],v[184:187]\nds_read_b64_tr_b16 v[194:195],v226 offset:4608\nglobal_load_ushort v67,v236,s[8:9] offset:96\n" \
"M v[188:191],a[20:23],v[196:199],v[188:191]\nds_read_b64_tr_b16 v[208:209],v226 offset:8704\nds_read_b64_tr_b16 v[210:211],v226 offset:12800\nM v[188:191],a[28:31],v[212:215],v[188:191]\nglobal_load_ushort v83,v237,s[8:9] offset:96\nM v[128:131],a[32:35],v[200:203],v[128:131]\nds_read_b64_tr_b16 v[196:197],v227 offset:512\ns_waitcnt lgkmcnt(7)\n" \
"M v[128:131],a[40:43],v[204:207],v[128:131]\nds_read_b64_tr_b16 v[198:199],v227 offset:4608\nglobal_load_ushort v99,v235,s[20:21] offset:96\nM v[132:135],a[36:39],v[200:203],v[132:135]\nds_read_b64_tr_b16 v[212:213],v227 offset:8704\nds_read_b64_tr_b16 v[214:215],v227 offset:12800\nM v[132:135],a[44:47],v[204:207],v[132:135]\nglobal_load_ushort v68,v236,s[8:9] offset:128\n" \
"M v[136:139],a[32:35],v[216:219],v[136:139]\nds_read_b64_tr_b16 v[200:201],v226 offset:1024\ns_waitcnt lgkmcnt(7)\nM v[136:139],a[40:43],v[220:223],v[136:139]\nds_read_b64_tr_b16 v[202:203],v226 offset:5120\nglobal_load_ushort v84,v237,s[8:9] offset:128\nM v[140:143],a[36:39],v[216:219],v[140:143]\nds_read_b64_tr_b16 v[204:205],v226 offset:9216\n" \
"ds_read_b64_tr_b16 v[206:207],v226 offset:13312\nM v[140:143],a[44:47],v[220:223],v[140:143]\nglobal_load_ushort v100,v235,s[20:21] offset:128\nM v[144:147],a[32:35],v[192:195],v[144:147]\nds_read_b64_tr_b16 v[216:217],v227 offset:1024\ns_waitcnt lgkmcnt(7)\nM v[144:147],a[40:43],v[208:211],v[144:147]\nds_read_b64_tr_b16 v[218:219],v227 offset:5120\n" \
"global_load_ushort v69,v236,s[8:9] offset:160\nM v[148:151],a[36:39],v[192:195],v[148:151]\nds_read_b64_tr_b16 v[220:221],v227 offset:9216\nds_read_b64_tr_b16 v[222:223],v227 offset:13312\nM v[148:151],a[44:47],v[208:211],v[148:151]\nglobal_load_ushort v85,v237,s[8:9] offset:160\nM v[152:155],a[32:35],v[196:199],v[152:155]\nds_read_b64_tr_b16 v[192:193],v226 offset:1536\n" \
"s_waitcnt lgkmcnt(7)\nM v[152:155],a[40:43],v[212:215],v[152:155]\nds_read_b64_tr_b16 v[194:195],v226 offset:5632\nglobal_load_ushort v101,v235,s[20:21] offset:160\nM v[156:159],a[36:39],v[196:199],v[156:159]\nds_read_b64_tr_b16 v[208:209],v226 offset:9728\nds_read_b64_tr_b16 v[210:211],v226 offset:13824\nM v[156:159],a[44:47],v[212:215],v[156:159]\n" \
"global_load_ushort v70,v236,s[8:9] offset:192\nM v[160:163],a[32:35],v[200:203],v[160:163]\nds_read_b64_tr_b16 v[196:197],v227 offset:1536\ns_waitcnt lgkmcnt(7)\nM v[160:163],a[40:43],v[204:207],v[160:163]\nds_read_b64_tr_b16 v[198:199],v227 offset:5632\nglobal_load_ushort v86,v237,s[8:9] offset:192\nM v[164:167],a[36:39],v[200:203],v[164:167]\n" \
"ds_read_b64_tr_b16 v[212:213],v227 offset:9728\nds_read_b64_tr_b16 v[214:215],v227 offset:13824\nM v[164:167],a[44:47],v[204:207],v[164:167]\ns_waitcnt vmcnt(20)\nv_cmp_eq_u32 vcc,0,v248\nglobal_load_ushort v102,v235,s[20:21] offset:192\nM v[168:171],a[32:35],v[216:219],v[168:171]\ns_cbranch_vccnz .Lps7\n" \
".Lpb7:\nbuffer_load_dword v248,v251,s[52:55],s79 offen sc1\ns_waitcnt lgkmcnt(4)\nM v[168:171],a[40:43],v[220:223],v[168:171]\nds_read_b64_tr_b16 v[200:201],v226 offset:32768\nds_read_b64_tr_b16 v[202:203],v226 offset:36864\nglobal_load_ushort v71,v236,s[8:9] offset:224\nM v[172:175],a[36:39],v[216:219],v[172:175]\n" \
"ds_read_b64_tr_b16 v[204:205],v226 offset:40960\nds_read_b64_tr_b16 v[206:207],v226 offset:45056\nM v[172:175],a[44:47],v[220:223],v[172:175]\nglobal_load_ushort v87,v237,s[8:9] offset:224\nM v[176:179],a[32:35],v[192:195],v[176:179]\nds_read_b64_tr_b16 v[216:217],v227 offset:32768\nds_read_b64_tr_b16 v[218:219],v227 offset:36864\nM v[176:179],a[40:43],v[208:211],v[176:179]\n" \
"global_load_ushort v103,v235,s[20:21] offset:224\nM v[180:183],a[36:39],v[192:195],v[180:183]\nds_read_b64_tr_b16 v[220:221],v227 offset:40960\nds_read_b64_tr_b16 v[222:223],v227 offset:45056\nM v[180:183],a[44:47],v[208:211],v[180:183]\nds_read_b64_tr_b16 v[192:193],v226 offset:33280\ns_waitcnt lgkmcnt(9)\nM v[184:187],a[32:35],v[196:199],v[184:187]\n" \
"ds_read_b64_tr_b16 v[194:195],v226 offset:37376\nds_read_b64_tr_b16 v[208:209],v226 offset:41472\nM v[184:187],a[40:43],v[212:215],v[184:187]\nds_read_b64_tr_b16 v[210:211],v226 offset:45568\nds_read_b64 v[112:113],v239 offset:0\nM v[188:191],a[36:39],v[196:199],v[188:191]\nds_read_b64_tr_b16 v[196:197],v227 offset:33280\nds_read_b64_tr_b16 v[198:199],v227 offset:37376\n" \
"M v[188:191],a[44:47],v[212:215],v[188:191]\nds_read_b64_tr_b16 v[212:213],v227 offset:41472\ns_waitcnt lgkmcnt(11)\nM v[128:131],a[48:51],v[200:203],v[128:131]\nds_read_b64_tr_b16 v[214:215],v227 offset:45568\nds_read_b64 v[114:115],v239 offset:4096\nM v[128:131],a[56:59],v[204:207],v[128:131]\nds_read_b64 v[116:117],v239 offset:8192\n" \
"ds_read_b64 v[118:119],v239 offset:12288\nM v[132:135],a[52:55],v[200:203],v[132:135]\nds_read_b64_tr_b16 v[200:201],v226 offset:33792\nds_read_b64_tr_b16 v[202:203],v226 offset:37888\nM v[132:135],a[60:63],v[204:207],v[132:135]\nds_read_b64_tr_b16 v[204:205],v226 offset:41984\ns_waitcnt lgkmcnt(13)\nM v[136:139],a[48:51],v[216:219],v[136:139]\n" \
"ds_read_b64_tr_b16 v[206:207],v226 offset:46080\nM v[136:139],a[56:59],v[220:223],v[136:139]\nM v[140:143],a[52:55],v[216:219],v[140:143]\nds_read_b64_tr_b16 v[216:217],v227 offset:33792\nds_read_b64_tr_b16 v[218:219],v227 offset:37888\nM v[140:143],a[60:63],v[220:223],v[140:143]\nds_read_b64_tr_b16 v[220:221],v227 offset:41984\nds_read_b64_tr_b16 v[222:223],v227 offset:46080\n" \
"M v[144:147],a[48:51],v[192:195],v[144:147]\ns_waitcnt lgkmcnt(11)\nM v[144:147],a[56:59],v[208:211],v[144:147]\nv_cvt_pk_bf16_f32 v128,v128,v136\nv_cvt_pk_bf16_f32 v129,v129,v137\nM v[148:151],a[52:55],v[192:195],v[148:151]\nds_read_b64_tr_b16 v[192:193],v226 offset:34304\nds_read_b64_tr_b16 v[194:195],v226 offset:38400\n" \
"M v[148:151],a[60:63],v[208:211],v[148:151]\nds_read_b64_tr_b16 v[208:209],v226 offset:42496\nds_read_b64_tr_b16 v[210:211],v226 offset:46592\nM v[152:155],a[48:51],v[196:199],v[152:155]\nv_add_u32 v226,131072,v242\nv_cvt_pk_bf16_f32 v130,v130,v138\nM v[152:155],a[56:59],v[212:215],v[152:155]\nv_cvt_pk_bf16_f32 v131,v131,v139\n" \
"v_min_u32 v136,-2,v128\nM v[156:159],a[52:55],v[196:199],v[156:159]\nds_read_b64_tr_b16 v[196:197],v227 offset:34304\nds_read_b64_tr_b16 v[198:199],v227 offset:38400\nM v[156:159],a[60:63],v[212:215],v[156:159]\nds_read_b64_tr_b16 v[212:213],v227 offset:42496\ns_waitcnt lgkmcnt(7)\nM v[160:163],a[48:51],v[200:203],v[160:163]\n" \
"ds_read_b64_tr_b16 v[214:215],v227 offset:46592\nv_xor_b32 v227,32,v226\nM v[160:163],a[56:59],v[204:207],v[160:163]\nv_cvt_pk_bf16_f32 v144,v144,v152\nv_min_u32 v137,-2,v144\nM v[164:167],a[52:55],v[200:203],v[164:167]\nds_read_b64_tr_b16 v[200:201],v226 offset:0\nds_read_b64_tr_b16 v[202:203],v226 offset:4096\n" \
"M v[164:167],a[60:63],v[204:207],v[164:167]\nds_read_b64_tr_b16 v[204:205],v226 offset:8192\nds_read_b64_tr_b16 v[206:207],v226 offset:12288\nM v[168:171],a[48:51],v[216:219],v[168:171]\nv_cvt_pk_bf16_f32 v145,v145,v153\nv_cvt_pk_bf16_f32 v146,v146,v154\nM v[168:171],a[56:59],v[220:223],v[168:171]\nv_cvt_pk_bf16_f32 v147,v147,v155\n" \
"v_min_u32 v152,-2,v129\nM v[172:175],a[52:55],v[216:219],v[172:175]\ns_waitcnt vmcnt(3)\nv_cmp_eq_u32 vcc,0,v248\nM v[172:175],a[60:63],v[220:223],v[172:175]\ns_cbranch_vccnz .Lps8\n.Lpb8:\ns_waitcnt lgkmcnt(4)\n" \
"M v[176:179],a[48:51],v[192:195],v[176:179]\nbuffer_load_dwordx4 a[64:67],v240,s[52:55],s65 offen offset:0 sc1\nM v[176:179],a[56:59],v[208:211],v[176:179]\nds_read_b64_tr_b16 v[216:217],v227 offset:0\nds_read_b64_tr_b16 v[218:219],v227 offset:4096\nM v[180:183],a[52:55],v[192:195],v[180:183]\nds_read_b64_tr_b16 v[220:221],v227 offset:8192\nds_read_b64_tr_b16 v[222:223],v227 offset:12288\n" \
"M v[180:183],a[60:63],v[208:211],v[180:183]\nds_read_b64_tr_b16 v[192:193],v226 offset:512\nds_read_b64_tr_b16 v[194:195],v226 offset:4608\nM v[184:187],a[48:51],v[196:199],v[184:187]\nbuffer_load_dwordx4 a[68:71],v240,s[52:55],s65 offen offset:1024 sc1\nM v[184:187],a[56:59],v[212:215],v[184:187]\nds_read_b64_tr_b16 v[208:209],v226 offset:8704\nds_read_b64_tr_b16 v[210:211],v226 offset:12800\n" \
"M v[188:191],a[52:55],v[196:199],v[188:191]\nds_read_b64_tr_b16 v[196:197],v227 offset:512\nds_read_b64_tr_b16 v[198:199],v227 offset:4608\nM v[188:191],a[60:63],v[212:215],v[188:191]\nds_read_b64_tr_b16 v[212:213],v227 offset:8704\ns_waitcnt lgkmcnt(11)\nM v[8:11],v[0:3],v[200:203],0\nbuffer_load_dwordx4 a[72:75],v240,s[52:55],s65 offen offset:2048 sc1\n" \
"M v[8:11],v[16:19],v[204:207],v[8:11]\nds_read_b64_tr_b16 v[214:215],v227 offset:12800\nv_cvt_pk_bf16_f32 v160,v160,v168\nM v[12:15],v[4:7],v[200:203],0\nds_read_b64_tr_b16 v[200:201],v226 offset:1024\nds_read_b64_tr_b16 v[202:203],v226 offset:5120\nM v[12:15],v[20:23],v[204:207],v[12:15]\nds_read_b64_tr_b16 v[204:205],v226 offset:9216\n" \
"s_waitcnt lgkmcnt(9)\nM v[24:27],v[0:3],v[216:219],0\nbuffer_load_dwordx4 a[76:79],v240,s[52:55],s65 offen offset:3072 sc1\nM v[24:27],v[16:19],v[220:223],v[24:27]\nds_read_b64_tr_b16 v[206:207],v226 offset:13312\ns_add_u32 s65,s65,4096\nM v[28:31],v[4:7],v[216:219],0\nds_read_b64_tr_b16 v[216:217],v227 offset:1024\n" \
"ds_read_b64_tr_b16 v[218:219],v227 offset:5120\nM v[28:31],v[20:23],v[220:223],v[28:31]\nds_read_b64_tr_b16 v[220:221],v227 offset:9216\nds_read_b64_tr_b16 v[222:223],v227 offset:13312\nM v[40:43],v[0:3],v[192:195],0\nbuffer_load_dwordx4 a[80:83],v240,s[52:55],s65 offen offset:0 sc1\ns_waitcnt lgkmcnt(8)\nM v[40:43],v[16:19],v[208:211],v[40:43]\n" \
"v_cvt_pk_bf16_f32 v176,v176,v184\nv_min_u32 v138,-2,v160\nM v[44:47],v[4:7],v[192:195],0\nds_read_b64_tr_b16 v[192:193],v226 offset:1536\nds_read_b64_tr_b16 v[194:195],v226 offset:5632\nM v[44:47],v[20:23],v[208:211],v[44:47]\nv_min_u32 v139,-2,v176\nds_read_b64_tr_b16 v[208:209],v226 offset:9728\n" \
"M v[56:59],v[0:3],v[196:199],0\nbuffer_load_dwordx4 a[84:87],v240,s[52:55],s65 offen offset:1024 sc1\nM v[56:59],v[16:19],v[212:215],v[56:59]\nds_read_b64_tr_b16 v[210:211],v226 offset:13824\nv_cvt_pk_bf16_f32 v161,v161,v169\nM v[60:63],v[4:7],v[196:199],0\nbuffer_store_dwordx4 v[136:139],v253,s[52:55],s33 offen offset:0 sc1\nM v[60:63],v[20:23],v[212:215],v[60:63]\n" \
"ds_read_b64_tr_b16 v[196:197],v227 offset:1536\ns_waitcnt lgkmcnt(5)\nM v[72:75],v[0:3],v[200:203],0\nbuffer_load_dwordx4 a[88:91],v240,s[52:55],s65 offen offset:2048 sc1\nM v[72:75],v[16:19],v[204:207],v[72:75]\nds_read_b64_tr_b16 v[198:199],v227 offset:5632\nds_read_b64_tr_b16 v[212:213],v227 offset:9728\nM v[76:79],v[4:7],v[200:203],0\n" \
"ds_read_b64_tr_b16 v[214:215],v227 offset:13824\nds_read_b64_tr_b16 v[200:201],v226 offset:16384\nM v[76:79],v[20:23],v[204:207],v[76:79]\nds_read_b64_tr_b16 v[202:203],v226 offset:20480\nds_read_b64_tr_b16 v[204:205],v226 offset:24576\nM v[88:91],v[0:3],v[216:219],0\nbuffer_load_dwordx4 a[92:95],v240,s[52:55],s65 offen offset:3072 sc1\nM v[88:91],v[16:19],v[220:223],v[88:91]\n" \
"ds_read_b64_tr_b16 v[206:207],v226 offset:28672\ns_add_u32 s65,s11,s67\nM v[92:95],v[4:7],v[216:219],0\nds_read_b64_tr_b16 v[216:217],v227 offset:16384\nds_read_b64_tr_b16 v[218:219],v227 offset:20480\nM v[92:95],v[20:23],v[220:223],v[92:95]\nds_read_b64_tr_b16 v[220:221],v227 offset:24576\ns_waitcnt lgkmcnt(10)\n" \
"M v[104:107],v[0:3],v[192:195],0\nbuffer_load_dwordx4 a[96:99],v240,s[52:55],s65 offen offset:0 sc1\nM v[104:107],v[16:19],v[208:211],v[104:107]\nds_read_b64_tr_b16 v[222:223],v227 offset:28672\nv_cvt_pk_bf16_f32 v177,v177,v185\nM v[108:111],v[4:7],v[192:195],0\nds_read_b64_tr_b16 v[192:193],v226 offset:16896\nds_read_b64_tr_b16 v[194:195],v226 offset:20992\n" \
"M v[108:111],v[20:23],v[208:211],v[108:111]\nv_min_u32 v153,-2,v145\ns_waitcnt lgkmcnt(7)\nM v[120:123],v[0:3],v[196:199],0\nbuffer_load_dwordx4 a[100:103],v240,s[52:55],s65 offen offset:1024 sc1\nM v[120:123],v[16:19],v[212:215],v[120:123]\nv_min_u32 v154,-2,v161\nv_min_u32 v155,-2,v177\n" \
"M v[124:127],v[4:7],v[196:199],0\nds_read_b64_tr_b16 v[208:209],v226 offset:25088\nds_read_b64_tr_b16 v[210:211],v226 offset:29184\nbuffer_store_dwordx4 v[152:155],v253,s[52:55],s33 offen offset:1024 sc1\nM v[124:127],v[20:23],v[212:215],v[124:127]\nds_read_b64_tr_b16 v[196:197],v227 offset:16896\nds_read_b64_tr_b16 v[198:199],v227 offset:20992\nM v[8:11],v[32:35],v[200:203],v[8:11]\n" \
"buffer_load_dwordx4 a[104:107],v240,s[52:55],s65 offen offset:2048 sc1\ns_waitcnt lgkmcnt(6)\nM v[8:11],v[48:51],v[204:207],v[8:11]\nds_read_b64_tr_b16 v[212:213],v227 offset:25088\nds_read_b64_tr_b16 v[214:215],v227 offset:29184\nM v[12:15],v[36:39],v[200:203],v[12:15]\nds_read_b64_tr_b16 v[200:201],v226 offset:17408\nds_read_b64_tr_b16 v[202:203],v226 offset:21504\n" \
"M v[12:15],v[52:55],v[204:207],v[12:15]\nds_read_b64_tr_b16 v[204:205],v226 offset:25600\nds_read_b64_tr_b16 v[206:207],v226 offset:29696\nM v[24:27],v[32:35],v[216:219],v[24:27]\nbuffer_load_dwordx4 a[108:111],v240,s[52:55],s65 offen offset:3072 sc1\nM v[24:27],v[48:51],v[220:223],v[24:27]\ns_add_u32 s65,s65,4096\nv_cvt_pk_bf16_f32 v162,v162,v170\n" \
"M v[28:31],v[36:39],v[216:219],v[28:31]\nds_read_b64_tr_b16 v[216:217],v227 offset:17408\nds_read_b64_tr_b16 v[218:219],v227 offset:21504\nM v[28:31],v[52:55],v[220:223],v[28:31]\nds_read_b64_tr_b16 v[220:221],v227 offset:25600\ns_waitcnt lgkmcnt(9)\nM v[40:43],v[32:35],v[192:195],v[40:43]\nbuffer_load_dwordx4 a[112:115],v240,s[52:55],s65 offen offset:0 sc1\n" \
"M v[40:43],v[48:51],v[208:211],v[40:43]\nds_read_b64_tr_b16 v[222:223],v227 offset:29696\nv_cvt_pk_bf16_f32 v163,v163,v171\nM v[44:47],v[36:39],v[192:195],v[44:47]\nds_read_b64_tr_b16 v[192:193],v226 offset:17920\nds_read_b64_tr_b16 v[194:195],v226 offset:22016\nM v[44:47],v[52:55],v[208:211],v[44:47]\nv_cvt_pk_bf16_f32 v178,v178,v186\n" \
"v_min_u32 v168,-2,v130\nM v[56:59],v[32:35],v[196:199],v[56:59]\nbuffer_load_dwordx4 a[116:119],v240,s[52:55],s65 offen offset:1024 sc1\ns_waitcnt lgkmcnt(6)\nM v[56:59],v[48:51],v[212:215],v[56:59]\nv_min_u32 v169,-2,v146\nv_min_u32 v170,-2,v162\nM v[60:63],v[36:39],v[196:199],v[60:63]\n" \
"v_min_u32 v171,-2,v178\nds_read_b64_tr_b16 v[208:209],v226 offset:26112\nds_read_b64_tr_b16 v[210:211],v226 offset:30208\nbuffer_store_dwordx4 v[168:171],v253,s[52:55],s33 offen offset:2048 sc1\nM v[60:63],v[52:55],v[212:215],v[60:63]\nds_read_b64_tr_b16 v[196:197],v227 offset:17920\nds_read_b64_tr_b16 v[198:199],v227 offset:22016\nM v[72:75],v[32:35],v[200:203],v[72:75]\n" \
"buffer_load_dwordx4 a[120:123],v240,s[52:55],s65 offen offset:2048 sc1\nM v[72:75],v[48:51],v[204:207],v[72:75]\nds_read_b64_tr_b16 v[212:213],v227 offset:26112\nds_read_b64_tr_b16 v[214:215],v227 offset:30208\nM v[76:79],v[36:39],v[200:203],v[76:79]\nv_add_u32 v226,0,v242\nds_read_b64_tr_b16 v[200:201],v226 offset:0\nM v[76:79],v[52:55],v[204:207],v[76:79]\n" \
"ds_read_b64_tr_b16 v[202:203],v226 offset:4096\ns_waitcnt lgkmcnt(8)\nM v[88:91],v[32:35],v[216:219],v[88:91]\nbuffer_load_dwordx4 a[124:127],v240,s[52:55],s65 offen offset:3072 sc1\nM v[88:91],v[48:51],v[220:223],v[88:91]\nds_read_b64_tr_b16 v[204:205],v226 offset:8192\nds_read_b64_tr_b16 v[206:207],v226 offset:12288\nM v[92:95],v[36:39],v[216:219],v[92:95]\n" \
"s_add_u32 s65,s14,s67\nv_xor_b32 v227,32,v226\nM v[92:95],v[52:55],v[220:223],v[92:95]\nds_read_b64_tr_b16 v[216:217],v227 offset:0\nds_read_b64_tr_b16 v[218:219],v227 offset:4096\nM v[104:107],v[32:35],v[192:195],v[104:107]\nbuffer_load_dwordx4 a[128:131],v240,s[52:55],s65 offen offset:0 sc1\ns_waitcnt lgkmcnt(6)\n" \
"M v[104:107],v[48:51],v[208:211],v[104:107]\nds_read_b64_tr_b16 v[220:221],v227 offset:8192\nds_read_b64_tr_b16 v[222:223],v227 offset:12288\nM v[108:111],v[36:39],v[192:195],v[108:111]\nv_cvt_pk_bf16_f32 v179,v179,v187\nds_read_b64_tr_b16 v[192:193],v226 offset:512\nM v[108:111],v[52:55],v[208:211],v[108:111]\nds_read_b64_tr_b16 v[194:195],v226 offset:4608\n" \
"v_min_u32 v184,-2,v131\nM v[120:123],v[32:35],v[196:199],v[120:123]\nbuffer_load_dwordx4 a[132:135],v240,s[52:55],s65 offen offset:1024 sc1\nM v[120:123],v[48:51],v[212:215],v[120:123]\nv_min_u32 v185,-2,v147\nv_min_u32 v186,-2,v163\nM v[124:127],v[36:39],v[196:199],v[124:127]\nv_min_u32 v187,-2,v179\n" \
"buffer_store_dwordx4 v[184:187],v253,s[52:55],s33 offen offset:3072 sc1\nds_read_b64_tr_b16 v[208:209],v226 offset:8704\nds_read_b64_tr_b16 v[210:211],v226 offset:12800\nM v[124:127],v[52:55],v[212:215],v[124:127]\nds_read_b64_tr_b16 v[196:197],v227 offset:512\ns_waitcnt vmcnt(18) lgkmcnt(7)\nM v[8:11],a[64:67],v[200:203],v[8:11]\nbuffer_load_dwordx4 a[136:139],v240,s[52:55],s65 offen offset:2048 sc1\n" \
"M v[8:11],a[72:75],v[204:207],v[8:11]\nds_read_b64_tr_b16 v[198:199],v227 offset:4608\nds_read_b64_tr_b16 v[212:213],v227 offset:8704\nM v[12:15],a[68:71],v[200:203],v[12:15]\nds_read_b64_tr_b16 v[214:215],v227 offset:12800\nds_read_b64_tr_b16 v[200:201],v226 offset:1024\nM v[12:15],a[76:79],v[204:207],v[12:15]\nds_read_b64_tr_b16 v[202:203],v226 offset:5120\n" \
"ds_read_b64_tr_b16 v[204:205],v226 offset:9216\nM v[24:27],a[64:67],v[216:219],v[24:27]\nbuffer_load_dwordx4 a[140:143],v240,s[52:55],s65 offen offset:3072 sc1\ns_waitcnt lgkmcnt(9)\nM v[24:27],a[72:75],v[220:223],v[24:27]\nds_read_b64_tr_b16 v[206:207],v226 offset:13312\ns_add_u32 s65,s65,4096\nM v[28:31],a[68:71],v[216:219],v[28:31]\n" \
"ds_read_b64_tr_b16 v[216:217],v227 offset:1024\nds_read_b64_tr_b16 v[218:219],v227 offset:5120\nM v[28:31],a[76:79],v[220:223],v[28:31]\nds_read_b64_tr_b16 v[220:221],v227 offset:9216\nds_read_b64_tr_b16 v[222:223],v227 offset:13312\nM v[40:43],a[64:67],v[192:195],v[40:43]\nbuffer_load_dwordx4 a[144:147],v240,s[52:55],s65 offen offset:0 sc1\ns_waitcnt lgkmcnt(9)\n" \
"M v[40:43],a[72:75],v[208:211],v[40:43]\nv_cvt_pk_bf16_f32 v132,v132,v140\nv_cvt_pk_bf16_f32 v133,v133,v141\nM v[44:47],a[68:71],v[192:195],v[44:47]\nv_cvt_pk_bf16_f32 v134,v134,v142\nv_cvt_pk_bf16_f32 v135,v135,v143\nM v[44:47],a[76:79],v[208:211],v[44:47]\nv_cvt_pk_bf16_f32 v148,v148,v156\n" \
"v_cvt_pk_bf16_f32 v164,v164,v172\nM v[56:59],a[64:67],v[196:199],v[56:59]\nbuffer_load_dwordx4 a[148:151],v240,s[52:55],s65 offen offset:1024 sc1\ns_waitcnt lgkmcnt(4)\nM v[56:59],a[72:75],v[212:215],v[56:59]\nv_cvt_pk_bf16_f32 v180,v180,v188\nds_read_b64_tr_b16 v[192:193],v226 offset:1536\nds_read_b64_tr_b16 v[194:195],v226 offset:5632\n" \
"M v[60:63],a[68:71],v[196:199],v[60:63]\nv_min_u32 v140,-2,v132\nv_min_u32 v141,-2,v148\nv_min_u32 v142,-2,v164\nv_min_u32 v143,-2,v180\nbuffer_store_dwordx4 v[140:143],v253,s[52:55],s38 offen offset:0 sc1\nds_read_b64_tr_b16 v[208:209],v226 offset:9728\nds_read_b64_tr_b16 v[210:211],v226 offset:13824\n" \
"M v[60:63],a[76:79],v[212:215],v[60:63]\nds_read_b64_tr_b16 v[196:197],v227 offset:1536\nds_read_b64_tr_b16 v[198:199],v227 offset:5632\nM v[72:75],a[64:67],v[200:203],v[72:75]\nbuffer_load_dwordx4 a[152:155],v240,s[52:55],s65 offen offset:2048 sc1\nM v[72:75],a[72:75],v[204:207],v[72:75]\nds_read_b64_tr_b16 v[212:213],v227 offset:9728\nds_read_b64_tr_b16 v[214:215],v227 offset:13824\n" \
"M v[76:79],a[68:71],v[200:203],v[76:79]\nds_read_b64_tr_b16 v[200:201],v226 offset:32768\nds_read_b64_tr_b16 v[202:203],v226 offset:36864\nM v[76:79],a[76:79],v[204:207],v[76:79]\nds_read_b64_tr_b16 v[204:205],v226 offset:40960\ns_waitcnt lgkmcnt(9)\nM v[88:91],a[64:67],v[216:219],v[88:91]\nbuffer_load_dwordx4 a[156:159],v240,s[52:55],s65 offen offset:3072 sc1\n" \
"M v[88:91],a[72:75],v[220:223],v[88:91]\nds_read_b64_tr_b16 v[206:207],v226 offset:45056\nv_cvt_pk_bf16_f32 v149,v149,v157\nM v[92:95],a[68:71],v[216:219],v[92:95]\nds_read_b64_tr_b16 v[216:217],v227 offset:32768\nds_read_b64_tr_b16 v[218:219],v227 offset:36864\nM v[92:95],a[76:79],v[220:223],v[92:95]\nds_read_b64_tr_b16 v[220:221],v227 offset:40960\n" \
"ds_read_b64_tr_b16 v[222:223],v227 offset:45056\nM v[104:107],a[64:67],v[192:195],v[104:107]\nv_cvt_pk_bf16_f32 v150,v150,v158\ns_waitcnt lgkmcnt(9)\nM v[104:107],a[72:75],v[208:211],v[104:107]\nv_cvt_pk_bf16_f32 v151,v151,v159\nv_cvt_pk_bf16_f32 v165,v165,v173\nM v[108:111],a[68:71],v[192:195],v[108:111]\n" \
"v_cvt_pk_bf16_f32 v181,v181,v189\nds_read_b64_tr_b16 v[192:193],v226 offset:33280\nM v[108:111],a[76:79],v[208:211],v[108:111]\nds_read_b64_tr_b16 v[194:195],v226 offset:37376\nv_min_u32 v156,-2,v133\nM v[120:123],a[64:67],v[196:199],v[120:123]\nv_min_u32 v157,-2,v149\ns_waitcnt lgkmcnt(6)\n" \
"M v[120:123],a[72:75],v[212:215],v[120:123]\nv_min_u32 v158,-2,v165\nv_min_u32 v159,-2,v181\nM v[124:127],a[68:71],v[196:199],v[124:127]\nbuffer_store_dwordx4 v[156:159],v253,s[52:55],s38 offen offset:1024 sc1\nds_read_b64_tr_b16 v[208:209],v226 offset:41472\nds_read_b64_tr_b16 v[210:211],v226 offset:45568\nM v[124:127],a[76:79],v[212:215],v[124:127]\n" \
"ds_read_b64_tr_b16 v[196:197],v227 offset:33280\ns_waitcnt vmcnt(24)\nM v[8:11],a[80:83],v[200:203],v[8:11]\nds_read_b64_tr_b16 v[198:199],v227 offset:37376\ns_waitcnt vmcnt(19)\nM v[8:11],a[88:91],v[204:207],v[8:11]\nds_read_b64_tr_b16 v[212:213],v227 offset:41472\nds_read_b64_tr_b16 v[214:215],v227 offset:45568\n" \
"M v[12:15],a[84:87],v[200:203],v[12:15]\nds_read_b64_tr_b16 v[200:201],v226 offset:33792\nds_read_b64_tr_b16 v[202:203],v226 offset:37888\nM v[12:15],a[92:95],v[204:207],v[12:15]\nds_read_b64_tr_b16 v[204:205],v226 offset:41984\ns_waitcnt lgkmcnt(9)\nM v[24:27],a[80:83],v[216:219],v[24:27]\nds_read_b64_tr_b16 v[206:207],v226 offset:46080\n" \
"v_cvt_pk_bf16_f32 v166,v166,v174\nM v[24:27],a[88:91],v[220:223],v[24:27]\nv_cvt_pk_bf16_f32 v167,v167,v175\nv_cvt_pk_bf16_f32 v182,v182,v190\nM v[28:31],a[84:87],v[216:219],v[28:31]\nds_read_b64_tr_b16 v[216:217],v227 offset:33792\nds_read_b64_tr_b16 v[218:219],v227 offset:37888\nM v[28:31],a[92:95],v[220:223],v[28:31]\n" \
"ds_read_b64_tr_b16 v[220:221],v227 offset:41984\nds_read_b64_tr_b16 v[222:223],v227 offset:46080\nM v[40:43],a[80:83],v[192:195],v[40:43]\nv_min_u32 v172,-2,v134\ns_waitcnt lgkmcnt(9)\nM v[40:43],a[88:91],v[208:211],v[40:43]\nv_min_u32 v173,-2,v150\nv_min_u32 v174,-2,v166\n" \
"M v[44:47],a[84:87],v[192:195],v[44:47]\nds_read_b64_tr_b16 v[192:193],v226 offset:34304\nds_read_b64_tr_b16 v[194:195],v226 offset:38400\nM v[44:47],a[92:95],v[208:211],v[44:47]\nv_min_u32 v175,-2,v182\nds_read_b64_tr_b16 v[208:209],v226 offset:42496\nM v[56:59],a[80:83],v[196:199],v[56:59]\nds_read_b64_tr_b16 v[210:211],v226 offset:46592\n" \
"s_waitcnt lgkmcnt(8)\nM v[56:59],a[88:91],v[212:215],v[56:59]\nv_cvt_pk_bf16_f32 v183,v183,v191\nv_min_u32 v188,-2,v135\nM v[60:63],a[84:87],v[196:199],v[60:63]\nbuffer_store_dwordx4 v[172:175],v253,s[52:55],s38 offen offset:2048 sc1\nM v[60:63],a[92:95],v[212:215],v[60:63]\nds_read_b64_tr_b16 v[196:197],v227 offset:34304\n" \
"ds_read_b64_tr_b16 v[198:199],v227 offset:38400\nM v[72:75],a[80:83],v[200:203],v[72:75]\nds_read_b64_tr_b16 v[212:213],v227 offset:42496\nds_read_b64_tr_b16 v[214:215],v227 offset:46592\nM v[72:75],a[88:91],v[204:207],v[72:75]\nv_min_u32 v189,-2,v151\nv_min_u32 v190,-2,v167\nM v[76:79],a[84:87],v[200:203],v[76:79]\n" \
"ds_read_b64_tr_b16 v[200:201],v226 offset:16384\nds_read_b64_tr_b16 v[202:203],v226 offset:20480\nM v[76:79],a[92:95],v[204:207],v[76:79]\nds_read_b64_tr_b16 v[204:205],v226 offset:24576\ns_waitcnt lgkmcnt(7)\nM v[88:91],a[80:83],v[216:219],v[88:91]\nds_read_b64_tr_b16 v[206:207],v226 offset:28672\nv_min_u32 v191,-2,v183\n" \
"M v[88:91],a[88:91],v[220:223],v[88:91]\nds_read_b128 v[0:3],v238 offset:0\nds_read_b128 v[4:7],v238 offset:16\nM v[92:95],a[84:87],v[216:219],v[92:95]\nds_read_b64_tr_b16 v[216:217],v227 offset:16384\nv_cmp_eq_u32 vcc,v228,v229\nM v[92:95],a[92:95],v[220:223],v[92:95]\nds_read_b64_tr_b16 v[218:219],v227 offset:20480\n" \
"ds_read_b64_tr_b16 v[220:221],v227 offset:24576\nM v[104:107],a[80:83],v[192:195],v[104:107]\nds_read_b64_tr_b16 v[222:223],v227 offset:28672\nds_read_b128 v[16:19],v238 offset:4096\nM v[104:107],a[88:91],v[208:211],v[104:107]\nds_read_b128 v[20:23],v238 offset:4112\nv_and_b32 v228,1,v249\nM v[108:111],a[84:87],v[192:195],v[108:111]\n" \
"ds_read_b64_tr_b16 v[192:193],v226 offset:16896\nv_lshlrev_b32 v228,4,v228\nM v[108:111],a[92:95],v[208:211],v[108:111]\nds_read_b64_tr_b16 v[194:195],v226 offset:20992\ns_waitcnt lgkmcnt(10)\nM v[120:123],a[80:83],v[196:199],v[120:123]\nds_read_b64_tr_b16 v[208:209],v226 offset:25088\nds_read_b64_tr_b16 v[210:211],v226 offset:29184\n" \
"M v[120:123],a[88:91],v[212:215],v[120:123]\nds_read_b128 v[32:35],v238 offset:8192\nds_read_b128 v[36:39],v238 offset:8208\nM v[124:127],a[84:87],v[196:199],v[124:127]\nbuffer_store_dwordx4 v[188:191],v253,s[52:55],s38 offen offset:3072 sc1\nM v[124:127],a[92:95],v[212:215],v[124:127]\nds_read_b64_tr_b16 v[196:197],v227 offset:16896\nds_read_b64_tr_b16 v[198:199],v227 offset:20992\n" \
"M v[8:11],a[96:99],v[200:203],v[8:11]\nds_read_b64_tr_b16 v[212:213],v227 offset:25088\ns_waitcnt vmcnt(16)\nM v[8:11],a[104:107],v[204:207],v[8:11]\nds_read_b64_tr_b16 v[214:215],v227 offset:29184\nds_read_b128 v[48:51],v238 offset:12288\nM v[12:15],a[100:103],v[200:203],v[12:15]\nds_read_b64_tr_b16 v[200:201],v226 offset:17408\n" \
"ds_read_b64_tr_b16 v[202:203],v226 offset:21504\nM v[12:15],a[108:111],v[204:207],v[12:15]\nds_read_b64_tr_b16 v[204:205],v226 offset:25600\ns_waitcnt lgkmcnt(11)\nM v[24:27],a[96:99],v[216:219],v[24:27]\nds_read_b64_tr_b16 v[206:207],v226 offset:29696\nds_read_b128 v[52:55],v238 offset:12304\nM v[24:27],a[104:107],v[220:223],v[24:27]\n" \
"v_mov_b32 v229,0x3f80\nv_lshlrev_b32 v228,v228,v229\nM v[28:31],a[100:103],v[216:219],v[28:31]\nds_read_b64_tr_b16 v[216:217],v227 offset:17408\nds_read_b64_tr_b16 v[218:219],v227 offset:21504\nM v[28:31],a[108:111],v[220:223],v[28:31]\nds_read_b64_tr_b16 v[220:221],v227 offset:25600\nds_read_b64_tr_b16 v[222:223],v227 offset:29696\n" \
"M v[40:43],a[96:99],v[192:195],v[40:43]\nv_cndmask_b32 v228,0,v228,vcc\ns_waitcnt lgkmcnt(10)\nM v[40:43],a[104:107],v[208:211],v[40:43]\nv_and_b32 v229,2,v249\nv_cmp_eq_u32 vcc,0,v229\nM v[44:47],a[100:103],v[192:195],v[44:47]\nds_read_b64_tr_b16 v[192:193],v226 offset:17920\n" \
"ds_read_b64_tr_b16 v[194:195],v226 offset:22016\nM v[44:47],a[108:111],v[208:211],v[44:47]\nds_read_b64_tr_b16 v[208:209],v226 offset:26112\nds_read_b64_tr_b16 v[210:211],v226 offset:30208\nM v[56:59],a[96:99],v[196:199],v[56:59]\nv_cndmask_b32 v229,0,v228,vcc\nv_and_b32 v244,15,v249\nM v[56:59],a[104:107],v[212:215],v[56:59]\n" \
"v_and_b32 v245,0x300,v240\nv_lshlrev_b32 v246,11,v249\nM v[60:63],a[100:103],v[196:199],v[60:63]\nds_read_b64_tr_b16 v[196:197],v227 offset:17920\nds_read_b64_tr_b16 v[198:199],v227 offset:22016\nM v[60:63],a[108:111],v[212:215],v[60:63]\nds_read_b64_tr_b16 v[212:213],v227 offset:26112\ns_waitcnt lgkmcnt(7)\n" \
"M v[72:75],a[96:99],v[200:203],v[72:75]\nds_read_b64_tr_b16 v[214:215],v227 offset:30208\nv_cmp_lt_u32 vcc,7,v249\nM v[72:75],a[104:107],v[204:207],v[72:75]\nv_mov_b32 v230,0x40000000\nv_lshl_or_b32 v244,v244,1,v245\nM v[76:79],a[100:103],v[200:203],v[76:79]\nds_read_b64_tr_b16 v[200:201],v226 offset:49152\n" \
"ds_read_b64_tr_b16 v[202:203],v226 offset:53248\nM v[76:79],a[108:111],v[204:207],v[76:79]\nds_read_b64_tr_b16 v[204:205],v226 offset:57344\nds_read_b64_tr_b16 v[206:207],v226 offset:61440\nM v[88:91],a[96:99],v[216:219],v[88:91]\ns_add_u32 s70,s64,114688\nv_cndmask_b32 v246,v246,v230,vcc\nM v[88:91],a[104:107],v[220:223],v[88:91]\n" \
"v_add_u32 v244,s70,v244\nv_add_u32 v245,s70,v240\nM v[92:95],a[100:103],v[216:219],v[92:95]\nds_read_b64_tr_b16 v[216:217],v227 offset:49152\nds_read_b64_tr_b16 v[218:219],v227 offset:53248\nM v[92:95],a[108:111],v[220:223],v[92:95]\nds_read_b64_tr_b16 v[220:221],v227 offset:57344\ns_waitcnt lgkmcnt(7)\n" \
"M v[104:107],a[96:99],v[192:195],v[104:107]\nds_read_b64_tr_b16 v[222:223],v227 offset:61440\ns_lshl_b32 s80,s60,11\nM v[104:107],a[104:107],v[208:211],v[104:107]\nv_and_b32 v230,3,v249\nv_lshrrev_b32 v231,2,v249\nM v[108:111],a[100:103],v[192:195],v[108:111]\nds_read_b64_tr_b16 v[192:193],v226 offset:49664\n" \
"ds_read_b64_tr_b16 v[194:195],v226 offset:53760\nM v[108:111],a[108:111],v[208:211],v[108:111]\nds_read_b64_tr_b16 v[208:209],v226 offset:57856\nds_read_b64_tr_b16 v[210:211],v226 offset:61952\nM v[120:123],a[96:99],v[196:199],v[120:123]\ns_lshl3_add_u32 s37,s6,s75\ns_add_u32 s80,s80,557056\nM v[120:123],a[104:107],v[212:215],v[120:123]\n" \
"v_lshl_add_u32 v231,v230,5,v231\ns_lshl_b32 s70,s7,8\nM v[124:127],a[100:103],v[196:199],v[124:127]\nds_read_b64_tr_b16 v[196:197],v227 offset:49664\nds_read_b64_tr_b16 v[198:199],v227 offset:53760\nM v[124:127],a[108:111],v[212:215],v[124:127]\nds_read_b64_tr_b16 v[212:213],v227 offset:57856\ns_waitcnt lgkmcnt(7)\n" \
"M v[8:11],a[112:115],v[200:203],v[8:11]\nds_read_b64_tr_b16 v[214:215],v227 offset:61952\ns_waitcnt vmcnt(11)\nM v[8:11],a[120:123],v[204:207],v[8:11]\nv_add_f32 v32,1.0,v32\nv_add_f32 v33,1.0,v33\nM v[12:15],a[116:119],v[200:203],v[12:15]\nds_read_b64_tr_b16 v[200:201],v226 offset:50176\n" \
"ds_read_b64_tr_b16 v[202:203],v226 offset:54272\nM v[12:15],a[124:127],v[204:207],v[12:15]\nds_read_b64_tr_b16 v[204:205],v226 offset:58368\nds_read_b64_tr_b16 v[206:207],v226 offset:62464\nM v[24:27],a[112:115],v[216:219],v[24:27]\nv_add_f32 v34,1.0,v34\nv_add_f32 v35,1.0,v35\nM v[24:27],a[120:123],v[220:223],v[24:27]\n" \
"v_add_f32 v36,1.0,v36\nv_add_f32 v37,1.0,v37\nM v[28:31],a[116:119],v[216:219],v[28:31]\nds_read_b64_tr_b16 v[216:217],v227 offset:50176\nds_read_b64_tr_b16 v[218:219],v227 offset:54272\nM v[28:31],a[124:127],v[220:223],v[28:31]\nds_read_b64_tr_b16 v[220:221],v227 offset:58368\ns_waitcnt lgkmcnt(7)\n" \
"M v[40:43],a[112:115],v[192:195],v[40:43]\nds_read_b64_tr_b16 v[222:223],v227 offset:62464\nv_add_f32 v38,1.0,v38\nM v[40:43],a[120:123],v[208:211],v[40:43]\nv_add_f32 v39,1.0,v39\nv_add_f32 v116,1.0,v116\nM v[44:47],a[116:119],v[192:195],v[44:47]\nds_read_b64_tr_b16 v[192:193],v226 offset:50688\n" \
"ds_read_b64_tr_b16 v[194:195],v226 offset:54784\nM v[44:47],a[124:127],v[208:211],v[44:47]\nds_read_b64_tr_b16 v[208:209],v226 offset:58880\nds_read_b64_tr_b16 v[210:211],v226 offset:62976\nM v[56:59],a[112:115],v[196:199],v[56:59]\nv_add_u32 v226,65536,v242\nv_add_f32 v117,1.0,v117\nM v[56:59],a[120:123],v[212:215],v[56:59]\n" \
"s_add_u32 s37,s37,557056\nv_lshlrev_b32 v251,5,v249\nM v[60:63],a[116:119],v[196:199],v[60:63]\nds_read_b64_tr_b16 v[196:197],v227 offset:50688\nds_read_b64_tr_b16 v[198:199],v227 offset:54784\nM v[60:63],a[124:127],v[212:215],v[60:63]\nds_read_b64_tr_b16 v[212:213],v227 offset:58880\ns_waitcnt lgkmcnt(7)\n" \
"M v[72:75],a[112:115],v[200:203],v[72:75]\nds_read_b64_tr_b16 v[214:215],v227 offset:62976\nv_xor_b32 v227,32,v226\nM v[72:75],a[120:123],v[204:207],v[72:75]\ns_or_b32 s80,s80,s75\nv_lshl_add_u32 v248,v231,1,s70\nM v[76:79],a[116:119],v[200:203],v[76:79]\nds_read_b64_tr_b16 v[200:201],v226 offset:0\n" \
"ds_read_b64_tr_b16 v[202:203],v226 offset:4096\nM v[76:79],a[124:127],v[204:207],v[76:79]\nds_read_b64_tr_b16 v[204:205],v226 offset:8192\nds_read_b64_tr_b16 v[206:207],v226 offset:12288\nM v[88:91],a[112:115],v[216:219],v[88:91]\nv_lshlrev_b32 v64,16,v64\nv_lshlrev_b32 v65,16,v65\nM v[88:91],a[120:123],v[220:223],v[88:91]\n" \
"v_lshlrev_b32 v66,16,v66\nv_lshlrev_b32 v67,16,v67\nM v[92:95],a[116:119],v[216:219],v[92:95]\nds_read_b64_tr_b16 v[216:217],v227 offset:0\nds_read_b64_tr_b16 v[218:219],v227 offset:4096\nM v[92:95],a[124:127],v[220:223],v[92:95]\nds_read_b64_tr_b16 v[220:221],v227 offset:8192\ns_waitcnt lgkmcnt(7)\n" \
"M v[104:107],a[112:115],v[192:195],v[104:107]\nds_read_b64_tr_b16 v[222:223],v227 offset:12288\nv_lshlrev_b32 v68,16,v68\nM v[104:107],a[120:123],v[208:211],v[104:107]\nv_lshlrev_b32 v69,16,v69\nv_lshlrev_b32 v70,16,v70\nM v[108:111],a[116:119],v[192:195],v[108:111]\nds_read_b64_tr_b16 v[192:193],v226 offset:512\n" \
"ds_read_b64_tr_b16 v[194:195],v226 offset:4608\nM v[108:111],a[124:127],v[208:211],v[108:111]\nds_read_b64_tr_b16 v[208:209],v226 offset:8704\nds_read_b64_tr_b16 v[210:211],v226 offset:12800\nM v[120:123],a[112:115],v[196:199],v[120:123]\nv_lshlrev_b32 v71,16,v71\nv_lshlrev_b32 v80,16,v80\nM v[120:123],a[120:123],v[212:215],v[120:123]\n" \
"v_lshlrev_b32 v81,16,v81\nv_lshlrev_b32 v82,16,v82\nM v[124:127],a[116:119],v[196:199],v[124:127]\nds_read_b64_tr_b16 v[196:197],v227 offset:512\nds_read_b64_tr_b16 v[198:199],v227 offset:4608\nM v[124:127],a[124:127],v[212:215],v[124:127]\nds_read_b64_tr_b16 v[212:213],v227 offset:8704\ns_waitcnt lgkmcnt(7)\n" \
"M v[8:11],a[128:131],v[200:203],v[8:11]\nds_read_b64_tr_b16 v[214:215],v227 offset:12800\ns_waitcnt vmcnt(6)\nM v[8:11],a[136:139],v[204:207],v[8:11]\nv_lshlrev_b32 v83,16,v83\nv_lshlrev_b32 v84,16,v84\nM v[12:15],a[132:135],v[200:203],v[12:15]\nds_read_b64_tr_b16 v[200:201],v226 offset:1024\n" \
"ds_read_b64_tr_b16 v[202:203],v226 offset:5120\nM v[12:15],a[140:143],v[204:207],v[12:15]\nds_read_b64_tr_b16 v[204:205],v226 offset:9216\nds_read_b64_tr_b16 v[206:207],v226 offset:13312\nM v[24:27],a[128:131],v[216:219],v[24:27]\nv_lshlrev_b32 v85,16,v85\nv_lshlrev_b32 v86,16,v86\nM v[24:27],a[136:139],v[220:223],v[24:27]\n" \
"v_lshlrev_b32 v87,16,v87\nv_lshlrev_b32 v96,16,v96\nM v[28:31],a[132:135],v[216:219],v[28:31]\nds_read_b64_tr_b16 v[216:217],v227 offset:1024\nds_read_b64_tr_b16 v[218:219],v227 offset:5120\nM v[28:31],a[140:143],v[220:223],v[28:31]\nds_read_b64_tr_b16 v[220:221],v227 offset:9216\ns_waitcnt lgkmcnt(7)\n" \
"M v[40:43],a[128:131],v[192:195],v[40:43]\nds_read_b64_tr_b16 v[222:223],v227 offset:13312\nv_lshlrev_b32 v97,16,v97\nM v[40:43],a[136:139],v[208:211],v[40:43]\nv_lshlrev_b32 v98,16,v98\nv_lshlrev_b32 v99,16,v99\nM v[44:47],a[132:135],v[192:195],v[44:47]\nds_read_b64_tr_b16 v[192:193],v226 offset:1536\n" \
"ds_read_b64_tr_b16 v[194:195],v226 offset:5632\nM v[44:47],a[140:143],v[208:211],v[44:47]\nds_read_b64_tr_b16 v[208:209],v226 offset:9728\nds_read_b64_tr_b16 v[210:211],v226 offset:13824\nM v[56:59],a[128:131],v[196:199],v[56:59]\nv_lshlrev_b32 v100,16,v100\nv_lshlrev_b32 v101,16,v101\nM v[56:59],a[136:139],v[212:215],v[56:59]\n" \
"v_lshlrev_b32 v102,16,v102\nv_lshlrev_b32 v103,16,v103\nM v[60:63],a[132:135],v[196:199],v[60:63]\nds_read_b64_tr_b16 v[196:197],v227 offset:1536\nds_read_b64_tr_b16 v[198:199],v227 offset:5632\nM v[60:63],a[140:143],v[212:215],v[60:63]\nds_read_b64_tr_b16 v[212:213],v227 offset:9728\ns_waitcnt lgkmcnt(7)\n" \
"M v[72:75],a[128:131],v[200:203],v[72:75]\nds_read_b64_tr_b16 v[214:215],v227 offset:13824\nv_cvt_pk_bf16_f32 v32,0,v32\nM v[72:75],a[136:139],v[204:207],v[72:75]\nv_cvt_pk_bf16_f32 v33,0,v33\nv_cvt_pk_bf16_f32 v34,0,v34\nM v[76:79],a[132:135],v[200:203],v[76:79]\nds_read_b64_tr_b16 v[200:201],v226 offset:32768\n" \
"ds_read_b64_tr_b16 v[202:203],v226 offset:36864\nM v[76:79],a[140:143],v[204:207],v[76:79]\nds_read_b64_tr_b16 v[204:205],v226 offset:40960\nds_read_b64_tr_b16 v[206:207],v226 offset:45056\nM v[88:91],a[128:131],v[216:219],v[88:91]\nv_cvt_pk_bf16_f32 v35,0,v35\nv_cvt_pk_bf16_f32 v36,0,v36\nM v[88:91],a[136:139],v[220:223],v[88:91]\n" \
"v_cvt_pk_bf16_f32 v37,0,v37\nv_cvt_pk_bf16_f32 v38,0,v38\nM v[92:95],a[132:135],v[216:219],v[92:95]\nds_read_b64_tr_b16 v[216:217],v227 offset:32768\nds_read_b64_tr_b16 v[218:219],v227 offset:36864\nM v[92:95],a[140:143],v[220:223],v[92:95]\nds_read_b64_tr_b16 v[220:221],v227 offset:40960\ns_waitcnt lgkmcnt(7)\n" \
"M v[104:107],a[128:131],v[192:195],v[104:107]\nds_read_b64_tr_b16 v[222:223],v227 offset:45056\nv_cvt_pk_bf16_f32 v39,0,v39\nM v[104:107],a[136:139],v[208:211],v[104:107]\nv_cvt_pk_bf16_f32 v116,0,v116\nv_cvt_pk_bf16_f32 v117,0,v117\nM v[108:111],a[132:135],v[192:195],v[108:111]\nds_read_b64_tr_b16 v[192:193],v226 offset:33280\n" \
"ds_read_b64_tr_b16 v[194:195],v226 offset:37376\nM v[108:111],a[140:143],v[208:211],v[108:111]\nds_read_b64_tr_b16 v[208:209],v226 offset:41472\nds_read_b64_tr_b16 v[210:211],v226 offset:45568\nM v[120:123],a[128:131],v[196:199],v[120:123]\nv_xor_b32 v228,v228,v229\ns_add_u32 s38,s78,16384\nM v[120:123],a[136:139],v[212:215],v[120:123]\n" \
"v_accvgpr_write_b32 a192,v229\nv_accvgpr_write_b32 a193,v228\nM v[124:127],a[132:135],v[196:199],v[124:127]\nds_read_b64_tr_b16 v[196:197],v227 offset:33280\nds_read_b64_tr_b16 v[198:199],v227 offset:37376\nM v[124:127],a[140:143],v[212:215],v[124:127]\nds_read_b64_tr_b16 v[212:213],v227 offset:41472\ns_waitcnt lgkmcnt(7)\n" \
"M v[8:11],a[144:147],v[200:203],v[8:11]\nds_read_b64_tr_b16 v[214:215],v227 offset:45568\ns_waitcnt vmcnt(3)\nM v[8:11],a[152:155],v[204:207],v[8:11]\nv_accvgpr_write_b32 a198,v229\nv_accvgpr_write_b32 a199,v228\nM v[12:15],a[148:151],v[200:203],v[12:15]\nds_read_b64_tr_b16 v[200:201],v226 offset:33792\n" \
"ds_read_b64_tr_b16 v[202:203],v226 offset:37888\nM v[12:15],a[156:159],v[204:207],v[12:15]\nds_read_b64_tr_b16 v[204:205],v226 offset:41984\nds_read_b64_tr_b16 v[206:207],v226 offset:46080\nM v[24:27],a[144:147],v[216:219],v[24:27]\nM v[24:27],a[152:155],v[220:223],v[24:27]\nM v[28:31],a[148:151],v[216:219],v[28:31]\nds_read_b64_tr_b16 v[216:217],v227 offset:33792\n" \
"ds_read_b64_tr_b16 v[218:219],v227 offset:37888\nM v[28:31],a[156:159],v[220:223],v[28:31]\nds_read_b64_tr_b16 v[220:221],v227 offset:41984\ns_waitcnt lgkmcnt(8)\nM v[40:43],a[144:147],v[192:195],v[40:43]\nds_read_b64_tr_b16 v[222:223],v227 offset:46080\nv_cvt_pk_bf16_f32 v8,v8,v24\nM v[40:43],a[152:155],v[208:211],v[40:43]\n" \
"v_cvt_pk_bf16_f32 v9,v9,v25\nv_cvt_pk_bf16_f32 v10,v10,v26\nM v[44:47],a[148:151],v[192:195],v[44:47]\nds_read_b64_tr_b16 v[192:193],v226 offset:34304\nds_read_b64_tr_b16 v[194:195],v226 offset:38400\nM v[44:47],a[156:159],v[208:211],v[44:47]\nds_read_b64_tr_b16 v[208:209],v226 offset:42496\nds_read_b64_tr_b16 v[210:211],v226 offset:46592\n" \
"M v[56:59],a[144:147],v[196:199],v[56:59]\nv_cvt_pk_bf16_f32 v11,v11,v27\ns_waitcnt lgkmcnt(8)\nM v[56:59],a[152:155],v[212:215],v[56:59]\nv_cvt_pk_bf16_f32 v12,v12,v28\nv_cvt_pk_bf16_f32 v13,v13,v29\nM v[60:63],a[148:151],v[196:199],v[60:63]\nds_read_b64_tr_b16 v[196:197],v227 offset:34304\n" \
"ds_read_b64_tr_b16 v[198:199],v227 offset:38400\nM v[60:63],a[156:159],v[212:215],v[60:63]\nds_read_b64_tr_b16 v[212:213],v227 offset:42496\nds_read_b64_tr_b16 v[214:215],v227 offset:46592\nM v[72:75],a[144:147],v[200:203],v[72:75]\nv_cvt_pk_bf16_f32 v40,v40,v56\nv_cvt_pk_bf16_f32 v41,v41,v57\nM v[72:75],a[152:155],v[204:207],v[72:75]\n" \
"v_cvt_pk_bf16_f32 v42,v42,v58\nv_cvt_pk_bf16_f32 v43,v43,v59\nM v[76:79],a[148:151],v[200:203],v[76:79]\nv_cvt_pk_bf16_f32 v14,v14,v30\nv_cvt_pk_bf16_f32 v15,v15,v31\nM v[76:79],a[156:159],v[204:207],v[76:79]\nv_cvt_pk_bf16_f32 v44,v44,v60\ns_waitcnt lgkmcnt(4)\n" \
"M v[88:91],a[144:147],v[216:219],v[88:91]\nv_cvt_pk_bf16_f32 v45,v45,v61\nv_cvt_pk_bf16_f32 v46,v46,v62\nM v[88:91],a[152:155],v[220:223],v[88:91]\nv_cvt_pk_bf16_f32 v47,v47,v63\nv_min_u32 v24,-2,v8\nM v[92:95],a[148:151],v[216:219],v[92:95]\nv_min_u32 v25,-2,v40\n" \
"v_min_u32 v56,-2,v9\nM v[92:95],a[156:159],v[220:223],v[92:95]\nv_min_u32 v57,-2,v41\nv_min_u32 v28,-2,v12\nM v[104:107],a[144:147],v[192:195],v[104:107]\nv_cvt_pk_bf16_f32 v72,v72,v88\nv_cvt_pk_bf16_f32 v73,v73,v89\nM v[104:107],a[152:155],v[208:211],v[104:107]\n" \
"v_cvt_pk_bf16_f32 v74,v74,v90\nv_cvt_pk_bf16_f32 v75,v75,v91\nM v[108:111],a[148:151],v[192:195],v[108:111]\nv_cvt_pk_bf16_f32 v76,v76,v92\nv_cvt_pk_bf16_f32 v77,v77,v93\nM v[108:111],a[156:159],v[208:211],v[108:111]\nv_cvt_pk_bf16_f32 v78,v78,v94\ns_waitcnt lgkmcnt(0)\n" \
"M v[120:123],a[144:147],v[196:199],v[120:123]\nv_cvt_pk_bf16_f32 v79,v79,v95\nv_min_u32 v26,-2,v72\nM v[120:123],a[152:155],v[212:215],v[120:123]\nv_min_u32 v58,-2,v73\nv_min_u32 v88,-2,v10\nM v[124:127],a[148:151],v[196:199],v[124:127]\nv_min_u32 v89,-2,v42\n" \
"v_min_u32 v90,-2,v74\nM v[124:127],a[156:159],v[212:215],v[124:127]\nv_min_u32 v29,-2,v44\nv_min_u32 v30,-2,v76\nv_cvt_pk_bf16_f32 v104,v104,v120\nv_cvt_pk_bf16_f32 v105,v105,v121\nv_cvt_pk_bf16_f32 v106,v106,v122\nv_cvt_pk_bf16_f32 v107,v107,v123\n" \
"v_min_u32 v27,-2,v104\nv_min_u32 v59,-2,v105\nv_cvt_pk_bf16_f32 v108,v108,v124\nv_cvt_pk_bf16_f32 v109,v109,v125\nv_cvt_pk_bf16_f32 v110,v110,v126\nv_cvt_pk_bf16_f32 v111,v111,v127\nv_min_u32 v91,-2,v106\nv_min_u32 v120,-2,v11\n" \
"v_min_u32 v121,-2,v43\nv_min_u32 v122,-2,v75\nv_min_u32 v123,-2,v107\nv_min_u32 v31,-2,v108\nv_min_u32 v60,-2,v13\nv_min_u32 v61,-2,v45\nv_min_u32 v62,-2,v77\nv_min_u32 v63,-2,v109\n" \
"v_min_u32 v92,-2,v14\nv_min_u32 v93,-2,v46\nv_min_u32 v94,-2,v78\nv_min_u32 v95,-2,v110\nv_min_u32 v124,-2,v15\nv_min_u32 v125,-2,v47\nv_min_u32 v126,-2,v79\nv_min_u32 v127,-2,v111\n" \
"buffer_store_dwordx4 v[24:27],v253,s[52:55],s78 offen offset:0 sc1\nbuffer_store_dwordx4 v[56:59],v253,s[52:55],s78 offen offset:1024 sc1\nbuffer_store_dwordx4 v[88:91],v253,s[52:55],s78 offen offset:2048 sc1\nbuffer_store_dwordx4 v[120:123],v253,s[52:55],s78 offen offset:3072 sc1\nbuffer_store_dwordx4 v[28:31],v253,s[52:55],s38 offen offset:0 sc1\nbuffer_store_dwordx4 v[60:63],v253,s[52:55],s38 offen offset:1024 sc1\nbuffer_store_dwordx4 v[92:95],v253,s[52:55],s38 offen offset:2048 sc1\nbuffer_store_dwordx4 v[124:127],v253,s[52:55],s38 offen offset:3072 sc1\n" \
".Ltail:\ns_add_u32 s72,s95,0\nbuffer_load_dwordx4 v[160:163],v240,s[52:55],s72 offen sc1\ns_add_u32 s72,s95,2097152\nbuffer_load_dwordx4 v[164:167],v240,s[52:55],s72 offen sc1\ns_add_u32 s72,s95,4194304\nbuffer_load_dwordx4 v[168:171],v240,s[52:55],s72 offen sc1\ns_add_u32 s72,s95,6291456\n" \
"buffer_load_dwordx4 v[172:175],v240,s[52:55],s72 offen sc1\ns_add_u32 s72,s95,8388608\nbuffer_load_dwordx4 v[176:179],v240,s[52:55],s72 offen sc1\ns_add_u32 s72,s95,10485760\nbuffer_load_dwordx4 v[180:183],v240,s[52:55],s72 offen sc1\ns_add_u32 s72,s95,12582912\nbuffer_load_dwordx4 v[184:187],v240,s[52:55],s72 offen sc1\ns_add_u32 s72,s95,14680064\n" \
"buffer_load_dwordx4 v[188:191],v240,s[52:55],s72 offen sc1\n.Ltail2:\ns_add_u32 s72,s74,0\nbuffer_load_dwordx4 v[128:131],v240,s[52:55],s72 offen sc1\ns_add_u32 s72,s74,2097152\nbuffer_load_dwordx4 v[132:135],v240,s[52:55],s72 offen sc1\ns_add_u32 s72,s74,4194304\nbuffer_load_dwordx4 v[136:139],v240,s[52:55],s72 offen sc1\n" \
"s_add_u32 s72,s74,6291456\nbuffer_load_dwordx4 v[140:143],v240,s[52:55],s72 offen sc1\ns_add_u32 s72,s74,8388608\nbuffer_load_dwordx4 v[144:147],v240,s[52:55],s72 offen sc1\ns_add_u32 s72,s74,10485760\nbuffer_load_dwordx4 v[148:151],v240,s[52:55],s72 offen sc1\ns_add_u32 s72,s74,12582912\nbuffer_load_dwordx4 v[152:155],v240,s[52:55],s72 offen sc1\n" \
"s_add_u32 s72,s74,14680064\nbuffer_load_dwordx4 v[156:159],v240,s[52:55],s72 offen sc1\ns_mov_b32 s94,0\ns_mov_b32 s29,1\ns_cmp_eq_u32 s3,0\ns_cbranch_scc1 .Lnp\ns_mov_b32 s70,0x05040100\ns_mov_b32 s71,0x07060302\n" \
"v_accvgpr_read_b32 v228,a212\nv_accvgpr_read_b32 v229,a213\nv_accvgpr_read_b32 v230,a214\nv_accvgpr_read_b32 v231,a215\nv_perm_b32 v192,v230,v228,s70\nv_perm_b32 v193,v230,v228,s71\nv_perm_b32 v194,v231,v229,s70\nv_perm_b32 v195,v231,v229,s71\n" \
"v_accvgpr_read_b32 v228,a216\nv_accvgpr_read_b32 v229,a217\nv_accvgpr_read_b32 v230,a218\nv_accvgpr_read_b32 v231,a219\nv_perm_b32 v196,v230,v228,s70\nv_perm_b32 v197,v230,v228,s71\nv_perm_b32 v198,v231,v229,s70\nv_perm_b32 v199,v231,v229,s71\n" \
"v_accvgpr_read_b32 v228,a220\nv_accvgpr_read_b32 v229,a221\nv_accvgpr_read_b32 v230,a222\nv_accvgpr_read_b32 v231,a223\nv_perm_b32 v200,v230,v228,s70\nv_perm_b32 v201,v230,v228,s71\nv_perm_b32 v202,v231,v229,s70\nv_perm_b32 v203,v231,v229,s71\n" \
"v_accvgpr_read_b32 v228,a224\nv_accvgpr_read_b32 v229,a225\nv_accvgpr_read_b32 v230,a226\nv_accvgpr_read_b32 v231,a227\nv_perm_b32 v204,v230,v228,s70\nv_perm_b32 v205,v230,v228,s71\nv_perm_b32 v206,v231,v229,s70\nv_perm_b32 v207,v231,v229,s71\n" \
"v_accvgpr_read_b32 v228,a228\nv_accvgpr_read_b32 v229,a229\nv_accvgpr_read_b32 v230,a230\nv_accvgpr_read_b32 v231,a231\nv_perm_b32 v208,v230,v228,s70\nv_perm_b32 v209,v230,v228,s71\nv_perm_b32 v210,v231,v229,s70\nv_perm_b32 v211,v231,v229,s71\n" \
"v_accvgpr_read_b32 v228,a232\nv_accvgpr_read_b32 v229,a233\nv_accvgpr_read_b32 v230,a234\nv_accvgpr_read_b32 v231,a235\nv_perm_b32 v212,v230,v228,s70\nv_perm_b32 v213,v230,v228,s71\nv_perm_b32 v214,v231,v229,s70\nv_perm_b32 v215,v231,v229,s71\n" \
"v_accvgpr_read_b32 v228,a236\nv_accvgpr_read_b32 v229,a237\nv_accvgpr_read_b32 v230,a238\nv_accvgpr_read_b32 v231,a239\nv_perm_b32 v216,v230,v228,s70\nv_perm_b32 v217,v230,v228,s71\nv_perm_b32 v218,v231,v229,s70\nv_perm_b32 v219,v231,v229,s71\n" \
"v_accvgpr_read_b32 v228,a240\nv_accvgpr_read_b32 v229,a241\nv_accvgpr_read_b32 v230,a242\nv_accvgpr_read_b32 v231,a243\nv_perm_b32 v220,v230,v228,s70\nv_perm_b32 v221,v230,v228,s71\nv_perm_b32 v222,v231,v229,s70\nv_perm_b32 v223,v231,v229,s71\n" \
"v_and_b32 v228,3,v249\nv_lshrrev_b32 v229,4,v249\nv_and_b32 v230,1,v229\nv_lshrrev_b32 v229,1,v229\nv_lshlrev_b32 v230,5,v230\nv_lshl_or_b32 v230,v228,6,v230\nv_lshl_or_b32 v230,v229,8,v230\ns_lshl2_add_u32 s70,s7,83008\n" \
"v_add_u32 v252,s70,v230\nv_and_b32 v230,0xf0,v240\nv_add_u32 v253,83008,v230\n.Lnp:\n.Lrow1:\ns_waitcnt vmcnt(15)\nM v[72:75],a[192:195],v[160:163],0\nM v[76:79],a[196:199],v[160:163],0\n" \
"s_waitcnt vmcnt(14)\nM v[72:75],a[192:195],v[164:167],v[72:75]\nM v[76:79],a[196:199],v[164:167],v[76:79]\ns_waitcnt vmcnt(13)\nM v[72:75],a[192:195],v[168:171],v[72:75]\nM v[76:79],a[196:199],v[168:171],v[76:79]\ns_waitcnt vmcnt(12)\nM v[72:75],a[192:195],v[172:175],v[72:75]\n" \
"M v[76:79],a[196:199],v[172:175],v[76:79]\ns_waitcnt vmcnt(11)\nM v[72:75],a[192:195],v[176:179],v[72:75]\nM v[76:79],a[196:199],v[176:179],v[76:79]\ns_waitcnt vmcnt(10)\nM v[72:75],a[192:195],v[180:183],v[72:75]\nM v[76:79],a[196:199],v[180:183],v[76:79]\ns_waitcnt vmcnt(9)\n" \
"M v[72:75],a[192:195],v[184:187],v[72:75]\nM v[76:79],a[196:199],v[184:187],v[76:79]\ns_waitcnt vmcnt(8)\nM v[72:75],a[192:195],v[188:191],v[72:75]\nM v[76:79],a[196:199],v[188:191],v[76:79]\ns_sub_u32 s38,56622112,s95\ns_lshr_b32 s38,s38,5\ns_nop 4\n" \
"v_cvt_pk_bf16_f32 v72,0,v72\nv_cvt_pk_bf16_f32 v73,0,v73\nv_cvt_pk_bf16_f32 v74,0,v74\nv_cvt_pk_bf16_f32 v75,0,v75\nv_cvt_pk_bf16_f32 v76,0,v76\nv_cvt_pk_bf16_f32 v77,0,v77\nv_cvt_pk_bf16_f32 v78,0,v78\nv_cvt_pk_bf16_f32 v79,0,v79\n" \
"v_pk_mul_f32 v[72:73],v[72:73],v[0:1]\nv_pk_mul_f32 v[74:75],v[74:75],v[2:3]\nv_pk_mul_f32 v[76:77],v[76:77],v[4:5]\nv_pk_mul_f32 v[78:79],v[78:79],v[6:7]\nv_cvt_pk_bf16_f32 v72,0,v72\nv_cvt_pk_bf16_f32 v73,0,v73\nv_cvt_pk_bf16_f32 v74,0,v74\nv_cvt_pk_bf16_f32 v75,0,v75\n" \
"v_cvt_pk_bf16_f32 v76,0,v76\nv_cvt_pk_bf16_f32 v77,0,v77\nv_cvt_pk_bf16_f32 v78,0,v78\nv_cvt_pk_bf16_f32 v79,0,v79\nv_pk_add_f32 v[72:73],v[72:73],v[64:65]\nv_pk_add_f32 v[74:75],v[74:75],v[66:67]\nv_pk_add_f32 v[76:77],v[76:77],v[68:69]\nv_pk_add_f32 v[78:79],v[78:79],v[70:71]\n" \
"v_cvt_pk_bf16_f32 v72,0,v72\nv_cvt_pk_bf16_f32 v73,0,v73\nv_cvt_pk_bf16_f32 v74,0,v74\nv_cvt_pk_bf16_f32 v75,0,v75\nv_cvt_pk_bf16_f32 v76,0,v76\nv_cvt_pk_bf16_f32 v77,0,v77\nv_cvt_pk_bf16_f32 v78,0,v78\nv_cvt_pk_bf16_f32 v79,0,v79\n" \
"v_readfirstlane_b32 s70,v72\ns_nop 1\nv_subrev_f32 v104,s70,v72\nv_subrev_f32 v105,s70,v73\nv_subrev_f32 v106,s70,v74\nv_subrev_f32 v107,s70,v75\nv_subrev_f32 v108,s70,v76\nv_subrev_f32 v109,s70,v77\n" \
"v_subrev_f32 v110,s70,v78\nv_subrev_f32 v111,s70,v79\nv_pk_mul_f32 v[124:125],v[104:105],v[104:105]\nv_pk_add_f32 v[120:121],v[104:105],v[106:107]\nv_pk_fma_f32 v[124:125],v[106:107],v[106:107],v[124:125]\nv_pk_fma_f32 v[124:125],v[108:109],v[108:109],v[124:125]\nv_pk_fma_f32 v[124:125],v[110:111],v[110:111],v[124:125]\nv_pk_add_f32 v[108:109],v[108:109],v[110:111]\n" \
"v_pk_add_f32 v[120:121],v[120:121],v[108:109]\nv_add_f32 v120,v120,v121\nv_add_f32 v121,v124,v125\ns_nop 1\nv_permlane16_swap_b32 v120,v121\nv_add_f32 v120,v120,v121\nv_mov_b32 v121,v120\ns_nop 1\n" \
"v_permlane32_swap_b32 v120,v121\nv_add_f32 v120,v120,v121\ns_nop 1\nv_add_f32_dpp v120,v120,v120 quad_perm:[1,0,3,2] row_mask:0xf bank_mask:0xf\ns_nop 1\nv_add_f32_dpp v120,v120,v120 quad_perm:[2,3,0,1] row_mask:0xf bank_mask:0xf\ns_nop 1\nv_add_f32_dpp v120,v120,v120 row_half_mirror row_mask:0xf bank_mask:0xf\n" \
"s_nop 1\nv_add_f32_dpp v120,v120,v120 row_mirror row_mask:0xf bank_mask:0xf\nv_mul_f32 v120,0x3b000000,v120\nv_mov_b32 v121,v120\nv_cmp_u_f32 vcc,v120,v120\ns_nop 0\nv_permlane16_swap_b32 v120,v121\ns_cbranch_vccnz .Lrs1\n" \
".Lrb1:\nv_fma_f32 v121,-v120,v120,v121\nv_add_f32 v122,s70,v120\nv_max_f32 v121,0,v121\nv_cvt_pk_bf16_f32 v122,0,v122\nv_cvt_pk_bf16_f32 v121,0,v121\nv_add_f32 v121,0x3727c5ac,v121\nv_cvt_pk_bf16_f32 v121,0,v121\n" \
"v_sqrt_f32 v121,v121\nv_pk_add_f32 v[88:89],v[72:73],v[122:123] op_sel_hi:[1,0] neg_lo:[0,1] neg_hi:[0,1]\nv_cvt_pk_bf16_f32 v121,0,v121\nv_rcp_f32 v123,v121\nv_cvt_pk_bf16_f32 v88,0,v88\nv_pk_add_f32 v[90:91],v[74:75],v[122:123] op_sel_hi:[1,0] neg_lo:[0,1] neg_hi:[0,1]\nv_pk_add_f32 v[92:93],v[76:77],v[122:123] op_sel_hi:[1,0] neg_lo:[0,1] neg_hi:[0,1]\nv_pk_add_f32 v[94:95],v[78:79],v[122:123] op_sel_hi:[1,0] neg_lo:[0,1] neg_hi:[0,1]\n" \
"v_cvt_pk_bf16_f32 v89,0,v89\nv_pk_mul_f32 v[88:89],v[88:89],v[122:123] op_sel:[0,1]\nv_cvt_pk_bf16_f32 v88,0,v88\nv_cvt_pk_bf16_f32 v89,0,v89\nv_pk_mul_f32 v[88:89],v[88:89],v[32:33]\nv_cvt_pk_bf16_f32 v88,0,v88\nv_cvt_pk_bf16_f32 v89,0,v89\nv_pk_add_f32 v[88:89],v[88:89],v[16:17]\n" \
"v_cvt_pk_bf16_f32 v88,0,v88\nv_cvt_pk_bf16_f32 v89,0,v89\nv_pk_mul_f32 v[124:125],v[88:89],v[96:97]\nv_cvt_pk_bf16_f32 v90,0,v90\nv_cvt_pk_bf16_f32 v91,0,v91\nv_pk_mul_f32 v[90:91],v[90:91],v[122:123] op_sel:[0,1]\nv_cvt_pk_bf16_f32 v90,0,v90\nv_cvt_pk_bf16_f32 v91,0,v91\n" \
"v_pk_mul_f32 v[90:91],v[90:91],v[34:35]\nv_cvt_pk_bf16_f32 v90,0,v90\nv_cvt_pk_bf16_f32 v91,0,v91\nv_pk_add_f32 v[90:91],v[90:91],v[18:19]\nv_cvt_pk_bf16_f32 v90,0,v90\nv_cvt_pk_bf16_f32 v91,0,v91\nv_pk_fma_f32 v[124:125],v[90:91],v[98:99],v[124:125]\nv_cvt_pk_bf16_f32 v92,0,v92\n" \
"v_cvt_pk_bf16_f32 v93,0,v93\nv_pk_mul_f32 v[92:93],v[92:93],v[122:123] op_sel:[0,1]\nv_cvt_pk_bf16_f32 v92,0,v92\nv_cvt_pk_bf16_f32 v93,0,v93\nv_pk_mul_f32 v[92:93],v[92:93],v[36:37]\nv_cvt_pk_bf16_f32 v92,0,v92\nv_cvt_pk_bf16_f32 v93,0,v93\nv_pk_add_f32 v[92:93],v[92:93],v[20:21]\n" \
"v_cvt_pk_bf16_f32 v92,0,v92\nv_cvt_pk_bf16_f32 v93,0,v93\nv_pk_fma_f32 v[124:125],v[92:93],v[100:101],v[124:125]\nv_cvt_pk_bf16_f32 v94,0,v94\nv_cvt_pk_bf16_f32 v95,0,v95\nv_pk_mul_f32 v[94:95],v[94:95],v[122:123] op_sel:[0,1]\nv_cvt_pk_bf16_f32 v94,0,v94\nv_cvt_pk_bf16_f32 v95,0,v95\n" \
"v_pk_mul_f32 v[94:95],v[94:95],v[38:39]\nv_cvt_pk_bf16_f32 v94,0,v94\nv_cvt_pk_bf16_f32 v95,0,v95\nv_pk_add_f32 v[94:95],v[94:95],v[22:23]\nv_cvt_pk_bf16_f32 v94,0,v94\nv_cvt_pk_bf16_f32 v95,0,v95\nv_pk_fma_f32 v[124:125],v[94:95],v[102:103],v[124:125]\nv_add_f32 v124,v124,v125\n" \
"v_mov_b32 v125,v124\nv_lshrrev_b32 v104,16,v121\nv_or_b32 v104,v104,v122\nv_permlane16_swap_b32 v124,v125\nv_add_f32 v124,v124,v125\nv_mov_b32 v125,v124\ns_nop 1\nv_permlane32_swap_b32 v124,v125\n" \
"v_add_f32 v124,v124,v125\ns_nop 1\nv_add_f32_dpp v124,v124,v124 quad_perm:[1,0,3,2] row_mask:0xf bank_mask:0xf\ns_nop 1\nv_add_f32_dpp v124,v124,v124 quad_perm:[2,3,0,1] row_mask:0xf bank_mask:0xf\ns_nop 1\nv_add_f32_dpp v124,v124,v124 row_half_mirror row_mask:0xf bank_mask:0xf\ns_nop 1\n" \
"v_add_f32_dpp v124,v124,v124 row_mirror row_mask:0xf bank_mask:0xf\nv_cvt_pk_bf16_f32 v124,0,v124\ns_nop 0\nv_readfirstlane_b32 s77,v124\ns_nop 0\ns_ashr_i32 s71,s77,31\ns_and_b32 s72,s77,0x7fffffff\ns_or_b32 s71,s71,0x80000000\n" \
"s_xor_b32 s77,s77,s71\ns_and_b32 s77,s77,0xffff0000\ns_cmp_gt_u32 s72,0x7f800000\ns_cselect_b32 s77,0,s77\ns_or_b32 s77,s77,s38\nv_mov_b32 v105,s77\nds_max_u64 v250,v[104:105]\n.Lrow0:\n" \
"s_waitcnt vmcnt(7)\nM v[8:11],a[192:195],v[128:131],0\nM v[12:15],a[196:199],v[128:131],0\ns_waitcnt vmcnt(6)\nM v[8:11],a[192:195],v[132:135],v[8:11]\nM v[12:15],a[196:199],v[132:135],v[12:15]\ns_waitcnt vmcnt(5)\nM v[8:11],a[192:195],v[136:139],v[8:11]\n" \
"M v[12:15],a[196:199],v[136:139],v[12:15]\ns_waitcnt vmcnt(4)\nM v[8:11],a[192:195],v[140:143],v[8:11]\nM v[12:15],a[196:199],v[140:143],v[12:15]\ns_waitcnt vmcnt(3)\nM v[8:11],a[192:195],v[144:147],v[8:11]\nM v[12:15],a[196:199],v[144:147],v[12:15]\ns_waitcnt vmcnt(2)\n" \
"M v[8:11],a[192:195],v[148:151],v[8:11]\nM v[12:15],a[196:199],v[148:151],v[12:15]\ns_waitcnt vmcnt(1)\nM v[8:11],a[192:195],v[152:155],v[8:11]\nM v[12:15],a[196:199],v[152:155],v[12:15]\ns_waitcnt vmcnt(0)\nM v[8:11],a[192:195],v[156:159],v[8:11]\nM v[12:15],a[196:199],v[156:159],v[12:15]\n" \
"s_sub_u32 s39,56622112,s74\ns_lshr_b32 s39,s39,5\ns_nop 4\nv_cvt_pk_bf16_f32 v8,0,v8\nv_cvt_pk_bf16_f32 v9,0,v9\nv_cvt_pk_bf16_f32 v10,0,v10\nv_cvt_pk_bf16_f32 v11,0,v11\nv_cvt_pk_bf16_f32 v12,0,v12\n" \
"v_cvt_pk_bf16_f32 v13,0,v13\nv_cvt_pk_bf16_f32 v14,0,v14\nv_cvt_pk_bf16_f32 v15,0,v15\nv_pk_mul_f32 v[8:9],v[8:9],v[0:1]\nv_pk_mul_f32 v[10:11],v[10:11],v[2:3]\nv_pk_mul_f32 v[12:13],v[12:13],v[4:5]\nv_pk_mul_f32 v[14:15],v[14:15],v[6:7]\nv_cvt_pk_bf16_f32 v8,0,v8\n" \
"v_cvt_pk_bf16_f32 v9,0,v9\nv_cvt_pk_bf16_f32 v10,0,v10\nv_cvt_pk_bf16_f32 v11,0,v11\nv_cvt_pk_bf16_f32 v12,0,v12\nv_cvt_pk_bf16_f32 v13,0,v13\nv_cvt_pk_bf16_f32 v14,0,v14\nv_cvt_pk_bf16_f32 v15,0,v15\nv_pk_add_f32 v[8:9],v[8:9],v[80:81]\n" \
"v_pk_add_f32 v[10:11],v[10:11],v[82:83]\nv_pk_add_f32 v[12:13],v[12:13],v[84:85]\nv_pk_add_f32 v[14:15],v[14:15],v[86:87]\nv_cvt_pk_bf16_f32 v8,0,v8\nv_cvt_pk_bf16_f32 v9,0,v9\nv_cvt_pk_bf16_f32 v10,0,v10\nv_cvt_pk_bf16_f32 v11,0,v11\nv_cvt_pk_bf16_f32 v12,0,v12\n" \
"v_cvt_pk_bf16_f32 v13,0,v13\nv_cvt_pk_bf16_f32 v14,0,v14\nv_cvt_pk_bf16_f32 v15,0,v15\nv_readfirstlane_b32 s70,v8\ns_nop 1\nv_subrev_f32 v40,s70,v8\nv_subrev_f32 v41,s70,v9\nv_subrev_f32 v42,s70,v10\n" \
"v_subrev_f32 v43,s70,v11\nv_subrev_f32 v44,s70,v12\nv_subrev_f32 v45,s70,v13\nv_subrev_f32 v46,s70,v14\nv_subrev_f32 v47,s70,v15\nv_pk_mul_f32 v[60:61],v[40:41],v[40:41]\nv_pk_add_f32 v[56:57],v[40:41],v[42:43]\nv_pk_fma_f32 v[60:61],v[42:43],v[42:43],v[60:61]\n" \
"v_pk_fma_f32 v[60:61],v[44:45],v[44:45],v[60:61]\nv_pk_fma_f32 v[60:61],v[46:47],v[46:47],v[60:61]\nv_pk_add_f32 v[44:45],v[44:45],v[46:47]\nv_pk_add_f32 v[56:57],v[56:57],v[44:45]\nv_add_f32 v56,v56,v57\nv_add_f32 v57,v60,v61\ns_nop 1\nv_permlane16_swap_b32 v56,v57\n" \
"v_add_f32 v56,v56,v57\nv_mov_b32 v57,v56\ns_nop 1\nv_permlane32_swap_b32 v56,v57\nv_add_f32 v56,v56,v57\ns_nop 1\nv_add_f32_dpp v56,v56,v56 quad_perm:[1,0,3,2] row_mask:0xf bank_mask:0xf\ns_nop 1\n" \
"v_add_f32_dpp v56,v56,v56 quad_perm:[2,3,0,1] row_mask:0xf bank_mask:0xf\ns_nop 1\nv_add_f32_dpp v56,v56,v56 row_half_mirror row_mask:0xf bank_mask:0xf\ns_nop 1\nv_add_f32_dpp v56,v56,v56 row_mirror row_mask:0xf bank_mask:0xf\nv_mul_f32 v56,0x3b000000,v56\nv_mov_b32 v57,v56\nv_cmp_u_f32 vcc,v56,v56\n" \
"s_nop 0\nv_permlane16_swap_b32 v56,v57\ns_cbranch_vccnz .Lrs0\n.Lrb0:\nbuffer_load_dwordx4 v[232:235],v251,s[52:55],s80 offen sc1\nbuffer_load_dwordx4 v[236:239],v251,s[52:55],s80 offen offset:16 sc1\nv_fma_f32 v57,-v56,v56,v57\nv_add_f32 v58,s70,v56\n" \
"v_max_f32 v57,0,v57\nv_cvt_pk_bf16_f32 v58,0,v58\nv_cvt_pk_bf16_f32 v57,0,v57\nv_add_f32 v57,0x3727c5ac,v57\nv_cvt_pk_bf16_f32 v57,0,v57\nv_sqrt_f32 v57,v57\nv_pk_add_f32 v[24:25],v[8:9],v[58:59] op_sel_hi:[1,0] neg_lo:[0,1] neg_hi:[0,1]\nv_cvt_pk_bf16_f32 v57,0,v57\n" \
"v_rcp_f32 v59,v57\nv_cvt_pk_bf16_f32 v24,0,v24\nv_pk_add_f32 v[26:27],v[10:11],v[58:59] op_sel_hi:[1,0] neg_lo:[0,1] neg_hi:[0,1]\nv_pk_add_f32 v[28:29],v[12:13],v[58:59] op_sel_hi:[1,0] neg_lo:[0,1] neg_hi:[0,1]\nv_pk_add_f32 v[30:31],v[14:15],v[58:59] op_sel_hi:[1,0] neg_lo:[0,1] neg_hi:[0,1]\nv_cvt_pk_bf16_f32 v25,0,v25\nv_pk_mul_f32 v[24:25],v[24:25],v[58:59] op_sel:[0,1]\nv_cvt_pk_bf16_f32 v24,0,v24\n" \
"v_cvt_pk_bf16_f32 v25,0,v25\nv_pk_mul_f32 v[24:25],v[24:25],v[32:33]\nv_cvt_pk_bf16_f32 v24,0,v24\nv_cvt_pk_bf16_f32 v25,0,v25\nv_pk_add_f32 v[24:25],v[24:25],v[16:17]\nv_cvt_pk_bf16_f32 v24,0,v24\nv_cvt_pk_bf16_f32 v25,0,v25\nv_pk_mul_f32 v[60:61],v[24:25],v[96:97]\n" \
"v_cvt_pk_bf16_f32 v26,0,v26\nv_cvt_pk_bf16_f32 v27,0,v27\nv_pk_mul_f32 v[26:27],v[26:27],v[58:59] op_sel:[0,1]\nv_cvt_pk_bf16_f32 v26,0,v26\nv_cvt_pk_bf16_f32 v27,0,v27\nv_pk_mul_f32 v[26:27],v[26:27],v[34:35]\nv_cvt_pk_bf16_f32 v26,0,v26\nv_cvt_pk_bf16_f32 v27,0,v27\n" \
"v_pk_add_f32 v[26:27],v[26:27],v[18:19]\nv_cvt_pk_bf16_f32 v26,0,v26\nv_cvt_pk_bf16_f32 v27,0,v27\nv_pk_fma_f32 v[60:61],v[26:27],v[98:99],v[60:61]\nv_cvt_pk_bf16_f32 v28,0,v28\nv_cvt_pk_bf16_f32 v29,0,v29\nv_pk_mul_f32 v[28:29],v[28:29],v[58:59] op_sel:[0,1]\nv_cvt_pk_bf16_f32 v28,0,v28\n" \
"v_cvt_pk_bf16_f32 v29,0,v29\nv_pk_mul_f32 v[28:29],v[28:29],v[36:37]\nv_cvt_pk_bf16_f32 v28,0,v28\nv_cvt_pk_bf16_f32 v29,0,v29\nv_pk_add_f32 v[28:29],v[28:29],v[20:21]\nv_cvt_pk_bf16_f32 v28,0,v28\nv_cvt_pk_bf16_f32 v29,0,v29\nv_pk_fma_f32 v[60:61],v[28:29],v[100:101],v[60:61]\n" \
"v_cvt_pk_bf16_f32 v30,0,v30\nv_cvt_pk_bf16_f32 v31,0,v31\nv_pk_mul_f32 v[30:31],v[30:31],v[58:59] op_sel:[0,1]\nv_cvt_pk_bf16_f32 v30,0,v30\nv_cvt_pk_bf16_f32 v31,0,v31\nv_pk_mul_f32 v[30:31],v[30:31],v[38:39]\nv_cvt_pk_bf16_f32 v30,0,v30\nv_cvt_pk_bf16_f32 v31,0,v31\n" \
"v_pk_add_f32 v[30:31],v[30:31],v[22:23]\nv_cvt_pk_bf16_f32 v30,0,v30\nv_cvt_pk_bf16_f32 v31,0,v31\nv_pk_fma_f32 v[60:61],v[30:31],v[102:103],v[60:61]\nv_add_f32 v60,v60,v61\nv_mov_b32 v61,v60\nv_lshrrev_b32 v40,16,v57\nv_or_b32 v40,v40,v58\n" \
"v_permlane16_swap_b32 v60,v61\nv_add_f32 v60,v60,v61\nv_mov_b32 v61,v60\ns_nop 1\nv_permlane32_swap_b32 v60,v61\nv_add_f32 v60,v60,v61\ns_nop 1\nv_add_f32_dpp v60,v60,v60 quad_perm:[1,0,3,2] row_mask:0xf bank_mask:0xf\n" \
"s_nop 1\nv_add_f32_dpp v60,v60,v60 quad_perm:[2,3,0,1] row_mask:0xf bank_mask:0xf\ns_nop 1\nv_add_f32_dpp v60,v60,v60 row_half_mirror row_mask:0xf bank_mask:0xf\ns_nop 1\nv_add_f32_dpp v60,v60,v60 row_mirror row_mask:0xf bank_mask:0xf\nv_cvt_pk_bf16_f32 v60,0,v60\ns_nop 0\n" \
"v_readfirstlane_b32 s76,v60\ns_nop 0\ns_ashr_i32 s71,s76,31\ns_and_b32 s72,s76,0x7fffffff\ns_or_b32 s71,s71,0x80000000\ns_xor_b32 s76,s76,s71\ns_and_b32 s76,s76,0xffff0000\ns_cmp_gt_u32 s72,0x7f800000\n" \
"s_cselect_b32 s76,0,s76\ns_or_b32 s76,s76,s39\nv_mov_b32 v41,s76\nds_max_u64 v250,v[40:41]\ns_waitcnt lgkmcnt(0)\ns_barrier\nds_read_b64 v[224:225],v250\ns_waitcnt lgkmcnt(0)\n" \
"buffer_store_dwordx2 v[224:225],v246,s[52:55],s37 offen sc1\nv_readfirstlane_b32 s97,v225\ns_sub_u32 s95,s95,54525952\ns_sub_u32 s74,s74,54525952\ns_mov_b32 s29,0\ns_cmp_eq_u32 s97,s77\ns_cselect_b32 s65,s95,0x40000000\ns_cselect_b32 s95,0x40000000,s95\n" \
"s_cmp_eq_u32 s97,s76\ns_cselect_b32 s66,s74,0x40000000\ns_cselect_b32 s74,0x40000000,s74\ns_lshr_b32 s70,s6,7\ns_or_b32 s38,s7,s70\ns_cmp_lg_u32 s3,0\ns_waitcnt vmcnt(1)\ns_cbranch_scc1 .Lkp\n" \
"v_pk_mul_f32 v[88:89],v[88:89],v[48:49]\nv_pk_mul_f32 v[90:91],v[90:91],v[50:51]\nv_pk_mul_f32 v[92:93],v[92:93],v[52:53]\nv_pk_mul_f32 v[94:95],v[94:95],v[54:55]\nv_cvt_pk_bf16_f32 v88,0,v88\nv_cvt_pk_bf16_f32 v89,0,v89\nv_cvt_pk_bf16_f32 v90,0,v90\nv_cvt_pk_bf16_f32 v91,0,v91\n" \
"v_cvt_pk_bf16_f32 v92,0,v92\nv_cvt_pk_bf16_f32 v93,0,v93\nv_cvt_pk_bf16_f32 v94,0,v94\nv_cvt_pk_bf16_f32 v95,0,v95\nv_pk_add_f32 v[88:89],v[88:89],v[72:73]\nv_pk_add_f32 v[90:91],v[90:91],v[74:75]\nv_pk_add_f32 v[92:93],v[92:93],v[76:77]\nv_pk_add_f32 v[94:95],v[94:95],v[78:79]\n" \
"v_cvt_pk_bf16_f32 v88,0,v88\nds_write_b16_d16_hi v244,v88 offset:0\nv_cvt_pk_bf16_f32 v89,0,v89\nds_write_b16_d16_hi v244,v89 offset:32\nv_cvt_pk_bf16_f32 v90,0,v90\nds_write_b16_d16_hi v244,v90 offset:64\nv_cvt_pk_bf16_f32 v91,0,v91\nds_write_b16_d16_hi v244,v91 offset:96\n" \
"v_cvt_pk_bf16_f32 v92,0,v92\nds_write_b16_d16_hi v244,v92 offset:128\nv_cvt_pk_bf16_f32 v93,0,v93\nds_write_b16_d16_hi v244,v93 offset:160\nv_cvt_pk_bf16_f32 v94,0,v94\nds_write_b16_d16_hi v244,v94 offset:192\nv_cvt_pk_bf16_f32 v95,0,v95\nds_write_b16_d16_hi v244,v95 offset:224\n" \
"ds_read_b128 v[64:67],v245 offset:0\nv_pk_mul_f32 v[24:25],v[24:25],v[48:49]\nv_pk_mul_f32 v[26:27],v[26:27],v[50:51]\nv_pk_mul_f32 v[28:29],v[28:29],v[52:53]\nv_pk_mul_f32 v[30:31],v[30:31],v[54:55]\nv_cvt_pk_bf16_f32 v24,0,v24\nv_cvt_pk_bf16_f32 v25,0,v25\nv_cvt_pk_bf16_f32 v26,0,v26\n" \
"v_cvt_pk_bf16_f32 v27,0,v27\nv_cvt_pk_bf16_f32 v28,0,v28\nv_cvt_pk_bf16_f32 v29,0,v29\nv_cvt_pk_bf16_f32 v30,0,v30\nv_cvt_pk_bf16_f32 v31,0,v31\nv_pk_add_f32 v[24:25],v[24:25],v[8:9]\nv_pk_add_f32 v[26:27],v[26:27],v[10:11]\nv_pk_add_f32 v[28:29],v[28:29],v[12:13]\n" \
"v_pk_add_f32 v[30:31],v[30:31],v[14:15]\nv_cvt_pk_bf16_f32 v24,0,v24\nds_write_b16_d16_hi v244,v24 offset:1024\nv_cvt_pk_bf16_f32 v25,0,v25\nds_write_b16_d16_hi v244,v25 offset:1056\nv_cvt_pk_bf16_f32 v26,0,v26\nds_write_b16_d16_hi v244,v26 offset:1088\nv_cvt_pk_bf16_f32 v27,0,v27\n" \
"ds_write_b16_d16_hi v244,v27 offset:1120\nv_cvt_pk_bf16_f32 v28,0,v28\nds_write_b16_d16_hi v244,v28 offset:1152\nv_cvt_pk_bf16_f32 v29,0,v29\nds_write_b16_d16_hi v244,v29 offset:1184\nv_cvt_pk_bf16_f32 v30,0,v30\nds_write_b16_d16_hi v244,v30 offset:1216\nv_cvt_pk_bf16_f32 v31,0,v31\n" \
"ds_write_b16_d16_hi v244,v31 offset:1248\nds_read_b128 v[68:71],v245 offset:1024\ns_waitcnt lgkmcnt(0)\nbuffer_store_dwordx4 v[64:67],v240,s[44:47],s95 offen sc1\nbuffer_store_dwordx4 v[68:71],v240,s[44:47],s74 offen sc1\ns_cmp_eq_u32 s7,0\ns_cselect_b32 s70,1,0\ns_xor_b32 s71,s65,s66\n" \
"s_or_b32 s70,s70,s71\ns_cbranch_scc0 .Lfin\ns_lshr_b32 s70,s6,3\ns_and_b32 s70,s70,15\ns_lshl4_add_u32 s98,s60,s70\nv_lshrrev_b32 v242,4,v249\ns_lshl_b32 s71,s98,2\nv_add_u32 v242,s71,v242\n" \
"v_and_b32 v244,15,v242\nv_bfe_u32 v245,v242,4,3\nv_lshl_add_u32 v244,v244,3,v245\nv_and_b32 v245,0x180,v242\nv_add_lshl_u32 v243,v244,v245,1\nv_lshlrev_b32 v244,1,v243\ns_mov_b32 s70,4096\nv_lshlrev_b32 v242,1,v242\n" \
"buffer_load_dword v62,v244,s[52:55],0 offen offset:0 sc1\nbuffer_load_dword v63,v244,s[52:55],0 offen offset:2048 sc1\nbuffer_load_dword v126,v244,s[52:55],s70 offen offset:0 sc1\nbuffer_load_dword v127,v244,s[52:55],s70 offen offset:2048 sc1\n.Lkp:\ns_cmp_lg_u32 s7,0\ns_cbranch_scc1 .Lkl\nv_max3_u32 v228,v233,v235,v237\n" \
"v_min3_u32 v229,v233,v235,v237\nv_max_u32 v228,v228,v239\nv_min_u32 v229,v229,v239\nv_cmp_lt_u32 vcc,s94,v228\nv_cmp_eq_u32 s[90:91],0,v229\ns_cbranch_vccnz .Lnew\ns_cmp_eq_u64 s[90:91],0\ns_cbranch_scc1 .Lv0\n" \
"s_sub_u32 s73,s73,1\ns_cselect_b32 s73,0,s73\ns_cbranch_scc1 .Lv0\ns_sleep 3\nbuffer_load_dwordx4 v[232:235],v251,s[52:55],s80 offen sc1\nbuffer_load_dwordx4 v[236:239],v251,s[52:55],s80 offen offset:16 sc1\ns_waitcnt vmcnt(0)\ns_branch .Lkp\n" \
".Lv0:\nv_mov_b32 v230,s96\ns_or_b32 s72,s94,2\nv_mov_b32 v231,s72\nds_write_b64 v250,v[230:231] offset:8\ns_branch .Lver\n.Lkl:\nds_read_b64 v[230:231],v250 offset:8\n" \
"s_mov_b32 s91,0\ns_waitcnt lgkmcnt(0)\nv_readfirstlane_b32 s72,v231\nv_readfirstlane_b32 s96,v230\ns_and_b32 s90,s72,2\ns_xor_b32 s72,s72,s90\ns_xor_b32 s90,s90,2\ns_cmp_lg_u32 s72,s94\n" \
"s_mov_b32 s94,s72\ns_cbranch_scc1 .Lsp\ns_cmp_eq_u32 s90,0\ns_cbranch_scc1 .Lver\ns_sub_u32 s73,s73,1\ns_cselect_b32 s73,0,s73\ns_cbranch_scc1 .Lver\ns_sleep 1\n" \
".Lyield:\ns_branch .Lkl\n.Lnew:\nv_mov_b32 v230,v228\nv_cmp_eq_u32 vcc,v228,v233\ns_cmp_eq_u64 s[90:91],0\ns_cselect_b32 s71,2,0\nv_max_u32_dpp v230,v230,v230 quad_perm:[1,0,3,2] row_mask:0xf bank_mask:0xf\n" \
"v_cndmask_b32 v229,v238,v232,vcc\nv_cmp_eq_u32 vcc,v228,v235\nv_max_u32_dpp v230,v230,v230 quad_perm:[2,3,0,1] row_mask:0xf bank_mask:0xf\ns_nop 1\nv_max_u32_dpp v230,v230,v230 row_half_mirror row_mask:0xf bank_mask:0xf\nv_cndmask_b32 v229,v229,v234,vcc\nv_cmp_eq_u32 vcc,v228,v237\nv_max_u32_dpp v230,v230,v230 row_mirror row_mask:0xf bank_mask:0xf\n" \
"v_mov_b32 v231,v230\nv_cndmask_b32 v229,v229,v236,vcc\ns_nop 0\nv_permlane16_swap_b32 v230,v231\nv_max_u32 v230,v230,v231\nv_mov_b32 v231,v230\ns_nop 1\nv_permlane32_swap_b32 v230,v231\n" \
"v_max_u32 v230,v230,v231\ns_nop 0\nv_readfirstlane_b32 s94,v230\ns_nop 1\nv_cmp_eq_u32 vcc,s94,v228\ns_nop 0\ns_ff1_i32_b64 s72,vcc\nv_readlane_b32 s96,v229,s72\n" \
"s_nop 1\nv_mov_b32 v230,s96\ns_or_b32 s72,s94,s71\nv_mov_b32 v231,s72\nds_write_b64 v250,v[230:231] offset:8\n.Lsp:\ns_not_b32 s15,s94\ns_bfe_u32 s15,s15,0xb0005\n" \
"s_lshl_b32 s15,s15,10\ns_cmp_eq_u32 s3,0\ns_cbranch_scc1 .Lkp\nv_add_u32 v70,114688,v241\ns_add_u32 s72,s15,54525952\nbuffer_load_dword v41,v241,s[52:55],s72 offen sc1\ns_add_u32 s72,s15,56623104\nbuffer_load_dword v43,v241,s[52:55],s72 offen sc1\n" \
"s_add_u32 s72,s15,58720256\nbuffer_load_dword v45,v241,s[52:55],s72 offen sc1\ns_add_u32 s72,s15,60817408\nbuffer_load_dword v47,v241,s[52:55],s72 offen sc1\ns_add_u32 s72,s15,62914560\nbuffer_load_dword v105,v241,s[52:55],s72 offen sc1\ns_add_u32 s72,s15,65011712\nbuffer_load_dword v107,v241,s[52:55],s72 offen sc1\n" \
"s_add_u32 s72,s15,67108864\nbuffer_load_dword v109,v241,s[52:55],s72 offen sc1\ns_add_u32 s72,s15,69206016\nbuffer_load_dword v111,v241,s[52:55],s72 offen sc1\nv_add_u32 v66,s15,v248\nglobal_load_ushort v64,v66,s[8:9]\nglobal_load_ushort v65,v66,s[8:9] offset:32\ns_nop 0\n" \
"ds_read_b32 v161,v70 offset:0\nds_read_b32 v163,v70 offset:1024\nds_read_b32 v165,v70 offset:2048\nds_read_b32 v167,v70 offset:3072\nds_read_b32 v169,v70 offset:4096\nds_read_b32 v171,v70 offset:5120\nds_read_b32 v173,v70 offset:6144\nds_read_b32 v175,v70 offset:7168\n" \
"ds_read_b32 v177,v70 offset:8192\nds_read_b32 v179,v70 offset:9216\nds_read_b32 v181,v70 offset:10240\nds_read_b32 v183,v70 offset:11264\nds_read_b32 v185,v70 offset:12288\nds_read_b32 v187,v70 offset:13312\nds_read_b32 v189,v70 offset:14336\nds_read_b32 v191,v70 offset:15360\n" \
"s_waitcnt lgkmcnt(15)\nv_lshlrev_b32 v160,16,v161\nv_and_b32 v161,0xffff0000,v161\ns_waitcnt lgkmcnt(14)\nv_lshlrev_b32 v162,16,v163\nv_and_b32 v163,0xffff0000,v163\ns_waitcnt lgkmcnt(13)\nv_lshlrev_b32 v164,16,v165\n" \
"v_and_b32 v165,0xffff0000,v165\ns_waitcnt lgkmcnt(12)\nv_lshlrev_b32 v166,16,v167\nv_and_b32 v167,0xffff0000,v167\ns_waitcnt lgkmcnt(11)\nv_lshlrev_b32 v168,16,v169\nv_and_b32 v169,0xffff0000,v169\ns_waitcnt lgkmcnt(10)\n" \
"v_lshlrev_b32 v170,16,v171\nv_and_b32 v171,0xffff0000,v171\ns_waitcnt lgkmcnt(9)\nv_lshlrev_b32 v172,16,v173\nv_and_b32 v173,0xffff0000,v173\ns_waitcnt lgkmcnt(8)\nv_lshlrev_b32 v174,16,v175\nv_and_b32 v175,0xffff0000,v175\n" \
"s_waitcnt lgkmcnt(7)\nv_lshlrev_b32 v176,16,v177\nv_and_b32 v177,0xffff0000,v177\ns_waitcnt lgkmcnt(6)\nv_lshlrev_b32 v178,16,v179\nv_and_b32 v179,0xffff0000,v179\ns_waitcnt lgkmcnt(5)\nv_lshlrev_b32 v180,16,v181\n" \
"v_and_b32 v181,0xffff0000,v181\ns_waitcnt lgkmcnt(4)\nv_lshlrev_b32 v182,16,v183\nv_and_b32 v183,0xffff0000,v183\ns_waitcnt lgkmcnt(3)\nv_lshlrev_b32 v184,16,v185\nv_and_b32 v185,0xffff0000,v185\ns_waitcnt lgkmcnt(1)\n" \
"v_lshlrev_b32 v186,16,v187\nv_and_b32 v187,0xffff0000,v187\nv_lshlrev_b32 v188,16,v189\nv_and_b32 v189,0xffff0000,v189\ns_waitcnt lgkmcnt(0)\nv_lshlrev_b32 v190,16,v191\nv_and_b32 v191,0xffff0000,v191\ns_waitcnt vmcnt(0)\n" \
"buffer_load_dwordx4 v[232:235],v251,s[52:55],s80 offen sc1\nbuffer_load_dwordx4 v[236:239],v251,s[52:55],s80 offen offset:16 sc1\ns_and_b32 s82,s96,0xffff0000\ns_lshl_b32 s83,s96,16\nv_rcp_f32 v68,s83\nv_lshlrev_b32 v40,16,v41\nv_and_b32 v41,0xffff0000,v41\nv_lshlrev_b32 v42,16,v43\n" \
"v_and_b32 v43,0xffff0000,v43\nv_lshlrev_b32 v44,16,v45\nv_and_b32 v45,0xffff0000,v45\nv_lshlrev_b32 v46,16,v47\nv_and_b32 v47,0xffff0000,v47\nv_lshlrev_b32 v104,16,v105\nv_and_b32 v105,0xffff0000,v105\nv_lshlrev_b32 v106,16,v107\n" \
"v_and_b32 v107,0xffff0000,v107\nv_lshlrev_b32 v108,16,v109\nv_and_b32 v109,0xffff0000,v109\nv_lshlrev_b32 v110,16,v111\nv_and_b32 v111,0xffff0000,v111\nv_pk_add_f32 v[40:41],v[40:41],v[42:43]\nv_pk_add_f32 v[44:45],v[44:45],v[46:47]\nv_pk_add_f32 v[104:105],v[104:105],v[106:107]\n" \
"v_pk_add_f32 v[108:109],v[108:109],v[110:111]\nv_pk_add_f32 v[40:41],v[40:41],v[44:45]\nv_pk_add_f32 v[104:105],v[104:105],v[108:109]\nv_pk_add_f32 v[40:41],v[40:41],v[104:105]\nv_lshlrev_b32 v64,16,v64\nv_lshlrev_b32 v65,16,v65\nv_cvt_pk_bf16_f32 v40,0,v40\nv_cvt_pk_bf16_f32 v41,0,v41\n" \
"v_pk_mul_f32 v[40:41],v[40:41],v[112:113]\nv_cvt_pk_bf16_f32 v40,0,v40\nv_cvt_pk_bf16_f32 v41,0,v41\nv_pk_add_f32 v[40:41],v[40:41],v[64:65]\nv_cvt_pk_bf16_f32 v40,0,v40\nv_cvt_pk_bf16_f32 v41,0,v41\nv_pk_add_f32 v[40:41],v[40:41],s[82:83] op_sel_hi:[1,0] neg_lo:[0,1] neg_hi:[0,1]\nv_cvt_pk_bf16_f32 v40,0,v40\n" \
"v_cvt_pk_bf16_f32 v41,0,v41\nv_pk_mul_f32 v[40:41],v[40:41],v[68:69] op_sel_hi:[1,0]\nv_cvt_pk_bf16_f32 v40,0,v40\nv_cvt_pk_bf16_f32 v41,0,v41\nv_pk_mul_f32 v[40:41],v[40:41],v[116:117]\nv_cvt_pk_bf16_f32 v40,0,v40\nv_cvt_pk_bf16_f32 v41,0,v41\nv_pk_add_f32 v[40:41],v[40:41],v[114:115]\n" \
"v_cvt_pk_bf16_f32 v40,v40,v41\ns_nop 1\nv_mov_b32_dpp v44,v40 quad_perm:[0,0,0,0] row_mask:0xf bank_mask:0xf\nv_mov_b32_dpp v45,v40 quad_perm:[1,1,1,1] row_mask:0xf bank_mask:0xf\nv_mov_b32_dpp v46,v40 quad_perm:[2,2,2,2] row_mask:0xf bank_mask:0xf\nv_mov_b32_dpp v47,v40 quad_perm:[3,3,3,3] row_mask:0xf bank_mask:0xf\nv_dot2_f32_bf16 v120,v192,v44,0\nv_dot2_f32_bf16 v121,v193,v44,0\n" \
"v_dot2_f32_bf16 v122,v194,v44,0\nv_dot2_f32_bf16 v123,v195,v44,0\nv_dot2_f32_bf16 v124,v208,v44,0\nv_dot2_f32_bf16 v125,v209,v44,0\nv_dot2_f32_bf16 v126,v210,v44,0\nv_dot2_f32_bf16 v127,v211,v44,0\nv_dot2_f32_bf16 v120,v196,v45,v120\nv_dot2_f32_bf16 v121,v197,v45,v121\n" \
"v_dot2_f32_bf16 v122,v198,v45,v122\nv_dot2_f32_bf16 v123,v199,v45,v123\nv_dot2_f32_bf16 v124,v212,v45,v124\nv_dot2_f32_bf16 v125,v213,v45,v125\nv_dot2_f32_bf16 v126,v214,v45,v126\nv_dot2_f32_bf16 v127,v215,v45,v127\nv_dot2_f32_bf16 v120,v200,v46,v120\nv_dot2_f32_bf16 v121,v201,v46,v121\n" \
"v_dot2_f32_bf16 v122,v202,v46,v122\nv_dot2_f32_bf16 v123,v203,v46,v123\nv_dot2_f32_bf16 v124,v216,v46,v124\nv_dot2_f32_bf16 v125,v217,v46,v125\nv_dot2_f32_bf16 v126,v218,v46,v126\nv_dot2_f32_bf16 v127,v219,v46,v127\nv_dot2_f32_bf16 v120,v204,v47,v120\nv_dot2_f32_bf16 v121,v205,v47,v121\n" \
"v_dot2_f32_bf16 v122,v206,v47,v122\nv_dot2_f32_bf16 v123,v207,v47,v123\nv_dot2_f32_bf16 v124,v220,v47,v124\nv_dot2_f32_bf16 v125,v221,v47,v125\nv_dot2_f32_bf16 v126,v222,v47,v126\nv_dot2_f32_bf16 v127,v223,v47,v127\nv_permlane32_swap_b32 v120,v124\nv_add_f32 v120,v120,v124\n" \
"v_permlane32_swap_b32 v121,v125\nv_add_f32 v121,v121,v125\nv_permlane32_swap_b32 v122,v126\nv_add_f32 v122,v122,v126\nv_permlane32_swap_b32 v123,v127\nv_add_f32 v123,v123,v127\nv_permlane16_swap_b32 v120,v122\nv_add_f32 v120,v120,v122\n" \
"v_permlane16_swap_b32 v121,v123\nv_add_f32 v121,v121,v123\nv_add_f32_dpp v120,v120,v120 row_ror:4 row_mask:0xf bank_mask:0xf\ns_nop 1\nv_add_f32_dpp v120,v120,v120 row_ror:8 row_mask:0xf bank_mask:0xf\nv_add_f32_dpp v121,v121,v121 row_ror:4 row_mask:0xf bank_mask:0xf\ns_nop 1\nv_add_f32_dpp v121,v121,v121 row_ror:8 row_mask:0xf bank_mask:0xf\n" \
"s_cmp_eq_u64 s[90:91],0\ns_cbranch_scc1 .Lver\ns_waitcnt vmcnt(0)\ns_branch .Lkp\n.Lver:\ns_cmp_eq_u32 s3,0\ns_cbranch_scc1 .Lnf\nds_write_b32 v252,v120\n" \
"ds_write_b32 v252,v121 offset:16\ns_lshr_b32 s70,s2,1\nv_and_b32 v64,4,v241\nv_lshrrev_b32 v241,3,v241\nv_lshlrev_b32 v241,11,v241\nv_lshl_or_b32 v241,v64,1,v241\ns_add_u32 s70,s70,98304\ns_waitcnt lgkmcnt(0)\n" \
"s_barrier\nds_read_b128 v[40:43],v253\nds_read_b128 v[44:47],v253 offset:256\ns_waitcnt lgkmcnt(1)\nv_pk_add_f32 v[40:41],v[40:41],v[42:43]\nv_add_f32 v40,v40,v41\nv_cvt_pk_bf16_f32 v40,0,v40\nv_mul_f32 v41,0xbfb8aa3b,v40\n" \
"v_exp_f32 v41,v41\ns_waitcnt lgkmcnt(0)\nv_pk_add_f32 v[44:45],v[44:45],v[46:47]\nv_add_f32 v41,1.0,v41\nv_rcp_f32 v41,v41\nv_add_f32 v44,v44,v45\nv_cvt_pk_bf16_f32 v44,0,v44\nv_mul_f32 v40,v40,v41\n" \
"v_cvt_pk_bf16_f32 v40,0,v40\nv_mul_f32 v40,v40,v44\nv_cvt_pk_bf16_f32 v40,0,v40\ns_nop 0\nv_readlane_b32 s8,v40,0\nv_readlane_b32 s9,v40,1\nv_readlane_b32 s10,v40,2\nv_readlane_b32 s11,v40,3\n" \
"v_readlane_b32 s12,v40,4\nv_readlane_b32 s13,v40,5\nv_readlane_b32 s14,v40,6\nv_readlane_b32 s15,v40,7\nv_readlane_b32 s16,v40,8\nv_readlane_b32 s17,v40,9\nv_readlane_b32 s18,v40,10\nv_readlane_b32 s19,v40,11\n" \
"v_readlane_b32 s20,v40,12\nv_readlane_b32 s21,v40,13\nv_readlane_b32 s22,v40,14\nv_readlane_b32 s23,v40,15\nv_pk_mul_f32 v[120:121],v[160:161],s[8:9] op_sel_hi:[1,0]\nv_pk_mul_f32 v[122:123],v[162:163],s[8:9] op_sel:[0,1]\nv_pk_fma_f32 v[120:121],v[164:165],s[10:11],v[120:121] op_sel_hi:[1,0,1]\nv_pk_fma_f32 v[122:123],v[166:167],s[10:11],v[122:123] op_sel:[0,1,0]\n" \
"v_pk_fma_f32 v[120:121],v[168:169],s[12:13],v[120:121] op_sel_hi:[1,0,1]\nv_pk_fma_f32 v[122:123],v[170:171],s[12:13],v[122:123] op_sel:[0,1,0]\nv_pk_fma_f32 v[120:121],v[172:173],s[14:15],v[120:121] op_sel_hi:[1,0,1]\nv_pk_fma_f32 v[122:123],v[174:175],s[14:15],v[122:123] op_sel:[0,1,0]\nv_pk_fma_f32 v[120:121],v[176:177],s[16:17],v[120:121] op_sel_hi:[1,0,1]\nv_pk_fma_f32 v[122:123],v[178:179],s[16:17],v[122:123] op_sel:[0,1,0]\nv_pk_fma_f32 v[120:121],v[180:181],s[18:19],v[120:121] op_sel_hi:[1,0,1]\nv_pk_fma_f32 v[122:123],v[182:183],s[18:19],v[122:123] op_sel:[0,1,0]\n" \
"v_pk_fma_f32 v[120:121],v[184:185],s[20:21],v[120:121] op_sel_hi:[1,0,1]\nv_pk_fma_f32 v[122:123],v[186:187],s[20:21],v[122:123] op_sel:[0,1,0]\nv_pk_fma_f32 v[120:121],v[188:189],s[22:23],v[120:121] op_sel_hi:[1,0,1]\nv_pk_fma_f32 v[122:123],v[190:191],s[22:23],v[122:123] op_sel:[0,1,0]\nv_pk_add_f32 v[120:121],v[120:121],v[122:123]\nv_min_u32 v120,-2,v120\nv_min_u32 v121,-2,v121\nbuffer_store_dwordx2 v[120:121],v241,s[52:55],s70 offen sc1\n" \
"v_pk_mul_f32 v[88:89],v[88:89],v[48:49]\nv_pk_mul_f32 v[90:91],v[90:91],v[50:51]\nv_pk_mul_f32 v[92:93],v[92:93],v[52:53]\nv_pk_mul_f32 v[94:95],v[94:95],v[54:55]\nv_cvt_pk_bf16_f32 v88,0,v88\nv_cvt_pk_bf16_f32 v89,0,v89\nv_cvt_pk_bf16_f32 v90,0,v90\nv_cvt_pk_bf16_f32 v91,0,v91\n" \
"v_cvt_pk_bf16_f32 v92,0,v92\nv_cvt_pk_bf16_f32 v93,0,v93\nv_cvt_pk_bf16_f32 v94,0,v94\nv_cvt_pk_bf16_f32 v95,0,v95\nv_pk_add_f32 v[88:89],v[88:89],v[72:73]\nv_pk_add_f32 v[90:91],v[90:91],v[74:75]\nv_pk_add_f32 v[92:93],v[92:93],v[76:77]\nv_pk_add_f32 v[94:95],v[94:95],v[78:79]\n" \
"v_cvt_pk_bf16_f32 v88,0,v88\nds_write_b16_d16_hi v244,v88 offset:0\nv_cvt_pk_bf16_f32 v89,0,v89\nds_write_b16_d16_hi v244,v89 offset:32\nv_cvt_pk_bf16_f32 v90,0,v90\nds_write_b16_d16_hi v244,v90 offset:64\nv_cvt_pk_bf16_f32 v91,0,v91\nds_write_b16_d16_hi v244,v91 offset:96\n" \
"v_cvt_pk_bf16_f32 v92,0,v92\nds_write_b16_d16_hi v244,v92 offset:128\nv_cvt_pk_bf16_f32 v93,0,v93\nds_write_b16_d16_hi v244,v93 offset:160\nv_cvt_pk_bf16_f32 v94,0,v94\nds_write_b16_d16_hi v244,v94 offset:192\nv_cvt_pk_bf16_f32 v95,0,v95\nds_write_b16_d16_hi v244,v95 offset:224\n" \
"ds_read_b128 v[64:67],v245 offset:0\nv_pk_mul_f32 v[24:25],v[24:25],v[48:49]\nv_pk_mul_f32 v[26:27],v[26:27],v[50:51]\nv_pk_mul_f32 v[28:29],v[28:29],v[52:53]\nv_pk_mul_f32 v[30:31],v[30:31],v[54:55]\nv_cvt_pk_bf16_f32 v24,0,v24\nv_cvt_pk_bf16_f32 v25,0,v25\nv_cvt_pk_bf16_f32 v26,0,v26\n" \
"v_cvt_pk_bf16_f32 v27,0,v27\nv_cvt_pk_bf16_f32 v28,0,v28\nv_cvt_pk_bf16_f32 v29,0,v29\nv_cvt_pk_bf16_f32 v30,0,v30\nv_cvt_pk_bf16_f32 v31,0,v31\nv_pk_add_f32 v[24:25],v[24:25],v[8:9]\nv_pk_add_f32 v[26:27],v[26:27],v[10:11]\nv_pk_add_f32 v[28:29],v[28:29],v[12:13]\n" \
"v_pk_add_f32 v[30:31],v[30:31],v[14:15]\nv_cvt_pk_bf16_f32 v24,0,v24\nds_write_b16_d16_hi v244,v24 offset:1024\nv_cvt_pk_bf16_f32 v25,0,v25\nds_write_b16_d16_hi v244,v25 offset:1056\nv_cvt_pk_bf16_f32 v26,0,v26\nds_write_b16_d16_hi v244,v26 offset:1088\nv_cvt_pk_bf16_f32 v27,0,v27\n" \
"ds_write_b16_d16_hi v244,v27 offset:1120\nv_cvt_pk_bf16_f32 v28,0,v28\nds_write_b16_d16_hi v244,v28 offset:1152\nv_cvt_pk_bf16_f32 v29,0,v29\nds_write_b16_d16_hi v244,v29 offset:1184\nv_cvt_pk_bf16_f32 v30,0,v30\nds_write_b16_d16_hi v244,v30 offset:1216\nv_cvt_pk_bf16_f32 v31,0,v31\n" \
"ds_write_b16_d16_hi v244,v31 offset:1248\nds_read_b128 v[68:71],v245 offset:1024\ns_waitcnt lgkmcnt(0)\nbuffer_store_dwordx4 v[64:67],v240,s[44:47],s95 offen sc1\nbuffer_store_dwordx4 v[68:71],v240,s[44:47],s74 offen sc1\n.Lnf:\ns_cmp_eq_u32 s94,s97\ns_cbranch_scc1 .Lwin\n" \
"buffer_store_dwordx4 v[64:67],v240,s[44:47],s65 offen sc1\nbuffer_store_dwordx4 v[68:71],v240,s[44:47],s66 offen sc1\n.Lwin:\ns_cmp_lg_u32 s38,0\ns_cbranch_scc1 .Lfin\ns_add_u32 s72,s15,54525952\nbuffer_load_ushort v128,v243,s[52:55],s72 offen sc1\ns_add_u32 s72,s15,56623104\n" \
"buffer_load_ushort v129,v243,s[52:55],s72 offen sc1\ns_add_u32 s72,s15,58720256\nbuffer_load_ushort v130,v243,s[52:55],s72 offen sc1\ns_add_u32 s72,s15,60817408\nbuffer_load_ushort v131,v243,s[52:55],s72 offen sc1\ns_add_u32 s72,s15,62914560\nbuffer_load_ushort v132,v243,s[52:55],s72 offen sc1\ns_add_u32 s72,s15,65011712\n" \
"buffer_load_ushort v133,v243,s[52:55],s72 offen sc1\ns_add_u32 s72,s15,67108864\nbuffer_load_ushort v134,v243,s[52:55],s72 offen sc1\ns_add_u32 s72,s15,69206016\nbuffer_load_ushort v135,v243,s[52:55],s72 offen sc1\nv_add_u32 v242,s15,v242\nglobal_load_ushort v144,v242,s[8:9]\nv_min_u32 v168,55,v249\n" \
"v_lshlrev_b32 v168,4,v168\ns_lshl_b32 s70,s98,11\ns_add_u32 s70,s70,98304\ns_add_u32 s71,s70,896\ns_and_b32 s82,s96,0xffff0000\ns_lshl_b32 s83,s96,16\nv_rcp_f32 v148,s83\ns_ashr_i32 s37,s94,31\n" \
"s_not_b32 s37,s37\ns_or_b32 s37,s37,0x80000000\ns_xor_b32 s37,s37,s94\ns_and_b32 s37,s37,0xffff0000\nv_mov_b32 v149,0xbfb8aa3b\nv_mul_f32 v149,s37,v149\nv_exp_f32 v149,v149\nv_and_b32 v150,15,v249\n" \
"v_add_f32 v149,1.0,v149\nv_rcp_f32 v149,v149\nv_cmp_eq_u32 vcc,0,v150\nv_cvt_pk_bf16_f32 v149,0,v149\nv_mov_b32 v150,0x40000000\nv_cndmask_b32 v150,v150,v242,vcc\nv_cmp_gt_u32 s[90:91],56,v249\ns_waitcnt vmcnt(8)\n" \
"v_lshlrev_b32 v128,16,v128\ns_waitcnt vmcnt(7)\nv_lshlrev_b32 v129,16,v129\nv_add_f32 v128,v128,v129\ns_waitcnt vmcnt(6)\nv_lshlrev_b32 v130,16,v130\ns_waitcnt vmcnt(5)\nv_lshlrev_b32 v131,16,v131\n" \
"v_add_f32 v130,v130,v131\nv_add_f32 v128,v128,v130\ns_waitcnt vmcnt(4)\nv_lshlrev_b32 v132,16,v132\ns_waitcnt vmcnt(3)\nv_lshlrev_b32 v133,16,v133\nv_add_f32 v132,v132,v133\ns_waitcnt vmcnt(2)\n" \
"v_lshlrev_b32 v134,16,v134\ns_waitcnt vmcnt(1)\nv_lshlrev_b32 v135,16,v135\nv_add_f32 v134,v134,v135\nv_add_f32 v132,v132,v134\nv_add_f32 v128,v128,v132\nv_cvt_pk_bf16_f32 v128,0,v128\nv_mul_f32 v128,v128,v62\n" \
"v_cvt_pk_bf16_f32 v128,0,v128\ns_waitcnt vmcnt(0)\nv_add_f32 v126,1.0,v126\nv_cvt_pk_bf16_f32 v126,0,v126\nv_lshlrev_b32 v144,16,v144\nv_add_f32 v128,v128,v144\nv_cvt_pk_bf16_f32 v128,0,v128\nv_subrev_f32 v129,s82,v128\n" \
"v_cvt_pk_bf16_f32 v129,0,v129\nv_mul_f32 v129,v129,v148\nv_cvt_pk_bf16_f32 v129,0,v129\nv_mul_f32 v129,v129,v126\nv_cvt_pk_bf16_f32 v129,0,v129\nv_add_f32 v129,v129,v63\nv_cvt_pk_bf16_f32 v129,0,v129\n.Lp2:\n" \
"buffer_load_dwordx4 v[160:163],v168,s[52:55],s70 offen sc1\nbuffer_load_dwordx4 v[164:167],v168,s[52:55],s71 offen sc1\ns_waitcnt vmcnt(0)\nv_max3_u32 v228,v160,v161,v162\nv_max3_u32 v229,v163,v164,v165\nv_max3_u32 v228,v228,v166,v167\nv_max_u32 v228,v228,v229\nv_pk_add_f32 v[160:161],v[160:161],v[164:165]\n" \
"v_cmp_eq_u32 vcc,-1,v228\nv_pk_add_f32 v[162:163],v[162:163],v[166:167]\ns_sub_u32 s73,s73,1\ns_cselect_b32 s73,0,s73\ns_cselect_b64 vcc,0,vcc\ns_cmp_lg_u64 vcc,0\ns_cselect_b32 s72,0x8000000,0\nv_cndmask_b32 v160,0,v160,s[90:91]\n" \
"v_cndmask_b32 v161,0,v161,s[90:91]\nv_cndmask_b32 v162,0,v162,s[90:91]\nv_cndmask_b32 v163,0,v163,s[90:91]\ns_nop 0\nv_permlane32_swap_b32 v160,v162\nv_permlane32_swap_b32 v161,v163\nv_pk_add_f32 v[160:161],v[160:161],v[162:163]\ns_nop 1\n" \
"v_permlane16_swap_b32 v160,v161\nv_add_f32 v160,v160,v161\ns_nop 1\nv_add_f32_dpp v160,v160,v160 quad_perm:[1,0,3,2] row_mask:0xf bank_mask:0xf\ns_nop 1\nv_add_f32_dpp v160,v160,v160 quad_perm:[2,3,0,1] row_mask:0xf bank_mask:0xf\ns_nop 1\nv_add_f32_dpp v160,v160,v160 row_half_mirror row_mask:0xf bank_mask:0xf\n" \
"s_nop 1\nv_add_f32_dpp v160,v160,v160 row_mirror row_mask:0xf bank_mask:0xf\nv_cvt_pk_bf16_f32 v160,0,v160\nv_mul_f32 v160,v160,v149\nv_cvt_pk_bf16_f32 v160,0,v160\nv_add_f32 v160,v160,v129\nv_cvt_pk_bf16_f32 v160,0,v160\nv_mul_f32 v160,v160,v127\n" \
"v_cvt_pk_bf16_f32 v160,0,v160\nv_add_f32 v160,v160,v128\nv_cvt_pk_bf16_f32 v160,0,v160\nbuffer_store_short_d16_hi v160,v150,s[44:47],s72 offen sc1\n.Lp2s:\ns_cbranch_scc1 .Lp2\ns_branch .Lfin\n.Lhs:\n" \
"s_sub_u32 s73,s73,1\ns_cselect_b32 s73,0,s73\ns_cbranch_scc1 .Lhb\ns_sleep 2\nbuffer_load_dwordx2 v[166:167],v165,s[52:55],s80 offen sc1\ns_waitcnt vmcnt(0)\nv_xor_b32 v166,v166,v230\nv_xor_b32 v167,v167,v231\n" \
"v_or_b32 v166,v166,v167\nv_cmp_ne_u32 vcc,0,v166\ns_nop 0\ns_cbranch_vccnz .Lhs\ns_branch .Lhb\n.Lps0:\ns_sub_u32 s73,s73,1\ns_cselect_b32 s73,0,s73\n" \
"s_cbranch_scc1 .Lpb0\ns_sleep 1\nbuffer_load_dword v248,v228,s[52:55],s80 offen sc1\ns_waitcnt vmcnt(0)\nv_cmp_eq_u32 vcc,0,v248\ns_nop 0\ns_cbranch_vccnz .Lps0\ns_branch .Lpb0\n" \
".Lps1:\ns_sub_u32 s73,s73,1\ns_cselect_b32 s73,0,s73\ns_cbranch_scc1 .Lpb1\ns_sleep 1\nbuffer_load_dword v248,v228,s[52:55],s80 offen sc1\ns_waitcnt vmcnt(0)\nv_cmp_eq_u32 vcc,0,v248\n" \
"s_nop 0\ns_cbranch_vccnz .Lps1\ns_branch .Lpb1\n.Lps2:\ns_sub_u32 s73,s73,1\ns_cselect_b32 s73,0,s73\ns_cbranch_scc1 .Lpb2\ns_sleep 1\n" \
"buffer_load_dword v248,v251,s[52:55],s80 offen sc1\ns_waitcnt vmcnt(0)\nv_cmp_eq_u32 vcc,0,v248\ns_nop 0\ns_cbranch_vccnz .Lps2\ns_branch .Lpb2\n.Lps3:\ns_sub_u32 s73,s73,1\n" \
"s_cselect_b32 s73,0,s73\ns_cbranch_scc1 .Lpb3\ns_sleep 1\nbuffer_load_dword v248,v251,s[52:55],s80 offen sc1\ns_waitcnt vmcnt(0)\nv_cmp_eq_u32 vcc,0,v248\ns_nop 0\ns_cbranch_vccnz .Lps3\n" \
"s_branch .Lpb3\n.Lps4:\ns_sub_u32 s73,s73,1\ns_cselect_b32 s73,0,s73\ns_cbranch_scc1 .Lpb4\ns_sleep 1\nbuffer_load_dword v248,v251,s[52:55],s80 offen sc1\ns_waitcnt vmcnt(0)\n" \
"v_cmp_eq_u32 vcc,0,v248\ns_nop 0\ns_cbranch_vccnz .Lps4\ns_branch .Lpb4\n.Lps5:\ns_sub_u32 s73,s73,1\ns_cselect_b32 s73,0,s73\ns_cbranch_scc1 .Lpb5\n" \
"s_sleep 1\nbuffer_load_dword v248,v251,s[52:55],s80 offen sc1\ns_waitcnt vmcnt(0)\nv_cmp_eq_u32 vcc,0,v248\ns_nop 0\ns_cbranch_vccnz .Lps5\ns_branch .Lpb5\n.Lps6:\n" \
"s_sub_u32 s73,s73,1\ns_cselect_b32 s73,0,s73\ns_cbranch_scc1 .Lpb6\ns_sleep 1\nbuffer_load_dword v248,v251,s[52:55],s80 offen sc1\ns_waitcnt vmcnt(0)\nv_and_b32 v248,s17,v248\nv_cmp_eq_u32 vcc,0,v248\n" \
"s_nop 0\ns_cbranch_vccnz .Lps6\ns_branch .Lpb6\n.Lps7:\ns_sub_u32 s73,s73,1\ns_cselect_b32 s73,0,s73\ns_cbranch_scc1 .Lpb7\ns_sleep 1\n" \
"buffer_load_dword v248,v254,s[52:55],s80 offen sc1\ns_waitcnt vmcnt(0)\nv_cmp_eq_u32 vcc,0,v248\ns_nop 0\ns_cbranch_vccnz .Lps7\ns_branch .Lpb7\n.Lps8:\ns_sub_u32 s73,s73,1\n" \
"s_cselect_b32 s73,0,s73\ns_cbranch_scc1 .Lpb8\ns_sleep 1\nbuffer_load_dword v248,v251,s[52:55],s79 offen sc1\ns_waitcnt vmcnt(0)\nv_cmp_eq_u32 vcc,0,v248\ns_nop 0\ns_cbranch_vccnz .Lps8\n" \
"s_branch .Lpb8\n.Lrs1:\ns_mov_b32 s71,0\n.Lrl1:\ns_mov_b32 s97,0\nv_max3_u32 v124,v160,v161,v162\nv_max3_u32 v124,v124,v163,v163\nv_cmp_eq_u32 vcc,-1,v124\n" \
"s_add_u32 s72,s95,0\ns_cbranch_vccz .Lrz10\ns_mov_b64 exec,vcc\ns_mov_b32 s97,1\nbuffer_load_dwordx4 v[160:163],v240,s[52:55],s72 offen sc1\ns_mov_b64 exec,-1\n.Lrz10:\nv_max3_u32 v124,v164,v165,v166\n" \
"v_max3_u32 v124,v124,v167,v167\nv_cmp_eq_u32 vcc,-1,v124\ns_add_u32 s72,s95,2097152\ns_cbranch_vccz .Lrz11\ns_mov_b64 exec,vcc\ns_mov_b32 s97,1\nbuffer_load_dwordx4 v[164:167],v240,s[52:55],s72 offen sc1\ns_mov_b64 exec,-1\n" \
".Lrz11:\nv_max3_u32 v124,v168,v169,v170\nv_max3_u32 v124,v124,v171,v171\nv_cmp_eq_u32 vcc,-1,v124\ns_add_u32 s72,s95,4194304\ns_cbranch_vccz .Lrz12\ns_mov_b64 exec,vcc\ns_mov_b32 s97,1\n" \
"buffer_load_dwordx4 v[168:171],v240,s[52:55],s72 offen sc1\ns_mov_b64 exec,-1\n.Lrz12:\nv_max3_u32 v124,v172,v173,v174\nv_max3_u32 v124,v124,v175,v175\nv_cmp_eq_u32 vcc,-1,v124\ns_add_u32 s72,s95,6291456\ns_cbranch_vccz .Lrz13\n" \
"s_mov_b64 exec,vcc\ns_mov_b32 s97,1\nbuffer_load_dwordx4 v[172:175],v240,s[52:55],s72 offen sc1\ns_mov_b64 exec,-1\n.Lrz13:\nv_max3_u32 v124,v176,v177,v178\nv_max3_u32 v124,v124,v179,v179\nv_cmp_eq_u32 vcc,-1,v124\n" \
"s_add_u32 s72,s95,8388608\ns_cbranch_vccz .Lrz14\ns_mov_b64 exec,vcc\ns_mov_b32 s97,1\nbuffer_load_dwordx4 v[176:179],v240,s[52:55],s72 offen sc1\ns_mov_b64 exec,-1\n.Lrz14:\nv_max3_u32 v124,v180,v181,v182\n" \
"v_max3_u32 v124,v124,v183,v183\nv_cmp_eq_u32 vcc,-1,v124\ns_add_u32 s72,s95,10485760\ns_cbranch_vccz .Lrz15\ns_mov_b64 exec,vcc\ns_mov_b32 s97,1\nbuffer_load_dwordx4 v[180:183],v240,s[52:55],s72 offen sc1\ns_mov_b64 exec,-1\n" \
".Lrz15:\nv_max3_u32 v124,v184,v185,v186\nv_max3_u32 v124,v124,v187,v187\nv_cmp_eq_u32 vcc,-1,v124\ns_add_u32 s72,s95,12582912\ns_cbranch_vccz .Lrz16\ns_mov_b64 exec,vcc\ns_mov_b32 s97,1\n" \
"buffer_load_dwordx4 v[184:187],v240,s[52:55],s72 offen sc1\ns_mov_b64 exec,-1\n.Lrz16:\nv_max3_u32 v124,v188,v189,v190\nv_max3_u32 v124,v124,v191,v191\nv_cmp_eq_u32 vcc,-1,v124\ns_add_u32 s72,s95,14680064\ns_cbranch_vccz .Lrz17\n" \
"s_mov_b64 exec,vcc\ns_mov_b32 s97,1\nbuffer_load_dwordx4 v[188:191],v240,s[52:55],s72 offen sc1\ns_mov_b64 exec,-1\n.Lrz17:\ns_cmp_lg_u32 s97,0\ns_cbranch_scc1 .Lrm1\ns_cmp_eq_u32 s71,0\n" \
"s_cbranch_scc1 .Lrb1\ns_branch .Lrow1\n.Lrm1:\ns_mov_b32 s71,1\ns_waitcnt vmcnt(0)\ns_sub_u32 s73,s73,1\ns_cselect_b32 s73,0,s73\ns_cbranch_scc1 .Lrb1\n" \
"s_sleep 2\ns_branch .Lrl1\n.Lrs0:\ns_mov_b32 s71,0\n.Lrl0:\ns_mov_b32 s97,0\nv_max3_u32 v60,v128,v129,v130\nv_max3_u32 v60,v60,v131,v131\n" \
"v_cmp_eq_u32 vcc,-1,v60\ns_add_u32 s72,s74,0\ns_cbranch_vccz .Lrz00\ns_mov_b64 exec,vcc\ns_mov_b32 s97,1\nbuffer_load_dwordx4 v[128:131],v240,s[52:55],s72 offen sc1\ns_mov_b64 exec,-1\n.Lrz00:\n" \
"v_max3_u32 v60,v132,v133,v134\nv_max3_u32 v60,v60,v135,v135\nv_cmp_eq_u32 vcc,-1,v60\ns_add_u32 s72,s74,2097152\ns_cbranch_vccz .Lrz01\ns_mov_b64 exec,vcc\ns_mov_b32 s97,1\nbuffer_load_dwordx4 v[132:135],v240,s[52:55],s72 offen sc1\n" \
"s_mov_b64 exec,-1\n.Lrz01:\nv_max3_u32 v60,v136,v137,v138\nv_max3_u32 v60,v60,v139,v139\nv_cmp_eq_u32 vcc,-1,v60\ns_add_u32 s72,s74,4194304\ns_cbranch_vccz .Lrz02\ns_mov_b64 exec,vcc\n" \
"s_mov_b32 s97,1\nbuffer_load_dwordx4 v[136:139],v240,s[52:55],s72 offen sc1\ns_mov_b64 exec,-1\n.Lrz02:\nv_max3_u32 v60,v140,v141,v142\nv_max3_u32 v60,v60,v143,v143\nv_cmp_eq_u32 vcc,-1,v60\ns_add_u32 s72,s74,6291456\n" \
"s_cbranch_vccz .Lrz03\ns_mov_b64 exec,vcc\ns_mov_b32 s97,1\nbuffer_load_dwordx4 v[140:143],v240,s[52:55],s72 offen sc1\ns_mov_b64 exec,-1\n.Lrz03:\nv_max3_u32 v60,v144,v145,v146\nv_max3_u32 v60,v60,v147,v147\n" \
"v_cmp_eq_u32 vcc,-1,v60\ns_add_u32 s72,s74,8388608\ns_cbranch_vccz .Lrz04\ns_mov_b64 exec,vcc\ns_mov_b32 s97,1\nbuffer_load_dwordx4 v[144:147],v240,s[52:55],s72 offen sc1\ns_mov_b64 exec,-1\n.Lrz04:\n" \
"v_max3_u32 v60,v148,v149,v150\nv_max3_u32 v60,v60,v151,v151\nv_cmp_eq_u32 vcc,-1,v60\ns_add_u32 s72,s74,10485760\ns_cbranch_vccz .Lrz05\ns_mov_b64 exec,vcc\ns_mov_b32 s97,1\nbuffer_load_dwordx4 v[148:151],v240,s[52:55],s72 offen sc1\n" \
"s_mov_b64 exec,-1\n.Lrz05:\nv_max3_u32 v60,v152,v153,v154\nv_max3_u32 v60,v60,v155,v155\nv_cmp_eq_u32 vcc,-1,v60\ns_add_u32 s72,s74,12582912\ns_cbranch_vccz .Lrz06\ns_mov_b64 exec,vcc\n" \
"s_mov_b32 s97,1\nbuffer_load_dwordx4 v[152:155],v240,s[52:55],s72 offen sc1\ns_mov_b64 exec,-1\n.Lrz06:\nv_max3_u32 v60,v156,v157,v158\nv_max3_u32 v60,v60,v159,v159\nv_cmp_eq_u32 vcc,-1,v60\ns_add_u32 s72,s74,14680064\n" \
"s_cbranch_vccz .Lrz07\ns_mov_b64 exec,vcc\ns_mov_b32 s97,1\nbuffer_load_dwordx4 v[156:159],v240,s[52:55],s72 offen sc1\ns_mov_b64 exec,-1\n.Lrz07:\ns_cmp_lg_u32 s97,0\ns_cbranch_scc1 .Lrm0\n" \
"s_cmp_eq_u32 s71,0\ns_cbranch_scc1 .Lrb0\ns_branch .Lrow0\n.Lrm0:\ns_mov_b32 s71,1\ns_waitcnt vmcnt(0)\ns_sub_u32 s73,s73,1\ns_cselect_b32 s73,0,s73\n" \
"s_cbranch_scc1 .Lrb0\ns_sleep 2\ns_branch .Lrl0\n.Lredo:\ns_waitcnt vmcnt(0) lgkmcnt(0)\ns_barrier\ns_mov_b64 s[90:91],0\ns_mov_b32 s99,0\n" \
"s_mov_b32 s17,1\ns_lshl_b32 s70,s63,19\ns_add_u32 s37,s70,3145728\ns_lshl_b32 s71,s62,15\ns_add_u32 s72,s37,s64\ns_add_u32 s72,s72,491520\ns_sub_u32 s72,s72,s71\ns_add_u32 m0,s64,0\n" \
"s_nop 0\nbuffer_load_dwordx4 v240,s[52:55],s72 offen offset:0 sc1 lds\nbuffer_load_dwordx4 v240,s[52:55],s72 offen offset:1024 sc1 lds\nbuffer_load_dwordx4 v240,s[52:55],s72 offen offset:2048 sc1 lds\nbuffer_load_dwordx4 v240,s[52:55],s72 offen offset:3072 sc1 lds\ns_add_u32 m0,s64,65536\ns_add_u32 s72,s72,16777216\nbuffer_load_dwordx4 v240,s[52:55],s72 offen offset:0 sc1 lds\n" \
"buffer_load_dwordx4 v240,s[52:55],s72 offen offset:1024 sc1 lds\nbuffer_load_dwordx4 v240,s[52:55],s72 offen offset:2048 sc1 lds\nbuffer_load_dwordx4 v240,s[52:55],s72 offen offset:3072 sc1 lds\ns_add_u32 s72,s37,s64\ns_add_u32 s72,s72,507904\ns_sub_u32 s72,s72,s71\ns_add_u32 m0,s64,16384\ns_nop 0\n" \
"buffer_load_dwordx4 v240,s[52:55],s72 offen offset:0 sc1 lds\nbuffer_load_dwordx4 v240,s[52:55],s72 offen offset:1024 sc1 lds\nbuffer_load_dwordx4 v240,s[52:55],s72 offen offset:2048 sc1 lds\nbuffer_load_dwordx4 v240,s[52:55],s72 offen offset:3072 sc1 lds\ns_add_u32 m0,s64,81920\ns_add_u32 s72,s72,16777216\nbuffer_load_dwordx4 v240,s[52:55],s72 offen offset:0 sc1 lds\nbuffer_load_dwordx4 v240,s[52:55],s72 offen offset:1024 sc1 lds\n" \
"buffer_load_dwordx4 v240,s[52:55],s72 offen offset:2048 sc1 lds\nbuffer_load_dwordx4 v240,s[52:55],s72 offen offset:3072 sc1 lds\ns_add_u32 s72,s37,s64\ns_add_u32 s72,s72,16384\ns_add_u32 s72,s72,s71\ns_add_u32 m0,s64,32768\ns_nop 0\nbuffer_load_dwordx4 v240,s[52:55],s72 offen offset:0 sc1 lds\n" \
"buffer_load_dwordx4 v240,s[52:55],s72 offen offset:1024 sc1 lds\nbuffer_load_dwordx4 v240,s[52:55],s72 offen offset:2048 sc1 lds\nbuffer_load_dwordx4 v240,s[52:55],s72 offen offset:3072 sc1 lds\ns_add_u32 m0,s64,98304\ns_add_u32 s72,s72,16777216\nbuffer_load_dwordx4 v240,s[52:55],s72 offen offset:0 sc1 lds\nbuffer_load_dwordx4 v240,s[52:55],s72 offen offset:1024 sc1 lds\nbuffer_load_dwordx4 v240,s[52:55],s72 offen offset:2048 sc1 lds\n" \
"buffer_load_dwordx4 v240,s[52:55],s72 offen offset:3072 sc1 lds\ns_add_u32 s72,s37,s64\ns_add_u32 s72,s72,0\ns_add_u32 s72,s72,s71\ns_add_u32 m0,s64,49152\ns_nop 0\nbuffer_load_dwordx4 v240,s[52:55],s72 offen offset:0 sc1 lds\nbuffer_load_dwordx4 v240,s[52:55],s72 offen offset:1024 sc1 lds\n" \
"buffer_load_dwordx4 v240,s[52:55],s72 offen offset:2048 sc1 lds\nbuffer_load_dwordx4 v240,s[52:55],s72 offen offset:3072 sc1 lds\ns_add_u32 m0,s64,114688\ns_add_u32 s72,s72,16777216\nbuffer_load_dwordx4 v240,s[52:55],s72 offen offset:0 sc1 lds\nbuffer_load_dwordx4 v240,s[52:55],s72 offen offset:1024 sc1 lds\nbuffer_load_dwordx4 v240,s[52:55],s72 offen offset:2048 sc1 lds\nbuffer_load_dwordx4 v240,s[52:55],s72 offen offset:3072 sc1 lds\n" \
"v_and_b32 v251,15,v249\nv_lshrrev_b32 v250,4,v249\nv_lshlrev_b32 v250,2,v250\nv_sub_u32 v251,v251,v250\ns_lshl_b32 s70,s62,7\ns_lshl_b32 s72,s7,5\ns_sub_u32 s38,s72,s70\ns_add_u32 s38,s38,1920\n" \
"s_sub_u32 s39,s70,s72\ns_add_u32 s39,s39,96\nv_add_u32 v225,s38,v251\nv_add_u32 v225,0,v225\nv_mov_b32 v224,0xff800000\nv_add_u32 v229,s38,v251\nv_add_u32 v229,16,v229\nv_mov_b32 v228,0xff800000\n" \
"v_add_u32 v233,s39,v251\nv_add_u32 v233,0,v233\nv_mov_b32 v232,0xff800000\nv_add_u32 v237,s39,v251\nv_add_u32 v237,16,v237\nv_mov_b32 v236,0xff800000\ns_sub_u32 s38,524288,s71\ns_add_u32 s38,s38,s37\n" \
".Lrl:\nbuffer_load_dwordx4 a[0:3],v240,s[52:55],s37 offen offset:0 sc1\nbuffer_load_dwordx4 a[4:7],v240,s[52:55],s37 offen offset:1024 sc1\nbuffer_load_dwordx4 a[8:11],v240,s[52:55],s37 offen offset:2048 sc1\nbuffer_load_dwordx4 a[12:15],v240,s[52:55],s37 offen offset:3072 sc1\ns_add_u32 s37,s37,4096\nbuffer_load_dwordx4 a[16:19],v240,s[52:55],s37 offen offset:0 sc1\nbuffer_load_dwordx4 a[20:23],v240,s[52:55],s37 offen offset:1024 sc1\n" \
"buffer_load_dwordx4 a[24:27],v240,s[52:55],s37 offen offset:2048 sc1\nbuffer_load_dwordx4 a[28:31],v240,s[52:55],s37 offen offset:3072 sc1\ns_add_u32 s37,s37,4096\nbuffer_load_dwordx4 a[32:35],v240,s[52:55],s37 offen offset:0 sc1\nbuffer_load_dwordx4 a[36:39],v240,s[52:55],s37 offen offset:1024 sc1\nbuffer_load_dwordx4 a[40:43],v240,s[52:55],s37 offen offset:2048 sc1\nbuffer_load_dwordx4 a[44:47],v240,s[52:55],s37 offen offset:3072 sc1\ns_add_u32 s37,s37,4096\n" \
"buffer_load_dwordx4 a[48:51],v240,s[52:55],s37 offen offset:0 sc1\nbuffer_load_dwordx4 a[52:55],v240,s[52:55],s37 offen offset:1024 sc1\nbuffer_load_dwordx4 a[56:59],v240,s[52:55],s37 offen offset:2048 sc1\nbuffer_load_dwordx4 a[60:63],v240,s[52:55],s37 offen offset:3072 sc1\ns_add_u32 s37,s37,4096\ns_waitcnt vmcnt(15)\nM v[128:131],a[0:3],a[128:131],0\ns_waitcnt vmcnt(14)\n" \
"M v[128:131],a[4:7],a[132:135],v[128:131]\ns_waitcnt vmcnt(13)\nM v[128:131],a[8:11],a[136:139],v[128:131]\ns_waitcnt vmcnt(12)\nM v[128:131],a[12:15],a[140:143],v[128:131]\nM v[132:135],a[0:3],a[144:147],0\nM v[132:135],a[4:7],a[148:151],v[132:135]\nM v[132:135],a[8:11],a[152:155],v[132:135]\n" \
"M v[132:135],a[12:15],a[156:159],v[132:135]\nM v[136:139],a[0:3],a[160:163],0\nM v[136:139],a[4:7],a[164:167],v[136:139]\nM v[136:139],a[8:11],a[168:171],v[136:139]\nM v[136:139],a[12:15],a[172:175],v[136:139]\nM v[140:143],a[0:3],a[176:179],0\nM v[140:143],a[4:7],a[180:183],v[140:143]\nM v[140:143],a[8:11],a[184:187],v[140:143]\n" \
"M v[140:143],a[12:15],a[188:191],v[140:143]\ns_waitcnt vmcnt(11)\nM v[144:147],a[16:19],a[128:131],0\ns_waitcnt vmcnt(10)\nM v[144:147],a[20:23],a[132:135],v[144:147]\ns_waitcnt vmcnt(9)\nM v[144:147],a[24:27],a[136:139],v[144:147]\ns_waitcnt vmcnt(8)\n" \
"M v[144:147],a[28:31],a[140:143],v[144:147]\nM v[148:151],a[16:19],a[144:147],0\nM v[148:151],a[20:23],a[148:151],v[148:151]\nM v[148:151],a[24:27],a[152:155],v[148:151]\nM v[148:151],a[28:31],a[156:159],v[148:151]\nM v[152:155],a[16:19],a[160:163],0\nM v[152:155],a[20:23],a[164:167],v[152:155]\nM v[152:155],a[24:27],a[168:171],v[152:155]\n" \
"M v[152:155],a[28:31],a[172:175],v[152:155]\nM v[156:159],a[16:19],a[176:179],0\nM v[156:159],a[20:23],a[180:183],v[156:159]\nM v[156:159],a[24:27],a[184:187],v[156:159]\nM v[156:159],a[28:31],a[188:191],v[156:159]\ns_waitcnt vmcnt(7)\nM v[160:163],a[32:35],a[128:131],0\ns_waitcnt vmcnt(6)\n" \
"M v[160:163],a[36:39],a[132:135],v[160:163]\ns_waitcnt vmcnt(5)\nM v[160:163],a[40:43],a[136:139],v[160:163]\ns_waitcnt vmcnt(4)\nM v[160:163],a[44:47],a[140:143],v[160:163]\nM v[164:167],a[32:35],a[144:147],0\nM v[164:167],a[36:39],a[148:151],v[164:167]\nM v[164:167],a[40:43],a[152:155],v[164:167]\n" \
"M v[164:167],a[44:47],a[156:159],v[164:167]\nM v[168:171],a[32:35],a[160:163],0\nM v[168:171],a[36:39],a[164:167],v[168:171]\nM v[168:171],a[40:43],a[168:171],v[168:171]\nM v[168:171],a[44:47],a[172:175],v[168:171]\nM v[172:175],a[32:35],a[176:179],0\nM v[172:175],a[36:39],a[180:183],v[172:175]\nM v[172:175],a[40:43],a[184:187],v[172:175]\n" \
"M v[172:175],a[44:47],a[188:191],v[172:175]\ns_waitcnt vmcnt(3)\nM v[176:179],a[48:51],a[128:131],0\ns_waitcnt vmcnt(2)\nM v[176:179],a[52:55],a[132:135],v[176:179]\ns_waitcnt vmcnt(1)\nM v[176:179],a[56:59],a[136:139],v[176:179]\ns_waitcnt vmcnt(0)\n" \
"M v[176:179],a[60:63],a[140:143],v[176:179]\nM v[180:183],a[48:51],a[144:147],0\nM v[180:183],a[52:55],a[148:151],v[180:183]\nM v[180:183],a[56:59],a[152:155],v[180:183]\nM v[180:183],a[60:63],a[156:159],v[180:183]\nM v[184:187],a[48:51],a[160:163],0\nM v[184:187],a[52:55],a[164:167],v[184:187]\nM v[184:187],a[56:59],a[168:171],v[184:187]\n" \
"M v[184:187],a[60:63],a[172:175],v[184:187]\nM v[188:191],a[48:51],a[176:179],0\nM v[188:191],a[52:55],a[180:183],v[188:191]\nM v[188:191],a[56:59],a[184:187],v[188:191]\nM v[188:191],a[60:63],a[188:191],v[188:191]\nv_cmp_le_i32 vcc,0,v225\ns_nop 1\nv_cndmask_b32 v128,v252,v128,vcc\n" \
"v_max_f32 v224,v224,v128\nv_cmp_le_i32 vcc,1,v225\ns_nop 1\nv_cndmask_b32 v129,v252,v129,vcc\nv_max_f32 v224,v224,v129\nv_cmp_le_i32 vcc,2,v225\ns_nop 1\nv_cndmask_b32 v130,v252,v130,vcc\n" \
"v_max_f32 v224,v224,v130\nv_cmp_le_i32 vcc,3,v225\ns_nop 1\nv_cndmask_b32 v131,v252,v131,vcc\nv_max_f32 v224,v224,v131\nv_cmp_le_i32 vcc,0,v229\ns_nop 1\nv_cndmask_b32 v132,v252,v132,vcc\n" \
"v_max_f32 v228,v228,v132\nv_cmp_le_i32 vcc,1,v229\ns_nop 1\nv_cndmask_b32 v133,v252,v133,vcc\nv_max_f32 v228,v228,v133\nv_cmp_le_i32 vcc,2,v229\ns_nop 1\nv_cndmask_b32 v134,v252,v134,vcc\n" \
"v_max_f32 v228,v228,v134\nv_cmp_le_i32 vcc,3,v229\ns_nop 1\nv_cndmask_b32 v135,v252,v135,vcc\nv_max_f32 v228,v228,v135\nv_cmp_le_i32 vcc,0,v233\ns_nop 1\nv_cndmask_b32 v136,v252,v136,vcc\n" \
"v_max_f32 v232,v232,v136\nv_cmp_le_i32 vcc,1,v233\ns_nop 1\nv_cndmask_b32 v137,v252,v137,vcc\nv_max_f32 v232,v232,v137\nv_cmp_le_i32 vcc,2,v233\ns_nop 1\nv_cndmask_b32 v138,v252,v138,vcc\n" \
"v_max_f32 v232,v232,v138\nv_cmp_le_i32 vcc,3,v233\ns_nop 1\nv_cndmask_b32 v139,v252,v139,vcc\nv_max_f32 v232,v232,v139\nv_cmp_le_i32 vcc,0,v237\ns_nop 1\nv_cndmask_b32 v140,v252,v140,vcc\n" \
"v_max_f32 v236,v236,v140\nv_cmp_le_i32 vcc,1,v237\ns_nop 1\nv_cndmask_b32 v141,v252,v141,vcc\nv_max_f32 v236,v236,v141\nv_cmp_le_i32 vcc,2,v237\ns_nop 1\nv_cndmask_b32 v142,v252,v142,vcc\n" \
"v_max_f32 v236,v236,v142\nv_cmp_le_i32 vcc,3,v237\ns_nop 1\nv_cndmask_b32 v143,v252,v143,vcc\nv_max_f32 v236,v236,v143\nv_cmp_le_i32 vcc,16,v225\ns_nop 1\nv_cndmask_b32 v144,v252,v144,vcc\n" \
"v_max_f32 v224,v224,v144\nv_cmp_le_i32 vcc,17,v225\ns_nop 1\nv_cndmask_b32 v145,v252,v145,vcc\nv_max_f32 v224,v224,v145\nv_cmp_le_i32 vcc,18,v225\ns_nop 1\nv_cndmask_b32 v146,v252,v146,vcc\n" \
"v_max_f32 v224,v224,v146\nv_cmp_le_i32 vcc,19,v225\ns_nop 1\nv_cndmask_b32 v147,v252,v147,vcc\nv_max_f32 v224,v224,v147\nv_cmp_le_i32 vcc,16,v229\ns_nop 1\nv_cndmask_b32 v148,v252,v148,vcc\n" \
"v_max_f32 v228,v228,v148\nv_cmp_le_i32 vcc,17,v229\ns_nop 1\nv_cndmask_b32 v149,v252,v149,vcc\nv_max_f32 v228,v228,v149\nv_cmp_le_i32 vcc,18,v229\ns_nop 1\nv_cndmask_b32 v150,v252,v150,vcc\n" \
"v_max_f32 v228,v228,v150\nv_cmp_le_i32 vcc,19,v229\ns_nop 1\nv_cndmask_b32 v151,v252,v151,vcc\nv_max_f32 v228,v228,v151\nv_cmp_le_i32 vcc,16,v233\ns_nop 1\nv_cndmask_b32 v152,v252,v152,vcc\n" \
"v_max_f32 v232,v232,v152\nv_cmp_le_i32 vcc,17,v233\ns_nop 1\nv_cndmask_b32 v153,v252,v153,vcc\nv_max_f32 v232,v232,v153\nv_cmp_le_i32 vcc,18,v233\ns_nop 1\nv_cndmask_b32 v154,v252,v154,vcc\n" \
"v_max_f32 v232,v232,v154\nv_cmp_le_i32 vcc,19,v233\ns_nop 1\nv_cndmask_b32 v155,v252,v155,vcc\nv_max_f32 v232,v232,v155\nv_cmp_le_i32 vcc,16,v237\ns_nop 1\nv_cndmask_b32 v156,v252,v156,vcc\n" \
"v_max_f32 v236,v236,v156\nv_cmp_le_i32 vcc,17,v237\ns_nop 1\nv_cndmask_b32 v157,v252,v157,vcc\nv_max_f32 v236,v236,v157\nv_cmp_le_i32 vcc,18,v237\ns_nop 1\nv_cndmask_b32 v158,v252,v158,vcc\n" \
"v_max_f32 v236,v236,v158\nv_cmp_le_i32 vcc,19,v237\ns_nop 1\nv_cndmask_b32 v159,v252,v159,vcc\nv_max_f32 v236,v236,v159\nv_cmp_le_i32 vcc,32,v225\ns_nop 1\nv_cndmask_b32 v160,v252,v160,vcc\n" \
"v_max_f32 v224,v224,v160\nv_cmp_le_i32 vcc,33,v225\ns_nop 1\nv_cndmask_b32 v161,v252,v161,vcc\nv_max_f32 v224,v224,v161\nv_cmp_le_i32 vcc,34,v225\ns_nop 1\nv_cndmask_b32 v162,v252,v162,vcc\n" \
"v_max_f32 v224,v224,v162\nv_cmp_le_i32 vcc,35,v225\ns_nop 1\nv_cndmask_b32 v163,v252,v163,vcc\nv_max_f32 v224,v224,v163\nv_cmp_le_i32 vcc,32,v229\ns_nop 1\nv_cndmask_b32 v164,v252,v164,vcc\n" \
"v_max_f32 v228,v228,v164\nv_cmp_le_i32 vcc,33,v229\ns_nop 1\nv_cndmask_b32 v165,v252,v165,vcc\nv_max_f32 v228,v228,v165\nv_cmp_le_i32 vcc,34,v229\ns_nop 1\nv_cndmask_b32 v166,v252,v166,vcc\n" \
"v_max_f32 v228,v228,v166\nv_cmp_le_i32 vcc,35,v229\ns_nop 1\nv_cndmask_b32 v167,v252,v167,vcc\nv_max_f32 v228,v228,v167\nv_cmp_le_i32 vcc,32,v233\ns_nop 1\nv_cndmask_b32 v168,v252,v168,vcc\n" \
"v_max_f32 v232,v232,v168\nv_cmp_le_i32 vcc,33,v233\ns_nop 1\nv_cndmask_b32 v169,v252,v169,vcc\nv_max_f32 v232,v232,v169\nv_cmp_le_i32 vcc,34,v233\ns_nop 1\nv_cndmask_b32 v170,v252,v170,vcc\n" \
"v_max_f32 v232,v232,v170\nv_cmp_le_i32 vcc,35,v233\ns_nop 1\nv_cndmask_b32 v171,v252,v171,vcc\nv_max_f32 v232,v232,v171\nv_cmp_le_i32 vcc,32,v237\ns_nop 1\nv_cndmask_b32 v172,v252,v172,vcc\n" \
"v_max_f32 v236,v236,v172\nv_cmp_le_i32 vcc,33,v237\ns_nop 1\nv_cndmask_b32 v173,v252,v173,vcc\nv_max_f32 v236,v236,v173\nv_cmp_le_i32 vcc,34,v237\ns_nop 1\nv_cndmask_b32 v174,v252,v174,vcc\n" \
"v_max_f32 v236,v236,v174\nv_cmp_le_i32 vcc,35,v237\ns_nop 1\nv_cndmask_b32 v175,v252,v175,vcc\nv_max_f32 v236,v236,v175\nv_cmp_le_i32 vcc,48,v225\ns_nop 1\nv_cndmask_b32 v176,v252,v176,vcc\n" \
"v_max_f32 v224,v224,v176\nv_cmp_le_i32 vcc,49,v225\ns_nop 1\nv_cndmask_b32 v177,v252,v177,vcc\nv_max_f32 v224,v224,v177\nv_cmp_le_i32 vcc,50,v225\ns_nop 1\nv_cndmask_b32 v178,v252,v178,vcc\n" \
"v_max_f32 v224,v224,v178\nv_cmp_le_i32 vcc,51,v225\ns_nop 1\nv_cndmask_b32 v179,v252,v179,vcc\nv_max_f32 v224,v224,v179\nv_cmp_le_i32 vcc,48,v229\ns_nop 1\nv_cndmask_b32 v180,v252,v180,vcc\n" \
"v_max_f32 v228,v228,v180\nv_cmp_le_i32 vcc,49,v229\ns_nop 1\nv_cndmask_b32 v181,v252,v181,vcc\nv_max_f32 v228,v228,v181\nv_cmp_le_i32 vcc,50,v229\ns_nop 1\nv_cndmask_b32 v182,v252,v182,vcc\n" \
"v_max_f32 v228,v228,v182\nv_cmp_le_i32 vcc,51,v229\ns_nop 1\nv_cndmask_b32 v183,v252,v183,vcc\nv_max_f32 v228,v228,v183\nv_cmp_le_i32 vcc,48,v233\ns_nop 1\nv_cndmask_b32 v184,v252,v184,vcc\n" \
"v_max_f32 v232,v232,v184\nv_cmp_le_i32 vcc,49,v233\ns_nop 1\nv_cndmask_b32 v185,v252,v185,vcc\nv_max_f32 v232,v232,v185\nv_cmp_le_i32 vcc,50,v233\ns_nop 1\nv_cndmask_b32 v186,v252,v186,vcc\n" \
"v_max_f32 v232,v232,v186\nv_cmp_le_i32 vcc,51,v233\ns_nop 1\nv_cndmask_b32 v187,v252,v187,vcc\nv_max_f32 v232,v232,v187\nv_cmp_le_i32 vcc,48,v237\ns_nop 1\nv_cndmask_b32 v188,v252,v188,vcc\n" \
"v_max_f32 v236,v236,v188\nv_cmp_le_i32 vcc,49,v237\ns_nop 1\nv_cndmask_b32 v189,v252,v189,vcc\nv_max_f32 v236,v236,v189\nv_cmp_le_i32 vcc,50,v237\ns_nop 1\nv_cndmask_b32 v190,v252,v190,vcc\n" \
"v_max_f32 v236,v236,v190\nv_cmp_le_i32 vcc,51,v237\ns_nop 1\nv_cndmask_b32 v191,v252,v191,vcc\nv_max_f32 v236,v236,v191\nv_subrev_u32 v225,64,v225\nv_subrev_u32 v229,64,v229\nv_subrev_u32 v233,64,v233\n" \
"v_subrev_u32 v237,64,v237\ns_cmp_lt_u32 s37,s38\ns_cbranch_scc1 .Lrl\ns_waitcnt vmcnt(0)\ns_barrier\nds_read_b128 a[0:3],v240 offset:0\nds_read_b128 a[4:7],v240 offset:1024\nds_read_b128 a[8:11],v240 offset:2048\n" \
"ds_read_b128 a[12:15],v240 offset:3072\nds_read_b128 a[16:19],v240 offset:4096\nds_read_b128 a[20:23],v240 offset:5120\nds_read_b128 a[24:27],v240 offset:6144\nds_read_b128 a[28:31],v240 offset:7168\nds_read_b128 a[32:35],v240 offset:8192\nds_read_b128 a[36:39],v240 offset:9216\nds_read_b128 a[40:43],v240 offset:10240\n" \
"ds_read_b128 a[44:47],v240 offset:11264\nds_read_b128 a[48:51],v240 offset:12288\nds_read_b128 a[52:55],v240 offset:13312\nds_read_b128 a[56:59],v240 offset:14336\nds_read_b128 a[60:63],v240 offset:15360\ns_cmp_eq_u32 s62,0\ns_cselect_b32 s36,0x8000,0\ns_add_u32 s36,s36,s64\n" \
"s_cmp_eq_u32 s36,s96\ns_cselect_b32 s72,0xc000,s98\ns_add_u32 s65,s36,s72\ns_cmp_eq_u32 s65,s96\ns_cselect_b32 s72,0xc000,s98\ns_add_u32 s66,s65,s72\ns_cmp_eq_u32 s66,s96\ns_cselect_b32 s72,0xc000,s98\n" \
"s_add_u32 s95,s66,s72\ns_add_u32 s97,s96,65536\ns_add_u32 s15,s64,s46\ns_cmp_gt_i32 s62,-1\ns_cselect_b32 s72,s98,0xfffe4000\ns_add_u32 s15,s15,s72\ns_mov_b32 s94,0xc000\ns_cmp_eq_u32 s62,7\n" \
"s_cselect_b32 s72,98304,0\ns_sub_u32 s97,s97,s72\ns_cmp_lt_u32 s62,4\ns_cselect_b32 s72,32768,0\ns_add_u32 s97,s97,s72\nv_and_b32 v251,31,v249\nv_lshlrev_b32 v251,2,v251\ns_lshl_b32 s80,s63,7\n" \
"s_add_u32 s80,s80,86016\nv_add_u32 v250,0xc000,v241\ns_waitcnt lgkmcnt(0)\ns_branch .Lattn\n.Lfin:\n" \
""
constexpr size_t WS_DBG = (size_t)51 << 20;
constexpr unsigned LDS_BYTES = 163840;
struct Args {
    const u16 *x, *ckv, *w_q, *w_uk, *w_uv, *w_o, *w_r, *wg, *wu, *wd, *cvec, *w_mod;
    char* ws; u16* out;
};
typedef const __attribute__((address_space(4))) Args* kargs_t;
#define STAMPD(i) do {} while (0)
#define C8(p, a) p #a "0", p #a "1", p #a "2", p #a "3", p #a "4", p #a "5", p #a "6", p #a "7", p #a "8", p #a "9"
#define C100(p, a) C8(p, a##0), C8(p, a##1), C8(p, a##2), C8(p, a##3), C8(p, a##4), C8(p, a##5), C8(p, a##6), C8(p, a##7), C8(p, a##8), C8(p, a##9)
#define C10_99(p) C8(p, 1), C8(p, 2), C8(p, 3), C8(p, 4), C8(p, 5), C8(p, 6), C8(p, 7), C8(p, 8), C8(p, 9)
#define C200_249(p) C8(p, 20), C8(p, 21), C8(p, 22), C8(p, 23), C8(p, 24)
#define BODY_CLOB "memory", "vcc", "scc", "s0", "s1", "s2", "s3", "s8", "s9", C8("s", 1), C8("s", 2), "s30", "s31", "s33", "s34", "s35", "s36", "s37", "s38", "s39", C8("s", 4), C8("s", 5), C8("s", 6), C8("s", 7), C8("s", 8), C8("s", 9), \
    C8("v", ), C10_99("v"), C100("v", 1), C200_249("v"), "v250", "v251", "v252", "v253", "v254", C8("a", ), C10_99("a"), C100("a", 1), C200_249("a"), "a250", "a251", "a252", "a253", "a254", "a255"

__global__ __launch_bounds__(256, 1) void k_all(const Args a_) {
    kargs_t ap = (kargs_t)__builtin_amdgcn_kernarg_segment_ptr(); unsigned bid = blockIdx.x, wid = rfl(threadIdx.x >> 6), sv_;
    asm volatile(BODY_ASM : "+{s[4:5]}"(ap), "+{s6}"(bid), "+{s7}"(wid), "={v255}"(sv_) :: BODY_CLOB);
    asm volatile(".Lcode_e:");
}
extern "C" void kernel_launch(void* const* d_in, const int* in_sizes, int n_in, void* d_out, int out_size, void* d_ws, size_t ws_size, hipStream_t stream) {
    Args a;
    a.x = (const u16*)d_in[0]; a.ckv = (const u16*)d_in[1]; a.w_q = (const u16*)d_in[2]; a.w_uk = (const u16*)d_in[3]; a.w_uv = (const u16*)d_in[4]; a.w_o = (const u16*)d_in[5];
    a.w_r = (const u16*)d_in[7]; a.wg = (const u16*)d_in[8]; a.wu = (const u16*)d_in[9]; a.wd = (const u16*)d_in[10]; a.cvec = (const u16*)d_in[11]; a.w_mod = (const u16*)d_in[12];
    a.ws = (char*)d_ws;
    a.out = (u16*)d_out;
    (void)hipFuncSetAttribute((const void*)k_all, hipFuncAttributeMaxDynamicSharedMemorySize, LDS_BYTES);
    k_all<<<256, 256, LDS_BYTES, stream>>>(a);
}
```

```cpp
#include <hip/hip_runtime.h>
#include <hip/hip_bf16.h>
#include <stdint.h>
typedef unsigned short u16;
#define DI __device__ __forceinline__
DI unsigned lane_id() { unsigned l; asm volatile("v_mbcnt_lo_u32_b32 %0, -1, 0\n\tv_mbcnt_hi_u32_b32 %0, -1, %0" : "=v"(l)); return l; }
DI unsigned rfl(unsigned x) { return __builtin_amdgcn_readfirstlane(x); }
#define BODY_ASM ".macro M a,b,c,d\nv_mfma_f32_16x16x32_bf16 \\a,\\b,\\c,\\d\n.endm\n" \
".Lcode_b:\ns_load_dwordx16 s[8:23],s[4:5],0x0\ns_load_dwordx8 s[24:31],s[4:5],0x40\ns_load_dwordx2 s[34:35],s[4:5],0x60\nv_mbcnt_lo_u32_b32 v249,-1,0\nv_mbcnt_hi_u32_b32 v249,-1,v249\ns_bfe_u32 s61,s6,0x20003\ns_lshl_b32 s64,s7,12\n" \
"s_and_b32 s60,s6,7\ns_lshr_b32 s62,s6,5\ns_lshl2_add_u32 s63,s60,s61\nv_lshlrev_b32 v240,4,v249\nv_lshrrev_b32 v0,3,v249\nv_and_b32 v2,7,v249\nv_bfe_u32 v6,v249,2,3\nv_lshrrev_b32 v7,5,v249\n" \
"v_and_b32 v8,48,v240\nv_lshlrev_b32 v6,13,v6\nv_lshl_or_b32 v7,v7,6,v8\nv_or_b32 v6,0x1000,v6\nv_or_b32 v243,v6,v7\nv_add_u32 v244,0xfffffc80,v243\nv_xor_b32 v6,32,v243\nv_add_u32 v245,63488,v6\n" \
"v_add_u32 v246,62592,v6\ns_lshl_b32 s68,s7,17\ns_lshl_b32 s70,s63,8\ns_add_u32 s68,s68,s70\ns_mov_b32 s42,4198400\ns_mov_b32 s43,0x20000\nv_lshrrev_b32 v6,4,v249\nv_xor_b32 v7,v6,v2\n" \
"v_lshlrev_b32 v7,4,v7\nv_lshl_or_b32 v224,v0,10,v7\nv_xor_b32 v7,64,v224\nv_add_u32 v225,7168,v7\nv_add_u32 v226,14336,v224\nv_add_u32 v227,21504,v7\ns_lshl_b32 s70,s62,1\ns_sub_u32 s71,s7,s70\n" \
"s_add_u32 s71,s71,30\ns_sub_u32 s72,s70,s7\ns_add_u32 s72,s72,3\ns_cmp_lt_u32 s7,2\ns_cselect_b32 s74,s71,s72\ns_lshl_b32 s66,s74,16\ns_mul_i32 s65,s7,24576\ns_mov_b32 s38,2097152\n" \
"s_mov_b32 s39,0x20000\ns_xor_b32 s94,s61,s62\ns_and_b32 s94,s94,1\ns_lshl_b32 s99,s94,19\ns_or_b32 s99,s99,s68\ns_waitcnt lgkmcnt(0)\ns_sub_u32 s40,s14,0x1000\ns_subb_u32 s41,s15,0\n" \
"s_mov_b64 s[36:37],s[10:11]\ns_add_u32 m0,s64,147456\ns_add_u32 s69,s68,0\nbuffer_load_dwordx4 v243,s[40:43],s69 offen offset:0 lds\nbuffer_load_dwordx4 v244,s[40:43],s69 offen offset:1024 lds\nbuffer_load_dwordx4 v245,s[40:43],s69 offen offset:2048 lds\nbuffer_load_dwordx4 v246,s[40:43],s69 offen offset:3072 lds\ns_add_u32 m0,s65,0\n" \
"s_xor_b32 s67,s66,0\nbuffer_load_dwordx4 v224,s[36:39],s67 offen offset:0 lds\nbuffer_load_dwordx4 v225,s[36:39],s67 offen offset:1024 lds\nbuffer_load_dwordx4 v226,s[36:39],s67 offen offset:2048 lds\nbuffer_load_dwordx4 v227,s[36:39],s67 offen offset:3072 lds\ns_add_u32 m0,s65,4096\ns_xor_b32 s67,s66,32768\nbuffer_load_dwordx4 v224,s[36:39],s67 offen offset:0 lds\n" \
"buffer_load_dwordx4 v225,s[36:39],s67 offen offset:1024 lds\nbuffer_load_dwordx4 v226,s[36:39],s67 offen offset:2048 lds\nbuffer_load_dwordx4 v227,s[36:39],s67 offen offset:3072 lds\ns_cmp_lt_u32 s6,192\ns_cselect_b32 s70,s6,0\ns_mul_i32 s71,s70,171\ns_lshr_b32 s71,s71,13\ns_mul_i32 s72,s71,48\n" \
"s_sub_u32 s70,s70,s72\ns_lshl2_add_u32 s72,s71,s7\ns_lshl_b32 s72,s72,5\nv_lshl_add_u32 v1,v0,2,s72\nv_mul_u32_u24 v3,6144,v1\nv_lshl_add_u32 v3,v2,4,v3\ns_lshl_b32 s72,s70,7\ns_add_u32 s72,s72,3072\n" \
"v_add_u32 v3,s72,v3\nv_add_u32 v4,12288,v3\nv_lshlrev_b32 v1,1,v1\nglobal_load_dwordx2 v[168:169],v1,s[28:29]\nglobal_load_dwordx4 v[152:155],v3,s[30:31] offset:-3072\nglobal_load_dwordx4 v[156:159],v3,s[30:31] offset:3072\nglobal_load_dwordx4 v[160:163],v4,s[30:31] offset:-3072\nglobal_load_dwordx4 v[164:167],v4,s[30:31] offset:3072\n" \
"s_mul_i32 s71,s71,12288\ns_lshl_b32 s70,s70,8\ns_add_u32 s79,s70,s71\ns_add_u32 s79,s79,786432\ns_cmp_eq_u32 s7,0\ns_cselect_b32 s75,0,0x8000000\ns_cmp_lt_u32 s6,192\ns_cselect_b32 s70,s75,0x8000000\n" \
"s_or_b32 s79,s79,s70\ns_mov_b64 s[52:53],s[34:35]\ns_mov_b32 s54,71303168\ns_mov_b32 s55,0x20000\ns_mov_b32 s70,94208\nbuffer_load_dwordx2 v[230:231],off,s[52:55],s70 sc1\ns_getpc_b64 s[56:57]\n.Lpc0:\n" \
"s_and_b32 s57,s57,0xffff\ns_mov_b32 s58,.Lcode_e-.Lpc0\ns_mov_b32 s59,0x20000\ns_lshr_b32 s70,s6,3\nv_and_b32 v251,1,v249\ns_lshl2_add_u32 s78,s70,s7\nv_lshlrev_b32 v251,14,v251\ns_lshl_b32 s78,s78,7\n" \
"buffer_load_dword v247,v251,s[56:59],s78 offen\nv_and_b32 v34,15,v249\nv_lshrrev_b32 v35,1,v34\nv_xor_b32 v35,v35,v6\nv_lshlrev_b32 v35,4,v35\nv_lshl_or_b32 v35,v34,7,v35\nv_add_u32 v228,s65,v35\nv_xor_b32 v229,64,v228\n" \
"v_bfe_u32 v34,v249,2,2\nv_and_b32 v35,3,v249\nv_lshlrev_b32 v35,3,v35\nv_lshl_or_b32 v35,v34,6,v35\nv_lshl_or_b32 v35,v6,11,v35\nv_and_b32 v34,16,v249\nv_lshl_or_b32 v35,v34,1,v35\nv_add_u32 v241,98304,v35\n" \
"v_xor_b32 v242,32,v241\ns_mov_b32 s73,0x400000\nv_mov_b32 v252,0xff800000\ns_mov_b32 s82,0x05040100\ns_mov_b32 s83,0x07060302\nv_lshlrev_b32 v190,5,v2\nv_lshl_or_b32 v190,v6,3,v190\ns_lshl_b32 s72,s7,8\n" \
"s_add_u32 s72,s72,114688\nv_add_u32 v190,s72,v190\ns_lshl_b32 s72,s7,3\ns_add_u32 s72,s72,115712\nv_lshl_add_u32 v191,v2,5,s72\nv_lshlrev_b32 v251,5,v2\nv_add_u32 v251,115712,v251\nv_lshlrev_b32 v250,2,v249\n" \
"v_add_u32 v250,114688,v250\ns_lshl_b32 s70,s62,7\ns_xor_b32 s71,s63,12\ns_lshl3_add_u32 s71,s71,s70\ns_lshl_b32 s72,s63,3\ns_sub_u32 s72,s72,s70\ns_add_u32 s72,s72,1792\ns_cmp_lt_u32 s63,16\n" \
"s_cselect_b32 s71,s71,s72\ns_lshl_b32 s71,s71,10\ns_lshl_b32 s72,s7,7\ns_add_u32 s71,s71,s72\nv_lshlrev_b32 v5,10,v2\nv_lshl_add_u32 v5,v0,4,v5\nv_add_u32 v5,s71,v5\ns_lshl_b32 s70,s6,11\n" \
"s_cmp_lt_u32 s6,128\ns_cselect_b32 s70,s70,0x8000000\ns_lshl_b32 s71,s7,9\ns_add_u32 s70,s70,s71\ns_add_u32 s80,s70,98304\nv_mov_b32 v28,-1\nv_mov_b32 v29,-1\nv_lshlrev_b32 v30,3,v249\n" \
"s_lshl_b32 s70,s6,7\ns_cmp_lt_u32 s6,224\ns_cselect_b32 s71,65536,0x40000000\ns_cmp_lt_u32 s6,128\ns_cselect_b32 s72,556928,0x40000000\nv_mov_b32 v31,s71\nv_mov_b32 v32,s72\nv_cmp_lt_u32 vcc,31,v249\n" \
"s_or_b32 s70,s70,s75\nv_mov_b32 v33,0\nv_cndmask_b32 v31,v31,v32,vcc\nv_lshl_add_u32 v31,v249,2,v31\ns_add_u32 m0,s64,98304\ns_add_u32 s69,s68,524288\nbuffer_load_dwordx4 v243,s[40:43],s69 offen offset:0 lds\nbuffer_load_dwordx4 v244,s[40:43],s69 offen offset:1024 lds\n" \
"buffer_load_dwordx4 v245,s[40:43],s69 offen offset:2048 lds\nbuffer_load_dwordx4 v246,s[40:43],s69 offen offset:3072 lds\ns_add_u32 m0,s65,8192\ns_xor_b32 s67,s66,128\nbuffer_load_dwordx4 v224,s[36:39],s67 offen offset:0 lds\nbuffer_load_dwordx4 v225,s[36:39],s67 offen offset:1024 lds\nbuffer_load_dwordx4 v226,s[36:39],s67 offen offset:2048 lds\nbuffer_load_dwordx4 v227,s[36:39],s67 offen offset:3072 lds\n" \
"s_add_u32 m0,s65,12288\ns_xor_b32 s67,s66,32896\nbuffer_load_dwordx4 v224,s[36:39],s67 offen offset:0 lds\nbuffer_load_dwordx4 v225,s[36:39],s67 offen offset:1024 lds\nbuffer_load_dwordx4 v226,s[36:39],s67 offen offset:2048 lds\nbuffer_load_dwordx4 v227,s[36:39],s67 offen offset:3072 lds\nglobal_load_dwordx4 v[128:131],v5,s[8:9]\nglobal_load_dwordx4 v[132:135],v5,s[8:9] offset:512\n" \
"buffer_store_dwordx2 v[28:29],v30,s[52:55],s80 offen sc1\nbuffer_store_dword v33,v31,s[52:55],s70 offen sc1\ns_waitcnt vmcnt(23)\ns_barrier\ns_add_u32 m0,s64,131072\ns_add_u32 s69,s68,1048576\nds_read_b128 a[0:3],v228 offset:0\nds_read_b128 a[4:7],v228 offset:2048\n" \
"ds_read_b128 a[8:11],v228 offset:4096\nds_read_b128 a[12:15],v228 offset:6144\nds_read_b128 a[16:19],v229 offset:0\nds_read_b128 a[20:23],v229 offset:2048\nds_read_b128 a[24:27],v229 offset:4096\nds_read_b128 a[28:31],v229 offset:6144\nds_read_b64_tr_b16 v[192:193],v241 offset:49152\nds_read_b64_tr_b16 v[194:195],v241 offset:49408\n" \
"ds_read_b64_tr_b16 v[196:197],v241 offset:57344\nds_read_b64_tr_b16 v[198:199],v241 offset:57600\nds_read_b64_tr_b16 v[200:201],v242 offset:49152\nds_read_b64_tr_b16 v[202:203],v242 offset:49408\nds_read_b64_tr_b16 v[204:205],v242 offset:57344\nds_read_b64_tr_b16 v[206:207],v242 offset:57600\nds_read_b64_tr_b16 v[208:209],v241 offset:49664\nds_read_b64_tr_b16 v[210:211],v241 offset:49920\n" \
"ds_read_b64_tr_b16 v[212:213],v241 offset:57856\nds_read_b64_tr_b16 v[214:215],v241 offset:58112\nds_read_b64_tr_b16 v[216:217],v242 offset:49664\nds_read_b64_tr_b16 v[218:219],v242 offset:49920\nds_read_b64_tr_b16 v[220:221],v242 offset:57856\nds_read_b64_tr_b16 v[222:223],v242 offset:58112\ns_waitcnt vmcnt(22)\nv_lshlrev_b32 v170,16,v168\n" \
"v_and_b32 v171,0xffff0000,v168\nv_lshlrev_b32 v172,16,v169\nv_and_b32 v173,0xffff0000,v169\nv_mul_f32 v184,0xbfb8aa3b,v170\nv_mul_f32 v185,0xbfb8aa3b,v171\nv_mul_f32 v186,0xbfb8aa3b,v172\nv_mul_f32 v187,0xbfb8aa3b,v173\nv_exp_f32 v184,v184\n" \
"v_exp_f32 v185,v185\nv_exp_f32 v186,v186\nv_exp_f32 v187,v187\nv_add_f32 v184,1.0,v184\nv_add_f32 v185,1.0,v185\nv_add_f32 v186,1.0,v186\nv_add_f32 v187,1.0,v187\nv_rcp_f32 v184,v184\n" \
"v_rcp_f32 v185,v185\nv_rcp_f32 v186,v186\nv_rcp_f32 v187,v187\nv_mul_f32 v170,v170,v184\nv_mul_f32 v171,v171,v185\nv_mul_f32 v172,v172,v186\nv_mul_f32 v173,v173,v187\nv_cvt_pk_bf16_f32 v170,v170,v171\n" \
"v_cvt_pk_bf16_f32 v171,v172,v173\ns_waitcnt lgkmcnt(5)\nM v[0:3],v[192:195],a[0:3],0\nbuffer_load_dwordx4 v243,s[40:43],s69 offen offset:0 lds\nM v[0:3],v[196:199],a[16:19],v[0:3]\nv_bfe_u32 v253,v249,1,2\ns_lshl2_add_u32 s76,s62,s7\nv_and_b32 v248,1,v249\n" \
"v_lshlrev_b32 v253,13,v253\ns_mul_i32 s76,s76,32768\ns_lshl_b32 s77,s63,8\nM v[4:7],v[192:195],a[4:7],0\nv_lshl_or_b32 v253,v248,7,v253\ns_add_u32 s76,s76,s77\nv_and_b32 v254,7,v249\nv_add_u32 v253,s76,v253\n" \
"M v[4:7],v[196:199],a[20:23],v[4:7]\nv_and_b32 v234,7,v249\ns_lshl3_add_u32 s72,s6,s75\nM v[64:67],v[192:195],a[8:11],0\nv_lshlrev_b32 v234,11,v234\ns_add_u32 s72,s72,573440\nM v[64:67],v[196:199],a[24:27],v[64:67]\nbuffer_load_dwordx4 v244,s[40:43],s69 offen offset:1024 lds\n" \
"M v[68:71],v[192:195],a[12:15],0\ns_waitcnt vmcnt(22)\nv_perm_b32 v182,v156,v152,s82\nv_perm_b32 v183,v156,v152,s83\nv_perm_b32 v184,v157,v153,s82\nv_perm_b32 v185,v157,v153,s83\nv_perm_b32 v186,v158,v154,s82\nv_perm_b32 v187,v158,v154,s83\n" \
"v_perm_b32 v188,v159,v155,s82\nv_perm_b32 v189,v159,v155,s83\nv_dot2_f32_bf16 v174,v182,v170,0\nv_dot2_f32_bf16 v175,v183,v170,0\nv_dot2_f32_bf16 v176,v184,v170,0\nv_dot2_f32_bf16 v177,v185,v170,0\nv_dot2_f32_bf16 v178,v186,v170,0\nv_dot2_f32_bf16 v179,v187,v170,0\n" \
"v_dot2_f32_bf16 v180,v188,v170,0\nv_dot2_f32_bf16 v181,v189,v170,0\ns_waitcnt vmcnt(20)\nv_perm_b32 v182,v164,v160,s82\nv_perm_b32 v183,v164,v160,s83\nv_perm_b32 v184,v165,v161,s82\nv_perm_b32 v185,v165,v161,s83\nv_perm_b32 v186,v166,v162,s82\n" \
"v_perm_b32 v187,v166,v162,s83\nv_perm_b32 v188,v167,v163,s82\nv_perm_b32 v189,v167,v163,s83\nv_dot2_f32_bf16 v174,v182,v171,v174\nv_dot2_f32_bf16 v175,v183,v171,v175\nv_dot2_f32_bf16 v176,v184,v171,v176\nv_dot2_f32_bf16 v177,v185,v171,v177\nv_dot2_f32_bf16 v178,v186,v171,v178\n" \
"v_dot2_f32_bf16 v179,v187,v171,v179\nv_dot2_f32_bf16 v180,v188,v171,v180\nv_dot2_f32_bf16 v181,v189,v171,v181\nv_permlane32_swap_b32 v174,v178\nv_permlane32_swap_b32 v175,v179\nv_permlane32_swap_b32 v176,v180\nv_permlane32_swap_b32 v177,v181\nv_add_f32 v174,v174,v178\n" \
"v_add_f32 v175,v175,v179\nv_add_f32 v176,v176,v180\nv_add_f32 v177,v177,v181\nds_read_b64_tr_b16 v[192:193],v241 offset:50176\nv_permlane16_swap_b32 v174,v176\nv_permlane16_swap_b32 v175,v177\nv_add_f32 v174,v174,v176\nv_add_f32 v175,v175,v177\n" \
"ds_read_b64_tr_b16 v[194:195],v241 offset:50432\nv_add_f32_dpp v174,v174,v174 row_ror:8 row_mask:0xf bank_mask:0xf\nv_add_f32_dpp v175,v175,v175 row_ror:8 row_mask:0xf bank_mask:0xf\nds_write_b64 v190,v[174:175]\nM v[68:71],v[196:199],a[28:31],v[68:71]\nds_read_b64_tr_b16 v[196:197],v241 offset:58368\nds_read_b64_tr_b16 v[198:199],v241 offset:58624\nM v[8:11],v[200:203],a[0:3],0\n" \
"s_waitcnt vmcnt(18)\nv_readfirstlane_b32 s92,v230\nv_readfirstlane_b32 s93,v231\nM v[8:11],v[204:207],a[16:19],v[8:11]\ns_add_u32 s70,s92,1\ns_addc_u32 s71,s93,0\nM v[12:15],v[200:203],a[4:7],0\nbuffer_load_dwordx4 v245,s[40:43],s69 offen offset:2048 lds\n" \
"M v[12:15],v[204:207],a[20:23],v[12:15]\ns_add_u32 s92,s92,0x7f4a7c15\ns_addc_u32 s93,s93,0x9e3779b9\nM v[72:75],v[200:203],a[8:11],0\nv_mov_b32 v230,s92\nv_mov_b32 v231,s93\nM v[72:75],v[204:207],a[24:27],v[72:75]\ns_lshl2_add_u32 s80,s60,s7\n" \
"s_lshl_b32 s80,s80,9\nM v[76:79],v[200:203],a[12:15],0\nds_read_b64_tr_b16 v[200:201],v242 offset:50176\nds_read_b64_tr_b16 v[202:203],v242 offset:50432\nM v[76:79],v[204:207],a[28:31],v[76:79]\nbuffer_load_dwordx4 v246,s[40:43],s69 offen offset:3072 lds\nds_read_b64_tr_b16 v[204:205],v242 offset:58368\nds_read_b64_tr_b16 v[206:207],v242 offset:58624\n" \
"M v[16:19],v[208:211],a[0:3],0\ns_add_u32 s80,s80,573440\ns_waitcnt lgkmcnt(6)\nM v[16:19],v[212:215],a[16:19],v[16:19]\ns_add_u32 m0,s65,16384\ns_xor_b32 s67,s66,256\nM v[20:23],v[208:211],a[4:7],0\nv_mov_b32 v232,s70\n" \
"v_mov_b32 v233,s71\nM v[20:23],v[212:215],a[20:23],v[20:23]\ns_lshr_b32 s70,s64,2\ns_add_u32 s71,s70,786432\nM v[80:83],v[208:211],a[8:11],0\nbuffer_load_dwordx4 v224,s[36:39],s67 offen offset:0 lds\nM v[80:83],v[212:215],a[24:27],v[80:83]\nv_lshlrev_b32 v254,14,v254\n" \
"s_add_u32 s78,s78,32768\nM v[84:87],v[208:211],a[12:15],0\nds_read_b64_tr_b16 v[208:209],v241 offset:50688\nds_read_b64_tr_b16 v[210:211],v241 offset:50944\nM v[84:87],v[212:215],a[28:31],v[84:87]\nds_read_b64_tr_b16 v[212:213],v241 offset:58880\nds_read_b64_tr_b16 v[214:215],v241 offset:59136\nM v[24:27],v[216:219],a[0:3],0\n" \
"s_mov_b32 s98,0x4000\ns_mov_b32 s0,28672\nM v[24:27],v[220:223],a[16:19],v[24:27]\nbuffer_load_dwordx4 v225,s[36:39],s67 offen offset:1024 lds\nM v[28:31],v[216:219],a[4:7],0\ns_add_u32 s10,s61,1\ns_add_u32 s11,s61,2\nM v[28:31],v[220:223],a[20:23],v[28:31]\n" \
"s_add_u32 s14,s61,3\ns_and_b32 s10,s10,3\nM v[88:91],v[216:219],a[8:11],0\ns_and_b32 s11,s11,3\ns_and_b32 s14,s14,3\nM v[88:91],v[220:223],a[24:27],v[88:91]\ns_mov_b32 s47,0x20000\ns_mov_b32 s51,0x20000\n" \
"M v[92:95],v[216:219],a[12:15],0\nbuffer_load_dwordx4 v226,s[36:39],s67 offen offset:2048 lds\nds_read_b64_tr_b16 v[216:217],v242 offset:50688\nds_read_b64_tr_b16 v[218:219],v242 offset:50944\nM v[92:95],v[220:223],a[28:31],v[92:95]\nds_read_b64_tr_b16 v[220:221],v242 offset:58880\nds_read_b64_tr_b16 v[222:223],v242 offset:59136\nM v[32:35],v[192:195],a[0:3],0\n" \
"s_lshl_b32 s10,s10,13\ns_waitcnt lgkmcnt(6)\nM v[32:35],v[196:199],a[16:19],v[32:35]\ns_lshl_b32 s11,s11,13\ns_lshl_b32 s14,s14,13\nM v[36:39],v[192:195],a[4:7],0\ns_mov_b32 s82,0xf0000\ns_mov_b32 s83,0xfff00ff\n" \
"M v[36:39],v[196:199],a[20:23],v[36:39]\nbuffer_load_dwordx4 v227,s[36:39],s67 offen offset:3072 lds\nM v[96:99],v[192:195],a[8:11],0\ns_mov_b32 s84,0x1f0001\ns_mov_b32 s85,0x1fff01ff\nM v[96:99],v[196:199],a[24:27],v[96:99]\ns_add_u32 m0,s65,20480\ns_xor_b32 s67,s66,33024\n" \
"M v[100:103],v[192:195],a[12:15],0\ns_mov_b32 s86,0x3f0003\ns_mov_b32 s87,0x3fff03ff\nM v[100:103],v[196:199],a[28:31],v[100:103]\ns_mov_b32 s88,0x7f0007\ns_mov_b32 s89,0x7fff07ff\nM v[40:43],v[200:203],a[0:3],0\nbuffer_load_dwordx4 v224,s[36:39],s67 offen offset:0 lds\n" \
"s_waitcnt vmcnt(11)\nv_lshlrev_b32 v136,16,v128\nv_and_b32 v137,0xffff0000,v128\nv_lshlrev_b32 v138,16,v129\nv_and_b32 v139,0xffff0000,v129\nv_lshlrev_b32 v140,16,v130\nv_and_b32 v141,0xffff0000,v130\nv_lshlrev_b32 v142,16,v131\n" \
"v_and_b32 v143,0xffff0000,v131\nv_lshlrev_b32 v144,16,v132\nv_and_b32 v145,0xffff0000,v132\nv_lshlrev_b32 v146,16,v133\nv_and_b32 v147,0xffff0000,v133\nv_lshlrev_b32 v148,16,v134\nv_and_b32 v149,0xffff0000,v134\nv_lshlrev_b32 v150,16,v135\n" \
"v_and_b32 v151,0xffff0000,v135\nv_add_f32 v168,v136,v140\nv_add_f32 v128,v144,v148\nv_add_f32 v169,v137,v141\nv_add_f32 v129,v145,v149\nv_add_f32 v170,v138,v142\nv_add_f32 v130,v146,v150\nv_add_f32 v171,v139,v143\n" \
"v_add_f32 v131,v147,v151\nv_add_f32 v168,v168,v128\nv_add_f32 v169,v169,v129\nv_add_f32 v170,v170,v130\nv_add_f32 v171,v171,v131\nv_add_f32 v168,v168,v169\nv_add_f32 v170,v170,v171\nv_add_f32 v168,v168,v170\n" \
"M v[40:43],v[204:207],a[16:19],v[40:43]\ns_mov_b64 s[90:91],-1\nv_add_f32_dpp v168,v168,v168 row_ror:8 row_mask:0xf bank_mask:0xf\nv_mov_b32 v170,v168\nM v[44:47],v[200:203],a[4:7],0\ns_mov_b32 s28,0x5f800000\nv_permlane16_swap_b32 v168,v170\nv_add_f32 v168,v168,v170\n" \
"v_mov_b32 v170,v168\ns_nop 1\nv_permlane32_swap_b32 v168,v170\nv_add_f32 v168,v168,v170\nv_mul_f32 v168,0x3c000000,v168\nv_sub_f32 v152,v136,v168\nv_sub_f32 v153,v137,v168\nv_sub_f32 v154,v138,v168\n" \
"v_sub_f32 v155,v139,v168\nv_sub_f32 v156,v140,v168\nv_sub_f32 v157,v141,v168\nv_sub_f32 v158,v142,v168\nv_sub_f32 v159,v143,v168\nv_mul_f32 v128,v152,v152\nv_mul_f32 v129,v153,v153\nv_mul_f32 v130,v154,v154\n" \
"v_mul_f32 v131,v155,v155\nv_sub_f32 v160,v144,v168\nv_sub_f32 v161,v145,v168\nv_sub_f32 v162,v146,v168\nv_sub_f32 v163,v147,v168\nv_fmac_f32 v128,v156,v156\nv_fmac_f32 v129,v157,v157\nv_fmac_f32 v130,v158,v158\n" \
"v_fmac_f32 v131,v159,v159\nv_sub_f32 v164,v148,v168\nv_sub_f32 v165,v149,v168\nv_sub_f32 v166,v150,v168\nv_sub_f32 v167,v151,v168\nv_fmac_f32 v128,v160,v160\nv_fmac_f32 v129,v161,v161\nv_fmac_f32 v130,v162,v162\n" \
"v_fmac_f32 v131,v163,v163\nv_fmac_f32 v128,v164,v164\nv_fmac_f32 v129,v165,v165\nv_fmac_f32 v130,v166,v166\nv_fmac_f32 v131,v167,v167\nv_add_f32 v128,v128,v129\nv_add_f32 v130,v130,v131\nv_add_f32 v169,v128,v130\n" \
"s_nop 1\nv_add_f32_dpp v169,v169,v169 row_ror:8 row_mask:0xf bank_mask:0xf\nv_mov_b32 v170,v169\ns_nop 1\nv_permlane16_swap_b32 v169,v170\nv_add_f32 v169,v169,v170\nv_mov_b32 v170,v169\ns_nop 1\n" \
"v_permlane32_swap_b32 v169,v170\nv_add_f32 v169,v169,v170\nds_write_b64 v191,v[168:169]\nM v[44:47],v[204:207],a[20:23],v[44:47]\nv_and_b32 v169,3,v249\nv_mul_u32_u24 v169,12288,v169\nM v[104:107],v[200:203],a[8:11],0\nv_and_b32 v170,28,v249\n" \
"v_add_u32 v169,v169,v170\nM v[104:107],v[204:207],a[24:27],v[104:107]\nbuffer_load_dwordx4 v225,s[36:39],s67 offen offset:1024 lds\nM v[108:111],v[200:203],a[12:15],0\nv_lshlrev_b32 v170,3,v170\nM v[108:111],v[204:207],a[28:31],v[108:111]\nM v[48:51],v[208:211],a[0:3],0\ns_waitcnt lgkmcnt(1)\n" \
"M v[48:51],v[212:215],a[16:19],v[48:51]\nM v[52:55],v[208:211],a[4:7],0\nbuffer_load_dwordx4 v226,s[36:39],s67 offen offset:2048 lds\nM v[52:55],v[212:215],a[20:23],v[52:55]\nM v[112:115],v[208:211],a[8:11],0\ns_barrier\nds_read_b128 a[32:35],v228 offset:8192\nds_read_b128 a[36:39],v228 offset:10240\n" \
"ds_read_b128 a[40:43],v228 offset:12288\nds_read_b128 a[44:47],v228 offset:14336\nds_read_b128 a[48:51],v229 offset:8192\nds_read_b128 a[52:55],v229 offset:10240\nds_read_b128 a[56:59],v229 offset:12288\nds_read_b128 a[60:63],v229 offset:14336\nds_read_b32 v184,v250 offset:0\nds_read_b32 v185,v250 offset:256\n" \
"ds_read_b32 v186,v250 offset:512\nds_read_b32 v187,v250 offset:768\ns_waitcnt lgkmcnt(2)\nv_add_f32 v184,v184,v185\ns_waitcnt lgkmcnt(0)\nv_add_f32 v186,v186,v187\nv_lshlrev_b32 v185,2,v249\nv_add_f32 v184,v184,v186\n" \
"buffer_store_dword v184,v185,s[52:55],s79 offen sc1\nds_read_b64_tr_b16 v[192:193],v241 offset:0\nds_read_b64_tr_b16 v[194:195],v241 offset:256\nds_read_b64_tr_b16 v[196:197],v241 offset:8192\nds_read_b64_tr_b16 v[198:199],v241 offset:8448\nds_read_b64_tr_b16 v[200:201],v242 offset:0\nds_read_b64_tr_b16 v[202:203],v242 offset:256\nds_read_b64_tr_b16 v[204:205],v242 offset:8192\n" \
"ds_read_b64_tr_b16 v[206:207],v242 offset:8448\nM v[112:115],v[212:215],a[24:27],v[112:115]\nv_bfe_u32 v250,v249,3,2\nM v[116:119],v[208:211],a[12:15],0\nds_read_b64_tr_b16 v[208:209],v241 offset:512\nds_read_b64_tr_b16 v[210:211],v241 offset:768\nM v[116:119],v[212:215],a[28:31],v[116:119]\nbuffer_load_dwordx4 v227,s[36:39],s67 offen offset:3072 lds\n" \
"ds_read_b64_tr_b16 v[212:213],v241 offset:8704\nds_read_b64_tr_b16 v[214:215],v241 offset:8960\nM v[56:59],v[216:219],a[0:3],0\nM v[56:59],v[220:223],a[16:19],v[56:59]\ns_add_u32 m0,s65,0\ns_xor_b32 s67,s66,384\nM v[60:63],v[216:219],a[4:7],0\nM v[60:63],v[220:223],a[20:23],v[60:63]\n" \
"M v[120:123],v[216:219],a[8:11],0\nbuffer_load_dwordx4 v224,s[36:39],s67 offen offset:0 lds\nM v[120:123],v[220:223],a[24:27],v[120:123]\nM v[124:127],v[216:219],a[12:15],0\nds_read_b64_tr_b16 v[216:217],v242 offset:512\nds_read_b64_tr_b16 v[218:219],v242 offset:768\nM v[124:127],v[220:223],a[28:31],v[124:127]\nds_read_b64_tr_b16 v[220:221],v242 offset:8704\n" \
"ds_read_b64_tr_b16 v[222:223],v242 offset:8960\ns_waitcnt lgkmcnt(11)\nM v[0:3],v[192:195],a[32:35],v[0:3]\nM v[0:3],v[196:199],a[48:51],v[0:3]\nbuffer_load_dwordx4 v225,s[36:39],s67 offen offset:1024 lds\nM v[4:7],v[192:195],a[36:39],v[4:7]\nM v[4:7],v[196:199],a[52:55],v[4:7]\nM v[64:67],v[192:195],a[40:43],v[64:67]\n" \
"M v[64:67],v[196:199],a[56:59],v[64:67]\nM v[68:71],v[192:195],a[44:47],v[68:71]\nbuffer_load_dwordx4 v226,s[36:39],s67 offen offset:2048 lds\nds_read_b64_tr_b16 v[192:193],v241 offset:1024\nds_read_b64_tr_b16 v[194:195],v241 offset:1280\nM v[68:71],v[196:199],a[60:63],v[68:71]\nds_read_b64_tr_b16 v[196:197],v241 offset:9216\nds_read_b64_tr_b16 v[198:199],v241 offset:9472\n" \
"s_waitcnt lgkmcnt(8)\nM v[8:11],v[200:203],a[32:35],v[8:11]\nM v[8:11],v[204:207],a[48:51],v[8:11]\nM v[12:15],v[200:203],a[36:39],v[12:15]\nM v[12:15],v[204:207],a[52:55],v[12:15]\nbuffer_load_dwordx4 v227,s[36:39],s67 offen offset:3072 lds\nM v[72:75],v[200:203],a[40:43],v[72:75]\nM v[72:75],v[204:207],a[56:59],v[72:75]\n" \
"s_add_u32 m0,s65,4096\ns_xor_b32 s67,s66,33152\nM v[76:79],v[200:203],a[44:47],v[76:79]\nds_read_b64_tr_b16 v[200:201],v242 offset:1024\nds_read_b64_tr_b16 v[202:203],v242 offset:1280\nM v[76:79],v[204:207],a[60:63],v[76:79]\nds_read_b64_tr_b16 v[204:205],v242 offset:9216\nds_read_b64_tr_b16 v[206:207],v242 offset:9472\n" \
"M v[16:19],v[208:211],a[32:35],v[16:19]\nbuffer_load_dwordx4 v224,s[36:39],s67 offen offset:0 lds\nM v[16:19],v[212:215],a[48:51],v[16:19]\nM v[20:23],v[208:211],a[36:39],v[20:23]\nM v[20:23],v[212:215],a[52:55],v[20:23]\nM v[80:83],v[208:211],a[40:43],v[80:83]\nM v[80:83],v[212:215],a[56:59],v[80:83]\nbuffer_load_dwordx4 v225,s[36:39],s67 offen offset:1024 lds\n" \
"M v[84:87],v[208:211],a[44:47],v[84:87]\nds_read_b64_tr_b16 v[208:209],v241 offset:1536\nds_read_b64_tr_b16 v[210:211],v241 offset:1792\nM v[84:87],v[212:215],a[60:63],v[84:87]\nds_read_b64_tr_b16 v[212:213],v241 offset:9728\nds_read_b64_tr_b16 v[214:215],v241 offset:9984\ns_waitcnt lgkmcnt(8)\nM v[24:27],v[216:219],a[32:35],v[24:27]\n" \
"M v[24:27],v[220:223],a[48:51],v[24:27]\nM v[28:31],v[216:219],a[36:39],v[28:31]\nbuffer_load_dwordx4 v226,s[36:39],s67 offen offset:2048 lds\nM v[28:31],v[220:223],a[52:55],v[28:31]\nM v[88:91],v[216:219],a[40:43],v[88:91]\nM v[88:91],v[220:223],a[56:59],v[88:91]\nM v[92:95],v[216:219],a[44:47],v[92:95]\nds_read_b64_tr_b16 v[216:217],v242 offset:1536\n" \
"ds_read_b64_tr_b16 v[218:219],v242 offset:1792\nM v[92:95],v[220:223],a[60:63],v[92:95]\nbuffer_load_dwordx4 v227,s[36:39],s67 offen offset:3072 lds\nds_read_b64_tr_b16 v[220:221],v242 offset:9728\nds_read_b64_tr_b16 v[222:223],v242 offset:9984\nM v[32:35],v[192:195],a[32:35],v[32:35]\nM v[32:35],v[196:199],a[48:51],v[32:35]\ns_add_u32 m0,s64,147456\n" \
"s_add_u32 s69,s68,1572864\nM v[36:39],v[192:195],a[36:39],v[36:39]\nM v[36:39],v[196:199],a[52:55],v[36:39]\nM v[96:99],v[192:195],a[40:43],v[96:99]\nbuffer_load_dwordx4 v243,s[40:43],s69 offen offset:0 lds\nM v[96:99],v[196:199],a[56:59],v[96:99]\nM v[100:103],v[192:195],a[44:47],v[100:103]\nM v[100:103],v[196:199],a[60:63],v[100:103]\n" \
"s_waitcnt lgkmcnt(4)\nM v[40:43],v[200:203],a[32:35],v[40:43]\nM v[40:43],v[204:207],a[48:51],v[40:43]\nbuffer_load_dwordx4 v244,s[40:43],s69 offen offset:1024 lds\nM v[44:47],v[200:203],a[36:39],v[44:47]\nM v[44:47],v[204:207],a[52:55],v[44:47]\nM v[104:107],v[200:203],a[40:43],v[104:107]\nM v[104:107],v[204:207],a[56:59],v[104:107]\n" \
"M v[108:111],v[200:203],a[44:47],v[108:111]\nbuffer_load_dwordx4 v245,s[40:43],s69 offen offset:2048 lds\nM v[108:111],v[204:207],a[60:63],v[108:111]\nM v[48:51],v[208:211],a[32:35],v[48:51]\nM v[48:51],v[212:215],a[48:51],v[48:51]\nM v[52:55],v[208:211],a[36:39],v[52:55]\nM v[52:55],v[212:215],a[52:55],v[52:55]\nbuffer_load_dwordx4 v246,s[40:43],s69 offen offset:3072 lds\n" \
"M v[112:115],v[208:211],a[40:43],v[112:115]\ns_waitcnt vmcnt(12) lgkmcnt(0)\ns_barrier\nds_read_b128 a[64:67],v228 offset:16384\nds_read_b128 a[68:71],v228 offset:18432\nds_read_b128 a[72:75],v228 offset:20480\nds_read_b128 a[76:79],v228 offset:22528\nds_read_b128 a[80:83],v229 offset:16384\n" \
"ds_read_b128 a[84:87],v229 offset:18432\nds_read_b128 a[88:91],v229 offset:20480\nds_read_b128 a[92:95],v229 offset:22528\nM v[112:115],v[212:215],a[56:59],v[112:115]\ns_add_u32 m0,s65,8192\ns_xor_b32 s67,s66,512\nds_read_b64_tr_b16 v[192:193],v241 offset:32768\nds_read_b64_tr_b16 v[194:195],v241 offset:33024\n" \
"M v[116:119],v[208:211],a[44:47],v[116:119]\nbuffer_store_dwordx2 v[230:231],v234,s[52:55],s72 offen sc1\nds_read_b64_tr_b16 v[196:197],v241 offset:40960\nds_read_b64_tr_b16 v[198:199],v241 offset:41216\nM v[116:119],v[212:215],a[60:63],v[116:119]\ns_cmp_eq_u32 s6,0\ns_cselect_b32 s72,s75,0x8000000\nM v[56:59],v[216:219],a[32:35],v[56:59]\n" \
"buffer_load_dwordx4 v224,s[36:39],s67 offen offset:0 lds\nM v[56:59],v[220:223],a[48:51],v[56:59]\ns_add_u32 s72,s72,94208\nM v[60:63],v[216:219],a[36:39],v[60:63]\nM v[60:63],v[220:223],a[52:55],v[60:63]\nds_read_b64_tr_b16 v[200:201],v242 offset:32768\nds_read_b64_tr_b16 v[202:203],v242 offset:33024\nds_read_b64_tr_b16 v[204:205],v242 offset:40960\n" \
"ds_read_b64_tr_b16 v[206:207],v242 offset:41216\nds_read_b64_tr_b16 v[208:209],v241 offset:33280\nds_read_b64_tr_b16 v[210:211],v241 offset:33536\nds_read_b64_tr_b16 v[212:213],v241 offset:41472\nds_read_b64_tr_b16 v[214:215],v241 offset:41728\nM v[120:123],v[216:219],a[40:43],v[120:123]\nM v[120:123],v[220:223],a[56:59],v[120:123]\nbuffer_load_dwordx4 v225,s[36:39],s67 offen offset:1024 lds\n" \
"M v[124:127],v[216:219],a[44:47],v[124:127]\nds_read_b64_tr_b16 v[216:217],v242 offset:33280\nds_read_b64_tr_b16 v[218:219],v242 offset:33536\nM v[124:127],v[220:223],a[60:63],v[124:127]\nds_read_b64_tr_b16 v[220:221],v242 offset:41472\nds_read_b64_tr_b16 v[222:223],v242 offset:41728\ns_waitcnt lgkmcnt(14)\nM v[0:3],v[192:195],a[64:67],v[0:3]\n" \
"s_waitcnt lgkmcnt(12)\nM v[0:3],v[196:199],a[80:83],v[0:3]\nM v[4:7],v[192:195],a[68:71],v[4:7]\nbuffer_load_dwordx4 v226,s[36:39],s67 offen offset:2048 lds\nM v[4:7],v[196:199],a[84:87],v[4:7]\nds_read_b128 v[156:159],v251\nM v[64:67],v[192:195],a[72:75],v[64:67]\nds_read_b128 v[160:163],v251 offset:16\n" \
"v_lshrrev_b32 v251,3,v249\nM v[64:67],v[196:199],a[88:91],v[64:67]\nM v[68:71],v[192:195],a[76:79],v[68:71]\nds_read_b64_tr_b16 v[192:193],v241 offset:33792\nds_read_b64_tr_b16 v[194:195],v241 offset:34048\nM v[68:71],v[196:199],a[92:95],v[68:71]\nbuffer_load_dwordx4 v227,s[36:39],s67 offen offset:3072 lds\nds_read_b64_tr_b16 v[196:197],v241 offset:41984\n" \
"ds_read_b64_tr_b16 v[198:199],v241 offset:42240\ns_waitcnt lgkmcnt(10)\nM v[8:11],v[200:203],a[64:67],v[8:11]\nM v[8:11],v[204:207],a[80:83],v[8:11]\ns_add_u32 m0,s65,12288\ns_xor_b32 s67,s66,33280\nM v[12:15],v[200:203],a[68:71],v[12:15]\nM v[12:15],v[204:207],a[84:87],v[12:15]\n" \
"M v[72:75],v[200:203],a[72:75],v[72:75]\nbuffer_load_dwordx4 v224,s[36:39],s67 offen offset:0 lds\nM v[72:75],v[204:207],a[88:91],v[72:75]\ns_waitcnt lgkmcnt(5)\nv_add_f32 v164,v156,v158\nM v[76:79],v[200:203],a[76:79],v[76:79]\nds_read_b64_tr_b16 v[200:201],v242 offset:33792\nds_read_b64_tr_b16 v[202:203],v242 offset:34048\n" \
"M v[76:79],v[204:207],a[92:95],v[76:79]\nds_read_b64_tr_b16 v[204:205],v242 offset:41984\nds_read_b64_tr_b16 v[206:207],v242 offset:42240\nM v[16:19],v[208:211],a[64:67],v[16:19]\ns_waitcnt lgkmcnt(8)\nv_add_f32 v165,v160,v162\nM v[16:19],v[212:215],a[80:83],v[16:19]\nbuffer_load_dwordx4 v225,s[36:39],s67 offen offset:1024 lds\n" \
"M v[20:23],v[208:211],a[68:71],v[20:23]\nv_add_f32 v164,v164,v165\nv_lshlrev_b32 v165,3,v249\nM v[20:23],v[212:215],a[84:87],v[20:23]\nv_mul_f32 v164,0x3e800000,v164\nv_sub_f32 v156,v156,v164\nM v[80:83],v[208:211],a[72:75],v[80:83]\nv_sub_f32 v158,v158,v164\n" \
"v_mul_f32 v156,v156,v156\nM v[80:83],v[212:215],a[88:91],v[80:83]\nv_sub_f32 v160,v160,v164\nv_fmac_f32 v156,v158,v158\nM v[84:87],v[208:211],a[76:79],v[84:87]\nbuffer_load_dwordx4 v226,s[36:39],s67 offen offset:2048 lds\nds_read_b64_tr_b16 v[208:209],v241 offset:34304\nds_read_b64_tr_b16 v[210:211],v241 offset:34560\n" \
"M v[84:87],v[212:215],a[92:95],v[84:87]\nds_read_b64_tr_b16 v[212:213],v241 offset:42496\nds_read_b64_tr_b16 v[214:215],v241 offset:42752\nM v[24:27],v[216:219],a[64:67],v[24:27]\nv_add_f32 v157,v157,v159\nv_add_f32 v161,v161,v163\nM v[24:27],v[220:223],a[80:83],v[24:27]\nv_sub_f32 v162,v162,v164\n" \
"v_fmac_f32 v156,v160,v160\nM v[28:31],v[216:219],a[68:71],v[28:31]\nglobal_load_dword v247,v253,s[16:17]\nM v[28:31],v[220:223],a[84:87],v[28:31]\nbuffer_load_dwordx4 v227,s[36:39],s67 offen offset:3072 lds\nM v[88:91],v[216:219],a[72:75],v[88:91]\nv_add_u32 v253,0x100000,v253\nv_add_f32 v157,v157,v161\n" \
"M v[88:91],v[220:223],a[88:91],v[88:91]\ns_add_u32 m0,s64,98304\ns_add_u32 s69,s68,2097152\nM v[92:95],v[216:219],a[76:79],v[92:95]\nds_read_b64_tr_b16 v[216:217],v242 offset:34304\nds_read_b64_tr_b16 v[218:219],v242 offset:34560\nM v[92:95],v[220:223],a[92:95],v[92:95]\nds_read_b64_tr_b16 v[220:221],v242 offset:42496\n" \
"ds_read_b64_tr_b16 v[222:223],v242 offset:42752\ns_waitcnt lgkmcnt(8)\nM v[32:35],v[192:195],a[64:67],v[32:35]\nbuffer_load_dwordx4 v243,s[40:43],s69 offen offset:0 lds\nM v[32:35],v[196:199],a[80:83],v[32:35]\nv_fmac_f32 v156,v162,v162\nv_fmac_f32 v157,0x43000000,v156\nM v[36:39],v[192:195],a[68:71],v[36:39]\n" \
"v_mul_f32 v157,0x3b000000,v157\nv_cvt_pk_bf16_f32 v157,0,v157\nM v[36:39],v[196:199],a[84:87],v[36:39]\nv_add_f32 v157,0x3727c5ac,v157\nv_cvt_pk_bf16_f32 v157,0,v157\nM v[96:99],v[192:195],a[72:75],v[96:99]\nv_sqrt_f32 v157,v157\nM v[96:99],v[196:199],a[88:91],v[96:99]\n" \
"buffer_load_dwordx4 v244,s[40:43],s69 offen offset:1024 lds\nM v[100:103],v[192:195],a[76:79],v[100:103]\nv_cvt_pk_bf16_f32 v164,0,v164\nv_cvt_pk_bf16_f32 v157,0,v157\nM v[100:103],v[196:199],a[92:95],v[100:103]\nv_rcp_f32 v157,v157\nM v[40:43],v[200:203],a[64:67],v[40:43]\nv_sub_f32 v136,v136,v164\n" \
"v_sub_f32 v137,v137,v164\nM v[40:43],v[204:207],a[80:83],v[40:43]\nv_sub_f32 v138,v138,v164\nv_sub_f32 v139,v139,v164\nM v[44:47],v[200:203],a[68:71],v[44:47]\nbuffer_load_dwordx4 v245,s[40:43],s69 offen offset:2048 lds\nM v[44:47],v[204:207],a[84:87],v[44:47]\nv_sub_f32 v140,v140,v164\n" \
"v_sub_f32 v141,v141,v164\nM v[104:107],v[200:203],a[72:75],v[104:107]\nv_sub_f32 v142,v142,v164\nv_sub_f32 v143,v143,v164\nM v[104:107],v[204:207],a[88:91],v[104:107]\nv_sub_f32 v144,v144,v164\nv_sub_f32 v145,v145,v164\nM v[108:111],v[200:203],a[76:79],v[108:111]\n" \
"v_sub_f32 v146,v146,v164\nv_sub_f32 v147,v147,v164\nM v[108:111],v[204:207],a[92:95],v[108:111]\nbuffer_load_dwordx4 v246,s[40:43],s69 offen offset:3072 lds\ns_waitcnt lgkmcnt(0)\nM v[48:51],v[208:211],a[64:67],v[48:51]\nv_sub_f32 v148,v148,v164\nv_sub_f32 v149,v149,v164\n" \
"M v[48:51],v[212:215],a[80:83],v[48:51]\ns_add_u32 m0,s65,16384\ns_xor_b32 s67,s66,640\nM v[52:55],v[208:211],a[68:71],v[52:55]\nv_sub_f32 v150,v150,v164\nv_sub_f32 v151,v151,v164\nM v[52:55],v[212:215],a[84:87],v[52:55]\nv_cvt_pk_bf16_f32 v136,0,v136\n" \
"v_cvt_pk_bf16_f32 v137,0,v137\nM v[112:115],v[208:211],a[72:75],v[112:115]\nbuffer_load_dwordx4 v224,s[36:39],s67 offen offset:0 lds\ns_waitcnt vmcnt(15)\ns_barrier\nds_read_b128 a[96:99],v228 offset:0\nds_read_b128 a[100:103],v228 offset:2048\nds_read_b128 a[104:107],v228 offset:4096\n" \
"ds_read_b128 a[108:111],v228 offset:6144\nds_read_b128 a[112:115],v229 offset:0\nds_read_b128 a[116:119],v229 offset:2048\nds_read_b128 a[120:123],v229 offset:4096\nds_read_b128 a[124:127],v229 offset:6144\nM v[112:115],v[212:215],a[88:91],v[112:115]\nds_read_b64_tr_b16 v[192:193],v241 offset:49152\nds_read_b64_tr_b16 v[194:195],v241 offset:49408\n" \
"M v[116:119],v[208:211],a[76:79],v[116:119]\nds_read_b64_tr_b16 v[196:197],v241 offset:57344\nds_read_b64_tr_b16 v[198:199],v241 offset:57600\nM v[116:119],v[212:215],a[92:95],v[116:119]\nv_cvt_pk_bf16_f32 v138,0,v138\nv_cvt_pk_bf16_f32 v139,0,v139\nM v[56:59],v[216:219],a[64:67],v[56:59]\nv_cvt_pk_bf16_f32 v140,0,v140\n" \
"v_cvt_pk_bf16_f32 v141,0,v141\nM v[56:59],v[220:223],a[80:83],v[56:59]\nbuffer_load_dwordx4 v225,s[36:39],s67 offen offset:1024 lds\nM v[60:63],v[216:219],a[68:71],v[60:63]\nv_cvt_pk_bf16_f32 v142,0,v142\nv_cvt_pk_bf16_f32 v143,0,v143\nM v[60:63],v[220:223],a[84:87],v[60:63]\nv_cvt_pk_bf16_f32 v144,0,v144\n" \
"v_cvt_pk_bf16_f32 v145,0,v145\nM v[120:123],v[216:219],a[72:75],v[120:123]\nv_cvt_pk_bf16_f32 v146,0,v146\nv_cvt_pk_bf16_f32 v147,0,v147\nM v[120:123],v[220:223],a[88:91],v[120:123]\nds_read_b64_tr_b16 v[200:201],v242 offset:49152\nds_read_b64_tr_b16 v[202:203],v242 offset:49408\nM v[124:127],v[216:219],a[76:79],v[124:127]\n" \
"buffer_load_dwordx4 v226,s[36:39],s67 offen offset:2048 lds\nds_read_b64_tr_b16 v[204:205],v242 offset:57344\nds_read_b64_tr_b16 v[206:207],v242 offset:57600\nM v[124:127],v[220:223],a[92:95],v[124:127]\nds_read_b64_tr_b16 v[208:209],v241 offset:49664\ns_waitcnt lgkmcnt(7)\nM v[0:3],v[192:195],a[96:99],v[0:3]\nds_read_b64_tr_b16 v[210:211],v241 offset:49920\n" \
"s_waitcnt lgkmcnt(6)\nM v[0:3],v[196:199],a[112:115],v[0:3]\nds_read_b64_tr_b16 v[212:213],v241 offset:57856\nds_read_b64_tr_b16 v[214:215],v241 offset:58112\nM v[4:7],v[192:195],a[100:103],v[4:7]\nglobal_load_dword v247,v253,s[16:17]\nM v[4:7],v[196:199],a[116:119],v[4:7]\nbuffer_load_dwordx4 v227,s[36:39],s67 offen offset:3072 lds\n" \
"M v[64:67],v[192:195],a[104:107],v[64:67]\nds_read_b64_tr_b16 v[216:217],v242 offset:49664\nds_read_b64_tr_b16 v[218:219],v242 offset:49920\nM v[64:67],v[196:199],a[120:123],v[64:67]\ns_add_u32 m0,s65,20480\ns_xor_b32 s67,s66,33408\nM v[68:71],v[192:195],a[108:111],v[68:71]\nds_read_b64_tr_b16 v[220:221],v242 offset:57856\n" \
"ds_read_b64_tr_b16 v[222:223],v242 offset:58112\nM v[68:71],v[196:199],a[124:127],v[68:71]\nds_read_b64_tr_b16 v[192:193],v241 offset:50176\ns_waitcnt lgkmcnt(11)\nM v[8:11],v[200:203],a[96:99],v[8:11]\nbuffer_load_dwordx4 v224,s[36:39],s67 offen offset:0 lds\ns_waitcnt lgkmcnt(9)\nM v[8:11],v[204:207],a[112:115],v[8:11]\n" \
"ds_read_b64_tr_b16 v[194:195],v241 offset:50432\nds_read_b64_tr_b16 v[196:197],v241 offset:58368\nM v[12:15],v[200:203],a[100:103],v[12:15]\nds_read_b64_tr_b16 v[198:199],v241 offset:58624\nv_add_u32 v253,0x100000,v253\nM v[12:15],v[204:207],a[116:119],v[12:15]\nv_cvt_pk_bf16_f32 v148,0,v148\nv_cvt_pk_bf16_f32 v149,0,v149\n" \
"M v[72:75],v[200:203],a[104:107],v[72:75]\nv_cvt_pk_bf16_f32 v150,0,v150\nv_cvt_pk_bf16_f32 v151,0,v151\nM v[72:75],v[204:207],a[120:123],v[72:75]\nbuffer_load_dwordx4 v225,s[36:39],s67 offen offset:1024 lds\nM v[76:79],v[200:203],a[108:111],v[76:79]\nds_read_b64_tr_b16 v[200:201],v242 offset:50176\nds_read_b64_tr_b16 v[202:203],v242 offset:50432\n" \
"M v[76:79],v[204:207],a[124:127],v[76:79]\nds_read_b64_tr_b16 v[204:205],v242 offset:58368\ns_waitcnt lgkmcnt(9)\nM v[16:19],v[208:211],a[96:99],v[16:19]\nds_read_b64_tr_b16 v[206:207],v242 offset:58624\nv_mul_f32 v136,v136,v157\nM v[16:19],v[212:215],a[112:115],v[16:19]\nv_mul_f32 v137,v137,v157\n" \
"v_mul_f32 v138,v138,v157\nM v[20:23],v[208:211],a[100:103],v[20:23]\nbuffer_load_dwordx4 v226,s[36:39],s67 offen offset:2048 lds\nM v[20:23],v[212:215],a[116:119],v[20:23]\nv_mul_f32 v139,v139,v157\nv_mul_f32 v140,v140,v157\nM v[80:83],v[208:211],a[104:107],v[80:83]\nv_mul_f32 v141,v141,v157\n" \
"v_mul_f32 v142,v142,v157\nM v[80:83],v[212:215],a[120:123],v[80:83]\nv_mul_f32 v143,v143,v157\nv_mul_f32 v144,v144,v157\nM v[84:87],v[208:211],a[108:111],v[84:87]\nds_read_b64_tr_b16 v[208:209],v241 offset:50688\nds_read_b64_tr_b16 v[210:211],v241 offset:50944\nM v[84:87],v[212:215],a[124:127],v[84:87]\n" \
"buffer_load_dwordx4 v227,s[36:39],s67 offen offset:3072 lds\nM v[24:27],v[216:219],a[96:99],v[24:27]\nds_read_b64_tr_b16 v[212:213],v241 offset:58880\ns_waitcnt lgkmcnt(6)\nM v[24:27],v[220:223],a[112:115],v[24:27]\ns_add_u32 m0,s64,114688\ns_add_u32 s69,s68,2621440\nM v[28:31],v[216:219],a[100:103],v[28:31]\n" \
"ds_read_b64_tr_b16 v[214:215],v241 offset:59136\nv_mul_f32 v145,v145,v157\nM v[28:31],v[220:223],a[116:119],v[28:31]\nv_mul_f32 v146,v146,v157\nv_mul_f32 v147,v147,v157\nM v[88:91],v[216:219],a[104:107],v[88:91]\nbuffer_load_dwordx4 v243,s[40:43],s69 offen offset:0 lds\nM v[88:91],v[220:223],a[120:123],v[88:91]\n" \
"v_mul_f32 v148,v148,v157\nv_mul_f32 v149,v149,v157\nM v[92:95],v[216:219],a[108:111],v[92:95]\nds_read_b64_tr_b16 v[216:217],v242 offset:50688\nds_read_b64_tr_b16 v[218:219],v242 offset:50944\nM v[92:95],v[220:223],a[124:127],v[92:95]\nds_read_b64_tr_b16 v[220:221],v242 offset:58880\nds_read_b64_tr_b16 v[222:223],v242 offset:59136\n" \
"M v[32:35],v[192:195],a[96:99],v[32:35]\nbuffer_load_dwordx2 v[166:167],v165,s[52:55],s80 offen sc1\nM v[32:35],v[196:199],a[112:115],v[32:35]\nbuffer_load_dwordx4 v244,s[40:43],s69 offen offset:1024 lds\nM v[36:39],v[192:195],a[100:103],v[36:39]\nv_mul_f32 v150,v150,v157\nv_mul_f32 v151,v151,v157\nM v[36:39],v[196:199],a[116:119],v[36:39]\n" \
"v_cvt_pk_bf16_f32 v144,0,v144\nv_cvt_pk_bf16_f32 v145,0,v145\nM v[96:99],v[192:195],a[104:107],v[96:99]\nv_cvt_pk_bf16_f32 v136,0,v136\nv_cvt_pk_bf16_f32 v137,0,v137\nM v[96:99],v[196:199],a[120:123],v[96:99]\nv_cvt_pk_bf16_f32 v138,0,v138\nv_cvt_pk_bf16_f32 v139,0,v139\n" \
"M v[100:103],v[192:195],a[108:111],v[100:103]\nbuffer_load_dwordx4 v245,s[40:43],s69 offen offset:2048 lds\nM v[100:103],v[196:199],a[124:127],v[100:103]\nv_cvt_pk_bf16_f32 v140,0,v140\ns_waitcnt lgkmcnt(4)\nM v[40:43],v[200:203],a[96:99],v[40:43]\nv_cvt_pk_bf16_f32 v141,0,v141\nv_cvt_pk_bf16_f32 v142,0,v142\n" \
"M v[40:43],v[204:207],a[112:115],v[40:43]\nv_cvt_pk_bf16_f32 v143,0,v143\nv_cvt_pk_bf16_f32 v146,0,v146\nM v[44:47],v[200:203],a[100:103],v[44:47]\nglobal_load_dword v247,v253,s[16:17]\nM v[44:47],v[204:207],a[116:119],v[44:47]\nbuffer_load_dwordx4 v246,s[40:43],s69 offen offset:3072 lds\nM v[104:107],v[200:203],a[104:107],v[104:107]\n" \
"v_add_u32 v253,0x100000,v253\nv_cvt_pk_bf16_f32 v147,0,v147\nM v[104:107],v[204:207],a[120:123],v[104:107]\ns_add_u32 m0,s65,0\ns_xor_b32 s67,s66,768\nM v[108:111],v[200:203],a[108:111],v[108:111]\nv_cvt_pk_bf16_f32 v148,0,v148\nv_cvt_pk_bf16_f32 v149,0,v149\n" \
"M v[108:111],v[204:207],a[124:127],v[108:111]\nv_cvt_pk_bf16_f32 v150,0,v150\nv_cvt_pk_bf16_f32 v151,0,v151\nM v[48:51],v[208:211],a[96:99],v[48:51]\nbuffer_load_dwordx4 v224,s[36:39],s67 offen offset:0 lds\nM v[48:51],v[212:215],a[112:115],v[48:51]\nM v[52:55],v[208:211],a[100:103],v[52:55]\nM v[52:55],v[212:215],a[116:119],v[52:55]\n" \
"M v[112:115],v[208:211],a[104:107],v[112:115]\ns_waitcnt vmcnt(16) lgkmcnt(0)\ns_barrier\nM v[112:115],v[212:215],a[120:123],v[112:115]\nbuffer_load_dwordx4 v225,s[36:39],s67 offen offset:1024 lds\nds_read_b128 a[128:131],v228 offset:8192\nds_read_b64_tr_b16 v[192:193],v241 offset:0\nds_read_b64_tr_b16 v[194:195],v241 offset:256\n" \
"M v[116:119],v[208:211],a[108:111],v[116:119]\nds_read_b128 a[144:147],v229 offset:8192\nds_read_b64_tr_b16 v[196:197],v241 offset:8192\nds_read_b64_tr_b16 v[198:199],v241 offset:8448\nM v[116:119],v[212:215],a[124:127],v[116:119]\nds_read_b128 a[132:135],v228 offset:10240\nM v[56:59],v[216:219],a[96:99],v[56:59]\nds_read_b128 a[148:151],v229 offset:10240\n" \
"M v[56:59],v[220:223],a[112:115],v[56:59]\nds_read_b128 a[136:139],v228 offset:12288\nM v[60:63],v[216:219],a[100:103],v[60:63]\nbuffer_load_dwordx4 v226,s[36:39],s67 offen offset:2048 lds\nds_read_b128 a[152:155],v229 offset:12288\nM v[60:63],v[220:223],a[116:119],v[60:63]\nds_read_b128 a[140:143],v228 offset:14336\nM v[120:123],v[216:219],a[104:107],v[120:123]\n" \
"ds_read_b128 a[156:159],v229 offset:14336\nM v[120:123],v[220:223],a[120:123],v[120:123]\nds_read_b64_tr_b16 v[200:201],v242 offset:0\nds_read_b64_tr_b16 v[202:203],v242 offset:256\nM v[124:127],v[216:219],a[108:111],v[124:127]\nds_read_b64_tr_b16 v[204:205],v242 offset:8192\nds_read_b64_tr_b16 v[206:207],v242 offset:8448\nM v[124:127],v[220:223],a[124:127],v[124:127]\n" \
"buffer_load_dwordx4 v227,s[36:39],s67 offen offset:3072 lds\ns_waitcnt lgkmcnt(13)\nM v[0:3],v[192:195],a[128:131],v[0:3]\nds_read_b64_tr_b16 v[208:209],v241 offset:512\ns_waitcnt lgkmcnt(11)\nM v[0:3],v[196:199],a[144:147],v[0:3]\ns_add_u32 m0,s65,4096\ns_xor_b32 s67,s66,33536\n" \
"s_waitcnt lgkmcnt(10)\nM v[4:7],v[192:195],a[132:135],v[4:7]\nds_read_b64_tr_b16 v[210:211],v241 offset:768\ns_waitcnt lgkmcnt(10)\nM v[4:7],v[196:199],a[148:151],v[4:7]\nds_read_b64_tr_b16 v[212:213],v241 offset:8704\ns_waitcnt lgkmcnt(10)\nM v[64:67],v[192:195],a[136:139],v[64:67]\n" \
"buffer_load_dwordx4 v224,s[36:39],s67 offen offset:0 lds\ns_waitcnt lgkmcnt(9)\nM v[64:67],v[196:199],a[152:155],v[64:67]\nds_read_b64_tr_b16 v[214:215],v241 offset:8960\ns_waitcnt lgkmcnt(9)\nM v[68:71],v[192:195],a[140:143],v[68:71]\nds_read_b64_tr_b16 v[216:217],v242 offset:512\ns_waitcnt lgkmcnt(9)\n" \
"M v[68:71],v[196:199],a[156:159],v[68:71]\nds_read_b64_tr_b16 v[218:219],v242 offset:768\ns_waitcnt lgkmcnt(8)\nM v[8:11],v[200:203],a[128:131],v[8:11]\nds_read_b64_tr_b16 v[220:221],v242 offset:8704\ns_waitcnt lgkmcnt(7)\nM v[8:11],v[204:207],a[144:147],v[8:11]\nbuffer_load_dwordx4 v225,s[36:39],s67 offen offset:1024 lds\n" \
"M v[12:15],v[200:203],a[132:135],v[12:15]\nds_read_b64_tr_b16 v[222:223],v242 offset:8960\nds_read_b64_tr_b16 v[192:193],v241 offset:1024\nM v[12:15],v[204:207],a[148:151],v[12:15]\nds_read_b64_tr_b16 v[194:195],v241 offset:1280\nds_read_b64_tr_b16 v[196:197],v241 offset:9216\nM v[72:75],v[200:203],a[136:139],v[72:75]\nds_read_b64_tr_b16 v[198:199],v241 offset:9472\n" \
"M v[72:75],v[204:207],a[152:155],v[72:75]\nM v[76:79],v[200:203],a[140:143],v[76:79]\nbuffer_load_dwordx4 v226,s[36:39],s67 offen offset:2048 lds\nM v[76:79],v[204:207],a[156:159],v[76:79]\nds_read_b64_tr_b16 v[200:201],v242 offset:1024\ns_waitcnt lgkmcnt(10)\nM v[16:19],v[208:211],a[128:131],v[16:19]\nds_read_b64_tr_b16 v[202:203],v242 offset:1280\n" \
"s_waitcnt lgkmcnt(10)\nM v[16:19],v[212:215],a[144:147],v[16:19]\nds_read_b64_tr_b16 v[204:205],v242 offset:9216\nds_read_b64_tr_b16 v[206:207],v242 offset:9472\nM v[20:23],v[208:211],a[132:135],v[20:23]\nglobal_load_dword v247,v253,s[16:17]\nM v[20:23],v[212:215],a[148:151],v[20:23]\nbuffer_load_dwordx4 v227,s[36:39],s67 offen offset:3072 lds\n" \
"M v[80:83],v[208:211],a[136:139],v[80:83]\nv_add_u32 v253,0xffd00000,v253\nM v[80:83],v[212:215],a[152:155],v[80:83]\ns_add_u32 m0,s64,131072\ns_add_u32 s69,s68,3145728\nM v[84:87],v[208:211],a[140:143],v[84:87]\nds_read_b64_tr_b16 v[208:209],v241 offset:1536\nds_read_b64_tr_b16 v[210:211],v241 offset:1792\n" \
"M v[84:87],v[212:215],a[156:159],v[84:87]\nds_read_b64_tr_b16 v[212:213],v241 offset:9728\ns_waitcnt lgkmcnt(7)\nM v[24:27],v[216:219],a[128:131],v[24:27]\nbuffer_load_dwordx4 v243,s[40:43],s69 offen offset:0 lds\nM v[24:27],v[220:223],a[144:147],v[24:27]\nds_read_b64_tr_b16 v[214:215],v241 offset:9984\nM v[28:31],v[216:219],a[132:135],v[28:31]\n" \
"M v[28:31],v[220:223],a[148:151],v[28:31]\nM v[88:91],v[216:219],a[136:139],v[88:91]\nM v[88:91],v[220:223],a[152:155],v[88:91]\nbuffer_load_dwordx4 v244,s[40:43],s69 offen offset:1024 lds\nM v[92:95],v[216:219],a[140:143],v[92:95]\nds_read_b64_tr_b16 v[216:217],v242 offset:1536\nds_read_b64_tr_b16 v[218:219],v242 offset:1792\nM v[92:95],v[220:223],a[156:159],v[92:95]\n" \
"ds_read_b64_tr_b16 v[220:221],v242 offset:9728\nds_read_b64_tr_b16 v[222:223],v242 offset:9984\nM v[32:35],v[192:195],a[128:131],v[32:35]\nM v[32:35],v[196:199],a[144:147],v[32:35]\nM v[36:39],v[192:195],a[132:135],v[36:39]\nbuffer_load_dwordx4 v245,s[40:43],s69 offen offset:2048 lds\nM v[36:39],v[196:199],a[148:151],v[36:39]\nM v[96:99],v[192:195],a[136:139],v[96:99]\n" \
"s_waitcnt vmcnt(16)\nv_xor_b32 v166,v166,v230\nM v[96:99],v[196:199],a[152:155],v[96:99]\nv_xor_b32 v167,v167,v231\nv_or_b32 v166,v166,v167\nM v[100:103],v[192:195],a[140:143],v[100:103]\nv_cmp_ne_u32 vcc,0,v166\nM v[100:103],v[196:199],a[156:159],v[100:103]\n" \
"buffer_load_dwordx4 v246,s[40:43],s69 offen offset:3072 lds\ns_waitcnt lgkmcnt(4)\nM v[40:43],v[200:203],a[128:131],v[40:43]\ns_cbranch_vccnz .Lhs\n.Lhb:\nv_bfe_u32 v231,v249,2,3\nM v[40:43],v[204:207],a[144:147],v[40:43]\ns_add_u32 m0,s65,8192\n" \
"s_xor_b32 s67,s66,896\nM v[44:47],v[200:203],a[132:135],v[44:47]\nv_mul_u32_u24 v231,3584,v231\nv_and_b32 v230,31,v249\nM v[44:47],v[204:207],a[148:151],v[44:47]\nv_lshlrev_b32 v230,7,v230\nM v[104:107],v[200:203],a[136:139],v[104:107]\nbuffer_load_dwordx4 v224,s[36:39],s67 offen offset:0 lds\n" \
"M v[104:107],v[204:207],a[152:155],v[104:107]\nM v[108:111],v[200:203],a[140:143],v[108:111]\nM v[108:111],v[204:207],a[156:159],v[108:111]\nM v[48:51],v[208:211],a[128:131],v[48:51]\nM v[48:51],v[212:215],a[144:147],v[48:51]\nbuffer_load_dwordx4 v225,s[36:39],s67 offen offset:1024 lds\nM v[52:55],v[208:211],a[132:135],v[52:55]\nM v[52:55],v[212:215],a[148:151],v[52:55]\n" \
"M v[112:115],v[208:211],a[136:139],v[112:115]\ns_waitcnt vmcnt(15) lgkmcnt(0)\ns_barrier\nM v[112:115],v[212:215],a[152:155],v[112:115]\nds_read_b128 a[160:163],v228 offset:16384\nds_read_b64_tr_b16 v[192:193],v241 offset:16384\nds_read_b64_tr_b16 v[194:195],v241 offset:16640\nM v[116:119],v[208:211],a[140:143],v[116:119]\n" \
"ds_read_b128 a[176:179],v229 offset:16384\nds_read_b64_tr_b16 v[196:197],v241 offset:24576\nds_read_b64_tr_b16 v[198:199],v241 offset:24832\nM v[116:119],v[212:215],a[156:159],v[116:119]\nds_read_b128 a[164:167],v228 offset:18432\nM v[56:59],v[216:219],a[128:131],v[56:59]\nbuffer_load_dwordx4 v[152:155],v240,s[52:55],s71 offen sc1\nds_read_b128 a[180:183],v229 offset:18432\n" \
"M v[56:59],v[220:223],a[144:147],v[56:59]\nbuffer_load_dwordx4 v226,s[36:39],s67 offen offset:2048 lds\ns_add_u32 s71,s70,798720\nds_read_b128 a[168:171],v228 offset:20480\nbuffer_load_dwordx4 v[156:159],v240,s[52:55],s71 offen sc1\nM v[60:63],v[216:219],a[132:135],v[60:63]\nds_read_b128 a[184:187],v229 offset:20480\ns_add_u32 s71,s70,811008\n" \
"M v[60:63],v[220:223],a[148:151],v[60:63]\nbuffer_load_dwordx4 v227,s[36:39],s67 offen offset:3072 lds\nbuffer_load_dwordx4 v[160:163],v240,s[52:55],s71 offen sc1\nds_read_b128 a[172:175],v228 offset:22528\nM v[120:123],v[216:219],a[136:139],v[120:123]\ns_add_u32 s71,s70,823296\nds_read_b128 a[188:191],v229 offset:22528\nbuffer_load_dwordx4 v[164:167],v240,s[52:55],s71 offen sc1\n" \
"M v[120:123],v[220:223],a[152:155],v[120:123]\ns_add_u32 m0,s65,12288\ns_xor_b32 s67,s66,33664\nds_read_b64_tr_b16 v[200:201],v242 offset:16384\nds_read_b64_tr_b16 v[202:203],v242 offset:16640\nM v[124:127],v[216:219],a[140:143],v[124:127]\nds_read_b64_tr_b16 v[204:205],v242 offset:24576\nds_read_b64_tr_b16 v[206:207],v242 offset:24832\n" \
"M v[124:127],v[220:223],a[156:159],v[124:127]\ns_lshl_b32 s71,s6,5\ns_waitcnt lgkmcnt(9)\nM v[0:3],v[192:195],a[160:163],v[0:3]\nds_read_b64_tr_b16 v[208:209],v241 offset:16896\ns_add_u32 s71,s71,790528\nM v[0:3],v[196:199],a[176:179],v[0:3]\nds_read_b64_tr_b16 v[210:211],v241 offset:17152\n" \
"ds_read_b64_tr_b16 v[212:213],v241 offset:25088\nM v[4:7],v[192:195],a[164:167],v[4:7]\nds_read_b64_tr_b16 v[214:215],v241 offset:25344\ns_waitcnt lgkmcnt(11)\nM v[4:7],v[196:199],a[180:183],v[4:7]\nds_read_b64_tr_b16 v[216:217],v242 offset:16896\nds_read_b64_tr_b16 v[218:219],v242 offset:17152\nM v[64:67],v[192:195],a[168:171],v[64:67]\n" \
"ds_read_b64_tr_b16 v[220:221],v242 offset:25088\ns_waitcnt lgkmcnt(13)\nM v[64:67],v[196:199],a[184:187],v[64:67]\nds_read_b64_tr_b16 v[222:223],v242 offset:25344\ns_waitcnt lgkmcnt(13)\nM v[68:71],v[192:195],a[172:175],v[68:71]\nds_read_b64_tr_b16 v[192:193],v241 offset:17408\ns_waitcnt lgkmcnt(13)\n" \
"M v[68:71],v[196:199],a[188:191],v[68:71]\nds_read_b64_tr_b16 v[194:195],v241 offset:17664\ns_waitcnt lgkmcnt(12)\nM v[8:11],v[200:203],a[160:163],v[8:11]\nds_read_b64_tr_b16 v[196:197],v241 offset:25600\ns_waitcnt lgkmcnt(11)\nM v[8:11],v[204:207],a[176:179],v[8:11]\ns_cmp_lt_u32 s7,2\n" \
"s_cselect_b32 s70,0,1.0\nM v[12:15],v[200:203],a[164:167],v[12:15]\nbuffer_load_dword v247,v254,s[56:59],s78 offen\nM v[12:15],v[204:207],a[180:183],v[12:15]\nds_read_b64_tr_b16 v[198:199],v241 offset:25856\nv_and_b32 v254,3,v249\nM v[72:75],v[200:203],a[168:171],v[72:75]\ns_cmp_eq_u32 s62,5\n" \
"s_cselect_b64 s[56:57],s[22:23],s[24:25]\nM v[72:75],v[204:207],a[184:187],v[72:75]\nv_lshl_add_u32 v231,v254,7,v231\nM v[76:79],v[200:203],a[172:175],v[76:79]\nglobal_load_dword v247,v253,s[12:13]\nM v[76:79],v[204:207],a[188:191],v[76:79]\nds_read_b64_tr_b16 v[200:201],v242 offset:17408\ns_waitcnt lgkmcnt(6)\n" \
"M v[16:19],v[208:211],a[160:163],v[16:19]\nds_read_b64_tr_b16 v[202:203],v242 offset:17664\nds_read_b64_tr_b16 v[204:205],v242 offset:25600\nM v[16:19],v[212:215],a[176:179],v[16:19]\nds_read_b64_tr_b16 v[206:207],v242 offset:25856\ns_add_u32 s69,s68,3670016\nM v[20:23],v[208:211],a[164:167],v[20:23]\nbuffer_load_dwordx4 v224,s[36:39],s67 offen offset:0 lds\n" \
"M v[20:23],v[212:215],a[180:183],v[20:23]\nv_add_u32 v253,0x100000,v253\nM v[80:83],v[208:211],a[168:171],v[80:83]\nM v[80:83],v[212:215],a[184:187],v[80:83]\nM v[84:87],v[208:211],a[172:175],v[84:87]\nbuffer_load_dwordx4 v225,s[36:39],s67 offen offset:1024 lds\nM v[84:87],v[212:215],a[188:191],v[84:87]\nds_read_b64_tr_b16 v[208:209],v241 offset:17920\n" \
"ds_read_b64_tr_b16 v[210:211],v241 offset:18176\nM v[24:27],v[216:219],a[160:163],v[24:27]\nds_read_b64_tr_b16 v[212:213],v241 offset:26112\ns_waitcnt lgkmcnt(7)\nM v[24:27],v[220:223],a[176:179],v[24:27]\nds_read_b64_tr_b16 v[214:215],v241 offset:26368\nM v[28:31],v[216:219],a[164:167],v[28:31]\nbuffer_load_dwordx4 v226,s[36:39],s67 offen offset:2048 lds\n" \
"M v[28:31],v[220:223],a[180:183],v[28:31]\nM v[88:91],v[216:219],a[168:171],v[88:91]\nM v[88:91],v[220:223],a[184:187],v[88:91]\nM v[92:95],v[216:219],a[172:175],v[92:95]\nbuffer_load_dwordx4 v227,s[36:39],s67 offen offset:3072 lds\nM v[92:95],v[220:223],a[188:191],v[92:95]\ns_add_u32 m0,s64,147456\nds_read_b64_tr_b16 v[216:217],v242 offset:17920\n" \
"M v[32:35],v[192:195],a[160:163],v[32:35]\nds_read_b64_tr_b16 v[218:219],v242 offset:18176\nds_read_b64_tr_b16 v[220:221],v242 offset:26112\nM v[32:35],v[196:199],a[176:179],v[32:35]\ns_cmp_eq_u32 s62,0\ns_cselect_b32 s36,0x8000,0\nM v[36:39],v[192:195],a[164:167],v[36:39]\nds_read_b64_tr_b16 v[222:223],v242 offset:26368\n" \
"s_add_u32 s36,s36,s64\nM v[36:39],v[196:199],a[180:183],v[36:39]\nbuffer_load_dwordx4 v243,s[40:43],s69 offen offset:0 lds\nM v[96:99],v[192:195],a[168:171],v[96:99]\nM v[96:99],v[196:199],a[184:187],v[96:99]\nM v[100:103],v[192:195],a[172:175],v[100:103]\nM v[100:103],v[196:199],a[188:191],v[100:103]\ns_waitcnt lgkmcnt(4)\n" \
"M v[40:43],v[200:203],a[160:163],v[40:43]\nbuffer_load_dwordx4 v244,s[40:43],s69 offen offset:1024 lds\nM v[40:43],v[204:207],a[176:179],v[40:43]\nM v[44:47],v[200:203],a[164:167],v[44:47]\nM v[44:47],v[204:207],a[180:183],v[44:47]\nM v[104:107],v[200:203],a[168:171],v[104:107]\nbuffer_load_dwordx4 v245,s[40:43],s69 offen offset:2048 lds\nM v[104:107],v[204:207],a[184:187],v[104:107]\n" \
"M v[108:111],v[200:203],a[172:175],v[108:111]\nM v[108:111],v[204:207],a[188:191],v[108:111]\nM v[48:51],v[208:211],a[160:163],v[48:51]\nbuffer_load_dwordx4 v246,s[40:43],s69 offen offset:3072 lds\nM v[48:51],v[212:215],a[176:179],v[48:51]\ns_sub_u32 s40,s16,0x1000\ns_subb_u32 s41,s17,0\nM v[52:55],v[208:211],a[164:167],v[52:55]\n" \
"s_add_u32 m0,s64,98304\ns_add_u32 s69,s68,0\nM v[52:55],v[212:215],a[180:183],v[52:55]\ns_mov_b32 s16,0\ns_mov_b32 s17,3\nM v[112:115],v[208:211],a[168:171],v[112:115]\ns_waitcnt vmcnt(18) lgkmcnt(0)\ns_barrier\n" \
"M v[112:115],v[212:215],a[184:187],v[112:115]\nds_read_b128 a[192:195],v228 offset:0\nds_read_b64_tr_b16 v[192:193],v241 offset:32768\nds_read_b64_tr_b16 v[194:195],v241 offset:33024\nM v[116:119],v[208:211],a[172:175],v[116:119]\nglobal_load_dword v247,v253,s[12:13]\nds_read_b128 a[208:211],v229 offset:0\nds_read_b64_tr_b16 v[196:197],v241 offset:40960\n" \
"ds_read_b64_tr_b16 v[198:199],v241 offset:41216\nM v[116:119],v[212:215],a[188:191],v[116:119]\nds_read_b128 a[196:199],v228 offset:2048\nv_add_u32 v253,0x100000,v253\nM v[56:59],v[216:219],a[160:163],v[56:59]\nds_read_b128 a[212:215],v229 offset:2048\nM v[56:59],v[220:223],a[176:179],v[56:59]\nds_read_b128 a[200:203],v228 offset:4096\n" \
"M v[60:63],v[216:219],a[164:167],v[60:63]\nds_read_b128 a[216:219],v229 offset:4096\nM v[60:63],v[220:223],a[180:183],v[60:63]\nds_read_b128 a[204:207],v228 offset:6144\nM v[120:123],v[216:219],a[168:171],v[120:123]\nds_read_b128 a[220:223],v229 offset:6144\nM v[120:123],v[220:223],a[184:187],v[120:123]\nds_read_b64_tr_b16 v[200:201],v242 offset:32768\n" \
"ds_read_b64_tr_b16 v[202:203],v242 offset:33024\nM v[124:127],v[216:219],a[172:175],v[124:127]\nds_read_b64_tr_b16 v[204:205],v242 offset:40960\nds_read_b64_tr_b16 v[206:207],v242 offset:41216\nM v[124:127],v[220:223],a[188:191],v[124:127]\ns_waitcnt vmcnt(14)\nv_add_f32 v152,v152,v156\ns_waitcnt lgkmcnt(13)\n" \
"M v[0:3],v[192:195],a[192:195],v[0:3]\nds_read_b64_tr_b16 v[208:209],v241 offset:33280\ns_waitcnt lgkmcnt(11)\nM v[0:3],v[196:199],a[208:211],v[0:3]\nds_read_b64_tr_b16 v[210:211],v241 offset:33536\ns_waitcnt lgkmcnt(11)\nM v[4:7],v[192:195],a[196:199],v[4:7]\nds_read_b64_tr_b16 v[212:213],v241 offset:41472\n" \
"s_waitcnt lgkmcnt(11)\nM v[4:7],v[196:199],a[212:215],v[4:7]\nds_read_b64_tr_b16 v[214:215],v241 offset:41728\ns_waitcnt lgkmcnt(11)\nM v[64:67],v[192:195],a[200:203],v[64:67]\nds_read_b64_tr_b16 v[216:217],v242 offset:33280\ns_waitcnt lgkmcnt(11)\nM v[64:67],v[196:199],a[216:219],v[64:67]\n" \
"ds_read_b64_tr_b16 v[218:219],v242 offset:33536\ns_waitcnt lgkmcnt(11)\nM v[68:71],v[192:195],a[204:207],v[68:71]\nds_read_b64_tr_b16 v[220:221],v242 offset:41472\ns_waitcnt lgkmcnt(11)\nM v[68:71],v[196:199],a[220:223],v[68:71]\nds_read_b64_tr_b16 v[222:223],v242 offset:41728\ns_waitcnt lgkmcnt(10)\n" \
"M v[8:11],v[200:203],a[192:195],v[8:11]\nds_read_b64_tr_b16 v[192:193],v241 offset:33792\ns_waitcnt lgkmcnt(9)\nM v[8:11],v[204:207],a[208:211],v[8:11]\nds_read_b64_tr_b16 v[194:195],v241 offset:34048\nds_read_b64_tr_b16 v[196:197],v241 offset:41984\nM v[12:15],v[200:203],a[196:199],v[12:15]\nds_read_b64_tr_b16 v[198:199],v241 offset:42240\n" \
"s_waitcnt vmcnt(11)\nv_add_f32 v160,v160,v164\nM v[12:15],v[204:207],a[212:215],v[12:15]\nv_add_f32 v153,v153,v157\nv_add_f32 v161,v161,v165\nM v[72:75],v[200:203],a[200:203],v[72:75]\nv_add_f32 v154,v154,v158\nv_add_f32 v162,v162,v166\n" \
"M v[72:75],v[204:207],a[216:219],v[72:75]\nv_add_f32 v155,v155,v159\nv_add_f32 v163,v163,v167\nM v[76:79],v[200:203],a[204:207],v[76:79]\nds_read_b64_tr_b16 v[200:201],v242 offset:33792\nds_read_b64_tr_b16 v[202:203],v242 offset:34048\nM v[76:79],v[204:207],a[220:223],v[76:79]\nds_read_b64_tr_b16 v[204:205],v242 offset:41984\n" \
"s_waitcnt lgkmcnt(9)\nM v[16:19],v[208:211],a[192:195],v[16:19]\nds_read_b64_tr_b16 v[206:207],v242 offset:42240\nv_add_f32 v152,v152,v160\nM v[16:19],v[212:215],a[208:211],v[16:19]\nbuffer_load_dword v168,v169,s[52:55],s71 offen sc1\nM v[20:23],v[208:211],a[196:199],v[20:23]\nv_add_f32 v153,v153,v161\n" \
"v_add_f32 v154,v154,v162\nM v[20:23],v[212:215],a[212:215],v[20:23]\nv_add_f32 v155,v155,v163\nv_cvt_pk_bf16_f32 v152,0,v152\nM v[80:83],v[208:211],a[200:203],v[80:83]\nv_cvt_pk_bf16_f32 v153,0,v153\nv_cvt_pk_bf16_f32 v154,0,v154\nM v[80:83],v[212:215],a[216:219],v[80:83]\n" \
"v_cvt_pk_bf16_f32 v155,0,v155\nv_add_f32 v152,s70,v152\nM v[84:87],v[208:211],a[204:207],v[84:87]\nds_read_b64_tr_b16 v[208:209],v241 offset:34304\nds_read_b64_tr_b16 v[210:211],v241 offset:34560\nM v[84:87],v[212:215],a[220:223],v[84:87]\nds_read_b64_tr_b16 v[212:213],v241 offset:42496\nds_read_b64_tr_b16 v[214:215],v241 offset:42752\n" \
"M v[24:27],v[216:219],a[192:195],v[24:27]\nv_add_f32 v153,s70,v153\ns_waitcnt lgkmcnt(7)\nM v[24:27],v[220:223],a[208:211],v[24:27]\nv_add_f32 v154,s70,v154\nv_add_f32 v155,s70,v155\nM v[28:31],v[216:219],a[196:199],v[28:31]\nglobal_load_dword v247,v253,s[12:13]\n" \
"M v[28:31],v[220:223],a[212:215],v[28:31]\ns_lshr_b32 s71,s64,2\nv_cvt_pk_bf16_f32 v152,0,v152\nM v[88:91],v[216:219],a[200:203],v[88:91]\nv_cvt_pk_bf16_f32 v153,0,v153\nv_cvt_pk_bf16_f32 v154,0,v154\nM v[88:91],v[220:223],a[216:219],v[88:91]\nv_cvt_pk_bf16_f32 v155,0,v155\n" \
"v_add_u32 v171,s71,v240\nM v[92:95],v[216:219],a[204:207],v[92:95]\nds_write_b128 v171,v[152:155] offset:16384\nM v[92:95],v[220:223],a[220:223],v[92:95]\nds_read_b64_tr_b16 v[216:217],v242 offset:34304\nds_read_b64_tr_b16 v[218:219],v242 offset:34560\nM v[32:35],v[192:195],a[192:195],v[32:35]\nds_read_b64_tr_b16 v[220:221],v242 offset:42496\n" \
"ds_read_b64_tr_b16 v[222:223],v242 offset:42752\nM v[32:35],v[196:199],a[208:211],v[32:35]\nv_add_u32 v253,0x100000,v253\ns_lshl_b32 s70,s7,8\nM v[36:39],v[192:195],a[196:199],v[36:39]\nbuffer_load_dwordx4 v243,s[40:43],s69 offen offset:0 lds\nM v[36:39],v[196:199],a[212:215],v[36:39]\nv_lshl_add_u32 v251,v251,5,s70\n" \
"s_lshl_b32 s71,s6,5\nM v[96:99],v[192:195],a[200:203],v[96:99]\ns_and_b32 s71,s71,0xfffffe00\ns_xor_b32 s70,s63,16\nM v[96:99],v[196:199],a[216:219],v[96:99]\ns_lshr_b32 s70,s70,2\ns_lshl3_add_u32 s70,s62,s70\nM v[100:103],v[192:195],a[204:207],v[100:103]\n" \
"buffer_load_dwordx4 v244,s[40:43],s69 offen offset:1024 lds\nM v[100:103],v[196:199],a[220:223],v[100:103]\ns_lshl_b32 s70,s70,15\ns_waitcnt lgkmcnt(5)\nM v[40:43],v[200:203],a[192:195],v[40:43]\ns_add_u32 s70,s70,s64\nM v[40:43],v[204:207],a[208:211],v[40:43]\nM v[44:47],v[200:203],a[196:199],v[44:47]\n" \
"buffer_load_dwordx4 v245,s[40:43],s69 offen offset:2048 lds\nM v[44:47],v[204:207],a[212:215],v[44:47]\nM v[104:107],v[200:203],a[200:203],v[104:107]\nM v[104:107],v[204:207],a[216:219],v[104:107]\nM v[108:111],v[200:203],a[204:207],v[108:111]\nbuffer_load_dwordx4 v246,s[40:43],s69 offen offset:3072 lds\nM v[108:111],v[204:207],a[220:223],v[108:111]\ns_add_u32 m0,s64,114688\n" \
"s_add_u32 s69,s68,524288\nM v[48:51],v[208:211],a[192:195],v[48:51]\nM v[48:51],v[212:215],a[208:211],v[48:51]\nM v[52:55],v[208:211],a[196:199],v[52:55]\nbuffer_store_dwordx2 v[232:233],off,s[52:55],s72 sc1\nM v[52:55],v[212:215],a[212:215],v[52:55]\ns_and_b32 s72,s6,1\ns_lshl_b32 s72,s72,8\n" \
"M v[112:115],v[208:211],a[200:203],v[112:115]\ns_waitcnt vmcnt(8) lgkmcnt(0)\ns_barrier\nM v[112:115],v[212:215],a[216:219],v[112:115]\nds_read_b128 a[224:227],v228 offset:8192\nds_read_b64_tr_b16 v[192:193],v241 offset:49152\nds_read_b64_tr_b16 v[194:195],v241 offset:49408\nM v[116:119],v[208:211],a[204:207],v[116:119]\n" \
"ds_read_b128 a[240:243],v229 offset:8192\nds_read_b64_tr_b16 v[196:197],v241 offset:57344\nds_read_b64_tr_b16 v[198:199],v241 offset:57600\nM v[116:119],v[212:215],a[220:223],v[116:119]\nds_read_b128 a[228:231],v228 offset:10240\ns_add_u32 s71,s71,s72\nM v[56:59],v[216:219],a[192:195],v[56:59]\nds_read_b128 a[244:247],v229 offset:10240\n" \
"s_and_b32 s72,s6,14\nM v[56:59],v[220:223],a[208:211],v[56:59]\nds_read_b128 a[232:235],v228 offset:12288\ns_lshl1_add_u32 s71,s72,s71\nM v[60:63],v[216:219],a[196:199],v[60:63]\nds_read_b128 a[248:251],v229 offset:12288\ns_or_b32 s71,s71,s75\nM v[60:63],v[220:223],a[212:215],v[60:63]\n" \
"ds_read_b128 a[236:239],v228 offset:14336\ns_mov_b32 s72,0x3e0293ee\nM v[120:123],v[216:219],a[200:203],v[120:123]\nds_read_b128 a[252:255],v229 offset:14336\nv_and_b32 v228,15,v249\nM v[120:123],v[220:223],a[216:219],v[120:123]\nds_read_b64_tr_b16 v[200:201],v242 offset:49152\nds_read_b64_tr_b16 v[202:203],v242 offset:49408\n" \
"M v[124:127],v[216:219],a[204:207],v[124:127]\nds_read_b64_tr_b16 v[204:205],v242 offset:57344\nds_read_b64_tr_b16 v[206:207],v242 offset:57600\nM v[124:127],v[220:223],a[220:223],v[124:127]\nv_and_b32 v229,16,v249\ns_waitcnt lgkmcnt(13)\nM v[0:3],v[192:195],a[224:227],v[0:3]\nds_read_b64_tr_b16 v[208:209],v241 offset:49664\n" \
"s_waitcnt lgkmcnt(11)\nM v[0:3],v[196:199],a[240:243],v[0:3]\nds_read_b64_tr_b16 v[210:211],v241 offset:49920\ns_waitcnt lgkmcnt(11)\nM v[4:7],v[192:195],a[228:231],v[4:7]\nds_read_b64_tr_b16 v[212:213],v241 offset:57856\ns_waitcnt lgkmcnt(11)\nM v[4:7],v[196:199],a[244:247],v[4:7]\n" \
"ds_read_b64_tr_b16 v[214:215],v241 offset:58112\ns_waitcnt lgkmcnt(11)\nM v[64:67],v[192:195],a[232:235],v[64:67]\nds_read_b64_tr_b16 v[216:217],v242 offset:49664\ns_waitcnt lgkmcnt(11)\nM v[64:67],v[196:199],a[248:251],v[64:67]\nds_read_b64_tr_b16 v[218:219],v242 offset:49920\ns_waitcnt lgkmcnt(11)\n" \
"M v[68:71],v[192:195],a[236:239],v[68:71]\nglobal_load_dword v247,v253,s[12:13]\ns_waitcnt lgkmcnt(10)\nM v[68:71],v[196:199],a[252:255],v[68:71]\nds_read_b64_tr_b16 v[220:221],v242 offset:57856\ns_waitcnt lgkmcnt(9)\nM v[8:11],v[200:203],a[224:227],v[8:11]\nds_read_b64_tr_b16 v[222:223],v242 offset:58112\n" \
"s_waitcnt lgkmcnt(8)\nM v[8:11],v[204:207],a[240:243],v[8:11]\nds_read_b64_tr_b16 v[192:193],v241 offset:50176\nds_read_b64_tr_b16 v[194:195],v241 offset:50432\nM v[12:15],v[200:203],a[228:231],v[12:15]\nds_read_b64_tr_b16 v[196:197],v241 offset:58368\nds_read_b64_tr_b16 v[198:199],v241 offset:58624\nM v[12:15],v[204:207],a[244:247],v[12:15]\n" \
"ds_read_b128 v[176:179],v251 offset:19456\nds_read_b128 v[160:163],v251 offset:17408\nM v[72:75],v[200:203],a[232:235],v[72:75]\nds_read_b128 v[172:175],v251 offset:18448\nv_cvt_pk_bf16_f32 v128,v0,v1\nM v[72:75],v[204:207],a[248:251],v[72:75]\nds_read_b128 v[152:155],v251 offset:16384\nv_cvt_pk_bf16_f32 v129,v2,v3\n" \
"M v[76:79],v[200:203],a[236:239],v[76:79]\nds_read_b64_tr_b16 v[200:201],v242 offset:50176\nv_cvt_pk_bf16_f32 v130,v8,v9\nM v[76:79],v[204:207],a[252:255],v[76:79]\nds_read_b64_tr_b16 v[202:203],v242 offset:50432\ns_waitcnt lgkmcnt(12)\nM v[16:19],v[208:211],a[224:227],v[16:19]\nds_read_b64_tr_b16 v[204:205],v242 offset:58368\n" \
"ds_read_b64_tr_b16 v[206:207],v242 offset:58624\nM v[16:19],v[212:215],a[240:243],v[16:19]\nds_read_b128 v[156:159],v251 offset:16400\nds_read_b128 v[180:183],v251 offset:19472\nM v[20:23],v[208:211],a[228:231],v[20:23]\nds_read_b128 v[164:167],v251 offset:17424\nv_cvt_pk_bf16_f32 v131,v10,v11\nM v[20:23],v[212:215],a[244:247],v[20:23]\n" \
"buffer_load_dwordx4 v243,s[40:43],s69 offen offset:0 lds\nM v[80:83],v[208:211],a[232:235],v[80:83]\nv_cvt_pk_bf16_f32 v132,v16,v17\nv_cvt_pk_bf16_f32 v133,v18,v19\nM v[80:83],v[212:215],a[248:251],v[80:83]\ns_waitcnt lgkmcnt(10)\nv_mul_f32 v144,v144,v176\nM v[84:87],v[208:211],a[236:239],v[84:87]\n" \
"ds_read_b64_tr_b16 v[208:209],v241 offset:50688\nds_read_b64_tr_b16 v[210:211],v241 offset:50944\nM v[84:87],v[212:215],a[252:255],v[84:87]\nds_read_b64_tr_b16 v[212:213],v241 offset:58880\nds_read_b64_tr_b16 v[214:215],v241 offset:59136\nM v[24:27],v[216:219],a[224:227],v[24:27]\ns_waitcnt vmcnt(8)\nv_add_f32_dpp v168,v168,v168 quad_perm:[1,0,3,2] row_mask:0xf bank_mask:0xf\n" \
"M v[24:27],v[220:223],a[240:243],v[24:27]\nv_mul_f32 v145,v145,v177\nv_add_f32_dpp v168,v168,v168 quad_perm:[2,3,0,1] row_mask:0xf bank_mask:0xf\nM v[28:31],v[216:219],a[228:231],v[28:31]\nv_cvt_pk_bf16_f32 v168,0,v168\nv_cvt_pk_bf16_f32 v144,0,v144\nM v[28:31],v[220:223],a[244:247],v[28:31]\nbuffer_store_dword v168,v170,s[52:55],s71 offen sc1\n" \
"M v[88:91],v[216:219],a[232:235],v[88:91]\nds_read_b128 v[168:171],v251 offset:18432\nv_cvt_pk_bf16_f32 v145,0,v145\nM v[88:91],v[220:223],a[248:251],v[88:91]\ns_waitcnt lgkmcnt(5)\nv_add_f32 v144,v144,v160\nM v[92:95],v[216:219],a[236:239],v[92:95]\nds_read_b64_tr_b16 v[216:217],v242 offset:50688\n" \
"ds_read_b64_tr_b16 v[218:219],v242 offset:50944\nM v[92:95],v[220:223],a[252:255],v[92:95]\nds_read_b64_tr_b16 v[220:221],v242 offset:58880\nds_read_b64_tr_b16 v[222:223],v242 offset:59136\nM v[32:35],v[192:195],a[224:227],v[32:35]\nv_add_f32 v145,v145,v161\nv_cvt_pk_bf16_f32 v144,0,v144\nM v[32:35],v[196:199],a[240:243],v[32:35]\n" \
"v_cvt_pk_bf16_f32 v145,0,v145\nv_mul_f32 v144,s72,v144\nM v[36:39],v[192:195],a[228:231],v[36:39]\nv_mul_f32 v145,s72,v145\nv_cvt_pk_bf16_f32 v236,v144,v145\nM v[36:39],v[196:199],a[244:247],v[36:39]\nv_cvt_pk_bf16_f32 v144,v4,v5\nv_cvt_pk_bf16_f32 v145,v6,v7\n" \
"M v[96:99],v[192:195],a[232:235],v[96:99]\nv_cvt_pk_bf16_f32 v160,v64,v65\nv_cvt_pk_bf16_f32 v161,v66,v67\nM v[96:99],v[196:199],a[248:251],v[96:99]\ns_and_b32 s71,s63,2\nv_mul_f32 v140,v140,v172\nM v[100:103],v[192:195],a[236:239],v[100:103]\ns_waitcnt lgkmcnt(4)\n" \
"v_mul_f32 v136,v136,v168\nM v[100:103],v[196:199],a[252:255],v[100:103]\nv_mul_f32 v137,v137,v169\nv_mul_f32 v138,v138,v170\nM v[40:43],v[200:203],a[224:227],v[40:43]\nv_mul_f32 v139,v139,v171\nv_mul_f32 v141,v141,v173\nM v[40:43],v[204:207],a[240:243],v[40:43]\n" \
"v_mul_f32 v142,v142,v174\nv_mul_f32 v143,v143,v175\nM v[44:47],v[200:203],a[228:231],v[44:47]\ns_lshl_b32 s71,s71,9\nv_cvt_pk_bf16_f32 v176,v68,v69\nM v[44:47],v[204:207],a[244:247],v[44:47]\nv_cvt_pk_bf16_f32 v177,v70,v71\nv_cvt_pk_bf16_f32 v136,0,v136\n" \
"M v[104:107],v[200:203],a[232:235],v[104:107]\nv_cvt_pk_bf16_f32 v137,0,v137\nv_cvt_pk_bf16_f32 v138,0,v138\nM v[104:107],v[204:207],a[248:251],v[104:107]\nv_cvt_pk_bf16_f32 v139,0,v139\nv_cvt_pk_bf16_f32 v140,0,v140\nM v[108:111],v[200:203],a[236:239],v[108:111]\nv_cvt_pk_bf16_f32 v141,0,v141\n" \
"v_cvt_pk_bf16_f32 v142,0,v142\nM v[108:111],v[204:207],a[252:255],v[108:111]\nv_cvt_pk_bf16_f32 v143,0,v143\nv_lshrrev_b32 v251,5,v249\nM v[48:51],v[208:211],a[224:227],v[48:51]\ns_add_u32 s70,s70,s71\nv_add_f32 v136,v136,v152\nM v[48:51],v[212:215],a[240:243],v[48:51]\n" \
"v_add_f32 v137,v137,v153\nv_add_f32 v138,v138,v154\nM v[52:55],v[208:211],a[228:231],v[52:55]\nv_add_f32 v139,v139,v155\nv_add_f32 v140,v140,v156\nM v[52:55],v[212:215],a[244:247],v[52:55]\nv_add_f32 v141,v141,v157\nv_add_f32 v142,v142,v158\n" \
"M v[112:115],v[208:211],a[232:235],v[112:115]\ns_waitcnt vmcnt(4) lgkmcnt(0)\ns_barrier\nM v[112:115],v[212:215],a[248:251],v[112:115]\nds_read_b64_tr_b16 v[192:193],v241 offset:0\nds_read_b64_tr_b16 v[194:195],v241 offset:256\nM v[116:119],v[208:211],a[236:239],v[116:119]\nds_read_b64_tr_b16 v[196:197],v241 offset:8192\n" \
"ds_read_b64_tr_b16 v[198:199],v241 offset:8448\nM v[116:119],v[212:215],a[252:255],v[116:119]\nds_read_b64_tr_b16 v[200:201],v242 offset:0\nds_read_b64_tr_b16 v[202:203],v242 offset:256\nM v[56:59],v[216:219],a[224:227],v[56:59]\nds_read_b64_tr_b16 v[204:205],v242 offset:8192\nds_read_b64_tr_b16 v[206:207],v242 offset:8448\nM v[56:59],v[220:223],a[240:243],v[56:59]\n" \
"ds_read_b64_tr_b16 v[208:209],v241 offset:512\nds_read_b64_tr_b16 v[210:211],v241 offset:768\nM v[60:63],v[216:219],a[228:231],v[60:63]\nv_add_f32 v143,v143,v159\nv_mul_f32 v146,v146,v178\nM v[60:63],v[220:223],a[244:247],v[60:63]\nv_mul_f32 v147,v147,v179\nv_lshlrev_b32 v251,11,v251\n" \
"M v[120:123],v[216:219],a[232:235],v[120:123]\ns_and_b32 s71,s63,1\nds_read_b64_tr_b16 v[212:213],v241 offset:8704\nM v[120:123],v[220:223],a[248:251],v[120:123]\nds_read_b64_tr_b16 v[214:215],v241 offset:8960\nv_cvt_pk_bf16_f32 v136,0,v136\nM v[124:127],v[216:219],a[236:239],v[124:127]\nv_cvt_pk_bf16_f32 v137,0,v137\n" \
"v_cvt_pk_bf16_f32 v138,0,v138\nM v[124:127],v[220:223],a[252:255],v[124:127]\nv_cvt_pk_bf16_f32 v139,0,v139\ns_waitcnt lgkmcnt(10)\nM v[0:3],a[0:3],v[192:195],0\nv_cvt_pk_bf16_f32 v140,0,v140\ns_waitcnt lgkmcnt(8)\nM v[0:3],a[16:19],v[196:199],v[0:3]\n" \
"v_cvt_pk_bf16_f32 v141,0,v141\nv_cvt_pk_bf16_f32 v142,0,v142\nM v[4:7],a[4:7],v[192:195],0\nv_cvt_pk_bf16_f32 v143,0,v143\nv_cvt_pk_bf16_f32 v146,0,v146\nM v[4:7],a[20:23],v[196:199],v[4:7]\nv_cvt_pk_bf16_f32 v147,0,v147\nv_lshl_or_b32 v251,v250,8,v251\n" \
"M v[64:67],a[8:11],v[192:195],0\ns_lshl_b32 s71,s71,7\nv_mul_f32 v136,s72,v136\nM v[64:67],a[24:27],v[196:199],v[64:67]\nv_mul_f32 v137,s72,v137\nv_mul_f32 v138,s72,v138\nM v[68:71],a[12:15],v[192:195],0\nv_mul_f32 v139,s72,v139\n" \
"v_mul_f32 v140,s72,v140\nM v[68:71],a[28:31],v[196:199],v[68:71]\nv_mul_f32 v141,s72,v141\nv_mul_f32 v142,s72,v142\nv_mul_f32 v143,s72,v143\nv_add_f32 v146,v146,v162\nv_add_f32 v147,v147,v163\nv_and_b32 v250,0x70,v240\n" \
"s_add_u32 s70,s70,s71\ns_waitcnt lgkmcnt(0)\nM v[8:11],a[0:3],v[200:203],0\nv_cvt_pk_bf16_f32 v232,v136,v137\nv_cvt_pk_bf16_f32 v233,v138,v139\nv_cvt_pk_bf16_f32 v234,v140,v141\nv_cvt_pk_bf16_f32 v235,v142,v143\nv_cvt_pk_bf16_f32 v146,0,v146\n" \
"v_cvt_pk_bf16_f32 v147,0,v147\nv_or_b32 v251,v251,v250\ns_add_u32 s70,s70,1048576\nv_mul_f32 v146,s72,v146\nv_mul_f32 v147,s72,v147\nbuffer_store_dwordx4 v[232:235],v251,s[52:55],s70 offen sc1\nM v[8:11],a[16:19],v[204:207],v[8:11]\nv_cvt_pk_bf16_f32 v237,v146,v147\n" \
"v_mul_f32 v148,v148,v180\nv_mul_f32 v149,v149,v181\nv_mul_f32 v150,v150,v182\nv_mul_f32 v151,v151,v183\nv_cvt_pk_bf16_f32 v146,v12,v13\nv_cvt_pk_bf16_f32 v147,v14,v15\nM v[12:15],a[4:7],v[200:203],0\nv_cvt_pk_bf16_f32 v148,0,v148\n" \
"v_cvt_pk_bf16_f32 v149,0,v149\nv_cvt_pk_bf16_f32 v150,0,v150\nv_cvt_pk_bf16_f32 v151,0,v151\nv_add_f32 v148,v148,v164\nv_add_f32 v149,v149,v165\nv_add_f32 v150,v150,v166\nv_add_f32 v151,v151,v167\nM v[12:15],a[20:23],v[204:207],v[12:15]\n" \
"v_cvt_pk_bf16_f32 v148,0,v148\nv_cvt_pk_bf16_f32 v149,0,v149\nv_cvt_pk_bf16_f32 v150,0,v150\nv_cvt_pk_bf16_f32 v151,0,v151\nv_mul_f32 v148,s72,v148\nv_mul_f32 v149,s72,v149\nv_mul_f32 v150,s72,v150\nv_mul_f32 v151,s72,v151\n" \
"v_cvt_pk_bf16_f32 v162,v72,v73\nv_cvt_pk_bf16_f32 v163,v74,v75\nM v[72:75],a[8:11],v[200:203],0\nv_cvt_pk_bf16_f32 v238,v148,v149\nv_cvt_pk_bf16_f32 v239,v150,v151\ns_add_u32 s71,s70,16384\nbuffer_load_dwordx4 v244,s[40:43],s69 offen offset:1024 lds\nbuffer_load_dwordx4 v245,s[40:43],s69 offen offset:2048 lds\n" \
"buffer_load_dwordx4 v246,s[40:43],s69 offen offset:3072 lds\nbuffer_store_dwordx4 v[236:239],v251,s[52:55],s71 offen sc1\nM v[72:75],a[24:27],v[204:207],v[72:75]\nv_cvt_pk_bf16_f32 v178,v76,v77\nv_cvt_pk_bf16_f32 v179,v78,v79\nds_read_b64_tr_b16 v[216:217],v242 offset:512\nds_read_b64_tr_b16 v[218:219],v242 offset:768\nM v[76:79],a[12:15],v[200:203],0\n" \
"ds_read_b64_tr_b16 v[220:221],v242 offset:8704\nds_read_b64_tr_b16 v[222:223],v242 offset:8960\nM v[76:79],a[28:31],v[204:207],v[76:79]\nv_cvt_pk_bf16_f32 v148,v20,v21\nv_cvt_pk_bf16_f32 v149,v22,v23\nM v[16:19],a[0:3],v[208:211],0\nv_cvt_pk_bf16_f32 v164,v80,v81\nv_cvt_pk_bf16_f32 v165,v82,v83\n" \
"M v[16:19],a[16:19],v[212:215],v[16:19]\nv_cvt_pk_bf16_f32 v180,v84,v85\nv_cvt_pk_bf16_f32 v181,v86,v87\nM v[20:23],a[4:7],v[208:211],0\nds_read_b64_tr_b16 v[192:193],v241 offset:1024\nds_read_b64_tr_b16 v[194:195],v241 offset:1280\nM v[20:23],a[20:23],v[212:215],v[20:23]\nds_read_b64_tr_b16 v[196:197],v241 offset:9216\n" \
"ds_read_b64_tr_b16 v[198:199],v241 offset:9472\nM v[80:83],a[8:11],v[208:211],0\nv_cvt_pk_bf16_f32 v134,v24,v25\nv_cvt_pk_bf16_f32 v135,v26,v27\nM v[80:83],a[24:27],v[212:215],v[80:83]\nv_cvt_pk_bf16_f32 v150,v28,v29\nv_cvt_pk_bf16_f32 v151,v30,v31\nM v[84:87],a[12:15],v[208:211],0\n" \
"v_cvt_pk_bf16_f32 v166,v88,v89\nv_cvt_pk_bf16_f32 v167,v90,v91\nM v[84:87],a[28:31],v[212:215],v[84:87]\nv_cvt_pk_bf16_f32 v182,v92,v93\ns_waitcnt lgkmcnt(6)\nM v[24:27],a[0:3],v[216:219],0\nv_cvt_pk_bf16_f32 v183,v94,v95\ns_waitcnt lgkmcnt(4)\n" \
"M v[24:27],a[16:19],v[220:223],v[24:27]\nds_read_b64_tr_b16 v[200:201],v242 offset:1024\nds_read_b64_tr_b16 v[202:203],v242 offset:1280\nM v[28:31],a[4:7],v[216:219],0\nds_read_b64_tr_b16 v[204:205],v242 offset:9216\nds_read_b64_tr_b16 v[206:207],v242 offset:9472\nM v[28:31],a[20:23],v[220:223],v[28:31]\nv_cvt_pk_bf16_f32 v136,v32,v33\n" \
"v_cvt_pk_bf16_f32 v137,v34,v35\nM v[88:91],a[8:11],v[216:219],0\nv_cvt_pk_bf16_f32 v152,v36,v37\nv_cvt_pk_bf16_f32 v153,v38,v39\nM v[88:91],a[24:27],v[220:223],v[88:91]\nv_cvt_pk_bf16_f32 v168,v96,v97\nv_cvt_pk_bf16_f32 v169,v98,v99\nM v[92:95],a[12:15],v[216:219],0\n" \
"s_add_u32 m0,s64,131072\ns_add_u32 s69,s68,1048576\nM v[92:95],a[28:31],v[220:223],v[92:95]\nv_cvt_pk_bf16_f32 v184,v100,v101\ns_waitcnt lgkmcnt(4)\nM v[32:35],a[0:3],v[192:195],0\nv_cvt_pk_bf16_f32 v185,v102,v103\nds_read_b64_tr_b16 v[208:209],v241 offset:1536\n" \
"M v[32:35],a[16:19],v[196:199],v[32:35]\nds_read_b64_tr_b16 v[210:211],v241 offset:1792\nds_read_b64_tr_b16 v[212:213],v241 offset:9728\nM v[36:39],a[4:7],v[192:195],0\nds_read_b64_tr_b16 v[214:215],v241 offset:9984\nv_cvt_pk_bf16_f32 v138,v40,v41\nM v[36:39],a[20:23],v[196:199],v[36:39]\nv_cvt_pk_bf16_f32 v139,v42,v43\n" \
"v_cvt_pk_bf16_f32 v154,v44,v45\nM v[96:99],a[8:11],v[192:195],0\nv_cvt_pk_bf16_f32 v155,v46,v47\nds_read_b64_tr_b16 v[216:217],v242 offset:1536\nM v[96:99],a[24:27],v[196:199],v[96:99]\nds_read_b64_tr_b16 v[218:219],v242 offset:1792\nds_read_b64_tr_b16 v[220:221],v242 offset:9728\nM v[100:103],a[12:15],v[192:195],0\n" \
"ds_read_b64_tr_b16 v[222:223],v242 offset:9984\nv_cvt_pk_bf16_f32 v170,v104,v105\nM v[100:103],a[28:31],v[196:199],v[100:103]\nv_cvt_pk_bf16_f32 v171,v106,v107\ns_waitcnt lgkmcnt(8)\nM v[40:43],a[0:3],v[200:203],0\nv_cvt_pk_bf16_f32 v186,v108,v109\nv_cvt_pk_bf16_f32 v187,v110,v111\n" \
"M v[40:43],a[16:19],v[204:207],v[40:43]\nv_cvt_pk_bf16_f32 v140,v48,v49\nv_cvt_pk_bf16_f32 v141,v50,v51\nM v[44:47],a[4:7],v[200:203],0\nv_cvt_pk_bf16_f32 v156,v52,v53\nv_cvt_pk_bf16_f32 v157,v54,v55\nM v[44:47],a[20:23],v[204:207],v[44:47]\nv_cvt_pk_bf16_f32 v172,v112,v113\n" \
"v_cvt_pk_bf16_f32 v173,v114,v115\nM v[104:107],a[8:11],v[200:203],0\nv_cvt_pk_bf16_f32 v188,v116,v117\nv_cvt_pk_bf16_f32 v189,v118,v119\nM v[104:107],a[24:27],v[204:207],v[104:107]\nv_cvt_pk_bf16_f32 v142,v56,v57\nv_cvt_pk_bf16_f32 v143,v58,v59\nM v[108:111],a[12:15],v[200:203],0\n" \
"v_cvt_pk_bf16_f32 v158,v60,v61\nv_cvt_pk_bf16_f32 v159,v62,v63\nM v[108:111],a[28:31],v[204:207],v[108:111]\nv_cvt_pk_bf16_f32 v174,v120,v121\ns_waitcnt lgkmcnt(2)\nM v[48:51],a[0:3],v[208:211],0\nv_cvt_pk_bf16_f32 v175,v122,v123\nv_cvt_pk_bf16_f32 v190,v124,v125\n" \
"M v[48:51],a[16:19],v[212:215],v[48:51]\nv_cvt_pk_bf16_f32 v191,v126,v127\ns_lshl_b32 s70,s62,5\nM v[52:55],a[4:7],v[208:211],0\ns_add_u32 s70,s70,s63\ns_lshl2_add_u32 s70,s70,s7\nM v[52:55],a[20:23],v[212:215],v[52:55]\ns_lshl2_add_u32 s79,s70,90112\n" \
"s_lshl_b32 s70,s60,4\nM v[112:115],a[8:11],v[208:211],0\ns_waitcnt vmcnt(1) lgkmcnt(0)\ns_barrier\nM v[112:115],a[24:27],v[212:215],v[112:115]\nds_read_b64_tr_b16 v[192:193],v241 offset:16384\nds_read_b64_tr_b16 v[194:195],v241 offset:16640\nM v[116:119],a[12:15],v[208:211],0\n" \
"ds_read_b64_tr_b16 v[196:197],v241 offset:24576\nds_read_b64_tr_b16 v[198:199],v241 offset:24832\nM v[116:119],a[28:31],v[212:215],v[116:119]\nds_read_b64_tr_b16 v[200:201],v242 offset:16384\nds_read_b64_tr_b16 v[202:203],v242 offset:16640\nM v[56:59],a[0:3],v[216:219],0\nds_read_b64_tr_b16 v[204:205],v242 offset:24576\nds_read_b64_tr_b16 v[206:207],v242 offset:24832\n" \
"M v[56:59],a[16:19],v[220:223],v[56:59]\nds_read_b64_tr_b16 v[208:209],v241 offset:16896\nds_read_b64_tr_b16 v[210:211],v241 offset:17152\nM v[60:63],a[4:7],v[216:219],0\nds_read_b64_tr_b16 v[212:213],v241 offset:25088\nds_read_b64_tr_b16 v[214:215],v241 offset:25344\nM v[60:63],a[20:23],v[220:223],v[60:63]\nbuffer_load_dwordx4 v243,s[40:43],s69 offen offset:0 lds\n" \
"M v[120:123],a[8:11],v[216:219],0\ns_sub_u32 s70,s70,s62\ns_add_u32 s70,s70,15\nM v[120:123],a[24:27],v[220:223],v[120:123]\ns_lshl2_add_u32 s70,s70,s7\ns_lshl_b32 s70,s70,15\nM v[124:127],a[12:15],v[216:219],0\nds_read_b64_tr_b16 v[216:217],v242 offset:16896\n" \
"ds_read_b64_tr_b16 v[218:219],v242 offset:17152\nM v[124:127],a[28:31],v[220:223],v[124:127]\ns_lshl_b32 s71,s61,8\ns_waitcnt lgkmcnt(12)\nM v[0:3],a[32:35],v[192:195],v[0:3]\nds_read_b64_tr_b16 v[220:221],v242 offset:25088\ns_waitcnt lgkmcnt(11)\nM v[0:3],a[48:51],v[196:199],v[0:3]\n" \
"buffer_load_dwordx4 v244,s[40:43],s69 offen offset:1024 lds\nM v[4:7],a[36:39],v[192:195],v[4:7]\nds_read_b64_tr_b16 v[222:223],v242 offset:25344\ns_add_u32 s70,s70,s71\nM v[4:7],a[52:55],v[196:199],v[4:7]\ns_add_u32 s78,s70,54525952\ns_lshl3_add_u32 s70,s62,s7\nM v[64:67],a[40:43],v[192:195],v[64:67]\n" \
"s_lshl_b32 s76,s70,15\ns_lshl_b32 s80,s70,6\nM v[64:67],a[56:59],v[196:199],v[64:67]\nbuffer_load_dwordx4 v245,s[40:43],s69 offen offset:2048 lds\nM v[68:71],a[44:47],v[192:195],v[68:71]\ns_add_u32 s76,s76,1048576\ns_add_u32 s77,s76,131072\nM v[68:71],a[60:63],v[196:199],v[68:71]\n" \
"s_lshl_b32 s70,s94,12\ns_waitcnt lgkmcnt(4)\nM v[8:11],a[32:35],v[200:203],v[8:11]\ns_or_b32 s76,s76,s70\ns_or_b32 s77,s77,s70\nM v[8:11],a[48:51],v[204:207],v[8:11]\nbuffer_load_dwordx4 v246,s[40:43],s69 offen offset:3072 lds\nM v[12:15],a[36:39],v[200:203],v[12:15]\n" \
"s_lshl_b32 s70,s63,5\ns_add_u32 s70,s70,s74\nM v[12:15],a[52:55],v[204:207],v[12:15]\ns_lshl_b32 s74,s70,14\ns_add_u32 s74,s74,3145728\nM v[72:75],a[40:43],v[200:203],v[72:75]\nds_read_b64_tr_b16 v[192:193],v241 offset:17408\nds_read_b64_tr_b16 v[194:195],v241 offset:17664\n" \
"M v[72:75],a[56:59],v[204:207],v[72:75]\nds_read_b64_tr_b16 v[196:197],v241 offset:25600\nds_read_b64_tr_b16 v[198:199],v241 offset:25856\nM v[76:79],a[44:47],v[200:203],v[76:79]\nds_read_b64_tr_b16 v[200:201],v242 offset:17408\nds_read_b64_tr_b16 v[202:203],v242 offset:17664\nM v[76:79],a[60:63],v[204:207],v[76:79]\nds_read_b64_tr_b16 v[204:205],v242 offset:25600\n" \
"ds_read_b64_tr_b16 v[206:207],v242 offset:25856\nM v[16:19],a[32:35],v[208:211],v[16:19]\nbuffer_store_dwordx4 v[128:131],v240,s[52:55],s74 offen offset:0 sc1\nM v[16:19],a[48:51],v[212:215],v[16:19]\ns_add_u32 m0,s64,147456\ns_add_u32 s69,s68,1572864\nM v[20:23],a[36:39],v[208:211],v[20:23]\nv_lshlrev_b32 v228,2,v228\n" \
"v_lshl_or_b32 v228,v229,4,v228\nM v[20:23],a[52:55],v[212:215],v[20:23]\ns_add_u32 s80,s80,90112\ns_lshl2_add_u32 s29,s70,81920\nM v[80:83],a[40:43],v[208:211],v[80:83]\nbuffer_load_dwordx4 v243,s[40:43],s69 offen offset:0 lds\nM v[80:83],a[56:59],v[212:215],v[80:83]\ns_lshl_b32 s70,s7,14\n" \
"v_add_u32 v250,s70,v240\nM v[84:87],a[44:47],v[208:211],v[84:87]\nds_read_b64_tr_b16 v[208:209],v241 offset:17920\nds_read_b64_tr_b16 v[210:211],v241 offset:18176\nM v[84:87],a[60:63],v[212:215],v[84:87]\nds_read_b64_tr_b16 v[212:213],v241 offset:26112\ns_waitcnt lgkmcnt(8)\nM v[24:27],a[32:35],v[216:219],v[24:27]\n" \
"ds_read_b64_tr_b16 v[214:215],v241 offset:26368\ns_lshl_b32 s70,s63,19\nM v[24:27],a[48:51],v[220:223],v[24:27]\nbuffer_load_dwordx4 v244,s[40:43],s69 offen offset:1024 lds\nM v[28:31],a[36:39],v[216:219],v[28:31]\nds_write_b64 v250,v[128:129] offset:0\ns_add_u32 s72,s78,16384\nM v[28:31],a[52:55],v[220:223],v[28:31]\n" \
"ds_write_b64 v250,v[130:131] offset:8\ns_mul_i32 s71,s60,14\nM v[88:91],a[40:43],v[216:219],v[88:91]\nds_write_b64 v250,v[132:133] offset:1024\nv_and_b32 v229,15,v249\nM v[88:91],a[56:59],v[220:223],v[88:91]\nbuffer_load_dwordx4 v245,s[40:43],s69 offen offset:2048 lds\nM v[92:95],a[44:47],v[216:219],v[92:95]\n" \
"ds_read_b64_tr_b16 v[216:217],v242 offset:17920\nds_read_b64_tr_b16 v[218:219],v242 offset:18176\nM v[92:95],a[60:63],v[220:223],v[92:95]\nds_read_b64_tr_b16 v[220:221],v242 offset:26112\nds_read_b64_tr_b16 v[222:223],v242 offset:26368\nM v[32:35],a[32:35],v[192:195],v[32:35]\nv_lshlrev_b32 v229,7,v229\ns_waitcnt lgkmcnt(10)\n" \
"M v[32:35],a[48:51],v[196:199],v[32:35]\nbuffer_load_dwordx4 v246,s[40:43],s69 offen offset:3072 lds\nM v[36:39],a[36:39],v[192:195],v[36:39]\ns_add_u32 m0,s64,98304\ns_add_u32 s69,s68,2097152\nM v[36:39],a[52:55],v[196:199],v[36:39]\nds_write_b64 v250,v[134:135] offset:1032\nv_add_u32 v253,0xffd00000,v253\n" \
"M v[96:99],a[40:43],v[192:195],v[96:99]\ns_add_u32 s44,s34,s70\ns_addc_u32 s45,s35,0\nM v[96:99],a[56:59],v[196:199],v[96:99]\nbuffer_store_dwordx4 v[132:135],v240,s[52:55],s74 offen offset:1024 sc1\nM v[100:103],a[44:47],v[192:195],v[100:103]\ns_lshl_b32 s70,s62,15\ns_add_u32 s96,s70,s64\n" \
"M v[100:103],a[60:63],v[196:199],v[100:103]\ns_sub_u32 s96,s96,16384\ns_sub_u32 s46,491520,s70\nM v[40:43],a[32:35],v[200:203],v[40:43]\ns_cmp_eq_u32 s36,s96\ns_cselect_b32 s72,0xc000,s98\ns_add_u32 s65,s36,s72\nM v[40:43],a[48:51],v[204:207],v[40:43]\n" \
"buffer_load_dwordx4 v243,s[40:43],s69 offen offset:0 lds\nM v[44:47],a[36:39],v[200:203],v[44:47]\ns_cmp_eq_u32 s65,s96\ns_cselect_b32 s72,0xc000,s98\ns_add_u32 s66,s65,s72\nM v[44:47],a[52:55],v[204:207],v[44:47]\ns_cmp_eq_u32 s66,s96\ns_cselect_b32 s72,0xc000,s98\n" \
"s_add_u32 s95,s66,s72\nM v[104:107],a[40:43],v[200:203],v[104:107]\ns_lshl2_add_u32 s70,s62,0\ns_sub_u32 s70,s70,s7\nM v[104:107],a[56:59],v[204:207],v[104:107]\nbuffer_load_dwordx4 v244,s[40:43],s69 offen offset:1024 lds\nM v[108:111],a[44:47],v[200:203],v[108:111]\nds_write_b64 v250,v[136:137] offset:2048\n" \
"s_lshl_b32 s70,s70,16\nM v[108:111],a[60:63],v[204:207],v[108:111]\ns_add_u32 s33,s78,s70\ns_waitcnt lgkmcnt(1)\nM v[48:51],a[32:35],v[208:211],v[48:51]\ns_cmp_eq_u32 s62,7\ns_cselect_b32 s46,229376,s46\nM v[48:51],a[48:51],v[212:215],v[48:51]\n" \
"s_waitcnt vmcnt(12)\nbuffer_store_dword v252,off,s[52:55],s79 sc1\nM v[52:55],a[36:39],v[208:211],v[52:55]\nbuffer_load_dwordx4 v245,s[40:43],s69 offen offset:2048 lds\nM v[52:55],a[52:55],v[212:215],v[52:55]\ns_cmp_gt_i32 s62,-1\ns_cselect_b32 s72,s98,0xfffe4000\nM v[112:115],a[40:43],v[208:211],v[112:115]\n" \
"s_waitcnt vmcnt(10)\ns_barrier\nM v[112:115],a[56:59],v[212:215],v[112:115]\nds_read_b64_tr_b16 v[192:193],v241 offset:32768\nds_read_b64_tr_b16 v[194:195],v241 offset:33024\nM v[116:119],a[44:47],v[208:211],v[116:119]\nds_read_b64_tr_b16 v[196:197],v241 offset:40960\nds_read_b64_tr_b16 v[198:199],v241 offset:41216\n" \
"M v[116:119],a[60:63],v[212:215],v[116:119]\nbuffer_store_dwordx4 v[136:139],v240,s[52:55],s74 offen offset:2048 sc1\nM v[56:59],a[32:35],v[216:219],v[56:59]\nds_read_b64_tr_b16 v[200:201],v242 offset:32768\nds_read_b64_tr_b16 v[202:203],v242 offset:33024\nM v[56:59],a[48:51],v[220:223],v[56:59]\nds_read_b64_tr_b16 v[204:205],v242 offset:40960\nds_read_b64_tr_b16 v[206:207],v242 offset:41216\n" \
"M v[60:63],a[36:39],v[216:219],v[60:63]\nds_read_b64_tr_b16 v[208:209],v241 offset:33280\nds_read_b64_tr_b16 v[210:211],v241 offset:33536\nM v[60:63],a[52:55],v[220:223],v[60:63]\nds_read_b64_tr_b16 v[212:213],v241 offset:41472\nds_read_b64_tr_b16 v[214:215],v241 offset:41728\nM v[120:123],a[40:43],v[216:219],v[120:123]\nbuffer_load_dwordx4 v246,s[40:43],s69 offen offset:3072 lds\n" \
"M v[120:123],a[56:59],v[220:223],v[120:123]\ns_add_u32 m0,s64,114688\ns_add_u32 s69,s68,2621440\nM v[124:127],a[44:47],v[216:219],v[124:127]\nds_read_b64_tr_b16 v[216:217],v242 offset:33280\nds_read_b64_tr_b16 v[218:219],v242 offset:33536\nM v[124:127],a[60:63],v[220:223],v[124:127]\nds_read_b64_tr_b16 v[220:221],v242 offset:41472\n" \
"s_waitcnt lgkmcnt(13)\nM v[0:3],a[64:67],v[192:195],v[0:3]\nds_read_b64_tr_b16 v[222:223],v242 offset:41728\ns_waitcnt lgkmcnt(12)\nM v[0:3],a[80:83],v[196:199],v[0:3]\nbuffer_load_dwordx4 v243,s[40:43],s69 offen offset:0 lds\nM v[4:7],a[68:71],v[192:195],v[4:7]\nds_write_b64 v250,v[138:139] offset:2056\n" \
"s_add_u32 s15,s64,s46\nM v[4:7],a[84:87],v[196:199],v[4:7]\ns_lshl3_add_u32 s70,s62,s7\ns_add_u32 s15,s15,s72\nM v[64:67],a[72:75],v[192:195],v[64:67]\ns_lshl3_add_u32 s70,s70,s60\ns_lshl_b32 s67,s70,15\nM v[64:67],a[88:91],v[196:199],v[64:67]\n" \
"buffer_load_dwordx4 v244,s[40:43],s69 offen offset:1024 lds\nM v[68:71],a[76:79],v[192:195],v[68:71]\nds_read_b64_tr_b16 v[192:193],v241 offset:33792\nds_read_b64_tr_b16 v[194:195],v241 offset:34048\nM v[68:71],a[92:95],v[196:199],v[68:71]\nds_read_b64_tr_b16 v[196:197],v241 offset:41984\ns_waitcnt lgkmcnt(8)\nM v[8:11],a[64:67],v[200:203],v[8:11]\n" \
"ds_read_b64_tr_b16 v[198:199],v241 offset:42240\ns_add_u32 s97,s96,65536\nM v[8:11],a[80:83],v[204:207],v[8:11]\nbuffer_store_dwordx4 v[140:143],v240,s[52:55],s74 offen offset:3072 sc1\nM v[12:15],a[68:71],v[200:203],v[12:15]\ns_add_u32 s74,s74,4096\ns_lshl3_add_u32 s70,s62,s60\nM v[12:15],a[84:87],v[204:207],v[12:15]\n" \
"ds_write_b64 v250,v[140:141] offset:3072\ns_lshl2_add_u32 s70,s70,s7\nM v[72:75],a[72:75],v[200:203],v[72:75]\ns_cmp_eq_u32 s62,7\ns_cselect_b32 s72,98304,0\nM v[72:75],a[88:91],v[204:207],v[72:75]\nbuffer_load_dwordx4 v245,s[40:43],s69 offen offset:2048 lds\nM v[76:79],a[76:79],v[200:203],v[76:79]\n" \
"ds_read_b64_tr_b16 v[200:201],v242 offset:33792\nds_read_b64_tr_b16 v[202:203],v242 offset:34048\nM v[76:79],a[92:95],v[204:207],v[76:79]\nds_read_b64_tr_b16 v[204:205],v242 offset:41984\nds_read_b64_tr_b16 v[206:207],v242 offset:42240\nM v[16:19],a[64:67],v[208:211],v[16:19]\ns_sub_u32 s97,s97,s72\ns_lshl4_add_u32 s79,s70,65536\n" \
"M v[16:19],a[80:83],v[212:215],v[16:19]\nbuffer_load_dwordx4 v246,s[40:43],s69 offen offset:3072 lds\nM v[20:23],a[68:71],v[208:211],v[20:23]\nds_write_b64 v250,v[142:143] offset:3080\ns_lshr_b32 s70,s6,3\nM v[20:23],a[84:87],v[212:215],v[20:23]\ns_cmp_lt_u32 s62,4\ns_cselect_b32 s72,32768,0\n" \
"M v[80:83],a[72:75],v[208:211],v[80:83]\ns_add_u32 s97,s97,s72\ns_sub_u32 s70,s70,16\nM v[80:83],a[88:91],v[212:215],v[80:83]\ns_add_u32 s2,s70,s71\ns_lshl_b32 s72,s60,1\nM v[84:87],a[76:79],v[208:211],v[84:87]\nds_read_b64_tr_b16 v[208:209],v241 offset:34304\n" \
"ds_read_b64_tr_b16 v[210:211],v241 offset:34560\nM v[84:87],a[92:95],v[212:215],v[84:87]\nds_read_b64_tr_b16 v[212:213],v241 offset:42496\ns_waitcnt lgkmcnt(8)\nM v[24:27],a[64:67],v[216:219],v[24:27]\nbuffer_store_dwordx4 v[144:147],v240,s[52:55],s74 offen offset:0 sc1\nM v[24:27],a[80:83],v[220:223],v[24:27]\nds_read_b64_tr_b16 v[214:215],v241 offset:42752\n" \
"s_mul_i32 s71,s60,448\nM v[28:31],a[68:71],v[216:219],v[28:31]\ns_add_u32 s72,s72,s62\ns_and_b32 s71,s71,0xffffff80\nM v[28:31],a[84:87],v[220:223],v[28:31]\ns_sub_u32 s72,s72,6\ns_sub_u32 s58,1835008,s71\nM v[88:91],a[72:75],v[216:219],v[88:91]\n" \
"s_lshl_b32 s72,s72,18\ns_lshl_b32 s2,s2,5\nM v[88:91],a[88:91],v[220:223],v[88:91]\ns_cmp_ge_u32 s62,6\ns_cselect_b64 s[56:57],s[18:19],s[56:57]\ns_cselect_b32 s71,s72,s71\ns_cselect_b32 s58,0x40000,s58\ns_cselect_b32 s0,2048,s0\n" \
"M v[92:95],a[76:79],v[216:219],v[92:95]\nds_read_b64_tr_b16 v[216:217],v242 offset:34304\nds_read_b64_tr_b16 v[218:219],v242 offset:34560\nM v[92:95],a[92:95],v[220:223],v[92:95]\nds_read_b64_tr_b16 v[220:221],v242 offset:42496\nds_read_b64_tr_b16 v[222:223],v242 offset:42752\nM v[32:35],a[64:67],v[192:195],v[32:35]\nds_write_b64 v250,v[144:145] offset:4096\n" \
"s_mul_i32 s72,s60,229376\nM v[32:35],a[80:83],v[196:199],v[32:35]\ns_cmp_eq_u32 s62,3\ns_cselect_b64 s[56:57],s[26:27],s[56:57]\ns_cselect_b32 s71,s72,s71\ns_cselect_b32 s58,229376,s58\ns_cselect_b32 s0,4096,s0\nM v[36:39],a[68:71],v[192:195],v[36:39]\n" \
"s_cmp_lt_u32 s70,14\ns_cselect_b32 s2,s2,0\ns_cselect_b32 s3,-1,0\nM v[36:39],a[84:87],v[196:199],v[36:39]\ns_lshl2_add_u32 s70,s61,s7\nv_lshrrev_b32 v253,4,v249\nM v[96:99],a[72:75],v[192:195],v[96:99]\nv_and_b32 v251,0xf0,v240\n" \
"s_mul_i32 s1,s70,s0\nM v[96:99],a[88:91],v[196:199],v[96:99]\ns_add_u32 s48,s44,19922944\ns_addc_u32 s49,s45,0\nM v[100:103],a[76:79],v[192:195],v[100:103]\nv_lshl_or_b32 v253,v253,12,v251\ns_lshl_b32 s70,s61,8\nM v[100:103],a[92:95],v[196:199],v[100:103]\n" \
"s_mov_b32 s50,s46\ns_waitcnt lgkmcnt(4)\nM v[40:43],a[64:67],v[200:203],v[40:43]\ns_add_u32 s44,s44,3145728\ns_addc_u32 s45,s45,0\nM v[40:43],a[80:83],v[204:207],v[40:43]\ns_mov_b32 s94,0xc000\ns_sub_u32 s33,s33,1867776\n" \
"M v[44:47],a[68:71],v[200:203],v[44:47]\nbuffer_store_dwordx4 v[148:151],v240,s[52:55],s74 offen offset:1024 sc1\nM v[44:47],a[84:87],v[204:207],v[44:47]\nds_write_b64 v250,v[146:147] offset:4104\ns_add_u32 s67,s67,36700160\nM v[104:107],a[72:75],v[200:203],v[104:107]\ns_cmp_lt_u32 s62,3\ns_cselect_b32 s58,0,s58\n" \
"M v[104:107],a[88:91],v[204:207],v[104:107]\ns_add_u32 s56,s56,s71\ns_addc_u32 s57,s57,0\nM v[108:111],a[76:79],v[200:203],v[108:111]\ns_lshl_b32 s0,s0,4\nM v[108:111],a[92:95],v[204:207],v[108:111]\nM v[48:51],a[64:67],v[208:211],v[48:51]\nM v[48:51],a[80:83],v[212:215],v[48:51]\n" \
"M v[52:55],a[68:71],v[208:211],v[52:55]\ns_add_u32 m0,s64,131072\ns_add_u32 s69,s68,3145728\nM v[52:55],a[84:87],v[212:215],v[52:55]\nM v[112:115],a[72:75],v[208:211],v[112:115]\ns_waitcnt vmcnt(14) lgkmcnt(1)\ns_barrier\nM v[112:115],a[88:91],v[212:215],v[112:115]\n" \
"ds_read_b64_tr_b16 v[192:193],v241 offset:49152\nds_read_b64_tr_b16 v[194:195],v241 offset:49408\nM v[116:119],a[76:79],v[208:211],v[116:119]\nds_read_b64_tr_b16 v[196:197],v241 offset:57344\nds_read_b64_tr_b16 v[198:199],v241 offset:57600\nM v[116:119],a[92:95],v[212:215],v[116:119]\nds_read_b64_tr_b16 v[200:201],v242 offset:49152\nds_read_b64_tr_b16 v[202:203],v242 offset:49408\n" \
"M v[56:59],a[64:67],v[216:219],v[56:59]\nds_read_b64_tr_b16 v[204:205],v242 offset:57344\nds_read_b64_tr_b16 v[206:207],v242 offset:57600\nM v[56:59],a[80:83],v[220:223],v[56:59]\nds_read_b64_tr_b16 v[208:209],v241 offset:49664\nds_read_b64_tr_b16 v[210:211],v241 offset:49920\nM v[60:63],a[68:71],v[216:219],v[60:63]\nds_read_b64_tr_b16 v[212:213],v241 offset:57856\n" \
"ds_read_b64_tr_b16 v[214:215],v241 offset:58112\nM v[60:63],a[84:87],v[220:223],v[60:63]\nbuffer_load_dwordx4 v243,s[40:43],s69 offen offset:0 lds\nM v[120:123],a[72:75],v[216:219],v[120:123]\nds_write_b64 v250,v[148:149] offset:5120\nM v[120:123],a[88:91],v[220:223],v[120:123]\nM v[124:127],a[76:79],v[216:219],v[124:127]\nds_read_b64_tr_b16 v[216:217],v242 offset:49664\n" \
"ds_read_b64_tr_b16 v[218:219],v242 offset:49920\nM v[124:127],a[92:95],v[220:223],v[124:127]\nds_read_b64_tr_b16 v[220:221],v242 offset:57856\ns_waitcnt lgkmcnt(14)\nM v[0:3],a[96:99],v[192:195],v[0:3]\nbuffer_store_dwordx4 v[152:155],v240,s[52:55],s74 offen offset:2048 sc1\ns_waitcnt lgkmcnt(12)\nM v[0:3],a[112:115],v[196:199],v[0:3]\n" \
"ds_read_b64_tr_b16 v[222:223],v242 offset:58112\nM v[4:7],a[100:103],v[192:195],v[4:7]\nds_write_b64 v250,v[150:151] offset:5128\nM v[4:7],a[116:119],v[196:199],v[4:7]\nM v[64:67],a[104:107],v[192:195],v[64:67]\nbuffer_load_dwordx4 v244,s[40:43],s69 offen offset:1024 lds\nM v[64:67],a[120:123],v[196:199],v[64:67]\nM v[68:71],a[108:111],v[192:195],v[68:71]\n" \
"ds_read_b64_tr_b16 v[192:193],v241 offset:50176\nds_read_b64_tr_b16 v[194:195],v241 offset:50432\nM v[68:71],a[124:127],v[196:199],v[68:71]\nds_read_b64_tr_b16 v[196:197],v241 offset:58368\ns_waitcnt lgkmcnt(9)\nM v[8:11],a[96:99],v[200:203],v[8:11]\nbuffer_load_dwordx4 v245,s[40:43],s69 offen offset:2048 lds\nM v[8:11],a[112:115],v[204:207],v[8:11]\n" \
"ds_read_b64_tr_b16 v[198:199],v241 offset:58624\nM v[12:15],a[100:103],v[200:203],v[12:15]\nM v[12:15],a[116:119],v[204:207],v[12:15]\nM v[72:75],a[104:107],v[200:203],v[72:75]\nbuffer_load_dwordx4 v246,s[40:43],s69 offen offset:3072 lds\nM v[72:75],a[120:123],v[204:207],v[72:75]\nM v[76:79],a[108:111],v[200:203],v[76:79]\nds_read_b64_tr_b16 v[200:201],v242 offset:50176\n" \
"ds_read_b64_tr_b16 v[202:203],v242 offset:50432\nM v[76:79],a[124:127],v[204:207],v[76:79]\nds_read_b64_tr_b16 v[204:205],v242 offset:58368\nds_read_b64_tr_b16 v[206:207],v242 offset:58624\nM v[16:19],a[96:99],v[208:211],v[16:19]\nbuffer_store_dwordx4 v[156:159],v240,s[52:55],s74 offen offset:3072 sc1\nM v[16:19],a[112:115],v[212:215],v[16:19]\nds_write_b64 v250,v[152:153] offset:6144\n" \
"s_add_u32 s74,s74,4096\nM v[20:23],a[100:103],v[208:211],v[20:23]\nM v[20:23],a[116:119],v[212:215],v[20:23]\nM v[80:83],a[104:107],v[208:211],v[80:83]\nM v[80:83],a[120:123],v[212:215],v[80:83]\nM v[84:87],a[108:111],v[208:211],v[84:87]\nds_read_b64_tr_b16 v[208:209],v241 offset:50688\nds_read_b64_tr_b16 v[210:211],v241 offset:50944\n" \
"M v[84:87],a[124:127],v[212:215],v[84:87]\nds_read_b64_tr_b16 v[212:213],v241 offset:58880\ns_waitcnt lgkmcnt(8)\nM v[24:27],a[96:99],v[216:219],v[24:27]\nds_read_b64_tr_b16 v[214:215],v241 offset:59136\nM v[24:27],a[112:115],v[220:223],v[24:27]\nM v[28:31],a[100:103],v[216:219],v[28:31]\nds_write_b64 v250,v[154:155] offset:6152\n" \
"M v[28:31],a[116:119],v[220:223],v[28:31]\nM v[88:91],a[104:107],v[216:219],v[88:91]\nM v[88:91],a[120:123],v[220:223],v[88:91]\nM v[92:95],a[108:111],v[216:219],v[92:95]\nds_read_b64_tr_b16 v[216:217],v242 offset:50688\nds_read_b64_tr_b16 v[218:219],v242 offset:50944\nM v[92:95],a[124:127],v[220:223],v[92:95]\nds_read_b64_tr_b16 v[220:221],v242 offset:58880\n" \
"ds_read_b64_tr_b16 v[222:223],v242 offset:59136\nM v[32:35],a[96:99],v[192:195],v[32:35]\nbuffer_store_dwordx4 v[160:163],v240,s[52:55],s74 offen offset:0 sc1\nM v[32:35],a[112:115],v[196:199],v[32:35]\nM v[36:39],a[100:103],v[192:195],v[36:39]\nM v[36:39],a[116:119],v[196:199],v[36:39]\nM v[96:99],a[104:107],v[192:195],v[96:99]\nM v[96:99],a[120:123],v[196:199],v[96:99]\n" \
"M v[100:103],a[108:111],v[192:195],v[100:103]\nds_write_b64 v250,v[156:157] offset:7168\nM v[100:103],a[124:127],v[196:199],v[100:103]\ns_waitcnt lgkmcnt(7)\nM v[40:43],a[96:99],v[200:203],v[40:43]\nM v[40:43],a[112:115],v[204:207],v[40:43]\nM v[44:47],a[100:103],v[200:203],v[44:47]\nM v[44:47],a[116:119],v[204:207],v[44:47]\n" \
"M v[104:107],a[104:107],v[200:203],v[104:107]\nM v[104:107],a[120:123],v[204:207],v[104:107]\nM v[108:111],a[108:111],v[200:203],v[108:111]\nM v[108:111],a[124:127],v[204:207],v[108:111]\nM v[48:51],a[96:99],v[208:211],v[48:51]\ns_waitcnt lgkmcnt(3)\nM v[48:51],a[112:115],v[212:215],v[48:51]\nM v[52:55],a[100:103],v[208:211],v[52:55]\n" \
"buffer_store_dwordx4 v[164:167],v240,s[52:55],s74 offen offset:1024 sc1\nM v[52:55],a[116:119],v[212:215],v[52:55]\ns_add_u32 m0,s64,147456\ns_add_u32 s69,s68,3670016\nM v[112:115],a[104:107],v[208:211],v[112:115]\ns_waitcnt vmcnt(15) lgkmcnt(1)\ns_barrier\nM v[112:115],a[120:123],v[212:215],v[112:115]\n" \
"ds_read_b64_tr_b16 v[192:193],v241 offset:0\nds_read_b64_tr_b16 v[194:195],v241 offset:256\nM v[116:119],a[108:111],v[208:211],v[116:119]\nds_read_b64_tr_b16 v[196:197],v241 offset:8192\nds_read_b64_tr_b16 v[198:199],v241 offset:8448\nM v[116:119],a[124:127],v[212:215],v[116:119]\nds_read_b64_tr_b16 v[200:201],v242 offset:0\nds_read_b64_tr_b16 v[202:203],v242 offset:256\n" \
"M v[56:59],a[96:99],v[216:219],v[56:59]\nds_read_b64_tr_b16 v[204:205],v242 offset:8192\nds_read_b64_tr_b16 v[206:207],v242 offset:8448\nM v[56:59],a[112:115],v[220:223],v[56:59]\nds_read_b64_tr_b16 v[208:209],v241 offset:512\nds_read_b64_tr_b16 v[210:211],v241 offset:768\nM v[60:63],a[100:103],v[216:219],v[60:63]\nds_read_b64_tr_b16 v[212:213],v241 offset:8704\n" \
"ds_read_b64_tr_b16 v[214:215],v241 offset:8960\nM v[60:63],a[116:119],v[220:223],v[60:63]\nbuffer_load_dwordx4 v243,s[40:43],s69 offen offset:0 lds\nM v[120:123],a[104:107],v[216:219],v[120:123]\nds_write_b64 v250,v[158:159] offset:7176\ns_lshl_b32 s68,s60,19\nM v[120:123],a[120:123],v[220:223],v[120:123]\ns_lshl2_add_u32 s68,s64,s68\n" \
"s_add_u32 s68,s68,s70\nM v[124:127],a[108:111],v[216:219],v[124:127]\nds_read_b64_tr_b16 v[216:217],v242 offset:512\nds_read_b64_tr_b16 v[218:219],v242 offset:768\nM v[124:127],a[124:127],v[220:223],v[124:127]\nds_read_b64_tr_b16 v[220:221],v242 offset:8704\ns_waitcnt lgkmcnt(14)\nM v[0:3],a[128:131],v[192:195],v[0:3]\n" \
"ds_read_b64_tr_b16 v[222:223],v242 offset:8960\ns_waitcnt lgkmcnt(13)\nM v[0:3],a[144:147],v[196:199],v[0:3]\nbuffer_load_dwordx4 v244,s[40:43],s69 offen offset:1024 lds\nM v[4:7],a[132:135],v[192:195],v[4:7]\nds_write_b64 v250,v[160:161] offset:8192\nM v[4:7],a[148:151],v[196:199],v[4:7]\nM v[64:67],a[136:139],v[192:195],v[64:67]\n" \
"M v[64:67],a[152:155],v[196:199],v[64:67]\nbuffer_store_dwordx4 v[168:171],v240,s[52:55],s74 offen offset:2048 sc1\nM v[68:71],a[140:143],v[192:195],v[68:71]\nds_read_b64_tr_b16 v[192:193],v241 offset:1024\nds_read_b64_tr_b16 v[194:195],v241 offset:1280\nM v[68:71],a[156:159],v[196:199],v[68:71]\nds_read_b64_tr_b16 v[196:197],v241 offset:9216\ns_waitcnt lgkmcnt(9)\n" \
"M v[8:11],a[128:131],v[200:203],v[8:11]\nds_read_b64_tr_b16 v[198:199],v241 offset:9472\nM v[8:11],a[144:147],v[204:207],v[8:11]\nbuffer_load_dwordx4 v245,s[40:43],s69 offen offset:2048 lds\nM v[12:15],a[132:135],v[200:203],v[12:15]\nds_write_b64 v250,v[162:163] offset:8200\nM v[12:15],a[148:151],v[204:207],v[12:15]\nM v[72:75],a[136:139],v[200:203],v[72:75]\n" \
"M v[72:75],a[152:155],v[204:207],v[72:75]\nbuffer_load_dwordx4 v246,s[40:43],s69 offen offset:3072 lds\nM v[76:79],a[140:143],v[200:203],v[76:79]\nds_read_b64_tr_b16 v[200:201],v242 offset:1024\nds_read_b64_tr_b16 v[202:203],v242 offset:1280\nM v[76:79],a[156:159],v[204:207],v[76:79]\nds_read_b64_tr_b16 v[204:205],v242 offset:9216\nds_read_b64_tr_b16 v[206:207],v242 offset:9472\n" \
"M v[16:19],a[128:131],v[208:211],v[16:19]\nM v[16:19],a[144:147],v[212:215],v[16:19]\nM v[20:23],a[132:135],v[208:211],v[20:23]\nM v[20:23],a[148:151],v[212:215],v[20:23]\nM v[80:83],a[136:139],v[208:211],v[80:83]\nM v[80:83],a[152:155],v[212:215],v[80:83]\nM v[84:87],a[140:143],v[208:211],v[84:87]\nbuffer_load_dword v248,v228,s[52:55],s80 offen sc1\n" \
"M v[84:87],a[156:159],v[212:215],v[84:87]\nds_read_b64_tr_b16 v[208:209],v241 offset:1536\ns_waitcnt lgkmcnt(6)\nM v[24:27],a[128:131],v[216:219],v[24:27]\nbuffer_store_dwordx4 v[172:175],v240,s[52:55],s74 offen offset:3072 sc1\nM v[24:27],a[144:147],v[220:223],v[24:27]\nds_read_b64_tr_b16 v[210:211],v241 offset:1792\nds_read_b64_tr_b16 v[212:213],v241 offset:9728\n" \
"M v[28:31],a[132:135],v[216:219],v[28:31]\nds_read_b64_tr_b16 v[214:215],v241 offset:9984\ns_add_u32 s74,s74,4096\nM v[28:31],a[148:151],v[220:223],v[28:31]\nds_write_b64 v250,v[164:165] offset:9216\nM v[88:91],a[136:139],v[216:219],v[88:91]\nM v[88:91],a[152:155],v[220:223],v[88:91]\nM v[92:95],a[140:143],v[216:219],v[92:95]\n" \
"ds_read_b64_tr_b16 v[216:217],v242 offset:1536\nds_read_b64_tr_b16 v[218:219],v242 offset:1792\nM v[92:95],a[156:159],v[220:223],v[92:95]\nds_read_b64_tr_b16 v[220:221],v242 offset:9728\nds_read_b64_tr_b16 v[222:223],v242 offset:9984\nM v[32:35],a[128:131],v[192:195],v[32:35]\nM v[32:35],a[144:147],v[196:199],v[32:35]\nM v[36:39],a[132:135],v[192:195],v[36:39]\n" \
"ds_write_b64 v250,v[166:167] offset:9224\nM v[36:39],a[148:151],v[196:199],v[36:39]\nM v[96:99],a[136:139],v[192:195],v[96:99]\nM v[96:99],a[152:155],v[196:199],v[96:99]\nM v[100:103],a[140:143],v[192:195],v[100:103]\nM v[100:103],a[156:159],v[196:199],v[100:103]\ns_waitcnt lgkmcnt(9)\nM v[40:43],a[128:131],v[200:203],v[40:43]\n" \
"buffer_store_dwordx4 v[176:179],v240,s[52:55],s74 offen offset:0 sc1\nM v[40:43],a[144:147],v[204:207],v[40:43]\nM v[44:47],a[132:135],v[200:203],v[44:47]\nM v[44:47],a[148:151],v[204:207],v[44:47]\nM v[104:107],a[136:139],v[200:203],v[104:107]\nM v[104:107],a[152:155],v[204:207],v[104:107]\nM v[108:111],a[140:143],v[200:203],v[108:111]\nds_write_b64 v250,v[168:169] offset:10240\n" \
"M v[108:111],a[156:159],v[204:207],v[108:111]\ns_waitcnt lgkmcnt(4)\nM v[48:51],a[128:131],v[208:211],v[48:51]\nM v[48:51],a[144:147],v[212:215],v[48:51]\nM v[52:55],a[132:135],v[208:211],v[52:55]\ns_sub_u32 s40,s12,0x1000\ns_subb_u32 s41,s13,0\nM v[52:55],a[148:151],v[212:215],v[52:55]\n" \
"s_add_u32 m0,s64,98304\ns_xor_b32 s69,s99,0\nM v[112:115],a[136:139],v[208:211],v[112:115]\ns_waitcnt vmcnt(18) lgkmcnt(1)\ns_barrier\nM v[112:115],a[152:155],v[212:215],v[112:115]\nds_read_b64_tr_b16 v[192:193],v241 offset:16384\nds_read_b64_tr_b16 v[194:195],v241 offset:16640\n" \
"M v[116:119],a[140:143],v[208:211],v[116:119]\nds_read_b64_tr_b16 v[196:197],v241 offset:24576\nds_read_b64_tr_b16 v[198:199],v241 offset:24832\nM v[116:119],a[156:159],v[212:215],v[116:119]\nds_read_b64_tr_b16 v[200:201],v242 offset:16384\nds_read_b64_tr_b16 v[202:203],v242 offset:16640\nM v[56:59],a[128:131],v[216:219],v[56:59]\nds_read_b64_tr_b16 v[204:205],v242 offset:24576\n" \
"ds_read_b64_tr_b16 v[206:207],v242 offset:24832\nM v[56:59],a[144:147],v[220:223],v[56:59]\nds_read_b64_tr_b16 v[208:209],v241 offset:16896\nds_read_b64_tr_b16 v[210:211],v241 offset:17152\nM v[60:63],a[132:135],v[216:219],v[60:63]\nbuffer_store_dwordx4 v[180:183],v240,s[52:55],s74 offen offset:1024 sc1\nM v[60:63],a[148:151],v[220:223],v[60:63]\nds_read_b64_tr_b16 v[212:213],v241 offset:25088\n" \
"ds_read_b64_tr_b16 v[214:215],v241 offset:25344\nM v[120:123],a[136:139],v[216:219],v[120:123]\nds_write_b64 v250,v[170:171] offset:10248\ns_lshl_b32 s12,s61,13\nM v[120:123],a[152:155],v[220:223],v[120:123]\nM v[124:127],a[140:143],v[216:219],v[124:127]\nds_read_b64_tr_b16 v[216:217],v242 offset:16896\nds_read_b64_tr_b16 v[218:219],v242 offset:17152\n" \
"M v[124:127],a[156:159],v[220:223],v[124:127]\nds_read_b64_tr_b16 v[220:221],v242 offset:25088\ns_waitcnt lgkmcnt(14)\nM v[0:3],a[160:163],v[192:195],v[0:3]\nds_read_b64_tr_b16 v[222:223],v242 offset:25344\ns_waitcnt lgkmcnt(13)\nM v[0:3],a[176:179],v[196:199],v[0:3]\nbuffer_load_dwordx4 v243,s[40:43],s69 offen offset:0 lds\n" \
"M v[4:7],a[164:167],v[192:195],v[4:7]\nM v[4:7],a[180:183],v[196:199],v[4:7]\nM v[64:67],a[168:171],v[192:195],v[64:67]\nM v[64:67],a[184:187],v[196:199],v[64:67]\nbuffer_load_dwordx4 v244,s[40:43],s69 offen offset:1024 lds\nM v[68:71],a[172:175],v[192:195],v[68:71]\nds_read_b64_tr_b16 v[192:193],v241 offset:17408\nds_read_b64_tr_b16 v[194:195],v241 offset:17664\n" \
"M v[68:71],a[188:191],v[196:199],v[68:71]\nds_read_b64_tr_b16 v[196:197],v241 offset:25600\ns_waitcnt lgkmcnt(9)\nM v[8:11],a[160:163],v[200:203],v[8:11]\nds_write_b64 v250,v[172:173] offset:11264\nds_read_b64_tr_b16 v[198:199],v241 offset:25856\nM v[8:11],a[176:179],v[204:207],v[8:11]\nbuffer_load_dwordx4 v245,s[40:43],s69 offen offset:2048 lds\n" \
"M v[12:15],a[164:167],v[200:203],v[12:15]\nM v[12:15],a[180:183],v[204:207],v[12:15]\nM v[72:75],a[168:171],v[200:203],v[72:75]\nM v[72:75],a[184:187],v[204:207],v[72:75]\nbuffer_store_dwordx4 v[184:187],v240,s[52:55],s74 offen offset:2048 sc1\nM v[76:79],a[172:175],v[200:203],v[76:79]\ns_waitcnt vmcnt(7)\nv_cmp_eq_u32 vcc,0,v248\n" \
"M v[76:79],a[188:191],v[204:207],v[76:79]\ns_cbranch_vccnz .Lps0\n.Lpb0:\nds_read_b64_tr_b16 v[200:201],v242 offset:17408\nM v[16:19],a[160:163],v[208:211],v[16:19]\nds_read_b64_tr_b16 v[202:203],v242 offset:17664\ns_waitcnt lgkmcnt(7)\nM v[16:19],a[176:179],v[212:215],v[16:19]\n" \
"ds_read_b64_tr_b16 v[204:205],v242 offset:25600\nds_read_b64_tr_b16 v[206:207],v242 offset:25856\nM v[20:23],a[164:167],v[208:211],v[20:23]\nbuffer_load_dwordx4 a[0:3],v240,s[52:55],s76 offen offset:0 sc1\nM v[20:23],a[180:183],v[212:215],v[20:23]\nds_write_b64 v250,v[174:175] offset:11272\nv_and_b32 v228,31,v249\nM v[80:83],a[168:171],v[208:211],v[80:83]\n" \
"s_lshl_b32 s80,s63,7\nv_lshlrev_b32 v228,2,v228\nM v[80:83],a[184:187],v[212:215],v[80:83]\ns_add_u32 s80,s80,81920\nv_mov_b32 v251,v228\nM v[84:87],a[172:175],v[208:211],v[84:87]\nds_read_b64_tr_b16 v[208:209],v241 offset:17920\nds_read_b64_tr_b16 v[210:211],v241 offset:18176\n" \
"M v[84:87],a[188:191],v[212:215],v[84:87]\nds_read_b64_tr_b16 v[212:213],v241 offset:26112\nds_read_b64_tr_b16 v[214:215],v241 offset:26368\nM v[24:27],a[160:163],v[216:219],v[24:27]\nbuffer_load_dwordx4 a[16:19],v240,s[52:55],s76 offen offset:2048 sc1\nM v[24:27],a[176:179],v[220:223],v[24:27]\nM v[28:31],a[164:167],v[216:219],v[28:31]\nM v[28:31],a[180:183],v[220:223],v[28:31]\n" \
"M v[88:91],a[168:171],v[216:219],v[88:91]\nbuffer_load_dwordx4 a[4:7],v240,s[52:55],s76 offen offset:1024 sc1\nM v[88:91],a[184:187],v[220:223],v[88:91]\nM v[92:95],a[172:175],v[216:219],v[92:95]\nds_read_b64_tr_b16 v[216:217],v242 offset:17920\nds_read_b64_tr_b16 v[218:219],v242 offset:18176\nM v[92:95],a[188:191],v[220:223],v[92:95]\nds_read_b64_tr_b16 v[220:221],v242 offset:26112\n" \
"s_waitcnt lgkmcnt(7)\nM v[32:35],a[160:163],v[192:195],v[32:35]\nbuffer_store_dwordx4 v[188:191],v240,s[52:55],s74 offen offset:3072 sc1\nM v[32:35],a[176:179],v[196:199],v[32:35]\nds_read_b64_tr_b16 v[222:223],v242 offset:26368\ns_add_u32 s74,s74,16764928\nM v[36:39],a[164:167],v[192:195],v[36:39]\nds_write_b64 v250,v[176:177] offset:12288\n" \
"M v[36:39],a[180:183],v[196:199],v[36:39]\nM v[96:99],a[168:171],v[192:195],v[96:99]\nbuffer_load_dwordx4 a[20:23],v240,s[52:55],s76 offen offset:3072 sc1\nM v[96:99],a[184:187],v[196:199],v[96:99]\ns_xor_b32 s76,s76,4096\nM v[100:103],a[172:175],v[192:195],v[100:103]\nM v[100:103],a[188:191],v[196:199],v[100:103]\nM v[40:43],a[160:163],v[200:203],v[40:43]\n" \
"buffer_load_dwordx4 a[8:11],v240,s[52:55],s77 offen offset:0 sc1\nM v[40:43],a[176:179],v[204:207],v[40:43]\nM v[44:47],a[164:167],v[200:203],v[44:47]\nds_write_b64 v250,v[178:179] offset:12296\nM v[44:47],a[180:183],v[204:207],v[44:47]\nM v[104:107],a[168:171],v[200:203],v[104:107]\nbuffer_load_dwordx4 a[24:27],v240,s[52:55],s77 offen offset:2048 sc1\nM v[104:107],a[184:187],v[204:207],v[104:107]\n" \
"M v[108:111],a[172:175],v[200:203],v[108:111]\nM v[108:111],a[188:191],v[204:207],v[108:111]\ns_waitcnt lgkmcnt(3)\nM v[48:51],a[160:163],v[208:211],v[48:51]\nbuffer_load_dwordx4 a[12:15],v240,s[52:55],s77 offen offset:1024 sc1\nM v[48:51],a[176:179],v[212:215],v[48:51]\nM v[52:55],a[164:167],v[208:211],v[52:55]\nM v[52:55],a[180:183],v[212:215],v[52:55]\n" \
"M v[112:115],a[168:171],v[208:211],v[112:115]\ns_waitcnt lgkmcnt(1)\ns_barrier\nM v[112:115],a[184:187],v[212:215],v[112:115]\nds_read_b64_tr_b16 v[192:193],v241 offset:32768\nds_read_b64_tr_b16 v[194:195],v241 offset:33024\nM v[116:119],a[172:175],v[208:211],v[116:119]\nds_read_b64_tr_b16 v[196:197],v241 offset:40960\n" \
"ds_read_b64_tr_b16 v[198:199],v241 offset:41216\nM v[116:119],a[188:191],v[212:215],v[116:119]\nds_read_b64_tr_b16 v[200:201],v242 offset:32768\nds_read_b64_tr_b16 v[202:203],v242 offset:33024\nM v[56:59],a[160:163],v[216:219],v[56:59]\nds_read_b64_tr_b16 v[204:205],v242 offset:40960\nds_read_b64_tr_b16 v[206:207],v242 offset:41216\nM v[56:59],a[176:179],v[220:223],v[56:59]\n" \
"ds_read_b64_tr_b16 v[208:209],v241 offset:33280\nds_read_b64_tr_b16 v[210:211],v241 offset:33536\nM v[60:63],a[164:167],v[216:219],v[60:63]\nds_read_b64_tr_b16 v[212:213],v241 offset:41472\nds_read_b64_tr_b16 v[214:215],v241 offset:41728\nM v[60:63],a[180:183],v[220:223],v[60:63]\nbuffer_load_dwordx4 a[28:31],v240,s[52:55],s77 offen offset:3072 sc1\nM v[120:123],a[168:171],v[216:219],v[120:123]\n" \
"ds_write_b64 v250,v[180:181] offset:13312\ns_xor_b32 s77,s77,4096\nM v[120:123],a[184:187],v[220:223],v[120:123]\nM v[124:127],a[172:175],v[216:219],v[124:127]\nds_read_b64_tr_b16 v[216:217],v242 offset:33280\nds_read_b64_tr_b16 v[218:219],v242 offset:33536\nM v[124:127],a[188:191],v[220:223],v[124:127]\nds_read_b64_tr_b16 v[220:221],v242 offset:41472\n" \
"s_waitcnt lgkmcnt(14)\nM v[0:3],a[192:195],v[192:195],v[0:3]\nds_read_b64_tr_b16 v[222:223],v242 offset:41728\ns_waitcnt lgkmcnt(13)\nM v[0:3],a[208:211],v[196:199],v[0:3]\nbuffer_load_dwordx4 v246,s[40:43],s69 offen offset:3072 lds\nM v[4:7],a[196:199],v[192:195],v[4:7]\ns_add_u32 m0,s64,114688\n" \
"s_xor_b32 s69,s99,524288\nM v[4:7],a[212:215],v[196:199],v[4:7]\nds_write_b64 v250,v[182:183] offset:13320\nM v[64:67],a[200:203],v[192:195],v[64:67]\nM v[64:67],a[216:219],v[196:199],v[64:67]\nbuffer_load_dwordx4 a[32:35],v240,s[52:55],s76 offen offset:0 sc1\nM v[68:71],a[204:207],v[192:195],v[68:71]\nds_read_b64_tr_b16 v[192:193],v241 offset:33792\n" \
"ds_read_b64_tr_b16 v[194:195],v241 offset:34048\nM v[68:71],a[220:223],v[196:199],v[68:71]\nds_read_b64_tr_b16 v[196:197],v241 offset:41984\ns_waitcnt lgkmcnt(9)\nM v[8:11],a[192:195],v[200:203],v[8:11]\nds_read_b64_tr_b16 v[198:199],v241 offset:42240\nM v[8:11],a[208:211],v[204:207],v[8:11]\nbuffer_load_dwordx4 a[48:51],v240,s[52:55],s76 offen offset:2048 sc1\n" \
"M v[12:15],a[196:199],v[200:203],v[12:15]\nM v[12:15],a[212:215],v[204:207],v[12:15]\nM v[72:75],a[200:203],v[200:203],v[72:75]\nM v[72:75],a[216:219],v[204:207],v[72:75]\nbuffer_load_dwordx4 a[36:39],v240,s[52:55],s76 offen offset:1024 sc1\nM v[76:79],a[204:207],v[200:203],v[76:79]\nds_read_b64_tr_b16 v[200:201],v242 offset:33792\nds_read_b64_tr_b16 v[202:203],v242 offset:34048\n" \
"M v[76:79],a[220:223],v[204:207],v[76:79]\nds_read_b64_tr_b16 v[204:205],v242 offset:41984\nds_read_b64_tr_b16 v[206:207],v242 offset:42240\nM v[16:19],a[192:195],v[208:211],v[16:19]\nds_write_b64 v250,v[184:185] offset:14336\nM v[16:19],a[208:211],v[212:215],v[16:19]\nbuffer_load_dwordx4 a[52:55],v240,s[52:55],s76 offen offset:3072 sc1\nM v[20:23],a[196:199],v[208:211],v[20:23]\n" \
"s_xor_b32 s76,s76,12288\nM v[20:23],a[212:215],v[212:215],v[20:23]\nM v[80:83],a[200:203],v[208:211],v[80:83]\nM v[80:83],a[216:219],v[212:215],v[80:83]\nbuffer_load_dwordx4 a[40:43],v240,s[52:55],s77 offen offset:0 sc1\nM v[84:87],a[204:207],v[208:211],v[84:87]\nds_read_b64_tr_b16 v[208:209],v241 offset:34304\nds_read_b64_tr_b16 v[210:211],v241 offset:34560\n" \
"M v[84:87],a[220:223],v[212:215],v[84:87]\nds_read_b64_tr_b16 v[212:213],v241 offset:42496\ns_waitcnt lgkmcnt(8)\nM v[24:27],a[192:195],v[216:219],v[24:27]\nds_read_b64_tr_b16 v[214:215],v241 offset:42752\nM v[24:27],a[208:211],v[220:223],v[24:27]\nbuffer_load_dwordx4 a[56:59],v240,s[52:55],s77 offen offset:2048 sc1\nM v[28:31],a[196:199],v[216:219],v[28:31]\n" \
"ds_write_b64 v250,v[186:187] offset:14344\nM v[28:31],a[212:215],v[220:223],v[28:31]\nM v[88:91],a[200:203],v[216:219],v[88:91]\nM v[88:91],a[216:219],v[220:223],v[88:91]\nbuffer_load_dwordx4 a[44:47],v240,s[52:55],s77 offen offset:1024 sc1\nM v[92:95],a[204:207],v[216:219],v[92:95]\nds_read_b64_tr_b16 v[216:217],v242 offset:34304\nds_read_b64_tr_b16 v[218:219],v242 offset:34560\n" \
"M v[92:95],a[220:223],v[220:223],v[92:95]\nds_read_b64_tr_b16 v[220:221],v242 offset:42496\nds_read_b64_tr_b16 v[222:223],v242 offset:42752\nM v[32:35],a[192:195],v[192:195],v[32:35]\nM v[32:35],a[208:211],v[196:199],v[32:35]\nbuffer_load_dwordx4 a[60:63],v240,s[52:55],s77 offen offset:3072 sc1\nM v[36:39],a[196:199],v[192:195],v[36:39]\ns_xor_b32 s77,s77,12288\n" \
"M v[36:39],a[212:215],v[196:199],v[36:39]\nM v[96:99],a[200:203],v[192:195],v[96:99]\nM v[96:99],a[216:219],v[196:199],v[96:99]\nbuffer_load_dwordx4 v243,s[40:43],s69 offen offset:0 lds\nM v[100:103],a[204:207],v[192:195],v[100:103]\nds_write_b64 v250,v[188:189] offset:15360\nM v[100:103],a[220:223],v[196:199],v[100:103]\ns_waitcnt lgkmcnt(6)\n" \
"M v[40:43],a[192:195],v[200:203],v[40:43]\nM v[40:43],a[208:211],v[204:207],v[40:43]\nbuffer_load_dwordx4 v244,s[40:43],s69 offen offset:1024 lds\nM v[44:47],a[196:199],v[200:203],v[44:47]\nM v[44:47],a[212:215],v[204:207],v[44:47]\nM v[104:107],a[200:203],v[200:203],v[104:107]\nM v[104:107],a[216:219],v[204:207],v[104:107]\nbuffer_load_dwordx4 v245,s[40:43],s69 offen offset:2048 lds\n" \
"M v[108:111],a[204:207],v[200:203],v[108:111]\nM v[108:111],a[220:223],v[204:207],v[108:111]\nM v[48:51],a[192:195],v[208:211],v[48:51]\nM v[48:51],a[208:211],v[212:215],v[48:51]\nbuffer_load_dwordx4 v246,s[40:43],s69 offen offset:3072 lds\nM v[52:55],a[196:199],v[208:211],v[52:55]\ns_add_u32 m0,s64,131072\ns_xor_b32 s69,s99,1048576\n" \
"M v[52:55],a[212:215],v[212:215],v[52:55]\nds_write_b64 v250,v[190:191] offset:15368\nv_add_u32 v250,65536,v250\nM v[112:115],a[200:203],v[208:211],v[112:115]\ns_waitcnt lgkmcnt(2)\ns_barrier\nM v[112:115],a[216:219],v[212:215],v[112:115]\nds_read_b64_tr_b16 v[192:193],v241 offset:49152\n" \
"ds_read_b64_tr_b16 v[194:195],v241 offset:49408\nM v[116:119],a[204:207],v[208:211],v[116:119]\nds_read_b64_tr_b16 v[196:197],v241 offset:57344\nds_read_b64_tr_b16 v[198:199],v241 offset:57600\nM v[116:119],a[220:223],v[212:215],v[116:119]\nds_read_b64_tr_b16 v[200:201],v242 offset:49152\nds_read_b64_tr_b16 v[202:203],v242 offset:49408\nM v[56:59],a[192:195],v[216:219],v[56:59]\n" \
"ds_read_b64_tr_b16 v[204:205],v242 offset:57344\nds_read_b64_tr_b16 v[206:207],v242 offset:57600\nM v[56:59],a[208:211],v[220:223],v[56:59]\nds_read_b64_tr_b16 v[208:209],v241 offset:49664\nds_read_b64_tr_b16 v[210:211],v241 offset:49920\nM v[60:63],a[196:199],v[216:219],v[60:63]\nds_read_b64_tr_b16 v[212:213],v241 offset:57856\nds_read_b64_tr_b16 v[214:215],v241 offset:58112\n" \
"M v[60:63],a[212:215],v[220:223],v[60:63]\nbuffer_load_dwordx4 a[64:67],v240,s[52:55],s76 offen offset:0 sc1\nM v[120:123],a[200:203],v[216:219],v[120:123]\nM v[120:123],a[216:219],v[220:223],v[120:123]\nM v[124:127],a[204:207],v[216:219],v[124:127]\nds_read_b64_tr_b16 v[216:217],v242 offset:49664\nds_read_b64_tr_b16 v[218:219],v242 offset:49920\nM v[124:127],a[220:223],v[220:223],v[124:127]\n" \
"ds_read_b64_tr_b16 v[220:221],v242 offset:57856\ns_waitcnt lgkmcnt(13)\nM v[0:3],a[224:227],v[192:195],v[0:3]\nds_read_b64_tr_b16 v[222:223],v242 offset:58112\ns_waitcnt lgkmcnt(12)\nM v[0:3],a[240:243],v[196:199],v[0:3]\nbuffer_load_dwordx4 a[68:71],v240,s[52:55],s76 offen offset:1024 sc1\nM v[4:7],a[228:231],v[192:195],v[4:7]\n" \
"M v[4:7],a[244:247],v[196:199],v[4:7]\nM v[64:67],a[232:235],v[192:195],v[64:67]\nM v[64:67],a[248:251],v[196:199],v[64:67]\nbuffer_load_dwordx4 a[80:83],v240,s[52:55],s76 offen offset:2048 sc1\nM v[68:71],a[236:239],v[192:195],v[68:71]\nds_read_b64_tr_b16 v[192:193],v241 offset:50176\nds_read_b64_tr_b16 v[194:195],v241 offset:50432\nM v[68:71],a[252:255],v[196:199],v[68:71]\n" \
"ds_read_b64_tr_b16 v[196:197],v241 offset:58368\ns_waitcnt lgkmcnt(7)\nM v[8:11],a[224:227],v[200:203],v[8:11]\nds_read_b64_tr_b16 v[198:199],v241 offset:58624\nv_cvt_pk_bf16_f32 v128,v0,v1\nM v[8:11],a[240:243],v[204:207],v[8:11]\nv_cvt_pk_bf16_f32 v129,v2,v3\nv_cvt_pk_bf16_f32 v130,v4,v5\n" \
"M v[12:15],a[228:231],v[200:203],v[12:15]\nv_cvt_pk_bf16_f32 v131,v6,v7\nv_cvt_pk_bf16_f32 v160,v64,v65\nM v[12:15],a[244:247],v[204:207],v[12:15]\nv_cvt_pk_bf16_f32 v161,v66,v67\nv_cvt_pk_bf16_f32 v162,v68,v69\nM v[72:75],a[232:235],v[200:203],v[72:75]\nv_cvt_pk_bf16_f32 v163,v70,v71\n" \
"v_cvt_pk_bf16_f32 v132,v8,v9\nM v[72:75],a[248:251],v[204:207],v[72:75]\nbuffer_load_dwordx4 a[84:87],v240,s[52:55],s76 offen offset:3072 sc1\nM v[76:79],a[236:239],v[200:203],v[76:79]\nds_read_b64_tr_b16 v[200:201],v242 offset:50176\nds_read_b64_tr_b16 v[202:203],v242 offset:50432\nM v[76:79],a[252:255],v[204:207],v[76:79]\nds_read_b64_tr_b16 v[204:205],v242 offset:58368\n" \
"ds_read_b64_tr_b16 v[206:207],v242 offset:58624\nM v[16:19],a[224:227],v[208:211],v[16:19]\nv_cvt_pk_bf16_f32 v133,v10,v11\nv_cvt_pk_bf16_f32 v134,v12,v13\nM v[16:19],a[240:243],v[212:215],v[16:19]\nv_cvt_pk_bf16_f32 v135,v14,v15\nv_cvt_pk_bf16_f32 v164,v72,v73\nM v[20:23],a[228:231],v[208:211],v[20:23]\n" \
"v_cvt_pk_bf16_f32 v165,v74,v75\nv_cvt_pk_bf16_f32 v166,v76,v77\nM v[20:23],a[244:247],v[212:215],v[20:23]\nv_cvt_pk_bf16_f32 v167,v78,v79\ns_xor_b32 s76,s76,4096\nM v[80:83],a[232:235],v[208:211],v[80:83]\nv_cvt_pk_bf16_f32 v136,v16,v17\nv_cvt_pk_bf16_f32 v137,v18,v19\n" \
"M v[80:83],a[248:251],v[212:215],v[80:83]\nbuffer_load_dwordx4 a[72:75],v240,s[52:55],s77 offen offset:0 sc1\nM v[84:87],a[236:239],v[208:211],v[84:87]\nds_read_b64_tr_b16 v[208:209],v241 offset:50688\nds_read_b64_tr_b16 v[210:211],v241 offset:50944\nM v[84:87],a[252:255],v[212:215],v[84:87]\nds_read_b64_tr_b16 v[212:213],v241 offset:58880\ns_waitcnt lgkmcnt(7)\n" \
"M v[24:27],a[224:227],v[216:219],v[24:27]\nds_read_b64_tr_b16 v[214:215],v241 offset:59136\nv_cvt_pk_bf16_f32 v138,v20,v21\nM v[24:27],a[240:243],v[220:223],v[24:27]\nv_cvt_pk_bf16_f32 v139,v22,v23\nv_cvt_pk_bf16_f32 v168,v80,v81\nM v[28:31],a[228:231],v[216:219],v[28:31]\nv_cvt_pk_bf16_f32 v169,v82,v83\n" \
"v_cvt_pk_bf16_f32 v170,v84,v85\nM v[28:31],a[244:247],v[220:223],v[28:31]\nbuffer_load_dwordx4 a[88:91],v240,s[52:55],s77 offen offset:2048 sc1\nM v[88:91],a[232:235],v[216:219],v[88:91]\nv_cvt_pk_bf16_f32 v171,v86,v87\nv_cvt_pk_bf16_f32 v140,v24,v25\nM v[88:91],a[248:251],v[220:223],v[88:91]\nv_cvt_pk_bf16_f32 v141,v26,v27\n" \
"M v[92:95],a[236:239],v[216:219],v[92:95]\nds_read_b64_tr_b16 v[216:217],v242 offset:50688\nds_read_b64_tr_b16 v[218:219],v242 offset:50944\nM v[92:95],a[252:255],v[220:223],v[92:95]\nds_read_b64_tr_b16 v[220:221],v242 offset:58880\nds_read_b64_tr_b16 v[222:223],v242 offset:59136\nM v[32:35],a[224:227],v[192:195],v[32:35]\nv_cvt_pk_bf16_f32 v142,v28,v29\n" \
"v_cvt_pk_bf16_f32 v143,v30,v31\nM v[32:35],a[240:243],v[196:199],v[32:35]\nv_cvt_pk_bf16_f32 v172,v88,v89\nv_cvt_pk_bf16_f32 v173,v90,v91\nM v[36:39],a[228:231],v[192:195],v[36:39]\nv_cvt_pk_bf16_f32 v174,v92,v93\nv_cvt_pk_bf16_f32 v175,v94,v95\nM v[36:39],a[244:247],v[196:199],v[36:39]\n" \
"buffer_load_dwordx4 a[76:79],v240,s[52:55],s77 offen offset:1024 sc1\nM v[96:99],a[232:235],v[192:195],v[96:99]\nv_cvt_pk_bf16_f32 v144,v32,v33\nv_cvt_pk_bf16_f32 v145,v34,v35\nM v[96:99],a[248:251],v[196:199],v[96:99]\nM v[100:103],a[236:239],v[192:195],v[100:103]\nM v[100:103],a[252:255],v[196:199],v[100:103]\ns_waitcnt vmcnt(20)\n" \
"buffer_store_dword v252,off,s[52:55],s29 sc1\ns_waitcnt lgkmcnt(4)\nM v[40:43],a[224:227],v[200:203],v[40:43]\nv_cvt_pk_bf16_f32 v146,v36,v37\nv_cvt_pk_bf16_f32 v147,v38,v39\nM v[40:43],a[240:243],v[204:207],v[40:43]\nv_cvt_pk_bf16_f32 v176,v96,v97\nv_cvt_pk_bf16_f32 v177,v98,v99\n" \
"M v[44:47],a[228:231],v[200:203],v[44:47]\nv_cvt_pk_bf16_f32 v178,v100,v101\nv_cvt_pk_bf16_f32 v179,v102,v103\nM v[44:47],a[244:247],v[204:207],v[44:47]\nbuffer_load_dwordx4 a[92:95],v240,s[52:55],s77 offen offset:3072 sc1\nM v[104:107],a[232:235],v[200:203],v[104:107]\nv_cvt_pk_bf16_f32 v148,v40,v41\nv_cvt_pk_bf16_f32 v149,v42,v43\n" \
"M v[104:107],a[248:251],v[204:207],v[104:107]\ns_xor_b32 s77,s77,4096\ns_add_u32 s29,s29,4096\nM v[108:111],a[236:239],v[200:203],v[108:111]\nv_cvt_pk_bf16_f32 v150,v44,v45\nv_cvt_pk_bf16_f32 v151,v46,v47\nM v[108:111],a[252:255],v[204:207],v[108:111]\nbuffer_load_dwordx4 v243,s[40:43],s69 offen offset:0 lds\n" \
"M v[48:51],a[224:227],v[208:211],v[48:51]\nv_cvt_pk_bf16_f32 v180,v104,v105\nv_cvt_pk_bf16_f32 v181,v106,v107\nM v[48:51],a[240:243],v[212:215],v[48:51]\nM v[52:55],a[228:231],v[208:211],v[52:55]\nM v[52:55],a[244:247],v[212:215],v[52:55]\nbuffer_load_dwordx4 v244,s[40:43],s69 offen offset:1024 lds\nM v[112:115],a[232:235],v[208:211],v[112:115]\n" \
"s_waitcnt vmcnt(15) lgkmcnt(0)\ns_barrier\nM v[112:115],a[248:251],v[212:215],v[112:115]\nds_read_b64_tr_b16 v[192:193],v241 offset:0\nds_read_b64_tr_b16 v[194:195],v241 offset:256\nM v[116:119],a[236:239],v[208:211],v[116:119]\nds_read_b64_tr_b16 v[196:197],v241 offset:8192\nds_read_b64_tr_b16 v[198:199],v241 offset:8448\n" \
"M v[116:119],a[252:255],v[212:215],v[116:119]\nds_read_b64_tr_b16 v[200:201],v242 offset:0\nds_read_b64_tr_b16 v[202:203],v242 offset:256\nM v[56:59],a[224:227],v[216:219],v[56:59]\nds_read_b64_tr_b16 v[204:205],v242 offset:8192\nds_read_b64_tr_b16 v[206:207],v242 offset:8448\nM v[56:59],a[240:243],v[220:223],v[56:59]\nds_read_b64_tr_b16 v[208:209],v241 offset:512\n" \
"ds_read_b64_tr_b16 v[210:211],v241 offset:768\nM v[60:63],a[228:231],v[216:219],v[60:63]\nds_read_b64_tr_b16 v[212:213],v241 offset:8704\nds_read_b64_tr_b16 v[214:215],v241 offset:8960\nM v[60:63],a[244:247],v[220:223],v[60:63]\nv_cvt_pk_bf16_f32 v182,v108,v109\nv_cvt_pk_bf16_f32 v183,v110,v111\nM v[120:123],a[232:235],v[216:219],v[120:123]\n" \
"v_cvt_pk_bf16_f32 v152,v48,v49\nv_cvt_pk_bf16_f32 v153,v50,v51\nM v[120:123],a[248:251],v[220:223],v[120:123]\nv_cvt_pk_bf16_f32 v154,v52,v53\nv_cvt_pk_bf16_f32 v155,v54,v55\nM v[124:127],a[236:239],v[216:219],v[124:127]\nds_read_b64_tr_b16 v[216:217],v242 offset:512\nds_read_b64_tr_b16 v[218:219],v242 offset:768\n" \
"M v[124:127],a[252:255],v[220:223],v[124:127]\nds_read_b64_tr_b16 v[220:221],v242 offset:8704\ns_waitcnt lgkmcnt(13)\nM v[0:3],v[192:195],a[0:3],0\nds_read_b64_tr_b16 v[222:223],v242 offset:8960\ns_waitcnt lgkmcnt(12)\nM v[0:3],v[196:199],a[16:19],v[0:3]\nv_cvt_pk_bf16_f32 v184,v112,v113\n" \
"v_cvt_pk_bf16_f32 v185,v114,v115\nM v[4:7],v[192:195],a[4:7],0\nv_cvt_pk_bf16_f32 v186,v116,v117\nv_cvt_pk_bf16_f32 v187,v118,v119\nM v[4:7],v[196:199],a[20:23],v[4:7]\nv_cvt_pk_bf16_f32 v156,v56,v57\nv_cvt_pk_bf16_f32 v157,v58,v59\nM v[64:67],v[192:195],a[8:11],0\n" \
"v_cvt_pk_bf16_f32 v158,v60,v61\nv_cvt_pk_bf16_f32 v159,v62,v63\nM v[64:67],v[196:199],a[24:27],v[64:67]\nv_cvt_pk_bf16_f32 v188,v120,v121\nv_cvt_pk_bf16_f32 v189,v122,v123\nM v[68:71],v[192:195],a[12:15],0\nds_read_b64_tr_b16 v[192:193],v241 offset:1024\nds_read_b64_tr_b16 v[194:195],v241 offset:1280\n" \
"M v[68:71],v[196:199],a[28:31],v[68:71]\nds_read_b64_tr_b16 v[196:197],v241 offset:9216\ns_waitcnt lgkmcnt(7)\nM v[8:11],v[200:203],a[0:3],0\nds_read_b64_tr_b16 v[198:199],v241 offset:9472\nv_cvt_pk_bf16_f32 v190,v124,v125\nM v[8:11],v[204:207],a[16:19],v[8:11]\nbuffer_load_dwordx4 v245,s[40:43],s69 offen offset:2048 lds\n" \
"M v[12:15],v[200:203],a[4:7],0\nv_cvt_pk_bf16_f32 v191,v126,v127\nM v[12:15],v[204:207],a[20:23],v[12:15]\nM v[72:75],v[200:203],a[8:11],0\nM v[72:75],v[204:207],a[24:27],v[72:75]\nbuffer_load_dwordx4 v246,s[40:43],s69 offen offset:3072 lds\nM v[76:79],v[200:203],a[12:15],0\nds_read_b64_tr_b16 v[200:201],v242 offset:1024\n" \
"ds_read_b64_tr_b16 v[202:203],v242 offset:1280\nM v[76:79],v[204:207],a[28:31],v[76:79]\nds_read_b64_tr_b16 v[204:205],v242 offset:9216\nds_read_b64_tr_b16 v[206:207],v242 offset:9472\nM v[16:19],v[208:211],a[0:3],0\ns_add_u32 m0,s64,147456\ns_xor_b32 s69,s99,1572864\nM v[16:19],v[212:215],a[16:19],v[16:19]\n" \
"buffer_load_dwordx4 a[96:99],v240,s[52:55],s76 offen offset:0 sc1\nM v[20:23],v[208:211],a[4:7],0\nM v[20:23],v[212:215],a[20:23],v[20:23]\nM v[80:83],v[208:211],a[8:11],0\nM v[80:83],v[212:215],a[24:27],v[80:83]\nbuffer_store_dwordx4 v[128:131],v240,s[52:55],s74 offen offset:0 sc1\nM v[84:87],v[208:211],a[12:15],0\nds_read_b64_tr_b16 v[208:209],v241 offset:1536\n" \
"ds_read_b64_tr_b16 v[210:211],v241 offset:1792\nM v[84:87],v[212:215],a[28:31],v[84:87]\nds_read_b64_tr_b16 v[212:213],v241 offset:9728\ns_waitcnt lgkmcnt(7)\nM v[24:27],v[216:219],a[0:3],0\nds_read_b64_tr_b16 v[214:215],v241 offset:9984\nM v[24:27],v[220:223],a[16:19],v[24:27]\nbuffer_load_dwordx4 a[112:115],v240,s[52:55],s76 offen offset:2048 sc1\n" \
"M v[28:31],v[216:219],a[4:7],0\nM v[28:31],v[220:223],a[20:23],v[28:31]\nM v[88:91],v[216:219],a[8:11],0\nM v[88:91],v[220:223],a[24:27],v[88:91]\nbuffer_load_dwordx4 a[100:103],v240,s[52:55],s76 offen offset:1024 sc1\nM v[92:95],v[216:219],a[12:15],0\nds_read_b64_tr_b16 v[216:217],v242 offset:1536\nds_read_b64_tr_b16 v[218:219],v242 offset:1792\n" \
"M v[92:95],v[220:223],a[28:31],v[92:95]\nds_read_b64_tr_b16 v[220:221],v242 offset:9728\nds_read_b64_tr_b16 v[222:223],v242 offset:9984\nM v[32:35],v[192:195],a[0:3],0\nM v[32:35],v[196:199],a[16:19],v[32:35]\nbuffer_load_dwordx4 a[116:119],v240,s[52:55],s76 offen offset:3072 sc1\nM v[36:39],v[192:195],a[4:7],0\ns_xor_b32 s76,s76,28672\n" \
"M v[36:39],v[196:199],a[20:23],v[36:39]\nM v[96:99],v[192:195],a[8:11],0\nM v[96:99],v[196:199],a[24:27],v[96:99]\nbuffer_load_dwordx4 a[104:107],v240,s[52:55],s77 offen offset:0 sc1\nM v[100:103],v[192:195],a[12:15],0\nM v[100:103],v[196:199],a[28:31],v[100:103]\ns_waitcnt lgkmcnt(4)\nM v[40:43],v[200:203],a[0:3],0\n" \
"M v[40:43],v[204:207],a[16:19],v[40:43]\nbuffer_load_dwordx4 a[120:123],v240,s[52:55],s77 offen offset:2048 sc1\nM v[44:47],v[200:203],a[4:7],0\nM v[44:47],v[204:207],a[20:23],v[44:47]\nM v[104:107],v[200:203],a[8:11],0\nM v[104:107],v[204:207],a[24:27],v[104:107]\nbuffer_load_dwordx4 a[108:111],v240,s[52:55],s77 offen offset:1024 sc1\nM v[108:111],v[200:203],a[12:15],0\n" \
"M v[108:111],v[204:207],a[28:31],v[108:111]\nM v[48:51],v[208:211],a[0:3],0\nM v[48:51],v[212:215],a[16:19],v[48:51]\nbuffer_load_dwordx4 a[124:127],v240,s[52:55],s77 offen offset:3072 sc1\nM v[52:55],v[208:211],a[4:7],0\ns_xor_b32 s77,s77,28672\nM v[52:55],v[212:215],a[20:23],v[52:55]\nM v[112:115],v[208:211],a[8:11],0\n" \
"s_waitcnt vmcnt(15) lgkmcnt(0)\ns_barrier\nM v[112:115],v[212:215],a[24:27],v[112:115]\nds_read_b64_tr_b16 v[192:193],v241 offset:16384\nds_read_b64_tr_b16 v[194:195],v241 offset:16640\nM v[116:119],v[208:211],a[12:15],0\nds_read_b64_tr_b16 v[196:197],v241 offset:24576\nds_read_b64_tr_b16 v[198:199],v241 offset:24832\n" \
"buffer_store_dwordx4 v[132:135],v240,s[52:55],s74 offen offset:1024 sc1\nM v[116:119],v[212:215],a[28:31],v[116:119]\nds_read_b64_tr_b16 v[200:201],v242 offset:16384\nds_read_b64_tr_b16 v[202:203],v242 offset:16640\nM v[56:59],v[216:219],a[0:3],0\nds_read_b64_tr_b16 v[204:205],v242 offset:24576\nds_read_b64_tr_b16 v[206:207],v242 offset:24832\nM v[56:59],v[220:223],a[16:19],v[56:59]\n" \
"ds_read_b64_tr_b16 v[208:209],v241 offset:16896\nds_read_b64_tr_b16 v[210:211],v241 offset:17152\nM v[60:63],v[216:219],a[4:7],0\nds_read_b64_tr_b16 v[212:213],v241 offset:25088\nds_read_b64_tr_b16 v[214:215],v241 offset:25344\nM v[60:63],v[220:223],a[20:23],v[60:63]\nbuffer_load_dwordx4 v243,s[40:43],s69 offen offset:0 lds\nM v[120:123],v[216:219],a[8:11],0\n" \
"M v[120:123],v[220:223],a[24:27],v[120:123]\nM v[124:127],v[216:219],a[12:15],0\nds_read_b64_tr_b16 v[216:217],v242 offset:16896\nds_read_b64_tr_b16 v[218:219],v242 offset:17152\nM v[124:127],v[220:223],a[28:31],v[124:127]\nds_read_b64_tr_b16 v[220:221],v242 offset:25088\ns_waitcnt lgkmcnt(13)\nM v[0:3],v[192:195],a[32:35],v[0:3]\n" \
"ds_read_b64_tr_b16 v[222:223],v242 offset:25344\ns_waitcnt lgkmcnt(12)\nM v[0:3],v[196:199],a[48:51],v[0:3]\nbuffer_load_dwordx4 v244,s[40:43],s69 offen offset:1024 lds\nM v[4:7],v[192:195],a[36:39],v[4:7]\nM v[4:7],v[196:199],a[52:55],v[4:7]\nM v[64:67],v[192:195],a[40:43],v[64:67]\nM v[64:67],v[196:199],a[56:59],v[64:67]\n" \
"buffer_load_dwordx4 v245,s[40:43],s69 offen offset:2048 lds\nM v[68:71],v[192:195],a[44:47],v[68:71]\nds_read_b64_tr_b16 v[192:193],v241 offset:17408\nds_read_b64_tr_b16 v[194:195],v241 offset:17664\nM v[68:71],v[196:199],a[60:63],v[68:71]\nds_read_b64_tr_b16 v[196:197],v241 offset:25600\ns_waitcnt lgkmcnt(7)\nM v[8:11],v[200:203],a[32:35],v[8:11]\n" \
"ds_read_b64_tr_b16 v[198:199],v241 offset:25856\nM v[8:11],v[204:207],a[48:51],v[8:11]\nbuffer_load_dwordx4 v246,s[40:43],s69 offen offset:3072 lds\nM v[12:15],v[200:203],a[36:39],v[12:15]\ns_add_u32 m0,s64,98304\ns_xor_b32 s69,s99,2097152\nM v[12:15],v[204:207],a[52:55],v[12:15]\nM v[72:75],v[200:203],a[40:43],v[72:75]\n" \
"M v[72:75],v[204:207],a[56:59],v[72:75]\nbuffer_load_dwordx4 a[128:131],v240,s[52:55],s76 offen offset:0 sc1\nM v[76:79],v[200:203],a[44:47],v[76:79]\nds_read_b64_tr_b16 v[200:201],v242 offset:17408\nds_read_b64_tr_b16 v[202:203],v242 offset:17664\nM v[76:79],v[204:207],a[60:63],v[76:79]\nds_read_b64_tr_b16 v[204:205],v242 offset:25600\nds_read_b64_tr_b16 v[206:207],v242 offset:25856\n" \
"M v[16:19],v[208:211],a[32:35],v[16:19]\nM v[16:19],v[212:215],a[48:51],v[16:19]\nbuffer_store_dwordx4 v[136:139],v240,s[52:55],s74 offen offset:2048 sc1\nM v[20:23],v[208:211],a[36:39],v[20:23]\nM v[20:23],v[212:215],a[52:55],v[20:23]\nM v[80:83],v[208:211],a[40:43],v[80:83]\nM v[80:83],v[212:215],a[56:59],v[80:83]\nbuffer_load_dwordx4 a[144:147],v240,s[52:55],s76 offen offset:2048 sc1\n" \
"M v[84:87],v[208:211],a[44:47],v[84:87]\nds_read_b64_tr_b16 v[208:209],v241 offset:17920\nds_read_b64_tr_b16 v[210:211],v241 offset:18176\nM v[84:87],v[212:215],a[60:63],v[84:87]\nds_read_b64_tr_b16 v[212:213],v241 offset:26112\ns_waitcnt lgkmcnt(7)\nM v[24:27],v[216:219],a[32:35],v[24:27]\nds_read_b64_tr_b16 v[214:215],v241 offset:26368\n" \
"M v[24:27],v[220:223],a[48:51],v[24:27]\nbuffer_load_dwordx4 a[132:135],v240,s[52:55],s76 offen offset:1024 sc1\nM v[28:31],v[216:219],a[36:39],v[28:31]\nM v[28:31],v[220:223],a[52:55],v[28:31]\nM v[88:91],v[216:219],a[40:43],v[88:91]\nM v[88:91],v[220:223],a[56:59],v[88:91]\nbuffer_load_dwordx4 a[148:151],v240,s[52:55],s76 offen offset:3072 sc1\nM v[92:95],v[216:219],a[44:47],v[92:95]\n" \
"ds_read_b64_tr_b16 v[216:217],v242 offset:17920\nds_read_b64_tr_b16 v[218:219],v242 offset:18176\nM v[92:95],v[220:223],a[60:63],v[92:95]\nds_read_b64_tr_b16 v[220:221],v242 offset:26112\nds_read_b64_tr_b16 v[222:223],v242 offset:26368\nM v[32:35],v[192:195],a[32:35],v[32:35]\ns_xor_b32 s76,s76,4096\nM v[32:35],v[196:199],a[48:51],v[32:35]\n" \
"buffer_load_dwordx4 a[136:139],v240,s[52:55],s77 offen offset:0 sc1\nM v[36:39],v[192:195],a[36:39],v[36:39]\nM v[36:39],v[196:199],a[52:55],v[36:39]\nM v[96:99],v[192:195],a[40:43],v[96:99]\nM v[96:99],v[196:199],a[56:59],v[96:99]\nbuffer_load_dwordx4 a[152:155],v240,s[52:55],s77 offen offset:2048 sc1\nM v[100:103],v[192:195],a[44:47],v[100:103]\nM v[100:103],v[196:199],a[60:63],v[100:103]\n" \
"s_waitcnt lgkmcnt(4)\nM v[40:43],v[200:203],a[32:35],v[40:43]\nM v[40:43],v[204:207],a[48:51],v[40:43]\nbuffer_load_dwordx4 a[140:143],v240,s[52:55],s77 offen offset:1024 sc1\nM v[44:47],v[200:203],a[36:39],v[44:47]\nM v[44:47],v[204:207],a[52:55],v[44:47]\nM v[104:107],v[200:203],a[40:43],v[104:107]\nM v[104:107],v[204:207],a[56:59],v[104:107]\n" \
"buffer_load_dwordx4 a[156:159],v240,s[52:55],s77 offen offset:3072 sc1\nM v[108:111],v[200:203],a[44:47],v[108:111]\ns_xor_b32 s77,s77,4096\nM v[108:111],v[204:207],a[60:63],v[108:111]\nM v[48:51],v[208:211],a[32:35],v[48:51]\nM v[48:51],v[212:215],a[48:51],v[48:51]\nbuffer_store_dwordx4 v[140:143],v240,s[52:55],s74 offen offset:3072 sc1\nM v[52:55],v[208:211],a[36:39],v[52:55]\n" \
"s_add_u32 s74,s74,4096\nM v[52:55],v[212:215],a[52:55],v[52:55]\nM v[112:115],v[208:211],a[40:43],v[112:115]\ns_waitcnt vmcnt(23) lgkmcnt(0)\ns_barrier\nM v[112:115],v[212:215],a[56:59],v[112:115]\nds_read_b64_tr_b16 v[192:193],v241 offset:32768\nds_read_b64_tr_b16 v[194:195],v241 offset:33024\n" \
"M v[116:119],v[208:211],a[44:47],v[116:119]\nds_read_b64_tr_b16 v[196:197],v241 offset:40960\nds_read_b64_tr_b16 v[198:199],v241 offset:41216\nM v[116:119],v[212:215],a[60:63],v[116:119]\nds_read_b64_tr_b16 v[200:201],v242 offset:32768\nds_read_b64_tr_b16 v[202:203],v242 offset:33024\nM v[56:59],v[216:219],a[32:35],v[56:59]\nds_read_b64_tr_b16 v[204:205],v242 offset:40960\n" \
"ds_read_b64_tr_b16 v[206:207],v242 offset:41216\nM v[56:59],v[220:223],a[48:51],v[56:59]\nds_read_b64_tr_b16 v[208:209],v241 offset:33280\nds_read_b64_tr_b16 v[210:211],v241 offset:33536\nM v[60:63],v[216:219],a[36:39],v[60:63]\nds_read_b64_tr_b16 v[212:213],v241 offset:41472\nds_read_b64_tr_b16 v[214:215],v241 offset:41728\nM v[60:63],v[220:223],a[52:55],v[60:63]\n" \
"buffer_load_dwordx4 v243,s[40:43],s69 offen offset:0 lds\nM v[120:123],v[216:219],a[40:43],v[120:123]\nM v[120:123],v[220:223],a[56:59],v[120:123]\nM v[124:127],v[216:219],a[44:47],v[124:127]\nds_read_b64_tr_b16 v[216:217],v242 offset:33280\nds_read_b64_tr_b16 v[218:219],v242 offset:33536\nM v[124:127],v[220:223],a[60:63],v[124:127]\nds_read_b64_tr_b16 v[220:221],v242 offset:41472\n" \
"s_waitcnt lgkmcnt(13)\nM v[0:3],v[192:195],a[64:67],v[0:3]\nds_read_b64_tr_b16 v[222:223],v242 offset:41728\ns_waitcnt lgkmcnt(12)\nM v[0:3],v[196:199],a[80:83],v[0:3]\nbuffer_load_dwordx4 v244,s[40:43],s69 offen offset:1024 lds\nM v[4:7],v[192:195],a[68:71],v[4:7]\nM v[4:7],v[196:199],a[84:87],v[4:7]\n" \
"M v[64:67],v[192:195],a[72:75],v[64:67]\nM v[64:67],v[196:199],a[88:91],v[64:67]\nbuffer_load_dwordx4 v245,s[40:43],s69 offen offset:2048 lds\nM v[68:71],v[192:195],a[76:79],v[68:71]\nds_read_b64_tr_b16 v[192:193],v241 offset:33792\nds_read_b64_tr_b16 v[194:195],v241 offset:34048\nM v[68:71],v[196:199],a[92:95],v[68:71]\nds_read_b64_tr_b16 v[196:197],v241 offset:41984\n" \
"s_waitcnt lgkmcnt(7)\nM v[8:11],v[200:203],a[64:67],v[8:11]\nds_read_b64_tr_b16 v[198:199],v241 offset:42240\nM v[8:11],v[204:207],a[80:83],v[8:11]\nbuffer_load_dwordx4 v246,s[40:43],s69 offen offset:3072 lds\nM v[12:15],v[200:203],a[68:71],v[12:15]\ns_add_u32 m0,s64,114688\ns_xor_b32 s69,s99,2621440\n" \
"M v[12:15],v[204:207],a[84:87],v[12:15]\nM v[72:75],v[200:203],a[72:75],v[72:75]\nM v[72:75],v[204:207],a[88:91],v[72:75]\nbuffer_store_dwordx4 v[144:147],v240,s[52:55],s74 offen offset:0 sc1\nM v[76:79],v[200:203],a[76:79],v[76:79]\nds_read_b64_tr_b16 v[200:201],v242 offset:33792\nds_read_b64_tr_b16 v[202:203],v242 offset:34048\nM v[76:79],v[204:207],a[92:95],v[76:79]\n" \
"ds_read_b64_tr_b16 v[204:205],v242 offset:41984\nds_read_b64_tr_b16 v[206:207],v242 offset:42240\nM v[16:19],v[208:211],a[64:67],v[16:19]\nM v[16:19],v[212:215],a[80:83],v[16:19]\nbuffer_load_dwordx4 a[160:163],v240,s[52:55],s76 offen offset:0 sc1\nM v[20:23],v[208:211],a[68:71],v[20:23]\nM v[20:23],v[212:215],a[84:87],v[20:23]\nM v[80:83],v[208:211],a[72:75],v[80:83]\n" \
"M v[80:83],v[212:215],a[88:91],v[80:83]\nbuffer_load_dwordx4 a[176:179],v240,s[52:55],s76 offen offset:2048 sc1\nM v[84:87],v[208:211],a[76:79],v[84:87]\nds_read_b64_tr_b16 v[208:209],v241 offset:34304\nds_read_b64_tr_b16 v[210:211],v241 offset:34560\nM v[84:87],v[212:215],a[92:95],v[84:87]\nds_read_b64_tr_b16 v[212:213],v241 offset:42496\ns_waitcnt lgkmcnt(7)\n" \
"M v[24:27],v[216:219],a[64:67],v[24:27]\nds_read_b64_tr_b16 v[214:215],v241 offset:42752\nM v[24:27],v[220:223],a[80:83],v[24:27]\nbuffer_load_dwordx4 a[164:167],v240,s[52:55],s76 offen offset:1024 sc1\nM v[28:31],v[216:219],a[68:71],v[28:31]\nM v[28:31],v[220:223],a[84:87],v[28:31]\nM v[88:91],v[216:219],a[72:75],v[88:91]\nM v[88:91],v[220:223],a[88:91],v[88:91]\n" \
"buffer_load_dwordx4 a[180:183],v240,s[52:55],s76 offen offset:3072 sc1\nM v[92:95],v[216:219],a[76:79],v[92:95]\nds_read_b64_tr_b16 v[216:217],v242 offset:34304\nds_read_b64_tr_b16 v[218:219],v242 offset:34560\nM v[92:95],v[220:223],a[92:95],v[92:95]\nds_read_b64_tr_b16 v[220:221],v242 offset:42496\nds_read_b64_tr_b16 v[222:223],v242 offset:42752\nM v[32:35],v[192:195],a[64:67],v[32:35]\n" \
"s_xor_b32 s76,s76,12288\nM v[32:35],v[196:199],a[80:83],v[32:35]\nbuffer_load_dwordx4 a[168:171],v240,s[52:55],s77 offen offset:0 sc1\nM v[36:39],v[192:195],a[68:71],v[36:39]\nM v[36:39],v[196:199],a[84:87],v[36:39]\nM v[96:99],v[192:195],a[72:75],v[96:99]\nM v[96:99],v[196:199],a[88:91],v[96:99]\nbuffer_load_dwordx4 a[184:187],v240,s[52:55],s77 offen offset:2048 sc1\n" \
"M v[100:103],v[192:195],a[76:79],v[100:103]\nM v[100:103],v[196:199],a[92:95],v[100:103]\ns_waitcnt lgkmcnt(4)\nM v[40:43],v[200:203],a[64:67],v[40:43]\nM v[40:43],v[204:207],a[80:83],v[40:43]\nbuffer_load_dwordx4 a[172:175],v240,s[52:55],s77 offen offset:1024 sc1\nM v[44:47],v[200:203],a[68:71],v[44:47]\nM v[44:47],v[204:207],a[84:87],v[44:47]\n" \
"M v[104:107],v[200:203],a[72:75],v[104:107]\nM v[104:107],v[204:207],a[88:91],v[104:107]\nbuffer_store_dwordx4 v[148:151],v240,s[52:55],s74 offen offset:1024 sc1\nM v[108:111],v[200:203],a[76:79],v[108:111]\nM v[108:111],v[204:207],a[92:95],v[108:111]\nM v[48:51],v[208:211],a[64:67],v[48:51]\nM v[48:51],v[212:215],a[80:83],v[48:51]\nbuffer_load_dwordx4 a[188:191],v240,s[52:55],s77 offen offset:3072 sc1\n" \
"M v[52:55],v[208:211],a[68:71],v[52:55]\ns_xor_b32 s77,s77,12288\nM v[52:55],v[212:215],a[84:87],v[52:55]\nM v[112:115],v[208:211],a[72:75],v[112:115]\ns_waitcnt vmcnt(23) lgkmcnt(0)\ns_barrier\nM v[112:115],v[212:215],a[88:91],v[112:115]\nds_read_b64_tr_b16 v[192:193],v241 offset:49152\n" \
"ds_read_b64_tr_b16 v[194:195],v241 offset:49408\nM v[116:119],v[208:211],a[76:79],v[116:119]\nds_read_b64_tr_b16 v[196:197],v241 offset:57344\nds_read_b64_tr_b16 v[198:199],v241 offset:57600\nM v[116:119],v[212:215],a[92:95],v[116:119]\nds_read_b64_tr_b16 v[200:201],v242 offset:49152\nds_read_b64_tr_b16 v[202:203],v242 offset:49408\nM v[56:59],v[216:219],a[64:67],v[56:59]\n" \
"ds_read_b64_tr_b16 v[204:205],v242 offset:57344\nds_read_b64_tr_b16 v[206:207],v242 offset:57600\nM v[56:59],v[220:223],a[80:83],v[56:59]\nds_read_b64_tr_b16 v[208:209],v241 offset:49664\nds_read_b64_tr_b16 v[210:211],v241 offset:49920\nM v[60:63],v[216:219],a[68:71],v[60:63]\nds_read_b64_tr_b16 v[212:213],v241 offset:57856\nds_read_b64_tr_b16 v[214:215],v241 offset:58112\n" \
"M v[60:63],v[220:223],a[84:87],v[60:63]\nbuffer_load_dwordx4 v243,s[40:43],s69 offen offset:0 lds\nM v[120:123],v[216:219],a[72:75],v[120:123]\nM v[120:123],v[220:223],a[88:91],v[120:123]\nM v[124:127],v[216:219],a[76:79],v[124:127]\nds_read_b64_tr_b16 v[216:217],v242 offset:49664\nds_read_b64_tr_b16 v[218:219],v242 offset:49920\nM v[124:127],v[220:223],a[92:95],v[124:127]\n" \
"ds_read_b64_tr_b16 v[220:221],v242 offset:57856\ns_waitcnt lgkmcnt(13)\nM v[0:3],v[192:195],a[96:99],v[0:3]\nds_read_b64_tr_b16 v[222:223],v242 offset:58112\ns_waitcnt lgkmcnt(12)\nM v[0:3],v[196:199],a[112:115],v[0:3]\nbuffer_load_dwordx4 v244,s[40:43],s69 offen offset:1024 lds\nM v[4:7],v[192:195],a[100:103],v[4:7]\n" \
"M v[4:7],v[196:199],a[116:119],v[4:7]\nM v[64:67],v[192:195],a[104:107],v[64:67]\nM v[64:67],v[196:199],a[120:123],v[64:67]\nbuffer_load_dwordx4 v245,s[40:43],s69 offen offset:2048 lds\nM v[68:71],v[192:195],a[108:111],v[68:71]\nds_read_b64_tr_b16 v[192:193],v241 offset:50176\nds_read_b64_tr_b16 v[194:195],v241 offset:50432\nM v[68:71],v[196:199],a[124:127],v[68:71]\n" \
"ds_read_b64_tr_b16 v[196:197],v241 offset:58368\ns_waitcnt lgkmcnt(7)\nM v[8:11],v[200:203],a[96:99],v[8:11]\nds_read_b64_tr_b16 v[198:199],v241 offset:58624\nM v[8:11],v[204:207],a[112:115],v[8:11]\nbuffer_store_dwordx4 v[152:155],v240,s[52:55],s74 offen offset:2048 sc1\nM v[12:15],v[200:203],a[100:103],v[12:15]\nM v[12:15],v[204:207],a[116:119],v[12:15]\n" \
"M v[72:75],v[200:203],a[104:107],v[72:75]\nM v[72:75],v[204:207],a[120:123],v[72:75]\nbuffer_load_dwordx4 v246,s[40:43],s69 offen offset:3072 lds\nM v[76:79],v[200:203],a[108:111],v[76:79]\nds_read_b64_tr_b16 v[200:201],v242 offset:50176\nds_read_b64_tr_b16 v[202:203],v242 offset:50432\nM v[76:79],v[204:207],a[124:127],v[76:79]\nds_read_b64_tr_b16 v[204:205],v242 offset:58368\n" \
"ds_read_b64_tr_b16 v[206:207],v242 offset:58624\nM v[16:19],v[208:211],a[96:99],v[16:19]\ns_add_u32 m0,s64,131072\ns_xor_b32 s69,s99,3145728\nM v[16:19],v[212:215],a[112:115],v[16:19]\nbuffer_load_dwordx4 a[192:195],v240,s[52:55],s76 offen offset:0 sc1\nM v[20:23],v[208:211],a[100:103],v[20:23]\nM v[20:23],v[212:215],a[116:119],v[20:23]\n" \
"M v[80:83],v[208:211],a[104:107],v[80:83]\nM v[80:83],v[212:215],a[120:123],v[80:83]\nbuffer_load_dwordx4 a[208:211],v240,s[52:55],s76 offen offset:2048 sc1\nM v[84:87],v[208:211],a[108:111],v[84:87]\nds_read_b64_tr_b16 v[208:209],v241 offset:50688\nds_read_b64_tr_b16 v[210:211],v241 offset:50944\nM v[84:87],v[212:215],a[124:127],v[84:87]\nds_read_b64_tr_b16 v[212:213],v241 offset:58880\n" \
"s_waitcnt lgkmcnt(7)\nM v[24:27],v[216:219],a[96:99],v[24:27]\nds_read_b64_tr_b16 v[214:215],v241 offset:59136\nM v[24:27],v[220:223],a[112:115],v[24:27]\nbuffer_load_dwordx4 a[196:199],v240,s[52:55],s76 offen offset:1024 sc1\nM v[28:31],v[216:219],a[100:103],v[28:31]\nM v[28:31],v[220:223],a[116:119],v[28:31]\nM v[88:91],v[216:219],a[104:107],v[88:91]\n" \
"M v[88:91],v[220:223],a[120:123],v[88:91]\nbuffer_load_dwordx4 a[212:215],v240,s[52:55],s76 offen offset:3072 sc1\nM v[92:95],v[216:219],a[108:111],v[92:95]\nds_read_b64_tr_b16 v[216:217],v242 offset:50688\nds_read_b64_tr_b16 v[218:219],v242 offset:50944\nM v[92:95],v[220:223],a[124:127],v[92:95]\nds_read_b64_tr_b16 v[220:221],v242 offset:58880\nds_read_b64_tr_b16 v[222:223],v242 offset:59136\n" \
"M v[32:35],v[192:195],a[96:99],v[32:35]\ns_xor_b32 s76,s76,4096\nM v[32:35],v[196:199],a[112:115],v[32:35]\nbuffer_load_dwordx4 a[200:203],v240,s[52:55],s77 offen offset:0 sc1\nM v[36:39],v[192:195],a[100:103],v[36:39]\nM v[36:39],v[196:199],a[116:119],v[36:39]\nM v[96:99],v[192:195],a[104:107],v[96:99]\nM v[96:99],v[196:199],a[120:123],v[96:99]\n" \
"buffer_load_dwordx4 a[216:219],v240,s[52:55],s77 offen offset:2048 sc1\nM v[100:103],v[192:195],a[108:111],v[100:103]\nM v[100:103],v[196:199],a[124:127],v[100:103]\ns_waitcnt lgkmcnt(4)\nM v[40:43],v[200:203],a[96:99],v[40:43]\nM v[40:43],v[204:207],a[112:115],v[40:43]\nbuffer_store_dwordx4 v[156:159],v240,s[52:55],s74 offen offset:3072 sc1\nM v[44:47],v[200:203],a[100:103],v[44:47]\n" \
"s_add_u32 s74,s74,4096\nM v[44:47],v[204:207],a[116:119],v[44:47]\nM v[104:107],v[200:203],a[104:107],v[104:107]\nM v[104:107],v[204:207],a[120:123],v[104:107]\nbuffer_load_dwordx4 a[204:207],v240,s[52:55],s77 offen offset:1024 sc1\nM v[108:111],v[200:203],a[108:111],v[108:111]\nM v[108:111],v[204:207],a[124:127],v[108:111]\nM v[48:51],v[208:211],a[96:99],v[48:51]\n" \
"M v[48:51],v[212:215],a[112:115],v[48:51]\nbuffer_load_dwordx4 a[220:223],v240,s[52:55],s77 offen offset:3072 sc1\nM v[52:55],v[208:211],a[100:103],v[52:55]\ns_xor_b32 s77,s77,4096\nM v[52:55],v[212:215],a[116:119],v[52:55]\nM v[112:115],v[208:211],a[104:107],v[112:115]\ns_waitcnt vmcnt(24) lgkmcnt(0)\ns_barrier\n" \
"M v[112:115],v[212:215],a[120:123],v[112:115]\nds_read_b64_tr_b16 v[192:193],v241 offset:0\nds_read_b64_tr_b16 v[194:195],v241 offset:256\nM v[116:119],v[208:211],a[108:111],v[116:119]\nds_read_b64_tr_b16 v[196:197],v241 offset:8192\nds_read_b64_tr_b16 v[198:199],v241 offset:8448\nM v[116:119],v[212:215],a[124:127],v[116:119]\nds_read_b64_tr_b16 v[200:201],v242 offset:0\n" \
"ds_read_b64_tr_b16 v[202:203],v242 offset:256\nM v[56:59],v[216:219],a[96:99],v[56:59]\nds_read_b64_tr_b16 v[204:205],v242 offset:8192\nds_read_b64_tr_b16 v[206:207],v242 offset:8448\nM v[56:59],v[220:223],a[112:115],v[56:59]\nds_read_b64_tr_b16 v[208:209],v241 offset:512\nds_read_b64_tr_b16 v[210:211],v241 offset:768\nM v[60:63],v[216:219],a[100:103],v[60:63]\n" \
"ds_read_b64_tr_b16 v[212:213],v241 offset:8704\nds_read_b64_tr_b16 v[214:215],v241 offset:8960\nM v[60:63],v[220:223],a[116:119],v[60:63]\nbuffer_load_dwordx4 v243,s[40:43],s69 offen offset:0 lds\nM v[120:123],v[216:219],a[104:107],v[120:123]\nM v[120:123],v[220:223],a[120:123],v[120:123]\nM v[124:127],v[216:219],a[108:111],v[124:127]\nds_read_b64_tr_b16 v[216:217],v242 offset:512\n" \
"ds_read_b64_tr_b16 v[218:219],v242 offset:768\nM v[124:127],v[220:223],a[124:127],v[124:127]\nds_read_b64_tr_b16 v[220:221],v242 offset:8704\ns_waitcnt lgkmcnt(13)\nM v[0:3],v[192:195],a[128:131],v[0:3]\nds_read_b64_tr_b16 v[222:223],v242 offset:8960\ns_waitcnt lgkmcnt(12)\nM v[0:3],v[196:199],a[144:147],v[0:3]\n" \
"buffer_load_dwordx4 v244,s[40:43],s69 offen offset:1024 lds\nM v[4:7],v[192:195],a[132:135],v[4:7]\nM v[4:7],v[196:199],a[148:151],v[4:7]\nM v[64:67],v[192:195],a[136:139],v[64:67]\nM v[64:67],v[196:199],a[152:155],v[64:67]\nbuffer_store_dwordx4 v[160:163],v240,s[52:55],s74 offen offset:0 sc1\nM v[68:71],v[192:195],a[140:143],v[68:71]\nds_read_b64_tr_b16 v[192:193],v241 offset:1024\n" \
"ds_read_b64_tr_b16 v[194:195],v241 offset:1280\nM v[68:71],v[196:199],a[156:159],v[68:71]\nds_read_b64_tr_b16 v[196:197],v241 offset:9216\ns_waitcnt lgkmcnt(7)\nM v[8:11],v[200:203],a[128:131],v[8:11]\nds_read_b64_tr_b16 v[198:199],v241 offset:9472\nM v[8:11],v[204:207],a[144:147],v[8:11]\nbuffer_load_dwordx4 v245,s[40:43],s69 offen offset:2048 lds\n" \
"M v[12:15],v[200:203],a[132:135],v[12:15]\nM v[12:15],v[204:207],a[148:151],v[12:15]\nM v[72:75],v[200:203],a[136:139],v[72:75]\nM v[72:75],v[204:207],a[152:155],v[72:75]\nbuffer_load_dwordx4 v246,s[40:43],s69 offen offset:3072 lds\nM v[76:79],v[200:203],a[140:143],v[76:79]\nds_read_b64_tr_b16 v[200:201],v242 offset:1024\nds_read_b64_tr_b16 v[202:203],v242 offset:1280\n" \
"M v[76:79],v[204:207],a[156:159],v[76:79]\nds_read_b64_tr_b16 v[204:205],v242 offset:9216\nds_read_b64_tr_b16 v[206:207],v242 offset:9472\nM v[16:19],v[208:211],a[128:131],v[16:19]\ns_add_u32 m0,s64,147456\ns_xor_b32 s69,s99,3670016\nM v[16:19],v[212:215],a[144:147],v[16:19]\nbuffer_load_dwordx4 a[224:227],v240,s[52:55],s76 offen offset:0 sc1\n" \
"M v[20:23],v[208:211],a[132:135],v[20:23]\ns_mov_b32 s99,0\nM v[20:23],v[212:215],a[148:151],v[20:23]\nM v[80:83],v[208:211],a[136:139],v[80:83]\nds_read_b128 a[0:3],v240 offset:0\nM v[80:83],v[212:215],a[152:155],v[80:83]\nbuffer_load_dword v248,v228,s[52:55],s80 offen sc1\nM v[84:87],v[208:211],a[140:143],v[84:87]\n" \
"ds_read_b64_tr_b16 v[208:209],v241 offset:1536\nds_read_b64_tr_b16 v[210:211],v241 offset:1792\nM v[84:87],v[212:215],a[156:159],v[84:87]\nds_read_b64_tr_b16 v[212:213],v241 offset:9728\ns_waitcnt lgkmcnt(8)\nM v[24:27],v[216:219],a[128:131],v[24:27]\nbuffer_load_dwordx4 a[240:243],v240,s[52:55],s76 offen offset:2048 sc1\nM v[24:27],v[220:223],a[144:147],v[24:27]\n" \
"ds_read_b64_tr_b16 v[214:215],v241 offset:9984\nM v[28:31],v[216:219],a[132:135],v[28:31]\nds_read_b128 a[4:7],v240 offset:1024\nM v[28:31],v[220:223],a[148:151],v[28:31]\nM v[88:91],v[216:219],a[136:139],v[88:91]\nbuffer_load_dwordx4 a[228:231],v240,s[52:55],s76 offen offset:1024 sc1\nM v[88:91],v[220:223],a[152:155],v[88:91]\nM v[92:95],v[216:219],a[140:143],v[92:95]\n" \
"ds_read_b64_tr_b16 v[216:217],v242 offset:1536\nds_read_b64_tr_b16 v[218:219],v242 offset:1792\nM v[92:95],v[220:223],a[156:159],v[92:95]\nds_read_b64_tr_b16 v[220:221],v242 offset:9728\nds_read_b64_tr_b16 v[222:223],v242 offset:9984\nM v[32:35],v[192:195],a[128:131],v[32:35]\nbuffer_load_dwordx4 a[244:247],v240,s[52:55],s76 offen offset:3072 sc1\nM v[32:35],v[196:199],a[144:147],v[32:35]\n" \
"ds_read_b128 a[8:11],v240 offset:2048\nM v[36:39],v[192:195],a[132:135],v[36:39]\nM v[36:39],v[196:199],a[148:151],v[36:39]\nM v[96:99],v[192:195],a[136:139],v[96:99]\nbuffer_store_dwordx4 v[164:167],v240,s[52:55],s74 offen offset:1024 sc1\nM v[96:99],v[196:199],a[152:155],v[96:99]\nM v[100:103],v[192:195],a[140:143],v[100:103]\nds_read_b128 a[12:15],v240 offset:3072\n" \
"M v[100:103],v[196:199],a[156:159],v[100:103]\ns_waitcnt lgkmcnt(7)\nM v[40:43],v[200:203],a[128:131],v[40:43]\nbuffer_load_dwordx4 a[232:235],v240,s[52:55],s77 offen offset:0 sc1\nM v[40:43],v[204:207],a[144:147],v[40:43]\nM v[44:47],v[200:203],a[132:135],v[44:47]\nM v[44:47],v[204:207],a[148:151],v[44:47]\nM v[104:107],v[200:203],a[136:139],v[104:107]\n" \
"buffer_load_dwordx4 a[248:251],v240,s[52:55],s77 offen offset:2048 sc1\nM v[104:107],v[204:207],a[152:155],v[104:107]\nds_read_b128 a[16:19],v240 offset:4096\nM v[108:111],v[200:203],a[140:143],v[108:111]\nM v[108:111],v[204:207],a[156:159],v[108:111]\nM v[48:51],v[208:211],a[128:131],v[48:51]\nbuffer_load_dwordx4 a[236:239],v240,s[52:55],s77 offen offset:1024 sc1\nM v[48:51],v[212:215],a[144:147],v[48:51]\n" \
"M v[52:55],v[208:211],a[132:135],v[52:55]\nds_read_b128 a[20:23],v240 offset:5120\nM v[52:55],v[212:215],a[148:151],v[52:55]\nM v[112:115],v[208:211],a[136:139],v[112:115]\ns_waitcnt vmcnt(17) lgkmcnt(3)\ns_barrier\nM v[112:115],v[212:215],a[152:155],v[112:115]\nds_read_b64_tr_b16 v[192:193],v241 offset:16384\n" \
"ds_read_b64_tr_b16 v[194:195],v241 offset:16640\nM v[116:119],v[208:211],a[140:143],v[116:119]\nds_read_b64_tr_b16 v[196:197],v241 offset:24576\nds_read_b64_tr_b16 v[198:199],v241 offset:24832\nM v[116:119],v[212:215],a[156:159],v[116:119]\nds_read_b64_tr_b16 v[200:201],v242 offset:16384\nds_read_b64_tr_b16 v[202:203],v242 offset:16640\nM v[56:59],v[216:219],a[128:131],v[56:59]\n" \
"ds_read_b64_tr_b16 v[204:205],v242 offset:24576\nds_read_b64_tr_b16 v[206:207],v242 offset:24832\nM v[56:59],v[220:223],a[144:147],v[56:59]\nds_read_b64_tr_b16 v[208:209],v241 offset:16896\nds_read_b64_tr_b16 v[210:211],v241 offset:17152\nM v[60:63],v[216:219],a[132:135],v[60:63]\nds_read_b64_tr_b16 v[212:213],v241 offset:25088\nds_read_b64_tr_b16 v[214:215],v241 offset:25344\n" \
"M v[60:63],v[220:223],a[148:151],v[60:63]\nbuffer_load_dwordx4 a[252:255],v240,s[52:55],s77 offen offset:3072 sc1\nM v[120:123],v[216:219],a[136:139],v[120:123]\nds_read_b128 a[24:27],v240 offset:6144\nM v[120:123],v[220:223],a[152:155],v[120:123]\nM v[124:127],v[216:219],a[140:143],v[124:127]\nds_read_b64_tr_b16 v[216:217],v242 offset:16896\nds_read_b64_tr_b16 v[218:219],v242 offset:17152\n" \
"M v[124:127],v[220:223],a[156:159],v[124:127]\nds_read_b64_tr_b16 v[220:221],v242 offset:25088\ns_waitcnt lgkmcnt(14)\nM v[0:3],v[192:195],a[160:163],v[0:3]\nds_read_b64_tr_b16 v[222:223],v242 offset:25344\ns_waitcnt lgkmcnt(13)\nM v[0:3],v[196:199],a[176:179],v[0:3]\nbuffer_store_dwordx4 v[168:171],v240,s[52:55],s74 offen offset:2048 sc1\n" \
"M v[4:7],v[192:195],a[164:167],v[4:7]\nds_read_b128 a[28:31],v240 offset:7168\nM v[4:7],v[196:199],a[180:183],v[4:7]\nM v[64:67],v[192:195],a[168:171],v[64:67]\nds_read_b128 a[32:35],v240 offset:8192\nM v[64:67],v[196:199],a[184:187],v[64:67]\nbuffer_load_dwordx4 v243,s[40:43],s69 offen offset:0 lds\nM v[68:71],v[192:195],a[172:175],v[68:71]\n" \
"ds_read_b64_tr_b16 v[192:193],v241 offset:17408\nds_read_b64_tr_b16 v[194:195],v241 offset:17664\nM v[68:71],v[196:199],a[188:191],v[68:71]\nds_read_b64_tr_b16 v[196:197],v241 offset:25600\ns_waitcnt lgkmcnt(10)\nM v[8:11],v[200:203],a[160:163],v[8:11]\nds_read_b64_tr_b16 v[198:199],v241 offset:25856\nM v[8:11],v[204:207],a[176:179],v[8:11]\n" \
"buffer_load_dwordx4 v244,s[40:43],s69 offen offset:1024 lds\nM v[12:15],v[200:203],a[164:167],v[12:15]\nds_read_b128 a[36:39],v240 offset:9216\nM v[12:15],v[204:207],a[180:183],v[12:15]\nM v[72:75],v[200:203],a[168:171],v[72:75]\nM v[72:75],v[204:207],a[184:187],v[72:75]\nbuffer_load_dwordx4 v245,s[40:43],s69 offen offset:2048 lds\nM v[76:79],v[200:203],a[172:175],v[76:79]\n" \
"ds_read_b64_tr_b16 v[200:201],v242 offset:17408\nds_read_b64_tr_b16 v[202:203],v242 offset:17664\nM v[76:79],v[204:207],a[188:191],v[76:79]\nds_read_b64_tr_b16 v[204:205],v242 offset:25600\nds_read_b64_tr_b16 v[206:207],v242 offset:25856\nM v[16:19],v[208:211],a[160:163],v[16:19]\nds_read_b128 a[40:43],v240 offset:10240\nM v[16:19],v[212:215],a[176:179],v[16:19]\n" \
"buffer_load_dwordx4 v246,s[40:43],s69 offen offset:3072 lds\nM v[20:23],v[208:211],a[164:167],v[20:23]\ns_sub_u32 s40,s18,0x1000\ns_subb_u32 s41,s19,0\nM v[20:23],v[212:215],a[180:183],v[20:23]\ns_waitcnt vmcnt(13)\nv_cmp_eq_u32 vcc,0,v248\nM v[80:83],v[208:211],a[168:171],v[80:83]\n" \
"s_cbranch_vccnz .Lps1\n.Lpb1:\ns_add_u32 s80,s80,4096\nM v[80:83],v[212:215],a[184:187],v[80:83]\ns_cmp_ge_u32 s62,6\ns_cselect_b64 vcc,-1,0\nM v[84:87],v[208:211],a[172:175],v[84:87]\nds_read_b64_tr_b16 v[208:209],v241 offset:17920\n" \
"ds_read_b64_tr_b16 v[210:211],v241 offset:18176\nM v[84:87],v[212:215],a[188:191],v[84:87]\nds_read_b64_tr_b16 v[212:213],v241 offset:26112\ns_waitcnt lgkmcnt(8)\nM v[24:27],v[216:219],a[160:163],v[24:27]\nds_read_b64_tr_b16 v[214:215],v241 offset:26368\nv_cndmask_b32 v254,v231,v229,vcc\nM v[24:27],v[220:223],a[176:179],v[24:27]\n" \
"s_cmp_eq_u32 s62,3\ns_cselect_b64 vcc,-1,0\nM v[28:31],v[216:219],a[164:167],v[28:31]\nv_cndmask_b32 v254,v254,v230,vcc\nv_bfe_u32 v230,v249,2,3\nM v[28:31],v[220:223],a[180:183],v[28:31]\nv_and_b32 v231,0x70,v243\nv_lshl_or_b32 v230,v230,10,v231\n" \
"M v[88:91],v[216:219],a[168:171],v[88:91]\nv_and_b32 v231,16,v249\nv_lshlrev_b32 v231,1,v231\nM v[88:91],v[220:223],a[184:187],v[88:91]\nv_xor_b32 v230,v230,v231\nv_add_u32 v243,4096,v230\nM v[92:95],v[216:219],a[172:175],v[92:95]\nbuffer_store_dwordx4 v[172:175],v240,s[52:55],s74 offen offset:3072 sc1\n" \
"M v[92:95],v[220:223],a[188:191],v[92:95]\nds_read_b64_tr_b16 v[216:217],v242 offset:17920\nds_read_b64_tr_b16 v[218:219],v242 offset:18176\nM v[32:35],v[192:195],a[160:163],v[32:35]\nds_read_b64_tr_b16 v[220:221],v242 offset:26112\nds_read_b64_tr_b16 v[222:223],v242 offset:26368\nM v[32:35],v[196:199],a[176:179],v[32:35]\ns_add_u32 s74,s74,4096\n" \
"v_add_u32 v244,3200,v230\nM v[36:39],v[192:195],a[164:167],v[36:39]\nv_add_u32 v245,10240,v230\nv_add_u32 v246,9344,v230\nM v[36:39],v[196:199],a[180:183],v[36:39]\nv_and_b32 v229,3,v249\nv_cmp_eq_u32 vcc,0,v229\nM v[96:99],v[192:195],a[168:171],v[96:99]\n" \
"v_mov_b32 v230,0x3f803f80\nv_cndmask_b32 v229,0,v230,vcc\nM v[96:99],v[196:199],a[184:187],v[96:99]\nv_and_b32 v230,0xff,v241\nv_and_b32 v231,16,v249\nM v[100:103],v[192:195],a[172:175],v[100:103]\nv_lshl_or_b32 v230,v231,4,v230\nv_and_b32 v231,32,v249\n" \
"M v[100:103],v[196:199],a[188:191],v[100:103]\nds_read_b128 a[44:47],v240 offset:11264\ns_waitcnt lgkmcnt(5)\nM v[40:43],v[200:203],a[160:163],v[40:43]\nds_read_b128 a[48:51],v240 offset:12288\nds_read_b128 a[52:55],v240 offset:13312\nM v[40:43],v[204:207],a[176:179],v[40:43]\nds_read_b128 a[56:59],v240 offset:14336\n" \
"M v[44:47],v[200:203],a[164:167],v[44:47]\nM v[44:47],v[204:207],a[180:183],v[44:47]\nM v[104:107],v[200:203],a[168:171],v[104:107]\nM v[104:107],v[204:207],a[184:187],v[104:107]\nM v[108:111],v[200:203],a[172:175],v[108:111]\nds_read_b128 a[60:63],v240 offset:15360\nM v[108:111],v[204:207],a[188:191],v[108:111]\nM v[48:51],v[208:211],a[160:163],v[48:51]\n" \
"M v[48:51],v[212:215],a[176:179],v[48:51]\nM v[52:55],v[208:211],a[164:167],v[52:55]\nM v[52:55],v[212:215],a[180:183],v[52:55]\nM v[112:115],v[208:211],a[168:171],v[112:115]\ns_waitcnt lgkmcnt(5)\ns_barrier\nM v[112:115],v[212:215],a[184:187],v[112:115]\nds_read_b64_tr_b16 v[192:193],v241 offset:32768\n" \
"ds_read_b64_tr_b16 v[194:195],v241 offset:33024\nM v[116:119],v[208:211],a[172:175],v[116:119]\nds_read_b64_tr_b16 v[196:197],v241 offset:40960\nds_read_b64_tr_b16 v[198:199],v241 offset:41216\nM v[116:119],v[212:215],a[188:191],v[116:119]\nds_read_b64_tr_b16 v[200:201],v242 offset:32768\nds_read_b64_tr_b16 v[202:203],v242 offset:33024\nM v[56:59],v[216:219],a[160:163],v[56:59]\n" \
"ds_read_b64_tr_b16 v[204:205],v242 offset:40960\nds_read_b64_tr_b16 v[206:207],v242 offset:41216\nM v[56:59],v[220:223],a[176:179],v[56:59]\nds_read_b64_tr_b16 v[208:209],v241 offset:33280\nds_read_b64_tr_b16 v[210:211],v241 offset:33536\nM v[60:63],v[216:219],a[164:167],v[60:63]\nds_read_b64_tr_b16 v[212:213],v241 offset:41472\nds_read_b64_tr_b16 v[214:215],v241 offset:41728\n" \
"M v[60:63],v[220:223],a[180:183],v[60:63]\nds_write_b64 v250,v[128:129] offset:0\nM v[120:123],v[216:219],a[168:171],v[120:123]\nbuffer_store_dwordx4 v[176:179],v240,s[52:55],s74 offen offset:0 sc1\nM v[120:123],v[220:223],a[184:187],v[120:123]\nds_write_b64 v250,v[130:131] offset:8\nM v[124:127],v[216:219],a[172:175],v[124:127]\nds_read_b64_tr_b16 v[216:217],v242 offset:33280\n" \
"ds_read_b64_tr_b16 v[218:219],v242 offset:33536\nM v[124:127],v[220:223],a[188:191],v[124:127]\nds_read_b64_tr_b16 v[220:221],v242 offset:41472\ns_waitcnt lgkmcnt(15)\nM v[0:3],v[192:195],a[192:195],v[0:3]\nds_read_b64_tr_b16 v[222:223],v242 offset:41728\ns_waitcnt lgkmcnt(14)\nM v[0:3],v[196:199],a[208:211],v[0:3]\n" \
"ds_write_b64 v250,v[132:133] offset:1024\nM v[4:7],v[192:195],a[196:199],v[4:7]\nds_write_b64 v250,v[134:135] offset:1032\nM v[4:7],v[196:199],a[212:215],v[4:7]\nds_write_b64 v250,v[136:137] offset:2048\nM v[64:67],v[192:195],a[200:203],v[64:67]\nM v[64:67],v[196:199],a[216:219],v[64:67]\nds_write_b64 v250,v[138:139] offset:2056\n" \
"M v[68:71],v[192:195],a[204:207],v[68:71]\nds_read_b64_tr_b16 v[192:193],v241 offset:33792\nds_read_b64_tr_b16 v[194:195],v241 offset:34048\nM v[68:71],v[196:199],a[220:223],v[68:71]\nds_read_b64_tr_b16 v[196:197],v241 offset:41984\ns_waitcnt lgkmcnt(12)\nM v[8:11],v[200:203],a[192:195],v[8:11]\nds_write_b64 v250,v[140:141] offset:3072\n" \
"ds_read_b64_tr_b16 v[198:199],v241 offset:42240\nM v[8:11],v[204:207],a[208:211],v[8:11]\nM v[12:15],v[200:203],a[196:199],v[12:15]\nM v[12:15],v[204:207],a[212:215],v[12:15]\nds_write_b64 v250,v[142:143] offset:3080\nM v[72:75],v[200:203],a[200:203],v[72:75]\nM v[72:75],v[204:207],a[216:219],v[72:75]\nM v[76:79],v[200:203],a[204:207],v[76:79]\n" \
"ds_read_b64_tr_b16 v[200:201],v242 offset:33792\nds_read_b64_tr_b16 v[202:203],v242 offset:34048\nM v[76:79],v[204:207],a[220:223],v[76:79]\nds_read_b64_tr_b16 v[204:205],v242 offset:41984\nds_read_b64_tr_b16 v[206:207],v242 offset:42240\nM v[16:19],v[208:211],a[192:195],v[16:19]\nds_write_b64 v250,v[144:145] offset:4096\nM v[16:19],v[212:215],a[208:211],v[16:19]\n" \
"ds_write_b64 v250,v[146:147] offset:4104\nM v[20:23],v[208:211],a[196:199],v[20:23]\nM v[20:23],v[212:215],a[212:215],v[20:23]\nM v[80:83],v[208:211],a[200:203],v[80:83]\nds_write_b64 v250,v[148:149] offset:5120\nM v[80:83],v[212:215],a[216:219],v[80:83]\nM v[84:87],v[208:211],a[204:207],v[84:87]\nds_read_b64_tr_b16 v[208:209],v241 offset:34304\n" \
"ds_read_b64_tr_b16 v[210:211],v241 offset:34560\nM v[84:87],v[212:215],a[220:223],v[84:87]\nds_read_b64_tr_b16 v[212:213],v241 offset:42496\ns_waitcnt lgkmcnt(11)\nM v[24:27],v[216:219],a[192:195],v[24:27]\nds_write_b64 v250,v[150:151] offset:5128\nds_read_b64_tr_b16 v[214:215],v241 offset:42752\nM v[24:27],v[220:223],a[208:211],v[24:27]\n" \
"M v[28:31],v[216:219],a[196:199],v[28:31]\nbuffer_store_dwordx4 v[180:183],v240,s[52:55],s74 offen offset:1024 sc1\nM v[28:31],v[220:223],a[212:215],v[28:31]\nds_write_b64 v250,v[152:153] offset:6144\nM v[88:91],v[216:219],a[200:203],v[88:91]\nM v[88:91],v[220:223],a[216:219],v[88:91]\nds_write_b64 v250,v[154:155] offset:6152\nM v[92:95],v[216:219],a[204:207],v[92:95]\n" \
"ds_read_b64_tr_b16 v[216:217],v242 offset:34304\nds_read_b64_tr_b16 v[218:219],v242 offset:34560\nM v[92:95],v[220:223],a[220:223],v[92:95]\nds_read_b64_tr_b16 v[220:221],v242 offset:42496\nds_read_b64_tr_b16 v[222:223],v242 offset:42752\nM v[32:35],v[192:195],a[192:195],v[32:35]\nds_write_b64 v250,v[156:157] offset:7168\nM v[32:35],v[196:199],a[208:211],v[32:35]\n" \
"M v[36:39],v[192:195],a[196:199],v[36:39]\nM v[36:39],v[196:199],a[212:215],v[36:39]\nds_write_b64 v250,v[158:159] offset:7176\nM v[96:99],v[192:195],a[200:203],v[96:99]\nM v[96:99],v[196:199],a[216:219],v[96:99]\nM v[100:103],v[192:195],a[204:207],v[100:103]\nds_write_b64 v250,v[160:161] offset:8192\nM v[100:103],v[196:199],a[220:223],v[100:103]\n" \
"s_waitcnt lgkmcnt(9)\nM v[40:43],v[200:203],a[192:195],v[40:43]\nM v[40:43],v[204:207],a[208:211],v[40:43]\nds_write_b64 v250,v[162:163] offset:8200\nM v[44:47],v[200:203],a[196:199],v[44:47]\nM v[44:47],v[204:207],a[212:215],v[44:47]\nM v[104:107],v[200:203],a[200:203],v[104:107]\nds_write_b64 v250,v[164:165] offset:9216\n" \
"M v[104:107],v[204:207],a[216:219],v[104:107]\nM v[108:111],v[200:203],a[204:207],v[108:111]\nM v[108:111],v[204:207],a[220:223],v[108:111]\nds_write_b64 v250,v[166:167] offset:9224\nM v[48:51],v[208:211],a[192:195],v[48:51]\nM v[48:51],v[212:215],a[208:211],v[48:51]\nM v[52:55],v[208:211],a[196:199],v[52:55]\nds_write_b64 v250,v[168:169] offset:10240\n" \
"M v[52:55],v[212:215],a[212:215],v[52:55]\nM v[112:115],v[208:211],a[200:203],v[112:115]\ns_waitcnt vmcnt(3) lgkmcnt(5)\ns_barrier\nM v[112:115],v[212:215],a[216:219],v[112:115]\nds_read_b64_tr_b16 v[192:193],v241 offset:49152\nds_read_b64_tr_b16 v[194:195],v241 offset:49408\nM v[116:119],v[208:211],a[204:207],v[116:119]\n" \
"ds_read_b64_tr_b16 v[196:197],v241 offset:57344\nds_read_b64_tr_b16 v[198:199],v241 offset:57600\nM v[116:119],v[212:215],a[220:223],v[116:119]\nds_read_b64_tr_b16 v[200:201],v242 offset:49152\nds_read_b64_tr_b16 v[202:203],v242 offset:49408\nM v[56:59],v[216:219],a[192:195],v[56:59]\nbuffer_store_dwordx4 v[184:187],v240,s[52:55],s74 offen offset:2048 sc1\nM v[56:59],v[220:223],a[208:211],v[56:59]\n" \
"ds_read_b64_tr_b16 v[204:205],v242 offset:57344\nds_read_b64_tr_b16 v[206:207],v242 offset:57600\nM v[60:63],v[216:219],a[196:199],v[60:63]\nds_read_b64_tr_b16 v[208:209],v241 offset:49664\nds_read_b64_tr_b16 v[210:211],v241 offset:49920\nM v[60:63],v[220:223],a[212:215],v[60:63]\nds_read_b64_tr_b16 v[212:213],v241 offset:57856\nds_read_b64_tr_b16 v[214:215],v241 offset:58112\n" \
"M v[120:123],v[216:219],a[200:203],v[120:123]\nds_write_b64 v250,v[170:171] offset:10248\nv_accvgpr_write_b32 a208,v229\nM v[120:123],v[220:223],a[216:219],v[120:123]\nds_write_b64 v250,v[172:173] offset:11264\nv_accvgpr_write_b32 a209,v229\nM v[124:127],v[216:219],a[204:207],v[124:127]\nds_read_b64_tr_b16 v[216:217],v242 offset:49664\n" \
"ds_read_b64_tr_b16 v[218:219],v242 offset:49920\nM v[124:127],v[220:223],a[220:223],v[124:127]\nds_read_b64_tr_b16 v[220:221],v242 offset:57856\ns_waitcnt lgkmcnt(15)\nM v[0:3],v[192:195],a[224:227],v[0:3]\nds_read_b64_tr_b16 v[222:223],v242 offset:58112\ns_waitcnt lgkmcnt(14)\nM v[0:3],v[196:199],a[240:243],v[0:3]\n" \
"ds_write_b64 v250,v[174:175] offset:11272\nv_accvgpr_write_b32 a210,v229\nM v[4:7],v[192:195],a[228:231],v[4:7]\nds_write_b64 v250,v[176:177] offset:12288\nv_accvgpr_write_b32 a211,v229\nM v[4:7],v[196:199],a[244:247],v[4:7]\nds_write_b64 v250,v[178:179] offset:12296\nM v[64:67],v[192:195],a[232:235],v[64:67]\n" \
"v_cvt_pk_bf16_f32 v0,v0,v1\nv_cvt_pk_bf16_f32 v1,v2,v3\nM v[64:67],v[196:199],a[248:251],v[64:67]\nds_write_b64 v250,v[180:181] offset:13312\nv_accvgpr_write_b32 a128,v0\nM v[68:71],v[192:195],a[236:239],v[68:71]\nds_read_b64_tr_b16 v[192:193],v241 offset:50176\nds_read_b64_tr_b16 v[194:195],v241 offset:50432\n" \
"M v[68:71],v[196:199],a[252:255],v[68:71]\nds_read_b64_tr_b16 v[196:197],v241 offset:58368\ns_waitcnt lgkmcnt(12)\nM v[8:11],v[200:203],a[224:227],v[8:11]\nds_read_b64_tr_b16 v[198:199],v241 offset:58624\nv_cvt_pk_bf16_f32 v4,v4,v5\nM v[8:11],v[204:207],a[240:243],v[8:11]\nv_cvt_pk_bf16_f32 v64,v64,v65\n" \
"v_cvt_pk_bf16_f32 v5,v6,v7\nM v[12:15],v[200:203],a[228:231],v[12:15]\nv_cvt_pk_bf16_f32 v68,v68,v69\nv_cvt_pk_bf16_f32 v65,v66,v67\nM v[12:15],v[204:207],a[244:247],v[12:15]\nds_write_b64 v250,v[182:183] offset:13320\nv_cvt_pk_bf16_f32 v69,v70,v71\nM v[72:75],v[200:203],a[232:235],v[72:75]\n" \
"ds_write_b64 v250,v[184:185] offset:14336\nv_cvt_pk_bf16_f32 v2,v8,v9\nM v[72:75],v[204:207],a[248:251],v[72:75]\nds_write_b64 v250,v[186:187] offset:14344\nv_cvt_pk_bf16_f32 v3,v10,v11\nM v[76:79],v[200:203],a[236:239],v[76:79]\nds_read_b64_tr_b16 v[200:201],v242 offset:50176\nds_read_b64_tr_b16 v[202:203],v242 offset:50432\n" \
"M v[76:79],v[204:207],a[252:255],v[76:79]\nds_read_b64_tr_b16 v[204:205],v242 offset:58368\nds_read_b64_tr_b16 v[206:207],v242 offset:58624\nM v[16:19],v[208:211],a[224:227],v[16:19]\nds_write_b64 v250,v[188:189] offset:15360\nv_cvt_pk_bf16_f32 v6,v12,v13\nM v[16:19],v[212:215],a[240:243],v[16:19]\nv_cvt_pk_bf16_f32 v7,v14,v15\n" \
"v_cvt_pk_bf16_f32 v66,v72,v73\nM v[20:23],v[208:211],a[228:231],v[20:23]\nv_cvt_pk_bf16_f32 v67,v74,v75\nv_cvt_pk_bf16_f32 v70,v76,v77\nM v[20:23],v[212:215],a[244:247],v[20:23]\nds_write_b64 v250,v[190:191] offset:15368\nv_cvt_pk_bf16_f32 v71,v78,v79\nM v[80:83],v[208:211],a[232:235],v[80:83]\n" \
"v_cvt_pk_bf16_f32 v16,v16,v17\nv_cvt_pk_bf16_f32 v17,v18,v19\nM v[80:83],v[212:215],a[248:251],v[80:83]\nv_add_u32 v250,114688,v240\nv_accvgpr_write_b32 a129,v1\nM v[84:87],v[208:211],a[236:239],v[84:87]\nbuffer_store_dwordx4 v[188:191],v240,s[52:55],s74 offen offset:3072 sc1\nM v[84:87],v[212:215],a[252:255],v[84:87]\n" \
"ds_read_b64_tr_b16 v[208:209],v241 offset:50688\ns_waitcnt lgkmcnt(7)\nM v[24:27],v[216:219],a[224:227],v[24:27]\nds_read_b64_tr_b16 v[210:211],v241 offset:50944\nds_read_b64_tr_b16 v[212:213],v241 offset:58880\nM v[24:27],v[220:223],a[240:243],v[24:27]\nds_read_b64_tr_b16 v[214:215],v241 offset:59136\nv_cvt_pk_bf16_f32 v20,v20,v21\n" \
"M v[28:31],v[216:219],a[228:231],v[28:31]\nv_cvt_pk_bf16_f32 v80,v80,v81\nv_cvt_pk_bf16_f32 v84,v84,v85\nM v[28:31],v[220:223],a[244:247],v[28:31]\nv_cvt_pk_bf16_f32 v21,v22,v23\nv_cvt_pk_bf16_f32 v81,v82,v83\nM v[88:91],v[216:219],a[232:235],v[88:91]\nv_cvt_pk_bf16_f32 v85,v86,v87\n" \
"v_cvt_pk_bf16_f32 v18,v24,v25\nM v[88:91],v[220:223],a[248:251],v[88:91]\nv_cvt_pk_bf16_f32 v19,v26,v27\nv_add_u32 v241,65536,v240\nM v[92:95],v[216:219],a[236:239],v[92:95]\nds_read_b64_tr_b16 v[216:217],v242 offset:50688\nds_read_b64_tr_b16 v[218:219],v242 offset:50944\nM v[92:95],v[220:223],a[252:255],v[92:95]\n" \
"ds_read_b64_tr_b16 v[220:221],v242 offset:58880\nds_read_b64_tr_b16 v[222:223],v242 offset:59136\nM v[32:35],v[192:195],a[224:227],v[32:35]\nv_cvt_pk_bf16_f32 v22,v28,v29\nv_cvt_pk_bf16_f32 v23,v30,v31\nM v[32:35],v[196:199],a[240:243],v[32:35]\nv_cvt_pk_bf16_f32 v82,v88,v89\nv_cvt_pk_bf16_f32 v83,v90,v91\n" \
"M v[36:39],v[192:195],a[228:231],v[36:39]\nv_cvt_pk_bf16_f32 v86,v92,v93\nv_cvt_pk_bf16_f32 v87,v94,v95\nM v[36:39],v[196:199],a[244:247],v[36:39]\nv_lshl_or_b32 v242,v231,6,v230\nv_accvgpr_write_b32 a130,v2\nM v[96:99],v[192:195],a[232:235],v[96:99]\nv_cvt_pk_bf16_f32 v32,v32,v33\n" \
"v_cvt_pk_bf16_f32 v33,v34,v35\nM v[96:99],v[196:199],a[248:251],v[96:99]\nv_accvgpr_write_b32 a131,v3\nv_accvgpr_write_b32 a144,v4\nM v[100:103],v[192:195],a[236:239],v[100:103]\nv_cvt_pk_bf16_f32 v36,v36,v37\nv_cvt_pk_bf16_f32 v37,v38,v39\nM v[100:103],v[196:199],a[252:255],v[100:103]\n" \
"v_accvgpr_write_b32 a145,v5\ns_waitcnt lgkmcnt(4)\nM v[40:43],v[200:203],a[224:227],v[40:43]\nv_cvt_pk_bf16_f32 v96,v96,v97\nv_cvt_pk_bf16_f32 v97,v98,v99\nM v[40:43],v[204:207],a[240:243],v[40:43]\nv_accvgpr_write_b32 a146,v6\nv_accvgpr_write_b32 a147,v7\n" \
"M v[44:47],v[200:203],a[228:231],v[44:47]\nv_cvt_pk_bf16_f32 v100,v100,v101\nv_cvt_pk_bf16_f32 v101,v102,v103\nM v[44:47],v[204:207],a[244:247],v[44:47]\nv_accvgpr_write_b32 a160,v64\nv_accvgpr_write_b32 a161,v65\nM v[104:107],v[200:203],a[232:235],v[104:107]\nv_cvt_pk_bf16_f32 v34,v40,v41\n" \
"v_cvt_pk_bf16_f32 v35,v42,v43\nM v[104:107],v[204:207],a[248:251],v[104:107]\nv_accvgpr_write_b32 a162,v66\nv_accvgpr_write_b32 a163,v67\nM v[108:111],v[200:203],a[236:239],v[108:111]\nv_cvt_pk_bf16_f32 v38,v44,v45\nv_cvt_pk_bf16_f32 v39,v46,v47\nM v[108:111],v[204:207],a[252:255],v[108:111]\n" \
"v_accvgpr_write_b32 a176,v68\nv_accvgpr_write_b32 a177,v69\nM v[48:51],v[208:211],a[224:227],v[48:51]\nv_cvt_pk_bf16_f32 v98,v104,v105\nv_cvt_pk_bf16_f32 v99,v106,v107\nM v[48:51],v[212:215],a[240:243],v[48:51]\nv_accvgpr_write_b32 a178,v70\nv_accvgpr_write_b32 a179,v71\n" \
"M v[52:55],v[208:211],a[228:231],v[52:55]\nv_cvt_pk_bf16_f32 v102,v108,v109\nv_cvt_pk_bf16_f32 v103,v110,v111\nM v[52:55],v[212:215],a[244:247],v[52:55]\nv_accvgpr_write_b32 a132,v16\nv_accvgpr_write_b32 a133,v17\nM v[112:115],v[208:211],a[232:235],v[112:115]\nv_cvt_pk_bf16_f32 v48,v48,v49\n" \
"v_cvt_pk_bf16_f32 v49,v50,v51\nM v[112:115],v[212:215],a[248:251],v[112:115]\nv_accvgpr_write_b32 a134,v18\nv_accvgpr_write_b32 a135,v19\nM v[116:119],v[208:211],a[236:239],v[116:119]\nv_cvt_pk_bf16_f32 v52,v52,v53\nv_cvt_pk_bf16_f32 v53,v54,v55\nM v[116:119],v[212:215],a[252:255],v[116:119]\n" \
"v_accvgpr_write_b32 a148,v20\ns_waitcnt lgkmcnt(0)\nM v[56:59],v[216:219],a[224:227],v[56:59]\nv_cvt_pk_bf16_f32 v112,v112,v113\nv_cvt_pk_bf16_f32 v113,v114,v115\nM v[56:59],v[220:223],a[240:243],v[56:59]\nv_accvgpr_write_b32 a149,v21\nv_accvgpr_write_b32 a150,v22\n" \
"M v[60:63],v[216:219],a[228:231],v[60:63]\nv_cvt_pk_bf16_f32 v116,v116,v117\nv_cvt_pk_bf16_f32 v117,v118,v119\nM v[60:63],v[220:223],a[244:247],v[60:63]\nv_accvgpr_write_b32 a244,0\nv_accvgpr_write_b32 a245,0\nM v[120:123],v[216:219],a[232:235],v[120:123]\nv_cvt_pk_bf16_f32 v50,v56,v57\n" \
"v_cvt_pk_bf16_f32 v51,v58,v59\nM v[120:123],v[220:223],a[248:251],v[120:123]\nv_accvgpr_write_b32 a248,-1\nv_accvgpr_write_b32 a249,-1\nM v[124:127],v[216:219],a[236:239],v[124:127]\nv_cvt_pk_bf16_f32 v54,v60,v61\nv_cvt_pk_bf16_f32 v55,v62,v63\nM v[124:127],v[220:223],a[252:255],v[124:127]\n" \
"s_waitcnt vmcnt(0)\nbuffer_store_dword v252,off,s[52:55],s29 sc1\nv_cvt_pk_bf16_f32 v114,v120,v121\nv_cvt_pk_bf16_f32 v115,v122,v123\nv_accvgpr_write_b32 a246,0\nv_accvgpr_write_b32 a250,-1\nv_accvgpr_write_b32 a247,0\nv_accvgpr_write_b32 a251,-1\n" \
"v_cvt_pk_bf16_f32 v118,v124,v125\nv_cvt_pk_bf16_f32 v119,v126,v127\nv_accvgpr_write_b32 a151,v23\nv_accvgpr_write_b32 a164,v80\nv_accvgpr_write_b32 a165,v81\nv_accvgpr_write_b32 a166,v82\nv_accvgpr_write_b32 a167,v83\nv_accvgpr_write_b32 a180,v84\n" \
"v_accvgpr_write_b32 a181,v85\nv_accvgpr_write_b32 a182,v86\nv_accvgpr_write_b32 a183,v87\nv_accvgpr_write_b32 a136,v32\nv_accvgpr_write_b32 a137,v33\nv_accvgpr_write_b32 a138,v34\nv_accvgpr_write_b32 a139,v35\nv_accvgpr_write_b32 a152,v36\n" \
"v_accvgpr_write_b32 a153,v37\nv_accvgpr_write_b32 a154,v38\nv_accvgpr_write_b32 a155,v39\nv_accvgpr_write_b32 a168,v96\nv_accvgpr_write_b32 a169,v97\nv_accvgpr_write_b32 a170,v98\nv_accvgpr_write_b32 a171,v99\nv_accvgpr_write_b32 a184,v100\n" \
"v_accvgpr_write_b32 a185,v101\nv_accvgpr_write_b32 a186,v102\nv_accvgpr_write_b32 a187,v103\nv_accvgpr_write_b32 a140,v48\nv_accvgpr_write_b32 a141,v49\nv_accvgpr_write_b32 a142,v50\nv_accvgpr_write_b32 a143,v51\nv_accvgpr_write_b32 a156,v52\n" \
"v_accvgpr_write_b32 a157,v53\nv_accvgpr_write_b32 a158,v54\nv_accvgpr_write_b32 a159,v55\nv_accvgpr_write_b32 a172,v112\nv_accvgpr_write_b32 a173,v113\nv_accvgpr_write_b32 a174,v114\nv_accvgpr_write_b32 a175,v115\nv_accvgpr_write_b32 a188,v116\n" \
"v_accvgpr_write_b32 a189,v117\nv_accvgpr_write_b32 a190,v118\nv_accvgpr_write_b32 a191,v119\ns_waitcnt lgkmcnt(0)\n.Lattn:\ns_cmp_lt_u32 s7,2\ns_cbranch_scc0 .Lp23\ns_cmp_eq_u32 s7,0\n" \
"s_cbranch_scc0 .Lpro1\n.Lpro0:\ns_barrier\nM v[64:67],a[0:3],a[128:131],0\nds_read_b128 a[32:35],v240 offset:40960\nds_read_b128 a[36:39],v240 offset:41984\nM v[64:67],a[4:7],a[132:135],v[64:67]\nds_read_b128 a[40:43],v240 offset:43008\n" \
"s_mov_b32 m0,s64\nv_mov_b32 v218,0\nM v[64:67],a[8:11],a[136:139],v[64:67]\nds_read_b128 a[44:47],v240 offset:44032\nbuffer_load_dwordx4 v240,s[44:47],s36 offen offset:0 sc1 lds\nM v[64:67],a[12:15],a[140:143],v[64:67]\nds_read_b128 a[48:51],v240 offset:45056\nv_mov_b32 v219,0\n" \
"s_add_u32 s70,s36,12288\nM v[68:71],a[12:15],a[156:159],0\nds_read_b128 a[12:15],v240 offset:35840\nM v[68:71],a[8:11],a[152:155],v[68:71]\nds_read_b128 a[8:11],v240 offset:34816\nM v[68:71],a[4:7],a[148:151],v[68:71]\nds_read_b128 a[4:7],v240 offset:33792\nv_max3_f32 v225,v64,v65,v66\n" \
"v_max_f32 v225,v225,v67\nM v[68:71],a[0:3],a[144:147],v[68:71]\nds_read_b128 a[0:3],v240 offset:32768\nv_cndmask_b32 v225,v224,v225,s[90:91]\nv_cndmask_b32 v64,v64,v252,s[82:83]\nM v[76:79],a[28:31],a[156:159],0\nds_read_b128 a[28:31],v240 offset:39936\nv_cndmask_b32 v65,v65,v252,s[84:85]\n" \
"M v[76:79],a[24:27],a[152:155],v[76:79]\nds_read_b128 a[24:27],v240 offset:38912\nv_max3_f32 v229,v68,v69,v70\nv_cndmask_b32 v66,v66,v252,s[86:87]\nM v[76:79],a[20:23],a[148:151],v[76:79]\nds_read_b128 a[20:23],v240 offset:37888\nbuffer_load_dwordx4 v240,s[44:47],s36 offen offset:1024 sc1 lds\nM v[76:79],a[16:19],a[144:147],v[76:79]\n" \
"ds_read_b128 a[16:19],v240 offset:36864\nv_cndmask_b32 v67,v67,v252,s[88:89]\ns_waitcnt lgkmcnt(4)\nM v[136:139],a[0:3],a[160:163],0\nds_read_b128 a[52:55],v240 offset:46080\nM v[136:139],a[4:7],a[164:167],v[136:139]\nds_read_b128 a[56:59],v240 offset:47104\nM v[136:139],a[8:11],a[168:171],v[136:139]\n" \
"v_max3_f32 v229,v229,v71,v76\nv_max3_f32 v229,v229,v77,v78\nv_max_f32 v229,v229,v79\nM v[136:139],a[12:15],a[172:175],v[136:139]\nv_cndmask_b32 v229,v228,v229,s[90:91]\nds_read_b128 a[60:63],v240 offset:48128\nds_read_b128 a[64:67],v241 offset:0\nM v[140:143],a[12:15],a[188:191],0\n" \
"v_permlane16_swap_b32 v225,v229\nv_max_f32 v225,v225,v229\nv_mov_b32 v229,v225\nM v[140:143],a[8:11],a[184:187],v[140:143]\nv_max3_f32 v233,v136,v137,v138\nv_permlane32_swap_b32 v225,v229\nM v[140:143],a[4:7],a[180:183],v[140:143]\nv_max_f32 v225,v225,v229\n" \
"v_mov_b32 v229,v225\nds_read_b128 a[68:71],v241 offset:1024\nM v[140:143],a[0:3],a[176:179],v[140:143]\nv_permlane16_swap_b32 v225,v229\nv_sub_f32 v224,0,v225\ns_waitcnt lgkmcnt(5)\nM v[152:155],a[16:19],a[160:163],0\nv_sub_f32 v226,0,v225\n" \
"v_sub_f32 v227,0,v225\nv_sub_f32 v225,0,v225\nM v[152:155],a[20:23],a[164:167],v[152:155]\nv_sub_f32 v228,0,v229\nv_sub_f32 v230,0,v229\nv_sub_f32 v231,0,v229\nM v[152:155],a[24:27],a[168:171],v[152:155]\nv_sub_f32 v229,0,v229\n" \
"v_max3_f32 v237,v140,v141,v142\nM v[152:155],a[28:31],a[172:175],v[152:155]\nds_read_b128 a[72:75],v241 offset:2048\nds_read_b128 a[76:79],v241 offset:3072\nv_add_f32 v64,v64,v224\nM v[156:159],a[28:31],a[188:191],0\nds_read_b128 a[80:83],v241 offset:4096\nv_add_f32 v65,v65,v224\n" \
"v_add_f32 v66,v66,v224\nM v[156:159],a[24:27],a[184:187],v[156:159]\nv_max3_f32 v233,v233,v139,v152\nv_max3_f32 v233,v233,v153,v154\nv_max_f32 v233,v233,v155\nM v[156:159],a[20:23],a[180:183],v[156:159]\nv_cndmask_b32 v233,v232,v233,s[90:91]\nds_read_b128 a[84:87],v241 offset:5120\n" \
"M v[156:159],a[16:19],a[176:179],v[156:159]\nds_read_b128 a[88:91],v241 offset:6144\nv_add_f32 v67,v67,v224\nds_read_b128 a[92:95],v241 offset:7168\nM v[128:131],a[0:3],a[128:131],v[224:227]\nv_exp_f32 v64,v64\nv_add_f32 v68,v68,v228\nM v[128:131],a[4:7],a[132:135],v[128:131]\n" \
"v_exp_f32 v65,v65\nv_max3_f32 v237,v237,v143,v156\nM v[128:131],a[8:11],a[136:139],v[128:131]\nv_max3_f32 v237,v237,v157,v158\nv_max_f32 v237,v237,v159\nv_cndmask_b32 v237,v236,v237,s[90:91]\nM v[128:131],a[12:15],a[140:143],v[128:131]\nv_exp_f32 v66,v66\n" \
"M v[132:135],a[12:15],a[156:159],v[228:231]\nv_permlane16_swap_b32 v233,v237\nv_max_f32 v233,v233,v237\nv_mov_b32 v237,v233\nM v[132:135],a[8:11],a[152:155],v[132:135]\nv_exp_f32 v67,v67\nv_permlane32_swap_b32 v233,v237\nM v[132:135],a[4:7],a[148:151],v[132:135]\n" \
"v_max_f32 v233,v233,v237\nv_mov_b32 v237,v233\nv_cvt_pk_bf16_f32 v64,v64,v65\nM v[132:135],a[0:3],a[144:147],v[132:135]\nv_permlane16_swap_b32 v233,v237\nv_sub_f32 v232,0,v233\nM v[144:147],a[16:19],a[128:131],v[224:227]\nv_sub_f32 v234,0,v233\n" \
"v_sub_f32 v235,0,v233\nv_sub_f32 v233,0,v233\nM v[144:147],a[20:23],a[132:135],v[144:147]\nv_sub_f32 v236,0,v237\nv_sub_f32 v238,0,v237\nv_sub_f32 v239,0,v237\nM v[144:147],a[24:27],a[136:139],v[144:147]\nv_sub_f32 v237,0,v237\n" \
"v_cvt_pk_bf16_f32 v65,v66,v67\nv_mov_b32 v66,0\nM v[144:147],a[28:31],a[140:143],v[144:147]\nv_mov_b32 v67,0\nbuffer_load_dwordx4 v240,s[44:47],s36 offen offset:2048 sc1 lds\nM v[148:151],a[28:31],a[156:159],v[228:231]\nv_add_f32 v69,v69,v228\nv_cndmask_b32 v76,v76,v252,s[82:83]\n" \
"M v[148:151],a[24:27],a[152:155],v[148:151]\nv_cndmask_b32 v77,v77,v252,s[84:85]\nv_cndmask_b32 v78,v78,v252,s[86:87]\nv_cndmask_b32 v79,v79,v252,s[88:89]\nM v[148:151],a[20:23],a[148:151],v[148:151]\nv_add_f32 v70,v70,v228\nv_add_f32 v71,v71,v228\nv_add_f32 v76,v76,v228\n" \
"M v[148:151],a[16:19],a[144:147],v[148:151]\nbuffer_load_dwordx4 v240,s[44:47],s36 offen offset:3072 sc1 lds\nv_add_f32 v77,v77,v228\nM v[160:163],a[32:35],a[128:131],v[224:227]\nv_exp_f32 v68,v68\nM v[160:163],a[36:39],a[132:135],v[160:163]\nv_exp_f32 v69,v69\nv_add_f32 v78,v78,v228\n" \
"M v[160:163],a[40:43],a[136:139],v[160:163]\nv_exp_f32 v70,v70\nv_add_f32 v79,v79,v228\nM v[160:163],a[44:47],a[140:143],v[160:163]\nv_exp_f32 v71,v71\nv_cvt_pk_bf16_f32 v68,v68,v69\nM v[164:167],a[44:47],a[156:159],v[228:231]\nv_exp_f32 v76,v76\n" \
"v_cvt_pk_bf16_f32 v69,v70,v71\nM v[164:167],a[40:43],a[152:155],v[164:167]\nv_exp_f32 v77,v77\nM v[164:167],a[36:39],a[148:151],v[164:167]\nv_exp_f32 v78,v78\nv_cvt_pk_bf16_f32 v70,v76,v77\nM v[164:167],a[32:35],a[144:147],v[164:167]\nv_exp_f32 v79,v79\n" \
"ds_read_b128 a[0:3],v240 offset:49152\nM v[176:179],a[48:51],a[128:131],v[224:227]\nv_cvt_pk_bf16_f32 v71,v78,v79\nds_read_b128 a[4:7],v240 offset:50176\ns_waitcnt lgkmcnt(2)\nM v[176:179],a[52:55],a[132:135],v[176:179]\nds_read_b128 a[8:11],v240 offset:51200\nds_read_b128 a[12:15],v240 offset:52224\n" \
"M v[176:179],a[56:59],a[136:139],v[176:179]\nds_read_b128 a[16:19],v240 offset:53248\nv_exp_f32 v128,v128\nM v[176:179],a[60:63],a[140:143],v[176:179]\nv_exp_f32 v129,v129\nds_read_b128 a[20:23],v240 offset:54272\nM v[180:183],a[60:63],a[156:159],v[228:231]\nv_exp_f32 v130,v130\n" \
"v_cvt_pk_bf16_f32 v192,v128,v129\nM v[180:183],a[56:59],a[152:155],v[180:183]\nv_exp_f32 v131,v131\nds_read_b128 a[24:27],v240 offset:55296\nM v[180:183],a[52:55],a[148:151],v[180:183]\nv_cvt_pk_bf16_f32 v193,v130,v131\nds_read_b128 a[28:31],v240 offset:56320\nM v[180:183],a[48:51],a[144:147],v[180:183]\n" \
"v_exp_f32 v132,v132\nv_add_f32 v136,v136,v232\nM v[168:171],a[32:35],a[160:163],v[232:235]\nv_exp_f32 v133,v133\nv_add_f32 v137,v137,v232\nM v[168:171],a[36:39],a[164:167],v[168:171]\nv_exp_f32 v134,v134\nv_cvt_pk_bf16_f32 v196,v132,v133\n" \
"M v[168:171],a[40:43],a[168:171],v[168:171]\nv_exp_f32 v135,v135\nM v[168:171],a[44:47],a[172:175],v[168:171]\nv_cvt_pk_bf16_f32 v197,v134,v135\nv_exp_f32 v144,v144\nM v[172:175],a[44:47],a[188:191],v[236:239]\nv_exp_f32 v145,v145\nv_add_f32 v138,v138,v232\n" \
"M v[172:175],a[40:43],a[184:187],v[172:175]\nv_exp_f32 v146,v146\nv_cvt_pk_bf16_f32 v194,v144,v145\nM v[172:175],a[36:39],a[180:183],v[172:175]\nv_exp_f32 v147,v147\nv_add_f32 v139,v139,v232\nM v[172:175],a[32:35],a[176:179],v[172:175]\nv_cvt_pk_bf16_f32 v195,v146,v147\n" \
"v_add_f32 v140,v140,v236\nM v[188:191],a[60:63],a[188:191],v[236:239]\nv_exp_f32 v148,v148\nv_add_f32 v141,v141,v236\nM v[188:191],a[56:59],a[184:187],v[188:191]\nv_exp_f32 v149,v149\nv_add_f32 v142,v142,v236\nM v[188:191],a[52:55],a[180:183],v[188:191]\n" \
"v_exp_f32 v150,v150\nv_cvt_pk_bf16_f32 v198,v148,v149\nM v[188:191],a[48:51],a[176:179],v[188:191]\nv_exp_f32 v151,v151\nM v[0:3],a[64:67],v[64:67],0\nv_cvt_pk_bf16_f32 v199,v150,v151\nv_exp_f32 v136,v136\nM v[8:11],a[68:71],v[64:67],0\n" \
"v_exp_f32 v137,v137\nv_add_f32 v143,v143,v236\nM v[16:19],a[72:75],v[64:67],0\nbuffer_load_dword v248,v251,s[52:55],s80 offen sc1\nv_cvt_pk_bf16_f32 v200,v136,v137\nM v[24:27],a[76:79],v[64:67],0\nv_exp_f32 v138,v138\nds_read_b128 a[32:35],v240 offset:57344\n" \
"M v[32:35],a[80:83],v[64:67],0\nv_exp_f32 v139,v139\nM v[40:43],a[84:87],v[64:67],0\nv_cvt_pk_bf16_f32 v201,v138,v139\nv_exp_f32 v140,v140\nM v[48:51],a[88:91],v[64:67],0\nv_exp_f32 v141,v141\nv_add_f32 v152,v152,v232\n" \
"M v[56:59],a[92:95],v[64:67],0\nv_exp_f32 v142,v142\nv_cvt_pk_bf16_f32 v204,v140,v141\nM a[192:195],a[208:211],v[64:67],0\nv_exp_f32 v143,v143\nM v[4:7],a[64:67],v[68:71],0\nv_cvt_pk_bf16_f32 v205,v142,v143\nv_add_f32 v153,v153,v232\n" \
"v_add_f32 v154,v154,v232\nM v[12:15],a[68:71],v[68:71],0\nv_add_f32 v155,v155,v232\nv_exp_f32 v152,v152\nM v[20:23],a[72:75],v[68:71],0\nv_exp_f32 v153,v153\nds_read_b128 a[36:39],v240 offset:58368\nM v[28:31],a[76:79],v[68:71],0\n" \
"v_exp_f32 v154,v154\nM v[36:39],a[80:83],v[68:71],0\nv_exp_f32 v155,v155\nv_cvt_pk_bf16_f32 v202,v152,v153\nM v[44:47],a[84:87],v[68:71],0\nv_cvt_pk_bf16_f32 v203,v154,v155\nds_read_b128 a[40:43],v240 offset:59392\nds_read_b128 a[44:47],v240 offset:60416\n" \
"M v[52:55],a[88:91],v[68:71],0\nv_add_f32 v156,v156,v236\nv_add_f32 v157,v157,v236\nv_add_f32 v158,v158,v236\nM v[60:63],a[92:95],v[68:71],0\nv_add_f32 v159,v159,v236\nv_exp_f32 v156,v156\nM a[196:199],a[208:211],v[68:71],0\n" \
"ds_read_b128 a[48:51],v240 offset:61440\ns_waitcnt lgkmcnt(4)\nM v[128:131],a[0:3],a[128:131],v[224:227]\nv_exp_f32 v157,v157\nds_read_b128 a[52:55],v240 offset:62464\nM v[128:131],a[4:7],a[132:135],v[128:131]\nv_exp_f32 v158,v158\nv_cvt_pk_bf16_f32 v206,v156,v157\n" \
"M v[128:131],a[8:11],a[136:139],v[128:131]\nv_exp_f32 v159,v159\nds_read_b128 a[56:59],v240 offset:63488\nM v[128:131],a[12:15],a[140:143],v[128:131]\nv_cvt_pk_bf16_f32 v207,v158,v159\nds_read_b128 a[60:63],v240 offset:64512\nM v[132:135],a[12:15],a[156:159],v[228:231]\nv_exp_f32 v160,v160\n" \
"v_cndmask_b32 v168,v168,v252,s[82:83]\nM v[132:135],a[8:11],a[152:155],v[132:135]\nv_exp_f32 v161,v161\nv_cndmask_b32 v169,v169,v252,s[84:85]\nM v[132:135],a[4:7],a[148:151],v[132:135]\nv_exp_f32 v162,v162\nv_cvt_pk_bf16_f32 v208,v160,v161\nM v[132:135],a[0:3],a[144:147],v[132:135]\n" \
"v_exp_f32 v163,v163\nv_cndmask_b32 v170,v170,v252,s[86:87]\nM v[144:147],a[16:19],a[128:131],v[224:227]\nv_cvt_pk_bf16_f32 v209,v162,v163\nv_cndmask_b32 v171,v171,v252,s[88:89]\nM v[144:147],a[20:23],a[132:135],v[144:147]\nv_exp_f32 v164,v164\nds_read_b128 a[64:67],v241 offset:32768\n" \
"M v[144:147],a[24:27],a[136:139],v[144:147]\nv_exp_f32 v165,v165\nds_read_b128 a[96:99],v241 offset:40960\nM v[144:147],a[28:31],a[140:143],v[144:147]\nv_exp_f32 v166,v166\nv_cvt_pk_bf16_f32 v212,v164,v165\nM v[148:151],a[28:31],a[156:159],v[228:231]\nv_exp_f32 v167,v167\n" \
"M v[148:151],a[24:27],a[152:155],v[148:151]\nv_cvt_pk_bf16_f32 v213,v166,v167\nv_exp_f32 v176,v176\nM v[148:151],a[20:23],a[148:151],v[148:151]\nv_exp_f32 v177,v177\nds_read_b128 a[68:71],v241 offset:33792\nM v[148:151],a[16:19],a[144:147],v[148:151]\nv_exp_f32 v178,v178\n" \
"v_cvt_pk_bf16_f32 v210,v176,v177\nM v[136:139],a[0:3],a[160:163],v[232:235]\nv_exp_f32 v179,v179\nds_read_b128 a[100:103],v241 offset:41984\nM v[136:139],a[4:7],a[164:167],v[136:139]\nv_cvt_pk_bf16_f32 v211,v178,v179\nds_read_b128 a[72:75],v241 offset:34816\nM v[136:139],a[8:11],a[168:171],v[136:139]\n" \
"v_exp_f32 v180,v180\nv_cndmask_b32 v188,v188,v252,s[82:83]\nM v[136:139],a[12:15],a[172:175],v[136:139]\nv_exp_f32 v181,v181\nv_cndmask_b32 v189,v189,v252,s[84:85]\nM v[140:143],a[12:15],a[188:191],v[236:239]\nv_exp_f32 v182,v182\nv_cvt_pk_bf16_f32 v214,v180,v181\n" \
"M v[140:143],a[8:11],a[184:187],v[140:143]\nv_exp_f32 v183,v183\nM v[140:143],a[4:7],a[180:183],v[140:143]\nv_cvt_pk_bf16_f32 v215,v182,v183\nv_exp_f32 v168,v168\nM v[140:143],a[0:3],a[176:179],v[140:143]\nv_exp_f32 v169,v169\nv_cndmask_b32 v190,v190,v252,s[86:87]\n" \
"M v[152:155],a[16:19],a[160:163],v[232:235]\nv_exp_f32 v170,v170\nv_cvt_pk_bf16_f32 v216,v168,v169\nM v[152:155],a[20:23],a[164:167],v[152:155]\nv_exp_f32 v171,v171\nv_cndmask_b32 v191,v191,v252,s[88:89]\nM v[152:155],a[24:27],a[168:171],v[152:155]\nv_cvt_pk_bf16_f32 v217,v170,v171\n" \
"ds_read_b128 a[104:107],v241 offset:43008\nM v[152:155],a[28:31],a[172:175],v[152:155]\nv_exp_f32 v172,v172\nds_read_b128 a[76:79],v241 offset:35840\nM v[156:159],a[28:31],a[188:191],v[236:239]\nv_exp_f32 v173,v173\nds_read_b128 a[108:111],v241 offset:44032\nM v[156:159],a[24:27],a[184:187],v[156:159]\n" \
"v_exp_f32 v174,v174\nv_cvt_pk_bf16_f32 v220,v172,v173\nM v[156:159],a[20:23],a[180:183],v[156:159]\nv_exp_f32 v175,v175\nM v[156:159],a[16:19],a[176:179],v[156:159]\nv_cvt_pk_bf16_f32 v221,v174,v175\nv_exp_f32 v188,v188\nM v[160:163],a[32:35],a[128:131],v[224:227]\n" \
"v_exp_f32 v189,v189\ns_waitcnt lgkmcnt(3)\nM v[160:163],a[36:39],a[132:135],v[160:163]\nv_exp_f32 v190,v190\nv_cvt_pk_bf16_f32 v222,v188,v189\nM v[160:163],a[40:43],a[136:139],v[160:163]\ns_waitcnt vmcnt(1)\ns_barrier\n" \
"ds_read_b128 a[80:83],v241 offset:36864\nM v[160:163],a[44:47],a[140:143],v[160:163]\nv_exp_f32 v191,v191\nM v[164:167],a[44:47],a[156:159],v[228:231]\nv_cvt_pk_bf16_f32 v223,v190,v191\nds_read_b128 a[112:115],v241 offset:45056\nds_read_b128 a[84:87],v241 offset:37888\nM v[164:167],a[40:43],a[152:155],v[164:167]\n" \
"ds_read_b128 a[116:119],v241 offset:46080\ns_waitcnt vmcnt(0)\nv_cmp_eq_u32 vcc,0,v248\nM v[164:167],a[36:39],a[148:151],v[164:167]\nds_read_b128 a[88:91],v241 offset:38912\ns_cbranch_vccnz .Lps2\n.Lpb2:\ns_add_u32 m0,s64,65536\n" \
"M v[164:167],a[32:35],a[144:147],v[164:167]\nbuffer_load_dwordx4 v240,s[48:51],s36 offen offset:0 sc1 lds\nM v[176:179],a[48:51],a[128:131],v[224:227]\nds_read_b128 a[120:123],v241 offset:47104\nv_exp_f32 v128,v128\nM v[176:179],a[52:55],a[132:135],v[176:179]\nds_read_b128 a[92:95],v241 offset:39936\nv_exp_f32 v129,v129\n" \
"M v[176:179],a[56:59],a[136:139],v[176:179]\nds_read_b128 a[124:127],v241 offset:48128\nv_exp_f32 v130,v130\nM v[176:179],a[60:63],a[140:143],v[176:179]\nbuffer_load_dwordx4 v240,s[48:51],s36 offen offset:1024 sc1 lds\nds_read_b128 a[0:3],v240 offset:0\nM v[180:183],a[60:63],a[156:159],v[228:231]\nds_read_b128 a[4:7],v240 offset:1024\n" \
"ds_read_b128 a[8:11],v240 offset:2048\nM v[180:183],a[56:59],a[152:155],v[180:183]\nds_read_b128 a[12:15],v240 offset:3072\nv_exp_f32 v131,v131\nM v[180:183],a[52:55],a[148:151],v[180:183]\nds_read_b128 a[16:19],v240 offset:4096\nv_exp_f32 v132,v132\nM v[180:183],a[48:51],a[144:147],v[180:183]\n" \
"buffer_load_dwordx4 v240,s[48:51],s36 offen offset:2048 sc1 lds\nds_read_b128 a[20:23],v240 offset:5120\nM v[168:171],a[32:35],a[160:163],v[232:235]\nv_exp_f32 v133,v133\nM v[168:171],a[36:39],a[164:167],v[168:171]\nds_read_b128 a[24:27],v240 offset:6144\nv_exp_f32 v134,v134\nM v[168:171],a[40:43],a[168:171],v[168:171]\n" \
"ds_read_b128 a[28:31],v240 offset:7168\nv_exp_f32 v135,v135\nM v[168:171],a[44:47],a[172:175],v[168:171]\nbuffer_load_dwordx4 v240,s[48:51],s36 offen offset:3072 sc1 lds\ns_add_u32 m0,m0,12288\nM v[172:175],a[44:47],a[188:191],v[236:239]\nv_exp_f32 v144,v144\nM v[172:175],a[40:43],a[184:187],v[172:175]\n" \
"v_exp_f32 v145,v145\nM v[172:175],a[36:39],a[180:183],v[172:175]\nv_exp_f32 v146,v146\nM v[172:175],a[32:35],a[176:179],v[172:175]\nbuffer_load_dwordx4 v240,s[48:51],s70 offen offset:0 sc1 lds\nds_read_b128 a[32:35],v240 offset:8192\nM v[184:187],a[48:51],a[160:163],v[232:235]\nds_read_b128 a[36:39],v240 offset:9216\n" \
"v_exp_f32 v147,v147\nM v[184:187],a[52:55],a[164:167],v[184:187]\nds_read_b128 a[40:43],v240 offset:10240\nds_read_b128 a[44:47],v240 offset:11264\nM v[184:187],a[56:59],a[168:171],v[184:187]\nv_exp_f32 v148,v148\nM v[184:187],a[60:63],a[172:175],v[184:187]\nbuffer_load_dwordx4 v240,s[48:51],s70 offen offset:1024 sc1 lds\n" \
"M v[188:191],a[60:63],a[188:191],v[236:239]\nv_exp_f32 v149,v149\nM v[188:191],a[56:59],a[184:187],v[188:191]\nv_exp_f32 v150,v150\nM v[188:191],a[52:55],a[180:183],v[188:191]\nv_exp_f32 v151,v151\nM v[188:191],a[48:51],a[176:179],v[188:191]\nbuffer_load_dwordx4 v240,s[48:51],s70 offen offset:2048 sc1 lds\n" \
"ds_read_b128 a[48:51],v240 offset:12288\nM v[0:3],a[64:67],v[192:195],v[0:3]\nds_read_b128 a[52:55],v240 offset:13312\nv_exp_f32 v136,v136\nM v[0:3],a[96:99],v[208:211],v[0:3]\nds_read_b128 a[56:59],v240 offset:14336\nv_exp_f32 v137,v137\nM v[8:11],a[68:71],v[192:195],v[8:11]\n" \
"v_exp_f32 v138,v138\nM v[8:11],a[100:103],v[208:211],v[8:11]\nbuffer_load_dwordx4 v240,s[48:51],s70 offen offset:3072 sc1 lds\ns_add_u32 m0,s64,16384\nM v[16:19],a[72:75],v[192:195],v[16:19]\ns_add_u32 s70,s65,12288\nds_read_b128 a[60:63],v240 offset:15360\ns_waitcnt lgkmcnt(4)\n" \
"M v[16:19],a[104:107],v[208:211],v[16:19]\nv_exp_f32 v139,v139\nM v[24:27],a[76:79],v[192:195],v[24:27]\nv_exp_f32 v152,v152\nM v[24:27],a[108:111],v[208:211],v[24:27]\nbuffer_load_dwordx4 v240,s[44:47],s65 offen offset:0 sc1 lds\nM v[32:35],a[80:83],v[192:195],v[32:35]\nv_exp_f32 v153,v153\n" \
"M v[32:35],a[112:115],v[208:211],v[32:35]\nv_exp_f32 v154,v154\nM v[40:43],a[84:87],v[192:195],v[40:43]\nv_exp_f32 v155,v155\nM v[40:43],a[116:119],v[208:211],v[40:43]\nbuffer_load_dwordx4 v240,s[44:47],s65 offen offset:1024 sc1 lds\nM v[48:51],a[88:91],v[192:195],v[48:51]\nv_exp_f32 v160,v160\n" \
"M v[48:51],a[120:123],v[208:211],v[48:51]\nv_exp_f32 v161,v161\nM v[56:59],a[92:95],v[192:195],v[56:59]\nv_exp_f32 v162,v162\nM v[56:59],a[124:127],v[208:211],v[56:59]\nbuffer_load_dwordx4 v240,s[44:47],s65 offen offset:2048 sc1 lds\nM a[192:195],a[208:211],v[192:195],a[192:195]\nv_exp_f32 v163,v163\n" \
"v_cvt_pk_bf16_f32 v192,v128,v129\nM a[192:195],a[208:211],v[208:211],a[192:195]\nv_exp_f32 v176,v176\nv_cvt_pk_bf16_f32 v193,v130,v131\nM v[4:7],a[64:67],v[196:199],v[4:7]\nv_exp_f32 v177,v177\nv_cvt_pk_bf16_f32 v194,v144,v145\nM v[4:7],a[96:99],v[212:215],v[4:7]\n" \
"buffer_load_dwordx4 v240,s[44:47],s65 offen offset:3072 sc1 lds\ns_add_u32 m0,m0,12288\nM v[12:15],a[68:71],v[196:199],v[12:15]\nv_exp_f32 v178,v178\nM v[12:15],a[100:103],v[212:215],v[12:15]\nv_exp_f32 v179,v179\nv_cvt_pk_bf16_f32 v195,v146,v147\nM v[20:23],a[72:75],v[196:199],v[20:23]\n" \
"v_exp_f32 v140,v140\nv_cvt_pk_bf16_f32 v208,v160,v161\nM v[20:23],a[104:107],v[212:215],v[20:23]\nbuffer_load_dwordx4 v240,s[44:47],s70 offen offset:0 sc1 lds\nv_cvt_pk_bf16_f32 v209,v162,v163\nM v[28:31],a[76:79],v[196:199],v[28:31]\nv_exp_f32 v141,v141\nM v[28:31],a[108:111],v[212:215],v[28:31]\n" \
"v_exp_f32 v142,v142\nv_cvt_pk_bf16_f32 v210,v176,v177\nM v[36:39],a[80:83],v[196:199],v[36:39]\nv_exp_f32 v143,v143\nv_cvt_pk_bf16_f32 v211,v178,v179\nM v[36:39],a[112:115],v[212:215],v[36:39]\nbuffer_load_dwordx4 v240,s[44:47],s70 offen offset:1024 sc1 lds\nM v[44:47],a[84:87],v[196:199],v[44:47]\n" \
"v_exp_f32 v156,v156\nM v[44:47],a[116:119],v[212:215],v[44:47]\nv_exp_f32 v157,v157\nM v[52:55],a[88:91],v[196:199],v[52:55]\nv_exp_f32 v158,v158\nM v[52:55],a[120:123],v[212:215],v[52:55]\nbuffer_load_dwordx4 v240,s[44:47],s70 offen offset:2048 sc1 lds\nM v[60:63],a[92:95],v[196:199],v[60:63]\n" \
"s_barrier\nv_exp_f32 v159,v159\nM v[60:63],a[124:127],v[212:215],v[60:63]\nv_exp_f32 v164,v164\nM a[196:199],a[208:211],v[196:199],a[196:199]\nv_exp_f32 v165,v165\nv_cvt_pk_bf16_f32 v196,v132,v133\nM a[196:199],a[208:211],v[212:215],a[196:199]\n" \
"buffer_load_dwordx4 v240,s[44:47],s70 offen offset:3072 sc1 lds\ns_add_u32 m0,s64,81920\nM v[64:67],a[64:67],v[200:203],0\ns_add_u32 s70,s65,12288\nv_exp_f32 v166,v166\nM v[64:67],a[96:99],v[216:219],v[64:67]\nv_exp_f32 v167,v167\nv_cvt_pk_bf16_f32 v197,v134,v135\n" \
"M v[72:75],a[68:71],v[200:203],0\nv_exp_f32 v180,v180\nM v[72:75],a[100:103],v[216:219],v[72:75]\nbuffer_load_dwordx4 v240,s[48:51],s65 offen offset:0 sc1 lds\nv_cvt_pk_bf16_f32 v198,v148,v149\nM v[80:83],a[72:75],v[200:203],0\nv_exp_f32 v181,v181\nv_cvt_pk_bf16_f32 v199,v150,v151\n" \
"M v[80:83],a[104:107],v[216:219],v[80:83]\nv_exp_f32 v182,v182\nM v[88:91],a[76:79],v[200:203],0\nv_exp_f32 v183,v183\nM v[88:91],a[108:111],v[216:219],v[88:91]\nbuffer_load_dwordx4 v240,s[48:51],s65 offen offset:1024 sc1 lds\nM v[96:99],a[80:83],v[200:203],0\nv_exp_f32 v168,v168\n" \
"M v[96:99],a[112:115],v[216:219],v[96:99]\nv_exp_f32 v169,v169\nM v[104:107],a[84:87],v[200:203],0\nv_exp_f32 v170,v170\nM v[104:107],a[116:119],v[216:219],v[104:107]\nbuffer_load_dwordx4 v240,s[48:51],s65 offen offset:2048 sc1 lds\nM v[112:115],a[88:91],v[200:203],0\nv_exp_f32 v171,v171\n" \
"M v[112:115],a[120:123],v[216:219],v[112:115]\nM v[120:123],a[92:95],v[200:203],0\nM v[120:123],a[124:127],v[216:219],v[120:123]\nbuffer_load_dwordx4 v240,s[48:51],s65 offen offset:3072 sc1 lds\nM a[200:203],a[208:211],v[200:203],0\ns_add_u32 m0,m0,12288\nv_cvt_pk_bf16_f32 v200,v136,v137\nv_cvt_pk_bf16_f32 v201,v138,v139\n" \
"M a[200:203],a[208:211],v[216:219],a[200:203]\nv_cvt_pk_bf16_f32 v202,v152,v153\nv_cvt_pk_bf16_f32 v203,v154,v155\nM v[68:71],a[64:67],v[204:207],0\nM v[68:71],a[96:99],v[220:223],v[68:71]\nbuffer_load_dwordx4 v240,s[48:51],s70 offen offset:0 sc1 lds\nM v[76:79],a[68:71],v[204:207],0\nM v[76:79],a[100:103],v[220:223],v[76:79]\n" \
"M v[84:87],a[72:75],v[204:207],0\nM v[84:87],a[104:107],v[220:223],v[84:87]\nbuffer_load_dwordx4 v240,s[48:51],s70 offen offset:1024 sc1 lds\nM v[92:95],a[76:79],v[204:207],0\nM v[92:95],a[108:111],v[220:223],v[92:95]\nM v[100:103],a[80:83],v[204:207],0\nM v[100:103],a[112:115],v[220:223],v[100:103]\nbuffer_load_dwordx4 v240,s[48:51],s70 offen offset:2048 sc1 lds\n" \
"M v[108:111],a[84:87],v[204:207],0\nbuffer_load_dwordx4 v240,s[48:51],s70 offen offset:3072 sc1 lds\ns_add_u32 m0,s64,32768\nM v[108:111],a[116:119],v[220:223],v[108:111]\ns_add_u32 s70,s66,12288\nM v[116:119],a[88:91],v[204:207],0\nbuffer_load_dwordx4 v240,s[44:47],s66 offen offset:0 sc1 lds\nbuffer_load_dwordx4 v240,s[44:47],s66 offen offset:1024 sc1 lds\n" \
"buffer_load_dwordx4 v240,s[44:47],s66 offen offset:2048 sc1 lds\nbuffer_load_dwordx4 v240,s[44:47],s66 offen offset:3072 sc1 lds\nM v[116:119],a[120:123],v[220:223],v[116:119]\ns_add_u32 m0,m0,12288\ns_nop 0\nbuffer_load_dwordx4 v240,s[44:47],s70 offen offset:0 sc1 lds\nbuffer_load_dwordx4 v240,s[44:47],s70 offen offset:1024 sc1 lds\nbuffer_load_dwordx4 v240,s[44:47],s70 offen offset:2048 sc1 lds\n" \
"buffer_load_dwordx4 v240,s[44:47],s70 offen offset:3072 sc1 lds\nM v[124:127],a[92:95],v[204:207],0\ns_add_u32 m0,s64,98304\ns_add_u32 s70,s66,12288\nM v[124:127],a[124:127],v[220:223],v[124:127]\nbuffer_load_dwordx4 v240,s[48:51],s66 offen offset:0 sc1 lds\nbuffer_load_dwordx4 v240,s[48:51],s66 offen offset:1024 sc1 lds\nbuffer_load_dwordx4 v240,s[48:51],s66 offen offset:2048 sc1 lds\n" \
"buffer_load_dwordx4 v240,s[48:51],s66 offen offset:3072 sc1 lds\nM a[204:207],a[208:211],v[204:207],0\ns_add_u32 m0,m0,12288\ns_nop 0\nbuffer_load_dwordx4 v240,s[48:51],s70 offen offset:0 sc1 lds\nbuffer_load_dwordx4 v240,s[48:51],s70 offen offset:1024 sc1 lds\nbuffer_load_dwordx4 v240,s[48:51],s70 offen offset:2048 sc1 lds\nbuffer_load_dwordx4 v240,s[48:51],s70 offen offset:3072 sc1 lds\n" \
"M a[204:207],a[208:211],v[220:223],a[204:207]\ns_cmp_eq_u32 s62,0\ns_cbranch_scc1 .Ltrans\ns_branch .Ldual\n.Lpro1:\ns_barrier\nM v[64:67],a[0:3],a[128:131],0\nds_read_b128 a[64:67],v241 offset:0\n" \
"ds_read_b128 a[96:99],v241 offset:8192\nM v[64:67],a[4:7],a[132:135],v[64:67]\nds_read_b128 a[68:71],v241 offset:1024\ns_mov_b32 m0,s64\nv_mov_b32 v202,0\nM v[64:67],a[8:11],a[136:139],v[64:67]\nds_read_b128 a[100:103],v241 offset:9216\nv_mov_b32 v203,0\n" \
"M v[64:67],a[12:15],a[140:143],v[64:67]\nds_read_b128 a[72:75],v241 offset:2048\nbuffer_load_dwordx4 v240,s[44:47],s36 offen offset:0 sc1 lds\nM v[68:71],a[12:15],a[156:159],0\nds_read_b128 a[12:15],v240 offset:35840\nM v[68:71],a[8:11],a[152:155],v[68:71]\nds_read_b128 a[8:11],v240 offset:34816\nM v[68:71],a[4:7],a[148:151],v[68:71]\n" \
"ds_read_b128 a[4:7],v240 offset:33792\nv_max3_f32 v225,v64,v65,v66\nM v[68:71],a[0:3],a[144:147],v[68:71]\nds_read_b128 a[0:3],v240 offset:32768\nbuffer_load_dwordx4 v240,s[44:47],s36 offen offset:1024 sc1 lds\nM v[72:75],a[16:19],a[128:131],0\nds_read_b128 a[104:107],v241 offset:10240\nM v[72:75],a[20:23],a[132:135],v[72:75]\n" \
"ds_read_b128 a[76:79],v241 offset:3072\nM v[72:75],a[24:27],a[136:139],v[72:75]\nds_read_b128 a[108:111],v241 offset:11264\nv_max3_f32 v229,v68,v69,v70\nM v[72:75],a[28:31],a[140:143],v[72:75]\nds_read_b128 a[80:83],v241 offset:4096\nbuffer_load_dwordx4 v240,s[44:47],s36 offen offset:2048 sc1 lds\nM v[76:79],a[28:31],a[156:159],0\n" \
"ds_read_b128 a[28:31],v240 offset:39936\nM v[76:79],a[24:27],a[152:155],v[76:79]\nds_read_b128 a[24:27],v240 offset:38912\nM v[76:79],a[20:23],a[148:151],v[76:79]\nds_read_b128 a[20:23],v240 offset:37888\nv_max3_f32 v225,v225,v67,v72\nv_max3_f32 v225,v225,v73,v74\nM v[76:79],a[16:19],a[144:147],v[76:79]\n" \
"ds_read_b128 a[16:19],v240 offset:36864\nv_max_f32 v225,v225,v75\nv_cndmask_b32 v225,v224,v225,s[90:91]\nM v[80:83],a[32:35],a[128:131],0\nbuffer_load_dwordx4 v240,s[44:47],s36 offen offset:3072 sc1 lds\nM v[80:83],a[36:39],a[132:135],v[80:83]\nds_read_b128 a[112:115],v241 offset:12288\nds_read_b128 a[84:87],v241 offset:5120\n" \
"v_max3_f32 v229,v229,v71,v76\nM v[80:83],a[40:43],a[136:139],v[80:83]\nv_max3_f32 v229,v229,v77,v78\nv_max_f32 v229,v229,v79\nv_cndmask_b32 v229,v228,v229,s[90:91]\nM v[80:83],a[44:47],a[140:143],v[80:83]\nds_read_b128 a[116:119],v241 offset:13312\nv_permlane16_swap_b32 v225,v229\n" \
"M v[84:87],a[44:47],a[156:159],0\nv_max_f32 v225,v225,v229\nv_mov_b32 v229,v225\nds_read_b128 a[44:47],v240 offset:44032\nM v[84:87],a[40:43],a[152:155],v[84:87]\nv_permlane32_swap_b32 v225,v229\nv_max_f32 v225,v225,v229\nM v[84:87],a[36:39],a[148:151],v[84:87]\n" \
"v_mov_b32 v229,v225\nds_read_b128 a[36:39],v240 offset:41984\nds_read_b128 a[40:43],v240 offset:43008\nM v[84:87],a[32:35],a[144:147],v[84:87]\nv_permlane16_swap_b32 v225,v229\nv_sub_f32 v224,0,v225\nv_sub_f32 v226,0,v225\nM v[92:95],a[60:63],a[156:159],0\n" \
"v_sub_f32 v227,0,v225\nv_sub_f32 v225,0,v225\nM v[92:95],a[56:59],a[152:155],v[92:95]\nv_sub_f32 v228,0,v229\nv_sub_f32 v230,0,v229\nv_sub_f32 v231,0,v229\nM v[92:95],a[52:55],a[148:151],v[92:95]\nv_sub_f32 v229,0,v229\n" \
"ds_read_b128 a[32:35],v240 offset:40960\nds_read_b128 a[52:55],v240 offset:46080\nM v[92:95],a[48:51],a[144:147],v[92:95]\nds_read_b128 a[48:51],v240 offset:45056\ns_waitcnt lgkmcnt(7)\nM v[136:139],a[0:3],a[160:163],0\nds_read_b128 a[56:59],v240 offset:47104\nv_add_f32 v64,v64,v224\n" \
"v_add_f32 v65,v65,v224\nM v[136:139],a[4:7],a[164:167],v[136:139]\nds_read_b128 a[60:63],v240 offset:48128\nv_add_f32 v66,v66,v224\nM v[136:139],a[8:11],a[168:171],v[136:139]\nv_add_f32 v67,v67,v224\nv_exp_f32 v64,v64\nM v[136:139],a[12:15],a[172:175],v[136:139]\n" \
"v_exp_f32 v65,v65\nv_cndmask_b32 v80,v80,v252,s[82:83]\nM v[140:143],a[12:15],a[188:191],0\nv_cndmask_b32 v81,v81,v252,s[84:85]\nv_add_f32 v72,v72,v224\nM v[140:143],a[8:11],a[184:187],v[140:143]\nv_exp_f32 v66,v66\nv_add_f32 v73,v73,v224\n" \
"M v[140:143],a[4:7],a[180:183],v[140:143]\nv_exp_f32 v67,v67\nv_add_f32 v74,v74,v224\nM v[140:143],a[0:3],a[176:179],v[140:143]\nv_add_f32 v75,v75,v224\nv_cndmask_b32 v82,v82,v252,s[86:87]\nM v[156:159],a[28:31],a[188:191],0\nv_cndmask_b32 v83,v83,v252,s[88:89]\n" \
"v_add_f32 v80,v80,v224\nv_add_f32 v81,v81,v224\nM v[156:159],a[24:27],a[184:187],v[156:159]\nv_exp_f32 v72,v72\nv_add_f32 v82,v82,v224\nM v[156:159],a[20:23],a[180:183],v[156:159]\nv_exp_f32 v73,v73\nM v[156:159],a[16:19],a[176:179],v[156:159]\n" \
"v_exp_f32 v74,v74\nv_add_f32 v83,v83,v224\nM v[128:131],a[0:3],a[128:131],v[224:227]\nv_exp_f32 v75,v75\nM v[128:131],a[4:7],a[132:135],v[128:131]\nv_exp_f32 v80,v80\nv_cvt_pk_bf16_f32 v64,v64,v65\nM v[128:131],a[8:11],a[136:139],v[128:131]\n" \
"v_exp_f32 v81,v81\nv_cvt_pk_bf16_f32 v65,v66,v67\nM v[128:131],a[12:15],a[140:143],v[128:131]\nv_exp_f32 v82,v82\nM v[132:135],a[12:15],a[156:159],v[228:231]\nv_exp_f32 v83,v83\nv_cvt_pk_bf16_f32 v66,v72,v73\nM v[132:135],a[8:11],a[152:155],v[132:135]\n" \
"v_cvt_pk_bf16_f32 v67,v74,v75\nv_cvt_pk_bf16_f32 v80,v80,v81\nv_cvt_pk_bf16_f32 v81,v82,v83\nM v[132:135],a[4:7],a[148:151],v[132:135]\nv_mov_b32 v82,0\nv_mov_b32 v83,0\nM v[132:135],a[0:3],a[144:147],v[132:135]\nds_read_b128 a[88:91],v241 offset:6144\n" \
"ds_read_b128 a[120:123],v241 offset:14336\nv_add_f32 v68,v68,v228\nM v[144:147],a[16:19],a[128:131],v[224:227]\nds_read_b128 a[92:95],v241 offset:7168\nv_add_f32 v69,v69,v228\nv_add_f32 v70,v70,v228\nM v[144:147],a[20:23],a[132:135],v[144:147]\nds_read_b128 a[124:127],v241 offset:15360\n" \
"v_add_f32 v71,v71,v228\nM v[144:147],a[24:27],a[136:139],v[144:147]\nv_exp_f32 v68,v68\nv_add_f32 v76,v76,v228\nM v[144:147],a[28:31],a[140:143],v[144:147]\nv_exp_f32 v69,v69\nM v[148:151],a[28:31],a[156:159],v[228:231]\nv_exp_f32 v70,v70\n" \
"v_add_f32 v77,v77,v228\nM v[148:151],a[24:27],a[152:155],v[148:151]\nv_exp_f32 v71,v71\nv_add_f32 v78,v78,v228\nM v[148:151],a[20:23],a[148:151],v[148:151]\nv_add_f32 v79,v79,v228\nv_add_f32 v84,v84,v228\nM v[148:151],a[16:19],a[144:147],v[148:151]\n" \
"v_add_f32 v85,v85,v228\nv_cndmask_b32 v92,v92,v252,s[82:83]\ns_waitcnt lgkmcnt(4)\nM v[160:163],a[32:35],a[128:131],v[224:227]\nv_exp_f32 v76,v76\nv_cndmask_b32 v93,v93,v252,s[84:85]\nM v[160:163],a[36:39],a[132:135],v[160:163]\nv_exp_f32 v77,v77\n" \
"M v[160:163],a[40:43],a[136:139],v[160:163]\nv_exp_f32 v78,v78\nv_cndmask_b32 v94,v94,v252,s[86:87]\nM v[160:163],a[44:47],a[140:143],v[160:163]\nv_exp_f32 v79,v79\nM v[164:167],a[44:47],a[156:159],v[228:231]\nv_cndmask_b32 v95,v95,v252,s[88:89]\nv_add_f32 v86,v86,v228\n" \
"v_add_f32 v87,v87,v228\nM v[164:167],a[40:43],a[152:155],v[164:167]\nv_exp_f32 v84,v84\nv_cvt_pk_bf16_f32 v68,v68,v69\nM v[164:167],a[36:39],a[148:151],v[164:167]\nv_exp_f32 v85,v85\nM v[164:167],a[32:35],a[144:147],v[164:167]\nv_exp_f32 v86,v86\n" \
"v_add_f32 v92,v92,v228\nM v[176:179],a[48:51],a[128:131],v[224:227]\nv_exp_f32 v87,v87\nv_add_f32 v93,v93,v228\nM v[176:179],a[52:55],a[132:135],v[176:179]\nv_add_f32 v94,v94,v228\nv_add_f32 v95,v95,v228\nM v[176:179],a[56:59],a[136:139],v[176:179]\n" \
"v_cvt_pk_bf16_f32 v69,v70,v71\nv_exp_f32 v92,v92\nM v[176:179],a[60:63],a[140:143],v[176:179]\nv_exp_f32 v93,v93\nv_cvt_pk_bf16_f32 v70,v76,v77\nM v[180:183],a[60:63],a[156:159],v[228:231]\nv_exp_f32 v94,v94\nM v[180:183],a[56:59],a[152:155],v[180:183]\n" \
"v_exp_f32 v95,v95\nv_cvt_pk_bf16_f32 v71,v78,v79\nM v[180:183],a[52:55],a[148:151],v[180:183]\nv_cvt_pk_bf16_f32 v84,v84,v85\nv_cvt_pk_bf16_f32 v85,v86,v87\nM v[180:183],a[48:51],a[144:147],v[180:183]\nv_cvt_pk_bf16_f32 v86,v92,v93\nv_cvt_pk_bf16_f32 v87,v94,v95\n" \
"ds_read_b128 a[0:3],v240 offset:49152\nM v[0:3],a[64:67],v[64:67],0\nds_read_b128 a[4:7],v240 offset:50176\nds_read_b128 a[8:11],v240 offset:51200\nv_max3_f32 v237,v140,v141,v142\nM v[0:3],a[96:99],v[80:83],v[0:3]\nds_read_b128 a[12:15],v240 offset:52224\nv_max3_f32 v237,v237,v143,v156\n" \
"M v[8:11],a[68:71],v[64:67],0\nds_read_b128 a[16:19],v240 offset:53248\nv_exp_f32 v128,v128\nM v[8:11],a[100:103],v[80:83],v[8:11]\nv_exp_f32 v129,v129\nds_read_b128 a[20:23],v240 offset:54272\nM v[16:19],a[72:75],v[64:67],0\nv_exp_f32 v130,v130\n" \
"M v[16:19],a[104:107],v[80:83],v[16:19]\nv_exp_f32 v131,v131\nv_cvt_pk_bf16_f32 v192,v128,v129\nM v[24:27],a[76:79],v[64:67],0\nbuffer_load_dword v248,v251,s[52:55],s80 offen sc1\nv_cvt_pk_bf16_f32 v193,v130,v131\nM v[24:27],a[108:111],v[80:83],v[24:27]\nds_read_b128 a[24:27],v240 offset:55296\n" \
"ds_read_b128 a[28:31],v240 offset:56320\nM v[32:35],a[80:83],v[64:67],0\nv_exp_f32 v132,v132\nv_max3_f32 v233,v136,v137,v138\nM v[32:35],a[112:115],v[80:83],v[32:35]\nv_exp_f32 v133,v133\nM v[40:43],a[84:87],v[64:67],0\nv_exp_f32 v134,v134\n" \
"v_cvt_pk_bf16_f32 v196,v132,v133\nM v[40:43],a[116:119],v[80:83],v[40:43]\nv_exp_f32 v135,v135\ns_waitcnt lgkmcnt(3)\nM v[48:51],a[88:91],v[64:67],0\nv_cvt_pk_bf16_f32 v197,v134,v135\nv_max3_f32 v237,v237,v157,v158\nM v[48:51],a[120:123],v[80:83],v[48:51]\n" \
"v_exp_f32 v144,v144\nv_max_f32 v233,v233,v139\nM v[56:59],a[92:95],v[64:67],0\nv_exp_f32 v145,v145\nv_max_f32 v237,v237,v159\nM v[56:59],a[124:127],v[80:83],v[56:59]\nv_exp_f32 v146,v146\nM a[192:195],a[208:211],v[64:67],0\n" \
"v_exp_f32 v147,v147\nv_cvt_pk_bf16_f32 v194,v144,v145\nM a[192:195],a[208:211],v[80:83],a[192:195]\nv_cvt_pk_bf16_f32 v195,v146,v147\nv_cndmask_b32 v233,v232,v233,s[90:91]\nM v[4:7],a[64:67],v[68:71],0\nv_cndmask_b32 v237,v236,v237,s[90:91]\nv_exp_f32 v148,v148\n" \
"M v[4:7],a[96:99],v[84:87],v[4:7]\nv_permlane16_swap_b32 v233,v237\nv_max_f32 v233,v233,v237\nv_mov_b32 v237,v233\nM v[12:15],a[68:71],v[68:71],0\nv_exp_f32 v149,v149\nM v[12:15],a[100:103],v[84:87],v[12:15]\nv_exp_f32 v150,v150\n" \
"v_permlane32_swap_b32 v233,v237\nM v[20:23],a[72:75],v[68:71],0\nv_exp_f32 v151,v151\nv_cvt_pk_bf16_f32 v198,v148,v149\nM v[20:23],a[104:107],v[84:87],v[20:23]\nv_cvt_pk_bf16_f32 v199,v150,v151\nv_max_f32 v233,v233,v237\nM v[28:31],a[76:79],v[68:71],0\n" \
"v_mov_b32 v237,v233\nv_cndmask_b32 v136,v136,v252,s[82:83]\nv_cndmask_b32 v137,v137,v252,s[84:85]\nM v[28:31],a[108:111],v[84:87],v[28:31]\nv_permlane16_swap_b32 v233,v237\nv_cndmask_b32 v138,v138,v252,s[86:87]\nv_cndmask_b32 v139,v139,v252,s[88:89]\nM v[36:39],a[80:83],v[68:71],0\n" \
"v_sub_f32 v232,0,v233\nv_sub_f32 v234,0,v233\nM v[36:39],a[112:115],v[84:87],v[36:39]\nv_sub_f32 v235,0,v233\nv_add_f32 v136,v136,v232\nv_add_f32 v137,v137,v232\nM v[44:47],a[84:87],v[68:71],0\nv_add_f32 v138,v138,v232\n" \
"v_add_f32 v139,v139,v232\nM v[44:47],a[116:119],v[84:87],v[44:47]\nv_sub_f32 v233,0,v233\nv_exp_f32 v136,v136\nM v[52:55],a[88:91],v[68:71],0\nv_exp_f32 v137,v137\nv_sub_f32 v236,0,v237\nM v[52:55],a[120:123],v[84:87],v[52:55]\n" \
"v_exp_f32 v138,v138\nM v[60:63],a[92:95],v[68:71],0\nv_exp_f32 v139,v139\nv_cvt_pk_bf16_f32 v200,v136,v137\nM v[60:63],a[124:127],v[84:87],v[60:63]\nv_cvt_pk_bf16_f32 v201,v138,v139\nv_sub_f32 v238,0,v237\nv_sub_f32 v239,0,v237\n" \
"M a[196:199],a[208:211],v[68:71],0\nv_add_f32 v140,v140,v236\nv_add_f32 v141,v141,v236\nM a[196:199],a[208:211],v[84:87],a[196:199]\nv_add_f32 v142,v142,v236\nv_add_f32 v143,v143,v236\nv_sub_f32 v237,0,v237\nM v[128:131],a[0:3],a[128:131],v[224:227]\n" \
"v_exp_f32 v140,v140\nds_read_b128 a[32:35],v240 offset:57344\nM v[128:131],a[4:7],a[132:135],v[128:131]\nv_exp_f32 v141,v141\nM v[128:131],a[8:11],a[136:139],v[128:131]\nv_exp_f32 v142,v142\nv_cvt_pk_bf16_f32 v204,v140,v141\nM v[128:131],a[12:15],a[140:143],v[128:131]\n" \
"v_exp_f32 v143,v143\nM v[132:135],a[12:15],a[156:159],v[228:231]\nv_cvt_pk_bf16_f32 v205,v142,v143\nds_read_b128 a[36:39],v240 offset:58368\nds_read_b128 a[40:43],v240 offset:59392\nM v[132:135],a[8:11],a[152:155],v[132:135]\nds_read_b128 a[44:47],v240 offset:60416\nv_cndmask_b32 v156,v156,v252,s[82:83]\n" \
"v_cndmask_b32 v157,v157,v252,s[84:85]\nM v[132:135],a[4:7],a[148:151],v[132:135]\nv_cndmask_b32 v158,v158,v252,s[86:87]\nv_cndmask_b32 v159,v159,v252,s[88:89]\nM v[132:135],a[0:3],a[144:147],v[132:135]\nv_add_f32 v156,v156,v236\nv_add_f32 v157,v157,v236\nv_add_f32 v158,v158,v236\n" \
"M v[144:147],a[16:19],a[128:131],v[224:227]\nv_add_f32 v159,v159,v236\nds_read_b128 a[48:51],v240 offset:61440\ns_waitcnt lgkmcnt(5)\nM v[144:147],a[20:23],a[132:135],v[144:147]\nv_exp_f32 v156,v156\nM v[144:147],a[24:27],a[136:139],v[144:147]\nv_exp_f32 v157,v157\n" \
"ds_read_b128 a[52:55],v240 offset:62464\nM v[144:147],a[28:31],a[140:143],v[144:147]\nv_exp_f32 v158,v158\nM v[148:151],a[28:31],a[156:159],v[228:231]\nv_exp_f32 v159,v159\nv_cvt_pk_bf16_f32 v206,v156,v157\nM v[148:151],a[24:27],a[152:155],v[148:151]\nv_cvt_pk_bf16_f32 v207,v158,v159\n" \
"v_exp_f32 v160,v160\nM v[148:151],a[20:23],a[148:151],v[148:151]\nv_exp_f32 v161,v161\nM v[148:151],a[16:19],a[144:147],v[148:151]\nv_exp_f32 v162,v162\nv_cvt_pk_bf16_f32 v208,v160,v161\nM v[136:139],a[0:3],a[160:163],v[232:235]\nv_exp_f32 v163,v163\n" \
"ds_read_b128 a[56:59],v240 offset:63488\nM v[136:139],a[4:7],a[164:167],v[136:139]\nv_cvt_pk_bf16_f32 v209,v162,v163\nds_read_b128 a[60:63],v240 offset:64512\nM v[136:139],a[8:11],a[168:171],v[136:139]\nv_exp_f32 v164,v164\nds_read_b128 a[64:67],v241 offset:32768\nM v[136:139],a[12:15],a[172:175],v[136:139]\n" \
"v_exp_f32 v165,v165\nds_read_b128 a[96:99],v241 offset:40960\nM v[140:143],a[12:15],a[188:191],v[236:239]\ns_waitcnt vmcnt(1)\ns_barrier\nM v[140:143],a[8:11],a[184:187],v[140:143]\nv_exp_f32 v166,v166\nv_cvt_pk_bf16_f32 v212,v164,v165\n" \
"M v[140:143],a[4:7],a[180:183],v[140:143]\nv_exp_f32 v167,v167\nM v[140:143],a[0:3],a[176:179],v[140:143]\nv_cvt_pk_bf16_f32 v213,v166,v167\nv_exp_f32 v176,v176\nM v[152:155],a[16:19],a[160:163],v[232:235]\nv_exp_f32 v177,v177\nds_read_b128 a[68:71],v241 offset:33792\n" \
"M v[152:155],a[20:23],a[164:167],v[152:155]\nv_exp_f32 v178,v178\nM v[152:155],a[24:27],a[168:171],v[152:155]\nv_exp_f32 v179,v179\nv_cvt_pk_bf16_f32 v210,v176,v177\nM v[152:155],a[28:31],a[172:175],v[152:155]\nv_cvt_pk_bf16_f32 v211,v178,v179\nv_exp_f32 v180,v180\n" \
"M v[156:159],a[28:31],a[188:191],v[236:239]\nv_exp_f32 v181,v181\nM v[156:159],a[24:27],a[184:187],v[156:159]\nv_exp_f32 v182,v182\nv_cvt_pk_bf16_f32 v214,v180,v181\nM v[156:159],a[20:23],a[180:183],v[156:159]\nv_exp_f32 v183,v183\nds_read_b128 a[100:103],v241 offset:41984\n" \
"M v[156:159],a[16:19],a[176:179],v[156:159]\nv_cvt_pk_bf16_f32 v215,v182,v183\ns_waitcnt lgkmcnt(2)\nM v[160:163],a[32:35],a[128:131],v[224:227]\nds_read_b128 a[72:75],v241 offset:34816\nds_read_b128 a[104:107],v241 offset:43008\ns_waitcnt vmcnt(0)\nv_cmp_eq_u32 vcc,0,v248\n" \
"M v[160:163],a[36:39],a[132:135],v[160:163]\nds_read_b128 a[76:79],v241 offset:35840\ns_cbranch_vccnz .Lps3\n.Lpb3:\nM v[160:163],a[40:43],a[136:139],v[160:163]\nds_read_b128 a[108:111],v241 offset:44032\nv_exp_f32 v128,v128\nM v[160:163],a[44:47],a[140:143],v[160:163]\n" \
"ds_read_b128 a[80:83],v241 offset:36864\nv_exp_f32 v129,v129\nM v[164:167],a[44:47],a[156:159],v[228:231]\nds_read_b128 a[112:115],v241 offset:45056\ns_add_u32 m0,s64,65536\nM v[164:167],a[40:43],a[152:155],v[164:167]\nds_read_b128 a[84:87],v241 offset:37888\nbuffer_load_dwordx4 v240,s[48:51],s36 offen offset:0 sc1 lds\n" \
"M v[164:167],a[36:39],a[148:151],v[164:167]\nds_read_b128 a[116:119],v241 offset:46080\nv_exp_f32 v130,v130\nM v[164:167],a[32:35],a[144:147],v[164:167]\nv_exp_f32 v131,v131\nM v[176:179],a[48:51],a[128:131],v[224:227]\nds_read_b128 a[88:91],v241 offset:38912\nv_exp_f32 v132,v132\n" \
"M v[176:179],a[52:55],a[132:135],v[176:179]\nds_read_b128 a[120:123],v241 offset:47104\nds_read_b128 a[92:95],v241 offset:39936\nM v[176:179],a[56:59],a[136:139],v[176:179]\nds_read_b128 a[124:127],v241 offset:48128\nbuffer_load_dwordx4 v240,s[48:51],s36 offen offset:1024 sc1 lds\nM v[176:179],a[60:63],a[140:143],v[176:179]\nds_read_b128 a[0:3],v240 offset:0\n" \
"v_exp_f32 v133,v133\nM v[180:183],a[60:63],a[156:159],v[228:231]\nv_exp_f32 v134,v134\nM v[180:183],a[56:59],a[152:155],v[180:183]\nds_read_b128 a[4:7],v240 offset:1024\nv_exp_f32 v135,v135\nM v[180:183],a[52:55],a[148:151],v[180:183]\nds_read_b128 a[8:11],v240 offset:2048\n" \
"buffer_load_dwordx4 v240,s[48:51],s36 offen offset:2048 sc1 lds\nM v[180:183],a[48:51],a[144:147],v[180:183]\nds_read_b128 a[12:15],v240 offset:3072\nds_read_b128 a[16:19],v240 offset:4096\nM v[168:171],a[32:35],a[160:163],v[232:235]\nds_read_b128 a[20:23],v240 offset:5120\nv_exp_f32 v144,v144\nM v[168:171],a[36:39],a[164:167],v[168:171]\n" \
"ds_read_b128 a[24:27],v240 offset:6144\nv_exp_f32 v145,v145\nM v[168:171],a[40:43],a[168:171],v[168:171]\nbuffer_load_dwordx4 v240,s[48:51],s36 offen offset:3072 sc1 lds\nM v[168:171],a[44:47],a[172:175],v[168:171]\nds_read_b128 a[28:31],v240 offset:7168\nv_exp_f32 v146,v146\nM v[172:175],a[44:47],a[188:191],v[236:239]\n" \
"v_exp_f32 v147,v147\nM v[172:175],a[40:43],a[184:187],v[172:175]\ns_add_u32 m0,s64,16384\nv_exp_f32 v148,v148\nM v[172:175],a[36:39],a[180:183],v[172:175]\nbuffer_load_dwordx4 v240,s[44:47],s65 offen offset:0 sc1 lds\nM v[172:175],a[32:35],a[176:179],v[172:175]\nds_read_b128 a[32:35],v240 offset:8192\n" \
"ds_read_b128 a[36:39],v240 offset:9216\nM v[184:187],a[48:51],a[160:163],v[232:235]\nds_read_b128 a[40:43],v240 offset:10240\nv_exp_f32 v149,v149\nM v[184:187],a[52:55],a[164:167],v[184:187]\nds_read_b128 a[44:47],v240 offset:11264\nv_exp_f32 v150,v150\nM v[184:187],a[56:59],a[168:171],v[184:187]\n" \
"buffer_load_dwordx4 v240,s[44:47],s65 offen offset:1024 sc1 lds\nM v[184:187],a[60:63],a[172:175],v[184:187]\nv_exp_f32 v151,v151\nM v[188:191],a[60:63],a[188:191],v[236:239]\nv_exp_f32 v136,v136\nM v[188:191],a[56:59],a[184:187],v[188:191]\nv_exp_f32 v137,v137\nM v[188:191],a[52:55],a[180:183],v[188:191]\n" \
"buffer_load_dwordx4 v240,s[44:47],s65 offen offset:2048 sc1 lds\nM v[188:191],a[48:51],a[176:179],v[188:191]\nds_read_b128 a[48:51],v240 offset:12288\nds_read_b128 a[52:55],v240 offset:13312\nM v[0:3],a[64:67],v[192:195],v[0:3]\nds_read_b128 a[56:59],v240 offset:14336\nv_exp_f32 v138,v138\nM v[0:3],a[96:99],v[208:211],v[0:3]\n" \
"v_exp_f32 v139,v139\ns_waitcnt lgkmcnt(4)\nM v[8:11],a[68:71],v[192:195],v[8:11]\nbuffer_load_dwordx4 v240,s[44:47],s65 offen offset:3072 sc1 lds\nM v[8:11],a[100:103],v[208:211],v[8:11]\ns_add_u32 m0,s64,81920\nv_exp_f32 v152,v152\nM v[16:19],a[72:75],v[192:195],v[16:19]\n" \
"ds_read_b128 a[60:63],v240 offset:15360\nv_exp_f32 v153,v153\nM v[16:19],a[104:107],v[208:211],v[16:19]\nv_exp_f32 v154,v154\nM v[24:27],a[76:79],v[192:195],v[24:27]\nbuffer_load_dwordx4 v240,s[48:51],s65 offen offset:0 sc1 lds\nM v[24:27],a[108:111],v[208:211],v[24:27]\nv_exp_f32 v155,v155\n" \
"M v[32:35],a[80:83],v[192:195],v[32:35]\nv_exp_f32 v160,v160\nM v[32:35],a[112:115],v[208:211],v[32:35]\nv_exp_f32 v161,v161\nM v[40:43],a[84:87],v[192:195],v[40:43]\nbuffer_load_dwordx4 v240,s[48:51],s65 offen offset:1024 sc1 lds\nM v[40:43],a[116:119],v[208:211],v[40:43]\nv_exp_f32 v162,v162\n" \
"M v[48:51],a[88:91],v[192:195],v[48:51]\nv_exp_f32 v163,v163\nM v[48:51],a[120:123],v[208:211],v[48:51]\nv_exp_f32 v176,v176\nM v[56:59],a[92:95],v[192:195],v[56:59]\nbuffer_load_dwordx4 v240,s[48:51],s65 offen offset:2048 sc1 lds\nM v[56:59],a[124:127],v[208:211],v[56:59]\nv_exp_f32 v177,v177\n" \
"M a[192:195],a[208:211],v[192:195],a[192:195]\nv_exp_f32 v178,v178\nM a[192:195],a[208:211],v[208:211],a[192:195]\nv_exp_f32 v179,v179\nv_cvt_pk_bf16_f32 v192,v128,v129\nM v[4:7],a[64:67],v[196:199],v[4:7]\ns_barrier\nv_cvt_pk_bf16_f32 v193,v130,v131\n" \
"M v[4:7],a[96:99],v[212:215],v[4:7]\nbuffer_load_dwordx4 v240,s[48:51],s65 offen offset:3072 sc1 lds\ns_add_u32 m0,s64,32768\nM v[12:15],a[68:71],v[196:199],v[12:15]\nv_exp_f32 v140,v140\nv_cvt_pk_bf16_f32 v194,v144,v145\nM v[12:15],a[100:103],v[212:215],v[12:15]\nv_exp_f32 v141,v141\n" \
"M v[20:23],a[72:75],v[196:199],v[20:23]\nv_exp_f32 v142,v142\nv_cvt_pk_bf16_f32 v195,v146,v147\nM v[20:23],a[104:107],v[212:215],v[20:23]\nbuffer_load_dwordx4 v240,s[44:47],s66 offen offset:0 sc1 lds\nv_cvt_pk_bf16_f32 v208,v160,v161\nM v[28:31],a[76:79],v[196:199],v[28:31]\nv_exp_f32 v143,v143\n" \
"M v[28:31],a[108:111],v[212:215],v[28:31]\nv_exp_f32 v156,v156\nv_cvt_pk_bf16_f32 v209,v162,v163\nM v[36:39],a[80:83],v[196:199],v[36:39]\nv_exp_f32 v157,v157\nM v[36:39],a[112:115],v[212:215],v[36:39]\nbuffer_load_dwordx4 v240,s[44:47],s66 offen offset:1024 sc1 lds\nv_cvt_pk_bf16_f32 v210,v176,v177\n" \
"M v[44:47],a[84:87],v[196:199],v[44:47]\nv_exp_f32 v158,v158\nv_cvt_pk_bf16_f32 v211,v178,v179\nM v[44:47],a[116:119],v[212:215],v[44:47]\nv_exp_f32 v159,v159\nM v[52:55],a[88:91],v[196:199],v[52:55]\nv_exp_f32 v164,v164\nM v[52:55],a[120:123],v[212:215],v[52:55]\n" \
"buffer_load_dwordx4 v240,s[44:47],s66 offen offset:2048 sc1 lds\nM v[60:63],a[92:95],v[196:199],v[60:63]\nv_exp_f32 v165,v165\nM v[60:63],a[124:127],v[212:215],v[60:63]\nv_exp_f32 v166,v166\nM a[196:199],a[208:211],v[196:199],a[196:199]\nv_exp_f32 v167,v167\nv_cvt_pk_bf16_f32 v196,v132,v133\n" \
"M a[196:199],a[208:211],v[212:215],a[196:199]\nbuffer_load_dwordx4 v240,s[44:47],s66 offen offset:3072 sc1 lds\nM v[64:67],a[64:67],v[200:203],0\ns_add_u32 m0,s64,98304\nv_exp_f32 v180,v180\nM v[72:75],a[68:71],v[200:203],0\nv_exp_f32 v181,v181\nM v[80:83],a[72:75],v[200:203],0\n" \
"v_exp_f32 v182,v182\nv_cvt_pk_bf16_f32 v197,v134,v135\nM v[88:91],a[76:79],v[200:203],0\nv_exp_f32 v183,v183\nv_cvt_pk_bf16_f32 v198,v148,v149\nM v[96:99],a[80:83],v[200:203],0\nv_exp_f32 v168,v168\nM v[104:107],a[84:87],v[200:203],0\n" \
"v_exp_f32 v169,v169\nv_cvt_pk_bf16_f32 v199,v150,v151\nM v[112:115],a[88:91],v[200:203],0\nv_exp_f32 v170,v170\nM v[120:123],a[92:95],v[200:203],0\nv_exp_f32 v171,v171\nM a[200:203],a[208:211],v[200:203],0\nv_cvt_pk_bf16_f32 v200,v136,v137\n" \
"v_cvt_pk_bf16_f32 v201,v138,v139\nv_cvt_pk_bf16_f32 v202,v152,v153\nM v[68:71],a[64:67],v[204:207],0\nv_cvt_pk_bf16_f32 v203,v154,v155\nbuffer_load_dwordx4 v240,s[48:51],s66 offen offset:0 sc1 lds\nM v[76:79],a[68:71],v[204:207],0\nM v[84:87],a[72:75],v[204:207],0\nM v[92:95],a[76:79],v[204:207],0\n" \
"M v[100:103],a[80:83],v[204:207],0\nbuffer_load_dwordx4 v240,s[48:51],s66 offen offset:1024 sc1 lds\nM v[108:111],a[84:87],v[204:207],0\nM v[116:119],a[88:91],v[204:207],0\nM v[124:127],a[92:95],v[204:207],0\nbuffer_load_dwordx4 v240,s[48:51],s66 offen offset:2048 sc1 lds\nbuffer_load_dwordx4 v240,s[48:51],s66 offen offset:3072 sc1 lds\nM a[204:207],a[208:211],v[204:207],0\n" \
"s_cmp_eq_u32 s62,0\ns_cbranch_scc1 .Ltrans\ns_branch .Ldual\n.Lp23:\ns_cmp_eq_u32 s7,2\ns_cbranch_scc0 .Lpro3\n.Lpro2:\ns_barrier\n" \
"M v[64:67],a[0:3],a[128:131],0\nds_read_b128 a[64:67],v241 offset:0\nds_read_b128 a[96:99],v241 offset:8192\nM v[64:67],a[4:7],a[132:135],v[64:67]\nds_read_b128 a[68:71],v241 offset:1024\ns_mov_b32 m0,s64\nv_mov_b32 v250,139264\nM v[64:67],a[8:11],a[136:139],v[64:67]\n" \
"ds_read_b128 a[100:103],v241 offset:9216\nbuffer_load_dwordx4 v240,s[44:47],s36 offen offset:0 sc1 lds\nM v[64:67],a[12:15],a[140:143],v[64:67]\nds_read_b128 a[72:75],v241 offset:2048\nv_mov_b32 v184,0xff800000\nv_mov_b32 v185,0xff800000\nM v[68:71],a[12:15],a[156:159],0\nds_read_b128 a[12:15],v240 offset:19456\n" \
"v_mov_b32 v186,0xff800000\nv_mov_b32 v187,0xff800000\nM v[68:71],a[8:11],a[152:155],v[68:71]\nds_read_b128 a[8:11],v240 offset:18432\nv_max3_f32 v225,v64,v65,v66\nM v[68:71],a[4:7],a[148:151],v[68:71]\nds_read_b128 a[4:7],v240 offset:17408\nbuffer_load_dwordx4 v240,s[44:47],s36 offen offset:1024 sc1 lds\n" \
"M v[68:71],a[0:3],a[144:147],v[68:71]\nds_read_b128 a[0:3],v240 offset:16384\nM v[72:75],a[16:19],a[128:131],0\nds_read_b128 a[104:107],v241 offset:10240\nM v[72:75],a[20:23],a[132:135],v[72:75]\nds_read_b128 a[76:79],v241 offset:3072\nM v[72:75],a[24:27],a[136:139],v[72:75]\nds_read_b128 a[108:111],v241 offset:11264\n" \
"buffer_load_dwordx4 v240,s[44:47],s36 offen offset:2048 sc1 lds\nM v[72:75],a[28:31],a[140:143],v[72:75]\nv_max3_f32 v229,v68,v69,v70\nds_write_b128 v250,a[244:247]\nM v[76:79],a[28:31],a[156:159],0\nds_read_b128 a[28:31],v240 offset:23552\nM v[76:79],a[24:27],a[152:155],v[76:79]\nds_read_b128 a[24:27],v240 offset:22528\n" \
"M v[76:79],a[20:23],a[148:151],v[76:79]\nds_read_b128 a[20:23],v240 offset:21504\nv_max3_f32 v225,v225,v67,v72\nv_max3_f32 v225,v225,v73,v74\nM v[76:79],a[16:19],a[144:147],v[76:79]\nv_max_f32 v225,v225,v75\nv_cndmask_b32 v225,v224,v225,s[90:91]\nds_read_b128 a[16:19],v240 offset:20480\n" \
"M v[80:83],a[32:35],a[128:131],0\nds_read_b128 a[80:83],v241 offset:4096\nbuffer_load_dwordx4 v240,s[44:47],s36 offen offset:3072 sc1 lds\nM v[80:83],a[36:39],a[132:135],v[80:83]\nds_read_b128 a[112:115],v241 offset:12288\nv_max3_f32 v229,v229,v71,v76\nv_max3_f32 v229,v229,v77,v78\nM v[80:83],a[40:43],a[136:139],v[80:83]\n" \
"v_max_f32 v229,v229,v79\nv_cndmask_b32 v229,v228,v229,s[90:91]\nds_read_b128 a[84:87],v241 offset:5120\nM v[80:83],a[44:47],a[140:143],v[80:83]\nv_permlane16_swap_b32 v225,v229\nv_max_f32 v225,v225,v229\nv_mov_b32 v229,v225\nM v[84:87],a[44:47],a[156:159],0\n" \
"ds_read_b128 a[116:119],v241 offset:13312\nv_permlane32_swap_b32 v225,v229\nv_max_f32 v225,v225,v229\nM v[84:87],a[40:43],a[152:155],v[84:87]\nv_mov_b32 v229,v225\nds_read_b128 a[88:91],v241 offset:6144\nds_read_b128 a[120:123],v241 offset:14336\nM v[84:87],a[36:39],a[148:151],v[84:87]\n" \
"v_permlane16_swap_b32 v225,v229\nv_sub_f32 v224,0,v225\nv_sub_f32 v226,0,v225\nM v[84:87],a[32:35],a[144:147],v[84:87]\nv_sub_f32 v227,0,v225\nv_sub_f32 v225,0,v225\nv_sub_f32 v228,0,v229\nM v[88:91],a[48:51],a[128:131],0\n" \
"v_sub_f32 v230,0,v229\nv_sub_f32 v231,0,v229\nM v[88:91],a[52:55],a[132:135],v[88:91]\nv_sub_f32 v229,0,v229\nv_add_f32 v64,v64,v224\nv_add_f32 v65,v65,v224\nM v[88:91],a[56:59],a[136:139],v[88:91]\nv_add_f32 v66,v66,v224\n" \
"v_add_f32 v67,v67,v224\nv_add_f32 v72,v72,v224\nM v[88:91],a[60:63],a[140:143],v[88:91]\nv_exp_f32 v64,v64\nv_add_f32 v73,v73,v224\nM v[92:95],a[60:63],a[156:159],0\nv_exp_f32 v65,v65\nv_add_f32 v74,v74,v224\n" \
"M v[92:95],a[56:59],a[152:155],v[92:95]\nv_exp_f32 v66,v66\nv_add_f32 v75,v75,v224\nM v[92:95],a[52:55],a[148:151],v[92:95]\nv_exp_f32 v67,v67\nv_add_f32 v80,v80,v224\nM v[92:95],a[48:51],a[144:147],v[92:95]\nv_add_f32 v81,v81,v224\n" \
"v_add_f32 v82,v82,v224\ns_waitcnt lgkmcnt(0)\nM v[96:99],a[0:3],a[128:131],v[224:227]\nv_exp_f32 v72,v72\nv_add_f32 v83,v83,v224\nM v[96:99],a[4:7],a[132:135],v[96:99]\nv_exp_f32 v73,v73\nv_cvt_pk_bf16_f32 v64,v64,v65\n" \
"M v[96:99],a[8:11],a[136:139],v[96:99]\nv_exp_f32 v74,v74\nv_add_f32 v88,v88,v224\nM v[96:99],a[12:15],a[140:143],v[96:99]\nv_exp_f32 v75,v75\nv_add_f32 v89,v89,v224\nM v[100:103],a[12:15],a[156:159],v[228:231]\nv_exp_f32 v80,v80\n" \
"v_add_f32 v90,v90,v224\nM v[100:103],a[8:11],a[152:155],v[100:103]\nv_exp_f32 v81,v81\nM v[100:103],a[4:7],a[148:151],v[100:103]\nv_exp_f32 v82,v82\nv_add_f32 v91,v91,v224\nM v[100:103],a[0:3],a[144:147],v[100:103]\nv_exp_f32 v83,v83\n" \
"v_cvt_pk_bf16_f32 v65,v66,v67\nM v[108:111],a[28:31],a[156:159],v[228:231]\nv_exp_f32 v88,v88\nv_cvt_pk_bf16_f32 v66,v72,v73\nM v[108:111],a[24:27],a[152:155],v[108:111]\nv_exp_f32 v89,v89\nv_cvt_pk_bf16_f32 v67,v74,v75\nM v[108:111],a[20:23],a[148:151],v[108:111]\n" \
"v_exp_f32 v90,v90\nv_exp_f32 v91,v91\nv_cvt_pk_bf16_f32 v80,v80,v81\nM v[108:111],a[16:19],a[144:147],v[108:111]\nv_cvt_pk_bf16_f32 v81,v82,v83\nv_cvt_pk_bf16_f32 v82,v88,v89\nv_cvt_pk_bf16_f32 v83,v90,v91\nM v[0:3],a[64:67],v[64:67],0\n" \
"ds_read_b128 a[92:95],v241 offset:7168\nds_read_b128 a[124:127],v241 offset:15360\nv_add_f32 v68,v68,v228\nM v[0:3],a[96:99],v[80:83],v[0:3]\nv_add_f32 v69,v69,v228\nv_add_f32 v70,v70,v228\nv_add_f32 v71,v71,v228\nM v[8:11],a[68:71],v[64:67],0\n" \
"v_exp_f32 v68,v68\nv_add_f32 v76,v76,v228\nM v[8:11],a[100:103],v[80:83],v[8:11]\nv_exp_f32 v69,v69\nv_add_f32 v77,v77,v228\nM v[16:19],a[72:75],v[64:67],0\nv_exp_f32 v70,v70\nv_add_f32 v78,v78,v228\n" \
"M v[16:19],a[104:107],v[80:83],v[16:19]\nv_exp_f32 v71,v71\nM v[24:27],a[76:79],v[64:67],0\nv_add_f32 v79,v79,v228\nv_add_f32 v84,v84,v228\nv_add_f32 v85,v85,v228\nM v[24:27],a[108:111],v[80:83],v[24:27]\nv_exp_f32 v76,v76\n" \
"v_add_f32 v86,v86,v228\nM v[32:35],a[80:83],v[64:67],0\nv_exp_f32 v77,v77\nv_add_f32 v87,v87,v228\nM v[32:35],a[112:115],v[80:83],v[32:35]\nv_exp_f32 v78,v78\nv_cvt_pk_bf16_f32 v68,v68,v69\nM v[40:43],a[84:87],v[64:67],0\n" \
"v_exp_f32 v79,v79\nv_add_f32 v92,v92,v228\nM v[40:43],a[116:119],v[80:83],v[40:43]\nv_exp_f32 v84,v84\nv_add_f32 v93,v93,v228\nM v[48:51],a[88:91],v[64:67],0\nv_exp_f32 v85,v85\nv_add_f32 v94,v94,v228\n" \
"M v[48:51],a[120:123],v[80:83],v[48:51]\nv_exp_f32 v86,v86\ns_waitcnt lgkmcnt(0)\nM v[56:59],a[92:95],v[64:67],0\nv_exp_f32 v87,v87\nv_add_f32 v95,v95,v228\nM v[56:59],a[124:127],v[80:83],v[56:59]\nv_cvt_pk_bf16_f32 v69,v70,v71\n" \
"v_exp_f32 v92,v92\nM a[192:195],a[208:211],v[64:67],0\nv_exp_f32 v93,v93\nv_exp_f32 v94,v94\nv_exp_f32 v95,v95\nv_cvt_pk_bf16_f32 v70,v76,v77\nv_cvt_pk_bf16_f32 v71,v78,v79\nv_cvt_pk_bf16_f32 v84,v84,v85\n" \
"M a[192:195],a[208:211],v[80:83],a[192:195]\nv_cvt_pk_bf16_f32 v85,v86,v87\nv_cvt_pk_bf16_f32 v86,v92,v93\nv_cvt_pk_bf16_f32 v87,v94,v95\nM v[4:7],a[64:67],v[68:71],0\nds_read_b128 a[0:3],v240 offset:32768\nds_read_b128 a[4:7],v240 offset:33792\nM v[4:7],a[96:99],v[84:87],v[4:7]\n" \
"ds_read_b128 a[8:11],v240 offset:34816\nv_cndmask_b32 v96,v96,v252,s[82:83]\nv_cndmask_b32 v97,v97,v252,s[84:85]\nM v[12:15],a[68:71],v[68:71],0\nds_read_b128 a[12:15],v240 offset:35840\nv_cndmask_b32 v98,v98,v252,s[86:87]\nv_cndmask_b32 v99,v99,v252,s[88:89]\nM v[12:15],a[100:103],v[84:87],v[12:15]\n" \
"ds_read_b128 a[16:19],v240 offset:36864\nv_exp_f32 v96,v96\nM v[20:23],a[72:75],v[68:71],0\nds_read_b128 a[20:23],v240 offset:37888\nv_exp_f32 v97,v97\nM v[20:23],a[104:107],v[84:87],v[20:23]\nds_read_b128 a[24:27],v240 offset:38912\nv_exp_f32 v98,v98\n" \
"M v[28:31],a[76:79],v[68:71],0\nds_read_b128 a[28:31],v240 offset:39936\nv_exp_f32 v99,v99\nM v[28:31],a[108:111],v[84:87],v[28:31]\nds_read_b128 a[32:35],v240 offset:40960\nv_cvt_pk_bf16_f32 v96,v96,v97\nv_cvt_pk_bf16_f32 v97,v98,v99\nM v[36:39],a[80:83],v[68:71],0\n" \
"ds_read_b128 a[36:39],v240 offset:41984\nv_mov_b32 v98,0\nv_mov_b32 v99,0\nM v[36:39],a[112:115],v[84:87],v[36:39]\nds_read_b128 a[40:43],v240 offset:43008\nv_exp_f32 v100,v100\nM v[44:47],a[84:87],v[68:71],0\nds_read_b128 a[44:47],v240 offset:44032\n" \
"v_exp_f32 v101,v101\nM v[44:47],a[116:119],v[84:87],v[44:47]\nds_read_b128 a[48:51],v240 offset:45056\nv_cndmask_b32 v108,v108,v252,s[82:83]\nv_cndmask_b32 v109,v109,v252,s[84:85]\nM v[52:55],a[88:91],v[68:71],0\nbuffer_load_dword v248,v251,s[52:55],s80 offen sc1\nM v[52:55],a[120:123],v[84:87],v[52:55]\n" \
"ds_read_b128 a[52:55],v240 offset:46080\nds_read_b128 a[56:59],v240 offset:47104\nv_cndmask_b32 v110,v110,v252,s[86:87]\nM v[60:63],a[92:95],v[68:71],0\nds_read_b128 a[60:63],v240 offset:48128\nv_cndmask_b32 v111,v111,v252,s[88:89]\nv_cvt_pk_bf16_f32 v100,v100,v101\nM v[60:63],a[124:127],v[84:87],v[60:63]\n" \
"ds_read_b128 a[64:67],v241 offset:16384\nv_exp_f32 v102,v102\nM a[196:199],a[208:211],v[68:71],0\nds_read_b128 a[68:71],v241 offset:17408\nv_exp_f32 v103,v103\nM a[196:199],a[208:211],v[84:87],a[196:199]\nds_read_b128 a[72:75],v241 offset:18432\nv_cvt_pk_bf16_f32 v101,v102,v103\n" \
"s_waitcnt lgkmcnt(8)\nM v[128:131],a[0:3],a[128:131],v[224:227]\nds_read_b128 a[76:79],v241 offset:19456\nv_exp_f32 v108,v108\nM v[128:131],a[4:7],a[132:135],v[128:131]\nds_read_b128 a[80:83],v241 offset:20480\nv_exp_f32 v109,v109\nM v[128:131],a[8:11],a[136:139],v[128:131]\n" \
"ds_read_b128 a[84:87],v241 offset:21504\nv_exp_f32 v110,v110\nM v[128:131],a[12:15],a[140:143],v[128:131]\nds_read_b128 a[88:91],v241 offset:22528\nv_exp_f32 v111,v111\nM v[132:135],a[12:15],a[156:159],v[228:231]\nds_read_b128 a[92:95],v241 offset:23552\nv_cvt_pk_bf16_f32 v102,v108,v109\n" \
"v_cvt_pk_bf16_f32 v103,v110,v111\nM v[132:135],a[8:11],a[152:155],v[132:135]\nds_read_b128 a[8:11],v240 offset:51200\nv_exp_f32 v128,v128\nM v[132:135],a[4:7],a[148:151],v[132:135]\nds_read_b128 a[4:7],v240 offset:50176\nv_exp_f32 v129,v129\nM v[132:135],a[0:3],a[144:147],v[132:135]\n" \
"ds_read_b128 a[0:3],v240 offset:49152\nv_cvt_pk_bf16_f32 v192,v128,v129\nM v[144:147],a[16:19],a[128:131],v[224:227]\nds_read_b128 a[12:15],v240 offset:52224\nv_exp_f32 v130,v130\nM v[144:147],a[20:23],a[132:135],v[144:147]\nv_exp_f32 v131,v131\nds_read_b128 a[96:99],v241 offset:40960\n" \
"M v[144:147],a[24:27],a[136:139],v[144:147]\nv_cvt_pk_bf16_f32 v193,v130,v131\nv_exp_f32 v132,v132\nM v[144:147],a[28:31],a[140:143],v[144:147]\nv_exp_f32 v133,v133\nds_read_b128 a[100:103],v241 offset:41984\nM v[148:151],a[28:31],a[156:159],v[228:231]\nds_read_b128 a[28:31],v240 offset:56320\n" \
"v_exp_f32 v134,v134\nM v[148:151],a[24:27],a[152:155],v[148:151]\nds_read_b128 a[24:27],v240 offset:55296\nv_exp_f32 v135,v135\nM v[148:151],a[20:23],a[148:151],v[148:151]\nds_read_b128 a[20:23],v240 offset:54272\nv_cvt_pk_bf16_f32 v196,v132,v133\nv_cvt_pk_bf16_f32 v197,v134,v135\n" \
"M v[148:151],a[16:19],a[144:147],v[148:151]\nds_read_b128 a[16:19],v240 offset:53248\nv_exp_f32 v144,v144\nM v[160:163],a[32:35],a[128:131],v[224:227]\nv_exp_f32 v145,v145\nds_read_b128 v[64:67],v250\nM v[160:163],a[36:39],a[132:135],v[160:163]\nv_exp_f32 v146,v146\n" \
"v_cvt_pk_bf16_f32 v194,v144,v145\nM v[160:163],a[40:43],a[136:139],v[160:163]\nv_exp_f32 v147,v147\ns_waitcnt lgkmcnt(6)\nM v[160:163],a[44:47],a[140:143],v[160:163]\nv_cvt_pk_bf16_f32 v195,v146,v147\nds_read_b128 v[68:71],v250\nM v[164:167],a[44:47],a[156:159],v[228:231]\n" \
"ds_read_b128 a[44:47],v240 offset:60416\nv_exp_f32 v148,v148\nM v[164:167],a[40:43],a[152:155],v[164:167]\nds_read_b128 a[40:43],v240 offset:59392\nv_exp_f32 v149,v149\nM v[164:167],a[36:39],a[148:151],v[164:167]\nds_read_b128 a[36:39],v240 offset:58368\nv_exp_f32 v150,v150\n" \
"M v[164:167],a[32:35],a[144:147],v[164:167]\nds_read_b128 a[32:35],v240 offset:57344\nv_exp_f32 v151,v151\nM v[176:179],a[48:51],a[128:131],v[224:227]\nv_cvt_pk_bf16_f32 v198,v148,v149\nv_cvt_pk_bf16_f32 v199,v150,v151\nds_read_b128 v[72:75],v250\nM v[176:179],a[52:55],a[132:135],v[176:179]\n" \
"v_exp_f32 v160,v160\nds_read_b128 v[76:79],v250\nM v[176:179],a[56:59],a[136:139],v[176:179]\nv_exp_f32 v161,v161\nds_read_b128 v[80:83],v250\nM v[176:179],a[60:63],a[140:143],v[176:179]\nv_exp_f32 v162,v162\nv_cvt_pk_bf16_f32 v208,v160,v161\n" \
"M v[180:183],a[60:63],a[156:159],v[228:231]\nv_exp_f32 v163,v163\nds_read_b128 a[60:63],v240 offset:64512\nM v[180:183],a[56:59],a[152:155],v[180:183]\nv_cvt_pk_bf16_f32 v209,v162,v163\nds_read_b128 a[56:59],v240 offset:63488\nds_read_b128 v[84:87],v250\nM v[180:183],a[52:55],a[148:151],v[180:183]\n" \
"ds_read_b128 a[52:55],v240 offset:62464\nv_exp_f32 v164,v164\nM v[180:183],a[48:51],a[144:147],v[180:183]\nds_read_b128 a[48:51],v240 offset:61440\nv_exp_f32 v165,v165\nM v[0:3],a[64:67],v[96:99],v[0:3]\nv_exp_f32 v166,v166\nM v[8:11],a[68:71],v[96:99],v[8:11]\n" \
"v_exp_f32 v167,v167\nv_cvt_pk_bf16_f32 v212,v164,v165\nM v[16:19],a[72:75],v[96:99],v[16:19]\nv_cvt_pk_bf16_f32 v213,v166,v167\nv_exp_f32 v176,v176\nM v[24:27],a[76:79],v[96:99],v[24:27]\nv_exp_f32 v177,v177\nds_read_b128 v[88:91],v250\n" \
"M v[32:35],a[80:83],v[96:99],v[32:35]\nv_exp_f32 v178,v178\nv_cvt_pk_bf16_f32 v210,v176,v177\nM v[40:43],a[84:87],v[96:99],v[40:43]\nv_exp_f32 v179,v179\nds_read_b128 v[92:95],v250\nM v[48:51],a[88:91],v[96:99],v[48:51]\nv_cvt_pk_bf16_f32 v211,v178,v179\n" \
"v_exp_f32 v180,v180\nM v[56:59],a[92:95],v[96:99],v[56:59]\nv_exp_f32 v181,v181\nds_read_b128 v[104:107],v250\nM a[192:195],a[208:211],v[96:99],a[192:195]\nv_exp_f32 v182,v182\nv_cvt_pk_bf16_f32 v214,v180,v181\nM v[4:7],a[64:67],v[100:103],v[4:7]\n" \
"v_exp_f32 v183,v183\nds_read_b128 a[64:67],v241 offset:32768\nM v[12:15],a[68:71],v[100:103],v[12:15]\nv_cvt_pk_bf16_f32 v215,v182,v183\nds_read_b128 a[68:71],v241 offset:33792\nds_read_b128 v[96:99],v250\nM v[20:23],a[72:75],v[100:103],v[20:23]\nds_read_b128 a[72:75],v241 offset:34816\n" \
"s_waitcnt vmcnt(0)\nv_cmp_eq_u32 vcc,0,v248\nM v[28:31],a[76:79],v[100:103],v[28:31]\ns_barrier\nds_read_b128 v[108:111],v250\nM v[36:39],a[80:83],v[100:103],v[36:39]\nds_read_b128 v[112:115],v250\ns_cbranch_vccnz .Lps4\n" \
".Lpb4:\ns_add_u32 m0,s64,65536\nM v[44:47],a[84:87],v[100:103],v[44:47]\nds_read_b128 v[116:119],v250\nbuffer_load_dwordx4 v240,s[48:51],s36 offen offset:0 sc1 lds\nM v[52:55],a[88:91],v[100:103],v[52:55]\nds_read_b128 v[120:123],v250\nM v[60:63],a[92:95],v[100:103],v[60:63]\n" \
"ds_read_b128 v[124:127],v250\nM a[196:199],a[208:211],v[100:103],a[196:199]\nds_read_b128 v[100:103],v250\nM v[136:139],a[0:3],a[160:163],0\nds_read_b128 a[200:203],v250\nbuffer_load_dwordx4 v240,s[48:51],s36 offen offset:1024 sc1 lds\nM v[136:139],a[4:7],a[164:167],v[136:139]\nds_read_b128 a[204:207],v250\n" \
"v_add_u32 v250,0xc000,v241\nM v[136:139],a[8:11],a[168:171],v[136:139]\nds_read_b128 a[104:107],v241 offset:43008\nM v[136:139],a[12:15],a[172:175],v[136:139]\nds_read_b128 a[76:79],v241 offset:35840\nM v[140:143],a[12:15],a[188:191],0\nds_read_b128 a[108:111],v241 offset:44032\nbuffer_load_dwordx4 v240,s[48:51],s36 offen offset:2048 sc1 lds\n" \
"M v[140:143],a[8:11],a[184:187],v[140:143]\nds_read_b128 a[80:83],v241 offset:36864\nM v[140:143],a[4:7],a[180:183],v[140:143]\nds_read_b128 a[112:115],v241 offset:45056\nv_max3_f32 v233,v136,v137,v138\nM v[140:143],a[0:3],a[176:179],v[140:143]\nds_read_b128 a[84:87],v241 offset:37888\ns_waitcnt lgkmcnt(11)\n" \
"M v[152:155],a[16:19],a[160:163],0\nds_read_b128 a[116:119],v241 offset:46080\nbuffer_load_dwordx4 v240,s[48:51],s36 offen offset:3072 sc1 lds\nM v[152:155],a[20:23],a[164:167],v[152:155]\nds_read_b128 a[88:91],v241 offset:38912\ns_add_u32 m0,s64,16384\nv_max3_f32 v237,v140,v141,v142\nM v[152:155],a[24:27],a[168:171],v[152:155]\n" \
"ds_read_b128 a[120:123],v241 offset:47104\nM v[152:155],a[28:31],a[172:175],v[152:155]\nds_read_b128 a[92:95],v241 offset:39936\nM v[156:159],a[28:31],a[188:191],0\nds_read_b128 a[124:127],v241 offset:48128\nbuffer_load_dwordx4 v240,s[44:47],s65 offen offset:0 sc1 lds\nM v[156:159],a[24:27],a[184:187],v[156:159]\nM v[156:159],a[20:23],a[180:183],v[156:159]\n" \
"M v[156:159],a[16:19],a[176:179],v[156:159]\nM v[128:131],a[0:3],a[128:131],v[224:227]\nv_max3_f32 v233,v233,v139,v152\nv_max3_f32 v233,v233,v153,v154\nv_max_f32 v233,v233,v155\nM v[128:131],a[4:7],a[132:135],v[128:131]\nv_cndmask_b32 v233,v232,v233,s[90:91]\nbuffer_load_dwordx4 v240,s[44:47],s65 offen offset:1024 sc1 lds\n" \
"M v[128:131],a[8:11],a[136:139],v[128:131]\nv_max3_f32 v237,v237,v143,v156\nv_max3_f32 v237,v237,v157,v158\nv_max_f32 v237,v237,v159\nM v[128:131],a[12:15],a[140:143],v[128:131]\nv_cndmask_b32 v237,v236,v237,s[90:91]\nM v[132:135],a[12:15],a[156:159],v[228:231]\nM v[132:135],a[8:11],a[152:155],v[132:135]\n" \
"v_permlane16_swap_b32 v233,v237\nv_max_f32 v233,v233,v237\nv_mov_b32 v237,v233\nM v[132:135],a[4:7],a[148:151],v[132:135]\nbuffer_load_dwordx4 v240,s[44:47],s65 offen offset:2048 sc1 lds\nv_permlane32_swap_b32 v233,v237\nM v[132:135],a[0:3],a[144:147],v[132:135]\nv_max_f32 v233,v233,v237\n" \
"v_mov_b32 v237,v233\nds_read_b128 a[0:3],v240 offset:0\nM v[144:147],a[16:19],a[128:131],v[224:227]\nv_permlane16_swap_b32 v233,v237\nv_sub_f32 v232,0,v233\nv_sub_f32 v234,0,v233\nM v[144:147],a[20:23],a[132:135],v[144:147]\nv_sub_f32 v235,0,v233\n" \
"v_sub_f32 v233,0,v233\nv_sub_f32 v236,0,v237\nM v[144:147],a[24:27],a[136:139],v[144:147]\nv_sub_f32 v238,0,v237\nv_sub_f32 v239,0,v237\nv_sub_f32 v237,0,v237\nM v[144:147],a[28:31],a[140:143],v[144:147]\nds_read_b128 a[4:7],v240 offset:1024\n" \
"ds_read_b128 a[8:11],v240 offset:2048\nv_add_f32 v136,v136,v232\nM v[148:151],a[28:31],a[156:159],v[228:231]\nds_read_b128 a[12:15],v240 offset:3072\nbuffer_load_dwordx4 v240,s[44:47],s65 offen offset:3072 sc1 lds\nM v[148:151],a[24:27],a[152:155],v[148:151]\ns_add_u32 m0,s64,81920\nv_add_f32 v137,v137,v232\n" \
"v_add_f32 v138,v138,v232\nM v[148:151],a[20:23],a[148:151],v[148:151]\nv_add_f32 v139,v139,v232\nv_add_f32 v152,v152,v232\nv_add_f32 v153,v153,v232\nM v[148:151],a[16:19],a[144:147],v[148:151]\nds_read_b128 a[16:19],v240 offset:4096\nds_read_b128 a[20:23],v240 offset:5120\n" \
"v_add_f32 v154,v154,v232\nM v[160:163],a[32:35],a[128:131],v[224:227]\nds_read_b128 a[24:27],v240 offset:6144\nv_add_f32 v155,v155,v232\nM v[160:163],a[36:39],a[132:135],v[160:163]\nds_read_b128 a[28:31],v240 offset:7168\nbuffer_load_dwordx4 v240,s[48:51],s65 offen offset:0 sc1 lds\nM v[160:163],a[40:43],a[136:139],v[160:163]\n" \
"v_exp_f32 v136,v136\nv_add_f32 v140,v140,v236\nM v[160:163],a[44:47],a[140:143],v[160:163]\nv_exp_f32 v137,v137\nv_add_f32 v141,v141,v236\nM v[164:167],a[44:47],a[156:159],v[228:231]\nv_exp_f32 v138,v138\nv_add_f32 v142,v142,v236\n" \
"M v[164:167],a[40:43],a[152:155],v[164:167]\nbuffer_load_dwordx4 v240,s[48:51],s65 offen offset:1024 sc1 lds\nv_add_f32 v143,v143,v236\nM v[164:167],a[36:39],a[148:151],v[164:167]\nv_exp_f32 v139,v139\nv_add_f32 v156,v156,v236\nM v[164:167],a[32:35],a[144:147],v[164:167]\nv_exp_f32 v152,v152\n" \
"v_add_f32 v157,v157,v236\nM v[176:179],a[48:51],a[128:131],v[224:227]\nv_exp_f32 v153,v153\nv_add_f32 v158,v158,v236\nM v[176:179],a[52:55],a[132:135],v[176:179]\nbuffer_load_dwordx4 v240,s[48:51],s65 offen offset:2048 sc1 lds\nv_add_f32 v159,v159,v236\nM v[176:179],a[56:59],a[136:139],v[176:179]\n" \
"v_exp_f32 v154,v154\nv_cvt_pk_bf16_f32 v200,v136,v137\nM v[176:179],a[60:63],a[140:143],v[176:179]\nv_exp_f32 v155,v155\nv_cvt_pk_bf16_f32 v201,v138,v139\nM v[180:183],a[60:63],a[156:159],v[228:231]\nv_exp_f32 v128,v128\nM v[180:183],a[56:59],a[152:155],v[180:183]\n" \
"buffer_load_dwordx4 v240,s[48:51],s65 offen offset:3072 sc1 lds\ns_add_u32 m0,s64,32768\nM v[180:183],a[52:55],a[148:151],v[180:183]\nv_exp_f32 v129,v129\nv_cvt_pk_bf16_f32 v202,v152,v153\nM v[180:183],a[48:51],a[144:147],v[180:183]\nv_exp_f32 v130,v130\nv_cvt_pk_bf16_f32 v203,v154,v155\n" \
"M v[168:171],a[32:35],a[160:163],v[232:235]\nv_exp_f32 v131,v131\nM v[168:171],a[36:39],a[164:167],v[168:171]\nbuffer_load_dwordx4 v240,s[44:47],s66 offen offset:0 sc1 lds\nM v[168:171],a[40:43],a[168:171],v[168:171]\nv_exp_f32 v132,v132\nM v[168:171],a[44:47],a[172:175],v[168:171]\nv_exp_f32 v133,v133\n" \
"M v[172:175],a[44:47],a[188:191],v[236:239]\nv_exp_f32 v134,v134\nM v[172:175],a[40:43],a[184:187],v[172:175]\nbuffer_load_dwordx4 v240,s[44:47],s66 offen offset:1024 sc1 lds\nM v[172:175],a[36:39],a[180:183],v[172:175]\nv_exp_f32 v135,v135\nM v[172:175],a[32:35],a[176:179],v[172:175]\nds_read_b128 a[32:35],v240 offset:8192\n" \
"ds_read_b128 a[36:39],v240 offset:9216\nv_cndmask_b32 v168,v168,v252,s[82:83]\nM v[188:191],a[60:63],a[188:191],v[236:239]\ns_waitcnt lgkmcnt(2)\ns_barrier\nds_read_b128 a[40:43],v240 offset:10240\nM v[188:191],a[56:59],a[184:187],v[188:191]\nds_read_b128 a[44:47],v240 offset:11264\n" \
"v_cndmask_b32 v169,v169,v252,s[84:85]\nM v[188:191],a[52:55],a[180:183],v[188:191]\nbuffer_load_dwordx4 v240,s[44:47],s66 offen offset:2048 sc1 lds\nv_cndmask_b32 v170,v170,v252,s[86:87]\nM v[188:191],a[48:51],a[176:179],v[188:191]\nds_read_b128 a[48:51],v240 offset:12288\nds_read_b128 a[52:55],v240 offset:13312\nv_cndmask_b32 v171,v171,v252,s[88:89]\n" \
"M v[0:3],a[64:67],v[192:195],v[0:3]\nds_read_b128 a[56:59],v240 offset:14336\nv_exp_f32 v144,v144\nM v[0:3],a[96:99],v[208:211],v[0:3]\nds_read_b128 a[60:63],v240 offset:15360\nv_exp_f32 v145,v145\nM v[8:11],a[68:71],v[192:195],v[8:11]\nbuffer_load_dwordx4 v240,s[44:47],s66 offen offset:3072 sc1 lds\n" \
"s_add_u32 m0,s64,98304\nM v[8:11],a[100:103],v[208:211],v[8:11]\nv_exp_f32 v146,v146\nv_cndmask_b32 v188,v188,v252,s[82:83]\nM v[16:19],a[72:75],v[192:195],v[16:19]\nv_exp_f32 v147,v147\nv_cndmask_b32 v189,v189,v252,s[84:85]\nM v[16:19],a[104:107],v[208:211],v[16:19]\n" \
"v_exp_f32 v148,v148\nv_cndmask_b32 v190,v190,v252,s[86:87]\nM v[24:27],a[76:79],v[192:195],v[24:27]\nv_exp_f32 v149,v149\nv_cndmask_b32 v191,v191,v252,s[88:89]\nM v[24:27],a[108:111],v[208:211],v[24:27]\nv_exp_f32 v150,v150\nM v[32:35],a[80:83],v[192:195],v[32:35]\n" \
"v_exp_f32 v151,v151\nM v[32:35],a[112:115],v[208:211],v[32:35]\nv_exp_f32 v160,v160\nM v[40:43],a[84:87],v[192:195],v[40:43]\nv_exp_f32 v161,v161\nM v[40:43],a[116:119],v[208:211],v[40:43]\nv_exp_f32 v162,v162\nM v[48:51],a[88:91],v[192:195],v[48:51]\n" \
"v_exp_f32 v163,v163\nM v[48:51],a[120:123],v[208:211],v[48:51]\nv_exp_f32 v176,v176\nM v[56:59],a[92:95],v[192:195],v[56:59]\nv_exp_f32 v177,v177\nM v[56:59],a[124:127],v[208:211],v[56:59]\nv_exp_f32 v178,v178\nM a[192:195],a[208:211],v[192:195],a[192:195]\n" \
"v_exp_f32 v179,v179\nv_cvt_pk_bf16_f32 v192,v128,v129\nM a[192:195],a[208:211],v[208:211],a[192:195]\nv_exp_f32 v140,v140\nv_cvt_pk_bf16_f32 v193,v130,v131\nM v[4:7],a[64:67],v[196:199],v[4:7]\nv_exp_f32 v141,v141\nv_cvt_pk_bf16_f32 v194,v144,v145\n" \
"M v[4:7],a[96:99],v[212:215],v[4:7]\nv_exp_f32 v142,v142\nv_cvt_pk_bf16_f32 v195,v146,v147\nM v[12:15],a[68:71],v[196:199],v[12:15]\nv_exp_f32 v143,v143\nv_cvt_pk_bf16_f32 v208,v160,v161\nM v[12:15],a[100:103],v[212:215],v[12:15]\nv_exp_f32 v156,v156\n" \
"v_cvt_pk_bf16_f32 v209,v162,v163\nM v[20:23],a[72:75],v[196:199],v[20:23]\nv_exp_f32 v157,v157\nM v[20:23],a[104:107],v[212:215],v[20:23]\nv_exp_f32 v158,v158\nv_cvt_pk_bf16_f32 v210,v176,v177\nM v[28:31],a[76:79],v[196:199],v[28:31]\nv_exp_f32 v159,v159\n" \
"v_cvt_pk_bf16_f32 v211,v178,v179\nM v[28:31],a[108:111],v[212:215],v[28:31]\nv_exp_f32 v164,v164\nM v[36:39],a[80:83],v[196:199],v[36:39]\nv_exp_f32 v165,v165\nM v[36:39],a[112:115],v[212:215],v[36:39]\nv_exp_f32 v166,v166\nM v[44:47],a[84:87],v[196:199],v[44:47]\n" \
"v_exp_f32 v167,v167\nM v[44:47],a[116:119],v[212:215],v[44:47]\nv_exp_f32 v180,v180\nM v[52:55],a[88:91],v[196:199],v[52:55]\nv_exp_f32 v181,v181\nM v[52:55],a[120:123],v[212:215],v[52:55]\nv_exp_f32 v182,v182\nM v[60:63],a[92:95],v[196:199],v[60:63]\n" \
"v_exp_f32 v183,v183\nM v[60:63],a[124:127],v[212:215],v[60:63]\nv_exp_f32 v168,v168\nM a[196:199],a[208:211],v[196:199],a[196:199]\nv_exp_f32 v169,v169\nv_exp_f32 v170,v170\nv_exp_f32 v171,v171\nv_cvt_pk_bf16_f32 v196,v132,v133\n" \
"v_cvt_pk_bf16_f32 v197,v134,v135\nv_cvt_pk_bf16_f32 v198,v148,v149\nv_cvt_pk_bf16_f32 v199,v150,v151\nbuffer_load_dwordx4 v240,s[48:51],s66 offen offset:0 sc1 lds\nbuffer_load_dwordx4 v240,s[48:51],s66 offen offset:1024 sc1 lds\nbuffer_load_dwordx4 v240,s[48:51],s66 offen offset:2048 sc1 lds\nbuffer_load_dwordx4 v240,s[48:51],s66 offen offset:3072 sc1 lds\nM a[196:199],a[208:211],v[212:215],a[196:199]\n" \
"s_cmp_eq_u32 s62,0\ns_cbranch_scc1 .Ltrans\ns_branch .Ldual\n.Lpro3:\ns_barrier\nM v[64:67],a[0:3],a[128:131],0\nds_read_b128 a[64:67],v241 offset:0\nds_read_b128 a[96:99],v241 offset:8192\n" \
"M v[64:67],a[4:7],a[132:135],v[64:67]\nds_read_b128 a[68:71],v241 offset:1024\ns_mov_b32 m0,s64\nv_mov_b32 v250,143360\nM v[64:67],a[8:11],a[136:139],v[64:67]\nds_read_b128 a[100:103],v241 offset:9216\nbuffer_load_dwordx4 v240,s[44:47],s36 offen offset:0 sc1 lds\nM v[64:67],a[12:15],a[140:143],v[64:67]\n" \
"ds_read_b128 a[72:75],v241 offset:2048\nv_mov_b32 v168,0\nM v[68:71],a[12:15],a[156:159],0\nds_read_b128 a[12:15],v240 offset:19456\nv_mov_b32 v169,0\nv_mov_b32 v170,0\nM v[68:71],a[8:11],a[152:155],v[68:71]\nds_read_b128 a[8:11],v240 offset:18432\n" \
"v_max3_f32 v225,v64,v65,v66\nv_mov_b32 v171,0\nM v[68:71],a[4:7],a[148:151],v[68:71]\nds_read_b128 a[4:7],v240 offset:17408\nv_mov_b32 v172,0xff800000\nM v[68:71],a[0:3],a[144:147],v[68:71]\nds_read_b128 a[0:3],v240 offset:16384\nbuffer_load_dwordx4 v240,s[44:47],s36 offen offset:1024 sc1 lds\n" \
"M v[72:75],a[16:19],a[128:131],0\nds_read_b128 a[104:107],v241 offset:10240\nv_mov_b32 v173,0xff800000\nv_mov_b32 v174,0xff800000\nM v[72:75],a[20:23],a[132:135],v[72:75]\nds_read_b128 a[76:79],v241 offset:3072\nv_max3_f32 v229,v68,v69,v70\nv_mov_b32 v175,0xff800000\n" \
"M v[72:75],a[24:27],a[136:139],v[72:75]\nds_read_b128 a[108:111],v241 offset:11264\nv_mov_b32 v184,0xff800000\nM v[72:75],a[28:31],a[140:143],v[72:75]\nds_read_b128 a[80:83],v241 offset:4096\nbuffer_load_dwordx4 v240,s[44:47],s36 offen offset:2048 sc1 lds\nM v[76:79],a[28:31],a[156:159],0\nds_read_b128 a[28:31],v240 offset:23552\n" \
"v_mov_b32 v185,0xff800000\nv_mov_b32 v186,0xff800000\nM v[76:79],a[24:27],a[152:155],v[76:79]\nds_read_b128 a[24:27],v240 offset:22528\nv_max3_f32 v225,v225,v67,v72\nM v[76:79],a[20:23],a[148:151],v[76:79]\nv_max3_f32 v225,v225,v73,v74\nv_max_f32 v225,v225,v75\n" \
"v_cndmask_b32 v225,v224,v225,s[90:91]\nM v[76:79],a[16:19],a[144:147],v[76:79]\nds_read_b128 a[16:19],v240 offset:20480\nds_read_b128 a[20:23],v240 offset:21504\nv_mov_b32 v187,0xff800000\nM v[80:83],a[32:35],a[128:131],0\nds_read_b128 a[112:115],v241 offset:12288\nbuffer_load_dwordx4 v240,s[44:47],s36 offen offset:3072 sc1 lds\n" \
"M v[80:83],a[36:39],a[132:135],v[80:83]\nds_read_b128 a[84:87],v241 offset:5120\nv_max3_f32 v229,v229,v71,v76\nM v[80:83],a[40:43],a[136:139],v[80:83]\nv_max3_f32 v229,v229,v77,v78\nv_max_f32 v229,v229,v79\nv_cndmask_b32 v229,v228,v229,s[90:91]\nM v[80:83],a[44:47],a[140:143],v[80:83]\n" \
"ds_read_b128 a[116:119],v241 offset:13312\nv_permlane16_swap_b32 v225,v229\nv_max_f32 v225,v225,v229\nM v[84:87],a[44:47],a[156:159],0\nv_mov_b32 v229,v225\nds_read_b128 a[44:47],v240 offset:27648\nM v[84:87],a[40:43],a[152:155],v[84:87]\nv_permlane32_swap_b32 v225,v229\n" \
"v_max_f32 v225,v225,v229\nv_mov_b32 v229,v225\nM v[84:87],a[36:39],a[148:151],v[84:87]\nds_read_b128 a[36:39],v240 offset:25600\nv_permlane16_swap_b32 v225,v229\nv_sub_f32 v224,0,v225\nM v[84:87],a[32:35],a[144:147],v[84:87]\nv_sub_f32 v226,0,v225\n" \
"v_sub_f32 v227,0,v225\nv_sub_f32 v225,0,v225\nM v[88:91],a[48:51],a[128:131],0\nv_sub_f32 v228,0,v229\nv_sub_f32 v230,0,v229\nM v[88:91],a[52:55],a[132:135],v[88:91]\nv_sub_f32 v231,0,v229\nv_sub_f32 v229,0,v229\n" \
"ds_read_b128 a[32:35],v240 offset:24576\nM v[88:91],a[56:59],a[136:139],v[88:91]\nds_read_b128 a[40:43],v240 offset:26624\nv_add_f32 v64,v64,v224\nv_add_f32 v65,v65,v224\nM v[88:91],a[60:63],a[140:143],v[88:91]\nv_add_f32 v66,v66,v224\nv_add_f32 v67,v67,v224\n" \
"M v[92:95],a[60:63],a[156:159],0\nds_read_b128 a[60:63],v240 offset:31744\nv_exp_f32 v64,v64\nM v[92:95],a[56:59],a[152:155],v[92:95]\nds_read_b128 a[56:59],v240 offset:30720\nv_exp_f32 v65,v65\nM v[92:95],a[52:55],a[148:151],v[92:95]\nds_read_b128 a[52:55],v240 offset:29696\n" \
"v_add_f32 v72,v72,v224\nM v[92:95],a[48:51],a[144:147],v[92:95]\nds_read_b128 a[48:51],v240 offset:28672\nv_add_f32 v73,v73,v224\ns_waitcnt lgkmcnt(6)\nM v[96:99],a[0:3],a[128:131],v[224:227]\nv_exp_f32 v66,v66\nv_add_f32 v74,v74,v224\n" \
"M v[96:99],a[4:7],a[132:135],v[96:99]\nv_exp_f32 v67,v67\nv_add_f32 v75,v75,v224\nM v[96:99],a[8:11],a[136:139],v[96:99]\nv_add_f32 v80,v80,v224\nv_add_f32 v81,v81,v224\nM v[96:99],a[12:15],a[140:143],v[96:99]\nv_exp_f32 v72,v72\n" \
"v_add_f32 v82,v82,v224\nM v[100:103],a[12:15],a[156:159],v[228:231]\nv_exp_f32 v73,v73\nv_add_f32 v83,v83,v224\nM v[100:103],a[8:11],a[152:155],v[100:103]\nv_exp_f32 v74,v74\nM v[100:103],a[4:7],a[148:151],v[100:103]\nv_exp_f32 v75,v75\n" \
"v_cvt_pk_bf16_f32 v64,v64,v65\nM v[100:103],a[0:3],a[144:147],v[100:103]\nv_exp_f32 v80,v80\nv_add_f32 v88,v88,v224\nM v[104:107],a[16:19],a[128:131],v[224:227]\nv_exp_f32 v81,v81\nv_add_f32 v89,v89,v224\nM v[104:107],a[20:23],a[132:135],v[104:107]\n" \
"v_exp_f32 v82,v82\nM v[104:107],a[24:27],a[136:139],v[104:107]\nv_exp_f32 v83,v83\nv_add_f32 v90,v90,v224\nM v[104:107],a[28:31],a[140:143],v[104:107]\nv_add_f32 v91,v91,v224\nv_cvt_pk_bf16_f32 v65,v66,v67\nv_cvt_pk_bf16_f32 v66,v72,v73\n" \
"M v[108:111],a[28:31],a[156:159],v[228:231]\nv_exp_f32 v88,v88\nM v[108:111],a[24:27],a[152:155],v[108:111]\nv_exp_f32 v89,v89\nv_cvt_pk_bf16_f32 v67,v74,v75\nM v[108:111],a[20:23],a[148:151],v[108:111]\nv_exp_f32 v90,v90\nv_cvt_pk_bf16_f32 v80,v80,v81\n" \
"M v[108:111],a[16:19],a[144:147],v[108:111]\nv_exp_f32 v91,v91\ns_waitcnt lgkmcnt(0)\nM v[112:115],a[32:35],a[128:131],v[224:227]\nv_cvt_pk_bf16_f32 v81,v82,v83\nv_cvt_pk_bf16_f32 v82,v88,v89\nM v[112:115],a[36:39],a[132:135],v[112:115]\nv_cvt_pk_bf16_f32 v83,v90,v91\n" \
"ds_read_b128 a[88:91],v241 offset:6144\nds_read_b128 a[120:123],v241 offset:14336\nM v[112:115],a[40:43],a[136:139],v[112:115]\nds_read_b128 a[92:95],v241 offset:7168\nv_add_f32 v68,v68,v228\nv_add_f32 v69,v69,v228\nM v[112:115],a[44:47],a[140:143],v[112:115]\nds_read_b128 a[124:127],v241 offset:15360\n" \
"v_add_f32 v70,v70,v228\nM v[116:119],a[44:47],a[156:159],v[228:231]\nv_add_f32 v71,v71,v228\nv_exp_f32 v68,v68\nM v[116:119],a[40:43],a[152:155],v[116:119]\nv_exp_f32 v69,v69\nv_add_f32 v76,v76,v228\nM v[116:119],a[36:39],a[148:151],v[116:119]\n" \
"v_exp_f32 v70,v70\nM v[116:119],a[32:35],a[144:147],v[116:119]\nv_exp_f32 v71,v71\nv_add_f32 v77,v77,v228\nM v[124:127],a[60:63],a[156:159],v[228:231]\nv_add_f32 v78,v78,v228\nv_add_f32 v79,v79,v228\nv_add_f32 v84,v84,v228\n" \
"M v[124:127],a[56:59],a[152:155],v[124:127]\nv_add_f32 v85,v85,v228\nv_exp_f32 v76,v76\nM v[124:127],a[52:55],a[148:151],v[124:127]\nv_exp_f32 v77,v77\nM v[124:127],a[48:51],a[144:147],v[124:127]\nv_exp_f32 v78,v78\nv_add_f32 v86,v86,v228\n" \
"M v[0:3],a[64:67],v[64:67],0\nv_exp_f32 v79,v79\nv_add_f32 v87,v87,v228\nM v[0:3],a[96:99],v[80:83],v[0:3]\nv_exp_f32 v84,v84\nM v[8:11],a[68:71],v[64:67],0\nv_exp_f32 v85,v85\nv_cvt_pk_bf16_f32 v68,v68,v69\n" \
"M v[8:11],a[100:103],v[80:83],v[8:11]\nv_exp_f32 v86,v86\nv_add_f32 v92,v92,v228\nM v[16:19],a[72:75],v[64:67],0\nv_exp_f32 v87,v87\nv_add_f32 v93,v93,v228\nM v[16:19],a[104:107],v[80:83],v[16:19]\nv_add_f32 v94,v94,v228\n" \
"v_add_f32 v95,v95,v228\nM v[24:27],a[76:79],v[64:67],0\nv_cvt_pk_bf16_f32 v69,v70,v71\nv_exp_f32 v92,v92\nM v[24:27],a[108:111],v[80:83],v[24:27]\nv_exp_f32 v93,v93\nv_cvt_pk_bf16_f32 v70,v76,v77\nM v[32:35],a[80:83],v[64:67],0\n" \
"v_exp_f32 v94,v94\nM v[32:35],a[112:115],v[80:83],v[32:35]\nv_exp_f32 v95,v95\nv_cvt_pk_bf16_f32 v71,v78,v79\nM v[40:43],a[84:87],v[64:67],0\nv_cvt_pk_bf16_f32 v84,v84,v85\nv_cvt_pk_bf16_f32 v85,v86,v87\nv_cvt_pk_bf16_f32 v86,v92,v93\n" \
"M v[40:43],a[116:119],v[80:83],v[40:43]\nv_cvt_pk_bf16_f32 v87,v94,v95\nds_read_b128 a[0:3],v240 offset:32768\ns_waitcnt lgkmcnt(1)\nM v[48:51],a[88:91],v[64:67],0\nds_read_b128 a[4:7],v240 offset:33792\nds_read_b128 a[8:11],v240 offset:34816\nM v[48:51],a[120:123],v[80:83],v[48:51]\n" \
"ds_read_b128 a[12:15],v240 offset:35840\nv_exp_f32 v96,v96\nM v[56:59],a[92:95],v[64:67],0\nbuffer_load_dword v248,v251,s[52:55],s80 offen sc1\nds_read_b128 a[16:19],v240 offset:36864\nM v[56:59],a[124:127],v[80:83],v[56:59]\nds_read_b128 a[20:23],v240 offset:37888\nv_cndmask_b32 v112,v112,v252,s[82:83]\n" \
"M a[192:195],a[208:211],v[64:67],0\nds_read_b128 a[24:27],v240 offset:38912\nv_exp_f32 v97,v97\nM a[192:195],a[208:211],v[80:83],a[192:195]\nds_read_b128 a[28:31],v240 offset:39936\nv_exp_f32 v98,v98\nM v[4:7],a[64:67],v[68:71],0\nds_read_b128 a[32:35],v240 offset:40960\n" \
"v_cndmask_b32 v113,v113,v252,s[84:85]\nM v[4:7],a[96:99],v[84:87],v[4:7]\nds_read_b128 a[36:39],v240 offset:41984\nv_exp_f32 v99,v99\nM v[12:15],a[68:71],v[68:71],0\nds_read_b128 a[40:43],v240 offset:43008\nv_exp_f32 v104,v104\nM v[12:15],a[100:103],v[84:87],v[12:15]\n" \
"ds_read_b128 a[44:47],v240 offset:44032\nv_exp_f32 v105,v105\nM v[20:23],a[72:75],v[68:71],0\nds_read_b128 a[48:51],v240 offset:45056\nv_cndmask_b32 v114,v114,v252,s[86:87]\nM v[20:23],a[104:107],v[84:87],v[20:23]\nds_read_b128 a[52:55],v240 offset:46080\nv_exp_f32 v106,v106\n" \
"M v[28:31],a[76:79],v[68:71],0\nds_read_b128 a[56:59],v240 offset:47104\nv_exp_f32 v107,v107\nM v[28:31],a[108:111],v[84:87],v[28:31]\nds_read_b128 a[60:63],v240 offset:48128\nv_cndmask_b32 v115,v115,v252,s[88:89]\nM v[36:39],a[80:83],v[68:71],0\nds_read_b128 a[64:67],v241 offset:16384\n" \
"v_exp_f32 v112,v112\nM v[36:39],a[112:115],v[84:87],v[36:39]\nds_read_b128 a[96:99],v241 offset:24576\nv_exp_f32 v113,v113\nM v[44:47],a[84:87],v[68:71],0\nds_read_b128 a[68:71],v241 offset:17408\nv_cvt_pk_bf16_f32 v96,v96,v97\nv_cvt_pk_bf16_f32 v97,v98,v99\n" \
"M v[44:47],a[116:119],v[84:87],v[44:47]\nds_read_b128 a[100:103],v241 offset:25600\nv_cvt_pk_bf16_f32 v98,v104,v105\nM v[52:55],a[88:91],v[68:71],0\nds_read_b128 a[72:75],v241 offset:18432\nv_exp_f32 v114,v114\nM v[52:55],a[120:123],v[84:87],v[52:55]\nds_read_b128 a[104:107],v241 offset:26624\n" \
"v_exp_f32 v115,v115\nM v[60:63],a[92:95],v[68:71],0\nds_read_b128 a[76:79],v241 offset:19456\nv_cvt_pk_bf16_f32 v99,v106,v107\nM v[60:63],a[124:127],v[84:87],v[60:63]\nds_read_b128 a[108:111],v241 offset:27648\nv_cvt_pk_bf16_f32 v112,v112,v113\nv_cvt_pk_bf16_f32 v113,v114,v115\n" \
"M a[196:199],a[208:211],v[68:71],0\nds_read_b128 a[80:83],v241 offset:20480\nv_mov_b32 v114,0\nv_mov_b32 v115,0\nM a[196:199],a[208:211],v[84:87],a[196:199]\nds_read_b128 a[112:115],v241 offset:28672\nv_cndmask_b32 v124,v124,v252,s[82:83]\ns_waitcnt lgkmcnt(9)\n" \
"M v[128:131],a[0:3],a[128:131],v[224:227]\nds_read_b128 a[84:87],v241 offset:21504\nv_cndmask_b32 v125,v125,v252,s[84:85]\nM v[128:131],a[4:7],a[132:135],v[128:131]\nds_read_b128 a[116:119],v241 offset:29696\nv_exp_f32 v100,v100\nM v[128:131],a[8:11],a[136:139],v[128:131]\nds_read_b128 a[88:91],v241 offset:22528\n" \
"v_exp_f32 v101,v101\nM v[128:131],a[12:15],a[140:143],v[128:131]\nds_read_b128 a[120:123],v241 offset:30720\nv_cvt_pk_bf16_f32 v100,v100,v101\nM v[132:135],a[12:15],a[156:159],v[228:231]\nds_read_b128 a[92:95],v241 offset:23552\nv_exp_f32 v102,v102\nM v[132:135],a[8:11],a[152:155],v[132:135]\n" \
"ds_read_b128 a[124:127],v241 offset:31744\nv_exp_f32 v103,v103\nM v[132:135],a[4:7],a[148:151],v[132:135]\nv_exp_f32 v108,v108\nv_cndmask_b32 v126,v126,v252,s[86:87]\nM v[132:135],a[0:3],a[144:147],v[132:135]\nv_exp_f32 v109,v109\nM v[144:147],a[16:19],a[128:131],v[224:227]\n" \
"v_exp_f32 v110,v110\nv_cndmask_b32 v127,v127,v252,s[88:89]\nM v[144:147],a[20:23],a[132:135],v[144:147]\nv_exp_f32 v111,v111\nv_cvt_pk_bf16_f32 v101,v102,v103\nM v[144:147],a[24:27],a[136:139],v[144:147]\nv_exp_f32 v116,v116\nM v[144:147],a[28:31],a[140:143],v[144:147]\n" \
"v_exp_f32 v117,v117\nv_cvt_pk_bf16_f32 v102,v108,v109\nM v[148:151],a[28:31],a[156:159],v[228:231]\nv_exp_f32 v118,v118\nv_cvt_pk_bf16_f32 v103,v110,v111\nM v[148:151],a[24:27],a[152:155],v[148:151]\nv_exp_f32 v119,v119\nM v[148:151],a[20:23],a[148:151],v[148:151]\n" \
"v_exp_f32 v124,v124\nv_cvt_pk_bf16_f32 v116,v116,v117\nM v[148:151],a[16:19],a[144:147],v[148:151]\nv_exp_f32 v125,v125\nv_cvt_pk_bf16_f32 v117,v118,v119\nM v[160:163],a[32:35],a[128:131],v[224:227]\nv_exp_f32 v126,v126\nv_cvt_pk_bf16_f32 v118,v124,v125\n" \
"M v[160:163],a[36:39],a[132:135],v[160:163]\nv_exp_f32 v127,v127\nM v[160:163],a[40:43],a[136:139],v[160:163]\nv_cvt_pk_bf16_f32 v119,v126,v127\nds_read_b128 a[0:3],v240 offset:49152\nds_read_b128 a[4:7],v240 offset:50176\nM v[160:163],a[44:47],a[140:143],v[160:163]\nds_read_b128 a[8:11],v240 offset:51200\n" \
"v_exp_f32 v128,v128\nM v[164:167],a[44:47],a[156:159],v[228:231]\nds_read_b128 a[12:15],v240 offset:52224\nv_mov_b32 v188,0xff800000\nM v[164:167],a[40:43],a[152:155],v[164:167]\nds_read_b128 a[28:31],v240 offset:56320\nv_exp_f32 v129,v129\nM v[164:167],a[36:39],a[148:151],v[164:167]\n" \
"ds_read_b128 a[24:27],v240 offset:55296\nv_exp_f32 v130,v130\nM v[164:167],a[32:35],a[144:147],v[164:167]\nds_read_b128 a[20:23],v240 offset:54272\nv_exp_f32 v131,v131\nM v[176:179],a[48:51],a[128:131],v[224:227]\nds_read_b128 a[16:19],v240 offset:53248\nv_cvt_pk_bf16_f32 v192,v128,v129\n" \
"M v[176:179],a[52:55],a[132:135],v[176:179]\nv_cvt_pk_bf16_f32 v193,v130,v131\nv_exp_f32 v132,v132\nM v[176:179],a[56:59],a[136:139],v[176:179]\nv_exp_f32 v133,v133\nds_read_b128 a[32:35],v240 offset:57344\nM v[176:179],a[60:63],a[140:143],v[176:179]\nv_exp_f32 v134,v134\n" \
"M v[180:183],a[60:63],a[156:159],v[228:231]\ns_waitcnt vmcnt(1) lgkmcnt(6)\ns_barrier\nv_cvt_pk_bf16_f32 v196,v132,v133\nM v[180:183],a[56:59],a[152:155],v[180:183]\nv_exp_f32 v135,v135\nds_read_b128 a[36:39],v240 offset:58368\nM v[180:183],a[52:55],a[148:151],v[180:183]\n" \
"v_cvt_pk_bf16_f32 v197,v134,v135\nv_exp_f32 v144,v144\nM v[180:183],a[48:51],a[144:147],v[180:183]\nv_exp_f32 v145,v145\nM v[0:3],a[64:67],v[96:99],v[0:3]\nv_exp_f32 v146,v146\nv_cvt_pk_bf16_f32 v194,v144,v145\nM v[0:3],a[96:99],v[112:115],v[0:3]\n" \
"v_exp_f32 v147,v147\nds_read_b128 a[40:43],v240 offset:59392\nM v[8:11],a[68:71],v[96:99],v[8:11]\nv_cvt_pk_bf16_f32 v195,v146,v147\nds_read_b128 a[44:47],v240 offset:60416\nM v[8:11],a[100:103],v[112:115],v[8:11]\nv_exp_f32 v148,v148\nds_read_b128 a[48:51],v240 offset:61440\n" \
"M v[16:19],a[72:75],v[96:99],v[16:19]\nv_exp_f32 v149,v149\nds_read_b128 a[52:55],v240 offset:62464\nM v[16:19],a[104:107],v[112:115],v[16:19]\nv_exp_f32 v150,v150\nM v[24:27],a[76:79],v[96:99],v[24:27]\nv_exp_f32 v151,v151\nv_cvt_pk_bf16_f32 v198,v148,v149\n" \
"M v[24:27],a[108:111],v[112:115],v[24:27]\nv_cvt_pk_bf16_f32 v199,v150,v151\nv_exp_f32 v160,v160\nM v[32:35],a[80:83],v[96:99],v[32:35]\nv_exp_f32 v161,v161\nds_read_b128 a[56:59],v240 offset:63488\nM v[32:35],a[112:115],v[112:115],v[32:35]\nv_exp_f32 v162,v162\n" \
"M v[40:43],a[84:87],v[96:99],v[40:43]\nv_exp_f32 v163,v163\nv_cvt_pk_bf16_f32 v208,v160,v161\nM v[40:43],a[116:119],v[112:115],v[40:43]\nv_cvt_pk_bf16_f32 v209,v162,v163\nds_read_b128 a[60:63],v240 offset:64512\ns_waitcnt vmcnt(0)\nv_cmp_eq_u32 vcc,0,v248\n" \
"M v[48:51],a[88:91],v[96:99],v[48:51]\nds_write_b128 v250,a[244:247]\nM v[48:51],a[120:123],v[112:115],v[48:51]\nv_exp_f32 v164,v164\nds_read_b128 v[64:67],v250\nM v[56:59],a[92:95],v[96:99],v[56:59]\nv_exp_f32 v165,v165\nds_read_b128 v[68:71],v250\n" \
"M v[56:59],a[124:127],v[112:115],v[56:59]\nv_exp_f32 v166,v166\nds_read_b128 v[72:75],v250\nM a[192:195],a[208:211],v[96:99],a[192:195]\nv_exp_f32 v167,v167\nM a[192:195],a[208:211],v[112:115],a[192:195]\nds_read_b128 v[76:79],v250\nds_read_b128 v[80:83],v250\n" \
"v_cvt_pk_bf16_f32 v212,v164,v165\nM v[4:7],a[64:67],v[100:103],v[4:7]\nds_read_b128 v[84:87],v250\nv_cvt_pk_bf16_f32 v213,v166,v167\ns_cbranch_vccnz .Lps5\n.Lpb5:\nM v[4:7],a[96:99],v[116:119],v[4:7]\nds_read_b128 v[88:91],v250\n" \
"v_mov_b32 v189,0xff800000\nM v[12:15],a[68:71],v[100:103],v[12:15]\nds_read_b128 v[92:95],v250\nv_exp_f32 v176,v176\nM v[12:15],a[100:103],v[116:119],v[12:15]\nds_read_b128 v[96:99],v250\nv_exp_f32 v177,v177\nM v[20:23],a[72:75],v[100:103],v[20:23]\n" \
"ds_read_b128 v[104:107],v250\nv_exp_f32 v178,v178\nM v[20:23],a[104:107],v[116:119],v[20:23]\nds_read_b128 v[108:111],v250\nv_cvt_pk_bf16_f32 v210,v176,v177\nM v[28:31],a[76:79],v[100:103],v[28:31]\nds_read_b128 v[112:115],v250\nv_exp_f32 v179,v179\n" \
"M v[28:31],a[108:111],v[116:119],v[28:31]\nds_read_b128 v[120:123],v250\nv_cvt_pk_bf16_f32 v211,v178,v179\nv_mov_b32 v190,0xff800000\nM v[36:39],a[80:83],v[100:103],v[36:39]\nds_read_b128 v[124:127],v250\nv_mov_b32 v191,0xff800000\nM v[36:39],a[112:115],v[116:119],v[36:39]\n" \
"ds_read_b128 a[200:203],v250\nv_exp_f32 v180,v180\nM v[44:47],a[84:87],v[100:103],v[44:47]\nds_read_b128 a[204:207],v250\nv_exp_f32 v181,v181\nM v[44:47],a[116:119],v[116:119],v[44:47]\nds_read_b128 a[64:67],v241 offset:32768\nv_cvt_pk_bf16_f32 v214,v180,v181\n" \
"M v[52:55],a[88:91],v[100:103],v[52:55]\nds_read_b128 a[96:99],v241 offset:40960\nv_exp_f32 v182,v182\nM v[52:55],a[120:123],v[116:119],v[52:55]\nds_read_b128 a[68:71],v241 offset:33792\nv_exp_f32 v183,v183\nM v[60:63],a[92:95],v[100:103],v[60:63]\nds_read_b128 a[100:103],v241 offset:41984\n" \
"v_cvt_pk_bf16_f32 v215,v182,v183\nv_mov_b32 v202,0\nM v[60:63],a[124:127],v[116:119],v[60:63]\nds_read_b128 a[72:75],v241 offset:34816\nv_mov_b32 v203,0\nM a[196:199],a[208:211],v[100:103],a[196:199]\nds_read_b128 v[100:103],v250\nM a[196:199],a[208:211],v[116:119],a[196:199]\n" \
"ds_read_b128 v[116:119],v250\nv_add_u32 v250,0xc000,v241\nM v[136:139],a[0:3],a[160:163],0\nds_read_b128 a[104:107],v241 offset:43008\nM v[136:139],a[4:7],a[164:167],v[136:139]\nds_read_b128 a[76:79],v241 offset:35840\nM v[136:139],a[8:11],a[168:171],v[136:139]\nds_read_b128 a[108:111],v241 offset:44032\n" \
"s_waitcnt lgkmcnt(10)\nM v[136:139],a[12:15],a[172:175],v[136:139]\nds_read_b128 a[80:83],v241 offset:36864\nM v[140:143],a[12:15],a[188:191],0\nds_read_b128 a[112:115],v241 offset:45056\nM v[140:143],a[8:11],a[184:187],v[140:143]\nds_read_b128 a[84:87],v241 offset:37888\nM v[140:143],a[4:7],a[180:183],v[140:143]\n" \
"ds_read_b128 a[116:119],v241 offset:46080\nM v[140:143],a[0:3],a[176:179],v[140:143]\nds_read_b128 a[88:91],v241 offset:38912\nv_max3_f32 v233,v136,v137,v138\nM v[156:159],a[28:31],a[188:191],0\nds_read_b128 a[120:123],v241 offset:47104\nv_max_f32 v233,v233,v139\nv_cndmask_b32 v233,v232,v233,s[90:91]\n" \
"M v[156:159],a[24:27],a[184:187],v[156:159]\nds_read_b128 a[92:95],v241 offset:39936\nv_max3_f32 v237,v140,v141,v142\nv_cndmask_b32 v136,v136,v252,s[82:83]\nM v[156:159],a[20:23],a[180:183],v[156:159]\nds_read_b128 a[124:127],v241 offset:48128\nv_cndmask_b32 v137,v137,v252,s[84:85]\nv_cndmask_b32 v138,v138,v252,s[86:87]\n" \
"M v[156:159],a[16:19],a[176:179],v[156:159]\nv_cndmask_b32 v139,v139,v252,s[88:89]\nM v[128:131],a[0:3],a[128:131],v[224:227]\nM v[128:131],a[4:7],a[132:135],v[128:131]\nM v[128:131],a[8:11],a[136:139],v[128:131]\nM v[128:131],a[12:15],a[140:143],v[128:131]\nM v[132:135],a[12:15],a[156:159],v[228:231]\nM v[132:135],a[8:11],a[152:155],v[132:135]\n" \
"M v[132:135],a[4:7],a[148:151],v[132:135]\nv_max3_f32 v237,v237,v143,v156\nv_max3_f32 v237,v237,v157,v158\nM v[132:135],a[0:3],a[144:147],v[132:135]\nds_read_b128 a[0:3],v240 offset:0\nds_read_b128 a[4:7],v240 offset:1024\nv_max_f32 v237,v237,v159\nM v[144:147],a[16:19],a[128:131],v[224:227]\n" \
"ds_read_b128 a[8:11],v240 offset:2048\nv_cndmask_b32 v237,v236,v237,s[90:91]\nv_cndmask_b32 v156,v156,v252,s[82:83]\nM v[144:147],a[20:23],a[132:135],v[144:147]\nds_read_b128 a[12:15],v240 offset:3072\nv_permlane16_swap_b32 v233,v237\nM v[144:147],a[24:27],a[136:139],v[144:147]\nv_max_f32 v233,v233,v237\n" \
"v_mov_b32 v237,v233\nv_cndmask_b32 v157,v157,v252,s[84:85]\nM v[144:147],a[28:31],a[140:143],v[144:147]\nv_permlane32_swap_b32 v233,v237\nv_max_f32 v233,v233,v237\nv_mov_b32 v237,v233\nM v[148:151],a[28:31],a[156:159],v[228:231]\nv_cndmask_b32 v158,v158,v252,s[86:87]\n" \
"v_permlane16_swap_b32 v233,v237\nM v[148:151],a[24:27],a[152:155],v[148:151]\nv_sub_f32 v232,0,v233\nv_add_f32 v136,v136,v232\nv_add_f32 v137,v137,v232\nM v[148:151],a[20:23],a[148:151],v[148:151]\nv_add_f32 v138,v138,v232\nv_add_f32 v139,v139,v232\n" \
"v_cndmask_b32 v159,v159,v252,s[88:89]\nM v[148:151],a[16:19],a[144:147],v[148:151]\nds_read_b128 a[16:19],v240 offset:4096\nds_read_b128 a[20:23],v240 offset:5120\nv_sub_f32 v236,0,v237\nM v[160:163],a[32:35],a[128:131],v[224:227]\nds_read_b128 a[24:27],v240 offset:6144\nv_sub_f32 v234,0,v233\n" \
"M v[160:163],a[36:39],a[132:135],v[160:163]\nds_read_b128 a[28:31],v240 offset:7168\nv_exp_f32 v136,v136\nM v[160:163],a[40:43],a[136:139],v[160:163]\nv_exp_f32 v137,v137\nv_sub_f32 v235,0,v233\nM v[160:163],a[44:47],a[140:143],v[160:163]\nv_exp_f32 v138,v138\n" \
"M v[164:167],a[44:47],a[156:159],v[228:231]\ns_waitcnt lgkmcnt(4)\ns_barrier\nv_sub_f32 v238,0,v237\nM v[164:167],a[40:43],a[152:155],v[164:167]\nv_exp_f32 v139,v139\nv_sub_f32 v239,0,v237\nM v[164:167],a[36:39],a[148:151],v[164:167]\n" \
"v_exp_f32 v128,v128\nv_add_f32 v140,v140,v236\nM v[164:167],a[32:35],a[144:147],v[164:167]\nds_read_b128 a[32:35],v240 offset:8192\nds_read_b128 a[36:39],v240 offset:9216\nM v[176:179],a[48:51],a[128:131],v[224:227]\nds_read_b128 a[40:43],v240 offset:10240\nv_exp_f32 v129,v129\n" \
"M v[176:179],a[52:55],a[132:135],v[176:179]\nds_read_b128 a[44:47],v240 offset:11264\nv_exp_f32 v130,v130\nM v[176:179],a[56:59],a[136:139],v[176:179]\nv_exp_f32 v131,v131\nM v[176:179],a[60:63],a[140:143],v[176:179]\nv_exp_f32 v132,v132\nv_add_f32 v141,v141,v236\n" \
"M v[180:183],a[60:63],a[156:159],v[228:231]\nv_exp_f32 v133,v133\nv_add_f32 v142,v142,v236\nM v[180:183],a[56:59],a[152:155],v[180:183]\nv_exp_f32 v134,v134\nv_add_f32 v143,v143,v236\nM v[180:183],a[52:55],a[148:151],v[180:183]\nv_exp_f32 v135,v135\n" \
"M v[180:183],a[48:51],a[144:147],v[180:183]\nds_read_b128 a[48:51],v240 offset:12288\nds_read_b128 a[52:55],v240 offset:13312\nv_add_f32 v156,v156,v236\nM v[0:3],a[64:67],v[192:195],v[0:3]\nds_read_b128 a[56:59],v240 offset:14336\nv_exp_f32 v144,v144\nM v[0:3],a[96:99],v[208:211],v[0:3]\n" \
"ds_read_b128 a[60:63],v240 offset:15360\nv_add_f32 v157,v157,v236\nM v[8:11],a[68:71],v[192:195],v[8:11]\nv_exp_f32 v145,v145\nv_add_f32 v158,v158,v236\nM v[8:11],a[100:103],v[208:211],v[8:11]\nv_exp_f32 v146,v146\nv_add_f32 v159,v159,v236\n" \
"M v[16:19],a[72:75],v[192:195],v[16:19]\nv_exp_f32 v147,v147\nM v[16:19],a[104:107],v[208:211],v[16:19]\nv_exp_f32 v148,v148\nv_sub_f32 v233,0,v233\nM v[24:27],a[76:79],v[192:195],v[24:27]\nv_exp_f32 v149,v149\nv_sub_f32 v237,0,v237\n" \
"M v[24:27],a[108:111],v[208:211],v[24:27]\nv_exp_f32 v150,v150\nv_cvt_pk_bf16_f32 v200,v136,v137\nM v[32:35],a[80:83],v[192:195],v[32:35]\nv_exp_f32 v151,v151\nM v[32:35],a[112:115],v[208:211],v[32:35]\nv_exp_f32 v160,v160\nv_cvt_pk_bf16_f32 v201,v138,v139\n" \
"M v[40:43],a[84:87],v[192:195],v[40:43]\nv_exp_f32 v161,v161\nM v[40:43],a[116:119],v[208:211],v[40:43]\nv_exp_f32 v162,v162\nM v[48:51],a[88:91],v[192:195],v[48:51]\nv_exp_f32 v163,v163\nM v[48:51],a[120:123],v[208:211],v[48:51]\nv_exp_f32 v176,v176\n" \
"M v[56:59],a[92:95],v[192:195],v[56:59]\nv_exp_f32 v177,v177\nM v[56:59],a[124:127],v[208:211],v[56:59]\nv_exp_f32 v178,v178\nM a[192:195],a[208:211],v[192:195],a[192:195]\nv_exp_f32 v179,v179\nv_cvt_pk_bf16_f32 v192,v128,v129\nM a[192:195],a[208:211],v[208:211],a[192:195]\n" \
"v_exp_f32 v140,v140\nv_cvt_pk_bf16_f32 v193,v130,v131\nM v[4:7],a[64:67],v[196:199],v[4:7]\nv_exp_f32 v141,v141\nM v[4:7],a[96:99],v[212:215],v[4:7]\nv_exp_f32 v142,v142\nv_cvt_pk_bf16_f32 v194,v144,v145\nM v[12:15],a[68:71],v[196:199],v[12:15]\n" \
"v_exp_f32 v143,v143\nv_cvt_pk_bf16_f32 v195,v146,v147\nM v[12:15],a[100:103],v[212:215],v[12:15]\nv_exp_f32 v156,v156\nv_cvt_pk_bf16_f32 v208,v160,v161\nM v[20:23],a[72:75],v[196:199],v[20:23]\nv_exp_f32 v157,v157\nM v[20:23],a[104:107],v[212:215],v[20:23]\n" \
"v_exp_f32 v158,v158\nv_cvt_pk_bf16_f32 v209,v162,v163\nM v[28:31],a[76:79],v[196:199],v[28:31]\nv_exp_f32 v159,v159\nv_cvt_pk_bf16_f32 v210,v176,v177\nM v[28:31],a[108:111],v[212:215],v[28:31]\nv_exp_f32 v164,v164\nM v[36:39],a[80:83],v[196:199],v[36:39]\n" \
"v_exp_f32 v165,v165\nv_cvt_pk_bf16_f32 v211,v178,v179\nM v[36:39],a[112:115],v[212:215],v[36:39]\nv_exp_f32 v166,v166\nM v[44:47],a[84:87],v[196:199],v[44:47]\nv_exp_f32 v167,v167\nM v[44:47],a[116:119],v[212:215],v[44:47]\nv_exp_f32 v180,v180\n" \
"M v[52:55],a[88:91],v[196:199],v[52:55]\nv_exp_f32 v181,v181\nM v[52:55],a[120:123],v[212:215],v[52:55]\nv_exp_f32 v182,v182\nM v[60:63],a[92:95],v[196:199],v[60:63]\nv_exp_f32 v183,v183\nM v[60:63],a[124:127],v[212:215],v[60:63]\nM a[196:199],a[208:211],v[196:199],a[196:199]\n" \
"v_cvt_pk_bf16_f32 v196,v132,v133\nv_cvt_pk_bf16_f32 v197,v134,v135\nv_cvt_pk_bf16_f32 v198,v148,v149\nv_cvt_pk_bf16_f32 v199,v150,v151\nM a[196:199],a[208:211],v[212:215],a[196:199]\ns_cmp_eq_u32 s62,0\ns_cbranch_scc1 .Ltrans\n.Ldual:\n" \
"s_waitcnt lgkmcnt(4)\nM v[128:131],a[0:3],a[128:131],v[224:227]\ns_add_u32 m0,s94,s64\nv_cvt_pk_bf16_f32 v204,v140,v141\nM v[128:131],a[4:7],a[132:135],v[128:131]\nds_read_b128 a[64:67],v250 offset:0\nds_read_b128 a[96:99],v250 offset:8192\nM v[128:131],a[8:11],a[136:139],v[128:131]\n" \
"v_cvt_pk_bf16_f32 v205,v142,v143\nv_cvt_pk_bf16_f32 v206,v156,v157\nM v[128:131],a[12:15],a[140:143],v[128:131]\nv_cvt_pk_bf16_f32 v207,v158,v159\nv_cvt_pk_bf16_f32 v212,v164,v165\nM v[132:135],a[12:15],a[156:159],v[228:231]\nbuffer_load_dwordx4 v240,s[44:47],s95 offen offset:0 sc1 lds\nM v[132:135],a[8:11],a[152:155],v[132:135]\n" \
"v_cvt_pk_bf16_f32 v213,v166,v167\nv_cvt_pk_bf16_f32 v216,v168,v169\nM v[132:135],a[4:7],a[148:151],v[132:135]\nv_cvt_pk_bf16_f32 v217,v170,v171\nv_cvt_pk_bf16_f32 v214,v180,v181\nM v[132:135],a[0:3],a[144:147],v[132:135]\nds_read_b128 a[68:71],v250 offset:1024\nds_read_b128 a[100:103],v250 offset:9216\n" \
"M v[136:139],a[0:3],a[160:163],v[232:235]\nv_exp_f32 v172,v172\nM v[136:139],a[4:7],a[164:167],v[136:139]\nv_exp_f32 v173,v173\nM v[136:139],a[8:11],a[168:171],v[136:139]\nv_exp_f32 v174,v174\nM v[136:139],a[12:15],a[172:175],v[136:139]\nv_exp_f32 v175,v175\n" \
"M v[140:143],a[12:15],a[188:191],v[236:239]\nv_cvt_pk_bf16_f32 v220,v172,v173\nv_cvt_pk_bf16_f32 v221,v174,v175\nM v[140:143],a[8:11],a[184:187],v[140:143]\nds_read_b128 a[72:75],v250 offset:2048\nds_read_b128 a[104:107],v250 offset:10240\nM v[140:143],a[4:7],a[180:183],v[140:143]\nv_cvt_pk_bf16_f32 v215,v182,v183\n" \
"s_add_u32 s94,s94,0x4000\nM v[140:143],a[0:3],a[176:179],v[140:143]\nv_exp_f32 v184,v184\nM v[144:147],a[16:19],a[128:131],v[224:227]\nv_exp_f32 v185,v185\nM v[144:147],a[20:23],a[132:135],v[144:147]\nv_exp_f32 v186,v186\nM v[144:147],a[24:27],a[136:139],v[144:147]\n" \
"v_exp_f32 v187,v187\nM v[144:147],a[28:31],a[140:143],v[144:147]\nds_read_b128 a[76:79],v250 offset:3072\nds_read_b128 a[108:111],v250 offset:11264\nM v[148:151],a[28:31],a[156:159],v[228:231]\nv_cvt_pk_bf16_f32 v218,v184,v185\nv_cvt_pk_bf16_f32 v219,v186,v187\nM v[148:151],a[24:27],a[152:155],v[148:151]\n" \
"v_exp_f32 v188,v188\nM v[148:151],a[20:23],a[148:151],v[148:151]\nbuffer_load_dwordx4 v240,s[44:47],s95 offen offset:1024 sc1 lds\nM v[148:151],a[16:19],a[144:147],v[148:151]\nv_exp_f32 v189,v189\nM v[152:155],a[16:19],a[160:163],v[232:235]\nv_exp_f32 v190,v190\nM v[152:155],a[20:23],a[164:167],v[152:155]\n" \
"ds_read_b128 a[80:83],v250 offset:4096\nds_read_b128 a[112:115],v250 offset:12288\nM v[152:155],a[24:27],a[168:171],v[152:155]\nv_exp_f32 v191,v191\nM v[152:155],a[28:31],a[172:175],v[152:155]\nv_cvt_pk_bf16_f32 v222,v188,v189\nv_cvt_pk_bf16_f32 v223,v190,v191\nM v[156:159],a[28:31],a[188:191],v[236:239]\n" \
"v_exp_f32 v128,v128\nM v[156:159],a[24:27],a[184:187],v[156:159]\nv_exp_f32 v129,v129\nM v[156:159],a[20:23],a[180:183],v[156:159]\nv_exp_f32 v130,v130\nM v[156:159],a[16:19],a[176:179],v[156:159]\nds_read_b128 a[84:87],v250 offset:5120\nds_read_b128 a[116:119],v250 offset:13312\n" \
"M v[160:163],a[32:35],a[128:131],v[224:227]\nv_exp_f32 v131,v131\nM v[160:163],a[36:39],a[132:135],v[160:163]\nv_exp_f32 v144,v144\nM v[160:163],a[40:43],a[136:139],v[160:163]\nv_exp_f32 v145,v145\nM v[160:163],a[44:47],a[140:143],v[160:163]\nv_exp_f32 v146,v146\n" \
"M v[164:167],a[44:47],a[156:159],v[228:231]\nv_exp_f32 v147,v147\nM v[164:167],a[40:43],a[152:155],v[164:167]\nds_read_b128 a[88:91],v250 offset:6144\nds_read_b128 a[120:123],v250 offset:14336\nM v[164:167],a[36:39],a[148:151],v[164:167]\nv_exp_f32 v132,v132\nM v[164:167],a[32:35],a[144:147],v[164:167]\n" \
"v_exp_f32 v133,v133\nM v[168:171],a[32:35],a[160:163],v[232:235]\nbuffer_load_dwordx4 v240,s[44:47],s95 offen offset:2048 sc1 lds\nM v[168:171],a[36:39],a[164:167],v[168:171]\nv_exp_f32 v134,v134\nM v[168:171],a[40:43],a[168:171],v[168:171]\nv_exp_f32 v135,v135\nM v[168:171],a[44:47],a[172:175],v[168:171]\n" \
"ds_read_b128 a[92:95],v250 offset:7168\nds_read_b128 a[124:127],v250 offset:15360\nM v[172:175],a[44:47],a[188:191],v[236:239]\nv_xor_b32 v250,0x18000,v250\ns_and_b32 s94,s94,0xffff\nM v[172:175],a[40:43],a[184:187],v[172:175]\nv_exp_f32 v148,v148\nM v[172:175],a[36:39],a[180:183],v[172:175]\n" \
"v_exp_f32 v149,v149\nM v[172:175],a[32:35],a[176:179],v[172:175]\ns_waitcnt lgkmcnt(4)\nM v[176:179],a[48:51],a[128:131],v[224:227]\nv_exp_f32 v150,v150\nM v[176:179],a[52:55],a[132:135],v[176:179]\nv_exp_f32 v151,v151\nM v[176:179],a[56:59],a[136:139],v[176:179]\n" \
"v_exp_f32 v136,v136\nM v[176:179],a[60:63],a[140:143],v[176:179]\nv_exp_f32 v137,v137\nM v[180:183],a[60:63],a[156:159],v[228:231]\nv_exp_f32 v138,v138\nM v[180:183],a[56:59],a[152:155],v[180:183]\nv_exp_f32 v139,v139\nM v[180:183],a[52:55],a[148:151],v[180:183]\n" \
"v_exp_f32 v152,v152\nM v[180:183],a[48:51],a[144:147],v[180:183]\nv_exp_f32 v153,v153\nM v[184:187],a[48:51],a[160:163],v[232:235]\nv_exp_f32 v154,v154\nM v[184:187],a[52:55],a[164:167],v[184:187]\nv_exp_f32 v155,v155\nM v[184:187],a[56:59],a[168:171],v[184:187]\n" \
"buffer_load_dwordx4 v240,s[44:47],s95 offen offset:3072 sc1 lds\nM v[184:187],a[60:63],a[172:175],v[184:187]\nv_exp_f32 v160,v160\nM v[188:191],a[60:63],a[188:191],v[236:239]\nv_exp_f32 v161,v161\nM v[188:191],a[56:59],a[184:187],v[188:191]\nv_exp_f32 v162,v162\nM v[188:191],a[52:55],a[180:183],v[188:191]\n" \
"v_exp_f32 v163,v163\nM v[188:191],a[48:51],a[176:179],v[188:191]\nv_exp_f32 v176,v176\nM v[0:3],a[64:67],v[192:195],v[0:3]\ns_waitcnt vmcnt(16) lgkmcnt(0)\ns_barrier\nM v[0:3],a[96:99],v[208:211],v[0:3]\nds_read_b128 a[0:3],v250 offset:0\n" \
"ds_read_b128 a[4:7],v250 offset:1024\nM v[8:11],a[68:71],v[192:195],v[8:11]\nv_exp_f32 v177,v177\nM v[8:11],a[100:103],v[208:211],v[8:11]\nv_exp_f32 v178,v178\nM v[16:19],a[72:75],v[192:195],v[16:19]\ns_add_u32 m0,m0,0x10000\nM v[16:19],a[104:107],v[208:211],v[16:19]\n" \
"buffer_load_dwordx4 v240,s[48:51],s95 offen offset:0 sc1 lds\nM v[24:27],a[76:79],v[192:195],v[24:27]\nv_exp_f32 v179,v179\nM v[24:27],a[108:111],v[208:211],v[24:27]\nv_exp_f32 v140,v140\nM v[32:35],a[80:83],v[192:195],v[32:35]\nv_exp_f32 v141,v141\nM v[32:35],a[112:115],v[208:211],v[32:35]\n" \
"ds_read_b128 a[8:11],v250 offset:2048\nds_read_b128 a[12:15],v250 offset:3072\nM v[40:43],a[84:87],v[192:195],v[40:43]\nv_exp_f32 v142,v142\nM v[40:43],a[116:119],v[208:211],v[40:43]\nv_exp_f32 v143,v143\nM v[48:51],a[88:91],v[192:195],v[48:51]\nv_exp_f32 v156,v156\n" \
"M v[48:51],a[120:123],v[208:211],v[48:51]\nv_exp_f32 v157,v157\nM v[56:59],a[92:95],v[192:195],v[56:59]\nv_exp_f32 v158,v158\nM v[56:59],a[124:127],v[208:211],v[56:59]\nv_exp_f32 v159,v159\nM a[192:195],a[208:211],v[192:195],a[192:195]\nv_exp_f32 v164,v164\n" \
"M a[192:195],a[208:211],v[208:211],a[192:195]\nds_read_b128 a[16:19],v250 offset:4096\nds_read_b128 a[20:23],v250 offset:5120\nM v[4:7],a[64:67],v[196:199],v[4:7]\nv_exp_f32 v165,v165\nM v[4:7],a[96:99],v[212:215],v[4:7]\nv_exp_f32 v166,v166\nM v[12:15],a[68:71],v[196:199],v[12:15]\n" \
"v_exp_f32 v167,v167\nM v[12:15],a[100:103],v[212:215],v[12:15]\nbuffer_load_dwordx4 v240,s[48:51],s95 offen offset:1024 sc1 lds\nM v[20:23],a[72:75],v[196:199],v[20:23]\nv_exp_f32 v180,v180\nM v[20:23],a[104:107],v[212:215],v[20:23]\nv_exp_f32 v181,v181\nM v[28:31],a[76:79],v[196:199],v[28:31]\n" \
"v_exp_f32 v182,v182\nM v[28:31],a[108:111],v[212:215],v[28:31]\nds_read_b128 a[24:27],v250 offset:6144\nds_read_b128 a[28:31],v250 offset:7168\nM v[36:39],a[80:83],v[196:199],v[36:39]\nv_exp_f32 v183,v183\nM v[36:39],a[112:115],v[212:215],v[36:39]\nv_exp_f32 v168,v168\n" \
"M v[44:47],a[84:87],v[196:199],v[44:47]\nv_exp_f32 v169,v169\nM v[44:47],a[116:119],v[212:215],v[44:47]\nv_exp_f32 v170,v170\nM v[52:55],a[88:91],v[196:199],v[52:55]\nv_exp_f32 v171,v171\nM v[52:55],a[120:123],v[212:215],v[52:55]\nv_cvt_pk_bf16_f32 v192,v128,v129\n" \
"v_cvt_pk_bf16_f32 v193,v130,v131\nM v[60:63],a[92:95],v[196:199],v[60:63]\nv_cvt_pk_bf16_f32 v194,v144,v145\nv_cvt_pk_bf16_f32 v195,v146,v147\nM v[60:63],a[124:127],v[212:215],v[60:63]\nds_read_b128 a[32:35],v250 offset:8192\nds_read_b128 a[36:39],v250 offset:9216\nM a[196:199],a[208:211],v[196:199],a[196:199]\n" \
"v_cvt_pk_bf16_f32 v208,v160,v161\nv_cvt_pk_bf16_f32 v209,v162,v163\nM a[196:199],a[208:211],v[212:215],a[196:199]\nv_cvt_pk_bf16_f32 v210,v176,v177\nv_cvt_pk_bf16_f32 v211,v178,v179\nM v[64:67],a[64:67],v[200:203],v[64:67]\nv_cvt_pk_bf16_f32 v196,v132,v133\nv_cvt_pk_bf16_f32 v197,v134,v135\n" \
"M v[64:67],a[96:99],v[216:219],v[64:67]\nbuffer_load_dwordx4 v240,s[48:51],s95 offen offset:2048 sc1 lds\nM v[72:75],a[68:71],v[200:203],v[72:75]\nv_cvt_pk_bf16_f32 v198,v148,v149\nv_cvt_pk_bf16_f32 v199,v150,v151\nM v[72:75],a[100:103],v[216:219],v[72:75]\nM v[80:83],a[72:75],v[200:203],v[80:83]\nM v[80:83],a[104:107],v[216:219],v[80:83]\n" \
"ds_read_b128 a[40:43],v250 offset:10240\nds_read_b128 a[44:47],v250 offset:11264\nM v[88:91],a[76:79],v[200:203],v[88:91]\nM v[88:91],a[108:111],v[216:219],v[88:91]\nM v[96:99],a[80:83],v[200:203],v[96:99]\nM v[96:99],a[112:115],v[216:219],v[96:99]\nM v[104:107],a[84:87],v[200:203],v[104:107]\nM v[104:107],a[116:119],v[216:219],v[104:107]\n" \
"M v[112:115],a[88:91],v[200:203],v[112:115]\nM v[112:115],a[120:123],v[216:219],v[112:115]\nds_read_b128 a[48:51],v250 offset:12288\nds_read_b128 a[52:55],v250 offset:13312\nM v[120:123],a[92:95],v[200:203],v[120:123]\nM v[120:123],a[124:127],v[216:219],v[120:123]\nM a[200:203],a[208:211],v[200:203],a[200:203]\nv_cvt_pk_bf16_f32 v200,v136,v137\n" \
"v_cvt_pk_bf16_f32 v201,v138,v139\nM a[200:203],a[208:211],v[216:219],a[200:203]\nbuffer_load_dwordx4 v240,s[48:51],s95 offen offset:3072 sc1 lds\nM v[68:71],a[64:67],v[204:207],v[68:71]\nv_cvt_pk_bf16_f32 v202,v152,v153\nv_cvt_pk_bf16_f32 v203,v154,v155\nM v[68:71],a[96:99],v[220:223],v[68:71]\ns_cmp_eq_u32 s95,s96\n" \
"s_cselect_b32 s72,0xc000,s98\ns_add_u32 s95,s95,s72\nM v[76:79],a[68:71],v[204:207],v[76:79]\nM v[76:79],a[100:103],v[220:223],v[76:79]\nds_read_b128 a[56:59],v250 offset:14336\nds_read_b128 a[60:63],v250 offset:15360\nM v[84:87],a[72:75],v[204:207],v[84:87]\nv_add_u32 v250,s94,v241\n" \
"M v[84:87],a[104:107],v[220:223],v[84:87]\nM v[92:95],a[76:79],v[204:207],v[92:95]\nM v[92:95],a[108:111],v[220:223],v[92:95]\nM v[100:103],a[80:83],v[204:207],v[100:103]\nM v[100:103],a[112:115],v[220:223],v[100:103]\nM v[108:111],a[84:87],v[204:207],v[108:111]\nM v[108:111],a[116:119],v[220:223],v[108:111]\nM v[116:119],a[88:91],v[204:207],v[116:119]\n" \
"buffer_load_dword v247,v254,s[56:59],s1 offen\nM v[116:119],a[120:123],v[220:223],v[116:119]\ns_add_u32 s1,s1,s0\nM v[124:127],a[92:95],v[204:207],v[124:127]\nM v[124:127],a[124:127],v[220:223],v[124:127]\nM a[204:207],a[208:211],v[204:207],a[204:207]\nM a[204:207],a[208:211],v[220:223],a[204:207]\ns_cmp_lg_u32 s95,s97\n" \
"s_cbranch_scc1 .Ldual\ns_cmp_lt_u32 s62,4\ns_cbranch_scc1 .Ltrans\n.Ldf:\ns_waitcnt lgkmcnt(4)\nM v[128:131],a[0:3],a[128:131],v[224:227]\ns_add_u32 m0,s94,s64\nv_cvt_pk_bf16_f32 v204,v140,v141\n" \
"M v[128:131],a[4:7],a[132:135],v[128:131]\nds_read_b128 a[64:67],v250 offset:0\nds_read_b128 a[96:99],v250 offset:8192\nM v[128:131],a[8:11],a[136:139],v[128:131]\nv_cvt_pk_bf16_f32 v205,v142,v143\nv_lshrrev_b32 v248,2,v249\nM v[128:131],a[12:15],a[140:143],v[128:131]\nv_mul_u32_u24 v248,3584,v248\n" \
"v_and_b32 v251,3,v249\nM v[132:135],a[12:15],a[156:159],v[228:231]\nbuffer_load_dwordx4 v240,s[44:47],s95 offen offset:0 sc1 lds\nM v[132:135],a[8:11],a[152:155],v[132:135]\ns_mul_i32 s70,s7,458752\nv_lshl_add_u32 v248,v251,3,v248\nM v[132:135],a[4:7],a[148:151],v[132:135]\ns_add_u32 s70,s70,s2\n" \
"v_add_u32 v248,s70,v248\nM v[132:135],a[0:3],a[144:147],v[132:135]\nds_read_b128 a[68:71],v250 offset:1024\nds_read_b128 a[100:103],v250 offset:9216\nM v[136:139],a[0:3],a[160:163],v[232:235]\nv_and_b32 v248,s3,v248\nv_cvt_pk_bf16_f32 v206,v156,v157\nM v[136:139],a[4:7],a[164:167],v[136:139]\n" \
"v_cvt_pk_bf16_f32 v207,v158,v159\nv_cvt_pk_bf16_f32 v212,v164,v165\nM v[136:139],a[8:11],a[168:171],v[136:139]\nv_cvt_pk_bf16_f32 v213,v166,v167\nv_cvt_pk_bf16_f32 v216,v168,v169\nM v[136:139],a[12:15],a[172:175],v[136:139]\nv_cvt_pk_bf16_f32 v217,v170,v171\ns_and_b32 s71,s3,57344\n" \
"M v[140:143],a[12:15],a[188:191],v[236:239]\nv_exp_f32 v172,v172\nM v[140:143],a[8:11],a[184:187],v[140:143]\nds_read_b128 a[72:75],v250 offset:2048\nds_read_b128 a[104:107],v250 offset:10240\nM v[140:143],a[4:7],a[180:183],v[140:143]\nglobal_load_dwordx2 a[212:213],v248,s[22:23]\nM v[140:143],a[0:3],a[176:179],v[140:143]\n" \
"v_exp_f32 v173,v173\nM v[144:147],a[16:19],a[128:131],v[224:227]\nv_exp_f32 v174,v174\nM v[144:147],a[20:23],a[132:135],v[144:147]\nv_exp_f32 v175,v175\nM v[144:147],a[24:27],a[136:139],v[144:147]\nv_cvt_pk_bf16_f32 v220,v172,v173\nv_cvt_pk_bf16_f32 v221,v174,v175\n" \
"M v[144:147],a[28:31],a[140:143],v[144:147]\nds_read_b128 a[76:79],v250 offset:3072\nds_read_b128 a[108:111],v250 offset:11264\nM v[148:151],a[28:31],a[156:159],v[228:231]\nv_cvt_pk_bf16_f32 v214,v180,v181\nv_cvt_pk_bf16_f32 v215,v182,v183\nM v[148:151],a[24:27],a[152:155],v[148:151]\nv_exp_f32 v184,v184\n" \
"M v[148:151],a[20:23],a[148:151],v[148:151]\nbuffer_load_dwordx4 v240,s[44:47],s95 offen offset:1024 sc1 lds\nM v[148:151],a[16:19],a[144:147],v[148:151]\nv_exp_f32 v185,v185\nM v[152:155],a[16:19],a[160:163],v[232:235]\nv_exp_f32 v186,v186\nM v[152:155],a[20:23],a[164:167],v[152:155]\nds_read_b128 a[80:83],v250 offset:4096\n" \
"ds_read_b128 a[112:115],v250 offset:12288\nM v[152:155],a[24:27],a[168:171],v[152:155]\nv_exp_f32 v187,v187\nM v[152:155],a[28:31],a[172:175],v[152:155]\nv_cvt_pk_bf16_f32 v218,v184,v185\nv_cvt_pk_bf16_f32 v219,v186,v187\nM v[156:159],a[28:31],a[188:191],v[236:239]\nv_exp_f32 v188,v188\n" \
"M v[156:159],a[24:27],a[184:187],v[156:159]\nv_exp_f32 v189,v189\nM v[156:159],a[20:23],a[180:183],v[156:159]\nv_exp_f32 v190,v190\nM v[156:159],a[16:19],a[176:179],v[156:159]\nds_read_b128 a[84:87],v250 offset:5120\nds_read_b128 a[116:119],v250 offset:13312\nM v[160:163],a[32:35],a[128:131],v[224:227]\n" \
"global_load_dwordx2 a[228:229],v248,s[24:25]\nM v[160:163],a[36:39],a[132:135],v[160:163]\nv_add_u32 v248,s71,v248\nv_cvt_pk_bf16_f32 v222,v188,v189\nM v[160:163],a[40:43],a[136:139],v[160:163]\nv_exp_f32 v191,v191\nM v[160:163],a[44:47],a[140:143],v[160:163]\nv_cvt_pk_bf16_f32 v223,v190,v191\n" \
"s_add_u32 s94,s94,0x4000\nM v[164:167],a[44:47],a[156:159],v[228:231]\nv_exp_f32 v128,v128\nM v[164:167],a[40:43],a[152:155],v[164:167]\nds_read_b128 a[88:91],v250 offset:6144\nds_read_b128 a[120:123],v250 offset:14336\nM v[164:167],a[36:39],a[148:151],v[164:167]\nv_exp_f32 v129,v129\n" \
"M v[164:167],a[32:35],a[144:147],v[164:167]\nv_exp_f32 v130,v130\nM v[168:171],a[32:35],a[160:163],v[232:235]\nbuffer_load_dwordx4 v240,s[44:47],s95 offen offset:2048 sc1 lds\nM v[168:171],a[36:39],a[164:167],v[168:171]\nv_exp_f32 v131,v131\nM v[168:171],a[40:43],a[168:171],v[168:171]\nv_exp_f32 v144,v144\n" \
"M v[168:171],a[44:47],a[172:175],v[168:171]\nds_read_b128 a[92:95],v250 offset:7168\nds_read_b128 a[124:127],v250 offset:15360\nM v[172:175],a[44:47],a[188:191],v[236:239]\nv_xor_b32 v250,0x18000,v250\ns_and_b32 s94,s94,0xffff\nM v[172:175],a[40:43],a[184:187],v[172:175]\nv_exp_f32 v145,v145\n" \
"M v[172:175],a[36:39],a[180:183],v[172:175]\nv_exp_f32 v146,v146\nM v[172:175],a[32:35],a[176:179],v[172:175]\ns_waitcnt lgkmcnt(4)\nM v[176:179],a[48:51],a[128:131],v[224:227]\nv_exp_f32 v147,v147\nM v[176:179],a[52:55],a[132:135],v[176:179]\nv_exp_f32 v132,v132\n" \
"M v[176:179],a[56:59],a[136:139],v[176:179]\nglobal_load_dwordx2 a[214:215],v248,s[22:23]\nM v[176:179],a[60:63],a[140:143],v[176:179]\nv_exp_f32 v133,v133\nM v[180:183],a[60:63],a[156:159],v[228:231]\nv_exp_f32 v134,v134\nM v[180:183],a[56:59],a[152:155],v[180:183]\nv_exp_f32 v135,v135\n" \
"M v[180:183],a[52:55],a[148:151],v[180:183]\nv_exp_f32 v148,v148\nM v[180:183],a[48:51],a[144:147],v[180:183]\nv_exp_f32 v149,v149\nM v[184:187],a[48:51],a[160:163],v[232:235]\nv_exp_f32 v150,v150\nM v[184:187],a[52:55],a[164:167],v[184:187]\nv_exp_f32 v151,v151\n" \
"M v[184:187],a[56:59],a[168:171],v[184:187]\nbuffer_load_dwordx4 v240,s[44:47],s95 offen offset:3072 sc1 lds\nM v[184:187],a[60:63],a[172:175],v[184:187]\nv_exp_f32 v136,v136\nM v[188:191],a[60:63],a[188:191],v[236:239]\nv_exp_f32 v137,v137\nM v[188:191],a[56:59],a[184:187],v[188:191]\nv_exp_f32 v138,v138\n" \
"M v[188:191],a[52:55],a[180:183],v[188:191]\nglobal_load_dwordx2 a[230:231],v248,s[24:25]\nM v[188:191],a[48:51],a[176:179],v[188:191]\ns_add_u32 m0,m0,0x10000\nv_add_u32 v248,s71,v248\nM v[0:3],a[64:67],v[192:195],v[0:3]\ns_waitcnt vmcnt(16) lgkmcnt(0)\ns_barrier\n" \
"M v[0:3],a[96:99],v[208:211],v[0:3]\nds_read_b128 a[0:3],v250 offset:0\nds_read_b128 a[4:7],v250 offset:1024\nM v[8:11],a[68:71],v[192:195],v[8:11]\nv_exp_f32 v139,v139\nM v[8:11],a[100:103],v[208:211],v[8:11]\nv_exp_f32 v152,v152\nM v[16:19],a[72:75],v[192:195],v[16:19]\n" \
"v_exp_f32 v153,v153\nM v[16:19],a[104:107],v[208:211],v[16:19]\nbuffer_load_dwordx4 v240,s[48:51],s95 offen offset:0 sc1 lds\nM v[24:27],a[76:79],v[192:195],v[24:27]\nv_exp_f32 v154,v154\nM v[24:27],a[108:111],v[208:211],v[24:27]\nv_exp_f32 v155,v155\nM v[32:35],a[80:83],v[192:195],v[32:35]\n" \
"v_exp_f32 v160,v160\nM v[32:35],a[112:115],v[208:211],v[32:35]\nds_read_b128 a[8:11],v250 offset:2048\nds_read_b128 a[12:15],v250 offset:3072\nM v[40:43],a[84:87],v[192:195],v[40:43]\nv_exp_f32 v161,v161\nM v[40:43],a[116:119],v[208:211],v[40:43]\nv_exp_f32 v162,v162\n" \
"M v[48:51],a[88:91],v[192:195],v[48:51]\nv_exp_f32 v163,v163\nM v[48:51],a[120:123],v[208:211],v[48:51]\nglobal_load_dwordx2 a[216:217],v248,s[22:23]\nM v[56:59],a[92:95],v[192:195],v[56:59]\nv_exp_f32 v176,v176\nM v[56:59],a[124:127],v[208:211],v[56:59]\nv_exp_f32 v177,v177\n" \
"M a[192:195],a[208:211],v[192:195],a[192:195]\nv_exp_f32 v178,v178\nM a[192:195],a[208:211],v[208:211],a[192:195]\nds_read_b128 a[16:19],v250 offset:4096\nds_read_b128 a[20:23],v250 offset:5120\nM v[4:7],a[64:67],v[196:199],v[4:7]\nv_exp_f32 v179,v179\nM v[4:7],a[96:99],v[212:215],v[4:7]\n" \
"v_exp_f32 v140,v140\nM v[12:15],a[68:71],v[196:199],v[12:15]\nv_exp_f32 v141,v141\nM v[12:15],a[100:103],v[212:215],v[12:15]\nbuffer_load_dwordx4 v240,s[48:51],s95 offen offset:1024 sc1 lds\nM v[20:23],a[72:75],v[196:199],v[20:23]\nv_exp_f32 v142,v142\nM v[20:23],a[104:107],v[212:215],v[20:23]\n" \
"v_exp_f32 v143,v143\nM v[28:31],a[76:79],v[196:199],v[28:31]\nv_exp_f32 v156,v156\nM v[28:31],a[108:111],v[212:215],v[28:31]\nds_read_b128 a[24:27],v250 offset:6144\nds_read_b128 a[28:31],v250 offset:7168\nM v[36:39],a[80:83],v[196:199],v[36:39]\nv_exp_f32 v157,v157\n" \
"M v[36:39],a[112:115],v[212:215],v[36:39]\nv_exp_f32 v158,v158\nM v[44:47],a[84:87],v[196:199],v[44:47]\nv_exp_f32 v159,v159\nM v[44:47],a[116:119],v[212:215],v[44:47]\nglobal_load_dwordx2 a[232:233],v248,s[24:25]\nM v[52:55],a[88:91],v[196:199],v[52:55]\nv_add_u32 v248,s71,v248\n" \
"v_cvt_pk_bf16_f32 v192,v128,v129\nM v[52:55],a[120:123],v[212:215],v[52:55]\nv_exp_f32 v164,v164\nM v[60:63],a[92:95],v[196:199],v[60:63]\nv_exp_f32 v165,v165\nM v[60:63],a[124:127],v[212:215],v[60:63]\nds_read_b128 a[32:35],v250 offset:8192\nds_read_b128 a[36:39],v250 offset:9216\n" \
"M a[196:199],a[208:211],v[196:199],a[196:199]\nv_exp_f32 v166,v166\nM a[196:199],a[208:211],v[212:215],a[196:199]\nv_exp_f32 v167,v167\nM v[64:67],a[64:67],v[200:203],v[64:67]\nv_exp_f32 v180,v180\nM v[64:67],a[96:99],v[216:219],v[64:67]\nbuffer_load_dwordx4 v240,s[48:51],s95 offen offset:2048 sc1 lds\n" \
"M v[72:75],a[68:71],v[200:203],v[72:75]\nv_exp_f32 v181,v181\nM v[72:75],a[100:103],v[216:219],v[72:75]\nv_exp_f32 v182,v182\nM v[80:83],a[72:75],v[200:203],v[80:83]\nv_exp_f32 v183,v183\nM v[80:83],a[104:107],v[216:219],v[80:83]\nds_read_b128 a[40:43],v250 offset:10240\n" \
"ds_read_b128 a[44:47],v250 offset:11264\nM v[88:91],a[76:79],v[200:203],v[88:91]\nv_exp_f32 v168,v168\nM v[88:91],a[108:111],v[216:219],v[88:91]\nv_exp_f32 v169,v169\nM v[96:99],a[80:83],v[200:203],v[96:99]\nv_exp_f32 v170,v170\nM v[96:99],a[112:115],v[216:219],v[96:99]\n" \
"global_load_dwordx2 a[218:219],v248,s[22:23]\nM v[104:107],a[84:87],v[200:203],v[104:107]\nv_exp_f32 v171,v171\nM v[104:107],a[116:119],v[216:219],v[104:107]\nv_cvt_pk_bf16_f32 v193,v130,v131\nv_cvt_pk_bf16_f32 v194,v144,v145\nM v[112:115],a[88:91],v[200:203],v[112:115]\nv_cvt_pk_bf16_f32 v195,v146,v147\n" \
"v_cvt_pk_bf16_f32 v208,v160,v161\nM v[112:115],a[120:123],v[216:219],v[112:115]\nds_read_b128 a[48:51],v250 offset:12288\nds_read_b128 a[52:55],v250 offset:13312\nM v[120:123],a[92:95],v[200:203],v[120:123]\nv_cvt_pk_bf16_f32 v209,v162,v163\nv_cvt_pk_bf16_f32 v210,v176,v177\nM v[120:123],a[124:127],v[216:219],v[120:123]\n" \
"v_cvt_pk_bf16_f32 v211,v178,v179\nv_cvt_pk_bf16_f32 v196,v132,v133\nM a[200:203],a[208:211],v[200:203],a[200:203]\nv_cvt_pk_bf16_f32 v197,v134,v135\nv_cvt_pk_bf16_f32 v198,v148,v149\nM a[200:203],a[208:211],v[216:219],a[200:203]\nbuffer_load_dwordx4 v240,s[48:51],s95 offen offset:3072 sc1 lds\nM v[68:71],a[64:67],v[204:207],v[68:71]\n" \
"v_cvt_pk_bf16_f32 v199,v150,v151\nv_cvt_pk_bf16_f32 v200,v136,v137\nM v[68:71],a[96:99],v[220:223],v[68:71]\nv_cvt_pk_bf16_f32 v201,v138,v139\nv_cvt_pk_bf16_f32 v202,v152,v153\nM v[76:79],a[68:71],v[204:207],v[76:79]\nv_cvt_pk_bf16_f32 v203,v154,v155\ns_add_u32 s95,s95,0x4000\n" \
"M v[76:79],a[100:103],v[220:223],v[76:79]\nds_read_b128 a[56:59],v250 offset:14336\nds_read_b128 a[60:63],v250 offset:15360\nM v[84:87],a[72:75],v[204:207],v[84:87]\nv_add_u32 v250,s94,v241\nM v[84:87],a[104:107],v[220:223],v[84:87]\nM v[92:95],a[76:79],v[204:207],v[92:95]\nM v[92:95],a[108:111],v[220:223],v[92:95]\n" \
"global_load_dwordx2 a[234:235],v248,s[24:25]\nM v[100:103],a[80:83],v[204:207],v[100:103]\nv_add_u32 v248,s71,v248\nM v[100:103],a[112:115],v[220:223],v[100:103]\nM v[108:111],a[84:87],v[204:207],v[108:111]\nM v[108:111],a[116:119],v[220:223],v[108:111]\nM v[116:119],a[88:91],v[204:207],v[116:119]\nM v[116:119],a[120:123],v[220:223],v[116:119]\n" \
"M v[124:127],a[92:95],v[204:207],v[124:127]\nM v[124:127],a[124:127],v[220:223],v[124:127]\nM a[204:207],a[208:211],v[204:207],a[204:207]\nM a[204:207],a[208:211],v[220:223],a[204:207]\ns_waitcnt lgkmcnt(4)\nM v[128:131],a[0:3],a[128:131],v[224:227]\ns_add_u32 m0,s94,s64\nv_cvt_pk_bf16_f32 v204,v140,v141\n" \
"M v[128:131],a[4:7],a[132:135],v[128:131]\nds_read_b128 a[64:67],v250 offset:0\nds_read_b128 a[96:99],v250 offset:8192\nM v[128:131],a[8:11],a[136:139],v[128:131]\nv_cvt_pk_bf16_f32 v205,v142,v143\nv_cvt_pk_bf16_f32 v206,v156,v157\nM v[128:131],a[12:15],a[140:143],v[128:131]\nv_cvt_pk_bf16_f32 v207,v158,v159\n" \
"v_cvt_pk_bf16_f32 v212,v164,v165\nM v[132:135],a[12:15],a[156:159],v[228:231]\nbuffer_load_dwordx4 v240,s[44:47],s95 offen offset:0 sc1 lds\nM v[132:135],a[8:11],a[152:155],v[132:135]\nv_cvt_pk_bf16_f32 v213,v166,v167\nv_cvt_pk_bf16_f32 v216,v168,v169\nM v[132:135],a[4:7],a[148:151],v[132:135]\nv_cvt_pk_bf16_f32 v217,v170,v171\n" \
"v_cvt_pk_bf16_f32 v214,v180,v181\nM v[132:135],a[0:3],a[144:147],v[132:135]\nds_read_b128 a[68:71],v250 offset:1024\nds_read_b128 a[100:103],v250 offset:9216\nM v[136:139],a[0:3],a[160:163],v[232:235]\nv_exp_f32 v172,v172\nM v[136:139],a[4:7],a[164:167],v[136:139]\nv_exp_f32 v173,v173\n" \
"M v[136:139],a[8:11],a[168:171],v[136:139]\nv_exp_f32 v174,v174\nM v[136:139],a[12:15],a[172:175],v[136:139]\nv_exp_f32 v175,v175\nM v[140:143],a[12:15],a[188:191],v[236:239]\nv_cvt_pk_bf16_f32 v220,v172,v173\nv_cvt_pk_bf16_f32 v221,v174,v175\nM v[140:143],a[8:11],a[184:187],v[140:143]\n" \
"ds_read_b128 a[72:75],v250 offset:2048\nds_read_b128 a[104:107],v250 offset:10240\nM v[140:143],a[4:7],a[180:183],v[140:143]\nglobal_load_dwordx2 a[220:221],v248,s[22:23]\nM v[140:143],a[0:3],a[176:179],v[140:143]\nv_cvt_pk_bf16_f32 v215,v182,v183\ns_add_u32 s94,s94,0x4000\nM v[144:147],a[16:19],a[128:131],v[224:227]\n" \
"v_exp_f32 v184,v184\nM v[144:147],a[20:23],a[132:135],v[144:147]\nv_exp_f32 v185,v185\nM v[144:147],a[24:27],a[136:139],v[144:147]\nv_exp_f32 v186,v186\nM v[144:147],a[28:31],a[140:143],v[144:147]\nds_read_b128 a[76:79],v250 offset:3072\nds_read_b128 a[108:111],v250 offset:11264\n" \
"M v[148:151],a[28:31],a[156:159],v[228:231]\nv_exp_f32 v187,v187\nM v[148:151],a[24:27],a[152:155],v[148:151]\nv_cvt_pk_bf16_f32 v218,v184,v185\nv_cvt_pk_bf16_f32 v219,v186,v187\nM v[148:151],a[20:23],a[148:151],v[148:151]\nbuffer_load_dwordx4 v240,s[44:47],s95 offen offset:1024 sc1 lds\nM v[148:151],a[16:19],a[144:147],v[148:151]\n" \
"v_exp_f32 v188,v188\nM v[152:155],a[16:19],a[160:163],v[232:235]\nv_exp_f32 v189,v189\nM v[152:155],a[20:23],a[164:167],v[152:155]\nds_read_b128 a[80:83],v250 offset:4096\nds_read_b128 a[112:115],v250 offset:12288\nM v[152:155],a[24:27],a[168:171],v[152:155]\nv_exp_f32 v190,v190\n" \
"M v[152:155],a[28:31],a[172:175],v[152:155]\nv_exp_f32 v191,v191\nM v[156:159],a[28:31],a[188:191],v[236:239]\nv_cvt_pk_bf16_f32 v222,v188,v189\nv_cvt_pk_bf16_f32 v223,v190,v191\nM v[156:159],a[24:27],a[184:187],v[156:159]\nv_exp_f32 v128,v128\nM v[156:159],a[20:23],a[180:183],v[156:159]\n" \
"v_exp_f32 v129,v129\nM v[156:159],a[16:19],a[176:179],v[156:159]\nds_read_b128 a[84:87],v250 offset:5120\nds_read_b128 a[116:119],v250 offset:13312\nM v[160:163],a[32:35],a[128:131],v[224:227]\nglobal_load_dwordx2 a[236:237],v248,s[24:25]\nM v[160:163],a[36:39],a[132:135],v[160:163]\nv_add_u32 v248,s71,v248\n" \
"s_and_b32 s94,s94,0xffff\nM v[160:163],a[40:43],a[136:139],v[160:163]\nv_exp_f32 v130,v130\nM v[160:163],a[44:47],a[140:143],v[160:163]\nv_exp_f32 v131,v131\nM v[164:167],a[44:47],a[156:159],v[228:231]\nv_exp_f32 v144,v144\nM v[164:167],a[40:43],a[152:155],v[164:167]\n" \
"ds_read_b128 a[88:91],v250 offset:6144\nds_read_b128 a[120:123],v250 offset:14336\nM v[164:167],a[36:39],a[148:151],v[164:167]\nv_exp_f32 v145,v145\nM v[164:167],a[32:35],a[144:147],v[164:167]\nv_exp_f32 v146,v146\nM v[168:171],a[32:35],a[160:163],v[232:235]\nbuffer_load_dwordx4 v240,s[44:47],s95 offen offset:2048 sc1 lds\n" \
"M v[168:171],a[36:39],a[164:167],v[168:171]\nv_exp_f32 v147,v147\nM v[168:171],a[40:43],a[168:171],v[168:171]\nv_exp_f32 v132,v132\nM v[168:171],a[44:47],a[172:175],v[168:171]\nds_read_b128 a[92:95],v250 offset:7168\nds_read_b128 a[124:127],v250 offset:15360\nM v[172:175],a[44:47],a[188:191],v[236:239]\n" \
"v_exp_f32 v133,v133\nM v[172:175],a[40:43],a[184:187],v[172:175]\nv_exp_f32 v134,v134\nM v[172:175],a[36:39],a[180:183],v[172:175]\nv_exp_f32 v135,v135\nM v[172:175],a[32:35],a[176:179],v[172:175]\nv_xor_b32 v250,0x18000,v250\ns_waitcnt lgkmcnt(3)\n" \
"M v[176:179],a[48:51],a[128:131],v[224:227]\nv_exp_f32 v148,v148\nM v[176:179],a[52:55],a[132:135],v[176:179]\nv_exp_f32 v149,v149\nM v[176:179],a[56:59],a[136:139],v[176:179]\nglobal_load_dwordx2 a[222:223],v248,s[22:23]\nM v[176:179],a[60:63],a[140:143],v[176:179]\nv_exp_f32 v150,v150\n" \
"M v[180:183],a[60:63],a[156:159],v[228:231]\nv_exp_f32 v151,v151\nM v[180:183],a[56:59],a[152:155],v[180:183]\nv_exp_f32 v136,v136\nM v[180:183],a[52:55],a[148:151],v[180:183]\nv_exp_f32 v137,v137\nM v[180:183],a[48:51],a[144:147],v[180:183]\nv_exp_f32 v138,v138\n" \
"M v[184:187],a[48:51],a[160:163],v[232:235]\nv_exp_f32 v139,v139\nM v[184:187],a[52:55],a[164:167],v[184:187]\nv_exp_f32 v152,v152\nM v[184:187],a[56:59],a[168:171],v[184:187]\nbuffer_load_dwordx4 v240,s[44:47],s95 offen offset:3072 sc1 lds\nM v[184:187],a[60:63],a[172:175],v[184:187]\nv_exp_f32 v153,v153\n" \
"M v[188:191],a[60:63],a[188:191],v[236:239]\nv_exp_f32 v154,v154\nM v[188:191],a[56:59],a[184:187],v[188:191]\nv_exp_f32 v155,v155\nM v[188:191],a[52:55],a[180:183],v[188:191]\nglobal_load_dwordx2 a[238:239],v248,s[24:25]\nM v[188:191],a[48:51],a[176:179],v[188:191]\ns_add_u32 m0,m0,0x10000\n" \
"v_add_u32 v248,s71,v248\nM v[0:3],a[64:67],v[192:195],v[0:3]\ns_waitcnt vmcnt(16) lgkmcnt(0)\ns_barrier\nM v[0:3],a[96:99],v[208:211],v[0:3]\nds_read_b128 a[0:3],v250 offset:0\nds_read_b128 a[4:7],v250 offset:1024\nM v[8:11],a[68:71],v[192:195],v[8:11]\n" \
"v_exp_f32 v160,v160\nM v[8:11],a[100:103],v[208:211],v[8:11]\nv_exp_f32 v161,v161\nM v[16:19],a[72:75],v[192:195],v[16:19]\nv_exp_f32 v162,v162\nM v[16:19],a[104:107],v[208:211],v[16:19]\nbuffer_load_dwordx4 v240,s[48:51],s95 offen offset:0 sc1 lds\nM v[24:27],a[76:79],v[192:195],v[24:27]\n" \
"v_exp_f32 v163,v163\nM v[24:27],a[108:111],v[208:211],v[24:27]\nv_exp_f32 v176,v176\nM v[32:35],a[80:83],v[192:195],v[32:35]\nv_exp_f32 v177,v177\nM v[32:35],a[112:115],v[208:211],v[32:35]\nds_read_b128 a[8:11],v250 offset:2048\nds_read_b128 a[12:15],v250 offset:3072\n" \
"M v[40:43],a[84:87],v[192:195],v[40:43]\nv_exp_f32 v178,v178\nM v[40:43],a[116:119],v[208:211],v[40:43]\nv_exp_f32 v179,v179\nM v[48:51],a[88:91],v[192:195],v[48:51]\nv_exp_f32 v140,v140\nM v[48:51],a[120:123],v[208:211],v[48:51]\nglobal_load_dwordx2 a[224:225],v248,s[22:23]\n" \
"M v[56:59],a[92:95],v[192:195],v[56:59]\nv_exp_f32 v141,v141\nM v[56:59],a[124:127],v[208:211],v[56:59]\nv_exp_f32 v142,v142\nM a[192:195],a[208:211],v[192:195],a[192:195]\nv_exp_f32 v143,v143\nM a[192:195],a[208:211],v[208:211],a[192:195]\nds_read_b128 a[16:19],v250 offset:4096\n" \
"ds_read_b128 a[20:23],v250 offset:5120\nM v[4:7],a[64:67],v[196:199],v[4:7]\nv_exp_f32 v156,v156\nM v[4:7],a[96:99],v[212:215],v[4:7]\nv_exp_f32 v157,v157\nM v[12:15],a[68:71],v[196:199],v[12:15]\nv_exp_f32 v158,v158\nM v[12:15],a[100:103],v[212:215],v[12:15]\n" \
"buffer_load_dwordx4 v240,s[48:51],s95 offen offset:1024 sc1 lds\nM v[20:23],a[72:75],v[196:199],v[20:23]\nv_exp_f32 v159,v159\nM v[20:23],a[104:107],v[212:215],v[20:23]\nv_exp_f32 v164,v164\nM v[28:31],a[76:79],v[196:199],v[28:31]\nv_exp_f32 v165,v165\nM v[28:31],a[108:111],v[212:215],v[28:31]\n" \
"ds_read_b128 a[24:27],v250 offset:6144\nds_read_b128 a[28:31],v250 offset:7168\nM v[36:39],a[80:83],v[196:199],v[36:39]\nv_exp_f32 v166,v166\nM v[36:39],a[112:115],v[212:215],v[36:39]\nv_exp_f32 v167,v167\nM v[44:47],a[84:87],v[196:199],v[44:47]\nv_exp_f32 v180,v180\n" \
"M v[44:47],a[116:119],v[212:215],v[44:47]\nglobal_load_dwordx2 a[240:241],v248,s[24:25]\nM v[52:55],a[88:91],v[196:199],v[52:55]\nv_add_u32 v248,s71,v248\nv_cvt_pk_bf16_f32 v192,v128,v129\nM v[52:55],a[120:123],v[212:215],v[52:55]\nv_exp_f32 v181,v181\nM v[60:63],a[92:95],v[196:199],v[60:63]\n" \
"v_exp_f32 v182,v182\nM v[60:63],a[124:127],v[212:215],v[60:63]\nds_read_b128 a[32:35],v250 offset:8192\nds_read_b128 a[36:39],v250 offset:9216\nM a[196:199],a[208:211],v[196:199],a[196:199]\nv_exp_f32 v183,v183\nM a[196:199],a[208:211],v[212:215],a[196:199]\nv_exp_f32 v168,v168\n" \
"M v[64:67],a[64:67],v[200:203],v[64:67]\nv_exp_f32 v169,v169\nM v[64:67],a[96:99],v[216:219],v[64:67]\nbuffer_load_dwordx4 v240,s[48:51],s95 offen offset:2048 sc1 lds\nM v[72:75],a[68:71],v[200:203],v[72:75]\nv_exp_f32 v170,v170\nM v[72:75],a[100:103],v[216:219],v[72:75]\nv_exp_f32 v171,v171\n" \
"M v[80:83],a[72:75],v[200:203],v[80:83]\nv_cvt_pk_bf16_f32 v193,v130,v131\nv_cvt_pk_bf16_f32 v194,v144,v145\nM v[80:83],a[104:107],v[216:219],v[80:83]\nds_read_b128 a[40:43],v250 offset:10240\nds_read_b128 a[44:47],v250 offset:11264\nM v[88:91],a[76:79],v[200:203],v[88:91]\nv_cvt_pk_bf16_f32 v195,v146,v147\n" \
"v_cvt_pk_bf16_f32 v208,v160,v161\nM v[88:91],a[108:111],v[216:219],v[88:91]\nv_cvt_pk_bf16_f32 v209,v162,v163\nv_cvt_pk_bf16_f32 v210,v176,v177\nM v[96:99],a[80:83],v[200:203],v[96:99]\nv_cvt_pk_bf16_f32 v211,v178,v179\nv_cvt_pk_bf16_f32 v196,v132,v133\nM v[96:99],a[112:115],v[216:219],v[96:99]\n" \
"global_load_dwordx2 a[226:227],v248,s[22:23]\nM v[104:107],a[84:87],v[200:203],v[104:107]\nv_cvt_pk_bf16_f32 v197,v134,v135\nv_cvt_pk_bf16_f32 v198,v148,v149\nM v[104:107],a[116:119],v[216:219],v[104:107]\nv_cvt_pk_bf16_f32 v199,v150,v151\nM v[112:115],a[88:91],v[200:203],v[112:115]\nM v[112:115],a[120:123],v[216:219],v[112:115]\n" \
"ds_read_b128 a[48:51],v250 offset:12288\nds_read_b128 a[52:55],v250 offset:13312\nM v[120:123],a[92:95],v[200:203],v[120:123]\nM v[120:123],a[124:127],v[216:219],v[120:123]\nM a[200:203],a[208:211],v[200:203],a[200:203]\nv_cvt_pk_bf16_f32 v200,v136,v137\nv_cvt_pk_bf16_f32 v201,v138,v139\nM a[200:203],a[208:211],v[216:219],a[200:203]\n" \
"buffer_load_dwordx4 v240,s[48:51],s95 offen offset:3072 sc1 lds\nM v[68:71],a[64:67],v[204:207],v[68:71]\nv_cvt_pk_bf16_f32 v202,v152,v153\nv_cvt_pk_bf16_f32 v203,v154,v155\nM v[68:71],a[96:99],v[220:223],v[68:71]\ns_add_u32 s95,s95,0x4000\nM v[76:79],a[68:71],v[204:207],v[76:79]\nM v[76:79],a[100:103],v[220:223],v[76:79]\n" \
"ds_read_b128 a[56:59],v250 offset:14336\nds_read_b128 a[60:63],v250 offset:15360\nM v[84:87],a[72:75],v[204:207],v[84:87]\nv_add_u32 v250,s94,v241\nM v[84:87],a[104:107],v[220:223],v[84:87]\nM v[92:95],a[76:79],v[204:207],v[92:95]\nM v[92:95],a[108:111],v[220:223],v[92:95]\nglobal_load_dwordx2 a[242:243],v248,s[24:25]\n" \
"M v[100:103],a[80:83],v[204:207],v[100:103]\nv_add_u32 v248,s71,v248\nM v[100:103],a[112:115],v[220:223],v[100:103]\nM v[108:111],a[84:87],v[204:207],v[108:111]\nM v[108:111],a[116:119],v[220:223],v[108:111]\nM v[116:119],a[88:91],v[204:207],v[116:119]\nM v[116:119],a[120:123],v[220:223],v[116:119]\nM v[124:127],a[92:95],v[204:207],v[124:127]\n" \
"M v[124:127],a[124:127],v[220:223],v[124:127]\nM a[204:207],a[208:211],v[204:207],a[204:207]\nM a[204:207],a[208:211],v[220:223],a[204:207]\ns_cmp_eq_u32 s62,7\ns_cbranch_scc0 .Ltrans\ns_waitcnt lgkmcnt(4)\nM v[136:139],a[0:3],a[160:163],v[232:235]\ns_add_u32 m0,s94,s64\n" \
"v_cvt_pk_bf16_f32 v204,v140,v141\nM v[136:139],a[4:7],a[164:167],v[136:139]\nds_read_b128 a[64:67],v250 offset:0\nds_read_b128 a[96:99],v250 offset:8192\nM v[136:139],a[8:11],a[168:171],v[136:139]\nbuffer_load_dwordx4 v240,s[44:47],s95 offen offset:0 sc1 lds\nM v[136:139],a[12:15],a[172:175],v[136:139]\nv_cvt_pk_bf16_f32 v205,v142,v143\n" \
"v_cvt_pk_bf16_f32 v206,v156,v157\nM v[140:143],a[12:15],a[188:191],v[236:239]\nds_read_b128 a[68:71],v250 offset:1024\nds_read_b128 a[100:103],v250 offset:9216\nM v[140:143],a[8:11],a[184:187],v[140:143]\nbuffer_store_dwordx4 a[248:251],v253,s[52:55],s33 offen offset:0 sc1\nM v[140:143],a[4:7],a[180:183],v[140:143]\nv_cvt_pk_bf16_f32 v207,v158,v159\n" \
"v_cvt_pk_bf16_f32 v216,v168,v169\nM v[140:143],a[0:3],a[176:179],v[140:143]\nds_read_b128 a[72:75],v250 offset:2048\nds_read_b128 a[104:107],v250 offset:10240\nM v[152:155],a[16:19],a[160:163],v[232:235]\nv_cvt_pk_bf16_f32 v217,v170,v171\ns_add_u32 s71,s33,16384\nM v[152:155],a[20:23],a[164:167],v[152:155]\n" \
"v_exp_f32 v172,v172\nM v[152:155],a[24:27],a[168:171],v[152:155]\nds_read_b128 a[76:79],v250 offset:3072\nds_read_b128 a[108:111],v250 offset:11264\nM v[152:155],a[28:31],a[172:175],v[152:155]\nbuffer_load_dwordx4 v240,s[44:47],s95 offen offset:1024 sc1 lds\nM v[156:159],a[28:31],a[188:191],v[236:239]\nv_exp_f32 v173,v173\n" \
"M v[156:159],a[24:27],a[184:187],v[156:159]\nds_read_b128 a[80:83],v250 offset:4096\nds_read_b128 a[112:115],v250 offset:12288\nM v[156:159],a[20:23],a[180:183],v[156:159]\nbuffer_store_dwordx4 a[248:251],v253,s[52:55],s33 offen offset:1024 sc1\nM v[156:159],a[16:19],a[176:179],v[156:159]\nv_exp_f32 v174,v174\nM v[168:171],a[32:35],a[160:163],v[232:235]\n" \
"ds_read_b128 a[84:87],v250 offset:5120\nds_read_b128 a[116:119],v250 offset:13312\nM v[168:171],a[36:39],a[164:167],v[168:171]\nv_exp_f32 v175,v175\nM v[168:171],a[40:43],a[168:171],v[168:171]\nv_cvt_pk_bf16_f32 v220,v172,v173\nv_cvt_pk_bf16_f32 v221,v174,v175\nM v[168:171],a[44:47],a[172:175],v[168:171]\n" \
"ds_read_b128 a[88:91],v250 offset:6144\nds_read_b128 a[120:123],v250 offset:14336\nM v[172:175],a[44:47],a[188:191],v[236:239]\nbuffer_load_dwordx4 v240,s[44:47],s95 offen offset:2048 sc1 lds\nM v[172:175],a[40:43],a[184:187],v[172:175]\nv_exp_f32 v184,v184\nM v[172:175],a[36:39],a[180:183],v[172:175]\nv_exp_f32 v185,v185\n" \
"v_exp_f32 v186,v186\nv_exp_f32 v187,v187\nds_read_b128 a[92:95],v250 offset:7168\nds_read_b128 a[124:127],v250 offset:15360\nM v[172:175],a[32:35],a[176:179],v[172:175]\nbuffer_store_dwordx4 a[248:251],v253,s[52:55],s33 offen offset:2048 sc1\nv_cvt_pk_bf16_f32 v218,v184,v185\nv_cvt_pk_bf16_f32 v219,v186,v187\n" \
"s_waitcnt lgkmcnt(6)\nM v[184:187],a[48:51],a[160:163],v[232:235]\nv_exp_f32 v188,v188\nM v[184:187],a[52:55],a[164:167],v[184:187]\nv_exp_f32 v189,v189\nM v[184:187],a[56:59],a[168:171],v[184:187]\nv_exp_f32 v190,v190\nv_exp_f32 v191,v191\n" \
"M v[184:187],a[60:63],a[172:175],v[184:187]\nv_cvt_pk_bf16_f32 v222,v188,v189\nv_cvt_pk_bf16_f32 v223,v190,v191\nM v[188:191],a[60:63],a[188:191],v[236:239]\nv_xor_b32 v250,0x18000,v250\ns_add_u32 s70,s78,16384\nM v[188:191],a[56:59],a[184:187],v[188:191]\nbuffer_load_dwordx4 v240,s[44:47],s95 offen offset:3072 sc1 lds\n" \
"M v[188:191],a[52:55],a[180:183],v[188:191]\ns_add_u32 m0,m0,0x10000\nv_cvt_pk_bf16_f32 v212,v164,v165\nM v[188:191],a[48:51],a[176:179],v[188:191]\nv_cvt_pk_bf16_f32 v213,v166,v167\nv_cvt_pk_bf16_f32 v214,v180,v181\nM v[64:67],a[64:67],v[200:203],v[64:67]\nbuffer_store_dwordx4 a[248:251],v253,s[52:55],s33 offen offset:3072 sc1\n" \
"M v[64:67],a[96:99],v[216:219],v[64:67]\nv_cvt_pk_bf16_f32 v215,v182,v183\ns_add_u32 s94,s94,0x4000\nM v[72:75],a[68:71],v[200:203],v[72:75]\ns_waitcnt vmcnt(16) lgkmcnt(0)\ns_barrier\nM v[72:75],a[100:103],v[216:219],v[72:75]\nds_read_b128 a[0:3],v250 offset:0\n" \
"ds_read_b128 a[4:7],v250 offset:1024\nM v[80:83],a[72:75],v[200:203],v[80:83]\nv_exp_f32 v136,v136\nM v[80:83],a[104:107],v[216:219],v[80:83]\nbuffer_store_dwordx4 a[248:251],v253,s[52:55],s71 offen offset:0 sc1\nM v[88:91],a[76:79],v[200:203],v[88:91]\nv_exp_f32 v137,v137\nM v[88:91],a[108:111],v[216:219],v[88:91]\n" \
"buffer_load_dwordx4 v240,s[48:51],s95 offen offset:0 sc1 lds\nM v[96:99],a[80:83],v[200:203],v[96:99]\nv_exp_f32 v138,v138\nM v[96:99],a[112:115],v[216:219],v[96:99]\nbuffer_store_dwordx4 a[248:251],v253,s[52:55],s71 offen offset:1024 sc1\nM v[104:107],a[84:87],v[200:203],v[104:107]\nv_exp_f32 v139,v139\nM v[104:107],a[116:119],v[216:219],v[104:107]\n" \
"ds_read_b128 a[8:11],v250 offset:2048\nds_read_b128 a[12:15],v250 offset:3072\nM v[112:115],a[88:91],v[200:203],v[112:115]\nv_exp_f32 v152,v152\nM v[112:115],a[120:123],v[216:219],v[112:115]\nbuffer_store_dwordx4 a[248:251],v253,s[52:55],s71 offen offset:2048 sc1\nM v[120:123],a[92:95],v[200:203],v[120:123]\nv_exp_f32 v153,v153\n" \
"M v[120:123],a[124:127],v[216:219],v[120:123]\nv_exp_f32 v154,v154\nM a[200:203],a[208:211],v[200:203],a[200:203]\nv_exp_f32 v155,v155\nM a[200:203],a[208:211],v[216:219],a[200:203]\nbuffer_store_dwordx4 a[248:251],v253,s[52:55],s71 offen offset:3072 sc1\nM v[68:71],a[64:67],v[204:207],v[68:71]\nv_exp_f32 v168,v168\n" \
"M v[68:71],a[96:99],v[220:223],v[68:71]\nds_read_b128 a[16:19],v250 offset:4096\nds_read_b128 a[20:23],v250 offset:5120\nM v[76:79],a[68:71],v[204:207],v[76:79]\nv_exp_f32 v169,v169\nM v[76:79],a[100:103],v[220:223],v[76:79]\nv_exp_f32 v170,v170\nM v[84:87],a[72:75],v[204:207],v[84:87]\n" \
"v_exp_f32 v171,v171\nM v[84:87],a[104:107],v[220:223],v[84:87]\nbuffer_load_dwordx4 v240,s[48:51],s95 offen offset:1024 sc1 lds\nM v[92:95],a[76:79],v[204:207],v[92:95]\nv_exp_f32 v184,v184\nM v[92:95],a[108:111],v[220:223],v[92:95]\nbuffer_store_dwordx4 a[248:251],v253,s[52:55],s78 offen offset:0 sc1\nM v[100:103],a[80:83],v[204:207],v[100:103]\n" \
"v_exp_f32 v185,v185\nM v[100:103],a[112:115],v[220:223],v[100:103]\nds_read_b128 a[24:27],v250 offset:6144\nds_read_b128 a[28:31],v250 offset:7168\nM v[108:111],a[84:87],v[204:207],v[108:111]\nv_exp_f32 v186,v186\nM v[108:111],a[116:119],v[220:223],v[108:111]\nbuffer_store_dwordx4 a[248:251],v253,s[52:55],s78 offen offset:1024 sc1\n" \
"M v[116:119],a[88:91],v[204:207],v[116:119]\nv_exp_f32 v187,v187\nM v[116:119],a[120:123],v[220:223],v[116:119]\ns_and_b32 s94,s94,0xffff\nv_cvt_pk_bf16_f32 v200,v136,v137\nM v[124:127],a[92:95],v[204:207],v[124:127]\nv_exp_f32 v140,v140\nM v[124:127],a[124:127],v[220:223],v[124:127]\n" \
"buffer_store_dwordx4 a[248:251],v253,s[52:55],s78 offen offset:2048 sc1\nM a[204:207],a[208:211],v[204:207],a[204:207]\nv_exp_f32 v141,v141\nM a[204:207],a[208:211],v[220:223],a[204:207]\nds_read_b128 a[32:35],v250 offset:8192\nds_read_b128 a[36:39],v250 offset:9216\nM v[0:3],a[64:67],v[192:195],v[0:3]\nv_exp_f32 v142,v142\n" \
"M v[0:3],a[96:99],v[208:211],v[0:3]\nv_exp_f32 v143,v143\nM v[8:11],a[68:71],v[192:195],v[8:11]\nv_exp_f32 v156,v156\nM v[8:11],a[100:103],v[208:211],v[8:11]\nbuffer_load_dwordx4 v240,s[48:51],s95 offen offset:2048 sc1 lds\nM v[16:19],a[72:75],v[192:195],v[16:19]\nv_exp_f32 v157,v157\n" \
"M v[16:19],a[104:107],v[208:211],v[16:19]\nbuffer_store_dwordx4 a[248:251],v253,s[52:55],s78 offen offset:3072 sc1\nM v[24:27],a[76:79],v[192:195],v[24:27]\nv_exp_f32 v158,v158\nM v[24:27],a[108:111],v[208:211],v[24:27]\nds_read_b128 a[40:43],v250 offset:10240\nds_read_b128 a[44:47],v250 offset:11264\nM v[32:35],a[80:83],v[192:195],v[32:35]\n" \
"v_exp_f32 v159,v159\nM v[32:35],a[112:115],v[208:211],v[32:35]\nbuffer_store_dwordx4 a[248:251],v253,s[52:55],s70 offen offset:0 sc1\nM v[40:43],a[84:87],v[192:195],v[40:43]\nv_cvt_pk_bf16_f32 v201,v138,v139\nv_cvt_pk_bf16_f32 v202,v152,v153\nM v[40:43],a[116:119],v[208:211],v[40:43]\nv_cvt_pk_bf16_f32 v203,v154,v155\n" \
"v_cvt_pk_bf16_f32 v216,v168,v169\nM v[48:51],a[88:91],v[192:195],v[48:51]\nv_cvt_pk_bf16_f32 v217,v170,v171\nv_cvt_pk_bf16_f32 v218,v184,v185\nM v[48:51],a[120:123],v[208:211],v[48:51]\nbuffer_store_dwordx4 a[248:251],v253,s[52:55],s70 offen offset:1024 sc1\nM v[56:59],a[92:95],v[192:195],v[56:59]\nv_cvt_pk_bf16_f32 v219,v186,v187\n" \
"M v[56:59],a[124:127],v[208:211],v[56:59]\nds_read_b128 a[48:51],v250 offset:12288\nds_read_b128 a[52:55],v250 offset:13312\nM a[192:195],a[208:211],v[192:195],a[192:195]\nM a[192:195],a[208:211],v[208:211],a[192:195]\nM v[4:7],a[64:67],v[196:199],v[4:7]\nM v[4:7],a[96:99],v[212:215],v[4:7]\nbuffer_load_dwordx4 v240,s[48:51],s95 offen offset:3072 sc1 lds\n" \
"M v[12:15],a[68:71],v[196:199],v[12:15]\ns_add_u32 s95,s95,0xffff4000\nM v[12:15],a[100:103],v[212:215],v[12:15]\nbuffer_store_dwordx4 a[248:251],v253,s[52:55],s70 offen offset:2048 sc1\nM v[20:23],a[72:75],v[196:199],v[20:23]\nM v[20:23],a[104:107],v[212:215],v[20:23]\nds_read_b128 a[56:59],v250 offset:14336\nds_read_b128 a[60:63],v250 offset:15360\n" \
"M v[28:31],a[76:79],v[196:199],v[28:31]\nv_add_u32 v250,s94,v241\nM v[28:31],a[108:111],v[212:215],v[28:31]\nbuffer_store_dwordx4 a[248:251],v253,s[52:55],s70 offen offset:3072 sc1\nM v[36:39],a[80:83],v[196:199],v[36:39]\nM v[36:39],a[112:115],v[212:215],v[36:39]\nM v[44:47],a[84:87],v[196:199],v[44:47]\nM v[44:47],a[116:119],v[212:215],v[44:47]\n" \
"M v[52:55],a[88:91],v[196:199],v[52:55]\nM v[52:55],a[120:123],v[212:215],v[52:55]\nM v[60:63],a[92:95],v[196:199],v[60:63]\nM v[60:63],a[124:127],v[212:215],v[60:63]\nM a[196:199],a[208:211],v[196:199],a[196:199]\nM a[196:199],a[208:211],v[212:215],a[196:199]\n.Lsl:\ns_waitcnt lgkmcnt(4)\n" \
"M v[136:139],a[0:3],a[160:163],v[232:235]\ns_add_u32 m0,s94,s64\nv_cvt_pk_bf16_f32 v204,v140,v141\nM v[136:139],a[4:7],a[164:167],v[136:139]\nds_read_b128 a[64:67],v250 offset:0\nds_read_b128 a[96:99],v250 offset:8192\nM v[136:139],a[8:11],a[168:171],v[136:139]\nbuffer_load_dwordx4 v240,s[44:47],s95 offen offset:0 sc1 lds\n" \
"M v[136:139],a[12:15],a[172:175],v[136:139]\nv_cvt_pk_bf16_f32 v205,v142,v143\nv_cvt_pk_bf16_f32 v206,v156,v157\nM v[140:143],a[12:15],a[188:191],v[236:239]\nds_read_b128 a[68:71],v250 offset:1024\nds_read_b128 a[100:103],v250 offset:9216\nM v[140:143],a[8:11],a[184:187],v[140:143]\nv_cvt_pk_bf16_f32 v207,v158,v159\n" \
"s_add_u32 s94,s94,0x4000\nM v[140:143],a[4:7],a[180:183],v[140:143]\nv_exp_f32 v172,v172\nM v[140:143],a[0:3],a[176:179],v[140:143]\nds_read_b128 a[72:75],v250 offset:2048\nds_read_b128 a[104:107],v250 offset:10240\nM v[152:155],a[16:19],a[160:163],v[232:235]\nv_exp_f32 v173,v173\n" \
"M v[152:155],a[20:23],a[164:167],v[152:155]\nv_exp_f32 v174,v174\nM v[152:155],a[24:27],a[168:171],v[152:155]\nds_read_b128 a[76:79],v250 offset:3072\nds_read_b128 a[108:111],v250 offset:11264\nM v[152:155],a[28:31],a[172:175],v[152:155]\nbuffer_load_dwordx4 v240,s[44:47],s95 offen offset:1024 sc1 lds\nM v[156:159],a[28:31],a[188:191],v[236:239]\n" \
"v_exp_f32 v175,v175\nM v[156:159],a[24:27],a[184:187],v[156:159]\nds_read_b128 a[80:83],v250 offset:4096\nds_read_b128 a[112:115],v250 offset:12288\nM v[156:159],a[20:23],a[180:183],v[156:159]\nv_cvt_pk_bf16_f32 v220,v172,v173\nv_cvt_pk_bf16_f32 v221,v174,v175\nM v[156:159],a[16:19],a[176:179],v[156:159]\n" \
"v_exp_f32 v188,v188\nM v[168:171],a[32:35],a[160:163],v[232:235]\nds_read_b128 a[84:87],v250 offset:5120\nds_read_b128 a[116:119],v250 offset:13312\nM v[168:171],a[36:39],a[164:167],v[168:171]\nv_exp_f32 v189,v189\nM v[168:171],a[40:43],a[168:171],v[168:171]\nv_exp_f32 v190,v190\n" \
"M v[168:171],a[44:47],a[172:175],v[168:171]\nds_read_b128 a[88:91],v250 offset:6144\nds_read_b128 a[120:123],v250 offset:14336\nM v[172:175],a[44:47],a[188:191],v[236:239]\nbuffer_load_dwordx4 v240,s[44:47],s95 offen offset:2048 sc1 lds\nM v[172:175],a[40:43],a[184:187],v[172:175]\nv_exp_f32 v191,v191\nM v[172:175],a[36:39],a[180:183],v[172:175]\n" \
"ds_read_b128 a[92:95],v250 offset:7168\nds_read_b128 a[124:127],v250 offset:15360\nM v[172:175],a[32:35],a[176:179],v[172:175]\nv_cvt_pk_bf16_f32 v222,v188,v189\ns_waitcnt lgkmcnt(7)\nM v[184:187],a[48:51],a[160:163],v[232:235]\nv_cvt_pk_bf16_f32 v223,v190,v191\nv_xor_b32 v250,0x18000,v250\n" \
"M v[184:187],a[52:55],a[164:167],v[184:187]\nv_exp_f32 v136,v136\nM v[184:187],a[56:59],a[168:171],v[184:187]\nv_exp_f32 v137,v137\nM v[184:187],a[60:63],a[172:175],v[184:187]\nv_exp_f32 v138,v138\nM v[188:191],a[60:63],a[188:191],v[236:239]\nv_exp_f32 v139,v139\n" \
"M v[188:191],a[56:59],a[184:187],v[188:191]\nbuffer_load_dwordx4 v240,s[44:47],s95 offen offset:3072 sc1 lds\nM v[188:191],a[52:55],a[180:183],v[188:191]\ns_add_u32 m0,m0,0x10000\ns_and_b32 s94,s94,0xffff\nM v[188:191],a[48:51],a[176:179],v[188:191]\nv_exp_f32 v152,v152\nM v[64:67],a[64:67],v[200:203],v[64:67]\n" \
"v_exp_f32 v153,v153\nM v[64:67],a[96:99],v[216:219],v[64:67]\nv_exp_f32 v154,v154\nM v[72:75],a[68:71],v[200:203],v[72:75]\ns_waitcnt vmcnt(16) lgkmcnt(0)\ns_barrier\nM v[72:75],a[100:103],v[216:219],v[72:75]\nds_read_b128 a[0:3],v250 offset:0\n" \
"ds_read_b128 a[4:7],v250 offset:1024\nM v[80:83],a[72:75],v[200:203],v[80:83]\nv_exp_f32 v155,v155\nM v[80:83],a[104:107],v[216:219],v[80:83]\nbuffer_load_dwordx4 v240,s[48:51],s95 offen offset:0 sc1 lds\nM v[88:91],a[76:79],v[200:203],v[88:91]\nv_exp_f32 v168,v168\nM v[88:91],a[108:111],v[216:219],v[88:91]\n" \
"ds_read_b128 a[8:11],v250 offset:2048\nds_read_b128 a[12:15],v250 offset:3072\nM v[96:99],a[80:83],v[200:203],v[96:99]\nv_exp_f32 v169,v169\nM v[96:99],a[112:115],v[216:219],v[96:99]\nv_exp_f32 v170,v170\nM v[104:107],a[84:87],v[200:203],v[104:107]\nv_exp_f32 v171,v171\n" \
"M v[104:107],a[116:119],v[216:219],v[104:107]\nds_read_b128 a[16:19],v250 offset:4096\nds_read_b128 a[20:23],v250 offset:5120\nM v[112:115],a[88:91],v[200:203],v[112:115]\nv_exp_f32 v184,v184\nM v[112:115],a[120:123],v[216:219],v[112:115]\nbuffer_load_dwordx4 v240,s[48:51],s95 offen offset:1024 sc1 lds\nM v[120:123],a[92:95],v[200:203],v[120:123]\n" \
"v_exp_f32 v185,v185\nM v[120:123],a[124:127],v[216:219],v[120:123]\nds_read_b128 a[24:27],v250 offset:6144\nds_read_b128 a[28:31],v250 offset:7168\nM a[200:203],a[208:211],v[200:203],a[200:203]\nv_exp_f32 v186,v186\nM a[200:203],a[208:211],v[216:219],a[200:203]\nv_exp_f32 v187,v187\n" \
"M v[68:71],a[64:67],v[204:207],v[68:71]\nv_exp_f32 v140,v140\nM v[68:71],a[96:99],v[220:223],v[68:71]\nds_read_b128 a[32:35],v250 offset:8192\nds_read_b128 a[36:39],v250 offset:9216\nM v[76:79],a[68:71],v[204:207],v[76:79]\nv_exp_f32 v141,v141\nM v[76:79],a[100:103],v[220:223],v[76:79]\n" \
"buffer_load_dwordx4 v240,s[48:51],s95 offen offset:2048 sc1 lds\nM v[84:87],a[72:75],v[204:207],v[84:87]\nv_exp_f32 v142,v142\nM v[84:87],a[104:107],v[220:223],v[84:87]\nds_read_b128 a[40:43],v250 offset:10240\nds_read_b128 a[44:47],v250 offset:11264\nM v[92:95],a[76:79],v[204:207],v[92:95]\nv_exp_f32 v143,v143\n" \
"M v[92:95],a[108:111],v[220:223],v[92:95]\nv_exp_f32 v156,v156\nM v[100:103],a[80:83],v[204:207],v[100:103]\nv_exp_f32 v157,v157\nM v[100:103],a[112:115],v[220:223],v[100:103]\nds_read_b128 a[48:51],v250 offset:12288\nds_read_b128 a[52:55],v250 offset:13312\nM v[108:111],a[84:87],v[204:207],v[108:111]\n" \
"v_exp_f32 v158,v158\nM v[108:111],a[116:119],v[220:223],v[108:111]\nbuffer_load_dwordx4 v240,s[48:51],s95 offen offset:3072 sc1 lds\nM v[116:119],a[88:91],v[204:207],v[116:119]\nv_exp_f32 v159,v159\nM v[116:119],a[120:123],v[220:223],v[116:119]\nds_read_b128 a[56:59],v250 offset:14336\nds_read_b128 a[60:63],v250 offset:15360\n" \
"M v[124:127],a[92:95],v[204:207],v[124:127]\nv_cvt_pk_bf16_f32 v200,v136,v137\nv_cvt_pk_bf16_f32 v201,v138,v139\nM v[124:127],a[124:127],v[220:223],v[124:127]\nv_cvt_pk_bf16_f32 v202,v152,v153\nv_cvt_pk_bf16_f32 v203,v154,v155\nM a[204:207],a[208:211],v[204:207],a[204:207]\nv_cvt_pk_bf16_f32 v216,v168,v169\n" \
"v_cvt_pk_bf16_f32 v217,v170,v171\nv_cvt_pk_bf16_f32 v218,v184,v185\nv_cvt_pk_bf16_f32 v219,v186,v187\nv_add_u32 v250,s94,v241\ns_add_u32 s95,s95,0x4000\nM a[204:207],a[208:211],v[220:223],a[204:207]\ns_cmp_lg_u32 s95,s96\ns_cbranch_scc1 .Lsl\n" \
"s_waitcnt lgkmcnt(4)\nM v[128:131],a[0:3],a[128:131],v[224:227]\ns_add_u32 m0,s94,s64\nv_cvt_pk_bf16_f32 v204,v140,v141\nM v[128:131],a[4:7],a[132:135],v[128:131]\nds_read_b128 a[64:67],v250 offset:0\nds_read_b128 a[96:99],v250 offset:8192\nM v[128:131],a[8:11],a[136:139],v[128:131]\n" \
"buffer_load_dwordx4 v240,s[44:47],s95 offen offset:0 sc1 lds\nM v[128:131],a[12:15],a[140:143],v[128:131]\nv_exp_f32 v188,v188\nM v[132:135],a[12:15],a[156:159],v[228:231]\nds_read_b128 a[68:71],v250 offset:1024\nds_read_b128 a[100:103],v250 offset:9216\nM v[132:135],a[8:11],a[152:155],v[132:135]\nv_exp_f32 v189,v189\n" \
"M v[132:135],a[4:7],a[148:151],v[132:135]\nv_exp_f32 v190,v190\nM v[132:135],a[0:3],a[144:147],v[132:135]\nds_read_b128 a[72:75],v250 offset:2048\nds_read_b128 a[104:107],v250 offset:10240\nM v[144:147],a[16:19],a[128:131],v[224:227]\nv_exp_f32 v191,v191\nM v[144:147],a[20:23],a[132:135],v[144:147]\n" \
"v_exp_f32 v172,v172\nM v[144:147],a[24:27],a[136:139],v[144:147]\nds_read_b128 a[76:79],v250 offset:3072\nds_read_b128 a[108:111],v250 offset:11264\nM v[144:147],a[28:31],a[140:143],v[144:147]\nbuffer_load_dwordx4 v240,s[44:47],s95 offen offset:1024 sc1 lds\nM v[148:151],a[28:31],a[156:159],v[228:231]\nv_exp_f32 v173,v173\n" \
"M v[148:151],a[24:27],a[152:155],v[148:151]\nds_read_b128 a[80:83],v250 offset:4096\nds_read_b128 a[112:115],v250 offset:12288\nM v[148:151],a[20:23],a[148:151],v[148:151]\nv_exp_f32 v174,v174\nM v[148:151],a[16:19],a[144:147],v[148:151]\nv_exp_f32 v175,v175\nM v[160:163],a[32:35],a[128:131],v[224:227]\n" \
"ds_read_b128 a[84:87],v250 offset:5120\nds_read_b128 a[116:119],v250 offset:13312\nM v[160:163],a[36:39],a[132:135],v[160:163]\nv_cvt_pk_bf16_f32 v205,v142,v143\nv_cvt_pk_bf16_f32 v206,v156,v157\nM v[160:163],a[40:43],a[136:139],v[160:163]\nv_cvt_pk_bf16_f32 v207,v158,v159\nv_cvt_pk_bf16_f32 v220,v172,v173\n" \
"M v[160:163],a[44:47],a[140:143],v[160:163]\nds_read_b128 a[88:91],v250 offset:6144\nds_read_b128 a[120:123],v250 offset:14336\nM v[164:167],a[44:47],a[156:159],v[228:231]\nbuffer_load_dwordx4 v240,s[44:47],s95 offen offset:2048 sc1 lds\nM v[164:167],a[40:43],a[152:155],v[164:167]\nv_cvt_pk_bf16_f32 v221,v174,v175\nv_cvt_pk_bf16_f32 v222,v188,v189\n" \
"M v[164:167],a[36:39],a[148:151],v[164:167]\nds_read_b128 a[92:95],v250 offset:7168\nds_read_b128 a[124:127],v250 offset:15360\nM v[164:167],a[32:35],a[144:147],v[164:167]\nv_xor_b32 v250,0x18000,v250\ns_waitcnt lgkmcnt(7)\nM v[176:179],a[48:51],a[128:131],v[224:227]\nv_cvt_pk_bf16_f32 v223,v190,v191\n" \
"s_add_u32 s94,s94,0x4000\nM v[176:179],a[52:55],a[132:135],v[176:179]\nv_exp_f32 v128,v128\nM v[176:179],a[56:59],a[136:139],v[176:179]\nv_exp_f32 v129,v129\nM v[176:179],a[60:63],a[140:143],v[176:179]\nv_exp_f32 v130,v130\nM v[180:183],a[60:63],a[156:159],v[228:231]\n" \
"v_exp_f32 v131,v131\nM v[180:183],a[56:59],a[152:155],v[180:183]\nbuffer_load_dwordx4 v240,s[44:47],s95 offen offset:3072 sc1 lds\nM v[180:183],a[52:55],a[148:151],v[180:183]\ns_add_u32 m0,m0,0x10000\ns_and_b32 s94,s94,0xffff\nM v[180:183],a[48:51],a[144:147],v[180:183]\nv_exp_f32 v144,v144\n" \
"M v[64:67],a[64:67],v[200:203],v[64:67]\nv_exp_f32 v145,v145\nM v[64:67],a[96:99],v[216:219],v[64:67]\nv_exp_f32 v146,v146\nM v[72:75],a[68:71],v[200:203],v[72:75]\ns_waitcnt vmcnt(16) lgkmcnt(0)\ns_barrier\nM v[72:75],a[100:103],v[216:219],v[72:75]\n" \
"ds_read_b128 a[0:3],v250 offset:0\nds_read_b128 a[4:7],v250 offset:1024\nM v[80:83],a[72:75],v[200:203],v[80:83]\nv_exp_f32 v147,v147\nM v[80:83],a[104:107],v[216:219],v[80:83]\nbuffer_load_dwordx4 v240,s[48:51],s95 offen offset:0 sc1 lds\nM v[88:91],a[76:79],v[200:203],v[88:91]\nv_exp_f32 v160,v160\n" \
"M v[88:91],a[108:111],v[216:219],v[88:91]\nds_read_b128 a[8:11],v250 offset:2048\nds_read_b128 a[12:15],v250 offset:3072\nM v[96:99],a[80:83],v[200:203],v[96:99]\nv_exp_f32 v161,v161\nM v[96:99],a[112:115],v[216:219],v[96:99]\nv_exp_f32 v162,v162\nM v[104:107],a[84:87],v[200:203],v[104:107]\n" \
"v_exp_f32 v163,v163\nM v[104:107],a[116:119],v[216:219],v[104:107]\nds_read_b128 a[16:19],v250 offset:4096\nds_read_b128 a[20:23],v250 offset:5120\nM v[112:115],a[88:91],v[200:203],v[112:115]\nv_exp_f32 v176,v176\nM v[112:115],a[120:123],v[216:219],v[112:115]\nbuffer_load_dwordx4 v240,s[48:51],s95 offen offset:1024 sc1 lds\n" \
"M v[120:123],a[92:95],v[200:203],v[120:123]\nv_exp_f32 v177,v177\nM v[120:123],a[124:127],v[216:219],v[120:123]\nds_read_b128 a[24:27],v250 offset:6144\nds_read_b128 a[28:31],v250 offset:7168\nM a[200:203],a[208:211],v[200:203],a[200:203]\nv_exp_f32 v178,v178\nM a[200:203],a[208:211],v[216:219],a[200:203]\n" \
"v_exp_f32 v179,v179\nM v[68:71],a[64:67],v[204:207],v[68:71]\nv_exp_f32 v132,v132\nM v[68:71],a[96:99],v[220:223],v[68:71]\nds_read_b128 a[32:35],v250 offset:8192\nds_read_b128 a[36:39],v250 offset:9216\nM v[76:79],a[68:71],v[204:207],v[76:79]\nv_exp_f32 v133,v133\n" \
"M v[76:79],a[100:103],v[220:223],v[76:79]\nbuffer_load_dwordx4 v240,s[48:51],s95 offen offset:2048 sc1 lds\nM v[84:87],a[72:75],v[204:207],v[84:87]\nv_exp_f32 v134,v134\nM v[84:87],a[104:107],v[220:223],v[84:87]\nds_read_b128 a[40:43],v250 offset:10240\nds_read_b128 a[44:47],v250 offset:11264\nM v[92:95],a[76:79],v[204:207],v[92:95]\n" \
"v_exp_f32 v135,v135\nM v[92:95],a[108:111],v[220:223],v[92:95]\nv_exp_f32 v148,v148\nM v[100:103],a[80:83],v[204:207],v[100:103]\nv_exp_f32 v149,v149\nM v[100:103],a[112:115],v[220:223],v[100:103]\nds_read_b128 a[48:51],v250 offset:12288\nds_read_b128 a[52:55],v250 offset:13312\n" \
"M v[108:111],a[84:87],v[204:207],v[108:111]\nv_exp_f32 v150,v150\nM v[108:111],a[116:119],v[220:223],v[108:111]\nbuffer_load_dwordx4 v240,s[48:51],s95 offen offset:3072 sc1 lds\nM v[116:119],a[88:91],v[204:207],v[116:119]\nv_exp_f32 v151,v151\nM v[116:119],a[120:123],v[220:223],v[116:119]\nds_read_b128 a[56:59],v250 offset:14336\n" \
"ds_read_b128 a[60:63],v250 offset:15360\nM v[124:127],a[92:95],v[204:207],v[124:127]\nv_cvt_pk_bf16_f32 v192,v128,v129\nv_cvt_pk_bf16_f32 v193,v130,v131\nM v[124:127],a[124:127],v[220:223],v[124:127]\nv_cvt_pk_bf16_f32 v194,v144,v145\nv_cvt_pk_bf16_f32 v195,v146,v147\nM a[204:207],a[208:211],v[204:207],a[204:207]\n" \
"v_cvt_pk_bf16_f32 v208,v160,v161\nv_cvt_pk_bf16_f32 v209,v162,v163\nv_cvt_pk_bf16_f32 v210,v176,v177\nv_cvt_pk_bf16_f32 v211,v178,v179\nv_add_u32 v250,s94,v241\ns_add_u32 s95,s95,0x4000\nM a[204:207],a[208:211],v[220:223],a[204:207]\ns_branch .Lse\n" \
".Ltrans:\ns_waitcnt lgkmcnt(4)\nM v[128:131],a[0:3],a[128:131],v[224:227]\ns_add_u32 m0,s94,s64\nv_cvt_pk_bf16_f32 v212,v164,v165\nM v[128:131],a[4:7],a[132:135],v[128:131]\nds_read_b128 a[64:67],v250 offset:0\nds_read_b128 a[96:99],v250 offset:8192\n" \
"M v[128:131],a[8:11],a[136:139],v[128:131]\nbuffer_load_dwordx4 v240,s[44:47],s95 offen offset:0 sc1 lds\nM v[128:131],a[12:15],a[140:143],v[128:131]\nv_cvt_pk_bf16_f32 v213,v166,v167\nv_cvt_pk_bf16_f32 v214,v180,v181\nM v[132:135],a[12:15],a[156:159],v[228:231]\nds_read_b128 a[68:71],v250 offset:1024\nds_read_b128 a[100:103],v250 offset:9216\n" \
"M v[132:135],a[8:11],a[152:155],v[132:135]\nbuffer_store_dwordx4 a[248:251],v253,s[52:55],s33 offen offset:0 sc1\nM v[132:135],a[4:7],a[148:151],v[132:135]\nv_cvt_pk_bf16_f32 v215,v182,v183\ns_add_u32 s71,s33,16384\nM v[132:135],a[0:3],a[144:147],v[132:135]\nds_read_b128 a[72:75],v250 offset:2048\nds_read_b128 a[104:107],v250 offset:10240\n" \
"M v[144:147],a[16:19],a[128:131],v[224:227]\nv_exp_f32 v184,v184\nM v[144:147],a[20:23],a[132:135],v[144:147]\nv_exp_f32 v185,v185\nM v[144:147],a[24:27],a[136:139],v[144:147]\nds_read_b128 a[76:79],v250 offset:3072\nds_read_b128 a[108:111],v250 offset:11264\nM v[144:147],a[28:31],a[140:143],v[144:147]\n" \
"buffer_load_dwordx4 v240,s[44:47],s95 offen offset:1024 sc1 lds\nM v[148:151],a[28:31],a[156:159],v[228:231]\nv_exp_f32 v186,v186\nM v[148:151],a[24:27],a[152:155],v[148:151]\nds_read_b128 a[80:83],v250 offset:4096\nds_read_b128 a[112:115],v250 offset:12288\nM v[148:151],a[20:23],a[148:151],v[148:151]\nbuffer_store_dwordx4 a[248:251],v253,s[52:55],s33 offen offset:1024 sc1\n" \
"M v[148:151],a[16:19],a[144:147],v[148:151]\nv_exp_f32 v187,v187\nM v[160:163],a[32:35],a[128:131],v[224:227]\nds_read_b128 a[84:87],v250 offset:5120\nds_read_b128 a[116:119],v250 offset:13312\nM v[160:163],a[36:39],a[132:135],v[160:163]\nv_cvt_pk_bf16_f32 v216,v168,v169\nv_cvt_pk_bf16_f32 v217,v170,v171\n" \
"M v[160:163],a[40:43],a[136:139],v[160:163]\nv_cvt_pk_bf16_f32 v218,v184,v185\nv_cvt_pk_bf16_f32 v219,v186,v187\nM v[160:163],a[44:47],a[140:143],v[160:163]\nds_read_b128 a[88:91],v250 offset:6144\nds_read_b128 a[120:123],v250 offset:14336\nM v[164:167],a[44:47],a[156:159],v[228:231]\nbuffer_load_dwordx4 v240,s[44:47],s95 offen offset:2048 sc1 lds\n" \
"M v[164:167],a[40:43],a[152:155],v[164:167]\ns_add_u32 s70,s78,16384\nv_cvt_pk_bf16_f32 v204,v140,v141\nM v[164:167],a[36:39],a[148:151],v[164:167]\nds_read_b128 a[92:95],v250 offset:7168\nds_read_b128 a[124:127],v250 offset:15360\nM v[164:167],a[32:35],a[144:147],v[164:167]\nbuffer_store_dwordx4 a[248:251],v253,s[52:55],s33 offen offset:2048 sc1\n" \
"s_waitcnt lgkmcnt(6)\nM v[176:179],a[48:51],a[128:131],v[224:227]\nv_xor_b32 v250,0x18000,v250\nv_cvt_pk_bf16_f32 v205,v142,v143\nM v[176:179],a[52:55],a[132:135],v[176:179]\nv_exp_f32 v172,v172\nM v[176:179],a[56:59],a[136:139],v[176:179]\nv_exp_f32 v173,v173\n" \
"M v[176:179],a[60:63],a[140:143],v[176:179]\nv_exp_f32 v174,v174\nM v[180:183],a[60:63],a[156:159],v[228:231]\nv_exp_f32 v175,v175\nM v[180:183],a[56:59],a[152:155],v[180:183]\nbuffer_load_dwordx4 v240,s[44:47],s95 offen offset:3072 sc1 lds\nM v[180:183],a[52:55],a[148:151],v[180:183]\ns_add_u32 m0,m0,0x10000\n" \
"v_cvt_pk_bf16_f32 v206,v156,v157\nM v[180:183],a[48:51],a[144:147],v[180:183]\nv_exp_f32 v188,v188\nM v[0:3],a[64:67],v[192:195],v[0:3]\nbuffer_store_dwordx4 a[248:251],v253,s[52:55],s33 offen offset:3072 sc1\nM v[0:3],a[96:99],v[208:211],v[0:3]\nv_exp_f32 v189,v189\nM v[8:11],a[68:71],v[192:195],v[8:11]\n" \
"s_waitcnt vmcnt(16) lgkmcnt(0)\ns_barrier\nM v[8:11],a[100:103],v[208:211],v[8:11]\nds_read_b128 a[0:3],v250 offset:0\nds_read_b128 a[4:7],v250 offset:1024\nM v[16:19],a[72:75],v[192:195],v[16:19]\nv_exp_f32 v190,v190\nM v[16:19],a[104:107],v[208:211],v[16:19]\n" \
"buffer_store_dwordx4 a[248:251],v253,s[52:55],s71 offen offset:0 sc1\nM v[24:27],a[76:79],v[192:195],v[24:27]\nv_exp_f32 v191,v191\nM v[24:27],a[108:111],v[208:211],v[24:27]\nbuffer_load_dwordx4 v240,s[48:51],s95 offen offset:0 sc1 lds\nM v[32:35],a[80:83],v[192:195],v[32:35]\nv_cvt_pk_bf16_f32 v207,v158,v159\nv_cvt_pk_bf16_f32 v220,v172,v173\n" \
"M v[32:35],a[112:115],v[208:211],v[32:35]\nbuffer_store_dwordx4 a[248:251],v253,s[52:55],s71 offen offset:1024 sc1\nM v[40:43],a[84:87],v[192:195],v[40:43]\nv_cvt_pk_bf16_f32 v221,v174,v175\nv_cvt_pk_bf16_f32 v222,v188,v189\nM v[40:43],a[116:119],v[208:211],v[40:43]\nds_read_b128 a[8:11],v250 offset:2048\nds_read_b128 a[12:15],v250 offset:3072\n" \
"M v[48:51],a[88:91],v[192:195],v[48:51]\nv_cvt_pk_bf16_f32 v223,v190,v191\ns_add_u32 s94,s94,0x4000\nM v[48:51],a[120:123],v[208:211],v[48:51]\nbuffer_store_dwordx4 a[248:251],v253,s[52:55],s71 offen offset:2048 sc1\nM v[56:59],a[92:95],v[192:195],v[56:59]\nv_exp_f32 v128,v128\nM v[56:59],a[124:127],v[208:211],v[56:59]\n" \
"v_exp_f32 v129,v129\nM a[192:195],a[208:211],v[192:195],a[192:195]\nv_exp_f32 v130,v130\nM a[192:195],a[208:211],v[208:211],a[192:195]\nbuffer_store_dwordx4 a[248:251],v253,s[52:55],s71 offen offset:3072 sc1\nM v[4:7],a[64:67],v[196:199],v[4:7]\nv_exp_f32 v131,v131\nM v[4:7],a[96:99],v[212:215],v[4:7]\n" \
"ds_read_b128 a[16:19],v250 offset:4096\nds_read_b128 a[20:23],v250 offset:5120\nM v[12:15],a[68:71],v[196:199],v[12:15]\nv_exp_f32 v144,v144\nM v[12:15],a[100:103],v[212:215],v[12:15]\nv_exp_f32 v145,v145\nM v[20:23],a[72:75],v[196:199],v[20:23]\nv_exp_f32 v146,v146\n" \
"M v[20:23],a[104:107],v[212:215],v[20:23]\nbuffer_load_dwordx4 v240,s[48:51],s95 offen offset:1024 sc1 lds\nM v[28:31],a[76:79],v[196:199],v[28:31]\nv_exp_f32 v147,v147\nM v[28:31],a[108:111],v[212:215],v[28:31]\nbuffer_store_dwordx4 a[248:251],v253,s[52:55],s78 offen offset:0 sc1\nM v[36:39],a[80:83],v[196:199],v[36:39]\nv_exp_f32 v160,v160\n" \
"M v[36:39],a[112:115],v[212:215],v[36:39]\nds_read_b128 a[24:27],v250 offset:6144\nds_read_b128 a[28:31],v250 offset:7168\nM v[44:47],a[84:87],v[196:199],v[44:47]\nv_exp_f32 v161,v161\nM v[44:47],a[116:119],v[212:215],v[44:47]\nbuffer_store_dwordx4 a[248:251],v253,s[52:55],s78 offen offset:1024 sc1\nM v[52:55],a[88:91],v[196:199],v[52:55]\n" \
"v_exp_f32 v162,v162\nM v[52:55],a[120:123],v[212:215],v[52:55]\nv_exp_f32 v163,v163\nM v[60:63],a[92:95],v[196:199],v[60:63]\nv_exp_f32 v176,v176\nM v[60:63],a[124:127],v[212:215],v[60:63]\nbuffer_store_dwordx4 a[248:251],v253,s[52:55],s78 offen offset:2048 sc1\nM a[196:199],a[208:211],v[196:199],a[196:199]\n" \
"v_exp_f32 v177,v177\nM a[196:199],a[208:211],v[212:215],a[196:199]\nds_read_b128 a[32:35],v250 offset:8192\nds_read_b128 a[36:39],v250 offset:9216\nM v[64:67],a[64:67],v[200:203],v[64:67]\nv_exp_f32 v178,v178\nM v[64:67],a[96:99],v[216:219],v[64:67]\nv_exp_f32 v179,v179\n" \
"M v[72:75],a[68:71],v[200:203],v[72:75]\ns_and_b32 s94,s94,0xffff\nv_cvt_pk_bf16_f32 v192,v128,v129\nM v[72:75],a[100:103],v[216:219],v[72:75]\nbuffer_load_dwordx4 v240,s[48:51],s95 offen offset:2048 sc1 lds\nM v[80:83],a[72:75],v[200:203],v[80:83]\nv_exp_f32 v132,v132\nM v[80:83],a[104:107],v[216:219],v[80:83]\n" \
"buffer_store_dwordx4 a[248:251],v253,s[52:55],s78 offen offset:3072 sc1\nM v[88:91],a[76:79],v[200:203],v[88:91]\nv_exp_f32 v133,v133\nM v[88:91],a[108:111],v[216:219],v[88:91]\nds_read_b128 a[40:43],v250 offset:10240\nds_read_b128 a[44:47],v250 offset:11264\nM v[96:99],a[80:83],v[200:203],v[96:99]\nv_exp_f32 v134,v134\n" \
"M v[96:99],a[112:115],v[216:219],v[96:99]\nbuffer_store_dwordx4 a[248:251],v253,s[52:55],s70 offen offset:0 sc1\nM v[104:107],a[84:87],v[200:203],v[104:107]\nv_exp_f32 v135,v135\nM v[104:107],a[116:119],v[216:219],v[104:107]\nv_exp_f32 v148,v148\nM v[112:115],a[88:91],v[200:203],v[112:115]\nv_exp_f32 v149,v149\n" \
"M v[112:115],a[120:123],v[216:219],v[112:115]\nbuffer_store_dwordx4 a[248:251],v253,s[52:55],s70 offen offset:1024 sc1\nM v[120:123],a[92:95],v[200:203],v[120:123]\nv_exp_f32 v150,v150\nM v[120:123],a[124:127],v[216:219],v[120:123]\nds_read_b128 a[48:51],v250 offset:12288\nds_read_b128 a[52:55],v250 offset:13312\nM a[200:203],a[208:211],v[200:203],a[200:203]\n" \
"v_exp_f32 v151,v151\nM a[200:203],a[208:211],v[216:219],a[200:203]\nv_cvt_pk_bf16_f32 v193,v130,v131\nv_cvt_pk_bf16_f32 v194,v144,v145\nM v[68:71],a[64:67],v[204:207],v[68:71]\nv_cvt_pk_bf16_f32 v195,v146,v147\nv_cvt_pk_bf16_f32 v208,v160,v161\nM v[68:71],a[96:99],v[220:223],v[68:71]\n" \
"buffer_load_dwordx4 v240,s[48:51],s95 offen offset:3072 sc1 lds\nM v[76:79],a[68:71],v[204:207],v[76:79]\nv_cvt_pk_bf16_f32 v209,v162,v163\nv_cvt_pk_bf16_f32 v210,v176,v177\nM v[76:79],a[100:103],v[220:223],v[76:79]\nbuffer_store_dwordx4 a[248:251],v253,s[52:55],s70 offen offset:2048 sc1\nM v[84:87],a[72:75],v[204:207],v[84:87]\nv_cvt_pk_bf16_f32 v211,v178,v179\n" \
"s_add_u32 s95,s95,0x4000\nM v[84:87],a[104:107],v[220:223],v[84:87]\nds_read_b128 a[56:59],v250 offset:14336\nds_read_b128 a[60:63],v250 offset:15360\nM v[92:95],a[76:79],v[204:207],v[92:95]\nv_add_u32 v250,s94,v241\nM v[92:95],a[108:111],v[220:223],v[92:95]\nbuffer_store_dwordx4 a[248:251],v253,s[52:55],s70 offen offset:3072 sc1\n" \
"M v[100:103],a[80:83],v[204:207],v[100:103]\nM v[100:103],a[112:115],v[220:223],v[100:103]\nM v[108:111],a[84:87],v[204:207],v[108:111]\nM v[108:111],a[116:119],v[220:223],v[108:111]\nM v[116:119],a[88:91],v[204:207],v[116:119]\nM v[116:119],a[120:123],v[220:223],v[116:119]\nM v[124:127],a[92:95],v[204:207],v[124:127]\nM v[124:127],a[124:127],v[220:223],v[124:127]\n" \
"M a[204:207],a[208:211],v[204:207],a[204:207]\nM a[204:207],a[208:211],v[220:223],a[204:207]\n.Lse:\ns_waitcnt lgkmcnt(4)\nM v[128:131],a[0:3],a[128:131],v[224:227]\ns_add_u32 m0,s94,s64\nv_accvgpr_read_b32 v232,a200\nv_cvt_pk_bf16_f32 v196,v132,v133\n" \
"v_cvt_pk_bf16_f32 v197,v134,v135\nM v[128:131],a[4:7],a[132:135],v[128:131]\nds_read_b128 a[64:67],v250 offset:0\nds_read_b128 a[96:99],v250 offset:8192\nv_accvgpr_read_b32 v236,a204\nv_add_f32 v233,v232,v236\nM v[128:131],a[8:11],a[136:139],v[128:131]\nbuffer_load_dwordx4 v240,s[44:47],s95 offen offset:0 sc1 lds\n" \
"v_rcp_f32 v232,v232\nM v[128:131],a[12:15],a[140:143],v[128:131]\nv_rcp_f32 v236,v236\nv_add_f32 v233,v233,v232\nv_add_f32 v233,v233,v236\nM v[132:135],a[12:15],a[156:159],v[228:231]\nds_read_b128 a[68:71],v250 offset:1024\nds_read_b128 a[100:103],v250 offset:9216\n" \
"v_cmp_ngt_f32 vcc,s28,v233\nv_mul_f32 v64,v64,v232\nM v[132:135],a[8:11],a[152:155],v[132:135]\ns_and_b64 vcc,vcc,s[90:91]\ns_cselect_b32 s39,0x8000000,s16\ns_cselect_b32 s16,s16,0x8000000\ns_cselect_b32 s71,2,1\ns_addc_u32 s99,s99,0\n" \
"M v[132:135],a[4:7],a[148:151],v[132:135]\nv_mul_f32 v65,v65,v232\ns_add_u32 s37,s12,s67\nv_mul_f32 v66,v66,v232\nv_mul_f32 v67,v67,v232\nM v[132:135],a[0:3],a[144:147],v[132:135]\nds_read_b128 a[72:75],v250 offset:2048\nds_read_b128 a[104:107],v250 offset:10240\n" \
"v_cvt_pk_bf16_f32 v64,v64,v65\ns_add_u32 s37,s37,1048576\nM v[144:147],a[16:19],a[128:131],v[224:227]\nv_mul_f32 v72,v72,v232\nv_mul_f32 v73,v73,v232\nv_mul_f32 v74,v74,v232\nv_mul_f32 v75,v75,v232\nv_cvt_pk_bf16_f32 v65,v66,v67\n" \
"M v[144:147],a[20:23],a[132:135],v[144:147]\ns_or_b32 s37,s37,s39\nv_cvt_pk_bf16_f32 v66,v72,v73\nv_cvt_pk_bf16_f32 v67,v74,v75\nbuffer_store_dwordx4 v[64:67],v240,s[52:55],s37 offen offset:0 sc1\nM v[144:147],a[24:27],a[136:139],v[144:147]\nds_read_b128 a[76:79],v250 offset:3072\nds_read_b128 a[108:111],v250 offset:11264\n" \
"v_cvt_pk_bf16_f32 v198,v148,v149\nv_cvt_pk_bf16_f32 v199,v150,v151\nv_mul_f32 v68,v68,v236\nM v[144:147],a[28:31],a[140:143],v[144:147]\nbuffer_load_dwordx4 v240,s[44:47],s95 offen offset:1024 sc1 lds\nv_mul_f32 v69,v69,v236\nv_mul_f32 v70,v70,v236\nM v[148:151],a[28:31],a[156:159],v[228:231]\n" \
"v_mul_f32 v71,v71,v236\nv_cvt_pk_bf16_f32 v68,v68,v69\nv_mul_f32 v76,v76,v236\nv_mul_f32 v77,v77,v236\nM v[148:151],a[24:27],a[152:155],v[148:151]\nds_read_b128 a[80:83],v250 offset:4096\nds_read_b128 a[112:115],v250 offset:12288\nv_mul_f32 v78,v78,v236\n" \
"v_mul_f32 v79,v79,v236\nM v[148:151],a[20:23],a[148:151],v[148:151]\nv_cvt_pk_bf16_f32 v69,v70,v71\nv_cvt_pk_bf16_f32 v70,v76,v77\nv_cvt_pk_bf16_f32 v71,v78,v79\nv_mul_f32 v80,v80,v232\nM v[148:151],a[16:19],a[144:147],v[148:151]\nv_exp_f32 v164,v164\n" \
"v_exp_f32 v165,v165\nM v[160:163],a[32:35],a[128:131],v[224:227]\nds_read_b128 a[84:87],v250 offset:5120\nds_read_b128 a[116:119],v250 offset:13312\nv_exp_f32 v166,v166\nv_cvt_pk_bf16_f32 v212,v164,v165\nM v[160:163],a[36:39],a[132:135],v[160:163]\nbuffer_store_dwordx4 v[68:71],v240,s[52:55],s37 offen offset:1024 sc1\n" \
"v_exp_f32 v167,v167\nM v[160:163],a[40:43],a[136:139],v[160:163]\nv_cvt_pk_bf16_f32 v213,v166,v167\nv_mul_f32 v81,v81,v232\nv_mul_f32 v82,v82,v232\nv_mul_f32 v83,v83,v232\nM v[160:163],a[44:47],a[140:143],v[160:163]\nds_read_b128 a[88:91],v250 offset:6144\n" \
"ds_read_b128 a[120:123],v250 offset:14336\nv_cvt_pk_bf16_f32 v80,v80,v81\nv_mul_f32 v88,v88,v232\nM v[164:167],a[44:47],a[156:159],v[228:231]\nbuffer_load_dwordx4 v240,s[44:47],s95 offen offset:2048 sc1 lds\nv_mul_f32 v89,v89,v232\nv_mul_f32 v90,v90,v232\nM v[164:167],a[40:43],a[152:155],v[164:167]\n" \
"v_mul_f32 v91,v91,v232\nv_cvt_pk_bf16_f32 v81,v82,v83\nv_cvt_pk_bf16_f32 v82,v88,v89\nv_cvt_pk_bf16_f32 v83,v90,v91\nv_mul_f32 v84,v84,v236\nM v[164:167],a[36:39],a[148:151],v[164:167]\nds_read_b128 a[92:95],v250 offset:7168\nds_read_b128 a[124:127],v250 offset:15360\n" \
"v_exp_f32 v180,v180\nM v[164:167],a[32:35],a[144:147],v[164:167]\nv_exp_f32 v181,v181\nv_mul_f32 v85,v85,v236\ns_waitcnt lgkmcnt(4)\nM v[176:179],a[48:51],a[128:131],v[224:227]\nv_exp_f32 v182,v182\nv_exp_f32 v183,v183\n" \
"M v[176:179],a[52:55],a[132:135],v[176:179]\nbuffer_store_dwordx4 v[80:83],v240,s[52:55],s37 offen offset:2048 sc1\nv_cvt_pk_bf16_f32 v214,v180,v181\nv_cvt_pk_bf16_f32 v215,v182,v183\nM v[176:179],a[56:59],a[136:139],v[176:179]\nv_mul_f32 v86,v86,v236\nv_mul_f32 v87,v87,v236\nv_cvt_pk_bf16_f32 v84,v84,v85\n" \
"v_mul_f32 v92,v92,v236\nM v[176:179],a[60:63],a[140:143],v[176:179]\nv_mul_f32 v93,v93,v236\nv_mul_f32 v94,v94,v236\nv_mul_f32 v95,v95,v236\nv_cvt_pk_bf16_f32 v85,v86,v87\nv_cvt_pk_bf16_f32 v86,v92,v93\nM v[180:183],a[60:63],a[156:159],v[228:231]\n" \
"v_cvt_pk_bf16_f32 v87,v94,v95\nv_xor_b32 v250,0x18000,v250\nv_mul_f32 v96,v96,v232\nv_mul_f32 v97,v97,v232\nM v[180:183],a[56:59],a[152:155],v[180:183]\nbuffer_load_dwordx4 v240,s[44:47],s95 offen offset:3072 sc1 lds\ns_add_u32 m0,m0,0x10000\nv_mul_f32 v98,v98,v232\n" \
"M v[180:183],a[52:55],a[148:151],v[180:183]\nv_mul_f32 v99,v99,v232\nv_cvt_pk_bf16_f32 v96,v96,v97\nv_mul_f32 v104,v104,v232\nv_mul_f32 v105,v105,v232\nM v[180:183],a[48:51],a[144:147],v[180:183]\nv_mul_f32 v106,v106,v232\nv_mul_f32 v107,v107,v232\n" \
"v_cvt_pk_bf16_f32 v97,v98,v99\ns_add_u32 s38,s37,4096\nM v[0:3],a[64:67],v[192:195],v[0:3]\nv_cvt_pk_bf16_f32 v98,v104,v105\nv_cvt_pk_bf16_f32 v99,v106,v107\nv_mul_f32 v100,v100,v236\nv_mul_f32 v101,v101,v236\nv_mul_f32 v102,v102,v236\n" \
"M v[0:3],a[96:99],v[208:211],v[0:3]\nbuffer_store_dwordx4 v[84:87],v240,s[52:55],s37 offen offset:3072 sc1\nv_mul_f32 v103,v103,v236\nv_cvt_pk_bf16_f32 v100,v100,v101\nM v[8:11],a[68:71],v[192:195],v[8:11]\ns_waitcnt vmcnt(16) lgkmcnt(0)\ns_barrier\nv_mul_f32 v108,v108,v236\n" \
"v_mul_f32 v109,v109,v236\nM v[8:11],a[100:103],v[208:211],v[8:11]\nds_read_b128 a[0:3],v250 offset:0\nds_read_b128 a[4:7],v250 offset:1024\nv_mul_f32 v110,v110,v236\nv_mul_f32 v111,v111,v236\nM v[16:19],a[72:75],v[192:195],v[16:19]\nv_cvt_pk_bf16_f32 v101,v102,v103\n" \
"v_cvt_pk_bf16_f32 v102,v108,v109\nv_cvt_pk_bf16_f32 v103,v110,v111\nv_mul_f32 v112,v112,v232\nM v[16:19],a[104:107],v[208:211],v[16:19]\nbuffer_load_dwordx4 v240,s[48:51],s95 offen offset:0 sc1 lds\nv_mul_f32 v113,v113,v232\nv_mul_f32 v114,v114,v232\nv_mul_f32 v115,v115,v232\n" \
"M v[24:27],a[76:79],v[192:195],v[24:27]\nv_cvt_pk_bf16_f32 v112,v112,v113\nv_mul_f32 v120,v120,v232\nv_mul_f32 v121,v121,v232\nv_mul_f32 v122,v122,v232\nM v[24:27],a[108:111],v[208:211],v[24:27]\nds_read_b128 a[8:11],v250 offset:2048\nds_read_b128 a[12:15],v250 offset:3072\n" \
"v_mul_f32 v123,v123,v232\nv_cvt_pk_bf16_f32 v113,v114,v115\nM v[32:35],a[80:83],v[192:195],v[32:35]\nv_cvt_pk_bf16_f32 v114,v120,v121\nv_cvt_pk_bf16_f32 v115,v122,v123\nv_mul_f32 v116,v116,v236\nv_mul_f32 v117,v117,v236\nM v[32:35],a[112:115],v[208:211],v[32:35]\n" \
"buffer_store_dwordx4 v[96:99],v240,s[52:55],s38 offen offset:0 sc1\nv_mul_f32 v118,v118,v236\nv_mul_f32 v119,v119,v236\nM v[40:43],a[84:87],v[192:195],v[40:43]\nv_cvt_pk_bf16_f32 v116,v116,v117\nv_mul_f32 v124,v124,v236\nv_mul_f32 v125,v125,v236\nv_mul_f32 v126,v126,v236\n" \
"M v[40:43],a[116:119],v[208:211],v[40:43]\nds_read_b128 a[16:19],v250 offset:4096\nds_read_b128 a[20:23],v250 offset:5120\nv_mul_f32 v127,v127,v236\nv_cvt_pk_bf16_f32 v117,v118,v119\nv_cvt_pk_bf16_f32 v118,v124,v125\nM v[48:51],a[88:91],v[192:195],v[48:51]\nv_cvt_pk_bf16_f32 v119,v126,v127\n" \
"v_exp_f32 v128,v128\ns_add_u32 s94,s94,0x4000\nM v[48:51],a[120:123],v[208:211],v[48:51]\nbuffer_load_dwordx4 v240,s[48:51],s95 offen offset:1024 sc1 lds\nv_exp_f32 v129,v129\nM v[56:59],a[92:95],v[192:195],v[56:59]\nv_exp_f32 v130,v130\nv_exp_f32 v131,v131\n" \
"M v[56:59],a[124:127],v[208:211],v[56:59]\nds_read_b128 a[24:27],v250 offset:6144\nds_read_b128 a[28:31],v250 offset:7168\nv_exp_f32 v144,v144\nM a[192:195],a[208:211],v[192:195],a[192:195]\nv_exp_f32 v145,v145\nv_exp_f32 v146,v146\ns_lshl2_add_u32 s77,s61,s79\n" \
"M a[192:195],a[208:211],v[208:211],a[192:195]\nbuffer_store_dwordx4 v[100:103],v240,s[52:55],s38 offen offset:1024 sc1\nv_exp_f32 v147,v147\nM v[4:7],a[64:67],v[196:199],v[4:7]\nv_exp_f32 v160,v160\nv_exp_f32 v161,v161\nM v[4:7],a[96:99],v[212:215],v[4:7]\nds_read_b128 a[32:35],v250 offset:8192\n" \
"ds_read_b128 a[36:39],v250 offset:9216\nv_exp_f32 v162,v162\nM v[12:15],a[68:71],v[196:199],v[12:15]\nv_exp_f32 v163,v163\nv_exp_f32 v176,v176\nM v[12:15],a[100:103],v[212:215],v[12:15]\nbuffer_load_dwordx4 v240,s[48:51],s95 offen offset:2048 sc1 lds\nv_exp_f32 v177,v177\n" \
"M v[20:23],a[72:75],v[196:199],v[20:23]\nv_exp_f32 v178,v178\nv_exp_f32 v179,v179\ns_and_b32 s94,s94,0xffff\nM v[20:23],a[104:107],v[212:215],v[20:23]\nds_read_b128 a[40:43],v250 offset:10240\nds_read_b128 a[44:47],v250 offset:11264\ns_add_u32 s77,s77,4096\n" \
"v_mov_b32 v233,s71\nM v[28:31],a[76:79],v[196:199],v[28:31]\nv_exp_f32 v132,v132\nv_exp_f32 v133,v133\nM v[28:31],a[108:111],v[212:215],v[28:31]\nbuffer_store_dwordx4 v[112:115],v240,s[52:55],s38 offen offset:2048 sc1\nv_exp_f32 v134,v134\nM v[36:39],a[80:83],v[196:199],v[36:39]\n" \
"v_exp_f32 v135,v135\nv_exp_f32 v148,v148\nM v[36:39],a[112:115],v[212:215],v[36:39]\nds_read_b128 a[48:51],v250 offset:12288\nds_read_b128 a[52:55],v250 offset:13312\nv_exp_f32 v149,v149\nv_cvt_pk_bf16_f32 v192,v128,v129\nM v[44:47],a[84:87],v[196:199],v[44:47]\n" \
"v_exp_f32 v150,v150\nv_exp_f32 v151,v151\nM v[44:47],a[116:119],v[212:215],v[44:47]\nbuffer_load_dwordx4 v240,s[48:51],s95 offen offset:3072 sc1 lds\nv_cvt_pk_bf16_f32 v193,v130,v131\nv_cvt_pk_bf16_f32 v194,v144,v145\nM v[52:55],a[88:91],v[196:199],v[52:55]\nv_cvt_pk_bf16_f32 v195,v146,v147\n" \
"v_cvt_pk_bf16_f32 v208,v160,v161\nv_cvt_pk_bf16_f32 v209,v162,v163\nv_cvt_pk_bf16_f32 v210,v176,v177\nM v[52:55],a[120:123],v[212:215],v[52:55]\nds_read_b128 a[56:59],v250 offset:14336\nds_read_b128 a[60:63],v250 offset:15360\nv_cvt_pk_bf16_f32 v211,v178,v179\nv_add_u32 v250,s94,v241\n" \
"M v[60:63],a[92:95],v[196:199],v[60:63]\ns_add_u32 s95,s95,0x4000\nM v[60:63],a[124:127],v[212:215],v[60:63]\nbuffer_store_dwordx4 v[116:119],v240,s[52:55],s38 offen offset:3072 sc1\nM a[196:199],a[208:211],v[196:199],a[196:199]\nM a[196:199],a[208:211],v[212:215],a[196:199]\ns_cmp_ge_u32 s95,s15\ns_cbranch_scc1 .Lsx\n" \
".Lsingle:\ns_waitcnt lgkmcnt(4)\nM v[128:131],a[0:3],a[128:131],v[224:227]\ns_xor_b32 m0,m0,0x14000\nv_cvt_pk_bf16_f32 v196,v132,v133\nM v[128:131],a[4:7],a[132:135],v[128:131]\nds_read_b128 a[64:67],v250 offset:0\nds_read_b128 a[96:99],v250 offset:8192\n" \
"M v[128:131],a[8:11],a[136:139],v[128:131]\nbuffer_load_dwordx4 v240,s[44:47],s95 offen offset:0 sc1 lds\nM v[128:131],a[12:15],a[140:143],v[128:131]\nv_cvt_pk_bf16_f32 v197,v134,v135\nv_cvt_pk_bf16_f32 v198,v148,v149\nM v[132:135],a[12:15],a[156:159],v[228:231]\nds_read_b128 a[68:71],v250 offset:1024\nds_read_b128 a[100:103],v250 offset:9216\n" \
"M v[132:135],a[8:11],a[152:155],v[132:135]\nv_exp_f32 v164,v164\nM v[132:135],a[4:7],a[148:151],v[132:135]\nv_exp_f32 v165,v165\nM v[132:135],a[0:3],a[144:147],v[132:135]\nds_read_b128 a[72:75],v250 offset:2048\nds_read_b128 a[104:107],v250 offset:10240\nM v[144:147],a[16:19],a[128:131],v[224:227]\n" \
"v_cvt_pk_bf16_f32 v199,v150,v151\nv_cvt_pk_bf16_f32 v212,v164,v165\nM v[144:147],a[20:23],a[132:135],v[144:147]\nv_exp_f32 v166,v166\nM v[144:147],a[24:27],a[136:139],v[144:147]\nds_read_b128 a[76:79],v250 offset:3072\nds_read_b128 a[108:111],v250 offset:11264\nM v[144:147],a[28:31],a[140:143],v[144:147]\n" \
"buffer_load_dwordx4 v240,s[44:47],s95 offen offset:1024 sc1 lds\nM v[148:151],a[28:31],a[156:159],v[228:231]\nv_exp_f32 v167,v167\nM v[148:151],a[24:27],a[152:155],v[148:151]\nds_read_b128 a[80:83],v250 offset:4096\nds_read_b128 a[112:115],v250 offset:12288\nM v[148:151],a[20:23],a[148:151],v[148:151]\nv_exp_f32 v180,v180\n" \
"M v[148:151],a[16:19],a[144:147],v[148:151]\nv_exp_f32 v181,v181\nM v[160:163],a[32:35],a[128:131],v[224:227]\nds_read_b128 a[84:87],v250 offset:5120\nds_read_b128 a[116:119],v250 offset:13312\nM v[160:163],a[36:39],a[132:135],v[160:163]\nv_cvt_pk_bf16_f32 v213,v166,v167\nv_cvt_pk_bf16_f32 v214,v180,v181\n" \
"M v[160:163],a[40:43],a[136:139],v[160:163]\nv_exp_f32 v182,v182\nM v[160:163],a[44:47],a[140:143],v[160:163]\nds_read_b128 a[88:91],v250 offset:6144\nds_read_b128 a[120:123],v250 offset:14336\nM v[164:167],a[44:47],a[156:159],v[228:231]\nbuffer_load_dwordx4 v240,s[44:47],s95 offen offset:2048 sc1 lds\nM v[164:167],a[40:43],a[152:155],v[164:167]\n" \
"v_exp_f32 v183,v183\nM v[164:167],a[36:39],a[148:151],v[164:167]\nds_read_b128 a[92:95],v250 offset:7168\nds_read_b128 a[124:127],v250 offset:15360\nM v[164:167],a[32:35],a[144:147],v[164:167]\nv_cvt_pk_bf16_f32 v215,v182,v183\ns_waitcnt lgkmcnt(7)\nM v[176:179],a[48:51],a[128:131],v[224:227]\n" \
"v_exp_f32 v128,v128\nM v[176:179],a[52:55],a[132:135],v[176:179]\nv_exp_f32 v129,v129\nM v[176:179],a[56:59],a[136:139],v[176:179]\nv_exp_f32 v130,v130\nM v[176:179],a[60:63],a[140:143],v[176:179]\nv_exp_f32 v131,v131\nM v[180:183],a[60:63],a[156:159],v[228:231]\n" \
"v_exp_f32 v132,v132\nM v[180:183],a[56:59],a[152:155],v[180:183]\nbuffer_load_dwordx4 v240,s[44:47],s95 offen offset:3072 sc1 lds\nM v[180:183],a[52:55],a[148:151],v[180:183]\nv_xor_b32 v250,0x18000,v250\ns_xor_b32 m0,m0,0x10000\nM v[180:183],a[48:51],a[144:147],v[180:183]\nv_exp_f32 v133,v133\n" \
"M v[0:3],a[64:67],v[192:195],v[0:3]\nv_exp_f32 v134,v134\nM v[0:3],a[96:99],v[208:211],v[0:3]\nv_exp_f32 v135,v135\nM v[8:11],a[68:71],v[192:195],v[8:11]\ns_waitcnt vmcnt(16) lgkmcnt(0)\ns_barrier\nM v[8:11],a[100:103],v[208:211],v[8:11]\n" \
"ds_read_b128 a[0:3],v250 offset:0\nds_read_b128 a[4:7],v250 offset:1024\nM v[16:19],a[72:75],v[192:195],v[16:19]\nv_exp_f32 v144,v144\nM v[16:19],a[104:107],v[208:211],v[16:19]\nbuffer_load_dwordx4 v240,s[48:51],s95 offen offset:0 sc1 lds\nM v[24:27],a[76:79],v[192:195],v[24:27]\nv_exp_f32 v145,v145\n" \
"M v[24:27],a[108:111],v[208:211],v[24:27]\nds_read_b128 a[8:11],v250 offset:2048\nds_read_b128 a[12:15],v250 offset:3072\nM v[32:35],a[80:83],v[192:195],v[32:35]\nv_exp_f32 v146,v146\nM v[32:35],a[112:115],v[208:211],v[32:35]\nv_exp_f32 v147,v147\nM v[40:43],a[84:87],v[192:195],v[40:43]\n" \
"v_exp_f32 v148,v148\nM v[40:43],a[116:119],v[208:211],v[40:43]\nds_read_b128 a[16:19],v250 offset:4096\nds_read_b128 a[20:23],v250 offset:5120\nM v[48:51],a[88:91],v[192:195],v[48:51]\nv_exp_f32 v149,v149\nM v[48:51],a[120:123],v[208:211],v[48:51]\nbuffer_load_dwordx4 v240,s[48:51],s95 offen offset:1024 sc1 lds\n" \
"M v[56:59],a[92:95],v[192:195],v[56:59]\nv_exp_f32 v150,v150\nM v[56:59],a[124:127],v[208:211],v[56:59]\nds_read_b128 a[24:27],v250 offset:6144\nds_read_b128 a[28:31],v250 offset:7168\nM a[192:195],a[208:211],v[192:195],a[192:195]\nv_cvt_pk_bf16_f32 v192,v128,v129\nv_cvt_pk_bf16_f32 v193,v130,v131\n" \
"M a[192:195],a[208:211],v[208:211],a[192:195]\nv_cvt_pk_bf16_f32 v194,v144,v145\nv_cvt_pk_bf16_f32 v195,v146,v147\nM v[4:7],a[64:67],v[196:199],v[4:7]\nv_exp_f32 v151,v151\nM v[4:7],a[96:99],v[212:215],v[4:7]\nds_read_b128 a[32:35],v250 offset:8192\nds_read_b128 a[36:39],v250 offset:9216\n" \
"M v[12:15],a[68:71],v[196:199],v[12:15]\nv_exp_f32 v160,v160\nM v[12:15],a[100:103],v[212:215],v[12:15]\nbuffer_load_dwordx4 v240,s[48:51],s95 offen offset:2048 sc1 lds\nM v[20:23],a[72:75],v[196:199],v[20:23]\nv_exp_f32 v161,v161\nM v[20:23],a[104:107],v[212:215],v[20:23]\nds_read_b128 a[40:43],v250 offset:10240\n" \
"ds_read_b128 a[44:47],v250 offset:11264\nM v[28:31],a[76:79],v[196:199],v[28:31]\nv_exp_f32 v162,v162\nM v[28:31],a[108:111],v[212:215],v[28:31]\nv_exp_f32 v163,v163\nM v[36:39],a[80:83],v[196:199],v[36:39]\nv_cvt_pk_bf16_f32 v208,v160,v161\nv_cvt_pk_bf16_f32 v209,v162,v163\n" \
"M v[36:39],a[112:115],v[212:215],v[36:39]\nds_read_b128 a[48:51],v250 offset:12288\nds_read_b128 a[52:55],v250 offset:13312\nM v[44:47],a[84:87],v[196:199],v[44:47]\nv_exp_f32 v164,v164\nM v[44:47],a[116:119],v[212:215],v[44:47]\nbuffer_load_dwordx4 v240,s[48:51],s95 offen offset:3072 sc1 lds\nM v[52:55],a[88:91],v[196:199],v[52:55]\n" \
"s_add_u32 s95,s95,0x4000\ns_xor_b32 m0,m0,0x1c000\nM v[52:55],a[120:123],v[212:215],v[52:55]\nds_read_b128 a[56:59],v250 offset:14336\nds_read_b128 a[60:63],v250 offset:15360\nM v[60:63],a[92:95],v[196:199],v[60:63]\nv_exp_f32 v165,v165\nM v[60:63],a[124:127],v[212:215],v[60:63]\n" \
"v_exp_f32 v166,v166\nM a[196:199],a[208:211],v[196:199],a[196:199]\nv_xor_b32 v250,0x14000,v250\nv_cvt_pk_bf16_f32 v196,v132,v133\nM a[196:199],a[208:211],v[212:215],a[196:199]\nv_cvt_pk_bf16_f32 v197,v134,v135\ns_waitcnt lgkmcnt(4)\nM v[128:131],a[0:3],a[128:131],v[224:227]\n" \
"v_cvt_pk_bf16_f32 v198,v148,v149\nv_cvt_pk_bf16_f32 v199,v150,v151\nM v[128:131],a[4:7],a[132:135],v[128:131]\nds_read_b128 a[64:67],v250 offset:0\nds_read_b128 a[96:99],v250 offset:8192\nM v[128:131],a[8:11],a[136:139],v[128:131]\nbuffer_load_dwordx4 v240,s[44:47],s95 offen offset:0 sc1 lds\nM v[128:131],a[12:15],a[140:143],v[128:131]\n" \
"v_exp_f32 v167,v167\nM v[132:135],a[12:15],a[156:159],v[228:231]\nds_read_b128 a[68:71],v250 offset:1024\nds_read_b128 a[100:103],v250 offset:9216\nM v[132:135],a[8:11],a[152:155],v[132:135]\nv_cvt_pk_bf16_f32 v212,v164,v165\nv_cvt_pk_bf16_f32 v213,v166,v167\nM v[132:135],a[4:7],a[148:151],v[132:135]\n" \
"v_exp_f32 v176,v176\nM v[132:135],a[0:3],a[144:147],v[132:135]\nds_read_b128 a[72:75],v250 offset:2048\nds_read_b128 a[104:107],v250 offset:10240\nM v[144:147],a[16:19],a[128:131],v[224:227]\nv_exp_f32 v177,v177\nM v[144:147],a[20:23],a[132:135],v[144:147]\nv_exp_f32 v178,v178\n" \
"M v[144:147],a[24:27],a[136:139],v[144:147]\nds_read_b128 a[76:79],v250 offset:3072\nds_read_b128 a[108:111],v250 offset:11264\nM v[144:147],a[28:31],a[140:143],v[144:147]\nbuffer_load_dwordx4 v240,s[44:47],s95 offen offset:1024 sc1 lds\nM v[148:151],a[28:31],a[156:159],v[228:231]\nv_exp_f32 v179,v179\nM v[148:151],a[24:27],a[152:155],v[148:151]\n" \
"ds_read_b128 a[80:83],v250 offset:4096\nds_read_b128 a[112:115],v250 offset:12288\nM v[148:151],a[20:23],a[148:151],v[148:151]\nv_cvt_pk_bf16_f32 v210,v176,v177\nv_cvt_pk_bf16_f32 v211,v178,v179\nM v[148:151],a[16:19],a[144:147],v[148:151]\nv_exp_f32 v180,v180\nM v[160:163],a[32:35],a[128:131],v[224:227]\n" \
"ds_read_b128 a[84:87],v250 offset:5120\nds_read_b128 a[116:119],v250 offset:13312\nM v[160:163],a[36:39],a[132:135],v[160:163]\nv_exp_f32 v181,v181\nM v[160:163],a[40:43],a[136:139],v[160:163]\nv_exp_f32 v182,v182\nM v[160:163],a[44:47],a[140:143],v[160:163]\nds_read_b128 a[88:91],v250 offset:6144\n" \
"ds_read_b128 a[120:123],v250 offset:14336\nM v[164:167],a[44:47],a[156:159],v[228:231]\nbuffer_load_dwordx4 v240,s[44:47],s95 offen offset:2048 sc1 lds\nM v[164:167],a[40:43],a[152:155],v[164:167]\nv_exp_f32 v183,v183\nM v[164:167],a[36:39],a[148:151],v[164:167]\nds_read_b128 a[92:95],v250 offset:7168\nds_read_b128 a[124:127],v250 offset:15360\n" \
"M v[164:167],a[32:35],a[144:147],v[164:167]\nv_cvt_pk_bf16_f32 v214,v180,v181\ns_waitcnt lgkmcnt(7)\nM v[176:179],a[48:51],a[128:131],v[224:227]\nv_cvt_pk_bf16_f32 v215,v182,v183\nv_xor_b32 v250,0x18000,v250\nM v[176:179],a[52:55],a[132:135],v[176:179]\nv_exp_f32 v128,v128\n" \
"M v[176:179],a[56:59],a[136:139],v[176:179]\nv_exp_f32 v129,v129\nM v[176:179],a[60:63],a[140:143],v[176:179]\nv_exp_f32 v130,v130\nM v[180:183],a[60:63],a[156:159],v[228:231]\nv_exp_f32 v131,v131\nM v[180:183],a[56:59],a[152:155],v[180:183]\nbuffer_load_dwordx4 v240,s[44:47],s95 offen offset:3072 sc1 lds\n" \
"M v[180:183],a[52:55],a[148:151],v[180:183]\nv_exp_f32 v144,v144\nM v[180:183],a[48:51],a[144:147],v[180:183]\nv_exp_f32 v145,v145\nM v[0:3],a[64:67],v[192:195],v[0:3]\nv_exp_f32 v146,v146\nM v[0:3],a[96:99],v[208:211],v[0:3]\nv_exp_f32 v147,v147\n" \
"M v[8:11],a[68:71],v[192:195],v[8:11]\ns_waitcnt vmcnt(16) lgkmcnt(0)\ns_barrier\nM v[8:11],a[100:103],v[208:211],v[8:11]\nds_read_b128 a[0:3],v250 offset:0\nds_read_b128 a[4:7],v250 offset:1024\nM v[16:19],a[72:75],v[192:195],v[16:19]\ns_xor_b32 m0,m0,0x10000\n" \
"M v[16:19],a[104:107],v[208:211],v[16:19]\nbuffer_load_dwordx4 v240,s[48:51],s95 offen offset:0 sc1 lds\nM v[24:27],a[76:79],v[192:195],v[24:27]\nv_exp_f32 v160,v160\nM v[24:27],a[108:111],v[208:211],v[24:27]\nds_read_b128 a[8:11],v250 offset:2048\nds_read_b128 a[12:15],v250 offset:3072\nM v[32:35],a[80:83],v[192:195],v[32:35]\n" \
"v_exp_f32 v161,v161\nM v[32:35],a[112:115],v[208:211],v[32:35]\nv_exp_f32 v162,v162\nM v[40:43],a[84:87],v[192:195],v[40:43]\nv_exp_f32 v163,v163\nM v[40:43],a[116:119],v[208:211],v[40:43]\nds_read_b128 a[16:19],v250 offset:4096\nds_read_b128 a[20:23],v250 offset:5120\n" \
"M v[48:51],a[88:91],v[192:195],v[48:51]\nv_exp_f32 v176,v176\nM v[48:51],a[120:123],v[208:211],v[48:51]\nbuffer_load_dwordx4 v240,s[48:51],s95 offen offset:1024 sc1 lds\nM v[56:59],a[92:95],v[192:195],v[56:59]\nv_exp_f32 v177,v177\nM v[56:59],a[124:127],v[208:211],v[56:59]\nds_read_b128 a[24:27],v250 offset:6144\n" \
"ds_read_b128 a[28:31],v250 offset:7168\nM a[192:195],a[208:211],v[192:195],a[192:195]\nv_exp_f32 v178,v178\nM a[192:195],a[208:211],v[208:211],a[192:195]\nv_exp_f32 v179,v179\nM v[4:7],a[64:67],v[196:199],v[4:7]\nv_exp_f32 v132,v132\nM v[4:7],a[96:99],v[212:215],v[4:7]\n" \
"ds_read_b128 a[32:35],v250 offset:8192\nds_read_b128 a[36:39],v250 offset:9216\nM v[12:15],a[68:71],v[196:199],v[12:15]\nv_exp_f32 v133,v133\nM v[12:15],a[100:103],v[212:215],v[12:15]\nbuffer_load_dwordx4 v240,s[48:51],s95 offen offset:2048 sc1 lds\nM v[20:23],a[72:75],v[196:199],v[20:23]\nv_exp_f32 v134,v134\n" \
"M v[20:23],a[104:107],v[212:215],v[20:23]\nds_read_b128 a[40:43],v250 offset:10240\nds_read_b128 a[44:47],v250 offset:11264\nM v[28:31],a[76:79],v[196:199],v[28:31]\nv_exp_f32 v135,v135\nM v[28:31],a[108:111],v[212:215],v[28:31]\nv_exp_f32 v148,v148\nM v[36:39],a[80:83],v[196:199],v[36:39]\n" \
"v_exp_f32 v149,v149\nM v[36:39],a[112:115],v[212:215],v[36:39]\nds_read_b128 a[48:51],v250 offset:12288\nds_read_b128 a[52:55],v250 offset:13312\nM v[44:47],a[84:87],v[196:199],v[44:47]\nv_exp_f32 v150,v150\nM v[44:47],a[116:119],v[212:215],v[44:47]\nbuffer_load_dwordx4 v240,s[48:51],s95 offen offset:3072 sc1 lds\n" \
"M v[52:55],a[88:91],v[196:199],v[52:55]\nv_exp_f32 v151,v151\nM v[52:55],a[120:123],v[212:215],v[52:55]\nds_read_b128 a[56:59],v250 offset:14336\nds_read_b128 a[60:63],v250 offset:15360\nM v[60:63],a[92:95],v[196:199],v[60:63]\nv_cvt_pk_bf16_f32 v192,v128,v129\nv_cvt_pk_bf16_f32 v193,v130,v131\n" \
"M v[60:63],a[124:127],v[212:215],v[60:63]\nv_cvt_pk_bf16_f32 v194,v144,v145\nv_cvt_pk_bf16_f32 v195,v146,v147\nM a[196:199],a[208:211],v[196:199],a[196:199]\nv_cvt_pk_bf16_f32 v208,v160,v161\nv_cvt_pk_bf16_f32 v209,v162,v163\nv_cvt_pk_bf16_f32 v210,v176,v177\nv_cvt_pk_bf16_f32 v211,v178,v179\n" \
"v_xor_b32 v250,0x1c000,v250\ns_add_u32 s95,s95,0x4000\nM a[196:199],a[208:211],v[212:215],a[196:199]\ns_cmp_lg_u32 s95,s15\ns_cbranch_scc1 .Lsingle\ns_and_b32 s94,m0,0x8000\ns_or_b32 s94,s94,0x4000\n.Lsx:\n" \
".Lsw:\ns_waitcnt lgkmcnt(4)\nM v[128:131],a[0:3],a[128:131],v[224:227]\ns_lshl4_add_u32 s69,s12,s68\ns_add_u32 m0,s64,131072\nM v[128:131],a[4:7],a[132:135],v[128:131]\nds_read_b128 a[64:67],v250 offset:0\nds_read_b128 a[96:99],v250 offset:8192\n" \
"v_cvt_pk_bf16_f32 v196,v132,v133\nM v[128:131],a[8:11],a[136:139],v[128:131]\nbuffer_load_dwordx4 v243,s[40:43],s69 offen offset:0 lds\nM v[128:131],a[12:15],a[140:143],v[128:131]\nv_cvt_pk_bf16_f32 v197,v134,v135\nv_cvt_pk_bf16_f32 v198,v148,v149\nv_cvt_pk_bf16_f32 v199,v150,v151\nM v[132:135],a[12:15],a[156:159],v[228:231]\n" \
"ds_read_b128 a[68:71],v250 offset:1024\nds_read_b128 a[100:103],v250 offset:9216\nM v[132:135],a[8:11],a[152:155],v[132:135]\nbuffer_load_dwordx4 v244,s[40:43],s69 offen offset:1024 lds\ns_add_u32 s94,s94,0x4000\nM v[132:135],a[4:7],a[148:151],v[132:135]\nv_exp_f32 v164,v164\nM v[132:135],a[0:3],a[144:147],v[132:135]\n" \
"ds_read_b128 a[72:75],v250 offset:2048\nds_read_b128 a[104:107],v250 offset:10240\ns_and_b32 s94,s94,0xffff\nM v[144:147],a[16:19],a[128:131],v[224:227]\nbuffer_load_dwordx4 v245,s[40:43],s69 offen offset:2048 lds\nM v[144:147],a[20:23],a[132:135],v[144:147]\nv_exp_f32 v165,v165\ns_add_u32 s95,s95,0x4000\n" \
"M v[144:147],a[24:27],a[136:139],v[144:147]\nds_read_b128 a[76:79],v250 offset:3072\nds_read_b128 a[108:111],v250 offset:11264\nM v[144:147],a[28:31],a[140:143],v[144:147]\nbuffer_load_dwordx4 v246,s[40:43],s69 offen offset:3072 lds\ns_lshl4_add_u32 s69,s12,s68\nM v[148:151],a[28:31],a[156:159],v[228:231]\ns_add_u32 s69,s69,0x10000\n" \
"s_add_u32 m0,s64,147456\nM v[148:151],a[24:27],a[152:155],v[148:151]\nds_read_b128 a[80:83],v250 offset:4096\nds_read_b128 a[112:115],v250 offset:12288\nv_cvt_pk_bf16_f32 v212,v164,v165\nM v[148:151],a[20:23],a[148:151],v[148:151]\nv_exp_f32 v166,v166\nM v[148:151],a[16:19],a[144:147],v[148:151]\n" \
"v_exp_f32 v167,v167\nM v[160:163],a[32:35],a[128:131],v[224:227]\nds_read_b128 a[84:87],v250 offset:5120\nds_read_b128 a[116:119],v250 offset:13312\nv_cvt_pk_bf16_f32 v213,v166,v167\nM v[160:163],a[36:39],a[132:135],v[160:163]\nbuffer_load_dwordx4 v243,s[40:43],s69 offen offset:0 lds\nM v[160:163],a[40:43],a[136:139],v[160:163]\n" \
"v_exp_f32 v180,v180\nM v[160:163],a[44:47],a[140:143],v[160:163]\nds_read_b128 a[88:91],v250 offset:6144\nds_read_b128 a[120:123],v250 offset:14336\nM v[164:167],a[44:47],a[156:159],v[228:231]\nv_exp_f32 v181,v181\nM v[164:167],a[40:43],a[152:155],v[164:167]\nbuffer_load_dwordx4 v244,s[40:43],s69 offen offset:1024 lds\n" \
"M v[164:167],a[36:39],a[148:151],v[164:167]\nds_read_b128 a[92:95],v250 offset:7168\nds_read_b128 a[124:127],v250 offset:15360\nv_cvt_pk_bf16_f32 v214,v180,v181\nM v[164:167],a[32:35],a[144:147],v[164:167]\nv_xor_b32 v250,0x18000,v250\ns_waitcnt lgkmcnt(6)\nM v[176:179],a[48:51],a[128:131],v[224:227]\n" \
"s_waitcnt vmcnt(6)\nbuffer_store_dword v233,off,s[52:55],s77 sc1\nM v[176:179],a[52:55],a[132:135],v[176:179]\nbuffer_load_dwordx4 v245,s[40:43],s69 offen offset:2048 lds\nM v[176:179],a[56:59],a[136:139],v[176:179]\nv_exp_f32 v182,v182\nv_exp_f32 v183,v183\nM v[176:179],a[60:63],a[140:143],v[176:179]\n" \
"v_cvt_pk_bf16_f32 v215,v182,v183\nM v[180:183],a[60:63],a[156:159],v[228:231]\nv_exp_f32 v128,v128\nM v[180:183],a[56:59],a[152:155],v[180:183]\nbuffer_load_dwordx4 v246,s[40:43],s69 offen offset:3072 lds\nM v[180:183],a[52:55],a[148:151],v[180:183]\nv_exp_f32 v129,v129\nM v[180:183],a[48:51],a[144:147],v[180:183]\n" \
"s_lshl4_add_u32 s69,s10,s68\nv_exp_f32 v130,v130\nM v[0:3],a[64:67],v[192:195],v[0:3]\nv_exp_f32 v131,v131\nM v[0:3],a[96:99],v[208:211],v[0:3]\ns_add_u32 m0,s64,0\nv_exp_f32 v144,v144\nM v[8:11],a[68:71],v[192:195],v[8:11]\n" \
"s_waitcnt vmcnt(16) lgkmcnt(0)\ns_barrier\nM v[8:11],a[100:103],v[208:211],v[8:11]\nds_read_b128 a[0:3],v250 offset:0\nds_read_b128 a[4:7],v250 offset:1024\nM v[16:19],a[72:75],v[192:195],v[16:19]\nv_exp_f32 v145,v145\nM v[16:19],a[104:107],v[208:211],v[16:19]\n" \
"buffer_load_dwordx4 v243,s[40:43],s69 offen offset:0 lds\nM v[24:27],a[76:79],v[192:195],v[24:27]\nv_exp_f32 v146,v146\nM v[24:27],a[108:111],v[208:211],v[24:27]\nds_read_b128 a[8:11],v250 offset:2048\nds_read_b128 a[12:15],v250 offset:3072\nM v[32:35],a[80:83],v[192:195],v[32:35]\nv_exp_f32 v147,v147\n" \
"M v[32:35],a[112:115],v[208:211],v[32:35]\nbuffer_load_dwordx4 v244,s[40:43],s69 offen offset:1024 lds\nM v[40:43],a[84:87],v[192:195],v[40:43]\nv_exp_f32 v160,v160\nM v[40:43],a[116:119],v[208:211],v[40:43]\nds_read_b128 a[16:19],v250 offset:4096\nds_read_b128 a[20:23],v250 offset:5120\nM v[48:51],a[88:91],v[192:195],v[48:51]\n" \
"v_exp_f32 v161,v161\nM v[48:51],a[120:123],v[208:211],v[48:51]\nbuffer_load_dwordx4 v245,s[40:43],s69 offen offset:2048 lds\nM v[56:59],a[92:95],v[192:195],v[56:59]\nv_exp_f32 v162,v162\nM v[56:59],a[124:127],v[208:211],v[56:59]\nds_read_b128 a[24:27],v250 offset:6144\nds_read_b128 a[28:31],v250 offset:7168\n" \
"M a[192:195],a[208:211],v[192:195],a[192:195]\nv_exp_f32 v163,v163\nv_cvt_pk_bf16_f32 v192,v128,v129\nM a[192:195],a[208:211],v[208:211],a[192:195]\nbuffer_load_dwordx4 v246,s[40:43],s69 offen offset:3072 lds\nM v[4:7],a[64:67],v[196:199],v[4:7]\ns_lshl4_add_u32 s69,s10,s68\ns_add_u32 s69,s69,0x10000\n" \
"s_add_u32 m0,s64,32768\nM v[4:7],a[96:99],v[212:215],v[4:7]\nds_read_b128 a[32:35],v250 offset:8192\nds_read_b128 a[36:39],v250 offset:9216\nM v[12:15],a[68:71],v[196:199],v[12:15]\nv_exp_f32 v176,v176\nv_cvt_pk_bf16_f32 v193,v130,v131\nM v[12:15],a[100:103],v[212:215],v[12:15]\n" \
"buffer_load_dwordx4 v243,s[40:43],s69 offen offset:0 lds\nM v[20:23],a[72:75],v[196:199],v[20:23]\nv_exp_f32 v177,v177\nv_cvt_pk_bf16_f32 v194,v144,v145\nM v[20:23],a[104:107],v[212:215],v[20:23]\nds_read_b128 a[40:43],v250 offset:10240\nds_read_b128 a[44:47],v250 offset:11264\nM v[28:31],a[76:79],v[196:199],v[28:31]\n" \
"v_exp_f32 v178,v178\nv_cvt_pk_bf16_f32 v195,v146,v147\nM v[28:31],a[108:111],v[212:215],v[28:31]\nbuffer_load_dwordx4 v244,s[40:43],s69 offen offset:1024 lds\nM v[36:39],a[80:83],v[196:199],v[36:39]\nv_exp_f32 v179,v179\nv_cvt_pk_bf16_f32 v208,v160,v161\nM v[36:39],a[112:115],v[212:215],v[36:39]\n" \
"ds_read_b128 a[48:51],v250 offset:12288\nds_read_b128 a[52:55],v250 offset:13312\nM v[44:47],a[84:87],v[196:199],v[44:47]\nv_exp_f32 v132,v132\nv_cvt_pk_bf16_f32 v209,v162,v163\nM v[44:47],a[116:119],v[212:215],v[44:47]\nbuffer_load_dwordx4 v245,s[40:43],s69 offen offset:2048 lds\nv_cvt_pk_bf16_f32 v210,v176,v177\n" \
"M v[52:55],a[88:91],v[196:199],v[52:55]\nv_exp_f32 v133,v133\nM v[52:55],a[120:123],v[212:215],v[52:55]\nds_read_b128 a[56:59],v250 offset:14336\nds_read_b128 a[60:63],v250 offset:15360\nv_cvt_pk_bf16_f32 v211,v178,v179\nM v[60:63],a[92:95],v[196:199],v[60:63]\nv_exp_f32 v134,v134\n" \
"M v[60:63],a[124:127],v[212:215],v[60:63]\nbuffer_load_dwordx4 v246,s[40:43],s69 offen offset:3072 lds\nv_add_u32 v250,s94,v241\nM a[196:199],a[208:211],v[196:199],a[196:199]\nv_exp_f32 v135,v135\nM a[196:199],a[208:211],v[212:215],a[196:199]\nv_exp_f32 v148,v148\nv_exp_f32 v149,v149\n" \
"v_exp_f32 v150,v150\nv_exp_f32 v151,v151\n.Lod:\ns_waitcnt lgkmcnt(2)\nM v[128:131],a[0:3],a[128:131],v[224:227]\nv_cvt_pk_bf16_f32 v196,v132,v133\nv_cvt_pk_bf16_f32 v197,v134,v135\nM v[128:131],a[4:7],a[132:135],v[128:131]\n" \
"ds_read_b128 a[64:67],v250 offset:0\nds_read_b128 a[96:99],v250 offset:8192\nM v[128:131],a[8:11],a[136:139],v[128:131]\ns_lshl2_add_u32 s39,s62,s7\ns_lshl_b32 s39,s39,5\nM v[128:131],a[12:15],a[140:143],v[128:131]\ns_add_u32 s39,s39,s63\nv_cvt_pk_bf16_f32 v198,v148,v149\n" \
"v_cvt_pk_bf16_f32 v199,v150,v151\nM v[132:135],a[12:15],a[156:159],v[228:231]\nds_read_b128 a[68:71],v250 offset:1024\nds_read_b128 a[100:103],v250 offset:9216\nM v[132:135],a[8:11],a[152:155],v[132:135]\ns_lshl2_add_u32 s39,s39,73728\nv_and_b32 v251,3,v249\nM v[132:135],a[4:7],a[148:151],v[132:135]\n" \
"s_waitcnt vmcnt(16)\nbuffer_store_dword v252,off,s[52:55],s39 sc1\nM v[132:135],a[0:3],a[144:147],v[132:135]\nds_read_b128 a[72:75],v250 offset:2048\nds_read_b128 a[104:107],v250 offset:10240\nv_lshlrev_b32 v251,2,v251\nM v[144:147],a[16:19],a[128:131],v[224:227]\nv_exp_f32 v164,v164\n" \
"M v[144:147],a[20:23],a[132:135],v[144:147]\nv_exp_f32 v165,v165\nM v[144:147],a[24:27],a[136:139],v[144:147]\nds_read_b128 a[76:79],v250 offset:3072\nds_read_b128 a[108:111],v250 offset:11264\nM v[144:147],a[28:31],a[140:143],v[144:147]\nv_exp_f32 v166,v166\nv_cvt_pk_bf16_f32 v212,v164,v165\n" \
"M v[148:151],a[28:31],a[156:159],v[228:231]\nv_exp_f32 v167,v167\nM v[148:151],a[24:27],a[152:155],v[148:151]\nds_read_b128 a[80:83],v250 offset:4096\nds_read_b128 a[112:115],v250 offset:12288\nM v[148:151],a[20:23],a[148:151],v[148:151]\nv_cvt_pk_bf16_f32 v213,v166,v167\ns_add_u32 s80,s79,4096\n" \
"M v[148:151],a[16:19],a[144:147],v[148:151]\nv_exp_f32 v180,v180\ns_add_u32 s94,s94,0x4000\nM v[160:163],a[32:35],a[128:131],v[224:227]\nds_read_b128 a[84:87],v250 offset:5120\nds_read_b128 a[116:119],v250 offset:13312\nM v[160:163],a[36:39],a[132:135],v[160:163]\nv_exp_f32 v181,v181\n" \
"M v[160:163],a[40:43],a[136:139],v[160:163]\nv_exp_f32 v182,v182\nM v[160:163],a[44:47],a[140:143],v[160:163]\nds_read_b128 a[88:91],v250 offset:6144\nds_read_b128 a[120:123],v250 offset:14336\nv_cvt_pk_bf16_f32 v214,v180,v181\nM v[164:167],a[44:47],a[156:159],v[228:231]\nbuffer_load_dword v248,v251,s[52:55],s80 offen sc1\n" \
"M v[164:167],a[40:43],a[152:155],v[164:167]\nv_exp_f32 v183,v183\nM v[164:167],a[36:39],a[148:151],v[164:167]\nds_read_b128 a[92:95],v250 offset:7168\nds_read_b128 a[124:127],v250 offset:15360\nM v[164:167],a[32:35],a[144:147],v[164:167]\nv_cvt_pk_bf16_f32 v215,v182,v183\ns_and_b32 s94,s94,0xffff\n" \
"v_add_u32 v250,s94,v241\nM v[176:179],a[48:51],a[128:131],v[224:227]\nv_exp_f32 v128,v128\nM v[176:179],a[52:55],a[132:135],v[176:179]\ns_add_u32 s65,s10,s67\ns_waitcnt lgkmcnt(4)\nM v[176:179],a[56:59],a[136:139],v[176:179]\nv_exp_f32 v129,v129\n" \
"M v[176:179],a[60:63],a[140:143],v[176:179]\nv_exp_f32 v130,v130\ns_add_u32 s65,s65,1048576\nM v[180:183],a[60:63],a[156:159],v[228:231]\nv_exp_f32 v131,v131\nM v[180:183],a[56:59],a[152:155],v[180:183]\nv_exp_f32 v144,v144\nM v[180:183],a[52:55],a[148:151],v[180:183]\n" \
"v_exp_f32 v145,v145\nM v[180:183],a[48:51],a[144:147],v[180:183]\nv_exp_f32 v146,v146\nv_add_u32 v226,131072,v242\nM v[0:3],a[64:67],v[192:195],v[0:3]\nv_exp_f32 v147,v147\nM v[0:3],a[96:99],v[208:211],v[0:3]\nv_exp_f32 v160,v160\n" \
"M v[8:11],a[68:71],v[192:195],v[8:11]\ns_waitcnt lgkmcnt(0)\ns_barrier\nM v[8:11],a[100:103],v[208:211],v[8:11]\ns_lshl4_add_u32 s69,s11,s68\ns_add_u32 m0,s64,16384\nv_xor_b32 v227,32,v226\nM v[16:19],a[72:75],v[192:195],v[16:19]\n" \
"v_exp_f32 v161,v161\nM v[16:19],a[104:107],v[208:211],v[16:19]\nv_exp_f32 v162,v162\nM v[24:27],a[76:79],v[192:195],v[24:27]\nv_exp_f32 v163,v163\nM v[24:27],a[108:111],v[208:211],v[24:27]\nv_exp_f32 v176,v176\ns_add_u32 s37,s12,s67\n" \
"M v[32:35],a[80:83],v[192:195],v[32:35]\nbuffer_load_dwordx4 v243,s[40:43],s69 offen offset:0 lds\nM v[32:35],a[112:115],v[208:211],v[32:35]\nv_exp_f32 v177,v177\nM v[40:43],a[84:87],v[192:195],v[40:43]\nv_exp_f32 v178,v178\nM v[40:43],a[116:119],v[208:211],v[40:43]\nv_exp_f32 v179,v179\n" \
"s_add_u32 s38,s37,4096\nM v[48:51],a[88:91],v[192:195],v[48:51]\nv_exp_f32 v132,v132\nM v[48:51],a[120:123],v[208:211],v[48:51]\nv_exp_f32 v133,v133\nM v[56:59],a[92:95],v[192:195],v[56:59]\nv_exp_f32 v134,v134\nM v[56:59],a[124:127],v[208:211],v[56:59]\n" \
"v_exp_f32 v135,v135\ns_lshl2_add_u32 s74,s63,s7\nM a[192:195],a[208:211],v[192:195],a[192:195]\nbuffer_load_dwordx4 v244,s[40:43],s69 offen offset:1024 lds\nM a[192:195],a[208:211],v[208:211],a[192:195]\nv_cvt_pk_bf16_f32 v192,v128,v129\nv_cvt_pk_bf16_f32 v193,v130,v131\nM v[4:7],a[64:67],v[196:199],v[4:7]\n" \
"ds_read_b128 a[64:67],v250 offset:0\nv_cvt_pk_bf16_f32 v194,v144,v145\nM v[4:7],a[96:99],v[212:215],v[4:7]\nds_read_b128 a[96:99],v250 offset:8192\nv_cvt_pk_bf16_f32 v195,v146,v147\nv_cvt_pk_bf16_f32 v208,v160,v161\nM v[12:15],a[68:71],v[196:199],v[12:15]\nds_read_b128 a[68:71],v250 offset:1024\n" \
"v_cvt_pk_bf16_f32 v209,v162,v163\nM v[12:15],a[100:103],v[212:215],v[12:15]\nds_read_b128 a[100:103],v250 offset:9216\nv_cvt_pk_bf16_f32 v210,v176,v177\nM v[20:23],a[72:75],v[196:199],v[20:23]\nds_read_b128 a[72:75],v250 offset:2048\nv_cvt_pk_bf16_f32 v211,v178,v179\nM v[20:23],a[104:107],v[212:215],v[20:23]\n" \
"ds_read_b128 a[104:107],v250 offset:10240\nv_exp_f32 v148,v148\nM v[28:31],a[76:79],v[196:199],v[28:31]\nbuffer_load_dwordx4 v245,s[40:43],s69 offen offset:2048 lds\nM v[28:31],a[108:111],v[212:215],v[28:31]\nds_read_b128 a[76:79],v250 offset:3072\nds_read_b128 a[108:111],v250 offset:11264\nM v[36:39],a[80:83],v[196:199],v[36:39]\n" \
"ds_read_b128 a[80:83],v250 offset:4096\ns_lshl_b32 s72,s62,7\nM v[36:39],a[112:115],v[212:215],v[36:39]\nds_read_b128 a[112:115],v250 offset:12288\nv_exp_f32 v149,v149\nM v[44:47],a[84:87],v[196:199],v[44:47]\nds_read_b128 a[84:87],v250 offset:5120\ns_add_u32 s95,s74,s72\n" \
"M v[44:47],a[116:119],v[212:215],v[44:47]\nds_read_b128 a[116:119],v250 offset:13312\ns_lshl2_add_u32 s76,s61,s79\nM v[52:55],a[88:91],v[196:199],v[52:55]\nds_read_b128 a[88:91],v250 offset:6144\ns_sub_u32 s74,s74,s72\nM v[52:55],a[120:123],v[212:215],v[52:55]\nds_read_b128 a[120:123],v250 offset:14336\n" \
"v_exp_f32 v150,v150\nM v[60:63],a[92:95],v[196:199],v[60:63]\nbuffer_load_dwordx4 v246,s[40:43],s69 offen offset:3072 lds\nM v[60:63],a[124:127],v[212:215],v[60:63]\ns_waitcnt vmcnt(4)\nv_and_b32 v248,s17,v248\nM a[196:199],a[208:211],v[196:199],a[196:199]\nv_cmp_eq_u32 vcc,0,v248\n" \
"ds_read_b128 a[92:95],v250 offset:7168\nM a[196:199],a[208:211],v[212:215],a[196:199]\ns_cbranch_vccnz .Lps6\n.Lpb6:\ns_lshl4_add_u32 s69,s11,s68\ns_waitcnt lgkmcnt(4)\nM v[0:3],a[64:67],v[192:195],v[0:3]\nbuffer_load_dwordx4 v[72:75],v240,s[52:55],s65 offen offset:0 sc1\n" \
"M v[0:3],a[96:99],v[208:211],v[0:3]\ns_add_u32 s69,s69,0x10000\ns_add_u32 m0,s64,49152\nM v[8:11],a[68:71],v[192:195],v[8:11]\nds_read_b128 a[124:127],v250 offset:15360\nv_cvt_pk_bf16_f32 v196,v132,v133\nM v[8:11],a[100:103],v[208:211],v[8:11]\nv_exp_f32 v151,v151\n" \
"v_cvt_pk_bf16_f32 v197,v134,v135\nM v[16:19],a[72:75],v[192:195],v[16:19]\nbuffer_load_dwordx4 v243,s[40:43],s69 offen offset:0 lds\nM v[16:19],a[104:107],v[208:211],v[16:19]\nv_exp_f32 v164,v164\nM v[24:27],a[76:79],v[192:195],v[24:27]\nv_exp_f32 v165,v165\nM v[24:27],a[108:111],v[208:211],v[24:27]\n" \
"v_exp_f32 v166,v166\nv_cvt_pk_bf16_f32 v198,v148,v149\nM v[32:35],a[80:83],v[192:195],v[32:35]\nbuffer_load_dwordx4 v[76:79],v240,s[52:55],s65 offen offset:1024 sc1\nM v[32:35],a[112:115],v[208:211],v[32:35]\nv_exp_f32 v167,v167\nM v[40:43],a[84:87],v[192:195],v[40:43]\nv_cvt_pk_bf16_f32 v199,v150,v151\n" \
"s_waitcnt lgkmcnt(0)\nM v[40:43],a[116:119],v[208:211],v[40:43]\nv_exp_f32 v180,v180\nv_cvt_pk_bf16_f32 v212,v164,v165\nM v[48:51],a[88:91],v[192:195],v[48:51]\nbuffer_load_dwordx4 v244,s[40:43],s69 offen offset:1024 lds\nM v[48:51],a[120:123],v[208:211],v[48:51]\nv_exp_f32 v181,v181\n" \
"M v[56:59],a[92:95],v[192:195],v[56:59]\nv_exp_f32 v182,v182\nM v[56:59],a[124:127],v[208:211],v[56:59]\nv_exp_f32 v183,v183\nv_cvt_pk_bf16_f32 v213,v166,v167\nM a[192:195],a[208:211],v[192:195],a[192:195]\nbuffer_load_dwordx4 v[88:91],v240,s[52:55],s65 offen offset:2048 sc1\nM a[192:195],a[208:211],v[208:211],a[192:195]\n" \
"v_cvt_pk_bf16_f32 v214,v180,v181\nv_cvt_pk_bf16_f32 v215,v182,v183\nM v[4:7],a[64:67],v[196:199],v[4:7]\ns_barrier\nds_read_b64_tr_b16 v[200:201],v226 offset:0\nM v[4:7],a[96:99],v[212:215],v[4:7]\nds_read_b64_tr_b16 v[202:203],v226 offset:4096\nds_read_b64_tr_b16 v[204:205],v226 offset:8192\n" \
"ds_read_b64_tr_b16 v[206:207],v226 offset:12288\nM v[12:15],a[68:71],v[196:199],v[12:15]\nbuffer_load_dwordx4 v245,s[40:43],s69 offen offset:2048 lds\nM v[12:15],a[100:103],v[212:215],v[12:15]\nds_read_b64_tr_b16 v[216:217],v227 offset:0\nds_read_b64_tr_b16 v[218:219],v227 offset:4096\nM v[20:23],a[72:75],v[196:199],v[20:23]\nds_read_b64_tr_b16 v[220:221],v227 offset:8192\n" \
"ds_read_b64_tr_b16 v[222:223],v227 offset:12288\nM v[20:23],a[104:107],v[212:215],v[20:23]\nds_read_b64_tr_b16 v[192:193],v226 offset:512\nds_read_b64_tr_b16 v[194:195],v226 offset:4608\nds_read_b64_tr_b16 v[208:209],v226 offset:8704\nM v[28:31],a[76:79],v[196:199],v[28:31]\nbuffer_load_dwordx4 v[92:95],v240,s[52:55],s65 offen offset:3072 sc1\nM v[28:31],a[108:111],v[212:215],v[28:31]\n" \
"s_add_u32 s65,s65,4096\nds_read_b64_tr_b16 v[210:211],v226 offset:12800\nM v[36:39],a[80:83],v[196:199],v[36:39]\nv_accvgpr_read_b32 v224,a192\nv_cmp_eq_u32 vcc,2,v248\nM v[36:39],a[112:115],v[212:215],v[36:39]\ns_cmp_lg_u64 vcc,0\ns_addc_u32 s99,s99,0\n" \
"v_xor_b32 v250,0x8000,v250\nM v[44:47],a[84:87],v[196:199],v[44:47]\nbuffer_load_dwordx4 v246,s[40:43],s69 offen offset:3072 lds\nM v[44:47],a[116:119],v[212:215],v[44:47]\nv_add_u32 v229,s64,v250\ns_lshl_b32 s72,s7,9\nM v[52:55],a[88:91],v[196:199],v[52:55]\nv_lshl_add_u32 v239,v249,3,s72\n" \
"s_lshr_b32 s72,s64,1\nM v[52:55],a[120:123],v[212:215],v[52:55]\nv_and_b32 v254,32,v249\ns_lshr_b32 s70,s60,1\nv_lshlrev_b32 v254,3,v254\nM v[60:63],a[92:95],v[196:199],v[60:63]\nbuffer_load_dwordx4 v[104:107],v240,s[52:55],s65 offen offset:0 sc1\nM v[60:63],a[124:127],v[212:215],v[60:63]\n" \
"s_lshl_b32 s70,s70,7\nv_lshl_add_u32 v254,v249,2,v254\nM a[196:199],a[208:211],v[196:199],a[196:199]\nds_read_b64_tr_b16 v[196:197],v227 offset:512\nds_read_b64_tr_b16 v[198:199],v227 offset:4608\nM a[196:199],a[208:211],v[212:215],a[196:199]\nds_read_b64_tr_b16 v[212:213],v227 offset:8704\nds_read_b64_tr_b16 v[214:215],v227 offset:12800\n" \
"s_waitcnt lgkmcnt(4)\nM v[128:131],v[64:67],v[200:203],0\ns_lshl_b32 s80,s62,9\nv_xor_b32 v254,s70,v254\ns_add_u32 s80,s80,73728\nv_and_b32 v236,15,v249\nM v[128:131],v[80:83],v[204:207],v[128:131]\nv_accvgpr_read_b32 v228,a196\n" \
"v_add_f32 v225,v224,v228\nv_rcp_f32 v224,v224\nv_lshrrev_b32 v235,4,v249\nM v[132:135],v[68:71],v[200:203],0\nv_mul_f32 v0,v0,v224\nv_mul_f32 v1,v1,v224\nv_mul_f32 v2,v2,v224\nv_mul_f32 v3,v3,v224\n" \
"M v[132:135],v[84:87],v[204:207],v[132:135]\nv_cvt_pk_bf16_f32 v0,v0,v1\nv_mul_f32 v8,v8,v224\nv_mul_f32 v9,v9,v224\nv_mul_f32 v10,v10,v224\nv_mul_f32 v11,v11,v224\nM v[136:139],v[64:67],v[216:219],0\nbuffer_load_dwordx4 v[108:111],v240,s[52:55],s65 offen offset:1024 sc1\n" \
"v_cvt_pk_bf16_f32 v1,v2,v3\nds_read_b64_tr_b16 v[200:201],v226 offset:1024\nM v[136:139],v[80:83],v[220:223],v[136:139]\nds_read_b64_tr_b16 v[202:203],v226 offset:5120\nv_cvt_pk_bf16_f32 v2,v8,v9\nv_cvt_pk_bf16_f32 v3,v10,v11\nds_read_b64_tr_b16 v[204:205],v226 offset:9216\nds_read_b64_tr_b16 v[206:207],v226 offset:13312\n" \
"M v[140:143],v[68:71],v[216:219],0\nbuffer_store_dwordx4 v[0:3],v240,s[52:55],s37 offen offset:0 sc1\nv_rcp_f32 v228,v228\nM v[140:143],v[84:87],v[220:223],v[140:143]\nv_mul_f32 v4,v4,v228\nv_mul_f32 v5,v5,v228\nds_read_b64_tr_b16 v[216:217],v227 offset:1024\nds_read_b64_tr_b16 v[218:219],v227 offset:5120\n" \
"v_mul_f32 v6,v6,v228\nM v[144:147],v[64:67],v[192:195],0\nv_mul_f32 v7,v7,v228\nv_cvt_pk_bf16_f32 v4,v4,v5\nv_mul_f32 v12,v12,v228\nv_mul_f32 v13,v13,v228\nM v[144:147],v[80:83],v[208:211],v[144:147]\nv_mul_f32 v14,v14,v228\n" \
"v_mul_f32 v15,v15,v228\nv_cvt_pk_bf16_f32 v5,v6,v7\nds_read_b64_tr_b16 v[220:221],v227 offset:9216\nds_read_b64_tr_b16 v[222:223],v227 offset:13312\nM v[148:151],v[68:71],v[192:195],0\nv_cvt_pk_bf16_f32 v6,v12,v13\nv_cvt_pk_bf16_f32 v7,v14,v15\nds_read_b64_tr_b16 v[192:193],v226 offset:1536\n" \
"ds_read_b64_tr_b16 v[194:195],v226 offset:5632\nM v[148:151],v[84:87],v[208:211],v[148:151]\nbuffer_store_dwordx4 v[4:7],v240,s[52:55],s37 offen offset:1024 sc1\nv_mul_f32 v16,v16,v224\nv_mul_f32 v17,v17,v224\ns_waitcnt lgkmcnt(6)\nM v[152:155],v[64:67],v[196:199],0\nbuffer_load_dwordx4 v[120:123],v240,s[52:55],s65 offen offset:2048 sc1\n" \
"ds_read_b64_tr_b16 v[208:209],v226 offset:9728\nds_read_b64_tr_b16 v[210:211],v226 offset:13824\nM v[152:155],v[80:83],v[212:215],v[152:155]\nv_mul_f32 v18,v18,v224\nv_mul_f32 v19,v19,v224\nv_cvt_pk_bf16_f32 v16,v16,v17\nv_mul_f32 v24,v24,v224\nv_mul_f32 v25,v25,v224\n" \
"M v[156:159],v[68:71],v[196:199],0\nv_mul_f32 v26,v26,v224\nv_mul_f32 v27,v27,v224\nv_cvt_pk_bf16_f32 v17,v18,v19\nv_cvt_pk_bf16_f32 v18,v24,v25\nM v[156:159],v[84:87],v[212:215],v[156:159]\nv_cvt_pk_bf16_f32 v19,v26,v27\nds_read_b64_tr_b16 v[196:197],v227 offset:1536\n" \
"ds_read_b64_tr_b16 v[198:199],v227 offset:5632\nds_read_b64_tr_b16 v[212:213],v227 offset:9728\nv_mul_f32 v20,v20,v228\nM v[160:163],v[64:67],v[200:203],0\nbuffer_store_dwordx4 v[16:19],v240,s[52:55],s37 offen offset:2048 sc1\nds_read_b64_tr_b16 v[214:215],v227 offset:13824\nv_mul_f32 v21,v21,v228\nM v[160:163],v[80:83],v[204:207],v[160:163]\n" \
"v_mul_f32 v22,v22,v228\nv_mul_f32 v23,v23,v228\nv_cvt_pk_bf16_f32 v20,v20,v21\nv_mul_f32 v28,v28,v228\nv_mul_f32 v29,v29,v228\nM v[164:167],v[68:71],v[200:203],0\nv_mul_f32 v30,v30,v228\nv_mul_f32 v31,v31,v228\n" \
"v_cvt_pk_bf16_f32 v21,v22,v23\nv_cvt_pk_bf16_f32 v22,v28,v29\nM v[164:167],v[84:87],v[204:207],v[164:167]\nv_cvt_pk_bf16_f32 v23,v30,v31\nds_read_b64_tr_b16 v[200:201],v226 offset:16384\nds_read_b64_tr_b16 v[202:203],v226 offset:20480\nds_read_b64_tr_b16 v[204:205],v226 offset:24576\ns_waitcnt lgkmcnt(6)\n" \
"M v[168:171],v[64:67],v[216:219],0\nbuffer_load_dwordx4 v[124:127],v240,s[52:55],s65 offen offset:3072 sc1\nds_read_b64_tr_b16 v[206:207],v226 offset:28672\nv_mul_f32 v32,v32,v224\nM v[168:171],v[80:83],v[220:223],v[168:171]\nbuffer_store_dwordx4 v[20:23],v240,s[52:55],s37 offen offset:3072 sc1\nv_mul_f32 v33,v33,v224\nv_mul_f32 v34,v34,v224\n" \
"v_mul_f32 v35,v35,v224\nM v[172:175],v[68:71],v[216:219],0\nv_cvt_pk_bf16_f32 v32,v32,v33\nv_mul_f32 v40,v40,v224\nv_mul_f32 v41,v41,v224\nv_mul_f32 v42,v42,v224\nM v[172:175],v[84:87],v[220:223],v[172:175]\nv_mul_f32 v43,v43,v224\n" \
"v_cvt_pk_bf16_f32 v33,v34,v35\nds_read_b64_tr_b16 v[216:217],v227 offset:16384\nds_read_b64_tr_b16 v[218:219],v227 offset:20480\nv_cvt_pk_bf16_f32 v34,v40,v41\nM v[176:179],v[64:67],v[192:195],0\nv_cvt_pk_bf16_f32 v35,v42,v43\nds_read_b64_tr_b16 v[220:221],v227 offset:24576\nds_read_b64_tr_b16 v[222:223],v227 offset:28672\n" \
"s_add_u32 s65,s11,s67\nM v[176:179],v[80:83],v[208:211],v[176:179]\ns_add_u32 s65,s65,1048576\nv_mul_f32 v36,v36,v228\nv_mul_f32 v37,v37,v228\nv_mul_f32 v38,v38,v228\nv_mul_f32 v39,v39,v228\nM v[180:183],v[68:71],v[192:195],0\n" \
"buffer_store_dwordx4 v[32:35],v240,s[52:55],s38 offen offset:0 sc1\nds_read_b64_tr_b16 v[192:193],v226 offset:16896\nds_read_b64_tr_b16 v[194:195],v226 offset:20992\nM v[180:183],v[84:87],v[208:211],v[180:183]\nv_cvt_pk_bf16_f32 v36,v36,v37\nv_mul_f32 v44,v44,v228\nv_mul_f32 v45,v45,v228\nv_mul_f32 v46,v46,v228\n" \
"s_waitcnt lgkmcnt(6)\nM v[184:187],v[64:67],v[196:199],0\nbuffer_load_dwordx4 a[0:3],v240,s[52:55],s65 offen offset:0 sc1\nv_mul_f32 v47,v47,v228\nv_cvt_pk_bf16_f32 v37,v38,v39\nM v[184:187],v[80:83],v[212:215],v[184:187]\nds_read_b64_tr_b16 v[208:209],v226 offset:25088\nds_read_b64_tr_b16 v[210:211],v226 offset:29184\n" \
"v_cvt_pk_bf16_f32 v38,v44,v45\nv_cvt_pk_bf16_f32 v39,v46,v47\nv_add_f32 v225,v225,v224\nM v[188:191],v[68:71],v[196:199],0\nds_read_b64_tr_b16 v[196:197],v227 offset:16896\nds_read_b64_tr_b16 v[198:199],v227 offset:20992\nv_add_f32 v225,v225,v228\nv_cmp_ngt_f32 vcc,s28,v225\n" \
"M v[188:191],v[84:87],v[212:215],v[188:191]\nbuffer_store_dwordx4 v[36:39],v240,s[52:55],s38 offen offset:1024 sc1\nv_mul_f32 v48,v48,v224\nv_mul_f32 v49,v49,v224\nds_read_b64_tr_b16 v[212:213],v227 offset:25088\nM v[128:131],v[96:99],v[200:203],v[128:131]\nds_read_b64_tr_b16 v[214:215],v227 offset:29184\ns_and_b64 vcc,vcc,s[90:91]\n" \
"s_addc_u32 s99,s99,0\nv_mul_f32 v50,v50,v224\nM v[128:131],v[112:115],v[204:207],v[128:131]\nv_mul_f32 v51,v51,v224\nv_cvt_pk_bf16_f32 v48,v48,v49\nv_mul_f32 v56,v56,v224\nv_mul_f32 v57,v57,v224\nv_mul_f32 v58,v58,v224\n" \
"M v[132:135],v[100:103],v[200:203],v[132:135]\nv_mul_f32 v59,v59,v224\nv_cvt_pk_bf16_f32 v49,v50,v51\nv_cvt_pk_bf16_f32 v50,v56,v57\nv_cvt_pk_bf16_f32 v51,v58,v59\nM v[132:135],v[116:119],v[204:207],v[132:135]\nv_mov_b32 v230,s99\nds_write_b32 v229,v230\n" \
"ds_read_b64_tr_b16 v[200:201],v226 offset:17408\nds_read_b64_tr_b16 v[202:203],v226 offset:21504\ns_waitcnt lgkmcnt(5)\nM v[136:139],v[96:99],v[216:219],v[136:139]\nbuffer_store_dwordx4 v[48:51],v240,s[52:55],s38 offen offset:2048 sc1\nbuffer_load_dwordx4 a[4:7],v240,s[52:55],s65 offen offset:1024 sc1\nM v[136:139],v[112:115],v[220:223],v[136:139]\nds_read_b64_tr_b16 v[204:205],v226 offset:25600\n" \
"ds_read_b64_tr_b16 v[206:207],v226 offset:29696\nv_mul_f32 v52,v52,v228\nv_mul_f32 v53,v53,v228\nv_mul_f32 v54,v54,v228\nM v[140:143],v[100:103],v[216:219],v[140:143]\nv_mul_f32 v55,v55,v228\nv_cvt_pk_bf16_f32 v52,v52,v53\nv_mul_f32 v60,v60,v228\n" \
"v_mul_f32 v61,v61,v228\nM v[140:143],v[116:119],v[220:223],v[140:143]\nv_mul_f32 v62,v62,v228\nv_mul_f32 v63,v63,v228\nv_cvt_pk_bf16_f32 v53,v54,v55\nv_cvt_pk_bf16_f32 v54,v60,v61\nv_cvt_pk_bf16_f32 v55,v62,v63\nM v[144:147],v[96:99],v[192:195],v[144:147]\n" \
"ds_read_b64_tr_b16 v[216:217],v227 offset:17408\nds_read_b64_tr_b16 v[218:219],v227 offset:21504\nds_read_b64_tr_b16 v[220:221],v227 offset:25600\ns_lshl4_add_u32 s69,s14,s68\nM v[144:147],v[112:115],v[208:211],v[144:147]\nbuffer_store_dwordx4 v[52:55],v240,s[52:55],s38 offen offset:3072 sc1\nds_read_b64_tr_b16 v[222:223],v227 offset:29696\ns_add_u32 m0,s64,65536\n" \
"v_lshl_add_u32 v229,v251,10,v250\nM v[148:151],v[100:103],v[192:195],v[148:151]\nds_read_b64_tr_b16 v[192:193],v226 offset:17920\nds_read_b64_tr_b16 v[194:195],v226 offset:22016\nv_lshlrev_b32 v236,1,v236\nv_lshl_or_b32 v235,v235,8,v236\nM v[148:151],v[116:119],v[208:211],v[148:151]\nds_read_b64_tr_b16 v[208:209],v226 offset:26112\n" \
"ds_read_b64_tr_b16 v[210:211],v226 offset:30208\nv_add_u32 v226,0,v242\ns_lshl_b32 s95,s95,10\nv_add_u32 v236,s95,v235\nM v[152:155],v[96:99],v[196:199],v[152:155]\nbuffer_load_dwordx4 a[8:11],v240,s[52:55],s65 offen offset:2048 sc1\ns_add_u32 s74,s74,1920\ns_waitcnt lgkmcnt(7)\n" \
"M v[152:155],v[112:115],v[212:215],v[152:155]\ns_lshl_b32 s74,s74,10\nv_add_u32 v237,s74,v235\ns_add_u32 s38,s33,16384\nv_lshlrev_b32 v238,1,v240\nv_add_u32 v238,83968,v238\nM v[156:159],v[100:103],v[196:199],v[156:159]\ns_barrier\n" \
"buffer_load_dwordx4 v243,s[40:43],s69 offen offset:0 lds\nds_read_b64_tr_b16 v[196:197],v227 offset:17920\nM v[156:159],v[116:119],v[212:215],v[156:159]\nds_read_b64_tr_b16 v[198:199],v227 offset:22016\nds_read_b32 v230,v229\nds_read_b64_tr_b16 v[212:213],v227 offset:26112\nv_add_u32 v239,83968,v239\nv_bfe_u32 v228,v249,2,2\n" \
"M v[160:163],v[96:99],v[200:203],v[160:163]\nds_read_b64_tr_b16 v[214:215],v227 offset:30208\nv_xor_b32 v227,32,v226\nv_lshrrev_b32 v229,4,v249\ns_mov_b32 s97,4096\nM v[160:163],v[112:115],v[204:207],v[160:163]\ns_add_u32 s95,s95,54525952\ns_add_u32 s74,s74,54525952\n" \
"M v[164:167],v[100:103],v[200:203],v[164:167]\nds_read_b64_tr_b16 v[200:201],v226 offset:0\nds_read_b64_tr_b16 v[202:203],v226 offset:4096\nM v[164:167],v[116:119],v[204:207],v[164:167]\nds_read_b64_tr_b16 v[204:205],v226 offset:8192\ns_waitcnt lgkmcnt(8)\nM v[168:171],v[96:99],v[216:219],v[168:171]\nbuffer_load_dwordx4 a[12:15],v240,s[52:55],s65 offen offset:3072 sc1\n" \
"M v[168:171],v[112:115],v[220:223],v[168:171]\nds_read_b64_tr_b16 v[206:207],v226 offset:12288\ns_add_u32 s65,s65,4096\nM v[172:175],v[100:103],v[216:219],v[172:175]\nbuffer_load_dwordx4 v244,s[40:43],s69 offen offset:1024 lds\nM v[172:175],v[116:119],v[220:223],v[172:175]\nds_read_b64_tr_b16 v[216:217],v227 offset:0\nds_read_b64_tr_b16 v[218:219],v227 offset:4096\n" \
"M v[176:179],v[96:99],v[192:195],v[176:179]\nds_read_b64_tr_b16 v[220:221],v227 offset:8192\nds_read_b64_tr_b16 v[222:223],v227 offset:12288\nM v[176:179],v[112:115],v[208:211],v[176:179]\ns_waitcnt lgkmcnt(8)\nv_cmp_ne_u32 vcc,0,v230\nM v[180:183],v[100:103],v[192:195],v[180:183]\ns_cbranch_vccnz .Lredo\n" \
"ds_read_b64_tr_b16 v[192:193],v226 offset:512\nM v[180:183],v[116:119],v[208:211],v[180:183]\nds_read_b64_tr_b16 v[194:195],v226 offset:4608\nds_read_b64_tr_b16 v[208:209],v226 offset:8704\nM v[184:187],v[96:99],v[196:199],v[184:187]\nbuffer_load_dwordx4 a[16:19],v240,s[52:55],s65 offen offset:0 sc1\nM v[184:187],v[112:115],v[212:215],v[184:187]\nds_read_b64_tr_b16 v[210:211],v226 offset:12800\n" \
"s_and_b32 s58,s3,16384\nM v[188:191],v[100:103],v[196:199],v[188:191]\nbuffer_load_dwordx4 v245,s[40:43],s69 offen offset:2048 lds\nM v[188:191],v[116:119],v[212:215],v[188:191]\nds_read_b64_tr_b16 v[196:197],v227 offset:512\ns_waitcnt vmcnt(27) lgkmcnt(6)\nM v[128:131],v[72:75],v[200:203],v[128:131]\nds_read_b64_tr_b16 v[198:199],v227 offset:4608\n" \
"s_waitcnt vmcnt(23)\nM v[128:131],v[88:91],v[204:207],v[128:131]\nds_read_b64_tr_b16 v[212:213],v227 offset:8704\nds_read_b64_tr_b16 v[214:215],v227 offset:12800\nM v[132:135],v[76:79],v[200:203],v[132:135]\nds_read_b64_tr_b16 v[200:201],v226 offset:1024\ns_waitcnt vmcnt(21)\nM v[132:135],v[92:95],v[204:207],v[132:135]\n" \
"ds_read_b64_tr_b16 v[202:203],v226 offset:5120\nds_read_b64_tr_b16 v[204:205],v226 offset:9216\nM v[136:139],v[72:75],v[216:219],v[136:139]\nbuffer_load_dwordx4 a[20:23],v240,s[52:55],s65 offen offset:1024 sc1\ns_waitcnt lgkmcnt(7)\nM v[136:139],v[88:91],v[220:223],v[136:139]\nds_read_b64_tr_b16 v[206:207],v226 offset:13312\nv_mov_b32 v250,82944\n" \
"M v[140:143],v[76:79],v[216:219],v[140:143]\nbuffer_load_dwordx4 v246,s[40:43],s69 offen offset:3072 lds\nM v[140:143],v[92:95],v[220:223],v[140:143]\nds_read_b64_tr_b16 v[216:217],v227 offset:1024\nds_read_b64_tr_b16 v[218:219],v227 offset:5120\nM v[144:147],v[72:75],v[192:195],v[144:147]\nds_read_b64_tr_b16 v[220:221],v227 offset:9216\nds_read_b64_tr_b16 v[222:223],v227 offset:13312\n" \
"M v[144:147],v[88:91],v[208:211],v[144:147]\ns_lshl4_add_u32 s69,s14,s68\ns_add_u32 s69,s69,0x10000\nM v[148:151],v[76:79],v[192:195],v[148:151]\nds_read_b64_tr_b16 v[192:193],v226 offset:1536\nds_read_b64_tr_b16 v[194:195],v226 offset:5632\nM v[148:151],v[92:95],v[208:211],v[148:151]\ns_add_u32 m0,s64,98304\n" \
"s_waitcnt lgkmcnt(7)\nM v[152:155],v[72:75],v[196:199],v[152:155]\nbuffer_load_dwordx4 a[24:27],v240,s[52:55],s65 offen offset:2048 sc1\nM v[152:155],v[88:91],v[212:215],v[152:155]\nds_read_b64_tr_b16 v[208:209],v226 offset:9728\nds_read_b64_tr_b16 v[210:211],v226 offset:13824\nM v[156:159],v[76:79],v[196:199],v[156:159]\nbuffer_load_dwordx4 v243,s[40:43],s69 offen offset:0 lds\n" \
"M v[156:159],v[92:95],v[212:215],v[156:159]\nds_read_b64_tr_b16 v[196:197],v227 offset:1536\nds_read_b64_tr_b16 v[198:199],v227 offset:5632\nM v[160:163],v[72:75],v[200:203],v[160:163]\nds_read_b64_tr_b16 v[212:213],v227 offset:9728\ns_waitcnt lgkmcnt(7)\nM v[160:163],v[88:91],v[204:207],v[160:163]\nds_read_b64_tr_b16 v[214:215],v227 offset:13824\n" \
"s_lshl_b32 s70,s2,9\nM v[164:167],v[76:79],v[200:203],v[164:167]\nds_read_b64_tr_b16 v[200:201],v226 offset:32768\nds_read_b64_tr_b16 v[202:203],v226 offset:36864\nM v[164:167],v[92:95],v[204:207],v[164:167]\nds_read_b64_tr_b16 v[204:205],v226 offset:40960\nds_read_b64_tr_b16 v[206:207],v226 offset:45056\nM v[168:171],v[72:75],v[216:219],v[168:171]\n" \
"buffer_load_dwordx4 a[28:31],v240,s[52:55],s65 offen offset:3072 sc1\nM v[168:171],v[88:91],v[220:223],v[168:171]\ns_add_u32 s65,s14,s67\ns_add_u32 s65,s65,1048576\nM v[172:175],v[76:79],v[216:219],v[172:175]\nbuffer_load_dwordx4 v244,s[40:43],s69 offen offset:1024 lds\nM v[172:175],v[92:95],v[220:223],v[172:175]\nds_read_b64_tr_b16 v[216:217],v227 offset:32768\n" \
"s_waitcnt lgkmcnt(6)\nM v[176:179],v[72:75],v[192:195],v[176:179]\nds_read_b64_tr_b16 v[218:219],v227 offset:36864\nds_read_b64_tr_b16 v[220:221],v227 offset:40960\nM v[176:179],v[88:91],v[208:211],v[176:179]\nds_read_b64_tr_b16 v[222:223],v227 offset:45056\ns_lshl_b32 s71,s7,8\nM v[180:183],v[76:79],v[192:195],v[180:183]\n" \
"ds_read_b64_tr_b16 v[192:193],v226 offset:33280\nds_read_b64_tr_b16 v[194:195],v226 offset:37376\nM v[180:183],v[92:95],v[208:211],v[180:183]\nds_read_b64_tr_b16 v[208:209],v226 offset:41472\nds_read_b64_tr_b16 v[210:211],v226 offset:45568\nM v[184:187],v[72:75],v[196:199],v[184:187]\nbuffer_load_dwordx4 a[32:35],v240,s[52:55],s65 offen offset:0 sc1\ns_waitcnt lgkmcnt(8)\n" \
"M v[184:187],v[88:91],v[212:215],v[184:187]\nds_write_b128 v250,a[244:247]\nM v[188:191],v[76:79],v[196:199],v[188:191]\nbuffer_load_dwordx4 v245,s[40:43],s69 offen offset:2048 lds\nM v[188:191],v[92:95],v[212:215],v[188:191]\nds_read_b64_tr_b16 v[196:197],v227 offset:33280\ns_waitcnt vmcnt(26)\nM v[128:131],v[104:107],v[200:203],v[128:131]\n" \
"ds_read_b64_tr_b16 v[198:199],v227 offset:37376\ns_waitcnt vmcnt(23)\nM v[128:131],v[120:123],v[204:207],v[128:131]\nds_read_b64_tr_b16 v[212:213],v227 offset:41472\nds_read_b64_tr_b16 v[214:215],v227 offset:45568\nM v[132:135],v[108:111],v[200:203],v[132:135]\nds_read_b64_tr_b16 v[200:201],v226 offset:33792\ns_waitcnt vmcnt(21)\n" \
"M v[132:135],v[124:127],v[204:207],v[132:135]\nds_read_b64_tr_b16 v[202:203],v226 offset:37888\ns_waitcnt lgkmcnt(7)\nM v[136:139],v[104:107],v[216:219],v[136:139]\nbuffer_load_dwordx4 a[36:39],v240,s[52:55],s65 offen offset:1024 sc1\nM v[136:139],v[120:123],v[220:223],v[136:139]\nds_read_b64_tr_b16 v[204:205],v226 offset:41984\nds_read_b64_tr_b16 v[206:207],v226 offset:46080\n" \
"M v[140:143],v[108:111],v[216:219],v[140:143]\nbuffer_load_dwordx4 v246,s[40:43],s69 offen offset:3072 lds\nM v[140:143],v[124:127],v[220:223],v[140:143]\nds_read_b64_tr_b16 v[216:217],v227 offset:33792\nds_read_b64_tr_b16 v[218:219],v227 offset:37888\nM v[144:147],v[104:107],v[192:195],v[144:147]\nds_read_b64_tr_b16 v[220:221],v227 offset:41984\nds_read_b64_tr_b16 v[222:223],v227 offset:46080\n" \
"M v[144:147],v[120:123],v[208:211],v[144:147]\nv_lshl_add_u32 v241,v249,2,s71\ns_add_u32 s70,s70,4096\nM v[148:151],v[108:111],v[192:195],v[148:151]\nds_read_b64_tr_b16 v[192:193],v226 offset:34304\nds_read_b64_tr_b16 v[194:195],v226 offset:38400\nM v[148:151],v[124:127],v[208:211],v[148:151]\nds_read_b64_tr_b16 v[208:209],v226 offset:42496\n" \
"s_waitcnt lgkmcnt(9)\nM v[152:155],v[104:107],v[196:199],v[152:155]\nbuffer_load_dwordx4 a[40:43],v240,s[52:55],s65 offen offset:2048 sc1\nM v[152:155],v[120:123],v[212:215],v[152:155]\nds_read_b64_tr_b16 v[210:211],v226 offset:46592\ns_add_u32 m0,s64,83968\nM v[156:159],v[108:111],v[196:199],v[156:159]\ns_waitcnt vmcnt(16)\n" \
"buffer_store_dword v252,off,s[52:55],s76 sc1\ns_barrier\nM v[156:159],v[124:127],v[212:215],v[156:159]\nds_read_b64_tr_b16 v[196:197],v227 offset:34304\nds_read_b64_tr_b16 v[198:199],v227 offset:38400\nM v[160:163],v[104:107],v[200:203],v[160:163]\nbuffer_load_dwordx4 v240,s[52:55],s72 offen offset:0 sc1 lds\ns_waitcnt lgkmcnt(6)\n" \
"M v[160:163],v[120:123],v[204:207],v[160:163]\nds_read_b64_tr_b16 v[212:213],v227 offset:42496\nds_read_b64_tr_b16 v[214:215],v227 offset:46592\nM v[164:167],v[108:111],v[200:203],v[164:167]\nds_read_b64_tr_b16 v[200:201],v226 offset:16384\nds_read_b64_tr_b16 v[202:203],v226 offset:20480\nM v[164:167],v[124:127],v[204:207],v[164:167]\nds_read_b64_tr_b16 v[204:205],v226 offset:24576\n" \
"ds_read_b64_tr_b16 v[206:207],v226 offset:28672\nM v[168:171],v[104:107],v[216:219],v[168:171]\nbuffer_load_dwordx4 v240,s[52:55],s72 offen offset:1024 sc1 lds\ns_lshl_b32 s72,s2,9\ns_add_u32 s56,s26,s72\ns_addc_u32 s57,s27,0\ns_add_u32 m0,s64,114688\nbuffer_load_dwordx4 a[44:47],v240,s[52:55],s65 offen offset:3072 sc1\n" \
"M v[168:171],v[120:123],v[220:223],v[168:171]\ns_add_u32 s65,s65,4096\nv_add_u32 v230,s70,v241\nM v[172:175],v[108:111],v[216:219],v[172:175]\nds_read_b64_tr_b16 v[216:217],v227 offset:16384\nds_read_b64_tr_b16 v[218:219],v227 offset:20480\nM v[172:175],v[124:127],v[220:223],v[172:175]\nds_read_b64_tr_b16 v[220:221],v227 offset:24576\n" \
"s_waitcnt lgkmcnt(7)\nM v[176:179],v[104:107],v[192:195],v[176:179]\nbuffer_load_dwordx4 v240,s[56:59],s64 offen offset:0 lds\nM v[176:179],v[120:123],v[208:211],v[176:179]\nds_read_b64_tr_b16 v[222:223],v227 offset:28672\nv_add_u32 v231,8192,v230\nM v[180:183],v[108:111],v[192:195],v[180:183]\nds_read_b64_tr_b16 v[192:193],v226 offset:16896\n" \
"ds_read_b64_tr_b16 v[194:195],v226 offset:20992\nM v[180:183],v[124:127],v[208:211],v[180:183]\nds_read_b64_tr_b16 v[208:209],v226 offset:25088\nds_read_b64_tr_b16 v[210:211],v226 offset:29184\nM v[184:187],v[104:107],v[196:199],v[184:187]\nbuffer_load_dwordx4 v240,s[56:59],s64 offen offset:1024 lds\nbuffer_load_dwordx4 a[48:51],v240,s[52:55],s65 offen offset:0 sc1\nM v[184:187],v[120:123],v[212:215],v[184:187]\n" \
"v_accvgpr_write_b32 a194,0\nv_accvgpr_write_b32 a195,0\nM v[188:191],v[108:111],v[196:199],v[188:191]\nds_read_b64_tr_b16 v[196:197],v227 offset:16896\nds_read_b64_tr_b16 v[198:199],v227 offset:20992\nM v[188:191],v[124:127],v[212:215],v[188:191]\nds_read_b64_tr_b16 v[212:213],v227 offset:25088\ns_waitcnt lgkmcnt(8)\n" \
"M v[128:131],a[0:3],v[200:203],v[128:131]\nbuffer_load_dwordx4 v240,s[56:59],s64 offen offset:2048 lds\nM v[128:131],a[8:11],v[204:207],v[128:131]\nds_read_b64_tr_b16 v[214:215],v227 offset:29184\nv_accvgpr_write_b32 a196,0\nM v[132:135],a[4:7],v[200:203],v[132:135]\nds_read_b64_tr_b16 v[200:201],v226 offset:17408\ns_waitcnt vmcnt(22)\n" \
"M v[132:135],a[12:15],v[204:207],v[132:135]\nds_read_b64_tr_b16 v[202:203],v226 offset:21504\nds_read_b64_tr_b16 v[204:205],v226 offset:25600\nM v[136:139],a[0:3],v[216:219],v[136:139]\nbuffer_load_dwordx4 v240,s[56:59],s64 offen offset:3072 lds\nbuffer_load_dwordx4 a[52:55],v240,s[52:55],s65 offen offset:1024 sc1\ns_waitcnt lgkmcnt(7)\nM v[136:139],a[8:11],v[220:223],v[136:139]\n" \
"ds_read_b64_tr_b16 v[206:207],v226 offset:29696\nv_accvgpr_write_b32 a197,0\nM v[140:143],a[4:7],v[216:219],v[140:143]\nds_read_b64_tr_b16 v[216:217],v227 offset:17408\nds_read_b64_tr_b16 v[218:219],v227 offset:21504\nM v[140:143],a[12:15],v[220:223],v[140:143]\nds_read_b64_tr_b16 v[220:221],v227 offset:25600\nds_read_b64_tr_b16 v[222:223],v227 offset:29696\n" \
"M v[144:147],a[0:3],v[192:195],v[144:147]\ns_load_dwordx2 s[44:45],s[4:5],0x68\ns_mov_b32 s46,2097152\nM v[144:147],a[8:11],v[208:211],v[144:147]\nM v[148:151],a[4:7],v[192:195],v[148:151]\nds_read_b64_tr_b16 v[192:193],v226 offset:17920\nds_read_b64_tr_b16 v[194:195],v226 offset:22016\nM v[148:151],a[12:15],v[208:211],v[148:151]\n" \
"ds_read_b64_tr_b16 v[208:209],v226 offset:26112\ns_waitcnt lgkmcnt(8)\nM v[152:155],a[0:3],v[196:199],v[152:155]\nbuffer_load_dwordx4 a[56:59],v240,s[52:55],s65 offen offset:2048 sc1\nM v[152:155],a[8:11],v[212:215],v[152:155]\nds_read_b64_tr_b16 v[210:211],v226 offset:30208\nM v[156:159],a[4:7],v[196:199],v[156:159]\nds_read_b64_tr_b16 v[196:197],v227 offset:17920\n" \
"ds_read_b64_tr_b16 v[198:199],v227 offset:22016\nM v[156:159],a[12:15],v[212:215],v[156:159]\nds_read_b64_tr_b16 v[212:213],v227 offset:26112\nds_read_b64_tr_b16 v[214:215],v227 offset:30208\nM v[160:163],a[0:3],v[200:203],v[160:163]\ns_waitcnt lgkmcnt(8)\nM v[160:163],a[8:11],v[204:207],v[160:163]\nM v[164:167],a[4:7],v[200:203],v[164:167]\n" \
"ds_read_b64_tr_b16 v[200:201],v226 offset:49152\nds_read_b64_tr_b16 v[202:203],v226 offset:53248\nM v[164:167],a[12:15],v[204:207],v[164:167]\nds_read_b64_tr_b16 v[204:205],v226 offset:57344\nds_read_b64_tr_b16 v[206:207],v226 offset:61440\nM v[168:171],a[0:3],v[216:219],v[168:171]\nbuffer_load_dwordx4 a[60:63],v240,s[52:55],s65 offen offset:3072 sc1\nM v[168:171],a[8:11],v[220:223],v[168:171]\n" \
"s_add_u32 s65,s10,s67\nM v[172:175],a[4:7],v[216:219],v[172:175]\nds_read_b64_tr_b16 v[216:217],v227 offset:49152\nds_read_b64_tr_b16 v[218:219],v227 offset:53248\nM v[172:175],a[12:15],v[220:223],v[172:175]\nds_read_b64_tr_b16 v[220:221],v227 offset:57344\ns_waitcnt lgkmcnt(8)\nM v[176:179],a[0:3],v[192:195],v[176:179]\n" \
"ds_read_b64_tr_b16 v[222:223],v227 offset:61440\nM v[176:179],a[8:11],v[208:211],v[176:179]\nM v[180:183],a[4:7],v[192:195],v[180:183]\nds_read_b64_tr_b16 v[192:193],v226 offset:49664\nds_read_b64_tr_b16 v[194:195],v226 offset:53760\nM v[180:183],a[12:15],v[208:211],v[180:183]\nds_read_b64_tr_b16 v[208:209],v226 offset:57856\nds_read_b64_tr_b16 v[210:211],v226 offset:61952\n" \
"M v[184:187],a[0:3],v[196:199],v[184:187]\ns_waitcnt lgkmcnt(8)\nM v[184:187],a[8:11],v[212:215],v[184:187]\nM v[188:191],a[4:7],v[196:199],v[188:191]\nds_read_b64_tr_b16 v[196:197],v227 offset:49664\nds_read_b64_tr_b16 v[198:199],v227 offset:53760\nM v[188:191],a[12:15],v[212:215],v[188:191]\nds_read_b64_tr_b16 v[212:213],v227 offset:57856\n" \
"s_waitcnt vmcnt(24)\nM v[128:131],a[16:19],v[200:203],v[128:131]\nds_read_b64_tr_b16 v[214:215],v227 offset:61952\ns_waitcnt vmcnt(20)\nM v[128:131],a[24:27],v[204:207],v[128:131]\nM v[132:135],a[20:23],v[200:203],v[132:135]\nds_read_b64_tr_b16 v[200:201],v226 offset:50176\ns_waitcnt vmcnt(18)\n" \
"M v[132:135],a[28:31],v[204:207],v[132:135]\nds_read_b64_tr_b16 v[202:203],v226 offset:54272\ns_waitcnt lgkmcnt(8)\nM v[136:139],a[16:19],v[216:219],v[136:139]\nds_read_b64_tr_b16 v[204:205],v226 offset:58368\nds_read_b64_tr_b16 v[206:207],v226 offset:62464\nM v[136:139],a[24:27],v[220:223],v[136:139]\nM v[140:143],a[20:23],v[216:219],v[140:143]\n" \
"ds_read_b64_tr_b16 v[216:217],v227 offset:50176\nds_read_b64_tr_b16 v[218:219],v227 offset:54272\nM v[140:143],a[28:31],v[220:223],v[140:143]\nds_read_b64_tr_b16 v[220:221],v227 offset:58368\nds_read_b64_tr_b16 v[222:223],v227 offset:62464\nM v[144:147],a[16:19],v[192:195],v[144:147]\nbuffer_load_dword v248,v254,s[52:55],s80 offen sc1\ns_waitcnt lgkmcnt(8)\n" \
"M v[144:147],a[24:27],v[208:211],v[144:147]\nM v[148:151],a[20:23],v[192:195],v[148:151]\nglobal_load_ushort v64,v236,s[8:9] offset:0\nM v[148:151],a[28:31],v[208:211],v[148:151]\nds_read_b64_tr_b16 v[192:193],v226 offset:50688\nds_read_b64_tr_b16 v[194:195],v226 offset:54784\nM v[152:155],a[16:19],v[196:199],v[152:155]\nglobal_load_ushort v80,v237,s[8:9] offset:0\n" \
"M v[152:155],a[24:27],v[212:215],v[152:155]\nds_read_b64_tr_b16 v[208:209],v226 offset:58880\nds_read_b64_tr_b16 v[210:211],v226 offset:62976\nM v[156:159],a[20:23],v[196:199],v[156:159]\ns_waitcnt vmcnt(6)\ns_barrier\nM v[156:159],a[28:31],v[212:215],v[156:159]\nglobal_load_ushort v96,v235,s[20:21] offset:0\n" \
"s_waitcnt lgkmcnt(4)\nM v[160:163],a[16:19],v[200:203],v[160:163]\nds_read_b64_tr_b16 v[196:197],v227 offset:50688\nds_read_b64_tr_b16 v[198:199],v227 offset:54784\nM v[160:163],a[24:27],v[204:207],v[160:163]\nglobal_load_ushort v65,v236,s[8:9] offset:32\nM v[164:167],a[20:23],v[200:203],v[164:167]\nds_read_b64_tr_b16 v[212:213],v227 offset:58880\n" \
"ds_read_b64_tr_b16 v[214:215],v227 offset:62976\nM v[164:167],a[28:31],v[204:207],v[164:167]\nglobal_load_ushort v81,v237,s[8:9] offset:32\nM v[168:171],a[16:19],v[216:219],v[168:171]\nv_add_u32 v226,65536,v242\nds_read_b64_tr_b16 v[200:201],v226 offset:0\nM v[168:171],a[24:27],v[220:223],v[168:171]\nds_read_b64_tr_b16 v[202:203],v226 offset:4096\n" \
"global_load_ushort v97,v235,s[20:21] offset:32\nM v[172:175],a[20:23],v[216:219],v[172:175]\nds_read_b64_tr_b16 v[204:205],v226 offset:8192\nds_read_b64_tr_b16 v[206:207],v226 offset:12288\nM v[172:175],a[28:31],v[220:223],v[172:175]\nglobal_load_ushort v66,v236,s[8:9] offset:64\ns_waitcnt lgkmcnt(6)\nM v[176:179],a[16:19],v[192:195],v[176:179]\n" \
"v_xor_b32 v227,32,v226\nds_read_b64_tr_b16 v[216:217],v227 offset:0\nM v[176:179],a[24:27],v[208:211],v[176:179]\nds_read_b64_tr_b16 v[218:219],v227 offset:4096\nglobal_load_ushort v82,v237,s[8:9] offset:64\nM v[180:183],a[20:23],v[192:195],v[180:183]\nds_read_b64_tr_b16 v[220:221],v227 offset:8192\nds_read_b64_tr_b16 v[222:223],v227 offset:12288\n" \
"M v[180:183],a[28:31],v[208:211],v[180:183]\nglobal_load_ushort v98,v235,s[20:21] offset:64\nM v[184:187],a[16:19],v[196:199],v[184:187]\nds_read_b64_tr_b16 v[192:193],v226 offset:512\ns_waitcnt lgkmcnt(7)\nM v[184:187],a[24:27],v[212:215],v[184:187]\nds_read_b64_tr_b16 v[194:195],v226 offset:4608\nglobal_load_ushort v67,v236,s[8:9] offset:96\n" \
"M v[188:191],a[20:23],v[196:199],v[188:191]\nds_read_b64_tr_b16 v[208:209],v226 offset:8704\nds_read_b64_tr_b16 v[210:211],v226 offset:12800\nM v[188:191],a[28:31],v[212:215],v[188:191]\nglobal_load_ushort v83,v237,s[8:9] offset:96\nM v[128:131],a[32:35],v[200:203],v[128:131]\nds_read_b64_tr_b16 v[196:197],v227 offset:512\ns_waitcnt lgkmcnt(7)\n" \
"M v[128:131],a[40:43],v[204:207],v[128:131]\nds_read_b64_tr_b16 v[198:199],v227 offset:4608\nglobal_load_ushort v99,v235,s[20:21] offset:96\nM v[132:135],a[36:39],v[200:203],v[132:135]\nds_read_b64_tr_b16 v[212:213],v227 offset:8704\nds_read_b64_tr_b16 v[214:215],v227 offset:12800\nM v[132:135],a[44:47],v[204:207],v[132:135]\nglobal_load_ushort v68,v236,s[8:9] offset:128\n" \
"M v[136:139],a[32:35],v[216:219],v[136:139]\nds_read_b64_tr_b16 v[200:201],v226 offset:1024\ns_waitcnt lgkmcnt(7)\nM v[136:139],a[40:43],v[220:223],v[136:139]\nds_read_b64_tr_b16 v[202:203],v226 offset:5120\nglobal_load_ushort v84,v237,s[8:9] offset:128\nM v[140:143],a[36:39],v[216:219],v[140:143]\nds_read_b64_tr_b16 v[204:205],v226 offset:9216\n" \
"ds_read_b64_tr_b16 v[206:207],v226 offset:13312\nM v[140:143],a[44:47],v[220:223],v[140:143]\nglobal_load_ushort v100,v235,s[20:21] offset:128\nM v[144:147],a[32:35],v[192:195],v[144:147]\nds_read_b64_tr_b16 v[216:217],v227 offset:1024\ns_waitcnt lgkmcnt(7)\nM v[144:147],a[40:43],v[208:211],v[144:147]\nds_read_b64_tr_b16 v[218:219],v227 offset:5120\n" \
"global_load_ushort v69,v236,s[8:9] offset:160\nM v[148:151],a[36:39],v[192:195],v[148:151]\nds_read_b64_tr_b16 v[220:221],v227 offset:9216\nds_read_b64_tr_b16 v[222:223],v227 offset:13312\nM v[148:151],a[44:47],v[208:211],v[148:151]\nglobal_load_ushort v85,v237,s[8:9] offset:160\nM v[152:155],a[32:35],v[196:199],v[152:155]\nds_read_b64_tr_b16 v[192:193],v226 offset:1536\n" \
"s_waitcnt lgkmcnt(7)\nM v[152:155],a[40:43],v[212:215],v[152:155]\nds_read_b64_tr_b16 v[194:195],v226 offset:5632\nglobal_load_ushort v101,v235,s[20:21] offset:160\nM v[156:159],a[36:39],v[196:199],v[156:159]\nds_read_b64_tr_b16 v[208:209],v226 offset:9728\nds_read_b64_tr_b16 v[210:211],v226 offset:13824\nM v[156:159],a[44:47],v[212:215],v[156:159]\n" \
"global_load_ushort v70,v236,s[8:9] offset:192\nM v[160:163],a[32:35],v[200:203],v[160:163]\nds_read_b64_tr_b16 v[196:197],v227 offset:1536\ns_waitcnt lgkmcnt(7)\nM v[160:163],a[40:43],v[204:207],v[160:163]\nds_read_b64_tr_b16 v[198:199],v227 offset:5632\nglobal_load_ushort v86,v237,s[8:9] offset:192\nM v[164:167],a[36:39],v[200:203],v[164:167]\n" \
"ds_read_b64_tr_b16 v[212:213],v227 offset:9728\nds_read_b64_tr_b16 v[214:215],v227 offset:13824\nM v[164:167],a[44:47],v[204:207],v[164:167]\ns_waitcnt vmcnt(20)\nv_cmp_eq_u32 vcc,0,v248\nglobal_load_ushort v102,v235,s[20:21] offset:192\nM v[168:171],a[32:35],v[216:219],v[168:171]\ns_cbranch_vccnz .Lps7\n" \
".Lpb7:\nbuffer_load_dword v248,v251,s[52:55],s79 offen sc1\ns_waitcnt lgkmcnt(4)\nM v[168:171],a[40:43],v[220:223],v[168:171]\nds_read_b64_tr_b16 v[200:201],v226 offset:32768\nds_read_b64_tr_b16 v[202:203],v226 offset:36864\nglobal_load_ushort v71,v236,s[8:9] offset:224\nM v[172:175],a[36:39],v[216:219],v[172:175]\n" \
"ds_read_b64_tr_b16 v[204:205],v226 offset:40960\nds_read_b64_tr_b16 v[206:207],v226 offset:45056\nM v[172:175],a[44:47],v[220:223],v[172:175]\nglobal_load_ushort v87,v237,s[8:9] offset:224\nM v[176:179],a[32:35],v[192:195],v[176:179]\nds_read_b64_tr_b16 v[216:217],v227 offset:32768\nds_read_b64_tr_b16 v[218:219],v227 offset:36864\nM v[176:179],a[40:43],v[208:211],v[176:179]\n" \
"global_load_ushort v103,v235,s[20:21] offset:224\nM v[180:183],a[36:39],v[192:195],v[180:183]\nds_read_b64_tr_b16 v[220:221],v227 offset:40960\nds_read_b64_tr_b16 v[222:223],v227 offset:45056\nM v[180:183],a[44:47],v[208:211],v[180:183]\nds_read_b64_tr_b16 v[192:193],v226 offset:33280\ns_waitcnt lgkmcnt(9)\nM v[184:187],a[32:35],v[196:199],v[184:187]\n" \
"ds_read_b64_tr_b16 v[194:195],v226 offset:37376\nds_read_b64_tr_b16 v[208:209],v226 offset:41472\nM v[184:187],a[40:43],v[212:215],v[184:187]\nds_read_b64_tr_b16 v[210:211],v226 offset:45568\nds_read_b64 v[112:113],v239 offset:0\nM v[188:191],a[36:39],v[196:199],v[188:191]\nds_read_b64_tr_b16 v[196:197],v227 offset:33280\nds_read_b64_tr_b16 v[198:199],v227 offset:37376\n" \
"M v[188:191],a[44:47],v[212:215],v[188:191]\nds_read_b64_tr_b16 v[212:213],v227 offset:41472\ns_waitcnt lgkmcnt(11)\nM v[128:131],a[48:51],v[200:203],v[128:131]\nds_read_b64_tr_b16 v[214:215],v227 offset:45568\nds_read_b64 v[114:115],v239 offset:4096\nM v[128:131],a[56:59],v[204:207],v[128:131]\nds_read_b64 v[116:117],v239 offset:8192\n" \
"ds_read_b64 v[118:119],v239 offset:12288\nM v[132:135],a[52:55],v[200:203],v[132:135]\nds_read_b64_tr_b16 v[200:201],v226 offset:33792\nds_read_b64_tr_b16 v[202:203],v226 offset:37888\nM v[132:135],a[60:63],v[204:207],v[132:135]\nds_read_b64_tr_b16 v[204:205],v226 offset:41984\ns_waitcnt lgkmcnt(13)\nM v[136:139],a[48:51],v[216:219],v[136:139]\n" \
"ds_read_b64_tr_b16 v[206:207],v226 offset:46080\nM v[136:139],a[56:59],v[220:223],v[136:139]\nM v[140:143],a[52:55],v[216:219],v[140:143]\nds_read_b64_tr_b16 v[216:217],v227 offset:33792\nds_read_b64_tr_b16 v[218:219],v227 offset:37888\nM v[140:143],a[60:63],v[220:223],v[140:143]\nds_read_b64_tr_b16 v[220:221],v227 offset:41984\nds_read_b64_tr_b16 v[222:223],v227 offset:46080\n" \
"M v[144:147],a[48:51],v[192:195],v[144:147]\ns_waitcnt lgkmcnt(11)\nM v[144:147],a[56:59],v[208:211],v[144:147]\nv_cvt_pk_bf16_f32 v128,v128,v136\nv_cvt_pk_bf16_f32 v129,v129,v137\nM v[148:151],a[52:55],v[192:195],v[148:151]\nds_read_b64_tr_b16 v[192:193],v226 offset:34304\nds_read_b64_tr_b16 v[194:195],v226 offset:38400\n" \
"M v[148:151],a[60:63],v[208:211],v[148:151]\nds_read_b64_tr_b16 v[208:209],v226 offset:42496\nds_read_b64_tr_b16 v[210:211],v226 offset:46592\nM v[152:155],a[48:51],v[196:199],v[152:155]\nv_add_u32 v226,131072,v242\nv_cvt_pk_bf16_f32 v130,v130,v138\nM v[152:155],a[56:59],v[212:215],v[152:155]\nv_cvt_pk_bf16_f32 v131,v131,v139\n" \
"v_min_u32 v136,-2,v128\nM v[156:159],a[52:55],v[196:199],v[156:159]\nds_read_b64_tr_b16 v[196:197],v227 offset:34304\nds_read_b64_tr_b16 v[198:199],v227 offset:38400\nM v[156:159],a[60:63],v[212:215],v[156:159]\nds_read_b64_tr_b16 v[212:213],v227 offset:42496\ns_waitcnt lgkmcnt(7)\nM v[160:163],a[48:51],v[200:203],v[160:163]\n" \
"ds_read_b64_tr_b16 v[214:215],v227 offset:46592\nv_xor_b32 v227,32,v226\nM v[160:163],a[56:59],v[204:207],v[160:163]\nv_cvt_pk_bf16_f32 v144,v144,v152\nv_min_u32 v137,-2,v144\nM v[164:167],a[52:55],v[200:203],v[164:167]\nds_read_b64_tr_b16 v[200:201],v226 offset:0\nds_read_b64_tr_b16 v[202:203],v226 offset:4096\n" \
"M v[164:167],a[60:63],v[204:207],v[164:167]\nds_read_b64_tr_b16 v[204:205],v226 offset:8192\nds_read_b64_tr_b16 v[206:207],v226 offset:12288\nM v[168:171],a[48:51],v[216:219],v[168:171]\nv_cvt_pk_bf16_f32 v145,v145,v153\nv_cvt_pk_bf16_f32 v146,v146,v154\nM v[168:171],a[56:59],v[220:223],v[168:171]\nv_cvt_pk_bf16_f32 v147,v147,v155\n" \
"v_min_u32 v152,-2,v129\nM v[172:175],a[52:55],v[216:219],v[172:175]\ns_waitcnt vmcnt(3)\nv_cmp_eq_u32 vcc,0,v248\nM v[172:175],a[60:63],v[220:223],v[172:175]\ns_cbranch_vccnz .Lps8\n.Lpb8:\ns_waitcnt lgkmcnt(4)\n" \
"M v[176:179],a[48:51],v[192:195],v[176:179]\nbuffer_load_dwordx4 a[64:67],v240,s[52:55],s65 offen offset:0 sc1\nM v[176:179],a[56:59],v[208:211],v[176:179]\nds_read_b64_tr_b16 v[216:217],v227 offset:0\nds_read_b64_tr_b16 v[218:219],v227 offset:4096\nM v[180:183],a[52:55],v[192:195],v[180:183]\nds_read_b64_tr_b16 v[220:221],v227 offset:8192\nds_read_b64_tr_b16 v[222:223],v227 offset:12288\n" \
"M v[180:183],a[60:63],v[208:211],v[180:183]\nds_read_b64_tr_b16 v[192:193],v226 offset:512\nds_read_b64_tr_b16 v[194:195],v226 offset:4608\nM v[184:187],a[48:51],v[196:199],v[184:187]\nbuffer_load_dwordx4 a[68:71],v240,s[52:55],s65 offen offset:1024 sc1\nM v[184:187],a[56:59],v[212:215],v[184:187]\nds_read_b64_tr_b16 v[208:209],v226 offset:8704\nds_read_b64_tr_b16 v[210:211],v226 offset:12800\n" \
"M v[188:191],a[52:55],v[196:199],v[188:191]\nds_read_b64_tr_b16 v[196:197],v227 offset:512\nds_read_b64_tr_b16 v[198:199],v227 offset:4608\nM v[188:191],a[60:63],v[212:215],v[188:191]\nds_read_b64_tr_b16 v[212:213],v227 offset:8704\ns_waitcnt lgkmcnt(11)\nM v[8:11],v[0:3],v[200:203],0\nbuffer_load_dwordx4 a[72:75],v240,s[52:55],s65 offen offset:2048 sc1\n" \
"M v[8:11],v[16:19],v[204:207],v[8:11]\nds_read_b64_tr_b16 v[214:215],v227 offset:12800\nv_cvt_pk_bf16_f32 v160,v160,v168\nM v[12:15],v[4:7],v[200:203],0\nds_read_b64_tr_b16 v[200:201],v226 offset:1024\nds_read_b64_tr_b16 v[202:203],v226 offset:5120\nM v[12:15],v[20:23],v[204:207],v[12:15]\nds_read_b64_tr_b16 v[204:205],v226 offset:9216\n" \
"s_waitcnt lgkmcnt(9)\nM v[24:27],v[0:3],v[216:219],0\nbuffer_load_dwordx4 a[76:79],v240,s[52:55],s65 offen offset:3072 sc1\nM v[24:27],v[16:19],v[220:223],v[24:27]\nds_read_b64_tr_b16 v[206:207],v226 offset:13312\ns_add_u32 s65,s65,4096\nM v[28:31],v[4:7],v[216:219],0\nds_read_b64_tr_b16 v[216:217],v227 offset:1024\n" \
"ds_read_b64_tr_b16 v[218:219],v227 offset:5120\nM v[28:31],v[20:23],v[220:223],v[28:31]\nds_read_b64_tr_b16 v[220:221],v227 offset:9216\nds_read_b64_tr_b16 v[222:223],v227 offset:13312\nM v[40:43],v[0:3],v[192:195],0\nbuffer_load_dwordx4 a[80:83],v240,s[52:55],s65 offen offset:0 sc1\ns_waitcnt lgkmcnt(8)\nM v[40:43],v[16:19],v[208:211],v[40:43]\n" \
"v_cvt_pk_bf16_f32 v176,v176,v184\nv_min_u32 v138,-2,v160\nM v[44:47],v[4:7],v[192:195],0\nds_read_b64_tr_b16 v[192:193],v226 offset:1536\nds_read_b64_tr_b16 v[194:195],v226 offset:5632\nM v[44:47],v[20:23],v[208:211],v[44:47]\nv_min_u32 v139,-2,v176\nds_read_b64_tr_b16 v[208:209],v226 offset:9728\n" \
"M v[56:59],v[0:3],v[196:199],0\nbuffer_load_dwordx4 a[84:87],v240,s[52:55],s65 offen offset:1024 sc1\nM v[56:59],v[16:19],v[212:215],v[56:59]\nds_read_b64_tr_b16 v[210:211],v226 offset:13824\nv_cvt_pk_bf16_f32 v161,v161,v169\nM v[60:63],v[4:7],v[196:199],0\nbuffer_store_dwordx4 v[136:139],v253,s[52:55],s33 offen offset:0 sc1\nM v[60:63],v[20:23],v[212:215],v[60:63]\n" \
"ds_read_b64_tr_b16 v[196:197],v227 offset:1536\ns_waitcnt lgkmcnt(5)\nM v[72:75],v[0:3],v[200:203],0\nbuffer_load_dwordx4 a[88:91],v240,s[52:55],s65 offen offset:2048 sc1\nM v[72:75],v[16:19],v[204:207],v[72:75]\nds_read_b64_tr_b16 v[198:199],v227 offset:5632\nds_read_b64_tr_b16 v[212:213],v227 offset:9728\nM v[76:79],v[4:7],v[200:203],0\n" \
"ds_read_b64_tr_b16 v[214:215],v227 offset:13824\nds_read_b64_tr_b16 v[200:201],v226 offset:16384\nM v[76:79],v[20:23],v[204:207],v[76:79]\nds_read_b64_tr_b16 v[202:203],v226 offset:20480\nds_read_b64_tr_b16 v[204:205],v226 offset:24576\nM v[88:91],v[0:3],v[216:219],0\nbuffer_load_dwordx4 a[92:95],v240,s[52:55],s65 offen offset:3072 sc1\nM v[88:91],v[16:19],v[220:223],v[88:91]\n" \
"ds_read_b64_tr_b16 v[206:207],v226 offset:28672\ns_add_u32 s65,s11,s67\nM v[92:95],v[4:7],v[216:219],0\nds_read_b64_tr_b16 v[216:217],v227 offset:16384\nds_read_b64_tr_b16 v[218:219],v227 offset:20480\nM v[92:95],v[20:23],v[220:223],v[92:95]\nds_read_b64_tr_b16 v[220:221],v227 offset:24576\ns_waitcnt lgkmcnt(10)\n" \
"M v[104:107],v[0:3],v[192:195],0\nbuffer_load_dwordx4 a[96:99],v240,s[52:55],s65 offen offset:0 sc1\nM v[104:107],v[16:19],v[208:211],v[104:107]\nds_read_b64_tr_b16 v[222:223],v227 offset:28672\nv_cvt_pk_bf16_f32 v177,v177,v185\nM v[108:111],v[4:7],v[192:195],0\nds_read_b64_tr_b16 v[192:193],v226 offset:16896\nds_read_b64_tr_b16 v[194:195],v226 offset:20992\n" \
"M v[108:111],v[20:23],v[208:211],v[108:111]\nv_min_u32 v153,-2,v145\ns_waitcnt lgkmcnt(7)\nM v[120:123],v[0:3],v[196:199],0\nbuffer_load_dwordx4 a[100:103],v240,s[52:55],s65 offen offset:1024 sc1\nM v[120:123],v[16:19],v[212:215],v[120:123]\nv_min_u32 v154,-2,v161\nv_min_u32 v155,-2,v177\n" \
"M v[124:127],v[4:7],v[196:199],0\nds_read_b64_tr_b16 v[208:209],v226 offset:25088\nds_read_b64_tr_b16 v[210:211],v226 offset:29184\nbuffer_store_dwordx4 v[152:155],v253,s[52:55],s33 offen offset:1024 sc1\nM v[124:127],v[20:23],v[212:215],v[124:127]\nds_read_b64_tr_b16 v[196:197],v227 offset:16896\nds_read_b64_tr_b16 v[198:199],v227 offset:20992\nM v[8:11],v[32:35],v[200:203],v[8:11]\n" \
"buffer_load_dwordx4 a[104:107],v240,s[52:55],s65 offen offset:2048 sc1\ns_waitcnt lgkmcnt(6)\nM v[8:11],v[48:51],v[204:207],v[8:11]\nds_read_b64_tr_b16 v[212:213],v227 offset:25088\nds_read_b64_tr_b16 v[214:215],v227 offset:29184\nM v[12:15],v[36:39],v[200:203],v[12:15]\nds_read_b64_tr_b16 v[200:201],v226 offset:17408\nds_read_b64_tr_b16 v[202:203],v226 offset:21504\n" \
"M v[12:15],v[52:55],v[204:207],v[12:15]\nds_read_b64_tr_b16 v[204:205],v226 offset:25600\nds_read_b64_tr_b16 v[206:207],v226 offset:29696\nM v[24:27],v[32:35],v[216:219],v[24:27]\nbuffer_load_dwordx4 a[108:111],v240,s[52:55],s65 offen offset:3072 sc1\nM v[24:27],v[48:51],v[220:223],v[24:27]\ns_add_u32 s65,s65,4096\nv_cvt_pk_bf16_f32 v162,v162,v170\n" \
"M v[28:31],v[36:39],v[216:219],v[28:31]\nds_read_b64_tr_b16 v[216:217],v227 offset:17408\nds_read_b64_tr_b16 v[218:219],v227 offset:21504\nM v[28:31],v[52:55],v[220:223],v[28:31]\nds_read_b64_tr_b16 v[220:221],v227 offset:25600\ns_waitcnt lgkmcnt(9)\nM v[40:43],v[32:35],v[192:195],v[40:43]\nbuffer_load_dwordx4 a[112:115],v240,s[52:55],s65 offen offset:0 sc1\n" \
"M v[40:43],v[48:51],v[208:211],v[40:43]\nds_read_b64_tr_b16 v[222:223],v227 offset:29696\nv_cvt_pk_bf16_f32 v163,v163,v171\nM v[44:47],v[36:39],v[192:195],v[44:47]\nds_read_b64_tr_b16 v[192:193],v226 offset:17920\nds_read_b64_tr_b16 v[194:195],v226 offset:22016\nM v[44:47],v[52:55],v[208:211],v[44:47]\nv_cvt_pk_bf16_f32 v178,v178,v186\n" \
"v_min_u32 v168,-2,v130\nM v[56:59],v[32:35],v[196:199],v[56:59]\nbuffer_load_dwordx4 a[116:119],v240,s[52:55],s65 offen offset:1024 sc1\ns_waitcnt lgkmcnt(6)\nM v[56:59],v[48:51],v[212:215],v[56:59]\nv_min_u32 v169,-2,v146\nv_min_u32 v170,-2,v162\nM v[60:63],v[36:39],v[196:199],v[60:63]\n" \
"v_min_u32 v171,-2,v178\nds_read_b64_tr_b16 v[208:209],v226 offset:26112\nds_read_b64_tr_b16 v[210:211],v226 offset:30208\nbuffer_store_dwordx4 v[168:171],v253,s[52:55],s33 offen offset:2048 sc1\nM v[60:63],v[52:55],v[212:215],v[60:63]\nds_read_b64_tr_b16 v[196:197],v227 offset:17920\nds_read_b64_tr_b16 v[198:199],v227 offset:22016\nM v[72:75],v[32:35],v[200:203],v[72:75]\n" \
"buffer_load_dwordx4 a[120:123],v240,s[52:55],s65 offen offset:2048 sc1\nM v[72:75],v[48:51],v[204:207],v[72:75]\nds_read_b64_tr_b16 v[212:213],v227 offset:26112\nds_read_b64_tr_b16 v[214:215],v227 offset:30208\nM v[76:79],v[36:39],v[200:203],v[76:79]\nv_add_u32 v226,0,v242\nds_read_b64_tr_b16 v[200:201],v226 offset:0\nM v[76:79],v[52:55],v[204:207],v[76:79]\n" \
"ds_read_b64_tr_b16 v[202:203],v226 offset:4096\ns_waitcnt lgkmcnt(8)\nM v[88:91],v[32:35],v[216:219],v[88:91]\nbuffer_load_dwordx4 a[124:127],v240,s[52:55],s65 offen offset:3072 sc1\nM v[88:91],v[48:51],v[220:223],v[88:91]\nds_read_b64_tr_b16 v[204:205],v226 offset:8192\nds_read_b64_tr_b16 v[206:207],v226 offset:12288\nM v[92:95],v[36:39],v[216:219],v[92:95]\n" \
"s_add_u32 s65,s14,s67\nv_xor_b32 v227,32,v226\nM v[92:95],v[52:55],v[220:223],v[92:95]\nds_read_b64_tr_b16 v[216:217],v227 offset:0\nds_read_b64_tr_b16 v[218:219],v227 offset:4096\nM v[104:107],v[32:35],v[192:195],v[104:107]\nbuffer_load_dwordx4 a[128:131],v240,s[52:55],s65 offen offset:0 sc1\ns_waitcnt lgkmcnt(6)\n" \
"M v[104:107],v[48:51],v[208:211],v[104:107]\nds_read_b64_tr_b16 v[220:221],v227 offset:8192\nds_read_b64_tr_b16 v[222:223],v227 offset:12288\nM v[108:111],v[36:39],v[192:195],v[108:111]\nv_cvt_pk_bf16_f32 v179,v179,v187\nds_read_b64_tr_b16 v[192:193],v226 offset:512\nM v[108:111],v[52:55],v[208:211],v[108:111]\nds_read_b64_tr_b16 v[194:195],v226 offset:4608\n" \
"v_min_u32 v184,-2,v131\nM v[120:123],v[32:35],v[196:199],v[120:123]\nbuffer_load_dwordx4 a[132:135],v240,s[52:55],s65 offen offset:1024 sc1\nM v[120:123],v[48:51],v[212:215],v[120:123]\nv_min_u32 v185,-2,v147\nv_min_u32 v186,-2,v163\nM v[124:127],v[36:39],v[196:199],v[124:127]\nv_min_u32 v187,-2,v179\n" \
"buffer_store_dwordx4 v[184:187],v253,s[52:55],s33 offen offset:3072 sc1\nds_read_b64_tr_b16 v[208:209],v226 offset:8704\nds_read_b64_tr_b16 v[210:211],v226 offset:12800\nM v[124:127],v[52:55],v[212:215],v[124:127]\nds_read_b64_tr_b16 v[196:197],v227 offset:512\ns_waitcnt vmcnt(18) lgkmcnt(7)\nM v[8:11],a[64:67],v[200:203],v[8:11]\nbuffer_load_dwordx4 a[136:139],v240,s[52:55],s65 offen offset:2048 sc1\n" \
"M v[8:11],a[72:75],v[204:207],v[8:11]\nds_read_b64_tr_b16 v[198:199],v227 offset:4608\nds_read_b64_tr_b16 v[212:213],v227 offset:8704\nM v[12:15],a[68:71],v[200:203],v[12:15]\nds_read_b64_tr_b16 v[214:215],v227 offset:12800\nds_read_b64_tr_b16 v[200:201],v226 offset:1024\nM v[12:15],a[76:79],v[204:207],v[12:15]\nds_read_b64_tr_b16 v[202:203],v226 offset:5120\n" \
"ds_read_b64_tr_b16 v[204:205],v226 offset:9216\nM v[24:27],a[64:67],v[216:219],v[24:27]\nbuffer_load_dwordx4 a[140:143],v240,s[52:55],s65 offen offset:3072 sc1\ns_waitcnt lgkmcnt(9)\nM v[24:27],a[72:75],v[220:223],v[24:27]\nds_read_b64_tr_b16 v[206:207],v226 offset:13312\ns_add_u32 s65,s65,4096\nM v[28:31],a[68:71],v[216:219],v[28:31]\n" \
"ds_read_b64_tr_b16 v[216:217],v227 offset:1024\nds_read_b64_tr_b16 v[218:219],v227 offset:5120\nM v[28:31],a[76:79],v[220:223],v[28:31]\nds_read_b64_tr_b16 v[220:221],v227 offset:9216\nds_read_b64_tr_b16 v[222:223],v227 offset:13312\nM v[40:43],a[64:67],v[192:195],v[40:43]\nbuffer_load_dwordx4 a[144:147],v240,s[52:55],s65 offen offset:0 sc1\ns_waitcnt lgkmcnt(9)\n" \
"M v[40:43],a[72:75],v[208:211],v[40:43]\nv_cvt_pk_bf16_f32 v132,v132,v140\nv_cvt_pk_bf16_f32 v133,v133,v141\nM v[44:47],a[68:71],v[192:195],v[44:47]\nv_cvt_pk_bf16_f32 v134,v134,v142\nv_cvt_pk_bf16_f32 v135,v135,v143\nM v[44:47],a[76:79],v[208:211],v[44:47]\nv_cvt_pk_bf16_f32 v148,v148,v156\n" \
"v_cvt_pk_bf16_f32 v164,v164,v172\nM v[56:59],a[64:67],v[196:199],v[56:59]\nbuffer_load_dwordx4 a[148:151],v240,s[52:55],s65 offen offset:1024 sc1\ns_waitcnt lgkmcnt(4)\nM v[56:59],a[72:75],v[212:215],v[56:59]\nv_cvt_pk_bf16_f32 v180,v180,v188\nds_read_b64_tr_b16 v[192:193],v226 offset:1536\nds_read_b64_tr_b16 v[194:195],v226 offset:5632\n" \
"M v[60:63],a[68:71],v[196:199],v[60:63]\nv_min_u32 v140,-2,v132\nv_min_u32 v141,-2,v148\nv_min_u32 v142,-2,v164\nv_min_u32 v143,-2,v180\nbuffer_store_dwordx4 v[140:143],v253,s[52:55],s38 offen offset:0 sc1\nds_read_b64_tr_b16 v[208:209],v226 offset:9728\nds_read_b64_tr_b16 v[210:211],v226 offset:13824\n" \
"M v[60:63],a[76:79],v[212:215],v[60:63]\nds_read_b64_tr_b16 v[196:197],v227 offset:1536\nds_read_b64_tr_b16 v[198:199],v227 offset:5632\nM v[72:75],a[64:67],v[200:203],v[72:75]\nbuffer_load_dwordx4 a[152:155],v240,s[52:55],s65 offen offset:2048 sc1\nM v[72:75],a[72:75],v[204:207],v[72:75]\nds_read_b64_tr_b16 v[212:213],v227 offset:9728\nds_read_b64_tr_b16 v[214:215],v227 offset:13824\n" \
"M v[76:79],a[68:71],v[200:203],v[76:79]\nds_read_b64_tr_b16 v[200:201],v226 offset:32768\nds_read_b64_tr_b16 v[202:203],v226 offset:36864\nM v[76:79],a[76:79],v[204:207],v[76:79]\nds_read_b64_tr_b16 v[204:205],v226 offset:40960\ns_waitcnt lgkmcnt(9)\nM v[88:91],a[64:67],v[216:219],v[88:91]\nbuffer_load_dwordx4 a[156:159],v240,s[52:55],s65 offen offset:3072 sc1\n" \
"M v[88:91],a[72:75],v[220:223],v[88:91]\nds_read_b64_tr_b16 v[206:207],v226 offset:45056\nv_cvt_pk_bf16_f32 v149,v149,v157\nM v[92:95],a[68:71],v[216:219],v[92:95]\nds_read_b64_tr_b16 v[216:217],v227 offset:32768\nds_read_b64_tr_b16 v[218:219],v227 offset:36864\nM v[92:95],a[76:79],v[220:223],v[92:95]\nds_read_b64_tr_b16 v[220:221],v227 offset:40960\n" \
"ds_read_b64_tr_b16 v[222:223],v227 offset:45056\nM v[104:107],a[64:67],v[192:195],v[104:107]\nv_cvt_pk_bf16_f32 v150,v150,v158\ns_waitcnt lgkmcnt(9)\nM v[104:107],a[72:75],v[208:211],v[104:107]\nv_cvt_pk_bf16_f32 v151,v151,v159\nv_cvt_pk_bf16_f32 v165,v165,v173\nM v[108:111],a[68:71],v[192:195],v[108:111]\n" \
"v_cvt_pk_bf16_f32 v181,v181,v189\nds_read_b64_tr_b16 v[192:193],v226 offset:33280\nM v[108:111],a[76:79],v[208:211],v[108:111]\nds_read_b64_tr_b16 v[194:195],v226 offset:37376\nv_min_u32 v156,-2,v133\nM v[120:123],a[64:67],v[196:199],v[120:123]\nv_min_u32 v157,-2,v149\ns_waitcnt lgkmcnt(6)\n" \
"M v[120:123],a[72:75],v[212:215],v[120:123]\nv_min_u32 v158,-2,v165\nv_min_u32 v159,-2,v181\nM v[124:127],a[68:71],v[196:199],v[124:127]\nbuffer_store_dwordx4 v[156:159],v253,s[52:55],s38 offen offset:1024 sc1\nds_read_b64_tr_b16 v[208:209],v226 offset:41472\nds_read_b64_tr_b16 v[210:211],v226 offset:45568\nM v[124:127],a[76:79],v[212:215],v[124:127]\n" \
"ds_read_b64_tr_b16 v[196:197],v227 offset:33280\ns_waitcnt vmcnt(24)\nM v[8:11],a[80:83],v[200:203],v[8:11]\nds_read_b64_tr_b16 v[198:199],v227 offset:37376\ns_waitcnt vmcnt(19)\nM v[8:11],a[88:91],v[204:207],v[8:11]\nds_read_b64_tr_b16 v[212:213],v227 offset:41472\nds_read_b64_tr_b16 v[214:215],v227 offset:45568\n" \
"M v[12:15],a[84:87],v[200:203],v[12:15]\nds_read_b64_tr_b16 v[200:201],v226 offset:33792\nds_read_b64_tr_b16 v[202:203],v226 offset:37888\nM v[12:15],a[92:95],v[204:207],v[12:15]\nds_read_b64_tr_b16 v[204:205],v226 offset:41984\ns_waitcnt lgkmcnt(9)\nM v[24:27],a[80:83],v[216:219],v[24:27]\nds_read_b64_tr_b16 v[206:207],v226 offset:46080\n" \
"v_cvt_pk_bf16_f32 v166,v166,v174\nM v[24:27],a[88:91],v[220:223],v[24:27]\nv_cvt_pk_bf16_f32 v167,v167,v175\nv_cvt_pk_bf16_f32 v182,v182,v190\nM v[28:31],a[84:87],v[216:219],v[28:31]\nds_read_b64_tr_b16 v[216:217],v227 offset:33792\nds_read_b64_tr_b16 v[218:219],v227 offset:37888\nM v[28:31],a[92:95],v[220:223],v[28:31]\n" \
"ds_read_b64_tr_b16 v[220:221],v227 offset:41984\nds_read_b64_tr_b16 v[222:223],v227 offset:46080\nM v[40:43],a[80:83],v[192:195],v[40:43]\nv_min_u32 v172,-2,v134\ns_waitcnt lgkmcnt(9)\nM v[40:43],a[88:91],v[208:211],v[40:43]\nv_min_u32 v173,-2,v150\nv_min_u32 v174,-2,v166\n" \
"M v[44:47],a[84:87],v[192:195],v[44:47]\nds_read_b64_tr_b16 v[192:193],v226 offset:34304\nds_read_b64_tr_b16 v[194:195],v226 offset:38400\nM v[44:47],a[92:95],v[208:211],v[44:47]\nv_min_u32 v175,-2,v182\nds_read_b64_tr_b16 v[208:209],v226 offset:42496\nM v[56:59],a[80:83],v[196:199],v[56:59]\nds_read_b64_tr_b16 v[210:211],v226 offset:46592\n" \
"s_waitcnt lgkmcnt(8)\nM v[56:59],a[88:91],v[212:215],v[56:59]\nv_cvt_pk_bf16_f32 v183,v183,v191\nv_min_u32 v188,-2,v135\nM v[60:63],a[84:87],v[196:199],v[60:63]\nbuffer_store_dwordx4 v[172:175],v253,s[52:55],s38 offen offset:2048 sc1\nM v[60:63],a[92:95],v[212:215],v[60:63]\nds_read_b64_tr_b16 v[196:197],v227 offset:34304\n" \
"ds_read_b64_tr_b16 v[198:199],v227 offset:38400\nM v[72:75],a[80:83],v[200:203],v[72:75]\nds_read_b64_tr_b16 v[212:213],v227 offset:42496\nds_read_b64_tr_b16 v[214:215],v227 offset:46592\nM v[72:75],a[88:91],v[204:207],v[72:75]\nv_min_u32 v189,-2,v151\nv_min_u32 v190,-2,v167\nM v[76:79],a[84:87],v[200:203],v[76:79]\n" \
"ds_read_b64_tr_b16 v[200:201],v226 offset:16384\nds_read_b64_tr_b16 v[202:203],v226 offset:20480\nM v[76:79],a[92:95],v[204:207],v[76:79]\nds_read_b64_tr_b16 v[204:205],v226 offset:24576\ns_waitcnt lgkmcnt(7)\nM v[88:91],a[80:83],v[216:219],v[88:91]\nds_read_b64_tr_b16 v[206:207],v226 offset:28672\nv_min_u32 v191,-2,v183\n" \
"M v[88:91],a[88:91],v[220:223],v[88:91]\nds_read_b128 v[0:3],v238 offset:0\nds_read_b128 v[4:7],v238 offset:16\nM v[92:95],a[84:87],v[216:219],v[92:95]\nds_read_b64_tr_b16 v[216:217],v227 offset:16384\nv_cmp_eq_u32 vcc,v228,v229\nM v[92:95],a[92:95],v[220:223],v[92:95]\nds_read_b64_tr_b16 v[218:219],v227 offset:20480\n" \
"ds_read_b64_tr_b16 v[220:221],v227 offset:24576\nM v[104:107],a[80:83],v[192:195],v[104:107]\nds_read_b64_tr_b16 v[222:223],v227 offset:28672\nds_read_b128 v[16:19],v238 offset:4096\nM v[104:107],a[88:91],v[208:211],v[104:107]\nds_read_b128 v[20:23],v238 offset:4112\nv_and_b32 v228,1,v249\nM v[108:111],a[84:87],v[192:195],v[108:111]\n" \
"ds_read_b64_tr_b16 v[192:193],v226 offset:16896\nv_lshlrev_b32 v228,4,v228\nM v[108:111],a[92:95],v[208:211],v[108:111]\nds_read_b64_tr_b16 v[194:195],v226 offset:20992\ns_waitcnt lgkmcnt(10)\nM v[120:123],a[80:83],v[196:199],v[120:123]\nds_read_b64_tr_b16 v[208:209],v226 offset:25088\nds_read_b64_tr_b16 v[210:211],v226 offset:29184\n" \
"M v[120:123],a[88:91],v[212:215],v[120:123]\nds_read_b128 v[32:35],v238 offset:8192\nds_read_b128 v[36:39],v238 offset:8208\nM v[124:127],a[84:87],v[196:199],v[124:127]\nbuffer_store_dwordx4 v[188:191],v253,s[52:55],s38 offen offset:3072 sc1\nM v[124:127],a[92:95],v[212:215],v[124:127]\nds_read_b64_tr_b16 v[196:197],v227 offset:16896\nds_read_b64_tr_b16 v[198:199],v227 offset:20992\n" \
"M v[8:11],a[96:99],v[200:203],v[8:11]\nds_read_b64_tr_b16 v[212:213],v227 offset:25088\ns_waitcnt vmcnt(16)\nM v[8:11],a[104:107],v[204:207],v[8:11]\nds_read_b64_tr_b16 v[214:215],v227 offset:29184\nds_read_b128 v[48:51],v238 offset:12288\nM v[12:15],a[100:103],v[200:203],v[12:15]\nds_read_b64_tr_b16 v[200:201],v226 offset:17408\n" \
"ds_read_b64_tr_b16 v[202:203],v226 offset:21504\nM v[12:15],a[108:111],v[204:207],v[12:15]\nds_read_b64_tr_b16 v[204:205],v226 offset:25600\ns_waitcnt lgkmcnt(11)\nM v[24:27],a[96:99],v[216:219],v[24:27]\nds_read_b64_tr_b16 v[206:207],v226 offset:29696\nds_read_b128 v[52:55],v238 offset:12304\nM v[24:27],a[104:107],v[220:223],v[24:27]\n" \
"v_mov_b32 v229,0x3f80\nv_lshlrev_b32 v228,v228,v229\nM v[28:31],a[100:103],v[216:219],v[28:31]\nds_read_b64_tr_b16 v[216:217],v227 offset:17408\nds_read_b64_tr_b16 v[218:219],v227 offset:21504\nM v[28:31],a[108:111],v[220:223],v[28:31]\nds_read_b64_tr_b16 v[220:221],v227 offset:25600\nds_read_b64_tr_b16 v[222:223],v227 offset:29696\n" \
"M v[40:43],a[96:99],v[192:195],v[40:43]\nv_cndmask_b32 v228,0,v228,vcc\ns_waitcnt lgkmcnt(10)\nM v[40:43],a[104:107],v[208:211],v[40:43]\nv_and_b32 v229,2,v249\nv_cmp_eq_u32 vcc,0,v229\nM v[44:47],a[100:103],v[192:195],v[44:47]\nds_read_b64_tr_b16 v[192:193],v226 offset:17920\n" \
"ds_read_b64_tr_b16 v[194:195],v226 offset:22016\nM v[44:47],a[108:111],v[208:211],v[44:47]\nds_read_b64_tr_b16 v[208:209],v226 offset:26112\nds_read_b64_tr_b16 v[210:211],v226 offset:30208\nM v[56:59],a[96:99],v[196:199],v[56:59]\nv_cndmask_b32 v229,0,v228,vcc\nv_and_b32 v244,15,v249\nM v[56:59],a[104:107],v[212:215],v[56:59]\n" \
"v_and_b32 v245,0x300,v240\nv_lshlrev_b32 v246,11,v249\nM v[60:63],a[100:103],v[196:199],v[60:63]\nds_read_b64_tr_b16 v[196:197],v227 offset:17920\nds_read_b64_tr_b16 v[198:199],v227 offset:22016\nM v[60:63],a[108:111],v[212:215],v[60:63]\nds_read_b64_tr_b16 v[212:213],v227 offset:26112\ns_waitcnt lgkmcnt(7)\n" \
"M v[72:75],a[96:99],v[200:203],v[72:75]\nds_read_b64_tr_b16 v[214:215],v227 offset:30208\nv_cmp_lt_u32 vcc,7,v249\nM v[72:75],a[104:107],v[204:207],v[72:75]\nv_mov_b32 v230,0x40000000\nv_lshl_or_b32 v244,v244,1,v245\nM v[76:79],a[100:103],v[200:203],v[76:79]\nds_read_b64_tr_b16 v[200:201],v226 offset:49152\n" \
"ds_read_b64_tr_b16 v[202:203],v226 offset:53248\nM v[76:79],a[108:111],v[204:207],v[76:79]\nds_read_b64_tr_b16 v[204:205],v226 offset:57344\nds_read_b64_tr_b16 v[206:207],v226 offset:61440\nM v[88:91],a[96:99],v[216:219],v[88:91]\ns_add_u32 s70,s64,114688\nv_cndmask_b32 v246,v246,v230,vcc\nM v[88:91],a[104:107],v[220:223],v[88:91]\n" \
"v_add_u32 v244,s70,v244\nv_add_u32 v245,s70,v240\nM v[92:95],a[100:103],v[216:219],v[92:95]\nds_read_b64_tr_b16 v[216:217],v227 offset:49152\nds_read_b64_tr_b16 v[218:219],v227 offset:53248\nM v[92:95],a[108:111],v[220:223],v[92:95]\nds_read_b64_tr_b16 v[220:221],v227 offset:57344\ns_waitcnt lgkmcnt(7)\n" \
"M v[104:107],a[96:99],v[192:195],v[104:107]\nds_read_b64_tr_b16 v[222:223],v227 offset:61440\ns_lshl_b32 s80,s60,11\nM v[104:107],a[104:107],v[208:211],v[104:107]\nv_and_b32 v230,3,v249\nv_lshrrev_b32 v231,2,v249\nM v[108:111],a[100:103],v[192:195],v[108:111]\nds_read_b64_tr_b16 v[192:193],v226 offset:49664\n" \
"ds_read_b64_tr_b16 v[194:195],v226 offset:53760\nM v[108:111],a[108:111],v[208:211],v[108:111]\nds_read_b64_tr_b16 v[208:209],v226 offset:57856\nds_read_b64_tr_b16 v[210:211],v226 offset:61952\nM v[120:123],a[96:99],v[196:199],v[120:123]\ns_lshl3_add_u32 s37,s6,s75\ns_add_u32 s80,s80,557056\nM v[120:123],a[104:107],v[212:215],v[120:123]\n" \
"v_lshl_add_u32 v231,v230,5,v231\ns_lshl_b32 s70,s7,8\nM v[124:127],a[100:103],v[196:199],v[124:127]\nds_read_b64_tr_b16 v[196:197],v227 offset:49664\nds_read_b64_tr_b16 v[198:199],v227 offset:53760\nM v[124:127],a[108:111],v[212:215],v[124:127]\nds_read_b64_tr_b16 v[212:213],v227 offset:57856\ns_waitcnt lgkmcnt(7)\n" \
"M v[8:11],a[112:115],v[200:203],v[8:11]\nds_read_b64_tr_b16 v[214:215],v227 offset:61952\ns_waitcnt vmcnt(11)\nM v[8:11],a[120:123],v[204:207],v[8:11]\nv_add_f32 v32,1.0,v32\nv_add_f32 v33,1.0,v33\nM v[12:15],a[116:119],v[200:203],v[12:15]\nds_read_b64_tr_b16 v[200:201],v226 offset:50176\n" \
"ds_read_b64_tr_b16 v[202:203],v226 offset:54272\nM v[12:15],a[124:127],v[204:207],v[12:15]\nds_read_b64_tr_b16 v[204:205],v226 offset:58368\nds_read_b64_tr_b16 v[206:207],v226 offset:62464\nM v[24:27],a[112:115],v[216:219],v[24:27]\nv_add_f32 v34,1.0,v34\nv_add_f32 v35,1.0,v35\nM v[24:27],a[120:123],v[220:223],v[24:27]\n" \
"v_add_f32 v36,1.0,v36\nv_add_f32 v37,1.0,v37\nM v[28:31],a[116:119],v[216:219],v[28:31]\nds_read_b64_tr_b16 v[216:217],v227 offset:50176\nds_read_b64_tr_b16 v[218:219],v227 offset:54272\nM v[28:31],a[124:127],v[220:223],v[28:31]\nds_read_b64_tr_b16 v[220:221],v227 offset:58368\ns_waitcnt lgkmcnt(7)\n" \
"M v[40:43],a[112:115],v[192:195],v[40:43]\nds_read_b64_tr_b16 v[222:223],v227 offset:62464\nv_add_f32 v38,1.0,v38\nM v[40:43],a[120:123],v[208:211],v[40:43]\nv_add_f32 v39,1.0,v39\nv_add_f32 v116,1.0,v116\nM v[44:47],a[116:119],v[192:195],v[44:47]\nds_read_b64_tr_b16 v[192:193],v226 offset:50688\n" \
"ds_read_b64_tr_b16 v[194:195],v226 offset:54784\nM v[44:47],a[124:127],v[208:211],v[44:47]\nds_read_b64_tr_b16 v[208:209],v226 offset:58880\nds_read_b64_tr_b16 v[210:211],v226 offset:62976\nM v[56:59],a[112:115],v[196:199],v[56:59]\nv_add_u32 v226,65536,v242\nv_add_f32 v117,1.0,v117\nM v[56:59],a[120:123],v[212:215],v[56:59]\n" \
"s_add_u32 s37,s37,557056\nv_lshlrev_b32 v251,5,v249\nM v[60:63],a[116:119],v[196:199],v[60:63]\nds_read_b64_tr_b16 v[196:197],v227 offset:50688\nds_read_b64_tr_b16 v[198:199],v227 offset:54784\nM v[60:63],a[124:127],v[212:215],v[60:63]\nds_read_b64_tr_b16 v[212:213],v227 offset:58880\ns_waitcnt lgkmcnt(7)\n" \
"M v[72:75],a[112:115],v[200:203],v[72:75]\nds_read_b64_tr_b16 v[214:215],v227 offset:62976\nv_xor_b32 v227,32,v226\nM v[72:75],a[120:123],v[204:207],v[72:75]\ns_or_b32 s80,s80,s75\nv_lshl_add_u32 v248,v231,1,s70\nM v[76:79],a[116:119],v[200:203],v[76:79]\nds_read_b64_tr_b16 v[200:201],v226 offset:0\n" \
"ds_read_b64_tr_b16 v[202:203],v226 offset:4096\nM v[76:79],a[124:127],v[204:207],v[76:79]\nds_read_b64_tr_b16 v[204:205],v226 offset:8192\nds_read_b64_tr_b16 v[206:207],v226 offset:12288\nM v[88:91],a[112:115],v[216:219],v[88:91]\nv_lshlrev_b32 v64,16,v64\nv_lshlrev_b32 v65,16,v65\nM v[88:91],a[120:123],v[220:223],v[88:91]\n" \
"v_lshlrev_b32 v66,16,v66\nv_lshlrev_b32 v67,16,v67\nM v[92:95],a[116:119],v[216:219],v[92:95]\nds_read_b64_tr_b16 v[216:217],v227 offset:0\nds_read_b64_tr_b16 v[218:219],v227 offset:4096\nM v[92:95],a[124:127],v[220:223],v[92:95]\nds_read_b64_tr_b16 v[220:221],v227 offset:8192\ns_waitcnt lgkmcnt(7)\n" \
"M v[104:107],a[112:115],v[192:195],v[104:107]\nds_read_b64_tr_b16 v[222:223],v227 offset:12288\nv_lshlrev_b32 v68,16,v68\nM v[104:107],a[120:123],v[208:211],v[104:107]\nv_lshlrev_b32 v69,16,v69\nv_lshlrev_b32 v70,16,v70\nM v[108:111],a[116:119],v[192:195],v[108:111]\nds_read_b64_tr_b16 v[192:193],v226 offset:512\n" \
"ds_read_b64_tr_b16 v[194:195],v226 offset:4608\nM v[108:111],a[124:127],v[208:211],v[108:111]\nds_read_b64_tr_b16 v[208:209],v226 offset:8704\nds_read_b64_tr_b16 v[210:211],v226 offset:12800\nM v[120:123],a[112:115],v[196:199],v[120:123]\nv_lshlrev_b32 v71,16,v71\nv_lshlrev_b32 v80,16,v80\nM v[120:123],a[120:123],v[212:215],v[120:123]\n" \
"v_lshlrev_b32 v81,16,v81\nv_lshlrev_b32 v82,16,v82\nM v[124:127],a[116:119],v[196:199],v[124:127]\nds_read_b64_tr_b16 v[196:197],v227 offset:512\nds_read_b64_tr_b16 v[198:199],v227 offset:4608\nM v[124:127],a[124:127],v[212:215],v[124:127]\nds_read_b64_tr_b16 v[212:213],v227 offset:8704\ns_waitcnt lgkmcnt(7)\n" \
"M v[8:11],a[128:131],v[200:203],v[8:11]\nds_read_b64_tr_b16 v[214:215],v227 offset:12800\ns_waitcnt vmcnt(6)\nM v[8:11],a[136:139],v[204:207],v[8:11]\nv_lshlrev_b32 v83,16,v83\nv_lshlrev_b32 v84,16,v84\nM v[12:15],a[132:135],v[200:203],v[12:15]\nds_read_b64_tr_b16 v[200:201],v226 offset:1024\n" \
"ds_read_b64_tr_b16 v[202:203],v226 offset:5120\nM v[12:15],a[140:143],v[204:207],v[12:15]\nds_read_b64_tr_b16 v[204:205],v226 offset:9216\nds_read_b64_tr_b16 v[206:207],v226 offset:13312\nM v[24:27],a[128:131],v[216:219],v[24:27]\nv_lshlrev_b32 v85,16,v85\nv_lshlrev_b32 v86,16,v86\nM v[24:27],a[136:139],v[220:223],v[24:27]\n" \
"v_lshlrev_b32 v87,16,v87\nv_lshlrev_b32 v96,16,v96\nM v[28:31],a[132:135],v[216:219],v[28:31]\nds_read_b64_tr_b16 v[216:217],v227 offset:1024\nds_read_b64_tr_b16 v[218:219],v227 offset:5120\nM v[28:31],a[140:143],v[220:223],v[28:31]\nds_read_b64_tr_b16 v[220:221],v227 offset:9216\ns_waitcnt lgkmcnt(7)\n" \
"M v[40:43],a[128:131],v[192:195],v[40:43]\nds_read_b64_tr_b16 v[222:223],v227 offset:13312\nv_lshlrev_b32 v97,16,v97\nM v[40:43],a[136:139],v[208:211],v[40:43]\nv_lshlrev_b32 v98,16,v98\nv_lshlrev_b32 v99,16,v99\nM v[44:47],a[132:135],v[192:195],v[44:47]\nds_read_b64_tr_b16 v[192:193],v226 offset:1536\n" \
"ds_read_b64_tr_b16 v[194:195],v226 offset:5632\nM v[44:47],a[140:143],v[208:211],v[44:47]\nds_read_b64_tr_b16 v[208:209],v226 offset:9728\nds_read_b64_tr_b16 v[210:211],v226 offset:13824\nM v[56:59],a[128:131],v[196:199],v[56:59]\nv_lshlrev_b32 v100,16,v100\nv_lshlrev_b32 v101,16,v101\nM v[56:59],a[136:139],v[212:215],v[56:59]\n" \
"v_lshlrev_b32 v102,16,v102\nv_lshlrev_b32 v103,16,v103\nM v[60:63],a[132:135],v[196:199],v[60:63]\nds_read_b64_tr_b16 v[196:197],v227 offset:1536\nds_read_b64_tr_b16 v[198:199],v227 offset:5632\nM v[60:63],a[140:143],v[212:215],v[60:63]\nds_read_b64_tr_b16 v[212:213],v227 offset:9728\ns_waitcnt lgkmcnt(7)\n" \
"M v[72:75],a[128:131],v[200:203],v[72:75]\nds_read_b64_tr_b16 v[214:215],v227 offset:13824\nv_cvt_pk_bf16_f32 v32,0,v32\nM v[72:75],a[136:139],v[204:207],v[72:75]\nv_cvt_pk_bf16_f32 v33,0,v33\nv_cvt_pk_bf16_f32 v34,0,v34\nM v[76:79],a[132:135],v[200:203],v[76:79]\nds_read_b64_tr_b16 v[200:201],v226 offset:32768\n" \
"ds_read_b64_tr_b16 v[202:203],v226 offset:36864\nM v[76:79],a[140:143],v[204:207],v[76:79]\nds_read_b64_tr_b16 v[204:205],v226 offset:40960\nds_read_b64_tr_b16 v[206:207],v226 offset:45056\nM v[88:91],a[128:131],v[216:219],v[88:91]\nv_cvt_pk_bf16_f32 v35,0,v35\nv_cvt_pk_bf16_f32 v36,0,v36\nM v[88:91],a[136:139],v[220:223],v[88:91]\n" \
"v_cvt_pk_bf16_f32 v37,0,v37\nv_cvt_pk_bf16_f32 v38,0,v38\nM v[92:95],a[132:135],v[216:219],v[92:95]\nds_read_b64_tr_b16 v[216:217],v227 offset:32768\nds_read_b64_tr_b16 v[218:219],v227 offset:36864\nM v[92:95],a[140:143],v[220:223],v[92:95]\nds_read_b64_tr_b16 v[220:221],v227 offset:40960\ns_waitcnt lgkmcnt(7)\n" \
"M v[104:107],a[128:131],v[192:195],v[104:107]\nds_read_b64_tr_b16 v[222:223],v227 offset:45056\nv_cvt_pk_bf16_f32 v39,0,v39\nM v[104:107],a[136:139],v[208:211],v[104:107]\nv_cvt_pk_bf16_f32 v116,0,v116\nv_cvt_pk_bf16_f32 v117,0,v117\nM v[108:111],a[132:135],v[192:195],v[108:111]\nds_read_b64_tr_b16 v[192:193],v226 offset:33280\n" \
"ds_read_b64_tr_b16 v[194:195],v226 offset:37376\nM v[108:111],a[140:143],v[208:211],v[108:111]\nds_read_b64_tr_b16 v[208:209],v226 offset:41472\nds_read_b64_tr_b16 v[210:211],v226 offset:45568\nM v[120:123],a[128:131],v[196:199],v[120:123]\nv_xor_b32 v228,v228,v229\ns_add_u32 s38,s78,16384\nM v[120:123],a[136:139],v[212:215],v[120:123]\n" \
"v_accvgpr_write_b32 a192,v229\nv_accvgpr_write_b32 a193,v228\nM v[124:127],a[132:135],v[196:199],v[124:127]\nds_read_b64_tr_b16 v[196:197],v227 offset:33280\nds_read_b64_tr_b16 v[198:199],v227 offset:37376\nM v[124:127],a[140:143],v[212:215],v[124:127]\nds_read_b64_tr_b16 v[212:213],v227 offset:41472\ns_waitcnt lgkmcnt(7)\n" \
"M v[8:11],a[144:147],v[200:203],v[8:11]\nds_read_b64_tr_b16 v[214:215],v227 offset:45568\ns_waitcnt vmcnt(3)\nM v[8:11],a[152:155],v[204:207],v[8:11]\nv_accvgpr_write_b32 a198,v229\nv_accvgpr_write_b32 a199,v228\nM v[12:15],a[148:151],v[200:203],v[12:15]\nds_read_b64_tr_b16 v[200:201],v226 offset:33792\n" \
"ds_read_b64_tr_b16 v[202:203],v226 offset:37888\nM v[12:15],a[156:159],v[204:207],v[12:15]\nds_read_b64_tr_b16 v[204:205],v226 offset:41984\nds_read_b64_tr_b16 v[206:207],v226 offset:46080\nM v[24:27],a[144:147],v[216:219],v[24:27]\nM v[24:27],a[152:155],v[220:223],v[24:27]\nM v[28:31],a[148:151],v[216:219],v[28:31]\nds_read_b64_tr_b16 v[216:217],v227 offset:33792\n" \
"ds_read_b64_tr_b16 v[218:219],v227 offset:37888\nM v[28:31],a[156:159],v[220:223],v[28:31]\nds_read_b64_tr_b16 v[220:221],v227 offset:41984\ns_waitcnt lgkmcnt(8)\nM v[40:43],a[144:147],v[192:195],v[40:43]\nds_read_b64_tr_b16 v[222:223],v227 offset:46080\nv_cvt_pk_bf16_f32 v8,v8,v24\nM v[40:43],a[152:155],v[208:211],v[40:43]\n" \
"v_cvt_pk_bf16_f32 v9,v9,v25\nv_cvt_pk_bf16_f32 v10,v10,v26\nM v[44:47],a[148:151],v[192:195],v[44:47]\nds_read_b64_tr_b16 v[192:193],v226 offset:34304\nds_read_b64_tr_b16 v[194:195],v226 offset:38400\nM v[44:47],a[156:159],v[208:211],v[44:47]\nds_read_b64_tr_b16 v[208:209],v226 offset:42496\nds_read_b64_tr_b16 v[210:211],v226 offset:46592\n" \
"M v[56:59],a[144:147],v[196:199],v[56:59]\nv_cvt_pk_bf16_f32 v11,v11,v27\ns_waitcnt lgkmcnt(8)\nM v[56:59],a[152:155],v[212:215],v[56:59]\nv_cvt_pk_bf16_f32 v12,v12,v28\nv_cvt_pk_bf16_f32 v13,v13,v29\nM v[60:63],a[148:151],v[196:199],v[60:63]\nds_read_b64_tr_b16 v[196:197],v227 offset:34304\n" \
"ds_read_b64_tr_b16 v[198:199],v227 offset:38400\nM v[60:63],a[156:159],v[212:215],v[60:63]\nds_read_b64_tr_b16 v[212:213],v227 offset:42496\nds_read_b64_tr_b16 v[214:215],v227 offset:46592\nM v[72:75],a[144:147],v[200:203],v[72:75]\nv_cvt_pk_bf16_f32 v40,v40,v56\nv_cvt_pk_bf16_f32 v41,v41,v57\nM v[72:75],a[152:155],v[204:207],v[72:75]\n" \
"v_cvt_pk_bf16_f32 v42,v42,v58\nv_cvt_pk_bf16_f32 v43,v43,v59\nM v[76:79],a[148:151],v[200:203],v[76:79]\nv_cvt_pk_bf16_f32 v14,v14,v30\nv_cvt_pk_bf16_f32 v15,v15,v31\nM v[76:79],a[156:159],v[204:207],v[76:79]\nv_cvt_pk_bf16_f32 v44,v44,v60\ns_waitcnt lgkmcnt(4)\n" \
"M v[88:91],a[144:147],v[216:219],v[88:91]\nv_cvt_pk_bf16_f32 v45,v45,v61\nv_cvt_pk_bf16_f32 v46,v46,v62\nM v[88:91],a[152:155],v[220:223],v[88:91]\nv_cvt_pk_bf16_f32 v47,v47,v63\nv_min_u32 v24,-2,v8\nM v[92:95],a[148:151],v[216:219],v[92:95]\nv_min_u32 v25,-2,v40\n" \
"v_min_u32 v56,-2,v9\nM v[92:95],a[156:159],v[220:223],v[92:95]\nv_min_u32 v57,-2,v41\nv_min_u32 v28,-2,v12\nM v[104:107],a[144:147],v[192:195],v[104:107]\nv_cvt_pk_bf16_f32 v72,v72,v88\nv_cvt_pk_bf16_f32 v73,v73,v89\nM v[104:107],a[152:155],v[208:211],v[104:107]\n" \
"v_cvt_pk_bf16_f32 v74,v74,v90\nv_cvt_pk_bf16_f32 v75,v75,v91\nM v[108:111],a[148:151],v[192:195],v[108:111]\nv_cvt_pk_bf16_f32 v76,v76,v92\nv_cvt_pk_bf16_f32 v77,v77,v93\nM v[108:111],a[156:159],v[208:211],v[108:111]\nv_cvt_pk_bf16_f32 v78,v78,v94\ns_waitcnt lgkmcnt(0)\n" \
"M v[120:123],a[144:147],v[196:199],v[120:123]\nv_cvt_pk_bf16_f32 v79,v79,v95\nv_min_u32 v26,-2,v72\nM v[120:123],a[152:155],v[212:215],v[120:123]\nv_min_u32 v58,-2,v73\nv_min_u32 v88,-2,v10\nM v[124:127],a[148:151],v[196:199],v[124:127]\nv_min_u32 v89,-2,v42\n" \
"v_min_u32 v90,-2,v74\nM v[124:127],a[156:159],v[212:215],v[124:127]\nv_min_u32 v29,-2,v44\nv_min_u32 v30,-2,v76\nv_cvt_pk_bf16_f32 v104,v104,v120\nv_cvt_pk_bf16_f32 v105,v105,v121\nv_cvt_pk_bf16_f32 v106,v106,v122\nv_cvt_pk_bf16_f32 v107,v107,v123\n" \
"v_min_u32 v27,-2,v104\nv_min_u32 v59,-2,v105\nv_cvt_pk_bf16_f32 v108,v108,v124\nv_cvt_pk_bf16_f32 v109,v109,v125\nv_cvt_pk_bf16_f32 v110,v110,v126\nv_cvt_pk_bf16_f32 v111,v111,v127\nv_min_u32 v91,-2,v106\nv_min_u32 v120,-2,v11\n" \
"v_min_u32 v121,-2,v43\nv_min_u32 v122,-2,v75\nv_min_u32 v123,-2,v107\nv_min_u32 v31,-2,v108\nv_min_u32 v60,-2,v13\nv_min_u32 v61,-2,v45\nv_min_u32 v62,-2,v77\nv_min_u32 v63,-2,v109\n" \
"v_min_u32 v92,-2,v14\nv_min_u32 v93,-2,v46\nv_min_u32 v94,-2,v78\nv_min_u32 v95,-2,v110\nv_min_u32 v124,-2,v15\nv_min_u32 v125,-2,v47\nv_min_u32 v126,-2,v79\nv_min_u32 v127,-2,v111\n" \
"buffer_store_dwordx4 v[24:27],v253,s[52:55],s78 offen offset:0 sc1\nbuffer_store_dwordx4 v[56:59],v253,s[52:55],s78 offen offset:1024 sc1\nbuffer_store_dwordx4 v[88:91],v253,s[52:55],s78 offen offset:2048 sc1\nbuffer_store_dwordx4 v[120:123],v253,s[52:55],s78 offen offset:3072 sc1\nbuffer_store_dwordx4 v[28:31],v253,s[52:55],s38 offen offset:0 sc1\nbuffer_store_dwordx4 v[60:63],v253,s[52:55],s38 offen offset:1024 sc1\nbuffer_store_dwordx4 v[92:95],v253,s[52:55],s38 offen offset:2048 sc1\nbuffer_store_dwordx4 v[124:127],v253,s[52:55],s38 offen offset:3072 sc1\n" \
".Ltail:\ns_add_u32 s72,s95,0\nbuffer_load_dwordx4 v[160:163],v240,s[52:55],s72 offen sc1\ns_add_u32 s72,s95,2097152\nbuffer_load_dwordx4 v[164:167],v240,s[52:55],s72 offen sc1\ns_add_u32 s72,s95,4194304\nbuffer_load_dwordx4 v[168:171],v240,s[52:55],s72 offen sc1\ns_add_u32 s72,s95,6291456\n" \
"buffer_load_dwordx4 v[172:175],v240,s[52:55],s72 offen sc1\ns_add_u32 s72,s95,8388608\nbuffer_load_dwordx4 v[176:179],v240,s[52:55],s72 offen sc1\ns_add_u32 s72,s95,10485760\nbuffer_load_dwordx4 v[180:183],v240,s[52:55],s72 offen sc1\ns_add_u32 s72,s95,12582912\nbuffer_load_dwordx4 v[184:187],v240,s[52:55],s72 offen sc1\ns_add_u32 s72,s95,14680064\n" \
"buffer_load_dwordx4 v[188:191],v240,s[52:55],s72 offen sc1\n.Ltail2:\ns_add_u32 s72,s74,0\nbuffer_load_dwordx4 v[128:131],v240,s[52:55],s72 offen sc1\ns_add_u32 s72,s74,2097152\nbuffer_load_dwordx4 v[132:135],v240,s[52:55],s72 offen sc1\ns_add_u32 s72,s74,4194304\nbuffer_load_dwordx4 v[136:139],v240,s[52:55],s72 offen sc1\n" \
"s_add_u32 s72,s74,6291456\nbuffer_load_dwordx4 v[140:143],v240,s[52:55],s72 offen sc1\ns_add_u32 s72,s74,8388608\nbuffer_load_dwordx4 v[144:147],v240,s[52:55],s72 offen sc1\ns_add_u32 s72,s74,10485760\nbuffer_load_dwordx4 v[148:151],v240,s[52:55],s72 offen sc1\ns_add_u32 s72,s74,12582912\nbuffer_load_dwordx4 v[152:155],v240,s[52:55],s72 offen sc1\n" \
"s_add_u32 s72,s74,14680064\nbuffer_load_dwordx4 v[156:159],v240,s[52:55],s72 offen sc1\ns_mov_b32 s94,0\ns_mov_b32 s29,1\ns_cmp_eq_u32 s3,0\ns_cbranch_scc1 .Lnp\ns_mov_b32 s70,0x05040100\ns_mov_b32 s71,0x07060302\n" \
"v_accvgpr_read_b32 v228,a212\nv_accvgpr_read_b32 v229,a213\nv_accvgpr_read_b32 v230,a214\nv_accvgpr_read_b32 v231,a215\nv_perm_b32 v192,v230,v228,s70\nv_perm_b32 v193,v230,v228,s71\nv_perm_b32 v194,v231,v229,s70\nv_perm_b32 v195,v231,v229,s71\n" \
"v_accvgpr_read_b32 v228,a216\nv_accvgpr_read_b32 v229,a217\nv_accvgpr_read_b32 v230,a218\nv_accvgpr_read_b32 v231,a219\nv_perm_b32 v196,v230,v228,s70\nv_perm_b32 v197,v230,v228,s71\nv_perm_b32 v198,v231,v229,s70\nv_perm_b32 v199,v231,v229,s71\n" \
"v_accvgpr_read_b32 v228,a220\nv_accvgpr_read_b32 v229,a221\nv_accvgpr_read_b32 v230,a222\nv_accvgpr_read_b32 v231,a223\nv_perm_b32 v200,v230,v228,s70\nv_perm_b32 v201,v230,v228,s71\nv_perm_b32 v202,v231,v229,s70\nv_perm_b32 v203,v231,v229,s71\n" \
"v_accvgpr_read_b32 v228,a224\nv_accvgpr_read_b32 v229,a225\nv_accvgpr_read_b32 v230,a226\nv_accvgpr_read_b32 v231,a227\nv_perm_b32 v204,v230,v228,s70\nv_perm_b32 v205,v230,v228,s71\nv_perm_b32 v206,v231,v229,s70\nv_perm_b32 v207,v231,v229,s71\n" \
"v_accvgpr_read_b32 v228,a228\nv_accvgpr_read_b32 v229,a229\nv_accvgpr_read_b32 v230,a230\nv_accvgpr_read_b32 v231,a231\nv_perm_b32 v208,v230,v228,s70\nv_perm_b32 v209,v230,v228,s71\nv_perm_b32 v210,v231,v229,s70\nv_perm_b32 v211,v231,v229,s71\n" \
"v_accvgpr_read_b32 v228,a232\nv_accvgpr_read_b32 v229,a233\nv_accvgpr_read_b32 v230,a234\nv_accvgpr_read_b32 v231,a235\nv_perm_b32 v212,v230,v228,s70\nv_perm_b32 v213,v230,v228,s71\nv_perm_b32 v214,v231,v229,s70\nv_perm_b32 v215,v231,v229,s71\n" \
"v_accvgpr_read_b32 v228,a236\nv_accvgpr_read_b32 v229,a237\nv_accvgpr_read_b32 v230,a238\nv_accvgpr_read_b32 v231,a239\nv_perm_b32 v216,v230,v228,s70\nv_perm_b32 v217,v230,v228,s71\nv_perm_b32 v218,v231,v229,s70\nv_perm_b32 v219,v231,v229,s71\n" \
"v_accvgpr_read_b32 v228,a240\nv_accvgpr_read_b32 v229,a241\nv_accvgpr_read_b32 v230,a242\nv_accvgpr_read_b32 v231,a243\nv_perm_b32 v220,v230,v228,s70\nv_perm_b32 v221,v230,v228,s71\nv_perm_b32 v222,v231,v229,s70\nv_perm_b32 v223,v231,v229,s71\n" \
"v_and_b32 v228,3,v249\nv_lshrrev_b32 v229,4,v249\nv_and_b32 v230,1,v229\nv_lshrrev_b32 v229,1,v229\nv_lshlrev_b32 v230,5,v230\nv_lshl_or_b32 v230,v228,6,v230\nv_lshl_or_b32 v230,v229,8,v230\ns_lshl2_add_u32 s70,s7,83008\n" \
"v_add_u32 v252,s70,v230\nv_and_b32 v230,0xf0,v240\nv_add_u32 v253,83008,v230\n.Lnp:\n.Lrow1:\ns_waitcnt vmcnt(15)\nM v[72:75],a[192:195],v[160:163],0\nM v[76:79],a[196:199],v[160:163],0\n" \
"s_waitcnt vmcnt(14)\nM v[72:75],a[192:195],v[164:167],v[72:75]\nM v[76:79],a[196:199],v[164:167],v[76:79]\ns_waitcnt vmcnt(13)\nM v[72:75],a[192:195],v[168:171],v[72:75]\nM v[76:79],a[196:199],v[168:171],v[76:79]\ns_waitcnt vmcnt(12)\nM v[72:75],a[192:195],v[172:175],v[72:75]\n" \
"M v[76:79],a[196:199],v[172:175],v[76:79]\ns_waitcnt vmcnt(11)\nM v[72:75],a[192:195],v[176:179],v[72:75]\nM v[76:79],a[196:199],v[176:179],v[76:79]\ns_waitcnt vmcnt(10)\nM v[72:75],a[192:195],v[180:183],v[72:75]\nM v[76:79],a[196:199],v[180:183],v[76:79]\ns_waitcnt vmcnt(9)\n" \
"M v[72:75],a[192:195],v[184:187],v[72:75]\nM v[76:79],a[196:199],v[184:187],v[76:79]\ns_waitcnt vmcnt(8)\nM v[72:75],a[192:195],v[188:191],v[72:75]\nM v[76:79],a[196:199],v[188:191],v[76:79]\ns_sub_u32 s38,56622112,s95\ns_lshr_b32 s38,s38,5\ns_nop 4\n" \
"v_cvt_pk_bf16_f32 v72,0,v72\nv_cvt_pk_bf16_f32 v73,0,v73\nv_cvt_pk_bf16_f32 v74,0,v74\nv_cvt_pk_bf16_f32 v75,0,v75\nv_cvt_pk_bf16_f32 v76,0,v76\nv_cvt_pk_bf16_f32 v77,0,v77\nv_cvt_pk_bf16_f32 v78,0,v78\nv_cvt_pk_bf16_f32 v79,0,v79\n" \
"v_pk_mul_f32 v[72:73],v[72:73],v[0:1]\nv_pk_mul_f32 v[74:75],v[74:75],v[2:3]\nv_pk_mul_f32 v[76:77],v[76:77],v[4:5]\nv_pk_mul_f32 v[78:79],v[78:79],v[6:7]\nv_cvt_pk_bf16_f32 v72,0,v72\nv_cvt_pk_bf16_f32 v73,0,v73\nv_cvt_pk_bf16_f32 v74,0,v74\nv_cvt_pk_bf16_f32 v75,0,v75\n" \
"v_cvt_pk_bf16_f32 v76,0,v76\nv_cvt_pk_bf16_f32 v77,0,v77\nv_cvt_pk_bf16_f32 v78,0,v78\nv_cvt_pk_bf16_f32 v79,0,v79\nv_pk_add_f32 v[72:73],v[72:73],v[64:65]\nv_pk_add_f32 v[74:75],v[74:75],v[66:67]\nv_pk_add_f32 v[76:77],v[76:77],v[68:69]\nv_pk_add_f32 v[78:79],v[78:79],v[70:71]\n" \
"v_cvt_pk_bf16_f32 v72,0,v72\nv_cvt_pk_bf16_f32 v73,0,v73\nv_cvt_pk_bf16_f32 v74,0,v74\nv_cvt_pk_bf16_f32 v75,0,v75\nv_cvt_pk_bf16_f32 v76,0,v76\nv_cvt_pk_bf16_f32 v77,0,v77\nv_cvt_pk_bf16_f32 v78,0,v78\nv_cvt_pk_bf16_f32 v79,0,v79\n" \
"v_readfirstlane_b32 s70,v72\ns_nop 1\nv_subrev_f32 v104,s70,v72\nv_subrev_f32 v105,s70,v73\nv_subrev_f32 v106,s70,v74\nv_subrev_f32 v107,s70,v75\nv_subrev_f32 v108,s70,v76\nv_subrev_f32 v109,s70,v77\n" \
"v_subrev_f32 v110,s70,v78\nv_subrev_f32 v111,s70,v79\nv_pk_mul_f32 v[124:125],v[104:105],v[104:105]\nv_pk_add_f32 v[120:121],v[104:105],v[106:107]\nv_pk_fma_f32 v[124:125],v[106:107],v[106:107],v[124:125]\nv_pk_fma_f32 v[124:125],v[108:109],v[108:109],v[124:125]\nv_pk_fma_f32 v[124:125],v[110:111],v[110:111],v[124:125]\nv_pk_add_f32 v[108:109],v[108:109],v[110:111]\n" \
"v_pk_add_f32 v[120:121],v[120:121],v[108:109]\nv_add_f32 v120,v120,v121\nv_add_f32 v121,v124,v125\ns_nop 1\nv_permlane16_swap_b32 v120,v121\nv_add_f32 v120,v120,v121\nv_mov_b32 v121,v120\ns_nop 1\n" \
"v_permlane32_swap_b32 v120,v121\nv_add_f32 v120,v120,v121\ns_nop 1\nv_add_f32_dpp v120,v120,v120 quad_perm:[1,0,3,2] row_mask:0xf bank_mask:0xf\ns_nop 1\nv_add_f32_dpp v120,v120,v120 quad_perm:[2,3,0,1] row_mask:0xf bank_mask:0xf\ns_nop 1\nv_add_f32_dpp v120,v120,v120 row_half_mirror row_mask:0xf bank_mask:0xf\n" \
"s_nop 1\nv_add_f32_dpp v120,v120,v120 row_mirror row_mask:0xf bank_mask:0xf\nv_mul_f32 v120,0x3b000000,v120\nv_mov_b32 v121,v120\nv_cmp_u_f32 vcc,v120,v120\ns_nop 0\nv_permlane16_swap_b32 v120,v121\ns_cbranch_vccnz .Lrs1\n" \
".Lrb1:\nv_fma_f32 v121,-v120,v120,v121\nv_add_f32 v122,s70,v120\nv_max_f32 v121,0,v121\nv_cvt_pk_bf16_f32 v122,0,v122\nv_cvt_pk_bf16_f32 v121,0,v121\nv_add_f32 v121,0x3727c5ac,v121\nv_cvt_pk_bf16_f32 v121,0,v121\n" \
"v_sqrt_f32 v121,v121\nv_pk_add_f32 v[88:89],v[72:73],v[122:123] op_sel_hi:[1,0] neg_lo:[0,1] neg_hi:[0,1]\nv_cvt_pk_bf16_f32 v121,0,v121\nv_rcp_f32 v123,v121\nv_cvt_pk_bf16_f32 v88,0,v88\nv_pk_add_f32 v[90:91],v[74:75],v[122:123] op_sel_hi:[1,0] neg_lo:[0,1] neg_hi:[0,1]\nv_pk_add_f32 v[92:93],v[76:77],v[122:123] op_sel_hi:[1,0] neg_lo:[0,1] neg_hi:[0,1]\nv_pk_add_f32 v[94:95],v[78:79],v[122:123] op_sel_hi:[1,0] neg_lo:[0,1] neg_hi:[0,1]\n" \
"v_cvt_pk_bf16_f32 v89,0,v89\nv_pk_mul_f32 v[88:89],v[88:89],v[122:123] op_sel:[0,1]\nv_cvt_pk_bf16_f32 v88,0,v88\nv_cvt_pk_bf16_f32 v89,0,v89\nv_pk_mul_f32 v[88:89],v[88:89],v[32:33]\nv_cvt_pk_bf16_f32 v88,0,v88\nv_cvt_pk_bf16_f32 v89,0,v89\nv_pk_add_f32 v[88:89],v[88:89],v[16:17]\n" \
"v_cvt_pk_bf16_f32 v88,0,v88\nv_cvt_pk_bf16_f32 v89,0,v89\nv_pk_mul_f32 v[124:125],v[88:89],v[96:97]\nv_cvt_pk_bf16_f32 v90,0,v90\nv_cvt_pk_bf16_f32 v91,0,v91\nv_pk_mul_f32 v[90:91],v[90:91],v[122:123] op_sel:[0,1]\nv_cvt_pk_bf16_f32 v90,0,v90\nv_cvt_pk_bf16_f32 v91,0,v91\n" \
"v_pk_mul_f32 v[90:91],v[90:91],v[34:35]\nv_cvt_pk_bf16_f32 v90,0,v90\nv_cvt_pk_bf16_f32 v91,0,v91\nv_pk_add_f32 v[90:91],v[90:91],v[18:19]\nv_cvt_pk_bf16_f32 v90,0,v90\nv_cvt_pk_bf16_f32 v91,0,v91\nv_pk_fma_f32 v[124:125],v[90:91],v[98:99],v[124:125]\nv_cvt_pk_bf16_f32 v92,0,v92\n" \
"v_cvt_pk_bf16_f32 v93,0,v93\nv_pk_mul_f32 v[92:93],v[92:93],v[122:123] op_sel:[0,1]\nv_cvt_pk_bf16_f32 v92,0,v92\nv_cvt_pk_bf16_f32 v93,0,v93\nv_pk_mul_f32 v[92:93],v[92:93],v[36:37]\nv_cvt_pk_bf16_f32 v92,0,v92\nv_cvt_pk_bf16_f32 v93,0,v93\nv_pk_add_f32 v[92:93],v[92:93],v[20:21]\n" \
"v_cvt_pk_bf16_f32 v92,0,v92\nv_cvt_pk_bf16_f32 v93,0,v93\nv_pk_fma_f32 v[124:125],v[92:93],v[100:101],v[124:125]\nv_cvt_pk_bf16_f32 v94,0,v94\nv_cvt_pk_bf16_f32 v95,0,v95\nv_pk_mul_f32 v[94:95],v[94:95],v[122:123] op_sel:[0,1]\nv_cvt_pk_bf16_f32 v94,0,v94\nv_cvt_pk_bf16_f32 v95,0,v95\n" \
"v_pk_mul_f32 v[94:95],v[94:95],v[38:39]\nv_cvt_pk_bf16_f32 v94,0,v94\nv_cvt_pk_bf16_f32 v95,0,v95\nv_pk_add_f32 v[94:95],v[94:95],v[22:23]\nv_cvt_pk_bf16_f32 v94,0,v94\nv_cvt_pk_bf16_f32 v95,0,v95\nv_pk_fma_f32 v[124:125],v[94:95],v[102:103],v[124:125]\nv_add_f32 v124,v124,v125\n" \
"v_mov_b32 v125,v124\nv_lshrrev_b32 v104,16,v121\nv_or_b32 v104,v104,v122\nv_permlane16_swap_b32 v124,v125\nv_add_f32 v124,v124,v125\nv_mov_b32 v125,v124\ns_nop 1\nv_permlane32_swap_b32 v124,v125\n" \
"v_add_f32 v124,v124,v125\ns_nop 1\nv_add_f32_dpp v124,v124,v124 quad_perm:[1,0,3,2] row_mask:0xf bank_mask:0xf\ns_nop 1\nv_add_f32_dpp v124,v124,v124 quad_perm:[2,3,0,1] row_mask:0xf bank_mask:0xf\ns_nop 1\nv_add_f32_dpp v124,v124,v124 row_half_mirror row_mask:0xf bank_mask:0xf\ns_nop 1\n" \
"v_add_f32_dpp v124,v124,v124 row_mirror row_mask:0xf bank_mask:0xf\nv_cvt_pk_bf16_f32 v124,0,v124\ns_nop 0\nv_readfirstlane_b32 s77,v124\ns_nop 0\ns_ashr_i32 s71,s77,31\ns_and_b32 s72,s77,0x7fffffff\ns_or_b32 s71,s71,0x80000000\n" \
"s_xor_b32 s77,s77,s71\ns_and_b32 s77,s77,0xffff0000\ns_cmp_gt_u32 s72,0x7f800000\ns_cselect_b32 s77,0,s77\ns_or_b32 s77,s77,s38\nv_mov_b32 v105,s77\nds_max_u64 v250,v[104:105]\n.Lrow0:\n" \
"s_waitcnt vmcnt(7)\nM v[8:11],a[192:195],v[128:131],0\nM v[12:15],a[196:199],v[128:131],0\ns_waitcnt vmcnt(6)\nM v[8:11],a[192:195],v[132:135],v[8:11]\nM v[12:15],a[196:199],v[132:135],v[12:15]\ns_waitcnt vmcnt(5)\nM v[8:11],a[192:195],v[136:139],v[8:11]\n" \
"M v[12:15],a[196:199],v[136:139],v[12:15]\ns_waitcnt vmcnt(4)\nM v[8:11],a[192:195],v[140:143],v[8:11]\nM v[12:15],a[196:199],v[140:143],v[12:15]\ns_waitcnt vmcnt(3)\nM v[8:11],a[192:195],v[144:147],v[8:11]\nM v[12:15],a[196:199],v[144:147],v[12:15]\ns_waitcnt vmcnt(2)\n" \
"M v[8:11],a[192:195],v[148:151],v[8:11]\nM v[12:15],a[196:199],v[148:151],v[12:15]\ns_waitcnt vmcnt(1)\nM v[8:11],a[192:195],v[152:155],v[8:11]\nM v[12:15],a[196:199],v[152:155],v[12:15]\ns_waitcnt vmcnt(0)\nM v[8:11],a[192:195],v[156:159],v[8:11]\nM v[12:15],a[196:199],v[156:159],v[12:15]\n" \
"s_sub_u32 s39,56622112,s74\ns_lshr_b32 s39,s39,5\ns_nop 4\nv_cvt_pk_bf16_f32 v8,0,v8\nv_cvt_pk_bf16_f32 v9,0,v9\nv_cvt_pk_bf16_f32 v10,0,v10\nv_cvt_pk_bf16_f32 v11,0,v11\nv_cvt_pk_bf16_f32 v12,0,v12\n" \
"v_cvt_pk_bf16_f32 v13,0,v13\nv_cvt_pk_bf16_f32 v14,0,v14\nv_cvt_pk_bf16_f32 v15,0,v15\nv_pk_mul_f32 v[8:9],v[8:9],v[0:1]\nv_pk_mul_f32 v[10:11],v[10:11],v[2:3]\nv_pk_mul_f32 v[12:13],v[12:13],v[4:5]\nv_pk_mul_f32 v[14:15],v[14:15],v[6:7]\nv_cvt_pk_bf16_f32 v8,0,v8\n" \
"v_cvt_pk_bf16_f32 v9,0,v9\nv_cvt_pk_bf16_f32 v10,0,v10\nv_cvt_pk_bf16_f32 v11,0,v11\nv_cvt_pk_bf16_f32 v12,0,v12\nv_cvt_pk_bf16_f32 v13,0,v13\nv_cvt_pk_bf16_f32 v14,0,v14\nv_cvt_pk_bf16_f32 v15,0,v15\nv_pk_add_f32 v[8:9],v[8:9],v[80:81]\n" \
"v_pk_add_f32 v[10:11],v[10:11],v[82:83]\nv_pk_add_f32 v[12:13],v[12:13],v[84:85]\nv_pk_add_f32 v[14:15],v[14:15],v[86:87]\nv_cvt_pk_bf16_f32 v8,0,v8\nv_cvt_pk_bf16_f32 v9,0,v9\nv_cvt_pk_bf16_f32 v10,0,v10\nv_cvt_pk_bf16_f32 v11,0,v11\nv_cvt_pk_bf16_f32 v12,0,v12\n" \
"v_cvt_pk_bf16_f32 v13,0,v13\nv_cvt_pk_bf16_f32 v14,0,v14\nv_cvt_pk_bf16_f32 v15,0,v15\nv_readfirstlane_b32 s70,v8\ns_nop 1\nv_subrev_f32 v40,s70,v8\nv_subrev_f32 v41,s70,v9\nv_subrev_f32 v42,s70,v10\n" \
"v_subrev_f32 v43,s70,v11\nv_subrev_f32 v44,s70,v12\nv_subrev_f32 v45,s70,v13\nv_subrev_f32 v46,s70,v14\nv_subrev_f32 v47,s70,v15\nv_pk_mul_f32 v[60:61],v[40:41],v[40:41]\nv_pk_add_f32 v[56:57],v[40:41],v[42:43]\nv_pk_fma_f32 v[60:61],v[42:43],v[42:43],v[60:61]\n" \
"v_pk_fma_f32 v[60:61],v[44:45],v[44:45],v[60:61]\nv_pk_fma_f32 v[60:61],v[46:47],v[46:47],v[60:61]\nv_pk_add_f32 v[44:45],v[44:45],v[46:47]\nv_pk_add_f32 v[56:57],v[56:57],v[44:45]\nv_add_f32 v56,v56,v57\nv_add_f32 v57,v60,v61\ns_nop 1\nv_permlane16_swap_b32 v56,v57\n" \
"v_add_f32 v56,v56,v57\nv_mov_b32 v57,v56\ns_nop 1\nv_permlane32_swap_b32 v56,v57\nv_add_f32 v56,v56,v57\ns_nop 1\nv_add_f32_dpp v56,v56,v56 quad_perm:[1,0,3,2] row_mask:0xf bank_mask:0xf\ns_nop 1\n" \
"v_add_f32_dpp v56,v56,v56 quad_perm:[2,3,0,1] row_mask:0xf bank_mask:0xf\ns_nop 1\nv_add_f32_dpp v56,v56,v56 row_half_mirror row_mask:0xf bank_mask:0xf\ns_nop 1\nv_add_f32_dpp v56,v56,v56 row_mirror row_mask:0xf bank_mask:0xf\nv_mul_f32 v56,0x3b000000,v56\nv_mov_b32 v57,v56\nv_cmp_u_f32 vcc,v56,v56\n" \
"s_nop 0\nv_permlane16_swap_b32 v56,v57\ns_cbranch_vccnz .Lrs0\n.Lrb0:\nbuffer_load_dwordx4 v[232:235],v251,s[52:55],s80 offen sc1\nbuffer_load_dwordx4 v[236:239],v251,s[52:55],s80 offen offset:16 sc1\nv_fma_f32 v57,-v56,v56,v57\nv_add_f32 v58,s70,v56\n" \
"v_max_f32 v57,0,v57\nv_cvt_pk_bf16_f32 v58,0,v58\nv_cvt_pk_bf16_f32 v57,0,v57\nv_add_f32 v57,0x3727c5ac,v57\nv_cvt_pk_bf16_f32 v57,0,v57\nv_sqrt_f32 v57,v57\nv_pk_add_f32 v[24:25],v[8:9],v[58:59] op_sel_hi:[1,0] neg_lo:[0,1] neg_hi:[0,1]\nv_cvt_pk_bf16_f32 v57,0,v57\n" \
"v_rcp_f32 v59,v57\nv_cvt_pk_bf16_f32 v24,0,v24\nv_pk_add_f32 v[26:27],v[10:11],v[58:59] op_sel_hi:[1,0] neg_lo:[0,1] neg_hi:[0,1]\nv_pk_add_f32 v[28:29],v[12:13],v[58:59] op_sel_hi:[1,0] neg_lo:[0,1] neg_hi:[0,1]\nv_pk_add_f32 v[30:31],v[14:15],v[58:59] op_sel_hi:[1,0] neg_lo:[0,1] neg_hi:[0,1]\nv_cvt_pk_bf16_f32 v25,0,v25\nv_pk_mul_f32 v[24:25],v[24:25],v[58:59] op_sel:[0,1]\nv_cvt_pk_bf16_f32 v24,0,v24\n" \
"v_cvt_pk_bf16_f32 v25,0,v25\nv_pk_mul_f32 v[24:25],v[24:25],v[32:33]\nv_cvt_pk_bf16_f32 v24,0,v24\nv_cvt_pk_bf16_f32 v25,0,v25\nv_pk_add_f32 v[24:25],v[24:25],v[16:17]\nv_cvt_pk_bf16_f32 v24,0,v24\nv_cvt_pk_bf16_f32 v25,0,v25\nv_pk_mul_f32 v[60:61],v[24:25],v[96:97]\n" \
"v_cvt_pk_bf16_f32 v26,0,v26\nv_cvt_pk_bf16_f32 v27,0,v27\nv_pk_mul_f32 v[26:27],v[26:27],v[58:59] op_sel:[0,1]\nv_cvt_pk_bf16_f32 v26,0,v26\nv_cvt_pk_bf16_f32 v27,0,v27\nv_pk_mul_f32 v[26:27],v[26:27],v[34:35]\nv_cvt_pk_bf16_f32 v26,0,v26\nv_cvt_pk_bf16_f32 v27,0,v27\n" \
"v_pk_add_f32 v[26:27],v[26:27],v[18:19]\nv_cvt_pk_bf16_f32 v26,0,v26\nv_cvt_pk_bf16_f32 v27,0,v27\nv_pk_fma_f32 v[60:61],v[26:27],v[98:99],v[60:61]\nv_cvt_pk_bf16_f32 v28,0,v28\nv_cvt_pk_bf16_f32 v29,0,v29\nv_pk_mul_f32 v[28:29],v[28:29],v[58:59] op_sel:[0,1]\nv_cvt_pk_bf16_f32 v28,0,v28\n" \
"v_cvt_pk_bf16_f32 v29,0,v29\nv_pk_mul_f32 v[28:29],v[28:29],v[36:37]\nv_cvt_pk_bf16_f32 v28,0,v28\nv_cvt_pk_bf16_f32 v29,0,v29\nv_pk_add_f32 v[28:29],v[28:29],v[20:21]\nv_cvt_pk_bf16_f32 v28,0,v28\nv_cvt_pk_bf16_f32 v29,0,v29\nv_pk_fma_f32 v[60:61],v[28:29],v[100:101],v[60:61]\n" \
"v_cvt_pk_bf16_f32 v30,0,v30\nv_cvt_pk_bf16_f32 v31,0,v31\nv_pk_mul_f32 v[30:31],v[30:31],v[58:59] op_sel:[0,1]\nv_cvt_pk_bf16_f32 v30,0,v30\nv_cvt_pk_bf16_f32 v31,0,v31\nv_pk_mul_f32 v[30:31],v[30:31],v[38:39]\nv_cvt_pk_bf16_f32 v30,0,v30\nv_cvt_pk_bf16_f32 v31,0,v31\n" \
"v_pk_add_f32 v[30:31],v[30:31],v[22:23]\nv_cvt_pk_bf16_f32 v30,0,v30\nv_cvt_pk_bf16_f32 v31,0,v31\nv_pk_fma_f32 v[60:61],v[30:31],v[102:103],v[60:61]\nv_add_f32 v60,v60,v61\nv_mov_b32 v61,v60\nv_lshrrev_b32 v40,16,v57\nv_or_b32 v40,v40,v58\n" \
"v_permlane16_swap_b32 v60,v61\nv_add_f32 v60,v60,v61\nv_mov_b32 v61,v60\ns_nop 1\nv_permlane32_swap_b32 v60,v61\nv_add_f32 v60,v60,v61\ns_nop 1\nv_add_f32_dpp v60,v60,v60 quad_perm:[1,0,3,2] row_mask:0xf bank_mask:0xf\n" \
"s_nop 1\nv_add_f32_dpp v60,v60,v60 quad_perm:[2,3,0,1] row_mask:0xf bank_mask:0xf\ns_nop 1\nv_add_f32_dpp v60,v60,v60 row_half_mirror row_mask:0xf bank_mask:0xf\ns_nop 1\nv_add_f32_dpp v60,v60,v60 row_mirror row_mask:0xf bank_mask:0xf\nv_cvt_pk_bf16_f32 v60,0,v60\ns_nop 0\n" \
"v_readfirstlane_b32 s76,v60\ns_nop 0\ns_ashr_i32 s71,s76,31\ns_and_b32 s72,s76,0x7fffffff\ns_or_b32 s71,s71,0x80000000\ns_xor_b32 s76,s76,s71\ns_and_b32 s76,s76,0xffff0000\ns_cmp_gt_u32 s72,0x7f800000\n" \
"s_cselect_b32 s76,0,s76\ns_or_b32 s76,s76,s39\nv_mov_b32 v41,s76\nds_max_u64 v250,v[40:41]\ns_waitcnt lgkmcnt(0)\ns_barrier\nds_read_b64 v[224:225],v250\ns_waitcnt lgkmcnt(0)\n" \
"buffer_store_dwordx2 v[224:225],v246,s[52:55],s37 offen sc1\nv_readfirstlane_b32 s97,v225\ns_sub_u32 s95,s95,54525952\ns_sub_u32 s74,s74,54525952\ns_mov_b32 s29,0\ns_cmp_eq_u32 s97,s77\ns_cselect_b32 s65,s95,0x40000000\ns_cselect_b32 s95,0x40000000,s95\n" \
"s_cmp_eq_u32 s97,s76\ns_cselect_b32 s66,s74,0x40000000\ns_cselect_b32 s74,0x40000000,s74\ns_lshr_b32 s70,s6,7\ns_or_b32 s38,s7,s70\ns_cmp_lg_u32 s3,0\ns_waitcnt vmcnt(1)\ns_cbranch_scc1 .Lkp\n" \
"v_pk_mul_f32 v[88:89],v[88:89],v[48:49]\nv_pk_mul_f32 v[90:91],v[90:91],v[50:51]\nv_pk_mul_f32 v[92:93],v[92:93],v[52:53]\nv_pk_mul_f32 v[94:95],v[94:95],v[54:55]\nv_cvt_pk_bf16_f32 v88,0,v88\nv_cvt_pk_bf16_f32 v89,0,v89\nv_cvt_pk_bf16_f32 v90,0,v90\nv_cvt_pk_bf16_f32 v91,0,v91\n" \
"v_cvt_pk_bf16_f32 v92,0,v92\nv_cvt_pk_bf16_f32 v93,0,v93\nv_cvt_pk_bf16_f32 v94,0,v94\nv_cvt_pk_bf16_f32 v95,0,v95\nv_pk_add_f32 v[88:89],v[88:89],v[72:73]\nv_pk_add_f32 v[90:91],v[90:91],v[74:75]\nv_pk_add_f32 v[92:93],v[92:93],v[76:77]\nv_pk_add_f32 v[94:95],v[94:95],v[78:79]\n" \
"v_cvt_pk_bf16_f32 v88,0,v88\nds_write_b16_d16_hi v244,v88 offset:0\nv_cvt_pk_bf16_f32 v89,0,v89\nds_write_b16_d16_hi v244,v89 offset:32\nv_cvt_pk_bf16_f32 v90,0,v90\nds_write_b16_d16_hi v244,v90 offset:64\nv_cvt_pk_bf16_f32 v91,0,v91\nds_write_b16_d16_hi v244,v91 offset:96\n" \
"v_cvt_pk_bf16_f32 v92,0,v92\nds_write_b16_d16_hi v244,v92 offset:128\nv_cvt_pk_bf16_f32 v93,0,v93\nds_write_b16_d16_hi v244,v93 offset:160\nv_cvt_pk_bf16_f32 v94,0,v94\nds_write_b16_d16_hi v244,v94 offset:192\nv_cvt_pk_bf16_f32 v95,0,v95\nds_write_b16_d16_hi v244,v95 offset:224\n" \
"ds_read_b128 v[64:67],v245 offset:0\nv_pk_mul_f32 v[24:25],v[24:25],v[48:49]\nv_pk_mul_f32 v[26:27],v[26:27],v[50:51]\nv_pk_mul_f32 v[28:29],v[28:29],v[52:53]\nv_pk_mul_f32 v[30:31],v[30:31],v[54:55]\nv_cvt_pk_bf16_f32 v24,0,v24\nv_cvt_pk_bf16_f32 v25,0,v25\nv_cvt_pk_bf16_f32 v26,0,v26\n" \
"v_cvt_pk_bf16_f32 v27,0,v27\nv_cvt_pk_bf16_f32 v28,0,v28\nv_cvt_pk_bf16_f32 v29,0,v29\nv_cvt_pk_bf16_f32 v30,0,v30\nv_cvt_pk_bf16_f32 v31,0,v31\nv_pk_add_f32 v[24:25],v[24:25],v[8:9]\nv_pk_add_f32 v[26:27],v[26:27],v[10:11]\nv_pk_add_f32 v[28:29],v[28:29],v[12:13]\n" \
"v_pk_add_f32 v[30:31],v[30:31],v[14:15]\nv_cvt_pk_bf16_f32 v24,0,v24\nds_write_b16_d16_hi v244,v24 offset:1024\nv_cvt_pk_bf16_f32 v25,0,v25\nds_write_b16_d16_hi v244,v25 offset:1056\nv_cvt_pk_bf16_f32 v26,0,v26\nds_write_b16_d16_hi v244,v26 offset:1088\nv_cvt_pk_bf16_f32 v27,0,v27\n" \
"ds_write_b16_d16_hi v244,v27 offset:1120\nv_cvt_pk_bf16_f32 v28,0,v28\nds_write_b16_d16_hi v244,v28 offset:1152\nv_cvt_pk_bf16_f32 v29,0,v29\nds_write_b16_d16_hi v244,v29 offset:1184\nv_cvt_pk_bf16_f32 v30,0,v30\nds_write_b16_d16_hi v244,v30 offset:1216\nv_cvt_pk_bf16_f32 v31,0,v31\n" \
"ds_write_b16_d16_hi v244,v31 offset:1248\nds_read_b128 v[68:71],v245 offset:1024\ns_waitcnt lgkmcnt(0)\nbuffer_store_dwordx4 v[64:67],v240,s[44:47],s95 offen sc1\nbuffer_store_dwordx4 v[68:71],v240,s[44:47],s74 offen sc1\ns_cmp_eq_u32 s7,0\ns_cselect_b32 s70,1,0\ns_xor_b32 s71,s65,s66\n" \
"s_or_b32 s70,s70,s71\ns_cbranch_scc0 .Lfin\ns_lshr_b32 s70,s6,3\ns_and_b32 s70,s70,15\ns_lshl4_add_u32 s98,s60,s70\nv_lshrrev_b32 v242,4,v249\ns_lshl_b32 s71,s98,2\nv_add_u32 v242,s71,v242\n" \
"v_and_b32 v244,15,v242\nv_bfe_u32 v245,v242,4,3\nv_lshl_add_u32 v244,v244,3,v245\nv_and_b32 v245,0x180,v242\nv_add_lshl_u32 v243,v244,v245,1\nv_lshlrev_b32 v244,1,v243\ns_mov_b32 s70,4096\nv_lshlrev_b32 v242,1,v242\n" \
"buffer_load_dword v62,v244,s[52:55],0 offen offset:0 sc1\nbuffer_load_dword v63,v244,s[52:55],0 offen offset:2048 sc1\nbuffer_load_dword v126,v244,s[52:55],s70 offen offset:0 sc1\nbuffer_load_dword v127,v244,s[52:55],s70 offen offset:2048 sc1\n.Lkp:\ns_cmp_lg_u32 s7,0\ns_cbranch_scc1 .Lkl\nv_max3_u32 v228,v233,v235,v237\n" \
"v_min3_u32 v229,v233,v235,v237\nv_max_u32 v228,v228,v239\nv_min_u32 v229,v229,v239\nv_cmp_lt_u32 vcc,s94,v228\nv_cmp_eq_u32 s[90:91],0,v229\ns_cbranch_vccnz .Lnew\ns_cmp_eq_u64 s[90:91],0\ns_cbranch_scc1 .Lv0\n" \
"s_sub_u32 s73,s73,1\ns_cselect_b32 s73,0,s73\ns_cbranch_scc1 .Lv0\ns_sleep 3\nbuffer_load_dwordx4 v[232:235],v251,s[52:55],s80 offen sc1\nbuffer_load_dwordx4 v[236:239],v251,s[52:55],s80 offen offset:16 sc1\ns_waitcnt vmcnt(0)\ns_branch .Lkp\n" \
".Lv0:\nv_mov_b32 v230,s96\ns_or_b32 s72,s94,2\nv_mov_b32 v231,s72\nds_write_b64 v250,v[230:231] offset:8\ns_branch .Lver\n.Lkl:\nds_read_b64 v[230:231],v250 offset:8\n" \
"s_mov_b32 s91,0\ns_waitcnt lgkmcnt(0)\nv_readfirstlane_b32 s72,v231\nv_readfirstlane_b32 s96,v230\ns_and_b32 s90,s72,2\ns_xor_b32 s72,s72,s90\ns_xor_b32 s90,s90,2\ns_cmp_lg_u32 s72,s94\n" \
"s_mov_b32 s94,s72\ns_cbranch_scc1 .Lsp\ns_cmp_eq_u32 s90,0\ns_cbranch_scc1 .Lver\ns_sub_u32 s73,s73,1\ns_cselect_b32 s73,0,s73\ns_cbranch_scc1 .Lver\ns_sleep 1\n" \
".Lyield:\ns_branch .Lkl\n.Lnew:\nv_mov_b32 v230,v228\nv_cmp_eq_u32 vcc,v228,v233\ns_cmp_eq_u64 s[90:91],0\ns_cselect_b32 s71,2,0\nv_max_u32_dpp v230,v230,v230 quad_perm:[1,0,3,2] row_mask:0xf bank_mask:0xf\n" \
"v_cndmask_b32 v229,v238,v232,vcc\nv_cmp_eq_u32 vcc,v228,v235\nv_max_u32_dpp v230,v230,v230 quad_perm:[2,3,0,1] row_mask:0xf bank_mask:0xf\ns_nop 1\nv_max_u32_dpp v230,v230,v230 row_half_mirror row_mask:0xf bank_mask:0xf\nv_cndmask_b32 v229,v229,v234,vcc\nv_cmp_eq_u32 vcc,v228,v237\nv_max_u32_dpp v230,v230,v230 row_mirror row_mask:0xf bank_mask:0xf\n" \
"v_mov_b32 v231,v230\nv_cndmask_b32 v229,v229,v236,vcc\ns_nop 0\nv_permlane16_swap_b32 v230,v231\nv_max_u32 v230,v230,v231\nv_mov_b32 v231,v230\ns_nop 1\nv_permlane32_swap_b32 v230,v231\n" \
"v_max_u32 v230,v230,v231\ns_nop 0\nv_readfirstlane_b32 s94,v230\ns_nop 1\nv_cmp_eq_u32 vcc,s94,v228\ns_nop 0\ns_ff1_i32_b64 s72,vcc\nv_readlane_b32 s96,v229,s72\n" \
"s_nop 1\nv_mov_b32 v230,s96\ns_or_b32 s72,s94,s71\nv_mov_b32 v231,s72\nds_write_b64 v250,v[230:231] offset:8\n.Lsp:\ns_not_b32 s15,s94\ns_bfe_u32 s15,s15,0xb0005\n" \
"s_lshl_b32 s15,s15,10\ns_cmp_eq_u32 s3,0\ns_cbranch_scc1 .Lkp\nv_add_u32 v70,114688,v241\ns_add_u32 s72,s15,54525952\nbuffer_load_dword v41,v241,s[52:55],s72 offen sc1\ns_add_u32 s72,s15,56623104\nbuffer_load_dword v43,v241,s[52:55],s72 offen sc1\n" \
"s_add_u32 s72,s15,58720256\nbuffer_load_dword v45,v241,s[52:55],s72 offen sc1\ns_add_u32 s72,s15,60817408\nbuffer_load_dword v47,v241,s[52:55],s72 offen sc1\ns_add_u32 s72,s15,62914560\nbuffer_load_dword v105,v241,s[52:55],s72 offen sc1\ns_add_u32 s72,s15,65011712\nbuffer_load_dword v107,v241,s[52:55],s72 offen sc1\n" \
"s_add_u32 s72,s15,67108864\nbuffer_load_dword v109,v241,s[52:55],s72 offen sc1\ns_add_u32 s72,s15,69206016\nbuffer_load_dword v111,v241,s[52:55],s72 offen sc1\nv_add_u32 v66,s15,v248\nglobal_load_ushort v64,v66,s[8:9]\nglobal_load_ushort v65,v66,s[8:9] offset:32\ns_nop 0\n" \
"ds_read_b32 v161,v70 offset:0\nds_read_b32 v163,v70 offset:1024\nds_read_b32 v165,v70 offset:2048\nds_read_b32 v167,v70 offset:3072\nds_read_b32 v169,v70 offset:4096\nds_read_b32 v171,v70 offset:5120\nds_read_b32 v173,v70 offset:6144\nds_read_b32 v175,v70 offset:7168\n" \
"ds_read_b32 v177,v70 offset:8192\nds_read_b32 v179,v70 offset:9216\nds_read_b32 v181,v70 offset:10240\nds_read_b32 v183,v70 offset:11264\nds_read_b32 v185,v70 offset:12288\nds_read_b32 v187,v70 offset:13312\nds_read_b32 v189,v70 offset:14336\nds_read_b32 v191,v70 offset:15360\n" \
"s_waitcnt lgkmcnt(15)\nv_lshlrev_b32 v160,16,v161\nv_and_b32 v161,0xffff0000,v161\ns_waitcnt lgkmcnt(14)\nv_lshlrev_b32 v162,16,v163\nv_and_b32 v163,0xffff0000,v163\ns_waitcnt lgkmcnt(13)\nv_lshlrev_b32 v164,16,v165\n" \
"v_and_b32 v165,0xffff0000,v165\ns_waitcnt lgkmcnt(12)\nv_lshlrev_b32 v166,16,v167\nv_and_b32 v167,0xffff0000,v167\ns_waitcnt lgkmcnt(11)\nv_lshlrev_b32 v168,16,v169\nv_and_b32 v169,0xffff0000,v169\ns_waitcnt lgkmcnt(10)\n" \
"v_lshlrev_b32 v170,16,v171\nv_and_b32 v171,0xffff0000,v171\ns_waitcnt lgkmcnt(9)\nv_lshlrev_b32 v172,16,v173\nv_and_b32 v173,0xffff0000,v173\ns_waitcnt lgkmcnt(8)\nv_lshlrev_b32 v174,16,v175\nv_and_b32 v175,0xffff0000,v175\n" \
"s_waitcnt lgkmcnt(7)\nv_lshlrev_b32 v176,16,v177\nv_and_b32 v177,0xffff0000,v177\ns_waitcnt lgkmcnt(6)\nv_lshlrev_b32 v178,16,v179\nv_and_b32 v179,0xffff0000,v179\ns_waitcnt lgkmcnt(5)\nv_lshlrev_b32 v180,16,v181\n" \
"v_and_b32 v181,0xffff0000,v181\ns_waitcnt lgkmcnt(4)\nv_lshlrev_b32 v182,16,v183\nv_and_b32 v183,0xffff0000,v183\ns_waitcnt lgkmcnt(3)\nv_lshlrev_b32 v184,16,v185\nv_and_b32 v185,0xffff0000,v185\ns_waitcnt lgkmcnt(1)\n" \
"v_lshlrev_b32 v186,16,v187\nv_and_b32 v187,0xffff0000,v187\nv_lshlrev_b32 v188,16,v189\nv_and_b32 v189,0xffff0000,v189\ns_waitcnt lgkmcnt(0)\nv_lshlrev_b32 v190,16,v191\nv_and_b32 v191,0xffff0000,v191\ns_waitcnt vmcnt(0)\n" \
"buffer_load_dwordx4 v[232:235],v251,s[52:55],s80 offen sc1\nbuffer_load_dwordx4 v[236:239],v251,s[52:55],s80 offen offset:16 sc1\ns_and_b32 s82,s96,0xffff0000\ns_lshl_b32 s83,s96,16\nv_rcp_f32 v68,s83\nv_lshlrev_b32 v40,16,v41\nv_and_b32 v41,0xffff0000,v41\nv_lshlrev_b32 v42,16,v43\n" \
"v_and_b32 v43,0xffff0000,v43\nv_lshlrev_b32 v44,16,v45\nv_and_b32 v45,0xffff0000,v45\nv_lshlrev_b32 v46,16,v47\nv_and_b32 v47,0xffff0000,v47\nv_lshlrev_b32 v104,16,v105\nv_and_b32 v105,0xffff0000,v105\nv_lshlrev_b32 v106,16,v107\n" \
"v_and_b32 v107,0xffff0000,v107\nv_lshlrev_b32 v108,16,v109\nv_and_b32 v109,0xffff0000,v109\nv_lshlrev_b32 v110,16,v111\nv_and_b32 v111,0xffff0000,v111\nv_pk_add_f32 v[40:41],v[40:41],v[42:43]\nv_pk_add_f32 v[44:45],v[44:45],v[46:47]\nv_pk_add_f32 v[104:105],v[104:105],v[106:107]\n" \
"v_pk_add_f32 v[108:109],v[108:109],v[110:111]\nv_pk_add_f32 v[40:41],v[40:41],v[44:45]\nv_pk_add_f32 v[104:105],v[104:105],v[108:109]\nv_pk_add_f32 v[40:41],v[40:41],v[104:105]\nv_lshlrev_b32 v64,16,v64\nv_lshlrev_b32 v65,16,v65\nv_cvt_pk_bf16_f32 v40,0,v40\nv_cvt_pk_bf16_f32 v41,0,v41\n" \
"v_pk_mul_f32 v[40:41],v[40:41],v[112:113]\nv_cvt_pk_bf16_f32 v40,0,v40\nv_cvt_pk_bf16_f32 v41,0,v41\nv_pk_add_f32 v[40:41],v[40:41],v[64:65]\nv_cvt_pk_bf16_f32 v40,0,v40\nv_cvt_pk_bf16_f32 v41,0,v41\nv_pk_add_f32 v[40:41],v[40:41],s[82:83] op_sel_hi:[1,0] neg_lo:[0,1] neg_hi:[0,1]\nv_cvt_pk_bf16_f32 v40,0,v40\n" \
"v_cvt_pk_bf16_f32 v41,0,v41\nv_pk_mul_f32 v[40:41],v[40:41],v[68:69] op_sel_hi:[1,0]\nv_cvt_pk_bf16_f32 v40,0,v40\nv_cvt_pk_bf16_f32 v41,0,v41\nv_pk_mul_f32 v[40:41],v[40:41],v[116:117]\nv_cvt_pk_bf16_f32 v40,0,v40\nv_cvt_pk_bf16_f32 v41,0,v41\nv_pk_add_f32 v[40:41],v[40:41],v[114:115]\n" \
"v_cvt_pk_bf16_f32 v40,v40,v41\ns_nop 1\nv_mov_b32_dpp v44,v40 quad_perm:[0,0,0,0] row_mask:0xf bank_mask:0xf\nv_mov_b32_dpp v45,v40 quad_perm:[1,1,1,1] row_mask:0xf bank_mask:0xf\nv_mov_b32_dpp v46,v40 quad_perm:[2,2,2,2] row_mask:0xf bank_mask:0xf\nv_mov_b32_dpp v47,v40 quad_perm:[3,3,3,3] row_mask:0xf bank_mask:0xf\nv_dot2_f32_bf16 v120,v192,v44,0\nv_dot2_f32_bf16 v121,v193,v44,0\n" \
"v_dot2_f32_bf16 v122,v194,v44,0\nv_dot2_f32_bf16 v123,v195,v44,0\nv_dot2_f32_bf16 v124,v208,v44,0\nv_dot2_f32_bf16 v125,v209,v44,0\nv_dot2_f32_bf16 v126,v210,v44,0\nv_dot2_f32_bf16 v127,v211,v44,0\nv_dot2_f32_bf16 v120,v196,v45,v120\nv_dot2_f32_bf16 v121,v197,v45,v121\n" \
"v_dot2_f32_bf16 v122,v198,v45,v122\nv_dot2_f32_bf16 v123,v199,v45,v123\nv_dot2_f32_bf16 v124,v212,v45,v124\nv_dot2_f32_bf16 v125,v213,v45,v125\nv_dot2_f32_bf16 v126,v214,v45,v126\nv_dot2_f32_bf16 v127,v215,v45,v127\nv_dot2_f32_bf16 v120,v200,v46,v120\nv_dot2_f32_bf16 v121,v201,v46,v121\n" \
"v_dot2_f32_bf16 v122,v202,v46,v122\nv_dot2_f32_bf16 v123,v203,v46,v123\nv_dot2_f32_bf16 v124,v216,v46,v124\nv_dot2_f32_bf16 v125,v217,v46,v125\nv_dot2_f32_bf16 v126,v218,v46,v126\nv_dot2_f32_bf16 v127,v219,v46,v127\nv_dot2_f32_bf16 v120,v204,v47,v120\nv_dot2_f32_bf16 v121,v205,v47,v121\n" \
"v_dot2_f32_bf16 v122,v206,v47,v122\nv_dot2_f32_bf16 v123,v207,v47,v123\nv_dot2_f32_bf16 v124,v220,v47,v124\nv_dot2_f32_bf16 v125,v221,v47,v125\nv_dot2_f32_bf16 v126,v222,v47,v126\nv_dot2_f32_bf16 v127,v223,v47,v127\nv_permlane32_swap_b32 v120,v124\nv_add_f32 v120,v120,v124\n" \
"v_permlane32_swap_b32 v121,v125\nv_add_f32 v121,v121,v125\nv_permlane32_swap_b32 v122,v126\nv_add_f32 v122,v122,v126\nv_permlane32_swap_b32 v123,v127\nv_add_f32 v123,v123,v127\nv_permlane16_swap_b32 v120,v122\nv_add_f32 v120,v120,v122\n" \
"v_permlane16_swap_b32 v121,v123\nv_add_f32 v121,v121,v123\nv_add_f32_dpp v120,v120,v120 row_ror:4 row_mask:0xf bank_mask:0xf\ns_nop 1\nv_add_f32_dpp v120,v120,v120 row_ror:8 row_mask:0xf bank_mask:0xf\nv_add_f32_dpp v121,v121,v121 row_ror:4 row_mask:0xf bank_mask:0xf\ns_nop 1\nv_add_f32_dpp v121,v121,v121 row_ror:8 row_mask:0xf bank_mask:0xf\n" \
"s_cmp_eq_u64 s[90:91],0\ns_cbranch_scc1 .Lver\ns_waitcnt vmcnt(0)\ns_branch .Lkp\n.Lver:\ns_cmp_eq_u32 s3,0\ns_cbranch_scc1 .Lnf\nds_write_b32 v252,v120\n" \
"ds_write_b32 v252,v121 offset:16\ns_lshr_b32 s70,s2,1\nv_and_b32 v64,4,v241\nv_lshrrev_b32 v241,3,v241\nv_lshlrev_b32 v241,11,v241\nv_lshl_or_b32 v241,v64,1,v241\ns_add_u32 s70,s70,98304\ns_waitcnt lgkmcnt(0)\n" \
"s_barrier\nds_read_b128 v[40:43],v253\nds_read_b128 v[44:47],v253 offset:256\ns_waitcnt lgkmcnt(1)\nv_pk_add_f32 v[40:41],v[40:41],v[42:43]\nv_add_f32 v40,v40,v41\nv_cvt_pk_bf16_f32 v40,0,v40\nv_mul_f32 v41,0xbfb8aa3b,v40\n" \
"v_exp_f32 v41,v41\ns_waitcnt lgkmcnt(0)\nv_pk_add_f32 v[44:45],v[44:45],v[46:47]\nv_add_f32 v41,1.0,v41\nv_rcp_f32 v41,v41\nv_add_f32 v44,v44,v45\nv_cvt_pk_bf16_f32 v44,0,v44\nv_mul_f32 v40,v40,v41\n" \
"v_cvt_pk_bf16_f32 v40,0,v40\nv_mul_f32 v40,v40,v44\nv_cvt_pk_bf16_f32 v40,0,v40\ns_nop 0\nv_readlane_b32 s8,v40,0\nv_readlane_b32 s9,v40,1\nv_readlane_b32 s10,v40,2\nv_readlane_b32 s11,v40,3\n" \
"v_readlane_b32 s12,v40,4\nv_readlane_b32 s13,v40,5\nv_readlane_b32 s14,v40,6\nv_readlane_b32 s15,v40,7\nv_readlane_b32 s16,v40,8\nv_readlane_b32 s17,v40,9\nv_readlane_b32 s18,v40,10\nv_readlane_b32 s19,v40,11\n" \
"v_readlane_b32 s20,v40,12\nv_readlane_b32 s21,v40,13\nv_readlane_b32 s22,v40,14\nv_readlane_b32 s23,v40,15\nv_pk_mul_f32 v[120:121],v[160:161],s[8:9] op_sel_hi:[1,0]\nv_pk_mul_f32 v[122:123],v[162:163],s[8:9] op_sel:[0,1]\nv_pk_fma_f32 v[120:121],v[164:165],s[10:11],v[120:121] op_sel_hi:[1,0,1]\nv_pk_fma_f32 v[122:123],v[166:167],s[10:11],v[122:123] op_sel:[0,1,0]\n" \
"v_pk_fma_f32 v[120:121],v[168:169],s[12:13],v[120:121] op_sel_hi:[1,0,1]\nv_pk_fma_f32 v[122:123],v[170:171],s[12:13],v[122:123] op_sel:[0,1,0]\nv_pk_fma_f32 v[120:121],v[172:173],s[14:15],v[120:121] op_sel_hi:[1,0,1]\nv_pk_fma_f32 v[122:123],v[174:175],s[14:15],v[122:123] op_sel:[0,1,0]\nv_pk_fma_f32 v[120:121],v[176:177],s[16:17],v[120:121] op_sel_hi:[1,0,1]\nv_pk_fma_f32 v[122:123],v[178:179],s[16:17],v[122:123] op_sel:[0,1,0]\nv_pk_fma_f32 v[120:121],v[180:181],s[18:19],v[120:121] op_sel_hi:[1,0,1]\nv_pk_fma_f32 v[122:123],v[182:183],s[18:19],v[122:123] op_sel:[0,1,0]\n" \
"v_pk_fma_f32 v[120:121],v[184:185],s[20:21],v[120:121] op_sel_hi:[1,0,1]\nv_pk_fma_f32 v[122:123],v[186:187],s[20:21],v[122:123] op_sel:[0,1,0]\nv_pk_fma_f32 v[120:121],v[188:189],s[22:23],v[120:121] op_sel_hi:[1,0,1]\nv_pk_fma_f32 v[122:123],v[190:191],s[22:23],v[122:123] op_sel:[0,1,0]\nv_pk_add_f32 v[120:121],v[120:121],v[122:123]\nv_min_u32 v120,-2,v120\nv_min_u32 v121,-2,v121\nbuffer_store_dwordx2 v[120:121],v241,s[52:55],s70 offen sc1\n" \
"v_pk_mul_f32 v[88:89],v[88:89],v[48:49]\nv_pk_mul_f32 v[90:91],v[90:91],v[50:51]\nv_pk_mul_f32 v[92:93],v[92:93],v[52:53]\nv_pk_mul_f32 v[94:95],v[94:95],v[54:55]\nv_cvt_pk_bf16_f32 v88,0,v88\nv_cvt_pk_bf16_f32 v89,0,v89\nv_cvt_pk_bf16_f32 v90,0,v90\nv_cvt_pk_bf16_f32 v91,0,v91\n" \
"v_cvt_pk_bf16_f32 v92,0,v92\nv_cvt_pk_bf16_f32 v93,0,v93\nv_cvt_pk_bf16_f32 v94,0,v94\nv_cvt_pk_bf16_f32 v95,0,v95\nv_pk_add_f32 v[88:89],v[88:89],v[72:73]\nv_pk_add_f32 v[90:91],v[90:91],v[74:75]\nv_pk_add_f32 v[92:93],v[92:93],v[76:77]\nv_pk_add_f32 v[94:95],v[94:95],v[78:79]\n" \
"v_cvt_pk_bf16_f32 v88,0,v88\nds_write_b16_d16_hi v244,v88 offset:0\nv_cvt_pk_bf16_f32 v89,0,v89\nds_write_b16_d16_hi v244,v89 offset:32\nv_cvt_pk_bf16_f32 v90,0,v90\nds_write_b16_d16_hi v244,v90 offset:64\nv_cvt_pk_bf16_f32 v91,0,v91\nds_write_b16_d16_hi v244,v91 offset:96\n" \
"v_cvt_pk_bf16_f32 v92,0,v92\nds_write_b16_d16_hi v244,v92 offset:128\nv_cvt_pk_bf16_f32 v93,0,v93\nds_write_b16_d16_hi v244,v93 offset:160\nv_cvt_pk_bf16_f32 v94,0,v94\nds_write_b16_d16_hi v244,v94 offset:192\nv_cvt_pk_bf16_f32 v95,0,v95\nds_write_b16_d16_hi v244,v95 offset:224\n" \
"ds_read_b128 v[64:67],v245 offset:0\nv_pk_mul_f32 v[24:25],v[24:25],v[48:49]\nv_pk_mul_f32 v[26:27],v[26:27],v[50:51]\nv_pk_mul_f32 v[28:29],v[28:29],v[52:53]\nv_pk_mul_f32 v[30:31],v[30:31],v[54:55]\nv_cvt_pk_bf16_f32 v24,0,v24\nv_cvt_pk_bf16_f32 v25,0,v25\nv_cvt_pk_bf16_f32 v26,0,v26\n" \
"v_cvt_pk_bf16_f32 v27,0,v27\nv_cvt_pk_bf16_f32 v28,0,v28\nv_cvt_pk_bf16_f32 v29,0,v29\nv_cvt_pk_bf16_f32 v30,0,v30\nv_cvt_pk_bf16_f32 v31,0,v31\nv_pk_add_f32 v[24:25],v[24:25],v[8:9]\nv_pk_add_f32 v[26:27],v[26:27],v[10:11]\nv_pk_add_f32 v[28:29],v[28:29],v[12:13]\n" \
"v_pk_add_f32 v[30:31],v[30:31],v[14:15]\nv_cvt_pk_bf16_f32 v24,0,v24\nds_write_b16_d16_hi v244,v24 offset:1024\nv_cvt_pk_bf16_f32 v25,0,v25\nds_write_b16_d16_hi v244,v25 offset:1056\nv_cvt_pk_bf16_f32 v26,0,v26\nds_write_b16_d16_hi v244,v26 offset:1088\nv_cvt_pk_bf16_f32 v27,0,v27\n" \
"ds_write_b16_d16_hi v244,v27 offset:1120\nv_cvt_pk_bf16_f32 v28,0,v28\nds_write_b16_d16_hi v244,v28 offset:1152\nv_cvt_pk_bf16_f32 v29,0,v29\nds_write_b16_d16_hi v244,v29 offset:1184\nv_cvt_pk_bf16_f32 v30,0,v30\nds_write_b16_d16_hi v244,v30 offset:1216\nv_cvt_pk_bf16_f32 v31,0,v31\n" \
"ds_write_b16_d16_hi v244,v31 offset:1248\nds_read_b128 v[68:71],v245 offset:1024\ns_waitcnt lgkmcnt(0)\nbuffer_store_dwordx4 v[64:67],v240,s[44:47],s95 offen sc1\nbuffer_store_dwordx4 v[68:71],v240,s[44:47],s74 offen sc1\n.Lnf:\ns_cmp_eq_u32 s94,s97\ns_cbranch_scc1 .Lwin\n" \
"buffer_store_dwordx4 v[64:67],v240,s[44:47],s65 offen sc1\nbuffer_store_dwordx4 v[68:71],v240,s[44:47],s66 offen sc1\n.Lwin:\ns_cmp_lg_u32 s38,0\ns_cbranch_scc1 .Lfin\ns_add_u32 s72,s15,54525952\nbuffer_load_ushort v128,v243,s[52:55],s72 offen sc1\ns_add_u32 s72,s15,56623104\n" \
"buffer_load_ushort v129,v243,s[52:55],s72 offen sc1\ns_add_u32 s72,s15,58720256\nbuffer_load_ushort v130,v243,s[52:55],s72 offen sc1\ns_add_u32 s72,s15,60817408\nbuffer_load_ushort v131,v243,s[52:55],s72 offen sc1\ns_add_u32 s72,s15,62914560\nbuffer_load_ushort v132,v243,s[52:55],s72 offen sc1\ns_add_u32 s72,s15,65011712\n" \
"buffer_load_ushort v133,v243,s[52:55],s72 offen sc1\ns_add_u32 s72,s15,67108864\nbuffer_load_ushort v134,v243,s[52:55],s72 offen sc1\ns_add_u32 s72,s15,69206016\nbuffer_load_ushort v135,v243,s[52:55],s72 offen sc1\nv_add_u32 v242,s15,v242\nglobal_load_ushort v144,v242,s[8:9]\nv_min_u32 v168,55,v249\n" \
"v_lshlrev_b32 v168,4,v168\ns_lshl_b32 s70,s98,11\ns_add_u32 s70,s70,98304\ns_add_u32 s71,s70,896\ns_and_b32 s82,s96,0xffff0000\ns_lshl_b32 s83,s96,16\nv_rcp_f32 v148,s83\ns_ashr_i32 s37,s94,31\n" \
"s_not_b32 s37,s37\ns_or_b32 s37,s37,0x80000000\ns_xor_b32 s37,s37,s94\ns_and_b32 s37,s37,0xffff0000\nv_mov_b32 v149,0xbfb8aa3b\nv_mul_f32 v149,s37,v149\nv_exp_f32 v149,v149\nv_and_b32 v150,15,v249\n" \
"v_add_f32 v149,1.0,v149\nv_rcp_f32 v149,v149\nv_cmp_eq_u32 vcc,0,v150\nv_cvt_pk_bf16_f32 v149,0,v149\nv_mov_b32 v150,0x40000000\nv_cndmask_b32 v150,v150,v242,vcc\nv_cmp_gt_u32 s[90:91],56,v249\ns_waitcnt vmcnt(8)\n" \
"v_lshlrev_b32 v128,16,v128\ns_waitcnt vmcnt(7)\nv_lshlrev_b32 v129,16,v129\nv_add_f32 v128,v128,v129\ns_waitcnt vmcnt(6)\nv_lshlrev_b32 v130,16,v130\ns_waitcnt vmcnt(5)\nv_lshlrev_b32 v131,16,v131\n" \
"v_add_f32 v130,v130,v131\nv_add_f32 v128,v128,v130\ns_waitcnt vmcnt(4)\nv_lshlrev_b32 v132,16,v132\ns_waitcnt vmcnt(3)\nv_lshlrev_b32 v133,16,v133\nv_add_f32 v132,v132,v133\ns_waitcnt vmcnt(2)\n" \
"v_lshlrev_b32 v134,16,v134\ns_waitcnt vmcnt(1)\nv_lshlrev_b32 v135,16,v135\nv_add_f32 v134,v134,v135\nv_add_f32 v132,v132,v134\nv_add_f32 v128,v128,v132\nv_cvt_pk_bf16_f32 v128,0,v128\nv_mul_f32 v128,v128,v62\n" \
"v_cvt_pk_bf16_f32 v128,0,v128\ns_waitcnt vmcnt(0)\nv_add_f32 v126,1.0,v126\nv_cvt_pk_bf16_f32 v126,0,v126\nv_lshlrev_b32 v144,16,v144\nv_add_f32 v128,v128,v144\nv_cvt_pk_bf16_f32 v128,0,v128\nv_subrev_f32 v129,s82,v128\n" \
"v_cvt_pk_bf16_f32 v129,0,v129\nv_mul_f32 v129,v129,v148\nv_cvt_pk_bf16_f32 v129,0,v129\nv_mul_f32 v129,v129,v126\nv_cvt_pk_bf16_f32 v129,0,v129\nv_add_f32 v129,v129,v63\nv_cvt_pk_bf16_f32 v129,0,v129\n.Lp2:\n" \
"buffer_load_dwordx4 v[160:163],v168,s[52:55],s70 offen sc1\nbuffer_load_dwordx4 v[164:167],v168,s[52:55],s71 offen sc1\ns_waitcnt vmcnt(0)\nv_max3_u32 v228,v160,v161,v162\nv_max3_u32 v229,v163,v164,v165\nv_max3_u32 v228,v228,v166,v167\nv_max_u32 v228,v228,v229\nv_pk_add_f32 v[160:161],v[160:161],v[164:165]\n" \
"v_cmp_eq_u32 vcc,-1,v228\nv_pk_add_f32 v[162:163],v[162:163],v[166:167]\ns_sub_u32 s73,s73,1\ns_cselect_b32 s73,0,s73\ns_cselect_b64 vcc,0,vcc\ns_cmp_lg_u64 vcc,0\ns_cselect_b32 s72,0x8000000,0\nv_cndmask_b32 v160,0,v160,s[90:91]\n" \
"v_cndmask_b32 v161,0,v161,s[90:91]\nv_cndmask_b32 v162,0,v162,s[90:91]\nv_cndmask_b32 v163,0,v163,s[90:91]\ns_nop 0\nv_permlane32_swap_b32 v160,v162\nv_permlane32_swap_b32 v161,v163\nv_pk_add_f32 v[160:161],v[160:161],v[162:163]\ns_nop 1\n" \
"v_permlane16_swap_b32 v160,v161\nv_add_f32 v160,v160,v161\ns_nop 1\nv_add_f32_dpp v160,v160,v160 quad_perm:[1,0,3,2] row_mask:0xf bank_mask:0xf\ns_nop 1\nv_add_f32_dpp v160,v160,v160 quad_perm:[2,3,0,1] row_mask:0xf bank_mask:0xf\ns_nop 1\nv_add_f32_dpp v160,v160,v160 row_half_mirror row_mask:0xf bank_mask:0xf\n" \
"s_nop 1\nv_add_f32_dpp v160,v160,v160 row_mirror row_mask:0xf bank_mask:0xf\nv_cvt_pk_bf16_f32 v160,0,v160\nv_mul_f32 v160,v160,v149\nv_cvt_pk_bf16_f32 v160,0,v160\nv_add_f32 v160,v160,v129\nv_cvt_pk_bf16_f32 v160,0,v160\nv_mul_f32 v160,v160,v127\n" \
"v_cvt_pk_bf16_f32 v160,0,v160\nv_add_f32 v160,v160,v128\nv_cvt_pk_bf16_f32 v160,0,v160\nbuffer_store_short_d16_hi v160,v150,s[44:47],s72 offen sc1\n.Lp2s:\ns_cbranch_scc1 .Lp2\ns_branch .Lfin\n.Lhs:\n" \
"s_sub_u32 s73,s73,1\ns_cselect_b32 s73,0,s73\ns_cbranch_scc1 .Lhb\ns_sleep 2\nbuffer_load_dwordx2 v[166:167],v165,s[52:55],s80 offen sc1\ns_waitcnt vmcnt(0)\nv_xor_b32 v166,v166,v230\nv_xor_b32 v167,v167,v231\n" \
"v_or_b32 v166,v166,v167\nv_cmp_ne_u32 vcc,0,v166\ns_nop 0\ns_cbranch_vccnz .Lhs\ns_branch .Lhb\n.Lps0:\ns_sub_u32 s73,s73,1\ns_cselect_b32 s73,0,s73\n" \
"s_cbranch_scc1 .Lpb0\ns_sleep 1\nbuffer_load_dword v248,v228,s[52:55],s80 offen sc1\ns_waitcnt vmcnt(0)\nv_cmp_eq_u32 vcc,0,v248\ns_nop 0\ns_cbranch_vccnz .Lps0\ns_branch .Lpb0\n" \
".Lps1:\ns_sub_u32 s73,s73,1\ns_cselect_b32 s73,0,s73\ns_cbranch_scc1 .Lpb1\ns_sleep 1\nbuffer_load_dword v248,v228,s[52:55],s80 offen sc1\ns_waitcnt vmcnt(0)\nv_cmp_eq_u32 vcc,0,v248\n" \
"s_nop 0\ns_cbranch_vccnz .Lps1\ns_branch .Lpb1\n.Lps2:\ns_sub_u32 s73,s73,1\ns_cselect_b32 s73,0,s73\ns_cbranch_scc1 .Lpb2\ns_sleep 1\n" \
"buffer_load_dword v248,v251,s[52:55],s80 offen sc1\ns_waitcnt vmcnt(0)\nv_cmp_eq_u32 vcc,0,v248\ns_nop 0\ns_cbranch_vccnz .Lps2\ns_branch .Lpb2\n.Lps3:\ns_sub_u32 s73,s73,1\n" \
"s_cselect_b32 s73,0,s73\ns_cbranch_scc1 .Lpb3\ns_sleep 1\nbuffer_load_dword v248,v251,s[52:55],s80 offen sc1\ns_waitcnt vmcnt(0)\nv_cmp_eq_u32 vcc,0,v248\ns_nop 0\ns_cbranch_vccnz .Lps3\n" \
"s_branch .Lpb3\n.Lps4:\ns_sub_u32 s73,s73,1\ns_cselect_b32 s73,0,s73\ns_cbranch_scc1 .Lpb4\ns_sleep 1\nbuffer_load_dword v248,v251,s[52:55],s80 offen sc1\ns_waitcnt vmcnt(0)\n" \
"v_cmp_eq_u32 vcc,0,v248\ns_nop 0\ns_cbranch_vccnz .Lps4\ns_branch .Lpb4\n.Lps5:\ns_sub_u32 s73,s73,1\ns_cselect_b32 s73,0,s73\ns_cbranch_scc1 .Lpb5\n" \
"s_sleep 1\nbuffer_load_dword v248,v251,s[52:55],s80 offen sc1\ns_waitcnt vmcnt(0)\nv_cmp_eq_u32 vcc,0,v248\ns_nop 0\ns_cbranch_vccnz .Lps5\ns_branch .Lpb5\n.Lps6:\n" \
"s_sub_u32 s73,s73,1\ns_cselect_b32 s73,0,s73\ns_cbranch_scc1 .Lpb6\ns_sleep 1\nbuffer_load_dword v248,v251,s[52:55],s80 offen sc1\ns_waitcnt vmcnt(0)\nv_and_b32 v248,s17,v248\nv_cmp_eq_u32 vcc,0,v248\n" \
"s_nop 0\ns_cbranch_vccnz .Lps6\ns_branch .Lpb6\n.Lps7:\ns_sub_u32 s73,s73,1\ns_cselect_b32 s73,0,s73\ns_cbranch_scc1 .Lpb7\ns_sleep 1\n" \
"buffer_load_dword v248,v254,s[52:55],s80 offen sc1\ns_waitcnt vmcnt(0)\nv_cmp_eq_u32 vcc,0,v248\ns_nop 0\ns_cbranch_vccnz .Lps7\ns_branch .Lpb7\n.Lps8:\ns_sub_u32 s73,s73,1\n" \
"s_cselect_b32 s73,0,s73\ns_cbranch_scc1 .Lpb8\ns_sleep 1\nbuffer_load_dword v248,v251,s[52:55],s79 offen sc1\ns_waitcnt vmcnt(0)\nv_cmp_eq_u32 vcc,0,v248\ns_nop 0\ns_cbranch_vccnz .Lps8\n" \
"s_branch .Lpb8\n.Lrs1:\ns_mov_b32 s71,0\n.Lrl1:\ns_mov_b32 s97,0\nv_max3_u32 v124,v160,v161,v162\nv_max3_u32 v124,v124,v163,v163\nv_cmp_eq_u32 vcc,-1,v124\n" \
"s_add_u32 s72,s95,0\ns_cbranch_vccz .Lrz10\ns_mov_b64 exec,vcc\ns_mov_b32 s97,1\nbuffer_load_dwordx4 v[160:163],v240,s[52:55],s72 offen sc1\ns_mov_b64 exec,-1\n.Lrz10:\nv_max3_u32 v124,v164,v165,v166\n" \
"v_max3_u32 v124,v124,v167,v167\nv_cmp_eq_u32 vcc,-1,v124\ns_add_u32 s72,s95,2097152\ns_cbranch_vccz .Lrz11\ns_mov_b64 exec,vcc\ns_mov_b32 s97,1\nbuffer_load_dwordx4 v[164:167],v240,s[52:55],s72 offen sc1\ns_mov_b64 exec,-1\n" \
".Lrz11:\nv_max3_u32 v124,v168,v169,v170\nv_max3_u32 v124,v124,v171,v171\nv_cmp_eq_u32 vcc,-1,v124\ns_add_u32 s72,s95,4194304\ns_cbranch_vccz .Lrz12\ns_mov_b64 exec,vcc\ns_mov_b32 s97,1\n" \
"buffer_load_dwordx4 v[168:171],v240,s[52:55],s72 offen sc1\ns_mov_b64 exec,-1\n.Lrz12:\nv_max3_u32 v124,v172,v173,v174\nv_max3_u32 v124,v124,v175,v175\nv_cmp_eq_u32 vcc,-1,v124\ns_add_u32 s72,s95,6291456\ns_cbranch_vccz .Lrz13\n" \
"s_mov_b64 exec,vcc\ns_mov_b32 s97,1\nbuffer_load_dwordx4 v[172:175],v240,s[52:55],s72 offen sc1\ns_mov_b64 exec,-1\n.Lrz13:\nv_max3_u32 v124,v176,v177,v178\nv_max3_u32 v124,v124,v179,v179\nv_cmp_eq_u32 vcc,-1,v124\n" \
"s_add_u32 s72,s95,8388608\ns_cbranch_vccz .Lrz14\ns_mov_b64 exec,vcc\ns_mov_b32 s97,1\nbuffer_load_dwordx4 v[176:179],v240,s[52:55],s72 offen sc1\ns_mov_b64 exec,-1\n.Lrz14:\nv_max3_u32 v124,v180,v181,v182\n" \
"v_max3_u32 v124,v124,v183,v183\nv_cmp_eq_u32 vcc,-1,v124\ns_add_u32 s72,s95,10485760\ns_cbranch_vccz .Lrz15\ns_mov_b64 exec,vcc\ns_mov_b32 s97,1\nbuffer_load_dwordx4 v[180:183],v240,s[52:55],s72 offen sc1\ns_mov_b64 exec,-1\n" \
".Lrz15:\nv_max3_u32 v124,v184,v185,v186\nv_max3_u32 v124,v124,v187,v187\nv_cmp_eq_u32 vcc,-1,v124\ns_add_u32 s72,s95,12582912\ns_cbranch_vccz .Lrz16\ns_mov_b64 exec,vcc\ns_mov_b32 s97,1\n" \
"buffer_load_dwordx4 v[184:187],v240,s[52:55],s72 offen sc1\ns_mov_b64 exec,-1\n.Lrz16:\nv_max3_u32 v124,v188,v189,v190\nv_max3_u32 v124,v124,v191,v191\nv_cmp_eq_u32 vcc,-1,v124\ns_add_u32 s72,s95,14680064\ns_cbranch_vccz .Lrz17\n" \
"s_mov_b64 exec,vcc\ns_mov_b32 s97,1\nbuffer_load_dwordx4 v[188:191],v240,s[52:55],s72 offen sc1\ns_mov_b64 exec,-1\n.Lrz17:\ns_cmp_lg_u32 s97,0\ns_cbranch_scc1 .Lrm1\ns_cmp_eq_u32 s71,0\n" \
"s_cbranch_scc1 .Lrb1\ns_branch .Lrow1\n.Lrm1:\ns_mov_b32 s71,1\ns_waitcnt vmcnt(0)\ns_sub_u32 s73,s73,1\ns_cselect_b32 s73,0,s73\ns_cbranch_scc1 .Lrb1\n" \
"s_sleep 2\ns_branch .Lrl1\n.Lrs0:\ns_mov_b32 s71,0\n.Lrl0:\ns_mov_b32 s97,0\nv_max3_u32 v60,v128,v129,v130\nv_max3_u32 v60,v60,v131,v131\n" \
"v_cmp_eq_u32 vcc,-1,v60\ns_add_u32 s72,s74,0\ns_cbranch_vccz .Lrz00\ns_mov_b64 exec,vcc\ns_mov_b32 s97,1\nbuffer_load_dwordx4 v[128:131],v240,s[52:55],s72 offen sc1\ns_mov_b64 exec,-1\n.Lrz00:\n" \
"v_max3_u32 v60,v132,v133,v134\nv_max3_u32 v60,v60,v135,v135\nv_cmp_eq_u32 vcc,-1,v60\ns_add_u32 s72,s74,2097152\ns_cbranch_vccz .Lrz01\ns_mov_b64 exec,vcc\ns_mov_b32 s97,1\nbuffer_load_dwordx4 v[132:135],v240,s[52:55],s72 offen sc1\n" \
"s_mov_b64 exec,-1\n.Lrz01:\nv_max3_u32 v60,v136,v137,v138\nv_max3_u32 v60,v60,v139,v139\nv_cmp_eq_u32 vcc,-1,v60\ns_add_u32 s72,s74,4194304\ns_cbranch_vccz .Lrz02\ns_mov_b64 exec,vcc\n" \
"s_mov_b32 s97,1\nbuffer_load_dwordx4 v[136:139],v240,s[52:55],s72 offen sc1\ns_mov_b64 exec,-1\n.Lrz02:\nv_max3_u32 v60,v140,v141,v142\nv_max3_u32 v60,v60,v143,v143\nv_cmp_eq_u32 vcc,-1,v60\ns_add_u32 s72,s74,6291456\n" \
"s_cbranch_vccz .Lrz03\ns_mov_b64 exec,vcc\ns_mov_b32 s97,1\nbuffer_load_dwordx4 v[140:143],v240,s[52:55],s72 offen sc1\ns_mov_b64 exec,-1\n.Lrz03:\nv_max3_u32 v60,v144,v145,v146\nv_max3_u32 v60,v60,v147,v147\n" \
"v_cmp_eq_u32 vcc,-1,v60\ns_add_u32 s72,s74,8388608\ns_cbranch_vccz .Lrz04\ns_mov_b64 exec,vcc\ns_mov_b32 s97,1\nbuffer_load_dwordx4 v[144:147],v240,s[52:55],s72 offen sc1\ns_mov_b64 exec,-1\n.Lrz04:\n" \
"v_max3_u32 v60,v148,v149,v150\nv_max3_u32 v60,v60,v151,v151\nv_cmp_eq_u32 vcc,-1,v60\ns_add_u32 s72,s74,10485760\ns_cbranch_vccz .Lrz05\ns_mov_b64 exec,vcc\ns_mov_b32 s97,1\nbuffer_load_dwordx4 v[148:151],v240,s[52:55],s72 offen sc1\n" \
"s_mov_b64 exec,-1\n.Lrz05:\nv_max3_u32 v60,v152,v153,v154\nv_max3_u32 v60,v60,v155,v155\nv_cmp_eq_u32 vcc,-1,v60\ns_add_u32 s72,s74,12582912\ns_cbranch_vccz .Lrz06\ns_mov_b64 exec,vcc\n" \
"s_mov_b32 s97,1\nbuffer_load_dwordx4 v[152:155],v240,s[52:55],s72 offen sc1\ns_mov_b64 exec,-1\n.Lrz06:\nv_max3_u32 v60,v156,v157,v158\nv_max3_u32 v60,v60,v159,v159\nv_cmp_eq_u32 vcc,-1,v60\ns_add_u32 s72,s74,14680064\n" \
"s_cbranch_vccz .Lrz07\ns_mov_b64 exec,vcc\ns_mov_b32 s97,1\nbuffer_load_dwordx4 v[156:159],v240,s[52:55],s72 offen sc1\ns_mov_b64 exec,-1\n.Lrz07:\ns_cmp_lg_u32 s97,0\ns_cbranch_scc1 .Lrm0\n" \
"s_cmp_eq_u32 s71,0\ns_cbranch_scc1 .Lrb0\ns_branch .Lrow0\n.Lrm0:\ns_mov_b32 s71,1\ns_waitcnt vmcnt(0)\ns_sub_u32 s73,s73,1\ns_cselect_b32 s73,0,s73\n" \
"s_cbranch_scc1 .Lrb0\ns_sleep 2\ns_branch .Lrl0\n.Lredo:\ns_waitcnt vmcnt(0) lgkmcnt(0)\ns_barrier\ns_mov_b64 s[90:91],0\ns_mov_b32 s99,0\n" \
"s_mov_b32 s17,1\ns_lshl_b32 s70,s63,19\ns_add_u32 s37,s70,3145728\ns_lshl_b32 s71,s62,15\ns_add_u32 s72,s37,s64\ns_add_u32 s72,s72,491520\ns_sub_u32 s72,s72,s71\ns_add_u32 m0,s64,0\n" \
"s_nop 0\nbuffer_load_dwordx4 v240,s[52:55],s72 offen offset:0 sc1 lds\nbuffer_load_dwordx4 v240,s[52:55],s72 offen offset:1024 sc1 lds\nbuffer_load_dwordx4 v240,s[52:55],s72 offen offset:2048 sc1 lds\nbuffer_load_dwordx4 v240,s[52:55],s72 offen offset:3072 sc1 lds\ns_add_u32 m0,s64,65536\ns_add_u32 s72,s72,16777216\nbuffer_load_dwordx4 v240,s[52:55],s72 offen offset:0 sc1 lds\n" \
"buffer_load_dwordx4 v240,s[52:55],s72 offen offset:1024 sc1 lds\nbuffer_load_dwordx4 v240,s[52:55],s72 offen offset:2048 sc1 lds\nbuffer_load_dwordx4 v240,s[52:55],s72 offen offset:3072 sc1 lds\ns_add_u32 s72,s37,s64\ns_add_u32 s72,s72,507904\ns_sub_u32 s72,s72,s71\ns_add_u32 m0,s64,16384\ns_nop 0\n" \
"buffer_load_dwordx4 v240,s[52:55],s72 offen offset:0 sc1 lds\nbuffer_load_dwordx4 v240,s[52:55],s72 offen offset:1024 sc1 lds\nbuffer_load_dwordx4 v240,s[52:55],s72 offen offset:2048 sc1 lds\nbuffer_load_dwordx4 v240,s[52:55],s72 offen offset:3072 sc1 lds\ns_add_u32 m0,s64,81920\ns_add_u32 s72,s72,16777216\nbuffer_load_dwordx4 v240,s[52:55],s72 offen offset:0 sc1 lds\nbuffer_load_dwordx4 v240,s[52:55],s72 offen offset:1024 sc1 lds\n" \
"buffer_load_dwordx4 v240,s[52:55],s72 offen offset:2048 sc1 lds\nbuffer_load_dwordx4 v240,s[52:55],s72 offen offset:3072 sc1 lds\ns_add_u32 s72,s37,s64\ns_add_u32 s72,s72,16384\ns_add_u32 s72,s72,s71\ns_add_u32 m0,s64,32768\ns_nop 0\nbuffer_load_dwordx4 v240,s[52:55],s72 offen offset:0 sc1 lds\n" \
"buffer_load_dwordx4 v240,s[52:55],s72 offen offset:1024 sc1 lds\nbuffer_load_dwordx4 v240,s[52:55],s72 offen offset:2048 sc1 lds\nbuffer_load_dwordx4 v240,s[52:55],s72 offen offset:3072 sc1 lds\ns_add_u32 m0,s64,98304\ns_add_u32 s72,s72,16777216\nbuffer_load_dwordx4 v240,s[52:55],s72 offen offset:0 sc1 lds\nbuffer_load_dwordx4 v240,s[52:55],s72 offen offset:1024 sc1 lds\nbuffer_load_dwordx4 v240,s[52:55],s72 offen offset:2048 sc1 lds\n" \
"buffer_load_dwordx4 v240,s[52:55],s72 offen offset:3072 sc1 lds\ns_add_u32 s72,s37,s64\ns_add_u32 s72,s72,0\ns_add_u32 s72,s72,s71\ns_add_u32 m0,s64,49152\ns_nop 0\nbuffer_load_dwordx4 v240,s[52:55],s72 offen offset:0 sc1 lds\nbuffer_load_dwordx4 v240,s[52:55],s72 offen offset:1024 sc1 lds\n" \
"buffer_load_dwordx4 v240,s[52:55],s72 offen offset:2048 sc1 lds\nbuffer_load_dwordx4 v240,s[52:55],s72 offen offset:3072 sc1 lds\ns_add_u32 m0,s64,114688\ns_add_u32 s72,s72,16777216\nbuffer_load_dwordx4 v240,s[52:55],s72 offen offset:0 sc1 lds\nbuffer_load_dwordx4 v240,s[52:55],s72 offen offset:1024 sc1 lds\nbuffer_load_dwordx4 v240,s[52:55],s72 offen offset:2048 sc1 lds\nbuffer_load_dwordx4 v240,s[52:55],s72 offen offset:3072 sc1 lds\n" \
"v_and_b32 v251,15,v249\nv_lshrrev_b32 v250,4,v249\nv_lshlrev_b32 v250,2,v250\nv_sub_u32 v251,v251,v250\ns_lshl_b32 s70,s62,7\ns_lshl_b32 s72,s7,5\ns_sub_u32 s38,s72,s70\ns_add_u32 s38,s38,1920\n" \
"s_sub_u32 s39,s70,s72\ns_add_u32 s39,s39,96\nv_add_u32 v225,s38,v251\nv_add_u32 v225,0,v225\nv_mov_b32 v224,0xff800000\nv_add_u32 v229,s38,v251\nv_add_u32 v229,16,v229\nv_mov_b32 v228,0xff800000\n" \
"v_add_u32 v233,s39,v251\nv_add_u32 v233,0,v233\nv_mov_b32 v232,0xff800000\nv_add_u32 v237,s39,v251\nv_add_u32 v237,16,v237\nv_mov_b32 v236,0xff800000\ns_sub_u32 s38,524288,s71\ns_add_u32 s38,s38,s37\n" \
".Lrl:\nbuffer_load_dwordx4 a[0:3],v240,s[52:55],s37 offen offset:0 sc1\nbuffer_load_dwordx4 a[4:7],v240,s[52:55],s37 offen offset:1024 sc1\nbuffer_load_dwordx4 a[8:11],v240,s[52:55],s37 offen offset:2048 sc1\nbuffer_load_dwordx4 a[12:15],v240,s[52:55],s37 offen offset:3072 sc1\ns_add_u32 s37,s37,4096\nbuffer_load_dwordx4 a[16:19],v240,s[52:55],s37 offen offset:0 sc1\nbuffer_load_dwordx4 a[20:23],v240,s[52:55],s37 offen offset:1024 sc1\n" \
"buffer_load_dwordx4 a[24:27],v240,s[52:55],s37 offen offset:2048 sc1\nbuffer_load_dwordx4 a[28:31],v240,s[52:55],s37 offen offset:3072 sc1\ns_add_u32 s37,s37,4096\nbuffer_load_dwordx4 a[32:35],v240,s[52:55],s37 offen offset:0 sc1\nbuffer_load_dwordx4 a[36:39],v240,s[52:55],s37 offen offset:1024 sc1\nbuffer_load_dwordx4 a[40:43],v240,s[52:55],s37 offen offset:2048 sc1\nbuffer_load_dwordx4 a[44:47],v240,s[52:55],s37 offen offset:3072 sc1\ns_add_u32 s37,s37,4096\n" \
"buffer_load_dwordx4 a[48:51],v240,s[52:55],s37 offen offset:0 sc1\nbuffer_load_dwordx4 a[52:55],v240,s[52:55],s37 offen offset:1024 sc1\nbuffer_load_dwordx4 a[56:59],v240,s[52:55],s37 offen offset:2048 sc1\nbuffer_load_dwordx4 a[60:63],v240,s[52:55],s37 offen offset:3072 sc1\ns_add_u32 s37,s37,4096\ns_waitcnt vmcnt(15)\nM v[128:131],a[0:3],a[128:131],0\ns_waitcnt vmcnt(14)\n" \
"M v[128:131],a[4:7],a[132:135],v[128:131]\ns_waitcnt vmcnt(13)\nM v[128:131],a[8:11],a[136:139],v[128:131]\ns_waitcnt vmcnt(12)\nM v[128:131],a[12:15],a[140:143],v[128:131]\nM v[132:135],a[0:3],a[144:147],0\nM v[132:135],a[4:7],a[148:151],v[132:135]\nM v[132:135],a[8:11],a[152:155],v[132:135]\n" \
"M v[132:135],a[12:15],a[156:159],v[132:135]\nM v[136:139],a[0:3],a[160:163],0\nM v[136:139],a[4:7],a[164:167],v[136:139]\nM v[136:139],a[8:11],a[168:171],v[136:139]\nM v[136:139],a[12:15],a[172:175],v[136:139]\nM v[140:143],a[0:3],a[176:179],0\nM v[140:143],a[4:7],a[180:183],v[140:143]\nM v[140:143],a[8:11],a[184:187],v[140:143]\n" \
"M v[140:143],a[12:15],a[188:191],v[140:143]\ns_waitcnt vmcnt(11)\nM v[144:147],a[16:19],a[128:131],0\ns_waitcnt vmcnt(10)\nM v[144:147],a[20:23],a[132:135],v[144:147]\ns_waitcnt vmcnt(9)\nM v[144:147],a[24:27],a[136:139],v[144:147]\ns_waitcnt vmcnt(8)\n" \
"M v[144:147],a[28:31],a[140:143],v[144:147]\nM v[148:151],a[16:19],a[144:147],0\nM v[148:151],a[20:23],a[148:151],v[148:151]\nM v[148:151],a[24:27],a[152:155],v[148:151]\nM v[148:151],a[28:31],a[156:159],v[148:151]\nM v[152:155],a[16:19],a[160:163],0\nM v[152:155],a[20:23],a[164:167],v[152:155]\nM v[152:155],a[24:27],a[168:171],v[152:155]\n" \
"M v[152:155],a[28:31],a[172:175],v[152:155]\nM v[156:159],a[16:19],a[176:179],0\nM v[156:159],a[20:23],a[180:183],v[156:159]\nM v[156:159],a[24:27],a[184:187],v[156:159]\nM v[156:159],a[28:31],a[188:191],v[156:159]\ns_waitcnt vmcnt(7)\nM v[160:163],a[32:35],a[128:131],0\ns_waitcnt vmcnt(6)\n" \
"M v[160:163],a[36:39],a[132:135],v[160:163]\ns_waitcnt vmcnt(5)\nM v[160:163],a[40:43],a[136:139],v[160:163]\ns_waitcnt vmcnt(4)\nM v[160:163],a[44:47],a[140:143],v[160:163]\nM v[164:167],a[32:35],a[144:147],0\nM v[164:167],a[36:39],a[148:151],v[164:167]\nM v[164:167],a[40:43],a[152:155],v[164:167]\n" \
"M v[164:167],a[44:47],a[156:159],v[164:167]\nM v[168:171],a[32:35],a[160:163],0\nM v[168:171],a[36:39],a[164:167],v[168:171]\nM v[168:171],a[40:43],a[168:171],v[168:171]\nM v[168:171],a[44:47],a[172:175],v[168:171]\nM v[172:175],a[32:35],a[176:179],0\nM v[172:175],a[36:39],a[180:183],v[172:175]\nM v[172:175],a[40:43],a[184:187],v[172:175]\n" \
"M v[172:175],a[44:47],a[188:191],v[172:175]\ns_waitcnt vmcnt(3)\nM v[176:179],a[48:51],a[128:131],0\ns_waitcnt vmcnt(2)\nM v[176:179],a[52:55],a[132:135],v[176:179]\ns_waitcnt vmcnt(1)\nM v[176:179],a[56:59],a[136:139],v[176:179]\ns_waitcnt vmcnt(0)\n" \
"M v[176:179],a[60:63],a[140:143],v[176:179]\nM v[180:183],a[48:51],a[144:147],0\nM v[180:183],a[52:55],a[148:151],v[180:183]\nM v[180:183],a[56:59],a[152:155],v[180:183]\nM v[180:183],a[60:63],a[156:159],v[180:183]\nM v[184:187],a[48:51],a[160:163],0\nM v[184:187],a[52:55],a[164:167],v[184:187]\nM v[184:187],a[56:59],a[168:171],v[184:187]\n" \
"M v[184:187],a[60:63],a[172:175],v[184:187]\nM v[188:191],a[48:51],a[176:179],0\nM v[188:191],a[52:55],a[180:183],v[188:191]\nM v[188:191],a[56:59],a[184:187],v[188:191]\nM v[188:191],a[60:63],a[188:191],v[188:191]\nv_cmp_le_i32 vcc,0,v225\ns_nop 1\nv_cndmask_b32 v128,v252,v128,vcc\n" \
"v_max_f32 v224,v224,v128\nv_cmp_le_i32 vcc,1,v225\ns_nop 1\nv_cndmask_b32 v129,v252,v129,vcc\nv_max_f32 v224,v224,v129\nv_cmp_le_i32 vcc,2,v225\ns_nop 1\nv_cndmask_b32 v130,v252,v130,vcc\n" \
"v_max_f32 v224,v224,v130\nv_cmp_le_i32 vcc,3,v225\ns_nop 1\nv_cndmask_b32 v131,v252,v131,vcc\nv_max_f32 v224,v224,v131\nv_cmp_le_i32 vcc,0,v229\ns_nop 1\nv_cndmask_b32 v132,v252,v132,vcc\n" \
"v_max_f32 v228,v228,v132\nv_cmp_le_i32 vcc,1,v229\ns_nop 1\nv_cndmask_b32 v133,v252,v133,vcc\nv_max_f32 v228,v228,v133\nv_cmp_le_i32 vcc,2,v229\ns_nop 1\nv_cndmask_b32 v134,v252,v134,vcc\n" \
"v_max_f32 v228,v228,v134\nv_cmp_le_i32 vcc,3,v229\ns_nop 1\nv_cndmask_b32 v135,v252,v135,vcc\nv_max_f32 v228,v228,v135\nv_cmp_le_i32 vcc,0,v233\ns_nop 1\nv_cndmask_b32 v136,v252,v136,vcc\n" \
"v_max_f32 v232,v232,v136\nv_cmp_le_i32 vcc,1,v233\ns_nop 1\nv_cndmask_b32 v137,v252,v137,vcc\nv_max_f32 v232,v232,v137\nv_cmp_le_i32 vcc,2,v233\ns_nop 1\nv_cndmask_b32 v138,v252,v138,vcc\n" \
"v_max_f32 v232,v232,v138\nv_cmp_le_i32 vcc,3,v233\ns_nop 1\nv_cndmask_b32 v139,v252,v139,vcc\nv_max_f32 v232,v232,v139\nv_cmp_le_i32 vcc,0,v237\ns_nop 1\nv_cndmask_b32 v140,v252,v140,vcc\n" \
"v_max_f32 v236,v236,v140\nv_cmp_le_i32 vcc,1,v237\ns_nop 1\nv_cndmask_b32 v141,v252,v141,vcc\nv_max_f32 v236,v236,v141\nv_cmp_le_i32 vcc,2,v237\ns_nop 1\nv_cndmask_b32 v142,v252,v142,vcc\n" \
"v_max_f32 v236,v236,v142\nv_cmp_le_i32 vcc,3,v237\ns_nop 1\nv_cndmask_b32 v143,v252,v143,vcc\nv_max_f32 v236,v236,v143\nv_cmp_le_i32 vcc,16,v225\ns_nop 1\nv_cndmask_b32 v144,v252,v144,vcc\n" \
"v_max_f32 v224,v224,v144\nv_cmp_le_i32 vcc,17,v225\ns_nop 1\nv_cndmask_b32 v145,v252,v145,vcc\nv_max_f32 v224,v224,v145\nv_cmp_le_i32 vcc,18,v225\ns_nop 1\nv_cndmask_b32 v146,v252,v146,vcc\n" \
"v_max_f32 v224,v224,v146\nv_cmp_le_i32 vcc,19,v225\ns_nop 1\nv_cndmask_b32 v147,v252,v147,vcc\nv_max_f32 v224,v224,v147\nv_cmp_le_i32 vcc,16,v229\ns_nop 1\nv_cndmask_b32 v148,v252,v148,vcc\n" \
"v_max_f32 v228,v228,v148\nv_cmp_le_i32 vcc,17,v229\ns_nop 1\nv_cndmask_b32 v149,v252,v149,vcc\nv_max_f32 v228,v228,v149\nv_cmp_le_i32 vcc,18,v229\ns_nop 1\nv_cndmask_b32 v150,v252,v150,vcc\n" \
"v_max_f32 v228,v228,v150\nv_cmp_le_i32 vcc,19,v229\ns_nop 1\nv_cndmask_b32 v151,v252,v151,vcc\nv_max_f32 v228,v228,v151\nv_cmp_le_i32 vcc,16,v233\ns_nop 1\nv_cndmask_b32 v152,v252,v152,vcc\n" \
"v_max_f32 v232,v232,v152\nv_cmp_le_i32 vcc,17,v233\ns_nop 1\nv_cndmask_b32 v153,v252,v153,vcc\nv_max_f32 v232,v232,v153\nv_cmp_le_i32 vcc,18,v233\ns_nop 1\nv_cndmask_b32 v154,v252,v154,vcc\n" \
"v_max_f32 v232,v232,v154\nv_cmp_le_i32 vcc,19,v233\ns_nop 1\nv_cndmask_b32 v155,v252,v155,vcc\nv_max_f32 v232,v232,v155\nv_cmp_le_i32 vcc,16,v237\ns_nop 1\nv_cndmask_b32 v156,v252,v156,vcc\n" \
"v_max_f32 v236,v236,v156\nv_cmp_le_i32 vcc,17,v237\ns_nop 1\nv_cndmask_b32 v157,v252,v157,vcc\nv_max_f32 v236,v236,v157\nv_cmp_le_i32 vcc,18,v237\ns_nop 1\nv_cndmask_b32 v158,v252,v158,vcc\n" \
"v_max_f32 v236,v236,v158\nv_cmp_le_i32 vcc,19,v237\ns_nop 1\nv_cndmask_b32 v159,v252,v159,vcc\nv_max_f32 v236,v236,v159\nv_cmp_le_i32 vcc,32,v225\ns_nop 1\nv_cndmask_b32 v160,v252,v160,vcc\n" \
"v_max_f32 v224,v224,v160\nv_cmp_le_i32 vcc,33,v225\ns_nop 1\nv_cndmask_b32 v161,v252,v161,vcc\nv_max_f32 v224,v224,v161\nv_cmp_le_i32 vcc,34,v225\ns_nop 1\nv_cndmask_b32 v162,v252,v162,vcc\n" \
"v_max_f32 v224,v224,v162\nv_cmp_le_i32 vcc,35,v225\ns_nop 1\nv_cndmask_b32 v163,v252,v163,vcc\nv_max_f32 v224,v224,v163\nv_cmp_le_i32 vcc,32,v229\ns_nop 1\nv_cndmask_b32 v164,v252,v164,vcc\n" \
"v_max_f32 v228,v228,v164\nv_cmp_le_i32 vcc,33,v229\ns_nop 1\nv_cndmask_b32 v165,v252,v165,vcc\nv_max_f32 v228,v228,v165\nv_cmp_le_i32 vcc,34,v229\ns_nop 1\nv_cndmask_b32 v166,v252,v166,vcc\n" \
"v_max_f32 v228,v228,v166\nv_cmp_le_i32 vcc,35,v229\ns_nop 1\nv_cndmask_b32 v167,v252,v167,vcc\nv_max_f32 v228,v228,v167\nv_cmp_le_i32 vcc,32,v233\ns_nop 1\nv_cndmask_b32 v168,v252,v168,vcc\n" \
"v_max_f32 v232,v232,v168\nv_cmp_le_i32 vcc,33,v233\ns_nop 1\nv_cndmask_b32 v169,v252,v169,vcc\nv_max_f32 v232,v232,v169\nv_cmp_le_i32 vcc,34,v233\ns_nop 1\nv_cndmask_b32 v170,v252,v170,vcc\n" \
"v_max_f32 v232,v232,v170\nv_cmp_le_i32 vcc,35,v233\ns_nop 1\nv_cndmask_b32 v171,v252,v171,vcc\nv_max_f32 v232,v232,v171\nv_cmp_le_i32 vcc,32,v237\ns_nop 1\nv_cndmask_b32 v172,v252,v172,vcc\n" \
"v_max_f32 v236,v236,v172\nv_cmp_le_i32 vcc,33,v237\ns_nop 1\nv_cndmask_b32 v173,v252,v173,vcc\nv_max_f32 v236,v236,v173\nv_cmp_le_i32 vcc,34,v237\ns_nop 1\nv_cndmask_b32 v174,v252,v174,vcc\n" \
"v_max_f32 v236,v236,v174\nv_cmp_le_i32 vcc,35,v237\ns_nop 1\nv_cndmask_b32 v175,v252,v175,vcc\nv_max_f32 v236,v236,v175\nv_cmp_le_i32 vcc,48,v225\ns_nop 1\nv_cndmask_b32 v176,v252,v176,vcc\n" \
"v_max_f32 v224,v224,v176\nv_cmp_le_i32 vcc,49,v225\ns_nop 1\nv_cndmask_b32 v177,v252,v177,vcc\nv_max_f32 v224,v224,v177\nv_cmp_le_i32 vcc,50,v225\ns_nop 1\nv_cndmask_b32 v178,v252,v178,vcc\n" \
"v_max_f32 v224,v224,v178\nv_cmp_le_i32 vcc,51,v225\ns_nop 1\nv_cndmask_b32 v179,v252,v179,vcc\nv_max_f32 v224,v224,v179\nv_cmp_le_i32 vcc,48,v229\ns_nop 1\nv_cndmask_b32 v180,v252,v180,vcc\n" \
"v_max_f32 v228,v228,v180\nv_cmp_le_i32 vcc,49,v229\ns_nop 1\nv_cndmask_b32 v181,v252,v181,vcc\nv_max_f32 v228,v228,v181\nv_cmp_le_i32 vcc,50,v229\ns_nop 1\nv_cndmask_b32 v182,v252,v182,vcc\n" \
"v_max_f32 v228,v228,v182\nv_cmp_le_i32 vcc,51,v229\ns_nop 1\nv_cndmask_b32 v183,v252,v183,vcc\nv_max_f32 v228,v228,v183\nv_cmp_le_i32 vcc,48,v233\ns_nop 1\nv_cndmask_b32 v184,v252,v184,vcc\n" \
"v_max_f32 v232,v232,v184\nv_cmp_le_i32 vcc,49,v233\ns_nop 1\nv_cndmask_b32 v185,v252,v185,vcc\nv_max_f32 v232,v232,v185\nv_cmp_le_i32 vcc,50,v233\ns_nop 1\nv_cndmask_b32 v186,v252,v186,vcc\n" \
"v_max_f32 v232,v232,v186\nv_cmp_le_i32 vcc,51,v233\ns_nop 1\nv_cndmask_b32 v187,v252,v187,vcc\nv_max_f32 v232,v232,v187\nv_cmp_le_i32 vcc,48,v237\ns_nop 1\nv_cndmask_b32 v188,v252,v188,vcc\n" \
"v_max_f32 v236,v236,v188\nv_cmp_le_i32 vcc,49,v237\ns_nop 1\nv_cndmask_b32 v189,v252,v189,vcc\nv_max_f32 v236,v236,v189\nv_cmp_le_i32 vcc,50,v237\ns_nop 1\nv_cndmask_b32 v190,v252,v190,vcc\n" \
"v_max_f32 v236,v236,v190\nv_cmp_le_i32 vcc,51,v237\ns_nop 1\nv_cndmask_b32 v191,v252,v191,vcc\nv_max_f32 v236,v236,v191\nv_subrev_u32 v225,64,v225\nv_subrev_u32 v229,64,v229\nv_subrev_u32 v233,64,v233\n" \
"v_subrev_u32 v237,64,v237\ns_cmp_lt_u32 s37,s38\ns_cbranch_scc1 .Lrl\ns_waitcnt vmcnt(0)\ns_barrier\nds_read_b128 a[0:3],v240 offset:0\nds_read_b128 a[4:7],v240 offset:1024\nds_read_b128 a[8:11],v240 offset:2048\n" \
"ds_read_b128 a[12:15],v240 offset:3072\nds_read_b128 a[16:19],v240 offset:4096\nds_read_b128 a[20:23],v240 offset:5120\nds_read_b128 a[24:27],v240 offset:6144\nds_read_b128 a[28:31],v240 offset:7168\nds_read_b128 a[32:35],v240 offset:8192\nds_read_b128 a[36:39],v240 offset:9216\nds_read_b128 a[40:43],v240 offset:10240\n" \
"ds_read_b128 a[44:47],v240 offset:11264\nds_read_b128 a[48:51],v240 offset:12288\nds_read_b128 a[52:55],v240 offset:13312\nds_read_b128 a[56:59],v240 offset:14336\nds_read_b128 a[60:63],v240 offset:15360\ns_cmp_eq_u32 s62,0\ns_cselect_b32 s36,0x8000,0\ns_add_u32 s36,s36,s64\n" \
"s_cmp_eq_u32 s36,s96\ns_cselect_b32 s72,0xc000,s98\ns_add_u32 s65,s36,s72\ns_cmp_eq_u32 s65,s96\ns_cselect_b32 s72,0xc000,s98\ns_add_u32 s66,s65,s72\ns_cmp_eq_u32 s66,s96\ns_cselect_b32 s72,0xc000,s98\n" \
"s_add_u32 s95,s66,s72\ns_add_u32 s97,s96,65536\ns_add_u32 s15,s64,s46\ns_cmp_gt_i32 s62,-1\ns_cselect_b32 s72,s98,0xfffe4000\ns_add_u32 s15,s15,s72\ns_mov_b32 s94,0xc000\ns_cmp_eq_u32 s62,7\n" \
"s_cselect_b32 s72,98304,0\ns_sub_u32 s97,s97,s72\ns_cmp_lt_u32 s62,4\ns_cselect_b32 s72,32768,0\ns_add_u32 s97,s97,s72\nv_and_b32 v251,31,v249\nv_lshlrev_b32 v251,2,v251\ns_lshl_b32 s80,s63,7\n" \
"s_add_u32 s80,s80,86016\nv_add_u32 v250,0xc000,v241\ns_waitcnt lgkmcnt(0)\ns_branch .Lattn\n.Lfin:\n" \
""
constexpr size_t WS_DBG = (size_t)51 << 20;
constexpr unsigned LDS_BYTES = 163840;
struct Args {
    const u16 *x, *ckv, *w_q, *w_uk, *w_uv, *w_o, *w_r, *wg, *wu, *wd, *cvec, *w_mod;
    char* ws; u16* out;
};
typedef const __attribute__((address_space(4))) Args* kargs_t;
#define STAMPD(i) do {} while (0)
#define C8(p, a) p #a "0", p #a "1", p #a "2", p #a "3", p #a "4", p #a "5", p #a "6", p #a "7", p #a "8", p #a "9"
#define C100(p, a) C8(p, a##0), C8(p, a##1), C8(p, a##2), C8(p, a##3), C8(p, a##4), C8(p, a##5), C8(p, a##6), C8(p, a##7), C8(p, a##8), C8(p, a##9)
#define C10_99(p) C8(p, 1), C8(p, 2), C8(p, 3), C8(p, 4), C8(p, 5), C8(p, 6), C8(p, 7), C8(p, 8), C8(p, 9)
#define C200_249(p) C8(p, 20), C8(p, 21), C8(p, 22), C8(p, 23), C8(p, 24)
#define BODY_CLOB "memory", "vcc", "scc", "s0", "s1", "s2", "s3", "s8", "s9", C8("s", 1), C8("s", 2), "s30", "s31", "s33", "s34", "s35", "s36", "s37", "s38", "s39", C8("s", 4), C8("s", 5), C8("s", 6), C8("s", 7), C8("s", 8), C8("s", 9), \
    C8("v", ), C10_99("v"), C100("v", 1), C200_249("v"), "v250", "v251", "v252", "v253", "v254", C8("a", ), C10_99("a"), C100("a", 1), C200_249("a"), "a250", "a251", "a252", "a253", "a254", "a255"

__global__ __launch_bounds__(256, 1) void k_all(const Args a_) {
    kargs_t ap = (kargs_t)__builtin_amdgcn_kernarg_segment_ptr(); unsigned bid = blockIdx.x, wid = rfl(threadIdx.x >> 6), sv_;
    asm volatile(BODY_ASM : "+{s[4:5]}"(ap), "+{s6}"(bid), "+{s7}"(wid), "={v255}"(sv_) :: BODY_CLOB);
    asm volatile(".Lcode_e:");
}
extern "C" void kernel_launch(void* const* d_in, const int* in_sizes, int n_in, void* d_out, int out_size, void* d_ws, size_t ws_size, hipStream_t stream) {
    Args a;
    a.x = (const u16*)d_in[0]; a.ckv = (const u16*)d_in[1]; a.w_q = (const u16*)d_in[2]; a.w_uk = (const u16*)d_in[3]; a.w_uv = (const u16*)d_in[4]; a.w_o = (const u16*)d_in[5];
    a.w_r = (const u16*)d_in[7]; a.wg = (const u16*)d_in[8]; a.wu = (const u16*)d_in[9]; a.wd = (const u16*)d_in[10]; a.cvec = (const u16*)d_in[11]; a.w_mod = (const u16*)d_in[12];
    a.ws = (char*)d_ws;
    a.out = (u16*)d_out;
    (void)hipFuncSetAttribute((const void*)k_all, hipFuncAttributeMaxDynamicSharedMemorySize, LDS_BYTES);
    k_all<<<256, 256, LDS_BYTES, stream>>>(a);
}
```
